```python
import torch

def _softmax(x, axis=-1):
    return torch.softmax(x, dim=axis)

def _gelu(x):
    return torch.nn.functional.gelu(x, approximate='tanh')

def _silu(x):
    return torch.nn.functional.silu(x)

def _sigmoid(x):
    return torch.sigmoid(x)

def _layernorm(x, axis=-1, eps=1e-05):
    m = x.mean(dim=axis, keepdim=True)
    v = x.var(dim=axis, keepdim=True, unbiased=False)
    return (x - m) / torch.sqrt(v + eps)

def _scatter_add(tgt, idx, src, axis=0):
    return tgt.clone().index_add_(axis, idx, src)

def _segment_sum(src, seg, n):
    out = torch.zeros((n,) + src.shape[1:], dtype=src.dtype)
    return out.index_add_(0, seg, src)

def _pad(x, pw):
    flat = tuple((v for pair in reversed(pw) for v in pair))
    return torch.nn.functional.pad(x, flat)
kernel_name = 'interleave_layers__memory_aug_recurrent_mem_tr__mixer_s_mlp__prompt'

def setup_inputs():

    _w = lambda a: (lambda t: t.to(torch.bfloat16) if t.is_floating_point() else t)(torch.as_tensor(a))

    def _A_setup():
        B, S, Dm = (1, 2048, 640)
        H, Dh = (32, 192)
        M, seg = (8, 32)
        seg = min(seg, S)
        S = S // seg * seg
        return {'x': _w((lambda *s: torch.randn(*s, dtype=torch.bfloat16) * 0.05)(B, S, Dm)), 'mem0': _w((lambda *s: torch.randn(*s, dtype=torch.bfloat16) * 0.8)(B, M, Dm)), 'Wq': _w((lambda *s: torch.randn(*s, dtype=torch.bfloat16) * 0.8)(Dm, H, Dh) * Dm ** (-0.5)), 'Wk': _w((lambda *s: torch.randn(*s, dtype=torch.bfloat16) * 0.8)(Dm, H, Dh) * Dm ** (-0.5)), 'Wv': _w((lambda *s: torch.randn(*s, dtype=torch.bfloat16) * 0.8)(Dm, H, Dh) * Dm ** (-0.5)), 'Wo': _w((lambda *s: torch.randn(*s, dtype=torch.bfloat16) * 0.8)(H, Dh, Dm) * (H * Dh) ** (-0.5)), 'seg_len': seg}
    _A_in = _A_setup()

    def _B_setup():
        B, S, D, F = (1, 2048, 640, 1536)
        return {'x': _w((lambda *s: torch.randn(*s, dtype=torch.bfloat16) * 0.05)(B, S, D)), 'w1': _w((lambda *s: torch.randn(*s, dtype=torch.bfloat16) * 0.8)(D, D) * D ** (-0.5)), 'w2': _w((lambda *s: torch.randn(*s, dtype=torch.bfloat16) * 0.8)(D, D) * D ** (-0.5)), 'w3': _w((lambda *s: torch.randn(*s, dtype=torch.bfloat16) * 0.8)(D, F) * D ** (-0.5)), 'w4': _w((lambda *s: torch.randn(*s, dtype=torch.bfloat16) * 0.8)(F, D) * F ** (-0.5))}
    _B_in = _B_setup()
    return {'A_x': _A_in['x'], 'A_mem0': _A_in['mem0'], 'A_Wq': _A_in['Wq'], 'A_Wk': _A_in['Wk'], 'A_Wv': _A_in['Wv'], 'A_Wo': _A_in['Wo'], 'A_seg_len': _A_in['seg_len'], 'B_x': _B_in['x'], 'B_w1': _B_in['w1'], 'B_w2': _B_in['w2'], 'B_w3': _B_in['w3'], 'B_w4': _B_in['w4']}

def reference(A_x, A_mem0, A_Wq, A_Wk, A_Wv, A_Wo, A_seg_len, B_x, B_w1, B_w2, B_w3, B_w4):

    def _A_ref(x, mem0, Wq, Wk, Wv, Wo, seg_len):
        B, S, Dm = x.shape
        H, Dh = (Wq.shape[1], Wq.shape[2])
        M = mem0.shape[1]
        scale = Dh ** (-0.5)
        n_seg = S // seg_len
        L = M + seg_len
        idx = torch.arange(L)
        mask = (idx[:, None] < M) | (idx[:, None] >= idx[None, :])
        mem = mem0
        outs = []
        for s in range(n_seg):
            xs = x[:, s * seg_len:(s + 1) * seg_len, :].clone()
            h = torch.concatenate([mem, xs], axis=1)
            q = torch.einsum('btd,dhe->bhte', h, Wq)
            k = torch.einsum('btd,dhe->bhte', h, Wk)
            v = torch.einsum('btd,dhe->bhte', h, Wv)
            scores = torch.einsum('bhqe,bhke->bhqk', q, k) * scale
            scores = torch.where(mask[None, None], scores, -1000000000.0)
            p = _softmax(scores, axis=-1)
            o = torch.einsum('bhqk,bhke->bhqe', p, v)
            o = torch.einsum('bhte,hed->btd', o, Wo)
            mem = o[:, :M, :].clone()
            outs.append(o[:, M:, :])
        return torch.concatenate(outs, axis=1)

    def _B_ref(x, w1, w2, w3, w4):
        B, S, D = x.shape
        h = _gelu(_layernorm(x, axis=-1) @ w1)
        c = D // 4
        g0 = _pad(h[:, :, 0 * c:1 * c], ((0, 0), (1, 0), (0, 0)))[:, :S, :]
        g1 = _pad(h[:, :, 1 * c:2 * c], ((0, 0), (0, 1), (0, 0)))[:, 1:, :]
        g2 = _pad(h[:, :, 2 * c:3 * c], ((0, 0), (2, 0), (0, 0)))[:, :S, :]
        g3 = _pad(h[:, :, 3 * c:], ((0, 0), (0, 2), (0, 0)))[:, 2:, :]
        hs = torch.concatenate([g0, g1, g2, g3], axis=-1) @ w2
        x1 = x + hs
        h2 = _gelu(_layernorm(x1, axis=-1) @ w3) @ w4
        return x1 + h2
    x = A_x
    x = x + _A_ref(_layernorm(x, axis=-1), A_mem0, A_Wq, A_Wk, A_Wv, A_Wo, A_seg_len)
    x = x + _B_ref(_layernorm(x, axis=-1), B_w1, B_w2, B_w3, B_w4)
    x = x + _A_ref(_layernorm(x, axis=-1), A_mem0, A_Wq, A_Wk, A_Wv, A_Wo, A_seg_len)
    x = x + _B_ref(_layernorm(x, axis=-1), B_w1, B_w2, B_w3, B_w4)
    return x
```

```cpp
#include <hip/hip_runtime.h>
#include <stdint.h>
#include <type_traits>

typedef unsigned short bf16_t;
using bf16x2 = __attribute__((ext_vector_type(2))) __bf16;
using bf16x8 = __attribute__((ext_vector_type(8))) __bf16;
using bf16x16 = __attribute__((ext_vector_type(16))) __bf16;
using f32x2 = __attribute__((ext_vector_type(2))) float;
using f32x4 = __attribute__((ext_vector_type(4))) float;
using u32x2 = __attribute__((ext_vector_type(2))) unsigned;
using u32x4 = __attribute__((ext_vector_type(4))) unsigned;
typedef __amdgpu_buffer_rsrc_t rsrc_t;

__device__ __forceinline__ unsigned pack2(float a, float b) {
    bf16x2 r = __builtin_convertvector((f32x2){a, b}, bf16x2);
    return __builtin_bit_cast(unsigned, r);
}
__device__ __forceinline__ float rbf(float f) { return __uint_as_float(pack2(f, 0.f) << 16); }
__device__ __forceinline__ float lo2f(unsigned u) { return __uint_as_float(u << 16); }
__device__ __forceinline__ float hi2f(unsigned u) { return __uint_as_float(u & 0xFFFF0000u); }
__device__ __forceinline__ rsrc_t mkrsrc(const void* p, unsigned bytes) {
    return __builtin_amdgcn_make_buffer_rsrc((void*)p, 0, bytes, 0x00020000);
}

constexpr int S_ = 2048, DM = 640, NH = 32, DH = 192, HD = NH * DH  , SEG = 32, NSEG = 64;
constexpr int FF = 1536;

#define XMAP
#define NCPY 4
constexpr unsigned NSLOT = 4, NCP = NCPY;
constexpr unsigned XP_COPY = 20480, XP_SLOT = NCP * XP_COPY;
constexpr unsigned XM_COPY = 10240, XM_SLOT = NCP * XM_COPY;
constexpr unsigned VM_SLOT = 40 * 8 * 1024;
constexpr unsigned PX_SLOT = 8 * 8192;
constexpr unsigned RG_XP = 0, RG_XM = RG_XP + NSLOT * XP_SLOT, RG_VM = RG_XM + NSLOT * XM_SLOT, RG_PX = RG_VM + NSLOT * VM_SLOT;
constexpr unsigned VX_SLOT = 40 * 32 * 1024;
constexpr unsigned RG_ST = RG_PX + NSLOT * PX_SLOT, RG_L = RG_ST + NSEG * 8 * 256;
constexpr unsigned FL0 = 2 * RG_L, FL_VO = 512, FL_BYTES = FL_VO + 2 * 128 * 128;
constexpr unsigned ACT = S_ * DM * 2;
constexpr unsigned D_XR = 0, D_XN = ACT, D_XA = 2 * ACT, D_HS = 3 * ACT, D_X1 = 4 * ACT, D_H2 = 5 * ACT, D_SS = D_H2 + S_ * FF * 2;
#define PXROT 5
#define W4SL 6
#define RXDEL 4
#define VXD 32
#define VXAUX 16
constexpr unsigned D_VX = D_SS + S_ * 8, D_L = D_VX + VXD * VX_SLOT;
constexpr unsigned DATA0 = (FL0 + FL_BYTES + 4095) & ~4095u;
constexpr int AH = 3;
constexpr unsigned WT0 = DATA0 + 2 * D_L;
constexpr unsigned WT_QKV = WT0, WT_O = WT_QKV + 3 * HD * DM * 2, WT_1 = WT_O + HD * DM * 2, WT_2 = WT_1 + DM * DM * 2;
constexpr unsigned WT_3 = WT_2 + DM * DM * 2, WT_4 = WT_3 + DM * FF * 2, WS_END = WT_4 + DM * FF * 2;

struct TileJob { const bf16_t* W; bf16_t* out; int perm, N, ksh, blk0; };
struct TileJobs { TileJob j[8]; };
__device__ __forceinline__ void tile_body(const TileJobs& J, int b) {
    __shared__ __attribute__((aligned(16))) bf16_t t[32][136];
    int ji = 0;
#pragma unroll
    for (int i = 1; i < 8; ++i) if (b >= J.j[i].blk0) ji = i;
    const bf16_t* W = J.j[ji].W; bf16_t* out = J.j[ji].out;
    const int N = J.j[ji].N, ksh = J.j[ji].ksh, perm = J.j[ji].perm;
    b -= J.j[ji].blk0;
    const int nb = N / 128;
    const int ksg = b / nb, n0 = (b % nb) * 128;
    const int tid = threadIdx.x;
    const int grp = ksg / ksh, ks = ksg % ksh;
#pragma unroll
    for (int i = 0; i < 2; ++i) {
        const int u = tid + i * 256, r = u >> 4, c = u & 15;
        const int row = perm ? grp * DH + ks * 16 + (r >> 4) * 96 + (r & 15) : ksg * 32 + r;
        *(u32x4*)&t[r][c * 8] = *(const u32x4*)(W + (size_t)row * N + n0 + c * 8);
    }
    __syncthreads();
    const int lane = tid & 63;
#pragma unroll
    for (int i = 0; i < 2; ++i) {
        const int ntl = (tid >> 6) + i * 4, n = ntl * 16 + (lane & 15);
        unsigned w[4];
#pragma unroll
        for (int j = 0; j < 4; ++j) {
            const int kk = perm ? (j >> 1) * 16 + (lane >> 4) * 4 + (j & 1) * 2 : (lane >> 4) * 8 + 2 * j;
            w[j] = (unsigned)t[kk][n] | ((unsigned)t[kk + 1][n] << 16);
        }
        *(u32x4*)(out + ((size_t)(grp * (N / 16) + n0 / 16 + ntl) * ksh + ks) * 512 + lane * 8) = (u32x4){w[0], w[1], w[2], w[3]};
    }
}

#define SB() __builtin_amdgcn_sched_barrier(0)
#define LAS __attribute__((address_space(3)))
template <class T> __device__ __forceinline__ T lds_ld(unsigned a) { return *(const LAS T*)(a); }
template <class T> __device__ __forceinline__ void lds_st(unsigned a, T v) { *(LAS T*)(a) = v; }
constexpr unsigned OOB = 0x7FFFFF00u;
template <int N> __device__ __forceinline__ void wait_vm() { asm volatile("s_waitcnt vmcnt(%0)" :: "i"(N) : "memory"); }
__device__ __forceinline__ void bar() {
    asm volatile("s_waitcnt lgkmcnt(0)" ::: "memory");
    __builtin_amdgcn_s_barrier();
    asm volatile("" ::: "memory");
}
__device__ __forceinline__ int lane_id() {
    int ln;
    asm volatile("v_mbcnt_lo_u32_b32 %0, -1, 0\n\tv_mbcnt_hi_u32_b32 %0, -1, %0" : "=v"(ln));
    return ln;
}
__device__ __forceinline__ void swap16(unsigned& a, unsigned& b) { asm volatile("s_nop 3\n\tv_permlane16_swap_b32 %0, %1\n\ts_nop 1" : "+v"(a), "+v"(b)); }
__device__ __forceinline__ void swap32(unsigned& a, unsigned& b) { asm volatile("s_nop 3\n\tv_permlane32_swap_b32 %0, %1\n\ts_nop 1" : "+v"(a), "+v"(b)); }
__device__ __forceinline__ float sum_x16(float x) { unsigned a = __float_as_uint(x), b = a; swap16(a, b); return __uint_as_float(a) + __uint_as_float(b); }
__device__ __forceinline__ float sum_x32(float x) { unsigned a = __float_as_uint(x), b = a; swap32(a, b); return __uint_as_float(a) + __uint_as_float(b); }
__device__ __forceinline__ float max_x16(float x) { unsigned a = __float_as_uint(x), b = a; swap16(a, b); return __builtin_fmaxf(__uint_as_float(a), __uint_as_float(b)); }
__device__ __forceinline__ float max_x32(float x) { unsigned a = __float_as_uint(x), b = a; swap32(a, b); return __builtin_fmaxf(__uint_as_float(a), __uint_as_float(b)); }
__device__ __forceinline__ void sum2_rows(float& s, float& q) {
    unsigned a = __float_as_uint(s), b = a, c = __float_as_uint(q), d = c;
    asm volatile("s_nop 2\n\tv_permlane16_swap_b32 %0, %1\n\tv_permlane16_swap_b32 %2, %3\n\tv_add_f32 %0, %0, %1\n\tv_add_f32 %2, %2, %3\n\tv_mov_b32 %1, %0\n\tv_mov_b32 %3, %2\n\t"
                 "s_nop 1\n\tv_permlane32_swap_b32 %0, %1\n\tv_permlane32_swap_b32 %2, %3\n\tv_add_f32 %0, %0, %1\n\tv_add_f32 %2, %2, %3" : "+v"(a), "+v"(b), "+v"(c), "+v"(d));
    s = __uint_as_float(a); q = __uint_as_float(c);
}
__device__ __forceinline__ u32x4 merge16(u32x2 a, u32x2 b) {
    unsigned a0 = a[0], a1 = a[1], b0 = b[0], b1 = b[1];
    asm volatile("s_nop 3\n\tv_permlane16_swap_b32 %0, %2\n\tv_permlane16_swap_b32 %1, %3\n\ts_nop 1" : "+v"(a0), "+v"(a1), "+v"(b0), "+v"(b1));
    return (u32x4){a0, a1, b0, b1};
}
template <int AUXV>
__device__ __forceinline__ void st128_(u32x4 v, rsrc_t r, unsigned voff, unsigned soff) {
    __builtin_amdgcn_raw_buffer_store_b128(v, r, voff, soff, AUXV);
    asm volatile("s_nop 1" :: "v"(v) : "memory");
}
#define st128(V, R, VO_, SO_) st128_<16>(V, R, VO_, SO_)
#define LD128(R, VO_, SO_) __builtin_amdgcn_raw_buffer_load_b128(R, VO_, SO_, 16)
#define LD64(R, VO_, SO_) __builtin_amdgcn_raw_buffer_load_b64(R, VO_, SO_, 16)
#define BF8(X) __builtin_bit_cast(bf16x8, X)
__device__ __forceinline__ float gelu_tanh(float x) {
    const float c0 = -2.0f * 0.7978845608028654f * 1.4426950408889634f, c1 = c0 * 0.044715f;
    const float e = __builtin_amdgcn_exp2f(x * __builtin_fmaf(x * x, c1, c0));
    return x * __builtin_amdgcn_rcpf(1.0f + e);
}
#define MFMA(A_, B_, C_) C_ = __builtin_amdgcn_mfma_f32_16x16x32_bf16(A_, B_, C_, 0, 0, 0)
#define SMF(A_, B_, C_) C_ = __builtin_amdgcn_smfmac_f32_16x16x64_bf16(A_, B_, C_, sidx, 0, 0)
__device__ __forceinline__ float ln_den(float var) { return rbf(__builtin_amdgcn_sqrtf(rbf(rbf(var) + 1e-5f))); }
__device__ __forceinline__ float rcp(float x) { return __builtin_amdgcn_rcpf(x); }

struct Params {
    char* ws;
    const bf16_t* mem0;
    bf16_t* out;
};

#define RLX_AGENT __ATOMIC_RELAXED, __HIP_MEMORY_SCOPE_AGENT
#define SENT 0xFFFFFFFFu
constexpr int SPIN_CAP = 400000;
__device__ __forceinline__ bool spin_fail(int spins, const Params& P) {
    if ((spins & 1023) == 1023) {
        const rsrc_t rt = mkrsrc(P.ws + FL0, FL_BYTES);
        unsigned o;
        asm volatile("v_mov_b32 %0, 0x100" : "=v"(o));
        if (__builtin_amdgcn_readfirstlane(__builtin_amdgcn_raw_buffer_load_b32(rt, o, 0, 16)) != 0u) return true;
        if (spins > SPIN_CAP) { __builtin_amdgcn_raw_buffer_store_b32(o, rt, o, 0, 16); return true; }
    }
    return false;
}
__device__ __forceinline__ bool ok4(u32x4 v) { return v[0] != SENT && v[1] != SENT && v[2] != SENT && v[3] != SENT; }
__device__ __forceinline__ unsigned nosent(unsigned d) { return d < 0xFFFFFFFEu ? d : 0xFFFFFFFEu; }
__device__ __forceinline__ u32x4 nosent4(u32x4 v) { return (u32x4){nosent(v[0]), nosent(v[1]), nosent(v[2]), nosent(v[3])}; }
__device__ __forceinline__ unsigned umax3(unsigned a, unsigned b, unsigned c) { const unsigned m = a > b ? a : b; return m > c ? m : c; }
__device__ __forceinline__ unsigned umax4(unsigned m, u32x4 v) { return umax3(umax3(m, v[0], v[1]), v[2], v[3]); }
__device__ __forceinline__ u32x4 sent4() { unsigned sv; asm volatile("v_mov_b32 %0, -1" : "=v"(sv)); return (u32x4){sv, sv, sv, sv}; }
template <int N>
__device__ __forceinline__ void poll(rsrc_t rs, unsigned base, const unsigned (&off)[N], u32x4 (&v)[N], const Params& P) {
    for (int spins = 0;; ++spins) {
        unsigned m = 0;
#pragma unroll
        for (int k = 0; k < N; ++k) v[k] = LD128(rs, off[k], base);
#pragma unroll
        for (int k = 0; k < N; ++k) m = umax4(m, v[k]);
        if (__all(m != SENT)) return;
        asm volatile("" ::: "memory");
        if (spin_fail(spins, P)) return;
    }
}
__device__ __forceinline__ bf16x16 cat16(bf16x8 a, bf16x8 b) {
    return __builtin_shufflevector(a, b, 0, 1, 2, 3, 4, 5, 6, 7, 8, 9, 10, 11, 12, 13, 14, 15);
}
__device__ __forceinline__ bf16x8 half8(bf16x16 v, int h) {
    return h ? __builtin_shufflevector(v, v, 8, 9, 10, 11, 12, 13, 14, 15) : __builtin_shufflevector(v, v, 0, 1, 2, 3, 4, 5, 6, 7);
}
__device__ __forceinline__ bf16x16 lds_frag(unsigned a, unsigned step) { return cat16(lds_ld<bf16x8>(a), lds_ld<bf16x8>(a + step)); }
template <int I>
__device__ __forceinline__ void dmaq(const char* g, unsigned l16, unsigned dst) {
    asm volatile("s_mov_b32 m0, %2\n\ts_nop 0\n\tglobal_load_lds_dwordx4 %0, %1 offset:%3 sc1" :: "v"(l16), "s"(g), "s"(dst), "i"(I * 1024) : "memory");
}
__device__ __forceinline__ void dma4(const char* g, unsigned l16, unsigned dst) {
    asm volatile("s_mov_b32 m0, %2\n\ts_nop 0\n\tglobal_load_lds_dwordx4 %0, %1 sc1\n\tglobal_load_lds_dwordx4 %0, %1 offset:1024 sc1\n\t"
                 "global_load_lds_dwordx4 %0, %1 offset:2048 sc1\n\tglobal_load_lds_dwordx4 %0, %1 offset:3072 sc1" :: "v"(l16), "s"(g), "s"(dst) : "memory");
}
__device__ __forceinline__ unsigned toff(int row, int col, int ncols) {
    return (unsigned)(row >> 4) * (unsigned)(ncols * 32) + (unsigned)(col >> 3) * 256u + (unsigned)(row & 15) * 16u + (unsigned)(col & 7) * 2u;
}

#define ASM_ST
#define ASM_ST_DECL
#define ASM_ST_OUT
#define ASM_ST_SAVE(k)
#define ASM_ST2
#define ASM_ST2_OUT
#define ASM_ST2_SAVE(k)
#define STDECL
#define ST(k)
#define STFLUSH(t)

constexpr unsigned L_MEM = 0;
constexpr unsigned L_Q = 10240;
constexpr unsigned L_KM = 13312;
constexpr unsigned L_QX = 16384;
constexpr unsigned L_KX = 28672;
constexpr unsigned L_XB = 40960;
constexpr unsigned L_XA = 49152;
constexpr unsigned L_W = 81920;
constexpr int MEGA_LDS = 163840;
constexpr int wi(int i, int j) { return (i >> 1) * 5 + ((i & 1) ? 2 + j : j - 1); }

#define RPUB2
#define ASM_XG_LO_A \
    "s_waitcnt lgkmcnt(0)\n\t" \
    "ds_read_b128 v[224:227], %[xa] offset:0\n\t" \
    "ds_read_b128 v[244:247], %[wl] offset:0\n\t" \
    "ds_read_b128 v[228:231], %[xa] offset:16384\n\t" \
    "ds_read_b128 v[232:235], %[xa] offset:1024\n\t" \
    "ds_read_b128 v[236:239], %[xa] offset:17408\n\t" \
    "ds_read_b128 v[248:251], %[wl] offset:1024\n\t" \
    "s_waitcnt lgkmcnt(5)\n\t" \
    "v_mfma_f32_16x16x32_bf16 v[204:207], v[0:3], v[224:227], 0\n\t" \
    "v_xor_b32 v252, 0x800, %[xa]\n\t" \
    "ds_read_b128 v[240:243], v252 offset:0\n\t" \
    "v_mfma_f32_16x16x32_bf16 v[208:211], v[8:11], v[224:227], 0\n\t" \
    "s_waitcnt lgkmcnt(5)\n\t" \
    "v_mfma_f32_16x16x32_bf16 v[200:203], v[244:247], v[224:227], 0\n\t" \
    "s_waitcnt lgkmcnt(4)\n\t" \
    "v_mfma_f32_16x16x32_bf16 v[216:219], v[0:3], v[228:231], 0\n\t" \
    "ds_read_b128 v[224:227], v252 offset:16384\n\t" \
    "v_mfma_f32_16x16x32_bf16 v[220:223], v[8:11], v[228:231], 0\n\t" \
    "v_mfma_f32_16x16x32_bf16 v[212:215], v[244:247], v[228:231], 0\n\t" \
    "s_waitcnt lgkmcnt(4)\n\t" \
    "v_mfma_f32_16x16x32_bf16 v[204:207], v[4:7], v[232:235], v[204:207]\n\t" \
    "ds_read_b128 v[228:231], v252 offset:1024\n\t" \
    "v_mfma_f32_16x16x32_bf16 v[208:211], v[12:15], v[232:235], v[208:211]\n\t" \
    "ds_read_b128 v[244:247], %[wl] offset:2048\n\t" \
    "s_waitcnt lgkmcnt(4)\n\t" \
    "v_mfma_f32_16x16x32_bf16 v[200:203], v[248:251], v[232:235], v[200:203]\n\t" \
    "v_mfma_f32_16x16x32_bf16 v[216:219], v[4:7], v[236:239], v[216:219]\n\t" \
    "ds_read_b128 v[232:235], v252 offset:17408\n\t" \
    "v_mfma_f32_16x16x32_bf16 v[220:223], v[12:15], v[236:239], v[220:223]\n\t" \
    "v_mfma_f32_16x16x32_bf16 v[212:215], v[248:251], v[236:239], v[212:215]\n\t" \
    "s_waitcnt lgkmcnt(4)\n\t" \
    "v_mfma_f32_16x16x32_bf16 v[204:207], v[24:27], v[240:243], v[204:207]\n\t" \
    "v_xor_b32 v252, 0x1000, %[xa]\n\t" \
    "ds_read_b128 v[236:239], v252 offset:0\n\t" \
    "v_mfma_f32_16x16x32_bf16 v[208:211], v[32:35], v[240:243], v[208:211]\n\t" \
    "ds_read_b128 v[248:251], %[wl] offset:3072\n\t" \
    "v_mfma_f32_16x16x32_bf16 v[200:203], v[16:19], v[240:243], v[200:203]\n\t" \
    "s_waitcnt lgkmcnt(5)\n\t" \
    "v_mfma_f32_16x16x32_bf16 v[216:219], v[24:27], v[224:227], v[216:219]\n\t" \
    "ds_read_b128 v[240:243], v252 offset:16384\n\t" \
    "v_mfma_f32_16x16x32_bf16 v[220:223], v[32:35], v[224:227], v[220:223]\n\t" \
    "v_mfma_f32_16x16x32_bf16 v[212:215], v[16:19], v[224:227], v[212:215]\n\t" \
    "s_waitcnt lgkmcnt(5)\n\t" \
    "v_mfma_f32_16x16x32_bf16 v[204:207], v[28:31], v[228:231], v[204:207]\n\t" \
    "ds_read_b128 v[224:227], v252 offset:1024\n\t" \
    "v_mfma_f32_16x16x32_bf16 v[208:211], v[36:39], v[228:231], v[208:211]\n\t" \
    "v_mfma_f32_16x16x32_bf16 v[200:203], v[20:23], v[228:231], v[200:203]\n\t" \
    "s_waitcnt lgkmcnt(4)\n\t" \
    "v_mfma_f32_16x16x32_bf16 v[216:219], v[28:31], v[232:235], v[216:219]\n\t" \
    "ds_read_b128 v[228:231], v252 offset:17408\n\t" \
    "v_mfma_f32_16x16x32_bf16 v[220:223], v[36:39], v[232:235], v[220:223]\n\t" \
    "v_mfma_f32_16x16x32_bf16 v[212:215], v[20:23], v[232:235], v[212:215]\n\t" \
    "s_waitcnt lgkmcnt(4)\n\t" \
    "v_mfma_f32_16x16x32_bf16 v[204:207], v[40:43], v[236:239], v[204:207]\n\t" \
    "v_xor_b32 v252, 0x1800, %[xa]\n\t" \
    "ds_read_b128 v[232:235], v252 offset:0\n\t" \
    "v_mfma_f32_16x16x32_bf16 v[208:211], v[48:51], v[236:239], v[208:211]\n\t" \
    "v_mfma_f32_16x16x32_bf16 v[200:203], v[244:247], v[236:239], v[200:203]\n\t" \
    "s_waitcnt lgkmcnt(3)\n\t" \
    "v_mfma_f32_16x16x32_bf16 v[216:219], v[40:43], v[240:243], v[216:219]\n\t" \
    "ds_read_b128 v[236:239], v252 offset:16384\n\t" \
    "v_mfma_f32_16x16x32_bf16 v[220:223], v[48:51], v[240:243], v[220:223]\n\t" \
    "v_mfma_f32_16x16x32_bf16 v[212:215], v[244:247], v[240:243], v[212:215]\n\t" \
    "s_waitcnt lgkmcnt(3)\n\t" \
    "v_mfma_f32_16x16x32_bf16 v[204:207], v[44:47], v[224:227], v[204:207]\n\t" \
    "ds_read_b128 v[240:243], v252 offset:1024\n\t" \
    "v_mfma_f32_16x16x32_bf16 v[208:211], v[52:55], v[224:227], v[208:211]\n\t" \
    "ds_read_b128 v[244:247], %[wl] offset:8192\n\t" \
    "v_mfma_f32_16x16x32_bf16 v[200:203], v[248:251], v[224:227], v[200:203]\n\t" \
    "s_waitcnt lgkmcnt(4)\n\t" \
    "v_mfma_f32_16x16x32_bf16 v[216:219], v[44:47], v[228:231], v[216:219]\n\t" \
    "ds_read_b128 v[224:227], v252 offset:17408\n\t" \
    "v_mfma_f32_16x16x32_bf16 v[220:223], v[52:55], v[228:231], v[220:223]\n\t" \
    "v_mfma_f32_16x16x32_bf16 v[212:215], v[248:251], v[228:231], v[212:215]\n\t" \
    "s_waitcnt lgkmcnt(4)\n\t" \
    "v_mfma_f32_16x16x32_bf16 v[204:207], v[64:67], v[232:235], v[204:207]\n\t" \
    "v_and_b32 v252, 0x3ff, %[xa]\n\t" \
    "ds_read_b128 v[228:231], v252 offset:40960\n\t" \
    "v_mfma_f32_16x16x32_bf16 v[208:211], v[72:75], v[232:235], v[208:211]\n\t" \
    "ds_read_b128 v[248:251], %[wl] offset:9216\n\t" \
    "v_mfma_f32_16x16x32_bf16 v[200:203], v[56:59], v[232:235], v[200:203]\n\t" \
    "s_waitcnt lgkmcnt(5)\n\t" \
    "v_mfma_f32_16x16x32_bf16 v[216:219], v[64:67], v[236:239], v[216:219]\n\t" \
    "ds_read_b128 v[232:235], v252 offset:45056\n\t" \
    "v_mfma_f32_16x16x32_bf16 v[220:223], v[72:75], v[236:239], v[220:223]\n\t" \
    "v_mfma_f32_16x16x32_bf16 v[212:215], v[56:59], v[236:239], v[212:215]\n\t" \
    "s_waitcnt lgkmcnt(5)\n\t" \
    "v_mfma_f32_16x16x32_bf16 v[204:207], v[68:71], v[240:243], v[204:207]\n\t" \
    "ds_read_b128 v[236:239], v252 offset:41984\n\t" \
    "v_mfma_f32_16x16x32_bf16 v[208:211], v[76:79], v[240:243], v[208:211]\n\t" \
    "v_mfma_f32_16x16x32_bf16 v[200:203], v[60:63], v[240:243], v[200:203]\n\t" \
    "s_waitcnt lgkmcnt(4)\n\t" \
    "v_mfma_f32_16x16x32_bf16 v[216:219], v[68:71], v[224:227], v[216:219]\n\t" \
    "ds_read_b128 v[240:243], v252 offset:46080\n\t" \
    "v_mfma_f32_16x16x32_bf16 v[220:223], v[76:79], v[224:227], v[220:223]\n\t" \
    "v_mfma_f32_16x16x32_bf16 v[212:215], v[60:63], v[224:227], v[212:215]\n\t" \
    "s_waitcnt lgkmcnt(4)\n\t" \
    "v_mfma_f32_16x16x32_bf16 v[204:207], v[160:163], v[228:231], v[204:207]\n\t" \
    "v_mfma_f32_16x16x32_bf16 v[208:211], v[168:171], v[228:231], v[208:211]\n\t" \
    "v_mfma_f32_16x16x32_bf16 v[200:203], v[244:247], v[228:231], v[200:203]\n\t" \
    "s_waitcnt lgkmcnt(2)\n\t" \
    "v_mfma_f32_16x16x32_bf16 v[216:219], v[160:163], v[232:235], v[216:219]\n\t" \
    "v_mfma_f32_16x16x32_bf16 v[220:223], v[168:171], v[232:235], v[220:223]\n\t" \
    "v_mfma_f32_16x16x32_bf16 v[212:215], v[244:247], v[232:235], v[212:215]\n\t" \
    "s_waitcnt lgkmcnt(1)\n\t" \
    "v_mfma_f32_16x16x32_bf16 v[204:207], v[164:167], v[236:239], v[204:207]\n\t" \
    "v_mfma_f32_16x16x32_bf16 v[208:211], v[172:175], v[236:239], v[208:211]\n\t" \
    "v_mfma_f32_16x16x32_bf16 v[200:203], v[248:251], v[236:239], v[200:203]\n\t" \
    "s_waitcnt lgkmcnt(0)\n\t" \
    "v_mfma_f32_16x16x32_bf16 v[216:219], v[164:167], v[240:243], v[216:219]\n\t" \
    "v_mfma_f32_16x16x32_bf16 v[220:223], v[172:175], v[240:243], v[220:223]\n\t" \
    "v_mfma_f32_16x16x32_bf16 v[212:215], v[248:251], v[240:243], v[212:215]\n\t" \
    "s_nop 7\n\t"
#define ASM_XG_LO_B \
    "s_waitcnt lgkmcnt(0)\n\t" \
    "v_xor_b32 v252, 0x2000, %[xa]\n\t" \
    "ds_read_b128 v[224:227], v252 offset:0\n\t" \
    "ds_read_b128 v[244:247], %[wl] offset:4096\n\t" \
    "ds_read_b128 v[228:231], v252 offset:16384\n\t" \
    "ds_read_b128 v[232:235], v252 offset:1024\n\t" \
    "ds_read_b128 v[236:239], v252 offset:17408\n\t" \
    "ds_read_b128 v[248:251], %[wl] offset:5120\n\t" \
    "s_waitcnt lgkmcnt(5)\n\t" \
    "v_mfma_f32_16x16x32_bf16 v[204:207], v[80:83], v[224:227], v[204:207]\n\t" \
    "v_xor_b32 v252, 0x2800, %[xa]\n\t" \
    "ds_read_b128 v[240:243], v252 offset:0\n\t" \
    "v_mfma_f32_16x16x32_bf16 v[208:211], v[88:91], v[224:227], v[208:211]\n\t" \
    "s_waitcnt lgkmcnt(5)\n\t" \
    "v_mfma_f32_16x16x32_bf16 v[200:203], v[244:247], v[224:227], v[200:203]\n\t" \
    "s_waitcnt lgkmcnt(4)\n\t" \
    "v_mfma_f32_16x16x32_bf16 v[216:219], v[80:83], v[228:231], v[216:219]\n\t" \
    "ds_read_b128 v[224:227], v252 offset:16384\n\t" \
    "v_mfma_f32_16x16x32_bf16 v[220:223], v[88:91], v[228:231], v[220:223]\n\t" \
    "v_mfma_f32_16x16x32_bf16 v[212:215], v[244:247], v[228:231], v[212:215]\n\t" \
    "s_waitcnt lgkmcnt(4)\n\t" \
    "v_mfma_f32_16x16x32_bf16 v[204:207], v[84:87], v[232:235], v[204:207]\n\t" \
    "ds_read_b128 v[228:231], v252 offset:1024\n\t" \
    "v_mfma_f32_16x16x32_bf16 v[208:211], v[92:95], v[232:235], v[208:211]\n\t" \
    "ds_read_b128 v[244:247], %[wl] offset:6144\n\t" \
    "s_waitcnt lgkmcnt(4)\n\t" \
    "v_mfma_f32_16x16x32_bf16 v[200:203], v[248:251], v[232:235], v[200:203]\n\t" \
    "v_mfma_f32_16x16x32_bf16 v[216:219], v[84:87], v[236:239], v[216:219]\n\t" \
    "ds_read_b128 v[232:235], v252 offset:17408\n\t" \
    "v_mfma_f32_16x16x32_bf16 v[220:223], v[92:95], v[236:239], v[220:223]\n\t" \
    "v_mfma_f32_16x16x32_bf16 v[212:215], v[248:251], v[236:239], v[212:215]\n\t" \
    "s_waitcnt lgkmcnt(4)\n\t" \
    "v_mfma_f32_16x16x32_bf16 v[204:207], v[104:107], v[240:243], v[204:207]\n\t" \
    "v_xor_b32 v252, 0x3000, %[xa]\n\t" \
    "ds_read_b128 v[236:239], v252 offset:0\n\t" \
    "v_mfma_f32_16x16x32_bf16 v[208:211], v[112:115], v[240:243], v[208:211]\n\t" \
    "ds_read_b128 v[248:251], %[wl] offset:7168\n\t" \
    "v_mfma_f32_16x16x32_bf16 v[200:203], v[96:99], v[240:243], v[200:203]\n\t" \
    "s_waitcnt lgkmcnt(5)\n\t" \
    "v_mfma_f32_16x16x32_bf16 v[216:219], v[104:107], v[224:227], v[216:219]\n\t" \
    "ds_read_b128 v[240:243], v252 offset:16384\n\t" \
    "v_mfma_f32_16x16x32_bf16 v[220:223], v[112:115], v[224:227], v[220:223]\n\t" \
    "v_mfma_f32_16x16x32_bf16 v[212:215], v[96:99], v[224:227], v[212:215]\n\t" \
    "s_waitcnt lgkmcnt(5)\n\t" \
    "v_mfma_f32_16x16x32_bf16 v[204:207], v[108:111], v[228:231], v[204:207]\n\t" \
    "ds_read_b128 v[224:227], v252 offset:1024\n\t" \
    "v_mfma_f32_16x16x32_bf16 v[208:211], v[116:119], v[228:231], v[208:211]\n\t" \
    "v_mfma_f32_16x16x32_bf16 v[200:203], v[100:103], v[228:231], v[200:203]\n\t" \
    "s_waitcnt lgkmcnt(4)\n\t" \
    "v_mfma_f32_16x16x32_bf16 v[216:219], v[108:111], v[232:235], v[216:219]\n\t" \
    "ds_read_b128 v[228:231], v252 offset:17408\n\t" \
    "v_mfma_f32_16x16x32_bf16 v[220:223], v[116:119], v[232:235], v[220:223]\n\t" \
    "v_mfma_f32_16x16x32_bf16 v[212:215], v[100:103], v[232:235], v[212:215]\n\t" \
    "s_waitcnt lgkmcnt(4)\n\t" \
    "v_mfma_f32_16x16x32_bf16 v[204:207], v[120:123], v[236:239], v[204:207]\n\t" \
    "v_xor_b32 v252, 0x3800, %[xa]\n\t" \
    "ds_read_b128 v[232:235], v252 offset:0\n\t" \
    "v_mfma_f32_16x16x32_bf16 v[208:211], v[128:131], v[236:239], v[208:211]\n\t" \
    "v_mfma_f32_16x16x32_bf16 v[200:203], v[244:247], v[236:239], v[200:203]\n\t" \
    "s_waitcnt lgkmcnt(3)\n\t" \
    "v_mfma_f32_16x16x32_bf16 v[216:219], v[120:123], v[240:243], v[216:219]\n\t" \
    "ds_read_b128 v[236:239], v252 offset:16384\n\t" \
    "v_mfma_f32_16x16x32_bf16 v[220:223], v[128:131], v[240:243], v[220:223]\n\t" \
    "v_mfma_f32_16x16x32_bf16 v[212:215], v[244:247], v[240:243], v[212:215]\n\t" \
    "s_waitcnt lgkmcnt(3)\n\t" \
    "v_mfma_f32_16x16x32_bf16 v[204:207], v[124:127], v[224:227], v[204:207]\n\t" \
    "ds_read_b128 v[240:243], v252 offset:1024\n\t" \
    "v_mfma_f32_16x16x32_bf16 v[208:211], v[132:135], v[224:227], v[208:211]\n\t" \
    "v_mfma_f32_16x16x32_bf16 v[200:203], v[248:251], v[224:227], v[200:203]\n\t" \
    "s_waitcnt lgkmcnt(3)\n\t" \
    "v_mfma_f32_16x16x32_bf16 v[216:219], v[124:127], v[228:231], v[216:219]\n\t" \
    "ds_read_b128 v[224:227], v252 offset:17408\n\t" \
    "v_mfma_f32_16x16x32_bf16 v[220:223], v[132:135], v[228:231], v[220:223]\n\t" \
    "v_mfma_f32_16x16x32_bf16 v[212:215], v[248:251], v[228:231], v[212:215]\n\t" \
    "s_waitcnt lgkmcnt(3)\n\t" \
    "v_mfma_f32_16x16x32_bf16 v[204:207], v[144:147], v[232:235], v[204:207]\n\t" \
    "v_and_b32 v252, 0x3ff, %[xa]\n\t" \
    "ds_read_b128 v[228:231], v252 offset:43008\n\t" \
    "v_mov_b64 v[244:245], 0\n\t" \
    "v_mfma_f32_16x16x32_bf16 v[208:211], v[152:155], v[232:235], v[208:211]\n\t" \
    "v_mov_b64 v[246:247], 0\n\t" \
    "v_mfma_f32_16x16x32_bf16 v[200:203], v[136:139], v[232:235], v[200:203]\n\t" \
    "v_mov_b64 v[248:249], 0\n\t" \
    "s_waitcnt lgkmcnt(3)\n\t" \
    "v_mfma_f32_16x16x32_bf16 v[216:219], v[144:147], v[236:239], v[216:219]\n\t" \
    "ds_read_b128 v[232:235], v252 offset:44032\n\t" \
    "v_mov_b64 v[250:251], 0\n\t" \
    "v_mfma_f32_16x16x32_bf16 v[220:223], v[152:155], v[236:239], v[220:223]\n\t" \
    "v_mfma_f32_16x16x32_bf16 v[212:215], v[136:139], v[236:239], v[212:215]\n\t" \
    "s_waitcnt lgkmcnt(3)\n\t" \
    "v_mfma_f32_16x16x32_bf16 v[204:207], v[148:151], v[240:243], v[204:207]\n\t" \
    "ds_read_b128 v[236:239], v252 offset:47104\n\t" \
    "v_mfma_f32_16x16x32_bf16 v[208:211], v[156:159], v[240:243], v[208:211]\n\t" \
    "v_mfma_f32_16x16x32_bf16 v[200:203], v[140:143], v[240:243], v[200:203]\n\t" \
    "s_waitcnt lgkmcnt(3)\n\t" \
    "v_mfma_f32_16x16x32_bf16 v[216:219], v[148:151], v[224:227], v[216:219]\n\t" \
    "ds_read_b128 v[240:243], v252 offset:48128\n\t" \
    "v_mfma_f32_16x16x32_bf16 v[220:223], v[156:159], v[224:227], v[220:223]\n\t" \
    "v_mfma_f32_16x16x32_bf16 v[212:215], v[140:143], v[224:227], v[212:215]\n\t" \
    "s_waitcnt lgkmcnt(3)\n\t" \
    "v_mfma_f32_16x16x32_bf16 v[204:207], v[184:187], v[228:231], v[204:207]\n\t" \
    "v_add_u32 v252, %[qs], v252\n\t" \
    "v_mfma_f32_16x16x32_bf16 v[208:211], v[192:195], v[228:231], v[208:211]\n\t" \
    "v_mov_b64 v[224:225], 0\n\t" \
    "v_mfma_f32_16x16x32_bf16 v[200:203], v[176:179], v[228:231], v[200:203]\n\t" \
    "v_mov_b64 v[226:227], 0\n\t" \
    "s_waitcnt lgkmcnt(2)\n\t" \
    "v_mfma_f32_16x16x32_bf16 v[204:207], v[188:191], v[232:235], v[204:207]\n\t" \
    "v_mfma_f32_16x16x32_bf16 v[208:211], v[196:199], v[232:235], v[208:211]\n\t" \
    "v_mfma_f32_16x16x32_bf16 v[200:203], v[180:183], v[232:235], v[200:203]\n\t" \
    "s_waitcnt lgkmcnt(1)\n\t" \
    "v_mfma_f32_16x16x32_bf16 v[216:219], v[184:187], v[236:239], v[216:219]\n\t" \
    "ds_read_b128 v[228:231], %[wl] offset:2048\n\t" \
    "ds_read_b128 v[232:235], %[wl] offset:3072\n\t" \
    "v_mfma_f32_16x16x32_bf16 v[220:223], v[192:195], v[236:239], v[220:223]\n\t" \
    "v_cvt_pk_bf16_f32 v204, v204, v205\n\t" \
    "v_cvt_pk_bf16_f32 v205, v206, v207\n\t" \
    "v_mfma_f32_16x16x32_bf16 v[212:215], v[176:179], v[236:239], v[212:215]\n\t" \
    "ds_write_b64 v252, v[204:205] offset:1024\n\t" \
    "v_cvt_pk_bf16_f32 v208, v208, v209\n\t" \
    "s_waitcnt lgkmcnt(3)\n\t" \
    "v_mfma_f32_16x16x32_bf16 v[216:219], v[188:191], v[240:243], v[216:219]\n\t" \
    "v_cvt_pk_bf16_f32 v209, v210, v211\n\t" \
    "ds_write_b64 v252, v[208:209] offset:2048\n\t" \
    "v_mfma_f32_16x16x32_bf16 v[220:223], v[196:199], v[240:243], v[220:223]\n\t" \
    "v_cvt_pk_bf16_f32 v200, v200, v201\n\t" \
    "v_cvt_pk_bf16_f32 v201, v202, v203\n\t" \
    "v_mfma_f32_16x16x32_bf16 v[212:215], v[180:183], v[240:243], v[212:215]\n\t" \
    "ds_write_b64 v252, v[200:201] offset:0\n\t" \
    "ds_read_b128 v[200:203], %[wl] offset:0\n\t" \
    "ds_read_b128 v[204:207], %[wl] offset:1024\n\t" \
    "v_cvt_pk_bf16_f32 v216, v216, v217\n\t" \
    "v_cvt_pk_bf16_f32 v217, v218, v219\n\t" \
    "ds_write_b64 v252, v[216:217] offset:7168\n\t" \
    "v_cvt_pk_bf16_f32 v220, v220, v221\n\t" \
    "v_cvt_pk_bf16_f32 v221, v222, v223\n\t" \
    "ds_write_b64 v252, v[220:221] offset:8192\n\t" \
    "v_cvt_pk_bf16_f32 v212, v212, v213\n\t" \
    "v_cvt_pk_bf16_f32 v213, v214, v215\n\t" \
    "ds_write_b64 v252, v[212:213] offset:6144\n\t" \
    "s_waitcnt lgkmcnt(0)\n\t"
#define ASM_XG_HI_A \
    "s_waitcnt lgkmcnt(0)\n\t" \
    "v_xor_b32 v252, 0x2000, %[xa]\n\t" \
    "ds_read_b128 v[224:227], v252 offset:0\n\t" \
    "ds_read_b128 v[244:247], %[wl] offset:4096\n\t" \
    "ds_read_b128 v[228:231], v252 offset:16384\n\t" \
    "ds_read_b128 v[232:235], v252 offset:1024\n\t" \
    "ds_read_b128 v[236:239], v252 offset:17408\n\t" \
    "ds_read_b128 v[248:251], %[wl] offset:5120\n\t" \
    "s_waitcnt lgkmcnt(5)\n\t" \
    "v_mfma_f32_16x16x32_bf16 v[204:207], v[80:83], v[224:227], 0\n\t" \
    "v_xor_b32 v252, 0x2800, %[xa]\n\t" \
    "ds_read_b128 v[240:243], v252 offset:0\n\t" \
    "v_mfma_f32_16x16x32_bf16 v[208:211], v[88:91], v[224:227], 0\n\t" \
    "s_waitcnt lgkmcnt(5)\n\t" \
    "v_mfma_f32_16x16x32_bf16 v[200:203], v[244:247], v[224:227], 0\n\t" \
    "s_waitcnt lgkmcnt(4)\n\t" \
    "v_mfma_f32_16x16x32_bf16 v[216:219], v[80:83], v[228:231], 0\n\t" \
    "ds_read_b128 v[224:227], v252 offset:16384\n\t" \
    "v_mfma_f32_16x16x32_bf16 v[220:223], v[88:91], v[228:231], 0\n\t" \
    "v_mfma_f32_16x16x32_bf16 v[212:215], v[244:247], v[228:231], 0\n\t" \
    "s_waitcnt lgkmcnt(4)\n\t" \
    "v_mfma_f32_16x16x32_bf16 v[204:207], v[84:87], v[232:235], v[204:207]\n\t" \
    "ds_read_b128 v[228:231], v252 offset:1024\n\t" \
    "v_mfma_f32_16x16x32_bf16 v[208:211], v[92:95], v[232:235], v[208:211]\n\t" \
    "ds_read_b128 v[244:247], %[wl] offset:6144\n\t" \
    "s_waitcnt lgkmcnt(4)\n\t" \
    "v_mfma_f32_16x16x32_bf16 v[200:203], v[248:251], v[232:235], v[200:203]\n\t" \
    "v_mfma_f32_16x16x32_bf16 v[216:219], v[84:87], v[236:239], v[216:219]\n\t" \
    "ds_read_b128 v[232:235], v252 offset:17408\n\t" \
    "v_mfma_f32_16x16x32_bf16 v[220:223], v[92:95], v[236:239], v[220:223]\n\t" \
    "v_mfma_f32_16x16x32_bf16 v[212:215], v[248:251], v[236:239], v[212:215]\n\t" \
    "s_waitcnt lgkmcnt(4)\n\t" \
    "v_mfma_f32_16x16x32_bf16 v[204:207], v[104:107], v[240:243], v[204:207]\n\t" \
    "v_xor_b32 v252, 0x3000, %[xa]\n\t" \
    "ds_read_b128 v[236:239], v252 offset:0\n\t" \
    "v_mfma_f32_16x16x32_bf16 v[208:211], v[112:115], v[240:243], v[208:211]\n\t" \
    "ds_read_b128 v[248:251], %[wl] offset:7168\n\t" \
    "v_mfma_f32_16x16x32_bf16 v[200:203], v[96:99], v[240:243], v[200:203]\n\t" \
    "s_waitcnt lgkmcnt(5)\n\t" \
    "v_mfma_f32_16x16x32_bf16 v[216:219], v[104:107], v[224:227], v[216:219]\n\t" \
    "ds_read_b128 v[240:243], v252 offset:16384\n\t" \
    "v_mfma_f32_16x16x32_bf16 v[220:223], v[112:115], v[224:227], v[220:223]\n\t" \
    "v_mfma_f32_16x16x32_bf16 v[212:215], v[96:99], v[224:227], v[212:215]\n\t" \
    "s_waitcnt lgkmcnt(5)\n\t" \
    "v_mfma_f32_16x16x32_bf16 v[204:207], v[108:111], v[228:231], v[204:207]\n\t" \
    "ds_read_b128 v[224:227], v252 offset:1024\n\t" \
    "v_mfma_f32_16x16x32_bf16 v[208:211], v[116:119], v[228:231], v[208:211]\n\t" \
    "v_mfma_f32_16x16x32_bf16 v[200:203], v[100:103], v[228:231], v[200:203]\n\t" \
    "s_waitcnt lgkmcnt(4)\n\t" \
    "v_mfma_f32_16x16x32_bf16 v[216:219], v[108:111], v[232:235], v[216:219]\n\t" \
    "ds_read_b128 v[228:231], v252 offset:17408\n\t" \
    "v_mfma_f32_16x16x32_bf16 v[220:223], v[116:119], v[232:235], v[220:223]\n\t" \
    "v_mfma_f32_16x16x32_bf16 v[212:215], v[100:103], v[232:235], v[212:215]\n\t" \
    "s_waitcnt lgkmcnt(4)\n\t" \
    "v_mfma_f32_16x16x32_bf16 v[204:207], v[120:123], v[236:239], v[204:207]\n\t" \
    "v_xor_b32 v252, 0x3800, %[xa]\n\t" \
    "ds_read_b128 v[232:235], v252 offset:0\n\t" \
    "v_mfma_f32_16x16x32_bf16 v[208:211], v[128:131], v[236:239], v[208:211]\n\t" \
    "v_mfma_f32_16x16x32_bf16 v[200:203], v[244:247], v[236:239], v[200:203]\n\t" \
    "s_waitcnt lgkmcnt(3)\n\t" \
    "v_mfma_f32_16x16x32_bf16 v[216:219], v[120:123], v[240:243], v[216:219]\n\t" \
    "ds_read_b128 v[236:239], v252 offset:16384\n\t" \
    "v_mfma_f32_16x16x32_bf16 v[220:223], v[128:131], v[240:243], v[220:223]\n\t" \
    "v_mfma_f32_16x16x32_bf16 v[212:215], v[244:247], v[240:243], v[212:215]\n\t" \
    "s_waitcnt lgkmcnt(3)\n\t" \
    "v_mfma_f32_16x16x32_bf16 v[204:207], v[124:127], v[224:227], v[204:207]\n\t" \
    "ds_read_b128 v[240:243], v252 offset:1024\n\t" \
    "v_mfma_f32_16x16x32_bf16 v[208:211], v[132:135], v[224:227], v[208:211]\n\t" \
    "ds_read_b128 v[244:247], %[wl] offset:8192\n\t" \
    "v_mfma_f32_16x16x32_bf16 v[200:203], v[248:251], v[224:227], v[200:203]\n\t" \
    "s_waitcnt lgkmcnt(4)\n\t" \
    "v_mfma_f32_16x16x32_bf16 v[216:219], v[124:127], v[228:231], v[216:219]\n\t" \
    "ds_read_b128 v[224:227], v252 offset:17408\n\t" \
    "v_mfma_f32_16x16x32_bf16 v[220:223], v[132:135], v[228:231], v[220:223]\n\t" \
    "v_mfma_f32_16x16x32_bf16 v[212:215], v[248:251], v[228:231], v[212:215]\n\t" \
    "s_waitcnt lgkmcnt(4)\n\t" \
    "v_mfma_f32_16x16x32_bf16 v[204:207], v[144:147], v[232:235], v[204:207]\n\t" \
    "v_and_b32 v252, 0x3ff, %[xa]\n\t" \
    "ds_read_b128 v[228:231], v252 offset:40960\n\t" \
    "v_mfma_f32_16x16x32_bf16 v[208:211], v[152:155], v[232:235], v[208:211]\n\t" \
    "ds_read_b128 v[248:251], %[wl] offset:9216\n\t" \
    "v_mfma_f32_16x16x32_bf16 v[200:203], v[136:139], v[232:235], v[200:203]\n\t" \
    "s_waitcnt lgkmcnt(5)\n\t" \
    "v_mfma_f32_16x16x32_bf16 v[216:219], v[144:147], v[236:239], v[216:219]\n\t" \
    "ds_read_b128 v[232:235], v252 offset:45056\n\t" \
    "v_mfma_f32_16x16x32_bf16 v[220:223], v[152:155], v[236:239], v[220:223]\n\t" \
    "v_mfma_f32_16x16x32_bf16 v[212:215], v[136:139], v[236:239], v[212:215]\n\t" \
    "s_waitcnt lgkmcnt(5)\n\t" \
    "v_mfma_f32_16x16x32_bf16 v[204:207], v[148:151], v[240:243], v[204:207]\n\t" \
    "ds_read_b128 v[236:239], v252 offset:41984\n\t" \
    "v_mfma_f32_16x16x32_bf16 v[208:211], v[156:159], v[240:243], v[208:211]\n\t" \
    "v_mfma_f32_16x16x32_bf16 v[200:203], v[140:143], v[240:243], v[200:203]\n\t" \
    "s_waitcnt lgkmcnt(4)\n\t" \
    "v_mfma_f32_16x16x32_bf16 v[216:219], v[148:151], v[224:227], v[216:219]\n\t" \
    "ds_read_b128 v[240:243], v252 offset:46080\n\t" \
    "v_mfma_f32_16x16x32_bf16 v[220:223], v[156:159], v[224:227], v[220:223]\n\t" \
    "v_mfma_f32_16x16x32_bf16 v[212:215], v[140:143], v[224:227], v[212:215]\n\t" \
    "s_waitcnt lgkmcnt(4)\n\t" \
    "v_mfma_f32_16x16x32_bf16 v[204:207], v[160:163], v[228:231], v[204:207]\n\t" \
    "v_mfma_f32_16x16x32_bf16 v[208:211], v[168:171], v[228:231], v[208:211]\n\t" \
    "v_mfma_f32_16x16x32_bf16 v[200:203], v[244:247], v[228:231], v[200:203]\n\t" \
    "s_waitcnt lgkmcnt(2)\n\t" \
    "v_mfma_f32_16x16x32_bf16 v[216:219], v[160:163], v[232:235], v[216:219]\n\t" \
    "v_mfma_f32_16x16x32_bf16 v[220:223], v[168:171], v[232:235], v[220:223]\n\t" \
    "v_mfma_f32_16x16x32_bf16 v[212:215], v[244:247], v[232:235], v[212:215]\n\t" \
    "s_waitcnt lgkmcnt(1)\n\t" \
    "v_mfma_f32_16x16x32_bf16 v[204:207], v[164:167], v[236:239], v[204:207]\n\t" \
    "v_mfma_f32_16x16x32_bf16 v[208:211], v[172:175], v[236:239], v[208:211]\n\t" \
    "v_mfma_f32_16x16x32_bf16 v[200:203], v[248:251], v[236:239], v[200:203]\n\t" \
    "s_waitcnt lgkmcnt(0)\n\t" \
    "v_mfma_f32_16x16x32_bf16 v[216:219], v[164:167], v[240:243], v[216:219]\n\t" \
    "v_mfma_f32_16x16x32_bf16 v[220:223], v[172:175], v[240:243], v[220:223]\n\t" \
    "v_mfma_f32_16x16x32_bf16 v[212:215], v[248:251], v[240:243], v[212:215]\n\t" \
    "s_nop 7\n\t"
#define ASM_XG_HI_B \
    "s_waitcnt lgkmcnt(0)\n\t" \
    "ds_read_b128 v[224:227], %[xa] offset:0\n\t" \
    "ds_read_b128 v[244:247], %[wl] offset:0\n\t" \
    "ds_read_b128 v[228:231], %[xa] offset:16384\n\t" \
    "ds_read_b128 v[232:235], %[xa] offset:1024\n\t" \
    "ds_read_b128 v[236:239], %[xa] offset:17408\n\t" \
    "ds_read_b128 v[248:251], %[wl] offset:1024\n\t" \
    "s_waitcnt lgkmcnt(5)\n\t" \
    "v_mfma_f32_16x16x32_bf16 v[204:207], v[0:3], v[224:227], v[204:207]\n\t" \
    "v_xor_b32 v252, 0x800, %[xa]\n\t" \
    "ds_read_b128 v[240:243], v252 offset:0\n\t" \
    "v_mfma_f32_16x16x32_bf16 v[208:211], v[8:11], v[224:227], v[208:211]\n\t" \
    "s_waitcnt lgkmcnt(5)\n\t" \
    "v_mfma_f32_16x16x32_bf16 v[200:203], v[244:247], v[224:227], v[200:203]\n\t" \
    "s_waitcnt lgkmcnt(4)\n\t" \
    "v_mfma_f32_16x16x32_bf16 v[216:219], v[0:3], v[228:231], v[216:219]\n\t" \
    "ds_read_b128 v[224:227], v252 offset:16384\n\t" \
    "v_mfma_f32_16x16x32_bf16 v[220:223], v[8:11], v[228:231], v[220:223]\n\t" \
    "v_mfma_f32_16x16x32_bf16 v[212:215], v[244:247], v[228:231], v[212:215]\n\t" \
    "s_waitcnt lgkmcnt(4)\n\t" \
    "v_mfma_f32_16x16x32_bf16 v[204:207], v[4:7], v[232:235], v[204:207]\n\t" \
    "ds_read_b128 v[228:231], v252 offset:1024\n\t" \
    "v_mfma_f32_16x16x32_bf16 v[208:211], v[12:15], v[232:235], v[208:211]\n\t" \
    "ds_read_b128 v[244:247], %[wl] offset:2048\n\t" \
    "s_waitcnt lgkmcnt(4)\n\t" \
    "v_mfma_f32_16x16x32_bf16 v[200:203], v[248:251], v[232:235], v[200:203]\n\t" \
    "v_mfma_f32_16x16x32_bf16 v[216:219], v[4:7], v[236:239], v[216:219]\n\t" \
    "ds_read_b128 v[232:235], v252 offset:17408\n\t" \
    "v_mfma_f32_16x16x32_bf16 v[220:223], v[12:15], v[236:239], v[220:223]\n\t" \
    "v_mfma_f32_16x16x32_bf16 v[212:215], v[248:251], v[236:239], v[212:215]\n\t" \
    "s_waitcnt lgkmcnt(4)\n\t" \
    "v_mfma_f32_16x16x32_bf16 v[204:207], v[24:27], v[240:243], v[204:207]\n\t" \
    "v_xor_b32 v252, 0x1000, %[xa]\n\t" \
    "ds_read_b128 v[236:239], v252 offset:0\n\t" \
    "v_mfma_f32_16x16x32_bf16 v[208:211], v[32:35], v[240:243], v[208:211]\n\t" \
    "ds_read_b128 v[248:251], %[wl] offset:3072\n\t" \
    "v_mfma_f32_16x16x32_bf16 v[200:203], v[16:19], v[240:243], v[200:203]\n\t" \
    "s_waitcnt lgkmcnt(5)\n\t" \
    "v_mfma_f32_16x16x32_bf16 v[216:219], v[24:27], v[224:227], v[216:219]\n\t" \
    "ds_read_b128 v[240:243], v252 offset:16384\n\t" \
    "v_mfma_f32_16x16x32_bf16 v[220:223], v[32:35], v[224:227], v[220:223]\n\t" \
    "v_mfma_f32_16x16x32_bf16 v[212:215], v[16:19], v[224:227], v[212:215]\n\t" \
    "s_waitcnt lgkmcnt(5)\n\t" \
    "v_mfma_f32_16x16x32_bf16 v[204:207], v[28:31], v[228:231], v[204:207]\n\t" \
    "ds_read_b128 v[224:227], v252 offset:1024\n\t" \
    "v_mfma_f32_16x16x32_bf16 v[208:211], v[36:39], v[228:231], v[208:211]\n\t" \
    "v_mfma_f32_16x16x32_bf16 v[200:203], v[20:23], v[228:231], v[200:203]\n\t" \
    "s_waitcnt lgkmcnt(4)\n\t" \
    "v_mfma_f32_16x16x32_bf16 v[216:219], v[28:31], v[232:235], v[216:219]\n\t" \
    "ds_read_b128 v[228:231], v252 offset:17408\n\t" \
    "v_mfma_f32_16x16x32_bf16 v[220:223], v[36:39], v[232:235], v[220:223]\n\t" \
    "v_mfma_f32_16x16x32_bf16 v[212:215], v[20:23], v[232:235], v[212:215]\n\t" \
    "s_waitcnt lgkmcnt(4)\n\t" \
    "v_mfma_f32_16x16x32_bf16 v[204:207], v[40:43], v[236:239], v[204:207]\n\t" \
    "v_xor_b32 v252, 0x1800, %[xa]\n\t" \
    "ds_read_b128 v[232:235], v252 offset:0\n\t" \
    "v_mfma_f32_16x16x32_bf16 v[208:211], v[48:51], v[236:239], v[208:211]\n\t" \
    "v_mfma_f32_16x16x32_bf16 v[200:203], v[244:247], v[236:239], v[200:203]\n\t" \
    "s_waitcnt lgkmcnt(3)\n\t" \
    "v_mfma_f32_16x16x32_bf16 v[216:219], v[40:43], v[240:243], v[216:219]\n\t" \
    "ds_read_b128 v[236:239], v252 offset:16384\n\t" \
    "v_mfma_f32_16x16x32_bf16 v[220:223], v[48:51], v[240:243], v[220:223]\n\t" \
    "v_mfma_f32_16x16x32_bf16 v[212:215], v[244:247], v[240:243], v[212:215]\n\t" \
    "s_waitcnt lgkmcnt(3)\n\t" \
    "v_mfma_f32_16x16x32_bf16 v[204:207], v[44:47], v[224:227], v[204:207]\n\t" \
    "ds_read_b128 v[240:243], v252 offset:1024\n\t" \
    "v_mfma_f32_16x16x32_bf16 v[208:211], v[52:55], v[224:227], v[208:211]\n\t" \
    "v_mfma_f32_16x16x32_bf16 v[200:203], v[248:251], v[224:227], v[200:203]\n\t" \
    "s_waitcnt lgkmcnt(3)\n\t" \
    "v_mfma_f32_16x16x32_bf16 v[216:219], v[44:47], v[228:231], v[216:219]\n\t" \
    "ds_read_b128 v[224:227], v252 offset:17408\n\t" \
    "v_mfma_f32_16x16x32_bf16 v[220:223], v[52:55], v[228:231], v[220:223]\n\t" \
    "v_mfma_f32_16x16x32_bf16 v[212:215], v[248:251], v[228:231], v[212:215]\n\t" \
    "s_waitcnt lgkmcnt(3)\n\t" \
    "v_mfma_f32_16x16x32_bf16 v[204:207], v[64:67], v[232:235], v[204:207]\n\t" \
    "v_and_b32 v252, 0x3ff, %[xa]\n\t" \
    "ds_read_b128 v[228:231], v252 offset:43008\n\t" \
    "v_mov_b64 v[244:245], 0\n\t" \
    "v_mfma_f32_16x16x32_bf16 v[208:211], v[72:75], v[232:235], v[208:211]\n\t" \
    "v_mov_b64 v[246:247], 0\n\t" \
    "v_mfma_f32_16x16x32_bf16 v[200:203], v[56:59], v[232:235], v[200:203]\n\t" \
    "v_mov_b64 v[248:249], 0\n\t" \
    "s_waitcnt lgkmcnt(3)\n\t" \
    "v_mfma_f32_16x16x32_bf16 v[216:219], v[64:67], v[236:239], v[216:219]\n\t" \
    "ds_read_b128 v[232:235], v252 offset:44032\n\t" \
    "v_mov_b64 v[250:251], 0\n\t" \
    "v_mfma_f32_16x16x32_bf16 v[220:223], v[72:75], v[236:239], v[220:223]\n\t" \
    "v_mfma_f32_16x16x32_bf16 v[212:215], v[56:59], v[236:239], v[212:215]\n\t" \
    "s_waitcnt lgkmcnt(3)\n\t" \
    "v_mfma_f32_16x16x32_bf16 v[204:207], v[68:71], v[240:243], v[204:207]\n\t" \
    "ds_read_b128 v[236:239], v252 offset:47104\n\t" \
    "v_mfma_f32_16x16x32_bf16 v[208:211], v[76:79], v[240:243], v[208:211]\n\t" \
    "v_mfma_f32_16x16x32_bf16 v[200:203], v[60:63], v[240:243], v[200:203]\n\t" \
    "s_waitcnt lgkmcnt(3)\n\t" \
    "v_mfma_f32_16x16x32_bf16 v[216:219], v[68:71], v[224:227], v[216:219]\n\t" \
    "ds_read_b128 v[240:243], v252 offset:48128\n\t" \
    "v_mfma_f32_16x16x32_bf16 v[220:223], v[76:79], v[224:227], v[220:223]\n\t" \
    "v_mfma_f32_16x16x32_bf16 v[212:215], v[60:63], v[224:227], v[212:215]\n\t" \
    "s_waitcnt lgkmcnt(3)\n\t" \
    "v_mfma_f32_16x16x32_bf16 v[204:207], v[184:187], v[228:231], v[204:207]\n\t" \
    "v_add_u32 v252, %[qs], v252\n\t" \
    "v_mfma_f32_16x16x32_bf16 v[208:211], v[192:195], v[228:231], v[208:211]\n\t" \
    "v_mov_b64 v[224:225], 0\n\t" \
    "v_mfma_f32_16x16x32_bf16 v[200:203], v[176:179], v[228:231], v[200:203]\n\t" \
    "v_mov_b64 v[226:227], 0\n\t" \
    "s_waitcnt lgkmcnt(2)\n\t" \
    "v_mfma_f32_16x16x32_bf16 v[204:207], v[188:191], v[232:235], v[204:207]\n\t" \
    "v_mfma_f32_16x16x32_bf16 v[208:211], v[196:199], v[232:235], v[208:211]\n\t" \
    "v_mfma_f32_16x16x32_bf16 v[200:203], v[180:183], v[232:235], v[200:203]\n\t" \
    "s_waitcnt lgkmcnt(1)\n\t" \
    "v_mfma_f32_16x16x32_bf16 v[216:219], v[184:187], v[236:239], v[216:219]\n\t" \
    "ds_read_b128 v[228:231], %[wl] offset:2048\n\t" \
    "ds_read_b128 v[232:235], %[wl] offset:3072\n\t" \
    "v_mfma_f32_16x16x32_bf16 v[220:223], v[192:195], v[236:239], v[220:223]\n\t" \
    "v_cvt_pk_bf16_f32 v204, v204, v205\n\t" \
    "v_cvt_pk_bf16_f32 v205, v206, v207\n\t" \
    "v_mfma_f32_16x16x32_bf16 v[212:215], v[176:179], v[236:239], v[212:215]\n\t" \
    "ds_write_b64 v252, v[204:205] offset:1024\n\t" \
    "v_cvt_pk_bf16_f32 v208, v208, v209\n\t" \
    "s_waitcnt lgkmcnt(3)\n\t" \
    "v_mfma_f32_16x16x32_bf16 v[216:219], v[188:191], v[240:243], v[216:219]\n\t" \
    "v_cvt_pk_bf16_f32 v209, v210, v211\n\t" \
    "ds_write_b64 v252, v[208:209] offset:2048\n\t" \
    "v_mfma_f32_16x16x32_bf16 v[220:223], v[196:199], v[240:243], v[220:223]\n\t" \
    "v_cvt_pk_bf16_f32 v200, v200, v201\n\t" \
    "v_cvt_pk_bf16_f32 v201, v202, v203\n\t" \
    "v_mfma_f32_16x16x32_bf16 v[212:215], v[180:183], v[240:243], v[212:215]\n\t" \
    "ds_write_b64 v252, v[200:201] offset:0\n\t" \
    "ds_read_b128 v[200:203], %[wl] offset:0\n\t" \
    "ds_read_b128 v[204:207], %[wl] offset:1024\n\t" \
    "v_cvt_pk_bf16_f32 v216, v216, v217\n\t" \
    "v_cvt_pk_bf16_f32 v217, v218, v219\n\t" \
    "ds_write_b64 v252, v[216:217] offset:7168\n\t" \
    "v_cvt_pk_bf16_f32 v220, v220, v221\n\t" \
    "v_cvt_pk_bf16_f32 v221, v222, v223\n\t" \
    "ds_write_b64 v252, v[220:221] offset:8192\n\t" \
    "v_cvt_pk_bf16_f32 v212, v212, v213\n\t" \
    "v_cvt_pk_bf16_f32 v213, v214, v215\n\t" \
    "ds_write_b64 v252, v[212:213] offset:6144\n\t" \
    "s_waitcnt lgkmcnt(0)\n\t"
#define ASM_XG_LO_AB \
    "s_waitcnt lgkmcnt(0)\n\t" \
    "ds_read_b128 v[244:247], %[wl] offset:0\n\t" \
    "ds_read_b128 v[248:251], %[wl] offset:1024\n\t" \
    "s_barrier \n\t" \
    "ds_read_b128 v[224:227], %[xa] offset:0\n\t" \
    "ds_read_b128 v[228:231], %[xa] offset:16384\n\t" \
    "ds_read_b128 v[232:235], %[xa] offset:1024\n\t" \
    "ds_read_b128 v[236:239], %[xa] offset:17408\n\t" \
    "s_waitcnt lgkmcnt(3)\n\t" \
    "v_mfma_f32_16x16x32_bf16 v[204:207], v[0:3], v[224:227], 0\n\t" \
    "v_xor_b32 v252, 0x800, %[xa]\n\t" \
    "ds_read_b128 v[240:243], v252 offset:0\n\t" \
    "v_mfma_f32_16x16x32_bf16 v[208:211], v[8:11], v[224:227], 0\n\t" \
    "v_mfma_f32_16x16x32_bf16 v[200:203], v[244:247], v[224:227], 0\n\t" \
    "s_waitcnt lgkmcnt(3)\n\t" \
    "v_mfma_f32_16x16x32_bf16 v[216:219], v[0:3], v[228:231], 0\n\t" \
    "ds_read_b128 v[224:227], v252 offset:16384\n\t" \
    "v_mfma_f32_16x16x32_bf16 v[220:223], v[8:11], v[228:231], 0\n\t" \
    "v_mfma_f32_16x16x32_bf16 v[212:215], v[244:247], v[228:231], 0\n\t" \
    "s_waitcnt lgkmcnt(3)\n\t" \
    "v_mfma_f32_16x16x32_bf16 v[204:207], v[4:7], v[232:235], v[204:207]\n\t" \
    "ds_read_b128 v[228:231], v252 offset:1024\n\t" \
    "v_mfma_f32_16x16x32_bf16 v[208:211], v[12:15], v[232:235], v[208:211]\n\t" \
    "ds_read_b128 v[244:247], %[wl] offset:2048\n\t" \
    "v_mfma_f32_16x16x32_bf16 v[200:203], v[248:251], v[232:235], v[200:203]\n\t" \
    "s_waitcnt lgkmcnt(4)\n\t" \
    "v_mfma_f32_16x16x32_bf16 v[216:219], v[4:7], v[236:239], v[216:219]\n\t" \
    "ds_read_b128 v[232:235], v252 offset:17408\n\t" \
    "v_mfma_f32_16x16x32_bf16 v[220:223], v[12:15], v[236:239], v[220:223]\n\t" \
    "v_mfma_f32_16x16x32_bf16 v[212:215], v[248:251], v[236:239], v[212:215]\n\t" \
    "s_waitcnt lgkmcnt(4)\n\t" \
    "v_mfma_f32_16x16x32_bf16 v[204:207], v[24:27], v[240:243], v[204:207]\n\t" \
    "v_xor_b32 v252, 0x1000, %[xa]\n\t" \
    "ds_read_b128 v[236:239], v252 offset:0\n\t" \
    "v_mfma_f32_16x16x32_bf16 v[208:211], v[32:35], v[240:243], v[208:211]\n\t" \
    "ds_read_b128 v[248:251], %[wl] offset:3072\n\t" \
    "v_mfma_f32_16x16x32_bf16 v[200:203], v[16:19], v[240:243], v[200:203]\n\t" \
    "s_waitcnt lgkmcnt(5)\n\t" \
    "v_mfma_f32_16x16x32_bf16 v[216:219], v[24:27], v[224:227], v[216:219]\n\t" \
    "ds_read_b128 v[240:243], v252 offset:16384\n\t" \
    "v_mfma_f32_16x16x32_bf16 v[220:223], v[32:35], v[224:227], v[220:223]\n\t" \
    "v_mfma_f32_16x16x32_bf16 v[212:215], v[16:19], v[224:227], v[212:215]\n\t" \
    "s_waitcnt lgkmcnt(5)\n\t" \
    "v_mfma_f32_16x16x32_bf16 v[204:207], v[28:31], v[228:231], v[204:207]\n\t" \
    "ds_read_b128 v[224:227], v252 offset:1024\n\t" \
    "v_mfma_f32_16x16x32_bf16 v[208:211], v[36:39], v[228:231], v[208:211]\n\t" \
    "v_mfma_f32_16x16x32_bf16 v[200:203], v[20:23], v[228:231], v[200:203]\n\t" \
    "s_waitcnt lgkmcnt(4)\n\t" \
    "v_mfma_f32_16x16x32_bf16 v[216:219], v[28:31], v[232:235], v[216:219]\n\t" \
    "ds_read_b128 v[228:231], v252 offset:17408\n\t" \
    "v_mfma_f32_16x16x32_bf16 v[220:223], v[36:39], v[232:235], v[220:223]\n\t" \
    "v_mfma_f32_16x16x32_bf16 v[212:215], v[20:23], v[232:235], v[212:215]\n\t" \
    "s_waitcnt lgkmcnt(4)\n\t" \
    "v_mfma_f32_16x16x32_bf16 v[204:207], v[40:43], v[236:239], v[204:207]\n\t" \
    "v_xor_b32 v252, 0x1800, %[xa]\n\t" \
    "ds_read_b128 v[232:235], v252 offset:0\n\t" \
    "v_mfma_f32_16x16x32_bf16 v[208:211], v[48:51], v[236:239], v[208:211]\n\t" \
    "v_mfma_f32_16x16x32_bf16 v[200:203], v[244:247], v[236:239], v[200:203]\n\t" \
    "s_waitcnt lgkmcnt(3)\n\t" \
    "v_mfma_f32_16x16x32_bf16 v[216:219], v[40:43], v[240:243], v[216:219]\n\t" \
    "ds_read_b128 v[236:239], v252 offset:16384\n\t" \
    "v_mfma_f32_16x16x32_bf16 v[220:223], v[48:51], v[240:243], v[220:223]\n\t" \
    "v_mfma_f32_16x16x32_bf16 v[212:215], v[244:247], v[240:243], v[212:215]\n\t" \
    "s_waitcnt lgkmcnt(3)\n\t" \
    "v_mfma_f32_16x16x32_bf16 v[204:207], v[44:47], v[224:227], v[204:207]\n\t" \
    "ds_read_b128 v[240:243], v252 offset:1024\n\t" \
    "v_mfma_f32_16x16x32_bf16 v[208:211], v[52:55], v[224:227], v[208:211]\n\t" \
    "ds_read_b128 v[244:247], %[wl] offset:8192\n\t" \
    "v_mfma_f32_16x16x32_bf16 v[200:203], v[248:251], v[224:227], v[200:203]\n\t" \
    "s_waitcnt lgkmcnt(4)\n\t" \
    "v_mfma_f32_16x16x32_bf16 v[216:219], v[44:47], v[228:231], v[216:219]\n\t" \
    "ds_read_b128 v[224:227], v252 offset:17408\n\t" \
    "v_mfma_f32_16x16x32_bf16 v[220:223], v[52:55], v[228:231], v[220:223]\n\t" \
    "v_mfma_f32_16x16x32_bf16 v[212:215], v[248:251], v[228:231], v[212:215]\n\t" \
    "s_waitcnt lgkmcnt(4)\n\t" \
    "v_mfma_f32_16x16x32_bf16 v[204:207], v[64:67], v[232:235], v[204:207]\n\t" \
    "v_and_b32 v252, 0x3ff, %[xa]\n\t" \
    "ds_read_b128 v[228:231], v252 offset:40960\n\t" \
    "v_mfma_f32_16x16x32_bf16 v[208:211], v[72:75], v[232:235], v[208:211]\n\t" \
    "ds_read_b128 v[248:251], %[wl] offset:9216\n\t" \
    "v_mfma_f32_16x16x32_bf16 v[200:203], v[56:59], v[232:235], v[200:203]\n\t" \
    "s_waitcnt lgkmcnt(5)\n\t" \
    "v_mfma_f32_16x16x32_bf16 v[216:219], v[64:67], v[236:239], v[216:219]\n\t" \
    "ds_read_b128 v[232:235], v252 offset:45056\n\t" \
    "v_mfma_f32_16x16x32_bf16 v[220:223], v[72:75], v[236:239], v[220:223]\n\t" \
    "v_mfma_f32_16x16x32_bf16 v[212:215], v[56:59], v[236:239], v[212:215]\n\t" \
    "s_waitcnt lgkmcnt(5)\n\t" \
    "v_mfma_f32_16x16x32_bf16 v[204:207], v[68:71], v[240:243], v[204:207]\n\t" \
    "ds_read_b128 v[236:239], v252 offset:41984\n\t" \
    "v_mfma_f32_16x16x32_bf16 v[208:211], v[76:79], v[240:243], v[208:211]\n\t" \
    "v_mfma_f32_16x16x32_bf16 v[200:203], v[60:63], v[240:243], v[200:203]\n\t" \
    "s_waitcnt lgkmcnt(4)\n\t" \
    "v_mfma_f32_16x16x32_bf16 v[216:219], v[68:71], v[224:227], v[216:219]\n\t" \
    "ds_read_b128 v[240:243], v252 offset:46080\n\t" \
    "v_mfma_f32_16x16x32_bf16 v[220:223], v[76:79], v[224:227], v[220:223]\n\t" \
    "v_mfma_f32_16x16x32_bf16 v[212:215], v[60:63], v[224:227], v[212:215]\n\t" \
    "s_waitcnt lgkmcnt(4)\n\t" \
    "v_mfma_f32_16x16x32_bf16 v[204:207], v[160:163], v[228:231], v[204:207]\n\t" \
    "v_xor_b32 v252, 0x2000, %[xa]\n\t" \
    "ds_read_b128 v[224:227], v252 offset:0\n\t" \
    "v_mfma_f32_16x16x32_bf16 v[208:211], v[168:171], v[228:231], v[208:211]\n\t" \
    "v_mfma_f32_16x16x32_bf16 v[200:203], v[244:247], v[228:231], v[200:203]\n\t" \
    "s_waitcnt lgkmcnt(3)\n\t" \
    "v_mfma_f32_16x16x32_bf16 v[216:219], v[160:163], v[232:235], v[216:219]\n\t" \
    "ds_read_b128 v[228:231], v252 offset:16384\n\t" \
    "v_mfma_f32_16x16x32_bf16 v[220:223], v[168:171], v[232:235], v[220:223]\n\t" \
    "v_mfma_f32_16x16x32_bf16 v[212:215], v[244:247], v[232:235], v[212:215]\n\t" \
    "s_waitcnt lgkmcnt(3)\n\t" \
    "v_mfma_f32_16x16x32_bf16 v[204:207], v[164:167], v[236:239], v[204:207]\n\t" \
    "ds_read_b128 v[232:235], v252 offset:1024\n\t" \
    "v_mfma_f32_16x16x32_bf16 v[208:211], v[172:175], v[236:239], v[208:211]\n\t" \
    "ds_read_b128 v[244:247], %[wl] offset:4096\n\t" \
    "v_mfma_f32_16x16x32_bf16 v[200:203], v[248:251], v[236:239], v[200:203]\n\t" \
    "s_waitcnt lgkmcnt(4)\n\t" \
    "v_mfma_f32_16x16x32_bf16 v[216:219], v[164:167], v[240:243], v[216:219]\n\t" \
    "ds_read_b128 v[236:239], v252 offset:17408\n\t" \
    "v_mfma_f32_16x16x32_bf16 v[220:223], v[172:175], v[240:243], v[220:223]\n\t" \
    "v_mfma_f32_16x16x32_bf16 v[212:215], v[248:251], v[240:243], v[212:215]\n\t" \
    "s_waitcnt lgkmcnt(4)\n\t" \
    "v_mfma_f32_16x16x32_bf16 v[204:207], v[80:83], v[224:227], v[204:207]\n\t" \
    "v_xor_b32 v252, 0x2800, %[xa]\n\t" \
    "ds_read_b128 v[240:243], v252 offset:0\n\t" \
    "v_mfma_f32_16x16x32_bf16 v[208:211], v[88:91], v[224:227], v[208:211]\n\t" \
    "ds_read_b128 v[248:251], %[wl] offset:5120\n\t" \
    "s_waitcnt lgkmcnt(3)\n\t" \
    "v_mfma_f32_16x16x32_bf16 v[200:203], v[244:247], v[224:227], v[200:203]\n\t" \
    "v_mfma_f32_16x16x32_bf16 v[216:219], v[80:83], v[228:231], v[216:219]\n\t" \
    "ds_read_b128 v[224:227], v252 offset:16384\n\t" \
    "v_mfma_f32_16x16x32_bf16 v[220:223], v[88:91], v[228:231], v[220:223]\n\t" \
    "v_mfma_f32_16x16x32_bf16 v[212:215], v[244:247], v[228:231], v[212:215]\n\t" \
    "v_mfma_f32_16x16x32_bf16 v[204:207], v[84:87], v[232:235], v[204:207]\n\t" \
    "ds_read_b128 v[228:231], v252 offset:1024\n\t" \
    "v_mfma_f32_16x16x32_bf16 v[208:211], v[92:95], v[232:235], v[208:211]\n\t" \
    "ds_read_b128 v[244:247], %[wl] offset:6144\n\t" \
    "s_waitcnt lgkmcnt(3)\n\t" \
    "v_mfma_f32_16x16x32_bf16 v[200:203], v[248:251], v[232:235], v[200:203]\n\t" \
    "v_mfma_f32_16x16x32_bf16 v[216:219], v[84:87], v[236:239], v[216:219]\n\t" \
    "ds_read_b128 v[232:235], v252 offset:17408\n\t" \
    "v_mfma_f32_16x16x32_bf16 v[220:223], v[92:95], v[236:239], v[220:223]\n\t" \
    "v_mfma_f32_16x16x32_bf16 v[212:215], v[248:251], v[236:239], v[212:215]\n\t" \
    "v_mfma_f32_16x16x32_bf16 v[204:207], v[104:107], v[240:243], v[204:207]\n\t" \
    "v_xor_b32 v252, 0x3000, %[xa]\n\t" \
    "ds_read_b128 v[236:239], v252 offset:0\n\t" \
    "v_mfma_f32_16x16x32_bf16 v[208:211], v[112:115], v[240:243], v[208:211]\n\t" \
    "ds_read_b128 v[248:251], %[wl] offset:7168\n\t" \
    "v_mfma_f32_16x16x32_bf16 v[200:203], v[96:99], v[240:243], v[200:203]\n\t" \
    "s_waitcnt lgkmcnt(5)\n\t" \
    "v_mfma_f32_16x16x32_bf16 v[216:219], v[104:107], v[224:227], v[216:219]\n\t" \
    "ds_read_b128 v[240:243], v252 offset:16384\n\t" \
    "v_mfma_f32_16x16x32_bf16 v[220:223], v[112:115], v[224:227], v[220:223]\n\t" \
    "v_mfma_f32_16x16x32_bf16 v[212:215], v[96:99], v[224:227], v[212:215]\n\t" \
    "s_waitcnt lgkmcnt(5)\n\t" \
    "v_mfma_f32_16x16x32_bf16 v[204:207], v[108:111], v[228:231], v[204:207]\n\t" \
    "ds_read_b128 v[224:227], v252 offset:1024\n\t" \
    "v_mfma_f32_16x16x32_bf16 v[208:211], v[116:119], v[228:231], v[208:211]\n\t" \
    "v_mfma_f32_16x16x32_bf16 v[200:203], v[100:103], v[228:231], v[200:203]\n\t" \
    "s_waitcnt lgkmcnt(4)\n\t" \
    "v_mfma_f32_16x16x32_bf16 v[216:219], v[108:111], v[232:235], v[216:219]\n\t" \
    "ds_read_b128 v[228:231], v252 offset:17408\n\t" \
    "v_mfma_f32_16x16x32_bf16 v[220:223], v[116:119], v[232:235], v[220:223]\n\t" \
    "v_mfma_f32_16x16x32_bf16 v[212:215], v[100:103], v[232:235], v[212:215]\n\t" \
    "s_waitcnt lgkmcnt(4)\n\t" \
    "v_mfma_f32_16x16x32_bf16 v[204:207], v[120:123], v[236:239], v[204:207]\n\t" \
    "v_xor_b32 v252, 0x3800, %[xa]\n\t" \
    "ds_read_b128 v[232:235], v252 offset:0\n\t" \
    "v_mfma_f32_16x16x32_bf16 v[208:211], v[128:131], v[236:239], v[208:211]\n\t" \
    "v_mfma_f32_16x16x32_bf16 v[200:203], v[244:247], v[236:239], v[200:203]\n\t" \
    "s_waitcnt lgkmcnt(3)\n\t" \
    "v_mfma_f32_16x16x32_bf16 v[216:219], v[120:123], v[240:243], v[216:219]\n\t" \
    "ds_read_b128 v[236:239], v252 offset:16384\n\t" \
    "v_mfma_f32_16x16x32_bf16 v[220:223], v[128:131], v[240:243], v[220:223]\n\t" \
    "v_mfma_f32_16x16x32_bf16 v[212:215], v[244:247], v[240:243], v[212:215]\n\t" \
    "s_waitcnt lgkmcnt(3)\n\t" \
    "v_mfma_f32_16x16x32_bf16 v[204:207], v[124:127], v[224:227], v[204:207]\n\t" \
    "ds_read_b128 v[240:243], v252 offset:1024\n\t" \
    "v_mfma_f32_16x16x32_bf16 v[208:211], v[132:135], v[224:227], v[208:211]\n\t" \
    "v_mfma_f32_16x16x32_bf16 v[200:203], v[248:251], v[224:227], v[200:203]\n\t" \
    "s_waitcnt lgkmcnt(3)\n\t" \
    "v_mfma_f32_16x16x32_bf16 v[216:219], v[124:127], v[228:231], v[216:219]\n\t" \
    "ds_read_b128 v[224:227], v252 offset:17408\n\t" \
    "v_mfma_f32_16x16x32_bf16 v[220:223], v[132:135], v[228:231], v[220:223]\n\t" \
    "v_mfma_f32_16x16x32_bf16 v[212:215], v[248:251], v[228:231], v[212:215]\n\t" \
    "s_waitcnt lgkmcnt(3)\n\t" \
    "v_mfma_f32_16x16x32_bf16 v[204:207], v[144:147], v[232:235], v[204:207]\n\t" \
    "v_and_b32 v252, 0x3ff, %[xa]\n\t" \
    "ds_read_b128 v[228:231], v252 offset:43008\n\t" \
    "v_mov_b64 v[244:245], 0\n\t" \
    "v_mfma_f32_16x16x32_bf16 v[208:211], v[152:155], v[232:235], v[208:211]\n\t" \
    "v_mov_b64 v[246:247], 0\n\t" \
    "v_mfma_f32_16x16x32_bf16 v[200:203], v[136:139], v[232:235], v[200:203]\n\t" \
    "v_mov_b64 v[248:249], 0\n\t" \
    "s_waitcnt lgkmcnt(3)\n\t" \
    "v_mfma_f32_16x16x32_bf16 v[216:219], v[144:147], v[236:239], v[216:219]\n\t" \
    "ds_read_b128 v[232:235], v252 offset:44032\n\t" \
    "v_mov_b64 v[250:251], 0\n\t" \
    "v_mfma_f32_16x16x32_bf16 v[220:223], v[152:155], v[236:239], v[220:223]\n\t" \
    "v_mfma_f32_16x16x32_bf16 v[212:215], v[136:139], v[236:239], v[212:215]\n\t" \
    "s_waitcnt lgkmcnt(3)\n\t" \
    "v_mfma_f32_16x16x32_bf16 v[204:207], v[148:151], v[240:243], v[204:207]\n\t" \
    "ds_read_b128 v[236:239], v252 offset:47104\n\t" \
    "v_mfma_f32_16x16x32_bf16 v[208:211], v[156:159], v[240:243], v[208:211]\n\t" \
    "v_mfma_f32_16x16x32_bf16 v[200:203], v[140:143], v[240:243], v[200:203]\n\t" \
    "s_waitcnt lgkmcnt(3)\n\t" \
    "v_mfma_f32_16x16x32_bf16 v[216:219], v[148:151], v[224:227], v[216:219]\n\t" \
    "ds_read_b128 v[240:243], v252 offset:48128\n\t" \
    "v_mfma_f32_16x16x32_bf16 v[220:223], v[156:159], v[224:227], v[220:223]\n\t" \
    "v_mfma_f32_16x16x32_bf16 v[212:215], v[140:143], v[224:227], v[212:215]\n\t" \
    "s_waitcnt lgkmcnt(3)\n\t" \
    "v_mfma_f32_16x16x32_bf16 v[204:207], v[184:187], v[228:231], v[204:207]\n\t" \
    "v_add_u32 v252, %[qs], v252\n\t" \
    "v_mfma_f32_16x16x32_bf16 v[208:211], v[192:195], v[228:231], v[208:211]\n\t" \
    "v_mov_b64 v[224:225], 0\n\t" \
    "v_mfma_f32_16x16x32_bf16 v[200:203], v[176:179], v[228:231], v[200:203]\n\t" \
    "v_mov_b64 v[226:227], 0\n\t" \
    "s_waitcnt lgkmcnt(2)\n\t" \
    "v_mfma_f32_16x16x32_bf16 v[204:207], v[188:191], v[232:235], v[204:207]\n\t" \
    "v_mfma_f32_16x16x32_bf16 v[208:211], v[196:199], v[232:235], v[208:211]\n\t" \
    "v_mfma_f32_16x16x32_bf16 v[200:203], v[180:183], v[232:235], v[200:203]\n\t" \
    "s_waitcnt lgkmcnt(1)\n\t" \
    "v_mfma_f32_16x16x32_bf16 v[216:219], v[184:187], v[236:239], v[216:219]\n\t" \
    "ds_read_b128 v[228:231], %[wl] offset:2048\n\t" \
    "ds_read_b128 v[232:235], %[wl] offset:3072\n\t" \
    "v_mfma_f32_16x16x32_bf16 v[220:223], v[192:195], v[236:239], v[220:223]\n\t" \
    "v_cvt_pk_bf16_f32 v204, v204, v205\n\t" \
    "v_cvt_pk_bf16_f32 v205, v206, v207\n\t" \
    "v_mfma_f32_16x16x32_bf16 v[212:215], v[176:179], v[236:239], v[212:215]\n\t" \
    "ds_write_b64 v252, v[204:205] offset:1024\n\t" \
    "v_cvt_pk_bf16_f32 v208, v208, v209\n\t" \
    "s_waitcnt lgkmcnt(3)\n\t" \
    "v_mfma_f32_16x16x32_bf16 v[216:219], v[188:191], v[240:243], v[216:219]\n\t" \
    "v_cvt_pk_bf16_f32 v209, v210, v211\n\t" \
    "ds_write_b64 v252, v[208:209] offset:2048\n\t" \
    "v_mfma_f32_16x16x32_bf16 v[220:223], v[196:199], v[240:243], v[220:223]\n\t" \
    "v_cvt_pk_bf16_f32 v200, v200, v201\n\t" \
    "v_cvt_pk_bf16_f32 v201, v202, v203\n\t" \
    "v_mfma_f32_16x16x32_bf16 v[212:215], v[180:183], v[240:243], v[212:215]\n\t" \
    "ds_write_b64 v252, v[200:201] offset:0\n\t" \
    "ds_read_b128 v[200:203], %[wl] offset:0\n\t" \
    "ds_read_b128 v[204:207], %[wl] offset:1024\n\t" \
    "v_cvt_pk_bf16_f32 v216, v216, v217\n\t" \
    "v_cvt_pk_bf16_f32 v217, v218, v219\n\t" \
    "ds_write_b64 v252, v[216:217] offset:7168\n\t" \
    "v_cvt_pk_bf16_f32 v220, v220, v221\n\t" \
    "v_cvt_pk_bf16_f32 v221, v222, v223\n\t" \
    "ds_write_b64 v252, v[220:221] offset:8192\n\t" \
    "v_cvt_pk_bf16_f32 v212, v212, v213\n\t" \
    "v_cvt_pk_bf16_f32 v213, v214, v215\n\t" \
    "ds_write_b64 v252, v[212:213] offset:6144\n\t" \
    "s_waitcnt lgkmcnt(0)\n\t"
#define ASM_CRIT1 \
    "v_bfe_i32 v241, %[ab], 7, 1\n\t" \
    "v_and_b32 v241, 0xaaaa, v241\n\t" \
    "v_or_b32 v241, 0x4444, v241\n\t" \
    "v_xor_b32 v212, 0x400, %[ab]\n\t" \
    "v_xor_b32 v216, 0x800, %[ab]\n\t" \
    "v_smfmac_f32_16x16x64_bf16 v[224:227], v[208:211], v[200:207], v241\n\t" \
    "v_smfmac_f32_16x16x64_bf16 v[244:247], v[208:211], v[0:7], v241\n\t" \
    "v_smfmac_f32_16x16x64_bf16 v[248:251], v[208:211], v[8:15], v241\n\t" \
    "v_xor_b32 v220, 0xc00, %[ab]\n\t" \
    "v_xor_b32 v236, 0x1000, %[ab]\n\t" \
    "v_xor_b32 v240, 0x1400, %[ab]\n\t" \
    "s_waitcnt lgkmcnt(0)\n\t" \
    "s_barrier\n\t" ASM_ST \
    "ds_read_b128 v[212:215], v212 offset:0\n\t" \
    "ds_read_b128 v[216:219], v216 offset:0\n\t" \
    "ds_read_b128 v[220:223], v220 offset:0\n\t" \
    "ds_read_b128 v[236:239], v236 offset:0\n\t" \
    "ds_read_b128 v[208:211], v240 offset:0\n\t" \
    "ds_read_b128 v[200:203], %[wl] offset:4096\n\t" \
    "ds_read_b128 v[204:207], %[wl] offset:5120\n\t" \
    "s_waitcnt lgkmcnt(6)\n\t" \
    "v_smfmac_f32_16x16x64_bf16 v[224:227], v[212:215], v[16:23], v241\n\t" \
    "v_smfmac_f32_16x16x64_bf16 v[244:247], v[212:215], v[24:31], v241\n\t" \
    "v_xor_b32 v240, 0x1800, %[ab]\n\t" \
    "v_smfmac_f32_16x16x64_bf16 v[248:251], v[212:215], v[32:39], v241\n\t" \
    "ds_read_b128 v[212:215], v240 offset:0\n\t" \
    "s_waitcnt lgkmcnt(6)\n\t" \
    "v_smfmac_f32_16x16x64_bf16 v[224:227], v[216:219], v[228:235], v241\n\t" \
    "ds_read_b128 v[228:231], %[wl] offset:6144\n\t" \
    "ds_read_b128 v[232:235], %[wl] offset:7168\n\t" \
    "v_smfmac_f32_16x16x64_bf16 v[244:247], v[216:219], v[40:47], v241\n\t" \
    "v_xor_b32 v240, 0x1c00, %[ab]\n\t" \
    "v_smfmac_f32_16x16x64_bf16 v[248:251], v[216:219], v[48:55], v241\n\t" \
    "ds_read_b128 v[216:219], v240 offset:0\n\t" \
    "s_waitcnt lgkmcnt(8)\n\t" \
    "v_smfmac_f32_16x16x64_bf16 v[224:227], v[220:223], v[56:63], v241\n\t" \
    "v_smfmac_f32_16x16x64_bf16 v[244:247], v[220:223], v[64:71], v241\n\t" \
    "v_and_b32 v240, 0x3ff, %[ab]\n\t" \
    "v_smfmac_f32_16x16x64_bf16 v[248:251], v[220:223], v[72:79], v241\n\t" \
    "ds_read_b128 v[220:223], v240 offset:8192\n\t" \
    "s_waitcnt lgkmcnt(5)\n\t" \
    "v_smfmac_f32_16x16x64_bf16 v[224:227], v[236:239], v[200:207], v241\n\t" \
    "ds_read_b128 v[200:203], %[wl] offset:8192\n\t" \
    "ds_read_b128 v[204:207], %[wl] offset:9216\n\t" \
    "v_smfmac_f32_16x16x64_bf16 v[244:247], v[236:239], v[80:87], v241\n\t" \
    "v_smfmac_f32_16x16x64_bf16 v[248:251], v[236:239], v[88:95], v241\n\t" \
    "ds_read_b128 v[236:239], v240 offset:9216\n\t" \
    "v_smfmac_f32_16x16x64_bf16 v[244:247], v[208:211], v[104:111], v241\n\t" \
    "s_waitcnt lgkmcnt(7)\n\t" \
    "v_smfmac_f32_16x16x64_bf16 v[244:247], v[212:215], v[120:127], v241\n\t" \
    "s_waitcnt lgkmcnt(4)\n\t" \
    "v_smfmac_f32_16x16x64_bf16 v[244:247], v[216:219], v[144:151], v241\n\t" \
    "s_waitcnt lgkmcnt(3)\n\t" \
    "v_smfmac_f32_16x16x64_bf16 v[244:247], v[220:223], v[160:167], v241\n\t" \
    "s_waitcnt lgkmcnt(0)\n\t" \
    "v_smfmac_f32_16x16x64_bf16 v[244:247], v[236:239], v[184:191], v241\n\t" \
    "v_smfmac_f32_16x16x64_bf16 v[248:251], v[208:211], v[112:119], v241\n\t" \
    "v_smfmac_f32_16x16x64_bf16 v[248:251], v[212:215], v[128:135], v241\n\t" \
    "v_smfmac_f32_16x16x64_bf16 v[248:251], v[216:219], v[152:159], v241\n\t" \
    "v_permlane32_swap_b32 v244, v246\n\t" \
    "v_permlane32_swap_b32 v245, v247\n\t" \
    "v_smfmac_f32_16x16x64_bf16 v[248:251], v[220:223], v[168:175], v241\n\t" \
    "v_add_f32 v244, v244, v246\n\t" \
    "v_add_f32 v245, v245, v247\n\t" \
    "v_smfmac_f32_16x16x64_bf16 v[248:251], v[236:239], v[192:199], v241\n\t" \
    "v_cvt_pk_bf16_f32 v244, v244, v245\n\t" \
    "ds_write_b16 %[dst], v244 offset:512\n\t" \
    "ds_write_b16_d16_hi %[dst], v244 offset:528\n\t" \
    "v_smfmac_f32_16x16x64_bf16 v[224:227], v[208:211], v[96:103], v241\n\t" \
    "v_smfmac_f32_16x16x64_bf16 v[224:227], v[212:215], v[228:235], v241\n\t" \
    "v_smfmac_f32_16x16x64_bf16 v[224:227], v[216:219], v[136:143], v241\n\t" \
    "v_permlane32_swap_b32 v248, v250\n\t" \
    "v_permlane32_swap_b32 v249, v251\n\t" \
    "v_smfmac_f32_16x16x64_bf16 v[224:227], v[220:223], v[200:207], v241\n\t" \
    "v_add_f32 v248, v248, v250\n\t" \
    "v_add_f32 v249, v249, v251\n\t" \
    "v_smfmac_f32_16x16x64_bf16 v[224:227], v[236:239], v[176:183], v241\n\t" \
    "v_cvt_pk_bf16_f32 v248, v248, v249\n\t" \
    "ds_write_b16 %[dst], v248 offset:1024\n\t" \
    "ds_write_b16_d16_hi %[dst], v248 offset:1040\n\t" \
    "s_nop 4\n\t" \
    "v_permlane32_swap_b32 v224, v226\n\t" \
    "v_permlane32_swap_b32 v225, v227\n\t" \
    "v_add_f32 v224, v224, v226\n\t" \
    "v_add_f32 v225, v225, v227\n\t" \
    "v_cvt_pk_bf16_f32 v224, v224, v225\n\t" \
    "ds_write_b16 %[dst], v224 offset:0\n\t" \
    "ds_write_b16_d16_hi %[dst], v224 offset:16\n\t"
#define ASM_CRIT1_N \
    "v_bfe_i32 v241, %[ab], 7, 1\n\t" \
    "v_and_b32 v241, 0xaaaa, v241\n\t" \
    "v_or_b32 v241, 0x4444, v241\n\t" \
    "v_xor_b32 v212, 0x400, %[ab]\n\t" \
    "v_xor_b32 v216, 0x800, %[ab]\n\t" \
    "v_xor_b32 v220, 0xc00, %[ab]\n\t" \
    "v_xor_b32 v236, 0x1000, %[ab]\n\t" \
    "v_xor_b32 v240, 0x1400, %[ab]\n\t" \
    "s_waitcnt lgkmcnt(0)\n\t" \
    "s_barrier\n\t" ASM_ST \
    "ds_read_b128 v[208:211], %[ab] offset:0\n\t" \
    "ds_read_b128 v[212:215], v212 offset:0\n\t" \
    "ds_read_b128 v[216:219], v216 offset:0\n\t" \
    "ds_read_b128 v[220:223], v220 offset:0\n\t" \
    "ds_read_b128 v[236:239], v236 offset:0\n\t" \
    "s_waitcnt lgkmcnt(4)\n\t" \
    "v_smfmac_f32_16x16x64_bf16 v[224:227], v[208:211], v[200:207], v241\n\t" \
    "v_smfmac_f32_16x16x64_bf16 v[244:247], v[208:211], v[0:7], v241\n\t" \
    "v_smfmac_f32_16x16x64_bf16 v[248:251], v[208:211], v[8:15], v241\n\t" \
    "ds_read_b128 v[208:211], v240 offset:0\n\t" \
    "ds_read_b128 v[200:203], %[wl] offset:4096\n\t" \
    "ds_read_b128 v[204:207], %[wl] offset:5120\n\t" \
    "s_waitcnt lgkmcnt(6)\n\t" \
    "v_smfmac_f32_16x16x64_bf16 v[224:227], v[212:215], v[16:23], v241\n\t" \
    "v_smfmac_f32_16x16x64_bf16 v[244:247], v[212:215], v[24:31], v241\n\t" \
    "v_xor_b32 v240, 0x1800, %[ab]\n\t" \
    "v_smfmac_f32_16x16x64_bf16 v[248:251], v[212:215], v[32:39], v241\n\t" \
    "ds_read_b128 v[212:215], v240 offset:0\n\t" \
    "s_waitcnt lgkmcnt(6)\n\t" \
    "v_smfmac_f32_16x16x64_bf16 v[224:227], v[216:219], v[228:235], v241\n\t" \
    "ds_read_b128 v[228:231], %[wl] offset:6144\n\t" \
    "ds_read_b128 v[232:235], %[wl] offset:7168\n\t" \
    "v_smfmac_f32_16x16x64_bf16 v[244:247], v[216:219], v[40:47], v241\n\t" \
    "v_xor_b32 v240, 0x1c00, %[ab]\n\t" \
    "v_smfmac_f32_16x16x64_bf16 v[248:251], v[216:219], v[48:55], v241\n\t" \
    "ds_read_b128 v[216:219], v240 offset:0\n\t" \
    "s_waitcnt lgkmcnt(8)\n\t" \
    "v_smfmac_f32_16x16x64_bf16 v[224:227], v[220:223], v[56:63], v241\n\t" \
    "v_smfmac_f32_16x16x64_bf16 v[244:247], v[220:223], v[64:71], v241\n\t" \
    "v_and_b32 v240, 0x3ff, %[ab]\n\t" \
    "v_smfmac_f32_16x16x64_bf16 v[248:251], v[220:223], v[72:79], v241\n\t" \
    "ds_read_b128 v[220:223], v240 offset:8192\n\t" \
    "s_waitcnt lgkmcnt(5)\n\t" \
    "v_smfmac_f32_16x16x64_bf16 v[224:227], v[236:239], v[200:207], v241\n\t" \
    "ds_read_b128 v[200:203], %[wl] offset:8192\n\t" \
    "ds_read_b128 v[204:207], %[wl] offset:9216\n\t" \
    "v_smfmac_f32_16x16x64_bf16 v[244:247], v[236:239], v[80:87], v241\n\t" \
    "v_smfmac_f32_16x16x64_bf16 v[248:251], v[236:239], v[88:95], v241\n\t" \
    "ds_read_b128 v[236:239], v240 offset:9216\n\t" \
    "v_smfmac_f32_16x16x64_bf16 v[244:247], v[208:211], v[104:111], v241\n\t" \
    "s_waitcnt lgkmcnt(7)\n\t" \
    "v_smfmac_f32_16x16x64_bf16 v[244:247], v[212:215], v[120:127], v241\n\t" \
    "s_waitcnt lgkmcnt(4)\n\t" \
    "v_smfmac_f32_16x16x64_bf16 v[244:247], v[216:219], v[144:151], v241\n\t" \
    "s_waitcnt lgkmcnt(3)\n\t" \
    "v_smfmac_f32_16x16x64_bf16 v[244:247], v[220:223], v[160:167], v241\n\t" \
    "s_waitcnt lgkmcnt(0)\n\t" \
    "v_smfmac_f32_16x16x64_bf16 v[244:247], v[236:239], v[184:191], v241\n\t" \
    "v_smfmac_f32_16x16x64_bf16 v[248:251], v[208:211], v[112:119], v241\n\t" \
    "v_smfmac_f32_16x16x64_bf16 v[248:251], v[212:215], v[128:135], v241\n\t" \
    "v_smfmac_f32_16x16x64_bf16 v[248:251], v[216:219], v[152:159], v241\n\t" \
    "v_permlane32_swap_b32 v244, v246\n\t" \
    "v_permlane32_swap_b32 v245, v247\n\t" \
    "v_smfmac_f32_16x16x64_bf16 v[248:251], v[220:223], v[168:175], v241\n\t" \
    "v_add_f32 v244, v244, v246\n\t" \
    "v_add_f32 v245, v245, v247\n\t" \
    "v_smfmac_f32_16x16x64_bf16 v[248:251], v[236:239], v[192:199], v241\n\t" \
    "v_cvt_pk_bf16_f32 v244, v244, v245\n\t" \
    "ds_write_b16 %[dst], v244 offset:512\n\t" \
    "ds_write_b16_d16_hi %[dst], v244 offset:528\n\t" \
    "v_smfmac_f32_16x16x64_bf16 v[224:227], v[208:211], v[96:103], v241\n\t" \
    "v_smfmac_f32_16x16x64_bf16 v[224:227], v[212:215], v[228:235], v241\n\t" \
    "v_smfmac_f32_16x16x64_bf16 v[224:227], v[216:219], v[136:143], v241\n\t" \
    "v_permlane32_swap_b32 v248, v250\n\t" \
    "v_permlane32_swap_b32 v249, v251\n\t" \
    "v_smfmac_f32_16x16x64_bf16 v[224:227], v[220:223], v[200:207], v241\n\t" \
    "v_add_f32 v248, v248, v250\n\t" \
    "v_add_f32 v249, v249, v251\n\t" \
    "v_smfmac_f32_16x16x64_bf16 v[224:227], v[236:239], v[176:183], v241\n\t" \
    "v_cvt_pk_bf16_f32 v248, v248, v249\n\t" \
    "ds_write_b16 %[dst], v248 offset:1024\n\t" \
    "ds_write_b16_d16_hi %[dst], v248 offset:1040\n\t" \
    "s_nop 4\n\t" \
    "v_permlane32_swap_b32 v224, v226\n\t" \
    "v_permlane32_swap_b32 v225, v227\n\t" \
    "v_add_f32 v224, v224, v226\n\t" \
    "v_add_f32 v225, v225, v227\n\t" \
    "v_cvt_pk_bf16_f32 v224, v224, v225\n\t" \
    "ds_write_b16 %[dst], v224 offset:0\n\t" \
    "ds_write_b16_d16_hi %[dst], v224 offset:16\n\t"
#define ASM_VOX \
    "s_waitcnt lgkmcnt(0)\n\t" \
    "ds_read_b128 v[224:227], %[va] offset:0\n\t" \
    "ds_read_b128 v[248:251], %[wl] offset:0\n\t" \
    "ds_read_b128 v[228:231], %[va] offset:6144\n\t" \
    "ds_read_b128 v[232:235], %[va] offset:2048\n\t" \
    "ds_read_b128 v[236:239], %[va] offset:8192\n\t" \
    "ds_read_b128 v[240:243], %[va] offset:3072\n\t" \
    "s_waitcnt lgkmcnt(4)\n\t" \
    "v_mfma_f32_16x16x32_bf16 v[200:203], v[224:227], v[248:251], 0\n\t" \
    "ds_read_b128 v[244:247], %[va] offset:9216\n\t" \
    "s_waitcnt lgkmcnt(4)\n\t" \
    "v_mfma_f32_16x16x32_bf16 v[204:207], v[228:231], v[248:251], 0\n\t" \
    "ds_read_b128 v[224:227], %[va] offset:1024\n\t" \
    "s_waitcnt lgkmcnt(4)\n\t" \
    "v_mfma_f32_16x16x32_bf16 v[200:203], v[232:235], v[0:3], v[200:203]\n\t" \
    "ds_read_b128 v[228:231], %[va] offset:7168\n\t" \
    "ds_read_b128 v[248:251], %[wl] offset:1024\n\t" \
    "s_waitcnt lgkmcnt(5)\n\t" \
    "v_mfma_f32_16x16x32_bf16 v[204:207], v[236:239], v[0:3], v[204:207]\n\t" \
    "ds_read_b128 v[232:235], %[va] offset:4096\n\t" \
    "s_waitcnt lgkmcnt(5)\n\t" \
    "v_mfma_f32_16x16x32_bf16 v[200:203], v[240:243], v[4:7], v[200:203]\n\t" \
    "ds_read_b128 v[236:239], %[va] offset:10240\n\t" \
    "s_waitcnt lgkmcnt(5)\n\t" \
    "v_mfma_f32_16x16x32_bf16 v[204:207], v[244:247], v[4:7], v[204:207]\n\t" \
    "ds_read_b128 v[240:243], %[va] offset:5120\n\t" \
    "s_waitcnt lgkmcnt(3)\n\t" \
    "v_mfma_f32_16x16x32_bf16 v[200:203], v[224:227], v[248:251], v[200:203]\n\t" \
    "ds_read_b128 v[244:247], %[va] offset:11264\n\t" \
    "v_mfma_f32_16x16x32_bf16 v[208:211], v[224:227], v[20:23], 0\n\t" \
    "v_mfma_f32_16x16x32_bf16 v[204:207], v[228:231], v[248:251], v[204:207]\n\t" \
    "ds_read_b128 v[224:227], %[va] offset:0\n\t" \
    "v_mfma_f32_16x16x32_bf16 v[212:215], v[228:231], v[20:23], 0\n\t" \
    "s_waitcnt lgkmcnt(4)\n\t" \
    "v_mfma_f32_16x16x32_bf16 v[200:203], v[232:235], v[8:11], v[200:203]\n\t" \
    "ds_read_b128 v[228:231], %[va] offset:6144\n\t" \
    "ds_read_b128 v[248:251], %[wl] offset:2048\n\t" \
    "v_mfma_f32_16x16x32_bf16 v[208:211], v[232:235], v[32:35], v[208:211]\n\t" \
    "s_waitcnt lgkmcnt(5)\n\t" \
    "v_mfma_f32_16x16x32_bf16 v[204:207], v[236:239], v[8:11], v[204:207]\n\t" \
    "ds_read_b128 v[232:235], %[va] offset:2048\n\t" \
    "v_mfma_f32_16x16x32_bf16 v[212:215], v[236:239], v[32:35], v[212:215]\n\t" \
    "s_waitcnt lgkmcnt(5)\n\t" \
    "v_mfma_f32_16x16x32_bf16 v[200:203], v[240:243], v[12:15], v[200:203]\n\t" \
    "ds_read_b128 v[236:239], %[va] offset:8192\n\t" \
    "v_mfma_f32_16x16x32_bf16 v[208:211], v[240:243], v[36:39], v[208:211]\n\t" \
    "s_waitcnt lgkmcnt(5)\n\t" \
    "v_mfma_f32_16x16x32_bf16 v[204:207], v[244:247], v[12:15], v[204:207]\n\t" \
    "ds_read_b128 v[240:243], %[va] offset:3072\n\t" \
    "v_mfma_f32_16x16x32_bf16 v[212:215], v[244:247], v[36:39], v[212:215]\n\t" \
    "s_waitcnt lgkmcnt(3)\n\t" \
    "v_mfma_f32_16x16x32_bf16 v[216:219], v[224:227], v[248:251], 0\n\t" \
    "ds_read_b128 v[244:247], %[va] offset:9216\n\t" \
    "v_mfma_f32_16x16x32_bf16 v[208:211], v[224:227], v[16:19], v[208:211]\n\t" \
    "v_mfma_f32_16x16x32_bf16 v[220:223], v[228:231], v[248:251], 0\n\t" \
    "ds_read_b128 v[224:227], %[va] offset:1024\n\t" \
    "v_mfma_f32_16x16x32_bf16 v[212:215], v[228:231], v[16:19], v[212:215]\n\t" \
    "v_cvt_pk_bf16_f32 v200, v200, v201\n\t" \
    "s_waitcnt lgkmcnt(4)\n\t" \
    "v_mfma_f32_16x16x32_bf16 v[216:219], v[232:235], v[40:43], v[216:219]\n\t" \
    "ds_read_b128 v[228:231], %[va] offset:7168\n\t" \
    "ds_read_b128 v[248:251], %[wl] offset:3072\n\t" \
    "v_cvt_pk_bf16_f32 v201, v202, v203\n\t" \
    "v_mfma_f32_16x16x32_bf16 v[208:211], v[232:235], v[24:27], v[208:211]\n\t" \
    "v_cvt_pk_bf16_f32 v202, v204, v205\n\t" \
    "s_waitcnt lgkmcnt(5)\n\t" \
    "v_mfma_f32_16x16x32_bf16 v[220:223], v[236:239], v[40:43], v[220:223]\n\t" \
    "ds_read_b128 v[232:235], %[va] offset:4096\n\t" \
    "v_cvt_pk_bf16_f32 v203, v206, v207\n\t" \
    "v_mfma_f32_16x16x32_bf16 v[212:215], v[236:239], v[24:27], v[212:215]\n\t" \
    "buffer_store_dwordx4 v[200:203], %[l16], %[rd], %[so] offen sc1\n\t" \
    "s_waitcnt lgkmcnt(5)\n\t" \
    "v_mfma_f32_16x16x32_bf16 v[216:219], v[240:243], v[44:47], v[216:219]\n\t" \
    "ds_read_b128 v[236:239], %[va] offset:10240\n\t" \
    "s_add_u32 %[so], %[so], 0x8000\n\t" \
    "v_mfma_f32_16x16x32_bf16 v[208:211], v[240:243], v[28:31], v[208:211]\n\t" \
    "s_waitcnt lgkmcnt(5)\n\t" \
    "v_mfma_f32_16x16x32_bf16 v[220:223], v[244:247], v[44:47], v[220:223]\n\t" \
    "ds_read_b128 v[240:243], %[va] offset:5120\n\t" \
    "v_mfma_f32_16x16x32_bf16 v[212:215], v[244:247], v[28:31], v[212:215]\n\t" \
    "s_waitcnt lgkmcnt(3)\n\t" \
    "v_mfma_f32_16x16x32_bf16 v[216:219], v[224:227], v[248:251], v[216:219]\n\t" \
    "ds_read_b128 v[244:247], %[va] offset:11264\n\t" \
    "v_mfma_f32_16x16x32_bf16 v[200:203], v[224:227], v[60:63], 0\n\t" \
    "v_mfma_f32_16x16x32_bf16 v[220:223], v[228:231], v[248:251], v[220:223]\n\t" \
    "ds_read_b128 v[224:227], %[va] offset:0\n\t" \
    "v_mfma_f32_16x16x32_bf16 v[204:207], v[228:231], v[60:63], 0\n\t" \
    "v_cvt_pk_bf16_f32 v208, v208, v209\n\t" \
    "s_waitcnt lgkmcnt(4)\n\t" \
    "v_mfma_f32_16x16x32_bf16 v[216:219], v[232:235], v[48:51], v[216:219]\n\t" \
    "ds_read_b128 v[228:231], %[va] offset:6144\n\t" \
    "ds_read_b128 v[248:251], %[wl] offset:4096\n\t" \
    "v_cvt_pk_bf16_f32 v209, v210, v211\n\t" \
    "v_mfma_f32_16x16x32_bf16 v[200:203], v[232:235], v[72:75], v[200:203]\n\t" \
    "v_cvt_pk_bf16_f32 v210, v212, v213\n\t" \
    "s_waitcnt lgkmcnt(5)\n\t" \
    "v_mfma_f32_16x16x32_bf16 v[220:223], v[236:239], v[48:51], v[220:223]\n\t" \
    "ds_read_b128 v[232:235], %[va] offset:2048\n\t" \
    "v_cvt_pk_bf16_f32 v211, v214, v215\n\t" \
    "v_mfma_f32_16x16x32_bf16 v[204:207], v[236:239], v[72:75], v[204:207]\n\t" \
    "buffer_store_dwordx4 v[208:211], %[l16], %[rd], %[so] offen sc1\n\t" \
    "s_waitcnt lgkmcnt(5)\n\t" \
    "v_mfma_f32_16x16x32_bf16 v[216:219], v[240:243], v[52:55], v[216:219]\n\t" \
    "ds_read_b128 v[236:239], %[va] offset:8192\n\t" \
    "s_add_u32 %[so], %[so], 0x8000\n\t" \
    "v_mfma_f32_16x16x32_bf16 v[200:203], v[240:243], v[76:79], v[200:203]\n\t" \
    "s_waitcnt lgkmcnt(5)\n\t" \
    "v_mfma_f32_16x16x32_bf16 v[220:223], v[244:247], v[52:55], v[220:223]\n\t" \
    "ds_read_b128 v[240:243], %[va] offset:3072\n\t" \
    "v_mfma_f32_16x16x32_bf16 v[204:207], v[244:247], v[76:79], v[204:207]\n\t" \
    "s_waitcnt lgkmcnt(3)\n\t" \
    "v_mfma_f32_16x16x32_bf16 v[208:211], v[224:227], v[248:251], 0\n\t" \
    "ds_read_b128 v[244:247], %[va] offset:9216\n\t" \
    "v_mfma_f32_16x16x32_bf16 v[200:203], v[224:227], v[56:59], v[200:203]\n\t" \
    "v_mfma_f32_16x16x32_bf16 v[212:215], v[228:231], v[248:251], 0\n\t" \
    "ds_read_b128 v[224:227], %[va] offset:1024\n\t" \
    "v_mfma_f32_16x16x32_bf16 v[204:207], v[228:231], v[56:59], v[204:207]\n\t" \
    "v_cvt_pk_bf16_f32 v216, v216, v217\n\t" \
    "s_waitcnt lgkmcnt(4)\n\t" \
    "v_mfma_f32_16x16x32_bf16 v[208:211], v[232:235], v[80:83], v[208:211]\n\t" \
    "ds_read_b128 v[228:231], %[va] offset:7168\n\t" \
    "ds_read_b128 v[248:251], %[wl] offset:5120\n\t" \
    "v_cvt_pk_bf16_f32 v217, v218, v219\n\t" \
    "v_mfma_f32_16x16x32_bf16 v[200:203], v[232:235], v[64:67], v[200:203]\n\t" \
    "v_cvt_pk_bf16_f32 v218, v220, v221\n\t" \
    "s_waitcnt lgkmcnt(5)\n\t" \
    "v_mfma_f32_16x16x32_bf16 v[212:215], v[236:239], v[80:83], v[212:215]\n\t" \
    "ds_read_b128 v[232:235], %[va] offset:4096\n\t" \
    "v_cvt_pk_bf16_f32 v219, v222, v223\n\t" \
    "v_mfma_f32_16x16x32_bf16 v[204:207], v[236:239], v[64:67], v[204:207]\n\t" \
    "buffer_store_dwordx4 v[216:219], %[l16], %[rd], %[so] offen sc1\n\t" \
    "s_waitcnt lgkmcnt(5)\n\t" \
    "v_mfma_f32_16x16x32_bf16 v[208:211], v[240:243], v[84:87], v[208:211]\n\t" \
    "ds_read_b128 v[236:239], %[va] offset:10240\n\t" \
    "s_add_u32 %[so], %[so], 0x8000\n\t" \
    "v_mfma_f32_16x16x32_bf16 v[200:203], v[240:243], v[68:71], v[200:203]\n\t" \
    "s_waitcnt lgkmcnt(5)\n\t" \
    "v_mfma_f32_16x16x32_bf16 v[212:215], v[244:247], v[84:87], v[212:215]\n\t" \
    "ds_read_b128 v[240:243], %[va] offset:5120\n\t" \
    "v_mfma_f32_16x16x32_bf16 v[204:207], v[244:247], v[68:71], v[204:207]\n\t" \
    "s_waitcnt lgkmcnt(3)\n\t" \
    "v_mfma_f32_16x16x32_bf16 v[208:211], v[224:227], v[248:251], v[208:211]\n\t" \
    "ds_read_b128 v[244:247], %[va] offset:11264\n\t" \
    "v_mfma_f32_16x16x32_bf16 v[216:219], v[224:227], v[100:103], 0\n\t" \
    "v_mfma_f32_16x16x32_bf16 v[212:215], v[228:231], v[248:251], v[212:215]\n\t" \
    "ds_read_b128 v[224:227], %[va] offset:0\n\t" \
    "v_mfma_f32_16x16x32_bf16 v[220:223], v[228:231], v[100:103], 0\n\t" \
    "v_cvt_pk_bf16_f32 v200, v200, v201\n\t" \
    "s_waitcnt lgkmcnt(4)\n\t" \
    "v_mfma_f32_16x16x32_bf16 v[208:211], v[232:235], v[88:91], v[208:211]\n\t" \
    "ds_read_b128 v[228:231], %[va] offset:6144\n\t" \
    "ds_read_b128 v[248:251], %[wl] offset:6144\n\t" \
    "v_cvt_pk_bf16_f32 v201, v202, v203\n\t" \
    "v_mfma_f32_16x16x32_bf16 v[216:219], v[232:235], v[112:115], v[216:219]\n\t" \
    "v_cvt_pk_bf16_f32 v202, v204, v205\n\t" \
    "s_waitcnt lgkmcnt(5)\n\t" \
    "v_mfma_f32_16x16x32_bf16 v[212:215], v[236:239], v[88:91], v[212:215]\n\t" \
    "ds_read_b128 v[232:235], %[va] offset:2048\n\t" \
    "v_cvt_pk_bf16_f32 v203, v206, v207\n\t" \
    "v_mfma_f32_16x16x32_bf16 v[220:223], v[236:239], v[112:115], v[220:223]\n\t" \
    "buffer_store_dwordx4 v[200:203], %[l16], %[rd], %[so] offen sc1\n\t" \
    "s_waitcnt lgkmcnt(5)\n\t" \
    "v_mfma_f32_16x16x32_bf16 v[208:211], v[240:243], v[92:95], v[208:211]\n\t" \
    "ds_read_b128 v[236:239], %[va] offset:8192\n\t" \
    "s_add_u32 %[so], %[so], 0x8000\n\t" \
    "v_mfma_f32_16x16x32_bf16 v[216:219], v[240:243], v[116:119], v[216:219]\n\t" \
    "s_waitcnt lgkmcnt(5)\n\t" \
    "v_mfma_f32_16x16x32_bf16 v[212:215], v[244:247], v[92:95], v[212:215]\n\t" \
    "ds_read_b128 v[240:243], %[va] offset:3072\n\t" \
    "v_mfma_f32_16x16x32_bf16 v[220:223], v[244:247], v[116:119], v[220:223]\n\t" \
    "s_waitcnt lgkmcnt(3)\n\t" \
    "v_mfma_f32_16x16x32_bf16 v[200:203], v[224:227], v[248:251], 0\n\t" \
    "ds_read_b128 v[244:247], %[va] offset:9216\n\t" \
    "v_mfma_f32_16x16x32_bf16 v[216:219], v[224:227], v[96:99], v[216:219]\n\t" \
    "v_mfma_f32_16x16x32_bf16 v[204:207], v[228:231], v[248:251], 0\n\t" \
    "ds_read_b128 v[224:227], %[va] offset:1024\n\t" \
    "v_mfma_f32_16x16x32_bf16 v[220:223], v[228:231], v[96:99], v[220:223]\n\t" \
    "v_cvt_pk_bf16_f32 v208, v208, v209\n\t" \
    "s_waitcnt lgkmcnt(4)\n\t" \
    "v_mfma_f32_16x16x32_bf16 v[200:203], v[232:235], v[120:123], v[200:203]\n\t" \
    "ds_read_b128 v[228:231], %[va] offset:7168\n\t" \
    "ds_read_b128 v[248:251], %[wl] offset:7168\n\t" \
    "v_cvt_pk_bf16_f32 v209, v210, v211\n\t" \
    "v_mfma_f32_16x16x32_bf16 v[216:219], v[232:235], v[104:107], v[216:219]\n\t" \
    "v_cvt_pk_bf16_f32 v210, v212, v213\n\t" \
    "s_waitcnt lgkmcnt(5)\n\t" \
    "v_mfma_f32_16x16x32_bf16 v[204:207], v[236:239], v[120:123], v[204:207]\n\t" \
    "ds_read_b128 v[232:235], %[va] offset:4096\n\t" \
    "v_cvt_pk_bf16_f32 v211, v214, v215\n\t" \
    "v_mfma_f32_16x16x32_bf16 v[220:223], v[236:239], v[104:107], v[220:223]\n\t" \
    "buffer_store_dwordx4 v[208:211], %[l16], %[rd], %[so] offen sc1\n\t" \
    "s_waitcnt lgkmcnt(5)\n\t" \
    "v_mfma_f32_16x16x32_bf16 v[200:203], v[240:243], v[124:127], v[200:203]\n\t" \
    "ds_read_b128 v[236:239], %[va] offset:10240\n\t" \
    "s_add_u32 %[so], %[so], 0x8000\n\t" \
    "v_mfma_f32_16x16x32_bf16 v[216:219], v[240:243], v[108:111], v[216:219]\n\t" \
    "s_waitcnt lgkmcnt(5)\n\t" \
    "v_mfma_f32_16x16x32_bf16 v[204:207], v[244:247], v[124:127], v[204:207]\n\t" \
    "ds_read_b128 v[240:243], %[va] offset:5120\n\t" \
    "v_mfma_f32_16x16x32_bf16 v[220:223], v[244:247], v[108:111], v[220:223]\n\t" \
    "s_waitcnt lgkmcnt(3)\n\t" \
    "v_mfma_f32_16x16x32_bf16 v[200:203], v[224:227], v[248:251], v[200:203]\n\t" \
    "ds_read_b128 v[244:247], %[va] offset:11264\n\t" \
    "v_mfma_f32_16x16x32_bf16 v[208:211], v[224:227], v[140:143], 0\n\t" \
    "v_mfma_f32_16x16x32_bf16 v[204:207], v[228:231], v[248:251], v[204:207]\n\t" \
    "ds_read_b128 v[224:227], %[va] offset:0\n\t" \
    "v_mfma_f32_16x16x32_bf16 v[212:215], v[228:231], v[140:143], 0\n\t" \
    "v_cvt_pk_bf16_f32 v216, v216, v217\n\t" \
    "s_waitcnt lgkmcnt(4)\n\t" \
    "v_mfma_f32_16x16x32_bf16 v[200:203], v[232:235], v[128:131], v[200:203]\n\t" \
    "ds_read_b128 v[228:231], %[va] offset:6144\n\t" \
    "ds_read_b128 v[248:251], %[wl] offset:8192\n\t" \
    "v_cvt_pk_bf16_f32 v217, v218, v219\n\t" \
    "v_mfma_f32_16x16x32_bf16 v[208:211], v[232:235], v[152:155], v[208:211]\n\t" \
    "v_cvt_pk_bf16_f32 v218, v220, v221\n\t" \
    "s_waitcnt lgkmcnt(5)\n\t" \
    "v_mfma_f32_16x16x32_bf16 v[204:207], v[236:239], v[128:131], v[204:207]\n\t" \
    "ds_read_b128 v[232:235], %[va] offset:2048\n\t" \
    "v_cvt_pk_bf16_f32 v219, v222, v223\n\t" \
    "v_mfma_f32_16x16x32_bf16 v[212:215], v[236:239], v[152:155], v[212:215]\n\t" \
    "buffer_store_dwordx4 v[216:219], %[l16], %[rd], %[so] offen sc1\n\t" \
    "s_waitcnt lgkmcnt(5)\n\t" \
    "v_mfma_f32_16x16x32_bf16 v[200:203], v[240:243], v[132:135], v[200:203]\n\t" \
    "ds_read_b128 v[236:239], %[va] offset:8192\n\t" \
    "s_add_u32 %[so], %[so], 0x8000\n\t" \
    "v_mfma_f32_16x16x32_bf16 v[208:211], v[240:243], v[156:159], v[208:211]\n\t" \
    "s_waitcnt lgkmcnt(5)\n\t" \
    "v_mfma_f32_16x16x32_bf16 v[204:207], v[244:247], v[132:135], v[204:207]\n\t" \
    "ds_read_b128 v[240:243], %[va] offset:3072\n\t" \
    "v_mfma_f32_16x16x32_bf16 v[212:215], v[244:247], v[156:159], v[212:215]\n\t" \
    "s_waitcnt lgkmcnt(3)\n\t" \
    "v_mfma_f32_16x16x32_bf16 v[216:219], v[224:227], v[248:251], 0\n\t" \
    "ds_read_b128 v[244:247], %[va] offset:9216\n\t" \
    "v_mfma_f32_16x16x32_bf16 v[208:211], v[224:227], v[136:139], v[208:211]\n\t" \
    "v_mfma_f32_16x16x32_bf16 v[220:223], v[228:231], v[248:251], 0\n\t" \
    "ds_read_b128 v[224:227], %[va] offset:1024\n\t" \
    "v_mfma_f32_16x16x32_bf16 v[212:215], v[228:231], v[136:139], v[212:215]\n\t" \
    "v_cvt_pk_bf16_f32 v200, v200, v201\n\t" \
    "s_waitcnt lgkmcnt(4)\n\t" \
    "v_mfma_f32_16x16x32_bf16 v[216:219], v[232:235], v[160:163], v[216:219]\n\t" \
    "ds_read_b128 v[228:231], %[va] offset:7168\n\t" \
    "ds_read_b128 v[248:251], %[wl] offset:9216\n\t" \
    "v_cvt_pk_bf16_f32 v201, v202, v203\n\t" \
    "v_mfma_f32_16x16x32_bf16 v[208:211], v[232:235], v[144:147], v[208:211]\n\t" \
    "v_cvt_pk_bf16_f32 v202, v204, v205\n\t" \
    "s_waitcnt lgkmcnt(5)\n\t" \
    "v_mfma_f32_16x16x32_bf16 v[220:223], v[236:239], v[160:163], v[220:223]\n\t" \
    "ds_read_b128 v[232:235], %[va] offset:4096\n\t" \
    "v_cvt_pk_bf16_f32 v203, v206, v207\n\t" \
    "v_mfma_f32_16x16x32_bf16 v[212:215], v[236:239], v[144:147], v[212:215]\n\t" \
    "buffer_store_dwordx4 v[200:203], %[l16], %[rd], %[so] offen sc1\n\t" \
    "s_waitcnt lgkmcnt(5)\n\t" \
    "v_mfma_f32_16x16x32_bf16 v[216:219], v[240:243], v[164:167], v[216:219]\n\t" \
    "ds_read_b128 v[236:239], %[va] offset:10240\n\t" \
    "s_add_u32 %[so], %[so], 0x8000\n\t" \
    "v_mfma_f32_16x16x32_bf16 v[208:211], v[240:243], v[148:151], v[208:211]\n\t" \
    "s_waitcnt lgkmcnt(5)\n\t" \
    "v_mfma_f32_16x16x32_bf16 v[220:223], v[244:247], v[164:167], v[220:223]\n\t" \
    "ds_read_b128 v[240:243], %[va] offset:5120\n\t" \
    "v_mfma_f32_16x16x32_bf16 v[212:215], v[244:247], v[148:151], v[212:215]\n\t" \
    "s_waitcnt lgkmcnt(3)\n\t" \
    "v_mfma_f32_16x16x32_bf16 v[216:219], v[224:227], v[248:251], v[216:219]\n\t" \
    "ds_read_b128 v[244:247], %[va] offset:11264\n\t" \
    "v_mfma_f32_16x16x32_bf16 v[200:203], v[224:227], v[180:183], 0\n\t" \
    "v_mfma_f32_16x16x32_bf16 v[220:223], v[228:231], v[248:251], v[220:223]\n\t" \
    "ds_read_b128 v[224:227], %[va] offset:0\n\t" \
    "v_mfma_f32_16x16x32_bf16 v[204:207], v[228:231], v[180:183], 0\n\t" \
    "v_cvt_pk_bf16_f32 v208, v208, v209\n\t" \
    "s_waitcnt lgkmcnt(4)\n\t" \
    "v_mfma_f32_16x16x32_bf16 v[216:219], v[232:235], v[168:171], v[216:219]\n\t" \
    "ds_read_b128 v[228:231], %[va] offset:6144\n\t" \
    "v_cvt_pk_bf16_f32 v209, v210, v211\n\t" \
    "v_mfma_f32_16x16x32_bf16 v[200:203], v[232:235], v[192:195], v[200:203]\n\t" \
    "v_cvt_pk_bf16_f32 v210, v212, v213\n\t" \
    "s_waitcnt lgkmcnt(4)\n\t" \
    "v_mfma_f32_16x16x32_bf16 v[220:223], v[236:239], v[168:171], v[220:223]\n\t" \
    "ds_read_b128 v[232:235], %[va] offset:2048\n\t" \
    "v_cvt_pk_bf16_f32 v211, v214, v215\n\t" \
    "v_mfma_f32_16x16x32_bf16 v[204:207], v[236:239], v[192:195], v[204:207]\n\t" \
    "buffer_store_dwordx4 v[208:211], %[l16], %[rd], %[so] offen sc1\n\t" \
    "s_waitcnt lgkmcnt(4)\n\t" \
    "v_mfma_f32_16x16x32_bf16 v[216:219], v[240:243], v[172:175], v[216:219]\n\t" \
    "ds_read_b128 v[236:239], %[va] offset:8192\n\t" \
    "s_add_u32 %[so], %[so], 0x8000\n\t" \
    "v_mfma_f32_16x16x32_bf16 v[200:203], v[240:243], v[196:199], v[200:203]\n\t" \
    "s_waitcnt lgkmcnt(4)\n\t" \
    "v_mfma_f32_16x16x32_bf16 v[220:223], v[244:247], v[172:175], v[220:223]\n\t" \
    "ds_read_b128 v[240:243], %[va] offset:3072\n\t" \
    "v_mfma_f32_16x16x32_bf16 v[204:207], v[244:247], v[196:199], v[204:207]\n\t" \
    "s_barrier \n\t" \
    "s_waitcnt lgkmcnt(4)\n\t" \
    "v_mfma_f32_16x16x32_bf16 v[200:203], v[224:227], v[176:179], v[200:203]\n\t" \
    "ds_read_b128 v[244:247], %[va] offset:9216\n\t" \
    "s_waitcnt lgkmcnt(4)\n\t" \
    "v_mfma_f32_16x16x32_bf16 v[204:207], v[228:231], v[176:179], v[204:207]\n\t" \
    "s_waitcnt lgkmcnt(3)\n\t" \
    "v_mfma_f32_16x16x32_bf16 v[200:203], v[232:235], v[184:187], v[200:203]\n\t" \
    "s_waitcnt lgkmcnt(2)\n\t" \
    "v_mfma_f32_16x16x32_bf16 v[204:207], v[236:239], v[184:187], v[204:207]\n\t" \
    "v_cvt_pk_bf16_f32 v216, v216, v217\n\t" \
    "s_waitcnt lgkmcnt(1)\n\t" \
    "v_mfma_f32_16x16x32_bf16 v[200:203], v[240:243], v[188:191], v[200:203]\n\t" \
    "v_cvt_pk_bf16_f32 v217, v218, v219\n\t" \
    "s_waitcnt lgkmcnt(0)\n\t" \
    "v_mfma_f32_16x16x32_bf16 v[204:207], v[244:247], v[188:191], v[204:207]\n\t" \
    "v_cvt_pk_bf16_f32 v218, v220, v221\n\t" \
    "v_cvt_pk_bf16_f32 v219, v222, v223\n\t" \
    "buffer_store_dwordx4 v[216:219], %[l16], %[rd], %[so] offen sc1\n\t" \
    "s_add_u32 %[so], %[so], 0x8000\n\t" \
    "s_nop 0\n\t" \
    "v_cvt_pk_bf16_f32 v200, v200, v201\n\t" \
    "v_cvt_pk_bf16_f32 v201, v202, v203\n\t" \
    "s_nop 1\n\t" \
    "v_cvt_pk_bf16_f32 v202, v204, v205\n\t" \
    "v_cvt_pk_bf16_f32 v203, v206, v207\n\t" \
    "buffer_store_dwordx4 v[200:203], %[l16], %[rd], %[so] offen sc1\n\t" \
    "s_add_u32 %[so], %[so], 0x8000\n\t" \
    "s_nop 1\n\t"
#define ASM_CRIT2 \
    "s_waitcnt lgkmcnt(0)\n\t" \
    "s_barrier\n\t" ASM_ST \
    "ds_read_b128 v[204:207], v200 offset:11264\n\t" \
    "ds_read_b128 v[208:211], v200 offset:12288\n\t" \
    "ds_read_b128 v[200:203], v200 offset:10240\n\t" \
    "s_waitcnt lgkmcnt(2)\n\t" \
    "v_smfmac_f32_16x16x64_bf16 v[224:227], v[204:207], v[24:31], %[sidx]\n\t" \
    "v_smfmac_f32_16x16x64_bf16 v[220:223], v[204:207], v[0:7], %[sidx]\n\t" \
    "s_waitcnt lgkmcnt(1)\n\t" \
    "v_smfmac_f32_16x16x64_bf16 v[224:227], v[208:211], v[32:39], %[sidx]\n\t" \
    "v_smfmac_f32_16x16x64_bf16 v[220:223], v[208:211], v[8:15], %[sidx]\n\t" \
    "s_waitcnt lgkmcnt(0)\n\t" \
    "v_smfmac_f32_16x16x64_bf16 v[224:227], v[200:203], v[16:23], %[sidx]\n\t" \
    "v_smfmac_f32_16x16x64_bf16 v[220:223], v[200:203], v[212:219], %[sidx]\n\t" \
    "ds_read_b128 v[212:215], %[wl] offset:2048\n\t" \
    "ds_read_b128 v[216:219], %[wl] offset:3072\n\t" \
    "v_smfmac_f32_16x16x64_bf16 v[232:235], v[204:207], v[64:71], %[sidx]\n\t" \
    "v_smfmac_f32_16x16x64_bf16 v[228:231], v[204:207], v[40:47], %[sidx]\n\t" \
    "v_smfmac_f32_16x16x64_bf16 v[232:235], v[208:211], v[72:79], %[sidx]\n\t" \
    "v_permlane32_swap_b32 v220, v224\n\t" \
    "v_permlane32_swap_b32 v221, v225\n\t" \
    "v_permlane32_swap_b32 v222, v226\n\t" \
    "v_smfmac_f32_16x16x64_bf16 v[228:231], v[208:211], v[48:55], %[sidx]\n\t" \
    "v_permlane32_swap_b32 v223, v227\n\t" \
    "v_add_f32 v220, v220, v224\n\t" \
    "v_add_f32 v221, v221, v225\n\t" \
    "v_smfmac_f32_16x16x64_bf16 v[232:235], v[200:203], v[56:63], %[sidx]\n\t" \
    "v_add_f32 v222, v222, v226\n\t" \
    "v_add_f32 v223, v223, v227\n\t" \
    "v_cvt_pk_bf16_f32 v226, v220, v221\n\t" \
    "s_waitcnt lgkmcnt(0)\n\t" \
    "v_smfmac_f32_16x16x64_bf16 v[228:231], v[200:203], v[212:219], %[sidx]\n\t" \
    "ds_read_b128 v[212:215], %[wl] offset:4096\n\t" \
    "ds_read_b128 v[216:219], %[wl] offset:5120\n\t" \
    "v_cvt_pk_bf16_f32 v227, v222, v223\n\t" \
    "v_min_u32 v226, -2, v226\n\t" \
    "v_min_u32 v227, -2, v227\n\t" \
    "v_smfmac_f32_16x16x64_bf16 v[240:243], v[204:207], v[104:111], %[sidx]\n\t" \
    "v_smfmac_f32_16x16x64_bf16 v[236:239], v[204:207], v[80:87], %[sidx]\n\t" \
    "v_smfmac_f32_16x16x64_bf16 v[240:243], v[208:211], v[112:119], %[sidx]\n\t" \
    "v_permlane32_swap_b32 v228, v232\n\t" \
    "v_permlane32_swap_b32 v229, v233\n\t" \
    "v_permlane32_swap_b32 v230, v234\n\t" \
    "v_smfmac_f32_16x16x64_bf16 v[236:239], v[208:211], v[88:95], %[sidx]\n\t" \
    "v_permlane32_swap_b32 v231, v235\n\t" \
    "v_add_f32 v228, v228, v232\n\t" \
    "v_add_f32 v229, v229, v233\n\t" \
    "v_smfmac_f32_16x16x64_bf16 v[240:243], v[200:203], v[96:103], %[sidx]\n\t" \
    "v_add_f32 v230, v230, v234\n\t" \
    "v_add_f32 v231, v231, v235\n\t" \
    "v_cvt_pk_bf16_f32 v228, v228, v229\n\t" \
    "s_waitcnt lgkmcnt(0)\n\t" \
    "v_smfmac_f32_16x16x64_bf16 v[236:239], v[200:203], v[212:219], %[sidx]\n\t" \
    "ds_read_b128 v[212:215], %[wl] offset:6144\n\t" \
    "ds_read_b128 v[216:219], %[wl] offset:7168\n\t" \
    "v_cvt_pk_bf16_f32 v229, v230, v231\n\t" \
    "v_min_u32 v228, -2, v228\n\t" \
    "v_min_u32 v229, -2, v229\n\t" \
    "s_nop 1\n\t" \
    "v_permlane16_swap_b32 v226, v228\n\t" \
    "v_permlane16_swap_b32 v227, v229\n\t" \
    "buffer_store_dwordx4 v[226:229], %[vo], %[rr], %[so] offen sc1\n\t" \
    "s_add_u32 %[so], %[so], 0x8000\n\t" \
    "v_smfmac_f32_16x16x64_bf16 v[248:251], v[204:207], v[144:151], %[sidx]\n\t" \
    "v_mov_b64 v[220:221], 0\n\t" \
    "v_mov_b64 v[222:223], 0\n\t" \
    "v_smfmac_f32_16x16x64_bf16 v[244:247], v[204:207], v[120:127], %[sidx]\n\t" \
    "v_mov_b64 v[224:225], 0\n\t" \
    "v_mov_b64 v[226:227], 0\n\t" \
    "v_smfmac_f32_16x16x64_bf16 v[248:251], v[208:211], v[152:159], %[sidx]\n\t" \
    "v_permlane32_swap_b32 v236, v240\n\t" \
    "v_permlane32_swap_b32 v237, v241\n\t" \
    "v_permlane32_swap_b32 v238, v242\n\t" \
    "v_smfmac_f32_16x16x64_bf16 v[244:247], v[208:211], v[128:135], %[sidx]\n\t" \
    "v_permlane32_swap_b32 v239, v243\n\t" \
    "v_add_f32 v236, v236, v240\n\t" \
    "v_add_f32 v237, v237, v241\n\t" \
    "v_smfmac_f32_16x16x64_bf16 v[248:251], v[200:203], v[136:143], %[sidx]\n\t" \
    "v_add_f32 v238, v238, v242\n\t" \
    "v_add_f32 v239, v239, v243\n\t" \
    "v_cvt_pk_bf16_f32 v242, v236, v237\n\t" \
    "s_waitcnt lgkmcnt(0)\n\t" \
    "v_smfmac_f32_16x16x64_bf16 v[244:247], v[200:203], v[212:219], %[sidx]\n\t" \
    "ds_read_b128 v[212:215], %[wl] offset:8192\n\t" \
    "ds_read_b128 v[216:219], %[wl] offset:9216\n\t" \
    "v_cvt_pk_bf16_f32 v243, v238, v239\n\t" \
    "v_min_u32 v242, -2, v242\n\t" \
    "v_min_u32 v243, -2, v243\n\t" \
    "v_smfmac_f32_16x16x64_bf16 v[224:227], v[204:207], v[184:191], %[sidx]\n\t" \
    "v_and_b32 v232, 0x3fff, %[vo]\n\t" \
    "v_lshrrev_b32 v233, 14, %[vo]\n\t" \
    "v_smfmac_f32_16x16x64_bf16 v[220:223], v[204:207], v[160:167], %[sidx]\n\t" \
    "v_lshl_add_u32 v232, v233, 3, v232\n\t" \
    "v_permlane32_swap_b32 v244, v248\n\t" \
    "v_smfmac_f32_16x16x64_bf16 v[224:227], v[208:211], v[192:199], %[sidx]\n\t" \
    "v_permlane32_swap_b32 v245, v249\n\t" \
    "v_permlane32_swap_b32 v246, v250\n\t" \
    "v_permlane32_swap_b32 v247, v251\n\t" \
    "v_smfmac_f32_16x16x64_bf16 v[220:223], v[208:211], v[168:175], %[sidx]\n\t" \
    "v_add_f32 v244, v244, v248\n\t" \
    "v_add_f32 v245, v245, v249\n\t" \
    "v_add_f32 v246, v246, v250\n\t" \
    "v_smfmac_f32_16x16x64_bf16 v[224:227], v[200:203], v[176:183], %[sidx]\n\t" \
    "v_add_f32 v247, v247, v251\n\t" \
    "v_cvt_pk_bf16_f32 v244, v244, v245\n\t" \
    "v_cvt_pk_bf16_f32 v245, v246, v247\n\t" \
    "s_waitcnt lgkmcnt(0)\n\t" \
    "v_smfmac_f32_16x16x64_bf16 v[220:223], v[200:203], v[212:219], %[sidx]\n\t" \
    "v_min_u32 v244, -2, v244\n\t" \
    "v_min_u32 v245, -2, v245\n\t" \
    "s_nop 1\n\t" \
    "v_permlane16_swap_b32 v242, v244\n\t" \
    "v_permlane16_swap_b32 v243, v245\n\t" \
    "buffer_store_dwordx4 v[242:245], %[vo], %[rr], %[so] offen sc1\n\t" \
    "s_add_u32 %[so], %[so], 0x8000\n\t" \
    "s_nop 0\n\t" \
    "v_permlane32_swap_b32 v220, v224\n\t" \
    "v_permlane32_swap_b32 v221, v225\n\t" \
    "v_permlane32_swap_b32 v222, v226\n\t" \
    "v_permlane32_swap_b32 v223, v227\n\t" \
    "v_add_f32 v220, v220, v224\n\t" \
    "v_add_f32 v221, v221, v225\n\t" \
    "v_add_f32 v222, v222, v226\n\t" \
    "v_add_f32 v223, v223, v227\n\t" \
    "v_cvt_pk_bf16_f32 v220, v220, v221\n\t" \
    "v_cvt_pk_bf16_f32 v221, v222, v223\n\t" \
    "v_min_u32 v220, -2, v220\n\t" \
    "v_min_u32 v221, -2, v221\n\t" \
    "buffer_store_dwordx2 v[220:221], v232, %[rr], %[so] offen sc1\n\t" \
    "s_barrier \n\t"
#define ASM_P4_0 \
    "ds_read_b128 v[200:203], %[qa] offset:0\n\t" \
    "ds_read_b128 v[204:207], %[qa] offset:1024\n\t" \
    "ds_read_b128 v[208:211], %[qa] offset:2048\n\t" \
    "ds_read_b128 v[212:215], %[qa] offset:3072\n\t" \
    "ds_read_b128 v[216:219], %[qa] offset:4096\n\t" \
    "ds_read_b128 v[220:223], %[qa] offset:5120\n\t" \
    "ds_read_b128 v[224:227], %[qa] offset:12288\n\t" \
    "ds_read_b128 v[228:231], %[qa] offset:13312\n\t" \
    "ds_read_b128 v[232:235], %[qa] offset:14336\n\t" \
    "ds_read_b128 v[236:239], %[qa] offset:15360\n\t" \
    "s_waitcnt lgkmcnt(0)\n\t" \
    "s_barrier\n\t" ASM_ST \
    "v_mfma_f32_16x16x32_bf16 v[240:243], v[224:227], v[200:203], v[240:243]\n\t" \
    "ds_read_b128 v[224:227], %[qa] offset:16384\n\t" \
    "v_mfma_f32_16x16x32_bf16 v[240:243], v[228:231], v[204:207], v[240:243]\n\t" \
    "ds_read_b128 v[228:231], %[qa] offset:17408\n\t" \
    "v_mfma_f32_16x16x32_bf16 v[240:243], v[232:235], v[208:211], v[240:243]\n\t" \
    "ds_read_b128 v[232:235], %[km] offset:0\n\t" \
    "v_mfma_f32_16x16x32_bf16 v[240:243], v[236:239], v[212:215], v[240:243]\n\t" \
    "ds_read_b128 v[236:239], %[km] offset:512\n\t" \
    "s_waitcnt lgkmcnt(3)\n\t" \
    "v_mfma_f32_16x16x32_bf16 v[240:243], v[224:227], v[216:219], v[240:243]\n\t" \
    "ds_read_b128 v[224:227], %[km] offset:1024\n\t" \
    "s_waitcnt lgkmcnt(3)\n\t" \
    "v_mfma_f32_16x16x32_bf16 v[240:243], v[228:231], v[220:223], v[240:243]\n\t" \
    "ds_read_b128 v[228:231], %[km] offset:1536\n\t" \
    "s_waitcnt lgkmcnt(3)\n\t" \
    "v_mfma_f32_16x16x32_bf16 v[248:251], v[232:235], v[200:203], 0\n\t" \
    "ds_read_b128 v[232:235], %[km] offset:2048\n\t" \
    "s_waitcnt lgkmcnt(3)\n\t" \
    "v_mfma_f32_16x16x32_bf16 v[248:251], v[236:239], v[204:207], v[248:251]\n\t" \
    "ds_read_b128 v[236:239], %[km] offset:2560\n\t" \
    "s_waitcnt lgkmcnt(3)\n\t" \
    "v_mfma_f32_16x16x32_bf16 v[248:251], v[224:227], v[208:211], v[248:251]\n\t" \
    "s_waitcnt lgkmcnt(2)\n\t" \
    "v_mfma_f32_16x16x32_bf16 v[248:251], v[228:231], v[212:215], v[248:251]\n\t" \
    "s_waitcnt lgkmcnt(1)\n\t" \
    "v_mfma_f32_16x16x32_bf16 v[248:251], v[232:235], v[216:219], v[248:251]\n\t" \
    "s_waitcnt lgkmcnt(0)\n\t" \
    "v_mfma_f32_16x16x32_bf16 v[248:251], v[236:239], v[220:223], v[248:251]\n\t" \
    "v_max3_f32 v224, v240, v241, v242\n\t" \
    "v_mov_b32 v225, v243\n\t" \
    "v_lshlrev_b32 v201, 23, %[qa]\n\t" \
    "v_and_b32 v201, 0x80000000, v201\n\t" \
    "v_and_b32 v200, 0x3ff, %[qa]\n\t" \
    "v_or_b32 v200, v201, v200\n\t" \
    "s_nop 1\n\t" \
    "v_max3_f32 v224, v224, v248, v249\n\t" \
    "v_max3_f32 v225, v225, v250, v251\n\t" \
    "v_max_f32 v224, v224, v225\n\t" \
    "v_mov_b32 v225, v224\n\t" \
    "s_nop 1\n\t" \
    "v_permlane16_swap_b32 v224, v225\n\t" \
    "v_max_f32 v224, v224, v225\n\t" \
    "v_mov_b32 v225, v224\n\t" \
    "s_nop 1\n\t" \
    "v_permlane32_swap_b32 v224, v225\n\t" \
    "v_max_f32 v224, v224, v225\n\t" \
    "v_mul_f32 v224, %[scl], v224\n\t" \
    "v_fma_f32 v240, v240, %[scl], -v224\n\t" \
    "v_fma_f32 v241, v241, %[scl], -v224\n\t" \
    "v_exp_f32 v240, v240\n\t" \
    "v_fma_f32 v242, v242, %[scl], -v224\n\t" \
    "v_exp_f32 v241, v241\n\t" \
    "v_fma_f32 v243, v243, %[scl], -v224\n\t" \
    "v_exp_f32 v242, v242\n\t" \
    "v_fma_f32 v248, v248, %[scl], -v224\n\t" \
    "v_exp_f32 v243, v243\n\t" \
    "v_fma_f32 v249, v249, %[scl], -v224\n\t" \
    "v_exp_f32 v248, v248\n\t" \
    "v_fma_f32 v250, v250, %[scl], -v224\n\t" \
    "v_exp_f32 v249, v249\n\t" \
    "v_fma_f32 v251, v251, %[scl], -v224\n\t" \
    "v_exp_f32 v250, v250\n\t" \
    "v_exp_f32 v251, v251\n\t" \
    "v_add_f32 v224, v240, v241\n\t" \
    "v_add_f32 v225, v242, v243\n\t" \
    "v_add_f32 v232, v248, v249\n\t" \
    "v_add_f32 v233, v250, v251\n\t" \
    "v_add_f32 v224, v224, v225\n\t" \
    "v_add_f32 v232, v232, v233\n\t" \
    "v_fma_f32 v224, v232, 0.5, v224\n\t" \
    "v_mov_b32 v225, v224\n\t" \
    "s_nop 1\n\t" \
    "v_permlane16_swap_b32 v224, v225\n\t" \
    "v_add_f32 v224, v224, v225\n\t" \
    "v_mov_b32 v225, v224\n\t" \
    "s_nop 1\n\t" \
    "v_permlane32_swap_b32 v224, v225\n\t" \
    "v_add_f32 v224, v224, v225\n\t" \
    "v_rcp_f32 v224, v224\n\t" \
    "s_nop 0\n\t" \
    "v_mul_f32 v240, v240, v224\n\t" \
    "v_mul_f32 v241, v241, v224\n\t" \
    "v_mul_f32 v242, v242, v224\n\t" \
    "v_mul_f32 v243, v243, v224\n\t" \
    "v_mul_f32 v248, v248, v224\n\t" \
    "v_mul_f32 v249, v249, v224\n\t" \
    "v_mul_f32 v250, v250, v224\n\t" \
    "v_mul_f32 v251, v251, v224\n\t" \
    "v_cvt_pk_bf16_f32 v228, |v240|, |v241|\n\t" \
    "v_cvt_pk_bf16_f32 v229, |v242|, |v243|\n\t" \
    "v_cvt_pk_bf16_f32 v232, |v248|, |v249|\n\t" \
    "v_cvt_pk_bf16_f32 v233, |v250|, |v251|\n\t" \
    "v_permlane16_swap_b32 v228, v230\n\t" \
    "v_permlane16_swap_b32 v229, v231\n\t" \
    "buffer_store_dwordx4 v[228:231], v200, %[rr], %[so] offen sc1\n\t" \
    "buffer_store_dwordx2 v[232:233], %[pm], %[rr], %[so] offen sc1\n\t" \
    "s_nop 1\n\t"
#define ASM_P4_1 \
    "ds_read_b128 v[200:203], %[qa] offset:0\n\t" \
    "ds_read_b128 v[204:207], %[qa] offset:1024\n\t" \
    "ds_read_b128 v[208:211], %[qa] offset:2048\n\t" \
    "ds_read_b128 v[212:215], %[qa] offset:3072\n\t" \
    "ds_read_b128 v[216:219], %[qa] offset:4096\n\t" \
    "ds_read_b128 v[220:223], %[qa] offset:5120\n\t" \
    "ds_read_b128 v[224:227], %[qa] offset:12288\n\t" \
    "ds_read_b128 v[228:231], %[qa] offset:13312\n\t" \
    "ds_read_b128 v[232:235], %[qa] offset:14336\n\t" \
    "ds_read_b128 v[236:239], %[qa] offset:15360\n\t" \
    "s_waitcnt lgkmcnt(0)\n\t" \
    "s_barrier\n\t" ASM_ST \
    "v_mfma_f32_16x16x32_bf16 v[240:243], v[224:227], v[200:203], v[240:243]\n\t" \
    "ds_read_b128 v[224:227], %[qa] offset:16384\n\t" \
    "v_mfma_f32_16x16x32_bf16 v[240:243], v[228:231], v[204:207], v[240:243]\n\t" \
    "ds_read_b128 v[228:231], %[qa] offset:17408\n\t" \
    "v_mfma_f32_16x16x32_bf16 v[240:243], v[232:235], v[208:211], v[240:243]\n\t" \
    "ds_read_b128 v[232:235], %[qa] offset:6144\n\t" \
    "v_mfma_f32_16x16x32_bf16 v[240:243], v[236:239], v[212:215], v[240:243]\n\t" \
    "ds_read_b128 v[236:239], %[qa] offset:7168\n\t" \
    "s_waitcnt lgkmcnt(3)\n\t" \
    "v_mfma_f32_16x16x32_bf16 v[240:243], v[224:227], v[216:219], v[240:243]\n\t" \
    "ds_read_b128 v[224:227], %[qa] offset:8192\n\t" \
    "s_waitcnt lgkmcnt(3)\n\t" \
    "v_mfma_f32_16x16x32_bf16 v[240:243], v[228:231], v[220:223], v[240:243]\n\t" \
    "ds_read_b128 v[228:231], %[qa] offset:9216\n\t" \
    "s_waitcnt lgkmcnt(3)\n\t" \
    "v_mfma_f32_16x16x32_bf16 v[244:247], v[232:235], v[200:203], 0\n\t" \
    "ds_read_b128 v[232:235], %[qa] offset:10240\n\t" \
    "s_waitcnt lgkmcnt(3)\n\t" \
    "v_mfma_f32_16x16x32_bf16 v[244:247], v[236:239], v[204:207], v[244:247]\n\t" \
    "ds_read_b128 v[236:239], %[qa] offset:11264\n\t" \
    "s_waitcnt lgkmcnt(3)\n\t" \
    "v_mfma_f32_16x16x32_bf16 v[244:247], v[224:227], v[208:211], v[244:247]\n\t" \
    "ds_read_b128 v[224:227], %[km] offset:0\n\t" \
    "s_waitcnt lgkmcnt(3)\n\t" \
    "v_mfma_f32_16x16x32_bf16 v[244:247], v[228:231], v[212:215], v[244:247]\n\t" \
    "ds_read_b128 v[228:231], %[km] offset:512\n\t" \
    "s_waitcnt lgkmcnt(3)\n\t" \
    "v_mfma_f32_16x16x32_bf16 v[244:247], v[232:235], v[216:219], v[244:247]\n\t" \
    "ds_read_b128 v[232:235], %[km] offset:1024\n\t" \
    "s_waitcnt lgkmcnt(3)\n\t" \
    "v_mfma_f32_16x16x32_bf16 v[244:247], v[236:239], v[220:223], v[244:247]\n\t" \
    "ds_read_b128 v[236:239], %[km] offset:1536\n\t" \
    "s_waitcnt lgkmcnt(3)\n\t" \
    "v_mfma_f32_16x16x32_bf16 v[248:251], v[224:227], v[200:203], 0\n\t" \
    "ds_read_b128 v[224:227], %[km] offset:2048\n\t" \
    "s_waitcnt lgkmcnt(3)\n\t" \
    "v_mfma_f32_16x16x32_bf16 v[248:251], v[228:231], v[204:207], v[248:251]\n\t" \
    "ds_read_b128 v[228:231], %[km] offset:2560\n\t" \
    "s_waitcnt lgkmcnt(3)\n\t" \
    "v_mfma_f32_16x16x32_bf16 v[248:251], v[232:235], v[208:211], v[248:251]\n\t" \
    "s_waitcnt lgkmcnt(2)\n\t" \
    "v_mfma_f32_16x16x32_bf16 v[248:251], v[236:239], v[212:215], v[248:251]\n\t" \
    "s_waitcnt lgkmcnt(1)\n\t" \
    "v_mfma_f32_16x16x32_bf16 v[248:251], v[224:227], v[216:219], v[248:251]\n\t" \
    "s_waitcnt lgkmcnt(0)\n\t" \
    "v_mfma_f32_16x16x32_bf16 v[248:251], v[228:231], v[220:223], v[248:251]\n\t" \
    "v_max3_f32 v224, v240, v241, v242\n\t" \
    "v_max3_f32 v225, v244, v245, v246\n\t" \
    "v_max3_f32 v224, v224, v243, v247\n\t" \
    "v_and_b32 v200, 0x3ff, %[qa]\n\t" \
    "s_nop 3\n\t" \
    "v_max3_f32 v224, v224, v248, v249\n\t" \
    "v_max3_f32 v225, v225, v250, v251\n\t" \
    "v_max_f32 v224, v224, v225\n\t" \
    "v_mov_b32 v225, v224\n\t" \
    "s_nop 1\n\t" \
    "v_permlane16_swap_b32 v224, v225\n\t" \
    "v_max_f32 v224, v224, v225\n\t" \
    "v_mov_b32 v225, v224\n\t" \
    "s_nop 1\n\t" \
    "v_permlane32_swap_b32 v224, v225\n\t" \
    "v_max_f32 v224, v224, v225\n\t" \
    "v_mul_f32 v224, %[scl], v224\n\t" \
    "v_fma_f32 v240, v240, %[scl], -v224\n\t" \
    "v_fma_f32 v241, v241, %[scl], -v224\n\t" \
    "v_exp_f32 v240, v240\n\t" \
    "v_fma_f32 v242, v242, %[scl], -v224\n\t" \
    "v_exp_f32 v241, v241\n\t" \
    "v_fma_f32 v243, v243, %[scl], -v224\n\t" \
    "v_exp_f32 v242, v242\n\t" \
    "v_fma_f32 v244, v244, %[scl], -v224\n\t" \
    "v_exp_f32 v243, v243\n\t" \
    "v_fma_f32 v245, v245, %[scl], -v224\n\t" \
    "v_exp_f32 v244, v244\n\t" \
    "v_fma_f32 v246, v246, %[scl], -v224\n\t" \
    "v_exp_f32 v245, v245\n\t" \
    "v_fma_f32 v247, v247, %[scl], -v224\n\t" \
    "v_exp_f32 v246, v246\n\t" \
    "v_fma_f32 v248, v248, %[scl], -v224\n\t" \
    "v_exp_f32 v247, v247\n\t" \
    "v_fma_f32 v249, v249, %[scl], -v224\n\t" \
    "v_exp_f32 v248, v248\n\t" \
    "v_fma_f32 v250, v250, %[scl], -v224\n\t" \
    "v_exp_f32 v249, v249\n\t" \
    "v_fma_f32 v251, v251, %[scl], -v224\n\t" \
    "v_exp_f32 v250, v250\n\t" \
    "v_exp_f32 v251, v251\n\t" \
    "v_add_f32 v224, v240, v241\n\t" \
    "v_add_f32 v225, v242, v243\n\t" \
    "v_add_f32 v226, v244, v245\n\t" \
    "v_add_f32 v227, v246, v247\n\t" \
    "v_add_f32 v232, v248, v249\n\t" \
    "v_add_f32 v233, v250, v251\n\t" \
    "v_add_f32 v224, v224, v225\n\t" \
    "v_add_f32 v226, v226, v227\n\t" \
    "v_add_f32 v232, v232, v233\n\t" \
    "v_add_f32 v224, v224, v226\n\t" \
    "v_fma_f32 v224, v232, 0.5, v224\n\t" \
    "v_mov_b32 v225, v224\n\t" \
    "s_nop 1\n\t" \
    "v_permlane16_swap_b32 v224, v225\n\t" \
    "v_add_f32 v224, v224, v225\n\t" \
    "v_mov_b32 v225, v224\n\t" \
    "s_nop 1\n\t" \
    "v_permlane32_swap_b32 v224, v225\n\t" \
    "v_add_f32 v224, v224, v225\n\t" \
    "v_rcp_f32 v224, v224\n\t" \
    "s_nop 0\n\t" \
    "v_mul_f32 v240, v240, v224\n\t" \
    "v_mul_f32 v241, v241, v224\n\t" \
    "v_mul_f32 v242, v242, v224\n\t" \
    "v_mul_f32 v243, v243, v224\n\t" \
    "v_mul_f32 v244, v244, v224\n\t" \
    "v_mul_f32 v245, v245, v224\n\t" \
    "v_mul_f32 v246, v246, v224\n\t" \
    "v_mul_f32 v247, v247, v224\n\t" \
    "v_mul_f32 v248, v248, v224\n\t" \
    "v_mul_f32 v249, v249, v224\n\t" \
    "v_mul_f32 v250, v250, v224\n\t" \
    "v_mul_f32 v251, v251, v224\n\t" \
    "v_cvt_pk_bf16_f32 v230, |v240|, |v241|\n\t" \
    "v_cvt_pk_bf16_f32 v231, |v242|, |v243|\n\t" \
    "v_cvt_pk_bf16_f32 v228, |v244|, |v245|\n\t" \
    "v_cvt_pk_bf16_f32 v229, |v246|, |v247|\n\t" \
    "v_cvt_pk_bf16_f32 v232, |v248|, |v249|\n\t" \
    "v_cvt_pk_bf16_f32 v233, |v250|, |v251|\n\t" \
    "buffer_store_dwordx4 v[228:231], v200, %[rr], %[so] offen sc1\n\t" \
    "buffer_store_dwordx2 v[232:233], %[pm], %[rr], %[so] offen sc1\n\t" \
    "s_nop 1\n\t"
#define ASM_RCRIT \
    "s_movk_i32 %[n], 0x400\n\t" \
    ".Lfa%=:\n\t" \
    "v_max_u32 v196, v180, v181\n\t" \
    "v_max3_u32 v196, v182, v183, v196\n\t" \
    "v_cmp_eq_u32 vcc, -1, v196\n\t" \
    "s_cbranch_vccz .Lha%=\n\t" \
    "buffer_load_dwordx4 v[180:183], %[ov], %[rs], %[sv] offen sc1\n\t" \
    "s_waitcnt vmcnt(12)\n\t" \
    "v_max_u32 v196, v180, v181\n\t" \
    "v_max3_u32 v196, v182, v183, v196\n\t" \
    "v_cmp_eq_u32 vcc, -1, v196\n\t" \
    "s_cbranch_vccz .Lha%=\n\t" \
    "s_nop 7\n\t" \
    "v_max_u32 v196, v180, v181\n\t" \
    "v_max3_u32 v196, v182, v183, v196\n\t" \
    "v_cmp_eq_u32 vcc, -1, v196\n\t" \
    "s_cbranch_vccz .Lha%=\n\t" \
    "s_nop 7\n\t" \
    "v_max_u32 v196, v180, v181\n\t" \
    "v_max3_u32 v196, v182, v183, v196\n\t" \
    "v_cmp_eq_u32 vcc, -1, v196\n\t" \
    "s_cbranch_vccz .Lha%=\n\t" \
    "s_nop 7\n\t" \
    "v_max_u32 v196, v180, v181\n\t" \
    "v_max3_u32 v196, v182, v183, v196\n\t" \
    "v_cmp_eq_u32 vcc, -1, v196\n\t" \
    "s_cbranch_vccz .Lha%=\n\t" \
    "s_nop 7\n\t" \
    "s_sub_u32 %[n], %[n], 1\n\t" \
    "s_cmp_lg_u32 %[n], 0\n\t" \
    "s_cbranch_scc1 .Lfa%=\n\t" \
    ".Lha%=:\n\t" \
    "s_cmp_eq_u32 %[n], 0\n\t" \
    "s_cbranch_scc1 .Lre%=\n\t" \
    ".Lfb%=:\n\t" \
    "s_mov_b32 %[a], 0\n\t" \
    "v_max_u32 v196, v184, v185\n\t" \
    "v_max3_u32 v196, v186, v187, v196\n\t" \
    "v_cmp_eq_u32 vcc, -1, v196\n\t" \
    "s_cbranch_vccz .Lgb0_%=\n\t" \
    "buffer_load_dwordx4 v[184:187], %[o0], %[rs], %[sp] offen sc1\n\t" \
    "s_mov_b32 %[a], 1\n\t" \
    ".Lgb0_%=:\n\t" \
    "v_max_u32 v196, v188, v189\n\t" \
    "v_max3_u32 v196, v190, v191, v196\n\t" \
    "v_cmp_eq_u32 vcc, -1, v196\n\t" \
    "s_cbranch_vccz .Lgb1_%=\n\t" \
    "buffer_load_dwordx4 v[188:191], %[o1], %[rs], %[sp] offen sc1\n\t" \
    "s_mov_b32 %[a], 1\n\t" \
    ".Lgb1_%=:\n\t" \
    "v_max_u32 v196, v192, v193\n\t" \
    "v_max3_u32 v196, v194, v195, v196\n\t" \
    "v_cmp_eq_u32 vcc, -1, v196\n\t" \
    "s_cbranch_vccz .Lgb2_%=\n\t" \
    "buffer_load_dwordx4 v[192:195], %[o2], %[rs], %[sp] offen sc1\n\t" \
    "s_mov_b32 %[a], 1\n\t" \
    ".Lgb2_%=:\n\t" \
    "s_cmp_eq_u32 %[a], 0\n\t" \
    "s_cbranch_scc1 .Lhb%=\n\t" \
    "s_waitcnt vmcnt(12)\n\t" \
    "v_max_u32 v196, v184, v185\n\t" \
    "v_max3_u32 v196, v186, v187, v196\n\t" \
    "v_max3_u32 v196, v188, v189, v196\n\t" \
    "v_max3_u32 v196, v190, v191, v196\n\t" \
    "v_max3_u32 v196, v192, v193, v196\n\t" \
    "v_max3_u32 v196, v194, v195, v196\n\t" \
    "v_cmp_eq_u32 vcc, -1, v196\n\t" \
    "s_cbranch_vccz .Lhb%=\n\t" \
    "s_nop 7\n\t" \
    "v_max_u32 v196, v184, v185\n\t" \
    "v_max3_u32 v196, v186, v187, v196\n\t" \
    "v_max3_u32 v196, v188, v189, v196\n\t" \
    "v_max3_u32 v196, v190, v191, v196\n\t" \
    "v_max3_u32 v196, v192, v193, v196\n\t" \
    "v_max3_u32 v196, v194, v195, v196\n\t" \
    "v_cmp_eq_u32 vcc, -1, v196\n\t" \
    "s_cbranch_vccz .Lhb%=\n\t" \
    "s_nop 7\n\t" \
    "v_max_u32 v196, v184, v185\n\t" \
    "v_max3_u32 v196, v186, v187, v196\n\t" \
    "v_max3_u32 v196, v188, v189, v196\n\t" \
    "v_max3_u32 v196, v190, v191, v196\n\t" \
    "v_max3_u32 v196, v192, v193, v196\n\t" \
    "v_max3_u32 v196, v194, v195, v196\n\t" \
    "v_cmp_eq_u32 vcc, -1, v196\n\t" \
    "s_cbranch_vccz .Lhb%=\n\t" \
    "s_nop 7\n\t" \
    "v_max_u32 v196, v184, v185\n\t" \
    "v_max3_u32 v196, v186, v187, v196\n\t" \
    "v_max3_u32 v196, v188, v189, v196\n\t" \
    "v_max3_u32 v196, v190, v191, v196\n\t" \
    "v_max3_u32 v196, v192, v193, v196\n\t" \
    "v_max3_u32 v196, v194, v195, v196\n\t" \
    "v_cmp_eq_u32 vcc, -1, v196\n\t" \
    "s_cbranch_vccz .Lhb%=\n\t" \
    "s_nop 7\n\t" \
    "v_max_u32 v196, v184, v185\n\t" \
    "v_max3_u32 v196, v186, v187, v196\n\t" \
    "v_max3_u32 v196, v188, v189, v196\n\t" \
    "v_max3_u32 v196, v190, v191, v196\n\t" \
    "v_max3_u32 v196, v192, v193, v196\n\t" \
    "v_max3_u32 v196, v194, v195, v196\n\t" \
    "v_cmp_eq_u32 vcc, -1, v196\n\t" \
    "s_cbranch_vccz .Lhb%=\n\t" \
    "s_nop 7\n\t" \
    "v_max_u32 v196, v184, v185\n\t" \
    "v_max3_u32 v196, v186, v187, v196\n\t" \
    "v_max3_u32 v196, v188, v189, v196\n\t" \
    "v_max3_u32 v196, v190, v191, v196\n\t" \
    "v_max3_u32 v196, v192, v193, v196\n\t" \
    "v_max3_u32 v196, v194, v195, v196\n\t" \
    "v_cmp_eq_u32 vcc, -1, v196\n\t" \
    "s_cbranch_vccz .Lhb%=\n\t" \
    "s_nop 7\n\t" \
    "v_max_u32 v196, v184, v185\n\t" \
    "v_max3_u32 v196, v186, v187, v196\n\t" \
    "v_max3_u32 v196, v188, v189, v196\n\t" \
    "v_max3_u32 v196, v190, v191, v196\n\t" \
    "v_max3_u32 v196, v192, v193, v196\n\t" \
    "v_max3_u32 v196, v194, v195, v196\n\t" \
    "v_cmp_eq_u32 vcc, -1, v196\n\t" \
    "s_cbranch_vccz .Lhb%=\n\t" \
    "s_nop 7\n\t" \
    "v_max_u32 v196, v184, v185\n\t" \
    "v_max3_u32 v196, v186, v187, v196\n\t" \
    "v_max3_u32 v196, v188, v189, v196\n\t" \
    "v_max3_u32 v196, v190, v191, v196\n\t" \
    "v_max3_u32 v196, v192, v193, v196\n\t" \
    "v_max3_u32 v196, v194, v195, v196\n\t" \
    "v_cmp_eq_u32 vcc, -1, v196\n\t" \
    "s_cbranch_vccz .Lhb%=\n\t" \
    "s_nop 7\n\t" \
    "s_sub_u32 %[n], %[n], 1\n\t" \
    "s_cmp_lg_u32 %[n], 0\n\t" \
    "s_cbranch_scc1 .Lfb%=\n\t" \
    "s_branch .Lre%=\n\t" \
    ".Lhb%=:\n\t" \
    ASM_ST2 \
    "v_mfma_f32_16x16x32_bf16 v[176:179], v[192:195], v[180:183], 0\n\t" \
    "v_smfmac_f32_16x16x64_bf16 v[176:179], v[184:187], v[160:167], %[sidx]\n\t" \
    "v_smfmac_f32_16x16x64_bf16 v[176:179], v[188:191], v[168:175], %[sidx]\n\t" \
    "s_cmp_eq_u32 %[pub], 0\n\t" \
    "s_nop 7\n\t" \
    "v_permlane32_swap_b32 v176, v178\n\t" \
    "v_permlane32_swap_b32 v177, v179\n\t" \
    "v_add_f32 v176, v176, v178\n\t" \
    "v_add_f32 v177, v177, v179\n\t" \
    "ds_write2_b32 %[rw], v176, v177 offset1:80\n\t" \
    "s_waitcnt lgkmcnt(0)\n\t" \
    "s_barrier\n\t" ASM_ST \
    "s_cbranch_scc1 .Lre%=\n\t" \
    "ds_read_b128 v[200:203], %[rd] offset:0\n\t" \
    "ds_read_b128 v[204:207], %[rd] offset:16\n\t" \
    "ds_read_b128 v[208:211], %[rd] offset:32\n\t" \
    "ds_read_b128 v[212:215], %[rd] offset:48\n\t" \
    "s_waitcnt lgkmcnt(2)\n\t" \
    "v_pk_add_f32 v[200:201], v[200:201], v[202:203]\n\t" \
    "v_pk_add_f32 v[204:205], v[204:205], v[206:207]\n\t" \
    "s_waitcnt lgkmcnt(0)\n\t" \
    "v_pk_add_f32 v[208:209], v[208:209], v[210:211]\n\t" \
    "v_pk_add_f32 v[212:213], v[212:213], v[214:215]\n\t" \
    "v_pk_add_f32 v[200:201], v[200:201], v[204:205]\n\t" \
    "v_pk_add_f32 v[208:209], v[208:209], v[212:213]\n\t" \
    "v_pk_add_f32 v[200:201], v[200:201], v[208:209]\n\t" \
    "v_cvt_pk_bf16_f32 v200, v200, v201\n\t" \
    "v_min_u32 v200, -2, v200\n\t" \
    "buffer_store_dword v200, %[oo], %[rs], %[so] offen sc1\n\t" \
    RPUB2 \
    ".Lre%=:\n\t"
#define ASM_RX \
    "s_cmp_eq_u32 %[fin], 0\n\t" \
    "s_cbranch_scc1 .Lx0%=\n\t" \
    "buffer_load_dwordx2 v[250:251], %[xo], %[rd], %[sx] offen sc1\n\t" \
    ".Lx0%=:\n\t" \
    "buffer_load_dwordx4 v[200:203], %[l16], %[rs], %[s0] offen offset:0 sc1\n\t" \
    "buffer_load_dwordx4 v[204:207], %[l16], %[rs], %[s0] offen offset:1024 sc1\n\t" \
    "buffer_load_dwordx4 v[208:211], %[l16], %[rs], %[s0] offen offset:2048 sc1\n\t" \
    "buffer_load_dwordx4 v[212:215], %[l16], %[rs], %[s0] offen offset:3072 sc1\n\t" \
    "buffer_load_dwordx4 v[216:219], %[l16], %[rs], %[s1] offen offset:0 sc1\n\t" \
    "buffer_load_dwordx4 v[220:223], %[l16], %[rs], %[s1] offen offset:1024 sc1\n\t" \
    "buffer_load_dwordx4 v[224:227], %[l16], %[rs], %[s1] offen offset:2048 sc1\n\t" \
    "buffer_load_dwordx4 v[228:231], %[l16], %[rs], %[s1] offen offset:3072 sc1\n\t" \
    "buffer_load_dword v248, %[fo], %[rf], %[fs] offen sc1\n\t" \
    "v_mov_b64 v[232:233], v[160:161]\n\t" \
    "v_mov_b64 v[234:235], v[164:165]\n\t" \
    "v_mov_b64 v[236:237], v[168:169]\n\t" \
    "v_mov_b64 v[238:239], v[172:173]\n\t" \
    "s_movk_i32 %[n], 0x200\n\t" \
    "s_nop 1\n\t" \
    "v_permlane16_swap_b32 v232, v234\n\t" \
    "v_permlane16_swap_b32 v233, v235\n\t" \
    "v_permlane16_swap_b32 v236, v238\n\t" \
    "v_permlane16_swap_b32 v237, v239\n\t" \
    "s_waitcnt vmcnt(8)\n\t" \
    "v_max_u32 v196, v200, v201\n\t" \
    "v_max3_u32 v196, v202, v203, v196\n\t" \
    "s_waitcnt vmcnt(7)\n\t" \
    "v_max3_u32 v196, v204, v205, v196\n\t" \
    "v_max3_u32 v196, v206, v207, v196\n\t" \
    "s_waitcnt vmcnt(6)\n\t" \
    "v_max3_u32 v196, v208, v209, v196\n\t" \
    "v_max3_u32 v196, v210, v211, v196\n\t" \
    "s_waitcnt vmcnt(5)\n\t" \
    "v_max3_u32 v196, v212, v213, v196\n\t" \
    "v_max3_u32 v196, v214, v215, v196\n\t" \
    "s_waitcnt vmcnt(4)\n\t" \
    "v_max3_u32 v196, v216, v217, v196\n\t" \
    "v_max3_u32 v196, v218, v219, v196\n\t" \
    "s_waitcnt vmcnt(3)\n\t" \
    "v_max3_u32 v196, v220, v221, v196\n\t" \
    "v_max3_u32 v196, v222, v223, v196\n\t" \
    "s_waitcnt vmcnt(2)\n\t" \
    "v_max3_u32 v196, v224, v225, v196\n\t" \
    "v_max3_u32 v196, v226, v227, v196\n\t" \
    "s_waitcnt vmcnt(1)\n\t" \
    "v_max3_u32 v196, v228, v229, v196\n\t" \
    "v_max3_u32 v196, v230, v231, v196\n\t" \
    "v_cmp_eq_u32 vcc, -1, v196\n\t" \
    "s_cbranch_vccz .Lxd%=\n\t" \
    ".Lxl%=:\n\t" \
    "s_mov_b32 %[a], 0\n\t" \
    "v_max_u32 v196, v200, v201\n\t" \
    "v_max3_u32 v196, v202, v203, v196\n\t" \
    "v_cmp_eq_u32 vcc, -1, v196\n\t" \
    "s_cbranch_vccz .Lxs0_%=\n\t" \
    "buffer_load_dwordx4 v[200:203], %[l16], %[rs], %[s0] offen offset:0 sc1\n\t" \
    "s_mov_b32 %[a], 1\n\t" \
    ".Lxs0_%=:\n\t" \
    "v_max_u32 v196, v204, v205\n\t" \
    "v_max3_u32 v196, v206, v207, v196\n\t" \
    "v_cmp_eq_u32 vcc, -1, v196\n\t" \
    "s_cbranch_vccz .Lxs1_%=\n\t" \
    "buffer_load_dwordx4 v[204:207], %[l16], %[rs], %[s0] offen offset:1024 sc1\n\t" \
    "s_mov_b32 %[a], 1\n\t" \
    ".Lxs1_%=:\n\t" \
    "v_max_u32 v196, v208, v209\n\t" \
    "v_max3_u32 v196, v210, v211, v196\n\t" \
    "v_cmp_eq_u32 vcc, -1, v196\n\t" \
    "s_cbranch_vccz .Lxs2_%=\n\t" \
    "buffer_load_dwordx4 v[208:211], %[l16], %[rs], %[s0] offen offset:2048 sc1\n\t" \
    "s_mov_b32 %[a], 1\n\t" \
    ".Lxs2_%=:\n\t" \
    "v_max_u32 v196, v212, v213\n\t" \
    "v_max3_u32 v196, v214, v215, v196\n\t" \
    "v_cmp_eq_u32 vcc, -1, v196\n\t" \
    "s_cbranch_vccz .Lxs3_%=\n\t" \
    "buffer_load_dwordx4 v[212:215], %[l16], %[rs], %[s0] offen offset:3072 sc1\n\t" \
    "s_mov_b32 %[a], 1\n\t" \
    ".Lxs3_%=:\n\t" \
    "v_max_u32 v196, v216, v217\n\t" \
    "v_max3_u32 v196, v218, v219, v196\n\t" \
    "v_cmp_eq_u32 vcc, -1, v196\n\t" \
    "s_cbranch_vccz .Lxs4_%=\n\t" \
    "buffer_load_dwordx4 v[216:219], %[l16], %[rs], %[s1] offen offset:0 sc1\n\t" \
    "s_mov_b32 %[a], 1\n\t" \
    ".Lxs4_%=:\n\t" \
    "v_max_u32 v196, v220, v221\n\t" \
    "v_max3_u32 v196, v222, v223, v196\n\t" \
    "v_cmp_eq_u32 vcc, -1, v196\n\t" \
    "s_cbranch_vccz .Lxs5_%=\n\t" \
    "buffer_load_dwordx4 v[220:223], %[l16], %[rs], %[s1] offen offset:1024 sc1\n\t" \
    "s_mov_b32 %[a], 1\n\t" \
    ".Lxs5_%=:\n\t" \
    "v_max_u32 v196, v224, v225\n\t" \
    "v_max3_u32 v196, v226, v227, v196\n\t" \
    "v_cmp_eq_u32 vcc, -1, v196\n\t" \
    "s_cbranch_vccz .Lxs6_%=\n\t" \
    "buffer_load_dwordx4 v[224:227], %[l16], %[rs], %[s1] offen offset:2048 sc1\n\t" \
    "s_mov_b32 %[a], 1\n\t" \
    ".Lxs6_%=:\n\t" \
    "v_max_u32 v196, v228, v229\n\t" \
    "v_max3_u32 v196, v230, v231, v196\n\t" \
    "v_cmp_eq_u32 vcc, -1, v196\n\t" \
    "s_cbranch_vccz .Lxs7_%=\n\t" \
    "buffer_load_dwordx4 v[228:231], %[l16], %[rs], %[s1] offen offset:3072 sc1\n\t" \
    "s_mov_b32 %[a], 1\n\t" \
    ".Lxs7_%=:\n\t" \
    "s_cmp_eq_u32 %[a], 0\n\t" \
    "s_cbranch_scc1 .Lxd%=\n\t" \
    "s_waitcnt vmcnt(0)\n\t" \
    "s_sub_u32 %[n], %[n], 1\n\t" \
    "s_cmp_lg_u32 %[n], 0\n\t" \
    "s_cbranch_scc1 .Lxl%=\n\t" \
    "s_branch .Lxe%=\n\t" \
    ".Lxd%=:\n\t" \
    ASM_ST2 \
    "v_mfma_f32_16x16x32_bf16 v[244:247], v[160:163], v[200:203], 0\n\t" \
    "v_mfma_f32_16x16x32_bf16 v[244:247], v[164:167], v[204:207], v[244:247]\n\t" \
    "v_mfma_f32_16x16x32_bf16 v[244:247], v[168:171], v[208:211], v[244:247]\n\t" \
    "v_mfma_f32_16x16x32_bf16 v[244:247], v[172:175], v[212:215], v[244:247]\n\t" \
    "v_mfma_f32_16x16x32_bf16 v[240:243], v[232:235], v[220:223], 0\n\t" \
    "v_mfma_f32_16x16x32_bf16 v[244:247], v[180:183], v[216:219], v[244:247]\n\t" \
    "v_mfma_f32_16x16x32_bf16 v[240:243], v[236:239], v[224:227], v[240:243]\n\t" \
    "v_mfma_f32_16x16x32_bf16 v[240:243], v[180:183], v[228:231], v[240:243]\n\t" \
    "s_waitcnt vmcnt(0)\n\t" \
    "v_cmp_ge_u32 vcc, %[tn], v248\n\t" \
    "s_mov_b32 %[a], 0\n\t" \
    "s_cbranch_vccnz .Lxv%=\n\t" \
    "buffer_load_dwordx4 v[160:163], %[l16], %[rd], %[sv] offen offset:0 sc1\n\t" \
    "buffer_load_dwordx4 v[164:167], %[l16], %[rd], %[sv] offen offset:1024 sc1\n\t" \
    "buffer_load_dwordx4 v[168:171], %[l16], %[rd], %[sv] offen offset:2048 sc1\n\t" \
    "buffer_load_dwordx4 v[172:175], %[l16], %[rd], %[sv] offen offset:3072 sc1\n\t" \
    "s_mov_b32 %[a], 1\n\t" \
    ".Lxv%=:\n\t" \
    "s_nop 7\n\t" \
    "ds_write_b128 %[pw], v[244:247] offset:8192\n\t" \
    "ds_write_b128 %[pw], v[240:243]\n\t" \
    "s_cmp_eq_u32 %[fin], 0\n\t" \
    "s_waitcnt lgkmcnt(0)\n\t" \
    "s_barrier\n\t" ASM_ST \
    "s_cbranch_scc1 .Lxe%=\n\t" \
    "ds_read_b128 v[200:203], %[pr] offset:0\n\t" \
    "ds_read_b128 v[204:207], %[pr] offset:1024\n\t" \
    "ds_read_b128 v[208:211], %[pr] offset:2048\n\t" \
    "ds_read_b128 v[212:215], %[pr] offset:3072\n\t" \
    "ds_read_b128 v[216:219], %[pr] offset:4096\n\t" \
    "ds_read_b128 v[220:223], %[pr] offset:5120\n\t" \
    "ds_read_b128 v[224:227], %[pr] offset:6144\n\t" \
    "ds_read_b128 v[228:231], %[pr] offset:7168\n\t" \
    "v_lshlrev_b32 v240, 16, v250\n\t" \
    "v_and_b32 v241, 0xffff0000, v250\n\t" \
    "v_lshlrev_b32 v242, 16, v251\n\t" \
    "v_and_b32 v243, 0xffff0000, v251\n\t" \
    "s_waitcnt lgkmcnt(6)\n\t" \
    "v_pk_add_f32 v[200:201], v[200:201], v[204:205]\n\t" \
    "v_pk_add_f32 v[202:203], v[202:203], v[206:207]\n\t" \
    "s_waitcnt lgkmcnt(4)\n\t" \
    "v_pk_add_f32 v[208:209], v[208:209], v[212:213]\n\t" \
    "v_pk_add_f32 v[210:211], v[210:211], v[214:215]\n\t" \
    "v_pk_add_f32 v[200:201], v[200:201], v[208:209]\n\t" \
    "v_pk_add_f32 v[202:203], v[202:203], v[210:211]\n\t" \
    "s_waitcnt lgkmcnt(2)\n\t" \
    "v_pk_add_f32 v[216:217], v[216:217], v[220:221]\n\t" \
    "v_pk_add_f32 v[218:219], v[218:219], v[222:223]\n\t" \
    "s_waitcnt lgkmcnt(0)\n\t" \
    "v_pk_add_f32 v[224:225], v[224:225], v[228:229]\n\t" \
    "v_pk_add_f32 v[226:227], v[226:227], v[230:231]\n\t" \
    "v_pk_add_f32 v[216:217], v[216:217], v[224:225]\n\t" \
    "v_pk_add_f32 v[218:219], v[218:219], v[226:227]\n\t" \
    "v_pk_add_f32 v[200:201], v[200:201], v[216:217]\n\t" \
    "v_pk_add_f32 v[202:203], v[202:203], v[218:219]\n\t" \
    "v_pk_add_f32 v[200:201], v[200:201], v[240:241]\n\t" \
    "v_pk_add_f32 v[202:203], v[202:203], v[242:243]\n\t" \
    "v_cvt_pk_bf16_f32 v200, v200, v201\n\t" \
    "v_cvt_pk_bf16_f32 v201, v202, v203\n\t" \
    "v_min_u32 v200, -2, v200\n\t" \
    "v_min_u32 v201, -2, v201\n\t" \
    "s_add_u32 %[sx], %[sx], 0x500000\n\t" \
    "buffer_store_dwordx2 v[200:201], %[xo], %[rd], %[sx] offen sc1\n\t" \
    ".Lxe%=:\n\t"
#define ASM_MPOLLV3 \
    "s_movk_i32 %[n], 0x400\n\t" \
    ".Lfm%=:\n\t" \
    "s_mov_b32 %[a], 0\n\t" \
    "v_max_u32 v252, v200, v201\n\t" \
    "v_max3_u32 v252, v202, v203, v252\n\t" \
    "v_cmp_eq_u32 vcc, -1, v252\n\t" \
    "s_cbranch_vccz .Lgm0_%=\n\t" \
    "buffer_load_dwordx4 v[200:203], %[l16], %[rs], %[s0] offen sc1\n\t" \
    "s_mov_b32 %[a], 1\n\t" \
    ".Lgm0_%=:\n\t" \
    "v_max_u32 v252, v204, v205\n\t" \
    "v_max3_u32 v252, v206, v207, v252\n\t" \
    "v_cmp_eq_u32 vcc, -1, v252\n\t" \
    "s_cbranch_vccz .Lgm1_%=\n\t" \
    "buffer_load_dwordx4 v[204:207], %[l16], %[rs], %[s1] offen sc1\n\t" \
    "s_mov_b32 %[a], 1\n\t" \
    ".Lgm1_%=:\n\t" \
    "v_max_u32 v252, v208, v209\n\t" \
    "v_max3_u32 v252, v210, v211, v252\n\t" \
    "v_cmp_eq_u32 vcc, -1, v252\n\t" \
    "s_cbranch_vccz .Lgm2_%=\n\t" \
    "buffer_load_dwordx4 v[208:211], %[l16], %[rs], %[s2] offen sc1\n\t" \
    "s_mov_b32 %[a], 1\n\t" \
    ".Lgm2_%=:\n\t" \
    "s_cmp_eq_u32 %[a], 0\n\t" \
    "s_cbranch_scc1 .Lhm%=\n\t" \
    "s_waitcnt vmcnt(12)\n\t" \
    "v_max_u32 v252, v200, v201\n\t" \
    "v_max3_u32 v252, v202, v203, v252\n\t" \
    "v_max3_u32 v252, v204, v205, v252\n\t" \
    "v_max3_u32 v252, v206, v207, v252\n\t" \
    "v_max3_u32 v252, v208, v209, v252\n\t" \
    "v_max3_u32 v252, v210, v211, v252\n\t" \
    "v_cmp_eq_u32 vcc, -1, v252\n\t" \
    "s_cbranch_vccz .Lhm%=\n\t" \
    "s_nop 7\n\t" \
    "v_max_u32 v252, v200, v201\n\t" \
    "v_max3_u32 v252, v202, v203, v252\n\t" \
    "v_max3_u32 v252, v204, v205, v252\n\t" \
    "v_max3_u32 v252, v206, v207, v252\n\t" \
    "v_max3_u32 v252, v208, v209, v252\n\t" \
    "v_max3_u32 v252, v210, v211, v252\n\t" \
    "v_cmp_eq_u32 vcc, -1, v252\n\t" \
    "s_cbranch_vccz .Lhm%=\n\t" \
    "s_nop 7\n\t" \
    "s_sub_u32 %[n], %[n], 1\n\t" \
    "s_cmp_lg_u32 %[n], 0\n\t" \
    "s_cbranch_scc1 .Lfm%=\n\t" \
    ".Lhm%=:\n\t"
#define ASM_MPOLLV2 \
    "s_movk_i32 %[n], 0x400\n\t" \
    ".Lfm%=:\n\t" \
    "s_mov_b32 %[a], 0\n\t" \
    "v_max_u32 v252, v200, v201\n\t" \
    "v_max3_u32 v252, v202, v203, v252\n\t" \
    "v_cmp_eq_u32 vcc, -1, v252\n\t" \
    "s_cbranch_vccz .Lgm0_%=\n\t" \
    "buffer_load_dwordx4 v[200:203], %[l16], %[rs], %[s0] offen sc1\n\t" \
    "s_mov_b32 %[a], 1\n\t" \
    ".Lgm0_%=:\n\t" \
    "v_max_u32 v252, v204, v205\n\t" \
    "v_max3_u32 v252, v206, v207, v252\n\t" \
    "v_cmp_eq_u32 vcc, -1, v252\n\t" \
    "s_cbranch_vccz .Lgm1_%=\n\t" \
    "buffer_load_dwordx4 v[204:207], %[l16], %[rs], %[s1] offen sc1\n\t" \
    "s_mov_b32 %[a], 1\n\t" \
    ".Lgm1_%=:\n\t" \
    "s_cmp_eq_u32 %[a], 0\n\t" \
    "s_cbranch_scc1 .Lhm%=\n\t" \
    "s_waitcnt vmcnt(12)\n\t" \
    "v_max_u32 v252, v200, v201\n\t" \
    "v_max3_u32 v252, v202, v203, v252\n\t" \
    "v_max3_u32 v252, v204, v205, v252\n\t" \
    "v_max3_u32 v252, v206, v207, v252\n\t" \
    "v_cmp_eq_u32 vcc, -1, v252\n\t" \
    "s_cbranch_vccz .Lhm%=\n\t" \
    "s_nop 7\n\t" \
    "v_max_u32 v252, v200, v201\n\t" \
    "v_max3_u32 v252, v202, v203, v252\n\t" \
    "v_max3_u32 v252, v204, v205, v252\n\t" \
    "v_max3_u32 v252, v206, v207, v252\n\t" \
    "v_cmp_eq_u32 vcc, -1, v252\n\t" \
    "s_cbranch_vccz .Lhm%=\n\t" \
    "s_nop 7\n\t" \
    "s_sub_u32 %[n], %[n], 1\n\t" \
    "s_cmp_lg_u32 %[n], 0\n\t" \
    "s_cbranch_scc1 .Lfm%=\n\t" \
    ".Lhm%=:\n\t"
#define ASM_W4_M \
    "buffer_load_dwordx4 v[184:187], %[l16], %[rs], %[s1] offen offset:2048 sc1\n\t" \
    "buffer_load_dwordx4 v[180:183], %[l16], %[rs], %[s1] offen offset:1024 sc1\n\t" \
    "buffer_load_dwordx4 v[176:179], %[l16], %[rs], %[s1] offen offset:0 sc1\n\t" \
    "buffer_load_dwordx4 v[172:175], %[l16], %[rs], %[s0] offen offset:3072 sc1\n\t" \
    "buffer_load_dwordx4 v[168:171], %[l16], %[rs], %[s0] offen offset:2048 sc1\n\t" \
    "buffer_load_dwordx4 v[164:167], %[l16], %[rs], %[s0] offen offset:1024 sc1\n\t" \
    "buffer_load_dwordx4 v[160:163], %[l16], %[rs], %[s0] offen offset:0 sc1\n\t" \
    "buffer_load_dwordx4 v[220:223], %[l16], %[rs], %[s3] offen offset:3072 sc1\n\t" \
    "v_mfma_f32_16x16x32_bf16 v[224:227], v[28:31], v[188:191], 0\n\t" \
    "v_mfma_f32_16x16x32_bf16 v[228:231], v[60:63], v[188:191], 0\n\t" \
    "v_mfma_f32_16x16x32_bf16 v[232:235], v[92:95], v[188:191], 0\n\t" \
    "v_mfma_f32_16x16x32_bf16 v[236:239], v[124:127], v[188:191], 0\n\t" \
    "v_mfma_f32_16x16x32_bf16 v[240:243], v[156:159], v[188:191], 0\n\t" \
    "s_waitcnt vmcnt(7)\n\t" \
    ".Lc06_%=:\n\t" \
    "v_max_u32 v244, v184, v185\n\t" \
    "v_max3_u32 v244, v186, v187, v244\n\t" \
    "v_cmp_eq_u32 vcc, -1, v244\n\t" \
    "s_cbranch_vccnz .Ls06_%=\n\t" \
    ".Lk06_%=:\n\t" \
    "v_mfma_f32_16x16x32_bf16 v[224:227], v[24:27], v[184:187], v[224:227]\n\t" \
    "v_mfma_f32_16x16x32_bf16 v[228:231], v[56:59], v[184:187], v[228:231]\n\t" \
    "v_mfma_f32_16x16x32_bf16 v[232:235], v[88:91], v[184:187], v[232:235]\n\t" \
    "v_mfma_f32_16x16x32_bf16 v[236:239], v[120:123], v[184:187], v[236:239]\n\t" \
    "v_mfma_f32_16x16x32_bf16 v[240:243], v[152:155], v[184:187], v[240:243]\n\t" \
    "s_waitcnt vmcnt(6)\n\t" \
    ".Lc05_%=:\n\t" \
    "v_max_u32 v244, v180, v181\n\t" \
    "v_max3_u32 v244, v182, v183, v244\n\t" \
    "v_cmp_eq_u32 vcc, -1, v244\n\t" \
    "s_cbranch_vccnz .Ls05_%=\n\t" \
    ".Lk05_%=:\n\t" \
    "v_mfma_f32_16x16x32_bf16 v[224:227], v[20:23], v[180:183], v[224:227]\n\t" \
    "v_mfma_f32_16x16x32_bf16 v[228:231], v[52:55], v[180:183], v[228:231]\n\t" \
    "v_mfma_f32_16x16x32_bf16 v[232:235], v[84:87], v[180:183], v[232:235]\n\t" \
    "v_mfma_f32_16x16x32_bf16 v[236:239], v[116:119], v[180:183], v[236:239]\n\t" \
    "v_mfma_f32_16x16x32_bf16 v[240:243], v[148:151], v[180:183], v[240:243]\n\t" \
    "s_waitcnt vmcnt(5)\n\t" \
    ".Lc04_%=:\n\t" \
    "v_max_u32 v244, v176, v177\n\t" \
    "v_max3_u32 v244, v178, v179, v244\n\t" \
    "v_cmp_eq_u32 vcc, -1, v244\n\t" \
    "s_cbranch_vccnz .Ls04_%=\n\t" \
    ".Lk04_%=:\n\t" \
    "v_mfma_f32_16x16x32_bf16 v[224:227], v[16:19], v[176:179], v[224:227]\n\t" \
    "v_mfma_f32_16x16x32_bf16 v[228:231], v[48:51], v[176:179], v[228:231]\n\t" \
    "v_mfma_f32_16x16x32_bf16 v[232:235], v[80:83], v[176:179], v[232:235]\n\t" \
    "v_mfma_f32_16x16x32_bf16 v[236:239], v[112:115], v[176:179], v[236:239]\n\t" \
    "v_mfma_f32_16x16x32_bf16 v[240:243], v[144:147], v[176:179], v[240:243]\n\t" \
    "s_waitcnt vmcnt(4)\n\t" \
    ".Lc03_%=:\n\t" \
    "v_max_u32 v244, v172, v173\n\t" \
    "v_max3_u32 v244, v174, v175, v244\n\t" \
    "v_cmp_eq_u32 vcc, -1, v244\n\t" \
    "s_cbranch_vccnz .Ls03_%=\n\t" \
    ".Lk03_%=:\n\t" \
    "v_mfma_f32_16x16x32_bf16 v[224:227], v[12:15], v[172:175], v[224:227]\n\t" \
    "v_mfma_f32_16x16x32_bf16 v[228:231], v[44:47], v[172:175], v[228:231]\n\t" \
    "v_mfma_f32_16x16x32_bf16 v[232:235], v[76:79], v[172:175], v[232:235]\n\t" \
    "v_mfma_f32_16x16x32_bf16 v[236:239], v[108:111], v[172:175], v[236:239]\n\t" \
    "v_mfma_f32_16x16x32_bf16 v[240:243], v[140:143], v[172:175], v[240:243]\n\t" \
    "s_waitcnt vmcnt(3)\n\t" \
    ".Lc02_%=:\n\t" \
    "v_max_u32 v244, v168, v169\n\t" \
    "v_max3_u32 v244, v170, v171, v244\n\t" \
    "v_cmp_eq_u32 vcc, -1, v244\n\t" \
    "s_cbranch_vccnz .Ls02_%=\n\t" \
    ".Lk02_%=:\n\t" \
    "v_mfma_f32_16x16x32_bf16 v[224:227], v[8:11], v[168:171], v[224:227]\n\t" \
    "v_mfma_f32_16x16x32_bf16 v[228:231], v[40:43], v[168:171], v[228:231]\n\t" \
    "v_mfma_f32_16x16x32_bf16 v[232:235], v[72:75], v[168:171], v[232:235]\n\t" \
    "v_mfma_f32_16x16x32_bf16 v[236:239], v[104:107], v[168:171], v[236:239]\n\t" \
    "v_mfma_f32_16x16x32_bf16 v[240:243], v[136:139], v[168:171], v[240:243]\n\t" \
    "s_waitcnt vmcnt(2)\n\t" \
    ".Lc01_%=:\n\t" \
    "v_max_u32 v244, v164, v165\n\t" \
    "v_max3_u32 v244, v166, v167, v244\n\t" \
    "v_cmp_eq_u32 vcc, -1, v244\n\t" \
    "s_cbranch_vccnz .Ls01_%=\n\t" \
    ".Lk01_%=:\n\t" \
    "v_mfma_f32_16x16x32_bf16 v[224:227], v[4:7], v[164:167], v[224:227]\n\t" \
    "v_mfma_f32_16x16x32_bf16 v[228:231], v[36:39], v[164:167], v[228:231]\n\t" \
    "v_mfma_f32_16x16x32_bf16 v[232:235], v[68:71], v[164:167], v[232:235]\n\t" \
    "v_mfma_f32_16x16x32_bf16 v[236:239], v[100:103], v[164:167], v[236:239]\n\t" \
    "v_mfma_f32_16x16x32_bf16 v[240:243], v[132:135], v[164:167], v[240:243]\n\t" \
    "s_waitcnt vmcnt(1)\n\t" \
    ".Lc00_%=:\n\t" \
    "v_max_u32 v244, v160, v161\n\t" \
    "v_max3_u32 v244, v162, v163, v244\n\t" \
    "v_cmp_eq_u32 vcc, -1, v244\n\t" \
    "s_cbranch_vccnz .Ls00_%=\n\t" \
    ".Lk00_%=:\n\t" \
    "v_mfma_f32_16x16x32_bf16 v[224:227], v[0:3], v[160:163], v[224:227]\n\t" \
    "v_mfma_f32_16x16x32_bf16 v[228:231], v[32:35], v[160:163], v[228:231]\n\t" \
    "v_mfma_f32_16x16x32_bf16 v[232:235], v[64:67], v[160:163], v[232:235]\n\t" \
    "v_mfma_f32_16x16x32_bf16 v[236:239], v[96:99], v[160:163], v[236:239]\n\t" \
    "v_mfma_f32_16x16x32_bf16 v[240:243], v[128:131], v[160:163], v[240:243]\n\t" \
    "s_waitcnt vmcnt(0)\n\t" \
    ".Lc17_%=:\n\t" \
    "v_max_u32 v244, v220, v221\n\t" \
    "v_max3_u32 v244, v222, v223, v244\n\t" \
    "v_cmp_eq_u32 vcc, -1, v244\n\t" \
    "s_cbranch_vccnz .Ls17_%=\n\t" \
    ".Lk17_%=:\n\t" \
    "buffer_load_dwordx4 v[216:219], %[l16], %[rs], %[s3] offen offset:2048 sc1\n\t" \
    "buffer_load_dwordx4 v[212:215], %[l16], %[rs], %[s3] offen offset:1024 sc1\n\t" \
    "buffer_load_dwordx4 v[208:211], %[l16], %[rs], %[s3] offen offset:0 sc1\n\t" \
    "buffer_load_dwordx4 v[204:207], %[l16], %[rs], %[s2] offen offset:3072 sc1\n\t" \
    "buffer_load_dwordx4 v[200:203], %[l16], %[rs], %[s2] offen offset:2048 sc1\n\t" \
    "buffer_load_dwordx4 v[196:199], %[l16], %[rs], %[s2] offen offset:1024 sc1\n\t" \
    "buffer_load_dwordx4 v[192:195], %[l16], %[rs], %[s2] offen offset:0 sc1\n\t" \
    "v_mfma_f32_16x16x32_bf16 v[160:163], v[28:31], v[220:223], 0\n\t" \
    "ds_write_b128 %[ld], v[224:227] offset:0\n\t" \
    "v_mfma_f32_16x16x32_bf16 v[164:167], v[60:63], v[220:223], 0\n\t" \
    "ds_write_b128 %[ld], v[228:231] offset:6144\n\t" \
    "v_mfma_f32_16x16x32_bf16 v[168:171], v[92:95], v[220:223], 0\n\t" \
    "ds_write_b128 %[ld], v[232:235] offset:12288\n\t" \
    "v_mfma_f32_16x16x32_bf16 v[172:175], v[124:127], v[220:223], 0\n\t" \
    "ds_write_b128 %[ld], v[236:239] offset:18432\n\t" \
    "v_mfma_f32_16x16x32_bf16 v[176:179], v[156:159], v[220:223], 0\n\t" \
    "ds_write_b128 %[ld], v[240:243] offset:24576\n\t" \
    "s_waitcnt vmcnt(6)\n\t" \
    ".Lc16_%=:\n\t" \
    "v_max_u32 v244, v216, v217\n\t" \
    "v_max3_u32 v244, v218, v219, v244\n\t" \
    "v_cmp_eq_u32 vcc, -1, v244\n\t" \
    "s_cbranch_vccnz .Ls16_%=\n\t" \
    ".Lk16_%=:\n\t" \
    "v_mfma_f32_16x16x32_bf16 v[160:163], v[24:27], v[216:219], v[160:163]\n\t" \
    "v_mfma_f32_16x16x32_bf16 v[164:167], v[56:59], v[216:219], v[164:167]\n\t" \
    "v_mfma_f32_16x16x32_bf16 v[168:171], v[88:91], v[216:219], v[168:171]\n\t" \
    "v_mfma_f32_16x16x32_bf16 v[172:175], v[120:123], v[216:219], v[172:175]\n\t" \
    "v_mfma_f32_16x16x32_bf16 v[176:179], v[152:155], v[216:219], v[176:179]\n\t" \
    "s_waitcnt vmcnt(5)\n\t" \
    ".Lc15_%=:\n\t" \
    "v_max_u32 v244, v212, v213\n\t" \
    "v_max3_u32 v244, v214, v215, v244\n\t" \
    "v_cmp_eq_u32 vcc, -1, v244\n\t" \
    "s_cbranch_vccnz .Ls15_%=\n\t" \
    ".Lk15_%=:\n\t" \
    "v_mfma_f32_16x16x32_bf16 v[160:163], v[20:23], v[212:215], v[160:163]\n\t" \
    "v_mfma_f32_16x16x32_bf16 v[164:167], v[52:55], v[212:215], v[164:167]\n\t" \
    "v_mfma_f32_16x16x32_bf16 v[168:171], v[84:87], v[212:215], v[168:171]\n\t" \
    "v_mfma_f32_16x16x32_bf16 v[172:175], v[116:119], v[212:215], v[172:175]\n\t" \
    "v_mfma_f32_16x16x32_bf16 v[176:179], v[148:151], v[212:215], v[176:179]\n\t" \
    "s_waitcnt vmcnt(4)\n\t" \
    ".Lc14_%=:\n\t" \
    "v_max_u32 v244, v208, v209\n\t" \
    "v_max3_u32 v244, v210, v211, v244\n\t" \
    "v_cmp_eq_u32 vcc, -1, v244\n\t" \
    "s_cbranch_vccnz .Ls14_%=\n\t" \
    ".Lk14_%=:\n\t" \
    "v_mfma_f32_16x16x32_bf16 v[160:163], v[16:19], v[208:211], v[160:163]\n\t" \
    "v_mfma_f32_16x16x32_bf16 v[164:167], v[48:51], v[208:211], v[164:167]\n\t" \
    "v_mfma_f32_16x16x32_bf16 v[168:171], v[80:83], v[208:211], v[168:171]\n\t" \
    "v_mfma_f32_16x16x32_bf16 v[172:175], v[112:115], v[208:211], v[172:175]\n\t" \
    "v_mfma_f32_16x16x32_bf16 v[176:179], v[144:147], v[208:211], v[176:179]\n\t" \
    "s_waitcnt vmcnt(3)\n\t" \
    ".Lc13_%=:\n\t" \
    "v_max_u32 v244, v204, v205\n\t" \
    "v_max3_u32 v244, v206, v207, v244\n\t" \
    "v_cmp_eq_u32 vcc, -1, v244\n\t" \
    "s_cbranch_vccnz .Ls13_%=\n\t" \
    ".Lk13_%=:\n\t" \
    "v_mfma_f32_16x16x32_bf16 v[160:163], v[12:15], v[204:207], v[160:163]\n\t" \
    "v_mfma_f32_16x16x32_bf16 v[164:167], v[44:47], v[204:207], v[164:167]\n\t" \
    "v_mfma_f32_16x16x32_bf16 v[168:171], v[76:79], v[204:207], v[168:171]\n\t" \
    "v_mfma_f32_16x16x32_bf16 v[172:175], v[108:111], v[204:207], v[172:175]\n\t" \
    "v_mfma_f32_16x16x32_bf16 v[176:179], v[140:143], v[204:207], v[176:179]\n\t" \
    "s_waitcnt vmcnt(2)\n\t" \
    ".Lc12_%=:\n\t" \
    "v_max_u32 v244, v200, v201\n\t" \
    "v_max3_u32 v244, v202, v203, v244\n\t" \
    "v_cmp_eq_u32 vcc, -1, v244\n\t" \
    "s_cbranch_vccnz .Ls12_%=\n\t" \
    ".Lk12_%=:\n\t" \
    "v_mfma_f32_16x16x32_bf16 v[160:163], v[8:11], v[200:203], v[160:163]\n\t" \
    "v_mfma_f32_16x16x32_bf16 v[164:167], v[40:43], v[200:203], v[164:167]\n\t" \
    "v_mfma_f32_16x16x32_bf16 v[168:171], v[72:75], v[200:203], v[168:171]\n\t" \
    "v_mfma_f32_16x16x32_bf16 v[172:175], v[104:107], v[200:203], v[172:175]\n\t" \
    "v_mfma_f32_16x16x32_bf16 v[176:179], v[136:139], v[200:203], v[176:179]\n\t" \
    "s_waitcnt vmcnt(1)\n\t" \
    ".Lc11_%=:\n\t" \
    "v_max_u32 v244, v196, v197\n\t" \
    "v_max3_u32 v244, v198, v199, v244\n\t" \
    "v_cmp_eq_u32 vcc, -1, v244\n\t" \
    "s_cbranch_vccnz .Ls11_%=\n\t" \
    ".Lk11_%=:\n\t" \
    "v_mfma_f32_16x16x32_bf16 v[160:163], v[4:7], v[196:199], v[160:163]\n\t" \
    "v_mfma_f32_16x16x32_bf16 v[164:167], v[36:39], v[196:199], v[164:167]\n\t" \
    "v_mfma_f32_16x16x32_bf16 v[168:171], v[68:71], v[196:199], v[168:171]\n\t" \
    "v_mfma_f32_16x16x32_bf16 v[172:175], v[100:103], v[196:199], v[172:175]\n\t" \
    "v_mfma_f32_16x16x32_bf16 v[176:179], v[132:135], v[196:199], v[176:179]\n\t" \
    "s_waitcnt vmcnt(0)\n\t" \
    ".Lc10_%=:\n\t" \
    "v_max_u32 v244, v192, v193\n\t" \
    "v_max3_u32 v244, v194, v195, v244\n\t" \
    "v_cmp_eq_u32 vcc, -1, v244\n\t" \
    "s_cbranch_vccnz .Ls10_%=\n\t" \
    ".Lk10_%=:\n\t" \
    "v_mfma_f32_16x16x32_bf16 v[160:163], v[0:3], v[192:195], v[160:163]\n\t" \
    "v_mfma_f32_16x16x32_bf16 v[164:167], v[32:35], v[192:195], v[164:167]\n\t" \
    "v_mfma_f32_16x16x32_bf16 v[168:171], v[64:67], v[192:195], v[168:171]\n\t" \
    "v_mfma_f32_16x16x32_bf16 v[172:175], v[96:99], v[192:195], v[172:175]\n\t" \
    "v_mfma_f32_16x16x32_bf16 v[176:179], v[128:131], v[192:195], v[176:179]\n\t" \
    "ds_write_b128 %[ld], v[160:163] offset:30720\n\t" \
    "ds_write_b128 %[ld], v[164:167] offset:36864\n\t" \
    "ds_write_b128 %[ld], v[168:171] offset:43008\n\t" \
    "s_nop 1\n\t" \
    "ds_write_b128 %[ld], v[172:175] offset:49152\n\t" \
    "s_nop 1\n\t" \
    "ds_write_b128 %[ld], v[176:179] offset:55296\n\t" \
    "s_branch .Lme%=\n\t" \
    ".Ls06_%=:\n\t" \
    "s_cmp_eq_u32 %[n], 0\n\t" \
    "s_cbranch_scc1 .Lk06_%=\n\t" \
    "s_sub_u32 %[n], %[n], 1\n\t" \
    "buffer_load_dwordx4 v[184:187], %[l16], %[rs], %[s1] offen offset:2048 sc1\n\t" \
    "s_waitcnt vmcnt(0)\n\t" \
    "s_branch .Lc06_%=\n\t" \
    ".Ls05_%=:\n\t" \
    "s_cmp_eq_u32 %[n], 0\n\t" \
    "s_cbranch_scc1 .Lk05_%=\n\t" \
    "s_sub_u32 %[n], %[n], 1\n\t" \
    "buffer_load_dwordx4 v[180:183], %[l16], %[rs], %[s1] offen offset:1024 sc1\n\t" \
    "s_waitcnt vmcnt(0)\n\t" \
    "s_branch .Lc05_%=\n\t" \
    ".Ls04_%=:\n\t" \
    "s_cmp_eq_u32 %[n], 0\n\t" \
    "s_cbranch_scc1 .Lk04_%=\n\t" \
    "s_sub_u32 %[n], %[n], 1\n\t" \
    "buffer_load_dwordx4 v[176:179], %[l16], %[rs], %[s1] offen offset:0 sc1\n\t" \
    "s_waitcnt vmcnt(0)\n\t" \
    "s_branch .Lc04_%=\n\t" \
    ".Ls03_%=:\n\t" \
    "s_cmp_eq_u32 %[n], 0\n\t" \
    "s_cbranch_scc1 .Lk03_%=\n\t" \
    "s_sub_u32 %[n], %[n], 1\n\t" \
    "buffer_load_dwordx4 v[172:175], %[l16], %[rs], %[s0] offen offset:3072 sc1\n\t" \
    "s_waitcnt vmcnt(0)\n\t" \
    "s_branch .Lc03_%=\n\t" \
    ".Ls02_%=:\n\t" \
    "s_cmp_eq_u32 %[n], 0\n\t" \
    "s_cbranch_scc1 .Lk02_%=\n\t" \
    "s_sub_u32 %[n], %[n], 1\n\t" \
    "buffer_load_dwordx4 v[168:171], %[l16], %[rs], %[s0] offen offset:2048 sc1\n\t" \
    "s_waitcnt vmcnt(0)\n\t" \
    "s_branch .Lc02_%=\n\t" \
    ".Ls01_%=:\n\t" \
    "s_cmp_eq_u32 %[n], 0\n\t" \
    "s_cbranch_scc1 .Lk01_%=\n\t" \
    "s_sub_u32 %[n], %[n], 1\n\t" \
    "buffer_load_dwordx4 v[164:167], %[l16], %[rs], %[s0] offen offset:1024 sc1\n\t" \
    "s_waitcnt vmcnt(0)\n\t" \
    "s_branch .Lc01_%=\n\t" \
    ".Ls00_%=:\n\t" \
    "s_cmp_eq_u32 %[n], 0\n\t" \
    "s_cbranch_scc1 .Lk00_%=\n\t" \
    "s_sub_u32 %[n], %[n], 1\n\t" \
    "buffer_load_dwordx4 v[160:163], %[l16], %[rs], %[s0] offen offset:0 sc1\n\t" \
    "s_waitcnt vmcnt(0)\n\t" \
    "s_branch .Lc00_%=\n\t" \
    ".Ls17_%=:\n\t" \
    "s_cmp_eq_u32 %[n], 0\n\t" \
    "s_cbranch_scc1 .Lk17_%=\n\t" \
    "s_sub_u32 %[n], %[n], 1\n\t" \
    "buffer_load_dwordx4 v[220:223], %[l16], %[rs], %[s3] offen offset:3072 sc1\n\t" \
    "s_waitcnt vmcnt(0)\n\t" \
    "s_branch .Lc17_%=\n\t" \
    ".Ls16_%=:\n\t" \
    "s_cmp_eq_u32 %[n], 0\n\t" \
    "s_cbranch_scc1 .Lk16_%=\n\t" \
    "s_sub_u32 %[n], %[n], 1\n\t" \
    "buffer_load_dwordx4 v[216:219], %[l16], %[rs], %[s3] offen offset:2048 sc1\n\t" \
    "s_waitcnt vmcnt(0)\n\t" \
    "s_branch .Lc16_%=\n\t" \
    ".Ls15_%=:\n\t" \
    "s_cmp_eq_u32 %[n], 0\n\t" \
    "s_cbranch_scc1 .Lk15_%=\n\t" \
    "s_sub_u32 %[n], %[n], 1\n\t" \
    "buffer_load_dwordx4 v[212:215], %[l16], %[rs], %[s3] offen offset:1024 sc1\n\t" \
    "s_waitcnt vmcnt(0)\n\t" \
    "s_branch .Lc15_%=\n\t" \
    ".Ls14_%=:\n\t" \
    "s_cmp_eq_u32 %[n], 0\n\t" \
    "s_cbranch_scc1 .Lk14_%=\n\t" \
    "s_sub_u32 %[n], %[n], 1\n\t" \
    "buffer_load_dwordx4 v[208:211], %[l16], %[rs], %[s3] offen offset:0 sc1\n\t" \
    "s_waitcnt vmcnt(0)\n\t" \
    "s_branch .Lc14_%=\n\t" \
    ".Ls13_%=:\n\t" \
    "s_cmp_eq_u32 %[n], 0\n\t" \
    "s_cbranch_scc1 .Lk13_%=\n\t" \
    "s_sub_u32 %[n], %[n], 1\n\t" \
    "buffer_load_dwordx4 v[204:207], %[l16], %[rs], %[s2] offen offset:3072 sc1\n\t" \
    "s_waitcnt vmcnt(0)\n\t" \
    "s_branch .Lc13_%=\n\t" \
    ".Ls12_%=:\n\t" \
    "s_cmp_eq_u32 %[n], 0\n\t" \
    "s_cbranch_scc1 .Lk12_%=\n\t" \
    "s_sub_u32 %[n], %[n], 1\n\t" \
    "buffer_load_dwordx4 v[200:203], %[l16], %[rs], %[s2] offen offset:2048 sc1\n\t" \
    "s_waitcnt vmcnt(0)\n\t" \
    "s_branch .Lc12_%=\n\t" \
    ".Ls11_%=:\n\t" \
    "s_cmp_eq_u32 %[n], 0\n\t" \
    "s_cbranch_scc1 .Lk11_%=\n\t" \
    "s_sub_u32 %[n], %[n], 1\n\t" \
    "buffer_load_dwordx4 v[196:199], %[l16], %[rs], %[s2] offen offset:1024 sc1\n\t" \
    "s_waitcnt vmcnt(0)\n\t" \
    "s_branch .Lc11_%=\n\t" \
    ".Ls10_%=:\n\t" \
    "s_cmp_eq_u32 %[n], 0\n\t" \
    "s_cbranch_scc1 .Lk10_%=\n\t" \
    "s_sub_u32 %[n], %[n], 1\n\t" \
    "buffer_load_dwordx4 v[192:195], %[l16], %[rs], %[s2] offen offset:0 sc1\n\t" \
    "s_waitcnt vmcnt(0)\n\t" \
    "s_branch .Lc10_%=\n\t" \
    ".Lme%=:\n\t"
#define ASM_POLL5_I \
    "buffer_load_dwordx4 v[232:235], %[l16], %[rs], %[s0] offen offset:0 sc1\n\t" \
    "buffer_load_dwordx4 v[236:239], %[l16], %[rs], %[s0] offen offset:1024 sc1\n\t" \
    "buffer_load_dwordx4 v[240:243], %[l16], %[rs], %[s0] offen offset:2048 sc1\n\t" \
    "buffer_load_dwordx4 v[244:247], %[l16], %[rs], %[s0] offen offset:3072 sc1\n\t" \
    "buffer_load_dwordx4 v[248:251], %[l16], %[rs], %[s1] offen offset:0 sc1\n\t"
#define ASM_POLL5_C \
    "s_movk_i32 %[n], 0x200\n\t" \
    "v_max_u32 v252, v232, v233\n\t" \
    "v_max3_u32 v252, v234, v235, v252\n\t" \
    "v_max3_u32 v252, v236, v237, v252\n\t" \
    "v_max3_u32 v252, v238, v239, v252\n\t" \
    "v_max3_u32 v252, v240, v241, v252\n\t" \
    "v_max3_u32 v252, v242, v243, v252\n\t" \
    "v_max3_u32 v252, v244, v245, v252\n\t" \
    "v_max3_u32 v252, v246, v247, v252\n\t" \
    "v_max3_u32 v252, v248, v249, v252\n\t" \
    "v_max3_u32 v252, v250, v251, v252\n\t" \
    "v_cmp_eq_u32 vcc, -1, v252\n\t" \
    "s_cbranch_vccz .Lhw%=\n\t" \
    ".Lfw%=:\n\t" \
    "s_mov_b32 %[a], 0\n\t" \
    "v_max_u32 v252, v232, v233\n\t" \
    "v_max3_u32 v252, v234, v235, v252\n\t" \
    "v_cmp_eq_u32 vcc, -1, v252\n\t" \
    "s_cbranch_vccz .Lgw0_%=\n\t" \
    "buffer_load_dwordx4 v[232:235], %[l16], %[rs], %[s0] offen offset:0 sc1\n\t" \
    "s_mov_b32 %[a], 1\n\t" \
    ".Lgw0_%=:\n\t" \
    "v_max_u32 v252, v236, v237\n\t" \
    "v_max3_u32 v252, v238, v239, v252\n\t" \
    "v_cmp_eq_u32 vcc, -1, v252\n\t" \
    "s_cbranch_vccz .Lgw1_%=\n\t" \
    "buffer_load_dwordx4 v[236:239], %[l16], %[rs], %[s0] offen offset:1024 sc1\n\t" \
    "s_mov_b32 %[a], 1\n\t" \
    ".Lgw1_%=:\n\t" \
    "v_max_u32 v252, v240, v241\n\t" \
    "v_max3_u32 v252, v242, v243, v252\n\t" \
    "v_cmp_eq_u32 vcc, -1, v252\n\t" \
    "s_cbranch_vccz .Lgw2_%=\n\t" \
    "buffer_load_dwordx4 v[240:243], %[l16], %[rs], %[s0] offen offset:2048 sc1\n\t" \
    "s_mov_b32 %[a], 1\n\t" \
    ".Lgw2_%=:\n\t" \
    "v_max_u32 v252, v244, v245\n\t" \
    "v_max3_u32 v252, v246, v247, v252\n\t" \
    "v_cmp_eq_u32 vcc, -1, v252\n\t" \
    "s_cbranch_vccz .Lgw3_%=\n\t" \
    "buffer_load_dwordx4 v[244:247], %[l16], %[rs], %[s0] offen offset:3072 sc1\n\t" \
    "s_mov_b32 %[a], 1\n\t" \
    ".Lgw3_%=:\n\t" \
    "v_max_u32 v252, v248, v249\n\t" \
    "v_max3_u32 v252, v250, v251, v252\n\t" \
    "v_cmp_eq_u32 vcc, -1, v252\n\t" \
    "s_cbranch_vccz .Lgw4_%=\n\t" \
    "buffer_load_dwordx4 v[248:251], %[l16], %[rs], %[s1] offen offset:0 sc1\n\t" \
    "s_mov_b32 %[a], 1\n\t" \
    ".Lgw4_%=:\n\t" \
    "s_cmp_eq_u32 %[a], 0\n\t" \
    "s_cbranch_scc1 .Lhw%=\n\t" \
    "s_waitcnt vmcnt(20)\n\t" \
    "v_max_u32 v252, v232, v233\n\t" \
    "v_max3_u32 v252, v234, v235, v252\n\t" \
    "v_max3_u32 v252, v236, v237, v252\n\t" \
    "v_max3_u32 v252, v238, v239, v252\n\t" \
    "v_max3_u32 v252, v240, v241, v252\n\t" \
    "v_max3_u32 v252, v242, v243, v252\n\t" \
    "v_max3_u32 v252, v244, v245, v252\n\t" \
    "v_max3_u32 v252, v246, v247, v252\n\t" \
    "v_max3_u32 v252, v248, v249, v252\n\t" \
    "v_max3_u32 v252, v250, v251, v252\n\t" \
    "v_cmp_eq_u32 vcc, -1, v252\n\t" \
    "s_cbranch_vccz .Lhw%=\n\t" \
    "s_sleep 1\n\t" \
    "v_max_u32 v252, v232, v233\n\t" \
    "v_max3_u32 v252, v234, v235, v252\n\t" \
    "v_max3_u32 v252, v236, v237, v252\n\t" \
    "v_max3_u32 v252, v238, v239, v252\n\t" \
    "v_max3_u32 v252, v240, v241, v252\n\t" \
    "v_max3_u32 v252, v242, v243, v252\n\t" \
    "v_max3_u32 v252, v244, v245, v252\n\t" \
    "v_max3_u32 v252, v246, v247, v252\n\t" \
    "v_max3_u32 v252, v248, v249, v252\n\t" \
    "v_max3_u32 v252, v250, v251, v252\n\t" \
    "v_cmp_eq_u32 vcc, -1, v252\n\t" \
    "s_cbranch_vccz .Lhw%=\n\t" \
    "s_sleep 1\n\t" \
    "v_max_u32 v252, v232, v233\n\t" \
    "v_max3_u32 v252, v234, v235, v252\n\t" \
    "v_max3_u32 v252, v236, v237, v252\n\t" \
    "v_max3_u32 v252, v238, v239, v252\n\t" \
    "v_max3_u32 v252, v240, v241, v252\n\t" \
    "v_max3_u32 v252, v242, v243, v252\n\t" \
    "v_max3_u32 v252, v244, v245, v252\n\t" \
    "v_max3_u32 v252, v246, v247, v252\n\t" \
    "v_max3_u32 v252, v248, v249, v252\n\t" \
    "v_max3_u32 v252, v250, v251, v252\n\t" \
    "v_cmp_eq_u32 vcc, -1, v252\n\t" \
    "s_cbranch_vccz .Lhw%=\n\t" \
    "s_sleep 1\n\t" \
    "v_max_u32 v252, v232, v233\n\t" \
    "v_max3_u32 v252, v234, v235, v252\n\t" \
    "v_max3_u32 v252, v236, v237, v252\n\t" \
    "v_max3_u32 v252, v238, v239, v252\n\t" \
    "v_max3_u32 v252, v240, v241, v252\n\t" \
    "v_max3_u32 v252, v242, v243, v252\n\t" \
    "v_max3_u32 v252, v244, v245, v252\n\t" \
    "v_max3_u32 v252, v246, v247, v252\n\t" \
    "v_max3_u32 v252, v248, v249, v252\n\t" \
    "v_max3_u32 v252, v250, v251, v252\n\t" \
    "v_cmp_eq_u32 vcc, -1, v252\n\t" \
    "s_cbranch_vccz .Lhw%=\n\t" \
    "s_sleep 1\n\t" \
    "v_max_u32 v252, v232, v233\n\t" \
    "v_max3_u32 v252, v234, v235, v252\n\t" \
    "v_max3_u32 v252, v236, v237, v252\n\t" \
    "v_max3_u32 v252, v238, v239, v252\n\t" \
    "v_max3_u32 v252, v240, v241, v252\n\t" \
    "v_max3_u32 v252, v242, v243, v252\n\t" \
    "v_max3_u32 v252, v244, v245, v252\n\t" \
    "v_max3_u32 v252, v246, v247, v252\n\t" \
    "v_max3_u32 v252, v248, v249, v252\n\t" \
    "v_max3_u32 v252, v250, v251, v252\n\t" \
    "v_cmp_eq_u32 vcc, -1, v252\n\t" \
    "s_cbranch_vccz .Lhw%=\n\t" \
    "s_sleep 1\n\t" \
    "v_max_u32 v252, v232, v233\n\t" \
    "v_max3_u32 v252, v234, v235, v252\n\t" \
    "v_max3_u32 v252, v236, v237, v252\n\t" \
    "v_max3_u32 v252, v238, v239, v252\n\t" \
    "v_max3_u32 v252, v240, v241, v252\n\t" \
    "v_max3_u32 v252, v242, v243, v252\n\t" \
    "v_max3_u32 v252, v244, v245, v252\n\t" \
    "v_max3_u32 v252, v246, v247, v252\n\t" \
    "v_max3_u32 v252, v248, v249, v252\n\t" \
    "v_max3_u32 v252, v250, v251, v252\n\t" \
    "v_cmp_eq_u32 vcc, -1, v252\n\t" \
    "s_cbranch_vccz .Lhw%=\n\t" \
    "s_sleep 1\n\t" \
    "s_sub_u32 %[n], %[n], 1\n\t" \
    "s_cmp_lg_u32 %[n], 0\n\t" \
    "s_cbranch_scc1 .Lfw%=\n\t" \
    ".Lhw%=:\n\t"
#define ASM_POLLS3 \
    "buffer_load_dwordx4 v[208:211], %[l16], %[rs], %[s0] offen offset:0 sc1\n\t" \
    "buffer_load_dwordx4 v[212:215], %[l16], %[rs], %[s1] offen offset:0 sc1\n\t" \
    "buffer_load_dwordx4 v[216:219], %[l16], %[rs], %[s2] offen offset:0 sc1\n\t" \
    "s_movk_i32 %[n], 0x200\n\t" \
    "s_waitcnt vmcnt(2)\n\t" \
    "v_max_u32 v240, v208, v209\n\t" \
    "v_max3_u32 v240, v210, v211, v240\n\t" \
    "s_waitcnt vmcnt(1)\n\t" \
    "v_max3_u32 v240, v212, v213, v240\n\t" \
    "v_max3_u32 v240, v214, v215, v240\n\t" \
    "s_waitcnt vmcnt(0)\n\t" \
    "v_max3_u32 v240, v216, v217, v240\n\t" \
    "v_max3_u32 v240, v218, v219, v240\n\t" \
    "v_cmp_eq_u32 vcc, -1, v240\n\t" \
    "s_cbranch_vccz .Lpd%=\n\t" \
    ".Lpl%=:\n\t" \
    "s_mov_b32 %[a], 0\n\t" \
    "v_max_u32 v240, v208, v209\n\t" \
    "v_max3_u32 v240, v210, v211, v240\n\t" \
    "v_cmp_eq_u32 vcc, -1, v240\n\t" \
    "s_cbranch_vccz .Lps0_%=\n\t" \
    "buffer_load_dwordx4 v[208:211], %[l16], %[rs], %[s0] offen offset:0 sc1\n\t" \
    "s_mov_b32 %[a], 1\n\t" \
    ".Lps0_%=:\n\t" \
    "v_max_u32 v240, v212, v213\n\t" \
    "v_max3_u32 v240, v214, v215, v240\n\t" \
    "v_cmp_eq_u32 vcc, -1, v240\n\t" \
    "s_cbranch_vccz .Lps1_%=\n\t" \
    "buffer_load_dwordx4 v[212:215], %[l16], %[rs], %[s1] offen offset:0 sc1\n\t" \
    "s_mov_b32 %[a], 1\n\t" \
    ".Lps1_%=:\n\t" \
    "v_max_u32 v240, v216, v217\n\t" \
    "v_max3_u32 v240, v218, v219, v240\n\t" \
    "v_cmp_eq_u32 vcc, -1, v240\n\t" \
    "s_cbranch_vccz .Lps2_%=\n\t" \
    "buffer_load_dwordx4 v[216:219], %[l16], %[rs], %[s2] offen offset:0 sc1\n\t" \
    "s_mov_b32 %[a], 1\n\t" \
    ".Lps2_%=:\n\t" \
    "s_cmp_eq_u32 %[a], 0\n\t" \
    "s_cbranch_scc1 .Lpd%=\n\t" \
    "s_waitcnt vmcnt(0)\n\t" \
    "s_sub_u32 %[n], %[n], 1\n\t" \
    "s_cmp_lg_u32 %[n], 0\n\t" \
    "s_cbranch_scc1 .Lpl%=\n\t" \
    ".Lpd%=:\n\t"
#define ASM_POLLS2 \
    "buffer_load_dwordx4 v[208:211], %[l16], %[rs], %[s0] offen offset:0 sc1\n\t" \
    "buffer_load_dwordx4 v[212:215], %[l16], %[rs], %[s1] offen offset:0 sc1\n\t" \
    "s_movk_i32 %[n], 0x200\n\t" \
    "s_waitcnt vmcnt(1)\n\t" \
    "v_max_u32 v240, v208, v209\n\t" \
    "v_max3_u32 v240, v210, v211, v240\n\t" \
    "s_waitcnt vmcnt(0)\n\t" \
    "v_max3_u32 v240, v212, v213, v240\n\t" \
    "v_max3_u32 v240, v214, v215, v240\n\t" \
    "v_cmp_eq_u32 vcc, -1, v240\n\t" \
    "s_cbranch_vccz .Lpd%=\n\t" \
    ".Lpl%=:\n\t" \
    "s_mov_b32 %[a], 0\n\t" \
    "v_max_u32 v240, v208, v209\n\t" \
    "v_max3_u32 v240, v210, v211, v240\n\t" \
    "v_cmp_eq_u32 vcc, -1, v240\n\t" \
    "s_cbranch_vccz .Lps0_%=\n\t" \
    "buffer_load_dwordx4 v[208:211], %[l16], %[rs], %[s0] offen offset:0 sc1\n\t" \
    "s_mov_b32 %[a], 1\n\t" \
    ".Lps0_%=:\n\t" \
    "v_max_u32 v240, v212, v213\n\t" \
    "v_max3_u32 v240, v214, v215, v240\n\t" \
    "v_cmp_eq_u32 vcc, -1, v240\n\t" \
    "s_cbranch_vccz .Lps1_%=\n\t" \
    "buffer_load_dwordx4 v[212:215], %[l16], %[rs], %[s1] offen offset:0 sc1\n\t" \
    "s_mov_b32 %[a], 1\n\t" \
    ".Lps1_%=:\n\t" \
    "s_cmp_eq_u32 %[a], 0\n\t" \
    "s_cbranch_scc1 .Lpd%=\n\t" \
    "s_waitcnt vmcnt(0)\n\t" \
    "s_sub_u32 %[n], %[n], 1\n\t" \
    "s_cmp_lg_u32 %[n], 0\n\t" \
    "s_cbranch_scc1 .Lpl%=\n\t" \
    ".Lpd%=:\n\t"
#define ASM_POLLV3 \
    "buffer_load_dwordx4 v[200:203], %[l16], %[rs], %[s0] offen offset:0 sc1\n\t" \
    "buffer_load_dwordx4 v[204:207], %[l16], %[rs], %[s1] offen offset:0 sc1\n\t" \
    "buffer_load_dwordx4 v[208:211], %[l16], %[rs], %[s2] offen offset:0 sc1\n\t" \
    "s_movk_i32 %[n], 0x200\n\t" \
    "s_waitcnt vmcnt(2)\n\t" \
    "v_max_u32 v252, v200, v201\n\t" \
    "v_max3_u32 v252, v202, v203, v252\n\t" \
    "s_waitcnt vmcnt(1)\n\t" \
    "v_max3_u32 v252, v204, v205, v252\n\t" \
    "v_max3_u32 v252, v206, v207, v252\n\t" \
    "s_waitcnt vmcnt(0)\n\t" \
    "v_max3_u32 v252, v208, v209, v252\n\t" \
    "v_max3_u32 v252, v210, v211, v252\n\t" \
    "v_cmp_eq_u32 vcc, -1, v252\n\t" \
    "s_cbranch_vccz .Lpd%=\n\t" \
    ".Lpl%=:\n\t" \
    "s_mov_b32 %[a], 0\n\t" \
    "v_max_u32 v252, v200, v201\n\t" \
    "v_max3_u32 v252, v202, v203, v252\n\t" \
    "v_cmp_eq_u32 vcc, -1, v252\n\t" \
    "s_cbranch_vccz .Lps0_%=\n\t" \
    "buffer_load_dwordx4 v[200:203], %[l16], %[rs], %[s0] offen offset:0 sc1\n\t" \
    "s_mov_b32 %[a], 1\n\t" \
    ".Lps0_%=:\n\t" \
    "v_max_u32 v252, v204, v205\n\t" \
    "v_max3_u32 v252, v206, v207, v252\n\t" \
    "v_cmp_eq_u32 vcc, -1, v252\n\t" \
    "s_cbranch_vccz .Lps1_%=\n\t" \
    "buffer_load_dwordx4 v[204:207], %[l16], %[rs], %[s1] offen offset:0 sc1\n\t" \
    "s_mov_b32 %[a], 1\n\t" \
    ".Lps1_%=:\n\t" \
    "v_max_u32 v252, v208, v209\n\t" \
    "v_max3_u32 v252, v210, v211, v252\n\t" \
    "v_cmp_eq_u32 vcc, -1, v252\n\t" \
    "s_cbranch_vccz .Lps2_%=\n\t" \
    "buffer_load_dwordx4 v[208:211], %[l16], %[rs], %[s2] offen offset:0 sc1\n\t" \
    "s_mov_b32 %[a], 1\n\t" \
    ".Lps2_%=:\n\t" \
    "s_cmp_eq_u32 %[a], 0\n\t" \
    "s_cbranch_scc1 .Lpd%=\n\t" \
    "s_waitcnt vmcnt(0)\n\t" \
    "s_sub_u32 %[n], %[n], 1\n\t" \
    "s_cmp_lg_u32 %[n], 0\n\t" \
    "s_cbranch_scc1 .Lpl%=\n\t" \
    ".Lpd%=:\n\t"
#define ASM_POLLV2 \
    "buffer_load_dwordx4 v[200:203], %[l16], %[rs], %[s0] offen offset:0 sc1\n\t" \
    "buffer_load_dwordx4 v[204:207], %[l16], %[rs], %[s1] offen offset:0 sc1\n\t" \
    "s_movk_i32 %[n], 0x200\n\t" \
    "s_waitcnt vmcnt(1)\n\t" \
    "v_max_u32 v252, v200, v201\n\t" \
    "v_max3_u32 v252, v202, v203, v252\n\t" \
    "s_waitcnt vmcnt(0)\n\t" \
    "v_max3_u32 v252, v204, v205, v252\n\t" \
    "v_max3_u32 v252, v206, v207, v252\n\t" \
    "v_cmp_eq_u32 vcc, -1, v252\n\t" \
    "s_cbranch_vccz .Lpd%=\n\t" \
    ".Lpl%=:\n\t" \
    "s_mov_b32 %[a], 0\n\t" \
    "v_max_u32 v252, v200, v201\n\t" \
    "v_max3_u32 v252, v202, v203, v252\n\t" \
    "v_cmp_eq_u32 vcc, -1, v252\n\t" \
    "s_cbranch_vccz .Lps0_%=\n\t" \
    "buffer_load_dwordx4 v[200:203], %[l16], %[rs], %[s0] offen offset:0 sc1\n\t" \
    "s_mov_b32 %[a], 1\n\t" \
    ".Lps0_%=:\n\t" \
    "v_max_u32 v252, v204, v205\n\t" \
    "v_max3_u32 v252, v206, v207, v252\n\t" \
    "v_cmp_eq_u32 vcc, -1, v252\n\t" \
    "s_cbranch_vccz .Lps1_%=\n\t" \
    "buffer_load_dwordx4 v[204:207], %[l16], %[rs], %[s1] offen offset:0 sc1\n\t" \
    "s_mov_b32 %[a], 1\n\t" \
    ".Lps1_%=:\n\t" \
    "s_cmp_eq_u32 %[a], 0\n\t" \
    "s_cbranch_scc1 .Lpd%=\n\t" \
    "s_waitcnt vmcnt(0)\n\t" \
    "s_sub_u32 %[n], %[n], 1\n\t" \
    "s_cmp_lg_u32 %[n], 0\n\t" \
    "s_cbranch_scc1 .Lpl%=\n\t" \
    ".Lpd%=:\n\t"
__device__ __forceinline__ void poll5_issue(rsrc_t rs, unsigned l16, unsigned base, u32x4 (&v)[5]) {
    asm volatile(ASM_POLL5_I : "+{v[232:235]}"(v[0]), "+{v[236:239]}"(v[1]), "+{v[240:243]}"(v[2]), "+{v[244:247]}"(v[3]), "+{v[248:251]}"(v[4]) : [l16] "v"(l16), [rs] "s"(rs), [s0] "s"(base), [s1] "s"(base + 4096) : "memory");
}
__device__ __forceinline__ void poll5(rsrc_t rs, unsigned l16, unsigned base, u32x4 (&v)[5], const Params& P) {
#pragma unroll 1
    for (int spins = 1023;; spins += 1024) {
        unsigned n, a;
        asm volatile(ASM_POLL5_C : "+{v[232:235]}"(v[0]), "+{v[236:239]}"(v[1]), "+{v[240:243]}"(v[2]), "+{v[244:247]}"(v[3]), "+{v[248:251]}"(v[4]), [n] "=&s"(n), [a] "=&s"(a) : [l16] "v"(l16), [rs] "s"(rs), [s0] "s"(base), [s1] "s"(base + 4096) : "memory", "vcc", "scc", "v252");
        if (n || spin_fail(spins, P)) break;
    }
}
using u32x8 = __attribute__((ext_vector_type(8))) unsigned;
using u32x16 = __attribute__((ext_vector_type(16))) unsigned;
using u32x32 = __attribute__((ext_vector_type(32))) unsigned;
struct WRegs { u32x32 w[6]; u32x8 t; };
#define WREGS(W) "{v[0:31]}"(W.w[0]), "{v[32:63]}"(W.w[1]), "{v[64:95]}"(W.w[2]), "{v[96:127]}"(W.w[3]), "{v[128:159]}"(W.w[4]), "{v[160:191]}"(W.w[5]), "{v[192:199]}"(W.t)
__device__ __forceinline__ u32x32 cat32(u32x8 a, u32x8 b, u32x8 c, u32x8 d) {
    const u32x16 lo = __builtin_shufflevector(a, b, 0, 1, 2, 3, 4, 5, 6, 7, 8, 9, 10, 11, 12, 13, 14, 15);
    const u32x16 hi = __builtin_shufflevector(c, d, 0, 1, 2, 3, 4, 5, 6, 7, 8, 9, 10, 11, 12, 13, 14, 15);
    return __builtin_shufflevector(lo, hi, 0, 1, 2, 3, 4, 5, 6, 7, 8, 9, 10, 11, 12, 13, 14, 15, 16, 17, 18, 19, 20, 21, 22, 23, 24, 25, 26, 27, 28, 29, 30, 31);
}
template <class F>
__device__ __forceinline__ void load_weights(WRegs& W, unsigned wl, F src) {
    u32x8 f[25];
#pragma unroll
    for (int i = 0; i < 10; ++i)
#pragma unroll
        for (int j = 0; j < 3; ++j) {
            const bf16_t* p = src(i, j);
            const u32x4 a = *(const u32x4*)p, b = *(const u32x4*)(p + 512);
            if ((i & 1) || j) f[wi(i, j)] = __builtin_shufflevector(a, b, 0, 1, 2, 3, 4, 5, 6, 7);
            else { lds_st<u32x4>(wl + (i >> 1) * 2048, a); lds_st<u32x4>(wl + (i >> 1) * 2048 + 1024, b); }
        }
#pragma unroll
    for (int k = 0; k < 6; ++k) {
        const u32x16 lo = __builtin_shufflevector(f[4 * k], f[4 * k + 1], 0, 1, 2, 3, 4, 5, 6, 7, 8, 9, 10, 11, 12, 13, 14, 15);
        const u32x16 hi = __builtin_shufflevector(f[4 * k + 2], f[4 * k + 3], 0, 1, 2, 3, 4, 5, 6, 7, 8, 9, 10, 11, 12, 13, 14, 15);
        W.w[k] = __builtin_shufflevector(lo, hi, 0, 1, 2, 3, 4, 5, 6, 7, 8, 9, 10, 11, 12, 13, 14, 15, 16, 17, 18, 19, 20, 21, 22, 23, 24, 25, 26, 27, 28, 29, 30, 31);
    }
    W.t = f[24];
    asm volatile("" : "+{v[0:31]}"(W.w[0]), "+{v[32:63]}"(W.w[1]), "+{v[64:95]}"(W.w[2]), "+{v[96:127]}"(W.w[3]), "+{v[128:159]}"(W.w[4]), "+{v[160:191]}"(W.w[5]), "+{v[192:199]}"(W.t));
}
struct C1Regs { u32x8 lw0, lw1, z1; u32x4 z0; };
struct XAcc { u32x8 a0; u32x16 a1; };
template <bool HI>
__device__ __forceinline__ void xgemm_a(XAcc& A, const WRegs& W, unsigned xa, unsigned wl) {
#define XGA_ARGS : "=&{v[200:207]}"(A.a0), "=&{v[208:223]}"(A.a1) : WREGS(W), [xa] "v"(xa), [wl] "v"(wl) : "memory", "v224", "v225", "v226", "v227", "v228", "v229", "v230", "v231", "v232", "v233", "v234", "v235", "v236", "v237", "v238", "v239", "v240", "v241", "v242", "v243", "v244", "v245", "v246", "v247", "v248", "v249", "v250", "v251", "v252"
    if (HI) asm volatile(ASM_XG_HI_A XGA_ARGS); else asm volatile(ASM_XG_LO_A XGA_ARGS);
}
template <int V>
__device__ __forceinline__ void xgemm_b(XAcc& A, C1Regs& C, const WRegs& W, unsigned xa, unsigned wl, unsigned qs) {
#define XGB_ARGS(K) : K "{v[200:207]}"(A.a0), K "{v[208:223]}"(A.a1), "=&{v[224:227]}"(C.z0), "=&{v[228:235]}"(C.lw1), "=&{v[244:251]}"(C.z1) \
                    : WREGS(W), [xa] "v"(xa), [wl] "v"(wl), [qs] "s"(qs) : "memory", "v236", "v237", "v238", "v239", "v240", "v241", "v242", "v243", "v252"
    if (V == 0) asm volatile(ASM_XG_LO_B XGB_ARGS("+"));
    if (V == 1) asm volatile(ASM_XG_HI_B XGB_ARGS("+"));
    if (V == 2) asm volatile(ASM_XG_LO_AB XGB_ARGS("=&"));
    C.lw0 = A.a0;
}
__device__ __forceinline__ unsigned c1_dst(int wave, bool ktype) {
    const int ln = lane_id(), l15 = ln & 15, hf = (wave >> 1) & 1;
    const int rb16 = ((ln >> 2) & 4) * 16 + (ln >> 5) * 32;
    return (ktype ? L_KM + (l15 >> 2) * 128 + hf * 8 + (l15 & 3) * 2
                  : L_Q + (l15 >> 3) * 256 + ((l15 >> 1) & 1) * 128 + ((l15 >> 2) & 1) * 8 + hf * 4 + (l15 & 1) * 2) + (wave & 1) * 1536 + rb16;
}
template <bool VR>
__device__ __forceinline__ void mem_stage(const Params& P, rsrc_t rr, unsigned sbase, int w, unsigned abase, u32x4 (&v)[3]) {
#define PM_IN [l16] "v"(abase), [rs] "s"(rr), [s0] "s"(sbase), [s1] "s"(sbase + 4096)
#pragma unroll 1
    for (int spins = 1023;; spins += 1024) {
        unsigned n, a;
        if (w < 2) {
            if (VR) asm volatile(ASM_POLLV3 : "=&{v[200:203]}"(v[0]), "=&{v[204:207]}"(v[1]), "=&{v[208:211]}"(v[2]), [n] "=&s"(n), [a] "=&s"(a) : PM_IN, [s2] "s"(sbase + 8192) : "memory", "vcc", "scc", "v252");
            else asm volatile(ASM_POLLS3 : "=&{v[208:211]}"(v[0]), "=&{v[212:215]}"(v[1]), "=&{v[216:219]}"(v[2]), [n] "=&s"(n), [a] "=&s"(a) : PM_IN, [s2] "s"(sbase + 8192) : "memory", "vcc", "scc", "v240");
            lds_st<u32x4>(abase + 8192, v[2]);
        } else {
            if (VR) asm volatile(ASM_POLLV2 : "=&{v[200:203]}"(v[0]), "=&{v[204:207]}"(v[1]), [n] "=&s"(n), [a] "=&s"(a) : PM_IN : "memory", "vcc", "scc", "v252");
            else asm volatile(ASM_POLLS2 : "=&{v[208:211]}"(v[0]), "=&{v[212:215]}"(v[1]), [n] "=&s"(n), [a] "=&s"(a) : PM_IN : "memory", "vcc", "scc", "v240");
        }
        lds_st<u32x4>(abase, v[0]);
        lds_st<u32x4>(abase + 4096, v[1]);
        if (__builtin_amdgcn_readfirstlane(n) || spin_fail(spins, P)) break;
    }
}
template <bool POLL>
__device__ __forceinline__ void crit1(const Params& P, const WRegs& W, C1Regs& C, rsrc_t rr, unsigned sbase, int wave, unsigned wl, unsigned dsto
) {
    unsigned abase;
    asm volatile("v_and_b32 %0, 0x3ff, %1\n\tv_or_b32 %0, %2, %0" : "=&v"(abase) : "v"(wl), "s"(wave * 1024));
    ASM_ST_DECL;
#define C1_OUT "+{v[224:227]}"(C.z0), "+{v[244:251]}"(C.z1), "+{v[200:207]}"(C.lw0), "+{v[228:235]}"(C.lw1)
#define C1_IN WREGS(W), [ab] "v"(abase), [wl] "v"(wl), [dst] "v"(dsto)
    if constexpr (POLL) {
        asm volatile("" :: "v"(dsto));
        SB();
        ST(1);
        u32x4 v[3];
        mem_stage<false>(P, rr, sbase, wave, abase, v);
        ST(2);
        asm volatile(ASM_CRIT1 : C1_OUT, "+{v[208:211]}"(v[0]) ASM_ST_OUT : C1_IN : "memory", "v212", "v213", "v214", "v215", "v216", "v217", "v218", "v219", "v220", "v221", "v222", "v223", "v236", "v237", "v238", "v239", "v240", "v241");
    } else {
        asm volatile(ASM_CRIT1_N : C1_OUT ASM_ST_OUT : C1_IN : "memory", "v208", "v209", "v210", "v211", "v212", "v213", "v214", "v215", "v216", "v217", "v218", "v219", "v220", "v221", "v222", "v223", "v236", "v237", "v238", "v239", "v240", "v241");
    }
    SB();
    ASM_ST_SAVE(3);
}
#define STARG

#define SRG 5
__device__ __forceinline__ void s_role(const Params& P, int wave, int L, int h, int cpy) {
    const rsrc_t rr = mkrsrc(P.ws + L * RG_L, RG_L);
    const char* xn = P.ws + DATA0 + L * D_L + D_XN;
    auto xdma = [&](int seg, int hb) {
        const unsigned l16 = lane_id() * 16;
        const int mt = (wave >> 1) & 1;
        const char* g = xn + seg * 40960 + mt * 20480;
        if (hb == 0) {
            dma4(g + (wave & 1) * 4096, l16, L_XA + mt * 16384 + (wave & 1) * 4096);
            dmaq<0>(g + 16384 + (wave & 1) * 1024, l16, L_XB + mt * 4096 + (wave & 1) * 1024);
        } else {
            dma4(g + 8192, l16, L_XA + mt * 16384 + 8192); dma4(g + 12288, l16, L_XA + mt * 16384 + 12288);
            dmaq<2>(g + 16384, l16, L_XB + mt * 4096); dmaq<3>(g + 16384, l16, L_XB + mt * 4096);
        }
    };
    auto xcheck = [&](int seg, int hb) {
        for (int spins = 0;; ++spins) {
            const int mt = (wave >> 1) & 1;
            const unsigned l16 = lane_id() * 16;
            const unsigned d = L_XA + mt * 16384 + (hb ? 8192 : (wave & 1) * 4096) + l16, d2 = L_XB + mt * 4096 + (hb ? 2048 : (wave & 1) * 1024) + l16;
            unsigned m = umax4(0u, lds_ld<u32x4>(d2));
            if (hb) m = umax4(m, lds_ld<u32x4>(d2 + 1024));
#pragma unroll
            for (int k = 0; k < (hb ? 8 : 4); ++k) m = umax4(m, lds_ld<u32x4>(d + k * 1024));
            if (__all(m != SENT)) break;
            __builtin_amdgcn_s_sleep(8);
            if (spin_fail(spins * 8 + 7, P)) break;
            xdma(seg, hb);
            wait_vm<0>();
        }
    };
    const float SCL = 0.07216878364870322f * 1.4426950408889634f;
    auto run = [&](auto Cc) {
    constexpr int CLS = decltype(Cc)::value;
    WRegs wf;
    {
        const int lane = lane_id();
        const bf16_t* wsrc = (const bf16_t*)(P.ws + WT_QKV) + ((size_t)((wave >> 2) * 384 + h * 12 + (wave & 3) * 3) * 20) * 512 + lane * 8;
        load_weights(wf, L_W + wave * 10240 + lane * 16, [&](int i, int j) { return wsrc + (size_t)(j * 20 + 2 * (i < 8 ? (wave ^ i) : i)) * 512; });
    }
    if (CLS != 3) { xdma(0, 0); wait_vm<0>(); if (L) xcheck(0, 0); }
    else if (wave == 4 || wave == 7) { xdma(0, 1); wait_vm<0>(); if (L) xcheck(0, 1); }
    bar();
    const unsigned qs = (CLS == 3 ? L_KX : L_QX) + (wave & 1) * 3072 + ((wave >> 1) & 1) * 8;
    unsigned xa, wl, dsto;
    if (CLS == 3) { xa = L_XA + wave * 2048 + lane_id() * 16; wl = L_W + wave * 10240 + lane_id() * 16; dsto = c1_dst(wave, true); }
    STDECL;
    for (int t = 0; t < NSEG; ++t) {
        const unsigned slot = t & (NSLOT - 1);
        C1Regs c1;
        {
            XAcc acc;
            if (CLS != 3) { const unsigned l16 = lane_id() * 16; xa = L_XA + wave * 2048 + l16; wl = L_W + wave * 10240 + l16; }
            xgemm_a<CLS == 3>(acc, wf, xa, wl);
            bar();
            ST(7);
            if (CLS != 3 && t + 1 < NSEG) xdma(t + 1, 0);
            xgemm_b<CLS == 3>(acc, c1, wf, xa, wl, qs);
            ST(6);
            if (CLS != 3) {
                if (L && t + 1 < NSEG) { wait_vm<0>(); xcheck(t + 1, 0); }
                dsto = c1_dst(wave, false);
            }
        }
        ST(0);
        crit1<CLS != 3>(P, wf, c1, rr, RG_XM + slot * XM_SLOT + cpy * XM_COPY, wave, wl, dsto STARG);
        if constexpr (CLS == 0) {
            int ln = lane_id();
            const int sidx = ((ln & 15) < 8) ? 0x4444 : 0xEEEE;
            const unsigned l16 = ln * 16;
            bf16x16 F0, F1, F2, F3;
            F0 = lds_frag(L_KX + l16, 1024); F1 = lds_frag(L_KX + 6144 + l16, 1024); F2 = lds_frag(L_KX + 2048 + l16, 1024);
            f32x4 sc[3];
#pragma unroll
            for (int j = 0; j < 3; ++j) sc[j] = (f32x4){0.f, 0.f, 0.f, 0.f};
            unsigned kmo = L_KM + (ln >> 4) * 128 + (ln & 7) * 16;
            asm volatile("s_waitcnt lgkmcnt(0)" : "+v"(kmo), "+{v[244:247]}"(sc[0]), "+{v[252:255]}"(sc[1]), "+{v[240:243]}"(sc[2]),
                         "+v"(F0), "+v"(F1), "+v"(F2) :: "memory");
            SB();
            __builtin_amdgcn_s_barrier();
            asm volatile("" ::: "memory");
            __builtin_amdgcn_s_setprio(3);
            bf16x8 qa0 = lds_ld<bf16x8>(L_Q + l16);
            bf16x8 qa1 = lds_ld<bf16x8>(L_Q + 1024 + l16);
            F3 = lds_frag(kmo, 512);
            SB();
            SMF(qa0, F0, sc[0]);
            SB();
            F0 = lds_frag(kmo + 1024, 512);
            SMF(qa0, F1, sc[1]);
            SB();
            F1 = lds_frag(L_KX + 6144 + 2048 + l16, 1024);
            SMF(qa1, F2, sc[0]);
            SB();
            F2 = lds_frag(L_KX + 4096 + l16, 1024);
            SMF(qa0, F3, sc[2]);
            SB();
            qa0 = lds_ld<bf16x8>(L_Q + 2048 + l16);
            F3 = lds_frag(L_KX + 6144 + 4096 + l16, 1024);
            SMF(qa1, F0, sc[2]);
            SB();
            F0 = lds_frag(kmo + 2048, 512);
            SMF(qa1, F1, sc[1]);
            SB();
            ln = lane_id();
            const int l15 = ln & 15;
            const int rb16 = ((ln >> 2) & 4) * 16 + (ln >> 5) * 32;
            unsigned pb = L_Q + rb16 + (l15 & 7) * 2;
            unsigned pbx = L_Q + 128 + (l15 >> 3) * 256 + ((l15 >> 1) & 1) * 128 + rb16 + ((l15 >> 2) & 1) * 8 + (l15 & 1) * 2;
            unsigned poff = (l16 + ((ln < 8) ? h * 128 : 4096 - 128 + h * 512)) | ((ln >= 40) ? 0x40000000u : 0u);
            asm volatile("" : "+v"(pb), "+v"(pbx), "+v"(poff));
            SB();
            SMF(qa0, F2, sc[0]);
            SB();
            SMF(qa0, F3, sc[1]);
            SB();
            SMF(qa0, F0, sc[2]);
            SB();
            f32x4 mz;
            asm volatile(
                "s_nop 5\n\t"
                "v_permlane32_swap_b32 v244, v246\n\t"
                "v_permlane32_swap_b32 v245, v247\n\t"
                "v_permlane32_swap_b32 v252, v254\n\t"
                "v_permlane32_swap_b32 v253, v255\n\t"
                "v_permlane32_swap_b32 v240, v242\n\t"
                "v_permlane32_swap_b32 v241, v243\n\t"
                "v_pk_add_f32 v[244:245], v[244:245], v[246:247]\n\t"
                "v_pk_add_f32 v[246:247], v[252:253], v[254:255]\n\t"
                "v_pk_add_f32 v[240:241], v[240:241], v[242:243]\n\t"
                "s_nop 0\n\t"
                "v_max3_f32 v248, v244, v246, v240\n\t"
                "v_max3_f32 v249, v245, v247, v241\n\t"
                "s_nop 0\n\t"
                "v_max_f32_dpp v248, v248, v248 quad_perm:[1,0,3,2] row_mask:0xf bank_mask:0xf\n\t"
                "v_max_f32_dpp v249, v249, v249 quad_perm:[1,0,3,2] row_mask:0xf bank_mask:0xf\n\t"
                "s_nop 0\n\t"
                "v_max_f32_dpp v248, v248, v248 quad_perm:[2,3,0,1] row_mask:0xf bank_mask:0xf\n\t"
                "v_max_f32_dpp v249, v249, v249 quad_perm:[2,3,0,1] row_mask:0xf bank_mask:0xf\n\t"
                "s_nop 0\n\t"
                "v_max_f32_dpp v248, v248, v248 row_half_mirror row_mask:0xf bank_mask:0xf\n\t"
                "v_max_f32_dpp v249, v249, v249 row_half_mirror row_mask:0xf bank_mask:0xf\n\t"
                "s_nop 0\n\t"
                "v_max_f32_dpp v248, v248, v248 row_mirror row_mask:0xf bank_mask:0xf\n\t"
                "v_max_f32_dpp v249, v249, v249 row_mirror row_mask:0xf bank_mask:0xf\n\t"
                "v_mul_f32 v248, %[scl], v248\n\t"
                "v_mul_f32 v249, %[scl], v249\n\t"
                "v_fma_f32 v244, v244, %[scl], -v248\n\t"
                "v_fma_f32 v245, v245, %[scl], -v249\n\t"
                "v_fma_f32 v246, v246, %[scl], -v248\n\t"
                "v_fma_f32 v247, v247, %[scl], -v249\n\t"
                "v_exp_f32 v244, v244\n\t"
                "v_exp_f32 v245, v245\n\t"
                "v_fma_f32 v240, v240, %[scl], -v248\n\t"
                "v_fma_f32 v241, v241, %[scl], -v249\n\t"
                "v_exp_f32 v246, v246\n\t"
                "v_exp_f32 v247, v247\n\t"
                "v_exp_f32 v240, v240\n\t"
                "v_exp_f32 v241, v241\n\t"
                "v_add_f32 v250, v244, v246\n\t"
                "v_add_f32 v251, v245, v247\n\t"
                "v_fma_f32 v250, v240, 0.5, v250\n\t"
                "v_fma_f32 v251, v241, 0.5, v251\n\t"
                "s_nop 0\n\t"
                "v_add_f32_dpp v250, v250, v250 quad_perm:[1,0,3,2] row_mask:0xf bank_mask:0xf\n\t"
                "v_add_f32_dpp v251, v251, v251 quad_perm:[1,0,3,2] row_mask:0xf bank_mask:0xf\n\t"
                "s_nop 0\n\t"
                "v_add_f32_dpp v250, v250, v250 quad_perm:[2,3,0,1] row_mask:0xf bank_mask:0xf\n\t"
                "v_add_f32_dpp v251, v251, v251 quad_perm:[2,3,0,1] row_mask:0xf bank_mask:0xf\n\t"
                "s_nop 0\n\t"
                "v_add_f32_dpp v250, v250, v250 row_half_mirror row_mask:0xf bank_mask:0xf\n\t"
                "v_add_f32_dpp v251, v251, v251 row_half_mirror row_mask:0xf bank_mask:0xf\n\t"
                "s_nop 0\n\t"
                "v_add_f32_dpp v250, v250, v250 row_mirror row_mask:0xf bank_mask:0xf\n\t"
                "v_add_f32_dpp v251, v251, v251 row_mirror row_mask:0xf bank_mask:0xf\n\t"
                "v_rcp_f32 v250, v250\n\t"
                "v_rcp_f32 v251, v251\n\t"
                "s_nop 0\n\t"
                "v_pk_mul_f32 v[244:245], v[244:245], v[250:251]\n\t"
                "v_pk_mul_f32 v[246:247], v[246:247], v[250:251]\n\t"
                "v_pk_mul_f32 v[240:241], v[240:241], v[250:251]\n\t"
                "v_cvt_pk_bf16_f32 v244, |v244|, |v245|\n\t"
                "v_cvt_pk_bf16_f32 v246, |v246|, |v247|\n\t"
                "v_cvt_pk_bf16_f32 v240, |v240|, |v241|\n\t"
                "ds_write_b16 %[pbx], v244\n\t"
                "ds_write_b16_d16_hi %[pbx], v244 offset:16\n\t"
                "ds_write_b16 %[pbx], v246 offset:4\n\t"
                "ds_write_b16_d16_hi %[pbx], v246 offset:20\n\t"
                "ds_write_b16 %[pb], v240\n\t"
                "ds_write_b16_d16_hi %[pb], v240 offset:16"
                : "+{v[240:243]}"(sc[2]), "+{v[244:247]}"(sc[0]), "=&{v[248:251]}"(mz), "+{v[252:255]}"(sc[1])
                : [pb] "v"(pb), [pbx] "v"(pbx), [scl] "s"(SCL) : "memory");
            asm volatile("s_waitcnt lgkmcnt(0)" ::: "memory");
            const u32x4 v = lds_ld<u32x4>(L_Q + l16);
#pragma unroll
            for (int c = 0; c < (int)NCP; ++c) __builtin_amdgcn_raw_buffer_store_b128(v, rr, poff, RG_XP + slot * XP_SLOT + c * XP_COPY, 16);
            asm volatile("s_nop 1" :: "v"(v) : "memory");
            __builtin_amdgcn_s_setprio(0);
            ST(5);
        } else if constexpr (CLS == 1 || CLS == 4) {
            const int ln = lane_id(), l15 = ln & 15, l4 = ln >> 4;
            constexpr int mt = (CLS == 4);
            const unsigned qa = L_QX + mt * 6144 + ln * 16, kmo = L_KM + l4 * 128 + (ln & 7) * 16;
            const unsigned xo = mt ? (h & 3) * 1024 : 5120 + ((h >> 1) & 1) * 1024 + (h & 1) * 256;
            const unsigned pm = l4 < 2 ? (mt ? 4096 : 7168) + (h & 3) * 256 - xo + l15 * 16 + l4 * 8 : OOB;
            const unsigned so = RG_PX + slot * PX_SLOT + (h >> 2) * 8192 + xo;
            f32x4 sd;
#pragma unroll
            for (int r = 0; r < 4; ++r) sd[r] = (l4 * 4 + r > l15) ? -3.0e38f : 0.f;
            ASM_ST_DECL;
#define P4_ARGS : "+{v[240:243]}"(sd) ASM_ST_OUT : WREGS(wf), [qa] "v"(qa), [km] "v"(kmo), [pm] "v"(pm), [rr] "s"(rr), [so] "s"(so), [scl] "s"(SCL) : "memory", "v200", "v201", "v202", "v203", "v204", "v205", "v206", "v207", "v208", "v209", "v210", "v211", "v212", "v213", "v214", "v215", "v216", "v217", "v218", "v219", "v220", "v221", "v222", "v223", "v224", "v225", "v226", "v227", "v228", "v229", "v230", "v231", "v232", "v233", "v234", "v235", "v236", "v237", "v238", "v239", "v244", "v245", "v246", "v247", "v248", "v249", "v250", "v251"
            if constexpr (mt) asm volatile(ASM_P4_1 P4_ARGS); else asm volatile(ASM_P4_0 P4_ARGS);
            ASM_ST_SAVE(4);
            ST(5);
        } else {
            bar();
            ST(4);
            const int ln = lane_id();
            const unsigned l16 = ln * 16;
            const int l15 = ln & 15;
            {
                if (CLS == 3 && (wave == 4 || wave == 7)) {
                    if (t + 1 < NSEG) xdma(t + 1, 1);
                    wait_vm<0>();
                    const u32x4 s4 = sent4();
                    if (wave == 4) {
                        const unsigned poff = (l16 + ((ln < 8) ? h * 128 : 4096 - 128 + h * 512)) | ((ln >= 40 || t < 1) ? 0x40000000u : 0u);
#pragma unroll
                        for (int c = 0; c < (int)NCP; ++c) st128(s4, rr, poff, RG_XP + ((t - 1) & (NSLOT - 1)) * XP_SLOT + c * XP_COPY);
                    } else {
                        const unsigned pbase = RG_PX + ((t - 2) & (NSLOT - 1)) * PX_SLOT + (h >> 2) * 8192;
                        const unsigned o2 = ((ln & 16) ? 5120 + ((h >> 1) & 1) * 1024 + (h & 1) * 256 + (ln >> 5) * 512 : 4096 + (h & 3) * 256 + (ln >> 5) * 3072) + l15 * 16;
                        if (t >= 2) { st128(s4, rr, l16, pbase + (h & 3) * 1024); st128(s4, rr, o2, pbase); }
                    }
                    if (L && t + 1 < NSEG) xcheck(t + 1, 1);
                }
                ST(5);
            }
        }
        STFLUSH(t);
    }
    };
    if (wave == 0) run(std::integral_constant<int, 0>{});
    else if (wave == 1) run(std::integral_constant<int, 1>{});
    else if (wave == 2) run(std::integral_constant<int, 4>{});
    else if (wave == 3) run(std::integral_constant<int, 2>{});
    else run(std::integral_constant<int, 3>{});
}

__device__ __forceinline__ void v_role(const Params& P, int wave, int L, int h, int cpy) {
    const rsrc_t rr = mkrsrc(P.ws + L * RG_L, RG_L);
    WRegs wf;
    STDECL;
    if (wave < 4) {
        const char* xn = P.ws + DATA0 + L * D_L + D_XN;
        auto xdma = [&](int seg) {
            const unsigned l16 = lane_id() * 16;
            const char* g = xn + seg * 40960;
            dma4(g + wave * 4096, l16, L_XA + wave * 4096);
            dma4(g + 20480 + wave * 4096, l16, L_XA + 16384 + wave * 4096);
            dmaq<0>(g + 16384 + wave * 1024, l16, L_XB + wave * 1024);
            dmaq<0>(g + 20480 + 16384 + wave * 1024, l16, L_XB + 4096 + wave * 1024);
        };
        {
            const int lane = lane_id();
            const bf16_t* wsrc = (const bf16_t*)(P.ws + WT_QKV) + ((size_t)(768 + h * 12 + wave * 3) * 20) * 512 + lane * 8;
            load_weights(wf, L_W + wave * 10240 + lane * 16, [&](int i, int j) { return wsrc + (size_t)(j * 20 + 2 * (i < 8 ? (wave ^ i) : i)) * 512; });
        }
        auto xstage = [&](int seg) {
            for (int spins = 0;; ++spins) {
                xdma(seg);
                wait_vm<0>();
                if (!L) break;
                const unsigned l16 = lane_id() * 16, d = L_XA + wave * 4096 + l16;
                unsigned m = umax4(umax4(0u, lds_ld<u32x4>(L_XB + wave * 1024 + l16)), lds_ld<u32x4>(L_XB + 4096 + wave * 1024 + l16));
#pragma unroll
                for (int k = 0; k < 4; ++k) { m = umax4(m, lds_ld<u32x4>(d + k * 1024)); m = umax4(m, lds_ld<u32x4>(d + 16384 + k * 1024)); }
                if (__all(m != SENT)) break;
                __builtin_amdgcn_s_sleep(8);
                if (spin_fail(spins * 8 + 7, P)) break;
            }
        };
        const unsigned xa = L_XA + wave * 2048 + lane_id() * 16, wl = L_W + wave * 10240 + lane_id() * 16, dsto = c1_dst(wave, false);
        const unsigned qs = L_QX + (wave & 1) * 3072 + ((wave >> 1) & 1) * 8;
        for (int t = -2; t < NSEG - 1; ++t) {
            if (t + 2 < NSEG) xstage(t + 2);
            ST(7);
            XAcc acc;
            C1Regs c1;
            xgemm_b<2>(acc, c1, wf, xa, wl, qs + (t & 1) * 12288);
            ST(6);
            if (t >= -1) {
                crit1<false>(P, wf, c1, rr, RG_XM + ((t + 1) & (NSLOT - 1)) * XM_SLOT + cpy * XM_COPY, wave, wl, dsto STARG);
                bar();
                ST(4);
                STFLUSH(t + 1);
            }
            bar();
        }
    } else {
        const int w4 = wave - 4;
        const rsrc_t rd = mkrsrc(P.ws + DATA0 + L * D_L + D_VX, VXD * VX_SLOT);
        {
            const int lane = lane_id();
            const bf16_t* wsrc = (const bf16_t*)(P.ws + WT_O) + ((size_t)(h * 40 + w4 * 10) * 6) * 512 + lane * 8;
            load_weights(wf, L_W + wave * 10240 + lane * 16, [&](int i, int j) { return wsrc + (size_t)(i * 6 + 2 * j) * 512; });
        }
        bar();
        bar();
        for (int t = 0; t < NSEG; ++t) {
            const unsigned slot = t & (NSLOT - 1);
            ST(7);
            {
                const unsigned l16 = lane_id() * 16;
                const unsigned wl = L_W + wave * 10240 + l16, va = L_QX + (t & 1) * 12288 + l16;
                unsigned so = (t & (VXD - 1)) * VX_SLOT + (unsigned)(w4 * 10 * 32 + h) * 1024;
                asm volatile(ASM_VOX : [so] "+s"(so) : WREGS(wf), [l16] "v"(l16), [wl] "v"(wl), [va] "v"(va), [rd] "s"(rd) : "memory", "scc", "v200", "v201", "v202", "v203", "v204", "v205", "v206", "v207", "v208", "v209", "v210", "v211", "v212", "v213", "v214", "v215", "v216", "v217", "v218", "v219", "v220", "v221", "v222", "v223", "v224", "v225", "v226", "v227", "v228", "v229", "v230", "v231", "v232", "v233", "v234", "v235", "v236", "v237", "v238", "v239", "v240", "v241", "v242", "v243", "v244", "v245", "v246", "v247", "v248", "v249", "v250", "v251");
            }
            ST(0);
            unsigned wl = L_W + wave * 10240 + lane_id() * 16;
            bf16x16 lw = lds_frag(wl, 1024);
            u32x32 cz = {};
            unsigned vo = ((wl >> 8) & 1) * 16384 + ((wl >> 9) & 1) * 8192 + (wl & 255);
            asm volatile("" : "+{v[212:219]}"(lw), "+{v[220:251]}"(cz), "+v"(wl), "+v"(vo));
            wait_vm<0>();
            {
                const unsigned l16 = lane_id() * 16;
                if (l16 < 512) __builtin_amdgcn_raw_buffer_store_b32(t + 1, mkrsrc(P.ws + FL0, FL_BYTES), l16 >> 2, FL_VO + (L * 128 + w4 * 32 + h) * 128, 16);
                ST(1);
                u32x4 v[3];
                mem_stage<true>(P, rr, RG_XM + slot * XM_SLOT + cpy * XM_COPY, w4, L_MEM + w4 * 1024 + l16, v);
            }
            ST(2);
            bar();
            ST(3);
            unsigned l16 = wl & 1023u;
            const int sidx = (wl & 128u) ? 0xEEEE : 0x4444;
            unsigned so = RG_VM + slot * VM_SLOT + (unsigned)((w4 * 10 * 8 + (h >> 2)) * 1024 + (h & 3) * 256);
            ASM_ST_DECL;
            asm volatile(ASM_CRIT2 : [so] "+s"(so), "+{v[212:219]}"(lw), "+{v200}"(l16), "+{v[220:251]}"(cz) ASM_ST_OUT
                         : WREGS(wf), [wl] "v"(wl), [vo] "v"(vo), [sidx] "v"(sidx), [rr] "s"(rr)
                         : "memory", "scc", "v201", "v202", "v203", "v204", "v205", "v206", "v207", "v208", "v209", "v210", "v211");
            ASM_ST_SAVE(4);
            ST(5);
            STFLUSH(t);
        }
    }
}

constexpr unsigned R_RED = 0;
constexpr unsigned R_PART = 8192;
__device__ __forceinline__ void r_role(const Params& P, int wave, int L, int j, int cpy) {
    const int lane = lane_id(), l15 = lane & 15, l4 = lane >> 4;
    const unsigned l16 = lane * 16;
    const rsrc_t rr = mkrsrc(P.ws + L * RG_L, RG_L), rd = mkrsrc(P.ws + DATA0 + L * D_L, D_L);
    const int sidx = (l15 < 8) ? 0x4444 : 0xEEEE;
    unsigned offp[3];
    offp[0] = RG_XP + cpy * XP_COPY + 4096 + wave * 2048 + l16;
    offp[1] = offp[0] + 1024;
    offp[2] = (l15 < 8) ? RG_XP + cpy * XP_COPY + wave * 512 + l4 * 128 + l15 * 16 : OOB;
    const unsigned offv = RG_VM + (j * 8 + wave) * 1024 + l16;
    const unsigned offo = (wave < (int)NCP) ? RG_XM + wave * XM_COPY + j * 256 + lane * 4 : OOB;
    const int pubw = 1 - (wave >> 2);
    {
        const int row = (lane >> 2) & 7, col = 16 * j + 4 * (lane & 3) + 2 * (lane >> 5);
        __builtin_amdgcn_raw_buffer_store_b32(nosent(*(const unsigned*)(P.mem0 + row * DM + col)), rr, offo, 0, 16);
    }
    const int rb = 4 * (l4 & 1) + 2 * (lane >> 5);
    auto red_addr = [](int fl) { return R_RED + fl * 80 + (fl >> 4) * 16; };
    const unsigned rw0 = red_addr((((l15 >> 1) & 1) * 8 + rb) * 4 + (l15 >> 2)) + (l15 & 1) * 4 + (wave ^ (l15 >> 3)) * 8;
    const unsigned rrd = red_addr(lane);
    u32x16 vx;
    const rsrc_t rf = mkrsrc(P.ws + FL0, FL_BYTES);
    const unsigned fo = (lane & 3) * 128, fs = FL_VO + (L * 128 + (j / 10) * 32 + wave * 4) * 128, vxo = (unsigned)(j * 32 + wave * 4) * 1024;
    const unsigned xo = (l4 >> 1) * 256 + l15 * 16 + (l4 & 1) * 8, pw = R_PART + wave * 1024 + l16, pr = R_PART + (wave & 1) * 8192 + l16;
    const int fin = (wave + 2) >> 3;
    asm volatile("" : "={v[160:175]}"(vx));
    auto vx_fetch = [&](int t) {
        for (int spins = 0;; ++spins) {
            const unsigned f = __builtin_amdgcn_raw_buffer_load_b32(rf, fo, fs, 16);
            if (__all(f > (unsigned)t)) break;
            __builtin_amdgcn_s_sleep(2);
            if (spin_fail(spins, P)) break;
        }
        asm volatile("buffer_load_dwordx4 v[160:163], %1, %2, %3 offen sc1\n\tbuffer_load_dwordx4 v[164:167], %1, %2, %3 offen offset:1024 sc1\n\t"
                     "buffer_load_dwordx4 v[168:171], %1, %2, %3 offen offset:2048 sc1\n\tbuffer_load_dwordx4 v[172:175], %1, %2, %3 offen offset:3072 sc1"
                     : "+{v[160:175]}"(vx) : "v"(l16), "s"(rd), "s"(D_VX + (t & (VXD - 1)) * VX_SLOT + vxo) : "memory");
    };
    vx_fetch(0);
    STDECL;
    for (int t = 0; t < NSEG; ++t) {
        const unsigned slot = t & (NSLOT - 1);
        ST(0);
        u32x4 vm = sent4(), pm = vm;
        u32x8 pv = __builtin_shufflevector(vm, vm, 0, 1, 2, 3, 0, 1, 2, 3);
#pragma unroll 1
        for (int spins = 1023;; spins += 1024) {
            unsigned n, a;
            ASM_ST_DECL;
            asm volatile(ASM_RCRIT : "+{v[180:183]}"(vm), "+{v[184:191]}"(pv), "+{v[192:195]}"(pm), [n] "=&s"(n), [a] "=&s"(a) ASM_ST_OUT ASM_ST2_OUT
                         : [rs] "s"(rr), [ov] "v"(offv), [sv] "s"(slot * VM_SLOT), [o0] "v"(offp[0]), [o1] "v"(offp[1]), [o2] "v"(offp[2]), [sp] "s"(slot * XP_SLOT),
                           "{v[160:175]}"(vx), [sidx] "v"(sidx), [rw] "v"(rw0), [rd] "v"(rrd), [oo] "v"(offo),
                           [so] "s"(((t + 1) & (NSLOT - 1)) * XM_SLOT), [pub] "s"(pubw * (NSEG - 1 - t))
                         : "memory", "vcc", "scc", "v176", "v177", "v178", "v179", "v196", "v200", "v201", "v202", "v203", "v204", "v205", "v206", "v207", "v208", "v209", "v210", "v211", "v212", "v213", "v214", "v215");
            ASM_ST_SAVE(2); ASM_ST2_SAVE(1);
            if (__builtin_amdgcn_readfirstlane(n)) break;
            if (spin_fail(spins, P)) { bar(); break; }
        }
        ST(3);
        if (wave >= 4) __builtin_amdgcn_s_sleep(RXDEL);
        unsigned early;
#pragma unroll 1
        for (int spins = 1023;; spins += 1024) {
            unsigned n, sx = D_XR + (unsigned)(t * 2 + (wave & 1)) * 20480 + j * 512;
            const unsigned pxb = RG_PX + slot * PX_SLOT + wave * 8192;
            ASM_ST_DECL;
            asm volatile(ASM_RX : "+{v[160:175]}"(vx), [n] "=&s"(n), [a] "=&s"(early), [sx] "+s"(sx) ASM_ST_OUT ASM_ST2_OUT
                         : "{v[180:183]}"(vm), "{v[184:191]}"(pv), "{v[192:195]}"(pm), [l16] "v"(l16), [rs] "s"(rr), [s0] "s"(pxb), [s1] "s"(pxb + 4096), [rd] "s"(rd), [xo] "v"(xo),
                           [fo] "v"(fo), [rf] "s"(rf), [fs] "s"(fs), [tn] "s"(t + 1), [sv] "s"(D_VX + ((t + 1) & (VXD - 1)) * VX_SLOT + vxo),
                           [pw] "v"(pw), [pr] "v"(pr), [fin] "s"(fin)
                         : "memory", "vcc", "scc", "v196", "v200", "v201", "v202", "v203", "v204", "v205", "v206", "v207", "v208", "v209", "v210", "v211", "v212", "v213", "v214", "v215", "v216", "v217", "v218", "v219", "v220", "v221", "v222", "v223", "v224", "v225", "v226", "v227", "v228", "v229", "v230", "v231", "v232", "v233", "v234", "v235", "v236", "v237", "v238", "v239", "v240", "v241", "v242", "v243", "v244", "v245", "v246", "v247", "v248", "v249", "v250", "v251");
            ASM_ST_SAVE(4); ASM_ST2_SAVE(5);
            if (__builtin_amdgcn_readfirstlane(n)) break;
            early = 0;
            if (spin_fail(spins, P)) { bar(); break; }
        }
        ST(6);
        st128(sent4(), rr, offv, slot * VM_SLOT);
        __builtin_amdgcn_raw_buffer_store_b32(SENT, rr, offo, slot * XM_SLOT, 16);
        if (wave == 5 && t + AH < NSEG) st128(sent4(), rd, (lane >> 5) * 20480 + (lane & 31) * 16, D_XA + (unsigned)(t + AH) * 40960 + j * 512);
        if (t + 1 < NSEG && !__builtin_amdgcn_readfirstlane(early)) vx_fetch(t + 1);
        ST(7);
        STFLUSH(t);
    }
}

template <int WHICH>
__device__ __forceinline__ void w_role(const Params& P, int wave, int L, int cu) {
    constexpr int NT = (WHICH == 3) ? 2 : 1;
    constexpr int KS = 20, PW = 5;
    constexpr int N = (WHICH == 3) ? FF : DM;
    constexpr unsigned W_ST = 2 * 49152;
    const int ln = lane_id(), l15 = ln & 15, l4 = ln >> 4;
    const unsigned l16 = ln * 16;
    const rsrc_t rd = mkrsrc(P.ws + DATA0 + L * D_L, D_L);
    const int nt0 = (cu * 8 + wave) * NT;
    bf16x8 wf[NT][KS];
    {
        const bf16_t* w = (const bf16_t*)(P.ws + (WHICH == 1 ? WT_1 : WHICH == 2 ? WT_2 : WT_3)) + (size_t)nt0 * KS * 512 + ln * 8;
#pragma unroll
        for (int n = 0; n < NT; ++n)
#pragma unroll
            for (int k = 0; k < KS; ++k) wf[n][k] = *(const bf16x8*)(w + (n * KS + k) * 512);
    }
    f32x4 cs[NT];
    if (WHICH != 2) {
        const bf16x8 ones = __builtin_bit_cast(bf16x8, ((u32x4){0x3F803F80u, 0x3F803F80u, 0x3F803F80u, 0x3F803F80u}));
#pragma unroll
        for (int n = 0; n < NT; ++n) {
            cs[n] = (f32x4){0.f, 0.f, 0.f, 0.f};
#pragma unroll
            for (int k = 0; k < KS; ++k) MFMA(wf[n][k], ones, cs[n]);
        }
    }
    const int grp = nt0 / 10, sh = (WHICH == 1) ? ((grp & 1) ? -1 : 1) * ((grp >> 1) + 1) : 0;
    if (WHICH == 1) {
        const int zr = (grp & 1) ? S_ - 1 - l15 : l15;
        unsigned zv;
        asm volatile("v_mov_b32 %0, 0" : "=v"(zv));
        st128(((u32x4){zv, zv, zv, zv}), rd, (l15 <= (grp >> 1) && l4 < 2) ? toff(zr, nt0 * 16 + l4 * 8, DM) : OOB, D_HS);
    }
    constexpr unsigned D_IN = WHICH == 1 ? D_XA : WHICH == 2 ? D_HS : D_X1;
    constexpr unsigned D_OUT = WHICH == 1 ? D_HS : WHICH == 2 ? D_X1 : D_H2;
    u32x4 v[PW] = {};
    poll5_issue(rd, l16, D_IN + wave * PW * 1024, v);
    wait_vm<0>();
    STDECL;
    for (int t = 0; t < NSEG; ++t) {
        const unsigned ab = (t & 1) * 49152u;
        ST(0);
        {
            poll5(rd, l16, D_IN + (unsigned)t * 40960 + wave * PW * 1024, v, P);
            ST(1);
#pragma unroll
            for (int k = 0; k < PW; ++k) lds_st<u32x4>(ab + (wave * PW + k) * 1024 + l16, v[k]);
            if (WHICH != 2) {
                float s = 0.f, q = 0.f;
                const bf16x2 one2 = __builtin_bit_cast(bf16x2, 0x3F803F80u);
#pragma unroll
                for (int k = 0; k < PW; ++k)
#pragma unroll
                    for (int e = 0; e < 4; ++e) {
                        const unsigned u = v[k][e];
                        const bf16x2 x = __builtin_bit_cast(bf16x2, u);
                        s = __builtin_amdgcn_fdot2_f32_bf16(x, one2, s, false);
                        q = __builtin_amdgcn_fdot2_f32_bf16(x, x, q, false);
                    }
                sum2_rows(s, q);
                if (ln < 16) lds_st<f32x2>(W_ST + (t & 1) * 1024 + wave * 128 + ln * 8, (f32x2){s, q});
            }
        }
        bar();
        ST(2);
        u32x2 r1[2], sv[2];
        if (WHICH == 2) {
            const unsigned ro = toff(t * SEG + l15, nt0 * 16 + l4 * 4, DM);
#pragma unroll
            for (int mt = 0; mt < 2; ++mt) { r1[mt] = LD64(rd, ro + mt * (DM * 32), D_XA); sv[mt] = LD64(rd, (t * SEG + mt * 16 + l15) * 8, D_SS); }
        }
        float mr[2], rs[2];
        f32x2 st[2][4];
        if (WHICH != 2) {
#pragma unroll
            for (int w = 0; w < 8; ++w) st[w >> 2][w & 3] = lds_ld<f32x2>(W_ST + (t & 1) * 1024 + w * 128 + l15 * 8);
        }
        auto stats = [&](int mt) {
            const f32x2 a = (st[mt][0] + st[mt][1]) + (st[mt][2] + st[mt][3]);
            const float mean = a[0] * (1.0f / DM), var = __builtin_fmaxf(a[1] * (1.0f / DM) - mean * mean, 0.f);
            const float m1 = rbf(mean), d1 = ln_den(var), r1i = rcp(d1);
            if (WHICH == 1) {
                const float m2 = rbf((mean - m1) * r1i), d2 = ln_den(var * r1i * r1i);
                rs[mt] = r1i * rcp(d2); mr[mt] = -(m1 + m2 * d1) * rs[mt];
                if (cu == 0 && wave == 0)
                    __builtin_amdgcn_raw_buffer_store_b64((u32x2){__float_as_uint(m1), __float_as_uint(r1i)}, rd, ln < 16 ? (t * SEG + mt * 16 + ln) * 8 : OOB, D_SS, 16);
            } else { rs[mt] = r1i; mr[mt] = -m1 * r1i; }
            asm volatile("" : "+v"(rs[mt]), "+v"(mr[mt]));
        };
        f32x4 acc[NT][2];
        unsigned o[2][NT][2];
        auto fin = [&](int mt, int n, int hf) {
            float v[2];
#pragma unroll
            for (int e = 0; e < 2; ++e) {
                if (WHICH != 2) v[e] = gelu_tanh(__builtin_fmaf(acc[n][mt][hf * 2 + e], rs[mt], cs[n][hf * 2 + e] * mr[mt]));
                else v[e] = rbf(((e ? hi2f(r1[mt][hf]) : lo2f(r1[mt][hf])) - __uint_as_float(sv[mt][0])) * __uint_as_float(sv[mt][1])) + acc[n][mt][hf * 2 + e];
            }
            o[mt][n][hf] = nosent(pack2(v[0], v[1]));
            asm volatile("" : "+v"(o[mt][n][hf]));
        };
        const unsigned o3 = (unsigned)((l4 & 1) * 512 + (l4 >> 1) * 256 + l15 * 16), b3 = D_H2 + (unsigned)t * 98304 + (nt0 >> 1) * 1024;
#pragma unroll
        for (int n = 0; n < NT; ++n) { acc[n][0] = (f32x4){0.f, 0.f, 0.f, 0.f}; acc[n][1] = acc[n][0]; }
        {
            constexpr int RG = 8;
            bf16x8 a[RG];
#pragma unroll
            for (int u = 0; u < RG - 1; ++u) a[u] = lds_ld<bf16x8>(ab + l16 + u * 1024);
#pragma unroll
            for (int u = 0; u < 2 * KS; ++u) {
                if (u + RG - 1 < 2 * KS) a[(u + RG - 1) % RG] = lds_ld<bf16x8>(ab + l16 + (u + RG - 1) * 1024);
                SB();
#pragma unroll
                for (int n = 0; n < NT; ++n) MFMA(wf[n][u % KS], a[u % RG], acc[n][u / KS]);
                if (WHICH != 2 && (u == 4 || u == 12)) stats(u == 12);
                if (u == (WHICH == 3 ? 22 : 6)) poll5_issue(rd, l16, D_IN + (unsigned)(t + 1) * 40960 + wave * PW * 1024, v);
                if (WHICH == 3 && u == 38) wait_vm<0>();
                if (u >= 24 && u % 4 == 0 && (u - 24) / 4 < 2 * NT) fin(0, (u - 24) / 8, ((u - 24) / 4) & 1);
                if (WHICH == 3 && u == 38) st128(merge16((u32x2){o[0][0][0], o[0][0][1]}, (u32x2){o[0][NT - 1][0], o[0][NT - 1][1]}), rd, o3, b3);
                SB();
            }
        }
        ST(3);
#pragma unroll
        for (int k = 0; k < 2 * NT; ++k) fin(1, k >> 1, k & 1);
        if (WHICH == 3) {
            st128(merge16((u32x2){o[1][0][0], o[1][0][1]}, (u32x2){o[1][NT - 1][0], o[1][NT - 1][1]}), rd, o3, b3 + 49152);
            ST(4);
            if (t + AH < NSEG) { st128(sent4(), rd, l16, b3 + AH * 98304); st128(sent4(), rd, l16, b3 + AH * 98304 + 49152); }
        } else {
            wait_vm<0>();
            const int orow = t * SEG + (l4 & 1) * 16 + l15 + sh;
            st128(merge16((u32x2){o[0][0][0], o[0][0][1]}, (u32x2){o[1][0][0], o[1][0][1]}), rd, (unsigned)orow < (unsigned)S_ ? toff(orow, nt0 * 16 + (l4 >> 1) * 8, DM) : OOB, D_OUT);
            ST(4);
            if (t + AH < NSEG) st128(sent4(), rd, orow + AH * SEG < S_ ? toff(orow + AH * SEG, nt0 * 16 + (l4 >> 1) * 8, DM) : OOB, D_OUT);
        }
        STFLUSH(t);
    }
    wait_vm<0>();
    asm volatile("" :: "v"(v[0]), "v"(v[1]), "v"(v[2]), "v"(v[3]), "v"(v[4]));
}

__device__ __forceinline__ void w4_role(const Params& P, int wave, int L, int cu) {
    const int ln = lane_id(), l15 = ln & 15, l4 = ln >> 4;
    const unsigned l16 = ln * 16;
    const rsrc_t rd = mkrsrc(P.ws + DATA0 + L * D_L, D_L);
    STDECL;
    if (wave < 6) {
        u32x32 w[5];
        {
            const char* ws = P.ws + WT_4 + (size_t)(cu * 5 * 48 + wave * 8) * 1024 + l16;
#pragma unroll
            for (int n = 0; n < 5; ++n) {
                u32x8 f[4];
#pragma unroll
                for (int i = 0; i < 4; ++i) f[i] = __builtin_shufflevector(*(const u32x4*)(ws + n * 49152 + i * 2048), *(const u32x4*)(ws + n * 49152 + i * 2048 + 1024), 0, 1, 2, 3, 4, 5, 6, 7);
                w[n] = cat32(f[0], f[1], f[2], f[3]);
            }
            asm volatile("" : "+{v[0:31]}"(w[0]), "+{v[32:63]}"(w[1]), "+{v[64:95]}"(w[2]), "+{v[96:127]}"(w[3]), "+{v[128:159]}"(w[4]));
        }
        unsigned budget = 1u << 17;
        for (int t = 0; t < NSEG; ++t) {
            ST(0);
            const unsigned gb = D_H2 + (unsigned)t * 98304 + wave * 8192;
            u32x4 can;
            for (int spins = 0;; ++spins) {
                can = LD128(rd, l16, gb + 7168);
                if (__all(umax4(0u, can) != SENT)) break;
                __builtin_amdgcn_s_sleep(1);
                if (spin_fail(spins, P)) { budget = 0; break; }
            }
            ST(1);
            asm volatile(ASM_W4_M : [n] "+s"(budget)
                         : "{v[188:191]}"(can), "{v[0:31]}"(w[0]), "{v[32:63]}"(w[1]), "{v[64:95]}"(w[2]), "{v[96:127]}"(w[3]), "{v[128:159]}"(w[4]),
                           [ld] "v"(l16 + wave * 1024 + (t & 1) * 61440), [l16] "v"(l16), [rs] "s"(rd), [s0] "s"(gb), [s1] "s"(gb + 4096), [s2] "s"(gb + 49152), [s3] "s"(gb + 53248)
                         : "memory", "vcc", "scc", "v160", "v161", "v162", "v163", "v164", "v165", "v166", "v167", "v168", "v169", "v170", "v171", "v172", "v173", "v174", "v175", "v176", "v177", "v178", "v179", "v180", "v181", "v182", "v183", "v184", "v185", "v186", "v187", "v192", "v193", "v194", "v195", "v196", "v197", "v198", "v199", "v200", "v201", "v202", "v203", "v204", "v205", "v206", "v207", "v208", "v209", "v210", "v211", "v212", "v213", "v214", "v215", "v216", "v217", "v218", "v219", "v220", "v221", "v222", "v223", "v224", "v225", "v226", "v227", "v228", "v229", "v230", "v231", "v232", "v233", "v234", "v235", "v236", "v237", "v238", "v239", "v240", "v241", "v242", "v243", "v244");
            ST(2);
            bar();
            ST(3);
            STFLUSH(t);
        }
    } else {
        const int mt = wave - 6;
        const rsrc_t rn = mkrsrc(P.ws + DATA0 + D_L, D_L), rr = mkrsrc(P.ws, RG_L), ro = mkrsrc(P.out, ACT);
        const unsigned o8 = (l4 >> 1) * 256 + l15 * 16 + (l4 & 1) * 8, o16 = (l4 & 1) * 512 + (l4 >> 1) * 256 + l15 * 16;
        const unsigned q8 = l15 * (DM * 2) + l4 * 8, q16 = l15 * (DM * 2) + (l4 & 1) * 32 + (l4 >> 1) * 16;
        for (int t = 0; t < NSEG; ++t) {
            const unsigned sb = (unsigned)(t * 2 + mt) * 20480 + cu * 2560;
            ST(0);
            f32x4 res[5];
            for (int spins = 0;; ++spins) {
                u32x2 r1[5], r2[5];
                unsigned m = 0;
#pragma unroll
                for (int n = 0; n < 5; ++n) { r1[n] = LD64(rd, o8 + n * 512, D_X1 + sb); r2[n] = LD64(rd, o8 + n * 512, D_XA + sb); }
#pragma unroll
                for (int n = 0; n < 5; ++n) {
                    m = umax4(m, (u32x4){r1[n][0], r1[n][1], r2[n][0], r2[n][1]});
                    res[n] = (f32x4){lo2f(r1[n][0]), hi2f(r1[n][0]), lo2f(r1[n][1]), hi2f(r1[n][1])} + (f32x4){lo2f(r2[n][0]), hi2f(r2[n][0]), lo2f(r2[n][1]), hi2f(r2[n][1])};
                }
                if (__all(m != SENT)) break;
                __builtin_amdgcn_s_sleep(8);
                if (spin_fail(spins * 4 + 3, P)) break;
            }
            asm volatile("" : "+v"(res[0]), "+v"(res[1]), "+v"(res[2]), "+v"(res[3]), "+v"(res[4]));
            ST(1);
            bar();
            ST(2);
            {
                const unsigned la = (t & 1) * 61440 + mt * 30720 + l16;
                f32x4 c[30];
#pragma unroll
                for (int i = 0; i < 30; ++i) c[i] = lds_ld<f32x4>(la + ((i % 5) * 6 + i / 5) * 1024);
#pragma unroll
                for (int i = 0; i < 30; ++i) { res[i % 5] += c[i]; asm volatile("" : "+v"(res[i % 5])); }
            }
            u32x2 o[5];
#pragma unroll
            for (int n = 0; n < 5; ++n) o[n] = (u32x2){pack2(res[n][0], res[n][1]), pack2(res[n][2], res[n][3])};
            if (L) {
                const unsigned ob = (unsigned)(t * 2 + mt) * 20480 + cu * 160;
                st128_<0>(merge16(o[0], o[1]), ro, q16, ob); st128_<0>(merge16(o[2], o[3]), ro, q16, ob + 64);
                __builtin_amdgcn_raw_buffer_store_b64(o[4], ro, q8, ob + 128, 0);
            } else {
                float s = 0.f, q = 0.f;
                const bf16x2 one2 = __builtin_bit_cast(bf16x2, 0x3F803F80u);
#pragma unroll
                for (int e = 0; e < 10; ++e) {
                    const unsigned u = o[e >> 1][e & 1];
                    const bf16x2 x = __builtin_bit_cast(bf16x2, u);
                    s = __builtin_amdgcn_fdot2_f32_bf16(x, one2, s, false);
                    q = __builtin_amdgcn_fdot2_f32_bf16(x, x, q, false);
                }
                sum2_rows(s, q);
                const unsigned st = RG_ST + (unsigned)(t * 2 + mt) * 1024;
                __builtin_amdgcn_raw_buffer_store_b64((u32x2){nosent(__float_as_uint(s)), nosent(__float_as_uint(q))}, rr, ln < 16 ? ln * 8 : OOB, st + cu * 128, 16);
                ST(3);
                st128(merge16(o[0], o[1]), rn, o16, D_XR + sb); st128(merge16(o[2], o[3]), rn, o16, D_XR + sb + 1024);
                __builtin_amdgcn_raw_buffer_store_b64(o[4], rn, o8, D_XR + sb + 2048, 16);
                __builtin_amdgcn_s_sleep(W4SL);
                for (int spins = 0;; ++spins) {
                    const u32x2 a = LD64(rr, ln * 8, st), b = LD64(rr, ln * 8 + 512, st);
                    s = __uint_as_float(a[0]) + __uint_as_float(b[0]); q = __uint_as_float(a[1]) + __uint_as_float(b[1]);
                    if (__all(umax4(0u, (u32x4){a[0], a[1], b[0], b[1]}) != SENT)) break;
                    asm volatile("" ::: "memory");
                    if (spin_fail(spins, P)) break;
                }
                ST(4);
                sum2_rows(s, q);
                const float mean = s * (1.0f / DM), var = __builtin_fmaxf(q * (1.0f / DM) - mean * mean, 0.f);
                const float r = rcp(ln_den(var)), mr = -rbf(mean) * r;
#pragma unroll
                for (int n = 0; n < 5; ++n)
                    o[n] = (u32x2){nosent(pack2(__builtin_fmaf(res[n][0], r, mr), __builtin_fmaf(res[n][1], r, mr))), nosent(pack2(__builtin_fmaf(res[n][2], r, mr), __builtin_fmaf(res[n][3], r, mr)))};
                st128(merge16(o[0], o[1]), rn, o16, D_XN + sb); st128(merge16(o[2], o[3]), rn, o16, D_XN + sb + 1024);
                __builtin_amdgcn_raw_buffer_store_b64(o[4], rn, o8, D_XN + sb + 2048, 16);
                if (t + AH < NSEG) {
                    const u32x4 s4 = sent4();
                    st128(s4, rn, l16, D_XN + sb + AH * 40960); st128(s4, rn, l16, D_XN + sb + AH * 40960 + 1024);
                    st128(s4, rn, ln < 32 ? l16 : OOB, D_XN + sb + AH * 40960 + 2048);
                }
            }
            ST(5);
            STFLUSH(t);
        }
    }
}

__global__ __launch_bounds__(512, 2) void mega_kernel(Params P) {
    const int wg = blockIdx.x;
    const int wave = __builtin_amdgcn_readfirstlane(threadIdx.x >> 6);
    const int L = (wg >> 3) & 1, x = wg & 3, r = (wg >> 4) * 2 + ((wg >> 2) & 1), cpy = wg & (NCP - 1);
    if (r < 8) s_role(P, wave, L, x * 8 + r, cpy);
    else if (r < 16) v_role(P, wave ^ 4, L, x * 8 + r - 8, cpy);
    else if (r < 26) r_role(P, wave, L, (r - 16) * 4 + x, cpy);
    else {
        const int i = (r - 26) * 4 + x;
        if (i < 5) w_role<1>(P, wave, L, i);
        else if (i < 10) w_role<2>(P, wave, L, i - 5);
        else if (i < 16) w_role<3>(P, wave, L, i - 10);
        else w4_role(P, wave, L, i - 16);
    }
}

__device__ __forceinline__ void xtile_body(int mt, const bf16_t* __restrict__ x, char* __restrict__ ws) {
    __shared__ float red[2][16][17];
    const int r15 = threadIdx.x & 15, slot = threadIdx.x >> 4;
    u32x4 v[5];
    float s = 0.f;
#pragma unroll
    for (int c = 0; c < 5; ++c) {
        v[c] = *(const u32x4*)(x + (size_t)(mt * 16 + r15) * DM + (slot * 5 + c) * 8);
#pragma unroll
        for (int e = 0; e < 4; ++e) s += lo2f(v[c][e]) + hi2f(v[c][e]);
    }
    red[0][slot][r15] = s;
    __syncthreads();
    s = 0.f;
#pragma unroll
    for (int k = 0; k < 16; ++k) s += red[0][k][r15];
    const float mean = s * (1.0f / DM);
    float q = 0.f;
#pragma unroll
    for (int c = 0; c < 5; ++c)
#pragma unroll
        for (int e = 0; e < 4; ++e) { const float a = lo2f(v[c][e]) - mean, b = hi2f(v[c][e]) - mean; q += a * a + b * b; }
    red[1][slot][r15] = q;
    __syncthreads();
    q = 0.f;
#pragma unroll
    for (int k = 0; k < 16; ++k) q += red[1][k][r15];
    const float m1 = rbf(mean), r = 1.0f / ln_den(q * (1.0f / DM));
#pragma unroll
    for (int c = 0; c < 5; ++c) {
        const unsigned o = (unsigned)(mt * 80 + slot * 5 + c) * 256 + r15 * 16;
        *(u32x4*)(ws + DATA0 + D_XR + o) = v[c];
        u32x4 n;
#pragma unroll
        for (int e = 0; e < 4; ++e) n[e] = pack2(rbf(lo2f(v[c][e]) - m1) * r, rbf(hi2f(v[c][e]) - m1) * r);
        *(u32x4*)(ws + DATA0 + D_XN + o) = n;
    }
}

struct Prep1 { TileJobs J; const bf16_t* x; char* ws; };
__global__ __launch_bounds__(256) void prep1_kernel(Prep1 A) {
    int b = blockIdx.x;
    if (b < 256) {
        for (unsigned i = b * 256 + threadIdx.x; i < FL0 / 16; i += 256 * 256) ((u32x4*)A.ws)[i] = (u32x4){SENT, SENT, SENT, SENT};
        if (b == 0) for (unsigned i = threadIdx.x; i < FL_BYTES / 16; i += 256) ((u32x4*)(A.ws + FL0))[i] = (u32x4){0u, 0u, 0u, 0u};
        for (int u = b >> 3; u < 10; u += 32) {
            if (u == 0) continue;
            const unsigned n16 = (AH + 1) * ((u % 5 == 4) ? 98304 : 40960) / 16;
            u32x4* p = (u32x4*)(A.ws + DATA0 + (u / 5) * D_L + (u % 5 + 1) * ACT);
            for (unsigned i = (b & 7) * 256 + threadIdx.x; i < n16; i += 2048) p[i] = (u32x4){SENT, SENT, SENT, SENT};
        }
        return;
    }
    b -= 256;
    if (b < S_ / 16) { xtile_body(b, A.x, A.ws); return; }
    tile_body(A.J, b - S_ / 16);
}

extern "C" void kernel_launch(void* const* d_in, const int* in_sizes, int n_in, void* d_out, int out_size, void* d_ws,
                              size_t ws_size, hipStream_t stream) {
    char* ws = (char*)d_ws;
    Params P;
    P.ws = ws; P.mem0 = (const bf16_t*)d_in[1]; P.out = (bf16_t*)d_out;
    (void)hipFuncSetAttribute((const void*)mega_kernel, hipFuncAttributeMaxDynamicSharedMemorySize, MEGA_LDS);

    Prep1 A1;
    int blk = 0;
    auto job = [&](int i, int in, unsigned o, int Kd, int Nd, int ksh) { A1.J.j[i] = TileJob{(const bf16_t*)d_in[in], (bf16_t*)(ws + o), i == 3 ? 1 : 0, Nd, ksh, blk}; blk += (Kd / 32) * (Nd / 128); };
    job(0, 2, WT_QKV, DM, HD, 20);
    job(1, 3, WT_QKV + HD * DM * 2, DM, HD, 20);
    job(2, 4, WT_QKV + 2 * HD * DM * 2, DM, HD, 20);
    job(3, 5, WT_O, HD, DM, 6);
    job(4, 8, WT_1, DM, DM, 20);
    job(5, 9, WT_2, DM, DM, 20);
    job(6, 10, WT_3, DM, FF, 20);
    job(7, 11, WT_4, FF, DM, 48);
    A1.x = (const bf16_t*)d_in[0]; A1.ws = ws;
    prep1_kernel<<<256 + S_ / 16 + blk, 256, 0, stream>>>(A1);
    mega_kernel<<<256, 512, MEGA_LDS, stream>>>(P);
}
```

```cpp
#include <hip/hip_runtime.h>
#include <stdint.h>
#include <type_traits>
#include <cstdio>

typedef unsigned short bf16_t;
using bf16x2 = __attribute__((ext_vector_type(2))) __bf16;
using bf16x8 = __attribute__((ext_vector_type(8))) __bf16;
using bf16x16 = __attribute__((ext_vector_type(16))) __bf16;
using f32x2 = __attribute__((ext_vector_type(2))) float;
using f32x4 = __attribute__((ext_vector_type(4))) float;
using u32x2 = __attribute__((ext_vector_type(2))) unsigned;
using u32x4 = __attribute__((ext_vector_type(4))) unsigned;
typedef __amdgpu_buffer_rsrc_t rsrc_t;

__device__ __forceinline__ unsigned pack2(float a, float b) {
    bf16x2 r = __builtin_convertvector((f32x2){a, b}, bf16x2);
    return __builtin_bit_cast(unsigned, r);
}
__device__ __forceinline__ float rbf(float f) { return __uint_as_float(pack2(f, 0.f) << 16); }
__device__ __forceinline__ float lo2f(unsigned u) { return __uint_as_float(u << 16); }
__device__ __forceinline__ float hi2f(unsigned u) { return __uint_as_float(u & 0xFFFF0000u); }
__device__ __forceinline__ rsrc_t mkrsrc(const void* p, unsigned bytes) {
    return __builtin_amdgcn_make_buffer_rsrc((void*)p, 0, bytes, 0x00020000);
}

constexpr int S_ = 2048, DM = 640, NH = 32, DH = 192, HD = NH * DH  , SEG = 32, NSEG = 64;
constexpr int FF = 1536;

#define XMAP
#define NCPY 4
constexpr unsigned NSLOT = 4, NCP = NCPY;
constexpr unsigned XP_COPY = 20480, XP_SLOT = NCP * XP_COPY;
constexpr unsigned XM_COPY = 10240, XM_SLOT = NCP * XM_COPY;
constexpr unsigned VM_SLOT = 40 * 8 * 1024;
constexpr unsigned PX_SLOT = 8 * 8192;
constexpr unsigned RG_XP = 0, RG_XM = RG_XP + NSLOT * XP_SLOT, RG_VM = RG_XM + NSLOT * XM_SLOT, RG_PX = RG_VM + NSLOT * VM_SLOT;
constexpr unsigned VX_SLOT = 40 * 32 * 1024;
constexpr unsigned RG_ST = RG_PX + NSLOT * PX_SLOT, RG_L = RG_ST + NSEG * 8 * 256;
constexpr unsigned FL0 = 2 * RG_L, FL_VO = 512, FL_BYTES = FL_VO + 2 * 128 * 128;
constexpr unsigned ACT = S_ * DM * 2;
constexpr unsigned D_XR = 0, D_XN = ACT, D_XA = 2 * ACT, D_HS = 3 * ACT, D_X1 = 4 * ACT, D_H2 = 5 * ACT, D_SS = D_H2 + S_ * FF * 2;
#define PXROT 5
#define W4SL 6
#define RXDEL 4
#define VXD 32
#define VXAUX 16
constexpr unsigned D_VX = D_SS + S_ * 8, D_L = D_VX + VXD * VX_SLOT;
constexpr unsigned DATA0 = (FL0 + FL_BYTES + 4095) & ~4095u;
constexpr int AH = 3;
constexpr unsigned WT0 = DATA0 + 2 * D_L;
constexpr unsigned WT_QKV = WT0, WT_O = WT_QKV + 3 * HD * DM * 2, WT_1 = WT_O + HD * DM * 2, WT_2 = WT_1 + DM * DM * 2;
constexpr unsigned WT_3 = WT_2 + DM * DM * 2, WT_4 = WT_3 + DM * FF * 2, WS_END = WT_4 + DM * FF * 2;

struct TileJob { const bf16_t* W; bf16_t* out; int perm, N, ksh, blk0; };
struct TileJobs { TileJob j[8]; };
__device__ __forceinline__ void tile_body(const TileJobs& J, int b) {
    __shared__ __attribute__((aligned(16))) bf16_t t[32][136];
    int ji = 0;
#pragma unroll
    for (int i = 1; i < 8; ++i) if (b >= J.j[i].blk0) ji = i;
    const bf16_t* W = J.j[ji].W; bf16_t* out = J.j[ji].out;
    const int N = J.j[ji].N, ksh = J.j[ji].ksh, perm = J.j[ji].perm;
    b -= J.j[ji].blk0;
    const int nb = N / 128;
    const int ksg = b / nb, n0 = (b % nb) * 128;
    const int tid = threadIdx.x;
    const int grp = ksg / ksh, ks = ksg % ksh;
#pragma unroll
    for (int i = 0; i < 2; ++i) {
        const int u = tid + i * 256, r = u >> 4, c = u & 15;
        const int row = perm ? grp * DH + ks * 16 + (r >> 4) * 96 + (r & 15) : ksg * 32 + r;
        *(u32x4*)&t[r][c * 8] = *(const u32x4*)(W + (size_t)row * N + n0 + c * 8);
    }
    __syncthreads();
    const int lane = tid & 63;
#pragma unroll
    for (int i = 0; i < 2; ++i) {
        const int ntl = (tid >> 6) + i * 4, n = ntl * 16 + (lane & 15);
        unsigned w[4];
#pragma unroll
        for (int j = 0; j < 4; ++j) {
            const int kk = perm ? (j >> 1) * 16 + (lane >> 4) * 4 + (j & 1) * 2 : (lane >> 4) * 8 + 2 * j;
            w[j] = (unsigned)t[kk][n] | ((unsigned)t[kk + 1][n] << 16);
        }
        *(u32x4*)(out + ((size_t)(grp * (N / 16) + n0 / 16 + ntl) * ksh + ks) * 512 + lane * 8) = (u32x4){w[0], w[1], w[2], w[3]};
    }
}

#define SB() __builtin_amdgcn_sched_barrier(0)
#define LAS __attribute__((address_space(3)))
template <class T> __device__ __forceinline__ T lds_ld(unsigned a) { return *(const LAS T*)(a); }
template <class T> __device__ __forceinline__ void lds_st(unsigned a, T v) { *(LAS T*)(a) = v; }
constexpr unsigned OOB = 0x7FFFFF00u;
template <int N> __device__ __forceinline__ void wait_vm() { asm volatile("s_waitcnt vmcnt(%0)" :: "i"(N) : "memory"); }
__device__ __forceinline__ void bar() {
    asm volatile("s_waitcnt lgkmcnt(0)" ::: "memory");
    __builtin_amdgcn_s_barrier();
    asm volatile("" ::: "memory");
}
__device__ __forceinline__ int lane_id() {
    int ln;
    asm volatile("v_mbcnt_lo_u32_b32 %0, -1, 0\n\tv_mbcnt_hi_u32_b32 %0, -1, %0" : "=v"(ln));
    return ln;
}
__device__ __forceinline__ void swap16(unsigned& a, unsigned& b) { asm volatile("s_nop 3\n\tv_permlane16_swap_b32 %0, %1\n\ts_nop 1" : "+v"(a), "+v"(b)); }
__device__ __forceinline__ void swap32(unsigned& a, unsigned& b) { asm volatile("s_nop 3\n\tv_permlane32_swap_b32 %0, %1\n\ts_nop 1" : "+v"(a), "+v"(b)); }
__device__ __forceinline__ float sum_x16(float x) { unsigned a = __float_as_uint(x), b = a; swap16(a, b); return __uint_as_float(a) + __uint_as_float(b); }
__device__ __forceinline__ float sum_x32(float x) { unsigned a = __float_as_uint(x), b = a; swap32(a, b); return __uint_as_float(a) + __uint_as_float(b); }
__device__ __forceinline__ float max_x16(float x) { unsigned a = __float_as_uint(x), b = a; swap16(a, b); return __builtin_fmaxf(__uint_as_float(a), __uint_as_float(b)); }
__device__ __forceinline__ float max_x32(float x) { unsigned a = __float_as_uint(x), b = a; swap32(a, b); return __builtin_fmaxf(__uint_as_float(a), __uint_as_float(b)); }
__device__ __forceinline__ void sum2_rows(float& s, float& q) {
    unsigned a = __float_as_uint(s), b = a, c = __float_as_uint(q), d = c;
    asm volatile("s_nop 2\n\tv_permlane16_swap_b32 %0, %1\n\tv_permlane16_swap_b32 %2, %3\n\tv_add_f32 %0, %0, %1\n\tv_add_f32 %2, %2, %3\n\tv_mov_b32 %1, %0\n\tv_mov_b32 %3, %2\n\t"
                 "s_nop 1\n\tv_permlane32_swap_b32 %0, %1\n\tv_permlane32_swap_b32 %2, %3\n\tv_add_f32 %0, %0, %1\n\tv_add_f32 %2, %2, %3" : "+v"(a), "+v"(b), "+v"(c), "+v"(d));
    s = __uint_as_float(a); q = __uint_as_float(c);
}
__device__ __forceinline__ u32x4 merge16(u32x2 a, u32x2 b) {
    unsigned a0 = a[0], a1 = a[1], b0 = b[0], b1 = b[1];
    asm volatile("s_nop 3\n\tv_permlane16_swap_b32 %0, %2\n\tv_permlane16_swap_b32 %1, %3\n\ts_nop 1" : "+v"(a0), "+v"(a1), "+v"(b0), "+v"(b1));
    return (u32x4){a0, a1, b0, b1};
}
template <int AUXV>
__device__ __forceinline__ void st128_(u32x4 v, rsrc_t r, unsigned voff, unsigned soff) {
    __builtin_amdgcn_raw_buffer_store_b128(v, r, voff, soff, AUXV);
    asm volatile("s_nop 1" :: "v"(v) : "memory");
}
#define st128(V, R, VO_, SO_) st128_<16>(V, R, VO_, SO_)
#define LD128(R, VO_, SO_) __builtin_amdgcn_raw_buffer_load_b128(R, VO_, SO_, 16)
#define LD64(R, VO_, SO_) __builtin_amdgcn_raw_buffer_load_b64(R, VO_, SO_, 16)
#define BF8(X) __builtin_bit_cast(bf16x8, X)
__device__ __forceinline__ float gelu_tanh(float x) {
    const float c0 = -2.0f * 0.7978845608028654f * 1.4426950408889634f, c1 = c0 * 0.044715f;
    const float e = __builtin_amdgcn_exp2f(x * __builtin_fmaf(x * x, c1, c0));
    return x * __builtin_amdgcn_rcpf(1.0f + e);
}
#define MFMA(A_, B_, C_) C_ = __builtin_amdgcn_mfma_f32_16x16x32_bf16(A_, B_, C_, 0, 0, 0)
#define SMF(A_, B_, C_) C_ = __builtin_amdgcn_smfmac_f32_16x16x64_bf16(A_, B_, C_, sidx, 0, 0)
__device__ __forceinline__ float ln_den(float var) { return rbf(__builtin_amdgcn_sqrtf(rbf(rbf(var) + 1e-5f))); }
__device__ __forceinline__ float rcp(float x) { return __builtin_amdgcn_rcpf(x); }

struct Params {
    char* ws;
    const bf16_t* mem0;
    bf16_t* out;
};

#define RLX_AGENT __ATOMIC_RELAXED, __HIP_MEMORY_SCOPE_AGENT
#define SENT 0xFFFFFFFFu
constexpr int SPIN_CAP = 400000;
__device__ __forceinline__ bool spin_fail(int spins, const Params& P) {
    if ((spins & 1023) == 1023) {
        const rsrc_t rt = mkrsrc(P.ws + FL0, FL_BYTES);
        unsigned o;
        asm volatile("v_mov_b32 %0, 0x100" : "=v"(o));
        if (__builtin_amdgcn_readfirstlane(__builtin_amdgcn_raw_buffer_load_b32(rt, o, 0, 16)) != 0u) return true;
        if (spins > SPIN_CAP) { __builtin_amdgcn_raw_buffer_store_b32(o, rt, o, 0, 16); return true; }
    }
    return false;
}
__device__ __forceinline__ bool ok4(u32x4 v) { return v[0] != SENT && v[1] != SENT && v[2] != SENT && v[3] != SENT; }
__device__ __forceinline__ unsigned nosent(unsigned d) { return d < 0xFFFFFFFEu ? d : 0xFFFFFFFEu; }
__device__ __forceinline__ u32x4 nosent4(u32x4 v) { return (u32x4){nosent(v[0]), nosent(v[1]), nosent(v[2]), nosent(v[3])}; }
__device__ __forceinline__ unsigned umax3(unsigned a, unsigned b, unsigned c) { const unsigned m = a > b ? a : b; return m > c ? m : c; }
__device__ __forceinline__ unsigned umax4(unsigned m, u32x4 v) { return umax3(umax3(m, v[0], v[1]), v[2], v[3]); }
__device__ __forceinline__ u32x4 sent4() { unsigned sv; asm volatile("v_mov_b32 %0, -1" : "=v"(sv)); return (u32x4){sv, sv, sv, sv}; }
template <int N>
__device__ __forceinline__ void poll(rsrc_t rs, unsigned base, const unsigned (&off)[N], u32x4 (&v)[N], const Params& P) {
    for (int spins = 0;; ++spins) {
        unsigned m = 0;
#pragma unroll
        for (int k = 0; k < N; ++k) v[k] = LD128(rs, off[k], base);
#pragma unroll
        for (int k = 0; k < N; ++k) m = umax4(m, v[k]);
        if (__all(m != SENT)) return;
        asm volatile("" ::: "memory");
        if (spin_fail(spins, P)) return;
    }
}
__device__ __forceinline__ bf16x16 cat16(bf16x8 a, bf16x8 b) {
    return __builtin_shufflevector(a, b, 0, 1, 2, 3, 4, 5, 6, 7, 8, 9, 10, 11, 12, 13, 14, 15);
}
__device__ __forceinline__ bf16x8 half8(bf16x16 v, int h) {
    return h ? __builtin_shufflevector(v, v, 8, 9, 10, 11, 12, 13, 14, 15) : __builtin_shufflevector(v, v, 0, 1, 2, 3, 4, 5, 6, 7);
}
__device__ __forceinline__ bf16x16 lds_frag(unsigned a, unsigned step) { return cat16(lds_ld<bf16x8>(a), lds_ld<bf16x8>(a + step)); }
template <int I>
__device__ __forceinline__ void dmaq(const char* g, unsigned l16, unsigned dst) {
    asm volatile("s_mov_b32 m0, %2\n\ts_nop 0\n\tglobal_load_lds_dwordx4 %0, %1 offset:%3 sc1" :: "v"(l16), "s"(g), "s"(dst), "i"(I * 1024) : "memory");
}
__device__ __forceinline__ void dma4(const char* g, unsigned l16, unsigned dst) {
    asm volatile("s_mov_b32 m0, %2\n\ts_nop 0\n\tglobal_load_lds_dwordx4 %0, %1 sc1\n\tglobal_load_lds_dwordx4 %0, %1 offset:1024 sc1\n\t"
                 "global_load_lds_dwordx4 %0, %1 offset:2048 sc1\n\tglobal_load_lds_dwordx4 %0, %1 offset:3072 sc1" :: "v"(l16), "s"(g), "s"(dst) : "memory");
}
__device__ __forceinline__ unsigned toff(int row, int col, int ncols) {
    return (unsigned)(row >> 4) * (unsigned)(ncols * 32) + (unsigned)(col >> 3) * 256u + (unsigned)(row & 15) * 16u + (unsigned)(col & 7) * 2u;
}

#define ASM_ST
#define ASM_ST_DECL
#define ASM_ST_OUT
#define ASM_ST_SAVE(k)
#define ASM_ST2
#define ASM_ST2_OUT
#define ASM_ST2_SAVE(k)
#define STDECL
#define ST(k)
#define STFLUSH(t)

constexpr unsigned L_MEM = 0;
constexpr unsigned L_Q = 10240;
constexpr unsigned L_KM = 13312;
constexpr unsigned L_QX = 16384;
constexpr unsigned L_KX = 28672;
constexpr unsigned L_XB = 40960;
constexpr unsigned L_XA = 49152;
constexpr unsigned L_W = 81920;
constexpr int MEGA_LDS = 163840;
constexpr int wi(int i, int j) { return (i >> 1) * 5 + ((i & 1) ? 2 + j : j - 1); }

#define RPUB2
#define ASM_XG_LO_A \
    "s_waitcnt lgkmcnt(0)\n\t" \
    "ds_read_b128 v[224:227], %[xa] offset:0\n\t" \
    "ds_read_b128 v[244:247], %[wl] offset:0\n\t" \
    "ds_read_b128 v[228:231], %[xa] offset:16384\n\t" \
    "ds_read_b128 v[232:235], %[xa] offset:1024\n\t" \
    "ds_read_b128 v[236:239], %[xa] offset:17408\n\t" \
    "ds_read_b128 v[248:251], %[wl] offset:1024\n\t" \
    "s_waitcnt lgkmcnt(5)\n\t" \
    "v_mfma_f32_16x16x32_bf16 v[204:207], v[0:3], v[224:227], 0\n\t" \
    "v_xor_b32 v252, 0x800, %[xa]\n\t" \
    "ds_read_b128 v[240:243], v252 offset:0\n\t" \
    "v_mfma_f32_16x16x32_bf16 v[208:211], v[8:11], v[224:227], 0\n\t" \
    "s_waitcnt lgkmcnt(5)\n\t" \
    "v_mfma_f32_16x16x32_bf16 v[200:203], v[244:247], v[224:227], 0\n\t" \
    "s_waitcnt lgkmcnt(4)\n\t" \
    "v_mfma_f32_16x16x32_bf16 v[216:219], v[0:3], v[228:231], 0\n\t" \
    "ds_read_b128 v[224:227], v252 offset:16384\n\t" \
    "v_mfma_f32_16x16x32_bf16 v[220:223], v[8:11], v[228:231], 0\n\t" \
    "v_mfma_f32_16x16x32_bf16 v[212:215], v[244:247], v[228:231], 0\n\t" \
    "s_waitcnt lgkmcnt(4)\n\t" \
    "v_mfma_f32_16x16x32_bf16 v[204:207], v[4:7], v[232:235], v[204:207]\n\t" \
    "ds_read_b128 v[228:231], v252 offset:1024\n\t" \
    "v_mfma_f32_16x16x32_bf16 v[208:211], v[12:15], v[232:235], v[208:211]\n\t" \
    "ds_read_b128 v[244:247], %[wl] offset:2048\n\t" \
    "s_waitcnt lgkmcnt(4)\n\t" \
    "v_mfma_f32_16x16x32_bf16 v[200:203], v[248:251], v[232:235], v[200:203]\n\t" \
    "v_mfma_f32_16x16x32_bf16 v[216:219], v[4:7], v[236:239], v[216:219]\n\t" \
    "ds_read_b128 v[232:235], v252 offset:17408\n\t" \
    "v_mfma_f32_16x16x32_bf16 v[220:223], v[12:15], v[236:239], v[220:223]\n\t" \
    "v_mfma_f32_16x16x32_bf16 v[212:215], v[248:251], v[236:239], v[212:215]\n\t" \
    "s_waitcnt lgkmcnt(4)\n\t" \
    "v_mfma_f32_16x16x32_bf16 v[204:207], v[24:27], v[240:243], v[204:207]\n\t" \
    "v_xor_b32 v252, 0x1000, %[xa]\n\t" \
    "ds_read_b128 v[236:239], v252 offset:0\n\t" \
    "v_mfma_f32_16x16x32_bf16 v[208:211], v[32:35], v[240:243], v[208:211]\n\t" \
    "ds_read_b128 v[248:251], %[wl] offset:3072\n\t" \
    "v_mfma_f32_16x16x32_bf16 v[200:203], v[16:19], v[240:243], v[200:203]\n\t" \
    "s_waitcnt lgkmcnt(5)\n\t" \
    "v_mfma_f32_16x16x32_bf16 v[216:219], v[24:27], v[224:227], v[216:219]\n\t" \
    "ds_read_b128 v[240:243], v252 offset:16384\n\t" \
    "v_mfma_f32_16x16x32_bf16 v[220:223], v[32:35], v[224:227], v[220:223]\n\t" \
    "v_mfma_f32_16x16x32_bf16 v[212:215], v[16:19], v[224:227], v[212:215]\n\t" \
    "s_waitcnt lgkmcnt(5)\n\t" \
    "v_mfma_f32_16x16x32_bf16 v[204:207], v[28:31], v[228:231], v[204:207]\n\t" \
    "ds_read_b128 v[224:227], v252 offset:1024\n\t" \
    "v_mfma_f32_16x16x32_bf16 v[208:211], v[36:39], v[228:231], v[208:211]\n\t" \
    "v_mfma_f32_16x16x32_bf16 v[200:203], v[20:23], v[228:231], v[200:203]\n\t" \
    "s_waitcnt lgkmcnt(4)\n\t" \
    "v_mfma_f32_16x16x32_bf16 v[216:219], v[28:31], v[232:235], v[216:219]\n\t" \
    "ds_read_b128 v[228:231], v252 offset:17408\n\t" \
    "v_mfma_f32_16x16x32_bf16 v[220:223], v[36:39], v[232:235], v[220:223]\n\t" \
    "v_mfma_f32_16x16x32_bf16 v[212:215], v[20:23], v[232:235], v[212:215]\n\t" \
    "s_waitcnt lgkmcnt(4)\n\t" \
    "v_mfma_f32_16x16x32_bf16 v[204:207], v[40:43], v[236:239], v[204:207]\n\t" \
    "v_xor_b32 v252, 0x1800, %[xa]\n\t" \
    "ds_read_b128 v[232:235], v252 offset:0\n\t" \
    "v_mfma_f32_16x16x32_bf16 v[208:211], v[48:51], v[236:239], v[208:211]\n\t" \
    "v_mfma_f32_16x16x32_bf16 v[200:203], v[244:247], v[236:239], v[200:203]\n\t" \
    "s_waitcnt lgkmcnt(3)\n\t" \
    "v_mfma_f32_16x16x32_bf16 v[216:219], v[40:43], v[240:243], v[216:219]\n\t" \
    "ds_read_b128 v[236:239], v252 offset:16384\n\t" \
    "v_mfma_f32_16x16x32_bf16 v[220:223], v[48:51], v[240:243], v[220:223]\n\t" \
    "v_mfma_f32_16x16x32_bf16 v[212:215], v[244:247], v[240:243], v[212:215]\n\t" \
    "s_waitcnt lgkmcnt(3)\n\t" \
    "v_mfma_f32_16x16x32_bf16 v[204:207], v[44:47], v[224:227], v[204:207]\n\t" \
    "ds_read_b128 v[240:243], v252 offset:1024\n\t" \
    "v_mfma_f32_16x16x32_bf16 v[208:211], v[52:55], v[224:227], v[208:211]\n\t" \
    "ds_read_b128 v[244:247], %[wl] offset:8192\n\t" \
    "v_mfma_f32_16x16x32_bf16 v[200:203], v[248:251], v[224:227], v[200:203]\n\t" \
    "s_waitcnt lgkmcnt(4)\n\t" \
    "v_mfma_f32_16x16x32_bf16 v[216:219], v[44:47], v[228:231], v[216:219]\n\t" \
    "ds_read_b128 v[224:227], v252 offset:17408\n\t" \
    "v_mfma_f32_16x16x32_bf16 v[220:223], v[52:55], v[228:231], v[220:223]\n\t" \
    "v_mfma_f32_16x16x32_bf16 v[212:215], v[248:251], v[228:231], v[212:215]\n\t" \
    "s_waitcnt lgkmcnt(4)\n\t" \
    "v_mfma_f32_16x16x32_bf16 v[204:207], v[64:67], v[232:235], v[204:207]\n\t" \
    "v_and_b32 v252, 0x3ff, %[xa]\n\t" \
    "ds_read_b128 v[228:231], v252 offset:40960\n\t" \
    "v_mfma_f32_16x16x32_bf16 v[208:211], v[72:75], v[232:235], v[208:211]\n\t" \
    "ds_read_b128 v[248:251], %[wl] offset:9216\n\t" \
    "v_mfma_f32_16x16x32_bf16 v[200:203], v[56:59], v[232:235], v[200:203]\n\t" \
    "s_waitcnt lgkmcnt(5)\n\t" \
    "v_mfma_f32_16x16x32_bf16 v[216:219], v[64:67], v[236:239], v[216:219]\n\t" \
    "ds_read_b128 v[232:235], v252 offset:45056\n\t" \
    "v_mfma_f32_16x16x32_bf16 v[220:223], v[72:75], v[236:239], v[220:223]\n\t" \
    "v_mfma_f32_16x16x32_bf16 v[212:215], v[56:59], v[236:239], v[212:215]\n\t" \
    "s_waitcnt lgkmcnt(5)\n\t" \
    "v_mfma_f32_16x16x32_bf16 v[204:207], v[68:71], v[240:243], v[204:207]\n\t" \
    "ds_read_b128 v[236:239], v252 offset:41984\n\t" \
    "v_mfma_f32_16x16x32_bf16 v[208:211], v[76:79], v[240:243], v[208:211]\n\t" \
    "v_mfma_f32_16x16x32_bf16 v[200:203], v[60:63], v[240:243], v[200:203]\n\t" \
    "s_waitcnt lgkmcnt(4)\n\t" \
    "v_mfma_f32_16x16x32_bf16 v[216:219], v[68:71], v[224:227], v[216:219]\n\t" \
    "ds_read_b128 v[240:243], v252 offset:46080\n\t" \
    "v_mfma_f32_16x16x32_bf16 v[220:223], v[76:79], v[224:227], v[220:223]\n\t" \
    "v_mfma_f32_16x16x32_bf16 v[212:215], v[60:63], v[224:227], v[212:215]\n\t" \
    "s_waitcnt lgkmcnt(4)\n\t" \
    "v_mfma_f32_16x16x32_bf16 v[204:207], v[160:163], v[228:231], v[204:207]\n\t" \
    "v_mfma_f32_16x16x32_bf16 v[208:211], v[168:171], v[228:231], v[208:211]\n\t" \
    "v_mfma_f32_16x16x32_bf16 v[200:203], v[244:247], v[228:231], v[200:203]\n\t" \
    "s_waitcnt lgkmcnt(2)\n\t" \
    "v_mfma_f32_16x16x32_bf16 v[216:219], v[160:163], v[232:235], v[216:219]\n\t" \
    "v_mfma_f32_16x16x32_bf16 v[220:223], v[168:171], v[232:235], v[220:223]\n\t" \
    "v_mfma_f32_16x16x32_bf16 v[212:215], v[244:247], v[232:235], v[212:215]\n\t" \
    "s_waitcnt lgkmcnt(1)\n\t" \
    "v_mfma_f32_16x16x32_bf16 v[204:207], v[164:167], v[236:239], v[204:207]\n\t" \
    "v_mfma_f32_16x16x32_bf16 v[208:211], v[172:175], v[236:239], v[208:211]\n\t" \
    "v_mfma_f32_16x16x32_bf16 v[200:203], v[248:251], v[236:239], v[200:203]\n\t" \
    "s_waitcnt lgkmcnt(0)\n\t" \
    "v_mfma_f32_16x16x32_bf16 v[216:219], v[164:167], v[240:243], v[216:219]\n\t" \
    "v_mfma_f32_16x16x32_bf16 v[220:223], v[172:175], v[240:243], v[220:223]\n\t" \
    "v_mfma_f32_16x16x32_bf16 v[212:215], v[248:251], v[240:243], v[212:215]\n\t" \
    "s_nop 7\n\t"
#define ASM_XG_LO_B \
    "s_waitcnt lgkmcnt(0)\n\t" \
    "v_xor_b32 v252, 0x2000, %[xa]\n\t" \
    "ds_read_b128 v[224:227], v252 offset:0\n\t" \
    "ds_read_b128 v[244:247], %[wl] offset:4096\n\t" \
    "ds_read_b128 v[228:231], v252 offset:16384\n\t" \
    "ds_read_b128 v[232:235], v252 offset:1024\n\t" \
    "ds_read_b128 v[236:239], v252 offset:17408\n\t" \
    "ds_read_b128 v[248:251], %[wl] offset:5120\n\t" \
    "s_waitcnt lgkmcnt(5)\n\t" \
    "v_mfma_f32_16x16x32_bf16 v[204:207], v[80:83], v[224:227], v[204:207]\n\t" \
    "v_xor_b32 v252, 0x2800, %[xa]\n\t" \
    "ds_read_b128 v[240:243], v252 offset:0\n\t" \
    "v_mfma_f32_16x16x32_bf16 v[208:211], v[88:91], v[224:227], v[208:211]\n\t" \
    "s_waitcnt lgkmcnt(5)\n\t" \
    "v_mfma_f32_16x16x32_bf16 v[200:203], v[244:247], v[224:227], v[200:203]\n\t" \
    "s_waitcnt lgkmcnt(4)\n\t" \
    "v_mfma_f32_16x16x32_bf16 v[216:219], v[80:83], v[228:231], v[216:219]\n\t" \
    "ds_read_b128 v[224:227], v252 offset:16384\n\t" \
    "v_mfma_f32_16x16x32_bf16 v[220:223], v[88:91], v[228:231], v[220:223]\n\t" \
    "v_mfma_f32_16x16x32_bf16 v[212:215], v[244:247], v[228:231], v[212:215]\n\t" \
    "s_waitcnt lgkmcnt(4)\n\t" \
    "v_mfma_f32_16x16x32_bf16 v[204:207], v[84:87], v[232:235], v[204:207]\n\t" \
    "ds_read_b128 v[228:231], v252 offset:1024\n\t" \
    "v_mfma_f32_16x16x32_bf16 v[208:211], v[92:95], v[232:235], v[208:211]\n\t" \
    "ds_read_b128 v[244:247], %[wl] offset:6144\n\t" \
    "s_waitcnt lgkmcnt(4)\n\t" \
    "v_mfma_f32_16x16x32_bf16 v[200:203], v[248:251], v[232:235], v[200:203]\n\t" \
    "v_mfma_f32_16x16x32_bf16 v[216:219], v[84:87], v[236:239], v[216:219]\n\t" \
    "ds_read_b128 v[232:235], v252 offset:17408\n\t" \
    "v_mfma_f32_16x16x32_bf16 v[220:223], v[92:95], v[236:239], v[220:223]\n\t" \
    "v_mfma_f32_16x16x32_bf16 v[212:215], v[248:251], v[236:239], v[212:215]\n\t" \
    "s_waitcnt lgkmcnt(4)\n\t" \
    "v_mfma_f32_16x16x32_bf16 v[204:207], v[104:107], v[240:243], v[204:207]\n\t" \
    "v_xor_b32 v252, 0x3000, %[xa]\n\t" \
    "ds_read_b128 v[236:239], v252 offset:0\n\t" \
    "v_mfma_f32_16x16x32_bf16 v[208:211], v[112:115], v[240:243], v[208:211]\n\t" \
    "ds_read_b128 v[248:251], %[wl] offset:7168\n\t" \
    "v_mfma_f32_16x16x32_bf16 v[200:203], v[96:99], v[240:243], v[200:203]\n\t" \
    "s_waitcnt lgkmcnt(5)\n\t" \
    "v_mfma_f32_16x16x32_bf16 v[216:219], v[104:107], v[224:227], v[216:219]\n\t" \
    "ds_read_b128 v[240:243], v252 offset:16384\n\t" \
    "v_mfma_f32_16x16x32_bf16 v[220:223], v[112:115], v[224:227], v[220:223]\n\t" \
    "v_mfma_f32_16x16x32_bf16 v[212:215], v[96:99], v[224:227], v[212:215]\n\t" \
    "s_waitcnt lgkmcnt(5)\n\t" \
    "v_mfma_f32_16x16x32_bf16 v[204:207], v[108:111], v[228:231], v[204:207]\n\t" \
    "ds_read_b128 v[224:227], v252 offset:1024\n\t" \
    "v_mfma_f32_16x16x32_bf16 v[208:211], v[116:119], v[228:231], v[208:211]\n\t" \
    "v_mfma_f32_16x16x32_bf16 v[200:203], v[100:103], v[228:231], v[200:203]\n\t" \
    "s_waitcnt lgkmcnt(4)\n\t" \
    "v_mfma_f32_16x16x32_bf16 v[216:219], v[108:111], v[232:235], v[216:219]\n\t" \
    "ds_read_b128 v[228:231], v252 offset:17408\n\t" \
    "v_mfma_f32_16x16x32_bf16 v[220:223], v[116:119], v[232:235], v[220:223]\n\t" \
    "v_mfma_f32_16x16x32_bf16 v[212:215], v[100:103], v[232:235], v[212:215]\n\t" \
    "s_waitcnt lgkmcnt(4)\n\t" \
    "v_mfma_f32_16x16x32_bf16 v[204:207], v[120:123], v[236:239], v[204:207]\n\t" \
    "v_xor_b32 v252, 0x3800, %[xa]\n\t" \
    "ds_read_b128 v[232:235], v252 offset:0\n\t" \
    "v_mfma_f32_16x16x32_bf16 v[208:211], v[128:131], v[236:239], v[208:211]\n\t" \
    "v_mfma_f32_16x16x32_bf16 v[200:203], v[244:247], v[236:239], v[200:203]\n\t" \
    "s_waitcnt lgkmcnt(3)\n\t" \
    "v_mfma_f32_16x16x32_bf16 v[216:219], v[120:123], v[240:243], v[216:219]\n\t" \
    "ds_read_b128 v[236:239], v252 offset:16384\n\t" \
    "v_mfma_f32_16x16x32_bf16 v[220:223], v[128:131], v[240:243], v[220:223]\n\t" \
    "v_mfma_f32_16x16x32_bf16 v[212:215], v[244:247], v[240:243], v[212:215]\n\t" \
    "s_waitcnt lgkmcnt(3)\n\t" \
    "v_mfma_f32_16x16x32_bf16 v[204:207], v[124:127], v[224:227], v[204:207]\n\t" \
    "ds_read_b128 v[240:243], v252 offset:1024\n\t" \
    "v_mfma_f32_16x16x32_bf16 v[208:211], v[132:135], v[224:227], v[208:211]\n\t" \
    "v_mfma_f32_16x16x32_bf16 v[200:203], v[248:251], v[224:227], v[200:203]\n\t" \
    "s_waitcnt lgkmcnt(3)\n\t" \
    "v_mfma_f32_16x16x32_bf16 v[216:219], v[124:127], v[228:231], v[216:219]\n\t" \
    "ds_read_b128 v[224:227], v252 offset:17408\n\t" \
    "v_mfma_f32_16x16x32_bf16 v[220:223], v[132:135], v[228:231], v[220:223]\n\t" \
    "v_mfma_f32_16x16x32_bf16 v[212:215], v[248:251], v[228:231], v[212:215]\n\t" \
    "s_waitcnt lgkmcnt(3)\n\t" \
    "v_mfma_f32_16x16x32_bf16 v[204:207], v[144:147], v[232:235], v[204:207]\n\t" \
    "v_and_b32 v252, 0x3ff, %[xa]\n\t" \
    "ds_read_b128 v[228:231], v252 offset:43008\n\t" \
    "v_mov_b64 v[244:245], 0\n\t" \
    "v_mfma_f32_16x16x32_bf16 v[208:211], v[152:155], v[232:235], v[208:211]\n\t" \
    "v_mov_b64 v[246:247], 0\n\t" \
    "v_mfma_f32_16x16x32_bf16 v[200:203], v[136:139], v[232:235], v[200:203]\n\t" \
    "v_mov_b64 v[248:249], 0\n\t" \
    "s_waitcnt lgkmcnt(3)\n\t" \
    "v_mfma_f32_16x16x32_bf16 v[216:219], v[144:147], v[236:239], v[216:219]\n\t" \
    "ds_read_b128 v[232:235], v252 offset:44032\n\t" \
    "v_mov_b64 v[250:251], 0\n\t" \
    "v_mfma_f32_16x16x32_bf16 v[220:223], v[152:155], v[236:239], v[220:223]\n\t" \
    "v_mfma_f32_16x16x32_bf16 v[212:215], v[136:139], v[236:239], v[212:215]\n\t" \
    "s_waitcnt lgkmcnt(3)\n\t" \
    "v_mfma_f32_16x16x32_bf16 v[204:207], v[148:151], v[240:243], v[204:207]\n\t" \
    "ds_read_b128 v[236:239], v252 offset:47104\n\t" \
    "v_mfma_f32_16x16x32_bf16 v[208:211], v[156:159], v[240:243], v[208:211]\n\t" \
    "v_mfma_f32_16x16x32_bf16 v[200:203], v[140:143], v[240:243], v[200:203]\n\t" \
    "s_waitcnt lgkmcnt(3)\n\t" \
    "v_mfma_f32_16x16x32_bf16 v[216:219], v[148:151], v[224:227], v[216:219]\n\t" \
    "ds_read_b128 v[240:243], v252 offset:48128\n\t" \
    "v_mfma_f32_16x16x32_bf16 v[220:223], v[156:159], v[224:227], v[220:223]\n\t" \
    "v_mfma_f32_16x16x32_bf16 v[212:215], v[140:143], v[224:227], v[212:215]\n\t" \
    "s_waitcnt lgkmcnt(3)\n\t" \
    "v_mfma_f32_16x16x32_bf16 v[204:207], v[184:187], v[228:231], v[204:207]\n\t" \
    "v_add_u32 v252, %[qs], v252\n\t" \
    "v_mfma_f32_16x16x32_bf16 v[208:211], v[192:195], v[228:231], v[208:211]\n\t" \
    "v_mov_b64 v[224:225], 0\n\t" \
    "v_mfma_f32_16x16x32_bf16 v[200:203], v[176:179], v[228:231], v[200:203]\n\t" \
    "v_mov_b64 v[226:227], 0\n\t" \
    "s_waitcnt lgkmcnt(2)\n\t" \
    "v_mfma_f32_16x16x32_bf16 v[204:207], v[188:191], v[232:235], v[204:207]\n\t" \
    "v_mfma_f32_16x16x32_bf16 v[208:211], v[196:199], v[232:235], v[208:211]\n\t" \
    "v_mfma_f32_16x16x32_bf16 v[200:203], v[180:183], v[232:235], v[200:203]\n\t" \
    "s_waitcnt lgkmcnt(1)\n\t" \
    "v_mfma_f32_16x16x32_bf16 v[216:219], v[184:187], v[236:239], v[216:219]\n\t" \
    "ds_read_b128 v[228:231], %[wl] offset:2048\n\t" \
    "ds_read_b128 v[232:235], %[wl] offset:3072\n\t" \
    "v_mfma_f32_16x16x32_bf16 v[220:223], v[192:195], v[236:239], v[220:223]\n\t" \
    "v_cvt_pk_bf16_f32 v204, v204, v205\n\t" \
    "v_cvt_pk_bf16_f32 v205, v206, v207\n\t" \
    "v_mfma_f32_16x16x32_bf16 v[212:215], v[176:179], v[236:239], v[212:215]\n\t" \
    "ds_write_b64 v252, v[204:205] offset:1024\n\t" \
    "v_cvt_pk_bf16_f32 v208, v208, v209\n\t" \
    "s_waitcnt lgkmcnt(3)\n\t" \
    "v_mfma_f32_16x16x32_bf16 v[216:219], v[188:191], v[240:243], v[216:219]\n\t" \
    "v_cvt_pk_bf16_f32 v209, v210, v211\n\t" \
    "ds_write_b64 v252, v[208:209] offset:2048\n\t" \
    "v_mfma_f32_16x16x32_bf16 v[220:223], v[196:199], v[240:243], v[220:223]\n\t" \
    "v_cvt_pk_bf16_f32 v200, v200, v201\n\t" \
    "v_cvt_pk_bf16_f32 v201, v202, v203\n\t" \
    "v_mfma_f32_16x16x32_bf16 v[212:215], v[180:183], v[240:243], v[212:215]\n\t" \
    "ds_write_b64 v252, v[200:201] offset:0\n\t" \
    "ds_read_b128 v[200:203], %[wl] offset:0\n\t" \
    "ds_read_b128 v[204:207], %[wl] offset:1024\n\t" \
    "v_cvt_pk_bf16_f32 v216, v216, v217\n\t" \
    "v_cvt_pk_bf16_f32 v217, v218, v219\n\t" \
    "ds_write_b64 v252, v[216:217] offset:7168\n\t" \
    "v_cvt_pk_bf16_f32 v220, v220, v221\n\t" \
    "v_cvt_pk_bf16_f32 v221, v222, v223\n\t" \
    "ds_write_b64 v252, v[220:221] offset:8192\n\t" \
    "v_cvt_pk_bf16_f32 v212, v212, v213\n\t" \
    "v_cvt_pk_bf16_f32 v213, v214, v215\n\t" \
    "ds_write_b64 v252, v[212:213] offset:6144\n\t" \
    "s_waitcnt lgkmcnt(0)\n\t"
#define ASM_XG_HI_A \
    "s_waitcnt lgkmcnt(0)\n\t" \
    "v_xor_b32 v252, 0x2000, %[xa]\n\t" \
    "ds_read_b128 v[224:227], v252 offset:0\n\t" \
    "ds_read_b128 v[244:247], %[wl] offset:4096\n\t" \
    "ds_read_b128 v[228:231], v252 offset:16384\n\t" \
    "ds_read_b128 v[232:235], v252 offset:1024\n\t" \
    "ds_read_b128 v[236:239], v252 offset:17408\n\t" \
    "ds_read_b128 v[248:251], %[wl] offset:5120\n\t" \
    "s_waitcnt lgkmcnt(5)\n\t" \
    "v_mfma_f32_16x16x32_bf16 v[204:207], v[80:83], v[224:227], 0\n\t" \
    "v_xor_b32 v252, 0x2800, %[xa]\n\t" \
    "ds_read_b128 v[240:243], v252 offset:0\n\t" \
    "v_mfma_f32_16x16x32_bf16 v[208:211], v[88:91], v[224:227], 0\n\t" \
    "s_waitcnt lgkmcnt(5)\n\t" \
    "v_mfma_f32_16x16x32_bf16 v[200:203], v[244:247], v[224:227], 0\n\t" \
    "s_waitcnt lgkmcnt(4)\n\t" \
    "v_mfma_f32_16x16x32_bf16 v[216:219], v[80:83], v[228:231], 0\n\t" \
    "ds_read_b128 v[224:227], v252 offset:16384\n\t" \
    "v_mfma_f32_16x16x32_bf16 v[220:223], v[88:91], v[228:231], 0\n\t" \
    "v_mfma_f32_16x16x32_bf16 v[212:215], v[244:247], v[228:231], 0\n\t" \
    "s_waitcnt lgkmcnt(4)\n\t" \
    "v_mfma_f32_16x16x32_bf16 v[204:207], v[84:87], v[232:235], v[204:207]\n\t" \
    "ds_read_b128 v[228:231], v252 offset:1024\n\t" \
    "v_mfma_f32_16x16x32_bf16 v[208:211], v[92:95], v[232:235], v[208:211]\n\t" \
    "ds_read_b128 v[244:247], %[wl] offset:6144\n\t" \
    "s_waitcnt lgkmcnt(4)\n\t" \
    "v_mfma_f32_16x16x32_bf16 v[200:203], v[248:251], v[232:235], v[200:203]\n\t" \
    "v_mfma_f32_16x16x32_bf16 v[216:219], v[84:87], v[236:239], v[216:219]\n\t" \
    "ds_read_b128 v[232:235], v252 offset:17408\n\t" \
    "v_mfma_f32_16x16x32_bf16 v[220:223], v[92:95], v[236:239], v[220:223]\n\t" \
    "v_mfma_f32_16x16x32_bf16 v[212:215], v[248:251], v[236:239], v[212:215]\n\t" \
    "s_waitcnt lgkmcnt(4)\n\t" \
    "v_mfma_f32_16x16x32_bf16 v[204:207], v[104:107], v[240:243], v[204:207]\n\t" \
    "v_xor_b32 v252, 0x3000, %[xa]\n\t" \
    "ds_read_b128 v[236:239], v252 offset:0\n\t" \
    "v_mfma_f32_16x16x32_bf16 v[208:211], v[112:115], v[240:243], v[208:211]\n\t" \
    "ds_read_b128 v[248:251], %[wl] offset:7168\n\t" \
    "v_mfma_f32_16x16x32_bf16 v[200:203], v[96:99], v[240:243], v[200:203]\n\t" \
    "s_waitcnt lgkmcnt(5)\n\t" \
    "v_mfma_f32_16x16x32_bf16 v[216:219], v[104:107], v[224:227], v[216:219]\n\t" \
    "ds_read_b128 v[240:243], v252 offset:16384\n\t" \
    "v_mfma_f32_16x16x32_bf16 v[220:223], v[112:115], v[224:227], v[220:223]\n\t" \
    "v_mfma_f32_16x16x32_bf16 v[212:215], v[96:99], v[224:227], v[212:215]\n\t" \
    "s_waitcnt lgkmcnt(5)\n\t" \
    "v_mfma_f32_16x16x32_bf16 v[204:207], v[108:111], v[228:231], v[204:207]\n\t" \
    "ds_read_b128 v[224:227], v252 offset:1024\n\t" \
    "v_mfma_f32_16x16x32_bf16 v[208:211], v[116:119], v[228:231], v[208:211]\n\t" \
    "v_mfma_f32_16x16x32_bf16 v[200:203], v[100:103], v[228:231], v[200:203]\n\t" \
    "s_waitcnt lgkmcnt(4)\n\t" \
    "v_mfma_f32_16x16x32_bf16 v[216:219], v[108:111], v[232:235], v[216:219]\n\t" \
    "ds_read_b128 v[228:231], v252 offset:17408\n\t" \
    "v_mfma_f32_16x16x32_bf16 v[220:223], v[116:119], v[232:235], v[220:223]\n\t" \
    "v_mfma_f32_16x16x32_bf16 v[212:215], v[100:103], v[232:235], v[212:215]\n\t" \
    "s_waitcnt lgkmcnt(4)\n\t" \
    "v_mfma_f32_16x16x32_bf16 v[204:207], v[120:123], v[236:239], v[204:207]\n\t" \
    "v_xor_b32 v252, 0x3800, %[xa]\n\t" \
    "ds_read_b128 v[232:235], v252 offset:0\n\t" \
    "v_mfma_f32_16x16x32_bf16 v[208:211], v[128:131], v[236:239], v[208:211]\n\t" \
    "v_mfma_f32_16x16x32_bf16 v[200:203], v[244:247], v[236:239], v[200:203]\n\t" \
    "s_waitcnt lgkmcnt(3)\n\t" \
    "v_mfma_f32_16x16x32_bf16 v[216:219], v[120:123], v[240:243], v[216:219]\n\t" \
    "ds_read_b128 v[236:239], v252 offset:16384\n\t" \
    "v_mfma_f32_16x16x32_bf16 v[220:223], v[128:131], v[240:243], v[220:223]\n\t" \
    "v_mfma_f32_16x16x32_bf16 v[212:215], v[244:247], v[240:243], v[212:215]\n\t" \
    "s_waitcnt lgkmcnt(3)\n\t" \
    "v_mfma_f32_16x16x32_bf16 v[204:207], v[124:127], v[224:227], v[204:207]\n\t" \
    "ds_read_b128 v[240:243], v252 offset:1024\n\t" \
    "v_mfma_f32_16x16x32_bf16 v[208:211], v[132:135], v[224:227], v[208:211]\n\t" \
    "ds_read_b128 v[244:247], %[wl] offset:8192\n\t" \
    "v_mfma_f32_16x16x32_bf16 v[200:203], v[248:251], v[224:227], v[200:203]\n\t" \
    "s_waitcnt lgkmcnt(4)\n\t" \
    "v_mfma_f32_16x16x32_bf16 v[216:219], v[124:127], v[228:231], v[216:219]\n\t" \
    "ds_read_b128 v[224:227], v252 offset:17408\n\t" \
    "v_mfma_f32_16x16x32_bf16 v[220:223], v[132:135], v[228:231], v[220:223]\n\t" \
    "v_mfma_f32_16x16x32_bf16 v[212:215], v[248:251], v[228:231], v[212:215]\n\t" \
    "s_waitcnt lgkmcnt(4)\n\t" \
    "v_mfma_f32_16x16x32_bf16 v[204:207], v[144:147], v[232:235], v[204:207]\n\t" \
    "v_and_b32 v252, 0x3ff, %[xa]\n\t" \
    "ds_read_b128 v[228:231], v252 offset:40960\n\t" \
    "v_mfma_f32_16x16x32_bf16 v[208:211], v[152:155], v[232:235], v[208:211]\n\t" \
    "ds_read_b128 v[248:251], %[wl] offset:9216\n\t" \
    "v_mfma_f32_16x16x32_bf16 v[200:203], v[136:139], v[232:235], v[200:203]\n\t" \
    "s_waitcnt lgkmcnt(5)\n\t" \
    "v_mfma_f32_16x16x32_bf16 v[216:219], v[144:147], v[236:239], v[216:219]\n\t" \
    "ds_read_b128 v[232:235], v252 offset:45056\n\t" \
    "v_mfma_f32_16x16x32_bf16 v[220:223], v[152:155], v[236:239], v[220:223]\n\t" \
    "v_mfma_f32_16x16x32_bf16 v[212:215], v[136:139], v[236:239], v[212:215]\n\t" \
    "s_waitcnt lgkmcnt(5)\n\t" \
    "v_mfma_f32_16x16x32_bf16 v[204:207], v[148:151], v[240:243], v[204:207]\n\t" \
    "ds_read_b128 v[236:239], v252 offset:41984\n\t" \
    "v_mfma_f32_16x16x32_bf16 v[208:211], v[156:159], v[240:243], v[208:211]\n\t" \
    "v_mfma_f32_16x16x32_bf16 v[200:203], v[140:143], v[240:243], v[200:203]\n\t" \
    "s_waitcnt lgkmcnt(4)\n\t" \
    "v_mfma_f32_16x16x32_bf16 v[216:219], v[148:151], v[224:227], v[216:219]\n\t" \
    "ds_read_b128 v[240:243], v252 offset:46080\n\t" \
    "v_mfma_f32_16x16x32_bf16 v[220:223], v[156:159], v[224:227], v[220:223]\n\t" \
    "v_mfma_f32_16x16x32_bf16 v[212:215], v[140:143], v[224:227], v[212:215]\n\t" \
    "s_waitcnt lgkmcnt(4)\n\t" \
    "v_mfma_f32_16x16x32_bf16 v[204:207], v[160:163], v[228:231], v[204:207]\n\t" \
    "v_mfma_f32_16x16x32_bf16 v[208:211], v[168:171], v[228:231], v[208:211]\n\t" \
    "v_mfma_f32_16x16x32_bf16 v[200:203], v[244:247], v[228:231], v[200:203]\n\t" \
    "s_waitcnt lgkmcnt(2)\n\t" \
    "v_mfma_f32_16x16x32_bf16 v[216:219], v[160:163], v[232:235], v[216:219]\n\t" \
    "v_mfma_f32_16x16x32_bf16 v[220:223], v[168:171], v[232:235], v[220:223]\n\t" \
    "v_mfma_f32_16x16x32_bf16 v[212:215], v[244:247], v[232:235], v[212:215]\n\t" \
    "s_waitcnt lgkmcnt(1)\n\t" \
    "v_mfma_f32_16x16x32_bf16 v[204:207], v[164:167], v[236:239], v[204:207]\n\t" \
    "v_mfma_f32_16x16x32_bf16 v[208:211], v[172:175], v[236:239], v[208:211]\n\t" \
    "v_mfma_f32_16x16x32_bf16 v[200:203], v[248:251], v[236:239], v[200:203]\n\t" \
    "s_waitcnt lgkmcnt(0)\n\t" \
    "v_mfma_f32_16x16x32_bf16 v[216:219], v[164:167], v[240:243], v[216:219]\n\t" \
    "v_mfma_f32_16x16x32_bf16 v[220:223], v[172:175], v[240:243], v[220:223]\n\t" \
    "v_mfma_f32_16x16x32_bf16 v[212:215], v[248:251], v[240:243], v[212:215]\n\t" \
    "s_nop 7\n\t"
#define ASM_XG_HI_B \
    "s_waitcnt lgkmcnt(0)\n\t" \
    "ds_read_b128 v[224:227], %[xa] offset:0\n\t" \
    "ds_read_b128 v[244:247], %[wl] offset:0\n\t" \
    "ds_read_b128 v[228:231], %[xa] offset:16384\n\t" \
    "ds_read_b128 v[232:235], %[xa] offset:1024\n\t" \
    "ds_read_b128 v[236:239], %[xa] offset:17408\n\t" \
    "ds_read_b128 v[248:251], %[wl] offset:1024\n\t" \
    "s_waitcnt lgkmcnt(5)\n\t" \
    "v_mfma_f32_16x16x32_bf16 v[204:207], v[0:3], v[224:227], v[204:207]\n\t" \
    "v_xor_b32 v252, 0x800, %[xa]\n\t" \
    "ds_read_b128 v[240:243], v252 offset:0\n\t" \
    "v_mfma_f32_16x16x32_bf16 v[208:211], v[8:11], v[224:227], v[208:211]\n\t" \
    "s_waitcnt lgkmcnt(5)\n\t" \
    "v_mfma_f32_16x16x32_bf16 v[200:203], v[244:247], v[224:227], v[200:203]\n\t" \
    "s_waitcnt lgkmcnt(4)\n\t" \
    "v_mfma_f32_16x16x32_bf16 v[216:219], v[0:3], v[228:231], v[216:219]\n\t" \
    "ds_read_b128 v[224:227], v252 offset:16384\n\t" \
    "v_mfma_f32_16x16x32_bf16 v[220:223], v[8:11], v[228:231], v[220:223]\n\t" \
    "v_mfma_f32_16x16x32_bf16 v[212:215], v[244:247], v[228:231], v[212:215]\n\t" \
    "s_waitcnt lgkmcnt(4)\n\t" \
    "v_mfma_f32_16x16x32_bf16 v[204:207], v[4:7], v[232:235], v[204:207]\n\t" \
    "ds_read_b128 v[228:231], v252 offset:1024\n\t" \
    "v_mfma_f32_16x16x32_bf16 v[208:211], v[12:15], v[232:235], v[208:211]\n\t" \
    "ds_read_b128 v[244:247], %[wl] offset:2048\n\t" \
    "s_waitcnt lgkmcnt(4)\n\t" \
    "v_mfma_f32_16x16x32_bf16 v[200:203], v[248:251], v[232:235], v[200:203]\n\t" \
    "v_mfma_f32_16x16x32_bf16 v[216:219], v[4:7], v[236:239], v[216:219]\n\t" \
    "ds_read_b128 v[232:235], v252 offset:17408\n\t" \
    "v_mfma_f32_16x16x32_bf16 v[220:223], v[12:15], v[236:239], v[220:223]\n\t" \
    "v_mfma_f32_16x16x32_bf16 v[212:215], v[248:251], v[236:239], v[212:215]\n\t" \
    "s_waitcnt lgkmcnt(4)\n\t" \
    "v_mfma_f32_16x16x32_bf16 v[204:207], v[24:27], v[240:243], v[204:207]\n\t" \
    "v_xor_b32 v252, 0x1000, %[xa]\n\t" \
    "ds_read_b128 v[236:239], v252 offset:0\n\t" \
    "v_mfma_f32_16x16x32_bf16 v[208:211], v[32:35], v[240:243], v[208:211]\n\t" \
    "ds_read_b128 v[248:251], %[wl] offset:3072\n\t" \
    "v_mfma_f32_16x16x32_bf16 v[200:203], v[16:19], v[240:243], v[200:203]\n\t" \
    "s_waitcnt lgkmcnt(5)\n\t" \
    "v_mfma_f32_16x16x32_bf16 v[216:219], v[24:27], v[224:227], v[216:219]\n\t" \
    "ds_read_b128 v[240:243], v252 offset:16384\n\t" \
    "v_mfma_f32_16x16x32_bf16 v[220:223], v[32:35], v[224:227], v[220:223]\n\t" \
    "v_mfma_f32_16x16x32_bf16 v[212:215], v[16:19], v[224:227], v[212:215]\n\t" \
    "s_waitcnt lgkmcnt(5)\n\t" \
    "v_mfma_f32_16x16x32_bf16 v[204:207], v[28:31], v[228:231], v[204:207]\n\t" \
    "ds_read_b128 v[224:227], v252 offset:1024\n\t" \
    "v_mfma_f32_16x16x32_bf16 v[208:211], v[36:39], v[228:231], v[208:211]\n\t" \
    "v_mfma_f32_16x16x32_bf16 v[200:203], v[20:23], v[228:231], v[200:203]\n\t" \
    "s_waitcnt lgkmcnt(4)\n\t" \
    "v_mfma_f32_16x16x32_bf16 v[216:219], v[28:31], v[232:235], v[216:219]\n\t" \
    "ds_read_b128 v[228:231], v252 offset:17408\n\t" \
    "v_mfma_f32_16x16x32_bf16 v[220:223], v[36:39], v[232:235], v[220:223]\n\t" \
    "v_mfma_f32_16x16x32_bf16 v[212:215], v[20:23], v[232:235], v[212:215]\n\t" \
    "s_waitcnt lgkmcnt(4)\n\t" \
    "v_mfma_f32_16x16x32_bf16 v[204:207], v[40:43], v[236:239], v[204:207]\n\t" \
    "v_xor_b32 v252, 0x1800, %[xa]\n\t" \
    "ds_read_b128 v[232:235], v252 offset:0\n\t" \
    "v_mfma_f32_16x16x32_bf16 v[208:211], v[48:51], v[236:239], v[208:211]\n\t" \
    "v_mfma_f32_16x16x32_bf16 v[200:203], v[244:247], v[236:239], v[200:203]\n\t" \
    "s_waitcnt lgkmcnt(3)\n\t" \
    "v_mfma_f32_16x16x32_bf16 v[216:219], v[40:43], v[240:243], v[216:219]\n\t" \
    "ds_read_b128 v[236:239], v252 offset:16384\n\t" \
    "v_mfma_f32_16x16x32_bf16 v[220:223], v[48:51], v[240:243], v[220:223]\n\t" \
    "v_mfma_f32_16x16x32_bf16 v[212:215], v[244:247], v[240:243], v[212:215]\n\t" \
    "s_waitcnt lgkmcnt(3)\n\t" \
    "v_mfma_f32_16x16x32_bf16 v[204:207], v[44:47], v[224:227], v[204:207]\n\t" \
    "ds_read_b128 v[240:243], v252 offset:1024\n\t" \
    "v_mfma_f32_16x16x32_bf16 v[208:211], v[52:55], v[224:227], v[208:211]\n\t" \
    "v_mfma_f32_16x16x32_bf16 v[200:203], v[248:251], v[224:227], v[200:203]\n\t" \
    "s_waitcnt lgkmcnt(3)\n\t" \
    "v_mfma_f32_16x16x32_bf16 v[216:219], v[44:47], v[228:231], v[216:219]\n\t" \
    "ds_read_b128 v[224:227], v252 offset:17408\n\t" \
    "v_mfma_f32_16x16x32_bf16 v[220:223], v[52:55], v[228:231], v[220:223]\n\t" \
    "v_mfma_f32_16x16x32_bf16 v[212:215], v[248:251], v[228:231], v[212:215]\n\t" \
    "s_waitcnt lgkmcnt(3)\n\t" \
    "v_mfma_f32_16x16x32_bf16 v[204:207], v[64:67], v[232:235], v[204:207]\n\t" \
    "v_and_b32 v252, 0x3ff, %[xa]\n\t" \
    "ds_read_b128 v[228:231], v252 offset:43008\n\t" \
    "v_mov_b64 v[244:245], 0\n\t" \
    "v_mfma_f32_16x16x32_bf16 v[208:211], v[72:75], v[232:235], v[208:211]\n\t" \
    "v_mov_b64 v[246:247], 0\n\t" \
    "v_mfma_f32_16x16x32_bf16 v[200:203], v[56:59], v[232:235], v[200:203]\n\t" \
    "v_mov_b64 v[248:249], 0\n\t" \
    "s_waitcnt lgkmcnt(3)\n\t" \
    "v_mfma_f32_16x16x32_bf16 v[216:219], v[64:67], v[236:239], v[216:219]\n\t" \
    "ds_read_b128 v[232:235], v252 offset:44032\n\t" \
    "v_mov_b64 v[250:251], 0\n\t" \
    "v_mfma_f32_16x16x32_bf16 v[220:223], v[72:75], v[236:239], v[220:223]\n\t" \
    "v_mfma_f32_16x16x32_bf16 v[212:215], v[56:59], v[236:239], v[212:215]\n\t" \
    "s_waitcnt lgkmcnt(3)\n\t" \
    "v_mfma_f32_16x16x32_bf16 v[204:207], v[68:71], v[240:243], v[204:207]\n\t" \
    "ds_read_b128 v[236:239], v252 offset:47104\n\t" \
    "v_mfma_f32_16x16x32_bf16 v[208:211], v[76:79], v[240:243], v[208:211]\n\t" \
    "v_mfma_f32_16x16x32_bf16 v[200:203], v[60:63], v[240:243], v[200:203]\n\t" \
    "s_waitcnt lgkmcnt(3)\n\t" \
    "v_mfma_f32_16x16x32_bf16 v[216:219], v[68:71], v[224:227], v[216:219]\n\t" \
    "ds_read_b128 v[240:243], v252 offset:48128\n\t" \
    "v_mfma_f32_16x16x32_bf16 v[220:223], v[76:79], v[224:227], v[220:223]\n\t" \
    "v_mfma_f32_16x16x32_bf16 v[212:215], v[60:63], v[224:227], v[212:215]\n\t" \
    "s_waitcnt lgkmcnt(3)\n\t" \
    "v_mfma_f32_16x16x32_bf16 v[204:207], v[184:187], v[228:231], v[204:207]\n\t" \
    "v_add_u32 v252, %[qs], v252\n\t" \
    "v_mfma_f32_16x16x32_bf16 v[208:211], v[192:195], v[228:231], v[208:211]\n\t" \
    "v_mov_b64 v[224:225], 0\n\t" \
    "v_mfma_f32_16x16x32_bf16 v[200:203], v[176:179], v[228:231], v[200:203]\n\t" \
    "v_mov_b64 v[226:227], 0\n\t" \
    "s_waitcnt lgkmcnt(2)\n\t" \
    "v_mfma_f32_16x16x32_bf16 v[204:207], v[188:191], v[232:235], v[204:207]\n\t" \
    "v_mfma_f32_16x16x32_bf16 v[208:211], v[196:199], v[232:235], v[208:211]\n\t" \
    "v_mfma_f32_16x16x32_bf16 v[200:203], v[180:183], v[232:235], v[200:203]\n\t" \
    "s_waitcnt lgkmcnt(1)\n\t" \
    "v_mfma_f32_16x16x32_bf16 v[216:219], v[184:187], v[236:239], v[216:219]\n\t" \
    "ds_read_b128 v[228:231], %[wl] offset:2048\n\t" \
    "ds_read_b128 v[232:235], %[wl] offset:3072\n\t" \
    "v_mfma_f32_16x16x32_bf16 v[220:223], v[192:195], v[236:239], v[220:223]\n\t" \
    "v_cvt_pk_bf16_f32 v204, v204, v205\n\t" \
    "v_cvt_pk_bf16_f32 v205, v206, v207\n\t" \
    "v_mfma_f32_16x16x32_bf16 v[212:215], v[176:179], v[236:239], v[212:215]\n\t" \
    "ds_write_b64 v252, v[204:205] offset:1024\n\t" \
    "v_cvt_pk_bf16_f32 v208, v208, v209\n\t" \
    "s_waitcnt lgkmcnt(3)\n\t" \
    "v_mfma_f32_16x16x32_bf16 v[216:219], v[188:191], v[240:243], v[216:219]\n\t" \
    "v_cvt_pk_bf16_f32 v209, v210, v211\n\t" \
    "ds_write_b64 v252, v[208:209] offset:2048\n\t" \
    "v_mfma_f32_16x16x32_bf16 v[220:223], v[196:199], v[240:243], v[220:223]\n\t" \
    "v_cvt_pk_bf16_f32 v200, v200, v201\n\t" \
    "v_cvt_pk_bf16_f32 v201, v202, v203\n\t" \
    "v_mfma_f32_16x16x32_bf16 v[212:215], v[180:183], v[240:243], v[212:215]\n\t" \
    "ds_write_b64 v252, v[200:201] offset:0\n\t" \
    "ds_read_b128 v[200:203], %[wl] offset:0\n\t" \
    "ds_read_b128 v[204:207], %[wl] offset:1024\n\t" \
    "v_cvt_pk_bf16_f32 v216, v216, v217\n\t" \
    "v_cvt_pk_bf16_f32 v217, v218, v219\n\t" \
    "ds_write_b64 v252, v[216:217] offset:7168\n\t" \
    "v_cvt_pk_bf16_f32 v220, v220, v221\n\t" \
    "v_cvt_pk_bf16_f32 v221, v222, v223\n\t" \
    "ds_write_b64 v252, v[220:221] offset:8192\n\t" \
    "v_cvt_pk_bf16_f32 v212, v212, v213\n\t" \
    "v_cvt_pk_bf16_f32 v213, v214, v215\n\t" \
    "ds_write_b64 v252, v[212:213] offset:6144\n\t" \
    "s_waitcnt lgkmcnt(0)\n\t"
#define ASM_XG_LO_AB \
    "s_waitcnt lgkmcnt(0)\n\t" \
    "ds_read_b128 v[244:247], %[wl] offset:0\n\t" \
    "ds_read_b128 v[248:251], %[wl] offset:1024\n\t" \
    "s_barrier \n\t" \
    "ds_read_b128 v[224:227], %[xa] offset:0\n\t" \
    "ds_read_b128 v[228:231], %[xa] offset:16384\n\t" \
    "ds_read_b128 v[232:235], %[xa] offset:1024\n\t" \
    "ds_read_b128 v[236:239], %[xa] offset:17408\n\t" \
    "s_waitcnt lgkmcnt(3)\n\t" \
    "v_mfma_f32_16x16x32_bf16 v[204:207], v[0:3], v[224:227], 0\n\t" \
    "v_xor_b32 v252, 0x800, %[xa]\n\t" \
    "ds_read_b128 v[240:243], v252 offset:0\n\t" \
    "v_mfma_f32_16x16x32_bf16 v[208:211], v[8:11], v[224:227], 0\n\t" \
    "v_mfma_f32_16x16x32_bf16 v[200:203], v[244:247], v[224:227], 0\n\t" \
    "s_waitcnt lgkmcnt(3)\n\t" \
    "v_mfma_f32_16x16x32_bf16 v[216:219], v[0:3], v[228:231], 0\n\t" \
    "ds_read_b128 v[224:227], v252 offset:16384\n\t" \
    "v_mfma_f32_16x16x32_bf16 v[220:223], v[8:11], v[228:231], 0\n\t" \
    "v_mfma_f32_16x16x32_bf16 v[212:215], v[244:247], v[228:231], 0\n\t" \
    "s_waitcnt lgkmcnt(3)\n\t" \
    "v_mfma_f32_16x16x32_bf16 v[204:207], v[4:7], v[232:235], v[204:207]\n\t" \
    "ds_read_b128 v[228:231], v252 offset:1024\n\t" \
    "v_mfma_f32_16x16x32_bf16 v[208:211], v[12:15], v[232:235], v[208:211]\n\t" \
    "ds_read_b128 v[244:247], %[wl] offset:2048\n\t" \
    "v_mfma_f32_16x16x32_bf16 v[200:203], v[248:251], v[232:235], v[200:203]\n\t" \
    "s_waitcnt lgkmcnt(4)\n\t" \
    "v_mfma_f32_16x16x32_bf16 v[216:219], v[4:7], v[236:239], v[216:219]\n\t" \
    "ds_read_b128 v[232:235], v252 offset:17408\n\t" \
    "v_mfma_f32_16x16x32_bf16 v[220:223], v[12:15], v[236:239], v[220:223]\n\t" \
    "v_mfma_f32_16x16x32_bf16 v[212:215], v[248:251], v[236:239], v[212:215]\n\t" \
    "s_waitcnt lgkmcnt(4)\n\t" \
    "v_mfma_f32_16x16x32_bf16 v[204:207], v[24:27], v[240:243], v[204:207]\n\t" \
    "v_xor_b32 v252, 0x1000, %[xa]\n\t" \
    "ds_read_b128 v[236:239], v252 offset:0\n\t" \
    "v_mfma_f32_16x16x32_bf16 v[208:211], v[32:35], v[240:243], v[208:211]\n\t" \
    "ds_read_b128 v[248:251], %[wl] offset:3072\n\t" \
    "v_mfma_f32_16x16x32_bf16 v[200:203], v[16:19], v[240:243], v[200:203]\n\t" \
    "s_waitcnt lgkmcnt(5)\n\t" \
    "v_mfma_f32_16x16x32_bf16 v[216:219], v[24:27], v[224:227], v[216:219]\n\t" \
    "ds_read_b128 v[240:243], v252 offset:16384\n\t" \
    "v_mfma_f32_16x16x32_bf16 v[220:223], v[32:35], v[224:227], v[220:223]\n\t" \
    "v_mfma_f32_16x16x32_bf16 v[212:215], v[16:19], v[224:227], v[212:215]\n\t" \
    "s_waitcnt lgkmcnt(5)\n\t" \
    "v_mfma_f32_16x16x32_bf16 v[204:207], v[28:31], v[228:231], v[204:207]\n\t" \
    "ds_read_b128 v[224:227], v252 offset:1024\n\t" \
    "v_mfma_f32_16x16x32_bf16 v[208:211], v[36:39], v[228:231], v[208:211]\n\t" \
    "v_mfma_f32_16x16x32_bf16 v[200:203], v[20:23], v[228:231], v[200:203]\n\t" \
    "s_waitcnt lgkmcnt(4)\n\t" \
    "v_mfma_f32_16x16x32_bf16 v[216:219], v[28:31], v[232:235], v[216:219]\n\t" \
    "ds_read_b128 v[228:231], v252 offset:17408\n\t" \
    "v_mfma_f32_16x16x32_bf16 v[220:223], v[36:39], v[232:235], v[220:223]\n\t" \
    "v_mfma_f32_16x16x32_bf16 v[212:215], v[20:23], v[232:235], v[212:215]\n\t" \
    "s_waitcnt lgkmcnt(4)\n\t" \
    "v_mfma_f32_16x16x32_bf16 v[204:207], v[40:43], v[236:239], v[204:207]\n\t" \
    "v_xor_b32 v252, 0x1800, %[xa]\n\t" \
    "ds_read_b128 v[232:235], v252 offset:0\n\t" \
    "v_mfma_f32_16x16x32_bf16 v[208:211], v[48:51], v[236:239], v[208:211]\n\t" \
    "v_mfma_f32_16x16x32_bf16 v[200:203], v[244:247], v[236:239], v[200:203]\n\t" \
    "s_waitcnt lgkmcnt(3)\n\t" \
    "v_mfma_f32_16x16x32_bf16 v[216:219], v[40:43], v[240:243], v[216:219]\n\t" \
    "ds_read_b128 v[236:239], v252 offset:16384\n\t" \
    "v_mfma_f32_16x16x32_bf16 v[220:223], v[48:51], v[240:243], v[220:223]\n\t" \
    "v_mfma_f32_16x16x32_bf16 v[212:215], v[244:247], v[240:243], v[212:215]\n\t" \
    "s_waitcnt lgkmcnt(3)\n\t" \
    "v_mfma_f32_16x16x32_bf16 v[204:207], v[44:47], v[224:227], v[204:207]\n\t" \
    "ds_read_b128 v[240:243], v252 offset:1024\n\t" \
    "v_mfma_f32_16x16x32_bf16 v[208:211], v[52:55], v[224:227], v[208:211]\n\t" \
    "ds_read_b128 v[244:247], %[wl] offset:8192\n\t" \
    "v_mfma_f32_16x16x32_bf16 v[200:203], v[248:251], v[224:227], v[200:203]\n\t" \
    "s_waitcnt lgkmcnt(4)\n\t" \
    "v_mfma_f32_16x16x32_bf16 v[216:219], v[44:47], v[228:231], v[216:219]\n\t" \
    "ds_read_b128 v[224:227], v252 offset:17408\n\t" \
    "v_mfma_f32_16x16x32_bf16 v[220:223], v[52:55], v[228:231], v[220:223]\n\t" \
    "v_mfma_f32_16x16x32_bf16 v[212:215], v[248:251], v[228:231], v[212:215]\n\t" \
    "s_waitcnt lgkmcnt(4)\n\t" \
    "v_mfma_f32_16x16x32_bf16 v[204:207], v[64:67], v[232:235], v[204:207]\n\t" \
    "v_and_b32 v252, 0x3ff, %[xa]\n\t" \
    "ds_read_b128 v[228:231], v252 offset:40960\n\t" \
    "v_mfma_f32_16x16x32_bf16 v[208:211], v[72:75], v[232:235], v[208:211]\n\t" \
    "ds_read_b128 v[248:251], %[wl] offset:9216\n\t" \
    "v_mfma_f32_16x16x32_bf16 v[200:203], v[56:59], v[232:235], v[200:203]\n\t" \
    "s_waitcnt lgkmcnt(5)\n\t" \
    "v_mfma_f32_16x16x32_bf16 v[216:219], v[64:67], v[236:239], v[216:219]\n\t" \
    "ds_read_b128 v[232:235], v252 offset:45056\n\t" \
    "v_mfma_f32_16x16x32_bf16 v[220:223], v[72:75], v[236:239], v[220:223]\n\t" \
    "v_mfma_f32_16x16x32_bf16 v[212:215], v[56:59], v[236:239], v[212:215]\n\t" \
    "s_waitcnt lgkmcnt(5)\n\t" \
    "v_mfma_f32_16x16x32_bf16 v[204:207], v[68:71], v[240:243], v[204:207]\n\t" \
    "ds_read_b128 v[236:239], v252 offset:41984\n\t" \
    "v_mfma_f32_16x16x32_bf16 v[208:211], v[76:79], v[240:243], v[208:211]\n\t" \
    "v_mfma_f32_16x16x32_bf16 v[200:203], v[60:63], v[240:243], v[200:203]\n\t" \
    "s_waitcnt lgkmcnt(4)\n\t" \
    "v_mfma_f32_16x16x32_bf16 v[216:219], v[68:71], v[224:227], v[216:219]\n\t" \
    "ds_read_b128 v[240:243], v252 offset:46080\n\t" \
    "v_mfma_f32_16x16x32_bf16 v[220:223], v[76:79], v[224:227], v[220:223]\n\t" \
    "v_mfma_f32_16x16x32_bf16 v[212:215], v[60:63], v[224:227], v[212:215]\n\t" \
    "s_waitcnt lgkmcnt(4)\n\t" \
    "v_mfma_f32_16x16x32_bf16 v[204:207], v[160:163], v[228:231], v[204:207]\n\t" \
    "v_xor_b32 v252, 0x2000, %[xa]\n\t" \
    "ds_read_b128 v[224:227], v252 offset:0\n\t" \
    "v_mfma_f32_16x16x32_bf16 v[208:211], v[168:171], v[228:231], v[208:211]\n\t" \
    "v_mfma_f32_16x16x32_bf16 v[200:203], v[244:247], v[228:231], v[200:203]\n\t" \
    "s_waitcnt lgkmcnt(3)\n\t" \
    "v_mfma_f32_16x16x32_bf16 v[216:219], v[160:163], v[232:235], v[216:219]\n\t" \
    "ds_read_b128 v[228:231], v252 offset:16384\n\t" \
    "v_mfma_f32_16x16x32_bf16 v[220:223], v[168:171], v[232:235], v[220:223]\n\t" \
    "v_mfma_f32_16x16x32_bf16 v[212:215], v[244:247], v[232:235], v[212:215]\n\t" \
    "s_waitcnt lgkmcnt(3)\n\t" \
    "v_mfma_f32_16x16x32_bf16 v[204:207], v[164:167], v[236:239], v[204:207]\n\t" \
    "ds_read_b128 v[232:235], v252 offset:1024\n\t" \
    "v_mfma_f32_16x16x32_bf16 v[208:211], v[172:175], v[236:239], v[208:211]\n\t" \
    "ds_read_b128 v[244:247], %[wl] offset:4096\n\t" \
    "v_mfma_f32_16x16x32_bf16 v[200:203], v[248:251], v[236:239], v[200:203]\n\t" \
    "s_waitcnt lgkmcnt(4)\n\t" \
    "v_mfma_f32_16x16x32_bf16 v[216:219], v[164:167], v[240:243], v[216:219]\n\t" \
    "ds_read_b128 v[236:239], v252 offset:17408\n\t" \
    "v_mfma_f32_16x16x32_bf16 v[220:223], v[172:175], v[240:243], v[220:223]\n\t" \
    "v_mfma_f32_16x16x32_bf16 v[212:215], v[248:251], v[240:243], v[212:215]\n\t" \
    "s_waitcnt lgkmcnt(4)\n\t" \
    "v_mfma_f32_16x16x32_bf16 v[204:207], v[80:83], v[224:227], v[204:207]\n\t" \
    "v_xor_b32 v252, 0x2800, %[xa]\n\t" \
    "ds_read_b128 v[240:243], v252 offset:0\n\t" \
    "v_mfma_f32_16x16x32_bf16 v[208:211], v[88:91], v[224:227], v[208:211]\n\t" \
    "ds_read_b128 v[248:251], %[wl] offset:5120\n\t" \
    "s_waitcnt lgkmcnt(3)\n\t" \
    "v_mfma_f32_16x16x32_bf16 v[200:203], v[244:247], v[224:227], v[200:203]\n\t" \
    "v_mfma_f32_16x16x32_bf16 v[216:219], v[80:83], v[228:231], v[216:219]\n\t" \
    "ds_read_b128 v[224:227], v252 offset:16384\n\t" \
    "v_mfma_f32_16x16x32_bf16 v[220:223], v[88:91], v[228:231], v[220:223]\n\t" \
    "v_mfma_f32_16x16x32_bf16 v[212:215], v[244:247], v[228:231], v[212:215]\n\t" \
    "v_mfma_f32_16x16x32_bf16 v[204:207], v[84:87], v[232:235], v[204:207]\n\t" \
    "ds_read_b128 v[228:231], v252 offset:1024\n\t" \
    "v_mfma_f32_16x16x32_bf16 v[208:211], v[92:95], v[232:235], v[208:211]\n\t" \
    "ds_read_b128 v[244:247], %[wl] offset:6144\n\t" \
    "s_waitcnt lgkmcnt(3)\n\t" \
    "v_mfma_f32_16x16x32_bf16 v[200:203], v[248:251], v[232:235], v[200:203]\n\t" \
    "v_mfma_f32_16x16x32_bf16 v[216:219], v[84:87], v[236:239], v[216:219]\n\t" \
    "ds_read_b128 v[232:235], v252 offset:17408\n\t" \
    "v_mfma_f32_16x16x32_bf16 v[220:223], v[92:95], v[236:239], v[220:223]\n\t" \
    "v_mfma_f32_16x16x32_bf16 v[212:215], v[248:251], v[236:239], v[212:215]\n\t" \
    "v_mfma_f32_16x16x32_bf16 v[204:207], v[104:107], v[240:243], v[204:207]\n\t" \
    "v_xor_b32 v252, 0x3000, %[xa]\n\t" \
    "ds_read_b128 v[236:239], v252 offset:0\n\t" \
    "v_mfma_f32_16x16x32_bf16 v[208:211], v[112:115], v[240:243], v[208:211]\n\t" \
    "ds_read_b128 v[248:251], %[wl] offset:7168\n\t" \
    "v_mfma_f32_16x16x32_bf16 v[200:203], v[96:99], v[240:243], v[200:203]\n\t" \
    "s_waitcnt lgkmcnt(5)\n\t" \
    "v_mfma_f32_16x16x32_bf16 v[216:219], v[104:107], v[224:227], v[216:219]\n\t" \
    "ds_read_b128 v[240:243], v252 offset:16384\n\t" \
    "v_mfma_f32_16x16x32_bf16 v[220:223], v[112:115], v[224:227], v[220:223]\n\t" \
    "v_mfma_f32_16x16x32_bf16 v[212:215], v[96:99], v[224:227], v[212:215]\n\t" \
    "s_waitcnt lgkmcnt(5)\n\t" \
    "v_mfma_f32_16x16x32_bf16 v[204:207], v[108:111], v[228:231], v[204:207]\n\t" \
    "ds_read_b128 v[224:227], v252 offset:1024\n\t" \
    "v_mfma_f32_16x16x32_bf16 v[208:211], v[116:119], v[228:231], v[208:211]\n\t" \
    "v_mfma_f32_16x16x32_bf16 v[200:203], v[100:103], v[228:231], v[200:203]\n\t" \
    "s_waitcnt lgkmcnt(4)\n\t" \
    "v_mfma_f32_16x16x32_bf16 v[216:219], v[108:111], v[232:235], v[216:219]\n\t" \
    "ds_read_b128 v[228:231], v252 offset:17408\n\t" \
    "v_mfma_f32_16x16x32_bf16 v[220:223], v[116:119], v[232:235], v[220:223]\n\t" \
    "v_mfma_f32_16x16x32_bf16 v[212:215], v[100:103], v[232:235], v[212:215]\n\t" \
    "s_waitcnt lgkmcnt(4)\n\t" \
    "v_mfma_f32_16x16x32_bf16 v[204:207], v[120:123], v[236:239], v[204:207]\n\t" \
    "v_xor_b32 v252, 0x3800, %[xa]\n\t" \
    "ds_read_b128 v[232:235], v252 offset:0\n\t" \
    "v_mfma_f32_16x16x32_bf16 v[208:211], v[128:131], v[236:239], v[208:211]\n\t" \
    "v_mfma_f32_16x16x32_bf16 v[200:203], v[244:247], v[236:239], v[200:203]\n\t" \
    "s_waitcnt lgkmcnt(3)\n\t" \
    "v_mfma_f32_16x16x32_bf16 v[216:219], v[120:123], v[240:243], v[216:219]\n\t" \
    "ds_read_b128 v[236:239], v252 offset:16384\n\t" \
    "v_mfma_f32_16x16x32_bf16 v[220:223], v[128:131], v[240:243], v[220:223]\n\t" \
    "v_mfma_f32_16x16x32_bf16 v[212:215], v[244:247], v[240:243], v[212:215]\n\t" \
    "s_waitcnt lgkmcnt(3)\n\t" \
    "v_mfma_f32_16x16x32_bf16 v[204:207], v[124:127], v[224:227], v[204:207]\n\t" \
    "ds_read_b128 v[240:243], v252 offset:1024\n\t" \
    "v_mfma_f32_16x16x32_bf16 v[208:211], v[132:135], v[224:227], v[208:211]\n\t" \
    "v_mfma_f32_16x16x32_bf16 v[200:203], v[248:251], v[224:227], v[200:203]\n\t" \
    "s_waitcnt lgkmcnt(3)\n\t" \
    "v_mfma_f32_16x16x32_bf16 v[216:219], v[124:127], v[228:231], v[216:219]\n\t" \
    "ds_read_b128 v[224:227], v252 offset:17408\n\t" \
    "v_mfma_f32_16x16x32_bf16 v[220:223], v[132:135], v[228:231], v[220:223]\n\t" \
    "v_mfma_f32_16x16x32_bf16 v[212:215], v[248:251], v[228:231], v[212:215]\n\t" \
    "s_waitcnt lgkmcnt(3)\n\t" \
    "v_mfma_f32_16x16x32_bf16 v[204:207], v[144:147], v[232:235], v[204:207]\n\t" \
    "v_and_b32 v252, 0x3ff, %[xa]\n\t" \
    "ds_read_b128 v[228:231], v252 offset:43008\n\t" \
    "v_mov_b64 v[244:245], 0\n\t" \
    "v_mfma_f32_16x16x32_bf16 v[208:211], v[152:155], v[232:235], v[208:211]\n\t" \
    "v_mov_b64 v[246:247], 0\n\t" \
    "v_mfma_f32_16x16x32_bf16 v[200:203], v[136:139], v[232:235], v[200:203]\n\t" \
    "v_mov_b64 v[248:249], 0\n\t" \
    "s_waitcnt lgkmcnt(3)\n\t" \
    "v_mfma_f32_16x16x32_bf16 v[216:219], v[144:147], v[236:239], v[216:219]\n\t" \
    "ds_read_b128 v[232:235], v252 offset:44032\n\t" \
    "v_mov_b64 v[250:251], 0\n\t" \
    "v_mfma_f32_16x16x32_bf16 v[220:223], v[152:155], v[236:239], v[220:223]\n\t" \
    "v_mfma_f32_16x16x32_bf16 v[212:215], v[136:139], v[236:239], v[212:215]\n\t" \
    "s_waitcnt lgkmcnt(3)\n\t" \
    "v_mfma_f32_16x16x32_bf16 v[204:207], v[148:151], v[240:243], v[204:207]\n\t" \
    "ds_read_b128 v[236:239], v252 offset:47104\n\t" \
    "v_mfma_f32_16x16x32_bf16 v[208:211], v[156:159], v[240:243], v[208:211]\n\t" \
    "v_mfma_f32_16x16x32_bf16 v[200:203], v[140:143], v[240:243], v[200:203]\n\t" \
    "s_waitcnt lgkmcnt(3)\n\t" \
    "v_mfma_f32_16x16x32_bf16 v[216:219], v[148:151], v[224:227], v[216:219]\n\t" \
    "ds_read_b128 v[240:243], v252 offset:48128\n\t" \
    "v_mfma_f32_16x16x32_bf16 v[220:223], v[156:159], v[224:227], v[220:223]\n\t" \
    "v_mfma_f32_16x16x32_bf16 v[212:215], v[140:143], v[224:227], v[212:215]\n\t" \
    "s_waitcnt lgkmcnt(3)\n\t" \
    "v_mfma_f32_16x16x32_bf16 v[204:207], v[184:187], v[228:231], v[204:207]\n\t" \
    "v_add_u32 v252, %[qs], v252\n\t" \
    "v_mfma_f32_16x16x32_bf16 v[208:211], v[192:195], v[228:231], v[208:211]\n\t" \
    "v_mov_b64 v[224:225], 0\n\t" \
    "v_mfma_f32_16x16x32_bf16 v[200:203], v[176:179], v[228:231], v[200:203]\n\t" \
    "v_mov_b64 v[226:227], 0\n\t" \
    "s_waitcnt lgkmcnt(2)\n\t" \
    "v_mfma_f32_16x16x32_bf16 v[204:207], v[188:191], v[232:235], v[204:207]\n\t" \
    "v_mfma_f32_16x16x32_bf16 v[208:211], v[196:199], v[232:235], v[208:211]\n\t" \
    "v_mfma_f32_16x16x32_bf16 v[200:203], v[180:183], v[232:235], v[200:203]\n\t" \
    "s_waitcnt lgkmcnt(1)\n\t" \
    "v_mfma_f32_16x16x32_bf16 v[216:219], v[184:187], v[236:239], v[216:219]\n\t" \
    "ds_read_b128 v[228:231], %[wl] offset:2048\n\t" \
    "ds_read_b128 v[232:235], %[wl] offset:3072\n\t" \
    "v_mfma_f32_16x16x32_bf16 v[220:223], v[192:195], v[236:239], v[220:223]\n\t" \
    "v_cvt_pk_bf16_f32 v204, v204, v205\n\t" \
    "v_cvt_pk_bf16_f32 v205, v206, v207\n\t" \
    "v_mfma_f32_16x16x32_bf16 v[212:215], v[176:179], v[236:239], v[212:215]\n\t" \
    "ds_write_b64 v252, v[204:205] offset:1024\n\t" \
    "v_cvt_pk_bf16_f32 v208, v208, v209\n\t" \
    "s_waitcnt lgkmcnt(3)\n\t" \
    "v_mfma_f32_16x16x32_bf16 v[216:219], v[188:191], v[240:243], v[216:219]\n\t" \
    "v_cvt_pk_bf16_f32 v209, v210, v211\n\t" \
    "ds_write_b64 v252, v[208:209] offset:2048\n\t" \
    "v_mfma_f32_16x16x32_bf16 v[220:223], v[196:199], v[240:243], v[220:223]\n\t" \
    "v_cvt_pk_bf16_f32 v200, v200, v201\n\t" \
    "v_cvt_pk_bf16_f32 v201, v202, v203\n\t" \
    "v_mfma_f32_16x16x32_bf16 v[212:215], v[180:183], v[240:243], v[212:215]\n\t" \
    "ds_write_b64 v252, v[200:201] offset:0\n\t" \
    "ds_read_b128 v[200:203], %[wl] offset:0\n\t" \
    "ds_read_b128 v[204:207], %[wl] offset:1024\n\t" \
    "v_cvt_pk_bf16_f32 v216, v216, v217\n\t" \
    "v_cvt_pk_bf16_f32 v217, v218, v219\n\t" \
    "ds_write_b64 v252, v[216:217] offset:7168\n\t" \
    "v_cvt_pk_bf16_f32 v220, v220, v221\n\t" \
    "v_cvt_pk_bf16_f32 v221, v222, v223\n\t" \
    "ds_write_b64 v252, v[220:221] offset:8192\n\t" \
    "v_cvt_pk_bf16_f32 v212, v212, v213\n\t" \
    "v_cvt_pk_bf16_f32 v213, v214, v215\n\t" \
    "ds_write_b64 v252, v[212:213] offset:6144\n\t" \
    "s_waitcnt lgkmcnt(0)\n\t"
#define ASM_CRIT1 \
    "v_bfe_i32 v241, %[ab], 7, 1\n\t" \
    "v_and_b32 v241, 0xaaaa, v241\n\t" \
    "v_or_b32 v241, 0x4444, v241\n\t" \
    "v_xor_b32 v212, 0x400, %[ab]\n\t" \
    "v_xor_b32 v216, 0x800, %[ab]\n\t" \
    "v_smfmac_f32_16x16x64_bf16 v[224:227], v[208:211], v[200:207], v241\n\t" \
    "v_smfmac_f32_16x16x64_bf16 v[244:247], v[208:211], v[0:7], v241\n\t" \
    "v_smfmac_f32_16x16x64_bf16 v[248:251], v[208:211], v[8:15], v241\n\t" \
    "v_xor_b32 v220, 0xc00, %[ab]\n\t" \
    "v_xor_b32 v236, 0x1000, %[ab]\n\t" \
    "v_xor_b32 v240, 0x1400, %[ab]\n\t" \
    "s_waitcnt lgkmcnt(0)\n\t" \
    "s_barrier\n\t" ASM_ST \
    "ds_read_b128 v[212:215], v212 offset:0\n\t" \
    "ds_read_b128 v[216:219], v216 offset:0\n\t" \
    "ds_read_b128 v[220:223], v220 offset:0\n\t" \
    "ds_read_b128 v[236:239], v236 offset:0\n\t" \
    "ds_read_b128 v[208:211], v240 offset:0\n\t" \
    "ds_read_b128 v[200:203], %[wl] offset:4096\n\t" \
    "ds_read_b128 v[204:207], %[wl] offset:5120\n\t" \
    "s_waitcnt lgkmcnt(6)\n\t" \
    "v_smfmac_f32_16x16x64_bf16 v[224:227], v[212:215], v[16:23], v241\n\t" \
    "v_smfmac_f32_16x16x64_bf16 v[244:247], v[212:215], v[24:31], v241\n\t" \
    "v_xor_b32 v240, 0x1800, %[ab]\n\t" \
    "v_smfmac_f32_16x16x64_bf16 v[248:251], v[212:215], v[32:39], v241\n\t" \
    "ds_read_b128 v[212:215], v240 offset:0\n\t" \
    "s_waitcnt lgkmcnt(6)\n\t" \
    "v_smfmac_f32_16x16x64_bf16 v[224:227], v[216:219], v[228:235], v241\n\t" \
    "ds_read_b128 v[228:231], %[wl] offset:6144\n\t" \
    "ds_read_b128 v[232:235], %[wl] offset:7168\n\t" \
    "v_smfmac_f32_16x16x64_bf16 v[244:247], v[216:219], v[40:47], v241\n\t" \
    "v_xor_b32 v240, 0x1c00, %[ab]\n\t" \
    "v_smfmac_f32_16x16x64_bf16 v[248:251], v[216:219], v[48:55], v241\n\t" \
    "ds_read_b128 v[216:219], v240 offset:0\n\t" \
    "s_waitcnt lgkmcnt(8)\n\t" \
    "v_smfmac_f32_16x16x64_bf16 v[224:227], v[220:223], v[56:63], v241\n\t" \
    "v_smfmac_f32_16x16x64_bf16 v[244:247], v[220:223], v[64:71], v241\n\t" \
    "v_and_b32 v240, 0x3ff, %[ab]\n\t" \
    "v_smfmac_f32_16x16x64_bf16 v[248:251], v[220:223], v[72:79], v241\n\t" \
    "ds_read_b128 v[220:223], v240 offset:8192\n\t" \
    "s_waitcnt lgkmcnt(5)\n\t" \
    "v_smfmac_f32_16x16x64_bf16 v[224:227], v[236:239], v[200:207], v241\n\t" \
    "ds_read_b128 v[200:203], %[wl] offset:8192\n\t" \
    "ds_read_b128 v[204:207], %[wl] offset:9216\n\t" \
    "v_smfmac_f32_16x16x64_bf16 v[244:247], v[236:239], v[80:87], v241\n\t" \
    "v_smfmac_f32_16x16x64_bf16 v[248:251], v[236:239], v[88:95], v241\n\t" \
    "ds_read_b128 v[236:239], v240 offset:9216\n\t" \
    "v_smfmac_f32_16x16x64_bf16 v[244:247], v[208:211], v[104:111], v241\n\t" \
    "s_waitcnt lgkmcnt(7)\n\t" \
    "v_smfmac_f32_16x16x64_bf16 v[244:247], v[212:215], v[120:127], v241\n\t" \
    "s_waitcnt lgkmcnt(4)\n\t" \
    "v_smfmac_f32_16x16x64_bf16 v[244:247], v[216:219], v[144:151], v241\n\t" \
    "s_waitcnt lgkmcnt(3)\n\t" \
    "v_smfmac_f32_16x16x64_bf16 v[244:247], v[220:223], v[160:167], v241\n\t" \
    "s_waitcnt lgkmcnt(0)\n\t" \
    "v_smfmac_f32_16x16x64_bf16 v[244:247], v[236:239], v[184:191], v241\n\t" \
    "v_smfmac_f32_16x16x64_bf16 v[248:251], v[208:211], v[112:119], v241\n\t" \
    "v_smfmac_f32_16x16x64_bf16 v[248:251], v[212:215], v[128:135], v241\n\t" \
    "v_smfmac_f32_16x16x64_bf16 v[248:251], v[216:219], v[152:159], v241\n\t" \
    "v_permlane32_swap_b32 v244, v246\n\t" \
    "v_permlane32_swap_b32 v245, v247\n\t" \
    "v_smfmac_f32_16x16x64_bf16 v[248:251], v[220:223], v[168:175], v241\n\t" \
    "v_add_f32 v244, v244, v246\n\t" \
    "v_add_f32 v245, v245, v247\n\t" \
    "v_smfmac_f32_16x16x64_bf16 v[248:251], v[236:239], v[192:199], v241\n\t" \
    "v_cvt_pk_bf16_f32 v244, v244, v245\n\t" \
    "ds_write_b16 %[dst], v244 offset:512\n\t" \
    "ds_write_b16_d16_hi %[dst], v244 offset:528\n\t" \
    "v_smfmac_f32_16x16x64_bf16 v[224:227], v[208:211], v[96:103], v241\n\t" \
    "v_smfmac_f32_16x16x64_bf16 v[224:227], v[212:215], v[228:235], v241\n\t" \
    "v_smfmac_f32_16x16x64_bf16 v[224:227], v[216:219], v[136:143], v241\n\t" \
    "v_permlane32_swap_b32 v248, v250\n\t" \
    "v_permlane32_swap_b32 v249, v251\n\t" \
    "v_smfmac_f32_16x16x64_bf16 v[224:227], v[220:223], v[200:207], v241\n\t" \
    "v_add_f32 v248, v248, v250\n\t" \
    "v_add_f32 v249, v249, v251\n\t" \
    "v_smfmac_f32_16x16x64_bf16 v[224:227], v[236:239], v[176:183], v241\n\t" \
    "v_cvt_pk_bf16_f32 v248, v248, v249\n\t" \
    "ds_write_b16 %[dst], v248 offset:1024\n\t" \
    "ds_write_b16_d16_hi %[dst], v248 offset:1040\n\t" \
    "s_nop 4\n\t" \
    "v_permlane32_swap_b32 v224, v226\n\t" \
    "v_permlane32_swap_b32 v225, v227\n\t" \
    "v_add_f32 v224, v224, v226\n\t" \
    "v_add_f32 v225, v225, v227\n\t" \
    "v_cvt_pk_bf16_f32 v224, v224, v225\n\t" \
    "ds_write_b16 %[dst], v224 offset:0\n\t" \
    "ds_write_b16_d16_hi %[dst], v224 offset:16\n\t"
#define ASM_CRIT1_N \
    "v_bfe_i32 v241, %[ab], 7, 1\n\t" \
    "v_and_b32 v241, 0xaaaa, v241\n\t" \
    "v_or_b32 v241, 0x4444, v241\n\t" \
    "v_xor_b32 v212, 0x400, %[ab]\n\t" \
    "v_xor_b32 v216, 0x800, %[ab]\n\t" \
    "v_xor_b32 v220, 0xc00, %[ab]\n\t" \
    "v_xor_b32 v236, 0x1000, %[ab]\n\t" \
    "v_xor_b32 v240, 0x1400, %[ab]\n\t" \
    "s_waitcnt lgkmcnt(0)\n\t" \
    "s_barrier\n\t" ASM_ST \
    "ds_read_b128 v[208:211], %[ab] offset:0\n\t" \
    "ds_read_b128 v[212:215], v212 offset:0\n\t" \
    "ds_read_b128 v[216:219], v216 offset:0\n\t" \
    "ds_read_b128 v[220:223], v220 offset:0\n\t" \
    "ds_read_b128 v[236:239], v236 offset:0\n\t" \
    "s_waitcnt lgkmcnt(4)\n\t" \
    "v_smfmac_f32_16x16x64_bf16 v[224:227], v[208:211], v[200:207], v241\n\t" \
    "v_smfmac_f32_16x16x64_bf16 v[244:247], v[208:211], v[0:7], v241\n\t" \
    "v_smfmac_f32_16x16x64_bf16 v[248:251], v[208:211], v[8:15], v241\n\t" \
    "ds_read_b128 v[208:211], v240 offset:0\n\t" \
    "ds_read_b128 v[200:203], %[wl] offset:4096\n\t" \
    "ds_read_b128 v[204:207], %[wl] offset:5120\n\t" \
    "s_waitcnt lgkmcnt(6)\n\t" \
    "v_smfmac_f32_16x16x64_bf16 v[224:227], v[212:215], v[16:23], v241\n\t" \
    "v_smfmac_f32_16x16x64_bf16 v[244:247], v[212:215], v[24:31], v241\n\t" \
    "v_xor_b32 v240, 0x1800, %[ab]\n\t" \
    "v_smfmac_f32_16x16x64_bf16 v[248:251], v[212:215], v[32:39], v241\n\t" \
    "ds_read_b128 v[212:215], v240 offset:0\n\t" \
    "s_waitcnt lgkmcnt(6)\n\t" \
    "v_smfmac_f32_16x16x64_bf16 v[224:227], v[216:219], v[228:235], v241\n\t" \
    "ds_read_b128 v[228:231], %[wl] offset:6144\n\t" \
    "ds_read_b128 v[232:235], %[wl] offset:7168\n\t" \
    "v_smfmac_f32_16x16x64_bf16 v[244:247], v[216:219], v[40:47], v241\n\t" \
    "v_xor_b32 v240, 0x1c00, %[ab]\n\t" \
    "v_smfmac_f32_16x16x64_bf16 v[248:251], v[216:219], v[48:55], v241\n\t" \
    "ds_read_b128 v[216:219], v240 offset:0\n\t" \
    "s_waitcnt lgkmcnt(8)\n\t" \
    "v_smfmac_f32_16x16x64_bf16 v[224:227], v[220:223], v[56:63], v241\n\t" \
    "v_smfmac_f32_16x16x64_bf16 v[244:247], v[220:223], v[64:71], v241\n\t" \
    "v_and_b32 v240, 0x3ff, %[ab]\n\t" \
    "v_smfmac_f32_16x16x64_bf16 v[248:251], v[220:223], v[72:79], v241\n\t" \
    "ds_read_b128 v[220:223], v240 offset:8192\n\t" \
    "s_waitcnt lgkmcnt(5)\n\t" \
    "v_smfmac_f32_16x16x64_bf16 v[224:227], v[236:239], v[200:207], v241\n\t" \
    "ds_read_b128 v[200:203], %[wl] offset:8192\n\t" \
    "ds_read_b128 v[204:207], %[wl] offset:9216\n\t" \
    "v_smfmac_f32_16x16x64_bf16 v[244:247], v[236:239], v[80:87], v241\n\t" \
    "v_smfmac_f32_16x16x64_bf16 v[248:251], v[236:239], v[88:95], v241\n\t" \
    "ds_read_b128 v[236:239], v240 offset:9216\n\t" \
    "v_smfmac_f32_16x16x64_bf16 v[244:247], v[208:211], v[104:111], v241\n\t" \
    "s_waitcnt lgkmcnt(7)\n\t" \
    "v_smfmac_f32_16x16x64_bf16 v[244:247], v[212:215], v[120:127], v241\n\t" \
    "s_waitcnt lgkmcnt(4)\n\t" \
    "v_smfmac_f32_16x16x64_bf16 v[244:247], v[216:219], v[144:151], v241\n\t" \
    "s_waitcnt lgkmcnt(3)\n\t" \
    "v_smfmac_f32_16x16x64_bf16 v[244:247], v[220:223], v[160:167], v241\n\t" \
    "s_waitcnt lgkmcnt(0)\n\t" \
    "v_smfmac_f32_16x16x64_bf16 v[244:247], v[236:239], v[184:191], v241\n\t" \
    "v_smfmac_f32_16x16x64_bf16 v[248:251], v[208:211], v[112:119], v241\n\t" \
    "v_smfmac_f32_16x16x64_bf16 v[248:251], v[212:215], v[128:135], v241\n\t" \
    "v_smfmac_f32_16x16x64_bf16 v[248:251], v[216:219], v[152:159], v241\n\t" \
    "v_permlane32_swap_b32 v244, v246\n\t" \
    "v_permlane32_swap_b32 v245, v247\n\t" \
    "v_smfmac_f32_16x16x64_bf16 v[248:251], v[220:223], v[168:175], v241\n\t" \
    "v_add_f32 v244, v244, v246\n\t" \
    "v_add_f32 v245, v245, v247\n\t" \
    "v_smfmac_f32_16x16x64_bf16 v[248:251], v[236:239], v[192:199], v241\n\t" \
    "v_cvt_pk_bf16_f32 v244, v244, v245\n\t" \
    "ds_write_b16 %[dst], v244 offset:512\n\t" \
    "ds_write_b16_d16_hi %[dst], v244 offset:528\n\t" \
    "v_smfmac_f32_16x16x64_bf16 v[224:227], v[208:211], v[96:103], v241\n\t" \
    "v_smfmac_f32_16x16x64_bf16 v[224:227], v[212:215], v[228:235], v241\n\t" \
    "v_smfmac_f32_16x16x64_bf16 v[224:227], v[216:219], v[136:143], v241\n\t" \
    "v_permlane32_swap_b32 v248, v250\n\t" \
    "v_permlane32_swap_b32 v249, v251\n\t" \
    "v_smfmac_f32_16x16x64_bf16 v[224:227], v[220:223], v[200:207], v241\n\t" \
    "v_add_f32 v248, v248, v250\n\t" \
    "v_add_f32 v249, v249, v251\n\t" \
    "v_smfmac_f32_16x16x64_bf16 v[224:227], v[236:239], v[176:183], v241\n\t" \
    "v_cvt_pk_bf16_f32 v248, v248, v249\n\t" \
    "ds_write_b16 %[dst], v248 offset:1024\n\t" \
    "ds_write_b16_d16_hi %[dst], v248 offset:1040\n\t" \
    "s_nop 4\n\t" \
    "v_permlane32_swap_b32 v224, v226\n\t" \
    "v_permlane32_swap_b32 v225, v227\n\t" \
    "v_add_f32 v224, v224, v226\n\t" \
    "v_add_f32 v225, v225, v227\n\t" \
    "v_cvt_pk_bf16_f32 v224, v224, v225\n\t" \
    "ds_write_b16 %[dst], v224 offset:0\n\t" \
    "ds_write_b16_d16_hi %[dst], v224 offset:16\n\t"
#define ASM_VOX \
    "s_waitcnt lgkmcnt(0)\n\t" \
    "ds_read_b128 v[224:227], %[va] offset:0\n\t" \
    "ds_read_b128 v[248:251], %[wl] offset:0\n\t" \
    "ds_read_b128 v[228:231], %[va] offset:6144\n\t" \
    "ds_read_b128 v[232:235], %[va] offset:2048\n\t" \
    "ds_read_b128 v[236:239], %[va] offset:8192\n\t" \
    "ds_read_b128 v[240:243], %[va] offset:3072\n\t" \
    "s_waitcnt lgkmcnt(4)\n\t" \
    "v_mfma_f32_16x16x32_bf16 v[200:203], v[224:227], v[248:251], 0\n\t" \
    "ds_read_b128 v[244:247], %[va] offset:9216\n\t" \
    "s_waitcnt lgkmcnt(4)\n\t" \
    "v_mfma_f32_16x16x32_bf16 v[204:207], v[228:231], v[248:251], 0\n\t" \
    "ds_read_b128 v[224:227], %[va] offset:1024\n\t" \
    "s_waitcnt lgkmcnt(4)\n\t" \
    "v_mfma_f32_16x16x32_bf16 v[200:203], v[232:235], v[0:3], v[200:203]\n\t" \
    "ds_read_b128 v[228:231], %[va] offset:7168\n\t" \
    "ds_read_b128 v[248:251], %[wl] offset:1024\n\t" \
    "s_waitcnt lgkmcnt(5)\n\t" \
    "v_mfma_f32_16x16x32_bf16 v[204:207], v[236:239], v[0:3], v[204:207]\n\t" \
    "ds_read_b128 v[232:235], %[va] offset:4096\n\t" \
    "s_waitcnt lgkmcnt(5)\n\t" \
    "v_mfma_f32_16x16x32_bf16 v[200:203], v[240:243], v[4:7], v[200:203]\n\t" \
    "ds_read_b128 v[236:239], %[va] offset:10240\n\t" \
    "s_waitcnt lgkmcnt(5)\n\t" \
    "v_mfma_f32_16x16x32_bf16 v[204:207], v[244:247], v[4:7], v[204:207]\n\t" \
    "ds_read_b128 v[240:243], %[va] offset:5120\n\t" \
    "s_waitcnt lgkmcnt(3)\n\t" \
    "v_mfma_f32_16x16x32_bf16 v[200:203], v[224:227], v[248:251], v[200:203]\n\t" \
    "ds_read_b128 v[244:247], %[va] offset:11264\n\t" \
    "v_mfma_f32_16x16x32_bf16 v[208:211], v[224:227], v[20:23], 0\n\t" \
    "v_mfma_f32_16x16x32_bf16 v[204:207], v[228:231], v[248:251], v[204:207]\n\t" \
    "ds_read_b128 v[224:227], %[va] offset:0\n\t" \
    "v_mfma_f32_16x16x32_bf16 v[212:215], v[228:231], v[20:23], 0\n\t" \
    "s_waitcnt lgkmcnt(4)\n\t" \
    "v_mfma_f32_16x16x32_bf16 v[200:203], v[232:235], v[8:11], v[200:203]\n\t" \
    "ds_read_b128 v[228:231], %[va] offset:6144\n\t" \
    "ds_read_b128 v[248:251], %[wl] offset:2048\n\t" \
    "v_mfma_f32_16x16x32_bf16 v[208:211], v[232:235], v[32:35], v[208:211]\n\t" \
    "s_waitcnt lgkmcnt(5)\n\t" \
    "v_mfma_f32_16x16x32_bf16 v[204:207], v[236:239], v[8:11], v[204:207]\n\t" \
    "ds_read_b128 v[232:235], %[va] offset:2048\n\t" \
    "v_mfma_f32_16x16x32_bf16 v[212:215], v[236:239], v[32:35], v[212:215]\n\t" \
    "s_waitcnt lgkmcnt(5)\n\t" \
    "v_mfma_f32_16x16x32_bf16 v[200:203], v[240:243], v[12:15], v[200:203]\n\t" \
    "ds_read_b128 v[236:239], %[va] offset:8192\n\t" \
    "v_mfma_f32_16x16x32_bf16 v[208:211], v[240:243], v[36:39], v[208:211]\n\t" \
    "s_waitcnt lgkmcnt(5)\n\t" \
    "v_mfma_f32_16x16x32_bf16 v[204:207], v[244:247], v[12:15], v[204:207]\n\t" \
    "ds_read_b128 v[240:243], %[va] offset:3072\n\t" \
    "v_mfma_f32_16x16x32_bf16 v[212:215], v[244:247], v[36:39], v[212:215]\n\t" \
    "s_waitcnt lgkmcnt(3)\n\t" \
    "v_mfma_f32_16x16x32_bf16 v[216:219], v[224:227], v[248:251], 0\n\t" \
    "ds_read_b128 v[244:247], %[va] offset:9216\n\t" \
    "v_mfma_f32_16x16x32_bf16 v[208:211], v[224:227], v[16:19], v[208:211]\n\t" \
    "v_mfma_f32_16x16x32_bf16 v[220:223], v[228:231], v[248:251], 0\n\t" \
    "ds_read_b128 v[224:227], %[va] offset:1024\n\t" \
    "v_mfma_f32_16x16x32_bf16 v[212:215], v[228:231], v[16:19], v[212:215]\n\t" \
    "v_cvt_pk_bf16_f32 v200, v200, v201\n\t" \
    "s_waitcnt lgkmcnt(4)\n\t" \
    "v_mfma_f32_16x16x32_bf16 v[216:219], v[232:235], v[40:43], v[216:219]\n\t" \
    "ds_read_b128 v[228:231], %[va] offset:7168\n\t" \
    "ds_read_b128 v[248:251], %[wl] offset:3072\n\t" \
    "v_cvt_pk_bf16_f32 v201, v202, v203\n\t" \
    "v_mfma_f32_16x16x32_bf16 v[208:211], v[232:235], v[24:27], v[208:211]\n\t" \
    "v_cvt_pk_bf16_f32 v202, v204, v205\n\t" \
    "s_waitcnt lgkmcnt(5)\n\t" \
    "v_mfma_f32_16x16x32_bf16 v[220:223], v[236:239], v[40:43], v[220:223]\n\t" \
    "ds_read_b128 v[232:235], %[va] offset:4096\n\t" \
    "v_cvt_pk_bf16_f32 v203, v206, v207\n\t" \
    "v_mfma_f32_16x16x32_bf16 v[212:215], v[236:239], v[24:27], v[212:215]\n\t" \
    "buffer_store_dwordx4 v[200:203], %[l16], %[rd], %[so] offen sc1\n\t" \
    "s_waitcnt lgkmcnt(5)\n\t" \
    "v_mfma_f32_16x16x32_bf16 v[216:219], v[240:243], v[44:47], v[216:219]\n\t" \
    "ds_read_b128 v[236:239], %[va] offset:10240\n\t" \
    "s_add_u32 %[so], %[so], 0x8000\n\t" \
    "v_mfma_f32_16x16x32_bf16 v[208:211], v[240:243], v[28:31], v[208:211]\n\t" \
    "s_waitcnt lgkmcnt(5)\n\t" \
    "v_mfma_f32_16x16x32_bf16 v[220:223], v[244:247], v[44:47], v[220:223]\n\t" \
    "ds_read_b128 v[240:243], %[va] offset:5120\n\t" \
    "v_mfma_f32_16x16x32_bf16 v[212:215], v[244:247], v[28:31], v[212:215]\n\t" \
    "s_waitcnt lgkmcnt(3)\n\t" \
    "v_mfma_f32_16x16x32_bf16 v[216:219], v[224:227], v[248:251], v[216:219]\n\t" \
    "ds_read_b128 v[244:247], %[va] offset:11264\n\t" \
    "v_mfma_f32_16x16x32_bf16 v[200:203], v[224:227], v[60:63], 0\n\t" \
    "v_mfma_f32_16x16x32_bf16 v[220:223], v[228:231], v[248:251], v[220:223]\n\t" \
    "ds_read_b128 v[224:227], %[va] offset:0\n\t" \
    "v_mfma_f32_16x16x32_bf16 v[204:207], v[228:231], v[60:63], 0\n\t" \
    "v_cvt_pk_bf16_f32 v208, v208, v209\n\t" \
    "s_waitcnt lgkmcnt(4)\n\t" \
    "v_mfma_f32_16x16x32_bf16 v[216:219], v[232:235], v[48:51], v[216:219]\n\t" \
    "ds_read_b128 v[228:231], %[va] offset:6144\n\t" \
    "ds_read_b128 v[248:251], %[wl] offset:4096\n\t" \
    "v_cvt_pk_bf16_f32 v209, v210, v211\n\t" \
    "v_mfma_f32_16x16x32_bf16 v[200:203], v[232:235], v[72:75], v[200:203]\n\t" \
    "v_cvt_pk_bf16_f32 v210, v212, v213\n\t" \
    "s_waitcnt lgkmcnt(5)\n\t" \
    "v_mfma_f32_16x16x32_bf16 v[220:223], v[236:239], v[48:51], v[220:223]\n\t" \
    "ds_read_b128 v[232:235], %[va] offset:2048\n\t" \
    "v_cvt_pk_bf16_f32 v211, v214, v215\n\t" \
    "v_mfma_f32_16x16x32_bf16 v[204:207], v[236:239], v[72:75], v[204:207]\n\t" \
    "buffer_store_dwordx4 v[208:211], %[l16], %[rd], %[so] offen sc1\n\t" \
    "s_waitcnt lgkmcnt(5)\n\t" \
    "v_mfma_f32_16x16x32_bf16 v[216:219], v[240:243], v[52:55], v[216:219]\n\t" \
    "ds_read_b128 v[236:239], %[va] offset:8192\n\t" \
    "s_add_u32 %[so], %[so], 0x8000\n\t" \
    "v_mfma_f32_16x16x32_bf16 v[200:203], v[240:243], v[76:79], v[200:203]\n\t" \
    "s_waitcnt lgkmcnt(5)\n\t" \
    "v_mfma_f32_16x16x32_bf16 v[220:223], v[244:247], v[52:55], v[220:223]\n\t" \
    "ds_read_b128 v[240:243], %[va] offset:3072\n\t" \
    "v_mfma_f32_16x16x32_bf16 v[204:207], v[244:247], v[76:79], v[204:207]\n\t" \
    "s_waitcnt lgkmcnt(3)\n\t" \
    "v_mfma_f32_16x16x32_bf16 v[208:211], v[224:227], v[248:251], 0\n\t" \
    "ds_read_b128 v[244:247], %[va] offset:9216\n\t" \
    "v_mfma_f32_16x16x32_bf16 v[200:203], v[224:227], v[56:59], v[200:203]\n\t" \
    "v_mfma_f32_16x16x32_bf16 v[212:215], v[228:231], v[248:251], 0\n\t" \
    "ds_read_b128 v[224:227], %[va] offset:1024\n\t" \
    "v_mfma_f32_16x16x32_bf16 v[204:207], v[228:231], v[56:59], v[204:207]\n\t" \
    "v_cvt_pk_bf16_f32 v216, v216, v217\n\t" \
    "s_waitcnt lgkmcnt(4)\n\t" \
    "v_mfma_f32_16x16x32_bf16 v[208:211], v[232:235], v[80:83], v[208:211]\n\t" \
    "ds_read_b128 v[228:231], %[va] offset:7168\n\t" \
    "ds_read_b128 v[248:251], %[wl] offset:5120\n\t" \
    "v_cvt_pk_bf16_f32 v217, v218, v219\n\t" \
    "v_mfma_f32_16x16x32_bf16 v[200:203], v[232:235], v[64:67], v[200:203]\n\t" \
    "v_cvt_pk_bf16_f32 v218, v220, v221\n\t" \
    "s_waitcnt lgkmcnt(5)\n\t" \
    "v_mfma_f32_16x16x32_bf16 v[212:215], v[236:239], v[80:83], v[212:215]\n\t" \
    "ds_read_b128 v[232:235], %[va] offset:4096\n\t" \
    "v_cvt_pk_bf16_f32 v219, v222, v223\n\t" \
    "v_mfma_f32_16x16x32_bf16 v[204:207], v[236:239], v[64:67], v[204:207]\n\t" \
    "buffer_store_dwordx4 v[216:219], %[l16], %[rd], %[so] offen sc1\n\t" \
    "s_waitcnt lgkmcnt(5)\n\t" \
    "v_mfma_f32_16x16x32_bf16 v[208:211], v[240:243], v[84:87], v[208:211]\n\t" \
    "ds_read_b128 v[236:239], %[va] offset:10240\n\t" \
    "s_add_u32 %[so], %[so], 0x8000\n\t" \
    "v_mfma_f32_16x16x32_bf16 v[200:203], v[240:243], v[68:71], v[200:203]\n\t" \
    "s_waitcnt lgkmcnt(5)\n\t" \
    "v_mfma_f32_16x16x32_bf16 v[212:215], v[244:247], v[84:87], v[212:215]\n\t" \
    "ds_read_b128 v[240:243], %[va] offset:5120\n\t" \
    "v_mfma_f32_16x16x32_bf16 v[204:207], v[244:247], v[68:71], v[204:207]\n\t" \
    "s_waitcnt lgkmcnt(3)\n\t" \
    "v_mfma_f32_16x16x32_bf16 v[208:211], v[224:227], v[248:251], v[208:211]\n\t" \
    "ds_read_b128 v[244:247], %[va] offset:11264\n\t" \
    "v_mfma_f32_16x16x32_bf16 v[216:219], v[224:227], v[100:103], 0\n\t" \
    "v_mfma_f32_16x16x32_bf16 v[212:215], v[228:231], v[248:251], v[212:215]\n\t" \
    "ds_read_b128 v[224:227], %[va] offset:0\n\t" \
    "v_mfma_f32_16x16x32_bf16 v[220:223], v[228:231], v[100:103], 0\n\t" \
    "v_cvt_pk_bf16_f32 v200, v200, v201\n\t" \
    "s_waitcnt lgkmcnt(4)\n\t" \
    "v_mfma_f32_16x16x32_bf16 v[208:211], v[232:235], v[88:91], v[208:211]\n\t" \
    "ds_read_b128 v[228:231], %[va] offset:6144\n\t" \
    "ds_read_b128 v[248:251], %[wl] offset:6144\n\t" \
    "v_cvt_pk_bf16_f32 v201, v202, v203\n\t" \
    "v_mfma_f32_16x16x32_bf16 v[216:219], v[232:235], v[112:115], v[216:219]\n\t" \
    "v_cvt_pk_bf16_f32 v202, v204, v205\n\t" \
    "s_waitcnt lgkmcnt(5)\n\t" \
    "v_mfma_f32_16x16x32_bf16 v[212:215], v[236:239], v[88:91], v[212:215]\n\t" \
    "ds_read_b128 v[232:235], %[va] offset:2048\n\t" \
    "v_cvt_pk_bf16_f32 v203, v206, v207\n\t" \
    "v_mfma_f32_16x16x32_bf16 v[220:223], v[236:239], v[112:115], v[220:223]\n\t" \
    "buffer_store_dwordx4 v[200:203], %[l16], %[rd], %[so] offen sc1\n\t" \
    "s_waitcnt lgkmcnt(5)\n\t" \
    "v_mfma_f32_16x16x32_bf16 v[208:211], v[240:243], v[92:95], v[208:211]\n\t" \
    "ds_read_b128 v[236:239], %[va] offset:8192\n\t" \
    "s_add_u32 %[so], %[so], 0x8000\n\t" \
    "v_mfma_f32_16x16x32_bf16 v[216:219], v[240:243], v[116:119], v[216:219]\n\t" \
    "s_waitcnt lgkmcnt(5)\n\t" \
    "v_mfma_f32_16x16x32_bf16 v[212:215], v[244:247], v[92:95], v[212:215]\n\t" \
    "ds_read_b128 v[240:243], %[va] offset:3072\n\t" \
    "v_mfma_f32_16x16x32_bf16 v[220:223], v[244:247], v[116:119], v[220:223]\n\t" \
    "s_waitcnt lgkmcnt(3)\n\t" \
    "v_mfma_f32_16x16x32_bf16 v[200:203], v[224:227], v[248:251], 0\n\t" \
    "ds_read_b128 v[244:247], %[va] offset:9216\n\t" \
    "v_mfma_f32_16x16x32_bf16 v[216:219], v[224:227], v[96:99], v[216:219]\n\t" \
    "v_mfma_f32_16x16x32_bf16 v[204:207], v[228:231], v[248:251], 0\n\t" \
    "ds_read_b128 v[224:227], %[va] offset:1024\n\t" \
    "v_mfma_f32_16x16x32_bf16 v[220:223], v[228:231], v[96:99], v[220:223]\n\t" \
    "v_cvt_pk_bf16_f32 v208, v208, v209\n\t" \
    "s_waitcnt lgkmcnt(4)\n\t" \
    "v_mfma_f32_16x16x32_bf16 v[200:203], v[232:235], v[120:123], v[200:203]\n\t" \
    "ds_read_b128 v[228:231], %[va] offset:7168\n\t" \
    "ds_read_b128 v[248:251], %[wl] offset:7168\n\t" \
    "v_cvt_pk_bf16_f32 v209, v210, v211\n\t" \
    "v_mfma_f32_16x16x32_bf16 v[216:219], v[232:235], v[104:107], v[216:219]\n\t" \
    "v_cvt_pk_bf16_f32 v210, v212, v213\n\t" \
    "s_waitcnt lgkmcnt(5)\n\t" \
    "v_mfma_f32_16x16x32_bf16 v[204:207], v[236:239], v[120:123], v[204:207]\n\t" \
    "ds_read_b128 v[232:235], %[va] offset:4096\n\t" \
    "v_cvt_pk_bf16_f32 v211, v214, v215\n\t" \
    "v_mfma_f32_16x16x32_bf16 v[220:223], v[236:239], v[104:107], v[220:223]\n\t" \
    "buffer_store_dwordx4 v[208:211], %[l16], %[rd], %[so] offen sc1\n\t" \
    "s_waitcnt lgkmcnt(5)\n\t" \
    "v_mfma_f32_16x16x32_bf16 v[200:203], v[240:243], v[124:127], v[200:203]\n\t" \
    "ds_read_b128 v[236:239], %[va] offset:10240\n\t" \
    "s_add_u32 %[so], %[so], 0x8000\n\t" \
    "v_mfma_f32_16x16x32_bf16 v[216:219], v[240:243], v[108:111], v[216:219]\n\t" \
    "s_waitcnt lgkmcnt(5)\n\t" \
    "v_mfma_f32_16x16x32_bf16 v[204:207], v[244:247], v[124:127], v[204:207]\n\t" \
    "ds_read_b128 v[240:243], %[va] offset:5120\n\t" \
    "v_mfma_f32_16x16x32_bf16 v[220:223], v[244:247], v[108:111], v[220:223]\n\t" \
    "s_waitcnt lgkmcnt(3)\n\t" \
    "v_mfma_f32_16x16x32_bf16 v[200:203], v[224:227], v[248:251], v[200:203]\n\t" \
    "ds_read_b128 v[244:247], %[va] offset:11264\n\t" \
    "v_mfma_f32_16x16x32_bf16 v[208:211], v[224:227], v[140:143], 0\n\t" \
    "v_mfma_f32_16x16x32_bf16 v[204:207], v[228:231], v[248:251], v[204:207]\n\t" \
    "ds_read_b128 v[224:227], %[va] offset:0\n\t" \
    "v_mfma_f32_16x16x32_bf16 v[212:215], v[228:231], v[140:143], 0\n\t" \
    "v_cvt_pk_bf16_f32 v216, v216, v217\n\t" \
    "s_waitcnt lgkmcnt(4)\n\t" \
    "v_mfma_f32_16x16x32_bf16 v[200:203], v[232:235], v[128:131], v[200:203]\n\t" \
    "ds_read_b128 v[228:231], %[va] offset:6144\n\t" \
    "ds_read_b128 v[248:251], %[wl] offset:8192\n\t" \
    "v_cvt_pk_bf16_f32 v217, v218, v219\n\t" \
    "v_mfma_f32_16x16x32_bf16 v[208:211], v[232:235], v[152:155], v[208:211]\n\t" \
    "v_cvt_pk_bf16_f32 v218, v220, v221\n\t" \
    "s_waitcnt lgkmcnt(5)\n\t" \
    "v_mfma_f32_16x16x32_bf16 v[204:207], v[236:239], v[128:131], v[204:207]\n\t" \
    "ds_read_b128 v[232:235], %[va] offset:2048\n\t" \
    "v_cvt_pk_bf16_f32 v219, v222, v223\n\t" \
    "v_mfma_f32_16x16x32_bf16 v[212:215], v[236:239], v[152:155], v[212:215]\n\t" \
    "buffer_store_dwordx4 v[216:219], %[l16], %[rd], %[so] offen sc1\n\t" \
    "s_waitcnt lgkmcnt(5)\n\t" \
    "v_mfma_f32_16x16x32_bf16 v[200:203], v[240:243], v[132:135], v[200:203]\n\t" \
    "ds_read_b128 v[236:239], %[va] offset:8192\n\t" \
    "s_add_u32 %[so], %[so], 0x8000\n\t" \
    "v_mfma_f32_16x16x32_bf16 v[208:211], v[240:243], v[156:159], v[208:211]\n\t" \
    "s_waitcnt lgkmcnt(5)\n\t" \
    "v_mfma_f32_16x16x32_bf16 v[204:207], v[244:247], v[132:135], v[204:207]\n\t" \
    "ds_read_b128 v[240:243], %[va] offset:3072\n\t" \
    "v_mfma_f32_16x16x32_bf16 v[212:215], v[244:247], v[156:159], v[212:215]\n\t" \
    "s_waitcnt lgkmcnt(3)\n\t" \
    "v_mfma_f32_16x16x32_bf16 v[216:219], v[224:227], v[248:251], 0\n\t" \
    "ds_read_b128 v[244:247], %[va] offset:9216\n\t" \
    "v_mfma_f32_16x16x32_bf16 v[208:211], v[224:227], v[136:139], v[208:211]\n\t" \
    "v_mfma_f32_16x16x32_bf16 v[220:223], v[228:231], v[248:251], 0\n\t" \
    "ds_read_b128 v[224:227], %[va] offset:1024\n\t" \
    "v_mfma_f32_16x16x32_bf16 v[212:215], v[228:231], v[136:139], v[212:215]\n\t" \
    "v_cvt_pk_bf16_f32 v200, v200, v201\n\t" \
    "s_waitcnt lgkmcnt(4)\n\t" \
    "v_mfma_f32_16x16x32_bf16 v[216:219], v[232:235], v[160:163], v[216:219]\n\t" \
    "ds_read_b128 v[228:231], %[va] offset:7168\n\t" \
    "ds_read_b128 v[248:251], %[wl] offset:9216\n\t" \
    "v_cvt_pk_bf16_f32 v201, v202, v203\n\t" \
    "v_mfma_f32_16x16x32_bf16 v[208:211], v[232:235], v[144:147], v[208:211]\n\t" \
    "v_cvt_pk_bf16_f32 v202, v204, v205\n\t" \
    "s_waitcnt lgkmcnt(5)\n\t" \
    "v_mfma_f32_16x16x32_bf16 v[220:223], v[236:239], v[160:163], v[220:223]\n\t" \
    "ds_read_b128 v[232:235], %[va] offset:4096\n\t" \
    "v_cvt_pk_bf16_f32 v203, v206, v207\n\t" \
    "v_mfma_f32_16x16x32_bf16 v[212:215], v[236:239], v[144:147], v[212:215]\n\t" \
    "buffer_store_dwordx4 v[200:203], %[l16], %[rd], %[so] offen sc1\n\t" \
    "s_waitcnt lgkmcnt(5)\n\t" \
    "v_mfma_f32_16x16x32_bf16 v[216:219], v[240:243], v[164:167], v[216:219]\n\t" \
    "ds_read_b128 v[236:239], %[va] offset:10240\n\t" \
    "s_add_u32 %[so], %[so], 0x8000\n\t" \
    "v_mfma_f32_16x16x32_bf16 v[208:211], v[240:243], v[148:151], v[208:211]\n\t" \
    "s_waitcnt lgkmcnt(5)\n\t" \
    "v_mfma_f32_16x16x32_bf16 v[220:223], v[244:247], v[164:167], v[220:223]\n\t" \
    "ds_read_b128 v[240:243], %[va] offset:5120\n\t" \
    "v_mfma_f32_16x16x32_bf16 v[212:215], v[244:247], v[148:151], v[212:215]\n\t" \
    "s_waitcnt lgkmcnt(3)\n\t" \
    "v_mfma_f32_16x16x32_bf16 v[216:219], v[224:227], v[248:251], v[216:219]\n\t" \
    "ds_read_b128 v[244:247], %[va] offset:11264\n\t" \
    "v_mfma_f32_16x16x32_bf16 v[200:203], v[224:227], v[180:183], 0\n\t" \
    "v_mfma_f32_16x16x32_bf16 v[220:223], v[228:231], v[248:251], v[220:223]\n\t" \
    "ds_read_b128 v[224:227], %[va] offset:0\n\t" \
    "v_mfma_f32_16x16x32_bf16 v[204:207], v[228:231], v[180:183], 0\n\t" \
    "v_cvt_pk_bf16_f32 v208, v208, v209\n\t" \
    "s_waitcnt lgkmcnt(4)\n\t" \
    "v_mfma_f32_16x16x32_bf16 v[216:219], v[232:235], v[168:171], v[216:219]\n\t" \
    "ds_read_b128 v[228:231], %[va] offset:6144\n\t" \
    "v_cvt_pk_bf16_f32 v209, v210, v211\n\t" \
    "v_mfma_f32_16x16x32_bf16 v[200:203], v[232:235], v[192:195], v[200:203]\n\t" \
    "v_cvt_pk_bf16_f32 v210, v212, v213\n\t" \
    "s_waitcnt lgkmcnt(4)\n\t" \
    "v_mfma_f32_16x16x32_bf16 v[220:223], v[236:239], v[168:171], v[220:223]\n\t" \
    "ds_read_b128 v[232:235], %[va] offset:2048\n\t" \
    "v_cvt_pk_bf16_f32 v211, v214, v215\n\t" \
    "v_mfma_f32_16x16x32_bf16 v[204:207], v[236:239], v[192:195], v[204:207]\n\t" \
    "buffer_store_dwordx4 v[208:211], %[l16], %[rd], %[so] offen sc1\n\t" \
    "s_waitcnt lgkmcnt(4)\n\t" \
    "v_mfma_f32_16x16x32_bf16 v[216:219], v[240:243], v[172:175], v[216:219]\n\t" \
    "ds_read_b128 v[236:239], %[va] offset:8192\n\t" \
    "s_add_u32 %[so], %[so], 0x8000\n\t" \
    "v_mfma_f32_16x16x32_bf16 v[200:203], v[240:243], v[196:199], v[200:203]\n\t" \
    "s_waitcnt lgkmcnt(4)\n\t" \
    "v_mfma_f32_16x16x32_bf16 v[220:223], v[244:247], v[172:175], v[220:223]\n\t" \
    "ds_read_b128 v[240:243], %[va] offset:3072\n\t" \
    "v_mfma_f32_16x16x32_bf16 v[204:207], v[244:247], v[196:199], v[204:207]\n\t" \
    "s_barrier \n\t" \
    "s_waitcnt lgkmcnt(4)\n\t" \
    "v_mfma_f32_16x16x32_bf16 v[200:203], v[224:227], v[176:179], v[200:203]\n\t" \
    "ds_read_b128 v[244:247], %[va] offset:9216\n\t" \
    "s_waitcnt lgkmcnt(4)\n\t" \
    "v_mfma_f32_16x16x32_bf16 v[204:207], v[228:231], v[176:179], v[204:207]\n\t" \
    "s_waitcnt lgkmcnt(3)\n\t" \
    "v_mfma_f32_16x16x32_bf16 v[200:203], v[232:235], v[184:187], v[200:203]\n\t" \
    "s_waitcnt lgkmcnt(2)\n\t" \
    "v_mfma_f32_16x16x32_bf16 v[204:207], v[236:239], v[184:187], v[204:207]\n\t" \
    "v_cvt_pk_bf16_f32 v216, v216, v217\n\t" \
    "s_waitcnt lgkmcnt(1)\n\t" \
    "v_mfma_f32_16x16x32_bf16 v[200:203], v[240:243], v[188:191], v[200:203]\n\t" \
    "v_cvt_pk_bf16_f32 v217, v218, v219\n\t" \
    "s_waitcnt lgkmcnt(0)\n\t" \
    "v_mfma_f32_16x16x32_bf16 v[204:207], v[244:247], v[188:191], v[204:207]\n\t" \
    "v_cvt_pk_bf16_f32 v218, v220, v221\n\t" \
    "v_cvt_pk_bf16_f32 v219, v222, v223\n\t" \
    "buffer_store_dwordx4 v[216:219], %[l16], %[rd], %[so] offen sc1\n\t" \
    "s_add_u32 %[so], %[so], 0x8000\n\t" \
    "s_nop 0\n\t" \
    "v_cvt_pk_bf16_f32 v200, v200, v201\n\t" \
    "v_cvt_pk_bf16_f32 v201, v202, v203\n\t" \
    "s_nop 1\n\t" \
    "v_cvt_pk_bf16_f32 v202, v204, v205\n\t" \
    "v_cvt_pk_bf16_f32 v203, v206, v207\n\t" \
    "buffer_store_dwordx4 v[200:203], %[l16], %[rd], %[so] offen sc1\n\t" \
    "s_add_u32 %[so], %[so], 0x8000\n\t" \
    "s_nop 1\n\t"
#define ASM_CRIT2 \
    "s_waitcnt lgkmcnt(0)\n\t" \
    "s_barrier\n\t" ASM_ST \
    "ds_read_b128 v[204:207], v200 offset:11264\n\t" \
    "ds_read_b128 v[208:211], v200 offset:12288\n\t" \
    "ds_read_b128 v[200:203], v200 offset:10240\n\t" \
    "s_waitcnt lgkmcnt(2)\n\t" \
    "v_smfmac_f32_16x16x64_bf16 v[224:227], v[204:207], v[24:31], %[sidx]\n\t" \
    "v_smfmac_f32_16x16x64_bf16 v[220:223], v[204:207], v[0:7], %[sidx]\n\t" \
    "s_waitcnt lgkmcnt(1)\n\t" \
    "v_smfmac_f32_16x16x64_bf16 v[224:227], v[208:211], v[32:39], %[sidx]\n\t" \
    "v_smfmac_f32_16x16x64_bf16 v[220:223], v[208:211], v[8:15], %[sidx]\n\t" \
    "s_waitcnt lgkmcnt(0)\n\t" \
    "v_smfmac_f32_16x16x64_bf16 v[224:227], v[200:203], v[16:23], %[sidx]\n\t" \
    "v_smfmac_f32_16x16x64_bf16 v[220:223], v[200:203], v[212:219], %[sidx]\n\t" \
    "ds_read_b128 v[212:215], %[wl] offset:2048\n\t" \
    "ds_read_b128 v[216:219], %[wl] offset:3072\n\t" \
    "v_smfmac_f32_16x16x64_bf16 v[232:235], v[204:207], v[64:71], %[sidx]\n\t" \
    "v_smfmac_f32_16x16x64_bf16 v[228:231], v[204:207], v[40:47], %[sidx]\n\t" \
    "v_smfmac_f32_16x16x64_bf16 v[232:235], v[208:211], v[72:79], %[sidx]\n\t" \
    "v_permlane32_swap_b32 v220, v224\n\t" \
    "v_permlane32_swap_b32 v221, v225\n\t" \
    "v_permlane32_swap_b32 v222, v226\n\t" \
    "v_smfmac_f32_16x16x64_bf16 v[228:231], v[208:211], v[48:55], %[sidx]\n\t" \
    "v_permlane32_swap_b32 v223, v227\n\t" \
    "v_add_f32 v220, v220, v224\n\t" \
    "v_add_f32 v221, v221, v225\n\t" \
    "v_smfmac_f32_16x16x64_bf16 v[232:235], v[200:203], v[56:63], %[sidx]\n\t" \
    "v_add_f32 v222, v222, v226\n\t" \
    "v_add_f32 v223, v223, v227\n\t" \
    "v_cvt_pk_bf16_f32 v226, v220, v221\n\t" \
    "s_waitcnt lgkmcnt(0)\n\t" \
    "v_smfmac_f32_16x16x64_bf16 v[228:231], v[200:203], v[212:219], %[sidx]\n\t" \
    "ds_read_b128 v[212:215], %[wl] offset:4096\n\t" \
    "ds_read_b128 v[216:219], %[wl] offset:5120\n\t" \
    "v_cvt_pk_bf16_f32 v227, v222, v223\n\t" \
    "v_min_u32 v226, -2, v226\n\t" \
    "v_min_u32 v227, -2, v227\n\t" \
    "v_smfmac_f32_16x16x64_bf16 v[240:243], v[204:207], v[104:111], %[sidx]\n\t" \
    "v_smfmac_f32_16x16x64_bf16 v[236:239], v[204:207], v[80:87], %[sidx]\n\t" \
    "v_smfmac_f32_16x16x64_bf16 v[240:243], v[208:211], v[112:119], %[sidx]\n\t" \
    "v_permlane32_swap_b32 v228, v232\n\t" \
    "v_permlane32_swap_b32 v229, v233\n\t" \
    "v_permlane32_swap_b32 v230, v234\n\t" \
    "v_smfmac_f32_16x16x64_bf16 v[236:239], v[208:211], v[88:95], %[sidx]\n\t" \
    "v_permlane32_swap_b32 v231, v235\n\t" \
    "v_add_f32 v228, v228, v232\n\t" \
    "v_add_f32 v229, v229, v233\n\t" \
    "v_smfmac_f32_16x16x64_bf16 v[240:243], v[200:203], v[96:103], %[sidx]\n\t" \
    "v_add_f32 v230, v230, v234\n\t" \
    "v_add_f32 v231, v231, v235\n\t" \
    "v_cvt_pk_bf16_f32 v228, v228, v229\n\t" \
    "s_waitcnt lgkmcnt(0)\n\t" \
    "v_smfmac_f32_16x16x64_bf16 v[236:239], v[200:203], v[212:219], %[sidx]\n\t" \
    "ds_read_b128 v[212:215], %[wl] offset:6144\n\t" \
    "ds_read_b128 v[216:219], %[wl] offset:7168\n\t" \
    "v_cvt_pk_bf16_f32 v229, v230, v231\n\t" \
    "v_min_u32 v228, -2, v228\n\t" \
    "v_min_u32 v229, -2, v229\n\t" \
    "s_nop 1\n\t" \
    "v_permlane16_swap_b32 v226, v228\n\t" \
    "v_permlane16_swap_b32 v227, v229\n\t" \
    "buffer_store_dwordx4 v[226:229], %[vo], %[rr], %[so] offen sc1\n\t" \
    "s_add_u32 %[so], %[so], 0x8000\n\t" \
    "v_smfmac_f32_16x16x64_bf16 v[248:251], v[204:207], v[144:151], %[sidx]\n\t" \
    "v_mov_b64 v[220:221], 0\n\t" \
    "v_mov_b64 v[222:223], 0\n\t" \
    "v_smfmac_f32_16x16x64_bf16 v[244:247], v[204:207], v[120:127], %[sidx]\n\t" \
    "v_mov_b64 v[224:225], 0\n\t" \
    "v_mov_b64 v[226:227], 0\n\t" \
    "v_smfmac_f32_16x16x64_bf16 v[248:251], v[208:211], v[152:159], %[sidx]\n\t" \
    "v_permlane32_swap_b32 v236, v240\n\t" \
    "v_permlane32_swap_b32 v237, v241\n\t" \
    "v_permlane32_swap_b32 v238, v242\n\t" \
    "v_smfmac_f32_16x16x64_bf16 v[244:247], v[208:211], v[128:135], %[sidx]\n\t" \
    "v_permlane32_swap_b32 v239, v243\n\t" \
    "v_add_f32 v236, v236, v240\n\t" \
    "v_add_f32 v237, v237, v241\n\t" \
    "v_smfmac_f32_16x16x64_bf16 v[248:251], v[200:203], v[136:143], %[sidx]\n\t" \
    "v_add_f32 v238, v238, v242\n\t" \
    "v_add_f32 v239, v239, v243\n\t" \
    "v_cvt_pk_bf16_f32 v242, v236, v237\n\t" \
    "s_waitcnt lgkmcnt(0)\n\t" \
    "v_smfmac_f32_16x16x64_bf16 v[244:247], v[200:203], v[212:219], %[sidx]\n\t" \
    "ds_read_b128 v[212:215], %[wl] offset:8192\n\t" \
    "ds_read_b128 v[216:219], %[wl] offset:9216\n\t" \
    "v_cvt_pk_bf16_f32 v243, v238, v239\n\t" \
    "v_min_u32 v242, -2, v242\n\t" \
    "v_min_u32 v243, -2, v243\n\t" \
    "v_smfmac_f32_16x16x64_bf16 v[224:227], v[204:207], v[184:191], %[sidx]\n\t" \
    "v_and_b32 v232, 0x3fff, %[vo]\n\t" \
    "v_lshrrev_b32 v233, 14, %[vo]\n\t" \
    "v_smfmac_f32_16x16x64_bf16 v[220:223], v[204:207], v[160:167], %[sidx]\n\t" \
    "v_lshl_add_u32 v232, v233, 3, v232\n\t" \
    "v_permlane32_swap_b32 v244, v248\n\t" \
    "v_smfmac_f32_16x16x64_bf16 v[224:227], v[208:211], v[192:199], %[sidx]\n\t" \
    "v_permlane32_swap_b32 v245, v249\n\t" \
    "v_permlane32_swap_b32 v246, v250\n\t" \
    "v_permlane32_swap_b32 v247, v251\n\t" \
    "v_smfmac_f32_16x16x64_bf16 v[220:223], v[208:211], v[168:175], %[sidx]\n\t" \
    "v_add_f32 v244, v244, v248\n\t" \
    "v_add_f32 v245, v245, v249\n\t" \
    "v_add_f32 v246, v246, v250\n\t" \
    "v_smfmac_f32_16x16x64_bf16 v[224:227], v[200:203], v[176:183], %[sidx]\n\t" \
    "v_add_f32 v247, v247, v251\n\t" \
    "v_cvt_pk_bf16_f32 v244, v244, v245\n\t" \
    "v_cvt_pk_bf16_f32 v245, v246, v247\n\t" \
    "s_waitcnt lgkmcnt(0)\n\t" \
    "v_smfmac_f32_16x16x64_bf16 v[220:223], v[200:203], v[212:219], %[sidx]\n\t" \
    "v_min_u32 v244, -2, v244\n\t" \
    "v_min_u32 v245, -2, v245\n\t" \
    "s_nop 1\n\t" \
    "v_permlane16_swap_b32 v242, v244\n\t" \
    "v_permlane16_swap_b32 v243, v245\n\t" \
    "buffer_store_dwordx4 v[242:245], %[vo], %[rr], %[so] offen sc1\n\t" \
    "s_add_u32 %[so], %[so], 0x8000\n\t" \
    "s_nop 0\n\t" \
    "v_permlane32_swap_b32 v220, v224\n\t" \
    "v_permlane32_swap_b32 v221, v225\n\t" \
    "v_permlane32_swap_b32 v222, v226\n\t" \
    "v_permlane32_swap_b32 v223, v227\n\t" \
    "v_add_f32 v220, v220, v224\n\t" \
    "v_add_f32 v221, v221, v225\n\t" \
    "v_add_f32 v222, v222, v226\n\t" \
    "v_add_f32 v223, v223, v227\n\t" \
    "v_cvt_pk_bf16_f32 v220, v220, v221\n\t" \
    "v_cvt_pk_bf16_f32 v221, v222, v223\n\t" \
    "v_min_u32 v220, -2, v220\n\t" \
    "v_min_u32 v221, -2, v221\n\t" \
    "buffer_store_dwordx2 v[220:221], v232, %[rr], %[so] offen sc1\n\t" \
    "s_barrier \n\t"
#define ASM_P4_0 \
    "ds_read_b128 v[200:203], %[qa] offset:0\n\t" \
    "ds_read_b128 v[204:207], %[qa] offset:1024\n\t" \
    "ds_read_b128 v[208:211], %[qa] offset:2048\n\t" \
    "ds_read_b128 v[212:215], %[qa] offset:3072\n\t" \
    "ds_read_b128 v[216:219], %[qa] offset:4096\n\t" \
    "ds_read_b128 v[220:223], %[qa] offset:5120\n\t" \
    "ds_read_b128 v[224:227], %[qa] offset:12288\n\t" \
    "ds_read_b128 v[228:231], %[qa] offset:13312\n\t" \
    "ds_read_b128 v[232:235], %[qa] offset:14336\n\t" \
    "ds_read_b128 v[236:239], %[qa] offset:15360\n\t" \
    "s_waitcnt lgkmcnt(0)\n\t" \
    "s_barrier\n\t" ASM_ST \
    "v_mfma_f32_16x16x32_bf16 v[240:243], v[224:227], v[200:203], v[240:243]\n\t" \
    "ds_read_b128 v[224:227], %[qa] offset:16384\n\t" \
    "v_mfma_f32_16x16x32_bf16 v[240:243], v[228:231], v[204:207], v[240:243]\n\t" \
    "ds_read_b128 v[228:231], %[qa] offset:17408\n\t" \
    "v_mfma_f32_16x16x32_bf16 v[240:243], v[232:235], v[208:211], v[240:243]\n\t" \
    "ds_read_b128 v[232:235], %[km] offset:0\n\t" \
    "v_mfma_f32_16x16x32_bf16 v[240:243], v[236:239], v[212:215], v[240:243]\n\t" \
    "ds_read_b128 v[236:239], %[km] offset:512\n\t" \
    "s_waitcnt lgkmcnt(3)\n\t" \
    "v_mfma_f32_16x16x32_bf16 v[240:243], v[224:227], v[216:219], v[240:243]\n\t" \
    "ds_read_b128 v[224:227], %[km] offset:1024\n\t" \
    "s_waitcnt lgkmcnt(3)\n\t" \
    "v_mfma_f32_16x16x32_bf16 v[240:243], v[228:231], v[220:223], v[240:243]\n\t" \
    "ds_read_b128 v[228:231], %[km] offset:1536\n\t" \
    "s_waitcnt lgkmcnt(3)\n\t" \
    "v_mfma_f32_16x16x32_bf16 v[248:251], v[232:235], v[200:203], 0\n\t" \
    "ds_read_b128 v[232:235], %[km] offset:2048\n\t" \
    "s_waitcnt lgkmcnt(3)\n\t" \
    "v_mfma_f32_16x16x32_bf16 v[248:251], v[236:239], v[204:207], v[248:251]\n\t" \
    "ds_read_b128 v[236:239], %[km] offset:2560\n\t" \
    "s_waitcnt lgkmcnt(3)\n\t" \
    "v_mfma_f32_16x16x32_bf16 v[248:251], v[224:227], v[208:211], v[248:251]\n\t" \
    "s_waitcnt lgkmcnt(2)\n\t" \
    "v_mfma_f32_16x16x32_bf16 v[248:251], v[228:231], v[212:215], v[248:251]\n\t" \
    "s_waitcnt lgkmcnt(1)\n\t" \
    "v_mfma_f32_16x16x32_bf16 v[248:251], v[232:235], v[216:219], v[248:251]\n\t" \
    "s_waitcnt lgkmcnt(0)\n\t" \
    "v_mfma_f32_16x16x32_bf16 v[248:251], v[236:239], v[220:223], v[248:251]\n\t" \
    "v_max3_f32 v224, v240, v241, v242\n\t" \
    "v_mov_b32 v225, v243\n\t" \
    "v_lshlrev_b32 v201, 23, %[qa]\n\t" \
    "v_and_b32 v201, 0x80000000, v201\n\t" \
    "v_and_b32 v200, 0x3ff, %[qa]\n\t" \
    "v_or_b32 v200, v201, v200\n\t" \
    "s_nop 1\n\t" \
    "v_max3_f32 v224, v224, v248, v249\n\t" \
    "v_max3_f32 v225, v225, v250, v251\n\t" \
    "v_max_f32 v224, v224, v225\n\t" \
    "v_mov_b32 v225, v224\n\t" \
    "s_nop 1\n\t" \
    "v_permlane16_swap_b32 v224, v225\n\t" \
    "v_max_f32 v224, v224, v225\n\t" \
    "v_mov_b32 v225, v224\n\t" \
    "s_nop 1\n\t" \
    "v_permlane32_swap_b32 v224, v225\n\t" \
    "v_max_f32 v224, v224, v225\n\t" \
    "v_mul_f32 v224, %[scl], v224\n\t" \
    "v_fma_f32 v240, v240, %[scl], -v224\n\t" \
    "v_fma_f32 v241, v241, %[scl], -v224\n\t" \
    "v_exp_f32 v240, v240\n\t" \
    "v_fma_f32 v242, v242, %[scl], -v224\n\t" \
    "v_exp_f32 v241, v241\n\t" \
    "v_fma_f32 v243, v243, %[scl], -v224\n\t" \
    "v_exp_f32 v242, v242\n\t" \
    "v_fma_f32 v248, v248, %[scl], -v224\n\t" \
    "v_exp_f32 v243, v243\n\t" \
    "v_fma_f32 v249, v249, %[scl], -v224\n\t" \
    "v_exp_f32 v248, v248\n\t" \
    "v_fma_f32 v250, v250, %[scl], -v224\n\t" \
    "v_exp_f32 v249, v249\n\t" \
    "v_fma_f32 v251, v251, %[scl], -v224\n\t" \
    "v_exp_f32 v250, v250\n\t" \
    "v_exp_f32 v251, v251\n\t" \
    "v_add_f32 v224, v240, v241\n\t" \
    "v_add_f32 v225, v242, v243\n\t" \
    "v_add_f32 v232, v248, v249\n\t" \
    "v_add_f32 v233, v250, v251\n\t" \
    "v_add_f32 v224, v224, v225\n\t" \
    "v_add_f32 v232, v232, v233\n\t" \
    "v_fma_f32 v224, v232, 0.5, v224\n\t" \
    "v_mov_b32 v225, v224\n\t" \
    "s_nop 1\n\t" \
    "v_permlane16_swap_b32 v224, v225\n\t" \
    "v_add_f32 v224, v224, v225\n\t" \
    "v_mov_b32 v225, v224\n\t" \
    "s_nop 1\n\t" \
    "v_permlane32_swap_b32 v224, v225\n\t" \
    "v_add_f32 v224, v224, v225\n\t" \
    "v_rcp_f32 v224, v224\n\t" \
    "s_nop 0\n\t" \
    "v_mul_f32 v240, v240, v224\n\t" \
    "v_mul_f32 v241, v241, v224\n\t" \
    "v_mul_f32 v242, v242, v224\n\t" \
    "v_mul_f32 v243, v243, v224\n\t" \
    "v_mul_f32 v248, v248, v224\n\t" \
    "v_mul_f32 v249, v249, v224\n\t" \
    "v_mul_f32 v250, v250, v224\n\t" \
    "v_mul_f32 v251, v251, v224\n\t" \
    "v_cvt_pk_bf16_f32 v228, |v240|, |v241|\n\t" \
    "v_cvt_pk_bf16_f32 v229, |v242|, |v243|\n\t" \
    "v_cvt_pk_bf16_f32 v232, |v248|, |v249|\n\t" \
    "v_cvt_pk_bf16_f32 v233, |v250|, |v251|\n\t" \
    "v_permlane16_swap_b32 v228, v230\n\t" \
    "v_permlane16_swap_b32 v229, v231\n\t" \
    "buffer_store_dwordx4 v[228:231], v200, %[rr], %[so] offen sc1\n\t" \
    "buffer_store_dwordx2 v[232:233], %[pm], %[rr], %[so] offen sc1\n\t" \
    "s_nop 1\n\t"
#define ASM_P4_1 \
    "ds_read_b128 v[200:203], %[qa] offset:0\n\t" \
    "ds_read_b128 v[204:207], %[qa] offset:1024\n\t" \
    "ds_read_b128 v[208:211], %[qa] offset:2048\n\t" \
    "ds_read_b128 v[212:215], %[qa] offset:3072\n\t" \
    "ds_read_b128 v[216:219], %[qa] offset:4096\n\t" \
    "ds_read_b128 v[220:223], %[qa] offset:5120\n\t" \
    "ds_read_b128 v[224:227], %[qa] offset:12288\n\t" \
    "ds_read_b128 v[228:231], %[qa] offset:13312\n\t" \
    "ds_read_b128 v[232:235], %[qa] offset:14336\n\t" \
    "ds_read_b128 v[236:239], %[qa] offset:15360\n\t" \
    "s_waitcnt lgkmcnt(0)\n\t" \
    "s_barrier\n\t" ASM_ST \
    "v_mfma_f32_16x16x32_bf16 v[240:243], v[224:227], v[200:203], v[240:243]\n\t" \
    "ds_read_b128 v[224:227], %[qa] offset:16384\n\t" \
    "v_mfma_f32_16x16x32_bf16 v[240:243], v[228:231], v[204:207], v[240:243]\n\t" \
    "ds_read_b128 v[228:231], %[qa] offset:17408\n\t" \
    "v_mfma_f32_16x16x32_bf16 v[240:243], v[232:235], v[208:211], v[240:243]\n\t" \
    "ds_read_b128 v[232:235], %[qa] offset:6144\n\t" \
    "v_mfma_f32_16x16x32_bf16 v[240:243], v[236:239], v[212:215], v[240:243]\n\t" \
    "ds_read_b128 v[236:239], %[qa] offset:7168\n\t" \
    "s_waitcnt lgkmcnt(3)\n\t" \
    "v_mfma_f32_16x16x32_bf16 v[240:243], v[224:227], v[216:219], v[240:243]\n\t" \
    "ds_read_b128 v[224:227], %[qa] offset:8192\n\t" \
    "s_waitcnt lgkmcnt(3)\n\t" \
    "v_mfma_f32_16x16x32_bf16 v[240:243], v[228:231], v[220:223], v[240:243]\n\t" \
    "ds_read_b128 v[228:231], %[qa] offset:9216\n\t" \
    "s_waitcnt lgkmcnt(3)\n\t" \
    "v_mfma_f32_16x16x32_bf16 v[244:247], v[232:235], v[200:203], 0\n\t" \
    "ds_read_b128 v[232:235], %[qa] offset:10240\n\t" \
    "s_waitcnt lgkmcnt(3)\n\t" \
    "v_mfma_f32_16x16x32_bf16 v[244:247], v[236:239], v[204:207], v[244:247]\n\t" \
    "ds_read_b128 v[236:239], %[qa] offset:11264\n\t" \
    "s_waitcnt lgkmcnt(3)\n\t" \
    "v_mfma_f32_16x16x32_bf16 v[244:247], v[224:227], v[208:211], v[244:247]\n\t" \
    "ds_read_b128 v[224:227], %[km] offset:0\n\t" \
    "s_waitcnt lgkmcnt(3)\n\t" \
    "v_mfma_f32_16x16x32_bf16 v[244:247], v[228:231], v[212:215], v[244:247]\n\t" \
    "ds_read_b128 v[228:231], %[km] offset:512\n\t" \
    "s_waitcnt lgkmcnt(3)\n\t" \
    "v_mfma_f32_16x16x32_bf16 v[244:247], v[232:235], v[216:219], v[244:247]\n\t" \
    "ds_read_b128 v[232:235], %[km] offset:1024\n\t" \
    "s_waitcnt lgkmcnt(3)\n\t" \
    "v_mfma_f32_16x16x32_bf16 v[244:247], v[236:239], v[220:223], v[244:247]\n\t" \
    "ds_read_b128 v[236:239], %[km] offset:1536\n\t" \
    "s_waitcnt lgkmcnt(3)\n\t" \
    "v_mfma_f32_16x16x32_bf16 v[248:251], v[224:227], v[200:203], 0\n\t" \
    "ds_read_b128 v[224:227], %[km] offset:2048\n\t" \
    "s_waitcnt lgkmcnt(3)\n\t" \
    "v_mfma_f32_16x16x32_bf16 v[248:251], v[228:231], v[204:207], v[248:251]\n\t" \
    "ds_read_b128 v[228:231], %[km] offset:2560\n\t" \
    "s_waitcnt lgkmcnt(3)\n\t" \
    "v_mfma_f32_16x16x32_bf16 v[248:251], v[232:235], v[208:211], v[248:251]\n\t" \
    "s_waitcnt lgkmcnt(2)\n\t" \
    "v_mfma_f32_16x16x32_bf16 v[248:251], v[236:239], v[212:215], v[248:251]\n\t" \
    "s_waitcnt lgkmcnt(1)\n\t" \
    "v_mfma_f32_16x16x32_bf16 v[248:251], v[224:227], v[216:219], v[248:251]\n\t" \
    "s_waitcnt lgkmcnt(0)\n\t" \
    "v_mfma_f32_16x16x32_bf16 v[248:251], v[228:231], v[220:223], v[248:251]\n\t" \
    "v_max3_f32 v224, v240, v241, v242\n\t" \
    "v_max3_f32 v225, v244, v245, v246\n\t" \
    "v_max3_f32 v224, v224, v243, v247\n\t" \
    "v_and_b32 v200, 0x3ff, %[qa]\n\t" \
    "s_nop 3\n\t" \
    "v_max3_f32 v224, v224, v248, v249\n\t" \
    "v_max3_f32 v225, v225, v250, v251\n\t" \
    "v_max_f32 v224, v224, v225\n\t" \
    "v_mov_b32 v225, v224\n\t" \
    "s_nop 1\n\t" \
    "v_permlane16_swap_b32 v224, v225\n\t" \
    "v_max_f32 v224, v224, v225\n\t" \
    "v_mov_b32 v225, v224\n\t" \
    "s_nop 1\n\t" \
    "v_permlane32_swap_b32 v224, v225\n\t" \
    "v_max_f32 v224, v224, v225\n\t" \
    "v_mul_f32 v224, %[scl], v224\n\t" \
    "v_fma_f32 v240, v240, %[scl], -v224\n\t" \
    "v_fma_f32 v241, v241, %[scl], -v224\n\t" \
    "v_exp_f32 v240, v240\n\t" \
    "v_fma_f32 v242, v242, %[scl], -v224\n\t" \
    "v_exp_f32 v241, v241\n\t" \
    "v_fma_f32 v243, v243, %[scl], -v224\n\t" \
    "v_exp_f32 v242, v242\n\t" \
    "v_fma_f32 v244, v244, %[scl], -v224\n\t" \
    "v_exp_f32 v243, v243\n\t" \
    "v_fma_f32 v245, v245, %[scl], -v224\n\t" \
    "v_exp_f32 v244, v244\n\t" \
    "v_fma_f32 v246, v246, %[scl], -v224\n\t" \
    "v_exp_f32 v245, v245\n\t" \
    "v_fma_f32 v247, v247, %[scl], -v224\n\t" \
    "v_exp_f32 v246, v246\n\t" \
    "v_fma_f32 v248, v248, %[scl], -v224\n\t" \
    "v_exp_f32 v247, v247\n\t" \
    "v_fma_f32 v249, v249, %[scl], -v224\n\t" \
    "v_exp_f32 v248, v248\n\t" \
    "v_fma_f32 v250, v250, %[scl], -v224\n\t" \
    "v_exp_f32 v249, v249\n\t" \
    "v_fma_f32 v251, v251, %[scl], -v224\n\t" \
    "v_exp_f32 v250, v250\n\t" \
    "v_exp_f32 v251, v251\n\t" \
    "v_add_f32 v224, v240, v241\n\t" \
    "v_add_f32 v225, v242, v243\n\t" \
    "v_add_f32 v226, v244, v245\n\t" \
    "v_add_f32 v227, v246, v247\n\t" \
    "v_add_f32 v232, v248, v249\n\t" \
    "v_add_f32 v233, v250, v251\n\t" \
    "v_add_f32 v224, v224, v225\n\t" \
    "v_add_f32 v226, v226, v227\n\t" \
    "v_add_f32 v232, v232, v233\n\t" \
    "v_add_f32 v224, v224, v226\n\t" \
    "v_fma_f32 v224, v232, 0.5, v224\n\t" \
    "v_mov_b32 v225, v224\n\t" \
    "s_nop 1\n\t" \
    "v_permlane16_swap_b32 v224, v225\n\t" \
    "v_add_f32 v224, v224, v225\n\t" \
    "v_mov_b32 v225, v224\n\t" \
    "s_nop 1\n\t" \
    "v_permlane32_swap_b32 v224, v225\n\t" \
    "v_add_f32 v224, v224, v225\n\t" \
    "v_rcp_f32 v224, v224\n\t" \
    "s_nop 0\n\t" \
    "v_mul_f32 v240, v240, v224\n\t" \
    "v_mul_f32 v241, v241, v224\n\t" \
    "v_mul_f32 v242, v242, v224\n\t" \
    "v_mul_f32 v243, v243, v224\n\t" \
    "v_mul_f32 v244, v244, v224\n\t" \
    "v_mul_f32 v245, v245, v224\n\t" \
    "v_mul_f32 v246, v246, v224\n\t" \
    "v_mul_f32 v247, v247, v224\n\t" \
    "v_mul_f32 v248, v248, v224\n\t" \
    "v_mul_f32 v249, v249, v224\n\t" \
    "v_mul_f32 v250, v250, v224\n\t" \
    "v_mul_f32 v251, v251, v224\n\t" \
    "v_cvt_pk_bf16_f32 v230, |v240|, |v241|\n\t" \
    "v_cvt_pk_bf16_f32 v231, |v242|, |v243|\n\t" \
    "v_cvt_pk_bf16_f32 v228, |v244|, |v245|\n\t" \
    "v_cvt_pk_bf16_f32 v229, |v246|, |v247|\n\t" \
    "v_cvt_pk_bf16_f32 v232, |v248|, |v249|\n\t" \
    "v_cvt_pk_bf16_f32 v233, |v250|, |v251|\n\t" \
    "buffer_store_dwordx4 v[228:231], v200, %[rr], %[so] offen sc1\n\t" \
    "buffer_store_dwordx2 v[232:233], %[pm], %[rr], %[so] offen sc1\n\t" \
    "s_nop 1\n\t"
#define ASM_RCRIT \
    "s_movk_i32 %[n], 0x400\n\t" \
    ".Lfa%=:\n\t" \
    "v_max_u32 v196, v180, v181\n\t" \
    "v_max3_u32 v196, v182, v183, v196\n\t" \
    "v_cmp_eq_u32 vcc, -1, v196\n\t" \
    "s_cbranch_vccz .Lha%=\n\t" \
    "buffer_load_dwordx4 v[180:183], %[ov], %[rs], %[sv] offen sc1\n\t" \
    "s_waitcnt vmcnt(12)\n\t" \
    "v_max_u32 v196, v180, v181\n\t" \
    "v_max3_u32 v196, v182, v183, v196\n\t" \
    "v_cmp_eq_u32 vcc, -1, v196\n\t" \
    "s_cbranch_vccz .Lha%=\n\t" \
    "s_nop 7\n\t" \
    "v_max_u32 v196, v180, v181\n\t" \
    "v_max3_u32 v196, v182, v183, v196\n\t" \
    "v_cmp_eq_u32 vcc, -1, v196\n\t" \
    "s_cbranch_vccz .Lha%=\n\t" \
    "s_nop 7\n\t" \
    "v_max_u32 v196, v180, v181\n\t" \
    "v_max3_u32 v196, v182, v183, v196\n\t" \
    "v_cmp_eq_u32 vcc, -1, v196\n\t" \
    "s_cbranch_vccz .Lha%=\n\t" \
    "s_nop 7\n\t" \
    "v_max_u32 v196, v180, v181\n\t" \
    "v_max3_u32 v196, v182, v183, v196\n\t" \
    "v_cmp_eq_u32 vcc, -1, v196\n\t" \
    "s_cbranch_vccz .Lha%=\n\t" \
    "s_nop 7\n\t" \
    "s_sub_u32 %[n], %[n], 1\n\t" \
    "s_cmp_lg_u32 %[n], 0\n\t" \
    "s_cbranch_scc1 .Lfa%=\n\t" \
    ".Lha%=:\n\t" \
    "s_cmp_eq_u32 %[n], 0\n\t" \
    "s_cbranch_scc1 .Lre%=\n\t" \
    ".Lfb%=:\n\t" \
    "s_mov_b32 %[a], 0\n\t" \
    "v_max_u32 v196, v184, v185\n\t" \
    "v_max3_u32 v196, v186, v187, v196\n\t" \
    "v_cmp_eq_u32 vcc, -1, v196\n\t" \
    "s_cbranch_vccz .Lgb0_%=\n\t" \
    "buffer_load_dwordx4 v[184:187], %[o0], %[rs], %[sp] offen sc1\n\t" \
    "s_mov_b32 %[a], 1\n\t" \
    ".Lgb0_%=:\n\t" \
    "v_max_u32 v196, v188, v189\n\t" \
    "v_max3_u32 v196, v190, v191, v196\n\t" \
    "v_cmp_eq_u32 vcc, -1, v196\n\t" \
    "s_cbranch_vccz .Lgb1_%=\n\t" \
    "buffer_load_dwordx4 v[188:191], %[o1], %[rs], %[sp] offen sc1\n\t" \
    "s_mov_b32 %[a], 1\n\t" \
    ".Lgb1_%=:\n\t" \
    "v_max_u32 v196, v192, v193\n\t" \
    "v_max3_u32 v196, v194, v195, v196\n\t" \
    "v_cmp_eq_u32 vcc, -1, v196\n\t" \
    "s_cbranch_vccz .Lgb2_%=\n\t" \
    "buffer_load_dwordx4 v[192:195], %[o2], %[rs], %[sp] offen sc1\n\t" \
    "s_mov_b32 %[a], 1\n\t" \
    ".Lgb2_%=:\n\t" \
    "s_cmp_eq_u32 %[a], 0\n\t" \
    "s_cbranch_scc1 .Lhb%=\n\t" \
    "s_waitcnt vmcnt(12)\n\t" \
    "v_max_u32 v196, v184, v185\n\t" \
    "v_max3_u32 v196, v186, v187, v196\n\t" \
    "v_max3_u32 v196, v188, v189, v196\n\t" \
    "v_max3_u32 v196, v190, v191, v196\n\t" \
    "v_max3_u32 v196, v192, v193, v196\n\t" \
    "v_max3_u32 v196, v194, v195, v196\n\t" \
    "v_cmp_eq_u32 vcc, -1, v196\n\t" \
    "s_cbranch_vccz .Lhb%=\n\t" \
    "s_nop 7\n\t" \
    "v_max_u32 v196, v184, v185\n\t" \
    "v_max3_u32 v196, v186, v187, v196\n\t" \
    "v_max3_u32 v196, v188, v189, v196\n\t" \
    "v_max3_u32 v196, v190, v191, v196\n\t" \
    "v_max3_u32 v196, v192, v193, v196\n\t" \
    "v_max3_u32 v196, v194, v195, v196\n\t" \
    "v_cmp_eq_u32 vcc, -1, v196\n\t" \
    "s_cbranch_vccz .Lhb%=\n\t" \
    "s_nop 7\n\t" \
    "v_max_u32 v196, v184, v185\n\t" \
    "v_max3_u32 v196, v186, v187, v196\n\t" \
    "v_max3_u32 v196, v188, v189, v196\n\t" \
    "v_max3_u32 v196, v190, v191, v196\n\t" \
    "v_max3_u32 v196, v192, v193, v196\n\t" \
    "v_max3_u32 v196, v194, v195, v196\n\t" \
    "v_cmp_eq_u32 vcc, -1, v196\n\t" \
    "s_cbranch_vccz .Lhb%=\n\t" \
    "s_nop 7\n\t" \
    "v_max_u32 v196, v184, v185\n\t" \
    "v_max3_u32 v196, v186, v187, v196\n\t" \
    "v_max3_u32 v196, v188, v189, v196\n\t" \
    "v_max3_u32 v196, v190, v191, v196\n\t" \
    "v_max3_u32 v196, v192, v193, v196\n\t" \
    "v_max3_u32 v196, v194, v195, v196\n\t" \
    "v_cmp_eq_u32 vcc, -1, v196\n\t" \
    "s_cbranch_vccz .Lhb%=\n\t" \
    "s_nop 7\n\t" \
    "v_max_u32 v196, v184, v185\n\t" \
    "v_max3_u32 v196, v186, v187, v196\n\t" \
    "v_max3_u32 v196, v188, v189, v196\n\t" \
    "v_max3_u32 v196, v190, v191, v196\n\t" \
    "v_max3_u32 v196, v192, v193, v196\n\t" \
    "v_max3_u32 v196, v194, v195, v196\n\t" \
    "v_cmp_eq_u32 vcc, -1, v196\n\t" \
    "s_cbranch_vccz .Lhb%=\n\t" \
    "s_nop 7\n\t" \
    "v_max_u32 v196, v184, v185\n\t" \
    "v_max3_u32 v196, v186, v187, v196\n\t" \
    "v_max3_u32 v196, v188, v189, v196\n\t" \
    "v_max3_u32 v196, v190, v191, v196\n\t" \
    "v_max3_u32 v196, v192, v193, v196\n\t" \
    "v_max3_u32 v196, v194, v195, v196\n\t" \
    "v_cmp_eq_u32 vcc, -1, v196\n\t" \
    "s_cbranch_vccz .Lhb%=\n\t" \
    "s_nop 7\n\t" \
    "v_max_u32 v196, v184, v185\n\t" \
    "v_max3_u32 v196, v186, v187, v196\n\t" \
    "v_max3_u32 v196, v188, v189, v196\n\t" \
    "v_max3_u32 v196, v190, v191, v196\n\t" \
    "v_max3_u32 v196, v192, v193, v196\n\t" \
    "v_max3_u32 v196, v194, v195, v196\n\t" \
    "v_cmp_eq_u32 vcc, -1, v196\n\t" \
    "s_cbranch_vccz .Lhb%=\n\t" \
    "s_nop 7\n\t" \
    "v_max_u32 v196, v184, v185\n\t" \
    "v_max3_u32 v196, v186, v187, v196\n\t" \
    "v_max3_u32 v196, v188, v189, v196\n\t" \
    "v_max3_u32 v196, v190, v191, v196\n\t" \
    "v_max3_u32 v196, v192, v193, v196\n\t" \
    "v_max3_u32 v196, v194, v195, v196\n\t" \
    "v_cmp_eq_u32 vcc, -1, v196\n\t" \
    "s_cbranch_vccz .Lhb%=\n\t" \
    "s_nop 7\n\t" \
    "s_sub_u32 %[n], %[n], 1\n\t" \
    "s_cmp_lg_u32 %[n], 0\n\t" \
    "s_cbranch_scc1 .Lfb%=\n\t" \
    "s_branch .Lre%=\n\t" \
    ".Lhb%=:\n\t" \
    ASM_ST2 \
    "v_mfma_f32_16x16x32_bf16 v[176:179], v[192:195], v[180:183], 0\n\t" \
    "v_smfmac_f32_16x16x64_bf16 v[176:179], v[184:187], v[160:167], %[sidx]\n\t" \
    "v_smfmac_f32_16x16x64_bf16 v[176:179], v[188:191], v[168:175], %[sidx]\n\t" \
    "s_cmp_eq_u32 %[pub], 0\n\t" \
    "s_nop 7\n\t" \
    "v_permlane32_swap_b32 v176, v178\n\t" \
    "v_permlane32_swap_b32 v177, v179\n\t" \
    "v_add_f32 v176, v176, v178\n\t" \
    "v_add_f32 v177, v177, v179\n\t" \
    "ds_write2_b32 %[rw], v176, v177 offset1:80\n\t" \
    "s_waitcnt lgkmcnt(0)\n\t" \
    "s_barrier\n\t" ASM_ST \
    "s_cbranch_scc1 .Lre%=\n\t" \
    "ds_read_b128 v[200:203], %[rd] offset:0\n\t" \
    "ds_read_b128 v[204:207], %[rd] offset:16\n\t" \
    "ds_read_b128 v[208:211], %[rd] offset:32\n\t" \
    "ds_read_b128 v[212:215], %[rd] offset:48\n\t" \
    "s_waitcnt lgkmcnt(2)\n\t" \
    "v_pk_add_f32 v[200:201], v[200:201], v[202:203]\n\t" \
    "v_pk_add_f32 v[204:205], v[204:205], v[206:207]\n\t" \
    "s_waitcnt lgkmcnt(0)\n\t" \
    "v_pk_add_f32 v[208:209], v[208:209], v[210:211]\n\t" \
    "v_pk_add_f32 v[212:213], v[212:213], v[214:215]\n\t" \
    "v_pk_add_f32 v[200:201], v[200:201], v[204:205]\n\t" \
    "v_pk_add_f32 v[208:209], v[208:209], v[212:213]\n\t" \
    "v_pk_add_f32 v[200:201], v[200:201], v[208:209]\n\t" \
    "v_cvt_pk_bf16_f32 v200, v200, v201\n\t" \
    "v_min_u32 v200, -2, v200\n\t" \
    "buffer_store_dword v200, %[oo], %[rs], %[so] offen sc1\n\t" \
    RPUB2 \
    ".Lre%=:\n\t"
#define ASM_RX \
    "s_cmp_eq_u32 %[fin], 0\n\t" \
    "s_cbranch_scc1 .Lx0%=\n\t" \
    "buffer_load_dwordx2 v[250:251], %[xo], %[rd], %[sx] offen sc1\n\t" \
    ".Lx0%=:\n\t" \
    "buffer_load_dwordx4 v[200:203], %[l16], %[rs], %[s0] offen offset:0 sc1\n\t" \
    "buffer_load_dwordx4 v[204:207], %[l16], %[rs], %[s0] offen offset:1024 sc1\n\t" \
    "buffer_load_dwordx4 v[208:211], %[l16], %[rs], %[s0] offen offset:2048 sc1\n\t" \
    "buffer_load_dwordx4 v[212:215], %[l16], %[rs], %[s0] offen offset:3072 sc1\n\t" \
    "buffer_load_dwordx4 v[216:219], %[l16], %[rs], %[s1] offen offset:0 sc1\n\t" \
    "buffer_load_dwordx4 v[220:223], %[l16], %[rs], %[s1] offen offset:1024 sc1\n\t" \
    "buffer_load_dwordx4 v[224:227], %[l16], %[rs], %[s1] offen offset:2048 sc1\n\t" \
    "buffer_load_dwordx4 v[228:231], %[l16], %[rs], %[s1] offen offset:3072 sc1\n\t" \
    "buffer_load_dword v248, %[fo], %[rf], %[fs] offen sc1\n\t" \
    "v_mov_b64 v[232:233], v[160:161]\n\t" \
    "v_mov_b64 v[234:235], v[164:165]\n\t" \
    "v_mov_b64 v[236:237], v[168:169]\n\t" \
    "v_mov_b64 v[238:239], v[172:173]\n\t" \
    "s_movk_i32 %[n], 0x200\n\t" \
    "s_nop 1\n\t" \
    "v_permlane16_swap_b32 v232, v234\n\t" \
    "v_permlane16_swap_b32 v233, v235\n\t" \
    "v_permlane16_swap_b32 v236, v238\n\t" \
    "v_permlane16_swap_b32 v237, v239\n\t" \
    "s_waitcnt vmcnt(8)\n\t" \
    "v_max_u32 v196, v200, v201\n\t" \
    "v_max3_u32 v196, v202, v203, v196\n\t" \
    "s_waitcnt vmcnt(7)\n\t" \
    "v_max3_u32 v196, v204, v205, v196\n\t" \
    "v_max3_u32 v196, v206, v207, v196\n\t" \
    "s_waitcnt vmcnt(6)\n\t" \
    "v_max3_u32 v196, v208, v209, v196\n\t" \
    "v_max3_u32 v196, v210, v211, v196\n\t" \
    "s_waitcnt vmcnt(5)\n\t" \
    "v_max3_u32 v196, v212, v213, v196\n\t" \
    "v_max3_u32 v196, v214, v215, v196\n\t" \
    "s_waitcnt vmcnt(4)\n\t" \
    "v_max3_u32 v196, v216, v217, v196\n\t" \
    "v_max3_u32 v196, v218, v219, v196\n\t" \
    "s_waitcnt vmcnt(3)\n\t" \
    "v_max3_u32 v196, v220, v221, v196\n\t" \
    "v_max3_u32 v196, v222, v223, v196\n\t" \
    "s_waitcnt vmcnt(2)\n\t" \
    "v_max3_u32 v196, v224, v225, v196\n\t" \
    "v_max3_u32 v196, v226, v227, v196\n\t" \
    "s_waitcnt vmcnt(1)\n\t" \
    "v_max3_u32 v196, v228, v229, v196\n\t" \
    "v_max3_u32 v196, v230, v231, v196\n\t" \
    "v_cmp_eq_u32 vcc, -1, v196\n\t" \
    "s_cbranch_vccz .Lxd%=\n\t" \
    ".Lxl%=:\n\t" \
    "s_mov_b32 %[a], 0\n\t" \
    "v_max_u32 v196, v200, v201\n\t" \
    "v_max3_u32 v196, v202, v203, v196\n\t" \
    "v_cmp_eq_u32 vcc, -1, v196\n\t" \
    "s_cbranch_vccz .Lxs0_%=\n\t" \
    "buffer_load_dwordx4 v[200:203], %[l16], %[rs], %[s0] offen offset:0 sc1\n\t" \
    "s_mov_b32 %[a], 1\n\t" \
    ".Lxs0_%=:\n\t" \
    "v_max_u32 v196, v204, v205\n\t" \
    "v_max3_u32 v196, v206, v207, v196\n\t" \
    "v_cmp_eq_u32 vcc, -1, v196\n\t" \
    "s_cbranch_vccz .Lxs1_%=\n\t" \
    "buffer_load_dwordx4 v[204:207], %[l16], %[rs], %[s0] offen offset:1024 sc1\n\t" \
    "s_mov_b32 %[a], 1\n\t" \
    ".Lxs1_%=:\n\t" \
    "v_max_u32 v196, v208, v209\n\t" \
    "v_max3_u32 v196, v210, v211, v196\n\t" \
    "v_cmp_eq_u32 vcc, -1, v196\n\t" \
    "s_cbranch_vccz .Lxs2_%=\n\t" \
    "buffer_load_dwordx4 v[208:211], %[l16], %[rs], %[s0] offen offset:2048 sc1\n\t" \
    "s_mov_b32 %[a], 1\n\t" \
    ".Lxs2_%=:\n\t" \
    "v_max_u32 v196, v212, v213\n\t" \
    "v_max3_u32 v196, v214, v215, v196\n\t" \
    "v_cmp_eq_u32 vcc, -1, v196\n\t" \
    "s_cbranch_vccz .Lxs3_%=\n\t" \
    "buffer_load_dwordx4 v[212:215], %[l16], %[rs], %[s0] offen offset:3072 sc1\n\t" \
    "s_mov_b32 %[a], 1\n\t" \
    ".Lxs3_%=:\n\t" \
    "v_max_u32 v196, v216, v217\n\t" \
    "v_max3_u32 v196, v218, v219, v196\n\t" \
    "v_cmp_eq_u32 vcc, -1, v196\n\t" \
    "s_cbranch_vccz .Lxs4_%=\n\t" \
    "buffer_load_dwordx4 v[216:219], %[l16], %[rs], %[s1] offen offset:0 sc1\n\t" \
    "s_mov_b32 %[a], 1\n\t" \
    ".Lxs4_%=:\n\t" \
    "v_max_u32 v196, v220, v221\n\t" \
    "v_max3_u32 v196, v222, v223, v196\n\t" \
    "v_cmp_eq_u32 vcc, -1, v196\n\t" \
    "s_cbranch_vccz .Lxs5_%=\n\t" \
    "buffer_load_dwordx4 v[220:223], %[l16], %[rs], %[s1] offen offset:1024 sc1\n\t" \
    "s_mov_b32 %[a], 1\n\t" \
    ".Lxs5_%=:\n\t" \
    "v_max_u32 v196, v224, v225\n\t" \
    "v_max3_u32 v196, v226, v227, v196\n\t" \
    "v_cmp_eq_u32 vcc, -1, v196\n\t" \
    "s_cbranch_vccz .Lxs6_%=\n\t" \
    "buffer_load_dwordx4 v[224:227], %[l16], %[rs], %[s1] offen offset:2048 sc1\n\t" \
    "s_mov_b32 %[a], 1\n\t" \
    ".Lxs6_%=:\n\t" \
    "v_max_u32 v196, v228, v229\n\t" \
    "v_max3_u32 v196, v230, v231, v196\n\t" \
    "v_cmp_eq_u32 vcc, -1, v196\n\t" \
    "s_cbranch_vccz .Lxs7_%=\n\t" \
    "buffer_load_dwordx4 v[228:231], %[l16], %[rs], %[s1] offen offset:3072 sc1\n\t" \
    "s_mov_b32 %[a], 1\n\t" \
    ".Lxs7_%=:\n\t" \
    "s_cmp_eq_u32 %[a], 0\n\t" \
    "s_cbranch_scc1 .Lxd%=\n\t" \
    "s_waitcnt vmcnt(0)\n\t" \
    "s_sub_u32 %[n], %[n], 1\n\t" \
    "s_cmp_lg_u32 %[n], 0\n\t" \
    "s_cbranch_scc1 .Lxl%=\n\t" \
    "s_branch .Lxe%=\n\t" \
    ".Lxd%=:\n\t" \
    ASM_ST2 \
    "v_mfma_f32_16x16x32_bf16 v[244:247], v[160:163], v[200:203], 0\n\t" \
    "v_mfma_f32_16x16x32_bf16 v[244:247], v[164:167], v[204:207], v[244:247]\n\t" \
    "v_mfma_f32_16x16x32_bf16 v[244:247], v[168:171], v[208:211], v[244:247]\n\t" \
    "v_mfma_f32_16x16x32_bf16 v[244:247], v[172:175], v[212:215], v[244:247]\n\t" \
    "v_mfma_f32_16x16x32_bf16 v[240:243], v[232:235], v[220:223], 0\n\t" \
    "v_mfma_f32_16x16x32_bf16 v[244:247], v[180:183], v[216:219], v[244:247]\n\t" \
    "v_mfma_f32_16x16x32_bf16 v[240:243], v[236:239], v[224:227], v[240:243]\n\t" \
    "v_mfma_f32_16x16x32_bf16 v[240:243], v[180:183], v[228:231], v[240:243]\n\t" \
    "s_waitcnt vmcnt(0)\n\t" \
    "v_cmp_ge_u32 vcc, %[tn], v248\n\t" \
    "s_mov_b32 %[a], 0\n\t" \
    "s_cbranch_vccnz .Lxv%=\n\t" \
    "buffer_load_dwordx4 v[160:163], %[l16], %[rd], %[sv] offen offset:0 sc1\n\t" \
    "buffer_load_dwordx4 v[164:167], %[l16], %[rd], %[sv] offen offset:1024 sc1\n\t" \
    "buffer_load_dwordx4 v[168:171], %[l16], %[rd], %[sv] offen offset:2048 sc1\n\t" \
    "buffer_load_dwordx4 v[172:175], %[l16], %[rd], %[sv] offen offset:3072 sc1\n\t" \
    "s_mov_b32 %[a], 1\n\t" \
    ".Lxv%=:\n\t" \
    "s_nop 7\n\t" \
    "ds_write_b128 %[pw], v[244:247] offset:8192\n\t" \
    "ds_write_b128 %[pw], v[240:243]\n\t" \
    "s_cmp_eq_u32 %[fin], 0\n\t" \
    "s_waitcnt lgkmcnt(0)\n\t" \
    "s_barrier\n\t" ASM_ST \
    "s_cbranch_scc1 .Lxe%=\n\t" \
    "ds_read_b128 v[200:203], %[pr] offset:0\n\t" \
    "ds_read_b128 v[204:207], %[pr] offset:1024\n\t" \
    "ds_read_b128 v[208:211], %[pr] offset:2048\n\t" \
    "ds_read_b128 v[212:215], %[pr] offset:3072\n\t" \
    "ds_read_b128 v[216:219], %[pr] offset:4096\n\t" \
    "ds_read_b128 v[220:223], %[pr] offset:5120\n\t" \
    "ds_read_b128 v[224:227], %[pr] offset:6144\n\t" \
    "ds_read_b128 v[228:231], %[pr] offset:7168\n\t" \
    "v_lshlrev_b32 v240, 16, v250\n\t" \
    "v_and_b32 v241, 0xffff0000, v250\n\t" \
    "v_lshlrev_b32 v242, 16, v251\n\t" \
    "v_and_b32 v243, 0xffff0000, v251\n\t" \
    "s_waitcnt lgkmcnt(6)\n\t" \
    "v_pk_add_f32 v[200:201], v[200:201], v[204:205]\n\t" \
    "v_pk_add_f32 v[202:203], v[202:203], v[206:207]\n\t" \
    "s_waitcnt lgkmcnt(4)\n\t" \
    "v_pk_add_f32 v[208:209], v[208:209], v[212:213]\n\t" \
    "v_pk_add_f32 v[210:211], v[210:211], v[214:215]\n\t" \
    "v_pk_add_f32 v[200:201], v[200:201], v[208:209]\n\t" \
    "v_pk_add_f32 v[202:203], v[202:203], v[210:211]\n\t" \
    "s_waitcnt lgkmcnt(2)\n\t" \
    "v_pk_add_f32 v[216:217], v[216:217], v[220:221]\n\t" \
    "v_pk_add_f32 v[218:219], v[218:219], v[222:223]\n\t" \
    "s_waitcnt lgkmcnt(0)\n\t" \
    "v_pk_add_f32 v[224:225], v[224:225], v[228:229]\n\t" \
    "v_pk_add_f32 v[226:227], v[226:227], v[230:231]\n\t" \
    "v_pk_add_f32 v[216:217], v[216:217], v[224:225]\n\t" \
    "v_pk_add_f32 v[218:219], v[218:219], v[226:227]\n\t" \
    "v_pk_add_f32 v[200:201], v[200:201], v[216:217]\n\t" \
    "v_pk_add_f32 v[202:203], v[202:203], v[218:219]\n\t" \
    "v_pk_add_f32 v[200:201], v[200:201], v[240:241]\n\t" \
    "v_pk_add_f32 v[202:203], v[202:203], v[242:243]\n\t" \
    "v_cvt_pk_bf16_f32 v200, v200, v201\n\t" \
    "v_cvt_pk_bf16_f32 v201, v202, v203\n\t" \
    "v_min_u32 v200, -2, v200\n\t" \
    "v_min_u32 v201, -2, v201\n\t" \
    "s_add_u32 %[sx], %[sx], 0x500000\n\t" \
    "buffer_store_dwordx2 v[200:201], %[xo], %[rd], %[sx] offen sc1\n\t" \
    ".Lxe%=:\n\t"
#define ASM_MPOLLV3 \
    "s_movk_i32 %[n], 0x400\n\t" \
    ".Lfm%=:\n\t" \
    "s_mov_b32 %[a], 0\n\t" \
    "v_max_u32 v252, v200, v201\n\t" \
    "v_max3_u32 v252, v202, v203, v252\n\t" \
    "v_cmp_eq_u32 vcc, -1, v252\n\t" \
    "s_cbranch_vccz .Lgm0_%=\n\t" \
    "buffer_load_dwordx4 v[200:203], %[l16], %[rs], %[s0] offen sc1\n\t" \
    "s_mov_b32 %[a], 1\n\t" \
    ".Lgm0_%=:\n\t" \
    "v_max_u32 v252, v204, v205\n\t" \
    "v_max3_u32 v252, v206, v207, v252\n\t" \
    "v_cmp_eq_u32 vcc, -1, v252\n\t" \
    "s_cbranch_vccz .Lgm1_%=\n\t" \
    "buffer_load_dwordx4 v[204:207], %[l16], %[rs], %[s1] offen sc1\n\t" \
    "s_mov_b32 %[a], 1\n\t" \
    ".Lgm1_%=:\n\t" \
    "v_max_u32 v252, v208, v209\n\t" \
    "v_max3_u32 v252, v210, v211, v252\n\t" \
    "v_cmp_eq_u32 vcc, -1, v252\n\t" \
    "s_cbranch_vccz .Lgm2_%=\n\t" \
    "buffer_load_dwordx4 v[208:211], %[l16], %[rs], %[s2] offen sc1\n\t" \
    "s_mov_b32 %[a], 1\n\t" \
    ".Lgm2_%=:\n\t" \
    "s_cmp_eq_u32 %[a], 0\n\t" \
    "s_cbranch_scc1 .Lhm%=\n\t" \
    "s_waitcnt vmcnt(12)\n\t" \
    "v_max_u32 v252, v200, v201\n\t" \
    "v_max3_u32 v252, v202, v203, v252\n\t" \
    "v_max3_u32 v252, v204, v205, v252\n\t" \
    "v_max3_u32 v252, v206, v207, v252\n\t" \
    "v_max3_u32 v252, v208, v209, v252\n\t" \
    "v_max3_u32 v252, v210, v211, v252\n\t" \
    "v_cmp_eq_u32 vcc, -1, v252\n\t" \
    "s_cbranch_vccz .Lhm%=\n\t" \
    "s_nop 7\n\t" \
    "v_max_u32 v252, v200, v201\n\t" \
    "v_max3_u32 v252, v202, v203, v252\n\t" \
    "v_max3_u32 v252, v204, v205, v252\n\t" \
    "v_max3_u32 v252, v206, v207, v252\n\t" \
    "v_max3_u32 v252, v208, v209, v252\n\t" \
    "v_max3_u32 v252, v210, v211, v252\n\t" \
    "v_cmp_eq_u32 vcc, -1, v252\n\t" \
    "s_cbranch_vccz .Lhm%=\n\t" \
    "s_nop 7\n\t" \
    "s_sub_u32 %[n], %[n], 1\n\t" \
    "s_cmp_lg_u32 %[n], 0\n\t" \
    "s_cbranch_scc1 .Lfm%=\n\t" \
    ".Lhm%=:\n\t"
#define ASM_MPOLLV2 \
    "s_movk_i32 %[n], 0x400\n\t" \
    ".Lfm%=:\n\t" \
    "s_mov_b32 %[a], 0\n\t" \
    "v_max_u32 v252, v200, v201\n\t" \
    "v_max3_u32 v252, v202, v203, v252\n\t" \
    "v_cmp_eq_u32 vcc, -1, v252\n\t" \
    "s_cbranch_vccz .Lgm0_%=\n\t" \
    "buffer_load_dwordx4 v[200:203], %[l16], %[rs], %[s0] offen sc1\n\t" \
    "s_mov_b32 %[a], 1\n\t" \
    ".Lgm0_%=:\n\t" \
    "v_max_u32 v252, v204, v205\n\t" \
    "v_max3_u32 v252, v206, v207, v252\n\t" \
    "v_cmp_eq_u32 vcc, -1, v252\n\t" \
    "s_cbranch_vccz .Lgm1_%=\n\t" \
    "buffer_load_dwordx4 v[204:207], %[l16], %[rs], %[s1] offen sc1\n\t" \
    "s_mov_b32 %[a], 1\n\t" \
    ".Lgm1_%=:\n\t" \
    "s_cmp_eq_u32 %[a], 0\n\t" \
    "s_cbranch_scc1 .Lhm%=\n\t" \
    "s_waitcnt vmcnt(12)\n\t" \
    "v_max_u32 v252, v200, v201\n\t" \
    "v_max3_u32 v252, v202, v203, v252\n\t" \
    "v_max3_u32 v252, v204, v205, v252\n\t" \
    "v_max3_u32 v252, v206, v207, v252\n\t" \
    "v_cmp_eq_u32 vcc, -1, v252\n\t" \
    "s_cbranch_vccz .Lhm%=\n\t" \
    "s_nop 7\n\t" \
    "v_max_u32 v252, v200, v201\n\t" \
    "v_max3_u32 v252, v202, v203, v252\n\t" \
    "v_max3_u32 v252, v204, v205, v252\n\t" \
    "v_max3_u32 v252, v206, v207, v252\n\t" \
    "v_cmp_eq_u32 vcc, -1, v252\n\t" \
    "s_cbranch_vccz .Lhm%=\n\t" \
    "s_nop 7\n\t" \
    "s_sub_u32 %[n], %[n], 1\n\t" \
    "s_cmp_lg_u32 %[n], 0\n\t" \
    "s_cbranch_scc1 .Lfm%=\n\t" \
    ".Lhm%=:\n\t"
#define ASM_W4_M \
    "buffer_load_dwordx4 v[184:187], %[l16], %[rs], %[s1] offen offset:2048 sc1\n\t" \
    "buffer_load_dwordx4 v[180:183], %[l16], %[rs], %[s1] offen offset:1024 sc1\n\t" \
    "buffer_load_dwordx4 v[176:179], %[l16], %[rs], %[s1] offen offset:0 sc1\n\t" \
    "buffer_load_dwordx4 v[172:175], %[l16], %[rs], %[s0] offen offset:3072 sc1\n\t" \
    "buffer_load_dwordx4 v[168:171], %[l16], %[rs], %[s0] offen offset:2048 sc1\n\t" \
    "buffer_load_dwordx4 v[164:167], %[l16], %[rs], %[s0] offen offset:1024 sc1\n\t" \
    "buffer_load_dwordx4 v[160:163], %[l16], %[rs], %[s0] offen offset:0 sc1\n\t" \
    "buffer_load_dwordx4 v[220:223], %[l16], %[rs], %[s3] offen offset:3072 sc1\n\t" \
    "v_mfma_f32_16x16x32_bf16 v[224:227], v[28:31], v[188:191], 0\n\t" \
    "v_mfma_f32_16x16x32_bf16 v[228:231], v[60:63], v[188:191], 0\n\t" \
    "v_mfma_f32_16x16x32_bf16 v[232:235], v[92:95], v[188:191], 0\n\t" \
    "v_mfma_f32_16x16x32_bf16 v[236:239], v[124:127], v[188:191], 0\n\t" \
    "v_mfma_f32_16x16x32_bf16 v[240:243], v[156:159], v[188:191], 0\n\t" \
    "s_waitcnt vmcnt(7)\n\t" \
    ".Lc06_%=:\n\t" \
    "v_max_u32 v244, v184, v185\n\t" \
    "v_max3_u32 v244, v186, v187, v244\n\t" \
    "v_cmp_eq_u32 vcc, -1, v244\n\t" \
    "s_cbranch_vccnz .Ls06_%=\n\t" \
    ".Lk06_%=:\n\t" \
    "v_mfma_f32_16x16x32_bf16 v[224:227], v[24:27], v[184:187], v[224:227]\n\t" \
    "v_mfma_f32_16x16x32_bf16 v[228:231], v[56:59], v[184:187], v[228:231]\n\t" \
    "v_mfma_f32_16x16x32_bf16 v[232:235], v[88:91], v[184:187], v[232:235]\n\t" \
    "v_mfma_f32_16x16x32_bf16 v[236:239], v[120:123], v[184:187], v[236:239]\n\t" \
    "v_mfma_f32_16x16x32_bf16 v[240:243], v[152:155], v[184:187], v[240:243]\n\t" \
    "s_waitcnt vmcnt(6)\n\t" \
    ".Lc05_%=:\n\t" \
    "v_max_u32 v244, v180, v181\n\t" \
    "v_max3_u32 v244, v182, v183, v244\n\t" \
    "v_cmp_eq_u32 vcc, -1, v244\n\t" \
    "s_cbranch_vccnz .Ls05_%=\n\t" \
    ".Lk05_%=:\n\t" \
    "v_mfma_f32_16x16x32_bf16 v[224:227], v[20:23], v[180:183], v[224:227]\n\t" \
    "v_mfma_f32_16x16x32_bf16 v[228:231], v[52:55], v[180:183], v[228:231]\n\t" \
    "v_mfma_f32_16x16x32_bf16 v[232:235], v[84:87], v[180:183], v[232:235]\n\t" \
    "v_mfma_f32_16x16x32_bf16 v[236:239], v[116:119], v[180:183], v[236:239]\n\t" \
    "v_mfma_f32_16x16x32_bf16 v[240:243], v[148:151], v[180:183], v[240:243]\n\t" \
    "s_waitcnt vmcnt(5)\n\t" \
    ".Lc04_%=:\n\t" \
    "v_max_u32 v244, v176, v177\n\t" \
    "v_max3_u32 v244, v178, v179, v244\n\t" \
    "v_cmp_eq_u32 vcc, -1, v244\n\t" \
    "s_cbranch_vccnz .Ls04_%=\n\t" \
    ".Lk04_%=:\n\t" \
    "v_mfma_f32_16x16x32_bf16 v[224:227], v[16:19], v[176:179], v[224:227]\n\t" \
    "v_mfma_f32_16x16x32_bf16 v[228:231], v[48:51], v[176:179], v[228:231]\n\t" \
    "v_mfma_f32_16x16x32_bf16 v[232:235], v[80:83], v[176:179], v[232:235]\n\t" \
    "v_mfma_f32_16x16x32_bf16 v[236:239], v[112:115], v[176:179], v[236:239]\n\t" \
    "v_mfma_f32_16x16x32_bf16 v[240:243], v[144:147], v[176:179], v[240:243]\n\t" \
    "s_waitcnt vmcnt(4)\n\t" \
    ".Lc03_%=:\n\t" \
    "v_max_u32 v244, v172, v173\n\t" \
    "v_max3_u32 v244, v174, v175, v244\n\t" \
    "v_cmp_eq_u32 vcc, -1, v244\n\t" \
    "s_cbranch_vccnz .Ls03_%=\n\t" \
    ".Lk03_%=:\n\t" \
    "v_mfma_f32_16x16x32_bf16 v[224:227], v[12:15], v[172:175], v[224:227]\n\t" \
    "v_mfma_f32_16x16x32_bf16 v[228:231], v[44:47], v[172:175], v[228:231]\n\t" \
    "v_mfma_f32_16x16x32_bf16 v[232:235], v[76:79], v[172:175], v[232:235]\n\t" \
    "v_mfma_f32_16x16x32_bf16 v[236:239], v[108:111], v[172:175], v[236:239]\n\t" \
    "v_mfma_f32_16x16x32_bf16 v[240:243], v[140:143], v[172:175], v[240:243]\n\t" \
    "s_waitcnt vmcnt(3)\n\t" \
    ".Lc02_%=:\n\t" \
    "v_max_u32 v244, v168, v169\n\t" \
    "v_max3_u32 v244, v170, v171, v244\n\t" \
    "v_cmp_eq_u32 vcc, -1, v244\n\t" \
    "s_cbranch_vccnz .Ls02_%=\n\t" \
    ".Lk02_%=:\n\t" \
    "v_mfma_f32_16x16x32_bf16 v[224:227], v[8:11], v[168:171], v[224:227]\n\t" \
    "v_mfma_f32_16x16x32_bf16 v[228:231], v[40:43], v[168:171], v[228:231]\n\t" \
    "v_mfma_f32_16x16x32_bf16 v[232:235], v[72:75], v[168:171], v[232:235]\n\t" \
    "v_mfma_f32_16x16x32_bf16 v[236:239], v[104:107], v[168:171], v[236:239]\n\t" \
    "v_mfma_f32_16x16x32_bf16 v[240:243], v[136:139], v[168:171], v[240:243]\n\t" \
    "s_waitcnt vmcnt(2)\n\t" \
    ".Lc01_%=:\n\t" \
    "v_max_u32 v244, v164, v165\n\t" \
    "v_max3_u32 v244, v166, v167, v244\n\t" \
    "v_cmp_eq_u32 vcc, -1, v244\n\t" \
    "s_cbranch_vccnz .Ls01_%=\n\t" \
    ".Lk01_%=:\n\t" \
    "v_mfma_f32_16x16x32_bf16 v[224:227], v[4:7], v[164:167], v[224:227]\n\t" \
    "v_mfma_f32_16x16x32_bf16 v[228:231], v[36:39], v[164:167], v[228:231]\n\t" \
    "v_mfma_f32_16x16x32_bf16 v[232:235], v[68:71], v[164:167], v[232:235]\n\t" \
    "v_mfma_f32_16x16x32_bf16 v[236:239], v[100:103], v[164:167], v[236:239]\n\t" \
    "v_mfma_f32_16x16x32_bf16 v[240:243], v[132:135], v[164:167], v[240:243]\n\t" \
    "s_waitcnt vmcnt(1)\n\t" \
    ".Lc00_%=:\n\t" \
    "v_max_u32 v244, v160, v161\n\t" \
    "v_max3_u32 v244, v162, v163, v244\n\t" \
    "v_cmp_eq_u32 vcc, -1, v244\n\t" \
    "s_cbranch_vccnz .Ls00_%=\n\t" \
    ".Lk00_%=:\n\t" \
    "v_mfma_f32_16x16x32_bf16 v[224:227], v[0:3], v[160:163], v[224:227]\n\t" \
    "v_mfma_f32_16x16x32_bf16 v[228:231], v[32:35], v[160:163], v[228:231]\n\t" \
    "v_mfma_f32_16x16x32_bf16 v[232:235], v[64:67], v[160:163], v[232:235]\n\t" \
    "v_mfma_f32_16x16x32_bf16 v[236:239], v[96:99], v[160:163], v[236:239]\n\t" \
    "v_mfma_f32_16x16x32_bf16 v[240:243], v[128:131], v[160:163], v[240:243]\n\t" \
    "s_waitcnt vmcnt(0)\n\t" \
    ".Lc17_%=:\n\t" \
    "v_max_u32 v244, v220, v221\n\t" \
    "v_max3_u32 v244, v222, v223, v244\n\t" \
    "v_cmp_eq_u32 vcc, -1, v244\n\t" \
    "s_cbranch_vccnz .Ls17_%=\n\t" \
    ".Lk17_%=:\n\t" \
    "buffer_load_dwordx4 v[216:219], %[l16], %[rs], %[s3] offen offset:2048 sc1\n\t" \
    "buffer_load_dwordx4 v[212:215], %[l16], %[rs], %[s3] offen offset:1024 sc1\n\t" \
    "buffer_load_dwordx4 v[208:211], %[l16], %[rs], %[s3] offen offset:0 sc1\n\t" \
    "buffer_load_dwordx4 v[204:207], %[l16], %[rs], %[s2] offen offset:3072 sc1\n\t" \
    "buffer_load_dwordx4 v[200:203], %[l16], %[rs], %[s2] offen offset:2048 sc1\n\t" \
    "buffer_load_dwordx4 v[196:199], %[l16], %[rs], %[s2] offen offset:1024 sc1\n\t" \
    "buffer_load_dwordx4 v[192:195], %[l16], %[rs], %[s2] offen offset:0 sc1\n\t" \
    "v_mfma_f32_16x16x32_bf16 v[160:163], v[28:31], v[220:223], 0\n\t" \
    "ds_write_b128 %[ld], v[224:227] offset:0\n\t" \
    "v_mfma_f32_16x16x32_bf16 v[164:167], v[60:63], v[220:223], 0\n\t" \
    "ds_write_b128 %[ld], v[228:231] offset:6144\n\t" \
    "v_mfma_f32_16x16x32_bf16 v[168:171], v[92:95], v[220:223], 0\n\t" \
    "ds_write_b128 %[ld], v[232:235] offset:12288\n\t" \
    "v_mfma_f32_16x16x32_bf16 v[172:175], v[124:127], v[220:223], 0\n\t" \
    "ds_write_b128 %[ld], v[236:239] offset:18432\n\t" \
    "v_mfma_f32_16x16x32_bf16 v[176:179], v[156:159], v[220:223], 0\n\t" \
    "ds_write_b128 %[ld], v[240:243] offset:24576\n\t" \
    "s_waitcnt vmcnt(6)\n\t" \
    ".Lc16_%=:\n\t" \
    "v_max_u32 v244, v216, v217\n\t" \
    "v_max3_u32 v244, v218, v219, v244\n\t" \
    "v_cmp_eq_u32 vcc, -1, v244\n\t" \
    "s_cbranch_vccnz .Ls16_%=\n\t" \
    ".Lk16_%=:\n\t" \
    "v_mfma_f32_16x16x32_bf16 v[160:163], v[24:27], v[216:219], v[160:163]\n\t" \
    "v_mfma_f32_16x16x32_bf16 v[164:167], v[56:59], v[216:219], v[164:167]\n\t" \
    "v_mfma_f32_16x16x32_bf16 v[168:171], v[88:91], v[216:219], v[168:171]\n\t" \
    "v_mfma_f32_16x16x32_bf16 v[172:175], v[120:123], v[216:219], v[172:175]\n\t" \
    "v_mfma_f32_16x16x32_bf16 v[176:179], v[152:155], v[216:219], v[176:179]\n\t" \
    "s_waitcnt vmcnt(5)\n\t" \
    ".Lc15_%=:\n\t" \
    "v_max_u32 v244, v212, v213\n\t" \
    "v_max3_u32 v244, v214, v215, v244\n\t" \
    "v_cmp_eq_u32 vcc, -1, v244\n\t" \
    "s_cbranch_vccnz .Ls15_%=\n\t" \
    ".Lk15_%=:\n\t" \
    "v_mfma_f32_16x16x32_bf16 v[160:163], v[20:23], v[212:215], v[160:163]\n\t" \
    "v_mfma_f32_16x16x32_bf16 v[164:167], v[52:55], v[212:215], v[164:167]\n\t" \
    "v_mfma_f32_16x16x32_bf16 v[168:171], v[84:87], v[212:215], v[168:171]\n\t" \
    "v_mfma_f32_16x16x32_bf16 v[172:175], v[116:119], v[212:215], v[172:175]\n\t" \
    "v_mfma_f32_16x16x32_bf16 v[176:179], v[148:151], v[212:215], v[176:179]\n\t" \
    "s_waitcnt vmcnt(4)\n\t" \
    ".Lc14_%=:\n\t" \
    "v_max_u32 v244, v208, v209\n\t" \
    "v_max3_u32 v244, v210, v211, v244\n\t" \
    "v_cmp_eq_u32 vcc, -1, v244\n\t" \
    "s_cbranch_vccnz .Ls14_%=\n\t" \
    ".Lk14_%=:\n\t" \
    "v_mfma_f32_16x16x32_bf16 v[160:163], v[16:19], v[208:211], v[160:163]\n\t" \
    "v_mfma_f32_16x16x32_bf16 v[164:167], v[48:51], v[208:211], v[164:167]\n\t" \
    "v_mfma_f32_16x16x32_bf16 v[168:171], v[80:83], v[208:211], v[168:171]\n\t" \
    "v_mfma_f32_16x16x32_bf16 v[172:175], v[112:115], v[208:211], v[172:175]\n\t" \
    "v_mfma_f32_16x16x32_bf16 v[176:179], v[144:147], v[208:211], v[176:179]\n\t" \
    "s_waitcnt vmcnt(3)\n\t" \
    ".Lc13_%=:\n\t" \
    "v_max_u32 v244, v204, v205\n\t" \
    "v_max3_u32 v244, v206, v207, v244\n\t" \
    "v_cmp_eq_u32 vcc, -1, v244\n\t" \
    "s_cbranch_vccnz .Ls13_%=\n\t" \
    ".Lk13_%=:\n\t" \
    "v_mfma_f32_16x16x32_bf16 v[160:163], v[12:15], v[204:207], v[160:163]\n\t" \
    "v_mfma_f32_16x16x32_bf16 v[164:167], v[44:47], v[204:207], v[164:167]\n\t" \
    "v_mfma_f32_16x16x32_bf16 v[168:171], v[76:79], v[204:207], v[168:171]\n\t" \
    "v_mfma_f32_16x16x32_bf16 v[172:175], v[108:111], v[204:207], v[172:175]\n\t" \
    "v_mfma_f32_16x16x32_bf16 v[176:179], v[140:143], v[204:207], v[176:179]\n\t" \
    "s_waitcnt vmcnt(2)\n\t" \
    ".Lc12_%=:\n\t" \
    "v_max_u32 v244, v200, v201\n\t" \
    "v_max3_u32 v244, v202, v203, v244\n\t" \
    "v_cmp_eq_u32 vcc, -1, v244\n\t" \
    "s_cbranch_vccnz .Ls12_%=\n\t" \
    ".Lk12_%=:\n\t" \
    "v_mfma_f32_16x16x32_bf16 v[160:163], v[8:11], v[200:203], v[160:163]\n\t" \
    "v_mfma_f32_16x16x32_bf16 v[164:167], v[40:43], v[200:203], v[164:167]\n\t" \
    "v_mfma_f32_16x16x32_bf16 v[168:171], v[72:75], v[200:203], v[168:171]\n\t" \
    "v_mfma_f32_16x16x32_bf16 v[172:175], v[104:107], v[200:203], v[172:175]\n\t" \
    "v_mfma_f32_16x16x32_bf16 v[176:179], v[136:139], v[200:203], v[176:179]\n\t" \
    "s_waitcnt vmcnt(1)\n\t" \
    ".Lc11_%=:\n\t" \
    "v_max_u32 v244, v196, v197\n\t" \
    "v_max3_u32 v244, v198, v199, v244\n\t" \
    "v_cmp_eq_u32 vcc, -1, v244\n\t" \
    "s_cbranch_vccnz .Ls11_%=\n\t" \
    ".Lk11_%=:\n\t" \
    "v_mfma_f32_16x16x32_bf16 v[160:163], v[4:7], v[196:199], v[160:163]\n\t" \
    "v_mfma_f32_16x16x32_bf16 v[164:167], v[36:39], v[196:199], v[164:167]\n\t" \
    "v_mfma_f32_16x16x32_bf16 v[168:171], v[68:71], v[196:199], v[168:171]\n\t" \
    "v_mfma_f32_16x16x32_bf16 v[172:175], v[100:103], v[196:199], v[172:175]\n\t" \
    "v_mfma_f32_16x16x32_bf16 v[176:179], v[132:135], v[196:199], v[176:179]\n\t" \
    "s_waitcnt vmcnt(0)\n\t" \
    ".Lc10_%=:\n\t" \
    "v_max_u32 v244, v192, v193\n\t" \
    "v_max3_u32 v244, v194, v195, v244\n\t" \
    "v_cmp_eq_u32 vcc, -1, v244\n\t" \
    "s_cbranch_vccnz .Ls10_%=\n\t" \
    ".Lk10_%=:\n\t" \
    "v_mfma_f32_16x16x32_bf16 v[160:163], v[0:3], v[192:195], v[160:163]\n\t" \
    "v_mfma_f32_16x16x32_bf16 v[164:167], v[32:35], v[192:195], v[164:167]\n\t" \
    "v_mfma_f32_16x16x32_bf16 v[168:171], v[64:67], v[192:195], v[168:171]\n\t" \
    "v_mfma_f32_16x16x32_bf16 v[172:175], v[96:99], v[192:195], v[172:175]\n\t" \
    "v_mfma_f32_16x16x32_bf16 v[176:179], v[128:131], v[192:195], v[176:179]\n\t" \
    "ds_write_b128 %[ld], v[160:163] offset:30720\n\t" \
    "ds_write_b128 %[ld], v[164:167] offset:36864\n\t" \
    "ds_write_b128 %[ld], v[168:171] offset:43008\n\t" \
    "s_nop 1\n\t" \
    "ds_write_b128 %[ld], v[172:175] offset:49152\n\t" \
    "s_nop 1\n\t" \
    "ds_write_b128 %[ld], v[176:179] offset:55296\n\t" \
    "s_branch .Lme%=\n\t" \
    ".Ls06_%=:\n\t" \
    "s_cmp_eq_u32 %[n], 0\n\t" \
    "s_cbranch_scc1 .Lk06_%=\n\t" \
    "s_sub_u32 %[n], %[n], 1\n\t" \
    "buffer_load_dwordx4 v[184:187], %[l16], %[rs], %[s1] offen offset:2048 sc1\n\t" \
    "s_waitcnt vmcnt(0)\n\t" \
    "s_branch .Lc06_%=\n\t" \
    ".Ls05_%=:\n\t" \
    "s_cmp_eq_u32 %[n], 0\n\t" \
    "s_cbranch_scc1 .Lk05_%=\n\t" \
    "s_sub_u32 %[n], %[n], 1\n\t" \
    "buffer_load_dwordx4 v[180:183], %[l16], %[rs], %[s1] offen offset:1024 sc1\n\t" \
    "s_waitcnt vmcnt(0)\n\t" \
    "s_branch .Lc05_%=\n\t" \
    ".Ls04_%=:\n\t" \
    "s_cmp_eq_u32 %[n], 0\n\t" \
    "s_cbranch_scc1 .Lk04_%=\n\t" \
    "s_sub_u32 %[n], %[n], 1\n\t" \
    "buffer_load_dwordx4 v[176:179], %[l16], %[rs], %[s1] offen offset:0 sc1\n\t" \
    "s_waitcnt vmcnt(0)\n\t" \
    "s_branch .Lc04_%=\n\t" \
    ".Ls03_%=:\n\t" \
    "s_cmp_eq_u32 %[n], 0\n\t" \
    "s_cbranch_scc1 .Lk03_%=\n\t" \
    "s_sub_u32 %[n], %[n], 1\n\t" \
    "buffer_load_dwordx4 v[172:175], %[l16], %[rs], %[s0] offen offset:3072 sc1\n\t" \
    "s_waitcnt vmcnt(0)\n\t" \
    "s_branch .Lc03_%=\n\t" \
    ".Ls02_%=:\n\t" \
    "s_cmp_eq_u32 %[n], 0\n\t" \
    "s_cbranch_scc1 .Lk02_%=\n\t" \
    "s_sub_u32 %[n], %[n], 1\n\t" \
    "buffer_load_dwordx4 v[168:171], %[l16], %[rs], %[s0] offen offset:2048 sc1\n\t" \
    "s_waitcnt vmcnt(0)\n\t" \
    "s_branch .Lc02_%=\n\t" \
    ".Ls01_%=:\n\t" \
    "s_cmp_eq_u32 %[n], 0\n\t" \
    "s_cbranch_scc1 .Lk01_%=\n\t" \
    "s_sub_u32 %[n], %[n], 1\n\t" \
    "buffer_load_dwordx4 v[164:167], %[l16], %[rs], %[s0] offen offset:1024 sc1\n\t" \
    "s_waitcnt vmcnt(0)\n\t" \
    "s_branch .Lc01_%=\n\t" \
    ".Ls00_%=:\n\t" \
    "s_cmp_eq_u32 %[n], 0\n\t" \
    "s_cbranch_scc1 .Lk00_%=\n\t" \
    "s_sub_u32 %[n], %[n], 1\n\t" \
    "buffer_load_dwordx4 v[160:163], %[l16], %[rs], %[s0] offen offset:0 sc1\n\t" \
    "s_waitcnt vmcnt(0)\n\t" \
    "s_branch .Lc00_%=\n\t" \
    ".Ls17_%=:\n\t" \
    "s_cmp_eq_u32 %[n], 0\n\t" \
    "s_cbranch_scc1 .Lk17_%=\n\t" \
    "s_sub_u32 %[n], %[n], 1\n\t" \
    "buffer_load_dwordx4 v[220:223], %[l16], %[rs], %[s3] offen offset:3072 sc1\n\t" \
    "s_waitcnt vmcnt(0)\n\t" \
    "s_branch .Lc17_%=\n\t" \
    ".Ls16_%=:\n\t" \
    "s_cmp_eq_u32 %[n], 0\n\t" \
    "s_cbranch_scc1 .Lk16_%=\n\t" \
    "s_sub_u32 %[n], %[n], 1\n\t" \
    "buffer_load_dwordx4 v[216:219], %[l16], %[rs], %[s3] offen offset:2048 sc1\n\t" \
    "s_waitcnt vmcnt(0)\n\t" \
    "s_branch .Lc16_%=\n\t" \
    ".Ls15_%=:\n\t" \
    "s_cmp_eq_u32 %[n], 0\n\t" \
    "s_cbranch_scc1 .Lk15_%=\n\t" \
    "s_sub_u32 %[n], %[n], 1\n\t" \
    "buffer_load_dwordx4 v[212:215], %[l16], %[rs], %[s3] offen offset:1024 sc1\n\t" \
    "s_waitcnt vmcnt(0)\n\t" \
    "s_branch .Lc15_%=\n\t" \
    ".Ls14_%=:\n\t" \
    "s_cmp_eq_u32 %[n], 0\n\t" \
    "s_cbranch_scc1 .Lk14_%=\n\t" \
    "s_sub_u32 %[n], %[n], 1\n\t" \
    "buffer_load_dwordx4 v[208:211], %[l16], %[rs], %[s3] offen offset:0 sc1\n\t" \
    "s_waitcnt vmcnt(0)\n\t" \
    "s_branch .Lc14_%=\n\t" \
    ".Ls13_%=:\n\t" \
    "s_cmp_eq_u32 %[n], 0\n\t" \
    "s_cbranch_scc1 .Lk13_%=\n\t" \
    "s_sub_u32 %[n], %[n], 1\n\t" \
    "buffer_load_dwordx4 v[204:207], %[l16], %[rs], %[s2] offen offset:3072 sc1\n\t" \
    "s_waitcnt vmcnt(0)\n\t" \
    "s_branch .Lc13_%=\n\t" \
    ".Ls12_%=:\n\t" \
    "s_cmp_eq_u32 %[n], 0\n\t" \
    "s_cbranch_scc1 .Lk12_%=\n\t" \
    "s_sub_u32 %[n], %[n], 1\n\t" \
    "buffer_load_dwordx4 v[200:203], %[l16], %[rs], %[s2] offen offset:2048 sc1\n\t" \
    "s_waitcnt vmcnt(0)\n\t" \
    "s_branch .Lc12_%=\n\t" \
    ".Ls11_%=:\n\t" \
    "s_cmp_eq_u32 %[n], 0\n\t" \
    "s_cbranch_scc1 .Lk11_%=\n\t" \
    "s_sub_u32 %[n], %[n], 1\n\t" \
    "buffer_load_dwordx4 v[196:199], %[l16], %[rs], %[s2] offen offset:1024 sc1\n\t" \
    "s_waitcnt vmcnt(0)\n\t" \
    "s_branch .Lc11_%=\n\t" \
    ".Ls10_%=:\n\t" \
    "s_cmp_eq_u32 %[n], 0\n\t" \
    "s_cbranch_scc1 .Lk10_%=\n\t" \
    "s_sub_u32 %[n], %[n], 1\n\t" \
    "buffer_load_dwordx4 v[192:195], %[l16], %[rs], %[s2] offen offset:0 sc1\n\t" \
    "s_waitcnt vmcnt(0)\n\t" \
    "s_branch .Lc10_%=\n\t" \
    ".Lme%=:\n\t"
#define ASM_POLL5_I \
    "buffer_load_dwordx4 v[232:235], %[l16], %[rs], %[s0] offen offset:0 sc1\n\t" \
    "buffer_load_dwordx4 v[236:239], %[l16], %[rs], %[s0] offen offset:1024 sc1\n\t" \
    "buffer_load_dwordx4 v[240:243], %[l16], %[rs], %[s0] offen offset:2048 sc1\n\t" \
    "buffer_load_dwordx4 v[244:247], %[l16], %[rs], %[s0] offen offset:3072 sc1\n\t" \
    "buffer_load_dwordx4 v[248:251], %[l16], %[rs], %[s1] offen offset:0 sc1\n\t"
#define ASM_POLL5_C \
    "s_movk_i32 %[n], 0x200\n\t" \
    "v_max_u32 v252, v232, v233\n\t" \
    "v_max3_u32 v252, v234, v235, v252\n\t" \
    "v_max3_u32 v252, v236, v237, v252\n\t" \
    "v_max3_u32 v252, v238, v239, v252\n\t" \
    "v_max3_u32 v252, v240, v241, v252\n\t" \
    "v_max3_u32 v252, v242, v243, v252\n\t" \
    "v_max3_u32 v252, v244, v245, v252\n\t" \
    "v_max3_u32 v252, v246, v247, v252\n\t" \
    "v_max3_u32 v252, v248, v249, v252\n\t" \
    "v_max3_u32 v252, v250, v251, v252\n\t" \
    "v_cmp_eq_u32 vcc, -1, v252\n\t" \
    "s_cbranch_vccz .Lhw%=\n\t" \
    ".Lfw%=:\n\t" \
    "s_mov_b32 %[a], 0\n\t" \
    "v_max_u32 v252, v232, v233\n\t" \
    "v_max3_u32 v252, v234, v235, v252\n\t" \
    "v_cmp_eq_u32 vcc, -1, v252\n\t" \
    "s_cbranch_vccz .Lgw0_%=\n\t" \
    "buffer_load_dwordx4 v[232:235], %[l16], %[rs], %[s0] offen offset:0 sc1\n\t" \
    "s_mov_b32 %[a], 1\n\t" \
    ".Lgw0_%=:\n\t" \
    "v_max_u32 v252, v236, v237\n\t" \
    "v_max3_u32 v252, v238, v239, v252\n\t" \
    "v_cmp_eq_u32 vcc, -1, v252\n\t" \
    "s_cbranch_vccz .Lgw1_%=\n\t" \
    "buffer_load_dwordx4 v[236:239], %[l16], %[rs], %[s0] offen offset:1024 sc1\n\t" \
    "s_mov_b32 %[a], 1\n\t" \
    ".Lgw1_%=:\n\t" \
    "v_max_u32 v252, v240, v241\n\t" \
    "v_max3_u32 v252, v242, v243, v252\n\t" \
    "v_cmp_eq_u32 vcc, -1, v252\n\t" \
    "s_cbranch_vccz .Lgw2_%=\n\t" \
    "buffer_load_dwordx4 v[240:243], %[l16], %[rs], %[s0] offen offset:2048 sc1\n\t" \
    "s_mov_b32 %[a], 1\n\t" \
    ".Lgw2_%=:\n\t" \
    "v_max_u32 v252, v244, v245\n\t" \
    "v_max3_u32 v252, v246, v247, v252\n\t" \
    "v_cmp_eq_u32 vcc, -1, v252\n\t" \
    "s_cbranch_vccz .Lgw3_%=\n\t" \
    "buffer_load_dwordx4 v[244:247], %[l16], %[rs], %[s0] offen offset:3072 sc1\n\t" \
    "s_mov_b32 %[a], 1\n\t" \
    ".Lgw3_%=:\n\t" \
    "v_max_u32 v252, v248, v249\n\t" \
    "v_max3_u32 v252, v250, v251, v252\n\t" \
    "v_cmp_eq_u32 vcc, -1, v252\n\t" \
    "s_cbranch_vccz .Lgw4_%=\n\t" \
    "buffer_load_dwordx4 v[248:251], %[l16], %[rs], %[s1] offen offset:0 sc1\n\t" \
    "s_mov_b32 %[a], 1\n\t" \
    ".Lgw4_%=:\n\t" \
    "s_cmp_eq_u32 %[a], 0\n\t" \
    "s_cbranch_scc1 .Lhw%=\n\t" \
    "s_waitcnt vmcnt(20)\n\t" \
    "v_max_u32 v252, v232, v233\n\t" \
    "v_max3_u32 v252, v234, v235, v252\n\t" \
    "v_max3_u32 v252, v236, v237, v252\n\t" \
    "v_max3_u32 v252, v238, v239, v252\n\t" \
    "v_max3_u32 v252, v240, v241, v252\n\t" \
    "v_max3_u32 v252, v242, v243, v252\n\t" \
    "v_max3_u32 v252, v244, v245, v252\n\t" \
    "v_max3_u32 v252, v246, v247, v252\n\t" \
    "v_max3_u32 v252, v248, v249, v252\n\t" \
    "v_max3_u32 v252, v250, v251, v252\n\t" \
    "v_cmp_eq_u32 vcc, -1, v252\n\t" \
    "s_cbranch_vccz .Lhw%=\n\t" \
    "s_sleep 1\n\t" \
    "v_max_u32 v252, v232, v233\n\t" \
    "v_max3_u32 v252, v234, v235, v252\n\t" \
    "v_max3_u32 v252, v236, v237, v252\n\t" \
    "v_max3_u32 v252, v238, v239, v252\n\t" \
    "v_max3_u32 v252, v240, v241, v252\n\t" \
    "v_max3_u32 v252, v242, v243, v252\n\t" \
    "v_max3_u32 v252, v244, v245, v252\n\t" \
    "v_max3_u32 v252, v246, v247, v252\n\t" \
    "v_max3_u32 v252, v248, v249, v252\n\t" \
    "v_max3_u32 v252, v250, v251, v252\n\t" \
    "v_cmp_eq_u32 vcc, -1, v252\n\t" \
    "s_cbranch_vccz .Lhw%=\n\t" \
    "s_sleep 1\n\t" \
    "v_max_u32 v252, v232, v233\n\t" \
    "v_max3_u32 v252, v234, v235, v252\n\t" \
    "v_max3_u32 v252, v236, v237, v252\n\t" \
    "v_max3_u32 v252, v238, v239, v252\n\t" \
    "v_max3_u32 v252, v240, v241, v252\n\t" \
    "v_max3_u32 v252, v242, v243, v252\n\t" \
    "v_max3_u32 v252, v244, v245, v252\n\t" \
    "v_max3_u32 v252, v246, v247, v252\n\t" \
    "v_max3_u32 v252, v248, v249, v252\n\t" \
    "v_max3_u32 v252, v250, v251, v252\n\t" \
    "v_cmp_eq_u32 vcc, -1, v252\n\t" \
    "s_cbranch_vccz .Lhw%=\n\t" \
    "s_sleep 1\n\t" \
    "v_max_u32 v252, v232, v233\n\t" \
    "v_max3_u32 v252, v234, v235, v252\n\t" \
    "v_max3_u32 v252, v236, v237, v252\n\t" \
    "v_max3_u32 v252, v238, v239, v252\n\t" \
    "v_max3_u32 v252, v240, v241, v252\n\t" \
    "v_max3_u32 v252, v242, v243, v252\n\t" \
    "v_max3_u32 v252, v244, v245, v252\n\t" \
    "v_max3_u32 v252, v246, v247, v252\n\t" \
    "v_max3_u32 v252, v248, v249, v252\n\t" \
    "v_max3_u32 v252, v250, v251, v252\n\t" \
    "v_cmp_eq_u32 vcc, -1, v252\n\t" \
    "s_cbranch_vccz .Lhw%=\n\t" \
    "s_sleep 1\n\t" \
    "v_max_u32 v252, v232, v233\n\t" \
    "v_max3_u32 v252, v234, v235, v252\n\t" \
    "v_max3_u32 v252, v236, v237, v252\n\t" \
    "v_max3_u32 v252, v238, v239, v252\n\t" \
    "v_max3_u32 v252, v240, v241, v252\n\t" \
    "v_max3_u32 v252, v242, v243, v252\n\t" \
    "v_max3_u32 v252, v244, v245, v252\n\t" \
    "v_max3_u32 v252, v246, v247, v252\n\t" \
    "v_max3_u32 v252, v248, v249, v252\n\t" \
    "v_max3_u32 v252, v250, v251, v252\n\t" \
    "v_cmp_eq_u32 vcc, -1, v252\n\t" \
    "s_cbranch_vccz .Lhw%=\n\t" \
    "s_sleep 1\n\t" \
    "v_max_u32 v252, v232, v233\n\t" \
    "v_max3_u32 v252, v234, v235, v252\n\t" \
    "v_max3_u32 v252, v236, v237, v252\n\t" \
    "v_max3_u32 v252, v238, v239, v252\n\t" \
    "v_max3_u32 v252, v240, v241, v252\n\t" \
    "v_max3_u32 v252, v242, v243, v252\n\t" \
    "v_max3_u32 v252, v244, v245, v252\n\t" \
    "v_max3_u32 v252, v246, v247, v252\n\t" \
    "v_max3_u32 v252, v248, v249, v252\n\t" \
    "v_max3_u32 v252, v250, v251, v252\n\t" \
    "v_cmp_eq_u32 vcc, -1, v252\n\t" \
    "s_cbranch_vccz .Lhw%=\n\t" \
    "s_sleep 1\n\t" \
    "s_sub_u32 %[n], %[n], 1\n\t" \
    "s_cmp_lg_u32 %[n], 0\n\t" \
    "s_cbranch_scc1 .Lfw%=\n\t" \
    ".Lhw%=:\n\t"
#define ASM_POLLS3 \
    "buffer_load_dwordx4 v[208:211], %[l16], %[rs], %[s0] offen offset:0 sc1\n\t" \
    "buffer_load_dwordx4 v[212:215], %[l16], %[rs], %[s1] offen offset:0 sc1\n\t" \
    "buffer_load_dwordx4 v[216:219], %[l16], %[rs], %[s2] offen offset:0 sc1\n\t" \
    "s_movk_i32 %[n], 0x200\n\t" \
    "s_waitcnt vmcnt(2)\n\t" \
    "v_max_u32 v240, v208, v209\n\t" \
    "v_max3_u32 v240, v210, v211, v240\n\t" \
    "s_waitcnt vmcnt(1)\n\t" \
    "v_max3_u32 v240, v212, v213, v240\n\t" \
    "v_max3_u32 v240, v214, v215, v240\n\t" \
    "s_waitcnt vmcnt(0)\n\t" \
    "v_max3_u32 v240, v216, v217, v240\n\t" \
    "v_max3_u32 v240, v218, v219, v240\n\t" \
    "v_cmp_eq_u32 vcc, -1, v240\n\t" \
    "s_cbranch_vccz .Lpd%=\n\t" \
    ".Lpl%=:\n\t" \
    "s_mov_b32 %[a], 0\n\t" \
    "v_max_u32 v240, v208, v209\n\t" \
    "v_max3_u32 v240, v210, v211, v240\n\t" \
    "v_cmp_eq_u32 vcc, -1, v240\n\t" \
    "s_cbranch_vccz .Lps0_%=\n\t" \
    "buffer_load_dwordx4 v[208:211], %[l16], %[rs], %[s0] offen offset:0 sc1\n\t" \
    "s_mov_b32 %[a], 1\n\t" \
    ".Lps0_%=:\n\t" \
    "v_max_u32 v240, v212, v213\n\t" \
    "v_max3_u32 v240, v214, v215, v240\n\t" \
    "v_cmp_eq_u32 vcc, -1, v240\n\t" \
    "s_cbranch_vccz .Lps1_%=\n\t" \
    "buffer_load_dwordx4 v[212:215], %[l16], %[rs], %[s1] offen offset:0 sc1\n\t" \
    "s_mov_b32 %[a], 1\n\t" \
    ".Lps1_%=:\n\t" \
    "v_max_u32 v240, v216, v217\n\t" \
    "v_max3_u32 v240, v218, v219, v240\n\t" \
    "v_cmp_eq_u32 vcc, -1, v240\n\t" \
    "s_cbranch_vccz .Lps2_%=\n\t" \
    "buffer_load_dwordx4 v[216:219], %[l16], %[rs], %[s2] offen offset:0 sc1\n\t" \
    "s_mov_b32 %[a], 1\n\t" \
    ".Lps2_%=:\n\t" \
    "s_cmp_eq_u32 %[a], 0\n\t" \
    "s_cbranch_scc1 .Lpd%=\n\t" \
    "s_waitcnt vmcnt(0)\n\t" \
    "s_sub_u32 %[n], %[n], 1\n\t" \
    "s_cmp_lg_u32 %[n], 0\n\t" \
    "s_cbranch_scc1 .Lpl%=\n\t" \
    ".Lpd%=:\n\t"
#define ASM_POLLS2 \
    "buffer_load_dwordx4 v[208:211], %[l16], %[rs], %[s0] offen offset:0 sc1\n\t" \
    "buffer_load_dwordx4 v[212:215], %[l16], %[rs], %[s1] offen offset:0 sc1\n\t" \
    "s_movk_i32 %[n], 0x200\n\t" \
    "s_waitcnt vmcnt(1)\n\t" \
    "v_max_u32 v240, v208, v209\n\t" \
    "v_max3_u32 v240, v210, v211, v240\n\t" \
    "s_waitcnt vmcnt(0)\n\t" \
    "v_max3_u32 v240, v212, v213, v240\n\t" \
    "v_max3_u32 v240, v214, v215, v240\n\t" \
    "v_cmp_eq_u32 vcc, -1, v240\n\t" \
    "s_cbranch_vccz .Lpd%=\n\t" \
    ".Lpl%=:\n\t" \
    "s_mov_b32 %[a], 0\n\t" \
    "v_max_u32 v240, v208, v209\n\t" \
    "v_max3_u32 v240, v210, v211, v240\n\t" \
    "v_cmp_eq_u32 vcc, -1, v240\n\t" \
    "s_cbranch_vccz .Lps0_%=\n\t" \
    "buffer_load_dwordx4 v[208:211], %[l16], %[rs], %[s0] offen offset:0 sc1\n\t" \
    "s_mov_b32 %[a], 1\n\t" \
    ".Lps0_%=:\n\t" \
    "v_max_u32 v240, v212, v213\n\t" \
    "v_max3_u32 v240, v214, v215, v240\n\t" \
    "v_cmp_eq_u32 vcc, -1, v240\n\t" \
    "s_cbranch_vccz .Lps1_%=\n\t" \
    "buffer_load_dwordx4 v[212:215], %[l16], %[rs], %[s1] offen offset:0 sc1\n\t" \
    "s_mov_b32 %[a], 1\n\t" \
    ".Lps1_%=:\n\t" \
    "s_cmp_eq_u32 %[a], 0\n\t" \
    "s_cbranch_scc1 .Lpd%=\n\t" \
    "s_waitcnt vmcnt(0)\n\t" \
    "s_sub_u32 %[n], %[n], 1\n\t" \
    "s_cmp_lg_u32 %[n], 0\n\t" \
    "s_cbranch_scc1 .Lpl%=\n\t" \
    ".Lpd%=:\n\t"
#define ASM_POLLV3 \
    "buffer_load_dwordx4 v[200:203], %[l16], %[rs], %[s0] offen offset:0 sc1\n\t" \
    "buffer_load_dwordx4 v[204:207], %[l16], %[rs], %[s1] offen offset:0 sc1\n\t" \
    "buffer_load_dwordx4 v[208:211], %[l16], %[rs], %[s2] offen offset:0 sc1\n\t" \
    "s_movk_i32 %[n], 0x200\n\t" \
    "s_waitcnt vmcnt(2)\n\t" \
    "v_max_u32 v252, v200, v201\n\t" \
    "v_max3_u32 v252, v202, v203, v252\n\t" \
    "s_waitcnt vmcnt(1)\n\t" \
    "v_max3_u32 v252, v204, v205, v252\n\t" \
    "v_max3_u32 v252, v206, v207, v252\n\t" \
    "s_waitcnt vmcnt(0)\n\t" \
    "v_max3_u32 v252, v208, v209, v252\n\t" \
    "v_max3_u32 v252, v210, v211, v252\n\t" \
    "v_cmp_eq_u32 vcc, -1, v252\n\t" \
    "s_cbranch_vccz .Lpd%=\n\t" \
    ".Lpl%=:\n\t" \
    "s_mov_b32 %[a], 0\n\t" \
    "v_max_u32 v252, v200, v201\n\t" \
    "v_max3_u32 v252, v202, v203, v252\n\t" \
    "v_cmp_eq_u32 vcc, -1, v252\n\t" \
    "s_cbranch_vccz .Lps0_%=\n\t" \
    "buffer_load_dwordx4 v[200:203], %[l16], %[rs], %[s0] offen offset:0 sc1\n\t" \
    "s_mov_b32 %[a], 1\n\t" \
    ".Lps0_%=:\n\t" \
    "v_max_u32 v252, v204, v205\n\t" \
    "v_max3_u32 v252, v206, v207, v252\n\t" \
    "v_cmp_eq_u32 vcc, -1, v252\n\t" \
    "s_cbranch_vccz .Lps1_%=\n\t" \
    "buffer_load_dwordx4 v[204:207], %[l16], %[rs], %[s1] offen offset:0 sc1\n\t" \
    "s_mov_b32 %[a], 1\n\t" \
    ".Lps1_%=:\n\t" \
    "v_max_u32 v252, v208, v209\n\t" \
    "v_max3_u32 v252, v210, v211, v252\n\t" \
    "v_cmp_eq_u32 vcc, -1, v252\n\t" \
    "s_cbranch_vccz .Lps2_%=\n\t" \
    "buffer_load_dwordx4 v[208:211], %[l16], %[rs], %[s2] offen offset:0 sc1\n\t" \
    "s_mov_b32 %[a], 1\n\t" \
    ".Lps2_%=:\n\t" \
    "s_cmp_eq_u32 %[a], 0\n\t" \
    "s_cbranch_scc1 .Lpd%=\n\t" \
    "s_waitcnt vmcnt(0)\n\t" \
    "s_sub_u32 %[n], %[n], 1\n\t" \
    "s_cmp_lg_u32 %[n], 0\n\t" \
    "s_cbranch_scc1 .Lpl%=\n\t" \
    ".Lpd%=:\n\t"
#define ASM_POLLV2 \
    "buffer_load_dwordx4 v[200:203], %[l16], %[rs], %[s0] offen offset:0 sc1\n\t" \
    "buffer_load_dwordx4 v[204:207], %[l16], %[rs], %[s1] offen offset:0 sc1\n\t" \
    "s_movk_i32 %[n], 0x200\n\t" \
    "s_waitcnt vmcnt(1)\n\t" \
    "v_max_u32 v252, v200, v201\n\t" \
    "v_max3_u32 v252, v202, v203, v252\n\t" \
    "s_waitcnt vmcnt(0)\n\t" \
    "v_max3_u32 v252, v204, v205, v252\n\t" \
    "v_max3_u32 v252, v206, v207, v252\n\t" \
    "v_cmp_eq_u32 vcc, -1, v252\n\t" \
    "s_cbranch_vccz .Lpd%=\n\t" \
    ".Lpl%=:\n\t" \
    "s_mov_b32 %[a], 0\n\t" \
    "v_max_u32 v252, v200, v201\n\t" \
    "v_max3_u32 v252, v202, v203, v252\n\t" \
    "v_cmp_eq_u32 vcc, -1, v252\n\t" \
    "s_cbranch_vccz .Lps0_%=\n\t" \
    "buffer_load_dwordx4 v[200:203], %[l16], %[rs], %[s0] offen offset:0 sc1\n\t" \
    "s_mov_b32 %[a], 1\n\t" \
    ".Lps0_%=:\n\t" \
    "v_max_u32 v252, v204, v205\n\t" \
    "v_max3_u32 v252, v206, v207, v252\n\t" \
    "v_cmp_eq_u32 vcc, -1, v252\n\t" \
    "s_cbranch_vccz .Lps1_%=\n\t" \
    "buffer_load_dwordx4 v[204:207], %[l16], %[rs], %[s1] offen offset:0 sc1\n\t" \
    "s_mov_b32 %[a], 1\n\t" \
    ".Lps1_%=:\n\t" \
    "s_cmp_eq_u32 %[a], 0\n\t" \
    "s_cbranch_scc1 .Lpd%=\n\t" \
    "s_waitcnt vmcnt(0)\n\t" \
    "s_sub_u32 %[n], %[n], 1\n\t" \
    "s_cmp_lg_u32 %[n], 0\n\t" \
    "s_cbranch_scc1 .Lpl%=\n\t" \
    ".Lpd%=:\n\t"
__device__ __forceinline__ void poll5_issue(rsrc_t rs, unsigned l16, unsigned base, u32x4 (&v)[5]) {
    asm volatile(ASM_POLL5_I : "+{v[232:235]}"(v[0]), "+{v[236:239]}"(v[1]), "+{v[240:243]}"(v[2]), "+{v[244:247]}"(v[3]), "+{v[248:251]}"(v[4]) : [l16] "v"(l16), [rs] "s"(rs), [s0] "s"(base), [s1] "s"(base + 4096) : "memory");
}
__device__ __forceinline__ void poll5(rsrc_t rs, unsigned l16, unsigned base, u32x4 (&v)[5], const Params& P) {
#pragma unroll 1
    for (int spins = 1023;; spins += 1024) {
        unsigned n, a;
        asm volatile(ASM_POLL5_C : "+{v[232:235]}"(v[0]), "+{v[236:239]}"(v[1]), "+{v[240:243]}"(v[2]), "+{v[244:247]}"(v[3]), "+{v[248:251]}"(v[4]), [n] "=&s"(n), [a] "=&s"(a) : [l16] "v"(l16), [rs] "s"(rs), [s0] "s"(base), [s1] "s"(base + 4096) : "memory", "vcc", "scc", "v252");
        if (n || spin_fail(spins, P)) break;
    }
}
using u32x8 = __attribute__((ext_vector_type(8))) unsigned;
using u32x16 = __attribute__((ext_vector_type(16))) unsigned;
using u32x32 = __attribute__((ext_vector_type(32))) unsigned;
struct WRegs { u32x32 w[6]; u32x8 t; };
#define WREGS(W) "{v[0:31]}"(W.w[0]), "{v[32:63]}"(W.w[1]), "{v[64:95]}"(W.w[2]), "{v[96:127]}"(W.w[3]), "{v[128:159]}"(W.w[4]), "{v[160:191]}"(W.w[5]), "{v[192:199]}"(W.t)
__device__ __forceinline__ u32x32 cat32(u32x8 a, u32x8 b, u32x8 c, u32x8 d) {
    const u32x16 lo = __builtin_shufflevector(a, b, 0, 1, 2, 3, 4, 5, 6, 7, 8, 9, 10, 11, 12, 13, 14, 15);
    const u32x16 hi = __builtin_shufflevector(c, d, 0, 1, 2, 3, 4, 5, 6, 7, 8, 9, 10, 11, 12, 13, 14, 15);
    return __builtin_shufflevector(lo, hi, 0, 1, 2, 3, 4, 5, 6, 7, 8, 9, 10, 11, 12, 13, 14, 15, 16, 17, 18, 19, 20, 21, 22, 23, 24, 25, 26, 27, 28, 29, 30, 31);
}
template <class F>
__device__ __forceinline__ void load_weights(WRegs& W, unsigned wl, F src) {
    u32x8 f[25];
#pragma unroll
    for (int i = 0; i < 10; ++i)
#pragma unroll
        for (int j = 0; j < 3; ++j) {
            const bf16_t* p = src(i, j);
            const u32x4 a = *(const u32x4*)p, b = *(const u32x4*)(p + 512);
            if ((i & 1) || j) f[wi(i, j)] = __builtin_shufflevector(a, b, 0, 1, 2, 3, 4, 5, 6, 7);
            else { lds_st<u32x4>(wl + (i >> 1) * 2048, a); lds_st<u32x4>(wl + (i >> 1) * 2048 + 1024, b); }
        }
#pragma unroll
    for (int k = 0; k < 6; ++k) {
        const u32x16 lo = __builtin_shufflevector(f[4 * k], f[4 * k + 1], 0, 1, 2, 3, 4, 5, 6, 7, 8, 9, 10, 11, 12, 13, 14, 15);
        const u32x16 hi = __builtin_shufflevector(f[4 * k + 2], f[4 * k + 3], 0, 1, 2, 3, 4, 5, 6, 7, 8, 9, 10, 11, 12, 13, 14, 15);
        W.w[k] = __builtin_shufflevector(lo, hi, 0, 1, 2, 3, 4, 5, 6, 7, 8, 9, 10, 11, 12, 13, 14, 15, 16, 17, 18, 19, 20, 21, 22, 23, 24, 25, 26, 27, 28, 29, 30, 31);
    }
    W.t = f[24];
    asm volatile("" : "+{v[0:31]}"(W.w[0]), "+{v[32:63]}"(W.w[1]), "+{v[64:95]}"(W.w[2]), "+{v[96:127]}"(W.w[3]), "+{v[128:159]}"(W.w[4]), "+{v[160:191]}"(W.w[5]), "+{v[192:199]}"(W.t));
}
struct C1Regs { u32x8 lw0, lw1, z1; u32x4 z0; };
struct XAcc { u32x8 a0; u32x16 a1; };
template <bool HI>
__device__ __forceinline__ void xgemm_a(XAcc& A, const WRegs& W, unsigned xa, unsigned wl) {
#define XGA_ARGS : "=&{v[200:207]}"(A.a0), "=&{v[208:223]}"(A.a1) : WREGS(W), [xa] "v"(xa), [wl] "v"(wl) : "memory", "v224", "v225", "v226", "v227", "v228", "v229", "v230", "v231", "v232", "v233", "v234", "v235", "v236", "v237", "v238", "v239", "v240", "v241", "v242", "v243", "v244", "v245", "v246", "v247", "v248", "v249", "v250", "v251", "v252"
    if (HI) asm volatile(ASM_XG_HI_A XGA_ARGS); else asm volatile(ASM_XG_LO_A XGA_ARGS);
}
template <int V>
__device__ __forceinline__ void xgemm_b(XAcc& A, C1Regs& C, const WRegs& W, unsigned xa, unsigned wl, unsigned qs) {
#define XGB_ARGS(K) : K "{v[200:207]}"(A.a0), K "{v[208:223]}"(A.a1), "=&{v[224:227]}"(C.z0), "=&{v[228:235]}"(C.lw1), "=&{v[244:251]}"(C.z1) \
                    : WREGS(W), [xa] "v"(xa), [wl] "v"(wl), [qs] "s"(qs) : "memory", "v236", "v237", "v238", "v239", "v240", "v241", "v242", "v243", "v252"
    if (V == 0) asm volatile(ASM_XG_LO_B XGB_ARGS("+"));
    if (V == 1) asm volatile(ASM_XG_HI_B XGB_ARGS("+"));
    if (V == 2) asm volatile(ASM_XG_LO_AB XGB_ARGS("=&"));
    C.lw0 = A.a0;
}
__device__ __forceinline__ unsigned c1_dst(int wave, bool ktype) {
    const int ln = lane_id(), l15 = ln & 15, hf = (wave >> 1) & 1;
    const int rb16 = ((ln >> 2) & 4) * 16 + (ln >> 5) * 32;
    return (ktype ? L_KM + (l15 >> 2) * 128 + hf * 8 + (l15 & 3) * 2
                  : L_Q + (l15 >> 3) * 256 + ((l15 >> 1) & 1) * 128 + ((l15 >> 2) & 1) * 8 + hf * 4 + (l15 & 1) * 2) + (wave & 1) * 1536 + rb16;
}
template <bool VR>
__device__ __forceinline__ void mem_stage(const Params& P, rsrc_t rr, unsigned sbase, int w, unsigned abase, u32x4 (&v)[3]) {
#define PM_IN [l16] "v"(abase), [rs] "s"(rr), [s0] "s"(sbase), [s1] "s"(sbase + 4096)
#pragma unroll 1
    for (int spins = 1023;; spins += 1024) {
        unsigned n, a;
        if (w < 2) {
            if (VR) asm volatile(ASM_POLLV3 : "=&{v[200:203]}"(v[0]), "=&{v[204:207]}"(v[1]), "=&{v[208:211]}"(v[2]), [n] "=&s"(n), [a] "=&s"(a) : PM_IN, [s2] "s"(sbase + 8192) : "memory", "vcc", "scc", "v252");
            else asm volatile(ASM_POLLS3 : "=&{v[208:211]}"(v[0]), "=&{v[212:215]}"(v[1]), "=&{v[216:219]}"(v[2]), [n] "=&s"(n), [a] "=&s"(a) : PM_IN, [s2] "s"(sbase + 8192) : "memory", "vcc", "scc", "v240");
            lds_st<u32x4>(abase + 8192, v[2]);
        } else {
            if (VR) asm volatile(ASM_POLLV2 : "=&{v[200:203]}"(v[0]), "=&{v[204:207]}"(v[1]), [n] "=&s"(n), [a] "=&s"(a) : PM_IN : "memory", "vcc", "scc", "v252");
            else asm volatile(ASM_POLLS2 : "=&{v[208:211]}"(v[0]), "=&{v[212:215]}"(v[1]), [n] "=&s"(n), [a] "=&s"(a) : PM_IN : "memory", "vcc", "scc", "v240");
        }
        lds_st<u32x4>(abase, v[0]);
        lds_st<u32x4>(abase + 4096, v[1]);
        if (__builtin_amdgcn_readfirstlane(n) || spin_fail(spins, P)) break;
    }
}
template <bool POLL>
__device__ __forceinline__ void crit1(const Params& P, const WRegs& W, C1Regs& C, rsrc_t rr, unsigned sbase, int wave, unsigned wl, unsigned dsto
) {
    unsigned abase;
    asm volatile("v_and_b32 %0, 0x3ff, %1\n\tv_or_b32 %0, %2, %0" : "=&v"(abase) : "v"(wl), "s"(wave * 1024));
    ASM_ST_DECL;
#define C1_OUT "+{v[224:227]}"(C.z0), "+{v[244:251]}"(C.z1), "+{v[200:207]}"(C.lw0), "+{v[228:235]}"(C.lw1)
#define C1_IN WREGS(W), [ab] "v"(abase), [wl] "v"(wl), [dst] "v"(dsto)
    if constexpr (POLL) {
        asm volatile("" :: "v"(dsto));
        SB();
        ST(1);
        u32x4 v[3];
        mem_stage<false>(P, rr, sbase, wave, abase, v);
        ST(2);
        asm volatile(ASM_CRIT1 : C1_OUT, "+{v[208:211]}"(v[0]) ASM_ST_OUT : C1_IN : "memory", "v212", "v213", "v214", "v215", "v216", "v217", "v218", "v219", "v220", "v221", "v222", "v223", "v236", "v237", "v238", "v239", "v240", "v241");
    } else {
        asm volatile(ASM_CRIT1_N : C1_OUT ASM_ST_OUT : C1_IN : "memory", "v208", "v209", "v210", "v211", "v212", "v213", "v214", "v215", "v216", "v217", "v218", "v219", "v220", "v221", "v222", "v223", "v236", "v237", "v238", "v239", "v240", "v241");
    }
    SB();
    ASM_ST_SAVE(3);
}
#define STARG

#define SRG 5
__device__ __forceinline__ void s_role(const Params& P, int wave, int L, int h, int cpy) {
    const rsrc_t rr = mkrsrc(P.ws + L * RG_L, RG_L);
    const char* xn = P.ws + DATA0 + L * D_L + D_XN;
    auto xdma = [&](int seg, int hb) {
        const unsigned l16 = lane_id() * 16;
        const int mt = (wave >> 1) & 1;
        const char* g = xn + seg * 40960 + mt * 20480;
        if (hb == 0) {
            dma4(g + (wave & 1) * 4096, l16, L_XA + mt * 16384 + (wave & 1) * 4096);
            dmaq<0>(g + 16384 + (wave & 1) * 1024, l16, L_XB + mt * 4096 + (wave & 1) * 1024);
        } else {
            dma4(g + 8192, l16, L_XA + mt * 16384 + 8192); dma4(g + 12288, l16, L_XA + mt * 16384 + 12288);
            dmaq<2>(g + 16384, l16, L_XB + mt * 4096); dmaq<3>(g + 16384, l16, L_XB + mt * 4096);
        }
    };
    auto xcheck = [&](int seg, int hb) {
        for (int spins = 0;; ++spins) {
            const int mt = (wave >> 1) & 1;
            const unsigned l16 = lane_id() * 16;
            const unsigned d = L_XA + mt * 16384 + (hb ? 8192 : (wave & 1) * 4096) + l16, d2 = L_XB + mt * 4096 + (hb ? 2048 : (wave & 1) * 1024) + l16;
            unsigned m = umax4(0u, lds_ld<u32x4>(d2));
            if (hb) m = umax4(m, lds_ld<u32x4>(d2 + 1024));
#pragma unroll
            for (int k = 0; k < (hb ? 8 : 4); ++k) m = umax4(m, lds_ld<u32x4>(d + k * 1024));
            if (__all(m != SENT)) break;
            __builtin_amdgcn_s_sleep(8);
            if (spin_fail(spins * 8 + 7, P)) break;
            xdma(seg, hb);
            wait_vm<0>();
        }
    };
    const float SCL = 0.07216878364870322f * 1.4426950408889634f;
    auto run = [&](auto Cc) {
    constexpr int CLS = decltype(Cc)::value;
    WRegs wf;
    {
        const int lane = lane_id();
        const bf16_t* wsrc = (const bf16_t*)(P.ws + WT_QKV) + ((size_t)((wave >> 2) * 384 + h * 12 + (wave & 3) * 3) * 20) * 512 + lane * 8;
        load_weights(wf, L_W + wave * 10240 + lane * 16, [&](int i, int j) { return wsrc + (size_t)(j * 20 + 2 * (i < 8 ? (wave ^ i) : i)) * 512; });
    }
    if (CLS != 3) { xdma(0, 0); wait_vm<0>(); if (L) xcheck(0, 0); }
    else if (wave == 4 || wave == 7) { xdma(0, 1); wait_vm<0>(); if (L) xcheck(0, 1); }
    bar();
    const unsigned qs = (CLS == 3 ? L_KX : L_QX) + (wave & 1) * 3072 + ((wave >> 1) & 1) * 8;
    unsigned xa, wl, dsto;
    if (CLS == 3) { xa = L_XA + wave * 2048 + lane_id() * 16; wl = L_W + wave * 10240 + lane_id() * 16; dsto = c1_dst(wave, true); }
    STDECL;
    for (int t = 0; t < NSEG; ++t) {
        const unsigned slot = t & (NSLOT - 1);
        C1Regs c1;
        {
            XAcc acc;
            if (CLS != 3) { const unsigned l16 = lane_id() * 16; xa = L_XA + wave * 2048 + l16; wl = L_W + wave * 10240 + l16; }
            xgemm_a<CLS == 3>(acc, wf, xa, wl);
            bar();
            ST(7);
            if (CLS != 3 && t + 1 < NSEG) xdma(t + 1, 0);
            xgemm_b<CLS == 3>(acc, c1, wf, xa, wl, qs);
            ST(6);
            if (CLS != 3) {
                if (L && t + 1 < NSEG) { wait_vm<0>(); xcheck(t + 1, 0); }
                dsto = c1_dst(wave, false);
            }
        }
        ST(0);
        crit1<CLS != 3>(P, wf, c1, rr, RG_XM + slot * XM_SLOT + cpy * XM_COPY, wave, wl, dsto STARG);
        if constexpr (CLS == 0) {
            int ln = lane_id();
            const int sidx = ((ln & 15) < 8) ? 0x4444 : 0xEEEE;
            const unsigned l16 = ln * 16;
            bf16x16 F0, F1, F2, F3;
            F0 = lds_frag(L_KX + l16, 1024); F1 = lds_frag(L_KX + 6144 + l16, 1024); F2 = lds_frag(L_KX + 2048 + l16, 1024);
            f32x4 sc[3];
#pragma unroll
            for (int j = 0; j < 3; ++j) sc[j] = (f32x4){0.f, 0.f, 0.f, 0.f};
            unsigned kmo = L_KM + (ln >> 4) * 128 + (ln & 7) * 16;
            asm volatile("s_waitcnt lgkmcnt(0)" : "+v"(kmo), "+{v[244:247]}"(sc[0]), "+{v[252:255]}"(sc[1]), "+{v[240:243]}"(sc[2]),
                         "+v"(F0), "+v"(F1), "+v"(F2) :: "memory");
            SB();
            __builtin_amdgcn_s_barrier();
            asm volatile("" ::: "memory");
            __builtin_amdgcn_s_setprio(3);
            bf16x8 qa0 = lds_ld<bf16x8>(L_Q + l16);
            bf16x8 qa1 = lds_ld<bf16x8>(L_Q + 1024 + l16);
            F3 = lds_frag(kmo, 512);
            SB();
            SMF(qa0, F0, sc[0]);
            SB();
            F0 = lds_frag(kmo + 1024, 512);
            SMF(qa0, F1, sc[1]);
            SB();
            F1 = lds_frag(L_KX + 6144 + 2048 + l16, 1024);
            SMF(qa1, F2, sc[0]);
            SB();
            F2 = lds_frag(L_KX + 4096 + l16, 1024);
            SMF(qa0, F3, sc[2]);
            SB();
            qa0 = lds_ld<bf16x8>(L_Q + 2048 + l16);
            F3 = lds_frag(L_KX + 6144 + 4096 + l16, 1024);
            SMF(qa1, F0, sc[2]);
            SB();
            F0 = lds_frag(kmo + 2048, 512);
            SMF(qa1, F1, sc[1]);
            SB();
            ln = lane_id();
            const int l15 = ln & 15;
            const int rb16 = ((ln >> 2) & 4) * 16 + (ln >> 5) * 32;
            unsigned pb = L_Q + rb16 + (l15 & 7) * 2;
            unsigned pbx = L_Q + 128 + (l15 >> 3) * 256 + ((l15 >> 1) & 1) * 128 + rb16 + ((l15 >> 2) & 1) * 8 + (l15 & 1) * 2;
            unsigned poff = (l16 + ((ln < 8) ? h * 128 : 4096 - 128 + h * 512)) | ((ln >= 40) ? 0x40000000u : 0u);
            asm volatile("" : "+v"(pb), "+v"(pbx), "+v"(poff));
            SB();
            SMF(qa0, F2, sc[0]);
            SB();
            SMF(qa0, F3, sc[1]);
            SB();
            SMF(qa0, F0, sc[2]);
            SB();
            f32x4 mz;
            asm volatile(
                "s_nop 5\n\t"
                "v_permlane32_swap_b32 v244, v246\n\t"
                "v_permlane32_swap_b32 v245, v247\n\t"
                "v_permlane32_swap_b32 v252, v254\n\t"
                "v_permlane32_swap_b32 v253, v255\n\t"
                "v_permlane32_swap_b32 v240, v242\n\t"
                "v_permlane32_swap_b32 v241, v243\n\t"
                "v_pk_add_f32 v[244:245], v[244:245], v[246:247]\n\t"
                "v_pk_add_f32 v[246:247], v[252:253], v[254:255]\n\t"
                "v_pk_add_f32 v[240:241], v[240:241], v[242:243]\n\t"
                "s_nop 0\n\t"
                "v_max3_f32 v248, v244, v246, v240\n\t"
                "v_max3_f32 v249, v245, v247, v241\n\t"
                "s_nop 0\n\t"
                "v_max_f32_dpp v248, v248, v248 quad_perm:[1,0,3,2] row_mask:0xf bank_mask:0xf\n\t"
                "v_max_f32_dpp v249, v249, v249 quad_perm:[1,0,3,2] row_mask:0xf bank_mask:0xf\n\t"
                "s_nop 0\n\t"
                "v_max_f32_dpp v248, v248, v248 quad_perm:[2,3,0,1] row_mask:0xf bank_mask:0xf\n\t"
                "v_max_f32_dpp v249, v249, v249 quad_perm:[2,3,0,1] row_mask:0xf bank_mask:0xf\n\t"
                "s_nop 0\n\t"
                "v_max_f32_dpp v248, v248, v248 row_half_mirror row_mask:0xf bank_mask:0xf\n\t"
                "v_max_f32_dpp v249, v249, v249 row_half_mirror row_mask:0xf bank_mask:0xf\n\t"
                "s_nop 0\n\t"
                "v_max_f32_dpp v248, v248, v248 row_mirror row_mask:0xf bank_mask:0xf\n\t"
                "v_max_f32_dpp v249, v249, v249 row_mirror row_mask:0xf bank_mask:0xf\n\t"
                "v_mul_f32 v248, %[scl], v248\n\t"
                "v_mul_f32 v249, %[scl], v249\n\t"
                "v_fma_f32 v244, v244, %[scl], -v248\n\t"
                "v_fma_f32 v245, v245, %[scl], -v249\n\t"
                "v_fma_f32 v246, v246, %[scl], -v248\n\t"
                "v_fma_f32 v247, v247, %[scl], -v249\n\t"
                "v_exp_f32 v244, v244\n\t"
                "v_exp_f32 v245, v245\n\t"
                "v_fma_f32 v240, v240, %[scl], -v248\n\t"
                "v_fma_f32 v241, v241, %[scl], -v249\n\t"
                "v_exp_f32 v246, v246\n\t"
                "v_exp_f32 v247, v247\n\t"
                "v_exp_f32 v240, v240\n\t"
                "v_exp_f32 v241, v241\n\t"
                "v_add_f32 v250, v244, v246\n\t"
                "v_add_f32 v251, v245, v247\n\t"
                "v_fma_f32 v250, v240, 0.5, v250\n\t"
                "v_fma_f32 v251, v241, 0.5, v251\n\t"
                "s_nop 0\n\t"
                "v_add_f32_dpp v250, v250, v250 quad_perm:[1,0,3,2] row_mask:0xf bank_mask:0xf\n\t"
                "v_add_f32_dpp v251, v251, v251 quad_perm:[1,0,3,2] row_mask:0xf bank_mask:0xf\n\t"
                "s_nop 0\n\t"
                "v_add_f32_dpp v250, v250, v250 quad_perm:[2,3,0,1] row_mask:0xf bank_mask:0xf\n\t"
                "v_add_f32_dpp v251, v251, v251 quad_perm:[2,3,0,1] row_mask:0xf bank_mask:0xf\n\t"
                "s_nop 0\n\t"
                "v_add_f32_dpp v250, v250, v250 row_half_mirror row_mask:0xf bank_mask:0xf\n\t"
                "v_add_f32_dpp v251, v251, v251 row_half_mirror row_mask:0xf bank_mask:0xf\n\t"
                "s_nop 0\n\t"
                "v_add_f32_dpp v250, v250, v250 row_mirror row_mask:0xf bank_mask:0xf\n\t"
                "v_add_f32_dpp v251, v251, v251 row_mirror row_mask:0xf bank_mask:0xf\n\t"
                "v_rcp_f32 v250, v250\n\t"
                "v_rcp_f32 v251, v251\n\t"
                "s_nop 0\n\t"
                "v_pk_mul_f32 v[244:245], v[244:245], v[250:251]\n\t"
                "v_pk_mul_f32 v[246:247], v[246:247], v[250:251]\n\t"
                "v_pk_mul_f32 v[240:241], v[240:241], v[250:251]\n\t"
                "v_cvt_pk_bf16_f32 v244, |v244|, |v245|\n\t"
                "v_cvt_pk_bf16_f32 v246, |v246|, |v247|\n\t"
                "v_cvt_pk_bf16_f32 v240, |v240|, |v241|\n\t"
                "ds_write_b16 %[pbx], v244\n\t"
                "ds_write_b16_d16_hi %[pbx], v244 offset:16\n\t"
                "ds_write_b16 %[pbx], v246 offset:4\n\t"
                "ds_write_b16_d16_hi %[pbx], v246 offset:20\n\t"
                "ds_write_b16 %[pb], v240\n\t"
                "ds_write_b16_d16_hi %[pb], v240 offset:16"
                : "+{v[240:243]}"(sc[2]), "+{v[244:247]}"(sc[0]), "=&{v[248:251]}"(mz), "+{v[252:255]}"(sc[1])
                : [pb] "v"(pb), [pbx] "v"(pbx), [scl] "s"(SCL) : "memory");
            asm volatile("s_waitcnt lgkmcnt(0)" ::: "memory");
            const u32x4 v = lds_ld<u32x4>(L_Q + l16);
#pragma unroll
            for (int c = 0; c < (int)NCP; ++c) __builtin_amdgcn_raw_buffer_store_b128(v, rr, poff, RG_XP + slot * XP_SLOT + c * XP_COPY, 16);
            asm volatile("s_nop 1" :: "v"(v) : "memory");
            __builtin_amdgcn_s_setprio(0);
            ST(5);
        } else if constexpr (CLS == 1 || CLS == 4) {
            const int ln = lane_id(), l15 = ln & 15, l4 = ln >> 4;
            constexpr int mt = (CLS == 4);
            const unsigned qa = L_QX + mt * 6144 + ln * 16, kmo = L_KM + l4 * 128 + (ln & 7) * 16;
            const unsigned xo = mt ? (h & 3) * 1024 : 5120 + ((h >> 1) & 1) * 1024 + (h & 1) * 256;
            const unsigned pm = l4 < 2 ? (mt ? 4096 : 7168) + (h & 3) * 256 - xo + l15 * 16 + l4 * 8 : OOB;
            const unsigned so = RG_PX + slot * PX_SLOT + (h >> 2) * 8192 + xo;
            f32x4 sd;
#pragma unroll
            for (int r = 0; r < 4; ++r) sd[r] = (l4 * 4 + r > l15) ? -3.0e38f : 0.f;
            ASM_ST_DECL;
#define P4_ARGS : "+{v[240:243]}"(sd) ASM_ST_OUT : WREGS(wf), [qa] "v"(qa), [km] "v"(kmo), [pm] "v"(pm), [rr] "s"(rr), [so] "s"(so), [scl] "s"(SCL) : "memory", "v200", "v201", "v202", "v203", "v204", "v205", "v206", "v207", "v208", "v209", "v210", "v211", "v212", "v213", "v214", "v215", "v216", "v217", "v218", "v219", "v220", "v221", "v222", "v223", "v224", "v225", "v226", "v227", "v228", "v229", "v230", "v231", "v232", "v233", "v234", "v235", "v236", "v237", "v238", "v239", "v244", "v245", "v246", "v247", "v248", "v249", "v250", "v251"
            if constexpr (mt) asm volatile(ASM_P4_1 P4_ARGS); else asm volatile(ASM_P4_0 P4_ARGS);
            ASM_ST_SAVE(4);
            ST(5);
        } else {
            bar();
            ST(4);
            const int ln = lane_id();
            const unsigned l16 = ln * 16;
            const int l15 = ln & 15;
            {
                if (CLS == 3 && (wave == 4 || wave == 7)) {
                    if (t + 1 < NSEG) xdma(t + 1, 1);
                    wait_vm<0>();
                    const u32x4 s4 = sent4();
                    if (wave == 4) {
                        const unsigned poff = (l16 + ((ln < 8) ? h * 128 : 4096 - 128 + h * 512)) | ((ln >= 40 || t < 1) ? 0x40000000u : 0u);
#pragma unroll
                        for (int c = 0; c < (int)NCP; ++c) st128(s4, rr, poff, RG_XP + ((t - 1) & (NSLOT - 1)) * XP_SLOT + c * XP_COPY);
                    } else {
                        const unsigned pbase = RG_PX + ((t - 2) & (NSLOT - 1)) * PX_SLOT + (h >> 2) * 8192;
                        const unsigned o2 = ((ln & 16) ? 5120 + ((h >> 1) & 1) * 1024 + (h & 1) * 256 + (ln >> 5) * 512 : 4096 + (h & 3) * 256 + (ln >> 5) * 3072) + l15 * 16;
                        if (t >= 2) { st128(s4, rr, l16, pbase + (h & 3) * 1024); st128(s4, rr, o2, pbase); }
                    }
                    if (L && t + 1 < NSEG) xcheck(t + 1, 1);
                }
                ST(5);
            }
        }
        STFLUSH(t);
    }
    };
    if (wave == 0) run(std::integral_constant<int, 0>{});
    else if (wave == 1) run(std::integral_constant<int, 1>{});
    else if (wave == 2) run(std::integral_constant<int, 4>{});
    else if (wave == 3) run(std::integral_constant<int, 2>{});
    else run(std::integral_constant<int, 3>{});
}

__device__ __forceinline__ void v_role(const Params& P, int wave, int L, int h, int cpy) {
    const rsrc_t rr = mkrsrc(P.ws + L * RG_L, RG_L);
    WRegs wf;
    STDECL;
    if (wave < 4) {
        const char* xn = P.ws + DATA0 + L * D_L + D_XN;
        auto xdma = [&](int seg) {
            const unsigned l16 = lane_id() * 16;
            const char* g = xn + seg * 40960;
            dma4(g + wave * 4096, l16, L_XA + wave * 4096);
            dma4(g + 20480 + wave * 4096, l16, L_XA + 16384 + wave * 4096);
            dmaq<0>(g + 16384 + wave * 1024, l16, L_XB + wave * 1024);
            dmaq<0>(g + 20480 + 16384 + wave * 1024, l16, L_XB + 4096 + wave * 1024);
        };
        {
            const int lane = lane_id();
            const bf16_t* wsrc = (const bf16_t*)(P.ws + WT_QKV) + ((size_t)(768 + h * 12 + wave * 3) * 20) * 512 + lane * 8;
            load_weights(wf, L_W + wave * 10240 + lane * 16, [&](int i, int j) { return wsrc + (size_t)(j * 20 + 2 * (i < 8 ? (wave ^ i) : i)) * 512; });
        }
        auto xstage = [&](int seg) {
            for (int spins = 0;; ++spins) {
                xdma(seg);
                wait_vm<0>();
                if (!L) break;
                const unsigned l16 = lane_id() * 16, d = L_XA + wave * 4096 + l16;
                unsigned m = umax4(umax4(0u, lds_ld<u32x4>(L_XB + wave * 1024 + l16)), lds_ld<u32x4>(L_XB + 4096 + wave * 1024 + l16));
#pragma unroll
                for (int k = 0; k < 4; ++k) { m = umax4(m, lds_ld<u32x4>(d + k * 1024)); m = umax4(m, lds_ld<u32x4>(d + 16384 + k * 1024)); }
                if (__all(m != SENT)) break;
                __builtin_amdgcn_s_sleep(8);
                if (spin_fail(spins * 8 + 7, P)) break;
            }
        };
        const unsigned xa = L_XA + wave * 2048 + lane_id() * 16, wl = L_W + wave * 10240 + lane_id() * 16, dsto = c1_dst(wave, false);
        const unsigned qs = L_QX + (wave & 1) * 3072 + ((wave >> 1) & 1) * 8;
        for (int t = -2; t < NSEG - 1; ++t) {
            if (t + 2 < NSEG) xstage(t + 2);
            ST(7);
            XAcc acc;
            C1Regs c1;
            xgemm_b<2>(acc, c1, wf, xa, wl, qs + (t & 1) * 12288);
            ST(6);
            if (t >= -1) {
                crit1<false>(P, wf, c1, rr, RG_XM + ((t + 1) & (NSLOT - 1)) * XM_SLOT + cpy * XM_COPY, wave, wl, dsto STARG);
                bar();
                ST(4);
                STFLUSH(t + 1);
            }
            bar();
        }
    } else {
        const int w4 = wave - 4;
        const rsrc_t rd = mkrsrc(P.ws + DATA0 + L * D_L + D_VX, VXD * VX_SLOT);
        {
            const int lane = lane_id();
            const bf16_t* wsrc = (const bf16_t*)(P.ws + WT_O) + ((size_t)(h * 40 + w4 * 10) * 6) * 512 + lane * 8;
            load_weights(wf, L_W + wave * 10240 + lane * 16, [&](int i, int j) { return wsrc + (size_t)(i * 6 + 2 * j) * 512; });
        }
        bar();
        bar();
        for (int t = 0; t < NSEG; ++t) {
            const unsigned slot = t & (NSLOT - 1);
            ST(7);
            {
                const unsigned l16 = lane_id() * 16;
                const unsigned wl = L_W + wave * 10240 + l16, va = L_QX + (t & 1) * 12288 + l16;
                unsigned so = (t & (VXD - 1)) * VX_SLOT + (unsigned)(w4 * 10 * 32 + h) * 1024;
                asm volatile(ASM_VOX : [so] "+s"(so) : WREGS(wf), [l16] "v"(l16), [wl] "v"(wl), [va] "v"(va), [rd] "s"(rd) : "memory", "scc", "v200", "v201", "v202", "v203", "v204", "v205", "v206", "v207", "v208", "v209", "v210", "v211", "v212", "v213", "v214", "v215", "v216", "v217", "v218", "v219", "v220", "v221", "v222", "v223", "v224", "v225", "v226", "v227", "v228", "v229", "v230", "v231", "v232", "v233", "v234", "v235", "v236", "v237", "v238", "v239", "v240", "v241", "v242", "v243", "v244", "v245", "v246", "v247", "v248", "v249", "v250", "v251");
            }
            ST(0);
            unsigned wl = L_W + wave * 10240 + lane_id() * 16;
            bf16x16 lw = lds_frag(wl, 1024);
            u32x32 cz = {};
            unsigned vo = ((wl >> 8) & 1) * 16384 + ((wl >> 9) & 1) * 8192 + (wl & 255);
            asm volatile("" : "+{v[212:219]}"(lw), "+{v[220:251]}"(cz), "+v"(wl), "+v"(vo));
            wait_vm<0>();
            {
                const unsigned l16 = lane_id() * 16;
                if (l16 < 512) __builtin_amdgcn_raw_buffer_store_b32(t + 1, mkrsrc(P.ws + FL0, FL_BYTES), l16 >> 2, FL_VO + (L * 128 + w4 * 32 + h) * 128, 16);
                ST(1);
                u32x4 v[3];
                mem_stage<true>(P, rr, RG_XM + slot * XM_SLOT + cpy * XM_COPY, w4, L_MEM + w4 * 1024 + l16, v);
            }
            ST(2);
            bar();
            ST(3);
            unsigned l16 = wl & 1023u;
            const int sidx = (wl & 128u) ? 0xEEEE : 0x4444;
            unsigned so = RG_VM + slot * VM_SLOT + (unsigned)((w4 * 10 * 8 + (h >> 2)) * 1024 + (h & 3) * 256);
            ASM_ST_DECL;
            asm volatile(ASM_CRIT2 : [so] "+s"(so), "+{v[212:219]}"(lw), "+{v200}"(l16), "+{v[220:251]}"(cz) ASM_ST_OUT
                         : WREGS(wf), [wl] "v"(wl), [vo] "v"(vo), [sidx] "v"(sidx), [rr] "s"(rr)
                         : "memory", "scc", "v201", "v202", "v203", "v204", "v205", "v206", "v207", "v208", "v209", "v210", "v211");
            ASM_ST_SAVE(4);
            ST(5);
            STFLUSH(t);
        }
    }
}

constexpr unsigned R_RED = 0;
constexpr unsigned R_PART = 8192;
__device__ __forceinline__ void r_role(const Params& P, int wave, int L, int j, int cpy) {
    const int lane = lane_id(), l15 = lane & 15, l4 = lane >> 4;
    const unsigned l16 = lane * 16;
    const rsrc_t rr = mkrsrc(P.ws + L * RG_L, RG_L), rd = mkrsrc(P.ws + DATA0 + L * D_L, D_L);
    const int sidx = (l15 < 8) ? 0x4444 : 0xEEEE;
    unsigned offp[3];
    offp[0] = RG_XP + cpy * XP_COPY + 4096 + wave * 2048 + l16;
    offp[1] = offp[0] + 1024;
    offp[2] = (l15 < 8) ? RG_XP + cpy * XP_COPY + wave * 512 + l4 * 128 + l15 * 16 : OOB;
    const unsigned offv = RG_VM + (j * 8 + wave) * 1024 + l16;
    const unsigned offo = (wave < (int)NCP) ? RG_XM + wave * XM_COPY + j * 256 + lane * 4 : OOB;
    const int pubw = 1 - (wave >> 2);
    {
        const int row = (lane >> 2) & 7, col = 16 * j + 4 * (lane & 3) + 2 * (lane >> 5);
        __builtin_amdgcn_raw_buffer_store_b32(nosent(*(const unsigned*)(P.mem0 + row * DM + col)), rr, offo, 0, 16);
    }
    const int rb = 4 * (l4 & 1) + 2 * (lane >> 5);
    auto red_addr = [](int fl) { return R_RED + fl * 80 + (fl >> 4) * 16; };
    const unsigned rw0 = red_addr((((l15 >> 1) & 1) * 8 + rb) * 4 + (l15 >> 2)) + (l15 & 1) * 4 + (wave ^ (l15 >> 3)) * 8;
    const unsigned rrd = red_addr(lane);
    u32x16 vx;
    const rsrc_t rf = mkrsrc(P.ws + FL0, FL_BYTES);
    const unsigned fo = (lane & 3) * 128, fs = FL_VO + (L * 128 + (j / 10) * 32 + wave * 4) * 128, vxo = (unsigned)(j * 32 + wave * 4) * 1024;
    const unsigned xo = (l4 >> 1) * 256 + l15 * 16 + (l4 & 1) * 8, pw = R_PART + wave * 1024 + l16, pr = R_PART + (wave & 1) * 8192 + l16;
    const int fin = (wave + 2) >> 3;
    asm volatile("" : "={v[160:175]}"(vx));
    auto vx_fetch = [&](int t) {
        for (int spins = 0;; ++spins) {
            const unsigned f = __builtin_amdgcn_raw_buffer_load_b32(rf, fo, fs, 16);
            if (__all(f > (unsigned)t)) break;
            __builtin_amdgcn_s_sleep(2);
            if (spin_fail(spins, P)) break;
        }
        asm volatile("buffer_load_dwordx4 v[160:163], %1, %2, %3 offen sc1\n\tbuffer_load_dwordx4 v[164:167], %1, %2, %3 offen offset:1024 sc1\n\t"
                     "buffer_load_dwordx4 v[168:171], %1, %2, %3 offen offset:2048 sc1\n\tbuffer_load_dwordx4 v[172:175], %1, %2, %3 offen offset:3072 sc1"
                     : "+{v[160:175]}"(vx) : "v"(l16), "s"(rd), "s"(D_VX + (t & (VXD - 1)) * VX_SLOT + vxo) : "memory");
    };
    vx_fetch(0);
    STDECL;
    for (int t = 0; t < NSEG; ++t) {
        const unsigned slot = t & (NSLOT - 1);
        ST(0);
        u32x4 vm = sent4(), pm = vm;
        u32x8 pv = __builtin_shufflevector(vm, vm, 0, 1, 2, 3, 0, 1, 2, 3);
#pragma unroll 1
        for (int spins = 1023;; spins += 1024) {
            unsigned n, a;
            ASM_ST_DECL;
            asm volatile(ASM_RCRIT : "+{v[180:183]}"(vm), "+{v[184:191]}"(pv), "+{v[192:195]}"(pm), [n] "=&s"(n), [a] "=&s"(a) ASM_ST_OUT ASM_ST2_OUT
                         : [rs] "s"(rr), [ov] "v"(offv), [sv] "s"(slot * VM_SLOT), [o0] "v"(offp[0]), [o1] "v"(offp[1]), [o2] "v"(offp[2]), [sp] "s"(slot * XP_SLOT),
                           "{v[160:175]}"(vx), [sidx] "v"(sidx), [rw] "v"(rw0), [rd] "v"(rrd), [oo] "v"(offo),
                           [so] "s"(((t + 1) & (NSLOT - 1)) * XM_SLOT), [pub] "s"(pubw * (NSEG - 1 - t))
                         : "memory", "vcc", "scc", "v176", "v177", "v178", "v179", "v196", "v200", "v201", "v202", "v203", "v204", "v205", "v206", "v207", "v208", "v209", "v210", "v211", "v212", "v213", "v214", "v215");
            ASM_ST_SAVE(2); ASM_ST2_SAVE(1);
            if (__builtin_amdgcn_readfirstlane(n)) break;
            if (spin_fail(spins, P)) { bar(); break; }
        }
        ST(3);
        if (wave >= 4) __builtin_amdgcn_s_sleep(RXDEL);
        unsigned early;
#pragma unroll 1
        for (int spins = 1023;; spins += 1024) {
            unsigned n, sx = D_XR + (unsigned)(t * 2 + (wave & 1)) * 20480 + j * 512;
            const unsigned pxb = RG_PX + slot * PX_SLOT + wave * 8192;
            ASM_ST_DECL;
            asm volatile(ASM_RX : "+{v[160:175]}"(vx), [n] "=&s"(n), [a] "=&s"(early), [sx] "+s"(sx) ASM_ST_OUT ASM_ST2_OUT
                         : "{v[180:183]}"(vm), "{v[184:191]}"(pv), "{v[192:195]}"(pm), [l16] "v"(l16), [rs] "s"(rr), [s0] "s"(pxb), [s1] "s"(pxb + 4096), [rd] "s"(rd), [xo] "v"(xo),
                           [fo] "v"(fo), [rf] "s"(rf), [fs] "s"(fs), [tn] "s"(t + 1), [sv] "s"(D_VX + ((t + 1) & (VXD - 1)) * VX_SLOT + vxo),
                           [pw] "v"(pw), [pr] "v"(pr), [fin] "s"(fin)
                         : "memory", "vcc", "scc", "v196", "v200", "v201", "v202", "v203", "v204", "v205", "v206", "v207", "v208", "v209", "v210", "v211", "v212", "v213", "v214", "v215", "v216", "v217", "v218", "v219", "v220", "v221", "v222", "v223", "v224", "v225", "v226", "v227", "v228", "v229", "v230", "v231", "v232", "v233", "v234", "v235", "v236", "v237", "v238", "v239", "v240", "v241", "v242", "v243", "v244", "v245", "v246", "v247", "v248", "v249", "v250", "v251");
            ASM_ST_SAVE(4); ASM_ST2_SAVE(5);
            if (__builtin_amdgcn_readfirstlane(n)) break;
            early = 0;
            if (spin_fail(spins, P)) { bar(); break; }
        }
        ST(6);
        st128(sent4(), rr, offv, slot * VM_SLOT);
        __builtin_amdgcn_raw_buffer_store_b32(SENT, rr, offo, slot * XM_SLOT, 16);
        if (wave == 5 && t + AH < NSEG) st128(sent4(), rd, (lane >> 5) * 20480 + (lane & 31) * 16, D_XA + (unsigned)(t + AH) * 40960 + j * 512);
        if (t + 1 < NSEG && !__builtin_amdgcn_readfirstlane(early)) vx_fetch(t + 1);
        ST(7);
        STFLUSH(t);
    }
}

template <int WHICH>
__device__ __forceinline__ void w_role(const Params& P, int wave, int L, int cu) {
    constexpr int NT = (WHICH == 3) ? 2 : 1;
    constexpr int KS = 20, PW = 5;
    constexpr int N = (WHICH == 3) ? FF : DM;
    constexpr unsigned W_ST = 2 * 49152;
    const int ln = lane_id(), l15 = ln & 15, l4 = ln >> 4;
    const unsigned l16 = ln * 16;
    const rsrc_t rd = mkrsrc(P.ws + DATA0 + L * D_L, D_L);
    const int nt0 = (cu * 8 + wave) * NT;
    bf16x8 wf[NT][KS];
    {
        const bf16_t* w = (const bf16_t*)(P.ws + (WHICH == 1 ? WT_1 : WHICH == 2 ? WT_2 : WT_3)) + (size_t)nt0 * KS * 512 + ln * 8;
#pragma unroll
        for (int n = 0; n < NT; ++n)
#pragma unroll
            for (int k = 0; k < KS; ++k) wf[n][k] = *(const bf16x8*)(w + (n * KS + k) * 512);
    }
    f32x4 cs[NT];
    if (WHICH != 2) {
        const bf16x8 ones = __builtin_bit_cast(bf16x8, ((u32x4){0x3F803F80u, 0x3F803F80u, 0x3F803F80u, 0x3F803F80u}));
#pragma unroll
        for (int n = 0; n < NT; ++n) {
            cs[n] = (f32x4){0.f, 0.f, 0.f, 0.f};
#pragma unroll
            for (int k = 0; k < KS; ++k) MFMA(wf[n][k], ones, cs[n]);
        }
    }
    const int grp = nt0 / 10, sh = (WHICH == 1) ? ((grp & 1) ? -1 : 1) * ((grp >> 1) + 1) : 0;
    if (WHICH == 1) {
        const int zr = (grp & 1) ? S_ - 1 - l15 : l15;
        unsigned zv;
        asm volatile("v_mov_b32 %0, 0" : "=v"(zv));
        st128(((u32x4){zv, zv, zv, zv}), rd, (l15 <= (grp >> 1) && l4 < 2) ? toff(zr, nt0 * 16 + l4 * 8, DM) : OOB, D_HS);
    }
    constexpr unsigned D_IN = WHICH == 1 ? D_XA : WHICH == 2 ? D_HS : D_X1;
    constexpr unsigned D_OUT = WHICH == 1 ? D_HS : WHICH == 2 ? D_X1 : D_H2;
    u32x4 v[PW] = {};
    poll5_issue(rd, l16, D_IN + wave * PW * 1024, v);
    wait_vm<0>();
    STDECL;
    for (int t = 0; t < NSEG; ++t) {
        const unsigned ab = (t & 1) * 49152u;
        ST(0);
        {
            poll5(rd, l16, D_IN + (unsigned)t * 40960 + wave * PW * 1024, v, P);
            ST(1);
#pragma unroll
            for (int k = 0; k < PW; ++k) lds_st<u32x4>(ab + (wave * PW + k) * 1024 + l16, v[k]);
            if (WHICH != 2) {
                float s = 0.f, q = 0.f;
                const bf16x2 one2 = __builtin_bit_cast(bf16x2, 0x3F803F80u);
#pragma unroll
                for (int k = 0; k < PW; ++k)
#pragma unroll
                    for (int e = 0; e < 4; ++e) {
                        const unsigned u = v[k][e];
                        const bf16x2 x = __builtin_bit_cast(bf16x2, u);
                        s = __builtin_amdgcn_fdot2_f32_bf16(x, one2, s, false);
                        q = __builtin_amdgcn_fdot2_f32_bf16(x, x, q, false);
                    }
                sum2_rows(s, q);
                if (ln < 16) lds_st<f32x2>(W_ST + (t & 1) * 1024 + wave * 128 + ln * 8, (f32x2){s, q});
            }
        }
        bar();
        ST(2);
        u32x2 r1[2], sv[2];
        if (WHICH == 2) {
            const unsigned ro = toff(t * SEG + l15, nt0 * 16 + l4 * 4, DM);
#pragma unroll
            for (int mt = 0; mt < 2; ++mt) { r1[mt] = LD64(rd, ro + mt * (DM * 32), D_XA); sv[mt] = LD64(rd, (t * SEG + mt * 16 + l15) * 8, D_SS); }
        }
        float mr[2], rs[2];
        f32x2 st[2][4];
        if (WHICH != 2) {
#pragma unroll
            for (int w = 0; w < 8; ++w) st[w >> 2][w & 3] = lds_ld<f32x2>(W_ST + (t & 1) * 1024 + w * 128 + l15 * 8);
        }
        auto stats = [&](int mt) {
            const f32x2 a = (st[mt][0] + st[mt][1]) + (st[mt][2] + st[mt][3]);
            const float mean = a[0] * (1.0f / DM), var = __builtin_fmaxf(a[1] * (1.0f / DM) - mean * mean, 0.f);
            const float m1 = rbf(mean), d1 = ln_den(var), r1i = rcp(d1);
            if (WHICH == 1) {
                const float m2 = rbf((mean - m1) * r1i), d2 = ln_den(var * r1i * r1i);
                rs[mt] = r1i * rcp(d2); mr[mt] = -(m1 + m2 * d1) * rs[mt];
                if (cu == 0 && wave == 0)
                    __builtin_amdgcn_raw_buffer_store_b64((u32x2){__float_as_uint(m1), __float_as_uint(r1i)}, rd, ln < 16 ? (t * SEG + mt * 16 + ln) * 8 : OOB, D_SS, 16);
            } else { rs[mt] = r1i; mr[mt] = -m1 * r1i; }
            asm volatile("" : "+v"(rs[mt]), "+v"(mr[mt]));
        };
        f32x4 acc[NT][2];
        unsigned o[2][NT][2];
        auto fin = [&](int mt, int n, int hf) {
            float v[2];
#pragma unroll
            for (int e = 0; e < 2; ++e) {
                if (WHICH != 2) v[e] = gelu_tanh(__builtin_fmaf(acc[n][mt][hf * 2 + e], rs[mt], cs[n][hf * 2 + e] * mr[mt]));
                else v[e] = rbf(((e ? hi2f(r1[mt][hf]) : lo2f(r1[mt][hf])) - __uint_as_float(sv[mt][0])) * __uint_as_float(sv[mt][1])) + acc[n][mt][hf * 2 + e];
            }
            o[mt][n][hf] = nosent(pack2(v[0], v[1]));
            asm volatile("" : "+v"(o[mt][n][hf]));
        };
        const unsigned o3 = (unsigned)((l4 & 1) * 512 + (l4 >> 1) * 256 + l15 * 16), b3 = D_H2 + (unsigned)t * 98304 + (nt0 >> 1) * 1024;
#pragma unroll
        for (int n = 0; n < NT; ++n) { acc[n][0] = (f32x4){0.f, 0.f, 0.f, 0.f}; acc[n][1] = acc[n][0]; }
        {
            constexpr int RG = 8;
            bf16x8 a[RG];
#pragma unroll
            for (int u = 0; u < RG - 1; ++u) a[u] = lds_ld<bf16x8>(ab + l16 + u * 1024);
#pragma unroll
            for (int u = 0; u < 2 * KS; ++u) {
                if (u + RG - 1 < 2 * KS) a[(u + RG - 1) % RG] = lds_ld<bf16x8>(ab + l16 + (u + RG - 1) * 1024);
                SB();
#pragma unroll
                for (int n = 0; n < NT; ++n) MFMA(wf[n][u % KS], a[u % RG], acc[n][u / KS]);
                if (WHICH != 2 && (u == 4 || u == 12)) stats(u == 12);
                if (u == (WHICH == 3 ? 22 : 6)) poll5_issue(rd, l16, D_IN + (unsigned)(t + 1) * 40960 + wave * PW * 1024, v);
                if (WHICH == 3 && u == 38) wait_vm<0>();
                if (u >= 24 && u % 4 == 0 && (u - 24) / 4 < 2 * NT) fin(0, (u - 24) / 8, ((u - 24) / 4) & 1);
                if (WHICH == 3 && u == 38) st128(merge16((u32x2){o[0][0][0], o[0][0][1]}, (u32x2){o[0][NT - 1][0], o[0][NT - 1][1]}), rd, o3, b3);
                SB();
            }
        }
        ST(3);
#pragma unroll
        for (int k = 0; k < 2 * NT; ++k) fin(1, k >> 1, k & 1);
        if (WHICH == 3) {
            st128(merge16((u32x2){o[1][0][0], o[1][0][1]}, (u32x2){o[1][NT - 1][0], o[1][NT - 1][1]}), rd, o3, b3 + 49152);
            ST(4);
            if (t + AH < NSEG) { st128(sent4(), rd, l16, b3 + AH * 98304); st128(sent4(), rd, l16, b3 + AH * 98304 + 49152); }
        } else {
            wait_vm<0>();
            const int orow = t * SEG + (l4 & 1) * 16 + l15 + sh;
            st128(merge16((u32x2){o[0][0][0], o[0][0][1]}, (u32x2){o[1][0][0], o[1][0][1]}), rd, (unsigned)orow < (unsigned)S_ ? toff(orow, nt0 * 16 + (l4 >> 1) * 8, DM) : OOB, D_OUT);
            ST(4);
            if (t + AH < NSEG) st128(sent4(), rd, orow + AH * SEG < S_ ? toff(orow + AH * SEG, nt0 * 16 + (l4 >> 1) * 8, DM) : OOB, D_OUT);
        }
        STFLUSH(t);
    }
    wait_vm<0>();
    asm volatile("" :: "v"(v[0]), "v"(v[1]), "v"(v[2]), "v"(v[3]), "v"(v[4]));
}

__device__ __forceinline__ void w4_role(const Params& P, int wave, int L, int cu) {
    const int ln = lane_id(), l15 = ln & 15, l4 = ln >> 4;
    const unsigned l16 = ln * 16;
    const rsrc_t rd = mkrsrc(P.ws + DATA0 + L * D_L, D_L);
    STDECL;
    if (wave < 6) {
        u32x32 w[5];
        {
            const char* ws = P.ws + WT_4 + (size_t)(cu * 5 * 48 + wave * 8) * 1024 + l16;
#pragma unroll
            for (int n = 0; n < 5; ++n) {
                u32x8 f[4];
#pragma unroll
                for (int i = 0; i < 4; ++i) f[i] = __builtin_shufflevector(*(const u32x4*)(ws + n * 49152 + i * 2048), *(const u32x4*)(ws + n * 49152 + i * 2048 + 1024), 0, 1, 2, 3, 4, 5, 6, 7);
                w[n] = cat32(f[0], f[1], f[2], f[3]);
            }
            asm volatile("" : "+{v[0:31]}"(w[0]), "+{v[32:63]}"(w[1]), "+{v[64:95]}"(w[2]), "+{v[96:127]}"(w[3]), "+{v[128:159]}"(w[4]));
        }
        unsigned budget = 1u << 17;
        for (int t = 0; t < NSEG; ++t) {
            ST(0);
            const unsigned gb = D_H2 + (unsigned)t * 98304 + wave * 8192;
            u32x4 can;
            for (int spins = 0;; ++spins) {
                can = LD128(rd, l16, gb + 7168);
                if (__all(umax4(0u, can) != SENT)) break;
                __builtin_amdgcn_s_sleep(1);
                if (spin_fail(spins, P)) { budget = 0; break; }
            }
            ST(1);
            asm volatile(ASM_W4_M : [n] "+s"(budget)
                         : "{v[188:191]}"(can), "{v[0:31]}"(w[0]), "{v[32:63]}"(w[1]), "{v[64:95]}"(w[2]), "{v[96:127]}"(w[3]), "{v[128:159]}"(w[4]),
                           [ld] "v"(l16 + wave * 1024 + (t & 1) * 61440), [l16] "v"(l16), [rs] "s"(rd), [s0] "s"(gb), [s1] "s"(gb + 4096), [s2] "s"(gb + 49152), [s3] "s"(gb + 53248)
                         : "memory", "vcc", "scc", "v160", "v161", "v162", "v163", "v164", "v165", "v166", "v167", "v168", "v169", "v170", "v171", "v172", "v173", "v174", "v175", "v176", "v177", "v178", "v179", "v180", "v181", "v182", "v183", "v184", "v185", "v186", "v187", "v192", "v193", "v194", "v195", "v196", "v197", "v198", "v199", "v200", "v201", "v202", "v203", "v204", "v205", "v206", "v207", "v208", "v209", "v210", "v211", "v212", "v213", "v214", "v215", "v216", "v217", "v218", "v219", "v220", "v221", "v222", "v223", "v224", "v225", "v226", "v227", "v228", "v229", "v230", "v231", "v232", "v233", "v234", "v235", "v236", "v237", "v238", "v239", "v240", "v241", "v242", "v243", "v244");
            ST(2);
            bar();
            ST(3);
            STFLUSH(t);
        }
    } else {
        const int mt = wave - 6;
        const rsrc_t rn = mkrsrc(P.ws + DATA0 + D_L, D_L), rr = mkrsrc(P.ws, RG_L), ro = mkrsrc(P.out, ACT);
        const unsigned o8 = (l4 >> 1) * 256 + l15 * 16 + (l4 & 1) * 8, o16 = (l4 & 1) * 512 + (l4 >> 1) * 256 + l15 * 16;
        const unsigned q8 = l15 * (DM * 2) + l4 * 8, q16 = l15 * (DM * 2) + (l4 & 1) * 32 + (l4 >> 1) * 16;
        for (int t = 0; t < NSEG; ++t) {
            const unsigned sb = (unsigned)(t * 2 + mt) * 20480 + cu * 2560;
            ST(0);
            f32x4 res[5];
            for (int spins = 0;; ++spins) {
                u32x2 r1[5], r2[5];
                unsigned m = 0;
#pragma unroll
                for (int n = 0; n < 5; ++n) { r1[n] = LD64(rd, o8 + n * 512, D_X1 + sb); r2[n] = LD64(rd, o8 + n * 512, D_XA + sb); }
#pragma unroll
                for (int n = 0; n < 5; ++n) {
                    m = umax4(m, (u32x4){r1[n][0], r1[n][1], r2[n][0], r2[n][1]});
                    res[n] = (f32x4){lo2f(r1[n][0]), hi2f(r1[n][0]), lo2f(r1[n][1]), hi2f(r1[n][1])} + (f32x4){lo2f(r2[n][0]), hi2f(r2[n][0]), lo2f(r2[n][1]), hi2f(r2[n][1])};
                }
                if (__all(m != SENT)) break;
                __builtin_amdgcn_s_sleep(8);
                if (spin_fail(spins * 4 + 3, P)) break;
            }
            asm volatile("" : "+v"(res[0]), "+v"(res[1]), "+v"(res[2]), "+v"(res[3]), "+v"(res[4]));
            ST(1);
            bar();
            ST(2);
            {
                const unsigned la = (t & 1) * 61440 + mt * 30720 + l16;
                f32x4 c[30];
#pragma unroll
                for (int i = 0; i < 30; ++i) c[i] = lds_ld<f32x4>(la + ((i % 5) * 6 + i / 5) * 1024);
#pragma unroll
                for (int i = 0; i < 30; ++i) { res[i % 5] += c[i]; asm volatile("" : "+v"(res[i % 5])); }
            }
            u32x2 o[5];
#pragma unroll
            for (int n = 0; n < 5; ++n) o[n] = (u32x2){pack2(res[n][0], res[n][1]), pack2(res[n][2], res[n][3])};
            if (L) {
                const unsigned ob = (unsigned)(t * 2 + mt) * 20480 + cu * 160;
                st128_<0>(merge16(o[0], o[1]), ro, q16, ob); st128_<0>(merge16(o[2], o[3]), ro, q16, ob + 64);
                __builtin_amdgcn_raw_buffer_store_b64(o[4], ro, q8, ob + 128, 0);
            } else {
                float s = 0.f, q = 0.f;
                const bf16x2 one2 = __builtin_bit_cast(bf16x2, 0x3F803F80u);
#pragma unroll
                for (int e = 0; e < 10; ++e) {
                    const unsigned u = o[e >> 1][e & 1];
                    const bf16x2 x = __builtin_bit_cast(bf16x2, u);
                    s = __builtin_amdgcn_fdot2_f32_bf16(x, one2, s, false);
                    q = __builtin_amdgcn_fdot2_f32_bf16(x, x, q, false);
                }
                sum2_rows(s, q);
                const unsigned st = RG_ST + (unsigned)(t * 2 + mt) * 1024;
                __builtin_amdgcn_raw_buffer_store_b64((u32x2){nosent(__float_as_uint(s)), nosent(__float_as_uint(q))}, rr, ln < 16 ? ln * 8 : OOB, st + cu * 128, 16);
                ST(3);
                st128(merge16(o[0], o[1]), rn, o16, D_XR + sb); st128(merge16(o[2], o[3]), rn, o16, D_XR + sb + 1024);
                __builtin_amdgcn_raw_buffer_store_b64(o[4], rn, o8, D_XR + sb + 2048, 16);
                __builtin_amdgcn_s_sleep(W4SL);
                for (int spins = 0;; ++spins) {
                    const u32x2 a = LD64(rr, ln * 8, st), b = LD64(rr, ln * 8 + 512, st);
                    s = __uint_as_float(a[0]) + __uint_as_float(b[0]); q = __uint_as_float(a[1]) + __uint_as_float(b[1]);
                    if (__all(umax4(0u, (u32x4){a[0], a[1], b[0], b[1]}) != SENT)) break;
                    asm volatile("" ::: "memory");
                    if (spin_fail(spins, P)) break;
                }
                ST(4);
                sum2_rows(s, q);
                const float mean = s * (1.0f / DM), var = __builtin_fmaxf(q * (1.0f / DM) - mean * mean, 0.f);
                const float r = rcp(ln_den(var)), mr = -rbf(mean) * r;
#pragma unroll
                for (int n = 0; n < 5; ++n)
                    o[n] = (u32x2){nosent(pack2(__builtin_fmaf(res[n][0], r, mr), __builtin_fmaf(res[n][1], r, mr))), nosent(pack2(__builtin_fmaf(res[n][2], r, mr), __builtin_fmaf(res[n][3], r, mr)))};
                st128(merge16(o[0], o[1]), rn, o16, D_XN + sb); st128(merge16(o[2], o[3]), rn, o16, D_XN + sb + 1024);
                __builtin_amdgcn_raw_buffer_store_b64(o[4], rn, o8, D_XN + sb + 2048, 16);
                if (t + AH < NSEG) {
                    const u32x4 s4 = sent4();
                    st128(s4, rn, l16, D_XN + sb + AH * 40960); st128(s4, rn, l16, D_XN + sb + AH * 40960 + 1024);
                    st128(s4, rn, ln < 32 ? l16 : OOB, D_XN + sb + AH * 40960 + 2048);
                }
            }
            ST(5);
            STFLUSH(t);
        }
    }
}

__global__ __launch_bounds__(512, 2) void mega_kernel(Params P) {
    const int wg = blockIdx.x;
    const int wave = __builtin_amdgcn_readfirstlane(threadIdx.x >> 6);
    const int L = (wg >> 3) & 1, x = wg & 3, r = (wg >> 4) * 2 + ((wg >> 2) & 1), cpy = wg & (NCP - 1);
    if (r < 8) s_role(P, wave, L, x * 8 + r, cpy);
    else if (r < 16) v_role(P, wave ^ 4, L, x * 8 + r - 8, cpy);
    else if (r < 26) r_role(P, wave, L, (r - 16) * 4 + x, cpy);
    else {
        const int i = (r - 26) * 4 + x;
        if (i < 5) w_role<1>(P, wave, L, i);
        else if (i < 10) w_role<2>(P, wave, L, i - 5);
        else if (i < 16) w_role<3>(P, wave, L, i - 10);
        else w4_role(P, wave, L, i - 16);
    }
}

__device__ __forceinline__ void xtile_body(int mt, const bf16_t* __restrict__ x, char* __restrict__ ws) {
    __shared__ float red[2][16][17];
    const int r15 = threadIdx.x & 15, slot = threadIdx.x >> 4;
    u32x4 v[5];
    float s = 0.f;
#pragma unroll
    for (int c = 0; c < 5; ++c) {
        v[c] = *(const u32x4*)(x + (size_t)(mt * 16 + r15) * DM + (slot * 5 + c) * 8);
#pragma unroll
        for (int e = 0; e < 4; ++e) s += lo2f(v[c][e]) + hi2f(v[c][e]);
    }
    red[0][slot][r15] = s;
    __syncthreads();
    s = 0.f;
#pragma unroll
    for (int k = 0; k < 16; ++k) s += red[0][k][r15];
    const float mean = s * (1.0f / DM);
    float q = 0.f;
#pragma unroll
    for (int c = 0; c < 5; ++c)
#pragma unroll
        for (int e = 0; e < 4; ++e) { const float a = lo2f(v[c][e]) - mean, b = hi2f(v[c][e]) - mean; q += a * a + b * b; }
    red[1][slot][r15] = q;
    __syncthreads();
    q = 0.f;
#pragma unroll
    for (int k = 0; k < 16; ++k) q += red[1][k][r15];
    const float m1 = rbf(mean), r = 1.0f / ln_den(q * (1.0f / DM));
#pragma unroll
    for (int c = 0; c < 5; ++c) {
        const unsigned o = (unsigned)(mt * 80 + slot * 5 + c) * 256 + r15 * 16;
        *(u32x4*)(ws + DATA0 + D_XR + o) = v[c];
        u32x4 n;
#pragma unroll
        for (int e = 0; e < 4; ++e) n[e] = pack2(rbf(lo2f(v[c][e]) - m1) * r, rbf(hi2f(v[c][e]) - m1) * r);
        *(u32x4*)(ws + DATA0 + D_XN + o) = n;
    }
}

struct Prep1 { TileJobs J; const bf16_t* x; char* ws; };
__global__ __launch_bounds__(256) void prep1_kernel(Prep1 A) {
    int b = blockIdx.x;
    if (b < 256) {
        for (unsigned i = b * 256 + threadIdx.x; i < FL0 / 16; i += 256 * 256) ((u32x4*)A.ws)[i] = (u32x4){SENT, SENT, SENT, SENT};
        if (b == 0) for (unsigned i = threadIdx.x; i < FL_BYTES / 16; i += 256) ((u32x4*)(A.ws + FL0))[i] = (u32x4){0u, 0u, 0u, 0u};
        for (int u = b >> 3; u < 10; u += 32) {
            if (u == 0) continue;
            const unsigned n16 = (AH + 1) * ((u % 5 == 4) ? 98304 : 40960) / 16;
            u32x4* p = (u32x4*)(A.ws + DATA0 + (u / 5) * D_L + (u % 5 + 1) * ACT);
            for (unsigned i = (b & 7) * 256 + threadIdx.x; i < n16; i += 2048) p[i] = (u32x4){SENT, SENT, SENT, SENT};
        }
        return;
    }
    b -= 256;
    if (b < S_ / 16) { xtile_body(b, A.x, A.ws); return; }
    tile_body(A.J, b - S_ / 16);
}

__device__ __forceinline__ unsigned long long clk_() { unsigned long long c; asm volatile("s_memtime %0\n\ts_waitcnt lgkmcnt(0)" : "=s"(c) :: "memory"); return c; }
__global__ void calib_kernel(char* ws) {
    if (blockIdx.x) return;
    char* b = (char*)(((size_t)ws + 0x1FFFFF) & ~(size_t)0x1FFFFF);
    const rsrc_t r = mkrsrc(b, 254u << 20);
    unsigned m[4] = {0, 0, 0, 0};
    for (int p = 0; p < 127; ++p) {
        unsigned best[2];
        for (int k = 0; k < 2; ++k) { best[k] = 1 << 30;
            for (int rep = 0; rep < 4; ++rep) { unsigned long long t1 = clk_(); __builtin_amdgcn_raw_buffer_store_b32(0, r, k * 8192, p << 21, 16); asm volatile("s_waitcnt vmcnt(0)" ::: "memory"); best[k] = min(best[k], (unsigned)(clk_() - t1)); } }
        const unsigned bit = best[0] > best[1];
        if (p < 32) m[0] |= bit << p; else if (p < 64) m[1] |= bit << (p - 32); else if (p < 96) m[2] |= bit << (p - 64); else m[3] |= bit << (p - 96);
    }
    if (threadIdx.x == 0) printf("CALIB ws %p xcc %u mask %08x %08x %08x %08x\n", ws, __builtin_amdgcn_s_getreg((3 << 11) | 20) & 0xF, m[0], m[1], m[2], m[3]);
}
extern "C" void kernel_launch(void* const* d_in, const int* in_sizes, int n_in, void* d_out, int out_size, void* d_ws,
                              size_t ws_size, hipStream_t stream) {
    char* ws = (char*)d_ws;
    Params P;
    P.ws = ws; P.mem0 = (const bf16_t*)d_in[1]; P.out = (bf16_t*)d_out;
    (void)hipFuncSetAttribute((const void*)mega_kernel, hipFuncAttributeMaxDynamicSharedMemorySize, MEGA_LDS);

    Prep1 A1;
    int blk = 0;
    auto job = [&](int i, int in, unsigned o, int Kd, int Nd, int ksh) { A1.J.j[i] = TileJob{(const bf16_t*)d_in[in], (bf16_t*)(ws + o), i == 3 ? 1 : 0, Nd, ksh, blk}; blk += (Kd / 32) * (Nd / 128); };
    job(0, 2, WT_QKV, DM, HD, 20);
    job(1, 3, WT_QKV + HD * DM * 2, DM, HD, 20);
    job(2, 4, WT_QKV + 2 * HD * DM * 2, DM, HD, 20);
    job(3, 5, WT_O, HD, DM, 6);
    job(4, 8, WT_1, DM, DM, 20);
    job(5, 9, WT_2, DM, DM, 20);
    job(6, 10, WT_3, DM, FF, 20);
    job(7, 11, WT_4, FF, DM, 48);
    A1.x = (const bf16_t*)d_in[0]; A1.ws = ws;
    fprintf(stderr, "HOST ws %p out %p in0 %p in2 %p\n", d_ws, d_out, d_in[0], d_in[2]);
    calib_kernel<<<8, 64, 0, stream>>>(ws);
    prep1_kernel<<<256 + S_ / 16 + blk, 256, 0, stream>>>(A1);
    mega_kernel<<<256, 512, MEGA_LDS, stream>>>(P);
}
```

```cpp
#include <hip/hip_runtime.h>
#include <stdint.h>
#include <type_traits>

typedef unsigned short bf16_t;
using bf16x2 = __attribute__((ext_vector_type(2))) __bf16;
using bf16x8 = __attribute__((ext_vector_type(8))) __bf16;
using bf16x16 = __attribute__((ext_vector_type(16))) __bf16;
using f32x2 = __attribute__((ext_vector_type(2))) float;
using f32x4 = __attribute__((ext_vector_type(4))) float;
using u32x2 = __attribute__((ext_vector_type(2))) unsigned;
using u32x4 = __attribute__((ext_vector_type(4))) unsigned;
typedef __amdgpu_buffer_rsrc_t rsrc_t;

__device__ __forceinline__ unsigned pack2(float a, float b) {
    bf16x2 r = __builtin_convertvector((f32x2){a, b}, bf16x2);
    return __builtin_bit_cast(unsigned, r);
}
__device__ __forceinline__ float rbf(float f) { return __uint_as_float(pack2(f, 0.f) << 16); }
__device__ __forceinline__ float lo2f(unsigned u) { return __uint_as_float(u << 16); }
__device__ __forceinline__ float hi2f(unsigned u) { return __uint_as_float(u & 0xFFFF0000u); }
__device__ __forceinline__ rsrc_t mkrsrc(const void* p, unsigned bytes) {
    return __builtin_amdgcn_make_buffer_rsrc((void*)p, 0, bytes, 0x00020000);
}

constexpr int S_ = 2048, DM = 640, NH = 32, DH = 192, HD = NH * DH  , SEG = 32, NSEG = 64;
constexpr int FF = 1536;

#define XMAP
#define NCPY 4
constexpr unsigned NSLOT = 4, NCP = NCPY;
#define FL_TK_DEF
#define DLY_L 14
#define DLY_S 3
#define DLY_W 10
#define VTRY 3
#define VSL 1
#define NRR 5
constexpr unsigned BLK = 16384, RG_SLOT = 4u << 20;
constexpr unsigned XP_COPY = 3 * BLK, XP_SLOT = RG_SLOT;
constexpr unsigned XM_COPY = 2 * BLK, XM_SLOT = RG_SLOT;
constexpr unsigned VM_SLOT = RG_SLOT;
constexpr unsigned PX_SLOT = RG_SLOT;
constexpr unsigned RG_XP = 0, RG_XM = 12 * BLK, RG_VM = 20 * BLK, RG_PX = 60 * BLK;
constexpr unsigned RG_ST = 68 * BLK, RG_FV = 84 * BLK, RG_FH = 86 * BLK, RG_FX = 88 * BLK, RG_L = NSLOT * RG_SLOT;
constexpr unsigned FL0 = 2u << 20, FL_TK = 512, FL_BYTES = FL_TK + 8 * 128;
constexpr unsigned ACT = S_ * DM * 2;
#define PXROT 5
#define W4SL 6
#define RXDEL 4
#define VXD 32
#define VXAUX 16
constexpr unsigned PG0 = RG_L, PG_L = 80u << 21;
constexpr unsigned PG_XA = 0, PG_HS = 5 * BLK, PG_X1 = 10 * BLK, PG_H2 = 15 * BLK, PG_SS = 27 * BLK, PG_XR = 28 * BLK, PG_XN = 33 * BLK, PG_LX = 10 * BLK, PG_HV = 48 * BLK;
__device__ __forceinline__ unsigned pgb(unsigned p, int par) { return p << 21 | ((p ^ par) & 1) << 13; }
__device__ __forceinline__ constexpr unsigned blin(unsigned o) { return o + (o & ~8191u); }
constexpr int AH = 3;
constexpr unsigned WS_END = PG0 + PG_L;

#define SB() __builtin_amdgcn_sched_barrier(0)
#define LAS __attribute__((address_space(3)))
template <class T> __device__ __forceinline__ T lds_ld(unsigned a) { return *(const LAS T*)(a); }
template <class T> __device__ __forceinline__ void lds_st(unsigned a, T v) { *(LAS T*)(a) = v; }
constexpr unsigned OOB = 0x7FFFFF00u;
template <int N> __device__ __forceinline__ void wait_vm() { asm volatile("s_waitcnt vmcnt(%0)" :: "i"(N) : "memory"); }
__device__ __forceinline__ void bar() {
    asm volatile("s_waitcnt lgkmcnt(0)" ::: "memory");
    __builtin_amdgcn_s_barrier();
    asm volatile("" ::: "memory");
}
__device__ __forceinline__ int lane_id() {
    int ln;
    asm volatile("v_mbcnt_lo_u32_b32 %0, -1, 0\n\tv_mbcnt_hi_u32_b32 %0, -1, %0" : "=v"(ln));
    return ln;
}
__device__ __forceinline__ void swap16(unsigned& a, unsigned& b) { asm volatile("s_nop 3\n\tv_permlane16_swap_b32 %0, %1\n\ts_nop 1" : "+v"(a), "+v"(b)); }
__device__ __forceinline__ void swap32(unsigned& a, unsigned& b) { asm volatile("s_nop 3\n\tv_permlane32_swap_b32 %0, %1\n\ts_nop 1" : "+v"(a), "+v"(b)); }
__device__ __forceinline__ float sum_x16(float x) { unsigned a = __float_as_uint(x), b = a; swap16(a, b); return __uint_as_float(a) + __uint_as_float(b); }
__device__ __forceinline__ float sum_x32(float x) { unsigned a = __float_as_uint(x), b = a; swap32(a, b); return __uint_as_float(a) + __uint_as_float(b); }
__device__ __forceinline__ float max_x16(float x) { unsigned a = __float_as_uint(x), b = a; swap16(a, b); return __builtin_fmaxf(__uint_as_float(a), __uint_as_float(b)); }
__device__ __forceinline__ float max_x32(float x) { unsigned a = __float_as_uint(x), b = a; swap32(a, b); return __builtin_fmaxf(__uint_as_float(a), __uint_as_float(b)); }
__device__ __forceinline__ void sum2_rows(float& s, float& q) {
    unsigned a = __float_as_uint(s), b = a, c = __float_as_uint(q), d = c;
    asm volatile("s_nop 2\n\tv_permlane16_swap_b32 %0, %1\n\tv_permlane16_swap_b32 %2, %3\n\tv_add_f32 %0, %0, %1\n\tv_add_f32 %2, %2, %3\n\tv_mov_b32 %1, %0\n\tv_mov_b32 %3, %2\n\t"
                 "s_nop 1\n\tv_permlane32_swap_b32 %0, %1\n\tv_permlane32_swap_b32 %2, %3\n\tv_add_f32 %0, %0, %1\n\tv_add_f32 %2, %2, %3" : "+v"(a), "+v"(b), "+v"(c), "+v"(d));
    s = __uint_as_float(a); q = __uint_as_float(c);
}
__device__ __forceinline__ u32x4 merge16(u32x2 a, u32x2 b) {
    unsigned a0 = a[0], a1 = a[1], b0 = b[0], b1 = b[1];
    asm volatile("s_nop 3\n\tv_permlane16_swap_b32 %0, %2\n\tv_permlane16_swap_b32 %1, %3\n\ts_nop 1" : "+v"(a0), "+v"(a1), "+v"(b0), "+v"(b1));
    return (u32x4){a0, a1, b0, b1};
}
template <int AUXV>
__device__ __forceinline__ void st128_(u32x4 v, rsrc_t r, unsigned voff, unsigned soff) {
    __builtin_amdgcn_raw_buffer_store_b128(v, r, voff, soff, AUXV);
    asm volatile("s_nop 1" :: "v"(v) : "memory");
}
#define st128(V, R, VO_, SO_) st128_<16>(V, R, VO_, SO_)
#define LD128(R, VO_, SO_) __builtin_amdgcn_raw_buffer_load_b128(R, VO_, SO_, 16)
#define LD64(R, VO_, SO_) __builtin_amdgcn_raw_buffer_load_b64(R, VO_, SO_, 16)
#define BF8(X) __builtin_bit_cast(bf16x8, X)
__device__ __forceinline__ float gelu_tanh(float x) {
    const float c0 = -2.0f * 0.7978845608028654f * 1.4426950408889634f, c1 = c0 * 0.044715f;
    const float e = __builtin_amdgcn_exp2f(x * __builtin_fmaf(x * x, c1, c0));
    return x * __builtin_amdgcn_rcpf(1.0f + e);
}
#define MFMA(A_, B_, C_) C_ = __builtin_amdgcn_mfma_f32_16x16x32_bf16(A_, B_, C_, 0, 0, 0)
#define SMF(A_, B_, C_) C_ = __builtin_amdgcn_smfmac_f32_16x16x64_bf16(A_, B_, C_, sidx, 0, 0)
__device__ __forceinline__ float ln_den(float var) { return rbf(__builtin_amdgcn_sqrtf(rbf(rbf(var) + 1e-5f))); }
__device__ __forceinline__ float rcp(float x) { return __builtin_amdgcn_rcpf(x); }

struct Params {
    char* ws;
    const bf16_t* mem0;
    const bf16_t* w[8];
    bf16_t* out;
};

#define RLX_AGENT __ATOMIC_RELAXED, __HIP_MEMORY_SCOPE_AGENT
#define SENT 0xFFFFFFFFu
constexpr int SPIN_CAP = 400000;
__device__ __forceinline__ bool spin_fail(int spins, const Params& P) {
    if ((spins & 1023) == 1023) {
        const rsrc_t rt = mkrsrc(P.ws + FL0, FL_BYTES);
        unsigned o;
        asm volatile("v_mov_b32 %0, 0x100" : "=v"(o));
        if (__builtin_amdgcn_readfirstlane(__builtin_amdgcn_raw_buffer_load_b32(rt, o, 0, 16)) != 0u) return true;
        if (spins > SPIN_CAP) { __builtin_amdgcn_raw_buffer_store_b32(o, rt, o, 0, 16); return true; }
    }
    return false;
}
__device__ __forceinline__ bool ok4(u32x4 v) { return v[0] != SENT && v[1] != SENT && v[2] != SENT && v[3] != SENT; }
__device__ __forceinline__ unsigned nosent(unsigned d) { return d < 0xFFFFFFFEu ? d : 0xFFFFFFFEu; }
__device__ __forceinline__ u32x4 nosent4(u32x4 v) { return (u32x4){nosent(v[0]), nosent(v[1]), nosent(v[2]), nosent(v[3])}; }
__device__ __forceinline__ unsigned umax3(unsigned a, unsigned b, unsigned c) { const unsigned m = a > b ? a : b; return m > c ? m : c; }
__device__ __forceinline__ unsigned umax4(unsigned m, u32x4 v) { return umax3(umax3(m, v[0], v[1]), v[2], v[3]); }
__device__ __forceinline__ u32x4 sent4() { unsigned sv; asm volatile("v_mov_b32 %0, -1" : "=v"(sv)); return (u32x4){sv, sv, sv, sv}; }
template <int N>
__device__ __forceinline__ void poll(rsrc_t rs, unsigned base, const unsigned (&off)[N], u32x4 (&v)[N], const Params& P) {
    for (int spins = 0;; ++spins) {
        unsigned m = 0;
#pragma unroll
        for (int k = 0; k < N; ++k) v[k] = LD128(rs, off[k], base);
#pragma unroll
        for (int k = 0; k < N; ++k) m = umax4(m, v[k]);
        if (__all(m != SENT)) return;
        asm volatile("" ::: "memory");
        if (spin_fail(spins, P)) return;
    }
}
__device__ __forceinline__ bf16x16 cat16(bf16x8 a, bf16x8 b) {
    return __builtin_shufflevector(a, b, 0, 1, 2, 3, 4, 5, 6, 7, 8, 9, 10, 11, 12, 13, 14, 15);
}
__device__ __forceinline__ bf16x8 half8(bf16x16 v, int h) {
    return h ? __builtin_shufflevector(v, v, 8, 9, 10, 11, 12, 13, 14, 15) : __builtin_shufflevector(v, v, 0, 1, 2, 3, 4, 5, 6, 7);
}
__device__ __forceinline__ bf16x16 lds_frag(unsigned a, unsigned step) { return cat16(lds_ld<bf16x8>(a), lds_ld<bf16x8>(a + step)); }
template <int I>
__device__ __forceinline__ void dmaq(const char* g, unsigned l16, unsigned dst) {
    asm volatile("s_mov_b32 m0, %2\n\ts_nop 0\n\tglobal_load_lds_dwordx4 %0, %1 offset:%3 sc1" :: "v"(l16), "s"(g), "s"(dst), "i"(I * 1024) : "memory");
}
__device__ __forceinline__ void dma4(const char* g, unsigned l16, unsigned dst) {
    asm volatile("s_mov_b32 m0, %2\n\ts_nop 0\n\tglobal_load_lds_dwordx4 %0, %1 sc1\n\tglobal_load_lds_dwordx4 %0, %1 offset:1024 sc1\n\t"
                 "global_load_lds_dwordx4 %0, %1 offset:2048 sc1\n\tglobal_load_lds_dwordx4 %0, %1 offset:3072 sc1" :: "v"(l16), "s"(g), "s"(dst) : "memory");
}
__device__ __forceinline__ unsigned toff(int row, int col, int par) {
    return pgb(row >> 5, par) + blin((unsigned)((row >> 4) & 1) * 20480u + (unsigned)(col >> 3) * 256u) + (unsigned)(row & 15) * 16u;
}

#define ASM_ST
#define ASM_ST_DECL
#define ASM_ST_OUT
#define ASM_ST_SAVE(k)
#define ASM_ST2
#define ASM_ST2_OUT
#define ASM_ST2_SAVE(k)
#define STDECL
#define ST(k)
#define STFLUSH(t)

constexpr unsigned L_MEM = 0;
constexpr unsigned L_Q = 10240;
constexpr unsigned L_KM = 13312;
constexpr unsigned L_QX = 16384;
constexpr unsigned L_KX = 28672;
constexpr unsigned L_XB = 40960;
constexpr unsigned L_XA = 49152;
constexpr unsigned L_W = 81920;
constexpr int MEGA_LDS = 163840;
constexpr int wi(int i, int j) { return (i >> 1) * 5 + ((i & 1) ? 2 + j : j - 1); }

#define RPUB2
#define WNT ""
#define ASM_WLOAD3 \
    "s_xor_b32 %[t], %[wx], 0\n\t" \
    "s_mul_i32 %[t], %[t], 0xc0000\n\t" \
    "buffer_load_dwordx4 v[200:203], %[o0], %[rs], %[t] offen" WNT "\n\t" \
    "buffer_load_dwordx4 v[0:3], %[o1], %[rs], %[t] offen" WNT "\n\t" \
    "buffer_load_dwordx4 v[8:11], %[o2], %[rs], %[t] offen" WNT "\n\t" \
    "s_add_u32 %[t], %[t], 0x18000\n\t" \
    "buffer_load_dwordx4 v[16:19], %[o0], %[rs], %[t] offen" WNT "\n\t" \
    "buffer_load_dwordx4 v[24:27], %[o1], %[rs], %[t] offen" WNT "\n\t" \
    "buffer_load_dwordx4 v[32:35], %[o2], %[rs], %[t] offen" WNT "\n\t" \
    "s_add_u32 %[t], %[t], 0x18000\n\t" \
    "buffer_load_dwordx4 v[208:211], %[o0], %[rs], %[t] offen" WNT "\n\t" \
    "buffer_load_dwordx4 v[40:43], %[o1], %[rs], %[t] offen" WNT "\n\t" \
    "buffer_load_dwordx4 v[48:51], %[o2], %[rs], %[t] offen" WNT "\n\t" \
    "s_add_u32 %[t], %[t], 0x18000\n\t" \
    "buffer_load_dwordx4 v[56:59], %[o0], %[rs], %[t] offen" WNT "\n\t" \
    "buffer_load_dwordx4 v[64:67], %[o1], %[rs], %[t] offen" WNT "\n\t" \
    "buffer_load_dwordx4 v[72:75], %[o2], %[rs], %[t] offen" WNT "\n\t" \
    "s_add_u32 %[t], %[t], 0x18000\n\t" \
    "buffer_load_dwordx4 v[204:207], %[o0], %[rs], %[t] offen" WNT "\n\t" \
    "buffer_load_dwordx4 v[4:7], %[o1], %[rs], %[t] offen" WNT "\n\t" \
    "buffer_load_dwordx4 v[12:15], %[o2], %[rs], %[t] offen" WNT "\n\t" \
    "s_add_u32 %[t], %[t], 0x18000\n\t" \
    "buffer_load_dwordx4 v[20:23], %[o0], %[rs], %[t] offen" WNT "\n\t" \
    "buffer_load_dwordx4 v[28:31], %[o1], %[rs], %[t] offen" WNT "\n\t" \
    "buffer_load_dwordx4 v[36:39], %[o2], %[rs], %[t] offen" WNT "\n\t" \
    "s_add_u32 %[t], %[t], 0x18000\n\t" \
    "buffer_load_dwordx4 v[212:215], %[o0], %[rs], %[t] offen" WNT "\n\t" \
    "buffer_load_dwordx4 v[44:47], %[o1], %[rs], %[t] offen" WNT "\n\t" \
    "buffer_load_dwordx4 v[52:55], %[o2], %[rs], %[t] offen" WNT "\n\t" \
    "s_add_u32 %[t], %[t], 0x18000\n\t" \
    "buffer_load_dwordx4 v[60:63], %[o0], %[rs], %[t] offen" WNT "\n\t" \
    "buffer_load_dwordx4 v[68:71], %[o1], %[rs], %[t] offen" WNT "\n\t" \
    "buffer_load_dwordx4 v[76:79], %[o2], %[rs], %[t] offen" WNT "\n\t" \
    "s_add_u32 %[t], %[t], 0x18000\n\t" \
    "s_waitcnt vmcnt(21)\n\t" \
    "ds_write_b128 %[s0], v[200:203] offset:0\n\t" \
    "ds_write_b128 %[s1], v[0:3] offset:0\n\t" \
    "ds_write_b128 %[s2], v[8:11] offset:0\n\t" \
    "s_waitcnt vmcnt(18)\n\t" \
    "ds_write_b128 %[s0], v[16:19] offset:3456\n\t" \
    "ds_write_b128 %[s1], v[24:27] offset:3456\n\t" \
    "ds_write_b128 %[s2], v[32:35] offset:3456\n\t" \
    "s_waitcnt vmcnt(15)\n\t" \
    "ds_write_b128 %[s0], v[208:211] offset:6912\n\t" \
    "ds_write_b128 %[s1], v[40:43] offset:6912\n\t" \
    "ds_write_b128 %[s2], v[48:51] offset:6912\n\t" \
    "s_waitcnt vmcnt(12)\n\t" \
    "ds_write_b128 %[s0], v[56:59] offset:10368\n\t" \
    "ds_write_b128 %[s1], v[64:67] offset:10368\n\t" \
    "ds_write_b128 %[s2], v[72:75] offset:10368\n\t" \
    "s_waitcnt lgkmcnt(0)\n\t" \
    "s_barrier \n\t" \
    "ds_read_b64_tr_b16 v[200:201], %[t0] offset:0\n\t" \
    "ds_read_b64_tr_b16 v[202:203], %[t0] offset:1664\n\t" \
    "ds_read_b64_tr_b16 v[0:1], %[t0] offset:128\n\t" \
    "ds_read_b64_tr_b16 v[2:3], %[t0] offset:1792\n\t" \
    "ds_read_b64_tr_b16 v[8:9], %[t0] offset:256\n\t" \
    "ds_read_b64_tr_b16 v[10:11], %[t0] offset:1920\n\t" \
    "ds_read_b64_tr_b16 v[16:17], %[t1] offset:0\n\t" \
    "ds_read_b64_tr_b16 v[18:19], %[t1] offset:1664\n\t" \
    "ds_read_b64_tr_b16 v[24:25], %[t1] offset:128\n\t" \
    "ds_read_b64_tr_b16 v[26:27], %[t1] offset:1792\n\t" \
    "ds_read_b64_tr_b16 v[32:33], %[t1] offset:256\n\t" \
    "ds_read_b64_tr_b16 v[34:35], %[t1] offset:1920\n\t" \
    "ds_read_b64_tr_b16 v[208:209], %[t2] offset:0\n\t" \
    "ds_read_b64_tr_b16 v[210:211], %[t2] offset:1664\n\t" \
    "ds_read_b64_tr_b16 v[40:41], %[t2] offset:128\n\t" \
    "ds_read_b64_tr_b16 v[42:43], %[t2] offset:1792\n\t" \
    "ds_read_b64_tr_b16 v[48:49], %[t2] offset:256\n\t" \
    "ds_read_b64_tr_b16 v[50:51], %[t2] offset:1920\n\t" \
    "ds_read_b64_tr_b16 v[56:57], %[t3] offset:0\n\t" \
    "ds_read_b64_tr_b16 v[58:59], %[t3] offset:1664\n\t" \
    "ds_read_b64_tr_b16 v[64:65], %[t3] offset:128\n\t" \
    "ds_read_b64_tr_b16 v[66:67], %[t3] offset:1792\n\t" \
    "ds_read_b64_tr_b16 v[72:73], %[t3] offset:256\n\t" \
    "ds_read_b64_tr_b16 v[74:75], %[t3] offset:1920\n\t" \
    "s_xor_b32 %[t], %[wx], 4\n\t" \
    "s_mul_i32 %[t], %[t], 0xc0000\n\t" \
    "buffer_load_dwordx4 v[216:219], %[o0], %[rs], %[t] offen" WNT "\n\t" \
    "buffer_load_dwordx4 v[80:83], %[o1], %[rs], %[t] offen" WNT "\n\t" \
    "buffer_load_dwordx4 v[88:91], %[o2], %[rs], %[t] offen" WNT "\n\t" \
    "s_add_u32 %[t], %[t], 0x18000\n\t" \
    "buffer_load_dwordx4 v[96:99], %[o0], %[rs], %[t] offen" WNT "\n\t" \
    "buffer_load_dwordx4 v[104:107], %[o1], %[rs], %[t] offen" WNT "\n\t" \
    "buffer_load_dwordx4 v[112:115], %[o2], %[rs], %[t] offen" WNT "\n\t" \
    "s_add_u32 %[t], %[t], 0x18000\n\t" \
    "buffer_load_dwordx4 v[224:227], %[o0], %[rs], %[t] offen" WNT "\n\t" \
    "buffer_load_dwordx4 v[120:123], %[o1], %[rs], %[t] offen" WNT "\n\t" \
    "buffer_load_dwordx4 v[128:131], %[o2], %[rs], %[t] offen" WNT "\n\t" \
    "s_add_u32 %[t], %[t], 0x18000\n\t" \
    "buffer_load_dwordx4 v[136:139], %[o0], %[rs], %[t] offen" WNT "\n\t" \
    "buffer_load_dwordx4 v[144:147], %[o1], %[rs], %[t] offen" WNT "\n\t" \
    "buffer_load_dwordx4 v[152:155], %[o2], %[rs], %[t] offen" WNT "\n\t" \
    "s_add_u32 %[t], %[t], 0x18000\n\t" \
    "s_waitcnt lgkmcnt(0)\n\t" \
    "s_barrier \n\t" \
    "s_waitcnt vmcnt(21)\n\t" \
    "ds_write_b128 %[s0], v[204:207] offset:0\n\t" \
    "ds_write_b128 %[s1], v[4:7] offset:0\n\t" \
    "ds_write_b128 %[s2], v[12:15] offset:0\n\t" \
    "s_waitcnt vmcnt(18)\n\t" \
    "ds_write_b128 %[s0], v[20:23] offset:3456\n\t" \
    "ds_write_b128 %[s1], v[28:31] offset:3456\n\t" \
    "ds_write_b128 %[s2], v[36:39] offset:3456\n\t" \
    "s_waitcnt vmcnt(15)\n\t" \
    "ds_write_b128 %[s0], v[212:215] offset:6912\n\t" \
    "ds_write_b128 %[s1], v[44:47] offset:6912\n\t" \
    "ds_write_b128 %[s2], v[52:55] offset:6912\n\t" \
    "s_waitcnt vmcnt(12)\n\t" \
    "ds_write_b128 %[s0], v[60:63] offset:10368\n\t" \
    "ds_write_b128 %[s1], v[68:71] offset:10368\n\t" \
    "ds_write_b128 %[s2], v[76:79] offset:10368\n\t" \
    "s_waitcnt lgkmcnt(0)\n\t" \
    "s_barrier \n\t" \
    "ds_read_b64_tr_b16 v[204:205], %[t0] offset:0\n\t" \
    "ds_read_b64_tr_b16 v[206:207], %[t0] offset:1664\n\t" \
    "ds_read_b64_tr_b16 v[4:5], %[t0] offset:128\n\t" \
    "ds_read_b64_tr_b16 v[6:7], %[t0] offset:1792\n\t" \
    "ds_read_b64_tr_b16 v[12:13], %[t0] offset:256\n\t" \
    "ds_read_b64_tr_b16 v[14:15], %[t0] offset:1920\n\t" \
    "ds_read_b64_tr_b16 v[20:21], %[t1] offset:0\n\t" \
    "ds_read_b64_tr_b16 v[22:23], %[t1] offset:1664\n\t" \
    "ds_read_b64_tr_b16 v[28:29], %[t1] offset:128\n\t" \
    "ds_read_b64_tr_b16 v[30:31], %[t1] offset:1792\n\t" \
    "ds_read_b64_tr_b16 v[36:37], %[t1] offset:256\n\t" \
    "ds_read_b64_tr_b16 v[38:39], %[t1] offset:1920\n\t" \
    "ds_read_b64_tr_b16 v[212:213], %[t2] offset:0\n\t" \
    "ds_read_b64_tr_b16 v[214:215], %[t2] offset:1664\n\t" \
    "ds_read_b64_tr_b16 v[44:45], %[t2] offset:128\n\t" \
    "ds_read_b64_tr_b16 v[46:47], %[t2] offset:1792\n\t" \
    "ds_read_b64_tr_b16 v[52:53], %[t2] offset:256\n\t" \
    "ds_read_b64_tr_b16 v[54:55], %[t2] offset:1920\n\t" \
    "ds_read_b64_tr_b16 v[60:61], %[t3] offset:0\n\t" \
    "ds_read_b64_tr_b16 v[62:63], %[t3] offset:1664\n\t" \
    "ds_read_b64_tr_b16 v[68:69], %[t3] offset:128\n\t" \
    "ds_read_b64_tr_b16 v[70:71], %[t3] offset:1792\n\t" \
    "ds_read_b64_tr_b16 v[76:77], %[t3] offset:256\n\t" \
    "ds_read_b64_tr_b16 v[78:79], %[t3] offset:1920\n\t" \
    "buffer_load_dwordx4 v[220:223], %[o0], %[rs], %[t] offen" WNT "\n\t" \
    "buffer_load_dwordx4 v[84:87], %[o1], %[rs], %[t] offen" WNT "\n\t" \
    "buffer_load_dwordx4 v[92:95], %[o2], %[rs], %[t] offen" WNT "\n\t" \
    "s_add_u32 %[t], %[t], 0x18000\n\t" \
    "buffer_load_dwordx4 v[100:103], %[o0], %[rs], %[t] offen" WNT "\n\t" \
    "buffer_load_dwordx4 v[108:111], %[o1], %[rs], %[t] offen" WNT "\n\t" \
    "buffer_load_dwordx4 v[116:119], %[o2], %[rs], %[t] offen" WNT "\n\t" \
    "s_add_u32 %[t], %[t], 0x18000\n\t" \
    "buffer_load_dwordx4 v[228:231], %[o0], %[rs], %[t] offen" WNT "\n\t" \
    "buffer_load_dwordx4 v[124:127], %[o1], %[rs], %[t] offen" WNT "\n\t" \
    "buffer_load_dwordx4 v[132:135], %[o2], %[rs], %[t] offen" WNT "\n\t" \
    "s_add_u32 %[t], %[t], 0x18000\n\t" \
    "buffer_load_dwordx4 v[140:143], %[o0], %[rs], %[t] offen" WNT "\n\t" \
    "buffer_load_dwordx4 v[148:151], %[o1], %[rs], %[t] offen" WNT "\n\t" \
    "buffer_load_dwordx4 v[156:159], %[o2], %[rs], %[t] offen" WNT "\n\t" \
    "s_add_u32 %[t], %[t], 0x18000\n\t" \
    "s_waitcnt lgkmcnt(0)\n\t" \
    "s_barrier \n\t" \
    "s_waitcnt vmcnt(21)\n\t" \
    "ds_write_b128 %[s0], v[216:219] offset:0\n\t" \
    "ds_write_b128 %[s1], v[80:83] offset:0\n\t" \
    "ds_write_b128 %[s2], v[88:91] offset:0\n\t" \
    "s_waitcnt vmcnt(18)\n\t" \
    "ds_write_b128 %[s0], v[96:99] offset:3456\n\t" \
    "ds_write_b128 %[s1], v[104:107] offset:3456\n\t" \
    "ds_write_b128 %[s2], v[112:115] offset:3456\n\t" \
    "s_waitcnt vmcnt(15)\n\t" \
    "ds_write_b128 %[s0], v[224:227] offset:6912\n\t" \
    "ds_write_b128 %[s1], v[120:123] offset:6912\n\t" \
    "ds_write_b128 %[s2], v[128:131] offset:6912\n\t" \
    "s_waitcnt vmcnt(12)\n\t" \
    "ds_write_b128 %[s0], v[136:139] offset:10368\n\t" \
    "ds_write_b128 %[s1], v[144:147] offset:10368\n\t" \
    "ds_write_b128 %[s2], v[152:155] offset:10368\n\t" \
    "s_waitcnt lgkmcnt(0)\n\t" \
    "s_barrier \n\t" \
    "ds_read_b64_tr_b16 v[216:217], %[t0] offset:0\n\t" \
    "ds_read_b64_tr_b16 v[218:219], %[t0] offset:1664\n\t" \
    "ds_read_b64_tr_b16 v[80:81], %[t0] offset:128\n\t" \
    "ds_read_b64_tr_b16 v[82:83], %[t0] offset:1792\n\t" \
    "ds_read_b64_tr_b16 v[88:89], %[t0] offset:256\n\t" \
    "ds_read_b64_tr_b16 v[90:91], %[t0] offset:1920\n\t" \
    "ds_read_b64_tr_b16 v[96:97], %[t1] offset:0\n\t" \
    "ds_read_b64_tr_b16 v[98:99], %[t1] offset:1664\n\t" \
    "ds_read_b64_tr_b16 v[104:105], %[t1] offset:128\n\t" \
    "ds_read_b64_tr_b16 v[106:107], %[t1] offset:1792\n\t" \
    "ds_read_b64_tr_b16 v[112:113], %[t1] offset:256\n\t" \
    "ds_read_b64_tr_b16 v[114:115], %[t1] offset:1920\n\t" \
    "ds_read_b64_tr_b16 v[224:225], %[t2] offset:0\n\t" \
    "ds_read_b64_tr_b16 v[226:227], %[t2] offset:1664\n\t" \
    "ds_read_b64_tr_b16 v[120:121], %[t2] offset:128\n\t" \
    "ds_read_b64_tr_b16 v[122:123], %[t2] offset:1792\n\t" \
    "ds_read_b64_tr_b16 v[128:129], %[t2] offset:256\n\t" \
    "ds_read_b64_tr_b16 v[130:131], %[t2] offset:1920\n\t" \
    "ds_read_b64_tr_b16 v[136:137], %[t3] offset:0\n\t" \
    "ds_read_b64_tr_b16 v[138:139], %[t3] offset:1664\n\t" \
    "ds_read_b64_tr_b16 v[144:145], %[t3] offset:128\n\t" \
    "ds_read_b64_tr_b16 v[146:147], %[t3] offset:1792\n\t" \
    "ds_read_b64_tr_b16 v[152:153], %[t3] offset:256\n\t" \
    "ds_read_b64_tr_b16 v[154:155], %[t3] offset:1920\n\t" \
    "s_and_b32 %[t], %[wx], 3\n\t" \
    "s_mul_i32 %[t], %[t], 0x60000\n\t" \
    "s_add_u32 %[t], %[t], 0x600000\n\t" \
    "buffer_load_dwordx4 v[232:235], %[o0], %[rs], %[t] offen" WNT "\n\t" \
    "buffer_load_dwordx4 v[160:163], %[o1], %[rs], %[t] offen" WNT "\n\t" \
    "buffer_load_dwordx4 v[168:171], %[o2], %[rs], %[t] offen" WNT "\n\t" \
    "s_add_u32 %[t], %[t], 0x18000\n\t" \
    "buffer_load_dwordx4 v[236:239], %[o0], %[rs], %[t] offen" WNT "\n\t" \
    "buffer_load_dwordx4 v[164:167], %[o1], %[rs], %[t] offen" WNT "\n\t" \
    "buffer_load_dwordx4 v[172:175], %[o2], %[rs], %[t] offen" WNT "\n\t" \
    "s_add_u32 %[t], %[t], 0x18000\n\t" \
    "buffer_load_dwordx4 v[176:179], %[o0], %[rs], %[t] offen" WNT "\n\t" \
    "buffer_load_dwordx4 v[184:187], %[o1], %[rs], %[t] offen" WNT "\n\t" \
    "buffer_load_dwordx4 v[192:195], %[o2], %[rs], %[t] offen" WNT "\n\t" \
    "s_add_u32 %[t], %[t], 0x18000\n\t" \
    "buffer_load_dwordx4 v[180:183], %[o0], %[rs], %[t] offen" WNT "\n\t" \
    "buffer_load_dwordx4 v[188:191], %[o1], %[rs], %[t] offen" WNT "\n\t" \
    "buffer_load_dwordx4 v[196:199], %[o2], %[rs], %[t] offen" WNT "\n\t" \
    "s_add_u32 %[t], %[t], 0x18000\n\t" \
    "s_waitcnt lgkmcnt(0)\n\t" \
    "s_barrier \n\t" \
    "s_waitcnt vmcnt(21)\n\t" \
    "ds_write_b128 %[s0], v[220:223] offset:0\n\t" \
    "ds_write_b128 %[s1], v[84:87] offset:0\n\t" \
    "ds_write_b128 %[s2], v[92:95] offset:0\n\t" \
    "s_waitcnt vmcnt(18)\n\t" \
    "ds_write_b128 %[s0], v[100:103] offset:3456\n\t" \
    "ds_write_b128 %[s1], v[108:111] offset:3456\n\t" \
    "ds_write_b128 %[s2], v[116:119] offset:3456\n\t" \
    "s_waitcnt vmcnt(15)\n\t" \
    "ds_write_b128 %[s0], v[228:231] offset:6912\n\t" \
    "ds_write_b128 %[s1], v[124:127] offset:6912\n\t" \
    "ds_write_b128 %[s2], v[132:135] offset:6912\n\t" \
    "s_waitcnt vmcnt(12)\n\t" \
    "ds_write_b128 %[s0], v[140:143] offset:10368\n\t" \
    "ds_write_b128 %[s1], v[148:151] offset:10368\n\t" \
    "ds_write_b128 %[s2], v[156:159] offset:10368\n\t" \
    "s_waitcnt lgkmcnt(0)\n\t" \
    "s_barrier \n\t" \
    "ds_read_b64_tr_b16 v[220:221], %[t0] offset:0\n\t" \
    "ds_read_b64_tr_b16 v[222:223], %[t0] offset:1664\n\t" \
    "ds_read_b64_tr_b16 v[84:85], %[t0] offset:128\n\t" \
    "ds_read_b64_tr_b16 v[86:87], %[t0] offset:1792\n\t" \
    "ds_read_b64_tr_b16 v[92:93], %[t0] offset:256\n\t" \
    "ds_read_b64_tr_b16 v[94:95], %[t0] offset:1920\n\t" \
    "ds_read_b64_tr_b16 v[100:101], %[t1] offset:0\n\t" \
    "ds_read_b64_tr_b16 v[102:103], %[t1] offset:1664\n\t" \
    "ds_read_b64_tr_b16 v[108:109], %[t1] offset:128\n\t" \
    "ds_read_b64_tr_b16 v[110:111], %[t1] offset:1792\n\t" \
    "ds_read_b64_tr_b16 v[116:117], %[t1] offset:256\n\t" \
    "ds_read_b64_tr_b16 v[118:119], %[t1] offset:1920\n\t" \
    "ds_read_b64_tr_b16 v[228:229], %[t2] offset:0\n\t" \
    "ds_read_b64_tr_b16 v[230:231], %[t2] offset:1664\n\t" \
    "ds_read_b64_tr_b16 v[124:125], %[t2] offset:128\n\t" \
    "ds_read_b64_tr_b16 v[126:127], %[t2] offset:1792\n\t" \
    "ds_read_b64_tr_b16 v[132:133], %[t2] offset:256\n\t" \
    "ds_read_b64_tr_b16 v[134:135], %[t2] offset:1920\n\t" \
    "ds_read_b64_tr_b16 v[140:141], %[t3] offset:0\n\t" \
    "ds_read_b64_tr_b16 v[142:143], %[t3] offset:1664\n\t" \
    "ds_read_b64_tr_b16 v[148:149], %[t3] offset:128\n\t" \
    "ds_read_b64_tr_b16 v[150:151], %[t3] offset:1792\n\t" \
    "ds_read_b64_tr_b16 v[156:157], %[t3] offset:256\n\t" \
    "ds_read_b64_tr_b16 v[158:159], %[t3] offset:1920\n\t" \
    "s_waitcnt lgkmcnt(0)\n\t" \
    "s_barrier \n\t" \
    "s_waitcnt vmcnt(9)\n\t" \
    "ds_write_b128 %[s0], v[232:235] offset:0\n\t" \
    "ds_write_b128 %[s1], v[160:163] offset:0\n\t" \
    "ds_write_b128 %[s2], v[168:171] offset:0\n\t" \
    "s_waitcnt vmcnt(6)\n\t" \
    "ds_write_b128 %[s0], v[236:239] offset:3456\n\t" \
    "ds_write_b128 %[s1], v[164:167] offset:3456\n\t" \
    "ds_write_b128 %[s2], v[172:175] offset:3456\n\t" \
    "s_waitcnt vmcnt(3)\n\t" \
    "ds_write_b128 %[s0], v[176:179] offset:6912\n\t" \
    "ds_write_b128 %[s1], v[184:187] offset:6912\n\t" \
    "ds_write_b128 %[s2], v[192:195] offset:6912\n\t" \
    "s_waitcnt vmcnt(0)\n\t" \
    "ds_write_b128 %[s0], v[180:183] offset:10368\n\t" \
    "ds_write_b128 %[s1], v[188:191] offset:10368\n\t" \
    "ds_write_b128 %[s2], v[196:199] offset:10368\n\t" \
    "s_waitcnt lgkmcnt(0)\n\t" \
    "s_barrier \n\t" \
    "ds_read_b64_tr_b16 v[232:233], %[t4] offset:0\n\t" \
    "ds_read_b64_tr_b16 v[234:235], %[t4] offset:1664\n\t" \
    "ds_read_b64_tr_b16 v[160:161], %[t4] offset:128\n\t" \
    "ds_read_b64_tr_b16 v[162:163], %[t4] offset:1792\n\t" \
    "ds_read_b64_tr_b16 v[168:169], %[t4] offset:256\n\t" \
    "ds_read_b64_tr_b16 v[170:171], %[t4] offset:1920\n\t" \
    "ds_read_b64_tr_b16 v[236:237], %[t4] offset:13824\n\t" \
    "ds_read_b64_tr_b16 v[238:239], %[t4] offset:15488\n\t" \
    "ds_read_b64_tr_b16 v[164:165], %[t4] offset:13952\n\t" \
    "ds_read_b64_tr_b16 v[166:167], %[t4] offset:15616\n\t" \
    "ds_read_b64_tr_b16 v[172:173], %[t4] offset:14080\n\t" \
    "ds_read_b64_tr_b16 v[174:175], %[t4] offset:15744\n\t" \
    "ds_read_b64_tr_b16 v[176:177], %[t4] offset:27648\n\t" \
    "ds_read_b64_tr_b16 v[178:179], %[t4] offset:29312\n\t" \
    "ds_read_b64_tr_b16 v[184:185], %[t4] offset:27776\n\t" \
    "ds_read_b64_tr_b16 v[186:187], %[t4] offset:29440\n\t" \
    "ds_read_b64_tr_b16 v[192:193], %[t4] offset:27904\n\t" \
    "ds_read_b64_tr_b16 v[194:195], %[t4] offset:29568\n\t" \
    "ds_read_b64_tr_b16 v[180:181], %[t4] offset:41472\n\t" \
    "ds_read_b64_tr_b16 v[182:183], %[t4] offset:43136\n\t" \
    "ds_read_b64_tr_b16 v[188:189], %[t4] offset:41600\n\t" \
    "ds_read_b64_tr_b16 v[190:191], %[t4] offset:43264\n\t" \
    "ds_read_b64_tr_b16 v[196:197], %[t4] offset:41728\n\t" \
    "ds_read_b64_tr_b16 v[198:199], %[t4] offset:43392\n\t" \
    "s_waitcnt lgkmcnt(0)\n\t" \
    "s_barrier \n\t" \
    "ds_write_b128 %[wl], v[200:203] offset:0\n\t" \
    "ds_write_b128 %[wl], v[204:207] offset:1024\n\t" \
    "ds_write_b128 %[wl], v[208:211] offset:2048\n\t" \
    "ds_write_b128 %[wl], v[212:215] offset:3072\n\t" \
    "ds_write_b128 %[wl], v[216:219] offset:4096\n\t" \
    "ds_write_b128 %[wl], v[220:223] offset:5120\n\t" \
    "ds_write_b128 %[wl], v[224:227] offset:6144\n\t" \
    "ds_write_b128 %[wl], v[228:231] offset:7168\n\t" \
    "ds_write_b128 %[wl], v[232:235] offset:8192\n\t" \
    "ds_write_b128 %[wl], v[236:239] offset:9216\n\t" \
    "s_waitcnt lgkmcnt(0)\n\t"
#define ASM_WLOADO \
    "s_mov_b32 %[t], 0x0\n\t" \
    "buffer_load_dwordx4 v[200:203], %[o0], %[rs], %[t] offen" WNT "\n\t" \
    "buffer_load_dwordx4 v[16:19], %[o1], %[rs], %[t] offen" WNT "\n\t" \
    "buffer_load_dwordx4 v[208:211], %[o2], %[rs], %[t] offen" WNT "\n\t" \
    "buffer_load_dwordx4 v[56:59], %[o3], %[rs], %[t] offen" WNT "\n\t" \
    "buffer_load_dwordx4 v[216:219], %[o4], %[rs], %[t] offen" WNT "\n\t" \
    "s_mov_b32 %[t], 0xa000\n\t" \
    "buffer_load_dwordx4 v[96:99], %[o0], %[rs], %[t] offen" WNT "\n\t" \
    "buffer_load_dwordx4 v[224:227], %[o1], %[rs], %[t] offen" WNT "\n\t" \
    "buffer_load_dwordx4 v[136:139], %[o2], %[rs], %[t] offen" WNT "\n\t" \
    "buffer_load_dwordx4 v[232:235], %[o3], %[rs], %[t] offen" WNT "\n\t" \
    "buffer_load_dwordx4 v[176:179], %[o4], %[rs], %[t] offen" WNT "\n\t" \
    "s_mov_b32 %[t], 0x14000\n\t" \
    "buffer_load_dwordx4 v[204:207], %[o0], %[rs], %[t] offen" WNT "\n\t" \
    "buffer_load_dwordx4 v[20:23], %[o1], %[rs], %[t] offen" WNT "\n\t" \
    "buffer_load_dwordx4 v[212:215], %[o2], %[rs], %[t] offen" WNT "\n\t" \
    "buffer_load_dwordx4 v[60:63], %[o3], %[rs], %[t] offen" WNT "\n\t" \
    "buffer_load_dwordx4 v[220:223], %[o4], %[rs], %[t] offen" WNT "\n\t" \
    "s_mov_b32 %[t], 0x1e000\n\t" \
    "buffer_load_dwordx4 v[100:103], %[o0], %[rs], %[t] offen" WNT "\n\t" \
    "buffer_load_dwordx4 v[228:231], %[o1], %[rs], %[t] offen" WNT "\n\t" \
    "buffer_load_dwordx4 v[140:143], %[o2], %[rs], %[t] offen" WNT "\n\t" \
    "buffer_load_dwordx4 v[236:239], %[o3], %[rs], %[t] offen" WNT "\n\t" \
    "buffer_load_dwordx4 v[180:183], %[o4], %[rs], %[t] offen" WNT "\n\t" \
    "s_waitcnt vmcnt(15)\n\t" \
    "ds_write_b128 %[s0], v[200:203] offset:0\n\t" \
    "ds_write_b128 %[s1], v[16:19] offset:0\n\t" \
    "ds_write_b128 %[s2], v[208:211] offset:0\n\t" \
    "ds_write_b128 %[s3], v[56:59] offset:0\n\t" \
    "ds_write_b128 %[s4], v[216:219] offset:0\n\t" \
    "s_waitcnt vmcnt(10)\n\t" \
    "ds_write_b128 %[s0], v[96:99] offset:5632\n\t" \
    "ds_write_b128 %[s1], v[224:227] offset:5632\n\t" \
    "ds_write_b128 %[s2], v[136:139] offset:5632\n\t" \
    "ds_write_b128 %[s3], v[232:235] offset:5632\n\t" \
    "ds_write_b128 %[s4], v[176:179] offset:5632\n\t" \
    "ds_read_b64_tr_b16 v[200:201], %[tr] offset:0\n\t" \
    "ds_read_b64_tr_b16 v[202:203], %[tr] offset:5632\n\t" \
    "ds_read_b64_tr_b16 v[16:17], %[tr] offset:32\n\t" \
    "ds_read_b64_tr_b16 v[18:19], %[tr] offset:5664\n\t" \
    "ds_read_b64_tr_b16 v[208:209], %[tr] offset:64\n\t" \
    "ds_read_b64_tr_b16 v[210:211], %[tr] offset:5696\n\t" \
    "ds_read_b64_tr_b16 v[56:57], %[tr] offset:96\n\t" \
    "ds_read_b64_tr_b16 v[58:59], %[tr] offset:5728\n\t" \
    "ds_read_b64_tr_b16 v[216:217], %[tr] offset:128\n\t" \
    "ds_read_b64_tr_b16 v[218:219], %[tr] offset:5760\n\t" \
    "ds_read_b64_tr_b16 v[96:97], %[tr] offset:160\n\t" \
    "ds_read_b64_tr_b16 v[98:99], %[tr] offset:5792\n\t" \
    "ds_read_b64_tr_b16 v[224:225], %[tr] offset:192\n\t" \
    "ds_read_b64_tr_b16 v[226:227], %[tr] offset:5824\n\t" \
    "ds_read_b64_tr_b16 v[136:137], %[tr] offset:224\n\t" \
    "ds_read_b64_tr_b16 v[138:139], %[tr] offset:5856\n\t" \
    "ds_read_b64_tr_b16 v[232:233], %[tr] offset:256\n\t" \
    "ds_read_b64_tr_b16 v[234:235], %[tr] offset:5888\n\t" \
    "ds_read_b64_tr_b16 v[176:177], %[tr] offset:288\n\t" \
    "ds_read_b64_tr_b16 v[178:179], %[tr] offset:5920\n\t" \
    "s_barrier \n\t" \
    "s_mov_b32 %[t], 0x28000\n\t" \
    "buffer_load_dwordx4 v[0:3], %[o0], %[rs], %[t] offen" WNT "\n\t" \
    "buffer_load_dwordx4 v[24:27], %[o1], %[rs], %[t] offen" WNT "\n\t" \
    "buffer_load_dwordx4 v[40:43], %[o2], %[rs], %[t] offen" WNT "\n\t" \
    "buffer_load_dwordx4 v[64:67], %[o3], %[rs], %[t] offen" WNT "\n\t" \
    "buffer_load_dwordx4 v[80:83], %[o4], %[rs], %[t] offen" WNT "\n\t" \
    "s_mov_b32 %[t], 0x32000\n\t" \
    "buffer_load_dwordx4 v[104:107], %[o0], %[rs], %[t] offen" WNT "\n\t" \
    "buffer_load_dwordx4 v[120:123], %[o1], %[rs], %[t] offen" WNT "\n\t" \
    "buffer_load_dwordx4 v[144:147], %[o2], %[rs], %[t] offen" WNT "\n\t" \
    "buffer_load_dwordx4 v[160:163], %[o3], %[rs], %[t] offen" WNT "\n\t" \
    "buffer_load_dwordx4 v[184:187], %[o4], %[rs], %[t] offen" WNT "\n\t" \
    "s_waitcnt lgkmcnt(0)\n\t" \
    "s_barrier \n\t" \
    "s_waitcnt vmcnt(15)\n\t" \
    "ds_write_b128 %[s0], v[204:207] offset:0\n\t" \
    "ds_write_b128 %[s1], v[20:23] offset:0\n\t" \
    "ds_write_b128 %[s2], v[212:215] offset:0\n\t" \
    "ds_write_b128 %[s3], v[60:63] offset:0\n\t" \
    "ds_write_b128 %[s4], v[220:223] offset:0\n\t" \
    "s_waitcnt vmcnt(10)\n\t" \
    "ds_write_b128 %[s0], v[100:103] offset:5632\n\t" \
    "ds_write_b128 %[s1], v[228:231] offset:5632\n\t" \
    "ds_write_b128 %[s2], v[140:143] offset:5632\n\t" \
    "ds_write_b128 %[s3], v[236:239] offset:5632\n\t" \
    "ds_write_b128 %[s4], v[180:183] offset:5632\n\t" \
    "ds_read_b64_tr_b16 v[204:205], %[tr] offset:0\n\t" \
    "ds_read_b64_tr_b16 v[206:207], %[tr] offset:5632\n\t" \
    "ds_read_b64_tr_b16 v[20:21], %[tr] offset:32\n\t" \
    "ds_read_b64_tr_b16 v[22:23], %[tr] offset:5664\n\t" \
    "ds_read_b64_tr_b16 v[212:213], %[tr] offset:64\n\t" \
    "ds_read_b64_tr_b16 v[214:215], %[tr] offset:5696\n\t" \
    "ds_read_b64_tr_b16 v[60:61], %[tr] offset:96\n\t" \
    "ds_read_b64_tr_b16 v[62:63], %[tr] offset:5728\n\t" \
    "ds_read_b64_tr_b16 v[220:221], %[tr] offset:128\n\t" \
    "ds_read_b64_tr_b16 v[222:223], %[tr] offset:5760\n\t" \
    "ds_read_b64_tr_b16 v[100:101], %[tr] offset:160\n\t" \
    "ds_read_b64_tr_b16 v[102:103], %[tr] offset:5792\n\t" \
    "ds_read_b64_tr_b16 v[228:229], %[tr] offset:192\n\t" \
    "ds_read_b64_tr_b16 v[230:231], %[tr] offset:5824\n\t" \
    "ds_read_b64_tr_b16 v[140:141], %[tr] offset:224\n\t" \
    "ds_read_b64_tr_b16 v[142:143], %[tr] offset:5856\n\t" \
    "ds_read_b64_tr_b16 v[236:237], %[tr] offset:256\n\t" \
    "ds_read_b64_tr_b16 v[238:239], %[tr] offset:5888\n\t" \
    "ds_read_b64_tr_b16 v[180:181], %[tr] offset:288\n\t" \
    "ds_read_b64_tr_b16 v[182:183], %[tr] offset:5920\n\t" \
    "s_barrier \n\t" \
    "s_mov_b32 %[t], 0x5000\n\t" \
    "buffer_load_dwordx4 v[4:7], %[o0], %[rs], %[t] offen" WNT "\n\t" \
    "buffer_load_dwordx4 v[28:31], %[o1], %[rs], %[t] offen" WNT "\n\t" \
    "buffer_load_dwordx4 v[44:47], %[o2], %[rs], %[t] offen" WNT "\n\t" \
    "buffer_load_dwordx4 v[68:71], %[o3], %[rs], %[t] offen" WNT "\n\t" \
    "buffer_load_dwordx4 v[84:87], %[o4], %[rs], %[t] offen" WNT "\n\t" \
    "s_mov_b32 %[t], 0xf000\n\t" \
    "buffer_load_dwordx4 v[108:111], %[o0], %[rs], %[t] offen" WNT "\n\t" \
    "buffer_load_dwordx4 v[124:127], %[o1], %[rs], %[t] offen" WNT "\n\t" \
    "buffer_load_dwordx4 v[148:151], %[o2], %[rs], %[t] offen" WNT "\n\t" \
    "buffer_load_dwordx4 v[164:167], %[o3], %[rs], %[t] offen" WNT "\n\t" \
    "buffer_load_dwordx4 v[188:191], %[o4], %[rs], %[t] offen" WNT "\n\t" \
    "s_waitcnt lgkmcnt(0)\n\t" \
    "s_barrier \n\t" \
    "s_waitcnt vmcnt(15)\n\t" \
    "ds_write_b128 %[s0], v[0:3] offset:0\n\t" \
    "ds_write_b128 %[s1], v[24:27] offset:0\n\t" \
    "ds_write_b128 %[s2], v[40:43] offset:0\n\t" \
    "ds_write_b128 %[s3], v[64:67] offset:0\n\t" \
    "ds_write_b128 %[s4], v[80:83] offset:0\n\t" \
    "s_waitcnt vmcnt(10)\n\t" \
    "ds_write_b128 %[s0], v[104:107] offset:5632\n\t" \
    "ds_write_b128 %[s1], v[120:123] offset:5632\n\t" \
    "ds_write_b128 %[s2], v[144:147] offset:5632\n\t" \
    "ds_write_b128 %[s3], v[160:163] offset:5632\n\t" \
    "ds_write_b128 %[s4], v[184:187] offset:5632\n\t" \
    "ds_read_b64_tr_b16 v[0:1], %[tr] offset:0\n\t" \
    "ds_read_b64_tr_b16 v[2:3], %[tr] offset:5632\n\t" \
    "ds_read_b64_tr_b16 v[24:25], %[tr] offset:32\n\t" \
    "ds_read_b64_tr_b16 v[26:27], %[tr] offset:5664\n\t" \
    "ds_read_b64_tr_b16 v[40:41], %[tr] offset:64\n\t" \
    "ds_read_b64_tr_b16 v[42:43], %[tr] offset:5696\n\t" \
    "ds_read_b64_tr_b16 v[64:65], %[tr] offset:96\n\t" \
    "ds_read_b64_tr_b16 v[66:67], %[tr] offset:5728\n\t" \
    "ds_read_b64_tr_b16 v[80:81], %[tr] offset:128\n\t" \
    "ds_read_b64_tr_b16 v[82:83], %[tr] offset:5760\n\t" \
    "ds_read_b64_tr_b16 v[104:105], %[tr] offset:160\n\t" \
    "ds_read_b64_tr_b16 v[106:107], %[tr] offset:5792\n\t" \
    "ds_read_b64_tr_b16 v[120:121], %[tr] offset:192\n\t" \
    "ds_read_b64_tr_b16 v[122:123], %[tr] offset:5824\n\t" \
    "ds_read_b64_tr_b16 v[144:145], %[tr] offset:224\n\t" \
    "ds_read_b64_tr_b16 v[146:147], %[tr] offset:5856\n\t" \
    "ds_read_b64_tr_b16 v[160:161], %[tr] offset:256\n\t" \
    "ds_read_b64_tr_b16 v[162:163], %[tr] offset:5888\n\t" \
    "ds_read_b64_tr_b16 v[184:185], %[tr] offset:288\n\t" \
    "ds_read_b64_tr_b16 v[186:187], %[tr] offset:5920\n\t" \
    "s_barrier \n\t" \
    "s_mov_b32 %[t], 0x19000\n\t" \
    "buffer_load_dwordx4 v[8:11], %[o0], %[rs], %[t] offen" WNT "\n\t" \
    "buffer_load_dwordx4 v[32:35], %[o1], %[rs], %[t] offen" WNT "\n\t" \
    "buffer_load_dwordx4 v[48:51], %[o2], %[rs], %[t] offen" WNT "\n\t" \
    "buffer_load_dwordx4 v[72:75], %[o3], %[rs], %[t] offen" WNT "\n\t" \
    "buffer_load_dwordx4 v[88:91], %[o4], %[rs], %[t] offen" WNT "\n\t" \
    "s_mov_b32 %[t], 0x23000\n\t" \
    "buffer_load_dwordx4 v[112:115], %[o0], %[rs], %[t] offen" WNT "\n\t" \
    "buffer_load_dwordx4 v[128:131], %[o1], %[rs], %[t] offen" WNT "\n\t" \
    "buffer_load_dwordx4 v[152:155], %[o2], %[rs], %[t] offen" WNT "\n\t" \
    "buffer_load_dwordx4 v[168:171], %[o3], %[rs], %[t] offen" WNT "\n\t" \
    "buffer_load_dwordx4 v[192:195], %[o4], %[rs], %[t] offen" WNT "\n\t" \
    "s_waitcnt lgkmcnt(0)\n\t" \
    "s_barrier \n\t" \
    "s_waitcnt vmcnt(15)\n\t" \
    "ds_write_b128 %[s0], v[4:7] offset:0\n\t" \
    "ds_write_b128 %[s1], v[28:31] offset:0\n\t" \
    "ds_write_b128 %[s2], v[44:47] offset:0\n\t" \
    "ds_write_b128 %[s3], v[68:71] offset:0\n\t" \
    "ds_write_b128 %[s4], v[84:87] offset:0\n\t" \
    "s_waitcnt vmcnt(10)\n\t" \
    "ds_write_b128 %[s0], v[108:111] offset:5632\n\t" \
    "ds_write_b128 %[s1], v[124:127] offset:5632\n\t" \
    "ds_write_b128 %[s2], v[148:151] offset:5632\n\t" \
    "ds_write_b128 %[s3], v[164:167] offset:5632\n\t" \
    "ds_write_b128 %[s4], v[188:191] offset:5632\n\t" \
    "ds_read_b64_tr_b16 v[4:5], %[tr] offset:0\n\t" \
    "ds_read_b64_tr_b16 v[6:7], %[tr] offset:5632\n\t" \
    "ds_read_b64_tr_b16 v[28:29], %[tr] offset:32\n\t" \
    "ds_read_b64_tr_b16 v[30:31], %[tr] offset:5664\n\t" \
    "ds_read_b64_tr_b16 v[44:45], %[tr] offset:64\n\t" \
    "ds_read_b64_tr_b16 v[46:47], %[tr] offset:5696\n\t" \
    "ds_read_b64_tr_b16 v[68:69], %[tr] offset:96\n\t" \
    "ds_read_b64_tr_b16 v[70:71], %[tr] offset:5728\n\t" \
    "ds_read_b64_tr_b16 v[84:85], %[tr] offset:128\n\t" \
    "ds_read_b64_tr_b16 v[86:87], %[tr] offset:5760\n\t" \
    "ds_read_b64_tr_b16 v[108:109], %[tr] offset:160\n\t" \
    "ds_read_b64_tr_b16 v[110:111], %[tr] offset:5792\n\t" \
    "ds_read_b64_tr_b16 v[124:125], %[tr] offset:192\n\t" \
    "ds_read_b64_tr_b16 v[126:127], %[tr] offset:5824\n\t" \
    "ds_read_b64_tr_b16 v[148:149], %[tr] offset:224\n\t" \
    "ds_read_b64_tr_b16 v[150:151], %[tr] offset:5856\n\t" \
    "ds_read_b64_tr_b16 v[164:165], %[tr] offset:256\n\t" \
    "ds_read_b64_tr_b16 v[166:167], %[tr] offset:5888\n\t" \
    "ds_read_b64_tr_b16 v[188:189], %[tr] offset:288\n\t" \
    "ds_read_b64_tr_b16 v[190:191], %[tr] offset:5920\n\t" \
    "s_barrier \n\t" \
    "s_mov_b32 %[t], 0x2d000\n\t" \
    "buffer_load_dwordx4 v[12:15], %[o0], %[rs], %[t] offen" WNT "\n\t" \
    "buffer_load_dwordx4 v[36:39], %[o1], %[rs], %[t] offen" WNT "\n\t" \
    "buffer_load_dwordx4 v[52:55], %[o2], %[rs], %[t] offen" WNT "\n\t" \
    "buffer_load_dwordx4 v[76:79], %[o3], %[rs], %[t] offen" WNT "\n\t" \
    "buffer_load_dwordx4 v[92:95], %[o4], %[rs], %[t] offen" WNT "\n\t" \
    "s_mov_b32 %[t], 0x37000\n\t" \
    "buffer_load_dwordx4 v[116:119], %[o0], %[rs], %[t] offen" WNT "\n\t" \
    "buffer_load_dwordx4 v[132:135], %[o1], %[rs], %[t] offen" WNT "\n\t" \
    "buffer_load_dwordx4 v[156:159], %[o2], %[rs], %[t] offen" WNT "\n\t" \
    "buffer_load_dwordx4 v[172:175], %[o3], %[rs], %[t] offen" WNT "\n\t" \
    "buffer_load_dwordx4 v[196:199], %[o4], %[rs], %[t] offen" WNT "\n\t" \
    "s_waitcnt lgkmcnt(0)\n\t" \
    "s_barrier \n\t" \
    "s_waitcnt vmcnt(15)\n\t" \
    "ds_write_b128 %[s0], v[8:11] offset:0\n\t" \
    "ds_write_b128 %[s1], v[32:35] offset:0\n\t" \
    "ds_write_b128 %[s2], v[48:51] offset:0\n\t" \
    "ds_write_b128 %[s3], v[72:75] offset:0\n\t" \
    "ds_write_b128 %[s4], v[88:91] offset:0\n\t" \
    "s_waitcnt vmcnt(10)\n\t" \
    "ds_write_b128 %[s0], v[112:115] offset:5632\n\t" \
    "ds_write_b128 %[s1], v[128:131] offset:5632\n\t" \
    "ds_write_b128 %[s2], v[152:155] offset:5632\n\t" \
    "ds_write_b128 %[s3], v[168:171] offset:5632\n\t" \
    "ds_write_b128 %[s4], v[192:195] offset:5632\n\t" \
    "ds_read_b64_tr_b16 v[8:9], %[tr] offset:0\n\t" \
    "ds_read_b64_tr_b16 v[10:11], %[tr] offset:5632\n\t" \
    "ds_read_b64_tr_b16 v[32:33], %[tr] offset:32\n\t" \
    "ds_read_b64_tr_b16 v[34:35], %[tr] offset:5664\n\t" \
    "ds_read_b64_tr_b16 v[48:49], %[tr] offset:64\n\t" \
    "ds_read_b64_tr_b16 v[50:51], %[tr] offset:5696\n\t" \
    "ds_read_b64_tr_b16 v[72:73], %[tr] offset:96\n\t" \
    "ds_read_b64_tr_b16 v[74:75], %[tr] offset:5728\n\t" \
    "ds_read_b64_tr_b16 v[88:89], %[tr] offset:128\n\t" \
    "ds_read_b64_tr_b16 v[90:91], %[tr] offset:5760\n\t" \
    "ds_read_b64_tr_b16 v[112:113], %[tr] offset:160\n\t" \
    "ds_read_b64_tr_b16 v[114:115], %[tr] offset:5792\n\t" \
    "ds_read_b64_tr_b16 v[128:129], %[tr] offset:192\n\t" \
    "ds_read_b64_tr_b16 v[130:131], %[tr] offset:5824\n\t" \
    "ds_read_b64_tr_b16 v[152:153], %[tr] offset:224\n\t" \
    "ds_read_b64_tr_b16 v[154:155], %[tr] offset:5856\n\t" \
    "ds_read_b64_tr_b16 v[168:169], %[tr] offset:256\n\t" \
    "ds_read_b64_tr_b16 v[170:171], %[tr] offset:5888\n\t" \
    "ds_read_b64_tr_b16 v[192:193], %[tr] offset:288\n\t" \
    "ds_read_b64_tr_b16 v[194:195], %[tr] offset:5920\n\t" \
    "s_waitcnt lgkmcnt(0)\n\t" \
    "s_barrier \n\t" \
    "s_waitcnt vmcnt(5)\n\t" \
    "ds_write_b128 %[s0], v[12:15] offset:0\n\t" \
    "ds_write_b128 %[s1], v[36:39] offset:0\n\t" \
    "ds_write_b128 %[s2], v[52:55] offset:0\n\t" \
    "ds_write_b128 %[s3], v[76:79] offset:0\n\t" \
    "ds_write_b128 %[s4], v[92:95] offset:0\n\t" \
    "s_waitcnt vmcnt(0)\n\t" \
    "ds_write_b128 %[s0], v[116:119] offset:5632\n\t" \
    "ds_write_b128 %[s1], v[132:135] offset:5632\n\t" \
    "ds_write_b128 %[s2], v[156:159] offset:5632\n\t" \
    "ds_write_b128 %[s3], v[172:175] offset:5632\n\t" \
    "ds_write_b128 %[s4], v[196:199] offset:5632\n\t" \
    "ds_read_b64_tr_b16 v[12:13], %[tr] offset:0\n\t" \
    "ds_read_b64_tr_b16 v[14:15], %[tr] offset:5632\n\t" \
    "ds_read_b64_tr_b16 v[36:37], %[tr] offset:32\n\t" \
    "ds_read_b64_tr_b16 v[38:39], %[tr] offset:5664\n\t" \
    "ds_read_b64_tr_b16 v[52:53], %[tr] offset:64\n\t" \
    "ds_read_b64_tr_b16 v[54:55], %[tr] offset:5696\n\t" \
    "ds_read_b64_tr_b16 v[76:77], %[tr] offset:96\n\t" \
    "ds_read_b64_tr_b16 v[78:79], %[tr] offset:5728\n\t" \
    "ds_read_b64_tr_b16 v[92:93], %[tr] offset:128\n\t" \
    "ds_read_b64_tr_b16 v[94:95], %[tr] offset:5760\n\t" \
    "ds_read_b64_tr_b16 v[116:117], %[tr] offset:160\n\t" \
    "ds_read_b64_tr_b16 v[118:119], %[tr] offset:5792\n\t" \
    "ds_read_b64_tr_b16 v[132:133], %[tr] offset:192\n\t" \
    "ds_read_b64_tr_b16 v[134:135], %[tr] offset:5824\n\t" \
    "ds_read_b64_tr_b16 v[156:157], %[tr] offset:224\n\t" \
    "ds_read_b64_tr_b16 v[158:159], %[tr] offset:5856\n\t" \
    "ds_read_b64_tr_b16 v[172:173], %[tr] offset:256\n\t" \
    "ds_read_b64_tr_b16 v[174:175], %[tr] offset:5888\n\t" \
    "ds_read_b64_tr_b16 v[196:197], %[tr] offset:288\n\t" \
    "ds_read_b64_tr_b16 v[198:199], %[tr] offset:5920\n\t" \
    "s_waitcnt lgkmcnt(0)\n\t" \
    "s_barrier \n\t" \
    "ds_write_b128 %[wl], v[200:203] offset:0\n\t" \
    "ds_write_b128 %[wl], v[204:207] offset:1024\n\t" \
    "ds_write_b128 %[wl], v[208:211] offset:2048\n\t" \
    "ds_write_b128 %[wl], v[212:215] offset:3072\n\t" \
    "ds_write_b128 %[wl], v[216:219] offset:4096\n\t" \
    "ds_write_b128 %[wl], v[220:223] offset:5120\n\t" \
    "ds_write_b128 %[wl], v[224:227] offset:6144\n\t" \
    "ds_write_b128 %[wl], v[228:231] offset:7168\n\t" \
    "ds_write_b128 %[wl], v[232:235] offset:8192\n\t" \
    "ds_write_b128 %[wl], v[236:239] offset:9216\n\t" \
    "s_waitcnt lgkmcnt(0)\n\t"
#define ASM_XG_S_LO \
    "s_waitcnt lgkmcnt(0)\n\t" \
    "ds_read_b128 v[224:227], %[xa] offset:0\n\t" \
    "ds_read_b128 v[244:247], %[wl] offset:0\n\t" \
    "ds_read_b128 v[228:231], %[xa] offset:16384\n\t" \
    "ds_read_b128 v[232:235], %[xa] offset:1024\n\t" \
    "ds_read_b128 v[236:239], %[xa] offset:17408\n\t" \
    "ds_read_b128 v[248:251], %[wl] offset:1024\n\t" \
    "s_waitcnt lgkmcnt(5)\n\t" \
    "v_mfma_f32_16x16x32_bf16 v[204:207], v[0:3], v[224:227], 0\n\t" \
    "v_xor_b32 v252, 0x800, %[xa]\n\t" \
    "ds_read_b128 v[240:243], v252 offset:0\n\t" \
    "v_mfma_f32_16x16x32_bf16 v[208:211], v[8:11], v[224:227], 0\n\t" \
    "s_waitcnt lgkmcnt(5)\n\t" \
    "v_mfma_f32_16x16x32_bf16 v[200:203], v[244:247], v[224:227], 0\n\t" \
    "s_waitcnt lgkmcnt(4)\n\t" \
    "v_mfma_f32_16x16x32_bf16 v[216:219], v[0:3], v[228:231], 0\n\t" \
    "ds_read_b128 v[224:227], v252 offset:16384\n\t" \
    "v_mfma_f32_16x16x32_bf16 v[220:223], v[8:11], v[228:231], 0\n\t" \
    "v_mfma_f32_16x16x32_bf16 v[212:215], v[244:247], v[228:231], 0\n\t" \
    "s_waitcnt lgkmcnt(4)\n\t" \
    "v_mfma_f32_16x16x32_bf16 v[204:207], v[4:7], v[232:235], v[204:207]\n\t" \
    "ds_read_b128 v[228:231], v252 offset:1024\n\t" \
    "v_mfma_f32_16x16x32_bf16 v[208:211], v[12:15], v[232:235], v[208:211]\n\t" \
    "ds_read_b128 v[244:247], %[wl] offset:2048\n\t" \
    "s_waitcnt lgkmcnt(4)\n\t" \
    "v_mfma_f32_16x16x32_bf16 v[200:203], v[248:251], v[232:235], v[200:203]\n\t" \
    "v_mfma_f32_16x16x32_bf16 v[216:219], v[4:7], v[236:239], v[216:219]\n\t" \
    "ds_read_b128 v[232:235], v252 offset:17408\n\t" \
    "v_mfma_f32_16x16x32_bf16 v[220:223], v[12:15], v[236:239], v[220:223]\n\t" \
    "v_mfma_f32_16x16x32_bf16 v[212:215], v[248:251], v[236:239], v[212:215]\n\t" \
    "s_waitcnt lgkmcnt(4)\n\t" \
    "v_mfma_f32_16x16x32_bf16 v[204:207], v[24:27], v[240:243], v[204:207]\n\t" \
    "v_xor_b32 v252, 0x1000, %[xa]\n\t" \
    "ds_read_b128 v[236:239], v252 offset:0\n\t" \
    "v_mfma_f32_16x16x32_bf16 v[208:211], v[32:35], v[240:243], v[208:211]\n\t" \
    "ds_read_b128 v[248:251], %[wl] offset:3072\n\t" \
    "v_mfma_f32_16x16x32_bf16 v[200:203], v[16:19], v[240:243], v[200:203]\n\t" \
    "s_waitcnt lgkmcnt(5)\n\t" \
    "v_mfma_f32_16x16x32_bf16 v[216:219], v[24:27], v[224:227], v[216:219]\n\t" \
    "ds_read_b128 v[240:243], v252 offset:16384\n\t" \
    "v_mfma_f32_16x16x32_bf16 v[220:223], v[32:35], v[224:227], v[220:223]\n\t" \
    "v_mfma_f32_16x16x32_bf16 v[212:215], v[16:19], v[224:227], v[212:215]\n\t" \
    "s_waitcnt lgkmcnt(5)\n\t" \
    "v_mfma_f32_16x16x32_bf16 v[204:207], v[28:31], v[228:231], v[204:207]\n\t" \
    "ds_read_b128 v[224:227], v252 offset:1024\n\t" \
    "v_mfma_f32_16x16x32_bf16 v[208:211], v[36:39], v[228:231], v[208:211]\n\t" \
    "v_mfma_f32_16x16x32_bf16 v[200:203], v[20:23], v[228:231], v[200:203]\n\t" \
    "s_waitcnt lgkmcnt(4)\n\t" \
    "v_mfma_f32_16x16x32_bf16 v[216:219], v[28:31], v[232:235], v[216:219]\n\t" \
    "ds_read_b128 v[228:231], v252 offset:17408\n\t" \
    "v_mfma_f32_16x16x32_bf16 v[220:223], v[36:39], v[232:235], v[220:223]\n\t" \
    "v_mfma_f32_16x16x32_bf16 v[212:215], v[20:23], v[232:235], v[212:215]\n\t" \
    "s_waitcnt lgkmcnt(4)\n\t" \
    "v_mfma_f32_16x16x32_bf16 v[204:207], v[40:43], v[236:239], v[204:207]\n\t" \
    "v_xor_b32 v252, 0x1800, %[xa]\n\t" \
    "ds_read_b128 v[232:235], v252 offset:0\n\t" \
    "v_mfma_f32_16x16x32_bf16 v[208:211], v[48:51], v[236:239], v[208:211]\n\t" \
    "v_mfma_f32_16x16x32_bf16 v[200:203], v[244:247], v[236:239], v[200:203]\n\t" \
    "s_waitcnt lgkmcnt(3)\n\t" \
    "v_mfma_f32_16x16x32_bf16 v[216:219], v[40:43], v[240:243], v[216:219]\n\t" \
    "ds_read_b128 v[236:239], v252 offset:16384\n\t" \
    "v_mfma_f32_16x16x32_bf16 v[220:223], v[48:51], v[240:243], v[220:223]\n\t" \
    "v_mfma_f32_16x16x32_bf16 v[212:215], v[244:247], v[240:243], v[212:215]\n\t" \
    "s_waitcnt lgkmcnt(3)\n\t" \
    "v_mfma_f32_16x16x32_bf16 v[204:207], v[44:47], v[224:227], v[204:207]\n\t" \
    "ds_read_b128 v[240:243], v252 offset:1024\n\t" \
    "v_mfma_f32_16x16x32_bf16 v[208:211], v[52:55], v[224:227], v[208:211]\n\t" \
    "ds_read_b128 v[244:247], %[wl] offset:8192\n\t" \
    "v_mfma_f32_16x16x32_bf16 v[200:203], v[248:251], v[224:227], v[200:203]\n\t" \
    "s_waitcnt lgkmcnt(4)\n\t" \
    "v_mfma_f32_16x16x32_bf16 v[216:219], v[44:47], v[228:231], v[216:219]\n\t" \
    "ds_read_b128 v[224:227], v252 offset:17408\n\t" \
    "v_mfma_f32_16x16x32_bf16 v[220:223], v[52:55], v[228:231], v[220:223]\n\t" \
    "v_mfma_f32_16x16x32_bf16 v[212:215], v[248:251], v[228:231], v[212:215]\n\t" \
    "s_waitcnt lgkmcnt(4)\n\t" \
    "v_mfma_f32_16x16x32_bf16 v[204:207], v[64:67], v[232:235], v[204:207]\n\t" \
    "v_and_b32 v252, 0x3ff, %[xa]\n\t" \
    "ds_read_b128 v[228:231], v252 offset:40960\n\t" \
    "v_mfma_f32_16x16x32_bf16 v[208:211], v[72:75], v[232:235], v[208:211]\n\t" \
    "ds_read_b128 v[248:251], %[wl] offset:9216\n\t" \
    "v_mfma_f32_16x16x32_bf16 v[200:203], v[56:59], v[232:235], v[200:203]\n\t" \
    "s_waitcnt lgkmcnt(5)\n\t" \
    "v_mfma_f32_16x16x32_bf16 v[216:219], v[64:67], v[236:239], v[216:219]\n\t" \
    "ds_read_b128 v[232:235], v252 offset:45056\n\t" \
    "v_mfma_f32_16x16x32_bf16 v[220:223], v[72:75], v[236:239], v[220:223]\n\t" \
    "v_mfma_f32_16x16x32_bf16 v[212:215], v[56:59], v[236:239], v[212:215]\n\t" \
    "s_waitcnt lgkmcnt(5)\n\t" \
    "v_mfma_f32_16x16x32_bf16 v[204:207], v[68:71], v[240:243], v[204:207]\n\t" \
    "ds_read_b128 v[236:239], v252 offset:41984\n\t" \
    "v_mfma_f32_16x16x32_bf16 v[208:211], v[76:79], v[240:243], v[208:211]\n\t" \
    "v_mfma_f32_16x16x32_bf16 v[200:203], v[60:63], v[240:243], v[200:203]\n\t" \
    "s_waitcnt lgkmcnt(4)\n\t" \
    "v_mfma_f32_16x16x32_bf16 v[216:219], v[68:71], v[224:227], v[216:219]\n\t" \
    "ds_read_b128 v[240:243], v252 offset:46080\n\t" \
    "v_mfma_f32_16x16x32_bf16 v[220:223], v[76:79], v[224:227], v[220:223]\n\t" \
    "v_mfma_f32_16x16x32_bf16 v[212:215], v[60:63], v[224:227], v[212:215]\n\t" \
    "s_waitcnt lgkmcnt(0)\n\t" \
    "s_barrier\n\t" ASM_ST \
    "v_mfma_f32_16x16x32_bf16 v[204:207], v[160:163], v[228:231], v[204:207]\n\t" \
    "v_xor_b32 v252, 0x2000, %[xa]\n\t" \
    "ds_read_b128 v[224:227], v252 offset:0\n\t" \
    "v_mfma_f32_16x16x32_bf16 v[208:211], v[168:171], v[228:231], v[208:211]\n\t" \
    "v_mfma_f32_16x16x32_bf16 v[200:203], v[244:247], v[228:231], v[200:203]\n\t" \
    "v_mfma_f32_16x16x32_bf16 v[216:219], v[160:163], v[232:235], v[216:219]\n\t" \
    "ds_read_b128 v[228:231], v252 offset:16384\n\t" \
    "v_mfma_f32_16x16x32_bf16 v[220:223], v[168:171], v[232:235], v[220:223]\n\t" \
    "v_mfma_f32_16x16x32_bf16 v[212:215], v[244:247], v[232:235], v[212:215]\n\t" \
    "v_mfma_f32_16x16x32_bf16 v[204:207], v[164:167], v[236:239], v[204:207]\n\t" \
    "ds_read_b128 v[232:235], v252 offset:1024\n\t" \
    "v_mfma_f32_16x16x32_bf16 v[208:211], v[172:175], v[236:239], v[208:211]\n\t" \
    "ds_read_b128 v[244:247], %[wl] offset:4096\n\t" \
    "v_mfma_f32_16x16x32_bf16 v[200:203], v[248:251], v[236:239], v[200:203]\n\t" \
    "v_mfma_f32_16x16x32_bf16 v[216:219], v[164:167], v[240:243], v[216:219]\n\t" \
    "ds_read_b128 v[236:239], v252 offset:17408\n\t" \
    "v_mfma_f32_16x16x32_bf16 v[220:223], v[172:175], v[240:243], v[220:223]\n\t" \
    "v_mfma_f32_16x16x32_bf16 v[212:215], v[248:251], v[240:243], v[212:215]\n\t" \
    "s_waitcnt lgkmcnt(4)\n\t" \
    "v_mfma_f32_16x16x32_bf16 v[204:207], v[80:83], v[224:227], v[204:207]\n\t" \
    "v_xor_b32 v252, 0x2800, %[xa]\n\t" \
    "ds_read_b128 v[240:243], v252 offset:0\n\t" \
    "v_mfma_f32_16x16x32_bf16 v[208:211], v[88:91], v[224:227], v[208:211]\n\t" \
    "ds_read_b128 v[248:251], %[wl] offset:5120\n\t" \
    "s_waitcnt lgkmcnt(3)\n\t" \
    "v_mfma_f32_16x16x32_bf16 v[200:203], v[244:247], v[224:227], v[200:203]\n\t" \
    "v_mfma_f32_16x16x32_bf16 v[216:219], v[80:83], v[228:231], v[216:219]\n\t" \
    "ds_read_b128 v[224:227], v252 offset:16384\n\t" \
    "v_mfma_f32_16x16x32_bf16 v[220:223], v[88:91], v[228:231], v[220:223]\n\t" \
    "v_mfma_f32_16x16x32_bf16 v[212:215], v[244:247], v[228:231], v[212:215]\n\t" \
    "v_mfma_f32_16x16x32_bf16 v[204:207], v[84:87], v[232:235], v[204:207]\n\t" \
    "ds_read_b128 v[228:231], v252 offset:1024\n\t" \
    "v_mfma_f32_16x16x32_bf16 v[208:211], v[92:95], v[232:235], v[208:211]\n\t" \
    "ds_read_b128 v[244:247], %[wl] offset:6144\n\t" \
    "s_waitcnt lgkmcnt(3)\n\t" \
    "v_mfma_f32_16x16x32_bf16 v[200:203], v[248:251], v[232:235], v[200:203]\n\t" \
    "v_mfma_f32_16x16x32_bf16 v[216:219], v[84:87], v[236:239], v[216:219]\n\t" \
    "ds_read_b128 v[232:235], v252 offset:17408\n\t" \
    "v_mfma_f32_16x16x32_bf16 v[220:223], v[92:95], v[236:239], v[220:223]\n\t" \
    "v_mfma_f32_16x16x32_bf16 v[212:215], v[248:251], v[236:239], v[212:215]\n\t" \
    "v_mfma_f32_16x16x32_bf16 v[204:207], v[104:107], v[240:243], v[204:207]\n\t" \
    "v_xor_b32 v252, 0x3000, %[xa]\n\t" \
    "ds_read_b128 v[236:239], v252 offset:0\n\t" \
    "v_mfma_f32_16x16x32_bf16 v[208:211], v[112:115], v[240:243], v[208:211]\n\t" \
    "ds_read_b128 v[248:251], %[wl] offset:7168\n\t" \
    "v_mfma_f32_16x16x32_bf16 v[200:203], v[96:99], v[240:243], v[200:203]\n\t" \
    "s_waitcnt lgkmcnt(5)\n\t" \
    "v_mfma_f32_16x16x32_bf16 v[216:219], v[104:107], v[224:227], v[216:219]\n\t" \
    "ds_read_b128 v[240:243], v252 offset:16384\n\t" \
    "v_mfma_f32_16x16x32_bf16 v[220:223], v[112:115], v[224:227], v[220:223]\n\t" \
    "v_mfma_f32_16x16x32_bf16 v[212:215], v[96:99], v[224:227], v[212:215]\n\t" \
    "s_waitcnt lgkmcnt(5)\n\t" \
    "v_mfma_f32_16x16x32_bf16 v[204:207], v[108:111], v[228:231], v[204:207]\n\t" \
    "ds_read_b128 v[224:227], v252 offset:1024\n\t" \
    "v_mfma_f32_16x16x32_bf16 v[208:211], v[116:119], v[228:231], v[208:211]\n\t" \
    "v_mfma_f32_16x16x32_bf16 v[200:203], v[100:103], v[228:231], v[200:203]\n\t" \
    "s_waitcnt lgkmcnt(4)\n\t" \
    "v_mfma_f32_16x16x32_bf16 v[216:219], v[108:111], v[232:235], v[216:219]\n\t" \
    "ds_read_b128 v[228:231], v252 offset:17408\n\t" \
    "v_mfma_f32_16x16x32_bf16 v[220:223], v[116:119], v[232:235], v[220:223]\n\t" \
    "v_mfma_f32_16x16x32_bf16 v[212:215], v[100:103], v[232:235], v[212:215]\n\t" \
    "s_waitcnt lgkmcnt(4)\n\t" \
    "v_mfma_f32_16x16x32_bf16 v[204:207], v[120:123], v[236:239], v[204:207]\n\t" \
    "v_xor_b32 v252, 0x3800, %[xa]\n\t" \
    "ds_read_b128 v[232:235], v252 offset:0\n\t" \
    "v_mfma_f32_16x16x32_bf16 v[208:211], v[128:131], v[236:239], v[208:211]\n\t" \
    "v_mfma_f32_16x16x32_bf16 v[200:203], v[244:247], v[236:239], v[200:203]\n\t" \
    "s_waitcnt lgkmcnt(3)\n\t" \
    "v_mfma_f32_16x16x32_bf16 v[216:219], v[120:123], v[240:243], v[216:219]\n\t" \
    "ds_read_b128 v[236:239], v252 offset:16384\n\t" \
    "v_mfma_f32_16x16x32_bf16 v[220:223], v[128:131], v[240:243], v[220:223]\n\t" \
    "v_mfma_f32_16x16x32_bf16 v[212:215], v[244:247], v[240:243], v[212:215]\n\t" \
    "s_waitcnt lgkmcnt(3)\n\t" \
    "v_mfma_f32_16x16x32_bf16 v[204:207], v[124:127], v[224:227], v[204:207]\n\t" \
    "ds_read_b128 v[240:243], v252 offset:1024\n\t" \
    "v_mfma_f32_16x16x32_bf16 v[208:211], v[132:135], v[224:227], v[208:211]\n\t" \
    "v_mfma_f32_16x16x32_bf16 v[200:203], v[248:251], v[224:227], v[200:203]\n\t" \
    "s_waitcnt lgkmcnt(3)\n\t" \
    "v_mfma_f32_16x16x32_bf16 v[216:219], v[124:127], v[228:231], v[216:219]\n\t" \
    "ds_read_b128 v[224:227], v252 offset:17408\n\t" \
    "v_mfma_f32_16x16x32_bf16 v[220:223], v[132:135], v[228:231], v[220:223]\n\t" \
    "v_mfma_f32_16x16x32_bf16 v[212:215], v[248:251], v[228:231], v[212:215]\n\t" \
    "s_waitcnt lgkmcnt(3)\n\t" \
    "v_mfma_f32_16x16x32_bf16 v[204:207], v[144:147], v[232:235], v[204:207]\n\t" \
    "v_and_b32 v252, 0x3ff, %[xa]\n\t" \
    "ds_read_b128 v[228:231], v252 offset:43008\n\t" \
    "v_mov_b64 v[244:245], 0\n\t" \
    "v_mfma_f32_16x16x32_bf16 v[208:211], v[152:155], v[232:235], v[208:211]\n\t" \
    "v_mov_b64 v[246:247], 0\n\t" \
    "v_mfma_f32_16x16x32_bf16 v[200:203], v[136:139], v[232:235], v[200:203]\n\t" \
    "v_mov_b64 v[248:249], 0\n\t" \
    "s_waitcnt lgkmcnt(3)\n\t" \
    "v_mfma_f32_16x16x32_bf16 v[216:219], v[144:147], v[236:239], v[216:219]\n\t" \
    "ds_read_b128 v[232:235], v252 offset:44032\n\t" \
    "v_mov_b64 v[250:251], 0\n\t" \
    "v_mfma_f32_16x16x32_bf16 v[220:223], v[152:155], v[236:239], v[220:223]\n\t" \
    "v_mfma_f32_16x16x32_bf16 v[212:215], v[136:139], v[236:239], v[212:215]\n\t" \
    "s_waitcnt lgkmcnt(3)\n\t" \
    "v_mfma_f32_16x16x32_bf16 v[204:207], v[148:151], v[240:243], v[204:207]\n\t" \
    "ds_read_b128 v[236:239], v252 offset:47104\n\t" \
    "v_mfma_f32_16x16x32_bf16 v[208:211], v[156:159], v[240:243], v[208:211]\n\t" \
    "v_mfma_f32_16x16x32_bf16 v[200:203], v[140:143], v[240:243], v[200:203]\n\t" \
    "s_waitcnt lgkmcnt(3)\n\t" \
    "v_mfma_f32_16x16x32_bf16 v[216:219], v[148:151], v[224:227], v[216:219]\n\t" \
    "ds_read_b128 v[240:243], v252 offset:48128\n\t" \
    "v_mfma_f32_16x16x32_bf16 v[220:223], v[156:159], v[224:227], v[220:223]\n\t" \
    "v_mfma_f32_16x16x32_bf16 v[212:215], v[140:143], v[224:227], v[212:215]\n\t" \
    "s_waitcnt lgkmcnt(3)\n\t" \
    "v_mfma_f32_16x16x32_bf16 v[204:207], v[184:187], v[228:231], v[204:207]\n\t" \
    "v_add_u32 v252, %[qs], v252\n\t" \
    "v_mfma_f32_16x16x32_bf16 v[208:211], v[192:195], v[228:231], v[208:211]\n\t" \
    "v_mov_b64 v[224:225], 0\n\t" \
    "v_mfma_f32_16x16x32_bf16 v[200:203], v[176:179], v[228:231], v[200:203]\n\t" \
    "v_mov_b64 v[226:227], 0\n\t" \
    "s_waitcnt lgkmcnt(2)\n\t" \
    "v_mfma_f32_16x16x32_bf16 v[204:207], v[188:191], v[232:235], v[204:207]\n\t" \
    "v_mfma_f32_16x16x32_bf16 v[208:211], v[196:199], v[232:235], v[208:211]\n\t" \
    "v_mfma_f32_16x16x32_bf16 v[200:203], v[180:183], v[232:235], v[200:203]\n\t" \
    "s_waitcnt lgkmcnt(1)\n\t" \
    "v_mfma_f32_16x16x32_bf16 v[216:219], v[184:187], v[236:239], v[216:219]\n\t" \
    "ds_read_b128 v[228:231], %[wl] offset:2048\n\t" \
    "ds_read_b128 v[232:235], %[wl] offset:3072\n\t" \
    "v_mfma_f32_16x16x32_bf16 v[220:223], v[192:195], v[236:239], v[220:223]\n\t" \
    "v_cvt_pk_bf16_f32 v204, v204, v205\n\t" \
    "v_cvt_pk_bf16_f32 v205, v206, v207\n\t" \
    "v_mfma_f32_16x16x32_bf16 v[212:215], v[176:179], v[236:239], v[212:215]\n\t" \
    "ds_write_b64 v252, v[204:205] offset:1024\n\t" \
    "v_cvt_pk_bf16_f32 v208, v208, v209\n\t" \
    "s_waitcnt lgkmcnt(3)\n\t" \
    "v_mfma_f32_16x16x32_bf16 v[216:219], v[188:191], v[240:243], v[216:219]\n\t" \
    "v_cvt_pk_bf16_f32 v209, v210, v211\n\t" \
    "ds_write_b64 v252, v[208:209] offset:2048\n\t" \
    "v_mfma_f32_16x16x32_bf16 v[220:223], v[196:199], v[240:243], v[220:223]\n\t" \
    "v_cvt_pk_bf16_f32 v200, v200, v201\n\t" \
    "v_cvt_pk_bf16_f32 v201, v202, v203\n\t" \
    "v_mfma_f32_16x16x32_bf16 v[212:215], v[180:183], v[240:243], v[212:215]\n\t" \
    "ds_write_b64 v252, v[200:201] offset:0\n\t" \
    "ds_read_b128 v[200:203], %[wl] offset:0\n\t" \
    "ds_read_b128 v[204:207], %[wl] offset:1024\n\t" \
    "v_cvt_pk_bf16_f32 v216, v216, v217\n\t" \
    "v_cvt_pk_bf16_f32 v217, v218, v219\n\t" \
    "ds_write_b64 v252, v[216:217] offset:7168\n\t" \
    "v_cvt_pk_bf16_f32 v220, v220, v221\n\t" \
    "v_cvt_pk_bf16_f32 v221, v222, v223\n\t" \
    "ds_write_b64 v252, v[220:221] offset:8192\n\t" \
    "v_cvt_pk_bf16_f32 v212, v212, v213\n\t" \
    "v_cvt_pk_bf16_f32 v213, v214, v215\n\t" \
    "ds_write_b64 v252, v[212:213] offset:6144\n\t" \
    "s_waitcnt lgkmcnt(0)\n\t"
#define ASM_XG_S_HI \
    "s_waitcnt lgkmcnt(0)\n\t" \
    "v_xor_b32 v252, 0x2000, %[xa]\n\t" \
    "ds_read_b128 v[224:227], v252 offset:0\n\t" \
    "ds_read_b128 v[244:247], %[wl] offset:4096\n\t" \
    "ds_read_b128 v[228:231], v252 offset:16384\n\t" \
    "ds_read_b128 v[232:235], v252 offset:1024\n\t" \
    "ds_read_b128 v[236:239], v252 offset:17408\n\t" \
    "ds_read_b128 v[248:251], %[wl] offset:5120\n\t" \
    "s_cmp_eq_u32 %[slp], 0\n\t" \
    "s_cbranch_scc1 .Lxs%=\n\t" \
    "s_sleep 10\n\t" \
    ".Lxs%=:\n\t" \
    "s_waitcnt lgkmcnt(5)\n\t" \
    "v_mfma_f32_16x16x32_bf16 v[204:207], v[80:83], v[224:227], 0\n\t" \
    "v_xor_b32 v252, 0x2800, %[xa]\n\t" \
    "ds_read_b128 v[240:243], v252 offset:0\n\t" \
    "v_mfma_f32_16x16x32_bf16 v[208:211], v[88:91], v[224:227], 0\n\t" \
    "s_waitcnt lgkmcnt(5)\n\t" \
    "v_mfma_f32_16x16x32_bf16 v[200:203], v[244:247], v[224:227], 0\n\t" \
    "s_waitcnt lgkmcnt(4)\n\t" \
    "v_mfma_f32_16x16x32_bf16 v[216:219], v[80:83], v[228:231], 0\n\t" \
    "ds_read_b128 v[224:227], v252 offset:16384\n\t" \
    "v_mfma_f32_16x16x32_bf16 v[220:223], v[88:91], v[228:231], 0\n\t" \
    "v_mfma_f32_16x16x32_bf16 v[212:215], v[244:247], v[228:231], 0\n\t" \
    "s_waitcnt lgkmcnt(4)\n\t" \
    "v_mfma_f32_16x16x32_bf16 v[204:207], v[84:87], v[232:235], v[204:207]\n\t" \
    "ds_read_b128 v[228:231], v252 offset:1024\n\t" \
    "v_mfma_f32_16x16x32_bf16 v[208:211], v[92:95], v[232:235], v[208:211]\n\t" \
    "ds_read_b128 v[244:247], %[wl] offset:6144\n\t" \
    "s_waitcnt lgkmcnt(4)\n\t" \
    "v_mfma_f32_16x16x32_bf16 v[200:203], v[248:251], v[232:235], v[200:203]\n\t" \
    "v_mfma_f32_16x16x32_bf16 v[216:219], v[84:87], v[236:239], v[216:219]\n\t" \
    "ds_read_b128 v[232:235], v252 offset:17408\n\t" \
    "v_mfma_f32_16x16x32_bf16 v[220:223], v[92:95], v[236:239], v[220:223]\n\t" \
    "v_mfma_f32_16x16x32_bf16 v[212:215], v[248:251], v[236:239], v[212:215]\n\t" \
    "s_waitcnt lgkmcnt(4)\n\t" \
    "v_mfma_f32_16x16x32_bf16 v[204:207], v[104:107], v[240:243], v[204:207]\n\t" \
    "v_xor_b32 v252, 0x3000, %[xa]\n\t" \
    "ds_read_b128 v[236:239], v252 offset:0\n\t" \
    "v_mfma_f32_16x16x32_bf16 v[208:211], v[112:115], v[240:243], v[208:211]\n\t" \
    "ds_read_b128 v[248:251], %[wl] offset:7168\n\t" \
    "v_mfma_f32_16x16x32_bf16 v[200:203], v[96:99], v[240:243], v[200:203]\n\t" \
    "s_waitcnt lgkmcnt(5)\n\t" \
    "v_mfma_f32_16x16x32_bf16 v[216:219], v[104:107], v[224:227], v[216:219]\n\t" \
    "ds_read_b128 v[240:243], v252 offset:16384\n\t" \
    "v_mfma_f32_16x16x32_bf16 v[220:223], v[112:115], v[224:227], v[220:223]\n\t" \
    "v_mfma_f32_16x16x32_bf16 v[212:215], v[96:99], v[224:227], v[212:215]\n\t" \
    "s_waitcnt lgkmcnt(5)\n\t" \
    "v_mfma_f32_16x16x32_bf16 v[204:207], v[108:111], v[228:231], v[204:207]\n\t" \
    "ds_read_b128 v[224:227], v252 offset:1024\n\t" \
    "v_mfma_f32_16x16x32_bf16 v[208:211], v[116:119], v[228:231], v[208:211]\n\t" \
    "v_mfma_f32_16x16x32_bf16 v[200:203], v[100:103], v[228:231], v[200:203]\n\t" \
    "s_waitcnt lgkmcnt(4)\n\t" \
    "v_mfma_f32_16x16x32_bf16 v[216:219], v[108:111], v[232:235], v[216:219]\n\t" \
    "ds_read_b128 v[228:231], v252 offset:17408\n\t" \
    "v_mfma_f32_16x16x32_bf16 v[220:223], v[116:119], v[232:235], v[220:223]\n\t" \
    "v_mfma_f32_16x16x32_bf16 v[212:215], v[100:103], v[232:235], v[212:215]\n\t" \
    "s_waitcnt lgkmcnt(4)\n\t" \
    "v_mfma_f32_16x16x32_bf16 v[204:207], v[120:123], v[236:239], v[204:207]\n\t" \
    "v_xor_b32 v252, 0x3800, %[xa]\n\t" \
    "ds_read_b128 v[232:235], v252 offset:0\n\t" \
    "v_mfma_f32_16x16x32_bf16 v[208:211], v[128:131], v[236:239], v[208:211]\n\t" \
    "v_mfma_f32_16x16x32_bf16 v[200:203], v[244:247], v[236:239], v[200:203]\n\t" \
    "s_waitcnt lgkmcnt(3)\n\t" \
    "v_mfma_f32_16x16x32_bf16 v[216:219], v[120:123], v[240:243], v[216:219]\n\t" \
    "ds_read_b128 v[236:239], v252 offset:16384\n\t" \
    "v_mfma_f32_16x16x32_bf16 v[220:223], v[128:131], v[240:243], v[220:223]\n\t" \
    "v_mfma_f32_16x16x32_bf16 v[212:215], v[244:247], v[240:243], v[212:215]\n\t" \
    "s_waitcnt lgkmcnt(3)\n\t" \
    "v_mfma_f32_16x16x32_bf16 v[204:207], v[124:127], v[224:227], v[204:207]\n\t" \
    "ds_read_b128 v[240:243], v252 offset:1024\n\t" \
    "v_mfma_f32_16x16x32_bf16 v[208:211], v[132:135], v[224:227], v[208:211]\n\t" \
    "ds_read_b128 v[244:247], %[wl] offset:8192\n\t" \
    "v_mfma_f32_16x16x32_bf16 v[200:203], v[248:251], v[224:227], v[200:203]\n\t" \
    "s_waitcnt lgkmcnt(4)\n\t" \
    "v_mfma_f32_16x16x32_bf16 v[216:219], v[124:127], v[228:231], v[216:219]\n\t" \
    "ds_read_b128 v[224:227], v252 offset:17408\n\t" \
    "v_mfma_f32_16x16x32_bf16 v[220:223], v[132:135], v[228:231], v[220:223]\n\t" \
    "v_mfma_f32_16x16x32_bf16 v[212:215], v[248:251], v[228:231], v[212:215]\n\t" \
    "s_waitcnt lgkmcnt(4)\n\t" \
    "v_mfma_f32_16x16x32_bf16 v[204:207], v[144:147], v[232:235], v[204:207]\n\t" \
    "v_and_b32 v252, 0x3ff, %[xa]\n\t" \
    "ds_read_b128 v[228:231], v252 offset:40960\n\t" \
    "v_mfma_f32_16x16x32_bf16 v[208:211], v[152:155], v[232:235], v[208:211]\n\t" \
    "ds_read_b128 v[248:251], %[wl] offset:9216\n\t" \
    "v_mfma_f32_16x16x32_bf16 v[200:203], v[136:139], v[232:235], v[200:203]\n\t" \
    "s_waitcnt lgkmcnt(5)\n\t" \
    "v_mfma_f32_16x16x32_bf16 v[216:219], v[144:147], v[236:239], v[216:219]\n\t" \
    "ds_read_b128 v[232:235], v252 offset:45056\n\t" \
    "v_mfma_f32_16x16x32_bf16 v[220:223], v[152:155], v[236:239], v[220:223]\n\t" \
    "v_mfma_f32_16x16x32_bf16 v[212:215], v[136:139], v[236:239], v[212:215]\n\t" \
    "s_waitcnt lgkmcnt(5)\n\t" \
    "v_mfma_f32_16x16x32_bf16 v[204:207], v[148:151], v[240:243], v[204:207]\n\t" \
    "ds_read_b128 v[236:239], v252 offset:41984\n\t" \
    "v_mfma_f32_16x16x32_bf16 v[208:211], v[156:159], v[240:243], v[208:211]\n\t" \
    "v_mfma_f32_16x16x32_bf16 v[200:203], v[140:143], v[240:243], v[200:203]\n\t" \
    "s_waitcnt lgkmcnt(4)\n\t" \
    "v_mfma_f32_16x16x32_bf16 v[216:219], v[148:151], v[224:227], v[216:219]\n\t" \
    "ds_read_b128 v[240:243], v252 offset:46080\n\t" \
    "v_mfma_f32_16x16x32_bf16 v[220:223], v[156:159], v[224:227], v[220:223]\n\t" \
    "v_mfma_f32_16x16x32_bf16 v[212:215], v[140:143], v[224:227], v[212:215]\n\t" \
    "s_waitcnt vmcnt(0) lgkmcnt(0)\n\t" \
    "s_barrier\n\t" ASM_ST \
    "s_mov_b32 m0, %[d0]\n\t" \
    "s_nop 0\n\t" \
    "global_load_lds_dwordx4 v252, %[g0] offset:0 sc1\n\t" \
    "global_load_lds_dwordx4 v252, %[g0] offset:1024 sc1\n\t" \
    "global_load_lds_dwordx4 v252, %[g0] offset:2048 sc1\n\t" \
    "global_load_lds_dwordx4 v252, %[g0] offset:3072 sc1\n\t" \
    "s_mov_b32 m0, %[d1]\n\t" \
    "s_nop 0\n\t" \
    "global_load_lds_dwordx4 v252, %[g1] sc1\n\t" \
    "v_mfma_f32_16x16x32_bf16 v[204:207], v[160:163], v[228:231], v[204:207]\n\t" \
    "ds_read_b128 v[224:227], %[xa] offset:0\n\t" \
    "v_mfma_f32_16x16x32_bf16 v[208:211], v[168:171], v[228:231], v[208:211]\n\t" \
    "v_mfma_f32_16x16x32_bf16 v[200:203], v[244:247], v[228:231], v[200:203]\n\t" \
    "v_mfma_f32_16x16x32_bf16 v[216:219], v[160:163], v[232:235], v[216:219]\n\t" \
    "ds_read_b128 v[228:231], %[xa] offset:16384\n\t" \
    "v_mfma_f32_16x16x32_bf16 v[220:223], v[168:171], v[232:235], v[220:223]\n\t" \
    "v_mfma_f32_16x16x32_bf16 v[212:215], v[244:247], v[232:235], v[212:215]\n\t" \
    "v_mfma_f32_16x16x32_bf16 v[204:207], v[164:167], v[236:239], v[204:207]\n\t" \
    "ds_read_b128 v[232:235], %[xa] offset:1024\n\t" \
    "v_mfma_f32_16x16x32_bf16 v[208:211], v[172:175], v[236:239], v[208:211]\n\t" \
    "ds_read_b128 v[244:247], %[wl] offset:0\n\t" \
    "v_mfma_f32_16x16x32_bf16 v[200:203], v[248:251], v[236:239], v[200:203]\n\t" \
    "v_mfma_f32_16x16x32_bf16 v[216:219], v[164:167], v[240:243], v[216:219]\n\t" \
    "ds_read_b128 v[236:239], %[xa] offset:17408\n\t" \
    "v_mfma_f32_16x16x32_bf16 v[220:223], v[172:175], v[240:243], v[220:223]\n\t" \
    "v_mfma_f32_16x16x32_bf16 v[212:215], v[248:251], v[240:243], v[212:215]\n\t" \
    "s_waitcnt lgkmcnt(4)\n\t" \
    "v_mfma_f32_16x16x32_bf16 v[204:207], v[0:3], v[224:227], v[204:207]\n\t" \
    "v_xor_b32 v252, 0x800, %[xa]\n\t" \
    "ds_read_b128 v[240:243], v252 offset:0\n\t" \
    "v_mfma_f32_16x16x32_bf16 v[208:211], v[8:11], v[224:227], v[208:211]\n\t" \
    "ds_read_b128 v[248:251], %[wl] offset:1024\n\t" \
    "s_waitcnt lgkmcnt(3)\n\t" \
    "v_mfma_f32_16x16x32_bf16 v[200:203], v[244:247], v[224:227], v[200:203]\n\t" \
    "v_mfma_f32_16x16x32_bf16 v[216:219], v[0:3], v[228:231], v[216:219]\n\t" \
    "ds_read_b128 v[224:227], v252 offset:16384\n\t" \
    "v_mfma_f32_16x16x32_bf16 v[220:223], v[8:11], v[228:231], v[220:223]\n\t" \
    "v_mfma_f32_16x16x32_bf16 v[212:215], v[244:247], v[228:231], v[212:215]\n\t" \
    "v_mfma_f32_16x16x32_bf16 v[204:207], v[4:7], v[232:235], v[204:207]\n\t" \
    "ds_read_b128 v[228:231], v252 offset:1024\n\t" \
    "v_mfma_f32_16x16x32_bf16 v[208:211], v[12:15], v[232:235], v[208:211]\n\t" \
    "ds_read_b128 v[244:247], %[wl] offset:2048\n\t" \
    "s_waitcnt lgkmcnt(3)\n\t" \
    "v_mfma_f32_16x16x32_bf16 v[200:203], v[248:251], v[232:235], v[200:203]\n\t" \
    "v_mfma_f32_16x16x32_bf16 v[216:219], v[4:7], v[236:239], v[216:219]\n\t" \
    "ds_read_b128 v[232:235], v252 offset:17408\n\t" \
    "v_mfma_f32_16x16x32_bf16 v[220:223], v[12:15], v[236:239], v[220:223]\n\t" \
    "v_mfma_f32_16x16x32_bf16 v[212:215], v[248:251], v[236:239], v[212:215]\n\t" \
    "v_mfma_f32_16x16x32_bf16 v[204:207], v[24:27], v[240:243], v[204:207]\n\t" \
    "v_xor_b32 v252, 0x1000, %[xa]\n\t" \
    "ds_read_b128 v[236:239], v252 offset:0\n\t" \
    "v_mfma_f32_16x16x32_bf16 v[208:211], v[32:35], v[240:243], v[208:211]\n\t" \
    "ds_read_b128 v[248:251], %[wl] offset:3072\n\t" \
    "v_mfma_f32_16x16x32_bf16 v[200:203], v[16:19], v[240:243], v[200:203]\n\t" \
    "s_waitcnt lgkmcnt(5)\n\t" \
    "v_mfma_f32_16x16x32_bf16 v[216:219], v[24:27], v[224:227], v[216:219]\n\t" \
    "ds_read_b128 v[240:243], v252 offset:16384\n\t" \
    "v_mfma_f32_16x16x32_bf16 v[220:223], v[32:35], v[224:227], v[220:223]\n\t" \
    "v_mfma_f32_16x16x32_bf16 v[212:215], v[16:19], v[224:227], v[212:215]\n\t" \
    "s_waitcnt lgkmcnt(5)\n\t" \
    "v_mfma_f32_16x16x32_bf16 v[204:207], v[28:31], v[228:231], v[204:207]\n\t" \
    "ds_read_b128 v[224:227], v252 offset:1024\n\t" \
    "v_mfma_f32_16x16x32_bf16 v[208:211], v[36:39], v[228:231], v[208:211]\n\t" \
    "v_mfma_f32_16x16x32_bf16 v[200:203], v[20:23], v[228:231], v[200:203]\n\t" \
    "s_waitcnt lgkmcnt(4)\n\t" \
    "v_mfma_f32_16x16x32_bf16 v[216:219], v[28:31], v[232:235], v[216:219]\n\t" \
    "ds_read_b128 v[228:231], v252 offset:17408\n\t" \
    "v_mfma_f32_16x16x32_bf16 v[220:223], v[36:39], v[232:235], v[220:223]\n\t" \
    "v_mfma_f32_16x16x32_bf16 v[212:215], v[20:23], v[232:235], v[212:215]\n\t" \
    "s_waitcnt lgkmcnt(4)\n\t" \
    "v_mfma_f32_16x16x32_bf16 v[204:207], v[40:43], v[236:239], v[204:207]\n\t" \
    "v_xor_b32 v252, 0x1800, %[xa]\n\t" \
    "ds_read_b128 v[232:235], v252 offset:0\n\t" \
    "v_mfma_f32_16x16x32_bf16 v[208:211], v[48:51], v[236:239], v[208:211]\n\t" \
    "v_mfma_f32_16x16x32_bf16 v[200:203], v[244:247], v[236:239], v[200:203]\n\t" \
    "s_waitcnt lgkmcnt(3)\n\t" \
    "v_mfma_f32_16x16x32_bf16 v[216:219], v[40:43], v[240:243], v[216:219]\n\t" \
    "ds_read_b128 v[236:239], v252 offset:16384\n\t" \
    "v_mfma_f32_16x16x32_bf16 v[220:223], v[48:51], v[240:243], v[220:223]\n\t" \
    "v_mfma_f32_16x16x32_bf16 v[212:215], v[244:247], v[240:243], v[212:215]\n\t" \
    "s_waitcnt lgkmcnt(3)\n\t" \
    "v_mfma_f32_16x16x32_bf16 v[204:207], v[44:47], v[224:227], v[204:207]\n\t" \
    "ds_read_b128 v[240:243], v252 offset:1024\n\t" \
    "v_mfma_f32_16x16x32_bf16 v[208:211], v[52:55], v[224:227], v[208:211]\n\t" \
    "v_mfma_f32_16x16x32_bf16 v[200:203], v[248:251], v[224:227], v[200:203]\n\t" \
    "s_waitcnt lgkmcnt(3)\n\t" \
    "v_mfma_f32_16x16x32_bf16 v[216:219], v[44:47], v[228:231], v[216:219]\n\t" \
    "ds_read_b128 v[224:227], v252 offset:17408\n\t" \
    "v_mfma_f32_16x16x32_bf16 v[220:223], v[52:55], v[228:231], v[220:223]\n\t" \
    "v_mfma_f32_16x16x32_bf16 v[212:215], v[248:251], v[228:231], v[212:215]\n\t" \
    "s_waitcnt lgkmcnt(3)\n\t" \
    "v_mfma_f32_16x16x32_bf16 v[204:207], v[64:67], v[232:235], v[204:207]\n\t" \
    "v_and_b32 v252, 0x3ff, %[xa]\n\t" \
    "ds_read_b128 v[228:231], v252 offset:43008\n\t" \
    "v_mov_b64 v[244:245], 0\n\t" \
    "v_mfma_f32_16x16x32_bf16 v[208:211], v[72:75], v[232:235], v[208:211]\n\t" \
    "v_mov_b64 v[246:247], 0\n\t" \
    "v_mfma_f32_16x16x32_bf16 v[200:203], v[56:59], v[232:235], v[200:203]\n\t" \
    "v_mov_b64 v[248:249], 0\n\t" \
    "s_waitcnt lgkmcnt(3)\n\t" \
    "v_mfma_f32_16x16x32_bf16 v[216:219], v[64:67], v[236:239], v[216:219]\n\t" \
    "ds_read_b128 v[232:235], v252 offset:44032\n\t" \
    "v_mov_b64 v[250:251], 0\n\t" \
    "v_mfma_f32_16x16x32_bf16 v[220:223], v[72:75], v[236:239], v[220:223]\n\t" \
    "v_mfma_f32_16x16x32_bf16 v[212:215], v[56:59], v[236:239], v[212:215]\n\t" \
    "s_waitcnt lgkmcnt(3)\n\t" \
    "v_mfma_f32_16x16x32_bf16 v[204:207], v[68:71], v[240:243], v[204:207]\n\t" \
    "ds_read_b128 v[236:239], v252 offset:47104\n\t" \
    "v_mfma_f32_16x16x32_bf16 v[208:211], v[76:79], v[240:243], v[208:211]\n\t" \
    "v_mfma_f32_16x16x32_bf16 v[200:203], v[60:63], v[240:243], v[200:203]\n\t" \
    "s_waitcnt lgkmcnt(3)\n\t" \
    "v_mfma_f32_16x16x32_bf16 v[216:219], v[68:71], v[224:227], v[216:219]\n\t" \
    "ds_read_b128 v[240:243], v252 offset:48128\n\t" \
    "v_mfma_f32_16x16x32_bf16 v[220:223], v[76:79], v[224:227], v[220:223]\n\t" \
    "v_mfma_f32_16x16x32_bf16 v[212:215], v[60:63], v[224:227], v[212:215]\n\t" \
    "s_waitcnt lgkmcnt(3)\n\t" \
    "v_mfma_f32_16x16x32_bf16 v[204:207], v[184:187], v[228:231], v[204:207]\n\t" \
    "v_add_u32 v252, %[qs], v252\n\t" \
    "v_mfma_f32_16x16x32_bf16 v[208:211], v[192:195], v[228:231], v[208:211]\n\t" \
    "v_mov_b64 v[224:225], 0\n\t" \
    "v_mfma_f32_16x16x32_bf16 v[200:203], v[176:179], v[228:231], v[200:203]\n\t" \
    "v_mov_b64 v[226:227], 0\n\t" \
    "s_waitcnt lgkmcnt(2)\n\t" \
    "v_mfma_f32_16x16x32_bf16 v[204:207], v[188:191], v[232:235], v[204:207]\n\t" \
    "v_mfma_f32_16x16x32_bf16 v[208:211], v[196:199], v[232:235], v[208:211]\n\t" \
    "v_mfma_f32_16x16x32_bf16 v[200:203], v[180:183], v[232:235], v[200:203]\n\t" \
    "s_waitcnt lgkmcnt(1)\n\t" \
    "v_mfma_f32_16x16x32_bf16 v[216:219], v[184:187], v[236:239], v[216:219]\n\t" \
    "ds_read_b128 v[228:231], %[wl] offset:2048\n\t" \
    "ds_read_b128 v[232:235], %[wl] offset:3072\n\t" \
    "v_mfma_f32_16x16x32_bf16 v[220:223], v[192:195], v[236:239], v[220:223]\n\t" \
    "v_cvt_pk_bf16_f32 v204, v204, v205\n\t" \
    "v_cvt_pk_bf16_f32 v205, v206, v207\n\t" \
    "v_mfma_f32_16x16x32_bf16 v[212:215], v[176:179], v[236:239], v[212:215]\n\t" \
    "ds_write_b64 v252, v[204:205] offset:1024\n\t" \
    "v_cvt_pk_bf16_f32 v208, v208, v209\n\t" \
    "s_waitcnt lgkmcnt(3)\n\t" \
    "v_mfma_f32_16x16x32_bf16 v[216:219], v[188:191], v[240:243], v[216:219]\n\t" \
    "v_cvt_pk_bf16_f32 v209, v210, v211\n\t" \
    "ds_write_b64 v252, v[208:209] offset:2048\n\t" \
    "v_mfma_f32_16x16x32_bf16 v[220:223], v[196:199], v[240:243], v[220:223]\n\t" \
    "v_cvt_pk_bf16_f32 v200, v200, v201\n\t" \
    "v_cvt_pk_bf16_f32 v201, v202, v203\n\t" \
    "v_mfma_f32_16x16x32_bf16 v[212:215], v[180:183], v[240:243], v[212:215]\n\t" \
    "ds_write_b64 v252, v[200:201] offset:0\n\t" \
    "ds_read_b128 v[200:203], %[wl] offset:0\n\t" \
    "ds_read_b128 v[204:207], %[wl] offset:1024\n\t" \
    "v_cvt_pk_bf16_f32 v216, v216, v217\n\t" \
    "v_cvt_pk_bf16_f32 v217, v218, v219\n\t" \
    "ds_write_b64 v252, v[216:217] offset:7168\n\t" \
    "v_cvt_pk_bf16_f32 v220, v220, v221\n\t" \
    "v_cvt_pk_bf16_f32 v221, v222, v223\n\t" \
    "ds_write_b64 v252, v[220:221] offset:8192\n\t" \
    "v_cvt_pk_bf16_f32 v212, v212, v213\n\t" \
    "v_cvt_pk_bf16_f32 v213, v214, v215\n\t" \
    "ds_write_b64 v252, v[212:213] offset:6144\n\t" \
    "s_waitcnt lgkmcnt(0)\n\t"
#define ASM_XG_S_LO2 \
    "s_waitcnt lgkmcnt(0)\n\t" \
    "ds_read_b128 v[224:227], %[xa] offset:0\n\t" \
    "ds_read_b128 v[228:231], %[xa] offset:16384\n\t" \
    "ds_read_b128 v[232:235], %[xa] offset:1024\n\t" \
    "ds_read_b128 v[236:239], %[xa] offset:17408\n\t" \
    "s_waitcnt lgkmcnt(3)\n\t" \
    "v_mfma_f32_16x16x32_bf16 v[204:207], v[0:3], v[224:227], 0\n\t" \
    "v_xor_b32 v252, 0x800, %[xa]\n\t" \
    "ds_read_b128 v[240:243], v252 offset:0\n\t" \
    "v_mfma_f32_16x16x32_bf16 v[208:211], v[8:11], v[224:227], 0\n\t" \
    "s_waitcnt lgkmcnt(3)\n\t" \
    "v_mfma_f32_16x16x32_bf16 v[216:219], v[0:3], v[228:231], 0\n\t" \
    "ds_read_b128 v[224:227], v252 offset:16384\n\t" \
    "v_mfma_f32_16x16x32_bf16 v[220:223], v[8:11], v[228:231], 0\n\t" \
    "s_waitcnt lgkmcnt(3)\n\t" \
    "v_mfma_f32_16x16x32_bf16 v[204:207], v[4:7], v[232:235], v[204:207]\n\t" \
    "ds_read_b128 v[228:231], v252 offset:1024\n\t" \
    "v_mfma_f32_16x16x32_bf16 v[208:211], v[12:15], v[232:235], v[208:211]\n\t" \
    "s_waitcnt lgkmcnt(3)\n\t" \
    "v_mfma_f32_16x16x32_bf16 v[216:219], v[4:7], v[236:239], v[216:219]\n\t" \
    "ds_read_b128 v[232:235], v252 offset:17408\n\t" \
    "v_mfma_f32_16x16x32_bf16 v[220:223], v[12:15], v[236:239], v[220:223]\n\t" \
    "s_waitcnt lgkmcnt(3)\n\t" \
    "v_mfma_f32_16x16x32_bf16 v[204:207], v[24:27], v[240:243], v[204:207]\n\t" \
    "v_xor_b32 v252, 0x1000, %[xa]\n\t" \
    "ds_read_b128 v[236:239], v252 offset:0\n\t" \
    "v_mfma_f32_16x16x32_bf16 v[208:211], v[32:35], v[240:243], v[208:211]\n\t" \
    "s_waitcnt lgkmcnt(3)\n\t" \
    "v_mfma_f32_16x16x32_bf16 v[216:219], v[24:27], v[224:227], v[216:219]\n\t" \
    "ds_read_b128 v[240:243], v252 offset:16384\n\t" \
    "v_mfma_f32_16x16x32_bf16 v[220:223], v[32:35], v[224:227], v[220:223]\n\t" \
    "s_waitcnt lgkmcnt(3)\n\t" \
    "v_mfma_f32_16x16x32_bf16 v[204:207], v[28:31], v[228:231], v[204:207]\n\t" \
    "ds_read_b128 v[224:227], v252 offset:1024\n\t" \
    "v_mfma_f32_16x16x32_bf16 v[208:211], v[36:39], v[228:231], v[208:211]\n\t" \
    "s_waitcnt lgkmcnt(3)\n\t" \
    "v_mfma_f32_16x16x32_bf16 v[216:219], v[28:31], v[232:235], v[216:219]\n\t" \
    "ds_read_b128 v[228:231], v252 offset:17408\n\t" \
    "v_mfma_f32_16x16x32_bf16 v[220:223], v[36:39], v[232:235], v[220:223]\n\t" \
    "s_waitcnt lgkmcnt(3)\n\t" \
    "v_mfma_f32_16x16x32_bf16 v[204:207], v[40:43], v[236:239], v[204:207]\n\t" \
    "v_xor_b32 v252, 0x1800, %[xa]\n\t" \
    "ds_read_b128 v[232:235], v252 offset:0\n\t" \
    "v_mfma_f32_16x16x32_bf16 v[208:211], v[48:51], v[236:239], v[208:211]\n\t" \
    "s_waitcnt lgkmcnt(3)\n\t" \
    "v_mfma_f32_16x16x32_bf16 v[216:219], v[40:43], v[240:243], v[216:219]\n\t" \
    "ds_read_b128 v[236:239], v252 offset:16384\n\t" \
    "v_mfma_f32_16x16x32_bf16 v[220:223], v[48:51], v[240:243], v[220:223]\n\t" \
    "s_waitcnt lgkmcnt(3)\n\t" \
    "v_mfma_f32_16x16x32_bf16 v[204:207], v[44:47], v[224:227], v[204:207]\n\t" \
    "ds_read_b128 v[240:243], v252 offset:1024\n\t" \
    "v_mfma_f32_16x16x32_bf16 v[208:211], v[52:55], v[224:227], v[208:211]\n\t" \
    "s_waitcnt lgkmcnt(3)\n\t" \
    "v_mfma_f32_16x16x32_bf16 v[216:219], v[44:47], v[228:231], v[216:219]\n\t" \
    "ds_read_b128 v[224:227], v252 offset:17408\n\t" \
    "v_mfma_f32_16x16x32_bf16 v[220:223], v[52:55], v[228:231], v[220:223]\n\t" \
    "s_waitcnt lgkmcnt(3)\n\t" \
    "v_mfma_f32_16x16x32_bf16 v[204:207], v[64:67], v[232:235], v[204:207]\n\t" \
    "v_and_b32 v252, 0x3ff, %[xa]\n\t" \
    "ds_read_b128 v[228:231], v252 offset:40960\n\t" \
    "v_mfma_f32_16x16x32_bf16 v[208:211], v[72:75], v[232:235], v[208:211]\n\t" \
    "s_waitcnt lgkmcnt(3)\n\t" \
    "v_mfma_f32_16x16x32_bf16 v[216:219], v[64:67], v[236:239], v[216:219]\n\t" \
    "ds_read_b128 v[232:235], v252 offset:45056\n\t" \
    "v_mfma_f32_16x16x32_bf16 v[220:223], v[72:75], v[236:239], v[220:223]\n\t" \
    "s_waitcnt lgkmcnt(3)\n\t" \
    "v_mfma_f32_16x16x32_bf16 v[204:207], v[68:71], v[240:243], v[204:207]\n\t" \
    "ds_read_b128 v[236:239], v252 offset:41984\n\t" \
    "v_mfma_f32_16x16x32_bf16 v[208:211], v[76:79], v[240:243], v[208:211]\n\t" \
    "s_waitcnt lgkmcnt(3)\n\t" \
    "v_mfma_f32_16x16x32_bf16 v[216:219], v[68:71], v[224:227], v[216:219]\n\t" \
    "ds_read_b128 v[240:243], v252 offset:46080\n\t" \
    "v_mfma_f32_16x16x32_bf16 v[220:223], v[76:79], v[224:227], v[220:223]\n\t" \
    "s_waitcnt lgkmcnt(0)\n\t" \
    "s_barrier\n\t" ASM_ST \
    "v_mfma_f32_16x16x32_bf16 v[204:207], v[160:163], v[228:231], v[204:207]\n\t" \
    "v_xor_b32 v252, 0x2000, %[xa]\n\t" \
    "ds_read_b128 v[224:227], v252 offset:0\n\t" \
    "v_mfma_f32_16x16x32_bf16 v[208:211], v[168:171], v[228:231], v[208:211]\n\t" \
    "v_mfma_f32_16x16x32_bf16 v[216:219], v[160:163], v[232:235], v[216:219]\n\t" \
    "ds_read_b128 v[228:231], v252 offset:16384\n\t" \
    "v_mfma_f32_16x16x32_bf16 v[220:223], v[168:171], v[232:235], v[220:223]\n\t" \
    "v_mfma_f32_16x16x32_bf16 v[204:207], v[164:167], v[236:239], v[204:207]\n\t" \
    "ds_read_b128 v[232:235], v252 offset:1024\n\t" \
    "v_mfma_f32_16x16x32_bf16 v[208:211], v[172:175], v[236:239], v[208:211]\n\t" \
    "v_mfma_f32_16x16x32_bf16 v[216:219], v[164:167], v[240:243], v[216:219]\n\t" \
    "ds_read_b128 v[236:239], v252 offset:17408\n\t" \
    "v_mfma_f32_16x16x32_bf16 v[220:223], v[172:175], v[240:243], v[220:223]\n\t" \
    "s_waitcnt lgkmcnt(3)\n\t" \
    "v_mfma_f32_16x16x32_bf16 v[204:207], v[80:83], v[224:227], v[204:207]\n\t" \
    "v_xor_b32 v252, 0x2800, %[xa]\n\t" \
    "ds_read_b128 v[240:243], v252 offset:0\n\t" \
    "v_mfma_f32_16x16x32_bf16 v[208:211], v[88:91], v[224:227], v[208:211]\n\t" \
    "s_waitcnt lgkmcnt(3)\n\t" \
    "v_mfma_f32_16x16x32_bf16 v[216:219], v[80:83], v[228:231], v[216:219]\n\t" \
    "ds_read_b128 v[224:227], v252 offset:16384\n\t" \
    "v_mfma_f32_16x16x32_bf16 v[220:223], v[88:91], v[228:231], v[220:223]\n\t" \
    "s_waitcnt lgkmcnt(3)\n\t" \
    "v_mfma_f32_16x16x32_bf16 v[204:207], v[84:87], v[232:235], v[204:207]\n\t" \
    "ds_read_b128 v[228:231], v252 offset:1024\n\t" \
    "v_mfma_f32_16x16x32_bf16 v[208:211], v[92:95], v[232:235], v[208:211]\n\t" \
    "s_waitcnt lgkmcnt(3)\n\t" \
    "v_mfma_f32_16x16x32_bf16 v[216:219], v[84:87], v[236:239], v[216:219]\n\t" \
    "ds_read_b128 v[232:235], v252 offset:17408\n\t" \
    "v_mfma_f32_16x16x32_bf16 v[220:223], v[92:95], v[236:239], v[220:223]\n\t" \
    "s_waitcnt lgkmcnt(3)\n\t" \
    "v_mfma_f32_16x16x32_bf16 v[204:207], v[104:107], v[240:243], v[204:207]\n\t" \
    "v_xor_b32 v252, 0x3000, %[xa]\n\t" \
    "ds_read_b128 v[236:239], v252 offset:0\n\t" \
    "v_mfma_f32_16x16x32_bf16 v[208:211], v[112:115], v[240:243], v[208:211]\n\t" \
    "s_waitcnt lgkmcnt(3)\n\t" \
    "v_mfma_f32_16x16x32_bf16 v[216:219], v[104:107], v[224:227], v[216:219]\n\t" \
    "ds_read_b128 v[240:243], v252 offset:16384\n\t" \
    "v_mfma_f32_16x16x32_bf16 v[220:223], v[112:115], v[224:227], v[220:223]\n\t" \
    "s_waitcnt lgkmcnt(3)\n\t" \
    "v_mfma_f32_16x16x32_bf16 v[204:207], v[108:111], v[228:231], v[204:207]\n\t" \
    "ds_read_b128 v[224:227], v252 offset:1024\n\t" \
    "v_mfma_f32_16x16x32_bf16 v[208:211], v[116:119], v[228:231], v[208:211]\n\t" \
    "s_waitcnt lgkmcnt(3)\n\t" \
    "v_mfma_f32_16x16x32_bf16 v[216:219], v[108:111], v[232:235], v[216:219]\n\t" \
    "ds_read_b128 v[228:231], v252 offset:17408\n\t" \
    "v_mfma_f32_16x16x32_bf16 v[220:223], v[116:119], v[232:235], v[220:223]\n\t" \
    "s_waitcnt lgkmcnt(3)\n\t" \
    "v_mfma_f32_16x16x32_bf16 v[204:207], v[120:123], v[236:239], v[204:207]\n\t" \
    "v_xor_b32 v252, 0x3800, %[xa]\n\t" \
    "ds_read_b128 v[232:235], v252 offset:0\n\t" \
    "v_mfma_f32_16x16x32_bf16 v[208:211], v[128:131], v[236:239], v[208:211]\n\t" \
    "s_waitcnt lgkmcnt(3)\n\t" \
    "v_mfma_f32_16x16x32_bf16 v[216:219], v[120:123], v[240:243], v[216:219]\n\t" \
    "ds_read_b128 v[236:239], v252 offset:16384\n\t" \
    "v_mfma_f32_16x16x32_bf16 v[220:223], v[128:131], v[240:243], v[220:223]\n\t" \
    "s_waitcnt lgkmcnt(3)\n\t" \
    "v_mfma_f32_16x16x32_bf16 v[204:207], v[124:127], v[224:227], v[204:207]\n\t" \
    "ds_read_b128 v[240:243], v252 offset:1024\n\t" \
    "v_mfma_f32_16x16x32_bf16 v[208:211], v[132:135], v[224:227], v[208:211]\n\t" \
    "s_waitcnt lgkmcnt(3)\n\t" \
    "v_mfma_f32_16x16x32_bf16 v[216:219], v[124:127], v[228:231], v[216:219]\n\t" \
    "ds_read_b128 v[224:227], v252 offset:17408\n\t" \
    "v_mfma_f32_16x16x32_bf16 v[220:223], v[132:135], v[228:231], v[220:223]\n\t" \
    "s_waitcnt lgkmcnt(3)\n\t" \
    "v_mfma_f32_16x16x32_bf16 v[204:207], v[144:147], v[232:235], v[204:207]\n\t" \
    "v_and_b32 v252, 0x3ff, %[xa]\n\t" \
    "ds_read_b128 v[228:231], v252 offset:43008\n\t" \
    "v_mov_b64 v[244:245], 0\n\t" \
    "v_mfma_f32_16x16x32_bf16 v[208:211], v[152:155], v[232:235], v[208:211]\n\t" \
    "v_mov_b64 v[246:247], 0\n\t" \
    "s_waitcnt lgkmcnt(3)\n\t" \
    "v_mfma_f32_16x16x32_bf16 v[216:219], v[144:147], v[236:239], v[216:219]\n\t" \
    "ds_read_b128 v[232:235], v252 offset:44032\n\t" \
    "v_mov_b64 v[248:249], 0\n\t" \
    "v_mfma_f32_16x16x32_bf16 v[220:223], v[152:155], v[236:239], v[220:223]\n\t" \
    "v_mov_b64 v[250:251], 0\n\t" \
    "s_waitcnt lgkmcnt(3)\n\t" \
    "v_mfma_f32_16x16x32_bf16 v[204:207], v[148:151], v[240:243], v[204:207]\n\t" \
    "ds_read_b128 v[236:239], v252 offset:47104\n\t" \
    "v_mfma_f32_16x16x32_bf16 v[208:211], v[156:159], v[240:243], v[208:211]\n\t" \
    "s_waitcnt lgkmcnt(3)\n\t" \
    "v_mfma_f32_16x16x32_bf16 v[216:219], v[148:151], v[224:227], v[216:219]\n\t" \
    "ds_read_b128 v[240:243], v252 offset:48128\n\t" \
    "v_mfma_f32_16x16x32_bf16 v[220:223], v[156:159], v[224:227], v[220:223]\n\t" \
    "s_waitcnt lgkmcnt(3)\n\t" \
    "v_mfma_f32_16x16x32_bf16 v[204:207], v[184:187], v[228:231], v[204:207]\n\t" \
    "v_add_u32 v252, %[qs], v252\n\t" \
    "v_mfma_f32_16x16x32_bf16 v[208:211], v[192:195], v[228:231], v[208:211]\n\t" \
    "v_mov_b64 v[224:225], 0\n\t" \
    "s_waitcnt lgkmcnt(2)\n\t" \
    "v_mfma_f32_16x16x32_bf16 v[204:207], v[188:191], v[232:235], v[204:207]\n\t" \
    "v_mov_b64 v[226:227], 0\n\t" \
    "v_mfma_f32_16x16x32_bf16 v[208:211], v[196:199], v[232:235], v[208:211]\n\t" \
    "s_waitcnt lgkmcnt(1)\n\t" \
    "v_mfma_f32_16x16x32_bf16 v[216:219], v[184:187], v[236:239], v[216:219]\n\t" \
    "ds_read_b128 v[228:231], %[wl] offset:2048\n\t" \
    "ds_read_b128 v[232:235], %[wl] offset:3072\n\t" \
    "v_mfma_f32_16x16x32_bf16 v[220:223], v[192:195], v[236:239], v[220:223]\n\t" \
    "v_cvt_pk_bf16_f32 v204, v204, v205\n\t" \
    "v_cvt_pk_bf16_f32 v205, v206, v207\n\t" \
    "s_waitcnt lgkmcnt(2)\n\t" \
    "v_mfma_f32_16x16x32_bf16 v[216:219], v[188:191], v[240:243], v[216:219]\n\t" \
    "ds_write_b64 v252, v[204:205] offset:1024\n\t" \
    "v_cvt_pk_bf16_f32 v208, v208, v209\n\t" \
    "v_mfma_f32_16x16x32_bf16 v[220:223], v[196:199], v[240:243], v[220:223]\n\t" \
    "v_cvt_pk_bf16_f32 v209, v210, v211\n\t" \
    "ds_write_b64 v252, v[208:209] offset:2048\n\t" \
    "ds_read_b128 v[200:203], %[wl] offset:0\n\t" \
    "ds_read_b128 v[204:207], %[wl] offset:1024\n\t" \
    "v_cvt_pk_bf16_f32 v216, v216, v217\n\t" \
    "v_cvt_pk_bf16_f32 v217, v218, v219\n\t" \
    "ds_write_b64 v252, v[216:217] offset:7168\n\t" \
    "s_nop 0\n\t" \
    "v_cvt_pk_bf16_f32 v220, v220, v221\n\t" \
    "v_cvt_pk_bf16_f32 v221, v222, v223\n\t" \
    "ds_write_b64 v252, v[220:221] offset:8192\n\t" \
    "s_waitcnt lgkmcnt(0)\n\t"
#define ASM_XG_S_LO1 \
    "s_waitcnt lgkmcnt(0)\n\t" \
    "ds_read_b128 v[224:227], %[xa] offset:0\n\t" \
    "ds_read_b128 v[228:231], %[xa] offset:16384\n\t" \
    "ds_read_b128 v[232:235], %[xa] offset:1024\n\t" \
    "ds_read_b128 v[236:239], %[xa] offset:17408\n\t" \
    "s_waitcnt lgkmcnt(3)\n\t" \
    "v_mfma_f32_16x16x32_bf16 v[208:211], v[8:11], v[224:227], 0\n\t" \
    "v_xor_b32 v252, 0x800, %[xa]\n\t" \
    "ds_read_b128 v[240:243], v252 offset:0\n\t" \
    "s_waitcnt lgkmcnt(3)\n\t" \
    "v_mfma_f32_16x16x32_bf16 v[220:223], v[8:11], v[228:231], 0\n\t" \
    "ds_read_b128 v[224:227], v252 offset:16384\n\t" \
    "s_waitcnt lgkmcnt(3)\n\t" \
    "v_mfma_f32_16x16x32_bf16 v[208:211], v[12:15], v[232:235], v[208:211]\n\t" \
    "ds_read_b128 v[228:231], v252 offset:1024\n\t" \
    "s_waitcnt lgkmcnt(3)\n\t" \
    "v_mfma_f32_16x16x32_bf16 v[220:223], v[12:15], v[236:239], v[220:223]\n\t" \
    "ds_read_b128 v[232:235], v252 offset:17408\n\t" \
    "s_waitcnt lgkmcnt(3)\n\t" \
    "v_mfma_f32_16x16x32_bf16 v[208:211], v[32:35], v[240:243], v[208:211]\n\t" \
    "v_xor_b32 v252, 0x1000, %[xa]\n\t" \
    "ds_read_b128 v[236:239], v252 offset:0\n\t" \
    "s_waitcnt lgkmcnt(3)\n\t" \
    "v_mfma_f32_16x16x32_bf16 v[220:223], v[32:35], v[224:227], v[220:223]\n\t" \
    "ds_read_b128 v[240:243], v252 offset:16384\n\t" \
    "s_waitcnt lgkmcnt(3)\n\t" \
    "v_mfma_f32_16x16x32_bf16 v[208:211], v[36:39], v[228:231], v[208:211]\n\t" \
    "ds_read_b128 v[224:227], v252 offset:1024\n\t" \
    "s_waitcnt lgkmcnt(3)\n\t" \
    "v_mfma_f32_16x16x32_bf16 v[220:223], v[36:39], v[232:235], v[220:223]\n\t" \
    "ds_read_b128 v[228:231], v252 offset:17408\n\t" \
    "s_waitcnt lgkmcnt(3)\n\t" \
    "v_mfma_f32_16x16x32_bf16 v[208:211], v[48:51], v[236:239], v[208:211]\n\t" \
    "v_xor_b32 v252, 0x1800, %[xa]\n\t" \
    "ds_read_b128 v[232:235], v252 offset:0\n\t" \
    "s_waitcnt lgkmcnt(3)\n\t" \
    "v_mfma_f32_16x16x32_bf16 v[220:223], v[48:51], v[240:243], v[220:223]\n\t" \
    "ds_read_b128 v[236:239], v252 offset:16384\n\t" \
    "s_waitcnt lgkmcnt(3)\n\t" \
    "v_mfma_f32_16x16x32_bf16 v[208:211], v[52:55], v[224:227], v[208:211]\n\t" \
    "ds_read_b128 v[240:243], v252 offset:1024\n\t" \
    "s_waitcnt lgkmcnt(3)\n\t" \
    "v_mfma_f32_16x16x32_bf16 v[220:223], v[52:55], v[228:231], v[220:223]\n\t" \
    "ds_read_b128 v[224:227], v252 offset:17408\n\t" \
    "s_waitcnt lgkmcnt(3)\n\t" \
    "v_mfma_f32_16x16x32_bf16 v[208:211], v[72:75], v[232:235], v[208:211]\n\t" \
    "v_and_b32 v252, 0x3ff, %[xa]\n\t" \
    "ds_read_b128 v[228:231], v252 offset:40960\n\t" \
    "s_waitcnt lgkmcnt(3)\n\t" \
    "v_mfma_f32_16x16x32_bf16 v[220:223], v[72:75], v[236:239], v[220:223]\n\t" \
    "ds_read_b128 v[232:235], v252 offset:45056\n\t" \
    "s_waitcnt lgkmcnt(3)\n\t" \
    "v_mfma_f32_16x16x32_bf16 v[208:211], v[76:79], v[240:243], v[208:211]\n\t" \
    "ds_read_b128 v[236:239], v252 offset:41984\n\t" \
    "s_waitcnt lgkmcnt(3)\n\t" \
    "v_mfma_f32_16x16x32_bf16 v[220:223], v[76:79], v[224:227], v[220:223]\n\t" \
    "ds_read_b128 v[240:243], v252 offset:46080\n\t" \
    "s_waitcnt lgkmcnt(0)\n\t" \
    "s_barrier\n\t" ASM_ST \
    "v_mfma_f32_16x16x32_bf16 v[208:211], v[168:171], v[228:231], v[208:211]\n\t" \
    "v_xor_b32 v252, 0x2000, %[xa]\n\t" \
    "ds_read_b128 v[224:227], v252 offset:0\n\t" \
    "v_mfma_f32_16x16x32_bf16 v[220:223], v[168:171], v[232:235], v[220:223]\n\t" \
    "ds_read_b128 v[228:231], v252 offset:16384\n\t" \
    "v_mfma_f32_16x16x32_bf16 v[208:211], v[172:175], v[236:239], v[208:211]\n\t" \
    "ds_read_b128 v[232:235], v252 offset:1024\n\t" \
    "v_mfma_f32_16x16x32_bf16 v[220:223], v[172:175], v[240:243], v[220:223]\n\t" \
    "ds_read_b128 v[236:239], v252 offset:17408\n\t" \
    "s_waitcnt lgkmcnt(3)\n\t" \
    "v_mfma_f32_16x16x32_bf16 v[208:211], v[88:91], v[224:227], v[208:211]\n\t" \
    "v_xor_b32 v252, 0x2800, %[xa]\n\t" \
    "ds_read_b128 v[240:243], v252 offset:0\n\t" \
    "s_waitcnt lgkmcnt(3)\n\t" \
    "v_mfma_f32_16x16x32_bf16 v[220:223], v[88:91], v[228:231], v[220:223]\n\t" \
    "ds_read_b128 v[224:227], v252 offset:16384\n\t" \
    "s_waitcnt lgkmcnt(3)\n\t" \
    "v_mfma_f32_16x16x32_bf16 v[208:211], v[92:95], v[232:235], v[208:211]\n\t" \
    "ds_read_b128 v[228:231], v252 offset:1024\n\t" \
    "s_waitcnt lgkmcnt(3)\n\t" \
    "v_mfma_f32_16x16x32_bf16 v[220:223], v[92:95], v[236:239], v[220:223]\n\t" \
    "ds_read_b128 v[232:235], v252 offset:17408\n\t" \
    "s_waitcnt lgkmcnt(3)\n\t" \
    "v_mfma_f32_16x16x32_bf16 v[208:211], v[112:115], v[240:243], v[208:211]\n\t" \
    "v_xor_b32 v252, 0x3000, %[xa]\n\t" \
    "ds_read_b128 v[236:239], v252 offset:0\n\t" \
    "s_waitcnt lgkmcnt(3)\n\t" \
    "v_mfma_f32_16x16x32_bf16 v[220:223], v[112:115], v[224:227], v[220:223]\n\t" \
    "ds_read_b128 v[240:243], v252 offset:16384\n\t" \
    "s_waitcnt lgkmcnt(3)\n\t" \
    "v_mfma_f32_16x16x32_bf16 v[208:211], v[116:119], v[228:231], v[208:211]\n\t" \
    "ds_read_b128 v[224:227], v252 offset:1024\n\t" \
    "s_waitcnt lgkmcnt(3)\n\t" \
    "v_mfma_f32_16x16x32_bf16 v[220:223], v[116:119], v[232:235], v[220:223]\n\t" \
    "ds_read_b128 v[228:231], v252 offset:17408\n\t" \
    "s_waitcnt lgkmcnt(3)\n\t" \
    "v_mfma_f32_16x16x32_bf16 v[208:211], v[128:131], v[236:239], v[208:211]\n\t" \
    "v_xor_b32 v252, 0x3800, %[xa]\n\t" \
    "ds_read_b128 v[232:235], v252 offset:0\n\t" \
    "s_waitcnt lgkmcnt(3)\n\t" \
    "v_mfma_f32_16x16x32_bf16 v[220:223], v[128:131], v[240:243], v[220:223]\n\t" \
    "ds_read_b128 v[236:239], v252 offset:16384\n\t" \
    "s_waitcnt lgkmcnt(3)\n\t" \
    "v_mfma_f32_16x16x32_bf16 v[208:211], v[132:135], v[224:227], v[208:211]\n\t" \
    "ds_read_b128 v[240:243], v252 offset:1024\n\t" \
    "s_waitcnt lgkmcnt(3)\n\t" \
    "v_mfma_f32_16x16x32_bf16 v[220:223], v[132:135], v[228:231], v[220:223]\n\t" \
    "ds_read_b128 v[224:227], v252 offset:17408\n\t" \
    "s_waitcnt lgkmcnt(3)\n\t" \
    "v_mfma_f32_16x16x32_bf16 v[208:211], v[152:155], v[232:235], v[208:211]\n\t" \
    "v_and_b32 v252, 0x3ff, %[xa]\n\t" \
    "ds_read_b128 v[228:231], v252 offset:43008\n\t" \
    "v_mov_b64 v[244:245], 0\n\t" \
    "s_waitcnt lgkmcnt(3)\n\t" \
    "v_mfma_f32_16x16x32_bf16 v[220:223], v[152:155], v[236:239], v[220:223]\n\t" \
    "ds_read_b128 v[232:235], v252 offset:44032\n\t" \
    "v_mov_b64 v[246:247], 0\n\t" \
    "s_waitcnt lgkmcnt(3)\n\t" \
    "v_mfma_f32_16x16x32_bf16 v[208:211], v[156:159], v[240:243], v[208:211]\n\t" \
    "ds_read_b128 v[236:239], v252 offset:47104\n\t" \
    "v_mov_b64 v[248:249], 0\n\t" \
    "s_waitcnt lgkmcnt(3)\n\t" \
    "v_mfma_f32_16x16x32_bf16 v[220:223], v[156:159], v[224:227], v[220:223]\n\t" \
    "ds_read_b128 v[240:243], v252 offset:48128\n\t" \
    "v_mov_b64 v[250:251], 0\n\t" \
    "s_waitcnt lgkmcnt(3)\n\t" \
    "v_mfma_f32_16x16x32_bf16 v[208:211], v[192:195], v[228:231], v[208:211]\n\t" \
    "v_add_u32 v252, %[qs], v252\n\t" \
    "s_waitcnt lgkmcnt(2)\n\t" \
    "v_mfma_f32_16x16x32_bf16 v[208:211], v[196:199], v[232:235], v[208:211]\n\t" \
    "v_mov_b64 v[224:225], 0\n\t" \
    "s_waitcnt lgkmcnt(1)\n\t" \
    "v_mfma_f32_16x16x32_bf16 v[220:223], v[192:195], v[236:239], v[220:223]\n\t" \
    "v_mov_b64 v[226:227], 0\n\t" \
    "ds_read_b128 v[228:231], %[wl] offset:2048\n\t" \
    "s_waitcnt lgkmcnt(1)\n\t" \
    "v_mfma_f32_16x16x32_bf16 v[220:223], v[196:199], v[240:243], v[220:223]\n\t" \
    "ds_read_b128 v[232:235], %[wl] offset:3072\n\t" \
    "v_cvt_pk_bf16_f32 v208, v208, v209\n\t" \
    "v_cvt_pk_bf16_f32 v209, v210, v211\n\t" \
    "ds_write_b64 v252, v[208:209] offset:2048\n\t" \
    "ds_read_b128 v[200:203], %[wl] offset:0\n\t" \
    "ds_read_b128 v[204:207], %[wl] offset:1024\n\t" \
    "s_nop 1\n\t" \
    "v_cvt_pk_bf16_f32 v220, v220, v221\n\t" \
    "v_cvt_pk_bf16_f32 v221, v222, v223\n\t" \
    "ds_write_b64 v252, v[220:221] offset:8192\n\t" \
    "s_waitcnt lgkmcnt(0)\n\t"
#define ASM_XG_H_HI \
    "buffer_load_dword %[fv], %[fv], %[rr], %[fs] offen sc1\n\t" \
    "s_waitcnt lgkmcnt(0)\n\t" \
    "v_xor_b32 v252, 0x2000, %[xa]\n\t" \
    "ds_read_b128 v[224:227], v252 offset:0\n\t" \
    "ds_read_b128 v[244:247], %[wl] offset:4096\n\t" \
    "ds_read_b128 v[228:231], v252 offset:16384\n\t" \
    "ds_read_b128 v[232:235], v252 offset:1024\n\t" \
    "ds_read_b128 v[236:239], v252 offset:17408\n\t" \
    "ds_read_b128 v[248:251], %[wl] offset:5120\n\t" \
    "s_waitcnt lgkmcnt(5)\n\t" \
    "v_mfma_f32_16x16x32_bf16 v[204:207], v[80:83], v[224:227], 0\n\t" \
    "v_xor_b32 v252, 0x2800, %[xa]\n\t" \
    "ds_read_b128 v[240:243], v252 offset:0\n\t" \
    "v_mfma_f32_16x16x32_bf16 v[208:211], v[88:91], v[224:227], 0\n\t" \
    "s_waitcnt lgkmcnt(5)\n\t" \
    "v_mfma_f32_16x16x32_bf16 v[200:203], v[244:247], v[224:227], 0\n\t" \
    "s_waitcnt lgkmcnt(4)\n\t" \
    "v_mfma_f32_16x16x32_bf16 v[216:219], v[80:83], v[228:231], 0\n\t" \
    "ds_read_b128 v[224:227], v252 offset:16384\n\t" \
    "v_mfma_f32_16x16x32_bf16 v[220:223], v[88:91], v[228:231], 0\n\t" \
    "v_mfma_f32_16x16x32_bf16 v[212:215], v[244:247], v[228:231], 0\n\t" \
    "s_waitcnt lgkmcnt(4)\n\t" \
    "v_mfma_f32_16x16x32_bf16 v[204:207], v[84:87], v[232:235], v[204:207]\n\t" \
    "ds_read_b128 v[228:231], v252 offset:1024\n\t" \
    "v_mfma_f32_16x16x32_bf16 v[208:211], v[92:95], v[232:235], v[208:211]\n\t" \
    "ds_read_b128 v[244:247], %[wl] offset:6144\n\t" \
    "s_waitcnt lgkmcnt(4)\n\t" \
    "v_mfma_f32_16x16x32_bf16 v[200:203], v[248:251], v[232:235], v[200:203]\n\t" \
    "v_mfma_f32_16x16x32_bf16 v[216:219], v[84:87], v[236:239], v[216:219]\n\t" \
    "ds_read_b128 v[232:235], v252 offset:17408\n\t" \
    "v_mfma_f32_16x16x32_bf16 v[220:223], v[92:95], v[236:239], v[220:223]\n\t" \
    "v_mfma_f32_16x16x32_bf16 v[212:215], v[248:251], v[236:239], v[212:215]\n\t" \
    "s_waitcnt lgkmcnt(4)\n\t" \
    "v_mfma_f32_16x16x32_bf16 v[204:207], v[104:107], v[240:243], v[204:207]\n\t" \
    "v_xor_b32 v252, 0x3000, %[xa]\n\t" \
    "ds_read_b128 v[236:239], v252 offset:0\n\t" \
    "v_mfma_f32_16x16x32_bf16 v[208:211], v[112:115], v[240:243], v[208:211]\n\t" \
    "ds_read_b128 v[248:251], %[wl] offset:7168\n\t" \
    "v_mfma_f32_16x16x32_bf16 v[200:203], v[96:99], v[240:243], v[200:203]\n\t" \
    "s_waitcnt lgkmcnt(5)\n\t" \
    "v_mfma_f32_16x16x32_bf16 v[216:219], v[104:107], v[224:227], v[216:219]\n\t" \
    "ds_read_b128 v[240:243], v252 offset:16384\n\t" \
    "v_mfma_f32_16x16x32_bf16 v[220:223], v[112:115], v[224:227], v[220:223]\n\t" \
    "v_mfma_f32_16x16x32_bf16 v[212:215], v[96:99], v[224:227], v[212:215]\n\t" \
    "s_waitcnt lgkmcnt(5)\n\t" \
    "v_mfma_f32_16x16x32_bf16 v[204:207], v[108:111], v[228:231], v[204:207]\n\t" \
    "ds_read_b128 v[224:227], v252 offset:1024\n\t" \
    "v_mfma_f32_16x16x32_bf16 v[208:211], v[116:119], v[228:231], v[208:211]\n\t" \
    "v_mfma_f32_16x16x32_bf16 v[200:203], v[100:103], v[228:231], v[200:203]\n\t" \
    "s_waitcnt lgkmcnt(4)\n\t" \
    "v_mfma_f32_16x16x32_bf16 v[216:219], v[108:111], v[232:235], v[216:219]\n\t" \
    "ds_read_b128 v[228:231], v252 offset:17408\n\t" \
    "v_mfma_f32_16x16x32_bf16 v[220:223], v[116:119], v[232:235], v[220:223]\n\t" \
    "v_mfma_f32_16x16x32_bf16 v[212:215], v[100:103], v[232:235], v[212:215]\n\t" \
    "s_waitcnt lgkmcnt(4)\n\t" \
    "v_mfma_f32_16x16x32_bf16 v[204:207], v[120:123], v[236:239], v[204:207]\n\t" \
    "v_xor_b32 v252, 0x3800, %[xa]\n\t" \
    "ds_read_b128 v[232:235], v252 offset:0\n\t" \
    "v_mfma_f32_16x16x32_bf16 v[208:211], v[128:131], v[236:239], v[208:211]\n\t" \
    "v_mfma_f32_16x16x32_bf16 v[200:203], v[244:247], v[236:239], v[200:203]\n\t" \
    "s_waitcnt lgkmcnt(3)\n\t" \
    "v_mfma_f32_16x16x32_bf16 v[216:219], v[120:123], v[240:243], v[216:219]\n\t" \
    "ds_read_b128 v[236:239], v252 offset:16384\n\t" \
    "v_mfma_f32_16x16x32_bf16 v[220:223], v[128:131], v[240:243], v[220:223]\n\t" \
    "v_mfma_f32_16x16x32_bf16 v[212:215], v[244:247], v[240:243], v[212:215]\n\t" \
    "s_waitcnt lgkmcnt(3)\n\t" \
    "v_mfma_f32_16x16x32_bf16 v[204:207], v[124:127], v[224:227], v[204:207]\n\t" \
    "ds_read_b128 v[240:243], v252 offset:1024\n\t" \
    "v_mfma_f32_16x16x32_bf16 v[208:211], v[132:135], v[224:227], v[208:211]\n\t" \
    "ds_read_b128 v[244:247], %[wl] offset:8192\n\t" \
    "v_mfma_f32_16x16x32_bf16 v[200:203], v[248:251], v[224:227], v[200:203]\n\t" \
    "s_waitcnt lgkmcnt(4)\n\t" \
    "v_mfma_f32_16x16x32_bf16 v[216:219], v[124:127], v[228:231], v[216:219]\n\t" \
    "ds_read_b128 v[224:227], v252 offset:17408\n\t" \
    "v_mfma_f32_16x16x32_bf16 v[220:223], v[132:135], v[228:231], v[220:223]\n\t" \
    "v_mfma_f32_16x16x32_bf16 v[212:215], v[248:251], v[228:231], v[212:215]\n\t" \
    "s_waitcnt lgkmcnt(4)\n\t" \
    "v_mfma_f32_16x16x32_bf16 v[204:207], v[144:147], v[232:235], v[204:207]\n\t" \
    "v_and_b32 v252, 0x3ff, %[xa]\n\t" \
    "ds_read_b128 v[228:231], v252 offset:40960\n\t" \
    "v_mfma_f32_16x16x32_bf16 v[208:211], v[152:155], v[232:235], v[208:211]\n\t" \
    "ds_read_b128 v[248:251], %[wl] offset:9216\n\t" \
    "v_mfma_f32_16x16x32_bf16 v[200:203], v[136:139], v[232:235], v[200:203]\n\t" \
    "s_waitcnt lgkmcnt(5)\n\t" \
    "v_mfma_f32_16x16x32_bf16 v[216:219], v[144:147], v[236:239], v[216:219]\n\t" \
    "ds_read_b128 v[232:235], v252 offset:45056\n\t" \
    "v_mfma_f32_16x16x32_bf16 v[220:223], v[152:155], v[236:239], v[220:223]\n\t" \
    "v_mfma_f32_16x16x32_bf16 v[212:215], v[136:139], v[236:239], v[212:215]\n\t" \
    "s_waitcnt lgkmcnt(5)\n\t" \
    "v_mfma_f32_16x16x32_bf16 v[204:207], v[148:151], v[240:243], v[204:207]\n\t" \
    "ds_read_b128 v[236:239], v252 offset:41984\n\t" \
    "v_mfma_f32_16x16x32_bf16 v[208:211], v[156:159], v[240:243], v[208:211]\n\t" \
    "v_mfma_f32_16x16x32_bf16 v[200:203], v[140:143], v[240:243], v[200:203]\n\t" \
    "s_waitcnt lgkmcnt(4)\n\t" \
    "v_mfma_f32_16x16x32_bf16 v[216:219], v[148:151], v[224:227], v[216:219]\n\t" \
    "ds_read_b128 v[240:243], v252 offset:46080\n\t" \
    "v_mfma_f32_16x16x32_bf16 v[220:223], v[156:159], v[224:227], v[220:223]\n\t" \
    "v_mfma_f32_16x16x32_bf16 v[212:215], v[140:143], v[224:227], v[212:215]\n\t" \
    "s_nop 3\n\t" \
    "v_lshrrev_b32 v224, 2, v252\n\t" \
    "v_mov_b32 v225, %[tv]\n\t" \
    "s_cmp_eq_u32 %[go], 0\n\t" \
    "s_waitcnt vmcnt(0) lgkmcnt(0)\n\t" \
    "s_cbranch_scc1 .Lxp%=\n\t" \
    "s_mov_b32 exec_hi, 0\n\t" \
    "buffer_store_dword v225, v224, %[rr], %[fp] offen sc1\n\t" \
    "s_mov_b32 exec_hi, -1\n\t" \
    ".Lxp%=:\n\t" \
    "v_cmp_ge_u32 vcc, %[t1], %[fv]\n\t" \
    "s_barrier\n\t" ASM_ST \
    "s_cmp_eq_u64 vcc, 0\n\t" \
    "s_cselect_b32 %[go], 1, %[frc]\n\t" \
    "s_cmp_lt_u32 %[t1], 64\n\t" \
    "s_cselect_b32 %[go], %[go], 0\n\t" \
    "s_cmp_eq_u32 %[go], 0\n\t" \
    "s_cbranch_scc1 .Lxn%=\n\t" \
    "s_mov_b32 m0, %[d0]\n\t" \
    "s_nop 0\n\t" \
    "global_load_lds_dwordx4 v252, %[g0] offset:0 sc1\n\t" \
    "global_load_lds_dwordx4 v252, %[g0] offset:1024 sc1\n\t" \
    "global_load_lds_dwordx4 v252, %[g0] offset:2048 sc1\n\t" \
    "global_load_lds_dwordx4 v252, %[g0] offset:3072 sc1\n\t" \
    "s_mov_b32 m0, %[d1]\n\t" \
    "s_nop 0\n\t" \
    "global_load_lds_dwordx4 v252, %[g1] sc1\n\t" \
    ".Lxn%=:\n\t" \
    "v_mfma_f32_16x16x32_bf16 v[204:207], v[160:163], v[228:231], v[204:207]\n\t" \
    "ds_read_b128 v[224:227], %[xa] offset:0\n\t" \
    "v_mfma_f32_16x16x32_bf16 v[208:211], v[168:171], v[228:231], v[208:211]\n\t" \
    "v_mfma_f32_16x16x32_bf16 v[200:203], v[244:247], v[228:231], v[200:203]\n\t" \
    "v_mfma_f32_16x16x32_bf16 v[216:219], v[160:163], v[232:235], v[216:219]\n\t" \
    "ds_read_b128 v[228:231], %[xa] offset:16384\n\t" \
    "v_mfma_f32_16x16x32_bf16 v[220:223], v[168:171], v[232:235], v[220:223]\n\t" \
    "v_mfma_f32_16x16x32_bf16 v[212:215], v[244:247], v[232:235], v[212:215]\n\t" \
    "v_mfma_f32_16x16x32_bf16 v[204:207], v[164:167], v[236:239], v[204:207]\n\t" \
    "ds_read_b128 v[232:235], %[xa] offset:1024\n\t" \
    "v_mfma_f32_16x16x32_bf16 v[208:211], v[172:175], v[236:239], v[208:211]\n\t" \
    "ds_read_b128 v[244:247], %[wl] offset:0\n\t" \
    "v_mfma_f32_16x16x32_bf16 v[200:203], v[248:251], v[236:239], v[200:203]\n\t" \
    "v_mfma_f32_16x16x32_bf16 v[216:219], v[164:167], v[240:243], v[216:219]\n\t" \
    "ds_read_b128 v[236:239], %[xa] offset:17408\n\t" \
    "v_mfma_f32_16x16x32_bf16 v[220:223], v[172:175], v[240:243], v[220:223]\n\t" \
    "v_mfma_f32_16x16x32_bf16 v[212:215], v[248:251], v[240:243], v[212:215]\n\t" \
    "s_waitcnt lgkmcnt(4)\n\t" \
    "v_mfma_f32_16x16x32_bf16 v[204:207], v[0:3], v[224:227], v[204:207]\n\t" \
    "v_xor_b32 v252, 0x800, %[xa]\n\t" \
    "ds_read_b128 v[240:243], v252 offset:0\n\t" \
    "v_mfma_f32_16x16x32_bf16 v[208:211], v[8:11], v[224:227], v[208:211]\n\t" \
    "ds_read_b128 v[248:251], %[wl] offset:1024\n\t" \
    "s_waitcnt lgkmcnt(3)\n\t" \
    "v_mfma_f32_16x16x32_bf16 v[200:203], v[244:247], v[224:227], v[200:203]\n\t" \
    "v_mfma_f32_16x16x32_bf16 v[216:219], v[0:3], v[228:231], v[216:219]\n\t" \
    "ds_read_b128 v[224:227], v252 offset:16384\n\t" \
    "v_mfma_f32_16x16x32_bf16 v[220:223], v[8:11], v[228:231], v[220:223]\n\t" \
    "v_mfma_f32_16x16x32_bf16 v[212:215], v[244:247], v[228:231], v[212:215]\n\t" \
    "v_mfma_f32_16x16x32_bf16 v[204:207], v[4:7], v[232:235], v[204:207]\n\t" \
    "ds_read_b128 v[228:231], v252 offset:1024\n\t" \
    "v_mfma_f32_16x16x32_bf16 v[208:211], v[12:15], v[232:235], v[208:211]\n\t" \
    "ds_read_b128 v[244:247], %[wl] offset:2048\n\t" \
    "s_waitcnt lgkmcnt(3)\n\t" \
    "v_mfma_f32_16x16x32_bf16 v[200:203], v[248:251], v[232:235], v[200:203]\n\t" \
    "v_mfma_f32_16x16x32_bf16 v[216:219], v[4:7], v[236:239], v[216:219]\n\t" \
    "ds_read_b128 v[232:235], v252 offset:17408\n\t" \
    "v_mfma_f32_16x16x32_bf16 v[220:223], v[12:15], v[236:239], v[220:223]\n\t" \
    "v_mfma_f32_16x16x32_bf16 v[212:215], v[248:251], v[236:239], v[212:215]\n\t" \
    "v_mfma_f32_16x16x32_bf16 v[204:207], v[24:27], v[240:243], v[204:207]\n\t" \
    "v_xor_b32 v252, 0x1000, %[xa]\n\t" \
    "ds_read_b128 v[236:239], v252 offset:0\n\t" \
    "v_mfma_f32_16x16x32_bf16 v[208:211], v[32:35], v[240:243], v[208:211]\n\t" \
    "ds_read_b128 v[248:251], %[wl] offset:3072\n\t" \
    "v_mfma_f32_16x16x32_bf16 v[200:203], v[16:19], v[240:243], v[200:203]\n\t" \
    "s_waitcnt lgkmcnt(5)\n\t" \
    "v_mfma_f32_16x16x32_bf16 v[216:219], v[24:27], v[224:227], v[216:219]\n\t" \
    "ds_read_b128 v[240:243], v252 offset:16384\n\t" \
    "v_mfma_f32_16x16x32_bf16 v[220:223], v[32:35], v[224:227], v[220:223]\n\t" \
    "v_mfma_f32_16x16x32_bf16 v[212:215], v[16:19], v[224:227], v[212:215]\n\t" \
    "s_waitcnt lgkmcnt(5)\n\t" \
    "v_mfma_f32_16x16x32_bf16 v[204:207], v[28:31], v[228:231], v[204:207]\n\t" \
    "ds_read_b128 v[224:227], v252 offset:1024\n\t" \
    "v_mfma_f32_16x16x32_bf16 v[208:211], v[36:39], v[228:231], v[208:211]\n\t" \
    "v_mfma_f32_16x16x32_bf16 v[200:203], v[20:23], v[228:231], v[200:203]\n\t" \
    "s_waitcnt lgkmcnt(4)\n\t" \
    "v_mfma_f32_16x16x32_bf16 v[216:219], v[28:31], v[232:235], v[216:219]\n\t" \
    "ds_read_b128 v[228:231], v252 offset:17408\n\t" \
    "v_mfma_f32_16x16x32_bf16 v[220:223], v[36:39], v[232:235], v[220:223]\n\t" \
    "v_mfma_f32_16x16x32_bf16 v[212:215], v[20:23], v[232:235], v[212:215]\n\t" \
    "s_waitcnt lgkmcnt(4)\n\t" \
    "v_mfma_f32_16x16x32_bf16 v[204:207], v[40:43], v[236:239], v[204:207]\n\t" \
    "v_xor_b32 v252, 0x1800, %[xa]\n\t" \
    "ds_read_b128 v[232:235], v252 offset:0\n\t" \
    "v_mfma_f32_16x16x32_bf16 v[208:211], v[48:51], v[236:239], v[208:211]\n\t" \
    "v_mfma_f32_16x16x32_bf16 v[200:203], v[244:247], v[236:239], v[200:203]\n\t" \
    "s_waitcnt lgkmcnt(3)\n\t" \
    "v_mfma_f32_16x16x32_bf16 v[216:219], v[40:43], v[240:243], v[216:219]\n\t" \
    "ds_read_b128 v[236:239], v252 offset:16384\n\t" \
    "v_mfma_f32_16x16x32_bf16 v[220:223], v[48:51], v[240:243], v[220:223]\n\t" \
    "v_mfma_f32_16x16x32_bf16 v[212:215], v[244:247], v[240:243], v[212:215]\n\t" \
    "s_waitcnt lgkmcnt(3)\n\t" \
    "v_mfma_f32_16x16x32_bf16 v[204:207], v[44:47], v[224:227], v[204:207]\n\t" \
    "ds_read_b128 v[240:243], v252 offset:1024\n\t" \
    "v_mfma_f32_16x16x32_bf16 v[208:211], v[52:55], v[224:227], v[208:211]\n\t" \
    "v_mfma_f32_16x16x32_bf16 v[200:203], v[248:251], v[224:227], v[200:203]\n\t" \
    "s_waitcnt lgkmcnt(3)\n\t" \
    "v_mfma_f32_16x16x32_bf16 v[216:219], v[44:47], v[228:231], v[216:219]\n\t" \
    "ds_read_b128 v[224:227], v252 offset:17408\n\t" \
    "v_mfma_f32_16x16x32_bf16 v[220:223], v[52:55], v[228:231], v[220:223]\n\t" \
    "v_mfma_f32_16x16x32_bf16 v[212:215], v[248:251], v[228:231], v[212:215]\n\t" \
    "s_waitcnt lgkmcnt(3)\n\t" \
    "v_mfma_f32_16x16x32_bf16 v[204:207], v[64:67], v[232:235], v[204:207]\n\t" \
    "v_and_b32 v252, 0x3ff, %[xa]\n\t" \
    "ds_read_b128 v[228:231], v252 offset:43008\n\t" \
    "v_mov_b64 v[244:245], 0\n\t" \
    "v_mfma_f32_16x16x32_bf16 v[208:211], v[72:75], v[232:235], v[208:211]\n\t" \
    "v_mov_b64 v[246:247], 0\n\t" \
    "v_mfma_f32_16x16x32_bf16 v[200:203], v[56:59], v[232:235], v[200:203]\n\t" \
    "v_mov_b64 v[248:249], 0\n\t" \
    "s_waitcnt lgkmcnt(3)\n\t" \
    "v_mfma_f32_16x16x32_bf16 v[216:219], v[64:67], v[236:239], v[216:219]\n\t" \
    "ds_read_b128 v[232:235], v252 offset:44032\n\t" \
    "v_mov_b64 v[250:251], 0\n\t" \
    "v_mfma_f32_16x16x32_bf16 v[220:223], v[72:75], v[236:239], v[220:223]\n\t" \
    "v_mfma_f32_16x16x32_bf16 v[212:215], v[56:59], v[236:239], v[212:215]\n\t" \
    "s_waitcnt lgkmcnt(3)\n\t" \
    "v_mfma_f32_16x16x32_bf16 v[204:207], v[68:71], v[240:243], v[204:207]\n\t" \
    "ds_read_b128 v[236:239], v252 offset:47104\n\t" \
    "v_mfma_f32_16x16x32_bf16 v[208:211], v[76:79], v[240:243], v[208:211]\n\t" \
    "v_mfma_f32_16x16x32_bf16 v[200:203], v[60:63], v[240:243], v[200:203]\n\t" \
    "s_waitcnt lgkmcnt(3)\n\t" \
    "v_mfma_f32_16x16x32_bf16 v[216:219], v[68:71], v[224:227], v[216:219]\n\t" \
    "ds_read_b128 v[240:243], v252 offset:48128\n\t" \
    "v_mfma_f32_16x16x32_bf16 v[220:223], v[76:79], v[224:227], v[220:223]\n\t" \
    "v_mfma_f32_16x16x32_bf16 v[212:215], v[60:63], v[224:227], v[212:215]\n\t" \
    "s_waitcnt lgkmcnt(3)\n\t" \
    "v_mfma_f32_16x16x32_bf16 v[204:207], v[184:187], v[228:231], v[204:207]\n\t" \
    "v_add_u32 v252, %[qs], v252\n\t" \
    "v_mfma_f32_16x16x32_bf16 v[208:211], v[192:195], v[228:231], v[208:211]\n\t" \
    "v_mov_b64 v[224:225], 0\n\t" \
    "v_mfma_f32_16x16x32_bf16 v[200:203], v[176:179], v[228:231], v[200:203]\n\t" \
    "v_mov_b64 v[226:227], 0\n\t" \
    "s_waitcnt lgkmcnt(2)\n\t" \
    "v_mfma_f32_16x16x32_bf16 v[204:207], v[188:191], v[232:235], v[204:207]\n\t" \
    "v_mfma_f32_16x16x32_bf16 v[208:211], v[196:199], v[232:235], v[208:211]\n\t" \
    "v_mfma_f32_16x16x32_bf16 v[200:203], v[180:183], v[232:235], v[200:203]\n\t" \
    "s_waitcnt lgkmcnt(1)\n\t" \
    "v_mfma_f32_16x16x32_bf16 v[216:219], v[184:187], v[236:239], v[216:219]\n\t" \
    "ds_read_b128 v[228:231], %[wl] offset:2048\n\t" \
    "ds_read_b128 v[232:235], %[wl] offset:3072\n\t" \
    "v_mfma_f32_16x16x32_bf16 v[220:223], v[192:195], v[236:239], v[220:223]\n\t" \
    "v_cvt_pk_bf16_f32 v204, v204, v205\n\t" \
    "v_cvt_pk_bf16_f32 v205, v206, v207\n\t" \
    "v_mfma_f32_16x16x32_bf16 v[212:215], v[176:179], v[236:239], v[212:215]\n\t" \
    "ds_write_b64 v252, v[204:205] offset:1024\n\t" \
    "v_cvt_pk_bf16_f32 v208, v208, v209\n\t" \
    "s_waitcnt lgkmcnt(3)\n\t" \
    "v_mfma_f32_16x16x32_bf16 v[216:219], v[188:191], v[240:243], v[216:219]\n\t" \
    "v_cvt_pk_bf16_f32 v209, v210, v211\n\t" \
    "ds_write_b64 v252, v[208:209] offset:2048\n\t" \
    "v_mfma_f32_16x16x32_bf16 v[220:223], v[196:199], v[240:243], v[220:223]\n\t" \
    "v_cvt_pk_bf16_f32 v200, v200, v201\n\t" \
    "v_cvt_pk_bf16_f32 v201, v202, v203\n\t" \
    "v_mfma_f32_16x16x32_bf16 v[212:215], v[180:183], v[240:243], v[212:215]\n\t" \
    "ds_write_b64 v252, v[200:201] offset:0\n\t" \
    "ds_read_b128 v[200:203], %[wl] offset:0\n\t" \
    "ds_read_b128 v[204:207], %[wl] offset:1024\n\t" \
    "v_cvt_pk_bf16_f32 v216, v216, v217\n\t" \
    "v_cvt_pk_bf16_f32 v217, v218, v219\n\t" \
    "ds_write_b64 v252, v[216:217] offset:7168\n\t" \
    "v_cvt_pk_bf16_f32 v220, v220, v221\n\t" \
    "v_cvt_pk_bf16_f32 v221, v222, v223\n\t" \
    "ds_write_b64 v252, v[220:221] offset:8192\n\t" \
    "v_cvt_pk_bf16_f32 v212, v212, v213\n\t" \
    "v_cvt_pk_bf16_f32 v213, v214, v215\n\t" \
    "ds_write_b64 v252, v[212:213] offset:6144\n\t" \
    "s_waitcnt lgkmcnt(0)\n\t"
#define ASM_CRIT1 \
    "v_bfe_i32 v241, %[ab], 7, 1\n\t" \
    "v_and_b32 v241, 0xaaaa, v241\n\t" \
    "v_or_b32 v241, 0x4444, v241\n\t" \
    "v_xor_b32 v212, 0x400, %[ab]\n\t" \
    "v_xor_b32 v216, 0x800, %[ab]\n\t" \
    "v_smfmac_f32_16x16x64_bf16 v[224:227], v[208:211], v[200:207], v241\n\t" \
    "v_smfmac_f32_16x16x64_bf16 v[244:247], v[208:211], v[0:7], v241\n\t" \
    "v_smfmac_f32_16x16x64_bf16 v[248:251], v[208:211], v[8:15], v241\n\t" \
    "v_xor_b32 v220, 0xc00, %[ab]\n\t" \
    "v_xor_b32 v236, 0x1000, %[ab]\n\t" \
    "v_xor_b32 v240, 0x1400, %[ab]\n\t" \
    "s_waitcnt lgkmcnt(0)\n\t" \
    "s_barrier\n\t" ASM_ST \
    "ds_read_b128 v[212:215], v212 offset:0\n\t" \
    "ds_read_b128 v[216:219], v216 offset:0\n\t" \
    "ds_read_b128 v[220:223], v220 offset:0\n\t" \
    "ds_read_b128 v[236:239], v236 offset:0\n\t" \
    "ds_read_b128 v[208:211], v240 offset:0\n\t" \
    "ds_read_b128 v[200:203], %[wl] offset:4096\n\t" \
    "ds_read_b128 v[204:207], %[wl] offset:5120\n\t" \
    "s_waitcnt lgkmcnt(6)\n\t" \
    "v_smfmac_f32_16x16x64_bf16 v[224:227], v[212:215], v[16:23], v241\n\t" \
    "v_smfmac_f32_16x16x64_bf16 v[244:247], v[212:215], v[24:31], v241\n\t" \
    "v_xor_b32 v240, 0x1800, %[ab]\n\t" \
    "v_smfmac_f32_16x16x64_bf16 v[248:251], v[212:215], v[32:39], v241\n\t" \
    "ds_read_b128 v[212:215], v240 offset:0\n\t" \
    "s_waitcnt lgkmcnt(6)\n\t" \
    "v_smfmac_f32_16x16x64_bf16 v[224:227], v[216:219], v[228:235], v241\n\t" \
    "ds_read_b128 v[228:231], %[wl] offset:6144\n\t" \
    "ds_read_b128 v[232:235], %[wl] offset:7168\n\t" \
    "v_smfmac_f32_16x16x64_bf16 v[244:247], v[216:219], v[40:47], v241\n\t" \
    "v_xor_b32 v240, 0x1c00, %[ab]\n\t" \
    "v_smfmac_f32_16x16x64_bf16 v[248:251], v[216:219], v[48:55], v241\n\t" \
    "ds_read_b128 v[216:219], v240 offset:0\n\t" \
    "s_waitcnt lgkmcnt(8)\n\t" \
    "v_smfmac_f32_16x16x64_bf16 v[224:227], v[220:223], v[56:63], v241\n\t" \
    "v_smfmac_f32_16x16x64_bf16 v[244:247], v[220:223], v[64:71], v241\n\t" \
    "v_and_b32 v240, 0x3ff, %[ab]\n\t" \
    "v_smfmac_f32_16x16x64_bf16 v[248:251], v[220:223], v[72:79], v241\n\t" \
    "ds_read_b128 v[220:223], v240 offset:8192\n\t" \
    "s_waitcnt lgkmcnt(5)\n\t" \
    "v_smfmac_f32_16x16x64_bf16 v[224:227], v[236:239], v[200:207], v241\n\t" \
    "ds_read_b128 v[200:203], %[wl] offset:8192\n\t" \
    "ds_read_b128 v[204:207], %[wl] offset:9216\n\t" \
    "v_smfmac_f32_16x16x64_bf16 v[244:247], v[236:239], v[80:87], v241\n\t" \
    "v_smfmac_f32_16x16x64_bf16 v[248:251], v[236:239], v[88:95], v241\n\t" \
    "ds_read_b128 v[236:239], v240 offset:9216\n\t" \
    "v_smfmac_f32_16x16x64_bf16 v[244:247], v[208:211], v[104:111], v241\n\t" \
    "s_waitcnt lgkmcnt(7)\n\t" \
    "v_smfmac_f32_16x16x64_bf16 v[244:247], v[212:215], v[120:127], v241\n\t" \
    "s_waitcnt lgkmcnt(4)\n\t" \
    "v_smfmac_f32_16x16x64_bf16 v[244:247], v[216:219], v[144:151], v241\n\t" \
    "s_waitcnt lgkmcnt(3)\n\t" \
    "v_smfmac_f32_16x16x64_bf16 v[244:247], v[220:223], v[160:167], v241\n\t" \
    "s_waitcnt lgkmcnt(0)\n\t" \
    "v_smfmac_f32_16x16x64_bf16 v[244:247], v[236:239], v[184:191], v241\n\t" \
    "v_smfmac_f32_16x16x64_bf16 v[248:251], v[208:211], v[112:119], v241\n\t" \
    "v_smfmac_f32_16x16x64_bf16 v[248:251], v[212:215], v[128:135], v241\n\t" \
    "v_smfmac_f32_16x16x64_bf16 v[248:251], v[216:219], v[152:159], v241\n\t" \
    "v_permlane32_swap_b32 v244, v246\n\t" \
    "v_permlane32_swap_b32 v245, v247\n\t" \
    "v_smfmac_f32_16x16x64_bf16 v[248:251], v[220:223], v[168:175], v241\n\t" \
    "v_add_f32 v244, v244, v246\n\t" \
    "v_add_f32 v245, v245, v247\n\t" \
    "v_smfmac_f32_16x16x64_bf16 v[248:251], v[236:239], v[192:199], v241\n\t" \
    "v_cvt_pk_bf16_f32 v244, v244, v245\n\t" \
    "ds_write_b16 %[dst], v244 offset:512\n\t" \
    "ds_write_b16_d16_hi %[dst], v244 offset:528\n\t" \
    "v_smfmac_f32_16x16x64_bf16 v[224:227], v[208:211], v[96:103], v241\n\t" \
    "v_smfmac_f32_16x16x64_bf16 v[224:227], v[212:215], v[228:235], v241\n\t" \
    "v_smfmac_f32_16x16x64_bf16 v[224:227], v[216:219], v[136:143], v241\n\t" \
    "v_permlane32_swap_b32 v248, v250\n\t" \
    "v_permlane32_swap_b32 v249, v251\n\t" \
    "v_smfmac_f32_16x16x64_bf16 v[224:227], v[220:223], v[200:207], v241\n\t" \
    "v_add_f32 v248, v248, v250\n\t" \
    "v_add_f32 v249, v249, v251\n\t" \
    "v_smfmac_f32_16x16x64_bf16 v[224:227], v[236:239], v[176:183], v241\n\t" \
    "v_cvt_pk_bf16_f32 v248, v248, v249\n\t" \
    "ds_write_b16 %[dst], v248 offset:1024\n\t" \
    "ds_write_b16_d16_hi %[dst], v248 offset:1040\n\t" \
    "s_nop 4\n\t" \
    "v_permlane32_swap_b32 v224, v226\n\t" \
    "v_permlane32_swap_b32 v225, v227\n\t" \
    "v_add_f32 v224, v224, v226\n\t" \
    "v_add_f32 v225, v225, v227\n\t" \
    "v_cvt_pk_bf16_f32 v224, v224, v225\n\t" \
    "ds_write_b16 %[dst], v224 offset:0\n\t" \
    "ds_write_b16_d16_hi %[dst], v224 offset:16\n\t"
#define ASM_CRIT1_N \
    "v_bfe_i32 v241, %[ab], 7, 1\n\t" \
    "v_and_b32 v241, 0xaaaa, v241\n\t" \
    "v_or_b32 v241, 0x4444, v241\n\t" \
    "v_xor_b32 v212, 0x400, %[ab]\n\t" \
    "v_xor_b32 v216, 0x800, %[ab]\n\t" \
    "v_xor_b32 v220, 0xc00, %[ab]\n\t" \
    "v_xor_b32 v236, 0x1000, %[ab]\n\t" \
    "v_xor_b32 v240, 0x1400, %[ab]\n\t" \
    "s_waitcnt lgkmcnt(0)\n\t" \
    "s_barrier\n\t" ASM_ST \
    "ds_read_b128 v[208:211], %[ab] offset:0\n\t" \
    "ds_read_b128 v[212:215], v212 offset:0\n\t" \
    "ds_read_b128 v[216:219], v216 offset:0\n\t" \
    "ds_read_b128 v[220:223], v220 offset:0\n\t" \
    "ds_read_b128 v[236:239], v236 offset:0\n\t" \
    "s_waitcnt lgkmcnt(4)\n\t" \
    "v_smfmac_f32_16x16x64_bf16 v[224:227], v[208:211], v[200:207], v241\n\t" \
    "v_smfmac_f32_16x16x64_bf16 v[244:247], v[208:211], v[0:7], v241\n\t" \
    "v_smfmac_f32_16x16x64_bf16 v[248:251], v[208:211], v[8:15], v241\n\t" \
    "ds_read_b128 v[208:211], v240 offset:0\n\t" \
    "ds_read_b128 v[200:203], %[wl] offset:4096\n\t" \
    "ds_read_b128 v[204:207], %[wl] offset:5120\n\t" \
    "s_waitcnt lgkmcnt(6)\n\t" \
    "v_smfmac_f32_16x16x64_bf16 v[224:227], v[212:215], v[16:23], v241\n\t" \
    "v_smfmac_f32_16x16x64_bf16 v[244:247], v[212:215], v[24:31], v241\n\t" \
    "v_xor_b32 v240, 0x1800, %[ab]\n\t" \
    "v_smfmac_f32_16x16x64_bf16 v[248:251], v[212:215], v[32:39], v241\n\t" \
    "ds_read_b128 v[212:215], v240 offset:0\n\t" \
    "s_waitcnt lgkmcnt(6)\n\t" \
    "v_smfmac_f32_16x16x64_bf16 v[224:227], v[216:219], v[228:235], v241\n\t" \
    "ds_read_b128 v[228:231], %[wl] offset:6144\n\t" \
    "ds_read_b128 v[232:235], %[wl] offset:7168\n\t" \
    "v_smfmac_f32_16x16x64_bf16 v[244:247], v[216:219], v[40:47], v241\n\t" \
    "v_xor_b32 v240, 0x1c00, %[ab]\n\t" \
    "v_smfmac_f32_16x16x64_bf16 v[248:251], v[216:219], v[48:55], v241\n\t" \
    "ds_read_b128 v[216:219], v240 offset:0\n\t" \
    "s_waitcnt lgkmcnt(8)\n\t" \
    "v_smfmac_f32_16x16x64_bf16 v[224:227], v[220:223], v[56:63], v241\n\t" \
    "v_smfmac_f32_16x16x64_bf16 v[244:247], v[220:223], v[64:71], v241\n\t" \
    "v_and_b32 v240, 0x3ff, %[ab]\n\t" \
    "v_smfmac_f32_16x16x64_bf16 v[248:251], v[220:223], v[72:79], v241\n\t" \
    "ds_read_b128 v[220:223], v240 offset:8192\n\t" \
    "s_waitcnt lgkmcnt(5)\n\t" \
    "v_smfmac_f32_16x16x64_bf16 v[224:227], v[236:239], v[200:207], v241\n\t" \
    "ds_read_b128 v[200:203], %[wl] offset:8192\n\t" \
    "ds_read_b128 v[204:207], %[wl] offset:9216\n\t" \
    "v_smfmac_f32_16x16x64_bf16 v[244:247], v[236:239], v[80:87], v241\n\t" \
    "v_smfmac_f32_16x16x64_bf16 v[248:251], v[236:239], v[88:95], v241\n\t" \
    "ds_read_b128 v[236:239], v240 offset:9216\n\t" \
    "v_smfmac_f32_16x16x64_bf16 v[244:247], v[208:211], v[104:111], v241\n\t" \
    "s_waitcnt lgkmcnt(7)\n\t" \
    "v_smfmac_f32_16x16x64_bf16 v[244:247], v[212:215], v[120:127], v241\n\t" \
    "s_waitcnt lgkmcnt(4)\n\t" \
    "v_smfmac_f32_16x16x64_bf16 v[244:247], v[216:219], v[144:151], v241\n\t" \
    "s_waitcnt lgkmcnt(3)\n\t" \
    "v_smfmac_f32_16x16x64_bf16 v[244:247], v[220:223], v[160:167], v241\n\t" \
    "s_waitcnt lgkmcnt(0)\n\t" \
    "v_smfmac_f32_16x16x64_bf16 v[244:247], v[236:239], v[184:191], v241\n\t" \
    "v_smfmac_f32_16x16x64_bf16 v[248:251], v[208:211], v[112:119], v241\n\t" \
    "v_smfmac_f32_16x16x64_bf16 v[248:251], v[212:215], v[128:135], v241\n\t" \
    "v_smfmac_f32_16x16x64_bf16 v[248:251], v[216:219], v[152:159], v241\n\t" \
    "v_permlane32_swap_b32 v244, v246\n\t" \
    "v_permlane32_swap_b32 v245, v247\n\t" \
    "v_smfmac_f32_16x16x64_bf16 v[248:251], v[220:223], v[168:175], v241\n\t" \
    "v_add_f32 v244, v244, v246\n\t" \
    "v_add_f32 v245, v245, v247\n\t" \
    "v_smfmac_f32_16x16x64_bf16 v[248:251], v[236:239], v[192:199], v241\n\t" \
    "v_cvt_pk_bf16_f32 v244, v244, v245\n\t" \
    "ds_write_b16 %[dst], v244 offset:512\n\t" \
    "ds_write_b16_d16_hi %[dst], v244 offset:528\n\t" \
    "v_smfmac_f32_16x16x64_bf16 v[224:227], v[208:211], v[96:103], v241\n\t" \
    "v_smfmac_f32_16x16x64_bf16 v[224:227], v[212:215], v[228:235], v241\n\t" \
    "v_smfmac_f32_16x16x64_bf16 v[224:227], v[216:219], v[136:143], v241\n\t" \
    "v_permlane32_swap_b32 v248, v250\n\t" \
    "v_permlane32_swap_b32 v249, v251\n\t" \
    "v_smfmac_f32_16x16x64_bf16 v[224:227], v[220:223], v[200:207], v241\n\t" \
    "v_add_f32 v248, v248, v250\n\t" \
    "v_add_f32 v249, v249, v251\n\t" \
    "v_smfmac_f32_16x16x64_bf16 v[224:227], v[236:239], v[176:183], v241\n\t" \
    "v_cvt_pk_bf16_f32 v248, v248, v249\n\t" \
    "ds_write_b16 %[dst], v248 offset:1024\n\t" \
    "ds_write_b16_d16_hi %[dst], v248 offset:1040\n\t" \
    "s_nop 4\n\t" \
    "v_permlane32_swap_b32 v224, v226\n\t" \
    "v_permlane32_swap_b32 v225, v227\n\t" \
    "v_add_f32 v224, v224, v226\n\t" \
    "v_add_f32 v225, v225, v227\n\t" \
    "v_cvt_pk_bf16_f32 v224, v224, v225\n\t" \
    "ds_write_b16 %[dst], v224 offset:0\n\t" \
    "ds_write_b16_d16_hi %[dst], v224 offset:16\n\t"
#define ASM_VOX \
    "s_waitcnt lgkmcnt(0)\n\t" \
    "ds_read_b128 v[224:227], %[va] offset:0\n\t" \
    "ds_read_b128 v[248:251], %[wl] offset:0\n\t" \
    "ds_read_b128 v[228:231], %[va] offset:6144\n\t" \
    "ds_read_b128 v[232:235], %[va] offset:2048\n\t" \
    "ds_read_b128 v[236:239], %[va] offset:8192\n\t" \
    "ds_read_b128 v[240:243], %[va] offset:3072\n\t" \
    "s_waitcnt lgkmcnt(4)\n\t" \
    "v_mfma_f32_16x16x32_bf16 v[200:203], v[224:227], v[248:251], 0\n\t" \
    "ds_read_b128 v[244:247], %[va] offset:9216\n\t" \
    "s_waitcnt lgkmcnt(4)\n\t" \
    "v_mfma_f32_16x16x32_bf16 v[204:207], v[228:231], v[248:251], 0\n\t" \
    "ds_read_b128 v[224:227], %[va] offset:1024\n\t" \
    "s_waitcnt lgkmcnt(4)\n\t" \
    "v_mfma_f32_16x16x32_bf16 v[200:203], v[232:235], v[0:3], v[200:203]\n\t" \
    "ds_read_b128 v[228:231], %[va] offset:7168\n\t" \
    "ds_read_b128 v[248:251], %[wl] offset:1024\n\t" \
    "s_waitcnt lgkmcnt(5)\n\t" \
    "v_mfma_f32_16x16x32_bf16 v[204:207], v[236:239], v[0:3], v[204:207]\n\t" \
    "ds_read_b128 v[232:235], %[va] offset:4096\n\t" \
    "s_waitcnt lgkmcnt(5)\n\t" \
    "v_mfma_f32_16x16x32_bf16 v[200:203], v[240:243], v[4:7], v[200:203]\n\t" \
    "ds_read_b128 v[236:239], %[va] offset:10240\n\t" \
    "s_waitcnt lgkmcnt(5)\n\t" \
    "v_mfma_f32_16x16x32_bf16 v[204:207], v[244:247], v[4:7], v[204:207]\n\t" \
    "ds_read_b128 v[240:243], %[va] offset:5120\n\t" \
    "s_waitcnt lgkmcnt(3)\n\t" \
    "v_mfma_f32_16x16x32_bf16 v[200:203], v[224:227], v[248:251], v[200:203]\n\t" \
    "ds_read_b128 v[244:247], %[va] offset:11264\n\t" \
    "v_mfma_f32_16x16x32_bf16 v[208:211], v[224:227], v[20:23], 0\n\t" \
    "v_mfma_f32_16x16x32_bf16 v[204:207], v[228:231], v[248:251], v[204:207]\n\t" \
    "ds_read_b128 v[224:227], %[va] offset:0\n\t" \
    "v_mfma_f32_16x16x32_bf16 v[212:215], v[228:231], v[20:23], 0\n\t" \
    "s_waitcnt lgkmcnt(4)\n\t" \
    "v_mfma_f32_16x16x32_bf16 v[200:203], v[232:235], v[8:11], v[200:203]\n\t" \
    "ds_read_b128 v[228:231], %[va] offset:6144\n\t" \
    "ds_read_b128 v[248:251], %[wl] offset:2048\n\t" \
    "v_mfma_f32_16x16x32_bf16 v[208:211], v[232:235], v[32:35], v[208:211]\n\t" \
    "s_waitcnt lgkmcnt(5)\n\t" \
    "v_mfma_f32_16x16x32_bf16 v[204:207], v[236:239], v[8:11], v[204:207]\n\t" \
    "ds_read_b128 v[232:235], %[va] offset:2048\n\t" \
    "v_mfma_f32_16x16x32_bf16 v[212:215], v[236:239], v[32:35], v[212:215]\n\t" \
    "s_waitcnt lgkmcnt(5)\n\t" \
    "v_mfma_f32_16x16x32_bf16 v[200:203], v[240:243], v[12:15], v[200:203]\n\t" \
    "ds_read_b128 v[236:239], %[va] offset:8192\n\t" \
    "v_mfma_f32_16x16x32_bf16 v[208:211], v[240:243], v[36:39], v[208:211]\n\t" \
    "s_waitcnt lgkmcnt(5)\n\t" \
    "v_mfma_f32_16x16x32_bf16 v[204:207], v[244:247], v[12:15], v[204:207]\n\t" \
    "ds_read_b128 v[240:243], %[va] offset:3072\n\t" \
    "v_mfma_f32_16x16x32_bf16 v[212:215], v[244:247], v[36:39], v[212:215]\n\t" \
    "s_waitcnt lgkmcnt(3)\n\t" \
    "v_mfma_f32_16x16x32_bf16 v[216:219], v[224:227], v[248:251], 0\n\t" \
    "ds_read_b128 v[244:247], %[va] offset:9216\n\t" \
    "v_mfma_f32_16x16x32_bf16 v[208:211], v[224:227], v[16:19], v[208:211]\n\t" \
    "v_mfma_f32_16x16x32_bf16 v[220:223], v[228:231], v[248:251], 0\n\t" \
    "ds_read_b128 v[224:227], %[va] offset:1024\n\t" \
    "v_mfma_f32_16x16x32_bf16 v[212:215], v[228:231], v[16:19], v[212:215]\n\t" \
    "v_cvt_pk_bf16_f32 v200, v200, v201\n\t" \
    "s_waitcnt lgkmcnt(4)\n\t" \
    "v_mfma_f32_16x16x32_bf16 v[216:219], v[232:235], v[40:43], v[216:219]\n\t" \
    "ds_read_b128 v[228:231], %[va] offset:7168\n\t" \
    "ds_read_b128 v[248:251], %[wl] offset:3072\n\t" \
    "v_cvt_pk_bf16_f32 v201, v202, v203\n\t" \
    "v_mfma_f32_16x16x32_bf16 v[208:211], v[232:235], v[24:27], v[208:211]\n\t" \
    "v_cvt_pk_bf16_f32 v202, v204, v205\n\t" \
    "s_waitcnt lgkmcnt(5)\n\t" \
    "v_mfma_f32_16x16x32_bf16 v[220:223], v[236:239], v[40:43], v[220:223]\n\t" \
    "ds_read_b128 v[232:235], %[va] offset:4096\n\t" \
    "v_cvt_pk_bf16_f32 v203, v206, v207\n\t" \
    "v_mfma_f32_16x16x32_bf16 v[212:215], v[236:239], v[24:27], v[212:215]\n\t" \
    "buffer_store_dwordx4 v[200:203], %[l16], %[rd], %[so] offen sc1\n\t" \
    "s_waitcnt lgkmcnt(5)\n\t" \
    "v_mfma_f32_16x16x32_bf16 v[216:219], v[240:243], v[44:47], v[216:219]\n\t" \
    "ds_read_b128 v[236:239], %[va] offset:10240\n\t" \
    "s_add_u32 %[so], %[so], 0x10000\n\t" \
    "v_mfma_f32_16x16x32_bf16 v[208:211], v[240:243], v[28:31], v[208:211]\n\t" \
    "s_waitcnt lgkmcnt(5)\n\t" \
    "v_mfma_f32_16x16x32_bf16 v[220:223], v[244:247], v[44:47], v[220:223]\n\t" \
    "ds_read_b128 v[240:243], %[va] offset:5120\n\t" \
    "v_mfma_f32_16x16x32_bf16 v[212:215], v[244:247], v[28:31], v[212:215]\n\t" \
    "s_waitcnt lgkmcnt(3)\n\t" \
    "v_mfma_f32_16x16x32_bf16 v[216:219], v[224:227], v[248:251], v[216:219]\n\t" \
    "ds_read_b128 v[244:247], %[va] offset:11264\n\t" \
    "v_mfma_f32_16x16x32_bf16 v[200:203], v[224:227], v[60:63], 0\n\t" \
    "v_mfma_f32_16x16x32_bf16 v[220:223], v[228:231], v[248:251], v[220:223]\n\t" \
    "ds_read_b128 v[224:227], %[va] offset:0\n\t" \
    "v_mfma_f32_16x16x32_bf16 v[204:207], v[228:231], v[60:63], 0\n\t" \
    "v_cvt_pk_bf16_f32 v208, v208, v209\n\t" \
    "s_waitcnt lgkmcnt(4)\n\t" \
    "v_mfma_f32_16x16x32_bf16 v[216:219], v[232:235], v[48:51], v[216:219]\n\t" \
    "ds_read_b128 v[228:231], %[va] offset:6144\n\t" \
    "ds_read_b128 v[248:251], %[wl] offset:4096\n\t" \
    "v_cvt_pk_bf16_f32 v209, v210, v211\n\t" \
    "v_mfma_f32_16x16x32_bf16 v[200:203], v[232:235], v[72:75], v[200:203]\n\t" \
    "v_cvt_pk_bf16_f32 v210, v212, v213\n\t" \
    "s_waitcnt lgkmcnt(5)\n\t" \
    "v_mfma_f32_16x16x32_bf16 v[220:223], v[236:239], v[48:51], v[220:223]\n\t" \
    "ds_read_b128 v[232:235], %[va] offset:2048\n\t" \
    "v_cvt_pk_bf16_f32 v211, v214, v215\n\t" \
    "v_mfma_f32_16x16x32_bf16 v[204:207], v[236:239], v[72:75], v[204:207]\n\t" \
    "buffer_store_dwordx4 v[208:211], %[l16], %[rd], %[so] offen sc1\n\t" \
    "s_waitcnt lgkmcnt(5)\n\t" \
    "v_mfma_f32_16x16x32_bf16 v[216:219], v[240:243], v[52:55], v[216:219]\n\t" \
    "ds_read_b128 v[236:239], %[va] offset:8192\n\t" \
    "s_add_u32 %[so], %[so], 0x10000\n\t" \
    "v_mfma_f32_16x16x32_bf16 v[200:203], v[240:243], v[76:79], v[200:203]\n\t" \
    "s_waitcnt lgkmcnt(5)\n\t" \
    "v_mfma_f32_16x16x32_bf16 v[220:223], v[244:247], v[52:55], v[220:223]\n\t" \
    "ds_read_b128 v[240:243], %[va] offset:3072\n\t" \
    "v_mfma_f32_16x16x32_bf16 v[204:207], v[244:247], v[76:79], v[204:207]\n\t" \
    "s_waitcnt lgkmcnt(3)\n\t" \
    "v_mfma_f32_16x16x32_bf16 v[208:211], v[224:227], v[248:251], 0\n\t" \
    "ds_read_b128 v[244:247], %[va] offset:9216\n\t" \
    "v_mfma_f32_16x16x32_bf16 v[200:203], v[224:227], v[56:59], v[200:203]\n\t" \
    "v_mfma_f32_16x16x32_bf16 v[212:215], v[228:231], v[248:251], 0\n\t" \
    "ds_read_b128 v[224:227], %[va] offset:1024\n\t" \
    "v_mfma_f32_16x16x32_bf16 v[204:207], v[228:231], v[56:59], v[204:207]\n\t" \
    "v_cvt_pk_bf16_f32 v216, v216, v217\n\t" \
    "s_waitcnt lgkmcnt(4)\n\t" \
    "v_mfma_f32_16x16x32_bf16 v[208:211], v[232:235], v[80:83], v[208:211]\n\t" \
    "ds_read_b128 v[228:231], %[va] offset:7168\n\t" \
    "ds_read_b128 v[248:251], %[wl] offset:5120\n\t" \
    "v_cvt_pk_bf16_f32 v217, v218, v219\n\t" \
    "v_mfma_f32_16x16x32_bf16 v[200:203], v[232:235], v[64:67], v[200:203]\n\t" \
    "v_cvt_pk_bf16_f32 v218, v220, v221\n\t" \
    "s_waitcnt lgkmcnt(5)\n\t" \
    "v_mfma_f32_16x16x32_bf16 v[212:215], v[236:239], v[80:83], v[212:215]\n\t" \
    "ds_read_b128 v[232:235], %[va] offset:4096\n\t" \
    "v_cvt_pk_bf16_f32 v219, v222, v223\n\t" \
    "v_mfma_f32_16x16x32_bf16 v[204:207], v[236:239], v[64:67], v[204:207]\n\t" \
    "buffer_store_dwordx4 v[216:219], %[l16], %[rd], %[so] offen sc1\n\t" \
    "s_waitcnt lgkmcnt(5)\n\t" \
    "v_mfma_f32_16x16x32_bf16 v[208:211], v[240:243], v[84:87], v[208:211]\n\t" \
    "ds_read_b128 v[236:239], %[va] offset:10240\n\t" \
    "s_add_u32 %[so], %[so], 0x10000\n\t" \
    "v_mfma_f32_16x16x32_bf16 v[200:203], v[240:243], v[68:71], v[200:203]\n\t" \
    "s_waitcnt lgkmcnt(5)\n\t" \
    "v_mfma_f32_16x16x32_bf16 v[212:215], v[244:247], v[84:87], v[212:215]\n\t" \
    "ds_read_b128 v[240:243], %[va] offset:5120\n\t" \
    "v_mfma_f32_16x16x32_bf16 v[204:207], v[244:247], v[68:71], v[204:207]\n\t" \
    "s_waitcnt lgkmcnt(3)\n\t" \
    "v_mfma_f32_16x16x32_bf16 v[208:211], v[224:227], v[248:251], v[208:211]\n\t" \
    "ds_read_b128 v[244:247], %[va] offset:11264\n\t" \
    "v_mfma_f32_16x16x32_bf16 v[216:219], v[224:227], v[100:103], 0\n\t" \
    "v_mfma_f32_16x16x32_bf16 v[212:215], v[228:231], v[248:251], v[212:215]\n\t" \
    "ds_read_b128 v[224:227], %[va] offset:0\n\t" \
    "v_mfma_f32_16x16x32_bf16 v[220:223], v[228:231], v[100:103], 0\n\t" \
    "v_cvt_pk_bf16_f32 v200, v200, v201\n\t" \
    "s_waitcnt lgkmcnt(4)\n\t" \
    "v_mfma_f32_16x16x32_bf16 v[208:211], v[232:235], v[88:91], v[208:211]\n\t" \
    "ds_read_b128 v[228:231], %[va] offset:6144\n\t" \
    "ds_read_b128 v[248:251], %[wl] offset:6144\n\t" \
    "v_cvt_pk_bf16_f32 v201, v202, v203\n\t" \
    "v_mfma_f32_16x16x32_bf16 v[216:219], v[232:235], v[112:115], v[216:219]\n\t" \
    "v_cvt_pk_bf16_f32 v202, v204, v205\n\t" \
    "s_waitcnt lgkmcnt(5)\n\t" \
    "v_mfma_f32_16x16x32_bf16 v[212:215], v[236:239], v[88:91], v[212:215]\n\t" \
    "ds_read_b128 v[232:235], %[va] offset:2048\n\t" \
    "v_cvt_pk_bf16_f32 v203, v206, v207\n\t" \
    "v_mfma_f32_16x16x32_bf16 v[220:223], v[236:239], v[112:115], v[220:223]\n\t" \
    "buffer_store_dwordx4 v[200:203], %[l16], %[rd], %[so] offen sc1\n\t" \
    "s_waitcnt lgkmcnt(5)\n\t" \
    "v_mfma_f32_16x16x32_bf16 v[208:211], v[240:243], v[92:95], v[208:211]\n\t" \
    "ds_read_b128 v[236:239], %[va] offset:8192\n\t" \
    "s_add_u32 %[so], %[so], 0x10000\n\t" \
    "v_mfma_f32_16x16x32_bf16 v[216:219], v[240:243], v[116:119], v[216:219]\n\t" \
    "s_waitcnt lgkmcnt(5)\n\t" \
    "v_mfma_f32_16x16x32_bf16 v[212:215], v[244:247], v[92:95], v[212:215]\n\t" \
    "ds_read_b128 v[240:243], %[va] offset:3072\n\t" \
    "v_mfma_f32_16x16x32_bf16 v[220:223], v[244:247], v[116:119], v[220:223]\n\t" \
    "s_waitcnt lgkmcnt(3)\n\t" \
    "v_mfma_f32_16x16x32_bf16 v[200:203], v[224:227], v[248:251], 0\n\t" \
    "ds_read_b128 v[244:247], %[va] offset:9216\n\t" \
    "v_mfma_f32_16x16x32_bf16 v[216:219], v[224:227], v[96:99], v[216:219]\n\t" \
    "v_mfma_f32_16x16x32_bf16 v[204:207], v[228:231], v[248:251], 0\n\t" \
    "ds_read_b128 v[224:227], %[va] offset:1024\n\t" \
    "v_mfma_f32_16x16x32_bf16 v[220:223], v[228:231], v[96:99], v[220:223]\n\t" \
    "v_cvt_pk_bf16_f32 v208, v208, v209\n\t" \
    "s_waitcnt lgkmcnt(4)\n\t" \
    "v_mfma_f32_16x16x32_bf16 v[200:203], v[232:235], v[120:123], v[200:203]\n\t" \
    "ds_read_b128 v[228:231], %[va] offset:7168\n\t" \
    "ds_read_b128 v[248:251], %[wl] offset:7168\n\t" \
    "v_cvt_pk_bf16_f32 v209, v210, v211\n\t" \
    "v_mfma_f32_16x16x32_bf16 v[216:219], v[232:235], v[104:107], v[216:219]\n\t" \
    "v_cvt_pk_bf16_f32 v210, v212, v213\n\t" \
    "s_waitcnt lgkmcnt(5)\n\t" \
    "v_mfma_f32_16x16x32_bf16 v[204:207], v[236:239], v[120:123], v[204:207]\n\t" \
    "ds_read_b128 v[232:235], %[va] offset:4096\n\t" \
    "v_cvt_pk_bf16_f32 v211, v214, v215\n\t" \
    "v_mfma_f32_16x16x32_bf16 v[220:223], v[236:239], v[104:107], v[220:223]\n\t" \
    "buffer_store_dwordx4 v[208:211], %[l16], %[rd], %[so] offen sc1\n\t" \
    "s_waitcnt lgkmcnt(5)\n\t" \
    "v_mfma_f32_16x16x32_bf16 v[200:203], v[240:243], v[124:127], v[200:203]\n\t" \
    "ds_read_b128 v[236:239], %[va] offset:10240\n\t" \
    "s_add_u32 %[so], %[so], 0x10000\n\t" \
    "v_mfma_f32_16x16x32_bf16 v[216:219], v[240:243], v[108:111], v[216:219]\n\t" \
    "s_waitcnt lgkmcnt(5)\n\t" \
    "v_mfma_f32_16x16x32_bf16 v[204:207], v[244:247], v[124:127], v[204:207]\n\t" \
    "ds_read_b128 v[240:243], %[va] offset:5120\n\t" \
    "v_mfma_f32_16x16x32_bf16 v[220:223], v[244:247], v[108:111], v[220:223]\n\t" \
    "s_waitcnt lgkmcnt(3)\n\t" \
    "v_mfma_f32_16x16x32_bf16 v[200:203], v[224:227], v[248:251], v[200:203]\n\t" \
    "ds_read_b128 v[244:247], %[va] offset:11264\n\t" \
    "v_mfma_f32_16x16x32_bf16 v[208:211], v[224:227], v[140:143], 0\n\t" \
    "v_mfma_f32_16x16x32_bf16 v[204:207], v[228:231], v[248:251], v[204:207]\n\t" \
    "ds_read_b128 v[224:227], %[va] offset:0\n\t" \
    "v_mfma_f32_16x16x32_bf16 v[212:215], v[228:231], v[140:143], 0\n\t" \
    "v_cvt_pk_bf16_f32 v216, v216, v217\n\t" \
    "s_waitcnt lgkmcnt(4)\n\t" \
    "v_mfma_f32_16x16x32_bf16 v[200:203], v[232:235], v[128:131], v[200:203]\n\t" \
    "ds_read_b128 v[228:231], %[va] offset:6144\n\t" \
    "ds_read_b128 v[248:251], %[wl] offset:8192\n\t" \
    "v_cvt_pk_bf16_f32 v217, v218, v219\n\t" \
    "v_mfma_f32_16x16x32_bf16 v[208:211], v[232:235], v[152:155], v[208:211]\n\t" \
    "v_cvt_pk_bf16_f32 v218, v220, v221\n\t" \
    "s_waitcnt lgkmcnt(5)\n\t" \
    "v_mfma_f32_16x16x32_bf16 v[204:207], v[236:239], v[128:131], v[204:207]\n\t" \
    "ds_read_b128 v[232:235], %[va] offset:2048\n\t" \
    "v_cvt_pk_bf16_f32 v219, v222, v223\n\t" \
    "v_mfma_f32_16x16x32_bf16 v[212:215], v[236:239], v[152:155], v[212:215]\n\t" \
    "buffer_store_dwordx4 v[216:219], %[l16], %[rd], %[so] offen sc1\n\t" \
    "s_waitcnt lgkmcnt(5)\n\t" \
    "v_mfma_f32_16x16x32_bf16 v[200:203], v[240:243], v[132:135], v[200:203]\n\t" \
    "ds_read_b128 v[236:239], %[va] offset:8192\n\t" \
    "s_add_u32 %[so], %[so], 0x10000\n\t" \
    "v_mfma_f32_16x16x32_bf16 v[208:211], v[240:243], v[156:159], v[208:211]\n\t" \
    "s_waitcnt lgkmcnt(5)\n\t" \
    "v_mfma_f32_16x16x32_bf16 v[204:207], v[244:247], v[132:135], v[204:207]\n\t" \
    "ds_read_b128 v[240:243], %[va] offset:3072\n\t" \
    "v_mfma_f32_16x16x32_bf16 v[212:215], v[244:247], v[156:159], v[212:215]\n\t" \
    "s_waitcnt lgkmcnt(3)\n\t" \
    "v_mfma_f32_16x16x32_bf16 v[216:219], v[224:227], v[248:251], 0\n\t" \
    "ds_read_b128 v[244:247], %[va] offset:9216\n\t" \
    "v_mfma_f32_16x16x32_bf16 v[208:211], v[224:227], v[136:139], v[208:211]\n\t" \
    "v_mfma_f32_16x16x32_bf16 v[220:223], v[228:231], v[248:251], 0\n\t" \
    "ds_read_b128 v[224:227], %[va] offset:1024\n\t" \
    "v_mfma_f32_16x16x32_bf16 v[212:215], v[228:231], v[136:139], v[212:215]\n\t" \
    "v_cvt_pk_bf16_f32 v200, v200, v201\n\t" \
    "s_waitcnt lgkmcnt(4)\n\t" \
    "v_mfma_f32_16x16x32_bf16 v[216:219], v[232:235], v[160:163], v[216:219]\n\t" \
    "ds_read_b128 v[228:231], %[va] offset:7168\n\t" \
    "ds_read_b128 v[248:251], %[wl] offset:9216\n\t" \
    "v_cvt_pk_bf16_f32 v201, v202, v203\n\t" \
    "v_mfma_f32_16x16x32_bf16 v[208:211], v[232:235], v[144:147], v[208:211]\n\t" \
    "v_cvt_pk_bf16_f32 v202, v204, v205\n\t" \
    "s_waitcnt lgkmcnt(5)\n\t" \
    "v_mfma_f32_16x16x32_bf16 v[220:223], v[236:239], v[160:163], v[220:223]\n\t" \
    "ds_read_b128 v[232:235], %[va] offset:4096\n\t" \
    "v_cvt_pk_bf16_f32 v203, v206, v207\n\t" \
    "v_mfma_f32_16x16x32_bf16 v[212:215], v[236:239], v[144:147], v[212:215]\n\t" \
    "buffer_store_dwordx4 v[200:203], %[l16], %[rd], %[so] offen sc1\n\t" \
    "s_waitcnt lgkmcnt(5)\n\t" \
    "v_mfma_f32_16x16x32_bf16 v[216:219], v[240:243], v[164:167], v[216:219]\n\t" \
    "ds_read_b128 v[236:239], %[va] offset:10240\n\t" \
    "s_add_u32 %[so], %[so], 0x10000\n\t" \
    "v_mfma_f32_16x16x32_bf16 v[208:211], v[240:243], v[148:151], v[208:211]\n\t" \
    "s_waitcnt lgkmcnt(5)\n\t" \
    "v_mfma_f32_16x16x32_bf16 v[220:223], v[244:247], v[164:167], v[220:223]\n\t" \
    "ds_read_b128 v[240:243], %[va] offset:5120\n\t" \
    "v_mfma_f32_16x16x32_bf16 v[212:215], v[244:247], v[148:151], v[212:215]\n\t" \
    "s_waitcnt lgkmcnt(3)\n\t" \
    "v_mfma_f32_16x16x32_bf16 v[216:219], v[224:227], v[248:251], v[216:219]\n\t" \
    "ds_read_b128 v[244:247], %[va] offset:11264\n\t" \
    "v_mfma_f32_16x16x32_bf16 v[200:203], v[224:227], v[180:183], 0\n\t" \
    "v_mfma_f32_16x16x32_bf16 v[220:223], v[228:231], v[248:251], v[220:223]\n\t" \
    "ds_read_b128 v[224:227], %[va] offset:0\n\t" \
    "v_mfma_f32_16x16x32_bf16 v[204:207], v[228:231], v[180:183], 0\n\t" \
    "v_cvt_pk_bf16_f32 v208, v208, v209\n\t" \
    "s_waitcnt lgkmcnt(4)\n\t" \
    "v_mfma_f32_16x16x32_bf16 v[216:219], v[232:235], v[168:171], v[216:219]\n\t" \
    "ds_read_b128 v[228:231], %[va] offset:6144\n\t" \
    "v_cvt_pk_bf16_f32 v209, v210, v211\n\t" \
    "v_mfma_f32_16x16x32_bf16 v[200:203], v[232:235], v[192:195], v[200:203]\n\t" \
    "v_cvt_pk_bf16_f32 v210, v212, v213\n\t" \
    "s_waitcnt lgkmcnt(4)\n\t" \
    "v_mfma_f32_16x16x32_bf16 v[220:223], v[236:239], v[168:171], v[220:223]\n\t" \
    "ds_read_b128 v[232:235], %[va] offset:2048\n\t" \
    "v_cvt_pk_bf16_f32 v211, v214, v215\n\t" \
    "v_mfma_f32_16x16x32_bf16 v[204:207], v[236:239], v[192:195], v[204:207]\n\t" \
    "buffer_store_dwordx4 v[208:211], %[l16], %[rd], %[so] offen sc1\n\t" \
    "s_waitcnt lgkmcnt(4)\n\t" \
    "v_mfma_f32_16x16x32_bf16 v[216:219], v[240:243], v[172:175], v[216:219]\n\t" \
    "ds_read_b128 v[236:239], %[va] offset:8192\n\t" \
    "s_add_u32 %[so], %[so], 0x10000\n\t" \
    "v_mfma_f32_16x16x32_bf16 v[200:203], v[240:243], v[196:199], v[200:203]\n\t" \
    "s_waitcnt lgkmcnt(4)\n\t" \
    "v_mfma_f32_16x16x32_bf16 v[220:223], v[244:247], v[172:175], v[220:223]\n\t" \
    "ds_read_b128 v[240:243], %[va] offset:3072\n\t" \
    "v_mfma_f32_16x16x32_bf16 v[204:207], v[244:247], v[196:199], v[204:207]\n\t" \
    "s_waitcnt lgkmcnt(4)\n\t" \
    "v_mfma_f32_16x16x32_bf16 v[200:203], v[224:227], v[176:179], v[200:203]\n\t" \
    "ds_read_b128 v[244:247], %[va] offset:9216\n\t" \
    "s_waitcnt lgkmcnt(4)\n\t" \
    "v_mfma_f32_16x16x32_bf16 v[204:207], v[228:231], v[176:179], v[204:207]\n\t" \
    "s_waitcnt lgkmcnt(3)\n\t" \
    "v_mfma_f32_16x16x32_bf16 v[200:203], v[232:235], v[184:187], v[200:203]\n\t" \
    "s_waitcnt lgkmcnt(2)\n\t" \
    "v_mfma_f32_16x16x32_bf16 v[204:207], v[236:239], v[184:187], v[204:207]\n\t" \
    "v_cvt_pk_bf16_f32 v216, v216, v217\n\t" \
    "s_waitcnt lgkmcnt(1)\n\t" \
    "v_mfma_f32_16x16x32_bf16 v[200:203], v[240:243], v[188:191], v[200:203]\n\t" \
    "v_cvt_pk_bf16_f32 v217, v218, v219\n\t" \
    "s_waitcnt lgkmcnt(0)\n\t" \
    "v_mfma_f32_16x16x32_bf16 v[204:207], v[244:247], v[188:191], v[204:207]\n\t" \
    "v_cvt_pk_bf16_f32 v218, v220, v221\n\t" \
    "v_cvt_pk_bf16_f32 v219, v222, v223\n\t" \
    "buffer_store_dwordx4 v[216:219], %[l16], %[rd], %[so] offen sc1\n\t" \
    "s_add_u32 %[so], %[so], 0x10000\n\t" \
    "s_nop 0\n\t" \
    "v_cvt_pk_bf16_f32 v200, v200, v201\n\t" \
    "v_cvt_pk_bf16_f32 v201, v202, v203\n\t" \
    "s_nop 1\n\t" \
    "v_cvt_pk_bf16_f32 v202, v204, v205\n\t" \
    "v_cvt_pk_bf16_f32 v203, v206, v207\n\t" \
    "buffer_store_dwordx4 v[200:203], %[l16], %[rd], %[so] offen sc1\n\t" \
    "s_add_u32 %[so], %[so], 0x10000\n\t" \
    "s_nop 1\n\t"
#define ASM_CRIT2 \
    "s_waitcnt lgkmcnt(0)\n\t" \
    "s_barrier\n\t" ASM_ST \
    "ds_read_b128 v[204:207], v200 offset:11264\n\t" \
    "ds_read_b128 v[208:211], v200 offset:12288\n\t" \
    "ds_read_b128 v[200:203], v200 offset:10240\n\t" \
    "s_waitcnt lgkmcnt(2)\n\t" \
    "v_smfmac_f32_16x16x64_bf16 v[224:227], v[204:207], v[24:31], %[sidx]\n\t" \
    "v_smfmac_f32_16x16x64_bf16 v[220:223], v[204:207], v[0:7], %[sidx]\n\t" \
    "s_waitcnt lgkmcnt(1)\n\t" \
    "v_smfmac_f32_16x16x64_bf16 v[224:227], v[208:211], v[32:39], %[sidx]\n\t" \
    "v_smfmac_f32_16x16x64_bf16 v[220:223], v[208:211], v[8:15], %[sidx]\n\t" \
    "s_waitcnt lgkmcnt(0)\n\t" \
    "v_smfmac_f32_16x16x64_bf16 v[224:227], v[200:203], v[16:23], %[sidx]\n\t" \
    "v_smfmac_f32_16x16x64_bf16 v[220:223], v[200:203], v[212:219], %[sidx]\n\t" \
    "ds_read_b128 v[212:215], %[wl] offset:2048\n\t" \
    "ds_read_b128 v[216:219], %[wl] offset:3072\n\t" \
    "v_smfmac_f32_16x16x64_bf16 v[232:235], v[204:207], v[64:71], %[sidx]\n\t" \
    "v_smfmac_f32_16x16x64_bf16 v[228:231], v[204:207], v[40:47], %[sidx]\n\t" \
    "v_smfmac_f32_16x16x64_bf16 v[232:235], v[208:211], v[72:79], %[sidx]\n\t" \
    "v_permlane32_swap_b32 v220, v224\n\t" \
    "v_permlane32_swap_b32 v221, v225\n\t" \
    "v_permlane32_swap_b32 v222, v226\n\t" \
    "v_smfmac_f32_16x16x64_bf16 v[228:231], v[208:211], v[48:55], %[sidx]\n\t" \
    "v_permlane32_swap_b32 v223, v227\n\t" \
    "v_add_f32 v220, v220, v224\n\t" \
    "v_add_f32 v221, v221, v225\n\t" \
    "v_smfmac_f32_16x16x64_bf16 v[232:235], v[200:203], v[56:63], %[sidx]\n\t" \
    "v_add_f32 v222, v222, v226\n\t" \
    "v_add_f32 v223, v223, v227\n\t" \
    "v_cvt_pk_bf16_f32 v226, v220, v221\n\t" \
    "s_waitcnt lgkmcnt(0)\n\t" \
    "v_smfmac_f32_16x16x64_bf16 v[228:231], v[200:203], v[212:219], %[sidx]\n\t" \
    "ds_read_b128 v[212:215], %[wl] offset:4096\n\t" \
    "ds_read_b128 v[216:219], %[wl] offset:5120\n\t" \
    "v_cvt_pk_bf16_f32 v227, v222, v223\n\t" \
    "v_min_u32 v226, -2, v226\n\t" \
    "v_min_u32 v227, -2, v227\n\t" \
    "v_smfmac_f32_16x16x64_bf16 v[240:243], v[204:207], v[104:111], %[sidx]\n\t" \
    "v_smfmac_f32_16x16x64_bf16 v[236:239], v[204:207], v[80:87], %[sidx]\n\t" \
    "v_smfmac_f32_16x16x64_bf16 v[240:243], v[208:211], v[112:119], %[sidx]\n\t" \
    "v_permlane32_swap_b32 v228, v232\n\t" \
    "v_permlane32_swap_b32 v229, v233\n\t" \
    "v_permlane32_swap_b32 v230, v234\n\t" \
    "v_smfmac_f32_16x16x64_bf16 v[236:239], v[208:211], v[88:95], %[sidx]\n\t" \
    "v_permlane32_swap_b32 v231, v235\n\t" \
    "v_add_f32 v228, v228, v232\n\t" \
    "v_add_f32 v229, v229, v233\n\t" \
    "v_smfmac_f32_16x16x64_bf16 v[240:243], v[200:203], v[96:103], %[sidx]\n\t" \
    "v_add_f32 v230, v230, v234\n\t" \
    "v_add_f32 v231, v231, v235\n\t" \
    "v_cvt_pk_bf16_f32 v228, v228, v229\n\t" \
    "s_waitcnt lgkmcnt(0)\n\t" \
    "v_smfmac_f32_16x16x64_bf16 v[236:239], v[200:203], v[212:219], %[sidx]\n\t" \
    "ds_read_b128 v[212:215], %[wl] offset:6144\n\t" \
    "ds_read_b128 v[216:219], %[wl] offset:7168\n\t" \
    "v_cvt_pk_bf16_f32 v229, v230, v231\n\t" \
    "v_min_u32 v228, -2, v228\n\t" \
    "v_min_u32 v229, -2, v229\n\t" \
    "s_nop 1\n\t" \
    "v_permlane16_swap_b32 v226, v228\n\t" \
    "v_permlane16_swap_b32 v227, v229\n\t" \
    "buffer_store_dwordx4 v[226:229], %[vo], %[rr], %[so] offen sc1\n\t" \
    "s_add_u32 %[so], %[so], 0x10000\n\t" \
    "v_smfmac_f32_16x16x64_bf16 v[248:251], v[204:207], v[144:151], %[sidx]\n\t" \
    "v_mov_b64 v[220:221], 0\n\t" \
    "v_mov_b64 v[222:223], 0\n\t" \
    "v_smfmac_f32_16x16x64_bf16 v[244:247], v[204:207], v[120:127], %[sidx]\n\t" \
    "v_mov_b64 v[224:225], 0\n\t" \
    "v_mov_b64 v[226:227], 0\n\t" \
    "v_smfmac_f32_16x16x64_bf16 v[248:251], v[208:211], v[152:159], %[sidx]\n\t" \
    "v_permlane32_swap_b32 v236, v240\n\t" \
    "v_permlane32_swap_b32 v237, v241\n\t" \
    "v_permlane32_swap_b32 v238, v242\n\t" \
    "v_smfmac_f32_16x16x64_bf16 v[244:247], v[208:211], v[128:135], %[sidx]\n\t" \
    "v_permlane32_swap_b32 v239, v243\n\t" \
    "v_add_f32 v236, v236, v240\n\t" \
    "v_add_f32 v237, v237, v241\n\t" \
    "v_smfmac_f32_16x16x64_bf16 v[248:251], v[200:203], v[136:143], %[sidx]\n\t" \
    "v_add_f32 v238, v238, v242\n\t" \
    "v_add_f32 v239, v239, v243\n\t" \
    "v_cvt_pk_bf16_f32 v242, v236, v237\n\t" \
    "s_waitcnt lgkmcnt(0)\n\t" \
    "v_smfmac_f32_16x16x64_bf16 v[244:247], v[200:203], v[212:219], %[sidx]\n\t" \
    "ds_read_b128 v[212:215], %[wl] offset:8192\n\t" \
    "ds_read_b128 v[216:219], %[wl] offset:9216\n\t" \
    "v_cvt_pk_bf16_f32 v243, v238, v239\n\t" \
    "v_min_u32 v242, -2, v242\n\t" \
    "v_min_u32 v243, -2, v243\n\t" \
    "v_smfmac_f32_16x16x64_bf16 v[224:227], v[204:207], v[184:191], %[sidx]\n\t" \
    "v_and_b32 v232, 0x7fff, %[vo]\n\t" \
    "v_lshrrev_b32 v233, 15, %[vo]\n\t" \
    "v_smfmac_f32_16x16x64_bf16 v[220:223], v[204:207], v[160:167], %[sidx]\n\t" \
    "v_lshl_add_u32 v232, v233, 3, v232\n\t" \
    "v_permlane32_swap_b32 v244, v248\n\t" \
    "v_smfmac_f32_16x16x64_bf16 v[224:227], v[208:211], v[192:199], %[sidx]\n\t" \
    "v_permlane32_swap_b32 v245, v249\n\t" \
    "v_permlane32_swap_b32 v246, v250\n\t" \
    "v_permlane32_swap_b32 v247, v251\n\t" \
    "v_smfmac_f32_16x16x64_bf16 v[220:223], v[208:211], v[168:175], %[sidx]\n\t" \
    "v_add_f32 v244, v244, v248\n\t" \
    "v_add_f32 v245, v245, v249\n\t" \
    "v_add_f32 v246, v246, v250\n\t" \
    "v_smfmac_f32_16x16x64_bf16 v[224:227], v[200:203], v[176:183], %[sidx]\n\t" \
    "v_add_f32 v247, v247, v251\n\t" \
    "v_cvt_pk_bf16_f32 v244, v244, v245\n\t" \
    "v_cvt_pk_bf16_f32 v245, v246, v247\n\t" \
    "s_waitcnt lgkmcnt(0)\n\t" \
    "v_smfmac_f32_16x16x64_bf16 v[220:223], v[200:203], v[212:219], %[sidx]\n\t" \
    "v_min_u32 v244, -2, v244\n\t" \
    "v_min_u32 v245, -2, v245\n\t" \
    "s_nop 1\n\t" \
    "v_permlane16_swap_b32 v242, v244\n\t" \
    "v_permlane16_swap_b32 v243, v245\n\t" \
    "buffer_store_dwordx4 v[242:245], %[vo], %[rr], %[so] offen sc1\n\t" \
    "s_add_u32 %[so], %[so], 0x10000\n\t" \
    "s_nop 0\n\t" \
    "v_permlane32_swap_b32 v220, v224\n\t" \
    "v_permlane32_swap_b32 v221, v225\n\t" \
    "v_permlane32_swap_b32 v222, v226\n\t" \
    "v_permlane32_swap_b32 v223, v227\n\t" \
    "v_add_f32 v220, v220, v224\n\t" \
    "v_add_f32 v221, v221, v225\n\t" \
    "v_add_f32 v222, v222, v226\n\t" \
    "v_add_f32 v223, v223, v227\n\t" \
    "v_cvt_pk_bf16_f32 v220, v220, v221\n\t" \
    "v_cvt_pk_bf16_f32 v221, v222, v223\n\t" \
    "v_min_u32 v220, -2, v220\n\t" \
    "v_min_u32 v221, -2, v221\n\t" \
    "buffer_store_dwordx2 v[220:221], v232, %[rr], %[so] offen sc1\n\t" \
    "s_nop 1\n\t"
#define ASM_P4_0 \
    "ds_read_b128 v[200:203], %[qa] offset:0\n\t" \
    "ds_read_b128 v[204:207], %[qa] offset:1024\n\t" \
    "ds_read_b128 v[208:211], %[qa] offset:2048\n\t" \
    "ds_read_b128 v[212:215], %[qa] offset:3072\n\t" \
    "ds_read_b128 v[216:219], %[qa] offset:4096\n\t" \
    "ds_read_b128 v[220:223], %[qa] offset:5120\n\t" \
    "ds_read_b128 v[224:227], %[qa] offset:12288\n\t" \
    "ds_read_b128 v[228:231], %[qa] offset:13312\n\t" \
    "ds_read_b128 v[232:235], %[qa] offset:14336\n\t" \
    "ds_read_b128 v[236:239], %[qa] offset:15360\n\t" \
    "s_waitcnt lgkmcnt(0)\n\t" \
    "s_barrier\n\t" ASM_ST \
    "v_mfma_f32_16x16x32_bf16 v[240:243], v[224:227], v[200:203], v[240:243]\n\t" \
    "ds_read_b128 v[224:227], %[qa] offset:16384\n\t" \
    "v_mfma_f32_16x16x32_bf16 v[240:243], v[228:231], v[204:207], v[240:243]\n\t" \
    "ds_read_b128 v[228:231], %[qa] offset:17408\n\t" \
    "v_mfma_f32_16x16x32_bf16 v[240:243], v[232:235], v[208:211], v[240:243]\n\t" \
    "ds_read_b128 v[232:235], %[km] offset:0\n\t" \
    "v_mfma_f32_16x16x32_bf16 v[240:243], v[236:239], v[212:215], v[240:243]\n\t" \
    "ds_read_b128 v[236:239], %[km] offset:512\n\t" \
    "s_waitcnt lgkmcnt(3)\n\t" \
    "v_mfma_f32_16x16x32_bf16 v[240:243], v[224:227], v[216:219], v[240:243]\n\t" \
    "ds_read_b128 v[224:227], %[km] offset:1024\n\t" \
    "s_waitcnt lgkmcnt(3)\n\t" \
    "v_mfma_f32_16x16x32_bf16 v[240:243], v[228:231], v[220:223], v[240:243]\n\t" \
    "ds_read_b128 v[228:231], %[km] offset:1536\n\t" \
    "s_waitcnt lgkmcnt(3)\n\t" \
    "v_mfma_f32_16x16x32_bf16 v[248:251], v[232:235], v[200:203], 0\n\t" \
    "ds_read_b128 v[232:235], %[km] offset:2048\n\t" \
    "s_waitcnt lgkmcnt(3)\n\t" \
    "v_mfma_f32_16x16x32_bf16 v[248:251], v[236:239], v[204:207], v[248:251]\n\t" \
    "ds_read_b128 v[236:239], %[km] offset:2560\n\t" \
    "s_waitcnt lgkmcnt(3)\n\t" \
    "v_mfma_f32_16x16x32_bf16 v[248:251], v[224:227], v[208:211], v[248:251]\n\t" \
    "s_waitcnt lgkmcnt(2)\n\t" \
    "v_mfma_f32_16x16x32_bf16 v[248:251], v[228:231], v[212:215], v[248:251]\n\t" \
    "s_waitcnt lgkmcnt(1)\n\t" \
    "v_mfma_f32_16x16x32_bf16 v[248:251], v[232:235], v[216:219], v[248:251]\n\t" \
    "s_waitcnt lgkmcnt(0)\n\t" \
    "v_mfma_f32_16x16x32_bf16 v[248:251], v[236:239], v[220:223], v[248:251]\n\t" \
    "v_max3_f32 v224, v240, v241, v242\n\t" \
    "v_mov_b32 v225, v243\n\t" \
    "v_lshlrev_b32 v201, 23, %[qa]\n\t" \
    "v_and_b32 v201, 0x80000000, v201\n\t" \
    "v_and_b32 v200, 0x3ff, %[qa]\n\t" \
    "v_or_b32 v200, v201, v200\n\t" \
    "s_nop 1\n\t" \
    "v_max3_f32 v224, v224, v248, v249\n\t" \
    "v_max3_f32 v225, v225, v250, v251\n\t" \
    "v_max_f32 v224, v224, v225\n\t" \
    "v_mov_b32 v225, v224\n\t" \
    "s_nop 1\n\t" \
    "v_permlane16_swap_b32 v224, v225\n\t" \
    "v_max_f32 v224, v224, v225\n\t" \
    "v_mov_b32 v225, v224\n\t" \
    "s_nop 1\n\t" \
    "v_permlane32_swap_b32 v224, v225\n\t" \
    "v_max_f32 v224, v224, v225\n\t" \
    "v_mul_f32 v224, %[scl], v224\n\t" \
    "v_fma_f32 v240, v240, %[scl], -v224\n\t" \
    "v_fma_f32 v241, v241, %[scl], -v224\n\t" \
    "v_exp_f32 v240, v240\n\t" \
    "v_fma_f32 v242, v242, %[scl], -v224\n\t" \
    "v_exp_f32 v241, v241\n\t" \
    "v_fma_f32 v243, v243, %[scl], -v224\n\t" \
    "v_exp_f32 v242, v242\n\t" \
    "v_fma_f32 v248, v248, %[scl], -v224\n\t" \
    "v_exp_f32 v243, v243\n\t" \
    "v_fma_f32 v249, v249, %[scl], -v224\n\t" \
    "v_exp_f32 v248, v248\n\t" \
    "v_fma_f32 v250, v250, %[scl], -v224\n\t" \
    "v_exp_f32 v249, v249\n\t" \
    "v_fma_f32 v251, v251, %[scl], -v224\n\t" \
    "v_exp_f32 v250, v250\n\t" \
    "v_exp_f32 v251, v251\n\t" \
    "v_add_f32 v224, v240, v241\n\t" \
    "v_add_f32 v225, v242, v243\n\t" \
    "v_add_f32 v232, v248, v249\n\t" \
    "v_add_f32 v233, v250, v251\n\t" \
    "v_add_f32 v224, v224, v225\n\t" \
    "v_add_f32 v232, v232, v233\n\t" \
    "v_fma_f32 v224, v232, 0.5, v224\n\t" \
    "v_mov_b32 v225, v224\n\t" \
    "s_nop 1\n\t" \
    "v_permlane16_swap_b32 v224, v225\n\t" \
    "v_add_f32 v224, v224, v225\n\t" \
    "v_mov_b32 v225, v224\n\t" \
    "s_nop 1\n\t" \
    "v_permlane32_swap_b32 v224, v225\n\t" \
    "v_add_f32 v224, v224, v225\n\t" \
    "v_rcp_f32 v224, v224\n\t" \
    "s_nop 0\n\t" \
    "v_mul_f32 v240, v240, v224\n\t" \
    "v_mul_f32 v241, v241, v224\n\t" \
    "v_mul_f32 v242, v242, v224\n\t" \
    "v_mul_f32 v243, v243, v224\n\t" \
    "v_mul_f32 v248, v248, v224\n\t" \
    "v_mul_f32 v249, v249, v224\n\t" \
    "v_mul_f32 v250, v250, v224\n\t" \
    "v_mul_f32 v251, v251, v224\n\t" \
    "v_cvt_pk_bf16_f32 v228, |v240|, |v241|\n\t" \
    "v_cvt_pk_bf16_f32 v229, |v242|, |v243|\n\t" \
    "v_cvt_pk_bf16_f32 v232, |v248|, |v249|\n\t" \
    "v_cvt_pk_bf16_f32 v233, |v250|, |v251|\n\t" \
    "v_permlane16_swap_b32 v228, v230\n\t" \
    "v_permlane16_swap_b32 v229, v231\n\t" \
    "buffer_store_dwordx4 v[228:231], v200, %[rr], %[so] offen sc1\n\t" \
    "buffer_store_dwordx2 v[232:233], %[pm], %[rr], %[so] offen sc1\n\t" \
    "s_nop 1\n\t"
#define ASM_P4_1 \
    "ds_read_b128 v[200:203], %[qa] offset:0\n\t" \
    "ds_read_b128 v[204:207], %[qa] offset:1024\n\t" \
    "ds_read_b128 v[208:211], %[qa] offset:2048\n\t" \
    "ds_read_b128 v[212:215], %[qa] offset:3072\n\t" \
    "ds_read_b128 v[216:219], %[qa] offset:4096\n\t" \
    "ds_read_b128 v[220:223], %[qa] offset:5120\n\t" \
    "ds_read_b128 v[224:227], %[qa] offset:12288\n\t" \
    "ds_read_b128 v[228:231], %[qa] offset:13312\n\t" \
    "ds_read_b128 v[232:235], %[qa] offset:14336\n\t" \
    "ds_read_b128 v[236:239], %[qa] offset:15360\n\t" \
    "s_waitcnt lgkmcnt(0)\n\t" \
    "s_barrier\n\t" ASM_ST \
    "v_mfma_f32_16x16x32_bf16 v[240:243], v[224:227], v[200:203], v[240:243]\n\t" \
    "ds_read_b128 v[224:227], %[qa] offset:16384\n\t" \
    "v_mfma_f32_16x16x32_bf16 v[240:243], v[228:231], v[204:207], v[240:243]\n\t" \
    "ds_read_b128 v[228:231], %[qa] offset:17408\n\t" \
    "v_mfma_f32_16x16x32_bf16 v[240:243], v[232:235], v[208:211], v[240:243]\n\t" \
    "ds_read_b128 v[232:235], %[qa] offset:6144\n\t" \
    "v_mfma_f32_16x16x32_bf16 v[240:243], v[236:239], v[212:215], v[240:243]\n\t" \
    "ds_read_b128 v[236:239], %[qa] offset:7168\n\t" \
    "s_waitcnt lgkmcnt(3)\n\t" \
    "v_mfma_f32_16x16x32_bf16 v[240:243], v[224:227], v[216:219], v[240:243]\n\t" \
    "ds_read_b128 v[224:227], %[qa] offset:8192\n\t" \
    "s_waitcnt lgkmcnt(3)\n\t" \
    "v_mfma_f32_16x16x32_bf16 v[240:243], v[228:231], v[220:223], v[240:243]\n\t" \
    "ds_read_b128 v[228:231], %[qa] offset:9216\n\t" \
    "s_waitcnt lgkmcnt(3)\n\t" \
    "v_mfma_f32_16x16x32_bf16 v[244:247], v[232:235], v[200:203], 0\n\t" \
    "ds_read_b128 v[232:235], %[qa] offset:10240\n\t" \
    "s_waitcnt lgkmcnt(3)\n\t" \
    "v_mfma_f32_16x16x32_bf16 v[244:247], v[236:239], v[204:207], v[244:247]\n\t" \
    "ds_read_b128 v[236:239], %[qa] offset:11264\n\t" \
    "s_waitcnt lgkmcnt(3)\n\t" \
    "v_mfma_f32_16x16x32_bf16 v[244:247], v[224:227], v[208:211], v[244:247]\n\t" \
    "ds_read_b128 v[224:227], %[km] offset:0\n\t" \
    "s_waitcnt lgkmcnt(3)\n\t" \
    "v_mfma_f32_16x16x32_bf16 v[244:247], v[228:231], v[212:215], v[244:247]\n\t" \
    "ds_read_b128 v[228:231], %[km] offset:512\n\t" \
    "s_waitcnt lgkmcnt(3)\n\t" \
    "v_mfma_f32_16x16x32_bf16 v[244:247], v[232:235], v[216:219], v[244:247]\n\t" \
    "ds_read_b128 v[232:235], %[km] offset:1024\n\t" \
    "s_waitcnt lgkmcnt(3)\n\t" \
    "v_mfma_f32_16x16x32_bf16 v[244:247], v[236:239], v[220:223], v[244:247]\n\t" \
    "ds_read_b128 v[236:239], %[km] offset:1536\n\t" \
    "s_waitcnt lgkmcnt(3)\n\t" \
    "v_mfma_f32_16x16x32_bf16 v[248:251], v[224:227], v[200:203], 0\n\t" \
    "ds_read_b128 v[224:227], %[km] offset:2048\n\t" \
    "s_waitcnt lgkmcnt(3)\n\t" \
    "v_mfma_f32_16x16x32_bf16 v[248:251], v[228:231], v[204:207], v[248:251]\n\t" \
    "ds_read_b128 v[228:231], %[km] offset:2560\n\t" \
    "s_waitcnt lgkmcnt(3)\n\t" \
    "v_mfma_f32_16x16x32_bf16 v[248:251], v[232:235], v[208:211], v[248:251]\n\t" \
    "s_waitcnt lgkmcnt(2)\n\t" \
    "v_mfma_f32_16x16x32_bf16 v[248:251], v[236:239], v[212:215], v[248:251]\n\t" \
    "s_waitcnt lgkmcnt(1)\n\t" \
    "v_mfma_f32_16x16x32_bf16 v[248:251], v[224:227], v[216:219], v[248:251]\n\t" \
    "s_waitcnt lgkmcnt(0)\n\t" \
    "v_mfma_f32_16x16x32_bf16 v[248:251], v[228:231], v[220:223], v[248:251]\n\t" \
    "v_max3_f32 v224, v240, v241, v242\n\t" \
    "v_max3_f32 v225, v244, v245, v246\n\t" \
    "v_max3_f32 v224, v224, v243, v247\n\t" \
    "v_and_b32 v200, 0x3ff, %[qa]\n\t" \
    "s_nop 3\n\t" \
    "v_max3_f32 v224, v224, v248, v249\n\t" \
    "v_max3_f32 v225, v225, v250, v251\n\t" \
    "v_max_f32 v224, v224, v225\n\t" \
    "v_mov_b32 v225, v224\n\t" \
    "s_nop 1\n\t" \
    "v_permlane16_swap_b32 v224, v225\n\t" \
    "v_max_f32 v224, v224, v225\n\t" \
    "v_mov_b32 v225, v224\n\t" \
    "s_nop 1\n\t" \
    "v_permlane32_swap_b32 v224, v225\n\t" \
    "v_max_f32 v224, v224, v225\n\t" \
    "v_mul_f32 v224, %[scl], v224\n\t" \
    "v_fma_f32 v240, v240, %[scl], -v224\n\t" \
    "v_fma_f32 v241, v241, %[scl], -v224\n\t" \
    "v_exp_f32 v240, v240\n\t" \
    "v_fma_f32 v242, v242, %[scl], -v224\n\t" \
    "v_exp_f32 v241, v241\n\t" \
    "v_fma_f32 v243, v243, %[scl], -v224\n\t" \
    "v_exp_f32 v242, v242\n\t" \
    "v_fma_f32 v244, v244, %[scl], -v224\n\t" \
    "v_exp_f32 v243, v243\n\t" \
    "v_fma_f32 v245, v245, %[scl], -v224\n\t" \
    "v_exp_f32 v244, v244\n\t" \
    "v_fma_f32 v246, v246, %[scl], -v224\n\t" \
    "v_exp_f32 v245, v245\n\t" \
    "v_fma_f32 v247, v247, %[scl], -v224\n\t" \
    "v_exp_f32 v246, v246\n\t" \
    "v_fma_f32 v248, v248, %[scl], -v224\n\t" \
    "v_exp_f32 v247, v247\n\t" \
    "v_fma_f32 v249, v249, %[scl], -v224\n\t" \
    "v_exp_f32 v248, v248\n\t" \
    "v_fma_f32 v250, v250, %[scl], -v224\n\t" \
    "v_exp_f32 v249, v249\n\t" \
    "v_fma_f32 v251, v251, %[scl], -v224\n\t" \
    "v_exp_f32 v250, v250\n\t" \
    "v_exp_f32 v251, v251\n\t" \
    "v_add_f32 v224, v240, v241\n\t" \
    "v_add_f32 v225, v242, v243\n\t" \
    "v_add_f32 v226, v244, v245\n\t" \
    "v_add_f32 v227, v246, v247\n\t" \
    "v_add_f32 v232, v248, v249\n\t" \
    "v_add_f32 v233, v250, v251\n\t" \
    "v_add_f32 v224, v224, v225\n\t" \
    "v_add_f32 v226, v226, v227\n\t" \
    "v_add_f32 v232, v232, v233\n\t" \
    "v_add_f32 v224, v224, v226\n\t" \
    "v_fma_f32 v224, v232, 0.5, v224\n\t" \
    "v_mov_b32 v225, v224\n\t" \
    "s_nop 1\n\t" \
    "v_permlane16_swap_b32 v224, v225\n\t" \
    "v_add_f32 v224, v224, v225\n\t" \
    "v_mov_b32 v225, v224\n\t" \
    "s_nop 1\n\t" \
    "v_permlane32_swap_b32 v224, v225\n\t" \
    "v_add_f32 v224, v224, v225\n\t" \
    "v_rcp_f32 v224, v224\n\t" \
    "s_nop 0\n\t" \
    "v_mul_f32 v240, v240, v224\n\t" \
    "v_mul_f32 v241, v241, v224\n\t" \
    "v_mul_f32 v242, v242, v224\n\t" \
    "v_mul_f32 v243, v243, v224\n\t" \
    "v_mul_f32 v244, v244, v224\n\t" \
    "v_mul_f32 v245, v245, v224\n\t" \
    "v_mul_f32 v246, v246, v224\n\t" \
    "v_mul_f32 v247, v247, v224\n\t" \
    "v_mul_f32 v248, v248, v224\n\t" \
    "v_mul_f32 v249, v249, v224\n\t" \
    "v_mul_f32 v250, v250, v224\n\t" \
    "v_mul_f32 v251, v251, v224\n\t" \
    "v_cvt_pk_bf16_f32 v230, |v240|, |v241|\n\t" \
    "v_cvt_pk_bf16_f32 v231, |v242|, |v243|\n\t" \
    "v_cvt_pk_bf16_f32 v228, |v244|, |v245|\n\t" \
    "v_cvt_pk_bf16_f32 v229, |v246|, |v247|\n\t" \
    "v_cvt_pk_bf16_f32 v232, |v248|, |v249|\n\t" \
    "v_cvt_pk_bf16_f32 v233, |v250|, |v251|\n\t" \
    "buffer_store_dwordx4 v[228:231], v200, %[rr], %[so] offen sc1\n\t" \
    "buffer_store_dwordx2 v[232:233], %[pm], %[rr], %[so] offen sc1\n\t" \
    "s_nop 1\n\t"
#define ASM_RCRIT \
    "s_movk_i32 %[n], 0x400\n\t" \
    ".Lfa%=:\n\t" \
    "s_mov_b32 %[a], 0\n\t" \
    "v_max_u32 v196, v176, v177\n\t" \
    "v_max3_u32 v196, v178, v179, v196\n\t" \
    "v_cmp_eq_u32 vcc, -1, v196\n\t" \
    "s_cbranch_vccz .Lga0_%=\n\t" \
    "buffer_load_dwordx4 v[176:179], %[ov], %[rs], %[sv] offen sc1\n\t" \
    "s_mov_b32 %[a], 1\n\t" \
    ".Lga0_%=:\n\t" \
    "v_max_u32 v196, v180, v181\n\t" \
    "v_max3_u32 v196, v182, v183, v196\n\t" \
    "v_cmp_eq_u32 vcc, -1, v196\n\t" \
    "s_cbranch_vccz .Lga1_%=\n\t" \
    "buffer_load_dwordx4 v[180:183], %[ov], %[rs], %[sv] offen offset:1024 sc1\n\t" \
    "s_mov_b32 %[a], 1\n\t" \
    ".Lga1_%=:\n\t" \
    "s_cmp_eq_u32 %[a], 0\n\t" \
    "s_cbranch_scc1 .Lha%=\n\t" \
    "s_waitcnt vmcnt(12)\n\t" \
    "v_max_u32 v196, v176, v177\n\t" \
    "v_max3_u32 v196, v178, v179, v196\n\t" \
    "v_max3_u32 v196, v180, v181, v196\n\t" \
    "v_max3_u32 v196, v182, v183, v196\n\t" \
    "v_cmp_eq_u32 vcc, -1, v196\n\t" \
    "s_cbranch_vccz .Lha%=\n\t" \
    "s_nop 7\n\t" \
    "v_max_u32 v196, v176, v177\n\t" \
    "v_max3_u32 v196, v178, v179, v196\n\t" \
    "v_max3_u32 v196, v180, v181, v196\n\t" \
    "v_max3_u32 v196, v182, v183, v196\n\t" \
    "v_cmp_eq_u32 vcc, -1, v196\n\t" \
    "s_cbranch_vccz .Lha%=\n\t" \
    "s_nop 7\n\t" \
    "v_max_u32 v196, v176, v177\n\t" \
    "v_max3_u32 v196, v178, v179, v196\n\t" \
    "v_max3_u32 v196, v180, v181, v196\n\t" \
    "v_max3_u32 v196, v182, v183, v196\n\t" \
    "v_cmp_eq_u32 vcc, -1, v196\n\t" \
    "s_cbranch_vccz .Lha%=\n\t" \
    "s_nop 7\n\t" \
    "v_max_u32 v196, v176, v177\n\t" \
    "v_max3_u32 v196, v178, v179, v196\n\t" \
    "v_max3_u32 v196, v180, v181, v196\n\t" \
    "v_max3_u32 v196, v182, v183, v196\n\t" \
    "v_cmp_eq_u32 vcc, -1, v196\n\t" \
    "s_cbranch_vccz .Lha%=\n\t" \
    "s_nop 7\n\t" \
    "s_sub_u32 %[n], %[n], 1\n\t" \
    "s_cmp_lg_u32 %[n], 0\n\t" \
    "s_cbranch_scc1 .Lfa%=\n\t" \
    ".Lha%=:\n\t" \
    "s_cmp_eq_u32 %[n], 0\n\t" \
    "s_cbranch_scc1 .Lre%=\n\t" \
    ".Lfb%=:\n\t" \
    "s_mov_b32 %[a], 0\n\t" \
    "v_max_u32 v196, v184, v185\n\t" \
    "v_max3_u32 v196, v186, v187, v196\n\t" \
    "v_cmp_eq_u32 vcc, -1, v196\n\t" \
    "s_cbranch_vccz .Lgb0_%=\n\t" \
    "buffer_load_dwordx4 v[184:187], %[o0], %[rs], %[sp] offen sc1\n\t" \
    "s_mov_b32 %[a], 1\n\t" \
    ".Lgb0_%=:\n\t" \
    "v_max_u32 v196, v188, v189\n\t" \
    "v_max3_u32 v196, v190, v191, v196\n\t" \
    "v_cmp_eq_u32 vcc, -1, v196\n\t" \
    "s_cbranch_vccz .Lgb1_%=\n\t" \
    "buffer_load_dwordx4 v[188:191], %[o1], %[rs], %[sp] offen sc1\n\t" \
    "s_mov_b32 %[a], 1\n\t" \
    ".Lgb1_%=:\n\t" \
    "v_max_u32 v196, v192, v193\n\t" \
    "v_max3_u32 v196, v194, v195, v196\n\t" \
    "v_cmp_eq_u32 vcc, -1, v196\n\t" \
    "s_cbranch_vccz .Lgb2_%=\n\t" \
    "buffer_load_dwordx4 v[192:195], %[o2], %[rs], %[sp] offen sc1\n\t" \
    "s_mov_b32 %[a], 1\n\t" \
    ".Lgb2_%=:\n\t" \
    "s_cmp_eq_u32 %[a], 0\n\t" \
    "s_cbranch_scc1 .Lhb%=\n\t" \
    "s_waitcnt vmcnt(12)\n\t" \
    "v_max_u32 v196, v184, v185\n\t" \
    "v_max3_u32 v196, v186, v187, v196\n\t" \
    "v_max3_u32 v196, v188, v189, v196\n\t" \
    "v_max3_u32 v196, v190, v191, v196\n\t" \
    "v_max3_u32 v196, v192, v193, v196\n\t" \
    "v_max3_u32 v196, v194, v195, v196\n\t" \
    "v_cmp_eq_u32 vcc, -1, v196\n\t" \
    "s_cbranch_vccz .Lhb%=\n\t" \
    "s_nop 7\n\t" \
    "v_max_u32 v196, v184, v185\n\t" \
    "v_max3_u32 v196, v186, v187, v196\n\t" \
    "v_max3_u32 v196, v188, v189, v196\n\t" \
    "v_max3_u32 v196, v190, v191, v196\n\t" \
    "v_max3_u32 v196, v192, v193, v196\n\t" \
    "v_max3_u32 v196, v194, v195, v196\n\t" \
    "v_cmp_eq_u32 vcc, -1, v196\n\t" \
    "s_cbranch_vccz .Lhb%=\n\t" \
    "s_nop 7\n\t" \
    "v_max_u32 v196, v184, v185\n\t" \
    "v_max3_u32 v196, v186, v187, v196\n\t" \
    "v_max3_u32 v196, v188, v189, v196\n\t" \
    "v_max3_u32 v196, v190, v191, v196\n\t" \
    "v_max3_u32 v196, v192, v193, v196\n\t" \
    "v_max3_u32 v196, v194, v195, v196\n\t" \
    "v_cmp_eq_u32 vcc, -1, v196\n\t" \
    "s_cbranch_vccz .Lhb%=\n\t" \
    "s_nop 7\n\t" \
    "v_max_u32 v196, v184, v185\n\t" \
    "v_max3_u32 v196, v186, v187, v196\n\t" \
    "v_max3_u32 v196, v188, v189, v196\n\t" \
    "v_max3_u32 v196, v190, v191, v196\n\t" \
    "v_max3_u32 v196, v192, v193, v196\n\t" \
    "v_max3_u32 v196, v194, v195, v196\n\t" \
    "v_cmp_eq_u32 vcc, -1, v196\n\t" \
    "s_cbranch_vccz .Lhb%=\n\t" \
    "s_nop 7\n\t" \
    "v_max_u32 v196, v184, v185\n\t" \
    "v_max3_u32 v196, v186, v187, v196\n\t" \
    "v_max3_u32 v196, v188, v189, v196\n\t" \
    "v_max3_u32 v196, v190, v191, v196\n\t" \
    "v_max3_u32 v196, v192, v193, v196\n\t" \
    "v_max3_u32 v196, v194, v195, v196\n\t" \
    "v_cmp_eq_u32 vcc, -1, v196\n\t" \
    "s_cbranch_vccz .Lhb%=\n\t" \
    "s_nop 7\n\t" \
    "v_max_u32 v196, v184, v185\n\t" \
    "v_max3_u32 v196, v186, v187, v196\n\t" \
    "v_max3_u32 v196, v188, v189, v196\n\t" \
    "v_max3_u32 v196, v190, v191, v196\n\t" \
    "v_max3_u32 v196, v192, v193, v196\n\t" \
    "v_max3_u32 v196, v194, v195, v196\n\t" \
    "v_cmp_eq_u32 vcc, -1, v196\n\t" \
    "s_cbranch_vccz .Lhb%=\n\t" \
    "s_nop 7\n\t" \
    "v_max_u32 v196, v184, v185\n\t" \
    "v_max3_u32 v196, v186, v187, v196\n\t" \
    "v_max3_u32 v196, v188, v189, v196\n\t" \
    "v_max3_u32 v196, v190, v191, v196\n\t" \
    "v_max3_u32 v196, v192, v193, v196\n\t" \
    "v_max3_u32 v196, v194, v195, v196\n\t" \
    "v_cmp_eq_u32 vcc, -1, v196\n\t" \
    "s_cbranch_vccz .Lhb%=\n\t" \
    "s_nop 7\n\t" \
    "v_max_u32 v196, v184, v185\n\t" \
    "v_max3_u32 v196, v186, v187, v196\n\t" \
    "v_max3_u32 v196, v188, v189, v196\n\t" \
    "v_max3_u32 v196, v190, v191, v196\n\t" \
    "v_max3_u32 v196, v192, v193, v196\n\t" \
    "v_max3_u32 v196, v194, v195, v196\n\t" \
    "v_cmp_eq_u32 vcc, -1, v196\n\t" \
    "s_cbranch_vccz .Lhb%=\n\t" \
    "s_nop 7\n\t" \
    "s_sub_u32 %[n], %[n], 1\n\t" \
    "s_cmp_lg_u32 %[n], 0\n\t" \
    "s_cbranch_scc1 .Lfb%=\n\t" \
    "s_branch .Lre%=\n\t" \
    ".Lhb%=:\n\t" \
    ASM_ST2 \
    "v_mfma_f32_16x16x32_bf16 v[160:163], v[192:195], v[176:179], 0\n\t" \
    "v_mfma_f32_16x16x32_bf16 v[164:167], v[192:195], v[180:183], 0\n\t" \
    "v_smfmac_f32_16x16x64_bf16 v[160:163], v[184:187], v[128:135], %[sidx]\n\t" \
    "v_smfmac_f32_16x16x64_bf16 v[164:167], v[184:187], v[144:151], %[sidx]\n\t" \
    "v_smfmac_f32_16x16x64_bf16 v[160:163], v[188:191], v[136:143], %[sidx]\n\t" \
    "v_smfmac_f32_16x16x64_bf16 v[164:167], v[188:191], v[152:159], %[sidx]\n\t" \
    "s_cmp_eq_u32 %[pub], 0\n\t" \
    "s_nop 7\n\t" \
    "v_permlane32_swap_b32 v160, v164\n\t" \
    "v_permlane32_swap_b32 v161, v165\n\t" \
    "v_permlane32_swap_b32 v162, v166\n\t" \
    "v_permlane32_swap_b32 v163, v167\n\t" \
    "v_add_f32 v160, v160, v164\n\t" \
    "v_add_f32 v161, v161, v165\n\t" \
    "ds_write2_b32 %[rw], v160, v161 offset1:80\n\t" \
    "v_add_f32 v162, v162, v166\n\t" \
    "v_add_f32 v163, v163, v167\n\t" \
    "ds_write2_b32 %[rw], v162, v163 offset0:160 offset1:240\n\t" \
    "s_waitcnt lgkmcnt(0)\n\t" \
    "s_barrier\n\t" ASM_ST \
    "s_cbranch_scc1 .Lre%=\n\t" \
    "ds_read_b128 v[200:203], %[rd] offset:0\n\t" \
    "ds_read_b128 v[204:207], %[rd] offset:16\n\t" \
    "ds_read_b128 v[208:211], %[rd] offset:32\n\t" \
    "ds_read_b128 v[212:215], %[rd] offset:48\n\t" \
    "s_waitcnt lgkmcnt(2)\n\t" \
    "v_pk_add_f32 v[200:201], v[200:201], v[202:203]\n\t" \
    "v_pk_add_f32 v[204:205], v[204:205], v[206:207]\n\t" \
    "s_waitcnt lgkmcnt(0)\n\t" \
    "v_pk_add_f32 v[208:209], v[208:209], v[210:211]\n\t" \
    "v_pk_add_f32 v[212:213], v[212:213], v[214:215]\n\t" \
    "v_pk_add_f32 v[200:201], v[200:201], v[204:205]\n\t" \
    "v_pk_add_f32 v[208:209], v[208:209], v[212:213]\n\t" \
    "v_pk_add_f32 v[200:201], v[200:201], v[208:209]\n\t" \
    "v_cvt_pk_bf16_f32 v200, v200, v201\n\t" \
    "v_min_u32 v200, -2, v200\n\t" \
    "buffer_store_dword v200, %[oo], %[rs], %[so] offen sc1\n\t" \
    ".Lre%=:\n\t"
#define ASM_RX \
    "s_cmp_eq_u32 %[fin], 0\n\t" \
    "s_cbranch_scc1 .Lx0%=\n\t" \
    "buffer_load_dwordx2 v[250:251], %[xo], %[rx], %[sx] offen sc1\n\t" \
    ".Lx0%=:\n\t" \
    "buffer_load_dword v248, %[fo], %[rs], %[fs] offen sc1\n\t" \
    "buffer_load_dwordx4 v[200:203], %[l16], %[rs], %[s0] offen offset:0 sc1\n\t" \
    "buffer_load_dwordx4 v[204:207], %[l16], %[rs], %[s0] offen offset:1024 sc1\n\t" \
    "buffer_load_dwordx4 v[208:211], %[l16], %[rs], %[s0] offen offset:2048 sc1\n\t" \
    "buffer_load_dwordx4 v[212:215], %[l16], %[rs], %[s0] offen offset:3072 sc1\n\t" \
    "buffer_load_dwordx4 v[216:219], %[l16], %[rs], %[s1] offen offset:0 sc1\n\t" \
    "buffer_load_dwordx4 v[220:223], %[l16], %[rs], %[s1] offen offset:1024 sc1\n\t" \
    "buffer_load_dwordx4 v[224:227], %[l16], %[rs], %[s1] offen offset:2048 sc1\n\t" \
    "buffer_load_dwordx4 v[228:231], %[l16], %[rs], %[s1] offen offset:3072 sc1\n\t" \
    "v_mov_b64 v[96:97], v[128:129]\n\t" \
    "v_mov_b64 v[98:99], v[132:133]\n\t" \
    "v_mov_b64 v[100:101], v[136:137]\n\t" \
    "v_mov_b64 v[102:103], v[140:141]\n\t" \
    "v_mov_b64 v[104:105], v[144:145]\n\t" \
    "v_mov_b64 v[106:107], v[148:149]\n\t" \
    "v_mov_b64 v[108:109], v[152:153]\n\t" \
    "v_mov_b64 v[110:111], v[156:157]\n\t" \
    "s_movk_i32 %[n], 0x200\n\t" \
    "s_nop 1\n\t" \
    "v_permlane16_swap_b32 v96, v98\n\t" \
    "v_permlane16_swap_b32 v97, v99\n\t" \
    "v_permlane16_swap_b32 v100, v102\n\t" \
    "v_permlane16_swap_b32 v101, v103\n\t" \
    "v_permlane16_swap_b32 v104, v106\n\t" \
    "v_permlane16_swap_b32 v105, v107\n\t" \
    "v_permlane16_swap_b32 v108, v110\n\t" \
    "v_permlane16_swap_b32 v109, v111\n\t" \
    "s_waitcnt vmcnt(7)\n\t" \
    "v_max_u32 v196, v200, v201\n\t" \
    "v_max3_u32 v196, v202, v203, v196\n\t" \
    "s_waitcnt vmcnt(6)\n\t" \
    "v_max3_u32 v196, v204, v205, v196\n\t" \
    "v_max3_u32 v196, v206, v207, v196\n\t" \
    "s_waitcnt vmcnt(5)\n\t" \
    "v_max3_u32 v196, v208, v209, v196\n\t" \
    "v_max3_u32 v196, v210, v211, v196\n\t" \
    "s_waitcnt vmcnt(4)\n\t" \
    "v_max3_u32 v196, v212, v213, v196\n\t" \
    "v_max3_u32 v196, v214, v215, v196\n\t" \
    "s_waitcnt vmcnt(3)\n\t" \
    "v_max3_u32 v196, v216, v217, v196\n\t" \
    "v_max3_u32 v196, v218, v219, v196\n\t" \
    "s_waitcnt vmcnt(2)\n\t" \
    "v_max3_u32 v196, v220, v221, v196\n\t" \
    "v_max3_u32 v196, v222, v223, v196\n\t" \
    "s_waitcnt vmcnt(1)\n\t" \
    "v_max3_u32 v196, v224, v225, v196\n\t" \
    "v_max3_u32 v196, v226, v227, v196\n\t" \
    "s_waitcnt vmcnt(0)\n\t" \
    "v_max3_u32 v196, v228, v229, v196\n\t" \
    "v_max3_u32 v196, v230, v231, v196\n\t" \
    "v_cmp_eq_u32 vcc, -1, v196\n\t" \
    "s_cbranch_vccz .Lxd%=\n\t" \
    ".Lxl%=:\n\t" \
    "s_mov_b32 %[a], 0\n\t" \
    "v_max_u32 v196, v200, v201\n\t" \
    "v_max3_u32 v196, v202, v203, v196\n\t" \
    "v_cmp_eq_u32 vcc, -1, v196\n\t" \
    "s_cbranch_vccz .Lxs0_%=\n\t" \
    "buffer_load_dwordx4 v[200:203], %[l16], %[rs], %[s0] offen offset:0 sc1\n\t" \
    "s_mov_b32 %[a], 1\n\t" \
    ".Lxs0_%=:\n\t" \
    "v_max_u32 v196, v204, v205\n\t" \
    "v_max3_u32 v196, v206, v207, v196\n\t" \
    "v_cmp_eq_u32 vcc, -1, v196\n\t" \
    "s_cbranch_vccz .Lxs1_%=\n\t" \
    "buffer_load_dwordx4 v[204:207], %[l16], %[rs], %[s0] offen offset:1024 sc1\n\t" \
    "s_mov_b32 %[a], 1\n\t" \
    ".Lxs1_%=:\n\t" \
    "v_max_u32 v196, v208, v209\n\t" \
    "v_max3_u32 v196, v210, v211, v196\n\t" \
    "v_cmp_eq_u32 vcc, -1, v196\n\t" \
    "s_cbranch_vccz .Lxs2_%=\n\t" \
    "buffer_load_dwordx4 v[208:211], %[l16], %[rs], %[s0] offen offset:2048 sc1\n\t" \
    "s_mov_b32 %[a], 1\n\t" \
    ".Lxs2_%=:\n\t" \
    "v_max_u32 v196, v212, v213\n\t" \
    "v_max3_u32 v196, v214, v215, v196\n\t" \
    "v_cmp_eq_u32 vcc, -1, v196\n\t" \
    "s_cbranch_vccz .Lxs3_%=\n\t" \
    "buffer_load_dwordx4 v[212:215], %[l16], %[rs], %[s0] offen offset:3072 sc1\n\t" \
    "s_mov_b32 %[a], 1\n\t" \
    ".Lxs3_%=:\n\t" \
    "v_max_u32 v196, v216, v217\n\t" \
    "v_max3_u32 v196, v218, v219, v196\n\t" \
    "v_cmp_eq_u32 vcc, -1, v196\n\t" \
    "s_cbranch_vccz .Lxs4_%=\n\t" \
    "buffer_load_dwordx4 v[216:219], %[l16], %[rs], %[s1] offen offset:0 sc1\n\t" \
    "s_mov_b32 %[a], 1\n\t" \
    ".Lxs4_%=:\n\t" \
    "v_max_u32 v196, v220, v221\n\t" \
    "v_max3_u32 v196, v222, v223, v196\n\t" \
    "v_cmp_eq_u32 vcc, -1, v196\n\t" \
    "s_cbranch_vccz .Lxs5_%=\n\t" \
    "buffer_load_dwordx4 v[220:223], %[l16], %[rs], %[s1] offen offset:1024 sc1\n\t" \
    "s_mov_b32 %[a], 1\n\t" \
    ".Lxs5_%=:\n\t" \
    "v_max_u32 v196, v224, v225\n\t" \
    "v_max3_u32 v196, v226, v227, v196\n\t" \
    "v_cmp_eq_u32 vcc, -1, v196\n\t" \
    "s_cbranch_vccz .Lxs6_%=\n\t" \
    "buffer_load_dwordx4 v[224:227], %[l16], %[rs], %[s1] offen offset:2048 sc1\n\t" \
    "s_mov_b32 %[a], 1\n\t" \
    ".Lxs6_%=:\n\t" \
    "v_max_u32 v196, v228, v229\n\t" \
    "v_max3_u32 v196, v230, v231, v196\n\t" \
    "v_cmp_eq_u32 vcc, -1, v196\n\t" \
    "s_cbranch_vccz .Lxs7_%=\n\t" \
    "buffer_load_dwordx4 v[228:231], %[l16], %[rs], %[s1] offen offset:3072 sc1\n\t" \
    "s_mov_b32 %[a], 1\n\t" \
    ".Lxs7_%=:\n\t" \
    "s_cmp_eq_u32 %[a], 0\n\t" \
    "s_cbranch_scc1 .Lxd%=\n\t" \
    "s_waitcnt vmcnt(0)\n\t" \
    "s_sub_u32 %[n], %[n], 1\n\t" \
    "s_cmp_lg_u32 %[n], 0\n\t" \
    "s_cbranch_scc1 .Lxl%=\n\t" \
    "s_branch .Lxe%=\n\t" \
    ".Lxd%=:\n\t" \
    ASM_ST2 \
    "v_mfma_f32_16x16x32_bf16 v[120:123], v[128:131], v[200:203], 0\n\t" \
    "v_mfma_f32_16x16x32_bf16 v[124:127], v[144:147], v[200:203], 0\n\t" \
    "v_mfma_f32_16x16x32_bf16 v[120:123], v[132:135], v[204:207], v[120:123]\n\t" \
    "v_mfma_f32_16x16x32_bf16 v[124:127], v[148:151], v[204:207], v[124:127]\n\t" \
    "v_mfma_f32_16x16x32_bf16 v[120:123], v[136:139], v[208:211], v[120:123]\n\t" \
    "v_mfma_f32_16x16x32_bf16 v[124:127], v[152:155], v[208:211], v[124:127]\n\t" \
    "v_mfma_f32_16x16x32_bf16 v[120:123], v[140:143], v[212:215], v[120:123]\n\t" \
    "v_mfma_f32_16x16x32_bf16 v[124:127], v[156:159], v[212:215], v[124:127]\n\t" \
    "v_mfma_f32_16x16x32_bf16 v[112:115], v[96:99], v[220:223], 0\n\t" \
    "v_mfma_f32_16x16x32_bf16 v[116:119], v[104:107], v[220:223], 0\n\t" \
    "v_mfma_f32_16x16x32_bf16 v[120:123], v[176:179], v[216:219], v[120:123]\n\t" \
    "v_mfma_f32_16x16x32_bf16 v[124:127], v[180:183], v[216:219], v[124:127]\n\t" \
    "v_mfma_f32_16x16x32_bf16 v[112:115], v[100:103], v[224:227], v[112:115]\n\t" \
    "v_mfma_f32_16x16x32_bf16 v[116:119], v[108:111], v[224:227], v[116:119]\n\t" \
    "v_mfma_f32_16x16x32_bf16 v[112:115], v[176:179], v[228:231], v[112:115]\n\t" \
    "v_mfma_f32_16x16x32_bf16 v[116:119], v[180:183], v[228:231], v[116:119]\n\t" \
    "s_nop 1\n\t" \
    "ds_write_b128 %[pw], v[120:123] offset:16384\n\t" \
    "ds_write_b128 %[pw], v[124:127] offset:24576\n\t" \
    "s_nop 3\n\t" \
    "ds_write_b128 %[pw], v[112:115] offset:0\n\t" \
    "ds_write_b128 %[pw], v[116:119] offset:8192\n\t" \
    "v_cmp_ge_u32 vcc, %[tn], v248\n\t" \
    "s_mov_b32 %[a], 0\n\t" \
    "s_cbranch_vccnz .Lxv%=\n\t" \
    "buffer_load_dwordx4 v[128:131], %[l16], %[rp], %[sv0] offen offset:0 sc1\n\t" \
    "buffer_load_dwordx4 v[132:135], %[l16], %[rp], %[sv0] offen offset:1024 sc1\n\t" \
    "buffer_load_dwordx4 v[136:139], %[l16], %[rp], %[sv0] offen offset:2048 sc1\n\t" \
    "buffer_load_dwordx4 v[140:143], %[l16], %[rp], %[sv0] offen offset:3072 sc1\n\t" \
    "buffer_load_dwordx4 v[144:147], %[l16], %[rp], %[sv1] offen offset:0 sc1\n\t" \
    "buffer_load_dwordx4 v[148:151], %[l16], %[rp], %[sv1] offen offset:1024 sc1\n\t" \
    "buffer_load_dwordx4 v[152:155], %[l16], %[rp], %[sv1] offen offset:2048 sc1\n\t" \
    "buffer_load_dwordx4 v[156:159], %[l16], %[rp], %[sv1] offen offset:3072 sc1\n\t" \
    "s_mov_b32 %[a], 1\n\t" \
    ".Lxv%=:\n\t" \
    "s_cmp_eq_u32 %[fin], 0\n\t" \
    "s_waitcnt lgkmcnt(0)\n\t" \
    "s_barrier\n\t" ASM_ST \
    "s_cbranch_scc1 .Lxe%=\n\t" \
    "ds_read_b128 v[200:203], %[pr] offset:0\n\t" \
    "ds_read_b128 v[204:207], %[pr] offset:1024\n\t" \
    "ds_read_b128 v[208:211], %[pr] offset:2048\n\t" \
    "ds_read_b128 v[212:215], %[pr] offset:3072\n\t" \
    "ds_read_b128 v[216:219], %[pr] offset:4096\n\t" \
    "ds_read_b128 v[220:223], %[pr] offset:5120\n\t" \
    "ds_read_b128 v[224:227], %[pr] offset:6144\n\t" \
    "ds_read_b128 v[228:231], %[pr] offset:7168\n\t" \
    "v_lshlrev_b32 v240, 16, v250\n\t" \
    "v_and_b32 v241, 0xffff0000, v250\n\t" \
    "v_lshlrev_b32 v242, 16, v251\n\t" \
    "v_and_b32 v243, 0xffff0000, v251\n\t" \
    "s_waitcnt lgkmcnt(6)\n\t" \
    "v_pk_add_f32 v[200:201], v[200:201], v[204:205]\n\t" \
    "v_pk_add_f32 v[202:203], v[202:203], v[206:207]\n\t" \
    "s_waitcnt lgkmcnt(4)\n\t" \
    "v_pk_add_f32 v[208:209], v[208:209], v[212:213]\n\t" \
    "v_pk_add_f32 v[210:211], v[210:211], v[214:215]\n\t" \
    "v_pk_add_f32 v[200:201], v[200:201], v[208:209]\n\t" \
    "v_pk_add_f32 v[202:203], v[202:203], v[210:211]\n\t" \
    "s_waitcnt lgkmcnt(2)\n\t" \
    "v_pk_add_f32 v[216:217], v[216:217], v[220:221]\n\t" \
    "v_pk_add_f32 v[218:219], v[218:219], v[222:223]\n\t" \
    "s_waitcnt lgkmcnt(0)\n\t" \
    "v_pk_add_f32 v[224:225], v[224:225], v[228:229]\n\t" \
    "v_pk_add_f32 v[226:227], v[226:227], v[230:231]\n\t" \
    "v_pk_add_f32 v[216:217], v[216:217], v[224:225]\n\t" \
    "v_pk_add_f32 v[218:219], v[218:219], v[226:227]\n\t" \
    "v_pk_add_f32 v[200:201], v[200:201], v[216:217]\n\t" \
    "v_pk_add_f32 v[202:203], v[202:203], v[218:219]\n\t" \
    "v_pk_add_f32 v[200:201], v[200:201], v[240:241]\n\t" \
    "v_pk_add_f32 v[202:203], v[202:203], v[242:243]\n\t" \
    "v_cvt_pk_bf16_f32 v200, v200, v201\n\t" \
    "v_cvt_pk_bf16_f32 v201, v202, v203\n\t" \
    "v_min_u32 v200, -2, v200\n\t" \
    "v_min_u32 v201, -2, v201\n\t" \
    "buffer_store_dwordx2 v[200:201], %[xo], %[rd], %[sa] offen sc1\n\t" \
    ".Lxe%=:\n\t"
#define ASM_W4_M \
    "buffer_load_dwordx4 v[184:187], %[l16], %[rs], %[s1] offen offset:2048 sc1\n\t" \
    "buffer_load_dwordx4 v[180:183], %[l16], %[rs], %[s1] offen offset:1024 sc1\n\t" \
    "buffer_load_dwordx4 v[176:179], %[l16], %[rs], %[s1] offen offset:0 sc1\n\t" \
    "buffer_load_dwordx4 v[172:175], %[l16], %[rs], %[s0] offen offset:3072 sc1\n\t" \
    "buffer_load_dwordx4 v[168:171], %[l16], %[rs], %[s0] offen offset:2048 sc1\n\t" \
    "buffer_load_dwordx4 v[164:167], %[l16], %[rs], %[s0] offen offset:1024 sc1\n\t" \
    "buffer_load_dwordx4 v[160:163], %[l16], %[rs], %[s0] offen offset:0 sc1\n\t" \
    "buffer_load_dwordx4 v[220:223], %[l16], %[rs], %[s3] offen offset:3072 sc1\n\t" \
    "v_mfma_f32_16x16x32_bf16 v[224:227], v[28:31], v[188:191], 0\n\t" \
    "v_mfma_f32_16x16x32_bf16 v[228:231], v[60:63], v[188:191], 0\n\t" \
    "v_mfma_f32_16x16x32_bf16 v[232:235], v[92:95], v[188:191], 0\n\t" \
    "v_mfma_f32_16x16x32_bf16 v[236:239], v[124:127], v[188:191], 0\n\t" \
    "v_mfma_f32_16x16x32_bf16 v[240:243], v[156:159], v[188:191], 0\n\t" \
    "s_waitcnt vmcnt(7)\n\t" \
    ".Lc06_%=:\n\t" \
    "v_max_u32 v244, v184, v185\n\t" \
    "v_max3_u32 v244, v186, v187, v244\n\t" \
    "v_cmp_eq_u32 vcc, -1, v244\n\t" \
    "s_cbranch_vccnz .Ls06_%=\n\t" \
    ".Lk06_%=:\n\t" \
    "v_mfma_f32_16x16x32_bf16 v[224:227], v[24:27], v[184:187], v[224:227]\n\t" \
    "v_mfma_f32_16x16x32_bf16 v[228:231], v[56:59], v[184:187], v[228:231]\n\t" \
    "v_mfma_f32_16x16x32_bf16 v[232:235], v[88:91], v[184:187], v[232:235]\n\t" \
    "v_mfma_f32_16x16x32_bf16 v[236:239], v[120:123], v[184:187], v[236:239]\n\t" \
    "v_mfma_f32_16x16x32_bf16 v[240:243], v[152:155], v[184:187], v[240:243]\n\t" \
    "s_waitcnt vmcnt(6)\n\t" \
    ".Lc05_%=:\n\t" \
    "v_max_u32 v244, v180, v181\n\t" \
    "v_max3_u32 v244, v182, v183, v244\n\t" \
    "v_cmp_eq_u32 vcc, -1, v244\n\t" \
    "s_cbranch_vccnz .Ls05_%=\n\t" \
    ".Lk05_%=:\n\t" \
    "v_mfma_f32_16x16x32_bf16 v[224:227], v[20:23], v[180:183], v[224:227]\n\t" \
    "v_mfma_f32_16x16x32_bf16 v[228:231], v[52:55], v[180:183], v[228:231]\n\t" \
    "v_mfma_f32_16x16x32_bf16 v[232:235], v[84:87], v[180:183], v[232:235]\n\t" \
    "v_mfma_f32_16x16x32_bf16 v[236:239], v[116:119], v[180:183], v[236:239]\n\t" \
    "v_mfma_f32_16x16x32_bf16 v[240:243], v[148:151], v[180:183], v[240:243]\n\t" \
    "s_waitcnt vmcnt(5)\n\t" \
    ".Lc04_%=:\n\t" \
    "v_max_u32 v244, v176, v177\n\t" \
    "v_max3_u32 v244, v178, v179, v244\n\t" \
    "v_cmp_eq_u32 vcc, -1, v244\n\t" \
    "s_cbranch_vccnz .Ls04_%=\n\t" \
    ".Lk04_%=:\n\t" \
    "v_mfma_f32_16x16x32_bf16 v[224:227], v[16:19], v[176:179], v[224:227]\n\t" \
    "v_mfma_f32_16x16x32_bf16 v[228:231], v[48:51], v[176:179], v[228:231]\n\t" \
    "v_mfma_f32_16x16x32_bf16 v[232:235], v[80:83], v[176:179], v[232:235]\n\t" \
    "v_mfma_f32_16x16x32_bf16 v[236:239], v[112:115], v[176:179], v[236:239]\n\t" \
    "v_mfma_f32_16x16x32_bf16 v[240:243], v[144:147], v[176:179], v[240:243]\n\t" \
    "s_waitcnt vmcnt(4)\n\t" \
    ".Lc03_%=:\n\t" \
    "v_max_u32 v244, v172, v173\n\t" \
    "v_max3_u32 v244, v174, v175, v244\n\t" \
    "v_cmp_eq_u32 vcc, -1, v244\n\t" \
    "s_cbranch_vccnz .Ls03_%=\n\t" \
    ".Lk03_%=:\n\t" \
    "v_mfma_f32_16x16x32_bf16 v[224:227], v[12:15], v[172:175], v[224:227]\n\t" \
    "v_mfma_f32_16x16x32_bf16 v[228:231], v[44:47], v[172:175], v[228:231]\n\t" \
    "v_mfma_f32_16x16x32_bf16 v[232:235], v[76:79], v[172:175], v[232:235]\n\t" \
    "v_mfma_f32_16x16x32_bf16 v[236:239], v[108:111], v[172:175], v[236:239]\n\t" \
    "v_mfma_f32_16x16x32_bf16 v[240:243], v[140:143], v[172:175], v[240:243]\n\t" \
    "s_waitcnt vmcnt(3)\n\t" \
    ".Lc02_%=:\n\t" \
    "v_max_u32 v244, v168, v169\n\t" \
    "v_max3_u32 v244, v170, v171, v244\n\t" \
    "v_cmp_eq_u32 vcc, -1, v244\n\t" \
    "s_cbranch_vccnz .Ls02_%=\n\t" \
    ".Lk02_%=:\n\t" \
    "v_mfma_f32_16x16x32_bf16 v[224:227], v[8:11], v[168:171], v[224:227]\n\t" \
    "v_mfma_f32_16x16x32_bf16 v[228:231], v[40:43], v[168:171], v[228:231]\n\t" \
    "v_mfma_f32_16x16x32_bf16 v[232:235], v[72:75], v[168:171], v[232:235]\n\t" \
    "v_mfma_f32_16x16x32_bf16 v[236:239], v[104:107], v[168:171], v[236:239]\n\t" \
    "v_mfma_f32_16x16x32_bf16 v[240:243], v[136:139], v[168:171], v[240:243]\n\t" \
    "s_waitcnt vmcnt(2)\n\t" \
    ".Lc01_%=:\n\t" \
    "v_max_u32 v244, v164, v165\n\t" \
    "v_max3_u32 v244, v166, v167, v244\n\t" \
    "v_cmp_eq_u32 vcc, -1, v244\n\t" \
    "s_cbranch_vccnz .Ls01_%=\n\t" \
    ".Lk01_%=:\n\t" \
    "v_mfma_f32_16x16x32_bf16 v[224:227], v[4:7], v[164:167], v[224:227]\n\t" \
    "v_mfma_f32_16x16x32_bf16 v[228:231], v[36:39], v[164:167], v[228:231]\n\t" \
    "v_mfma_f32_16x16x32_bf16 v[232:235], v[68:71], v[164:167], v[232:235]\n\t" \
    "v_mfma_f32_16x16x32_bf16 v[236:239], v[100:103], v[164:167], v[236:239]\n\t" \
    "v_mfma_f32_16x16x32_bf16 v[240:243], v[132:135], v[164:167], v[240:243]\n\t" \
    "s_waitcnt vmcnt(1)\n\t" \
    ".Lc00_%=:\n\t" \
    "v_max_u32 v244, v160, v161\n\t" \
    "v_max3_u32 v244, v162, v163, v244\n\t" \
    "v_cmp_eq_u32 vcc, -1, v244\n\t" \
    "s_cbranch_vccnz .Ls00_%=\n\t" \
    ".Lk00_%=:\n\t" \
    "v_mfma_f32_16x16x32_bf16 v[224:227], v[0:3], v[160:163], v[224:227]\n\t" \
    "v_mfma_f32_16x16x32_bf16 v[228:231], v[32:35], v[160:163], v[228:231]\n\t" \
    "v_mfma_f32_16x16x32_bf16 v[232:235], v[64:67], v[160:163], v[232:235]\n\t" \
    "v_mfma_f32_16x16x32_bf16 v[236:239], v[96:99], v[160:163], v[236:239]\n\t" \
    "v_mfma_f32_16x16x32_bf16 v[240:243], v[128:131], v[160:163], v[240:243]\n\t" \
    "s_waitcnt vmcnt(0)\n\t" \
    ".Lc17_%=:\n\t" \
    "v_max_u32 v244, v220, v221\n\t" \
    "v_max3_u32 v244, v222, v223, v244\n\t" \
    "v_cmp_eq_u32 vcc, -1, v244\n\t" \
    "s_cbranch_vccnz .Ls17_%=\n\t" \
    ".Lk17_%=:\n\t" \
    "buffer_load_dwordx4 v[216:219], %[l16], %[rs], %[s3] offen offset:2048 sc1\n\t" \
    "buffer_load_dwordx4 v[212:215], %[l16], %[rs], %[s3] offen offset:1024 sc1\n\t" \
    "buffer_load_dwordx4 v[208:211], %[l16], %[rs], %[s3] offen offset:0 sc1\n\t" \
    "buffer_load_dwordx4 v[204:207], %[l16], %[rs], %[s2] offen offset:3072 sc1\n\t" \
    "buffer_load_dwordx4 v[200:203], %[l16], %[rs], %[s2] offen offset:2048 sc1\n\t" \
    "buffer_load_dwordx4 v[196:199], %[l16], %[rs], %[s2] offen offset:1024 sc1\n\t" \
    "buffer_load_dwordx4 v[192:195], %[l16], %[rs], %[s2] offen offset:0 sc1\n\t" \
    "v_mfma_f32_16x16x32_bf16 v[160:163], v[28:31], v[220:223], 0\n\t" \
    "ds_write_b128 %[ld], v[224:227] offset:0\n\t" \
    "v_mfma_f32_16x16x32_bf16 v[164:167], v[60:63], v[220:223], 0\n\t" \
    "ds_write_b128 %[ld], v[228:231] offset:6144\n\t" \
    "v_mfma_f32_16x16x32_bf16 v[168:171], v[92:95], v[220:223], 0\n\t" \
    "ds_write_b128 %[ld], v[232:235] offset:12288\n\t" \
    "v_mfma_f32_16x16x32_bf16 v[172:175], v[124:127], v[220:223], 0\n\t" \
    "ds_write_b128 %[ld], v[236:239] offset:18432\n\t" \
    "v_mfma_f32_16x16x32_bf16 v[176:179], v[156:159], v[220:223], 0\n\t" \
    "ds_write_b128 %[ld], v[240:243] offset:24576\n\t" \
    "s_waitcnt vmcnt(6)\n\t" \
    ".Lc16_%=:\n\t" \
    "v_max_u32 v244, v216, v217\n\t" \
    "v_max3_u32 v244, v218, v219, v244\n\t" \
    "v_cmp_eq_u32 vcc, -1, v244\n\t" \
    "s_cbranch_vccnz .Ls16_%=\n\t" \
    ".Lk16_%=:\n\t" \
    "v_mfma_f32_16x16x32_bf16 v[160:163], v[24:27], v[216:219], v[160:163]\n\t" \
    "v_mfma_f32_16x16x32_bf16 v[164:167], v[56:59], v[216:219], v[164:167]\n\t" \
    "v_mfma_f32_16x16x32_bf16 v[168:171], v[88:91], v[216:219], v[168:171]\n\t" \
    "v_mfma_f32_16x16x32_bf16 v[172:175], v[120:123], v[216:219], v[172:175]\n\t" \
    "v_mfma_f32_16x16x32_bf16 v[176:179], v[152:155], v[216:219], v[176:179]\n\t" \
    "s_waitcnt vmcnt(5)\n\t" \
    ".Lc15_%=:\n\t" \
    "v_max_u32 v244, v212, v213\n\t" \
    "v_max3_u32 v244, v214, v215, v244\n\t" \
    "v_cmp_eq_u32 vcc, -1, v244\n\t" \
    "s_cbranch_vccnz .Ls15_%=\n\t" \
    ".Lk15_%=:\n\t" \
    "v_mfma_f32_16x16x32_bf16 v[160:163], v[20:23], v[212:215], v[160:163]\n\t" \
    "v_mfma_f32_16x16x32_bf16 v[164:167], v[52:55], v[212:215], v[164:167]\n\t" \
    "v_mfma_f32_16x16x32_bf16 v[168:171], v[84:87], v[212:215], v[168:171]\n\t" \
    "v_mfma_f32_16x16x32_bf16 v[172:175], v[116:119], v[212:215], v[172:175]\n\t" \
    "v_mfma_f32_16x16x32_bf16 v[176:179], v[148:151], v[212:215], v[176:179]\n\t" \
    "s_waitcnt vmcnt(4)\n\t" \
    ".Lc14_%=:\n\t" \
    "v_max_u32 v244, v208, v209\n\t" \
    "v_max3_u32 v244, v210, v211, v244\n\t" \
    "v_cmp_eq_u32 vcc, -1, v244\n\t" \
    "s_cbranch_vccnz .Ls14_%=\n\t" \
    ".Lk14_%=:\n\t" \
    "v_mfma_f32_16x16x32_bf16 v[160:163], v[16:19], v[208:211], v[160:163]\n\t" \
    "v_mfma_f32_16x16x32_bf16 v[164:167], v[48:51], v[208:211], v[164:167]\n\t" \
    "v_mfma_f32_16x16x32_bf16 v[168:171], v[80:83], v[208:211], v[168:171]\n\t" \
    "v_mfma_f32_16x16x32_bf16 v[172:175], v[112:115], v[208:211], v[172:175]\n\t" \
    "v_mfma_f32_16x16x32_bf16 v[176:179], v[144:147], v[208:211], v[176:179]\n\t" \
    "s_waitcnt vmcnt(3)\n\t" \
    ".Lc13_%=:\n\t" \
    "v_max_u32 v244, v204, v205\n\t" \
    "v_max3_u32 v244, v206, v207, v244\n\t" \
    "v_cmp_eq_u32 vcc, -1, v244\n\t" \
    "s_cbranch_vccnz .Ls13_%=\n\t" \
    ".Lk13_%=:\n\t" \
    "v_mfma_f32_16x16x32_bf16 v[160:163], v[12:15], v[204:207], v[160:163]\n\t" \
    "v_mfma_f32_16x16x32_bf16 v[164:167], v[44:47], v[204:207], v[164:167]\n\t" \
    "v_mfma_f32_16x16x32_bf16 v[168:171], v[76:79], v[204:207], v[168:171]\n\t" \
    "v_mfma_f32_16x16x32_bf16 v[172:175], v[108:111], v[204:207], v[172:175]\n\t" \
    "v_mfma_f32_16x16x32_bf16 v[176:179], v[140:143], v[204:207], v[176:179]\n\t" \
    "s_waitcnt vmcnt(2)\n\t" \
    ".Lc12_%=:\n\t" \
    "v_max_u32 v244, v200, v201\n\t" \
    "v_max3_u32 v244, v202, v203, v244\n\t" \
    "v_cmp_eq_u32 vcc, -1, v244\n\t" \
    "s_cbranch_vccnz .Ls12_%=\n\t" \
    ".Lk12_%=:\n\t" \
    "v_mfma_f32_16x16x32_bf16 v[160:163], v[8:11], v[200:203], v[160:163]\n\t" \
    "v_mfma_f32_16x16x32_bf16 v[164:167], v[40:43], v[200:203], v[164:167]\n\t" \
    "v_mfma_f32_16x16x32_bf16 v[168:171], v[72:75], v[200:203], v[168:171]\n\t" \
    "v_mfma_f32_16x16x32_bf16 v[172:175], v[104:107], v[200:203], v[172:175]\n\t" \
    "v_mfma_f32_16x16x32_bf16 v[176:179], v[136:139], v[200:203], v[176:179]\n\t" \
    "s_waitcnt vmcnt(1)\n\t" \
    ".Lc11_%=:\n\t" \
    "v_max_u32 v244, v196, v197\n\t" \
    "v_max3_u32 v244, v198, v199, v244\n\t" \
    "v_cmp_eq_u32 vcc, -1, v244\n\t" \
    "s_cbranch_vccnz .Ls11_%=\n\t" \
    ".Lk11_%=:\n\t" \
    "v_mfma_f32_16x16x32_bf16 v[160:163], v[4:7], v[196:199], v[160:163]\n\t" \
    "v_mfma_f32_16x16x32_bf16 v[164:167], v[36:39], v[196:199], v[164:167]\n\t" \
    "v_mfma_f32_16x16x32_bf16 v[168:171], v[68:71], v[196:199], v[168:171]\n\t" \
    "v_mfma_f32_16x16x32_bf16 v[172:175], v[100:103], v[196:199], v[172:175]\n\t" \
    "v_mfma_f32_16x16x32_bf16 v[176:179], v[132:135], v[196:199], v[176:179]\n\t" \
    "s_waitcnt vmcnt(0)\n\t" \
    ".Lc10_%=:\n\t" \
    "v_max_u32 v244, v192, v193\n\t" \
    "v_max3_u32 v244, v194, v195, v244\n\t" \
    "v_cmp_eq_u32 vcc, -1, v244\n\t" \
    "s_cbranch_vccnz .Ls10_%=\n\t" \
    ".Lk10_%=:\n\t" \
    "v_mfma_f32_16x16x32_bf16 v[160:163], v[0:3], v[192:195], v[160:163]\n\t" \
    "v_mfma_f32_16x16x32_bf16 v[164:167], v[32:35], v[192:195], v[164:167]\n\t" \
    "v_mfma_f32_16x16x32_bf16 v[168:171], v[64:67], v[192:195], v[168:171]\n\t" \
    "v_mfma_f32_16x16x32_bf16 v[172:175], v[96:99], v[192:195], v[172:175]\n\t" \
    "v_mfma_f32_16x16x32_bf16 v[176:179], v[128:131], v[192:195], v[176:179]\n\t" \
    "ds_write_b128 %[ld], v[160:163] offset:30720\n\t" \
    "ds_write_b128 %[ld], v[164:167] offset:36864\n\t" \
    "ds_write_b128 %[ld], v[168:171] offset:43008\n\t" \
    "s_nop 1\n\t" \
    "ds_write_b128 %[ld], v[172:175] offset:49152\n\t" \
    "s_nop 1\n\t" \
    "ds_write_b128 %[ld], v[176:179] offset:55296\n\t" \
    "s_branch .Lme%=\n\t" \
    ".Ls06_%=:\n\t" \
    "s_cmp_eq_u32 %[n], 0\n\t" \
    "s_cbranch_scc1 .Lk06_%=\n\t" \
    "s_sub_u32 %[n], %[n], 1\n\t" \
    "buffer_load_dwordx4 v[184:187], %[l16], %[rs], %[s1] offen offset:2048 sc1\n\t" \
    "s_waitcnt vmcnt(0)\n\t" \
    "s_branch .Lc06_%=\n\t" \
    ".Ls05_%=:\n\t" \
    "s_cmp_eq_u32 %[n], 0\n\t" \
    "s_cbranch_scc1 .Lk05_%=\n\t" \
    "s_sub_u32 %[n], %[n], 1\n\t" \
    "buffer_load_dwordx4 v[180:183], %[l16], %[rs], %[s1] offen offset:1024 sc1\n\t" \
    "s_waitcnt vmcnt(0)\n\t" \
    "s_branch .Lc05_%=\n\t" \
    ".Ls04_%=:\n\t" \
    "s_cmp_eq_u32 %[n], 0\n\t" \
    "s_cbranch_scc1 .Lk04_%=\n\t" \
    "s_sub_u32 %[n], %[n], 1\n\t" \
    "buffer_load_dwordx4 v[176:179], %[l16], %[rs], %[s1] offen offset:0 sc1\n\t" \
    "s_waitcnt vmcnt(0)\n\t" \
    "s_branch .Lc04_%=\n\t" \
    ".Ls03_%=:\n\t" \
    "s_cmp_eq_u32 %[n], 0\n\t" \
    "s_cbranch_scc1 .Lk03_%=\n\t" \
    "s_sub_u32 %[n], %[n], 1\n\t" \
    "buffer_load_dwordx4 v[172:175], %[l16], %[rs], %[s0] offen offset:3072 sc1\n\t" \
    "s_waitcnt vmcnt(0)\n\t" \
    "s_branch .Lc03_%=\n\t" \
    ".Ls02_%=:\n\t" \
    "s_cmp_eq_u32 %[n], 0\n\t" \
    "s_cbranch_scc1 .Lk02_%=\n\t" \
    "s_sub_u32 %[n], %[n], 1\n\t" \
    "buffer_load_dwordx4 v[168:171], %[l16], %[rs], %[s0] offen offset:2048 sc1\n\t" \
    "s_waitcnt vmcnt(0)\n\t" \
    "s_branch .Lc02_%=\n\t" \
    ".Ls01_%=:\n\t" \
    "s_cmp_eq_u32 %[n], 0\n\t" \
    "s_cbranch_scc1 .Lk01_%=\n\t" \
    "s_sub_u32 %[n], %[n], 1\n\t" \
    "buffer_load_dwordx4 v[164:167], %[l16], %[rs], %[s0] offen offset:1024 sc1\n\t" \
    "s_waitcnt vmcnt(0)\n\t" \
    "s_branch .Lc01_%=\n\t" \
    ".Ls00_%=:\n\t" \
    "s_cmp_eq_u32 %[n], 0\n\t" \
    "s_cbranch_scc1 .Lk00_%=\n\t" \
    "s_sub_u32 %[n], %[n], 1\n\t" \
    "buffer_load_dwordx4 v[160:163], %[l16], %[rs], %[s0] offen offset:0 sc1\n\t" \
    "s_waitcnt vmcnt(0)\n\t" \
    "s_branch .Lc00_%=\n\t" \
    ".Ls17_%=:\n\t" \
    "s_cmp_eq_u32 %[n], 0\n\t" \
    "s_cbranch_scc1 .Lk17_%=\n\t" \
    "s_sub_u32 %[n], %[n], 1\n\t" \
    "buffer_load_dwordx4 v[220:223], %[l16], %[rs], %[s3] offen offset:3072 sc1\n\t" \
    "s_waitcnt vmcnt(0)\n\t" \
    "s_branch .Lc17_%=\n\t" \
    ".Ls16_%=:\n\t" \
    "s_cmp_eq_u32 %[n], 0\n\t" \
    "s_cbranch_scc1 .Lk16_%=\n\t" \
    "s_sub_u32 %[n], %[n], 1\n\t" \
    "buffer_load_dwordx4 v[216:219], %[l16], %[rs], %[s3] offen offset:2048 sc1\n\t" \
    "s_waitcnt vmcnt(0)\n\t" \
    "s_branch .Lc16_%=\n\t" \
    ".Ls15_%=:\n\t" \
    "s_cmp_eq_u32 %[n], 0\n\t" \
    "s_cbranch_scc1 .Lk15_%=\n\t" \
    "s_sub_u32 %[n], %[n], 1\n\t" \
    "buffer_load_dwordx4 v[212:215], %[l16], %[rs], %[s3] offen offset:1024 sc1\n\t" \
    "s_waitcnt vmcnt(0)\n\t" \
    "s_branch .Lc15_%=\n\t" \
    ".Ls14_%=:\n\t" \
    "s_cmp_eq_u32 %[n], 0\n\t" \
    "s_cbranch_scc1 .Lk14_%=\n\t" \
    "s_sub_u32 %[n], %[n], 1\n\t" \
    "buffer_load_dwordx4 v[208:211], %[l16], %[rs], %[s3] offen offset:0 sc1\n\t" \
    "s_waitcnt vmcnt(0)\n\t" \
    "s_branch .Lc14_%=\n\t" \
    ".Ls13_%=:\n\t" \
    "s_cmp_eq_u32 %[n], 0\n\t" \
    "s_cbranch_scc1 .Lk13_%=\n\t" \
    "s_sub_u32 %[n], %[n], 1\n\t" \
    "buffer_load_dwordx4 v[204:207], %[l16], %[rs], %[s2] offen offset:3072 sc1\n\t" \
    "s_waitcnt vmcnt(0)\n\t" \
    "s_branch .Lc13_%=\n\t" \
    ".Ls12_%=:\n\t" \
    "s_cmp_eq_u32 %[n], 0\n\t" \
    "s_cbranch_scc1 .Lk12_%=\n\t" \
    "s_sub_u32 %[n], %[n], 1\n\t" \
    "buffer_load_dwordx4 v[200:203], %[l16], %[rs], %[s2] offen offset:2048 sc1\n\t" \
    "s_waitcnt vmcnt(0)\n\t" \
    "s_branch .Lc12_%=\n\t" \
    ".Ls11_%=:\n\t" \
    "s_cmp_eq_u32 %[n], 0\n\t" \
    "s_cbranch_scc1 .Lk11_%=\n\t" \
    "s_sub_u32 %[n], %[n], 1\n\t" \
    "buffer_load_dwordx4 v[196:199], %[l16], %[rs], %[s2] offen offset:1024 sc1\n\t" \
    "s_waitcnt vmcnt(0)\n\t" \
    "s_branch .Lc11_%=\n\t" \
    ".Ls10_%=:\n\t" \
    "s_cmp_eq_u32 %[n], 0\n\t" \
    "s_cbranch_scc1 .Lk10_%=\n\t" \
    "s_sub_u32 %[n], %[n], 1\n\t" \
    "buffer_load_dwordx4 v[192:195], %[l16], %[rs], %[s2] offen offset:0 sc1\n\t" \
    "s_waitcnt vmcnt(0)\n\t" \
    "s_branch .Lc10_%=\n\t" \
    ".Lme%=:\n\t"
#define ASM_POLL5_I \
    "buffer_load_dwordx4 v[232:235], %[l16], %[rs], %[s0] offen offset:0 sc1\n\t" \
    "buffer_load_dwordx4 v[236:239], %[l16], %[rs], %[s0] offen offset:1024 sc1\n\t" \
    "buffer_load_dwordx4 v[240:243], %[l16], %[rs], %[s0] offen offset:2048 sc1\n\t" \
    "buffer_load_dwordx4 v[244:247], %[l16], %[rs], %[s0] offen offset:3072 sc1\n\t" \
    "buffer_load_dwordx4 v[248:251], %[l16], %[rs], %[s1] offen offset:0 sc1\n\t"
#define ASM_POLL5_C \
    "s_movk_i32 %[n], 0x200\n\t" \
    "v_max_u32 v252, v232, v233\n\t" \
    "v_max3_u32 v252, v234, v235, v252\n\t" \
    "v_max3_u32 v252, v236, v237, v252\n\t" \
    "v_max3_u32 v252, v238, v239, v252\n\t" \
    "v_max3_u32 v252, v240, v241, v252\n\t" \
    "v_max3_u32 v252, v242, v243, v252\n\t" \
    "v_max3_u32 v252, v244, v245, v252\n\t" \
    "v_max3_u32 v252, v246, v247, v252\n\t" \
    "v_max3_u32 v252, v248, v249, v252\n\t" \
    "v_max3_u32 v252, v250, v251, v252\n\t" \
    "v_cmp_eq_u32 vcc, -1, v252\n\t" \
    "s_cbranch_vccz .Lhw%=\n\t" \
    ".Lfw%=:\n\t" \
    "s_mov_b32 %[a], 0\n\t" \
    "v_max_u32 v252, v232, v233\n\t" \
    "v_max3_u32 v252, v234, v235, v252\n\t" \
    "v_cmp_eq_u32 vcc, -1, v252\n\t" \
    "s_cbranch_vccz .Lgw0_%=\n\t" \
    "buffer_load_dwordx4 v[232:235], %[l16], %[rs], %[s0] offen offset:0 sc1\n\t" \
    "s_mov_b32 %[a], 1\n\t" \
    ".Lgw0_%=:\n\t" \
    "v_max_u32 v252, v236, v237\n\t" \
    "v_max3_u32 v252, v238, v239, v252\n\t" \
    "v_cmp_eq_u32 vcc, -1, v252\n\t" \
    "s_cbranch_vccz .Lgw1_%=\n\t" \
    "buffer_load_dwordx4 v[236:239], %[l16], %[rs], %[s0] offen offset:1024 sc1\n\t" \
    "s_mov_b32 %[a], 1\n\t" \
    ".Lgw1_%=:\n\t" \
    "v_max_u32 v252, v240, v241\n\t" \
    "v_max3_u32 v252, v242, v243, v252\n\t" \
    "v_cmp_eq_u32 vcc, -1, v252\n\t" \
    "s_cbranch_vccz .Lgw2_%=\n\t" \
    "buffer_load_dwordx4 v[240:243], %[l16], %[rs], %[s0] offen offset:2048 sc1\n\t" \
    "s_mov_b32 %[a], 1\n\t" \
    ".Lgw2_%=:\n\t" \
    "v_max_u32 v252, v244, v245\n\t" \
    "v_max3_u32 v252, v246, v247, v252\n\t" \
    "v_cmp_eq_u32 vcc, -1, v252\n\t" \
    "s_cbranch_vccz .Lgw3_%=\n\t" \
    "buffer_load_dwordx4 v[244:247], %[l16], %[rs], %[s0] offen offset:3072 sc1\n\t" \
    "s_mov_b32 %[a], 1\n\t" \
    ".Lgw3_%=:\n\t" \
    "v_max_u32 v252, v248, v249\n\t" \
    "v_max3_u32 v252, v250, v251, v252\n\t" \
    "v_cmp_eq_u32 vcc, -1, v252\n\t" \
    "s_cbranch_vccz .Lgw4_%=\n\t" \
    "buffer_load_dwordx4 v[248:251], %[l16], %[rs], %[s1] offen offset:0 sc1\n\t" \
    "s_mov_b32 %[a], 1\n\t" \
    ".Lgw4_%=:\n\t" \
    "s_cmp_eq_u32 %[a], 0\n\t" \
    "s_cbranch_scc1 .Lhw%=\n\t" \
    "s_waitcnt vmcnt(20)\n\t" \
    "v_max_u32 v252, v232, v233\n\t" \
    "v_max3_u32 v252, v234, v235, v252\n\t" \
    "v_max3_u32 v252, v236, v237, v252\n\t" \
    "v_max3_u32 v252, v238, v239, v252\n\t" \
    "v_max3_u32 v252, v240, v241, v252\n\t" \
    "v_max3_u32 v252, v242, v243, v252\n\t" \
    "v_max3_u32 v252, v244, v245, v252\n\t" \
    "v_max3_u32 v252, v246, v247, v252\n\t" \
    "v_max3_u32 v252, v248, v249, v252\n\t" \
    "v_max3_u32 v252, v250, v251, v252\n\t" \
    "v_cmp_eq_u32 vcc, -1, v252\n\t" \
    "s_cbranch_vccz .Lhw%=\n\t" \
    "s_sleep 1\n\t" \
    "v_max_u32 v252, v232, v233\n\t" \
    "v_max3_u32 v252, v234, v235, v252\n\t" \
    "v_max3_u32 v252, v236, v237, v252\n\t" \
    "v_max3_u32 v252, v238, v239, v252\n\t" \
    "v_max3_u32 v252, v240, v241, v252\n\t" \
    "v_max3_u32 v252, v242, v243, v252\n\t" \
    "v_max3_u32 v252, v244, v245, v252\n\t" \
    "v_max3_u32 v252, v246, v247, v252\n\t" \
    "v_max3_u32 v252, v248, v249, v252\n\t" \
    "v_max3_u32 v252, v250, v251, v252\n\t" \
    "v_cmp_eq_u32 vcc, -1, v252\n\t" \
    "s_cbranch_vccz .Lhw%=\n\t" \
    "s_sleep 1\n\t" \
    "v_max_u32 v252, v232, v233\n\t" \
    "v_max3_u32 v252, v234, v235, v252\n\t" \
    "v_max3_u32 v252, v236, v237, v252\n\t" \
    "v_max3_u32 v252, v238, v239, v252\n\t" \
    "v_max3_u32 v252, v240, v241, v252\n\t" \
    "v_max3_u32 v252, v242, v243, v252\n\t" \
    "v_max3_u32 v252, v244, v245, v252\n\t" \
    "v_max3_u32 v252, v246, v247, v252\n\t" \
    "v_max3_u32 v252, v248, v249, v252\n\t" \
    "v_max3_u32 v252, v250, v251, v252\n\t" \
    "v_cmp_eq_u32 vcc, -1, v252\n\t" \
    "s_cbranch_vccz .Lhw%=\n\t" \
    "s_sleep 1\n\t" \
    "v_max_u32 v252, v232, v233\n\t" \
    "v_max3_u32 v252, v234, v235, v252\n\t" \
    "v_max3_u32 v252, v236, v237, v252\n\t" \
    "v_max3_u32 v252, v238, v239, v252\n\t" \
    "v_max3_u32 v252, v240, v241, v252\n\t" \
    "v_max3_u32 v252, v242, v243, v252\n\t" \
    "v_max3_u32 v252, v244, v245, v252\n\t" \
    "v_max3_u32 v252, v246, v247, v252\n\t" \
    "v_max3_u32 v252, v248, v249, v252\n\t" \
    "v_max3_u32 v252, v250, v251, v252\n\t" \
    "v_cmp_eq_u32 vcc, -1, v252\n\t" \
    "s_cbranch_vccz .Lhw%=\n\t" \
    "s_sleep 1\n\t" \
    "v_max_u32 v252, v232, v233\n\t" \
    "v_max3_u32 v252, v234, v235, v252\n\t" \
    "v_max3_u32 v252, v236, v237, v252\n\t" \
    "v_max3_u32 v252, v238, v239, v252\n\t" \
    "v_max3_u32 v252, v240, v241, v252\n\t" \
    "v_max3_u32 v252, v242, v243, v252\n\t" \
    "v_max3_u32 v252, v244, v245, v252\n\t" \
    "v_max3_u32 v252, v246, v247, v252\n\t" \
    "v_max3_u32 v252, v248, v249, v252\n\t" \
    "v_max3_u32 v252, v250, v251, v252\n\t" \
    "v_cmp_eq_u32 vcc, -1, v252\n\t" \
    "s_cbranch_vccz .Lhw%=\n\t" \
    "s_sleep 1\n\t" \
    "v_max_u32 v252, v232, v233\n\t" \
    "v_max3_u32 v252, v234, v235, v252\n\t" \
    "v_max3_u32 v252, v236, v237, v252\n\t" \
    "v_max3_u32 v252, v238, v239, v252\n\t" \
    "v_max3_u32 v252, v240, v241, v252\n\t" \
    "v_max3_u32 v252, v242, v243, v252\n\t" \
    "v_max3_u32 v252, v244, v245, v252\n\t" \
    "v_max3_u32 v252, v246, v247, v252\n\t" \
    "v_max3_u32 v252, v248, v249, v252\n\t" \
    "v_max3_u32 v252, v250, v251, v252\n\t" \
    "v_cmp_eq_u32 vcc, -1, v252\n\t" \
    "s_cbranch_vccz .Lhw%=\n\t" \
    "s_sleep 1\n\t" \
    "s_sub_u32 %[n], %[n], 1\n\t" \
    "s_cmp_lg_u32 %[n], 0\n\t" \
    "s_cbranch_scc1 .Lfw%=\n\t" \
    ".Lhw%=:\n\t"
#define ASM_POLLS3 \
    "buffer_load_dwordx4 v[208:211], %[l16], %[rs], %[s0] offen offset:0 sc1\n\t" \
    "buffer_load_dwordx4 v[212:215], %[l16], %[rs], %[s1] offen offset:0 sc1\n\t" \
    "buffer_load_dwordx4 v[216:219], %[l16], %[rs], %[s2] offen offset:0 sc1\n\t" \
    "s_movk_i32 %[n], 0x200\n\t" \
    "s_waitcnt vmcnt(2)\n\t" \
    "v_max_u32 v240, v208, v209\n\t" \
    "v_max3_u32 v240, v210, v211, v240\n\t" \
    "s_waitcnt vmcnt(1)\n\t" \
    "v_max3_u32 v240, v212, v213, v240\n\t" \
    "v_max3_u32 v240, v214, v215, v240\n\t" \
    "s_waitcnt vmcnt(0)\n\t" \
    "v_max3_u32 v240, v216, v217, v240\n\t" \
    "v_max3_u32 v240, v218, v219, v240\n\t" \
    "v_cmp_eq_u32 vcc, -1, v240\n\t" \
    "s_cbranch_vccz .Lpd%=\n\t" \
    ".Lpl%=:\n\t" \
    "s_mov_b32 %[a], 0\n\t" \
    "v_max_u32 v240, v208, v209\n\t" \
    "v_max3_u32 v240, v210, v211, v240\n\t" \
    "v_cmp_eq_u32 vcc, -1, v240\n\t" \
    "s_cbranch_vccz .Lps0_%=\n\t" \
    "buffer_load_dwordx4 v[208:211], %[l16], %[rs], %[s0] offen offset:0 sc1\n\t" \
    "s_mov_b32 %[a], 1\n\t" \
    ".Lps0_%=:\n\t" \
    "v_max_u32 v240, v212, v213\n\t" \
    "v_max3_u32 v240, v214, v215, v240\n\t" \
    "v_cmp_eq_u32 vcc, -1, v240\n\t" \
    "s_cbranch_vccz .Lps1_%=\n\t" \
    "buffer_load_dwordx4 v[212:215], %[l16], %[rs], %[s1] offen offset:0 sc1\n\t" \
    "s_mov_b32 %[a], 1\n\t" \
    ".Lps1_%=:\n\t" \
    "v_max_u32 v240, v216, v217\n\t" \
    "v_max3_u32 v240, v218, v219, v240\n\t" \
    "v_cmp_eq_u32 vcc, -1, v240\n\t" \
    "s_cbranch_vccz .Lps2_%=\n\t" \
    "buffer_load_dwordx4 v[216:219], %[l16], %[rs], %[s2] offen offset:0 sc1\n\t" \
    "s_mov_b32 %[a], 1\n\t" \
    ".Lps2_%=:\n\t" \
    "s_cmp_eq_u32 %[a], 0\n\t" \
    "s_cbranch_scc1 .Lpd%=\n\t" \
    "s_waitcnt vmcnt(0)\n\t" \
    "s_sub_u32 %[n], %[n], 1\n\t" \
    "s_cmp_lg_u32 %[n], 0\n\t" \
    "s_cbranch_scc1 .Lpl%=\n\t" \
    ".Lpd%=:\n\t"
#define ASM_POLLS2 \
    "buffer_load_dwordx4 v[208:211], %[l16], %[rs], %[s0] offen offset:0 sc1\n\t" \
    "buffer_load_dwordx4 v[212:215], %[l16], %[rs], %[s1] offen offset:0 sc1\n\t" \
    "s_movk_i32 %[n], 0x200\n\t" \
    "s_waitcnt vmcnt(1)\n\t" \
    "v_max_u32 v240, v208, v209\n\t" \
    "v_max3_u32 v240, v210, v211, v240\n\t" \
    "s_waitcnt vmcnt(0)\n\t" \
    "v_max3_u32 v240, v212, v213, v240\n\t" \
    "v_max3_u32 v240, v214, v215, v240\n\t" \
    "v_cmp_eq_u32 vcc, -1, v240\n\t" \
    "s_cbranch_vccz .Lpd%=\n\t" \
    ".Lpl%=:\n\t" \
    "s_mov_b32 %[a], 0\n\t" \
    "v_max_u32 v240, v208, v209\n\t" \
    "v_max3_u32 v240, v210, v211, v240\n\t" \
    "v_cmp_eq_u32 vcc, -1, v240\n\t" \
    "s_cbranch_vccz .Lps0_%=\n\t" \
    "buffer_load_dwordx4 v[208:211], %[l16], %[rs], %[s0] offen offset:0 sc1\n\t" \
    "s_mov_b32 %[a], 1\n\t" \
    ".Lps0_%=:\n\t" \
    "v_max_u32 v240, v212, v213\n\t" \
    "v_max3_u32 v240, v214, v215, v240\n\t" \
    "v_cmp_eq_u32 vcc, -1, v240\n\t" \
    "s_cbranch_vccz .Lps1_%=\n\t" \
    "buffer_load_dwordx4 v[212:215], %[l16], %[rs], %[s1] offen offset:0 sc1\n\t" \
    "s_mov_b32 %[a], 1\n\t" \
    ".Lps1_%=:\n\t" \
    "s_cmp_eq_u32 %[a], 0\n\t" \
    "s_cbranch_scc1 .Lpd%=\n\t" \
    "s_waitcnt vmcnt(0)\n\t" \
    "s_sub_u32 %[n], %[n], 1\n\t" \
    "s_cmp_lg_u32 %[n], 0\n\t" \
    "s_cbranch_scc1 .Lpl%=\n\t" \
    ".Lpd%=:\n\t"
#define ASM_POLLV3 \
    "buffer_load_dwordx4 v[200:203], %[l16], %[rs], %[s0] offen offset:0 sc1\n\t" \
    "buffer_load_dwordx4 v[204:207], %[l16], %[rs], %[s1] offen offset:0 sc1\n\t" \
    "buffer_load_dwordx4 v[208:211], %[l16], %[rs], %[s2] offen offset:0 sc1\n\t" \
    "s_movk_i32 %[n], 0x200\n\t" \
    "s_waitcnt vmcnt(2)\n\t" \
    "v_max_u32 v252, v200, v201\n\t" \
    "v_max3_u32 v252, v202, v203, v252\n\t" \
    "s_waitcnt vmcnt(1)\n\t" \
    "v_max3_u32 v252, v204, v205, v252\n\t" \
    "v_max3_u32 v252, v206, v207, v252\n\t" \
    "s_waitcnt vmcnt(0)\n\t" \
    "v_max3_u32 v252, v208, v209, v252\n\t" \
    "v_max3_u32 v252, v210, v211, v252\n\t" \
    "v_cmp_eq_u32 vcc, -1, v252\n\t" \
    "s_cbranch_vccz .Lpd%=\n\t" \
    ".Lpl%=:\n\t" \
    "s_mov_b32 %[a], 0\n\t" \
    "v_max_u32 v252, v200, v201\n\t" \
    "v_max3_u32 v252, v202, v203, v252\n\t" \
    "v_cmp_eq_u32 vcc, -1, v252\n\t" \
    "s_cbranch_vccz .Lps0_%=\n\t" \
    "buffer_load_dwordx4 v[200:203], %[l16], %[rs], %[s0] offen offset:0 sc1\n\t" \
    "s_mov_b32 %[a], 1\n\t" \
    ".Lps0_%=:\n\t" \
    "v_max_u32 v252, v204, v205\n\t" \
    "v_max3_u32 v252, v206, v207, v252\n\t" \
    "v_cmp_eq_u32 vcc, -1, v252\n\t" \
    "s_cbranch_vccz .Lps1_%=\n\t" \
    "buffer_load_dwordx4 v[204:207], %[l16], %[rs], %[s1] offen offset:0 sc1\n\t" \
    "s_mov_b32 %[a], 1\n\t" \
    ".Lps1_%=:\n\t" \
    "v_max_u32 v252, v208, v209\n\t" \
    "v_max3_u32 v252, v210, v211, v252\n\t" \
    "v_cmp_eq_u32 vcc, -1, v252\n\t" \
    "s_cbranch_vccz .Lps2_%=\n\t" \
    "buffer_load_dwordx4 v[208:211], %[l16], %[rs], %[s2] offen offset:0 sc1\n\t" \
    "s_mov_b32 %[a], 1\n\t" \
    ".Lps2_%=:\n\t" \
    "s_cmp_eq_u32 %[a], 0\n\t" \
    "s_cbranch_scc1 .Lpd%=\n\t" \
    "s_waitcnt vmcnt(0)\n\t" \
    "s_sub_u32 %[n], %[n], 1\n\t" \
    "s_cmp_lg_u32 %[n], 0\n\t" \
    "s_cbranch_scc1 .Lpl%=\n\t" \
    ".Lpd%=:\n\t"
#define ASM_POLLV2 \
    "buffer_load_dwordx4 v[200:203], %[l16], %[rs], %[s0] offen offset:0 sc1\n\t" \
    "buffer_load_dwordx4 v[204:207], %[l16], %[rs], %[s1] offen offset:0 sc1\n\t" \
    "s_movk_i32 %[n], 0x200\n\t" \
    "s_waitcnt vmcnt(1)\n\t" \
    "v_max_u32 v252, v200, v201\n\t" \
    "v_max3_u32 v252, v202, v203, v252\n\t" \
    "s_waitcnt vmcnt(0)\n\t" \
    "v_max3_u32 v252, v204, v205, v252\n\t" \
    "v_max3_u32 v252, v206, v207, v252\n\t" \
    "v_cmp_eq_u32 vcc, -1, v252\n\t" \
    "s_cbranch_vccz .Lpd%=\n\t" \
    ".Lpl%=:\n\t" \
    "s_mov_b32 %[a], 0\n\t" \
    "v_max_u32 v252, v200, v201\n\t" \
    "v_max3_u32 v252, v202, v203, v252\n\t" \
    "v_cmp_eq_u32 vcc, -1, v252\n\t" \
    "s_cbranch_vccz .Lps0_%=\n\t" \
    "buffer_load_dwordx4 v[200:203], %[l16], %[rs], %[s0] offen offset:0 sc1\n\t" \
    "s_mov_b32 %[a], 1\n\t" \
    ".Lps0_%=:\n\t" \
    "v_max_u32 v252, v204, v205\n\t" \
    "v_max3_u32 v252, v206, v207, v252\n\t" \
    "v_cmp_eq_u32 vcc, -1, v252\n\t" \
    "s_cbranch_vccz .Lps1_%=\n\t" \
    "buffer_load_dwordx4 v[204:207], %[l16], %[rs], %[s1] offen offset:0 sc1\n\t" \
    "s_mov_b32 %[a], 1\n\t" \
    ".Lps1_%=:\n\t" \
    "s_cmp_eq_u32 %[a], 0\n\t" \
    "s_cbranch_scc1 .Lpd%=\n\t" \
    "s_waitcnt vmcnt(0)\n\t" \
    "s_sub_u32 %[n], %[n], 1\n\t" \
    "s_cmp_lg_u32 %[n], 0\n\t" \
    "s_cbranch_scc1 .Lpl%=\n\t" \
    ".Lpd%=:\n\t"
__device__ __forceinline__ void poll5_issue(rsrc_t rs, unsigned l16, unsigned base, unsigned base1, u32x4 (&v)[5]) {
    asm volatile(ASM_POLL5_I : "+{v[232:235]}"(v[0]), "+{v[236:239]}"(v[1]), "+{v[240:243]}"(v[2]), "+{v[244:247]}"(v[3]), "+{v[248:251]}"(v[4]) : [l16] "v"(l16), [rs] "s"(rs), [s0] "s"(base), [s1] "s"(base1) : "memory");
}
__device__ __forceinline__ void poll5(rsrc_t rs, unsigned l16, unsigned base, unsigned base1, u32x4 (&v)[5], const Params& P) {
#pragma unroll 1
    for (int spins = 1023;; spins += 1024) {
        unsigned n, a;
        asm volatile(ASM_POLL5_C : "+{v[232:235]}"(v[0]), "+{v[236:239]}"(v[1]), "+{v[240:243]}"(v[2]), "+{v[244:247]}"(v[3]), "+{v[248:251]}"(v[4]), [n] "=&s"(n), [a] "=&s"(a) : [l16] "v"(l16), [rs] "s"(rs), [s0] "s"(base), [s1] "s"(base1) : "memory", "vcc", "scc", "v252");
        if (n || spin_fail(spins, P)) break;
    }
}
using u32x8 = __attribute__((ext_vector_type(8))) unsigned;
using u32x16 = __attribute__((ext_vector_type(16))) unsigned;
using u32x32 = __attribute__((ext_vector_type(32))) unsigned;
struct WRegs { u32x32 w[6]; u32x8 t; };
#define WREGS(W) "{v[0:31]}"(W.w[0]), "{v[32:63]}"(W.w[1]), "{v[64:95]}"(W.w[2]), "{v[96:127]}"(W.w[3]), "{v[128:159]}"(W.w[4]), "{v[160:191]}"(W.w[5]), "{v[192:199]}"(W.t)
__device__ __forceinline__ u32x32 cat32(u32x8 a, u32x8 b, u32x8 c, u32x8 d) {
    const u32x16 lo = __builtin_shufflevector(a, b, 0, 1, 2, 3, 4, 5, 6, 7, 8, 9, 10, 11, 12, 13, 14, 15);
    const u32x16 hi = __builtin_shufflevector(c, d, 0, 1, 2, 3, 4, 5, 6, 7, 8, 9, 10, 11, 12, 13, 14, 15);
    return __builtin_shufflevector(lo, hi, 0, 1, 2, 3, 4, 5, 6, 7, 8, 9, 10, 11, 12, 13, 14, 15, 16, 17, 18, 19, 20, 21, 22, 23, 24, 25, 26, 27, 28, 29, 30, 31);
}
#define WOUT(W) "=&{v[0:31]}"(W.w[0]), "=&{v[32:63]}"(W.w[1]), "=&{v[64:95]}"(W.w[2]), "=&{v[96:127]}"(W.w[3]), "=&{v[128:159]}"(W.w[4]), "=&{v[160:191]}"(W.w[5]), "=&{v[192:199]}"(W.t)
#define WTMP "memory", "scc", "v200", "v201", "v202", "v203", "v204", "v205", "v206", "v207", "v208", "v209", "v210", "v211", "v212", "v213", "v214", "v215", "v216", "v217", "v218", "v219", "v220", "v221", "v222", "v223", "v224", "v225", "v226", "v227", "v228", "v229", "v230", "v231", "v232", "v233", "v234", "v235", "v236", "v237", "v238", "v239"
__device__ __forceinline__ void load_w3(WRegs& W, const bf16_t* m, int wave, unsigned c0, unsigned ls) {
    const int ln = lane_id();
    unsigned o[3], s[3], t;
#pragma unroll
    for (int k = 0; k < 3; ++k) {
        const int row = (k * 64 + ln) / 24, sg = (k * 64 + ln) % 24;
        o[k] = row * (HD * 2) + (c0 + (sg >> 3) * ls) % (HD * 2) + (sg & 7) * 16; s[k] = wave * 13824 + row * 416 + sg * 16;
    }
    const unsigned tr = (ln >> 4) * 3456 + ((ln >> 2) & 3) * 416 + (wave & 3) * 32 + (ln & 3) * 8;
    asm volatile(ASM_WLOAD3 : WOUT(W), [t] "=&s"(t) : [rs] "s"(mkrsrc(m, DM * HD * 2)), [wx] "s"(wave), [o0] "v"(o[0]), [o1] "v"(o[1]), [o2] "v"(o[2]), [s0] "v"(s[0]), [s1] "v"(s[1]), [s2] "v"(s[2]),
                 [t0] "v"(tr + wave * 13824), [t1] "v"(tr + (wave ^ 1) * 13824), [t2] "v"(tr + (wave ^ 2) * 13824), [t3] "v"(tr + (wave ^ 3) * 13824), [t4] "v"(tr + (wave & 4) * 13824),
                 [wl] "v"(L_W + wave * 10240 + ln * 16) : WTMP);
}
__device__ __forceinline__ void load_wo(WRegs& W, const bf16_t* m, int wave, int h) {
    const int ln = lane_id();
    unsigned o[5], s[5], t;
#pragma unroll
    for (int k = 0; k < 5; ++k) {
        const int row = (k * 64 + ln) / 20, sg = (k * 64 + ln) % 20;
        o[k] = (h * DH + row) * (DM * 2) + sg * 16 + (wave & 3) * 320; s[k] = wave * 13824 + row * 352 + sg * 16;
    }
    asm volatile(ASM_WLOADO : WOUT(W), [t] "=&s"(t) : [rs] "s"(mkrsrc(m, DM * HD * 2)), [o0] "v"(o[0]), [o1] "v"(o[1]), [o2] "v"(o[2]), [o3] "v"(o[3]), [o4] "v"(o[4]),
                 [s0] "v"(s[0]), [s1] "v"(s[1]), [s2] "v"(s[2]), [s3] "v"(s[3]), [s4] "v"(s[4]), [tr] "v"(wave * 13824 + (ln >> 2) * 352 + (ln & 3) * 8), [wl] "v"(L_W + wave * 10240 + ln * 16) : WTMP);
}
using s16x4 = __attribute__((ext_vector_type(4))) short;
template <int NT, int KS, int N>
__device__ __forceinline__ void load_wn(u32x4 (&f)[NT * KS], const bf16_t* m, int rows, int wave, int c0, int k0) {
    const int ln = lane_id();
    const rsrc_t rs = mkrsrc(m, rows * N * 2);
#pragma unroll
    for (int k = 0; k < KS; ++k)
#pragma unroll
        for (int n = 0; n < NT; ++n) f[n * KS + k] = __builtin_amdgcn_raw_buffer_load_b128(rs, ((n * 64 + ln) / (2 * NT)) * (N * 2) + ((n * 64 + ln) % (2 * NT)) * 16, (k0 + k) * (N * 64) + c0 * 32, 0);
    const unsigned st = wave * 10240 + ln * 16, tr = wave * 10240 + ((ln >> 4) * 8 + ((ln >> 2) & 3)) * (NT * 32) + (ln & 3) * 8;
#pragma unroll
    for (int k = 0; k < KS; ++k) {
#pragma unroll
        for (int n = 0; n < NT; ++n) lds_st<u32x4>(st + n * 1024, f[n * KS + k]);
        asm volatile("" ::: "memory");
#pragma unroll
        for (int n = 0; n < NT; ++n) {
            const u32x2 a = __builtin_bit_cast(u32x2, __builtin_amdgcn_ds_read_tr16_b64_v4i16((LAS s16x4*)(tr + n * 32)));
            const u32x2 b = __builtin_bit_cast(u32x2, __builtin_amdgcn_ds_read_tr16_b64_v4i16((LAS s16x4*)(tr + n * 32 + NT * 128)));
            f[n * KS + k] = (u32x4){a[0], a[1], b[0], b[1]};
        }
        asm volatile("" ::: "memory");
    }
}
struct C1Regs { u32x8 lw0, lw1, z1; u32x4 z0; };
#define XG_OUT "=&{v[200:207]}"(C.lw0), "=&{v[224:227]}"(C.z0), "=&{v[228:235]}"(C.lw1), "=&{v[244:251]}"(C.z1)
#define XG_IN WREGS(W), [xa] "v"(xa), [wl] "v"(wl), [qs] "s"(qs)
#define XG_CLOB "memory", "scc", "v208", "v209", "v210", "v211", "v212", "v213", "v214", "v215", "v216", "v217", "v218", "v219", "v220", "v221", "v222", "v223", "v236", "v237", "v238", "v239", "v240", "v241", "v242", "v243", "v252"
__device__ __forceinline__ void xn_wait(const Params& P, rsrc_t rr, int seg, int cpy) {
    for (int spins = 0;; ++spins) {
        const unsigned f = __builtin_amdgcn_raw_buffer_load_b32(rr, (lane_id() & 15) * 128, RG_FX + (cpy & 3) * 2048, 16);
        if (__all(f > (unsigned)seg)) break;
        __builtin_amdgcn_s_sleep(4);
        if (spin_fail(spins, P)) break;
    }
    asm volatile("" ::: "memory");
}
__device__ __forceinline__ unsigned c1_dst(int wave, bool ktype) {
    const int ln = lane_id(), l15 = ln & 15, hf = (wave >> 1) & 1;
    const int rb16 = ((ln >> 2) & 4) * 16 + (ln >> 5) * 32;
    return (ktype ? L_KM + (l15 >> 2) * 128 + hf * 8 + (l15 & 3) * 2
                  : L_Q + (l15 >> 3) * 256 + ((l15 >> 1) & 1) * 128 + ((l15 >> 2) & 1) * 8 + hf * 4 + (l15 & 1) * 2) + (wave & 1) * 1536 + rb16;
}
template <bool VR>
__device__ __forceinline__ void mem_stage(const Params& P, rsrc_t rr, unsigned sbase, int w, unsigned abase, u32x4 (&v)[3]) {
#define PM_IN [l16] "v"(abase), [rs] "s"(rr), [s0] "s"(sbase), [s1] "s"(sbase + 4096)
#pragma unroll 1
    for (int spins = 1023;; spins += 1024) {
        unsigned n, a;
        if (w < 2) {
            if (VR) asm volatile(ASM_POLLV3 : "=&{v[200:203]}"(v[0]), "=&{v[204:207]}"(v[1]), "=&{v[208:211]}"(v[2]), [n] "=&s"(n), [a] "=&s"(a) : PM_IN, [s2] "s"(sbase + BLK) : "memory", "vcc", "scc", "v252");
            else asm volatile(ASM_POLLS3 : "=&{v[208:211]}"(v[0]), "=&{v[212:215]}"(v[1]), "=&{v[216:219]}"(v[2]), [n] "=&s"(n), [a] "=&s"(a) : PM_IN, [s2] "s"(sbase + BLK) : "memory", "vcc", "scc", "v240");
            lds_st<u32x4>(abase + 8192, v[2]);
        } else {
            if (VR) asm volatile(ASM_POLLV2 : "=&{v[200:203]}"(v[0]), "=&{v[204:207]}"(v[1]), [n] "=&s"(n), [a] "=&s"(a) : PM_IN : "memory", "vcc", "scc", "v252");
            else asm volatile(ASM_POLLS2 : "=&{v[208:211]}"(v[0]), "=&{v[212:215]}"(v[1]), [n] "=&s"(n), [a] "=&s"(a) : PM_IN : "memory", "vcc", "scc", "v240");
        }
        lds_st<u32x4>(abase, v[0]);
        lds_st<u32x4>(abase + 4096, v[1]);
        if (__builtin_amdgcn_readfirstlane(n) || spin_fail(spins, P)) break;
    }
}
template <bool POLL>
__device__ __forceinline__ void crit1(const Params& P, const WRegs& W, C1Regs& C, rsrc_t rr, unsigned sbase, int wave, unsigned wl, unsigned dsto
) {
    unsigned abase;
    asm volatile("v_and_b32 %0, 0x3ff, %1\n\tv_or_b32 %0, %2, %0" : "=&v"(abase) : "v"(wl), "s"(wave * 1024));
    ASM_ST_DECL;
#define C1_OUT "+{v[224:227]}"(C.z0), "+{v[244:251]}"(C.z1), "+{v[200:207]}"(C.lw0), "+{v[228:235]}"(C.lw1)
#define C1_IN WREGS(W), [ab] "v"(abase), [wl] "v"(wl), [dst] "v"(dsto)
    if constexpr (POLL) {
        asm volatile("" :: "v"(dsto));
        SB();
        ST(1);
        u32x4 v[3];
        mem_stage<false>(P, rr, sbase, wave, abase, v);
        ST(2);
        asm volatile(ASM_CRIT1 : C1_OUT, "+{v[208:211]}"(v[0]) ASM_ST_OUT : C1_IN : "memory", "v212", "v213", "v214", "v215", "v216", "v217", "v218", "v219", "v220", "v221", "v222", "v223", "v236", "v237", "v238", "v239", "v240", "v241");
    } else {
        asm volatile(ASM_CRIT1_N : C1_OUT ASM_ST_OUT : C1_IN : "memory", "v208", "v209", "v210", "v211", "v212", "v213", "v214", "v215", "v216", "v217", "v218", "v219", "v220", "v221", "v222", "v223", "v236", "v237", "v238", "v239", "v240", "v241");
    }
    SB();
    ASM_ST_SAVE(3);
}
#define STARG

#define SRG 5
__device__ __forceinline__ void s_role(const Params& P, int wave, int L, int par, int h, int cpy) {
    const rsrc_t rr = mkrsrc(P.ws + par * 8192, RG_L);
    const char* xn = P.ws + PG0 + PG_XN + L * PG_LX;
    auto xdma = [&](int seg, int hb) {
        const unsigned l16 = lane_id() * 16;
        const int mt = (wave >> 1) & 1;
        const char* g = xn + pgb(seg, par);
        if (hb == 0) {
            dma4(g + blin(mt * 20480 + (wave & 1) * 4096), l16, L_XA + mt * 16384 + (wave & 1) * 4096);
            dmaq<0>(g + blin(mt * 20480 + 16384) + (wave & 1) * 1024, l16, L_XB + mt * 4096 + (wave & 1) * 1024);
        } else {
            dma4(g + blin(mt * 20480 + 8192), l16, L_XA + mt * 16384 + 8192); dma4(g + blin(mt * 20480 + 12288), l16, L_XA + mt * 16384 + 12288);
            dmaq<2>(g + blin(mt * 20480 + 16384), l16, L_XB + mt * 4096); dmaq<3>(g + blin(mt * 20480 + 16384), l16, L_XB + mt * 4096);
        }
    };
    const float SCL = 0.07216878364870322f * 1.4426950408889634f;
    auto run = [&](auto Cc) {
    constexpr int CLS = decltype(Cc)::value;
    WRegs wf;
    load_w3(wf, P.w[CLS == 3], wave, h * 384, 128);
    if (CLS == 3) {
        if (L) xn_wait(P, rr, 0, cpy);
        xdma(0, 0);
        if (wave == 4 || wave == 7) xdma(0, 1);
        wait_vm<0>();
    }
    bar();
    if (CLS == 3 && wave == 7 && L) xn_wait(P, rr, 1, cpy);
    const unsigned qs = (CLS == 3 ? L_KX : L_QX) + (wave & 1) * 3072 + ((wave >> 1) & 1) * 8;
    unsigned xa, wl, dsto;
    if (CLS == 3) { xa = L_XA + wave * 2048 + lane_id() * 16; wl = L_W + wave * 10240 + lane_id() * 16; dsto = c1_dst(wave, true); }
    STDECL;
    for (int t = 0; t < NSEG; ++t) {
        const unsigned slot = t & (NSLOT - 1);
        C1Regs C;
        {
            const WRegs& W = wf;
            ASM_ST_DECL;
            if constexpr (CLS == 3) {
                const int mt = (wave >> 1) & 1;
                const char* g = xn + pgb(t + 1 < NSEG ? t + 1 : t, par);
                asm volatile(ASM_XG_S_HI : XG_OUT ASM_ST_OUT : XG_IN, [slp] "s"(wave == 4 ? t : 0), [g0] "s"(g + blin(mt * 20480 + (wave & 1) * 4096)), [d0] "s"(L_XA + mt * 16384 + (wave & 1) * 4096),
                             [g1] "s"(g + blin(mt * 20480 + 16384) + (wave & 1) * 1024), [d1] "s"(L_XB + mt * 4096 + (wave & 1) * 1024) : XG_CLOB);
                wait_vm<0>();
            } else {
                const unsigned l16 = lane_id() * 16;
                xa = L_XA + wave * 2048 + l16; wl = L_W + wave * 10240 + l16;
                asm volatile(ASM_XG_S_LO2 : XG_OUT ASM_ST_OUT : XG_IN : XG_CLOB);
                if constexpr (CLS == 4 || CLS == 2) {
                    const int ln = lane_id(), u = h / 6, f = ((h % 6) / 3) * 12 + (CLS == 2) * 3 + h % 3;
                    for (int spins = 0;; ++spins) {
                        const unsigned fl = __builtin_amdgcn_raw_buffer_load_b32(rr, (ln & 3) * 128, RG_FH + BLK + u * 512, 16);
                        if (__all(fl > (unsigned)t)) break;
                        __builtin_amdgcn_s_sleep(2);
                        if (spin_fail(spins, P)) break;
                    }
                    asm volatile("" ::: "memory");
                    const char* g = P.ws + PG0 + pgb(t, par) + PG_HV + (16 + u) * 3 * BLK;
                    dmaq<0>(g + (f >> 3) * BLK + (f & 7) * 1024, ln * 16, L_QX + (CLS == 2) * 3072);
                    dmaq<0>(g + ((f + 6) >> 3) * BLK + ((f + 6) & 7) * 1024, ln * 16, L_QX + (CLS == 2) * 3072 + 6144);
                }
                dsto = c1_dst(wave, false);
            }
            ASM_ST_SAVE(7);
            ST(6);
        }
        ST(0);
        crit1<CLS != 3>(P, wf, C, rr, RG_XM + slot * XM_SLOT + cpy * XM_COPY, wave, wl, dsto STARG);
        if constexpr (CLS == 0) {
            int ln = lane_id();
            const int sidx = ((ln & 15) < 8) ? 0x4444 : 0xEEEE;
            const unsigned l16 = ln * 16;
            bf16x16 F0, F1, F2, F3;
            F0 = lds_frag(L_KX + l16, 1024); F1 = lds_frag(L_KX + 6144 + l16, 1024); F2 = lds_frag(L_KX + 2048 + l16, 1024);
            f32x4 sc[3];
#pragma unroll
            for (int j = 0; j < 3; ++j) sc[j] = (f32x4){0.f, 0.f, 0.f, 0.f};
            unsigned kmo = L_KM + (ln >> 4) * 128 + (ln & 7) * 16;
            asm volatile("s_waitcnt lgkmcnt(0)" : "+v"(kmo), "+{v[244:247]}"(sc[0]), "+{v[252:255]}"(sc[1]), "+{v[240:243]}"(sc[2]),
                         "+v"(F0), "+v"(F1), "+v"(F2) :: "memory");
            SB();
            __builtin_amdgcn_s_barrier();
            asm volatile("" ::: "memory");
            __builtin_amdgcn_s_setprio(3);
            bf16x8 qa0 = lds_ld<bf16x8>(L_Q + l16);
            bf16x8 qa1 = lds_ld<bf16x8>(L_Q + 1024 + l16);
            F3 = lds_frag(kmo, 512);
            SB();
            SMF(qa0, F0, sc[0]);
            SB();
            F0 = lds_frag(kmo + 1024, 512);
            SMF(qa0, F1, sc[1]);
            SB();
            F1 = lds_frag(L_KX + 6144 + 2048 + l16, 1024);
            SMF(qa1, F2, sc[0]);
            SB();
            F2 = lds_frag(L_KX + 4096 + l16, 1024);
            SMF(qa0, F3, sc[2]);
            SB();
            qa0 = lds_ld<bf16x8>(L_Q + 2048 + l16);
            F3 = lds_frag(L_KX + 6144 + 4096 + l16, 1024);
            SMF(qa1, F0, sc[2]);
            SB();
            F0 = lds_frag(kmo + 2048, 512);
            SMF(qa1, F1, sc[1]);
            SB();
            ln = lane_id();
            const int l15 = ln & 15;
            const int rb16 = ((ln >> 2) & 4) * 16 + (ln >> 5) * 32;
            unsigned pb = L_Q + rb16 + (l15 & 7) * 2;
            unsigned pbx = L_Q + 128 + (l15 >> 3) * 256 + ((l15 >> 1) & 1) * 128 + rb16 + ((l15 >> 2) & 1) * 8 + (l15 & 1) * 2;
            unsigned poff = (l16 + ((ln < 8) ? h * 128 : BLK - 128 + (h >> 4) * BLK + (h & 15) * 512)) | ((ln >= 40) ? 0x40000000u : 0u);
            asm volatile("" : "+v"(pb), "+v"(pbx), "+v"(poff));
            SB();
            SMF(qa0, F2, sc[0]);
            SB();
            SMF(qa0, F3, sc[1]);
            SB();
            SMF(qa0, F0, sc[2]);
            SB();
            f32x4 mz;
            asm volatile(
                "s_nop 5\n\t"
                "v_permlane32_swap_b32 v244, v246\n\t"
                "v_permlane32_swap_b32 v245, v247\n\t"
                "v_permlane32_swap_b32 v252, v254\n\t"
                "v_permlane32_swap_b32 v253, v255\n\t"
                "v_permlane32_swap_b32 v240, v242\n\t"
                "v_permlane32_swap_b32 v241, v243\n\t"
                "v_pk_add_f32 v[244:245], v[244:245], v[246:247]\n\t"
                "v_pk_add_f32 v[246:247], v[252:253], v[254:255]\n\t"
                "v_pk_add_f32 v[240:241], v[240:241], v[242:243]\n\t"
                "s_nop 0\n\t"
                "v_max3_f32 v248, v244, v246, v240\n\t"
                "v_max3_f32 v249, v245, v247, v241\n\t"
                "s_nop 0\n\t"
                "v_max_f32_dpp v248, v248, v248 quad_perm:[1,0,3,2] row_mask:0xf bank_mask:0xf\n\t"
                "v_max_f32_dpp v249, v249, v249 quad_perm:[1,0,3,2] row_mask:0xf bank_mask:0xf\n\t"
                "s_nop 0\n\t"
                "v_max_f32_dpp v248, v248, v248 quad_perm:[2,3,0,1] row_mask:0xf bank_mask:0xf\n\t"
                "v_max_f32_dpp v249, v249, v249 quad_perm:[2,3,0,1] row_mask:0xf bank_mask:0xf\n\t"
                "s_nop 0\n\t"
                "v_max_f32_dpp v248, v248, v248 row_half_mirror row_mask:0xf bank_mask:0xf\n\t"
                "v_max_f32_dpp v249, v249, v249 row_half_mirror row_mask:0xf bank_mask:0xf\n\t"
                "s_nop 0\n\t"
                "v_max_f32_dpp v248, v248, v248 row_mirror row_mask:0xf bank_mask:0xf\n\t"
                "v_max_f32_dpp v249, v249, v249 row_mirror row_mask:0xf bank_mask:0xf\n\t"
                "v_mul_f32 v248, %[scl], v248\n\t"
                "v_mul_f32 v249, %[scl], v249\n\t"
                "v_fma_f32 v244, v244, %[scl], -v248\n\t"
                "v_fma_f32 v245, v245, %[scl], -v249\n\t"
                "v_fma_f32 v246, v246, %[scl], -v248\n\t"
                "v_fma_f32 v247, v247, %[scl], -v249\n\t"
                "v_exp_f32 v244, v244\n\t"
                "v_exp_f32 v245, v245\n\t"
                "v_fma_f32 v240, v240, %[scl], -v248\n\t"
                "v_fma_f32 v241, v241, %[scl], -v249\n\t"
                "v_exp_f32 v246, v246\n\t"
                "v_exp_f32 v247, v247\n\t"
                "v_exp_f32 v240, v240\n\t"
                "v_exp_f32 v241, v241\n\t"
                "v_add_f32 v250, v244, v246\n\t"
                "v_add_f32 v251, v245, v247\n\t"
                "v_fma_f32 v250, v240, 0.5, v250\n\t"
                "v_fma_f32 v251, v241, 0.5, v251\n\t"
                "s_nop 0\n\t"
                "v_add_f32_dpp v250, v250, v250 quad_perm:[1,0,3,2] row_mask:0xf bank_mask:0xf\n\t"
                "v_add_f32_dpp v251, v251, v251 quad_perm:[1,0,3,2] row_mask:0xf bank_mask:0xf\n\t"
                "s_nop 0\n\t"
                "v_add_f32_dpp v250, v250, v250 quad_perm:[2,3,0,1] row_mask:0xf bank_mask:0xf\n\t"
                "v_add_f32_dpp v251, v251, v251 quad_perm:[2,3,0,1] row_mask:0xf bank_mask:0xf\n\t"
                "s_nop 0\n\t"
                "v_add_f32_dpp v250, v250, v250 row_half_mirror row_mask:0xf bank_mask:0xf\n\t"
                "v_add_f32_dpp v251, v251, v251 row_half_mirror row_mask:0xf bank_mask:0xf\n\t"
                "s_nop 0\n\t"
                "v_add_f32_dpp v250, v250, v250 row_mirror row_mask:0xf bank_mask:0xf\n\t"
                "v_add_f32_dpp v251, v251, v251 row_mirror row_mask:0xf bank_mask:0xf\n\t"
                "v_rcp_f32 v250, v250\n\t"
                "v_rcp_f32 v251, v251\n\t"
                "s_nop 0\n\t"
                "v_pk_mul_f32 v[244:245], v[244:245], v[250:251]\n\t"
                "v_pk_mul_f32 v[246:247], v[246:247], v[250:251]\n\t"
                "v_pk_mul_f32 v[240:241], v[240:241], v[250:251]\n\t"
                "v_cvt_pk_bf16_f32 v244, |v244|, |v245|\n\t"
                "v_cvt_pk_bf16_f32 v246, |v246|, |v247|\n\t"
                "v_cvt_pk_bf16_f32 v240, |v240|, |v241|\n\t"
                "ds_write_b16 %[pbx], v244\n\t"
                "ds_write_b16_d16_hi %[pbx], v244 offset:16\n\t"
                "ds_write_b16 %[pbx], v246 offset:4\n\t"
                "ds_write_b16_d16_hi %[pbx], v246 offset:20\n\t"
                "ds_write_b16 %[pb], v240\n\t"
                "ds_write_b16_d16_hi %[pb], v240 offset:16"
                : "+{v[240:243]}"(sc[2]), "+{v[244:247]}"(sc[0]), "=&{v[248:251]}"(mz), "+{v[252:255]}"(sc[1])
                : [pb] "v"(pb), [pbx] "v"(pbx), [scl] "s"(SCL) : "memory");
            asm volatile("s_waitcnt lgkmcnt(0)" ::: "memory");
            const u32x4 v = lds_ld<u32x4>(L_Q + l16);
#pragma unroll
            for (int c = 0; c < (int)NCP; ++c) __builtin_amdgcn_raw_buffer_store_b128(v, rr, poff, RG_XP + slot * XP_SLOT + c * XP_COPY, 16);
            asm volatile("s_wakeup\n\ts_nop 1" :: "v"(v) : "memory");
            __builtin_amdgcn_s_setprio(0);
            ST(5);
        } else if constexpr (CLS == 1 || CLS == 4) {
            const int ln = lane_id(), l15 = ln & 15, l4 = ln >> 4;
            constexpr int mt = (CLS == 4);
            const unsigned qa = L_QX + mt * 6144 + ln * 16, kmo = L_KM + l4 * 128 + (ln & 7) * 16;
            const unsigned xo = mt ? (h & 3) * 1024 : 5120 + ((h >> 1) & 1) * 1024 + (h & 1) * 256;
            const unsigned pm = l4 < 2 ? (mt ? 4096 : 7168) + (h & 3) * 256 - xo + l15 * 16 + l4 * 8 : OOB;
            const unsigned so = RG_PX + slot * PX_SLOT + (h >> 2) * BLK + xo;
            f32x4 sd;
#pragma unroll
            for (int r = 0; r < 4; ++r) sd[r] = (l4 * 4 + r > l15) ? -3.0e38f : 0.f;
            ASM_ST_DECL;
#define P4_ARGS : "+{v[240:243]}"(sd) ASM_ST_OUT : WREGS(wf), [qa] "v"(qa), [km] "v"(kmo), [pm] "v"(pm), [rr] "s"(rr), [so] "s"(so), [scl] "s"(SCL) : "memory", "v200", "v201", "v202", "v203", "v204", "v205", "v206", "v207", "v208", "v209", "v210", "v211", "v212", "v213", "v214", "v215", "v216", "v217", "v218", "v219", "v220", "v221", "v222", "v223", "v224", "v225", "v226", "v227", "v228", "v229", "v230", "v231", "v232", "v233", "v234", "v235", "v236", "v237", "v238", "v239", "v244", "v245", "v246", "v247", "v248", "v249", "v250", "v251"
            if constexpr (mt) asm volatile(ASM_P4_1 P4_ARGS); else asm volatile(ASM_P4_0 P4_ARGS);
            ASM_ST_SAVE(4);
            ST(5);
            if constexpr (mt) {
                const int ln = lane_id();
                const u32x4 s4 = sent4();
                const unsigned poff = (ln * 16 + ((ln < 8) ? h * 128 : BLK - 128 + (h >> 4) * BLK + (h & 15) * 512)) | ((ln >= 40 || t < 1) ? 0x40000000u : 0u);
#pragma unroll
                for (int c = 0; c < (int)NCP; ++c) st128(s4, rr, poff, RG_XP + ((t - 1) & (NSLOT - 1)) * XP_SLOT + c * XP_COPY);
                const unsigned pbase = RG_PX + ((t - 2) & (NSLOT - 1)) * PX_SLOT + (h >> 2) * BLK;
                const unsigned o2 = ((ln & 16) ? 5120 + ((h >> 1) & 1) * 1024 + (h & 1) * 256 + (ln >> 5) * 512 : 4096 + (h & 3) * 256 + (ln >> 5) * 3072) + (ln & 15) * 16;
                if (t >= 2) { st128(s4, rr, ln * 16, pbase + (h & 3) * 1024); st128(s4, rr, o2, pbase); }
            } else if (L && t + 2 < NSEG) xn_wait(P, rr, t + 2, cpy);
        } else {
            bar();
            ST(4);
            {
                if (CLS == 3 && (wave == 4 || wave == 7)) {
                    if (t + 1 < NSEG) xdma(t + 1, 1);
                }
                ST(5);
            }
        }
        STFLUSH(t);
    }
    };
    if (wave == 0) run(std::integral_constant<int, 0>{});
    else if (wave == 1) run(std::integral_constant<int, 1>{});
    else if (wave == 2) run(std::integral_constant<int, 4>{});
    else if (wave == 3) run(std::integral_constant<int, 2>{});
    else run(std::integral_constant<int, 3>{});
}

__device__ __forceinline__ void h_role(const Params& P, int wave, int L, int par, int i, int cpy) {
    const rsrc_t rr = mkrsrc(P.ws + par * 8192, RG_L), rp = mkrsrc(P.ws + PG0, PG_L);
    const char* xn = P.ws + PG0 + PG_XN + L * PG_LX;
    auto run = [&](auto Cc) {
    constexpr bool HI = decltype(Cc)::value;
    const int mt = (wave >> 1) & 1;
    auto xdma = [&](int seg, int hb) {
        const unsigned l16 = lane_id() * 16;
        const char* g = xn + pgb(seg, par);
        dma4(g + blin(mt * 20480 + hb * 8192 + (wave & 1) * 4096), l16, L_XA + mt * 16384 + hb * 8192 + (wave & 1) * 4096);
        dmaq<0>(g + blin(mt * 20480 + 16384) + hb * 2048 + (wave & 1) * 1024, l16, L_XB + mt * 4096 + hb * 2048 + (wave & 1) * 1024);
    };
    WRegs wf;
    load_w3(wf, i < 16 ? P.w[2] : P.w[0], wave, (i < 16 ? i * 2 + HI : (i - 16) * 6 + HI * 3) * 384, i < 16 ? 128 : 384);
    const unsigned qs = (HI ? L_KX : L_QX) + (wave & 1) * 3072 + mt * 8;
    const unsigned xa = L_XA + wave * 2048 + lane_id() * 16, wl = L_W + wave * 10240 + lane_id() * 16;
    const unsigned fp = RG_FH + (i >> 4) * BLK + ((i & 15) * 4 + (wave & 3)) * 128;
    int frc;
    asm volatile("s_xor_b32 %0, %1, 1" : "=s"(frc) : "s"(L) : "scc");
    unsigned go = 0, all = 0;
    STDECL;
    for (int t = 0; t <= NSEG; ++t) {
        ST(0);
        if constexpr (HI) {
            const unsigned l16 = lane_id() * 16;
            if (t) {
                u32x4 v[6];
                const int w4 = wave - 4;
#pragma unroll
                for (int k = 0; k < 6; ++k) v[k] = lds_ld<u32x4>(L_QX + ((k >> 1) * 8 + w4 * 2 + (k & 1)) * 1024 + l16);
                const unsigned so = pgb(t - 1, par) + PG_HV + i * 3 * BLK + w4 * 2048;
#pragma unroll
                for (int k = 0; k < 6; ++k) st128(v[k], rp, l16 + (k & 1) * 1024, so + (k >> 1) * BLK);
                if (go) xdma(t, 1);
                else {
                    wait_vm<0>();
                    if (l16 < 512) __builtin_amdgcn_raw_buffer_store_b32(t, rr, l16 >> 2, fp, 16);
                }
            }
            ST(1);
            if (t == NSEG) break;
            if (!go) { if (L) xn_wait(P, rr, t, cpy); xdma(t, 0); xdma(t, 1); wait_vm<5>(); }
            if (!all) bar();
        } else {
            if (t == NSEG) break;
            if (!all) bar();
        }
        ST(2);
        C1Regs C;
        const WRegs& W = wf;
        ASM_ST_DECL;
        if constexpr (HI) {
            const char* g = xn + pgb(t + 1 < NSEG ? t + 1 : t, par);
            unsigned fv = (lane_id() & 15) * 128;
            asm volatile(ASM_XG_H_HI : XG_OUT, [go] "+s"(go), [fv] "+v"(fv) ASM_ST_OUT : XG_IN, [rr] "s"(rr), [fs] "s"(RG_FX + (cpy & 3) * 2048), [fp] "s"(fp), [tv] "s"(t), [t1] "s"(t + 1),
                         [frc] "s"(frc), [g0] "s"(g + blin(mt * 20480 + (wave & 1) * 4096)), [d0] "s"(L_XA + mt * 16384 + (wave & 1) * 4096),
                         [g1] "s"(g + blin(mt * 20480 + 16384) + (wave & 1) * 1024), [d1] "s"(L_XB + mt * 4096 + (wave & 1) * 1024) : XG_CLOB, "vcc");
            go = __builtin_amdgcn_readfirstlane(go);
            lds_st<unsigned>(L_MEM + (wave & 3) * 4, go);
            wait_vm<0>();
        } else asm volatile(ASM_XG_S_LO : XG_OUT ASM_ST_OUT : XG_IN : XG_CLOB);
        ASM_ST_SAVE(3);
        ST(4);
        bar();
        const u32x4 gv = lds_ld<u32x4>(L_MEM);
        all = __builtin_amdgcn_readfirstlane(gv[0] & gv[1] & gv[2] & gv[3]);
        ST(5);
        STFLUSH(t);
    }
    };
    if (wave < 4) run(std::false_type{}); else run(std::true_type{});
}

__device__ __forceinline__ void v_role(const Params& P, int wave, int L, int par, int h, int cpy) {
    const rsrc_t rr = mkrsrc(P.ws + par * 8192, RG_L);
    WRegs wf;
    STDECL;
    if (wave < 4) {
        load_w3(wf, P.w[2], wave, h * 384, 128);
        const unsigned wl = L_W + wave * 10240 + lane_id() * 16, dsto = c1_dst(wave, false);
        auto vfetch = [&](int seg, int tries) {
            const int ln = lane_id();
            for (int spins = 0;; ++spins) {
                if (spins == tries) return false;
                const unsigned f = __builtin_amdgcn_raw_buffer_load_b32(rr, (ln & 3) * 128, RG_FH + (h >> 1) * 512, 16);
                if (__all(f > (unsigned)seg)) break;
                __builtin_amdgcn_s_sleep(4);
                if (spin_fail(spins, P)) break;
            }
            asm volatile("" ::: "memory");
            const char* g = P.ws + PG0 + pgb(seg, par) + PG_HV + (h >> 1) * 3 * BLK;
#pragma unroll
            for (int k = 0; k < 3; ++k) {
                const int f = (h & 1) * 12 + wave * 3 + k;
                dmaq<0>(g + (f >> 3) * BLK + (f & 7) * 1024, ln * 16, L_QX + (seg & 1) * 12288 + (wave * 3 + k) * 1024);
            }
            return true;
        };
        bool have = false;
        vfetch(0, -1);
        wait_vm<0>();
        bar();
        for (int t = 0; t < NSEG; ++t) {
            if (!have && t + 1 < NSEG) { vfetch(t + 1, -1); wait_vm<0>(); }
            ST(7);
            bar();
            have = t + 2 < NSEG && vfetch(t + 2, VTRY);
            C1Regs C;
            asm volatile("ds_read_b128 v[200:203], %4\n\tds_read_b128 v[204:207], %4 offset:1024\n\tds_read_b128 v[228:231], %4 offset:2048\n\tds_read_b128 v[232:235], %4 offset:3072\n\t"
                         "v_mov_b64 v[224:225], 0\n\tv_mov_b64 v[226:227], 0\n\tv_mov_b64 v[244:245], 0\n\tv_mov_b64 v[246:247], 0\n\tv_mov_b64 v[248:249], 0\n\tv_mov_b64 v[250:251], 0\n\ts_waitcnt lgkmcnt(0)"
                         : XG_OUT : "v"(wl) : "memory");
            ST(6);
            __builtin_amdgcn_s_sleep(VSL);
            crit1<true>(P, wf, C, rr, RG_XM + (t & (NSLOT - 1)) * XM_SLOT + cpy * XM_COPY, wave, wl, dsto STARG);
            bar();
            ST(4);
            STFLUSH(t);
        }
    } else {
        const int w4 = wave - 4;
        const rsrc_t rd = mkrsrc(P.ws + PG0, PG_L);
        load_wo(wf, P.w[3], wave, h);
        for (int t = -1; t < NSEG; ++t) {
            const unsigned slot = t & (NSLOT - 1);
            bar();
            ST(7);
            if (t + 1 < NSEG) {
                const unsigned l16 = lane_id() * 16;
                const unsigned wl = L_W + wave * 10240 + l16, va = L_QX + ((t + 1) & 1) * 12288 + l16;
                unsigned so = (((t + 1) & 7) << 22) + pgb(64 + (w4 >> 1), par) + ((w4 & 1) * 40 + (h >> 3)) * BLK + (h & 7) * 1024;
                asm volatile(ASM_VOX : [so] "+s"(so) : WREGS(wf), [l16] "v"(l16), [wl] "v"(wl), [va] "v"(va), [rd] "s"(rd) : "memory", "scc", "v200", "v201", "v202", "v203", "v204", "v205", "v206", "v207", "v208", "v209", "v210", "v211", "v212", "v213", "v214", "v215", "v216", "v217", "v218", "v219", "v220", "v221", "v222", "v223", "v224", "v225", "v226", "v227", "v228", "v229", "v230", "v231", "v232", "v233", "v234", "v235", "v236", "v237", "v238", "v239", "v240", "v241", "v242", "v243", "v244", "v245", "v246", "v247", "v248", "v249", "v250", "v251");
            }
            if (t < 0) continue;
            ST(0);
            unsigned wl = L_W + wave * 10240 + lane_id() * 16;
            bf16x16 lw = lds_frag(wl, 1024);
            u32x32 cz;
            asm volatile("v_mov_b64 v[220:221], 0\n\tv_mov_b64 v[222:223], 0\n\tv_mov_b64 v[224:225], 0\n\tv_mov_b64 v[226:227], 0\n\tv_mov_b64 v[228:229], 0\n\tv_mov_b64 v[230:231], 0\n\tv_mov_b64 v[232:233], 0\n\tv_mov_b64 v[234:235], 0\n\tv_mov_b64 v[236:237], 0\n\tv_mov_b64 v[238:239], 0\n\tv_mov_b64 v[240:241], 0\n\tv_mov_b64 v[242:243], 0\n\tv_mov_b64 v[244:245], 0\n\tv_mov_b64 v[246:247], 0\n\tv_mov_b64 v[248:249], 0\n\tv_mov_b64 v[250:251], 0" : "={v[220:251]}"(cz));
            unsigned vo = ((wl >> 8) & 1) * 2 * BLK + ((wl >> 9) & 1) * 1024 + (wl & 255);
            asm volatile("" : "+{v[212:219]}"(lw), "+{v[220:251]}"(cz), "+v"(wl), "+v"(vo));
            wait_vm<0>();
            unsigned l16 = wl & 1023u;
            if (l16 < 512 && t + 1 < NSEG) __builtin_amdgcn_raw_buffer_store_b32(t + 2, rr, l16 >> 2, RG_FV + (w4 >> 1) * BLK + ((w4 & 1) * 32 + h) * 128, 16);
            ST(1);
            const int sidx = (wl & 128u) ? 0xEEEE : 0x4444;
            unsigned so = RG_VM + slot * VM_SLOT + (unsigned)((w4 * 10 + (h >> 4)) * BLK + ((h >> 2) & 3) * 2048 + (h & 3) * 256);
            bar();
            ST(3);
            ASM_ST_DECL;
            asm volatile(ASM_CRIT2 : [so] "+s"(so), "+{v[212:219]}"(lw), "+{v200}"(l16), "+{v[220:251]}"(cz) ASM_ST_OUT
                         : WREGS(wf), [wl] "v"(wl), [vo] "v"(vo), [sidx] "v"(sidx), [rr] "s"(rr)
                         : "memory", "scc", "v201", "v202", "v203", "v204", "v205", "v206", "v207", "v208", "v209", "v210", "v211");
            ASM_ST_SAVE(4);
            ST(5);
            STFLUSH(t);
        }
    }
}

constexpr unsigned R_RED = 0;
constexpr unsigned R_PART = 16384;
__device__ __forceinline__ void r_role(const Params& P, int wave, int L, int par, int jj, int cpy) {
    const int lane = lane_id(), l15 = lane & 15, l4 = lane >> 4;
    const unsigned l16 = lane * 16;
    const rsrc_t rr = mkrsrc(P.ws + par * 8192, RG_L), rd = mkrsrc(P.ws + PG0, PG_L);
    const int sidx = (l15 < 8) ? 0x4444 : 0xEEEE;
    unsigned offp[3];
    offp[0] = RG_XP + cpy * XP_COPY + BLK + (wave >> 2) * BLK + (wave & 3) * 2048 + l16;
    offp[1] = offp[0] + 1024;
    offp[2] = (l15 < 8) ? RG_XP + cpy * XP_COPY + wave * 512 + l4 * 128 + l15 * 16 : OOB;
    const unsigned offv = RG_VM + (jj * 2 + (wave >> 2)) * BLK + (wave & 3) * 2048 + l16;
    const int j = jj * 2 + (wave >> 2);
    const unsigned offo = ((wave & 3) < (int)NCP) ? RG_XM + (wave & 3) * XM_COPY + j * 256 + (j >> 5) * 8192 + lane * 4 : OOB;
    {
        const int row = (lane >> 2) & 7, col = 16 * j + 4 * (lane & 3) + 2 * (lane >> 5);
        __builtin_amdgcn_raw_buffer_store_b32(nosent(*(const unsigned*)(P.mem0 + row * DM + col)), rr, offo, 0, 16);
    }
    auto red_addr = [](int fl) { return R_RED + fl * 80 + (fl >> 4) * 16; };
    const unsigned rw0 = (lane >> 5) * 5248 + red_addr((((l15 >> 1) & 1) * 8 + 4 * (l4 & 1)) * 4 + (l15 >> 2)) + (l15 & 1) * 4 + (wave ^ (l15 >> 3)) * 8;
    const unsigned rrd = (wave >> 2) * 5248 + red_addr(lane);
    u32x32 vx;
    const unsigned fo = (lane & 3) * 128, fs = RG_FV + (jj / 10) * BLK + (((jj / 5) & 1) * 32 + wave * 4) * 128, vxo = pgb(64 + jj / 10, par) + (unsigned)((jj % 10) * 8 + (wave >> 1)) * BLK + (wave & 1) * 4096;
    const rsrc_t rp = mkrsrc(P.ws + PG0, PG_L);
    const unsigned xo = (l4 >> 1) * 256 + l15 * 16 + (l4 & 1) * 8, pw = R_PART + wave * 1024 + l16, pr = R_PART + (wave & 3) * 8192 + l16;
    const int fin = wave >> 2;
    asm volatile("" : "={v[128:159]}"(vx));
    auto vx_fetch = [&](int t) {
        for (int spins = 0;; ++spins) {
            const unsigned f = __builtin_amdgcn_raw_buffer_load_b32(rr, fo, fs, 16);
            if (__all(f > (unsigned)t)) break;
            __builtin_amdgcn_s_sleep(2);
            if (spin_fail(spins, P)) break;
        }
        const unsigned sv = ((t & 7) << 22) + vxo;
        asm volatile("buffer_load_dwordx4 v[128:131], %1, %2, %3 offen sc1\n\tbuffer_load_dwordx4 v[132:135], %1, %2, %3 offen offset:1024 sc1\n\t"
                     "buffer_load_dwordx4 v[136:139], %1, %2, %3 offen offset:2048 sc1\n\tbuffer_load_dwordx4 v[140:143], %1, %2, %3 offen offset:3072 sc1\n\t"
                     "buffer_load_dwordx4 v[144:147], %1, %2, %4 offen sc1\n\tbuffer_load_dwordx4 v[148:151], %1, %2, %4 offen offset:1024 sc1\n\t"
                     "buffer_load_dwordx4 v[152:155], %1, %2, %4 offen offset:2048 sc1\n\tbuffer_load_dwordx4 v[156:159], %1, %2, %4 offen offset:3072 sc1"
                     : "+{v[128:159]}"(vx) : "v"(l16), "s"(rp), "s"(sv), "s"(sv + 4 * BLK) : "memory");
    };
    vx_fetch(0);
    STDECL;
    for (int t = 0; t < NSEG; ++t) {
        const unsigned slot = t & (NSLOT - 1);
        ST(0);
        u32x4 pm = sent4();
        u32x8 pv = __builtin_shufflevector(pm, pm, 0, 1, 2, 3, 0, 1, 2, 3), vm = pv;
#pragma unroll 1
        for (int spins = 1023;; spins += 1024) {
            unsigned n, a;
            ASM_ST_DECL;
            asm volatile(ASM_RCRIT : "+{v[176:183]}"(vm), "+{v[184:191]}"(pv), "+{v[192:195]}"(pm), [n] "=&s"(n), [a] "=&s"(a) ASM_ST_OUT ASM_ST2_OUT
                         : [rs] "s"(rr), [ov] "v"(offv), [sv] "s"(slot * VM_SLOT), [o0] "v"(offp[0]), [o1] "v"(offp[1]), [o2] "v"(offp[2]), [sp] "s"(slot * XP_SLOT),
                           "{v[128:159]}"(vx), [sidx] "v"(sidx), [rw] "v"(rw0), [rd] "v"(rrd), [oo] "v"(offo),
                           [so] "s"(((t + 1) & (NSLOT - 1)) * XM_SLOT), [pub] "s"(NSEG - 1 - t)
                         : "memory", "vcc", "scc", "v160", "v161", "v162", "v163", "v164", "v165", "v166", "v167", "v196", "v200", "v201", "v202", "v203", "v204", "v205", "v206", "v207", "v208", "v209", "v210", "v211", "v212", "v213", "v214", "v215");
            ASM_ST_SAVE(2); ASM_ST2_SAVE(1);
            if (__builtin_amdgcn_readfirstlane(n)) break;
            if (spin_fail(spins, P)) { bar(); break; }
        }
        ST(3);
        __builtin_amdgcn_s_sleep(RXDEL);
        unsigned early;
#pragma unroll 1
        for (int spins = 1023;; spins += 1024) {
            unsigned n;
            const unsigned sx = pgb(t, par) + blin(((wave >> 1) & 1) * 20480 + jj * 1024) + (wave & 1) * 512;
            const unsigned pxb = RG_PX + slot * PX_SLOT + wave * BLK, sv = (((t + 1) & 7) << 22) + vxo;
            ASM_ST_DECL;
            asm volatile(ASM_RX : "+{v[128:159]}"(vx), [n] "=&s"(n), [a] "=&s"(early) ASM_ST_OUT ASM_ST2_OUT
                         : "{v[176:183]}"(vm), "{v[184:191]}"(pv), "{v[192:195]}"(pm), [l16] "v"(l16), [rs] "s"(rr), [s0] "s"(pxb), [s1] "s"(pxb + 4096), [rd] "s"(rd), [rx] "s"(rd), [xo] "v"(xo),
                           [fo] "v"(fo), [fs] "s"(fs), [tn] "s"(t + 1), [sv0] "s"(sv), [sv1] "s"(sv + 4 * BLK), [rp] "s"(rp), [sx] "s"(PG_XR + L * PG_LX + sx), [sa] "s"(PG_XA + sx),
                           [pw] "v"(pw), [pr] "v"(pr), [fin] "s"(fin)
                         : "memory", "vcc", "scc", "v96", "v97", "v98", "v99", "v100", "v101", "v102", "v103", "v104", "v105", "v106", "v107", "v108", "v109", "v110", "v111", "v112", "v113", "v114", "v115", "v116", "v117", "v118", "v119", "v120", "v121", "v122", "v123", "v124", "v125", "v126", "v127", "v196", "v200", "v201", "v202", "v203", "v204", "v205", "v206", "v207", "v208", "v209", "v210", "v211", "v212", "v213", "v214", "v215", "v216", "v217", "v218", "v219", "v220", "v221", "v222", "v223", "v224", "v225", "v226", "v227", "v228", "v229", "v230", "v231", "v240", "v241", "v242", "v243", "v248", "v250", "v251");
            ASM_ST_SAVE(4); ASM_ST2_SAVE(5);
            if (__builtin_amdgcn_readfirstlane(n)) break;
            early = 0;
            if (spin_fail(spins, P)) { bar(); break; }
        }
        ST(6);
        if (t + 1 < NSEG && !__builtin_amdgcn_readfirstlane(early)) vx_fetch(t + 1);
        st128(sent4(), rr, offv, slot * VM_SLOT); st128(sent4(), rr, offv + 1024, slot * VM_SLOT);
        __builtin_amdgcn_raw_buffer_store_b32(SENT, rr, offo, slot * XM_SLOT, 16);
        if (wave < 2 && t + AH < NSEG) st128(sent4(), rd, l16, PG_XA + pgb(t + AH, par) + blin(wave * 20480 + jj * 1024));
        ST(7);
        STFLUSH(t);
    }
}

template <int WHICH>
__device__ __forceinline__ void w_role(const Params& P, int wave, int par, int cu) {
    constexpr int NT = (WHICH == 1) ? 1 : 2;
    constexpr int KS = 20, PW = 5;
    constexpr int N = (WHICH == 3) ? FF : DM;
    constexpr unsigned W_ST = 2 * 49152;
    const int ln = lane_id(), l15 = ln & 15, l4 = ln >> 4;
    const unsigned l16 = ln * 16;
    const rsrc_t rd = mkrsrc(P.ws + PG0, PG_L);
    constexpr unsigned D_IN = WHICH == 1 ? PG_XA : WHICH == 2 ? PG_HS : PG_X1;
    constexpr unsigned D_OUT = WHICH == 1 ? PG_HS : WHICH == 2 ? PG_X1 : PG_H2;
    const unsigned pc0 = (wave >> 2) * 20480 + (wave & 3) * 4096, pc1 = (wave >> 2) * 20480 + 16384 + (wave & 3) * 1024, in0 = D_IN + blin(pc0), in1 = D_IN + blin(pc1);
    if (WHICH == 2 && cu * 8 + wave >= 20) {
        bar();
        for (int t = 0; t < NSEG; ++t) {
            u32x4 v[PW] = {};
            poll5_issue(rd, l16, pgb(t, par) + in0, pgb(t, par) + in1, v);
            wait_vm<0>();
            poll5(rd, l16, pgb(t, par) + in0, pgb(t, par) + in1, v, P);
#pragma unroll
            for (int k = 0; k < PW; ++k) lds_st<u32x4>((t & 1) * 49152u + (k < 4 ? pc0 + k * 1024 : pc1) + l16, v[k]);
            bar();
            wait_vm<0>();
            asm volatile("" :: "v"(v[0]), "v"(v[1]), "v"(v[2]), "v"(v[3]), "v"(v[4]));
        }
        return;
    }
    const int nt0 = (cu * 8 + wave) * NT;
    bf16x8 wf[NT][KS];
    {
        u32x4 f[NT * KS];
        load_wn<NT, KS, N>(f, P.w[3 + WHICH], DM, wave, nt0, 0);
#pragma unroll
        for (int k = 0; k < NT * KS; ++k) wf[k / KS][k % KS] = BF8(f[k]);
    }
    bar();
    f32x4 cs[NT];
    if (WHICH != 2) {
        const bf16x8 ones = __builtin_bit_cast(bf16x8, ((u32x4){0x3F803F80u, 0x3F803F80u, 0x3F803F80u, 0x3F803F80u}));
#pragma unroll
        for (int n = 0; n < NT; ++n) {
            cs[n] = (f32x4){0.f, 0.f, 0.f, 0.f};
#pragma unroll
            for (int k = 0; k < KS; ++k) MFMA(wf[n][k], ones, cs[n]);
        }
    }
    const int grp = nt0 / 10, sh = (WHICH == 1) ? ((grp & 1) ? -1 : 1) * ((grp >> 1) + 1) : 0;
    if (WHICH == 1) {
        const int zr = (grp & 1) ? S_ - 1 - l15 : l15;
        unsigned zv;
        asm volatile("v_mov_b32 %0, 0" : "=v"(zv));
        st128(((u32x4){zv, zv, zv, zv}), rd, (l15 <= (grp >> 1) && l4 < 2) ? toff(zr, nt0 * 16 + l4 * 8, par) : OOB, PG_HS);
    }
    u32x4 v[PW] = {};
    poll5_issue(rd, l16, pgb(0, par) + in0, pgb(0, par) + in1, v);
    wait_vm<0>();
    STDECL;
    for (int t = 0; t < NSEG; ++t) {
        const unsigned ab = (t & 1) * 49152u, pg = pgb(t, par);
        ST(0);
        {
            poll5(rd, l16, pg + in0, pg + in1, v, P);
            ST(1);
#pragma unroll
            for (int k = 0; k < PW; ++k) lds_st<u32x4>(ab + (k < 4 ? pc0 + k * 1024 : pc1) + l16, v[k]);
            if (WHICH != 2) {
                float s = 0.f, q = 0.f;
                const bf16x2 one2 = __builtin_bit_cast(bf16x2, 0x3F803F80u);
#pragma unroll
                for (int k = 0; k < PW; ++k)
#pragma unroll
                    for (int e = 0; e < 4; ++e) {
                        const unsigned u = v[k][e];
                        const bf16x2 x = __builtin_bit_cast(bf16x2, u);
                        s = __builtin_amdgcn_fdot2_f32_bf16(x, one2, s, false);
                        q = __builtin_amdgcn_fdot2_f32_bf16(x, x, q, false);
                    }
                sum2_rows(s, q);
                if (ln < 16) lds_st<f32x2>(W_ST + (t & 1) * 1024 + wave * 128 + ln * 8, (f32x2){s, q});
            }
        }
        bar();
        ST(2);
        u32x2 r1[2][NT], sv[2];
        if (WHICH == 2) {
            const unsigned ro = (l4 >> 1) * 256 + l15 * 16 + (l4 & 1) * 8;
#pragma unroll
            for (int mt = 0; mt < 2; ++mt) { r1[mt][0] = LD64(rd, ro, pg + PG_XA + blin(mt * 20480 + nt0 * 512)); r1[mt][NT - 1] = LD64(rd, ro + 512, pg + PG_XA + blin(mt * 20480 + nt0 * 512)); sv[mt] = LD64(rd, (mt * 16 + l15) * 8, pg + PG_SS); }
        }
        float mr[2], rs[2];
        f32x2 st[2][4];
        if (WHICH != 2) {
#pragma unroll
            for (int w = 0; w < 8; ++w) st[w >> 2][w & 3] = lds_ld<f32x2>(W_ST + (t & 1) * 1024 + w * 128 + l15 * 8);
        }
        auto stats = [&](int mt) {
            const f32x2 a = (st[mt][0] + st[mt][1]) + (st[mt][2] + st[mt][3]);
            const float mean = a[0] * (1.0f / DM), var = __builtin_fmaxf(a[1] * (1.0f / DM) - mean * mean, 0.f);
            const float m1 = rbf(mean), d1 = ln_den(var), r1i = rcp(d1);
            if (WHICH == 1) {
                const float m2 = rbf((mean - m1) * r1i), d2 = ln_den(var * r1i * r1i);
                rs[mt] = r1i * rcp(d2); mr[mt] = -(m1 + m2 * d1) * rs[mt];
                if (cu == 0 && wave == 0)
                    __builtin_amdgcn_raw_buffer_store_b64((u32x2){__float_as_uint(m1), __float_as_uint(r1i)}, rd, ln < 16 ? (mt * 16 + ln) * 8 : OOB, pg + PG_SS, 16);
            } else { rs[mt] = r1i; mr[mt] = -m1 * r1i; }
            asm volatile("" : "+v"(rs[mt]), "+v"(mr[mt]));
        };
        f32x4 acc[NT][2];
        unsigned o[2][NT][2];
        auto fin = [&](int mt, int n, int hf) {
            float v[2];
#pragma unroll
            for (int e = 0; e < 2; ++e) {
                if (WHICH != 2) v[e] = gelu_tanh(__builtin_fmaf(acc[n][mt][hf * 2 + e], rs[mt], cs[n][hf * 2 + e] * mr[mt]));
                else v[e] = rbf(((e ? hi2f(r1[mt][n][hf]) : lo2f(r1[mt][n][hf])) - __uint_as_float(sv[mt][0])) * __uint_as_float(sv[mt][1])) + acc[n][mt][hf * 2 + e];
            }
            o[mt][n][hf] = nosent(pack2(v[0], v[1]));
            asm volatile("" : "+v"(o[mt][n][hf]));
        };
        const unsigned o3 = (unsigned)((l4 & 1) * 512 + (l4 >> 1) * 256 + l15 * 16), c3 = D_OUT + blin((nt0 >> 1) * 1024), d3 = D_OUT + blin(N * 32 + (nt0 >> 1) * 1024);
#pragma unroll
        for (int n = 0; n < NT; ++n) { acc[n][0] = (f32x4){0.f, 0.f, 0.f, 0.f}; acc[n][1] = acc[n][0]; }
        {
            constexpr int RG = 8;
            bf16x8 a[RG];
#pragma unroll
            for (int u = 0; u < RG - 1; ++u) a[u] = lds_ld<bf16x8>(ab + l16 + u * 1024);
#pragma unroll
            for (int u = 0; u < 2 * KS; ++u) {
                if (u + RG - 1 < 2 * KS) a[(u + RG - 1) % RG] = lds_ld<bf16x8>(ab + l16 + (u + RG - 1) * 1024);
                SB();
#pragma unroll
                for (int n = 0; n < NT; ++n) MFMA(wf[n][u % KS], a[u % RG], acc[n][u / KS]);
                if (WHICH != 2 && (u == 4 || u == 12)) stats(u == 12);
                if (u == (NT == 2 ? 22 : 6)) poll5_issue(rd, l16, pgb(t + 1, par) + in0, pgb(t + 1, par) + in1, v);
                if (NT == 2 && u == 38) wait_vm<0>();
                if (u >= 24 && u % 4 == 0 && (u - 24) / 4 < 2 * NT) fin(0, (u - 24) / 8, ((u - 24) / 4) & 1);
                if (NT == 2 && u == 38) st128(merge16((u32x2){o[0][0][0], o[0][0][1]}, (u32x2){o[0][NT - 1][0], o[0][NT - 1][1]}), rd, o3, pg + c3);
                SB();
            }
        }
        ST(3);
#pragma unroll
        for (int k = 0; k < 2 * NT; ++k) fin(1, k >> 1, k & 1);
        if (NT == 2) {
            st128(merge16((u32x2){o[1][0][0], o[1][0][1]}, (u32x2){o[1][NT - 1][0], o[1][NT - 1][1]}), rd, o3, pg + d3);
            ST(4);
            if (t + AH < NSEG) { st128(sent4(), rd, l16, pgb(t + AH, par) + c3); st128(sent4(), rd, l16, pgb(t + AH, par) + d3); }
        } else {
            wait_vm<0>();
            const int orow = t * SEG + (l4 & 1) * 16 + l15 + sh;
            st128(merge16((u32x2){o[0][0][0], o[0][0][1]}, (u32x2){o[1][0][0], o[1][0][1]}), rd, (unsigned)orow < (unsigned)S_ ? toff(orow, nt0 * 16 + (l4 >> 1) * 8, par) : OOB, D_OUT);
            ST(4);
            if (t + AH < NSEG) st128(sent4(), rd, orow + AH * SEG < S_ ? toff(orow + AH * SEG, nt0 * 16 + (l4 >> 1) * 8, par) : OOB, D_OUT);
        }
        STFLUSH(t);
    }
    wait_vm<0>();
    asm volatile("" :: "v"(v[0]), "v"(v[1]), "v"(v[2]), "v"(v[3]), "v"(v[4]));
}

__device__ __forceinline__ void w4_role(const Params& P, int wave, int L, int par, int cu) {
    const int ln = lane_id(), l15 = ln & 15, l4 = ln >> 4;
    const unsigned l16 = ln * 16;
    const rsrc_t rd = mkrsrc(P.ws + PG0, PG_L);
    STDECL;
    if (wave < 6) {
        u32x32 w[5];
        {
            u32x4 g[40];
            load_wn<5, 8, DM>(g, P.w[7], FF, wave, cu * 5, wave * 8);
#pragma unroll
            for (int n = 0; n < 5; ++n) {
                u32x8 f[4];
#pragma unroll
                for (int i = 0; i < 4; ++i) f[i] = __builtin_shufflevector(g[n * 8 + 2 * i], g[n * 8 + 2 * i + 1], 0, 1, 2, 3, 4, 5, 6, 7);
                w[n] = cat32(f[0], f[1], f[2], f[3]);
            }
            asm volatile("" : "+{v[0:31]}"(w[0]), "+{v[32:63]}"(w[1]), "+{v[64:95]}"(w[2]), "+{v[96:127]}"(w[3]), "+{v[128:159]}"(w[4]));
        }
        bar();
        unsigned budget = 1u << 17;
        for (int t = 0; t < NSEG; ++t) {
            ST(0);
            const unsigned gb = PG_H2 + pgb(t, par) + wave * BLK;
            u32x4 can;
            for (int spins = 0;; ++spins) {
                can = LD128(rd, l16, gb + 7168);
                if (__all(umax4(0u, can) != SENT)) break;
                __builtin_amdgcn_s_sleep(1);
                if (spin_fail(spins, P)) { budget = 0; break; }
            }
            ST(1);
            asm volatile(ASM_W4_M : [n] "+s"(budget)
                         : "{v[188:191]}"(can), "{v[0:31]}"(w[0]), "{v[32:63]}"(w[1]), "{v[64:95]}"(w[2]), "{v[96:127]}"(w[3]), "{v[128:159]}"(w[4]),
                           [ld] "v"(l16 + wave * 1024 + (t & 1) * 61440), [l16] "v"(l16), [rs] "s"(rd), [s0] "s"(gb), [s1] "s"(gb + 4096), [s2] "s"(gb + 6 * BLK), [s3] "s"(gb + 6 * BLK + 4096)
                         : "memory", "vcc", "scc", "v160", "v161", "v162", "v163", "v164", "v165", "v166", "v167", "v168", "v169", "v170", "v171", "v172", "v173", "v174", "v175", "v176", "v177", "v178", "v179", "v180", "v181", "v182", "v183", "v184", "v185", "v186", "v187", "v192", "v193", "v194", "v195", "v196", "v197", "v198", "v199", "v200", "v201", "v202", "v203", "v204", "v205", "v206", "v207", "v208", "v209", "v210", "v211", "v212", "v213", "v214", "v215", "v216", "v217", "v218", "v219", "v220", "v221", "v222", "v223", "v224", "v225", "v226", "v227", "v228", "v229", "v230", "v231", "v232", "v233", "v234", "v235", "v236", "v237", "v238", "v239", "v240", "v241", "v242", "v243", "v244");
            ST(2);
            bar();
            ST(3);
            STFLUSH(t);
        }
    } else {
        const int mt = wave - 6;
        bar();
        const rsrc_t rr = mkrsrc(P.ws + par * 8192, RG_L), ro = mkrsrc(P.out, ACT);
        const unsigned c0 = mt * 20480 + cu * 2560, o8 = (l4 >> 1) * 256 + l15 * 16 + (l4 & 1) * 8;
        const unsigned o16a = blin(c0 + (l4 & 1) * 512) + (l4 >> 1) * 256 + l15 * 16, o16b = blin(c0 + 1024 + (l4 & 1) * 512) + (l4 >> 1) * 256 + l15 * 16, o8c = blin(c0 + 2048) + o8;
        const unsigned q8 = l15 * (DM * 2) + l4 * 8, q16 = l15 * (DM * 2) + (l4 & 1) * 32 + (l4 >> 1) * 16;
        for (int t = 0; t < NSEG; ++t) {
            const unsigned sb = pgb(t, par), sn = (sb ^ 8192) + PG_LX;
            ST(0);
            f32x4 res[5];
            for (int spins = 0;; ++spins) {
                u32x2 r1[5], r2[5];
                unsigned m = 0;
#pragma unroll
                for (int n = 0; n < 5; ++n) { r1[n] = LD64(rd, o8 + blin(c0 + n * 512), PG_X1 + sb); r2[n] = LD64(rd, o8 + blin(c0 + n * 512), PG_XA + sb); }
#pragma unroll
                for (int n = 0; n < 5; ++n) {
                    m = umax4(m, (u32x4){r1[n][0], r1[n][1], r2[n][0], r2[n][1]});
                    res[n] = (f32x4){lo2f(r1[n][0]), hi2f(r1[n][0]), lo2f(r1[n][1]), hi2f(r1[n][1])} + (f32x4){lo2f(r2[n][0]), hi2f(r2[n][0]), lo2f(r2[n][1]), hi2f(r2[n][1])};
                }
                if (__all(m != SENT)) break;
                __builtin_amdgcn_s_sleep(8);
                if (spin_fail(spins * 4 + 3, P)) break;
            }
            asm volatile("" : "+v"(res[0]), "+v"(res[1]), "+v"(res[2]), "+v"(res[3]), "+v"(res[4]));
            ST(1);
            bar();
            ST(2);
            {
                const unsigned la = (t & 1) * 61440 + mt * 30720 + l16;
                f32x4 c[30];
#pragma unroll
                for (int i = 0; i < 30; ++i) c[i] = lds_ld<f32x4>(la + ((i % 5) * 6 + i / 5) * 1024);
#pragma unroll
                for (int i = 0; i < 30; ++i) { res[i % 5] += c[i]; asm volatile("" : "+v"(res[i % 5])); }
            }
            u32x2 o[5];
#pragma unroll
            for (int n = 0; n < 5; ++n) o[n] = (u32x2){pack2(res[n][0], res[n][1]), pack2(res[n][2], res[n][3])};
            if (L) {
                const unsigned ob = (unsigned)(t * 2 + mt) * 20480 + cu * 160;
                st128_<0>(merge16(o[0], o[1]), ro, q16, ob); st128_<0>(merge16(o[2], o[3]), ro, q16, ob + 64);
                __builtin_amdgcn_raw_buffer_store_b64(o[4], ro, q8, ob + 128, 0);
            } else {
                float s = 0.f, q = 0.f;
                const bf16x2 one2 = __builtin_bit_cast(bf16x2, 0x3F803F80u);
#pragma unroll
                for (int e = 0; e < 10; ++e) {
                    const unsigned u = o[e >> 1][e & 1];
                    const bf16x2 x = __builtin_bit_cast(bf16x2, u);
                    s = __builtin_amdgcn_fdot2_f32_bf16(x, one2, s, false);
                    q = __builtin_amdgcn_fdot2_f32_bf16(x, x, q, false);
                }
                sum2_rows(s, q);
                const unsigned st = RG_ST + (unsigned)(t >> 2) * BLK + (unsigned)((t & 3) * 2 + mt) * 1024;
                __builtin_amdgcn_raw_buffer_store_b64((u32x2){nosent(__float_as_uint(s)), nosent(__float_as_uint(q))}, rr, ln < 16 ? ln * 8 : OOB, st + cu * 128, 16);
                ST(3);
                st128(merge16(o[0], o[1]), rd, o16a, PG_XR + sn); st128(merge16(o[2], o[3]), rd, o16b, PG_XR + sn);
                __builtin_amdgcn_raw_buffer_store_b64(o[4], rd, o8c, PG_XR + sn, 16);
                __builtin_amdgcn_s_sleep(W4SL);
                for (int spins = 0;; ++spins) {
                    const u32x2 a = LD64(rr, ln * 8, st), b = LD64(rr, ln * 8 + 512, st);
                    s = __uint_as_float(a[0]) + __uint_as_float(b[0]); q = __uint_as_float(a[1]) + __uint_as_float(b[1]);
                    if (__all(umax4(0u, (u32x4){a[0], a[1], b[0], b[1]}) != SENT)) break;
                    asm volatile("" ::: "memory");
                    if (spin_fail(spins, P)) break;
                }
                ST(4);
                sum2_rows(s, q);
                const float mean = s * (1.0f / DM), var = __builtin_fmaxf(q * (1.0f / DM) - mean * mean, 0.f);
                const float r = rcp(ln_den(var)), mr = -rbf(mean) * r;
#pragma unroll
                for (int n = 0; n < 5; ++n)
                    o[n] = (u32x2){pack2(__builtin_fmaf(res[n][0], r, mr), __builtin_fmaf(res[n][1], r, mr)), pack2(__builtin_fmaf(res[n][2], r, mr), __builtin_fmaf(res[n][3], r, mr))};
                st128(merge16(o[0], o[1]), rd, o16a, PG_XN + sn); st128(merge16(o[2], o[3]), rd, o16b, PG_XN + sn);
                __builtin_amdgcn_raw_buffer_store_b64(o[4], rd, o8c, PG_XN + sn, 16);
                wait_vm<0>();
                st128(((u32x4){(unsigned)t + 1, (unsigned)t + 1, (unsigned)t + 1, (unsigned)t + 1}), mkrsrc(P.ws + (par ^ 1) * 8192, RG_L), ln < 32 ? (ln >> 3) * 2048 + (ln & 7) * 16 : OOB, RG_FX + (cu * 2 + mt) * 128);
            }
            ST(5);
            STFLUSH(t);
        }
    }
}

__global__ __launch_bounds__(512, 2) void mega_kernel(Params P) {
    const int wave = __builtin_amdgcn_readfirstlane(threadIdx.x >> 6);
    if (threadIdx.x == 0) {
        const unsigned c0 = *(const unsigned*)(P.ws + FL0 + FL_BYTES);
        unsigned g = (unsigned)__builtin_amdgcn_s_getreg((3 << 11) | 20) & 7u, k;
        while ((k = __hip_atomic_fetch_add((unsigned*)(P.ws + FL0 + FL_TK + g * 128), 1u, RLX_AGENT)) >= 32u) g = (g + 1) & 7u;
        lds_st<unsigned>(0, (c0 & 1u) << 8 | g << 5 | k);
    }
    bar();
    const unsigned tk = __builtin_amdgcn_readfirstlane(lds_ld<unsigned>(0));
    bar();
    const int g4 = (tk >> 5) & 3, k = tk & 31;
#define MAP 0
    const int L = (tk >> 7) & 1;
    const int x = g4, r = k, cpy = x;
    const int par = L ^ (tk >> 8);
    {
        const int i = (r - 26) * 4 + x;
        const int d = L * DLY_L + (r < 16 ? DLY_S : (i < 0 || i == 8 || i == 9) ? 0 : DLY_W);
        for (int k = 0; k < d; ++k) __builtin_amdgcn_s_sleep(32);
    }
    if (r < 8) s_role(P, wave, L, par, x * 8 + r, cpy);
    else if (r < 16) v_role(P, wave ^ 4, L, par, x * 8 + r - 8, cpy);
    else if (r < 21) r_role(P, wave, L, par, (r - 16) * 4 + x, cpy);
    else {
        const int i = (r - 26) * 4 + x;
        if (i < 0 || i == 8 || i == 9) h_role(P, wave, L, par, r < 25 ? x * 4 + r - 21 : i < 0 ? 16 + x : 12 + i, cpy);
        else if (i < 5) w_role<1>(P, wave, par, i);
        else if (i < 8) w_role<2>(P, wave, par, i - 5);
        else if (i < 16) w_role<3>(P, wave, par, i - 10);
        else w4_role(P, wave, L, par, i - 16);
    }
}

__device__ __forceinline__ void xtile_body(int mt, const bf16_t* __restrict__ x, char* __restrict__ ws) {
    __shared__ float red[2][16][17];
    const int r15 = threadIdx.x & 15, slot = threadIdx.x >> 4;
    u32x4 v[5];
    float s = 0.f;
#pragma unroll
    for (int c = 0; c < 5; ++c) {
        v[c] = *(const u32x4*)(x + (size_t)(mt * 16 + r15) * DM + (slot * 5 + c) * 8);
#pragma unroll
        for (int e = 0; e < 4; ++e) s += lo2f(v[c][e]) + hi2f(v[c][e]);
    }
    red[0][slot][r15] = s;
    __syncthreads();
    s = 0.f;
#pragma unroll
    for (int k = 0; k < 16; ++k) s += red[0][k][r15];
    const float mean = s * (1.0f / DM);
    float q = 0.f;
#pragma unroll
    for (int c = 0; c < 5; ++c)
#pragma unroll
        for (int e = 0; e < 4; ++e) { const float a = lo2f(v[c][e]) - mean, b = hi2f(v[c][e]) - mean; q += a * a + b * b; }
    red[1][slot][r15] = q;
    __syncthreads();
    q = 0.f;
#pragma unroll
    for (int k = 0; k < 16; ++k) q += red[1][k][r15];
    const float m1 = rbf(mean), r = 1.0f / ln_den(q * (1.0f / DM));
#pragma unroll
    for (int c = 0; c < 5; ++c) {
        const unsigned o = PG0 + toff(mt * 16 + r15, (slot * 5 + c) * 8, 0);
        *(u32x4*)(ws + PG_XR + o) = v[c]; *(u32x4*)(ws + PG_XR + (o ^ 8192)) = v[c];
        u32x4 n;
#pragma unroll
        for (int e = 0; e < 4; ++e) n[e] = pack2(rbf(lo2f(v[c][e]) - m1) * r, rbf(hi2f(v[c][e]) - m1) * r);
        *(u32x4*)(ws + PG_XN + o) = n; *(u32x4*)(ws + PG_XN + (o ^ 8192)) = n;
    }
}

struct Prep1 { const bf16_t* x; char* ws; };
__global__ __launch_bounds__(256) void prep1_kernel(Prep1 A) {
    int b = blockIdx.x;
    if (b == 0) {
        __shared__ unsigned best[4][2];
        const int w = threadIdx.x >> 6;
        const rsrc_t r = mkrsrc(A.ws, RG_SLOT);
        unsigned m[2] = {~0u, ~0u};
        for (int i = 0; i < 20; ++i) {
            const unsigned long long t0 = __builtin_amdgcn_s_memtime();
            __builtin_amdgcn_raw_buffer_store_b32(0u, r, (96 + w * 8 + ((i >> 1) & 7)) * BLK + (i & 1) * 8192 + (i >> 1) * 768, 0, 16);
            asm volatile("s_waitcnt vmcnt(0)" ::: "memory");
            const unsigned d = (unsigned)(__builtin_amdgcn_s_memtime() - t0);
            if (i & 1) m[1] = d < m[1] ? d : m[1]; else m[0] = d < m[0] ? d : m[0];
        }
        best[w][0] = m[0]; best[w][1] = m[1];
        __syncthreads();
        if (threadIdx.x == 0) {
            const unsigned a = best[0][0] + best[1][0] + best[2][0] + best[3][0], c = best[0][1] + best[1][1] + best[2][1] + best[3][1];
            *(u32x4*)(A.ws + FL0 + FL_BYTES) = (u32x4){(a > c ? 1u : 0u) ^ (((unsigned)__builtin_amdgcn_s_getreg((3 << 11) | 20) >> 2) & 1u), a, c, 0u};
        }
        return;
    }
    b -= 1;
    if (b < 256) {
        for (unsigned i = b * 256 + threadIdx.x; i < (4 * RG_ST + 16 * BLK) / 16; i += 256 * 256) {
            const unsigned sl = i / (RG_ST / 16);
            ((u32x4*)A.ws)[sl < 4 ? i + sl * ((RG_SLOT - RG_ST) / 16) : i - 3 * (RG_ST / 16)] = (u32x4){SENT, SENT, SENT, SENT};
        }
        if (b < 20) ((u32x4*)(A.ws + RG_FV))[b * 256 + threadIdx.x] = (u32x4){0u, 0u, 0u, 0u};
        if (b == 0) for (unsigned i = threadIdx.x; i < FL_BYTES / 16; i += 256) ((u32x4*)(A.ws + FL0))[i] = (u32x4){0u, 0u, 0u, 0u};
        for (unsigned i = (b & 63) * 256 + threadIdx.x; i < PG_SS / 16; i += 64 * 256) ((u32x4*)(A.ws + PG0 + ((b >> 6) << 21)))[i] = (u32x4){SENT, SENT, SENT, SENT};
        return;
    }
    b -= 256;
    xtile_body(b, A.x, A.ws);
}

extern "C" void kernel_launch(void* const* d_in, const int* in_sizes, int n_in, void* d_out, int out_size, void* d_ws,
                              size_t ws_size, hipStream_t stream) {
    char* ws = (char*)d_ws;
    Params P;
    P.ws = ws; P.mem0 = (const bf16_t*)d_in[1]; P.out = (bf16_t*)d_out;
    for (int i = 0; i < 8; ++i) P.w[i] = (const bf16_t*)d_in[i < 4 ? 2 + i : 4 + i];
    (void)hipFuncSetAttribute((const void*)mega_kernel, hipFuncAttributeMaxDynamicSharedMemorySize, MEGA_LDS);

    Prep1 A1;
    A1.x = (const bf16_t*)d_in[0]; A1.ws = ws;
    prep1_kernel<<<1 + 256 + S_ / 16, 256, 0, stream>>>(A1);
    mega_kernel<<<256, 512, MEGA_LDS, stream>>>(P);
}
```

```cpp
#include <hip/hip_runtime.h>
#include <stdint.h>
#include <type_traits>

typedef unsigned short bf16_t;
using bf16x2 = __attribute__((ext_vector_type(2))) __bf16;
using bf16x8 = __attribute__((ext_vector_type(8))) __bf16;
using bf16x16 = __attribute__((ext_vector_type(16))) __bf16;
using f32x2 = __attribute__((ext_vector_type(2))) float;
using f32x4 = __attribute__((ext_vector_type(4))) float;
using u32x2 = __attribute__((ext_vector_type(2))) unsigned;
using u32x4 = __attribute__((ext_vector_type(4))) unsigned;
typedef __amdgpu_buffer_rsrc_t rsrc_t;

__device__ __forceinline__ unsigned pack2(float a, float b) {
    bf16x2 r = __builtin_convertvector((f32x2){a, b}, bf16x2);
    return __builtin_bit_cast(unsigned, r);
}
__device__ __forceinline__ float rbf(float f) { return __uint_as_float(pack2(f, 0.f) << 16); }
__device__ __forceinline__ float lo2f(unsigned u) { return __uint_as_float(u << 16); }
__device__ __forceinline__ float hi2f(unsigned u) { return __uint_as_float(u & 0xFFFF0000u); }
__device__ __forceinline__ rsrc_t mkrsrc(const void* p, unsigned bytes) {
    return __builtin_amdgcn_make_buffer_rsrc((void*)p, 0, bytes, 0x00020000);
}

constexpr int S_ = 2048, DM = 640, NH = 32, DH = 192, HD = NH * DH  , SEG = 32, NSEG = 64;
constexpr int FF = 1536;

#define XMAP
#define NCPY 4
constexpr unsigned NSLOT = 4, NCP = NCPY;
#define FL_TK_DEF
#define DLY_L 14
#define DLY_S 0
#define DLY_W 10
#define DLY_H 0
#define EWAKE "s_wakeup"
#define LWAKE
#define CALN 8
#define RSL 8
#define VTRY 3
#define VSL 1
#define NRR 5
constexpr unsigned BLK = 16384, RG_SLOT = 4u << 20;
constexpr unsigned XP_COPY = 3 * BLK, XP_SLOT = RG_SLOT;
constexpr unsigned XM_COPY = 2 * BLK, XM_SLOT = RG_SLOT;
constexpr unsigned VM_SLOT = RG_SLOT;
constexpr unsigned PX_SLOT = RG_SLOT;
constexpr unsigned RG_XP = 0, RG_XM = 12 * BLK, RG_VM = 20 * BLK, RG_PX = 60 * BLK;
constexpr unsigned RG_ST = 68 * BLK, RG_FV = 84 * BLK, RG_FH = 86 * BLK, RG_FX = 88 * BLK, RG_L = NSLOT * RG_SLOT;
constexpr unsigned FL0 = 2u << 20, FL_TK = 512, FL_BYTES = FL_TK + 8 * 128;
constexpr unsigned ACT = S_ * DM * 2;
#define PXROT 5
#define W4SL 6
#define RXDEL 1
#define VXD 32
#define VXAUX 16
constexpr unsigned PG0 = RG_L, PG_L = 80u << 21;
constexpr unsigned PG_XA = 0, PG_HS = 5 * BLK, PG_X1 = 10 * BLK, PG_H2 = 15 * BLK, PG_SS = 27 * BLK, PG_XR = 28 * BLK, PG_XN = 33 * BLK, PG_LX = 10 * BLK, PG_HV = 48 * BLK;
__device__ __forceinline__ unsigned pgb(unsigned p, int par) { return p << 21 | ((p ^ par) & 1) << 13; }
__device__ __forceinline__ constexpr unsigned blin(unsigned o) { return o + (o & ~8191u); }
constexpr int HT0 = 2;
constexpr int AH = 3;
constexpr unsigned WS_END = PG0 + PG_L;

#define SB() __builtin_amdgcn_sched_barrier(0)
#define LAS __attribute__((address_space(3)))
template <class T> __device__ __forceinline__ T lds_ld(unsigned a) { return *(const LAS T*)(a); }
template <class T> __device__ __forceinline__ void lds_st(unsigned a, T v) { *(LAS T*)(a) = v; }
constexpr unsigned OOB = 0x7FFFFF00u;
template <int N> __device__ __forceinline__ void wait_vm() { asm volatile("s_waitcnt vmcnt(%0)" :: "i"(N) : "memory"); }
__device__ __forceinline__ void bar() {
    asm volatile("s_waitcnt lgkmcnt(0)" ::: "memory");
    __builtin_amdgcn_s_barrier();
    asm volatile("" ::: "memory");
}
__device__ __forceinline__ int lane_id() {
    int ln;
    asm volatile("v_mbcnt_lo_u32_b32 %0, -1, 0\n\tv_mbcnt_hi_u32_b32 %0, -1, %0" : "=v"(ln));
    return ln;
}
__device__ __forceinline__ void swap16(unsigned& a, unsigned& b) { asm volatile("s_nop 3\n\tv_permlane16_swap_b32 %0, %1\n\ts_nop 1" : "+v"(a), "+v"(b)); }
__device__ __forceinline__ void swap32(unsigned& a, unsigned& b) { asm volatile("s_nop 3\n\tv_permlane32_swap_b32 %0, %1\n\ts_nop 1" : "+v"(a), "+v"(b)); }
__device__ __forceinline__ float sum_x16(float x) { unsigned a = __float_as_uint(x), b = a; swap16(a, b); return __uint_as_float(a) + __uint_as_float(b); }
__device__ __forceinline__ float sum_x32(float x) { unsigned a = __float_as_uint(x), b = a; swap32(a, b); return __uint_as_float(a) + __uint_as_float(b); }
__device__ __forceinline__ float max_x16(float x) { unsigned a = __float_as_uint(x), b = a; swap16(a, b); return __builtin_fmaxf(__uint_as_float(a), __uint_as_float(b)); }
__device__ __forceinline__ float max_x32(float x) { unsigned a = __float_as_uint(x), b = a; swap32(a, b); return __builtin_fmaxf(__uint_as_float(a), __uint_as_float(b)); }
__device__ __forceinline__ void sum2_rows(float& s, float& q) {
    unsigned a = __float_as_uint(s), b = a, c = __float_as_uint(q), d = c;
    asm volatile("s_nop 2\n\tv_permlane16_swap_b32 %0, %1\n\tv_permlane16_swap_b32 %2, %3\n\tv_add_f32 %0, %0, %1\n\tv_add_f32 %2, %2, %3\n\tv_mov_b32 %1, %0\n\tv_mov_b32 %3, %2\n\t"
                 "s_nop 1\n\tv_permlane32_swap_b32 %0, %1\n\tv_permlane32_swap_b32 %2, %3\n\tv_add_f32 %0, %0, %1\n\tv_add_f32 %2, %2, %3" : "+v"(a), "+v"(b), "+v"(c), "+v"(d));
    s = __uint_as_float(a); q = __uint_as_float(c);
}
__device__ __forceinline__ u32x4 merge16(u32x2 a, u32x2 b) {
    unsigned a0 = a[0], a1 = a[1], b0 = b[0], b1 = b[1];
    asm volatile("s_nop 3\n\tv_permlane16_swap_b32 %0, %2\n\tv_permlane16_swap_b32 %1, %3\n\ts_nop 1" : "+v"(a0), "+v"(a1), "+v"(b0), "+v"(b1));
    return (u32x4){a0, a1, b0, b1};
}
template <int AUXV>
__device__ __forceinline__ void st128_(u32x4 v, rsrc_t r, unsigned voff, unsigned soff) {
    __builtin_amdgcn_raw_buffer_store_b128(v, r, voff, soff, AUXV);
    asm volatile("s_nop 1" :: "v"(v) : "memory");
}
#define st128(V, R, VO_, SO_) st128_<16>(V, R, VO_, SO_)
#define LD128(R, VO_, SO_) __builtin_amdgcn_raw_buffer_load_b128(R, VO_, SO_, 16)
#define LD64(R, VO_, SO_) __builtin_amdgcn_raw_buffer_load_b64(R, VO_, SO_, 16)
#define BF8(X) __builtin_bit_cast(bf16x8, X)
__device__ __forceinline__ float gelu_tanh(float x) {
    const float c0 = -2.0f * 0.7978845608028654f * 1.4426950408889634f, c1 = c0 * 0.044715f;
    const float e = __builtin_amdgcn_exp2f(x * __builtin_fmaf(x * x, c1, c0));
    return x * __builtin_amdgcn_rcpf(1.0f + e);
}
#define MFMA(A_, B_, C_) C_ = __builtin_amdgcn_mfma_f32_16x16x32_bf16(A_, B_, C_, 0, 0, 0)
#define SMF(A_, B_, C_) C_ = __builtin_amdgcn_smfmac_f32_16x16x64_bf16(A_, B_, C_, sidx, 0, 0)
__device__ __forceinline__ float ln_den(float var) { return rbf(__builtin_amdgcn_sqrtf(rbf(rbf(var) + 1e-5f))); }
__device__ __forceinline__ float rcp(float x) { return __builtin_amdgcn_rcpf(x); }

struct Params {
    char* ws;
    const bf16_t* mem0;
    const bf16_t* w[8];
    bf16_t* out;
};

#define RLX_AGENT __ATOMIC_RELAXED, __HIP_MEMORY_SCOPE_AGENT
#define SENT 0xFFFFFFFFu
constexpr int SPIN_CAP = 400000;
__device__ __forceinline__ bool spin_fail(int spins, const Params& P) {
    if ((spins & 1023) == 1023) {
        const rsrc_t rt = mkrsrc(P.ws + FL0, FL_BYTES);
        unsigned o;
        asm volatile("v_mov_b32 %0, 0x100" : "=v"(o));
        if (__builtin_amdgcn_readfirstlane(__builtin_amdgcn_raw_buffer_load_b32(rt, o, 0, 16)) != 0u) return true;
        if (spins > SPIN_CAP) { __builtin_amdgcn_raw_buffer_store_b32(o, rt, o, 0, 16); return true; }
    }
    return false;
}
__device__ __forceinline__ bool ok4(u32x4 v) { return v[0] != SENT && v[1] != SENT && v[2] != SENT && v[3] != SENT; }
__device__ __forceinline__ unsigned nosent(unsigned d) { return d < 0xFFFFFFFEu ? d : 0xFFFFFFFEu; }
__device__ __forceinline__ u32x4 nosent4(u32x4 v) { return (u32x4){nosent(v[0]), nosent(v[1]), nosent(v[2]), nosent(v[3])}; }
__device__ __forceinline__ unsigned umax3(unsigned a, unsigned b, unsigned c) { const unsigned m = a > b ? a : b; return m > c ? m : c; }
__device__ __forceinline__ unsigned umax4(unsigned m, u32x4 v) { return umax3(umax3(m, v[0], v[1]), v[2], v[3]); }
__device__ __forceinline__ u32x4 sent4() { unsigned sv; asm volatile("v_mov_b32 %0, -1" : "=v"(sv)); return (u32x4){sv, sv, sv, sv}; }
template <int N>
__device__ __forceinline__ void poll(rsrc_t rs, unsigned base, const unsigned (&off)[N], u32x4 (&v)[N], const Params& P) {
    for (int spins = 0;; ++spins) {
        unsigned m = 0;
#pragma unroll
        for (int k = 0; k < N; ++k) v[k] = LD128(rs, off[k], base);
#pragma unroll
        for (int k = 0; k < N; ++k) m = umax4(m, v[k]);
        if (__all(m != SENT)) return;
        asm volatile("" ::: "memory");
        if (spin_fail(spins, P)) return;
    }
}
__device__ __forceinline__ bf16x16 cat16(bf16x8 a, bf16x8 b) {
    return __builtin_shufflevector(a, b, 0, 1, 2, 3, 4, 5, 6, 7, 8, 9, 10, 11, 12, 13, 14, 15);
}
__device__ __forceinline__ bf16x8 half8(bf16x16 v, int h) {
    return h ? __builtin_shufflevector(v, v, 8, 9, 10, 11, 12, 13, 14, 15) : __builtin_shufflevector(v, v, 0, 1, 2, 3, 4, 5, 6, 7);
}
__device__ __forceinline__ bf16x16 lds_frag(unsigned a, unsigned step) { return cat16(lds_ld<bf16x8>(a), lds_ld<bf16x8>(a + step)); }
template <int I>
__device__ __forceinline__ void dmaq(const char* g, unsigned l16, unsigned dst) {
    asm volatile("s_mov_b32 m0, %2\n\ts_nop 0\n\tglobal_load_lds_dwordx4 %0, %1 offset:%3 sc1" :: "v"(l16), "s"(g), "s"(dst), "i"(I * 1024) : "memory");
}
__device__ __forceinline__ void dma4(const char* g, unsigned l16, unsigned dst) {
    asm volatile("s_mov_b32 m0, %2\n\ts_nop 0\n\tglobal_load_lds_dwordx4 %0, %1 sc1\n\tglobal_load_lds_dwordx4 %0, %1 offset:1024 sc1\n\t"
                 "global_load_lds_dwordx4 %0, %1 offset:2048 sc1\n\tglobal_load_lds_dwordx4 %0, %1 offset:3072 sc1" :: "v"(l16), "s"(g), "s"(dst) : "memory");
}
__device__ __forceinline__ unsigned toff(int row, int col, int par) {
    return pgb(row >> 5, par) + blin((unsigned)((row >> 4) & 1) * 20480u + (unsigned)(col >> 3) * 256u) + (unsigned)(row & 15) * 16u;
}

#define ASM_ST
#define ASM_ST_DECL
#define ASM_ST_OUT
#define ASM_ST_SAVE(k)
#define ASM_ST2
#define ASM_ST2_OUT
#define ASM_ST2_SAVE(k)
#define STDECL
#define ST(k)
#define STC(k)
#define STFLUSH(t)

#define LDECL
#define LA()
#define LB(t)
#define LF()
constexpr unsigned L_MEM = 0;
constexpr unsigned L_Q = 10240;
constexpr unsigned L_KM = 13312;
constexpr unsigned L_QX = 16384;
constexpr unsigned L_KX = 28672;
constexpr unsigned L_XB = 40960;
constexpr unsigned L_XA = 49152;
constexpr unsigned L_W = 81920;
constexpr int MEGA_LDS = 163840;
constexpr int wi(int i, int j) { return (i >> 1) * 5 + ((i & 1) ? 2 + j : j - 1); }

#define RPUB2
#define WNT " nt"
#define ASM_WLOAD3 \
    "s_xor_b32 %[t], %[wx], 0\n\t" \
    "s_mul_i32 %[t], %[t], 0xc0000\n\t" \
    "buffer_load_dwordx4 v[200:203], %[o0], %[rs], %[t] offen" WNT "\n\t" \
    "buffer_load_dwordx4 v[0:3], %[o1], %[rs], %[t] offen" WNT "\n\t" \
    "buffer_load_dwordx4 v[8:11], %[o2], %[rs], %[t] offen" WNT "\n\t" \
    "s_add_u32 %[t], %[t], 0x18000\n\t" \
    "buffer_load_dwordx4 v[16:19], %[o0], %[rs], %[t] offen" WNT "\n\t" \
    "buffer_load_dwordx4 v[24:27], %[o1], %[rs], %[t] offen" WNT "\n\t" \
    "buffer_load_dwordx4 v[32:35], %[o2], %[rs], %[t] offen" WNT "\n\t" \
    "s_add_u32 %[t], %[t], 0x18000\n\t" \
    "buffer_load_dwordx4 v[208:211], %[o0], %[rs], %[t] offen" WNT "\n\t" \
    "buffer_load_dwordx4 v[40:43], %[o1], %[rs], %[t] offen" WNT "\n\t" \
    "buffer_load_dwordx4 v[48:51], %[o2], %[rs], %[t] offen" WNT "\n\t" \
    "s_add_u32 %[t], %[t], 0x18000\n\t" \
    "buffer_load_dwordx4 v[56:59], %[o0], %[rs], %[t] offen" WNT "\n\t" \
    "buffer_load_dwordx4 v[64:67], %[o1], %[rs], %[t] offen" WNT "\n\t" \
    "buffer_load_dwordx4 v[72:75], %[o2], %[rs], %[t] offen" WNT "\n\t" \
    "s_add_u32 %[t], %[t], 0x18000\n\t" \
    "buffer_load_dwordx4 v[204:207], %[o0], %[rs], %[t] offen" WNT "\n\t" \
    "buffer_load_dwordx4 v[4:7], %[o1], %[rs], %[t] offen" WNT "\n\t" \
    "buffer_load_dwordx4 v[12:15], %[o2], %[rs], %[t] offen" WNT "\n\t" \
    "s_add_u32 %[t], %[t], 0x18000\n\t" \
    "buffer_load_dwordx4 v[20:23], %[o0], %[rs], %[t] offen" WNT "\n\t" \
    "buffer_load_dwordx4 v[28:31], %[o1], %[rs], %[t] offen" WNT "\n\t" \
    "buffer_load_dwordx4 v[36:39], %[o2], %[rs], %[t] offen" WNT "\n\t" \
    "s_add_u32 %[t], %[t], 0x18000\n\t" \
    "buffer_load_dwordx4 v[212:215], %[o0], %[rs], %[t] offen" WNT "\n\t" \
    "buffer_load_dwordx4 v[44:47], %[o1], %[rs], %[t] offen" WNT "\n\t" \
    "buffer_load_dwordx4 v[52:55], %[o2], %[rs], %[t] offen" WNT "\n\t" \
    "s_add_u32 %[t], %[t], 0x18000\n\t" \
    "buffer_load_dwordx4 v[60:63], %[o0], %[rs], %[t] offen" WNT "\n\t" \
    "buffer_load_dwordx4 v[68:71], %[o1], %[rs], %[t] offen" WNT "\n\t" \
    "buffer_load_dwordx4 v[76:79], %[o2], %[rs], %[t] offen" WNT "\n\t" \
    "s_add_u32 %[t], %[t], 0x18000\n\t" \
    "s_waitcnt vmcnt(21)\n\t" \
    "ds_write_b128 %[s0], v[200:203] offset:0\n\t" \
    "ds_write_b128 %[s1], v[0:3] offset:0\n\t" \
    "ds_write_b128 %[s2], v[8:11] offset:0\n\t" \
    "s_waitcnt vmcnt(18)\n\t" \
    "ds_write_b128 %[s0], v[16:19] offset:3456\n\t" \
    "ds_write_b128 %[s1], v[24:27] offset:3456\n\t" \
    "ds_write_b128 %[s2], v[32:35] offset:3456\n\t" \
    "s_waitcnt vmcnt(15)\n\t" \
    "ds_write_b128 %[s0], v[208:211] offset:6912\n\t" \
    "ds_write_b128 %[s1], v[40:43] offset:6912\n\t" \
    "ds_write_b128 %[s2], v[48:51] offset:6912\n\t" \
    "s_waitcnt vmcnt(12)\n\t" \
    "ds_write_b128 %[s0], v[56:59] offset:10368\n\t" \
    "ds_write_b128 %[s1], v[64:67] offset:10368\n\t" \
    "ds_write_b128 %[s2], v[72:75] offset:10368\n\t" \
    "s_waitcnt lgkmcnt(0)\n\t" \
    "s_barrier \n\t" \
    "ds_read_b64_tr_b16 v[200:201], %[t0] offset:0\n\t" \
    "ds_read_b64_tr_b16 v[202:203], %[t0] offset:1664\n\t" \
    "ds_read_b64_tr_b16 v[0:1], %[t0] offset:128\n\t" \
    "ds_read_b64_tr_b16 v[2:3], %[t0] offset:1792\n\t" \
    "ds_read_b64_tr_b16 v[8:9], %[t0] offset:256\n\t" \
    "ds_read_b64_tr_b16 v[10:11], %[t0] offset:1920\n\t" \
    "ds_read_b64_tr_b16 v[16:17], %[t1] offset:0\n\t" \
    "ds_read_b64_tr_b16 v[18:19], %[t1] offset:1664\n\t" \
    "ds_read_b64_tr_b16 v[24:25], %[t1] offset:128\n\t" \
    "ds_read_b64_tr_b16 v[26:27], %[t1] offset:1792\n\t" \
    "ds_read_b64_tr_b16 v[32:33], %[t1] offset:256\n\t" \
    "ds_read_b64_tr_b16 v[34:35], %[t1] offset:1920\n\t" \
    "ds_read_b64_tr_b16 v[208:209], %[t2] offset:0\n\t" \
    "ds_read_b64_tr_b16 v[210:211], %[t2] offset:1664\n\t" \
    "ds_read_b64_tr_b16 v[40:41], %[t2] offset:128\n\t" \
    "ds_read_b64_tr_b16 v[42:43], %[t2] offset:1792\n\t" \
    "ds_read_b64_tr_b16 v[48:49], %[t2] offset:256\n\t" \
    "ds_read_b64_tr_b16 v[50:51], %[t2] offset:1920\n\t" \
    "ds_read_b64_tr_b16 v[56:57], %[t3] offset:0\n\t" \
    "ds_read_b64_tr_b16 v[58:59], %[t3] offset:1664\n\t" \
    "ds_read_b64_tr_b16 v[64:65], %[t3] offset:128\n\t" \
    "ds_read_b64_tr_b16 v[66:67], %[t3] offset:1792\n\t" \
    "ds_read_b64_tr_b16 v[72:73], %[t3] offset:256\n\t" \
    "ds_read_b64_tr_b16 v[74:75], %[t3] offset:1920\n\t" \
    "s_xor_b32 %[t], %[wx], 4\n\t" \
    "s_mul_i32 %[t], %[t], 0xc0000\n\t" \
    "buffer_load_dwordx4 v[216:219], %[o0], %[rs], %[t] offen" WNT "\n\t" \
    "buffer_load_dwordx4 v[80:83], %[o1], %[rs], %[t] offen" WNT "\n\t" \
    "buffer_load_dwordx4 v[88:91], %[o2], %[rs], %[t] offen" WNT "\n\t" \
    "s_add_u32 %[t], %[t], 0x18000\n\t" \
    "buffer_load_dwordx4 v[96:99], %[o0], %[rs], %[t] offen" WNT "\n\t" \
    "buffer_load_dwordx4 v[104:107], %[o1], %[rs], %[t] offen" WNT "\n\t" \
    "buffer_load_dwordx4 v[112:115], %[o2], %[rs], %[t] offen" WNT "\n\t" \
    "s_add_u32 %[t], %[t], 0x18000\n\t" \
    "buffer_load_dwordx4 v[224:227], %[o0], %[rs], %[t] offen" WNT "\n\t" \
    "buffer_load_dwordx4 v[120:123], %[o1], %[rs], %[t] offen" WNT "\n\t" \
    "buffer_load_dwordx4 v[128:131], %[o2], %[rs], %[t] offen" WNT "\n\t" \
    "s_add_u32 %[t], %[t], 0x18000\n\t" \
    "buffer_load_dwordx4 v[136:139], %[o0], %[rs], %[t] offen" WNT "\n\t" \
    "buffer_load_dwordx4 v[144:147], %[o1], %[rs], %[t] offen" WNT "\n\t" \
    "buffer_load_dwordx4 v[152:155], %[o2], %[rs], %[t] offen" WNT "\n\t" \
    "s_add_u32 %[t], %[t], 0x18000\n\t" \
    "s_waitcnt lgkmcnt(0)\n\t" \
    "s_barrier \n\t" \
    "s_waitcnt vmcnt(21)\n\t" \
    "ds_write_b128 %[s0], v[204:207] offset:0\n\t" \
    "ds_write_b128 %[s1], v[4:7] offset:0\n\t" \
    "ds_write_b128 %[s2], v[12:15] offset:0\n\t" \
    "s_waitcnt vmcnt(18)\n\t" \
    "ds_write_b128 %[s0], v[20:23] offset:3456\n\t" \
    "ds_write_b128 %[s1], v[28:31] offset:3456\n\t" \
    "ds_write_b128 %[s2], v[36:39] offset:3456\n\t" \
    "s_waitcnt vmcnt(15)\n\t" \
    "ds_write_b128 %[s0], v[212:215] offset:6912\n\t" \
    "ds_write_b128 %[s1], v[44:47] offset:6912\n\t" \
    "ds_write_b128 %[s2], v[52:55] offset:6912\n\t" \
    "s_waitcnt vmcnt(12)\n\t" \
    "ds_write_b128 %[s0], v[60:63] offset:10368\n\t" \
    "ds_write_b128 %[s1], v[68:71] offset:10368\n\t" \
    "ds_write_b128 %[s2], v[76:79] offset:10368\n\t" \
    "s_waitcnt lgkmcnt(0)\n\t" \
    "s_barrier \n\t" \
    "ds_read_b64_tr_b16 v[204:205], %[t0] offset:0\n\t" \
    "ds_read_b64_tr_b16 v[206:207], %[t0] offset:1664\n\t" \
    "ds_read_b64_tr_b16 v[4:5], %[t0] offset:128\n\t" \
    "ds_read_b64_tr_b16 v[6:7], %[t0] offset:1792\n\t" \
    "ds_read_b64_tr_b16 v[12:13], %[t0] offset:256\n\t" \
    "ds_read_b64_tr_b16 v[14:15], %[t0] offset:1920\n\t" \
    "ds_read_b64_tr_b16 v[20:21], %[t1] offset:0\n\t" \
    "ds_read_b64_tr_b16 v[22:23], %[t1] offset:1664\n\t" \
    "ds_read_b64_tr_b16 v[28:29], %[t1] offset:128\n\t" \
    "ds_read_b64_tr_b16 v[30:31], %[t1] offset:1792\n\t" \
    "ds_read_b64_tr_b16 v[36:37], %[t1] offset:256\n\t" \
    "ds_read_b64_tr_b16 v[38:39], %[t1] offset:1920\n\t" \
    "ds_read_b64_tr_b16 v[212:213], %[t2] offset:0\n\t" \
    "ds_read_b64_tr_b16 v[214:215], %[t2] offset:1664\n\t" \
    "ds_read_b64_tr_b16 v[44:45], %[t2] offset:128\n\t" \
    "ds_read_b64_tr_b16 v[46:47], %[t2] offset:1792\n\t" \
    "ds_read_b64_tr_b16 v[52:53], %[t2] offset:256\n\t" \
    "ds_read_b64_tr_b16 v[54:55], %[t2] offset:1920\n\t" \
    "ds_read_b64_tr_b16 v[60:61], %[t3] offset:0\n\t" \
    "ds_read_b64_tr_b16 v[62:63], %[t3] offset:1664\n\t" \
    "ds_read_b64_tr_b16 v[68:69], %[t3] offset:128\n\t" \
    "ds_read_b64_tr_b16 v[70:71], %[t3] offset:1792\n\t" \
    "ds_read_b64_tr_b16 v[76:77], %[t3] offset:256\n\t" \
    "ds_read_b64_tr_b16 v[78:79], %[t3] offset:1920\n\t" \
    "buffer_load_dwordx4 v[220:223], %[o0], %[rs], %[t] offen" WNT "\n\t" \
    "buffer_load_dwordx4 v[84:87], %[o1], %[rs], %[t] offen" WNT "\n\t" \
    "buffer_load_dwordx4 v[92:95], %[o2], %[rs], %[t] offen" WNT "\n\t" \
    "s_add_u32 %[t], %[t], 0x18000\n\t" \
    "buffer_load_dwordx4 v[100:103], %[o0], %[rs], %[t] offen" WNT "\n\t" \
    "buffer_load_dwordx4 v[108:111], %[o1], %[rs], %[t] offen" WNT "\n\t" \
    "buffer_load_dwordx4 v[116:119], %[o2], %[rs], %[t] offen" WNT "\n\t" \
    "s_add_u32 %[t], %[t], 0x18000\n\t" \
    "buffer_load_dwordx4 v[228:231], %[o0], %[rs], %[t] offen" WNT "\n\t" \
    "buffer_load_dwordx4 v[124:127], %[o1], %[rs], %[t] offen" WNT "\n\t" \
    "buffer_load_dwordx4 v[132:135], %[o2], %[rs], %[t] offen" WNT "\n\t" \
    "s_add_u32 %[t], %[t], 0x18000\n\t" \
    "buffer_load_dwordx4 v[140:143], %[o0], %[rs], %[t] offen" WNT "\n\t" \
    "buffer_load_dwordx4 v[148:151], %[o1], %[rs], %[t] offen" WNT "\n\t" \
    "buffer_load_dwordx4 v[156:159], %[o2], %[rs], %[t] offen" WNT "\n\t" \
    "s_add_u32 %[t], %[t], 0x18000\n\t" \
    "s_waitcnt lgkmcnt(0)\n\t" \
    "s_barrier \n\t" \
    "s_waitcnt vmcnt(21)\n\t" \
    "ds_write_b128 %[s0], v[216:219] offset:0\n\t" \
    "ds_write_b128 %[s1], v[80:83] offset:0\n\t" \
    "ds_write_b128 %[s2], v[88:91] offset:0\n\t" \
    "s_waitcnt vmcnt(18)\n\t" \
    "ds_write_b128 %[s0], v[96:99] offset:3456\n\t" \
    "ds_write_b128 %[s1], v[104:107] offset:3456\n\t" \
    "ds_write_b128 %[s2], v[112:115] offset:3456\n\t" \
    "s_waitcnt vmcnt(15)\n\t" \
    "ds_write_b128 %[s0], v[224:227] offset:6912\n\t" \
    "ds_write_b128 %[s1], v[120:123] offset:6912\n\t" \
    "ds_write_b128 %[s2], v[128:131] offset:6912\n\t" \
    "s_waitcnt vmcnt(12)\n\t" \
    "ds_write_b128 %[s0], v[136:139] offset:10368\n\t" \
    "ds_write_b128 %[s1], v[144:147] offset:10368\n\t" \
    "ds_write_b128 %[s2], v[152:155] offset:10368\n\t" \
    "s_waitcnt lgkmcnt(0)\n\t" \
    "s_barrier \n\t" \
    "ds_read_b64_tr_b16 v[216:217], %[t0] offset:0\n\t" \
    "ds_read_b64_tr_b16 v[218:219], %[t0] offset:1664\n\t" \
    "ds_read_b64_tr_b16 v[80:81], %[t0] offset:128\n\t" \
    "ds_read_b64_tr_b16 v[82:83], %[t0] offset:1792\n\t" \
    "ds_read_b64_tr_b16 v[88:89], %[t0] offset:256\n\t" \
    "ds_read_b64_tr_b16 v[90:91], %[t0] offset:1920\n\t" \
    "ds_read_b64_tr_b16 v[96:97], %[t1] offset:0\n\t" \
    "ds_read_b64_tr_b16 v[98:99], %[t1] offset:1664\n\t" \
    "ds_read_b64_tr_b16 v[104:105], %[t1] offset:128\n\t" \
    "ds_read_b64_tr_b16 v[106:107], %[t1] offset:1792\n\t" \
    "ds_read_b64_tr_b16 v[112:113], %[t1] offset:256\n\t" \
    "ds_read_b64_tr_b16 v[114:115], %[t1] offset:1920\n\t" \
    "ds_read_b64_tr_b16 v[224:225], %[t2] offset:0\n\t" \
    "ds_read_b64_tr_b16 v[226:227], %[t2] offset:1664\n\t" \
    "ds_read_b64_tr_b16 v[120:121], %[t2] offset:128\n\t" \
    "ds_read_b64_tr_b16 v[122:123], %[t2] offset:1792\n\t" \
    "ds_read_b64_tr_b16 v[128:129], %[t2] offset:256\n\t" \
    "ds_read_b64_tr_b16 v[130:131], %[t2] offset:1920\n\t" \
    "ds_read_b64_tr_b16 v[136:137], %[t3] offset:0\n\t" \
    "ds_read_b64_tr_b16 v[138:139], %[t3] offset:1664\n\t" \
    "ds_read_b64_tr_b16 v[144:145], %[t3] offset:128\n\t" \
    "ds_read_b64_tr_b16 v[146:147], %[t3] offset:1792\n\t" \
    "ds_read_b64_tr_b16 v[152:153], %[t3] offset:256\n\t" \
    "ds_read_b64_tr_b16 v[154:155], %[t3] offset:1920\n\t" \
    "s_and_b32 %[t], %[wx], 3\n\t" \
    "s_mul_i32 %[t], %[t], 0x60000\n\t" \
    "s_add_u32 %[t], %[t], 0x600000\n\t" \
    "buffer_load_dwordx4 v[232:235], %[o0], %[rs], %[t] offen" WNT "\n\t" \
    "buffer_load_dwordx4 v[160:163], %[o1], %[rs], %[t] offen" WNT "\n\t" \
    "buffer_load_dwordx4 v[168:171], %[o2], %[rs], %[t] offen" WNT "\n\t" \
    "s_add_u32 %[t], %[t], 0x18000\n\t" \
    "buffer_load_dwordx4 v[236:239], %[o0], %[rs], %[t] offen" WNT "\n\t" \
    "buffer_load_dwordx4 v[164:167], %[o1], %[rs], %[t] offen" WNT "\n\t" \
    "buffer_load_dwordx4 v[172:175], %[o2], %[rs], %[t] offen" WNT "\n\t" \
    "s_add_u32 %[t], %[t], 0x18000\n\t" \
    "buffer_load_dwordx4 v[176:179], %[o0], %[rs], %[t] offen" WNT "\n\t" \
    "buffer_load_dwordx4 v[184:187], %[o1], %[rs], %[t] offen" WNT "\n\t" \
    "buffer_load_dwordx4 v[192:195], %[o2], %[rs], %[t] offen" WNT "\n\t" \
    "s_add_u32 %[t], %[t], 0x18000\n\t" \
    "buffer_load_dwordx4 v[180:183], %[o0], %[rs], %[t] offen" WNT "\n\t" \
    "buffer_load_dwordx4 v[188:191], %[o1], %[rs], %[t] offen" WNT "\n\t" \
    "buffer_load_dwordx4 v[196:199], %[o2], %[rs], %[t] offen" WNT "\n\t" \
    "s_add_u32 %[t], %[t], 0x18000\n\t" \
    "s_waitcnt lgkmcnt(0)\n\t" \
    "s_barrier \n\t" \
    "s_waitcnt vmcnt(21)\n\t" \
    "ds_write_b128 %[s0], v[220:223] offset:0\n\t" \
    "ds_write_b128 %[s1], v[84:87] offset:0\n\t" \
    "ds_write_b128 %[s2], v[92:95] offset:0\n\t" \
    "s_waitcnt vmcnt(18)\n\t" \
    "ds_write_b128 %[s0], v[100:103] offset:3456\n\t" \
    "ds_write_b128 %[s1], v[108:111] offset:3456\n\t" \
    "ds_write_b128 %[s2], v[116:119] offset:3456\n\t" \
    "s_waitcnt vmcnt(15)\n\t" \
    "ds_write_b128 %[s0], v[228:231] offset:6912\n\t" \
    "ds_write_b128 %[s1], v[124:127] offset:6912\n\t" \
    "ds_write_b128 %[s2], v[132:135] offset:6912\n\t" \
    "s_waitcnt vmcnt(12)\n\t" \
    "ds_write_b128 %[s0], v[140:143] offset:10368\n\t" \
    "ds_write_b128 %[s1], v[148:151] offset:10368\n\t" \
    "ds_write_b128 %[s2], v[156:159] offset:10368\n\t" \
    "s_waitcnt lgkmcnt(0)\n\t" \
    "s_barrier \n\t" \
    "ds_read_b64_tr_b16 v[220:221], %[t0] offset:0\n\t" \
    "ds_read_b64_tr_b16 v[222:223], %[t0] offset:1664\n\t" \
    "ds_read_b64_tr_b16 v[84:85], %[t0] offset:128\n\t" \
    "ds_read_b64_tr_b16 v[86:87], %[t0] offset:1792\n\t" \
    "ds_read_b64_tr_b16 v[92:93], %[t0] offset:256\n\t" \
    "ds_read_b64_tr_b16 v[94:95], %[t0] offset:1920\n\t" \
    "ds_read_b64_tr_b16 v[100:101], %[t1] offset:0\n\t" \
    "ds_read_b64_tr_b16 v[102:103], %[t1] offset:1664\n\t" \
    "ds_read_b64_tr_b16 v[108:109], %[t1] offset:128\n\t" \
    "ds_read_b64_tr_b16 v[110:111], %[t1] offset:1792\n\t" \
    "ds_read_b64_tr_b16 v[116:117], %[t1] offset:256\n\t" \
    "ds_read_b64_tr_b16 v[118:119], %[t1] offset:1920\n\t" \
    "ds_read_b64_tr_b16 v[228:229], %[t2] offset:0\n\t" \
    "ds_read_b64_tr_b16 v[230:231], %[t2] offset:1664\n\t" \
    "ds_read_b64_tr_b16 v[124:125], %[t2] offset:128\n\t" \
    "ds_read_b64_tr_b16 v[126:127], %[t2] offset:1792\n\t" \
    "ds_read_b64_tr_b16 v[132:133], %[t2] offset:256\n\t" \
    "ds_read_b64_tr_b16 v[134:135], %[t2] offset:1920\n\t" \
    "ds_read_b64_tr_b16 v[140:141], %[t3] offset:0\n\t" \
    "ds_read_b64_tr_b16 v[142:143], %[t3] offset:1664\n\t" \
    "ds_read_b64_tr_b16 v[148:149], %[t3] offset:128\n\t" \
    "ds_read_b64_tr_b16 v[150:151], %[t3] offset:1792\n\t" \
    "ds_read_b64_tr_b16 v[156:157], %[t3] offset:256\n\t" \
    "ds_read_b64_tr_b16 v[158:159], %[t3] offset:1920\n\t" \
    "s_waitcnt lgkmcnt(0)\n\t" \
    "s_barrier \n\t" \
    "s_waitcnt vmcnt(9)\n\t" \
    "ds_write_b128 %[s0], v[232:235] offset:0\n\t" \
    "ds_write_b128 %[s1], v[160:163] offset:0\n\t" \
    "ds_write_b128 %[s2], v[168:171] offset:0\n\t" \
    "s_waitcnt vmcnt(6)\n\t" \
    "ds_write_b128 %[s0], v[236:239] offset:3456\n\t" \
    "ds_write_b128 %[s1], v[164:167] offset:3456\n\t" \
    "ds_write_b128 %[s2], v[172:175] offset:3456\n\t" \
    "s_waitcnt vmcnt(3)\n\t" \
    "ds_write_b128 %[s0], v[176:179] offset:6912\n\t" \
    "ds_write_b128 %[s1], v[184:187] offset:6912\n\t" \
    "ds_write_b128 %[s2], v[192:195] offset:6912\n\t" \
    "s_waitcnt vmcnt(0)\n\t" \
    "ds_write_b128 %[s0], v[180:183] offset:10368\n\t" \
    "ds_write_b128 %[s1], v[188:191] offset:10368\n\t" \
    "ds_write_b128 %[s2], v[196:199] offset:10368\n\t" \
    "s_waitcnt lgkmcnt(0)\n\t" \
    "s_barrier \n\t" \
    "ds_read_b64_tr_b16 v[232:233], %[t4] offset:0\n\t" \
    "ds_read_b64_tr_b16 v[234:235], %[t4] offset:1664\n\t" \
    "ds_read_b64_tr_b16 v[160:161], %[t4] offset:128\n\t" \
    "ds_read_b64_tr_b16 v[162:163], %[t4] offset:1792\n\t" \
    "ds_read_b64_tr_b16 v[168:169], %[t4] offset:256\n\t" \
    "ds_read_b64_tr_b16 v[170:171], %[t4] offset:1920\n\t" \
    "ds_read_b64_tr_b16 v[236:237], %[t4] offset:13824\n\t" \
    "ds_read_b64_tr_b16 v[238:239], %[t4] offset:15488\n\t" \
    "ds_read_b64_tr_b16 v[164:165], %[t4] offset:13952\n\t" \
    "ds_read_b64_tr_b16 v[166:167], %[t4] offset:15616\n\t" \
    "ds_read_b64_tr_b16 v[172:173], %[t4] offset:14080\n\t" \
    "ds_read_b64_tr_b16 v[174:175], %[t4] offset:15744\n\t" \
    "ds_read_b64_tr_b16 v[176:177], %[t4] offset:27648\n\t" \
    "ds_read_b64_tr_b16 v[178:179], %[t4] offset:29312\n\t" \
    "ds_read_b64_tr_b16 v[184:185], %[t4] offset:27776\n\t" \
    "ds_read_b64_tr_b16 v[186:187], %[t4] offset:29440\n\t" \
    "ds_read_b64_tr_b16 v[192:193], %[t4] offset:27904\n\t" \
    "ds_read_b64_tr_b16 v[194:195], %[t4] offset:29568\n\t" \
    "ds_read_b64_tr_b16 v[180:181], %[t4] offset:41472\n\t" \
    "ds_read_b64_tr_b16 v[182:183], %[t4] offset:43136\n\t" \
    "ds_read_b64_tr_b16 v[188:189], %[t4] offset:41600\n\t" \
    "ds_read_b64_tr_b16 v[190:191], %[t4] offset:43264\n\t" \
    "ds_read_b64_tr_b16 v[196:197], %[t4] offset:41728\n\t" \
    "ds_read_b64_tr_b16 v[198:199], %[t4] offset:43392\n\t" \
    "s_waitcnt lgkmcnt(0)\n\t" \
    "s_barrier \n\t" \
    "ds_write_b128 %[wl], v[200:203] offset:0\n\t" \
    "ds_write_b128 %[wl], v[204:207] offset:1024\n\t" \
    "ds_write_b128 %[wl], v[208:211] offset:2048\n\t" \
    "ds_write_b128 %[wl], v[212:215] offset:3072\n\t" \
    "ds_write_b128 %[wl], v[216:219] offset:4096\n\t" \
    "ds_write_b128 %[wl], v[220:223] offset:5120\n\t" \
    "ds_write_b128 %[wl], v[224:227] offset:6144\n\t" \
    "ds_write_b128 %[wl], v[228:231] offset:7168\n\t" \
    "ds_write_b128 %[wl], v[232:235] offset:8192\n\t" \
    "ds_write_b128 %[wl], v[236:239] offset:9216\n\t" \
    "s_waitcnt lgkmcnt(0)\n\t"
#define ASM_WLOADO \
    "s_mov_b32 %[t], 0x0\n\t" \
    "buffer_load_dwordx4 v[200:203], %[o0], %[rs], %[t] offen" WNT "\n\t" \
    "buffer_load_dwordx4 v[16:19], %[o1], %[rs], %[t] offen" WNT "\n\t" \
    "buffer_load_dwordx4 v[208:211], %[o2], %[rs], %[t] offen" WNT "\n\t" \
    "buffer_load_dwordx4 v[56:59], %[o3], %[rs], %[t] offen" WNT "\n\t" \
    "buffer_load_dwordx4 v[216:219], %[o4], %[rs], %[t] offen" WNT "\n\t" \
    "s_mov_b32 %[t], 0xa000\n\t" \
    "buffer_load_dwordx4 v[96:99], %[o0], %[rs], %[t] offen" WNT "\n\t" \
    "buffer_load_dwordx4 v[224:227], %[o1], %[rs], %[t] offen" WNT "\n\t" \
    "buffer_load_dwordx4 v[136:139], %[o2], %[rs], %[t] offen" WNT "\n\t" \
    "buffer_load_dwordx4 v[232:235], %[o3], %[rs], %[t] offen" WNT "\n\t" \
    "buffer_load_dwordx4 v[176:179], %[o4], %[rs], %[t] offen" WNT "\n\t" \
    "s_mov_b32 %[t], 0x14000\n\t" \
    "buffer_load_dwordx4 v[204:207], %[o0], %[rs], %[t] offen" WNT "\n\t" \
    "buffer_load_dwordx4 v[20:23], %[o1], %[rs], %[t] offen" WNT "\n\t" \
    "buffer_load_dwordx4 v[212:215], %[o2], %[rs], %[t] offen" WNT "\n\t" \
    "buffer_load_dwordx4 v[60:63], %[o3], %[rs], %[t] offen" WNT "\n\t" \
    "buffer_load_dwordx4 v[220:223], %[o4], %[rs], %[t] offen" WNT "\n\t" \
    "s_mov_b32 %[t], 0x1e000\n\t" \
    "buffer_load_dwordx4 v[100:103], %[o0], %[rs], %[t] offen" WNT "\n\t" \
    "buffer_load_dwordx4 v[228:231], %[o1], %[rs], %[t] offen" WNT "\n\t" \
    "buffer_load_dwordx4 v[140:143], %[o2], %[rs], %[t] offen" WNT "\n\t" \
    "buffer_load_dwordx4 v[236:239], %[o3], %[rs], %[t] offen" WNT "\n\t" \
    "buffer_load_dwordx4 v[180:183], %[o4], %[rs], %[t] offen" WNT "\n\t" \
    "s_waitcnt vmcnt(15)\n\t" \
    "ds_write_b128 %[s0], v[200:203] offset:0\n\t" \
    "ds_write_b128 %[s1], v[16:19] offset:0\n\t" \
    "ds_write_b128 %[s2], v[208:211] offset:0\n\t" \
    "ds_write_b128 %[s3], v[56:59] offset:0\n\t" \
    "ds_write_b128 %[s4], v[216:219] offset:0\n\t" \
    "s_waitcnt vmcnt(10)\n\t" \
    "ds_write_b128 %[s0], v[96:99] offset:5632\n\t" \
    "ds_write_b128 %[s1], v[224:227] offset:5632\n\t" \
    "ds_write_b128 %[s2], v[136:139] offset:5632\n\t" \
    "ds_write_b128 %[s3], v[232:235] offset:5632\n\t" \
    "ds_write_b128 %[s4], v[176:179] offset:5632\n\t" \
    "ds_read_b64_tr_b16 v[200:201], %[tr] offset:0\n\t" \
    "ds_read_b64_tr_b16 v[202:203], %[tr] offset:5632\n\t" \
    "ds_read_b64_tr_b16 v[16:17], %[tr] offset:32\n\t" \
    "ds_read_b64_tr_b16 v[18:19], %[tr] offset:5664\n\t" \
    "ds_read_b64_tr_b16 v[208:209], %[tr] offset:64\n\t" \
    "ds_read_b64_tr_b16 v[210:211], %[tr] offset:5696\n\t" \
    "ds_read_b64_tr_b16 v[56:57], %[tr] offset:96\n\t" \
    "ds_read_b64_tr_b16 v[58:59], %[tr] offset:5728\n\t" \
    "ds_read_b64_tr_b16 v[216:217], %[tr] offset:128\n\t" \
    "ds_read_b64_tr_b16 v[218:219], %[tr] offset:5760\n\t" \
    "ds_read_b64_tr_b16 v[96:97], %[tr] offset:160\n\t" \
    "ds_read_b64_tr_b16 v[98:99], %[tr] offset:5792\n\t" \
    "ds_read_b64_tr_b16 v[224:225], %[tr] offset:192\n\t" \
    "ds_read_b64_tr_b16 v[226:227], %[tr] offset:5824\n\t" \
    "ds_read_b64_tr_b16 v[136:137], %[tr] offset:224\n\t" \
    "ds_read_b64_tr_b16 v[138:139], %[tr] offset:5856\n\t" \
    "ds_read_b64_tr_b16 v[232:233], %[tr] offset:256\n\t" \
    "ds_read_b64_tr_b16 v[234:235], %[tr] offset:5888\n\t" \
    "ds_read_b64_tr_b16 v[176:177], %[tr] offset:288\n\t" \
    "ds_read_b64_tr_b16 v[178:179], %[tr] offset:5920\n\t" \
    "s_barrier \n\t" \
    "s_mov_b32 %[t], 0x28000\n\t" \
    "buffer_load_dwordx4 v[0:3], %[o0], %[rs], %[t] offen" WNT "\n\t" \
    "buffer_load_dwordx4 v[24:27], %[o1], %[rs], %[t] offen" WNT "\n\t" \
    "buffer_load_dwordx4 v[40:43], %[o2], %[rs], %[t] offen" WNT "\n\t" \
    "buffer_load_dwordx4 v[64:67], %[o3], %[rs], %[t] offen" WNT "\n\t" \
    "buffer_load_dwordx4 v[80:83], %[o4], %[rs], %[t] offen" WNT "\n\t" \
    "s_mov_b32 %[t], 0x32000\n\t" \
    "buffer_load_dwordx4 v[104:107], %[o0], %[rs], %[t] offen" WNT "\n\t" \
    "buffer_load_dwordx4 v[120:123], %[o1], %[rs], %[t] offen" WNT "\n\t" \
    "buffer_load_dwordx4 v[144:147], %[o2], %[rs], %[t] offen" WNT "\n\t" \
    "buffer_load_dwordx4 v[160:163], %[o3], %[rs], %[t] offen" WNT "\n\t" \
    "buffer_load_dwordx4 v[184:187], %[o4], %[rs], %[t] offen" WNT "\n\t" \
    "s_waitcnt lgkmcnt(0)\n\t" \
    "s_barrier \n\t" \
    "s_waitcnt vmcnt(15)\n\t" \
    "ds_write_b128 %[s0], v[204:207] offset:0\n\t" \
    "ds_write_b128 %[s1], v[20:23] offset:0\n\t" \
    "ds_write_b128 %[s2], v[212:215] offset:0\n\t" \
    "ds_write_b128 %[s3], v[60:63] offset:0\n\t" \
    "ds_write_b128 %[s4], v[220:223] offset:0\n\t" \
    "s_waitcnt vmcnt(10)\n\t" \
    "ds_write_b128 %[s0], v[100:103] offset:5632\n\t" \
    "ds_write_b128 %[s1], v[228:231] offset:5632\n\t" \
    "ds_write_b128 %[s2], v[140:143] offset:5632\n\t" \
    "ds_write_b128 %[s3], v[236:239] offset:5632\n\t" \
    "ds_write_b128 %[s4], v[180:183] offset:5632\n\t" \
    "ds_read_b64_tr_b16 v[204:205], %[tr] offset:0\n\t" \
    "ds_read_b64_tr_b16 v[206:207], %[tr] offset:5632\n\t" \
    "ds_read_b64_tr_b16 v[20:21], %[tr] offset:32\n\t" \
    "ds_read_b64_tr_b16 v[22:23], %[tr] offset:5664\n\t" \
    "ds_read_b64_tr_b16 v[212:213], %[tr] offset:64\n\t" \
    "ds_read_b64_tr_b16 v[214:215], %[tr] offset:5696\n\t" \
    "ds_read_b64_tr_b16 v[60:61], %[tr] offset:96\n\t" \
    "ds_read_b64_tr_b16 v[62:63], %[tr] offset:5728\n\t" \
    "ds_read_b64_tr_b16 v[220:221], %[tr] offset:128\n\t" \
    "ds_read_b64_tr_b16 v[222:223], %[tr] offset:5760\n\t" \
    "ds_read_b64_tr_b16 v[100:101], %[tr] offset:160\n\t" \
    "ds_read_b64_tr_b16 v[102:103], %[tr] offset:5792\n\t" \
    "ds_read_b64_tr_b16 v[228:229], %[tr] offset:192\n\t" \
    "ds_read_b64_tr_b16 v[230:231], %[tr] offset:5824\n\t" \
    "ds_read_b64_tr_b16 v[140:141], %[tr] offset:224\n\t" \
    "ds_read_b64_tr_b16 v[142:143], %[tr] offset:5856\n\t" \
    "ds_read_b64_tr_b16 v[236:237], %[tr] offset:256\n\t" \
    "ds_read_b64_tr_b16 v[238:239], %[tr] offset:5888\n\t" \
    "ds_read_b64_tr_b16 v[180:181], %[tr] offset:288\n\t" \
    "ds_read_b64_tr_b16 v[182:183], %[tr] offset:5920\n\t" \
    "s_barrier \n\t" \
    "s_mov_b32 %[t], 0x5000\n\t" \
    "buffer_load_dwordx4 v[4:7], %[o0], %[rs], %[t] offen" WNT "\n\t" \
    "buffer_load_dwordx4 v[28:31], %[o1], %[rs], %[t] offen" WNT "\n\t" \
    "buffer_load_dwordx4 v[44:47], %[o2], %[rs], %[t] offen" WNT "\n\t" \
    "buffer_load_dwordx4 v[68:71], %[o3], %[rs], %[t] offen" WNT "\n\t" \
    "buffer_load_dwordx4 v[84:87], %[o4], %[rs], %[t] offen" WNT "\n\t" \
    "s_mov_b32 %[t], 0xf000\n\t" \
    "buffer_load_dwordx4 v[108:111], %[o0], %[rs], %[t] offen" WNT "\n\t" \
    "buffer_load_dwordx4 v[124:127], %[o1], %[rs], %[t] offen" WNT "\n\t" \
    "buffer_load_dwordx4 v[148:151], %[o2], %[rs], %[t] offen" WNT "\n\t" \
    "buffer_load_dwordx4 v[164:167], %[o3], %[rs], %[t] offen" WNT "\n\t" \
    "buffer_load_dwordx4 v[188:191], %[o4], %[rs], %[t] offen" WNT "\n\t" \
    "s_waitcnt lgkmcnt(0)\n\t" \
    "s_barrier \n\t" \
    "s_waitcnt vmcnt(15)\n\t" \
    "ds_write_b128 %[s0], v[0:3] offset:0\n\t" \
    "ds_write_b128 %[s1], v[24:27] offset:0\n\t" \
    "ds_write_b128 %[s2], v[40:43] offset:0\n\t" \
    "ds_write_b128 %[s3], v[64:67] offset:0\n\t" \
    "ds_write_b128 %[s4], v[80:83] offset:0\n\t" \
    "s_waitcnt vmcnt(10)\n\t" \
    "ds_write_b128 %[s0], v[104:107] offset:5632\n\t" \
    "ds_write_b128 %[s1], v[120:123] offset:5632\n\t" \
    "ds_write_b128 %[s2], v[144:147] offset:5632\n\t" \
    "ds_write_b128 %[s3], v[160:163] offset:5632\n\t" \
    "ds_write_b128 %[s4], v[184:187] offset:5632\n\t" \
    "ds_read_b64_tr_b16 v[0:1], %[tr] offset:0\n\t" \
    "ds_read_b64_tr_b16 v[2:3], %[tr] offset:5632\n\t" \
    "ds_read_b64_tr_b16 v[24:25], %[tr] offset:32\n\t" \
    "ds_read_b64_tr_b16 v[26:27], %[tr] offset:5664\n\t" \
    "ds_read_b64_tr_b16 v[40:41], %[tr] offset:64\n\t" \
    "ds_read_b64_tr_b16 v[42:43], %[tr] offset:5696\n\t" \
    "ds_read_b64_tr_b16 v[64:65], %[tr] offset:96\n\t" \
    "ds_read_b64_tr_b16 v[66:67], %[tr] offset:5728\n\t" \
    "ds_read_b64_tr_b16 v[80:81], %[tr] offset:128\n\t" \
    "ds_read_b64_tr_b16 v[82:83], %[tr] offset:5760\n\t" \
    "ds_read_b64_tr_b16 v[104:105], %[tr] offset:160\n\t" \
    "ds_read_b64_tr_b16 v[106:107], %[tr] offset:5792\n\t" \
    "ds_read_b64_tr_b16 v[120:121], %[tr] offset:192\n\t" \
    "ds_read_b64_tr_b16 v[122:123], %[tr] offset:5824\n\t" \
    "ds_read_b64_tr_b16 v[144:145], %[tr] offset:224\n\t" \
    "ds_read_b64_tr_b16 v[146:147], %[tr] offset:5856\n\t" \
    "ds_read_b64_tr_b16 v[160:161], %[tr] offset:256\n\t" \
    "ds_read_b64_tr_b16 v[162:163], %[tr] offset:5888\n\t" \
    "ds_read_b64_tr_b16 v[184:185], %[tr] offset:288\n\t" \
    "ds_read_b64_tr_b16 v[186:187], %[tr] offset:5920\n\t" \
    "s_barrier \n\t" \
    "s_mov_b32 %[t], 0x19000\n\t" \
    "buffer_load_dwordx4 v[8:11], %[o0], %[rs], %[t] offen" WNT "\n\t" \
    "buffer_load_dwordx4 v[32:35], %[o1], %[rs], %[t] offen" WNT "\n\t" \
    "buffer_load_dwordx4 v[48:51], %[o2], %[rs], %[t] offen" WNT "\n\t" \
    "buffer_load_dwordx4 v[72:75], %[o3], %[rs], %[t] offen" WNT "\n\t" \
    "buffer_load_dwordx4 v[88:91], %[o4], %[rs], %[t] offen" WNT "\n\t" \
    "s_mov_b32 %[t], 0x23000\n\t" \
    "buffer_load_dwordx4 v[112:115], %[o0], %[rs], %[t] offen" WNT "\n\t" \
    "buffer_load_dwordx4 v[128:131], %[o1], %[rs], %[t] offen" WNT "\n\t" \
    "buffer_load_dwordx4 v[152:155], %[o2], %[rs], %[t] offen" WNT "\n\t" \
    "buffer_load_dwordx4 v[168:171], %[o3], %[rs], %[t] offen" WNT "\n\t" \
    "buffer_load_dwordx4 v[192:195], %[o4], %[rs], %[t] offen" WNT "\n\t" \
    "s_waitcnt lgkmcnt(0)\n\t" \
    "s_barrier \n\t" \
    "s_waitcnt vmcnt(15)\n\t" \
    "ds_write_b128 %[s0], v[4:7] offset:0\n\t" \
    "ds_write_b128 %[s1], v[28:31] offset:0\n\t" \
    "ds_write_b128 %[s2], v[44:47] offset:0\n\t" \
    "ds_write_b128 %[s3], v[68:71] offset:0\n\t" \
    "ds_write_b128 %[s4], v[84:87] offset:0\n\t" \
    "s_waitcnt vmcnt(10)\n\t" \
    "ds_write_b128 %[s0], v[108:111] offset:5632\n\t" \
    "ds_write_b128 %[s1], v[124:127] offset:5632\n\t" \
    "ds_write_b128 %[s2], v[148:151] offset:5632\n\t" \
    "ds_write_b128 %[s3], v[164:167] offset:5632\n\t" \
    "ds_write_b128 %[s4], v[188:191] offset:5632\n\t" \
    "ds_read_b64_tr_b16 v[4:5], %[tr] offset:0\n\t" \
    "ds_read_b64_tr_b16 v[6:7], %[tr] offset:5632\n\t" \
    "ds_read_b64_tr_b16 v[28:29], %[tr] offset:32\n\t" \
    "ds_read_b64_tr_b16 v[30:31], %[tr] offset:5664\n\t" \
    "ds_read_b64_tr_b16 v[44:45], %[tr] offset:64\n\t" \
    "ds_read_b64_tr_b16 v[46:47], %[tr] offset:5696\n\t" \
    "ds_read_b64_tr_b16 v[68:69], %[tr] offset:96\n\t" \
    "ds_read_b64_tr_b16 v[70:71], %[tr] offset:5728\n\t" \
    "ds_read_b64_tr_b16 v[84:85], %[tr] offset:128\n\t" \
    "ds_read_b64_tr_b16 v[86:87], %[tr] offset:5760\n\t" \
    "ds_read_b64_tr_b16 v[108:109], %[tr] offset:160\n\t" \
    "ds_read_b64_tr_b16 v[110:111], %[tr] offset:5792\n\t" \
    "ds_read_b64_tr_b16 v[124:125], %[tr] offset:192\n\t" \
    "ds_read_b64_tr_b16 v[126:127], %[tr] offset:5824\n\t" \
    "ds_read_b64_tr_b16 v[148:149], %[tr] offset:224\n\t" \
    "ds_read_b64_tr_b16 v[150:151], %[tr] offset:5856\n\t" \
    "ds_read_b64_tr_b16 v[164:165], %[tr] offset:256\n\t" \
    "ds_read_b64_tr_b16 v[166:167], %[tr] offset:5888\n\t" \
    "ds_read_b64_tr_b16 v[188:189], %[tr] offset:288\n\t" \
    "ds_read_b64_tr_b16 v[190:191], %[tr] offset:5920\n\t" \
    "s_barrier \n\t" \
    "s_mov_b32 %[t], 0x2d000\n\t" \
    "buffer_load_dwordx4 v[12:15], %[o0], %[rs], %[t] offen" WNT "\n\t" \
    "buffer_load_dwordx4 v[36:39], %[o1], %[rs], %[t] offen" WNT "\n\t" \
    "buffer_load_dwordx4 v[52:55], %[o2], %[rs], %[t] offen" WNT "\n\t" \
    "buffer_load_dwordx4 v[76:79], %[o3], %[rs], %[t] offen" WNT "\n\t" \
    "buffer_load_dwordx4 v[92:95], %[o4], %[rs], %[t] offen" WNT "\n\t" \
    "s_mov_b32 %[t], 0x37000\n\t" \
    "buffer_load_dwordx4 v[116:119], %[o0], %[rs], %[t] offen" WNT "\n\t" \
    "buffer_load_dwordx4 v[132:135], %[o1], %[rs], %[t] offen" WNT "\n\t" \
    "buffer_load_dwordx4 v[156:159], %[o2], %[rs], %[t] offen" WNT "\n\t" \
    "buffer_load_dwordx4 v[172:175], %[o3], %[rs], %[t] offen" WNT "\n\t" \
    "buffer_load_dwordx4 v[196:199], %[o4], %[rs], %[t] offen" WNT "\n\t" \
    "s_waitcnt lgkmcnt(0)\n\t" \
    "s_barrier \n\t" \
    "s_waitcnt vmcnt(15)\n\t" \
    "ds_write_b128 %[s0], v[8:11] offset:0\n\t" \
    "ds_write_b128 %[s1], v[32:35] offset:0\n\t" \
    "ds_write_b128 %[s2], v[48:51] offset:0\n\t" \
    "ds_write_b128 %[s3], v[72:75] offset:0\n\t" \
    "ds_write_b128 %[s4], v[88:91] offset:0\n\t" \
    "s_waitcnt vmcnt(10)\n\t" \
    "ds_write_b128 %[s0], v[112:115] offset:5632\n\t" \
    "ds_write_b128 %[s1], v[128:131] offset:5632\n\t" \
    "ds_write_b128 %[s2], v[152:155] offset:5632\n\t" \
    "ds_write_b128 %[s3], v[168:171] offset:5632\n\t" \
    "ds_write_b128 %[s4], v[192:195] offset:5632\n\t" \
    "ds_read_b64_tr_b16 v[8:9], %[tr] offset:0\n\t" \
    "ds_read_b64_tr_b16 v[10:11], %[tr] offset:5632\n\t" \
    "ds_read_b64_tr_b16 v[32:33], %[tr] offset:32\n\t" \
    "ds_read_b64_tr_b16 v[34:35], %[tr] offset:5664\n\t" \
    "ds_read_b64_tr_b16 v[48:49], %[tr] offset:64\n\t" \
    "ds_read_b64_tr_b16 v[50:51], %[tr] offset:5696\n\t" \
    "ds_read_b64_tr_b16 v[72:73], %[tr] offset:96\n\t" \
    "ds_read_b64_tr_b16 v[74:75], %[tr] offset:5728\n\t" \
    "ds_read_b64_tr_b16 v[88:89], %[tr] offset:128\n\t" \
    "ds_read_b64_tr_b16 v[90:91], %[tr] offset:5760\n\t" \
    "ds_read_b64_tr_b16 v[112:113], %[tr] offset:160\n\t" \
    "ds_read_b64_tr_b16 v[114:115], %[tr] offset:5792\n\t" \
    "ds_read_b64_tr_b16 v[128:129], %[tr] offset:192\n\t" \
    "ds_read_b64_tr_b16 v[130:131], %[tr] offset:5824\n\t" \
    "ds_read_b64_tr_b16 v[152:153], %[tr] offset:224\n\t" \
    "ds_read_b64_tr_b16 v[154:155], %[tr] offset:5856\n\t" \
    "ds_read_b64_tr_b16 v[168:169], %[tr] offset:256\n\t" \
    "ds_read_b64_tr_b16 v[170:171], %[tr] offset:5888\n\t" \
    "ds_read_b64_tr_b16 v[192:193], %[tr] offset:288\n\t" \
    "ds_read_b64_tr_b16 v[194:195], %[tr] offset:5920\n\t" \
    "s_waitcnt lgkmcnt(0)\n\t" \
    "s_barrier \n\t" \
    "s_waitcnt vmcnt(5)\n\t" \
    "ds_write_b128 %[s0], v[12:15] offset:0\n\t" \
    "ds_write_b128 %[s1], v[36:39] offset:0\n\t" \
    "ds_write_b128 %[s2], v[52:55] offset:0\n\t" \
    "ds_write_b128 %[s3], v[76:79] offset:0\n\t" \
    "ds_write_b128 %[s4], v[92:95] offset:0\n\t" \
    "s_waitcnt vmcnt(0)\n\t" \
    "ds_write_b128 %[s0], v[116:119] offset:5632\n\t" \
    "ds_write_b128 %[s1], v[132:135] offset:5632\n\t" \
    "ds_write_b128 %[s2], v[156:159] offset:5632\n\t" \
    "ds_write_b128 %[s3], v[172:175] offset:5632\n\t" \
    "ds_write_b128 %[s4], v[196:199] offset:5632\n\t" \
    "ds_read_b64_tr_b16 v[12:13], %[tr] offset:0\n\t" \
    "ds_read_b64_tr_b16 v[14:15], %[tr] offset:5632\n\t" \
    "ds_read_b64_tr_b16 v[36:37], %[tr] offset:32\n\t" \
    "ds_read_b64_tr_b16 v[38:39], %[tr] offset:5664\n\t" \
    "ds_read_b64_tr_b16 v[52:53], %[tr] offset:64\n\t" \
    "ds_read_b64_tr_b16 v[54:55], %[tr] offset:5696\n\t" \
    "ds_read_b64_tr_b16 v[76:77], %[tr] offset:96\n\t" \
    "ds_read_b64_tr_b16 v[78:79], %[tr] offset:5728\n\t" \
    "ds_read_b64_tr_b16 v[92:93], %[tr] offset:128\n\t" \
    "ds_read_b64_tr_b16 v[94:95], %[tr] offset:5760\n\t" \
    "ds_read_b64_tr_b16 v[116:117], %[tr] offset:160\n\t" \
    "ds_read_b64_tr_b16 v[118:119], %[tr] offset:5792\n\t" \
    "ds_read_b64_tr_b16 v[132:133], %[tr] offset:192\n\t" \
    "ds_read_b64_tr_b16 v[134:135], %[tr] offset:5824\n\t" \
    "ds_read_b64_tr_b16 v[156:157], %[tr] offset:224\n\t" \
    "ds_read_b64_tr_b16 v[158:159], %[tr] offset:5856\n\t" \
    "ds_read_b64_tr_b16 v[172:173], %[tr] offset:256\n\t" \
    "ds_read_b64_tr_b16 v[174:175], %[tr] offset:5888\n\t" \
    "ds_read_b64_tr_b16 v[196:197], %[tr] offset:288\n\t" \
    "ds_read_b64_tr_b16 v[198:199], %[tr] offset:5920\n\t" \
    "s_waitcnt lgkmcnt(0)\n\t" \
    "s_barrier \n\t" \
    "ds_write_b128 %[wl], v[200:203] offset:0\n\t" \
    "ds_write_b128 %[wl], v[204:207] offset:1024\n\t" \
    "ds_write_b128 %[wl], v[208:211] offset:2048\n\t" \
    "ds_write_b128 %[wl], v[212:215] offset:3072\n\t" \
    "ds_write_b128 %[wl], v[216:219] offset:4096\n\t" \
    "ds_write_b128 %[wl], v[220:223] offset:5120\n\t" \
    "ds_write_b128 %[wl], v[224:227] offset:6144\n\t" \
    "ds_write_b128 %[wl], v[228:231] offset:7168\n\t" \
    "ds_write_b128 %[wl], v[232:235] offset:8192\n\t" \
    "ds_write_b128 %[wl], v[236:239] offset:9216\n\t" \
    "s_waitcnt lgkmcnt(0)\n\t"
#define ASM_XG_S_LO \
    "s_waitcnt lgkmcnt(0)\n\t" \
    "ds_read_b128 v[224:227], %[xa] offset:0\n\t" \
    "ds_read_b128 v[244:247], %[wl] offset:0\n\t" \
    "ds_read_b128 v[228:231], %[xa] offset:16384\n\t" \
    "ds_read_b128 v[232:235], %[xa] offset:1024\n\t" \
    "ds_read_b128 v[236:239], %[xa] offset:17408\n\t" \
    "ds_read_b128 v[248:251], %[wl] offset:1024\n\t" \
    "s_waitcnt lgkmcnt(5)\n\t" \
    "v_mfma_f32_16x16x32_bf16 v[204:207], v[0:3], v[224:227], 0\n\t" \
    "v_xor_b32 v252, 0x800, %[xa]\n\t" \
    "ds_read_b128 v[240:243], v252 offset:0\n\t" \
    "v_mfma_f32_16x16x32_bf16 v[208:211], v[8:11], v[224:227], 0\n\t" \
    "s_waitcnt lgkmcnt(5)\n\t" \
    "v_mfma_f32_16x16x32_bf16 v[200:203], v[244:247], v[224:227], 0\n\t" \
    "s_waitcnt lgkmcnt(4)\n\t" \
    "v_mfma_f32_16x16x32_bf16 v[216:219], v[0:3], v[228:231], 0\n\t" \
    "ds_read_b128 v[224:227], v252 offset:16384\n\t" \
    "v_mfma_f32_16x16x32_bf16 v[220:223], v[8:11], v[228:231], 0\n\t" \
    "v_mfma_f32_16x16x32_bf16 v[212:215], v[244:247], v[228:231], 0\n\t" \
    "s_waitcnt lgkmcnt(4)\n\t" \
    "v_mfma_f32_16x16x32_bf16 v[204:207], v[4:7], v[232:235], v[204:207]\n\t" \
    "ds_read_b128 v[228:231], v252 offset:1024\n\t" \
    "v_mfma_f32_16x16x32_bf16 v[208:211], v[12:15], v[232:235], v[208:211]\n\t" \
    "ds_read_b128 v[244:247], %[wl] offset:2048\n\t" \
    "s_waitcnt lgkmcnt(4)\n\t" \
    "v_mfma_f32_16x16x32_bf16 v[200:203], v[248:251], v[232:235], v[200:203]\n\t" \
    "v_mfma_f32_16x16x32_bf16 v[216:219], v[4:7], v[236:239], v[216:219]\n\t" \
    "ds_read_b128 v[232:235], v252 offset:17408\n\t" \
    "v_mfma_f32_16x16x32_bf16 v[220:223], v[12:15], v[236:239], v[220:223]\n\t" \
    "v_mfma_f32_16x16x32_bf16 v[212:215], v[248:251], v[236:239], v[212:215]\n\t" \
    "s_waitcnt lgkmcnt(4)\n\t" \
    "v_mfma_f32_16x16x32_bf16 v[204:207], v[24:27], v[240:243], v[204:207]\n\t" \
    "v_xor_b32 v252, 0x1000, %[xa]\n\t" \
    "ds_read_b128 v[236:239], v252 offset:0\n\t" \
    "v_mfma_f32_16x16x32_bf16 v[208:211], v[32:35], v[240:243], v[208:211]\n\t" \
    "ds_read_b128 v[248:251], %[wl] offset:3072\n\t" \
    "v_mfma_f32_16x16x32_bf16 v[200:203], v[16:19], v[240:243], v[200:203]\n\t" \
    "s_waitcnt lgkmcnt(5)\n\t" \
    "v_mfma_f32_16x16x32_bf16 v[216:219], v[24:27], v[224:227], v[216:219]\n\t" \
    "ds_read_b128 v[240:243], v252 offset:16384\n\t" \
    "v_mfma_f32_16x16x32_bf16 v[220:223], v[32:35], v[224:227], v[220:223]\n\t" \
    "v_mfma_f32_16x16x32_bf16 v[212:215], v[16:19], v[224:227], v[212:215]\n\t" \
    "s_waitcnt lgkmcnt(5)\n\t" \
    "v_mfma_f32_16x16x32_bf16 v[204:207], v[28:31], v[228:231], v[204:207]\n\t" \
    "ds_read_b128 v[224:227], v252 offset:1024\n\t" \
    "v_mfma_f32_16x16x32_bf16 v[208:211], v[36:39], v[228:231], v[208:211]\n\t" \
    "v_mfma_f32_16x16x32_bf16 v[200:203], v[20:23], v[228:231], v[200:203]\n\t" \
    "s_waitcnt lgkmcnt(4)\n\t" \
    "v_mfma_f32_16x16x32_bf16 v[216:219], v[28:31], v[232:235], v[216:219]\n\t" \
    "ds_read_b128 v[228:231], v252 offset:17408\n\t" \
    "v_mfma_f32_16x16x32_bf16 v[220:223], v[36:39], v[232:235], v[220:223]\n\t" \
    "v_mfma_f32_16x16x32_bf16 v[212:215], v[20:23], v[232:235], v[212:215]\n\t" \
    "s_waitcnt lgkmcnt(4)\n\t" \
    "v_mfma_f32_16x16x32_bf16 v[204:207], v[40:43], v[236:239], v[204:207]\n\t" \
    "v_xor_b32 v252, 0x1800, %[xa]\n\t" \
    "ds_read_b128 v[232:235], v252 offset:0\n\t" \
    "v_mfma_f32_16x16x32_bf16 v[208:211], v[48:51], v[236:239], v[208:211]\n\t" \
    "v_mfma_f32_16x16x32_bf16 v[200:203], v[244:247], v[236:239], v[200:203]\n\t" \
    "s_waitcnt lgkmcnt(3)\n\t" \
    "v_mfma_f32_16x16x32_bf16 v[216:219], v[40:43], v[240:243], v[216:219]\n\t" \
    "ds_read_b128 v[236:239], v252 offset:16384\n\t" \
    "v_mfma_f32_16x16x32_bf16 v[220:223], v[48:51], v[240:243], v[220:223]\n\t" \
    "v_mfma_f32_16x16x32_bf16 v[212:215], v[244:247], v[240:243], v[212:215]\n\t" \
    "s_waitcnt lgkmcnt(3)\n\t" \
    "v_mfma_f32_16x16x32_bf16 v[204:207], v[44:47], v[224:227], v[204:207]\n\t" \
    "ds_read_b128 v[240:243], v252 offset:1024\n\t" \
    "v_mfma_f32_16x16x32_bf16 v[208:211], v[52:55], v[224:227], v[208:211]\n\t" \
    "ds_read_b128 v[244:247], %[wl] offset:8192\n\t" \
    "v_mfma_f32_16x16x32_bf16 v[200:203], v[248:251], v[224:227], v[200:203]\n\t" \
    "s_waitcnt lgkmcnt(4)\n\t" \
    "v_mfma_f32_16x16x32_bf16 v[216:219], v[44:47], v[228:231], v[216:219]\n\t" \
    "ds_read_b128 v[224:227], v252 offset:17408\n\t" \
    "v_mfma_f32_16x16x32_bf16 v[220:223], v[52:55], v[228:231], v[220:223]\n\t" \
    "v_mfma_f32_16x16x32_bf16 v[212:215], v[248:251], v[228:231], v[212:215]\n\t" \
    "s_waitcnt lgkmcnt(4)\n\t" \
    "v_mfma_f32_16x16x32_bf16 v[204:207], v[64:67], v[232:235], v[204:207]\n\t" \
    "v_and_b32 v252, 0x3ff, %[xa]\n\t" \
    "ds_read_b128 v[228:231], v252 offset:40960\n\t" \
    "v_mfma_f32_16x16x32_bf16 v[208:211], v[72:75], v[232:235], v[208:211]\n\t" \
    "ds_read_b128 v[248:251], %[wl] offset:9216\n\t" \
    "v_mfma_f32_16x16x32_bf16 v[200:203], v[56:59], v[232:235], v[200:203]\n\t" \
    "s_waitcnt lgkmcnt(5)\n\t" \
    "v_mfma_f32_16x16x32_bf16 v[216:219], v[64:67], v[236:239], v[216:219]\n\t" \
    "ds_read_b128 v[232:235], v252 offset:45056\n\t" \
    "v_mfma_f32_16x16x32_bf16 v[220:223], v[72:75], v[236:239], v[220:223]\n\t" \
    "v_mfma_f32_16x16x32_bf16 v[212:215], v[56:59], v[236:239], v[212:215]\n\t" \
    "s_waitcnt lgkmcnt(5)\n\t" \
    "v_mfma_f32_16x16x32_bf16 v[204:207], v[68:71], v[240:243], v[204:207]\n\t" \
    "ds_read_b128 v[236:239], v252 offset:41984\n\t" \
    "v_mfma_f32_16x16x32_bf16 v[208:211], v[76:79], v[240:243], v[208:211]\n\t" \
    "v_mfma_f32_16x16x32_bf16 v[200:203], v[60:63], v[240:243], v[200:203]\n\t" \
    "s_waitcnt lgkmcnt(4)\n\t" \
    "v_mfma_f32_16x16x32_bf16 v[216:219], v[68:71], v[224:227], v[216:219]\n\t" \
    "ds_read_b128 v[240:243], v252 offset:46080\n\t" \
    "v_mfma_f32_16x16x32_bf16 v[220:223], v[76:79], v[224:227], v[220:223]\n\t" \
    "v_mfma_f32_16x16x32_bf16 v[212:215], v[60:63], v[224:227], v[212:215]\n\t" \
    "s_waitcnt lgkmcnt(0)\n\t" \
    "s_barrier\n\t" ASM_ST \
    "v_mfma_f32_16x16x32_bf16 v[204:207], v[160:163], v[228:231], v[204:207]\n\t" \
    "v_xor_b32 v252, 0x2000, %[xa]\n\t" \
    "ds_read_b128 v[224:227], v252 offset:0\n\t" \
    "v_mfma_f32_16x16x32_bf16 v[208:211], v[168:171], v[228:231], v[208:211]\n\t" \
    "v_mfma_f32_16x16x32_bf16 v[200:203], v[244:247], v[228:231], v[200:203]\n\t" \
    "v_mfma_f32_16x16x32_bf16 v[216:219], v[160:163], v[232:235], v[216:219]\n\t" \
    "ds_read_b128 v[228:231], v252 offset:16384\n\t" \
    "v_mfma_f32_16x16x32_bf16 v[220:223], v[168:171], v[232:235], v[220:223]\n\t" \
    "v_mfma_f32_16x16x32_bf16 v[212:215], v[244:247], v[232:235], v[212:215]\n\t" \
    "v_mfma_f32_16x16x32_bf16 v[204:207], v[164:167], v[236:239], v[204:207]\n\t" \
    "ds_read_b128 v[232:235], v252 offset:1024\n\t" \
    "v_mfma_f32_16x16x32_bf16 v[208:211], v[172:175], v[236:239], v[208:211]\n\t" \
    "ds_read_b128 v[244:247], %[wl] offset:4096\n\t" \
    "v_mfma_f32_16x16x32_bf16 v[200:203], v[248:251], v[236:239], v[200:203]\n\t" \
    "v_mfma_f32_16x16x32_bf16 v[216:219], v[164:167], v[240:243], v[216:219]\n\t" \
    "ds_read_b128 v[236:239], v252 offset:17408\n\t" \
    "v_mfma_f32_16x16x32_bf16 v[220:223], v[172:175], v[240:243], v[220:223]\n\t" \
    "v_mfma_f32_16x16x32_bf16 v[212:215], v[248:251], v[240:243], v[212:215]\n\t" \
    "s_waitcnt lgkmcnt(4)\n\t" \
    "v_mfma_f32_16x16x32_bf16 v[204:207], v[80:83], v[224:227], v[204:207]\n\t" \
    "v_xor_b32 v252, 0x2800, %[xa]\n\t" \
    "ds_read_b128 v[240:243], v252 offset:0\n\t" \
    "v_mfma_f32_16x16x32_bf16 v[208:211], v[88:91], v[224:227], v[208:211]\n\t" \
    "ds_read_b128 v[248:251], %[wl] offset:5120\n\t" \
    "s_waitcnt lgkmcnt(3)\n\t" \
    "v_mfma_f32_16x16x32_bf16 v[200:203], v[244:247], v[224:227], v[200:203]\n\t" \
    "v_mfma_f32_16x16x32_bf16 v[216:219], v[80:83], v[228:231], v[216:219]\n\t" \
    "ds_read_b128 v[224:227], v252 offset:16384\n\t" \
    "v_mfma_f32_16x16x32_bf16 v[220:223], v[88:91], v[228:231], v[220:223]\n\t" \
    "v_mfma_f32_16x16x32_bf16 v[212:215], v[244:247], v[228:231], v[212:215]\n\t" \
    "v_mfma_f32_16x16x32_bf16 v[204:207], v[84:87], v[232:235], v[204:207]\n\t" \
    "ds_read_b128 v[228:231], v252 offset:1024\n\t" \
    "v_mfma_f32_16x16x32_bf16 v[208:211], v[92:95], v[232:235], v[208:211]\n\t" \
    "ds_read_b128 v[244:247], %[wl] offset:6144\n\t" \
    "s_waitcnt lgkmcnt(3)\n\t" \
    "v_mfma_f32_16x16x32_bf16 v[200:203], v[248:251], v[232:235], v[200:203]\n\t" \
    "v_mfma_f32_16x16x32_bf16 v[216:219], v[84:87], v[236:239], v[216:219]\n\t" \
    "ds_read_b128 v[232:235], v252 offset:17408\n\t" \
    "v_mfma_f32_16x16x32_bf16 v[220:223], v[92:95], v[236:239], v[220:223]\n\t" \
    "v_mfma_f32_16x16x32_bf16 v[212:215], v[248:251], v[236:239], v[212:215]\n\t" \
    "v_mfma_f32_16x16x32_bf16 v[204:207], v[104:107], v[240:243], v[204:207]\n\t" \
    "v_xor_b32 v252, 0x3000, %[xa]\n\t" \
    "ds_read_b128 v[236:239], v252 offset:0\n\t" \
    "v_mfma_f32_16x16x32_bf16 v[208:211], v[112:115], v[240:243], v[208:211]\n\t" \
    "ds_read_b128 v[248:251], %[wl] offset:7168\n\t" \
    "v_mfma_f32_16x16x32_bf16 v[200:203], v[96:99], v[240:243], v[200:203]\n\t" \
    "s_waitcnt lgkmcnt(5)\n\t" \
    "v_mfma_f32_16x16x32_bf16 v[216:219], v[104:107], v[224:227], v[216:219]\n\t" \
    "ds_read_b128 v[240:243], v252 offset:16384\n\t" \
    "v_mfma_f32_16x16x32_bf16 v[220:223], v[112:115], v[224:227], v[220:223]\n\t" \
    "v_mfma_f32_16x16x32_bf16 v[212:215], v[96:99], v[224:227], v[212:215]\n\t" \
    "s_waitcnt lgkmcnt(5)\n\t" \
    "v_mfma_f32_16x16x32_bf16 v[204:207], v[108:111], v[228:231], v[204:207]\n\t" \
    "ds_read_b128 v[224:227], v252 offset:1024\n\t" \
    "v_mfma_f32_16x16x32_bf16 v[208:211], v[116:119], v[228:231], v[208:211]\n\t" \
    "v_mfma_f32_16x16x32_bf16 v[200:203], v[100:103], v[228:231], v[200:203]\n\t" \
    "s_waitcnt lgkmcnt(4)\n\t" \
    "v_mfma_f32_16x16x32_bf16 v[216:219], v[108:111], v[232:235], v[216:219]\n\t" \
    "ds_read_b128 v[228:231], v252 offset:17408\n\t" \
    "v_mfma_f32_16x16x32_bf16 v[220:223], v[116:119], v[232:235], v[220:223]\n\t" \
    "v_mfma_f32_16x16x32_bf16 v[212:215], v[100:103], v[232:235], v[212:215]\n\t" \
    "s_waitcnt lgkmcnt(4)\n\t" \
    "v_mfma_f32_16x16x32_bf16 v[204:207], v[120:123], v[236:239], v[204:207]\n\t" \
    "v_xor_b32 v252, 0x3800, %[xa]\n\t" \
    "ds_read_b128 v[232:235], v252 offset:0\n\t" \
    "v_mfma_f32_16x16x32_bf16 v[208:211], v[128:131], v[236:239], v[208:211]\n\t" \
    "v_mfma_f32_16x16x32_bf16 v[200:203], v[244:247], v[236:239], v[200:203]\n\t" \
    "s_waitcnt lgkmcnt(3)\n\t" \
    "v_mfma_f32_16x16x32_bf16 v[216:219], v[120:123], v[240:243], v[216:219]\n\t" \
    "ds_read_b128 v[236:239], v252 offset:16384\n\t" \
    "v_mfma_f32_16x16x32_bf16 v[220:223], v[128:131], v[240:243], v[220:223]\n\t" \
    "v_mfma_f32_16x16x32_bf16 v[212:215], v[244:247], v[240:243], v[212:215]\n\t" \
    "s_waitcnt lgkmcnt(3)\n\t" \
    "v_mfma_f32_16x16x32_bf16 v[204:207], v[124:127], v[224:227], v[204:207]\n\t" \
    "ds_read_b128 v[240:243], v252 offset:1024\n\t" \
    "v_mfma_f32_16x16x32_bf16 v[208:211], v[132:135], v[224:227], v[208:211]\n\t" \
    "v_mfma_f32_16x16x32_bf16 v[200:203], v[248:251], v[224:227], v[200:203]\n\t" \
    "s_waitcnt lgkmcnt(3)\n\t" \
    "v_mfma_f32_16x16x32_bf16 v[216:219], v[124:127], v[228:231], v[216:219]\n\t" \
    "ds_read_b128 v[224:227], v252 offset:17408\n\t" \
    "v_mfma_f32_16x16x32_bf16 v[220:223], v[132:135], v[228:231], v[220:223]\n\t" \
    "v_mfma_f32_16x16x32_bf16 v[212:215], v[248:251], v[228:231], v[212:215]\n\t" \
    "s_waitcnt lgkmcnt(3)\n\t" \
    "v_mfma_f32_16x16x32_bf16 v[204:207], v[144:147], v[232:235], v[204:207]\n\t" \
    "v_and_b32 v252, 0x3ff, %[xa]\n\t" \
    "ds_read_b128 v[228:231], v252 offset:43008\n\t" \
    "v_mov_b64 v[244:245], 0\n\t" \
    "v_mfma_f32_16x16x32_bf16 v[208:211], v[152:155], v[232:235], v[208:211]\n\t" \
    "v_mov_b64 v[246:247], 0\n\t" \
    "v_mfma_f32_16x16x32_bf16 v[200:203], v[136:139], v[232:235], v[200:203]\n\t" \
    "v_mov_b64 v[248:249], 0\n\t" \
    "s_waitcnt lgkmcnt(3)\n\t" \
    "v_mfma_f32_16x16x32_bf16 v[216:219], v[144:147], v[236:239], v[216:219]\n\t" \
    "ds_read_b128 v[232:235], v252 offset:44032\n\t" \
    "v_mov_b64 v[250:251], 0\n\t" \
    "v_mfma_f32_16x16x32_bf16 v[220:223], v[152:155], v[236:239], v[220:223]\n\t" \
    "v_mfma_f32_16x16x32_bf16 v[212:215], v[136:139], v[236:239], v[212:215]\n\t" \
    "s_waitcnt lgkmcnt(3)\n\t" \
    "v_mfma_f32_16x16x32_bf16 v[204:207], v[148:151], v[240:243], v[204:207]\n\t" \
    "ds_read_b128 v[236:239], v252 offset:47104\n\t" \
    "v_mfma_f32_16x16x32_bf16 v[208:211], v[156:159], v[240:243], v[208:211]\n\t" \
    "v_mfma_f32_16x16x32_bf16 v[200:203], v[140:143], v[240:243], v[200:203]\n\t" \
    "s_waitcnt lgkmcnt(3)\n\t" \
    "v_mfma_f32_16x16x32_bf16 v[216:219], v[148:151], v[224:227], v[216:219]\n\t" \
    "ds_read_b128 v[240:243], v252 offset:48128\n\t" \
    "v_mfma_f32_16x16x32_bf16 v[220:223], v[156:159], v[224:227], v[220:223]\n\t" \
    "v_mfma_f32_16x16x32_bf16 v[212:215], v[140:143], v[224:227], v[212:215]\n\t" \
    "s_waitcnt lgkmcnt(3)\n\t" \
    "v_mfma_f32_16x16x32_bf16 v[204:207], v[184:187], v[228:231], v[204:207]\n\t" \
    "v_add_u32 v252, %[qs], v252\n\t" \
    "v_mfma_f32_16x16x32_bf16 v[208:211], v[192:195], v[228:231], v[208:211]\n\t" \
    "v_mov_b64 v[224:225], 0\n\t" \
    "v_mfma_f32_16x16x32_bf16 v[200:203], v[176:179], v[228:231], v[200:203]\n\t" \
    "v_mov_b64 v[226:227], 0\n\t" \
    "s_waitcnt lgkmcnt(2)\n\t" \
    "v_mfma_f32_16x16x32_bf16 v[204:207], v[188:191], v[232:235], v[204:207]\n\t" \
    "v_mfma_f32_16x16x32_bf16 v[208:211], v[196:199], v[232:235], v[208:211]\n\t" \
    "v_mfma_f32_16x16x32_bf16 v[200:203], v[180:183], v[232:235], v[200:203]\n\t" \
    "s_waitcnt lgkmcnt(1)\n\t" \
    "v_mfma_f32_16x16x32_bf16 v[216:219], v[184:187], v[236:239], v[216:219]\n\t" \
    "ds_read_b128 v[228:231], %[wl] offset:2048\n\t" \
    "ds_read_b128 v[232:235], %[wl] offset:3072\n\t" \
    "v_mfma_f32_16x16x32_bf16 v[220:223], v[192:195], v[236:239], v[220:223]\n\t" \
    "v_cvt_pk_bf16_f32 v204, v204, v205\n\t" \
    "v_cvt_pk_bf16_f32 v205, v206, v207\n\t" \
    "v_mfma_f32_16x16x32_bf16 v[212:215], v[176:179], v[236:239], v[212:215]\n\t" \
    "ds_write_b64 v252, v[204:205] offset:1024\n\t" \
    "v_cvt_pk_bf16_f32 v208, v208, v209\n\t" \
    "s_waitcnt lgkmcnt(3)\n\t" \
    "v_mfma_f32_16x16x32_bf16 v[216:219], v[188:191], v[240:243], v[216:219]\n\t" \
    "v_cvt_pk_bf16_f32 v209, v210, v211\n\t" \
    "ds_write_b64 v252, v[208:209] offset:2048\n\t" \
    "v_mfma_f32_16x16x32_bf16 v[220:223], v[196:199], v[240:243], v[220:223]\n\t" \
    "v_cvt_pk_bf16_f32 v200, v200, v201\n\t" \
    "v_cvt_pk_bf16_f32 v201, v202, v203\n\t" \
    "v_mfma_f32_16x16x32_bf16 v[212:215], v[180:183], v[240:243], v[212:215]\n\t" \
    "ds_write_b64 v252, v[200:201] offset:0\n\t" \
    "ds_read_b128 v[200:203], %[wl] offset:0\n\t" \
    "ds_read_b128 v[204:207], %[wl] offset:1024\n\t" \
    "v_cvt_pk_bf16_f32 v216, v216, v217\n\t" \
    "v_cvt_pk_bf16_f32 v217, v218, v219\n\t" \
    "ds_write_b64 v252, v[216:217] offset:7168\n\t" \
    "v_cvt_pk_bf16_f32 v220, v220, v221\n\t" \
    "v_cvt_pk_bf16_f32 v221, v222, v223\n\t" \
    "ds_write_b64 v252, v[220:221] offset:8192\n\t" \
    "v_cvt_pk_bf16_f32 v212, v212, v213\n\t" \
    "v_cvt_pk_bf16_f32 v213, v214, v215\n\t" \
    "ds_write_b64 v252, v[212:213] offset:6144\n\t" \
    "s_waitcnt lgkmcnt(0)\n\t"
#define ASM_XG_S_HI \
    "s_waitcnt lgkmcnt(0)\n\t" \
    "v_xor_b32 v252, 0x2000, %[xa]\n\t" \
    "ds_read_b128 v[224:227], v252 offset:0\n\t" \
    "ds_read_b128 v[244:247], %[wl] offset:4096\n\t" \
    "ds_read_b128 v[228:231], v252 offset:16384\n\t" \
    "ds_read_b128 v[232:235], v252 offset:1024\n\t" \
    "ds_read_b128 v[236:239], v252 offset:17408\n\t" \
    "ds_read_b128 v[248:251], %[wl] offset:5120\n\t" \
    "s_cmp_eq_u32 %[slp], 0\n\t" \
    "s_cbranch_scc1 .Lxs%=\n\t" \
    "s_sleep 10\n\t" \
    ".Lxs%=:\n\t" \
    "s_waitcnt lgkmcnt(5)\n\t" \
    "v_mfma_f32_16x16x32_bf16 v[204:207], v[80:83], v[224:227], 0\n\t" \
    "v_xor_b32 v252, 0x2800, %[xa]\n\t" \
    "ds_read_b128 v[240:243], v252 offset:0\n\t" \
    "v_mfma_f32_16x16x32_bf16 v[208:211], v[88:91], v[224:227], 0\n\t" \
    "s_waitcnt lgkmcnt(5)\n\t" \
    "v_mfma_f32_16x16x32_bf16 v[200:203], v[244:247], v[224:227], 0\n\t" \
    "s_waitcnt lgkmcnt(4)\n\t" \
    "v_mfma_f32_16x16x32_bf16 v[216:219], v[80:83], v[228:231], 0\n\t" \
    "ds_read_b128 v[224:227], v252 offset:16384\n\t" \
    "v_mfma_f32_16x16x32_bf16 v[220:223], v[88:91], v[228:231], 0\n\t" \
    "v_mfma_f32_16x16x32_bf16 v[212:215], v[244:247], v[228:231], 0\n\t" \
    "s_waitcnt lgkmcnt(4)\n\t" \
    "v_mfma_f32_16x16x32_bf16 v[204:207], v[84:87], v[232:235], v[204:207]\n\t" \
    "ds_read_b128 v[228:231], v252 offset:1024\n\t" \
    "v_mfma_f32_16x16x32_bf16 v[208:211], v[92:95], v[232:235], v[208:211]\n\t" \
    "ds_read_b128 v[244:247], %[wl] offset:6144\n\t" \
    "s_waitcnt lgkmcnt(4)\n\t" \
    "v_mfma_f32_16x16x32_bf16 v[200:203], v[248:251], v[232:235], v[200:203]\n\t" \
    "v_mfma_f32_16x16x32_bf16 v[216:219], v[84:87], v[236:239], v[216:219]\n\t" \
    "ds_read_b128 v[232:235], v252 offset:17408\n\t" \
    "v_mfma_f32_16x16x32_bf16 v[220:223], v[92:95], v[236:239], v[220:223]\n\t" \
    "v_mfma_f32_16x16x32_bf16 v[212:215], v[248:251], v[236:239], v[212:215]\n\t" \
    "s_waitcnt lgkmcnt(4)\n\t" \
    "v_mfma_f32_16x16x32_bf16 v[204:207], v[104:107], v[240:243], v[204:207]\n\t" \
    "v_xor_b32 v252, 0x3000, %[xa]\n\t" \
    "ds_read_b128 v[236:239], v252 offset:0\n\t" \
    "v_mfma_f32_16x16x32_bf16 v[208:211], v[112:115], v[240:243], v[208:211]\n\t" \
    "ds_read_b128 v[248:251], %[wl] offset:7168\n\t" \
    "v_mfma_f32_16x16x32_bf16 v[200:203], v[96:99], v[240:243], v[200:203]\n\t" \
    "s_waitcnt lgkmcnt(5)\n\t" \
    "v_mfma_f32_16x16x32_bf16 v[216:219], v[104:107], v[224:227], v[216:219]\n\t" \
    "ds_read_b128 v[240:243], v252 offset:16384\n\t" \
    "v_mfma_f32_16x16x32_bf16 v[220:223], v[112:115], v[224:227], v[220:223]\n\t" \
    "v_mfma_f32_16x16x32_bf16 v[212:215], v[96:99], v[224:227], v[212:215]\n\t" \
    "s_waitcnt lgkmcnt(5)\n\t" \
    "v_mfma_f32_16x16x32_bf16 v[204:207], v[108:111], v[228:231], v[204:207]\n\t" \
    "ds_read_b128 v[224:227], v252 offset:1024\n\t" \
    "v_mfma_f32_16x16x32_bf16 v[208:211], v[116:119], v[228:231], v[208:211]\n\t" \
    "v_mfma_f32_16x16x32_bf16 v[200:203], v[100:103], v[228:231], v[200:203]\n\t" \
    "s_waitcnt lgkmcnt(4)\n\t" \
    "v_mfma_f32_16x16x32_bf16 v[216:219], v[108:111], v[232:235], v[216:219]\n\t" \
    "ds_read_b128 v[228:231], v252 offset:17408\n\t" \
    "v_mfma_f32_16x16x32_bf16 v[220:223], v[116:119], v[232:235], v[220:223]\n\t" \
    "v_mfma_f32_16x16x32_bf16 v[212:215], v[100:103], v[232:235], v[212:215]\n\t" \
    "s_waitcnt lgkmcnt(4)\n\t" \
    "v_mfma_f32_16x16x32_bf16 v[204:207], v[120:123], v[236:239], v[204:207]\n\t" \
    "v_xor_b32 v252, 0x3800, %[xa]\n\t" \
    "ds_read_b128 v[232:235], v252 offset:0\n\t" \
    "v_mfma_f32_16x16x32_bf16 v[208:211], v[128:131], v[236:239], v[208:211]\n\t" \
    "v_mfma_f32_16x16x32_bf16 v[200:203], v[244:247], v[236:239], v[200:203]\n\t" \
    "s_waitcnt lgkmcnt(3)\n\t" \
    "v_mfma_f32_16x16x32_bf16 v[216:219], v[120:123], v[240:243], v[216:219]\n\t" \
    "ds_read_b128 v[236:239], v252 offset:16384\n\t" \
    "v_mfma_f32_16x16x32_bf16 v[220:223], v[128:131], v[240:243], v[220:223]\n\t" \
    "v_mfma_f32_16x16x32_bf16 v[212:215], v[244:247], v[240:243], v[212:215]\n\t" \
    "s_waitcnt lgkmcnt(3)\n\t" \
    "v_mfma_f32_16x16x32_bf16 v[204:207], v[124:127], v[224:227], v[204:207]\n\t" \
    "ds_read_b128 v[240:243], v252 offset:1024\n\t" \
    "v_mfma_f32_16x16x32_bf16 v[208:211], v[132:135], v[224:227], v[208:211]\n\t" \
    "ds_read_b128 v[244:247], %[wl] offset:8192\n\t" \
    "v_mfma_f32_16x16x32_bf16 v[200:203], v[248:251], v[224:227], v[200:203]\n\t" \
    "s_waitcnt lgkmcnt(4)\n\t" \
    "v_mfma_f32_16x16x32_bf16 v[216:219], v[124:127], v[228:231], v[216:219]\n\t" \
    "ds_read_b128 v[224:227], v252 offset:17408\n\t" \
    "v_mfma_f32_16x16x32_bf16 v[220:223], v[132:135], v[228:231], v[220:223]\n\t" \
    "v_mfma_f32_16x16x32_bf16 v[212:215], v[248:251], v[228:231], v[212:215]\n\t" \
    "s_waitcnt lgkmcnt(4)\n\t" \
    "v_mfma_f32_16x16x32_bf16 v[204:207], v[144:147], v[232:235], v[204:207]\n\t" \
    "v_and_b32 v252, 0x3ff, %[xa]\n\t" \
    "ds_read_b128 v[228:231], v252 offset:40960\n\t" \
    "v_mfma_f32_16x16x32_bf16 v[208:211], v[152:155], v[232:235], v[208:211]\n\t" \
    "ds_read_b128 v[248:251], %[wl] offset:9216\n\t" \
    "v_mfma_f32_16x16x32_bf16 v[200:203], v[136:139], v[232:235], v[200:203]\n\t" \
    "s_waitcnt lgkmcnt(5)\n\t" \
    "v_mfma_f32_16x16x32_bf16 v[216:219], v[144:147], v[236:239], v[216:219]\n\t" \
    "ds_read_b128 v[232:235], v252 offset:45056\n\t" \
    "v_mfma_f32_16x16x32_bf16 v[220:223], v[152:155], v[236:239], v[220:223]\n\t" \
    "v_mfma_f32_16x16x32_bf16 v[212:215], v[136:139], v[236:239], v[212:215]\n\t" \
    "s_waitcnt lgkmcnt(5)\n\t" \
    "v_mfma_f32_16x16x32_bf16 v[204:207], v[148:151], v[240:243], v[204:207]\n\t" \
    "ds_read_b128 v[236:239], v252 offset:41984\n\t" \
    "v_mfma_f32_16x16x32_bf16 v[208:211], v[156:159], v[240:243], v[208:211]\n\t" \
    "v_mfma_f32_16x16x32_bf16 v[200:203], v[140:143], v[240:243], v[200:203]\n\t" \
    "s_waitcnt lgkmcnt(4)\n\t" \
    "v_mfma_f32_16x16x32_bf16 v[216:219], v[148:151], v[224:227], v[216:219]\n\t" \
    "ds_read_b128 v[240:243], v252 offset:46080\n\t" \
    "v_mfma_f32_16x16x32_bf16 v[220:223], v[156:159], v[224:227], v[220:223]\n\t" \
    "v_mfma_f32_16x16x32_bf16 v[212:215], v[140:143], v[224:227], v[212:215]\n\t" \
    "s_waitcnt vmcnt(0) lgkmcnt(0)\n\t" \
    "s_barrier\n\t" ASM_ST \
    "s_mov_b32 m0, %[d0]\n\t" \
    "s_nop 0\n\t" \
    "global_load_lds_dwordx4 v252, %[g0] offset:0 sc1\n\t" \
    "global_load_lds_dwordx4 v252, %[g0] offset:1024 sc1\n\t" \
    "global_load_lds_dwordx4 v252, %[g0] offset:2048 sc1\n\t" \
    "global_load_lds_dwordx4 v252, %[g0] offset:3072 sc1\n\t" \
    "s_mov_b32 m0, %[d1]\n\t" \
    "s_nop 0\n\t" \
    "global_load_lds_dwordx4 v252, %[g1] sc1\n\t" \
    "v_mfma_f32_16x16x32_bf16 v[204:207], v[160:163], v[228:231], v[204:207]\n\t" \
    "ds_read_b128 v[224:227], %[xa] offset:0\n\t" \
    "v_mfma_f32_16x16x32_bf16 v[208:211], v[168:171], v[228:231], v[208:211]\n\t" \
    "v_mfma_f32_16x16x32_bf16 v[200:203], v[244:247], v[228:231], v[200:203]\n\t" \
    "v_mfma_f32_16x16x32_bf16 v[216:219], v[160:163], v[232:235], v[216:219]\n\t" \
    "ds_read_b128 v[228:231], %[xa] offset:16384\n\t" \
    "v_mfma_f32_16x16x32_bf16 v[220:223], v[168:171], v[232:235], v[220:223]\n\t" \
    "v_mfma_f32_16x16x32_bf16 v[212:215], v[244:247], v[232:235], v[212:215]\n\t" \
    "v_mfma_f32_16x16x32_bf16 v[204:207], v[164:167], v[236:239], v[204:207]\n\t" \
    "ds_read_b128 v[232:235], %[xa] offset:1024\n\t" \
    "v_mfma_f32_16x16x32_bf16 v[208:211], v[172:175], v[236:239], v[208:211]\n\t" \
    "ds_read_b128 v[244:247], %[wl] offset:0\n\t" \
    "v_mfma_f32_16x16x32_bf16 v[200:203], v[248:251], v[236:239], v[200:203]\n\t" \
    "v_mfma_f32_16x16x32_bf16 v[216:219], v[164:167], v[240:243], v[216:219]\n\t" \
    "ds_read_b128 v[236:239], %[xa] offset:17408\n\t" \
    "v_mfma_f32_16x16x32_bf16 v[220:223], v[172:175], v[240:243], v[220:223]\n\t" \
    "v_mfma_f32_16x16x32_bf16 v[212:215], v[248:251], v[240:243], v[212:215]\n\t" \
    "s_waitcnt lgkmcnt(4)\n\t" \
    "v_mfma_f32_16x16x32_bf16 v[204:207], v[0:3], v[224:227], v[204:207]\n\t" \
    "v_xor_b32 v252, 0x800, %[xa]\n\t" \
    "ds_read_b128 v[240:243], v252 offset:0\n\t" \
    "v_mfma_f32_16x16x32_bf16 v[208:211], v[8:11], v[224:227], v[208:211]\n\t" \
    "ds_read_b128 v[248:251], %[wl] offset:1024\n\t" \
    "s_waitcnt lgkmcnt(3)\n\t" \
    "v_mfma_f32_16x16x32_bf16 v[200:203], v[244:247], v[224:227], v[200:203]\n\t" \
    "v_mfma_f32_16x16x32_bf16 v[216:219], v[0:3], v[228:231], v[216:219]\n\t" \
    "ds_read_b128 v[224:227], v252 offset:16384\n\t" \
    "v_mfma_f32_16x16x32_bf16 v[220:223], v[8:11], v[228:231], v[220:223]\n\t" \
    "v_mfma_f32_16x16x32_bf16 v[212:215], v[244:247], v[228:231], v[212:215]\n\t" \
    "v_mfma_f32_16x16x32_bf16 v[204:207], v[4:7], v[232:235], v[204:207]\n\t" \
    "ds_read_b128 v[228:231], v252 offset:1024\n\t" \
    "v_mfma_f32_16x16x32_bf16 v[208:211], v[12:15], v[232:235], v[208:211]\n\t" \
    "ds_read_b128 v[244:247], %[wl] offset:2048\n\t" \
    "s_waitcnt lgkmcnt(3)\n\t" \
    "v_mfma_f32_16x16x32_bf16 v[200:203], v[248:251], v[232:235], v[200:203]\n\t" \
    "v_mfma_f32_16x16x32_bf16 v[216:219], v[4:7], v[236:239], v[216:219]\n\t" \
    "ds_read_b128 v[232:235], v252 offset:17408\n\t" \
    "v_mfma_f32_16x16x32_bf16 v[220:223], v[12:15], v[236:239], v[220:223]\n\t" \
    "v_mfma_f32_16x16x32_bf16 v[212:215], v[248:251], v[236:239], v[212:215]\n\t" \
    "v_mfma_f32_16x16x32_bf16 v[204:207], v[24:27], v[240:243], v[204:207]\n\t" \
    "v_xor_b32 v252, 0x1000, %[xa]\n\t" \
    "ds_read_b128 v[236:239], v252 offset:0\n\t" \
    "v_mfma_f32_16x16x32_bf16 v[208:211], v[32:35], v[240:243], v[208:211]\n\t" \
    "ds_read_b128 v[248:251], %[wl] offset:3072\n\t" \
    "v_mfma_f32_16x16x32_bf16 v[200:203], v[16:19], v[240:243], v[200:203]\n\t" \
    "s_waitcnt lgkmcnt(5)\n\t" \
    "v_mfma_f32_16x16x32_bf16 v[216:219], v[24:27], v[224:227], v[216:219]\n\t" \
    "ds_read_b128 v[240:243], v252 offset:16384\n\t" \
    "v_mfma_f32_16x16x32_bf16 v[220:223], v[32:35], v[224:227], v[220:223]\n\t" \
    "v_mfma_f32_16x16x32_bf16 v[212:215], v[16:19], v[224:227], v[212:215]\n\t" \
    "s_waitcnt lgkmcnt(5)\n\t" \
    "v_mfma_f32_16x16x32_bf16 v[204:207], v[28:31], v[228:231], v[204:207]\n\t" \
    "ds_read_b128 v[224:227], v252 offset:1024\n\t" \
    "v_mfma_f32_16x16x32_bf16 v[208:211], v[36:39], v[228:231], v[208:211]\n\t" \
    "v_mfma_f32_16x16x32_bf16 v[200:203], v[20:23], v[228:231], v[200:203]\n\t" \
    "s_waitcnt lgkmcnt(4)\n\t" \
    "v_mfma_f32_16x16x32_bf16 v[216:219], v[28:31], v[232:235], v[216:219]\n\t" \
    "ds_read_b128 v[228:231], v252 offset:17408\n\t" \
    "v_mfma_f32_16x16x32_bf16 v[220:223], v[36:39], v[232:235], v[220:223]\n\t" \
    "v_mfma_f32_16x16x32_bf16 v[212:215], v[20:23], v[232:235], v[212:215]\n\t" \
    "s_waitcnt lgkmcnt(4)\n\t" \
    "v_mfma_f32_16x16x32_bf16 v[204:207], v[40:43], v[236:239], v[204:207]\n\t" \
    "v_xor_b32 v252, 0x1800, %[xa]\n\t" \
    "ds_read_b128 v[232:235], v252 offset:0\n\t" \
    "v_mfma_f32_16x16x32_bf16 v[208:211], v[48:51], v[236:239], v[208:211]\n\t" \
    "v_mfma_f32_16x16x32_bf16 v[200:203], v[244:247], v[236:239], v[200:203]\n\t" \
    "s_waitcnt lgkmcnt(3)\n\t" \
    "v_mfma_f32_16x16x32_bf16 v[216:219], v[40:43], v[240:243], v[216:219]\n\t" \
    "ds_read_b128 v[236:239], v252 offset:16384\n\t" \
    "v_mfma_f32_16x16x32_bf16 v[220:223], v[48:51], v[240:243], v[220:223]\n\t" \
    "v_mfma_f32_16x16x32_bf16 v[212:215], v[244:247], v[240:243], v[212:215]\n\t" \
    "s_waitcnt lgkmcnt(3)\n\t" \
    "v_mfma_f32_16x16x32_bf16 v[204:207], v[44:47], v[224:227], v[204:207]\n\t" \
    "ds_read_b128 v[240:243], v252 offset:1024\n\t" \
    "v_mfma_f32_16x16x32_bf16 v[208:211], v[52:55], v[224:227], v[208:211]\n\t" \
    "v_mfma_f32_16x16x32_bf16 v[200:203], v[248:251], v[224:227], v[200:203]\n\t" \
    "s_waitcnt lgkmcnt(3)\n\t" \
    "v_mfma_f32_16x16x32_bf16 v[216:219], v[44:47], v[228:231], v[216:219]\n\t" \
    "ds_read_b128 v[224:227], v252 offset:17408\n\t" \
    "v_mfma_f32_16x16x32_bf16 v[220:223], v[52:55], v[228:231], v[220:223]\n\t" \
    "v_mfma_f32_16x16x32_bf16 v[212:215], v[248:251], v[228:231], v[212:215]\n\t" \
    "s_waitcnt lgkmcnt(3)\n\t" \
    "v_mfma_f32_16x16x32_bf16 v[204:207], v[64:67], v[232:235], v[204:207]\n\t" \
    "v_and_b32 v252, 0x3ff, %[xa]\n\t" \
    "ds_read_b128 v[228:231], v252 offset:43008\n\t" \
    "v_mov_b64 v[244:245], 0\n\t" \
    "v_mfma_f32_16x16x32_bf16 v[208:211], v[72:75], v[232:235], v[208:211]\n\t" \
    "v_mov_b64 v[246:247], 0\n\t" \
    "v_mfma_f32_16x16x32_bf16 v[200:203], v[56:59], v[232:235], v[200:203]\n\t" \
    "v_mov_b64 v[248:249], 0\n\t" \
    "s_waitcnt lgkmcnt(3)\n\t" \
    "v_mfma_f32_16x16x32_bf16 v[216:219], v[64:67], v[236:239], v[216:219]\n\t" \
    "ds_read_b128 v[232:235], v252 offset:44032\n\t" \
    "v_mov_b64 v[250:251], 0\n\t" \
    "v_mfma_f32_16x16x32_bf16 v[220:223], v[72:75], v[236:239], v[220:223]\n\t" \
    "v_mfma_f32_16x16x32_bf16 v[212:215], v[56:59], v[236:239], v[212:215]\n\t" \
    "s_waitcnt lgkmcnt(3)\n\t" \
    "v_mfma_f32_16x16x32_bf16 v[204:207], v[68:71], v[240:243], v[204:207]\n\t" \
    "ds_read_b128 v[236:239], v252 offset:47104\n\t" \
    "v_mfma_f32_16x16x32_bf16 v[208:211], v[76:79], v[240:243], v[208:211]\n\t" \
    "v_mfma_f32_16x16x32_bf16 v[200:203], v[60:63], v[240:243], v[200:203]\n\t" \
    "s_waitcnt lgkmcnt(3)\n\t" \
    "v_mfma_f32_16x16x32_bf16 v[216:219], v[68:71], v[224:227], v[216:219]\n\t" \
    "ds_read_b128 v[240:243], v252 offset:48128\n\t" \
    "v_mfma_f32_16x16x32_bf16 v[220:223], v[76:79], v[224:227], v[220:223]\n\t" \
    "v_mfma_f32_16x16x32_bf16 v[212:215], v[60:63], v[224:227], v[212:215]\n\t" \
    "s_waitcnt lgkmcnt(3)\n\t" \
    "v_mfma_f32_16x16x32_bf16 v[204:207], v[184:187], v[228:231], v[204:207]\n\t" \
    "v_add_u32 v252, %[qs], v252\n\t" \
    "v_mfma_f32_16x16x32_bf16 v[208:211], v[192:195], v[228:231], v[208:211]\n\t" \
    "v_mov_b64 v[224:225], 0\n\t" \
    "v_mfma_f32_16x16x32_bf16 v[200:203], v[176:179], v[228:231], v[200:203]\n\t" \
    "v_mov_b64 v[226:227], 0\n\t" \
    "s_waitcnt lgkmcnt(2)\n\t" \
    "v_mfma_f32_16x16x32_bf16 v[204:207], v[188:191], v[232:235], v[204:207]\n\t" \
    "v_mfma_f32_16x16x32_bf16 v[208:211], v[196:199], v[232:235], v[208:211]\n\t" \
    "v_mfma_f32_16x16x32_bf16 v[200:203], v[180:183], v[232:235], v[200:203]\n\t" \
    "s_waitcnt lgkmcnt(1)\n\t" \
    "v_mfma_f32_16x16x32_bf16 v[216:219], v[184:187], v[236:239], v[216:219]\n\t" \
    "ds_read_b128 v[228:231], %[wl] offset:2048\n\t" \
    "ds_read_b128 v[232:235], %[wl] offset:3072\n\t" \
    "v_mfma_f32_16x16x32_bf16 v[220:223], v[192:195], v[236:239], v[220:223]\n\t" \
    "v_cvt_pk_bf16_f32 v204, v204, v205\n\t" \
    "v_cvt_pk_bf16_f32 v205, v206, v207\n\t" \
    "v_mfma_f32_16x16x32_bf16 v[212:215], v[176:179], v[236:239], v[212:215]\n\t" \
    "ds_write_b64 v252, v[204:205] offset:1024\n\t" \
    "v_cvt_pk_bf16_f32 v208, v208, v209\n\t" \
    "s_waitcnt lgkmcnt(3)\n\t" \
    "v_mfma_f32_16x16x32_bf16 v[216:219], v[188:191], v[240:243], v[216:219]\n\t" \
    "v_cvt_pk_bf16_f32 v209, v210, v211\n\t" \
    "ds_write_b64 v252, v[208:209] offset:2048\n\t" \
    "v_mfma_f32_16x16x32_bf16 v[220:223], v[196:199], v[240:243], v[220:223]\n\t" \
    "v_cvt_pk_bf16_f32 v200, v200, v201\n\t" \
    "v_cvt_pk_bf16_f32 v201, v202, v203\n\t" \
    "v_mfma_f32_16x16x32_bf16 v[212:215], v[180:183], v[240:243], v[212:215]\n\t" \
    "ds_write_b64 v252, v[200:201] offset:0\n\t" \
    "ds_read_b128 v[200:203], %[wl] offset:0\n\t" \
    "ds_read_b128 v[204:207], %[wl] offset:1024\n\t" \
    "v_cvt_pk_bf16_f32 v216, v216, v217\n\t" \
    "v_cvt_pk_bf16_f32 v217, v218, v219\n\t" \
    "ds_write_b64 v252, v[216:217] offset:7168\n\t" \
    "v_cvt_pk_bf16_f32 v220, v220, v221\n\t" \
    "v_cvt_pk_bf16_f32 v221, v222, v223\n\t" \
    "ds_write_b64 v252, v[220:221] offset:8192\n\t" \
    "v_cvt_pk_bf16_f32 v212, v212, v213\n\t" \
    "v_cvt_pk_bf16_f32 v213, v214, v215\n\t" \
    "ds_write_b64 v252, v[212:213] offset:6144\n\t" \
    "s_waitcnt lgkmcnt(0)\n\t"
#define ASM_XG_S_LO2 \
    "s_waitcnt lgkmcnt(0)\n\t" \
    "ds_read_b128 v[224:227], %[xa] offset:0\n\t" \
    "ds_read_b128 v[228:231], %[xa] offset:16384\n\t" \
    "ds_read_b128 v[232:235], %[xa] offset:1024\n\t" \
    "ds_read_b128 v[236:239], %[xa] offset:17408\n\t" \
    "s_waitcnt lgkmcnt(3)\n\t" \
    "v_mfma_f32_16x16x32_bf16 v[204:207], v[0:3], v[224:227], 0\n\t" \
    "v_xor_b32 v252, 0x800, %[xa]\n\t" \
    "ds_read_b128 v[240:243], v252 offset:0\n\t" \
    "v_mfma_f32_16x16x32_bf16 v[208:211], v[8:11], v[224:227], 0\n\t" \
    "s_waitcnt lgkmcnt(3)\n\t" \
    "v_mfma_f32_16x16x32_bf16 v[216:219], v[0:3], v[228:231], 0\n\t" \
    "ds_read_b128 v[224:227], v252 offset:16384\n\t" \
    "v_mfma_f32_16x16x32_bf16 v[220:223], v[8:11], v[228:231], 0\n\t" \
    "s_waitcnt lgkmcnt(3)\n\t" \
    "v_mfma_f32_16x16x32_bf16 v[204:207], v[4:7], v[232:235], v[204:207]\n\t" \
    "ds_read_b128 v[228:231], v252 offset:1024\n\t" \
    "v_mfma_f32_16x16x32_bf16 v[208:211], v[12:15], v[232:235], v[208:211]\n\t" \
    "s_waitcnt lgkmcnt(3)\n\t" \
    "v_mfma_f32_16x16x32_bf16 v[216:219], v[4:7], v[236:239], v[216:219]\n\t" \
    "ds_read_b128 v[232:235], v252 offset:17408\n\t" \
    "v_mfma_f32_16x16x32_bf16 v[220:223], v[12:15], v[236:239], v[220:223]\n\t" \
    "s_waitcnt lgkmcnt(3)\n\t" \
    "v_mfma_f32_16x16x32_bf16 v[204:207], v[24:27], v[240:243], v[204:207]\n\t" \
    "v_xor_b32 v252, 0x1000, %[xa]\n\t" \
    "ds_read_b128 v[236:239], v252 offset:0\n\t" \
    "v_mfma_f32_16x16x32_bf16 v[208:211], v[32:35], v[240:243], v[208:211]\n\t" \
    "s_waitcnt lgkmcnt(3)\n\t" \
    "v_mfma_f32_16x16x32_bf16 v[216:219], v[24:27], v[224:227], v[216:219]\n\t" \
    "ds_read_b128 v[240:243], v252 offset:16384\n\t" \
    "v_mfma_f32_16x16x32_bf16 v[220:223], v[32:35], v[224:227], v[220:223]\n\t" \
    "s_waitcnt lgkmcnt(3)\n\t" \
    "v_mfma_f32_16x16x32_bf16 v[204:207], v[28:31], v[228:231], v[204:207]\n\t" \
    "ds_read_b128 v[224:227], v252 offset:1024\n\t" \
    "v_mfma_f32_16x16x32_bf16 v[208:211], v[36:39], v[228:231], v[208:211]\n\t" \
    "s_waitcnt lgkmcnt(3)\n\t" \
    "v_mfma_f32_16x16x32_bf16 v[216:219], v[28:31], v[232:235], v[216:219]\n\t" \
    "ds_read_b128 v[228:231], v252 offset:17408\n\t" \
    "v_mfma_f32_16x16x32_bf16 v[220:223], v[36:39], v[232:235], v[220:223]\n\t" \
    "s_waitcnt lgkmcnt(3)\n\t" \
    "v_mfma_f32_16x16x32_bf16 v[204:207], v[40:43], v[236:239], v[204:207]\n\t" \
    "v_xor_b32 v252, 0x1800, %[xa]\n\t" \
    "ds_read_b128 v[232:235], v252 offset:0\n\t" \
    "v_mfma_f32_16x16x32_bf16 v[208:211], v[48:51], v[236:239], v[208:211]\n\t" \
    "s_waitcnt lgkmcnt(3)\n\t" \
    "v_mfma_f32_16x16x32_bf16 v[216:219], v[40:43], v[240:243], v[216:219]\n\t" \
    "ds_read_b128 v[236:239], v252 offset:16384\n\t" \
    "v_mfma_f32_16x16x32_bf16 v[220:223], v[48:51], v[240:243], v[220:223]\n\t" \
    "s_waitcnt lgkmcnt(3)\n\t" \
    "v_mfma_f32_16x16x32_bf16 v[204:207], v[44:47], v[224:227], v[204:207]\n\t" \
    "ds_read_b128 v[240:243], v252 offset:1024\n\t" \
    "v_mfma_f32_16x16x32_bf16 v[208:211], v[52:55], v[224:227], v[208:211]\n\t" \
    "s_waitcnt lgkmcnt(3)\n\t" \
    "v_mfma_f32_16x16x32_bf16 v[216:219], v[44:47], v[228:231], v[216:219]\n\t" \
    "ds_read_b128 v[224:227], v252 offset:17408\n\t" \
    "v_mfma_f32_16x16x32_bf16 v[220:223], v[52:55], v[228:231], v[220:223]\n\t" \
    "s_waitcnt lgkmcnt(3)\n\t" \
    "v_mfma_f32_16x16x32_bf16 v[204:207], v[64:67], v[232:235], v[204:207]\n\t" \
    "v_and_b32 v252, 0x3ff, %[xa]\n\t" \
    "ds_read_b128 v[228:231], v252 offset:40960\n\t" \
    "v_mfma_f32_16x16x32_bf16 v[208:211], v[72:75], v[232:235], v[208:211]\n\t" \
    "s_waitcnt lgkmcnt(3)\n\t" \
    "v_mfma_f32_16x16x32_bf16 v[216:219], v[64:67], v[236:239], v[216:219]\n\t" \
    "ds_read_b128 v[232:235], v252 offset:45056\n\t" \
    "v_mfma_f32_16x16x32_bf16 v[220:223], v[72:75], v[236:239], v[220:223]\n\t" \
    "s_waitcnt lgkmcnt(3)\n\t" \
    "v_mfma_f32_16x16x32_bf16 v[204:207], v[68:71], v[240:243], v[204:207]\n\t" \
    "ds_read_b128 v[236:239], v252 offset:41984\n\t" \
    "v_mfma_f32_16x16x32_bf16 v[208:211], v[76:79], v[240:243], v[208:211]\n\t" \
    "s_waitcnt lgkmcnt(3)\n\t" \
    "v_mfma_f32_16x16x32_bf16 v[216:219], v[68:71], v[224:227], v[216:219]\n\t" \
    "ds_read_b128 v[240:243], v252 offset:46080\n\t" \
    "v_mfma_f32_16x16x32_bf16 v[220:223], v[76:79], v[224:227], v[220:223]\n\t" \
    "s_waitcnt lgkmcnt(0)\n\t" \
    "s_barrier\n\t" ASM_ST \
    "v_mfma_f32_16x16x32_bf16 v[204:207], v[160:163], v[228:231], v[204:207]\n\t" \
    "v_xor_b32 v252, 0x2000, %[xa]\n\t" \
    "ds_read_b128 v[224:227], v252 offset:0\n\t" \
    "v_mfma_f32_16x16x32_bf16 v[208:211], v[168:171], v[228:231], v[208:211]\n\t" \
    "v_mfma_f32_16x16x32_bf16 v[216:219], v[160:163], v[232:235], v[216:219]\n\t" \
    "ds_read_b128 v[228:231], v252 offset:16384\n\t" \
    "v_mfma_f32_16x16x32_bf16 v[220:223], v[168:171], v[232:235], v[220:223]\n\t" \
    "v_mfma_f32_16x16x32_bf16 v[204:207], v[164:167], v[236:239], v[204:207]\n\t" \
    "ds_read_b128 v[232:235], v252 offset:1024\n\t" \
    "v_mfma_f32_16x16x32_bf16 v[208:211], v[172:175], v[236:239], v[208:211]\n\t" \
    "v_mfma_f32_16x16x32_bf16 v[216:219], v[164:167], v[240:243], v[216:219]\n\t" \
    "ds_read_b128 v[236:239], v252 offset:17408\n\t" \
    "v_mfma_f32_16x16x32_bf16 v[220:223], v[172:175], v[240:243], v[220:223]\n\t" \
    "s_waitcnt lgkmcnt(3)\n\t" \
    "v_mfma_f32_16x16x32_bf16 v[204:207], v[80:83], v[224:227], v[204:207]\n\t" \
    "v_xor_b32 v252, 0x2800, %[xa]\n\t" \
    "ds_read_b128 v[240:243], v252 offset:0\n\t" \
    "v_mfma_f32_16x16x32_bf16 v[208:211], v[88:91], v[224:227], v[208:211]\n\t" \
    "s_waitcnt lgkmcnt(3)\n\t" \
    "v_mfma_f32_16x16x32_bf16 v[216:219], v[80:83], v[228:231], v[216:219]\n\t" \
    "ds_read_b128 v[224:227], v252 offset:16384\n\t" \
    "v_mfma_f32_16x16x32_bf16 v[220:223], v[88:91], v[228:231], v[220:223]\n\t" \
    "s_waitcnt lgkmcnt(3)\n\t" \
    "v_mfma_f32_16x16x32_bf16 v[204:207], v[84:87], v[232:235], v[204:207]\n\t" \
    "ds_read_b128 v[228:231], v252 offset:1024\n\t" \
    "v_mfma_f32_16x16x32_bf16 v[208:211], v[92:95], v[232:235], v[208:211]\n\t" \
    "s_waitcnt lgkmcnt(3)\n\t" \
    "v_mfma_f32_16x16x32_bf16 v[216:219], v[84:87], v[236:239], v[216:219]\n\t" \
    "ds_read_b128 v[232:235], v252 offset:17408\n\t" \
    "v_mfma_f32_16x16x32_bf16 v[220:223], v[92:95], v[236:239], v[220:223]\n\t" \
    "s_waitcnt lgkmcnt(3)\n\t" \
    "v_mfma_f32_16x16x32_bf16 v[204:207], v[104:107], v[240:243], v[204:207]\n\t" \
    "v_xor_b32 v252, 0x3000, %[xa]\n\t" \
    "ds_read_b128 v[236:239], v252 offset:0\n\t" \
    "v_mfma_f32_16x16x32_bf16 v[208:211], v[112:115], v[240:243], v[208:211]\n\t" \
    "s_waitcnt lgkmcnt(3)\n\t" \
    "v_mfma_f32_16x16x32_bf16 v[216:219], v[104:107], v[224:227], v[216:219]\n\t" \
    "ds_read_b128 v[240:243], v252 offset:16384\n\t" \
    "v_mfma_f32_16x16x32_bf16 v[220:223], v[112:115], v[224:227], v[220:223]\n\t" \
    "s_waitcnt lgkmcnt(3)\n\t" \
    "v_mfma_f32_16x16x32_bf16 v[204:207], v[108:111], v[228:231], v[204:207]\n\t" \
    "ds_read_b128 v[224:227], v252 offset:1024\n\t" \
    "v_mfma_f32_16x16x32_bf16 v[208:211], v[116:119], v[228:231], v[208:211]\n\t" \
    "s_waitcnt lgkmcnt(3)\n\t" \
    "v_mfma_f32_16x16x32_bf16 v[216:219], v[108:111], v[232:235], v[216:219]\n\t" \
    "ds_read_b128 v[228:231], v252 offset:17408\n\t" \
    "v_mfma_f32_16x16x32_bf16 v[220:223], v[116:119], v[232:235], v[220:223]\n\t" \
    "s_waitcnt lgkmcnt(3)\n\t" \
    "v_mfma_f32_16x16x32_bf16 v[204:207], v[120:123], v[236:239], v[204:207]\n\t" \
    "v_xor_b32 v252, 0x3800, %[xa]\n\t" \
    "ds_read_b128 v[232:235], v252 offset:0\n\t" \
    "v_mfma_f32_16x16x32_bf16 v[208:211], v[128:131], v[236:239], v[208:211]\n\t" \
    "s_waitcnt lgkmcnt(3)\n\t" \
    "v_mfma_f32_16x16x32_bf16 v[216:219], v[120:123], v[240:243], v[216:219]\n\t" \
    "ds_read_b128 v[236:239], v252 offset:16384\n\t" \
    "v_mfma_f32_16x16x32_bf16 v[220:223], v[128:131], v[240:243], v[220:223]\n\t" \
    "s_waitcnt lgkmcnt(3)\n\t" \
    "v_mfma_f32_16x16x32_bf16 v[204:207], v[124:127], v[224:227], v[204:207]\n\t" \
    "ds_read_b128 v[240:243], v252 offset:1024\n\t" \
    "v_mfma_f32_16x16x32_bf16 v[208:211], v[132:135], v[224:227], v[208:211]\n\t" \
    "s_waitcnt lgkmcnt(3)\n\t" \
    "v_mfma_f32_16x16x32_bf16 v[216:219], v[124:127], v[228:231], v[216:219]\n\t" \
    "ds_read_b128 v[224:227], v252 offset:17408\n\t" \
    "v_mfma_f32_16x16x32_bf16 v[220:223], v[132:135], v[228:231], v[220:223]\n\t" \
    "s_waitcnt lgkmcnt(3)\n\t" \
    "v_mfma_f32_16x16x32_bf16 v[204:207], v[144:147], v[232:235], v[204:207]\n\t" \
    "v_and_b32 v252, 0x3ff, %[xa]\n\t" \
    "ds_read_b128 v[228:231], v252 offset:43008\n\t" \
    "v_mov_b64 v[244:245], 0\n\t" \
    "v_mfma_f32_16x16x32_bf16 v[208:211], v[152:155], v[232:235], v[208:211]\n\t" \
    "v_mov_b64 v[246:247], 0\n\t" \
    "s_waitcnt lgkmcnt(3)\n\t" \
    "v_mfma_f32_16x16x32_bf16 v[216:219], v[144:147], v[236:239], v[216:219]\n\t" \
    "ds_read_b128 v[232:235], v252 offset:44032\n\t" \
    "v_mov_b64 v[248:249], 0\n\t" \
    "v_mfma_f32_16x16x32_bf16 v[220:223], v[152:155], v[236:239], v[220:223]\n\t" \
    "v_mov_b64 v[250:251], 0\n\t" \
    "s_waitcnt lgkmcnt(3)\n\t" \
    "v_mfma_f32_16x16x32_bf16 v[204:207], v[148:151], v[240:243], v[204:207]\n\t" \
    "ds_read_b128 v[236:239], v252 offset:47104\n\t" \
    "v_mfma_f32_16x16x32_bf16 v[208:211], v[156:159], v[240:243], v[208:211]\n\t" \
    "s_waitcnt lgkmcnt(3)\n\t" \
    "v_mfma_f32_16x16x32_bf16 v[216:219], v[148:151], v[224:227], v[216:219]\n\t" \
    "ds_read_b128 v[240:243], v252 offset:48128\n\t" \
    "v_mfma_f32_16x16x32_bf16 v[220:223], v[156:159], v[224:227], v[220:223]\n\t" \
    "s_waitcnt lgkmcnt(3)\n\t" \
    "v_mfma_f32_16x16x32_bf16 v[204:207], v[184:187], v[228:231], v[204:207]\n\t" \
    "v_add_u32 v252, %[qs], v252\n\t" \
    "v_mfma_f32_16x16x32_bf16 v[208:211], v[192:195], v[228:231], v[208:211]\n\t" \
    "v_mov_b64 v[224:225], 0\n\t" \
    "s_waitcnt lgkmcnt(2)\n\t" \
    "v_mfma_f32_16x16x32_bf16 v[204:207], v[188:191], v[232:235], v[204:207]\n\t" \
    "v_mov_b64 v[226:227], 0\n\t" \
    "v_mfma_f32_16x16x32_bf16 v[208:211], v[196:199], v[232:235], v[208:211]\n\t" \
    "s_waitcnt lgkmcnt(1)\n\t" \
    "v_mfma_f32_16x16x32_bf16 v[216:219], v[184:187], v[236:239], v[216:219]\n\t" \
    "ds_read_b128 v[228:231], %[wl] offset:2048\n\t" \
    "ds_read_b128 v[232:235], %[wl] offset:3072\n\t" \
    "v_mfma_f32_16x16x32_bf16 v[220:223], v[192:195], v[236:239], v[220:223]\n\t" \
    "v_cvt_pk_bf16_f32 v204, v204, v205\n\t" \
    "v_cvt_pk_bf16_f32 v205, v206, v207\n\t" \
    "s_waitcnt lgkmcnt(2)\n\t" \
    "v_mfma_f32_16x16x32_bf16 v[216:219], v[188:191], v[240:243], v[216:219]\n\t" \
    "ds_write_b64 v252, v[204:205] offset:1024\n\t" \
    "v_cvt_pk_bf16_f32 v208, v208, v209\n\t" \
    "v_mfma_f32_16x16x32_bf16 v[220:223], v[196:199], v[240:243], v[220:223]\n\t" \
    "v_cvt_pk_bf16_f32 v209, v210, v211\n\t" \
    "ds_write_b64 v252, v[208:209] offset:2048\n\t" \
    "ds_read_b128 v[200:203], %[wl] offset:0\n\t" \
    "ds_read_b128 v[204:207], %[wl] offset:1024\n\t" \
    "v_cvt_pk_bf16_f32 v216, v216, v217\n\t" \
    "v_cvt_pk_bf16_f32 v217, v218, v219\n\t" \
    "ds_write_b64 v252, v[216:217] offset:7168\n\t" \
    "s_nop 0\n\t" \
    "v_cvt_pk_bf16_f32 v220, v220, v221\n\t" \
    "v_cvt_pk_bf16_f32 v221, v222, v223\n\t" \
    "ds_write_b64 v252, v[220:221] offset:8192\n\t" \
    "s_waitcnt lgkmcnt(0)\n\t"
#define ASM_XG_V \
    "s_waitcnt lgkmcnt(0)\n\t" \
    "ds_read_b128 v[224:227], %[xa] offset:0\n\t" \
    "ds_read_b128 v[244:247], %[wl] offset:0\n\t" \
    "ds_read_b128 v[228:231], %[xa] offset:16384\n\t" \
    "ds_read_b128 v[232:235], %[xa] offset:1024\n\t" \
    "ds_read_b128 v[236:239], %[xa] offset:17408\n\t" \
    "ds_read_b128 v[248:251], %[wl] offset:1024\n\t" \
    "s_waitcnt lgkmcnt(5)\n\t" \
    "v_mfma_f32_16x16x32_bf16 v[204:207], v[0:3], v[224:227], 0\n\t" \
    "v_xor_b32 v252, 0x800, %[xa]\n\t" \
    "ds_read_b128 v[240:243], v252 offset:0\n\t" \
    "v_mfma_f32_16x16x32_bf16 v[208:211], v[8:11], v[224:227], 0\n\t" \
    "s_waitcnt lgkmcnt(5)\n\t" \
    "v_mfma_f32_16x16x32_bf16 v[200:203], v[244:247], v[224:227], 0\n\t" \
    "s_waitcnt lgkmcnt(4)\n\t" \
    "v_mfma_f32_16x16x32_bf16 v[216:219], v[0:3], v[228:231], 0\n\t" \
    "ds_read_b128 v[224:227], v252 offset:16384\n\t" \
    "v_mfma_f32_16x16x32_bf16 v[220:223], v[8:11], v[228:231], 0\n\t" \
    "v_mfma_f32_16x16x32_bf16 v[212:215], v[244:247], v[228:231], 0\n\t" \
    "s_waitcnt lgkmcnt(4)\n\t" \
    "v_mfma_f32_16x16x32_bf16 v[204:207], v[4:7], v[232:235], v[204:207]\n\t" \
    "ds_read_b128 v[228:231], v252 offset:1024\n\t" \
    "v_mfma_f32_16x16x32_bf16 v[208:211], v[12:15], v[232:235], v[208:211]\n\t" \
    "ds_read_b128 v[244:247], %[wl] offset:2048\n\t" \
    "s_waitcnt lgkmcnt(4)\n\t" \
    "v_mfma_f32_16x16x32_bf16 v[200:203], v[248:251], v[232:235], v[200:203]\n\t" \
    "v_mfma_f32_16x16x32_bf16 v[216:219], v[4:7], v[236:239], v[216:219]\n\t" \
    "ds_read_b128 v[232:235], v252 offset:17408\n\t" \
    "v_mfma_f32_16x16x32_bf16 v[220:223], v[12:15], v[236:239], v[220:223]\n\t" \
    "v_mfma_f32_16x16x32_bf16 v[212:215], v[248:251], v[236:239], v[212:215]\n\t" \
    "s_waitcnt lgkmcnt(4)\n\t" \
    "v_mfma_f32_16x16x32_bf16 v[204:207], v[24:27], v[240:243], v[204:207]\n\t" \
    "v_xor_b32 v252, 0x1000, %[xa]\n\t" \
    "ds_read_b128 v[236:239], v252 offset:0\n\t" \
    "v_mfma_f32_16x16x32_bf16 v[208:211], v[32:35], v[240:243], v[208:211]\n\t" \
    "ds_read_b128 v[248:251], %[wl] offset:3072\n\t" \
    "v_mfma_f32_16x16x32_bf16 v[200:203], v[16:19], v[240:243], v[200:203]\n\t" \
    "s_waitcnt lgkmcnt(5)\n\t" \
    "v_mfma_f32_16x16x32_bf16 v[216:219], v[24:27], v[224:227], v[216:219]\n\t" \
    "ds_read_b128 v[240:243], v252 offset:16384\n\t" \
    "v_mfma_f32_16x16x32_bf16 v[220:223], v[32:35], v[224:227], v[220:223]\n\t" \
    "v_mfma_f32_16x16x32_bf16 v[212:215], v[16:19], v[224:227], v[212:215]\n\t" \
    "s_waitcnt lgkmcnt(5)\n\t" \
    "v_mfma_f32_16x16x32_bf16 v[204:207], v[28:31], v[228:231], v[204:207]\n\t" \
    "ds_read_b128 v[224:227], v252 offset:1024\n\t" \
    "v_mfma_f32_16x16x32_bf16 v[208:211], v[36:39], v[228:231], v[208:211]\n\t" \
    "v_mfma_f32_16x16x32_bf16 v[200:203], v[20:23], v[228:231], v[200:203]\n\t" \
    "s_waitcnt lgkmcnt(4)\n\t" \
    "v_mfma_f32_16x16x32_bf16 v[216:219], v[28:31], v[232:235], v[216:219]\n\t" \
    "ds_read_b128 v[228:231], v252 offset:17408\n\t" \
    "v_mfma_f32_16x16x32_bf16 v[220:223], v[36:39], v[232:235], v[220:223]\n\t" \
    "v_mfma_f32_16x16x32_bf16 v[212:215], v[20:23], v[232:235], v[212:215]\n\t" \
    "s_waitcnt lgkmcnt(4)\n\t" \
    "v_mfma_f32_16x16x32_bf16 v[204:207], v[40:43], v[236:239], v[204:207]\n\t" \
    "v_xor_b32 v252, 0x1800, %[xa]\n\t" \
    "ds_read_b128 v[232:235], v252 offset:0\n\t" \
    "v_mfma_f32_16x16x32_bf16 v[208:211], v[48:51], v[236:239], v[208:211]\n\t" \
    "v_mfma_f32_16x16x32_bf16 v[200:203], v[244:247], v[236:239], v[200:203]\n\t" \
    "s_waitcnt lgkmcnt(3)\n\t" \
    "v_mfma_f32_16x16x32_bf16 v[216:219], v[40:43], v[240:243], v[216:219]\n\t" \
    "ds_read_b128 v[236:239], v252 offset:16384\n\t" \
    "v_mfma_f32_16x16x32_bf16 v[220:223], v[48:51], v[240:243], v[220:223]\n\t" \
    "v_mfma_f32_16x16x32_bf16 v[212:215], v[244:247], v[240:243], v[212:215]\n\t" \
    "s_waitcnt lgkmcnt(3)\n\t" \
    "v_mfma_f32_16x16x32_bf16 v[204:207], v[44:47], v[224:227], v[204:207]\n\t" \
    "ds_read_b128 v[240:243], v252 offset:1024\n\t" \
    "v_mfma_f32_16x16x32_bf16 v[208:211], v[52:55], v[224:227], v[208:211]\n\t" \
    "ds_read_b128 v[244:247], %[wl] offset:8192\n\t" \
    "v_mfma_f32_16x16x32_bf16 v[200:203], v[248:251], v[224:227], v[200:203]\n\t" \
    "s_waitcnt lgkmcnt(4)\n\t" \
    "v_mfma_f32_16x16x32_bf16 v[216:219], v[44:47], v[228:231], v[216:219]\n\t" \
    "ds_read_b128 v[224:227], v252 offset:17408\n\t" \
    "v_mfma_f32_16x16x32_bf16 v[220:223], v[52:55], v[228:231], v[220:223]\n\t" \
    "v_mfma_f32_16x16x32_bf16 v[212:215], v[248:251], v[228:231], v[212:215]\n\t" \
    "s_waitcnt lgkmcnt(4)\n\t" \
    "v_mfma_f32_16x16x32_bf16 v[204:207], v[64:67], v[232:235], v[204:207]\n\t" \
    "v_and_b32 v252, 0x3ff, %[xa]\n\t" \
    "ds_read_b128 v[228:231], v252 offset:40960\n\t" \
    "v_mfma_f32_16x16x32_bf16 v[208:211], v[72:75], v[232:235], v[208:211]\n\t" \
    "ds_read_b128 v[248:251], %[wl] offset:9216\n\t" \
    "v_mfma_f32_16x16x32_bf16 v[200:203], v[56:59], v[232:235], v[200:203]\n\t" \
    "s_waitcnt lgkmcnt(5)\n\t" \
    "v_mfma_f32_16x16x32_bf16 v[216:219], v[64:67], v[236:239], v[216:219]\n\t" \
    "ds_read_b128 v[232:235], v252 offset:45056\n\t" \
    "v_mfma_f32_16x16x32_bf16 v[220:223], v[72:75], v[236:239], v[220:223]\n\t" \
    "v_mfma_f32_16x16x32_bf16 v[212:215], v[56:59], v[236:239], v[212:215]\n\t" \
    "s_waitcnt lgkmcnt(5)\n\t" \
    "v_mfma_f32_16x16x32_bf16 v[204:207], v[68:71], v[240:243], v[204:207]\n\t" \
    "ds_read_b128 v[236:239], v252 offset:41984\n\t" \
    "v_mfma_f32_16x16x32_bf16 v[208:211], v[76:79], v[240:243], v[208:211]\n\t" \
    "v_mfma_f32_16x16x32_bf16 v[200:203], v[60:63], v[240:243], v[200:203]\n\t" \
    "s_waitcnt lgkmcnt(4)\n\t" \
    "v_mfma_f32_16x16x32_bf16 v[216:219], v[68:71], v[224:227], v[216:219]\n\t" \
    "ds_read_b128 v[240:243], v252 offset:46080\n\t" \
    "v_mfma_f32_16x16x32_bf16 v[220:223], v[76:79], v[224:227], v[220:223]\n\t" \
    "v_mfma_f32_16x16x32_bf16 v[212:215], v[60:63], v[224:227], v[212:215]\n\t" \
    "s_waitcnt lgkmcnt(4)\n\t" \
    "v_mfma_f32_16x16x32_bf16 v[204:207], v[160:163], v[228:231], v[204:207]\n\t" \
    "v_xor_b32 v252, 0x2000, %[xa]\n\t" \
    "ds_read_b128 v[224:227], v252 offset:0\n\t" \
    "v_mfma_f32_16x16x32_bf16 v[208:211], v[168:171], v[228:231], v[208:211]\n\t" \
    "v_mfma_f32_16x16x32_bf16 v[200:203], v[244:247], v[228:231], v[200:203]\n\t" \
    "s_waitcnt lgkmcnt(3)\n\t" \
    "v_mfma_f32_16x16x32_bf16 v[216:219], v[160:163], v[232:235], v[216:219]\n\t" \
    "ds_read_b128 v[228:231], v252 offset:16384\n\t" \
    "v_mfma_f32_16x16x32_bf16 v[220:223], v[168:171], v[232:235], v[220:223]\n\t" \
    "v_mfma_f32_16x16x32_bf16 v[212:215], v[244:247], v[232:235], v[212:215]\n\t" \
    "s_waitcnt lgkmcnt(3)\n\t" \
    "v_mfma_f32_16x16x32_bf16 v[204:207], v[164:167], v[236:239], v[204:207]\n\t" \
    "ds_read_b128 v[232:235], v252 offset:1024\n\t" \
    "v_mfma_f32_16x16x32_bf16 v[208:211], v[172:175], v[236:239], v[208:211]\n\t" \
    "ds_read_b128 v[244:247], %[wl] offset:4096\n\t" \
    "v_mfma_f32_16x16x32_bf16 v[200:203], v[248:251], v[236:239], v[200:203]\n\t" \
    "s_waitcnt lgkmcnt(4)\n\t" \
    "v_mfma_f32_16x16x32_bf16 v[216:219], v[164:167], v[240:243], v[216:219]\n\t" \
    "ds_read_b128 v[236:239], v252 offset:17408\n\t" \
    "v_mfma_f32_16x16x32_bf16 v[220:223], v[172:175], v[240:243], v[220:223]\n\t" \
    "v_mfma_f32_16x16x32_bf16 v[212:215], v[248:251], v[240:243], v[212:215]\n\t" \
    "s_waitcnt lgkmcnt(4)\n\t" \
    "v_mfma_f32_16x16x32_bf16 v[204:207], v[80:83], v[224:227], v[204:207]\n\t" \
    "v_xor_b32 v252, 0x2800, %[xa]\n\t" \
    "ds_read_b128 v[240:243], v252 offset:0\n\t" \
    "v_mfma_f32_16x16x32_bf16 v[208:211], v[88:91], v[224:227], v[208:211]\n\t" \
    "ds_read_b128 v[248:251], %[wl] offset:5120\n\t" \
    "s_waitcnt lgkmcnt(3)\n\t" \
    "v_mfma_f32_16x16x32_bf16 v[200:203], v[244:247], v[224:227], v[200:203]\n\t" \
    "v_mfma_f32_16x16x32_bf16 v[216:219], v[80:83], v[228:231], v[216:219]\n\t" \
    "ds_read_b128 v[224:227], v252 offset:16384\n\t" \
    "v_mfma_f32_16x16x32_bf16 v[220:223], v[88:91], v[228:231], v[220:223]\n\t" \
    "v_mfma_f32_16x16x32_bf16 v[212:215], v[244:247], v[228:231], v[212:215]\n\t" \
    "v_mfma_f32_16x16x32_bf16 v[204:207], v[84:87], v[232:235], v[204:207]\n\t" \
    "ds_read_b128 v[228:231], v252 offset:1024\n\t" \
    "v_mfma_f32_16x16x32_bf16 v[208:211], v[92:95], v[232:235], v[208:211]\n\t" \
    "ds_read_b128 v[244:247], %[wl] offset:6144\n\t" \
    "s_waitcnt lgkmcnt(3)\n\t" \
    "v_mfma_f32_16x16x32_bf16 v[200:203], v[248:251], v[232:235], v[200:203]\n\t" \
    "v_mfma_f32_16x16x32_bf16 v[216:219], v[84:87], v[236:239], v[216:219]\n\t" \
    "ds_read_b128 v[232:235], v252 offset:17408\n\t" \
    "v_mfma_f32_16x16x32_bf16 v[220:223], v[92:95], v[236:239], v[220:223]\n\t" \
    "v_mfma_f32_16x16x32_bf16 v[212:215], v[248:251], v[236:239], v[212:215]\n\t" \
    "v_mfma_f32_16x16x32_bf16 v[204:207], v[104:107], v[240:243], v[204:207]\n\t" \
    "v_xor_b32 v252, 0x3000, %[xa]\n\t" \
    "ds_read_b128 v[236:239], v252 offset:0\n\t" \
    "v_mfma_f32_16x16x32_bf16 v[208:211], v[112:115], v[240:243], v[208:211]\n\t" \
    "ds_read_b128 v[248:251], %[wl] offset:7168\n\t" \
    "v_mfma_f32_16x16x32_bf16 v[200:203], v[96:99], v[240:243], v[200:203]\n\t" \
    "s_waitcnt lgkmcnt(5)\n\t" \
    "v_mfma_f32_16x16x32_bf16 v[216:219], v[104:107], v[224:227], v[216:219]\n\t" \
    "ds_read_b128 v[240:243], v252 offset:16384\n\t" \
    "v_mfma_f32_16x16x32_bf16 v[220:223], v[112:115], v[224:227], v[220:223]\n\t" \
    "v_mfma_f32_16x16x32_bf16 v[212:215], v[96:99], v[224:227], v[212:215]\n\t" \
    "s_waitcnt lgkmcnt(5)\n\t" \
    "v_mfma_f32_16x16x32_bf16 v[204:207], v[108:111], v[228:231], v[204:207]\n\t" \
    "ds_read_b128 v[224:227], v252 offset:1024\n\t" \
    "v_mfma_f32_16x16x32_bf16 v[208:211], v[116:119], v[228:231], v[208:211]\n\t" \
    "v_mfma_f32_16x16x32_bf16 v[200:203], v[100:103], v[228:231], v[200:203]\n\t" \
    "s_waitcnt lgkmcnt(4)\n\t" \
    "v_mfma_f32_16x16x32_bf16 v[216:219], v[108:111], v[232:235], v[216:219]\n\t" \
    "ds_read_b128 v[228:231], v252 offset:17408\n\t" \
    "v_mfma_f32_16x16x32_bf16 v[220:223], v[116:119], v[232:235], v[220:223]\n\t" \
    "v_mfma_f32_16x16x32_bf16 v[212:215], v[100:103], v[232:235], v[212:215]\n\t" \
    "s_waitcnt lgkmcnt(4)\n\t" \
    "v_mfma_f32_16x16x32_bf16 v[204:207], v[120:123], v[236:239], v[204:207]\n\t" \
    "v_xor_b32 v252, 0x3800, %[xa]\n\t" \
    "ds_read_b128 v[232:235], v252 offset:0\n\t" \
    "v_mfma_f32_16x16x32_bf16 v[208:211], v[128:131], v[236:239], v[208:211]\n\t" \
    "v_mfma_f32_16x16x32_bf16 v[200:203], v[244:247], v[236:239], v[200:203]\n\t" \
    "s_waitcnt lgkmcnt(3)\n\t" \
    "v_mfma_f32_16x16x32_bf16 v[216:219], v[120:123], v[240:243], v[216:219]\n\t" \
    "ds_read_b128 v[236:239], v252 offset:16384\n\t" \
    "v_mfma_f32_16x16x32_bf16 v[220:223], v[128:131], v[240:243], v[220:223]\n\t" \
    "v_mfma_f32_16x16x32_bf16 v[212:215], v[244:247], v[240:243], v[212:215]\n\t" \
    "s_waitcnt lgkmcnt(3)\n\t" \
    "v_mfma_f32_16x16x32_bf16 v[204:207], v[124:127], v[224:227], v[204:207]\n\t" \
    "ds_read_b128 v[240:243], v252 offset:1024\n\t" \
    "v_mfma_f32_16x16x32_bf16 v[208:211], v[132:135], v[224:227], v[208:211]\n\t" \
    "v_mfma_f32_16x16x32_bf16 v[200:203], v[248:251], v[224:227], v[200:203]\n\t" \
    "s_waitcnt lgkmcnt(3)\n\t" \
    "v_mfma_f32_16x16x32_bf16 v[216:219], v[124:127], v[228:231], v[216:219]\n\t" \
    "ds_read_b128 v[224:227], v252 offset:17408\n\t" \
    "v_mfma_f32_16x16x32_bf16 v[220:223], v[132:135], v[228:231], v[220:223]\n\t" \
    "v_mfma_f32_16x16x32_bf16 v[212:215], v[248:251], v[228:231], v[212:215]\n\t" \
    "s_waitcnt lgkmcnt(3)\n\t" \
    "v_mfma_f32_16x16x32_bf16 v[204:207], v[144:147], v[232:235], v[204:207]\n\t" \
    "v_and_b32 v252, 0x3ff, %[xa]\n\t" \
    "ds_read_b128 v[228:231], v252 offset:43008\n\t" \
    "v_mov_b64 v[244:245], 0\n\t" \
    "v_mfma_f32_16x16x32_bf16 v[208:211], v[152:155], v[232:235], v[208:211]\n\t" \
    "v_mov_b64 v[246:247], 0\n\t" \
    "v_mfma_f32_16x16x32_bf16 v[200:203], v[136:139], v[232:235], v[200:203]\n\t" \
    "v_mov_b64 v[248:249], 0\n\t" \
    "s_waitcnt lgkmcnt(3)\n\t" \
    "v_mfma_f32_16x16x32_bf16 v[216:219], v[144:147], v[236:239], v[216:219]\n\t" \
    "ds_read_b128 v[232:235], v252 offset:44032\n\t" \
    "v_mov_b64 v[250:251], 0\n\t" \
    "v_mfma_f32_16x16x32_bf16 v[220:223], v[152:155], v[236:239], v[220:223]\n\t" \
    "v_mfma_f32_16x16x32_bf16 v[212:215], v[136:139], v[236:239], v[212:215]\n\t" \
    "s_waitcnt lgkmcnt(3)\n\t" \
    "v_mfma_f32_16x16x32_bf16 v[204:207], v[148:151], v[240:243], v[204:207]\n\t" \
    "ds_read_b128 v[236:239], v252 offset:47104\n\t" \
    "v_mfma_f32_16x16x32_bf16 v[208:211], v[156:159], v[240:243], v[208:211]\n\t" \
    "v_mfma_f32_16x16x32_bf16 v[200:203], v[140:143], v[240:243], v[200:203]\n\t" \
    "s_waitcnt lgkmcnt(3)\n\t" \
    "v_mfma_f32_16x16x32_bf16 v[216:219], v[148:151], v[224:227], v[216:219]\n\t" \
    "ds_read_b128 v[240:243], v252 offset:48128\n\t" \
    "v_mfma_f32_16x16x32_bf16 v[220:223], v[156:159], v[224:227], v[220:223]\n\t" \
    "v_mfma_f32_16x16x32_bf16 v[212:215], v[140:143], v[224:227], v[212:215]\n\t" \
    "s_waitcnt lgkmcnt(3)\n\t" \
    "v_mfma_f32_16x16x32_bf16 v[204:207], v[184:187], v[228:231], v[204:207]\n\t" \
    "v_add_u32 v252, %[qs], v252\n\t" \
    "v_mfma_f32_16x16x32_bf16 v[208:211], v[192:195], v[228:231], v[208:211]\n\t" \
    "v_mov_b64 v[224:225], 0\n\t" \
    "v_mfma_f32_16x16x32_bf16 v[200:203], v[176:179], v[228:231], v[200:203]\n\t" \
    "v_mov_b64 v[226:227], 0\n\t" \
    "s_waitcnt lgkmcnt(2)\n\t" \
    "v_mfma_f32_16x16x32_bf16 v[204:207], v[188:191], v[232:235], v[204:207]\n\t" \
    "v_mfma_f32_16x16x32_bf16 v[208:211], v[196:199], v[232:235], v[208:211]\n\t" \
    "v_mfma_f32_16x16x32_bf16 v[200:203], v[180:183], v[232:235], v[200:203]\n\t" \
    "s_waitcnt lgkmcnt(1)\n\t" \
    "v_mfma_f32_16x16x32_bf16 v[216:219], v[184:187], v[236:239], v[216:219]\n\t" \
    "ds_read_b128 v[228:231], %[wl] offset:2048\n\t" \
    "ds_read_b128 v[232:235], %[wl] offset:3072\n\t" \
    "v_mfma_f32_16x16x32_bf16 v[220:223], v[192:195], v[236:239], v[220:223]\n\t" \
    "v_cvt_pk_bf16_f32 v204, v204, v205\n\t" \
    "v_cvt_pk_bf16_f32 v205, v206, v207\n\t" \
    "v_mfma_f32_16x16x32_bf16 v[212:215], v[176:179], v[236:239], v[212:215]\n\t" \
    "ds_write_b64 v252, v[204:205] offset:1024\n\t" \
    "v_cvt_pk_bf16_f32 v208, v208, v209\n\t" \
    "s_waitcnt lgkmcnt(3)\n\t" \
    "v_mfma_f32_16x16x32_bf16 v[216:219], v[188:191], v[240:243], v[216:219]\n\t" \
    "v_cvt_pk_bf16_f32 v209, v210, v211\n\t" \
    "ds_write_b64 v252, v[208:209] offset:2048\n\t" \
    "v_mfma_f32_16x16x32_bf16 v[220:223], v[196:199], v[240:243], v[220:223]\n\t" \
    "v_cvt_pk_bf16_f32 v200, v200, v201\n\t" \
    "v_cvt_pk_bf16_f32 v201, v202, v203\n\t" \
    "v_mfma_f32_16x16x32_bf16 v[212:215], v[180:183], v[240:243], v[212:215]\n\t" \
    "ds_write_b64 v252, v[200:201] offset:0\n\t" \
    "ds_read_b128 v[200:203], %[wl] offset:0\n\t" \
    "ds_read_b128 v[204:207], %[wl] offset:1024\n\t" \
    "v_cvt_pk_bf16_f32 v216, v216, v217\n\t" \
    "v_cvt_pk_bf16_f32 v217, v218, v219\n\t" \
    "ds_write_b64 v252, v[216:217] offset:7168\n\t" \
    "v_cvt_pk_bf16_f32 v220, v220, v221\n\t" \
    "v_cvt_pk_bf16_f32 v221, v222, v223\n\t" \
    "ds_write_b64 v252, v[220:221] offset:8192\n\t" \
    "v_cvt_pk_bf16_f32 v212, v212, v213\n\t" \
    "v_cvt_pk_bf16_f32 v213, v214, v215\n\t" \
    "ds_write_b64 v252, v[212:213] offset:6144\n\t" \
    "s_waitcnt lgkmcnt(0)\n\t"
#define ASM_XG_H_HI \
    "buffer_load_dword %[fv], %[fv], %[rr], %[fs] offen sc1\n\t" \
    "s_waitcnt lgkmcnt(0)\n\t" \
    "v_xor_b32 v252, 0x2000, %[xa]\n\t" \
    "ds_read_b128 v[224:227], v252 offset:0\n\t" \
    "ds_read_b128 v[244:247], %[wl] offset:4096\n\t" \
    "ds_read_b128 v[228:231], v252 offset:16384\n\t" \
    "ds_read_b128 v[232:235], v252 offset:1024\n\t" \
    "ds_read_b128 v[236:239], v252 offset:17408\n\t" \
    "ds_read_b128 v[248:251], %[wl] offset:5120\n\t" \
    "s_waitcnt lgkmcnt(5)\n\t" \
    "v_mfma_f32_16x16x32_bf16 v[204:207], v[80:83], v[224:227], 0\n\t" \
    "v_xor_b32 v252, 0x2800, %[xa]\n\t" \
    "ds_read_b128 v[240:243], v252 offset:0\n\t" \
    "v_mfma_f32_16x16x32_bf16 v[208:211], v[88:91], v[224:227], 0\n\t" \
    "s_waitcnt lgkmcnt(5)\n\t" \
    "v_mfma_f32_16x16x32_bf16 v[200:203], v[244:247], v[224:227], 0\n\t" \
    "s_waitcnt lgkmcnt(4)\n\t" \
    "v_mfma_f32_16x16x32_bf16 v[216:219], v[80:83], v[228:231], 0\n\t" \
    "ds_read_b128 v[224:227], v252 offset:16384\n\t" \
    "v_mfma_f32_16x16x32_bf16 v[220:223], v[88:91], v[228:231], 0\n\t" \
    "v_mfma_f32_16x16x32_bf16 v[212:215], v[244:247], v[228:231], 0\n\t" \
    "s_waitcnt lgkmcnt(4)\n\t" \
    "v_mfma_f32_16x16x32_bf16 v[204:207], v[84:87], v[232:235], v[204:207]\n\t" \
    "ds_read_b128 v[228:231], v252 offset:1024\n\t" \
    "v_mfma_f32_16x16x32_bf16 v[208:211], v[92:95], v[232:235], v[208:211]\n\t" \
    "ds_read_b128 v[244:247], %[wl] offset:6144\n\t" \
    "s_waitcnt lgkmcnt(4)\n\t" \
    "v_mfma_f32_16x16x32_bf16 v[200:203], v[248:251], v[232:235], v[200:203]\n\t" \
    "v_mfma_f32_16x16x32_bf16 v[216:219], v[84:87], v[236:239], v[216:219]\n\t" \
    "ds_read_b128 v[232:235], v252 offset:17408\n\t" \
    "v_mfma_f32_16x16x32_bf16 v[220:223], v[92:95], v[236:239], v[220:223]\n\t" \
    "v_mfma_f32_16x16x32_bf16 v[212:215], v[248:251], v[236:239], v[212:215]\n\t" \
    "s_waitcnt lgkmcnt(4)\n\t" \
    "v_mfma_f32_16x16x32_bf16 v[204:207], v[104:107], v[240:243], v[204:207]\n\t" \
    "v_xor_b32 v252, 0x3000, %[xa]\n\t" \
    "ds_read_b128 v[236:239], v252 offset:0\n\t" \
    "v_mfma_f32_16x16x32_bf16 v[208:211], v[112:115], v[240:243], v[208:211]\n\t" \
    "ds_read_b128 v[248:251], %[wl] offset:7168\n\t" \
    "v_mfma_f32_16x16x32_bf16 v[200:203], v[96:99], v[240:243], v[200:203]\n\t" \
    "s_waitcnt lgkmcnt(5)\n\t" \
    "v_mfma_f32_16x16x32_bf16 v[216:219], v[104:107], v[224:227], v[216:219]\n\t" \
    "ds_read_b128 v[240:243], v252 offset:16384\n\t" \
    "v_mfma_f32_16x16x32_bf16 v[220:223], v[112:115], v[224:227], v[220:223]\n\t" \
    "v_mfma_f32_16x16x32_bf16 v[212:215], v[96:99], v[224:227], v[212:215]\n\t" \
    "s_waitcnt lgkmcnt(5)\n\t" \
    "v_mfma_f32_16x16x32_bf16 v[204:207], v[108:111], v[228:231], v[204:207]\n\t" \
    "ds_read_b128 v[224:227], v252 offset:1024\n\t" \
    "v_mfma_f32_16x16x32_bf16 v[208:211], v[116:119], v[228:231], v[208:211]\n\t" \
    "v_mfma_f32_16x16x32_bf16 v[200:203], v[100:103], v[228:231], v[200:203]\n\t" \
    "s_waitcnt lgkmcnt(4)\n\t" \
    "v_mfma_f32_16x16x32_bf16 v[216:219], v[108:111], v[232:235], v[216:219]\n\t" \
    "ds_read_b128 v[228:231], v252 offset:17408\n\t" \
    "v_mfma_f32_16x16x32_bf16 v[220:223], v[116:119], v[232:235], v[220:223]\n\t" \
    "v_mfma_f32_16x16x32_bf16 v[212:215], v[100:103], v[232:235], v[212:215]\n\t" \
    "s_waitcnt lgkmcnt(4)\n\t" \
    "v_mfma_f32_16x16x32_bf16 v[204:207], v[120:123], v[236:239], v[204:207]\n\t" \
    "v_xor_b32 v252, 0x3800, %[xa]\n\t" \
    "ds_read_b128 v[232:235], v252 offset:0\n\t" \
    "v_mfma_f32_16x16x32_bf16 v[208:211], v[128:131], v[236:239], v[208:211]\n\t" \
    "v_mfma_f32_16x16x32_bf16 v[200:203], v[244:247], v[236:239], v[200:203]\n\t" \
    "s_waitcnt lgkmcnt(3)\n\t" \
    "v_mfma_f32_16x16x32_bf16 v[216:219], v[120:123], v[240:243], v[216:219]\n\t" \
    "ds_read_b128 v[236:239], v252 offset:16384\n\t" \
    "v_mfma_f32_16x16x32_bf16 v[220:223], v[128:131], v[240:243], v[220:223]\n\t" \
    "v_mfma_f32_16x16x32_bf16 v[212:215], v[244:247], v[240:243], v[212:215]\n\t" \
    "s_waitcnt lgkmcnt(3)\n\t" \
    "v_mfma_f32_16x16x32_bf16 v[204:207], v[124:127], v[224:227], v[204:207]\n\t" \
    "ds_read_b128 v[240:243], v252 offset:1024\n\t" \
    "v_mfma_f32_16x16x32_bf16 v[208:211], v[132:135], v[224:227], v[208:211]\n\t" \
    "ds_read_b128 v[244:247], %[wl] offset:8192\n\t" \
    "v_mfma_f32_16x16x32_bf16 v[200:203], v[248:251], v[224:227], v[200:203]\n\t" \
    "s_waitcnt lgkmcnt(4)\n\t" \
    "v_mfma_f32_16x16x32_bf16 v[216:219], v[124:127], v[228:231], v[216:219]\n\t" \
    "ds_read_b128 v[224:227], v252 offset:17408\n\t" \
    "v_mfma_f32_16x16x32_bf16 v[220:223], v[132:135], v[228:231], v[220:223]\n\t" \
    "v_mfma_f32_16x16x32_bf16 v[212:215], v[248:251], v[228:231], v[212:215]\n\t" \
    "s_waitcnt lgkmcnt(4)\n\t" \
    "v_mfma_f32_16x16x32_bf16 v[204:207], v[144:147], v[232:235], v[204:207]\n\t" \
    "v_and_b32 v252, 0x3ff, %[xa]\n\t" \
    "ds_read_b128 v[228:231], v252 offset:40960\n\t" \
    "v_mfma_f32_16x16x32_bf16 v[208:211], v[152:155], v[232:235], v[208:211]\n\t" \
    "ds_read_b128 v[248:251], %[wl] offset:9216\n\t" \
    "v_mfma_f32_16x16x32_bf16 v[200:203], v[136:139], v[232:235], v[200:203]\n\t" \
    "s_waitcnt lgkmcnt(5)\n\t" \
    "v_mfma_f32_16x16x32_bf16 v[216:219], v[144:147], v[236:239], v[216:219]\n\t" \
    "ds_read_b128 v[232:235], v252 offset:45056\n\t" \
    "v_mfma_f32_16x16x32_bf16 v[220:223], v[152:155], v[236:239], v[220:223]\n\t" \
    "v_mfma_f32_16x16x32_bf16 v[212:215], v[136:139], v[236:239], v[212:215]\n\t" \
    "s_waitcnt lgkmcnt(5)\n\t" \
    "v_mfma_f32_16x16x32_bf16 v[204:207], v[148:151], v[240:243], v[204:207]\n\t" \
    "ds_read_b128 v[236:239], v252 offset:41984\n\t" \
    "v_mfma_f32_16x16x32_bf16 v[208:211], v[156:159], v[240:243], v[208:211]\n\t" \
    "v_mfma_f32_16x16x32_bf16 v[200:203], v[140:143], v[240:243], v[200:203]\n\t" \
    "s_waitcnt lgkmcnt(4)\n\t" \
    "v_mfma_f32_16x16x32_bf16 v[216:219], v[148:151], v[224:227], v[216:219]\n\t" \
    "ds_read_b128 v[240:243], v252 offset:46080\n\t" \
    "v_mfma_f32_16x16x32_bf16 v[220:223], v[156:159], v[224:227], v[220:223]\n\t" \
    "v_mfma_f32_16x16x32_bf16 v[212:215], v[140:143], v[224:227], v[212:215]\n\t" \
    "s_nop 3\n\t" \
    "v_lshrrev_b32 v224, 2, v252\n\t" \
    "v_mov_b32 v225, %[tv]\n\t" \
    "s_cmp_eq_u32 %[go], 0\n\t" \
    "s_waitcnt vmcnt(0) lgkmcnt(0)\n\t" \
    "s_cbranch_scc1 .Lxp%=\n\t" \
    "s_mov_b32 exec_hi, 0\n\t" \
    "buffer_store_dword v225, v224, %[rr], %[fp] offen sc1\n\t" \
    "s_mov_b32 exec_hi, -1\n\t" \
    ".Lxp%=:\n\t" \
    "v_cmp_ge_u32 vcc, %[t1], %[fv]\n\t" \
    "s_barrier\n\t" ASM_ST \
    "s_cmp_eq_u64 vcc, 0\n\t" \
    "s_cselect_b32 %[go], 1, %[frc]\n\t" \
    "s_cmp_lt_u32 %[t1], 64\n\t" \
    "s_cselect_b32 %[go], %[go], 0\n\t" \
    "s_cmp_eq_u32 %[go], 0\n\t" \
    "s_cbranch_scc1 .Lxn%=\n\t" \
    "s_mov_b32 m0, %[d0]\n\t" \
    "s_nop 0\n\t" \
    "global_load_lds_dwordx4 v252, %[g0] offset:0 sc1\n\t" \
    "global_load_lds_dwordx4 v252, %[g0] offset:1024 sc1\n\t" \
    "global_load_lds_dwordx4 v252, %[g0] offset:2048 sc1\n\t" \
    "global_load_lds_dwordx4 v252, %[g0] offset:3072 sc1\n\t" \
    "s_mov_b32 m0, %[d1]\n\t" \
    "s_nop 0\n\t" \
    "global_load_lds_dwordx4 v252, %[g1] sc1\n\t" \
    ".Lxn%=:\n\t" \
    "v_mfma_f32_16x16x32_bf16 v[204:207], v[160:163], v[228:231], v[204:207]\n\t" \
    "ds_read_b128 v[224:227], %[xa] offset:0\n\t" \
    "v_mfma_f32_16x16x32_bf16 v[208:211], v[168:171], v[228:231], v[208:211]\n\t" \
    "v_mfma_f32_16x16x32_bf16 v[200:203], v[244:247], v[228:231], v[200:203]\n\t" \
    "v_mfma_f32_16x16x32_bf16 v[216:219], v[160:163], v[232:235], v[216:219]\n\t" \
    "ds_read_b128 v[228:231], %[xa] offset:16384\n\t" \
    "v_mfma_f32_16x16x32_bf16 v[220:223], v[168:171], v[232:235], v[220:223]\n\t" \
    "v_mfma_f32_16x16x32_bf16 v[212:215], v[244:247], v[232:235], v[212:215]\n\t" \
    "v_mfma_f32_16x16x32_bf16 v[204:207], v[164:167], v[236:239], v[204:207]\n\t" \
    "ds_read_b128 v[232:235], %[xa] offset:1024\n\t" \
    "v_mfma_f32_16x16x32_bf16 v[208:211], v[172:175], v[236:239], v[208:211]\n\t" \
    "ds_read_b128 v[244:247], %[wl] offset:0\n\t" \
    "v_mfma_f32_16x16x32_bf16 v[200:203], v[248:251], v[236:239], v[200:203]\n\t" \
    "v_mfma_f32_16x16x32_bf16 v[216:219], v[164:167], v[240:243], v[216:219]\n\t" \
    "ds_read_b128 v[236:239], %[xa] offset:17408\n\t" \
    "v_mfma_f32_16x16x32_bf16 v[220:223], v[172:175], v[240:243], v[220:223]\n\t" \
    "v_mfma_f32_16x16x32_bf16 v[212:215], v[248:251], v[240:243], v[212:215]\n\t" \
    "s_waitcnt lgkmcnt(4)\n\t" \
    "v_mfma_f32_16x16x32_bf16 v[204:207], v[0:3], v[224:227], v[204:207]\n\t" \
    "v_xor_b32 v252, 0x800, %[xa]\n\t" \
    "ds_read_b128 v[240:243], v252 offset:0\n\t" \
    "v_mfma_f32_16x16x32_bf16 v[208:211], v[8:11], v[224:227], v[208:211]\n\t" \
    "ds_read_b128 v[248:251], %[wl] offset:1024\n\t" \
    "s_waitcnt lgkmcnt(3)\n\t" \
    "v_mfma_f32_16x16x32_bf16 v[200:203], v[244:247], v[224:227], v[200:203]\n\t" \
    "v_mfma_f32_16x16x32_bf16 v[216:219], v[0:3], v[228:231], v[216:219]\n\t" \
    "ds_read_b128 v[224:227], v252 offset:16384\n\t" \
    "v_mfma_f32_16x16x32_bf16 v[220:223], v[8:11], v[228:231], v[220:223]\n\t" \
    "v_mfma_f32_16x16x32_bf16 v[212:215], v[244:247], v[228:231], v[212:215]\n\t" \
    "v_mfma_f32_16x16x32_bf16 v[204:207], v[4:7], v[232:235], v[204:207]\n\t" \
    "ds_read_b128 v[228:231], v252 offset:1024\n\t" \
    "v_mfma_f32_16x16x32_bf16 v[208:211], v[12:15], v[232:235], v[208:211]\n\t" \
    "ds_read_b128 v[244:247], %[wl] offset:2048\n\t" \
    "s_waitcnt lgkmcnt(3)\n\t" \
    "v_mfma_f32_16x16x32_bf16 v[200:203], v[248:251], v[232:235], v[200:203]\n\t" \
    "v_mfma_f32_16x16x32_bf16 v[216:219], v[4:7], v[236:239], v[216:219]\n\t" \
    "ds_read_b128 v[232:235], v252 offset:17408\n\t" \
    "v_mfma_f32_16x16x32_bf16 v[220:223], v[12:15], v[236:239], v[220:223]\n\t" \
    "v_mfma_f32_16x16x32_bf16 v[212:215], v[248:251], v[236:239], v[212:215]\n\t" \
    "v_mfma_f32_16x16x32_bf16 v[204:207], v[24:27], v[240:243], v[204:207]\n\t" \
    "v_xor_b32 v252, 0x1000, %[xa]\n\t" \
    "ds_read_b128 v[236:239], v252 offset:0\n\t" \
    "v_mfma_f32_16x16x32_bf16 v[208:211], v[32:35], v[240:243], v[208:211]\n\t" \
    "ds_read_b128 v[248:251], %[wl] offset:3072\n\t" \
    "v_mfma_f32_16x16x32_bf16 v[200:203], v[16:19], v[240:243], v[200:203]\n\t" \
    "s_waitcnt lgkmcnt(5)\n\t" \
    "v_mfma_f32_16x16x32_bf16 v[216:219], v[24:27], v[224:227], v[216:219]\n\t" \
    "ds_read_b128 v[240:243], v252 offset:16384\n\t" \
    "v_mfma_f32_16x16x32_bf16 v[220:223], v[32:35], v[224:227], v[220:223]\n\t" \
    "v_mfma_f32_16x16x32_bf16 v[212:215], v[16:19], v[224:227], v[212:215]\n\t" \
    "s_waitcnt lgkmcnt(5)\n\t" \
    "v_mfma_f32_16x16x32_bf16 v[204:207], v[28:31], v[228:231], v[204:207]\n\t" \
    "ds_read_b128 v[224:227], v252 offset:1024\n\t" \
    "v_mfma_f32_16x16x32_bf16 v[208:211], v[36:39], v[228:231], v[208:211]\n\t" \
    "v_mfma_f32_16x16x32_bf16 v[200:203], v[20:23], v[228:231], v[200:203]\n\t" \
    "s_waitcnt lgkmcnt(4)\n\t" \
    "v_mfma_f32_16x16x32_bf16 v[216:219], v[28:31], v[232:235], v[216:219]\n\t" \
    "ds_read_b128 v[228:231], v252 offset:17408\n\t" \
    "v_mfma_f32_16x16x32_bf16 v[220:223], v[36:39], v[232:235], v[220:223]\n\t" \
    "v_mfma_f32_16x16x32_bf16 v[212:215], v[20:23], v[232:235], v[212:215]\n\t" \
    "s_waitcnt lgkmcnt(4)\n\t" \
    "v_mfma_f32_16x16x32_bf16 v[204:207], v[40:43], v[236:239], v[204:207]\n\t" \
    "v_xor_b32 v252, 0x1800, %[xa]\n\t" \
    "ds_read_b128 v[232:235], v252 offset:0\n\t" \
    "v_mfma_f32_16x16x32_bf16 v[208:211], v[48:51], v[236:239], v[208:211]\n\t" \
    "v_mfma_f32_16x16x32_bf16 v[200:203], v[244:247], v[236:239], v[200:203]\n\t" \
    "s_waitcnt lgkmcnt(3)\n\t" \
    "v_mfma_f32_16x16x32_bf16 v[216:219], v[40:43], v[240:243], v[216:219]\n\t" \
    "ds_read_b128 v[236:239], v252 offset:16384\n\t" \
    "v_mfma_f32_16x16x32_bf16 v[220:223], v[48:51], v[240:243], v[220:223]\n\t" \
    "v_mfma_f32_16x16x32_bf16 v[212:215], v[244:247], v[240:243], v[212:215]\n\t" \
    "s_waitcnt lgkmcnt(3)\n\t" \
    "v_mfma_f32_16x16x32_bf16 v[204:207], v[44:47], v[224:227], v[204:207]\n\t" \
    "ds_read_b128 v[240:243], v252 offset:1024\n\t" \
    "v_mfma_f32_16x16x32_bf16 v[208:211], v[52:55], v[224:227], v[208:211]\n\t" \
    "v_mfma_f32_16x16x32_bf16 v[200:203], v[248:251], v[224:227], v[200:203]\n\t" \
    "s_waitcnt lgkmcnt(3)\n\t" \
    "v_mfma_f32_16x16x32_bf16 v[216:219], v[44:47], v[228:231], v[216:219]\n\t" \
    "ds_read_b128 v[224:227], v252 offset:17408\n\t" \
    "v_mfma_f32_16x16x32_bf16 v[220:223], v[52:55], v[228:231], v[220:223]\n\t" \
    "v_mfma_f32_16x16x32_bf16 v[212:215], v[248:251], v[228:231], v[212:215]\n\t" \
    "s_waitcnt lgkmcnt(3)\n\t" \
    "v_mfma_f32_16x16x32_bf16 v[204:207], v[64:67], v[232:235], v[204:207]\n\t" \
    "v_and_b32 v252, 0x3ff, %[xa]\n\t" \
    "ds_read_b128 v[228:231], v252 offset:43008\n\t" \
    "v_mov_b64 v[244:245], 0\n\t" \
    "v_mfma_f32_16x16x32_bf16 v[208:211], v[72:75], v[232:235], v[208:211]\n\t" \
    "v_mov_b64 v[246:247], 0\n\t" \
    "v_mfma_f32_16x16x32_bf16 v[200:203], v[56:59], v[232:235], v[200:203]\n\t" \
    "v_mov_b64 v[248:249], 0\n\t" \
    "s_waitcnt lgkmcnt(3)\n\t" \
    "v_mfma_f32_16x16x32_bf16 v[216:219], v[64:67], v[236:239], v[216:219]\n\t" \
    "ds_read_b128 v[232:235], v252 offset:44032\n\t" \
    "v_mov_b64 v[250:251], 0\n\t" \
    "v_mfma_f32_16x16x32_bf16 v[220:223], v[72:75], v[236:239], v[220:223]\n\t" \
    "v_mfma_f32_16x16x32_bf16 v[212:215], v[56:59], v[236:239], v[212:215]\n\t" \
    "s_waitcnt lgkmcnt(3)\n\t" \
    "v_mfma_f32_16x16x32_bf16 v[204:207], v[68:71], v[240:243], v[204:207]\n\t" \
    "ds_read_b128 v[236:239], v252 offset:47104\n\t" \
    "v_mfma_f32_16x16x32_bf16 v[208:211], v[76:79], v[240:243], v[208:211]\n\t" \
    "v_mfma_f32_16x16x32_bf16 v[200:203], v[60:63], v[240:243], v[200:203]\n\t" \
    "s_waitcnt lgkmcnt(3)\n\t" \
    "v_mfma_f32_16x16x32_bf16 v[216:219], v[68:71], v[224:227], v[216:219]\n\t" \
    "ds_read_b128 v[240:243], v252 offset:48128\n\t" \
    "v_mfma_f32_16x16x32_bf16 v[220:223], v[76:79], v[224:227], v[220:223]\n\t" \
    "v_mfma_f32_16x16x32_bf16 v[212:215], v[60:63], v[224:227], v[212:215]\n\t" \
    "s_waitcnt lgkmcnt(3)\n\t" \
    "v_mfma_f32_16x16x32_bf16 v[204:207], v[184:187], v[228:231], v[204:207]\n\t" \
    "v_add_u32 v252, %[qs], v252\n\t" \
    "v_mfma_f32_16x16x32_bf16 v[208:211], v[192:195], v[228:231], v[208:211]\n\t" \
    "v_mov_b64 v[224:225], 0\n\t" \
    "v_mfma_f32_16x16x32_bf16 v[200:203], v[176:179], v[228:231], v[200:203]\n\t" \
    "v_mov_b64 v[226:227], 0\n\t" \
    "s_waitcnt lgkmcnt(2)\n\t" \
    "v_mfma_f32_16x16x32_bf16 v[204:207], v[188:191], v[232:235], v[204:207]\n\t" \
    "v_mfma_f32_16x16x32_bf16 v[208:211], v[196:199], v[232:235], v[208:211]\n\t" \
    "v_mfma_f32_16x16x32_bf16 v[200:203], v[180:183], v[232:235], v[200:203]\n\t" \
    "s_waitcnt lgkmcnt(1)\n\t" \
    "v_mfma_f32_16x16x32_bf16 v[216:219], v[184:187], v[236:239], v[216:219]\n\t" \
    "ds_read_b128 v[228:231], %[wl] offset:2048\n\t" \
    "ds_read_b128 v[232:235], %[wl] offset:3072\n\t" \
    "v_mfma_f32_16x16x32_bf16 v[220:223], v[192:195], v[236:239], v[220:223]\n\t" \
    "v_cvt_pk_bf16_f32 v204, v204, v205\n\t" \
    "v_cvt_pk_bf16_f32 v205, v206, v207\n\t" \
    "v_mfma_f32_16x16x32_bf16 v[212:215], v[176:179], v[236:239], v[212:215]\n\t" \
    "ds_write_b64 v252, v[204:205] offset:1024\n\t" \
    "v_cvt_pk_bf16_f32 v208, v208, v209\n\t" \
    "s_waitcnt lgkmcnt(3)\n\t" \
    "v_mfma_f32_16x16x32_bf16 v[216:219], v[188:191], v[240:243], v[216:219]\n\t" \
    "v_cvt_pk_bf16_f32 v209, v210, v211\n\t" \
    "ds_write_b64 v252, v[208:209] offset:2048\n\t" \
    "v_mfma_f32_16x16x32_bf16 v[220:223], v[196:199], v[240:243], v[220:223]\n\t" \
    "v_cvt_pk_bf16_f32 v200, v200, v201\n\t" \
    "v_cvt_pk_bf16_f32 v201, v202, v203\n\t" \
    "v_mfma_f32_16x16x32_bf16 v[212:215], v[180:183], v[240:243], v[212:215]\n\t" \
    "ds_write_b64 v252, v[200:201] offset:0\n\t" \
    "ds_read_b128 v[200:203], %[wl] offset:0\n\t" \
    "ds_read_b128 v[204:207], %[wl] offset:1024\n\t" \
    "v_cvt_pk_bf16_f32 v216, v216, v217\n\t" \
    "v_cvt_pk_bf16_f32 v217, v218, v219\n\t" \
    "ds_write_b64 v252, v[216:217] offset:7168\n\t" \
    "v_cvt_pk_bf16_f32 v220, v220, v221\n\t" \
    "v_cvt_pk_bf16_f32 v221, v222, v223\n\t" \
    "ds_write_b64 v252, v[220:221] offset:8192\n\t" \
    "v_cvt_pk_bf16_f32 v212, v212, v213\n\t" \
    "v_cvt_pk_bf16_f32 v213, v214, v215\n\t" \
    "ds_write_b64 v252, v[212:213] offset:6144\n\t" \
    "s_waitcnt lgkmcnt(0)\n\t"
#define ASM_CRIT1 \
    "v_bfe_i32 v241, %[ab], 7, 1\n\t" \
    "v_and_b32 v241, 0xaaaa, v241\n\t" \
    "v_or_b32 v241, 0x4444, v241\n\t" \
    "v_xor_b32 v212, 0x400, %[ab]\n\t" \
    "v_xor_b32 v216, 0x800, %[ab]\n\t" \
    "v_smfmac_f32_16x16x64_bf16 v[224:227], v[208:211], v[200:207], v241\n\t" \
    "v_smfmac_f32_16x16x64_bf16 v[244:247], v[208:211], v[0:7], v241\n\t" \
    "v_smfmac_f32_16x16x64_bf16 v[248:251], v[208:211], v[8:15], v241\n\t" \
    "v_xor_b32 v220, 0xc00, %[ab]\n\t" \
    "v_xor_b32 v236, 0x1000, %[ab]\n\t" \
    "v_xor_b32 v240, 0x1400, %[ab]\n\t" \
    "s_waitcnt lgkmcnt(0)\n\t" \
    "s_barrier\n\t" ASM_ST \
    "ds_read_b128 v[212:215], v212 offset:0\n\t" \
    "ds_read_b128 v[216:219], v216 offset:0\n\t" \
    "ds_read_b128 v[220:223], v220 offset:0\n\t" \
    "ds_read_b128 v[236:239], v236 offset:0\n\t" \
    "ds_read_b128 v[208:211], v240 offset:0\n\t" \
    "ds_read_b128 v[200:203], %[wl] offset:4096\n\t" \
    "ds_read_b128 v[204:207], %[wl] offset:5120\n\t" \
    "s_waitcnt lgkmcnt(6)\n\t" \
    "v_smfmac_f32_16x16x64_bf16 v[224:227], v[212:215], v[16:23], v241\n\t" \
    "v_smfmac_f32_16x16x64_bf16 v[244:247], v[212:215], v[24:31], v241\n\t" \
    "v_xor_b32 v240, 0x1800, %[ab]\n\t" \
    "v_smfmac_f32_16x16x64_bf16 v[248:251], v[212:215], v[32:39], v241\n\t" \
    "ds_read_b128 v[212:215], v240 offset:0\n\t" \
    "s_waitcnt lgkmcnt(6)\n\t" \
    "v_smfmac_f32_16x16x64_bf16 v[224:227], v[216:219], v[228:235], v241\n\t" \
    "ds_read_b128 v[228:231], %[wl] offset:6144\n\t" \
    "ds_read_b128 v[232:235], %[wl] offset:7168\n\t" \
    "v_smfmac_f32_16x16x64_bf16 v[244:247], v[216:219], v[40:47], v241\n\t" \
    "v_xor_b32 v240, 0x1c00, %[ab]\n\t" \
    "v_smfmac_f32_16x16x64_bf16 v[248:251], v[216:219], v[48:55], v241\n\t" \
    "ds_read_b128 v[216:219], v240 offset:0\n\t" \
    "s_waitcnt lgkmcnt(8)\n\t" \
    "v_smfmac_f32_16x16x64_bf16 v[224:227], v[220:223], v[56:63], v241\n\t" \
    "v_smfmac_f32_16x16x64_bf16 v[244:247], v[220:223], v[64:71], v241\n\t" \
    "v_and_b32 v240, 0x3ff, %[ab]\n\t" \
    "v_smfmac_f32_16x16x64_bf16 v[248:251], v[220:223], v[72:79], v241\n\t" \
    "ds_read_b128 v[220:223], v240 offset:8192\n\t" \
    "s_waitcnt lgkmcnt(5)\n\t" \
    "v_smfmac_f32_16x16x64_bf16 v[224:227], v[236:239], v[200:207], v241\n\t" \
    "ds_read_b128 v[200:203], %[wl] offset:8192\n\t" \
    "ds_read_b128 v[204:207], %[wl] offset:9216\n\t" \
    "v_smfmac_f32_16x16x64_bf16 v[244:247], v[236:239], v[80:87], v241\n\t" \
    "v_smfmac_f32_16x16x64_bf16 v[248:251], v[236:239], v[88:95], v241\n\t" \
    "ds_read_b128 v[236:239], v240 offset:9216\n\t" \
    "v_smfmac_f32_16x16x64_bf16 v[244:247], v[208:211], v[104:111], v241\n\t" \
    "s_waitcnt lgkmcnt(7)\n\t" \
    "v_smfmac_f32_16x16x64_bf16 v[244:247], v[212:215], v[120:127], v241\n\t" \
    "s_waitcnt lgkmcnt(4)\n\t" \
    "v_smfmac_f32_16x16x64_bf16 v[244:247], v[216:219], v[144:151], v241\n\t" \
    "s_waitcnt lgkmcnt(3)\n\t" \
    "v_smfmac_f32_16x16x64_bf16 v[244:247], v[220:223], v[160:167], v241\n\t" \
    "s_waitcnt lgkmcnt(0)\n\t" \
    "v_smfmac_f32_16x16x64_bf16 v[244:247], v[236:239], v[184:191], v241\n\t" \
    "v_smfmac_f32_16x16x64_bf16 v[248:251], v[208:211], v[112:119], v241\n\t" \
    "v_smfmac_f32_16x16x64_bf16 v[248:251], v[212:215], v[128:135], v241\n\t" \
    "v_smfmac_f32_16x16x64_bf16 v[248:251], v[216:219], v[152:159], v241\n\t" \
    "v_permlane32_swap_b32 v244, v246\n\t" \
    "v_permlane32_swap_b32 v245, v247\n\t" \
    "v_smfmac_f32_16x16x64_bf16 v[248:251], v[220:223], v[168:175], v241\n\t" \
    "v_add_f32 v244, v244, v246\n\t" \
    "v_add_f32 v245, v245, v247\n\t" \
    "v_smfmac_f32_16x16x64_bf16 v[248:251], v[236:239], v[192:199], v241\n\t" \
    "v_cvt_pk_bf16_f32 v244, v244, v245\n\t" \
    "ds_write_b16 %[dst], v244 offset:512\n\t" \
    "ds_write_b16_d16_hi %[dst], v244 offset:528\n\t" \
    "v_smfmac_f32_16x16x64_bf16 v[224:227], v[208:211], v[96:103], v241\n\t" \
    "v_smfmac_f32_16x16x64_bf16 v[224:227], v[212:215], v[228:235], v241\n\t" \
    "v_smfmac_f32_16x16x64_bf16 v[224:227], v[216:219], v[136:143], v241\n\t" \
    "v_permlane32_swap_b32 v248, v250\n\t" \
    "v_permlane32_swap_b32 v249, v251\n\t" \
    "v_smfmac_f32_16x16x64_bf16 v[224:227], v[220:223], v[200:207], v241\n\t" \
    "v_add_f32 v248, v248, v250\n\t" \
    "v_add_f32 v249, v249, v251\n\t" \
    "v_smfmac_f32_16x16x64_bf16 v[224:227], v[236:239], v[176:183], v241\n\t" \
    "v_cvt_pk_bf16_f32 v248, v248, v249\n\t" \
    "ds_write_b16 %[dst], v248 offset:1024\n\t" \
    "ds_write_b16_d16_hi %[dst], v248 offset:1040\n\t" \
    "s_nop 4\n\t" \
    "v_permlane32_swap_b32 v224, v226\n\t" \
    "v_permlane32_swap_b32 v225, v227\n\t" \
    "v_add_f32 v224, v224, v226\n\t" \
    "v_add_f32 v225, v225, v227\n\t" \
    "v_cvt_pk_bf16_f32 v224, v224, v225\n\t" \
    "ds_write_b16 %[dst], v224 offset:0\n\t" \
    "ds_write_b16_d16_hi %[dst], v224 offset:16\n\t"
#define ASM_CRIT1_N \
    "v_bfe_i32 v241, %[ab], 7, 1\n\t" \
    "v_and_b32 v241, 0xaaaa, v241\n\t" \
    "v_or_b32 v241, 0x4444, v241\n\t" \
    "v_xor_b32 v212, 0x400, %[ab]\n\t" \
    "v_xor_b32 v216, 0x800, %[ab]\n\t" \
    "v_xor_b32 v220, 0xc00, %[ab]\n\t" \
    "v_xor_b32 v236, 0x1000, %[ab]\n\t" \
    "v_xor_b32 v240, 0x1400, %[ab]\n\t" \
    "s_waitcnt lgkmcnt(0)\n\t" \
    "s_barrier\n\t" ASM_ST \
    "ds_read_b128 v[208:211], %[ab] offset:0\n\t" \
    "ds_read_b128 v[212:215], v212 offset:0\n\t" \
    "ds_read_b128 v[216:219], v216 offset:0\n\t" \
    "ds_read_b128 v[220:223], v220 offset:0\n\t" \
    "ds_read_b128 v[236:239], v236 offset:0\n\t" \
    "s_waitcnt lgkmcnt(4)\n\t" \
    "v_smfmac_f32_16x16x64_bf16 v[224:227], v[208:211], v[200:207], v241\n\t" \
    "v_smfmac_f32_16x16x64_bf16 v[244:247], v[208:211], v[0:7], v241\n\t" \
    "v_smfmac_f32_16x16x64_bf16 v[248:251], v[208:211], v[8:15], v241\n\t" \
    "ds_read_b128 v[208:211], v240 offset:0\n\t" \
    "ds_read_b128 v[200:203], %[wl] offset:4096\n\t" \
    "ds_read_b128 v[204:207], %[wl] offset:5120\n\t" \
    "s_waitcnt lgkmcnt(6)\n\t" \
    "v_smfmac_f32_16x16x64_bf16 v[224:227], v[212:215], v[16:23], v241\n\t" \
    "v_smfmac_f32_16x16x64_bf16 v[244:247], v[212:215], v[24:31], v241\n\t" \
    "v_xor_b32 v240, 0x1800, %[ab]\n\t" \
    "v_smfmac_f32_16x16x64_bf16 v[248:251], v[212:215], v[32:39], v241\n\t" \
    "ds_read_b128 v[212:215], v240 offset:0\n\t" \
    "s_waitcnt lgkmcnt(6)\n\t" \
    "v_smfmac_f32_16x16x64_bf16 v[224:227], v[216:219], v[228:235], v241\n\t" \
    "ds_read_b128 v[228:231], %[wl] offset:6144\n\t" \
    "ds_read_b128 v[232:235], %[wl] offset:7168\n\t" \
    "v_smfmac_f32_16x16x64_bf16 v[244:247], v[216:219], v[40:47], v241\n\t" \
    "v_xor_b32 v240, 0x1c00, %[ab]\n\t" \
    "v_smfmac_f32_16x16x64_bf16 v[248:251], v[216:219], v[48:55], v241\n\t" \
    "ds_read_b128 v[216:219], v240 offset:0\n\t" \
    "s_waitcnt lgkmcnt(8)\n\t" \
    "v_smfmac_f32_16x16x64_bf16 v[224:227], v[220:223], v[56:63], v241\n\t" \
    "v_smfmac_f32_16x16x64_bf16 v[244:247], v[220:223], v[64:71], v241\n\t" \
    "v_and_b32 v240, 0x3ff, %[ab]\n\t" \
    "v_smfmac_f32_16x16x64_bf16 v[248:251], v[220:223], v[72:79], v241\n\t" \
    "ds_read_b128 v[220:223], v240 offset:8192\n\t" \
    "s_waitcnt lgkmcnt(5)\n\t" \
    "v_smfmac_f32_16x16x64_bf16 v[224:227], v[236:239], v[200:207], v241\n\t" \
    "ds_read_b128 v[200:203], %[wl] offset:8192\n\t" \
    "ds_read_b128 v[204:207], %[wl] offset:9216\n\t" \
    "v_smfmac_f32_16x16x64_bf16 v[244:247], v[236:239], v[80:87], v241\n\t" \
    "v_smfmac_f32_16x16x64_bf16 v[248:251], v[236:239], v[88:95], v241\n\t" \
    "ds_read_b128 v[236:239], v240 offset:9216\n\t" \
    "v_smfmac_f32_16x16x64_bf16 v[244:247], v[208:211], v[104:111], v241\n\t" \
    "s_waitcnt lgkmcnt(7)\n\t" \
    "v_smfmac_f32_16x16x64_bf16 v[244:247], v[212:215], v[120:127], v241\n\t" \
    "s_waitcnt lgkmcnt(4)\n\t" \
    "v_smfmac_f32_16x16x64_bf16 v[244:247], v[216:219], v[144:151], v241\n\t" \
    "s_waitcnt lgkmcnt(3)\n\t" \
    "v_smfmac_f32_16x16x64_bf16 v[244:247], v[220:223], v[160:167], v241\n\t" \
    "s_waitcnt lgkmcnt(0)\n\t" \
    "v_smfmac_f32_16x16x64_bf16 v[244:247], v[236:239], v[184:191], v241\n\t" \
    "v_smfmac_f32_16x16x64_bf16 v[248:251], v[208:211], v[112:119], v241\n\t" \
    "v_smfmac_f32_16x16x64_bf16 v[248:251], v[212:215], v[128:135], v241\n\t" \
    "v_smfmac_f32_16x16x64_bf16 v[248:251], v[216:219], v[152:159], v241\n\t" \
    "v_permlane32_swap_b32 v244, v246\n\t" \
    "v_permlane32_swap_b32 v245, v247\n\t" \
    "v_smfmac_f32_16x16x64_bf16 v[248:251], v[220:223], v[168:175], v241\n\t" \
    "v_add_f32 v244, v244, v246\n\t" \
    "v_add_f32 v245, v245, v247\n\t" \
    "v_smfmac_f32_16x16x64_bf16 v[248:251], v[236:239], v[192:199], v241\n\t" \
    "v_cvt_pk_bf16_f32 v244, v244, v245\n\t" \
    "ds_write_b16 %[dst], v244 offset:512\n\t" \
    "ds_write_b16_d16_hi %[dst], v244 offset:528\n\t" \
    "v_smfmac_f32_16x16x64_bf16 v[224:227], v[208:211], v[96:103], v241\n\t" \
    "v_smfmac_f32_16x16x64_bf16 v[224:227], v[212:215], v[228:235], v241\n\t" \
    "v_smfmac_f32_16x16x64_bf16 v[224:227], v[216:219], v[136:143], v241\n\t" \
    "v_permlane32_swap_b32 v248, v250\n\t" \
    "v_permlane32_swap_b32 v249, v251\n\t" \
    "v_smfmac_f32_16x16x64_bf16 v[224:227], v[220:223], v[200:207], v241\n\t" \
    "v_add_f32 v248, v248, v250\n\t" \
    "v_add_f32 v249, v249, v251\n\t" \
    "v_smfmac_f32_16x16x64_bf16 v[224:227], v[236:239], v[176:183], v241\n\t" \
    "v_cvt_pk_bf16_f32 v248, v248, v249\n\t" \
    "ds_write_b16 %[dst], v248 offset:1024\n\t" \
    "ds_write_b16_d16_hi %[dst], v248 offset:1040\n\t" \
    "s_nop 4\n\t" \
    "v_permlane32_swap_b32 v224, v226\n\t" \
    "v_permlane32_swap_b32 v225, v227\n\t" \
    "v_add_f32 v224, v224, v226\n\t" \
    "v_add_f32 v225, v225, v227\n\t" \
    "v_cvt_pk_bf16_f32 v224, v224, v225\n\t" \
    "ds_write_b16 %[dst], v224 offset:0\n\t" \
    "ds_write_b16_d16_hi %[dst], v224 offset:16\n\t"
#define ASM_VOX \
    "s_waitcnt lgkmcnt(0)\n\t" \
    "ds_read_b128 v[224:227], %[va] offset:0\n\t" \
    "ds_read_b128 v[248:251], %[wl] offset:0\n\t" \
    "ds_read_b128 v[228:231], %[va] offset:6144\n\t" \
    "ds_read_b128 v[232:235], %[va] offset:2048\n\t" \
    "ds_read_b128 v[236:239], %[va] offset:8192\n\t" \
    "ds_read_b128 v[240:243], %[va] offset:3072\n\t" \
    "s_waitcnt lgkmcnt(4)\n\t" \
    "v_mfma_f32_16x16x32_bf16 v[200:203], v[224:227], v[248:251], 0\n\t" \
    "ds_read_b128 v[244:247], %[va] offset:9216\n\t" \
    "s_waitcnt lgkmcnt(4)\n\t" \
    "v_mfma_f32_16x16x32_bf16 v[204:207], v[228:231], v[248:251], 0\n\t" \
    "ds_read_b128 v[224:227], %[va] offset:1024\n\t" \
    "s_waitcnt lgkmcnt(4)\n\t" \
    "v_mfma_f32_16x16x32_bf16 v[200:203], v[232:235], v[0:3], v[200:203]\n\t" \
    "ds_read_b128 v[228:231], %[va] offset:7168\n\t" \
    "ds_read_b128 v[248:251], %[wl] offset:1024\n\t" \
    "s_waitcnt lgkmcnt(5)\n\t" \
    "v_mfma_f32_16x16x32_bf16 v[204:207], v[236:239], v[0:3], v[204:207]\n\t" \
    "ds_read_b128 v[232:235], %[va] offset:4096\n\t" \
    "s_waitcnt lgkmcnt(5)\n\t" \
    "v_mfma_f32_16x16x32_bf16 v[200:203], v[240:243], v[4:7], v[200:203]\n\t" \
    "ds_read_b128 v[236:239], %[va] offset:10240\n\t" \
    "s_waitcnt lgkmcnt(5)\n\t" \
    "v_mfma_f32_16x16x32_bf16 v[204:207], v[244:247], v[4:7], v[204:207]\n\t" \
    "ds_read_b128 v[240:243], %[va] offset:5120\n\t" \
    "s_waitcnt lgkmcnt(3)\n\t" \
    "v_mfma_f32_16x16x32_bf16 v[200:203], v[224:227], v[248:251], v[200:203]\n\t" \
    "ds_read_b128 v[244:247], %[va] offset:11264\n\t" \
    "v_mfma_f32_16x16x32_bf16 v[208:211], v[224:227], v[20:23], 0\n\t" \
    "v_mfma_f32_16x16x32_bf16 v[204:207], v[228:231], v[248:251], v[204:207]\n\t" \
    "ds_read_b128 v[224:227], %[va] offset:0\n\t" \
    "v_mfma_f32_16x16x32_bf16 v[212:215], v[228:231], v[20:23], 0\n\t" \
    "s_waitcnt lgkmcnt(4)\n\t" \
    "v_mfma_f32_16x16x32_bf16 v[200:203], v[232:235], v[8:11], v[200:203]\n\t" \
    "ds_read_b128 v[228:231], %[va] offset:6144\n\t" \
    "ds_read_b128 v[248:251], %[wl] offset:2048\n\t" \
    "v_mfma_f32_16x16x32_bf16 v[208:211], v[232:235], v[32:35], v[208:211]\n\t" \
    "s_waitcnt lgkmcnt(5)\n\t" \
    "v_mfma_f32_16x16x32_bf16 v[204:207], v[236:239], v[8:11], v[204:207]\n\t" \
    "ds_read_b128 v[232:235], %[va] offset:2048\n\t" \
    "v_mfma_f32_16x16x32_bf16 v[212:215], v[236:239], v[32:35], v[212:215]\n\t" \
    "s_waitcnt lgkmcnt(5)\n\t" \
    "v_mfma_f32_16x16x32_bf16 v[200:203], v[240:243], v[12:15], v[200:203]\n\t" \
    "ds_read_b128 v[236:239], %[va] offset:8192\n\t" \
    "v_mfma_f32_16x16x32_bf16 v[208:211], v[240:243], v[36:39], v[208:211]\n\t" \
    "s_waitcnt lgkmcnt(5)\n\t" \
    "v_mfma_f32_16x16x32_bf16 v[204:207], v[244:247], v[12:15], v[204:207]\n\t" \
    "ds_read_b128 v[240:243], %[va] offset:3072\n\t" \
    "v_mfma_f32_16x16x32_bf16 v[212:215], v[244:247], v[36:39], v[212:215]\n\t" \
    "s_waitcnt lgkmcnt(3)\n\t" \
    "v_mfma_f32_16x16x32_bf16 v[216:219], v[224:227], v[248:251], 0\n\t" \
    "ds_read_b128 v[244:247], %[va] offset:9216\n\t" \
    "v_mfma_f32_16x16x32_bf16 v[208:211], v[224:227], v[16:19], v[208:211]\n\t" \
    "v_mfma_f32_16x16x32_bf16 v[220:223], v[228:231], v[248:251], 0\n\t" \
    "ds_read_b128 v[224:227], %[va] offset:1024\n\t" \
    "v_mfma_f32_16x16x32_bf16 v[212:215], v[228:231], v[16:19], v[212:215]\n\t" \
    "v_cvt_pk_bf16_f32 v200, v200, v201\n\t" \
    "s_waitcnt lgkmcnt(4)\n\t" \
    "v_mfma_f32_16x16x32_bf16 v[216:219], v[232:235], v[40:43], v[216:219]\n\t" \
    "ds_read_b128 v[228:231], %[va] offset:7168\n\t" \
    "ds_read_b128 v[248:251], %[wl] offset:3072\n\t" \
    "v_cvt_pk_bf16_f32 v201, v202, v203\n\t" \
    "v_mfma_f32_16x16x32_bf16 v[208:211], v[232:235], v[24:27], v[208:211]\n\t" \
    "v_cvt_pk_bf16_f32 v202, v204, v205\n\t" \
    "s_waitcnt lgkmcnt(5)\n\t" \
    "v_mfma_f32_16x16x32_bf16 v[220:223], v[236:239], v[40:43], v[220:223]\n\t" \
    "ds_read_b128 v[232:235], %[va] offset:4096\n\t" \
    "v_cvt_pk_bf16_f32 v203, v206, v207\n\t" \
    "v_mfma_f32_16x16x32_bf16 v[212:215], v[236:239], v[24:27], v[212:215]\n\t" \
    "buffer_store_dwordx4 v[200:203], %[l16], %[rd], %[so] offen sc1\n\t" \
    "s_waitcnt lgkmcnt(5)\n\t" \
    "v_mfma_f32_16x16x32_bf16 v[216:219], v[240:243], v[44:47], v[216:219]\n\t" \
    "ds_read_b128 v[236:239], %[va] offset:10240\n\t" \
    "s_add_u32 %[so], %[so], 0x10000\n\t" \
    "v_mfma_f32_16x16x32_bf16 v[208:211], v[240:243], v[28:31], v[208:211]\n\t" \
    "s_waitcnt lgkmcnt(5)\n\t" \
    "v_mfma_f32_16x16x32_bf16 v[220:223], v[244:247], v[44:47], v[220:223]\n\t" \
    "ds_read_b128 v[240:243], %[va] offset:5120\n\t" \
    "v_mfma_f32_16x16x32_bf16 v[212:215], v[244:247], v[28:31], v[212:215]\n\t" \
    "s_waitcnt lgkmcnt(3)\n\t" \
    "v_mfma_f32_16x16x32_bf16 v[216:219], v[224:227], v[248:251], v[216:219]\n\t" \
    "ds_read_b128 v[244:247], %[va] offset:11264\n\t" \
    "v_mfma_f32_16x16x32_bf16 v[200:203], v[224:227], v[60:63], 0\n\t" \
    "v_mfma_f32_16x16x32_bf16 v[220:223], v[228:231], v[248:251], v[220:223]\n\t" \
    "ds_read_b128 v[224:227], %[va] offset:0\n\t" \
    "v_mfma_f32_16x16x32_bf16 v[204:207], v[228:231], v[60:63], 0\n\t" \
    "v_cvt_pk_bf16_f32 v208, v208, v209\n\t" \
    "s_waitcnt lgkmcnt(4)\n\t" \
    "v_mfma_f32_16x16x32_bf16 v[216:219], v[232:235], v[48:51], v[216:219]\n\t" \
    "ds_read_b128 v[228:231], %[va] offset:6144\n\t" \
    "ds_read_b128 v[248:251], %[wl] offset:4096\n\t" \
    "v_cvt_pk_bf16_f32 v209, v210, v211\n\t" \
    "v_mfma_f32_16x16x32_bf16 v[200:203], v[232:235], v[72:75], v[200:203]\n\t" \
    "v_cvt_pk_bf16_f32 v210, v212, v213\n\t" \
    "s_waitcnt lgkmcnt(5)\n\t" \
    "v_mfma_f32_16x16x32_bf16 v[220:223], v[236:239], v[48:51], v[220:223]\n\t" \
    "ds_read_b128 v[232:235], %[va] offset:2048\n\t" \
    "v_cvt_pk_bf16_f32 v211, v214, v215\n\t" \
    "v_mfma_f32_16x16x32_bf16 v[204:207], v[236:239], v[72:75], v[204:207]\n\t" \
    "buffer_store_dwordx4 v[208:211], %[l16], %[rd], %[so] offen sc1\n\t" \
    "s_waitcnt lgkmcnt(5)\n\t" \
    "v_mfma_f32_16x16x32_bf16 v[216:219], v[240:243], v[52:55], v[216:219]\n\t" \
    "ds_read_b128 v[236:239], %[va] offset:8192\n\t" \
    "s_add_u32 %[so], %[so], 0x10000\n\t" \
    "v_mfma_f32_16x16x32_bf16 v[200:203], v[240:243], v[76:79], v[200:203]\n\t" \
    "s_waitcnt lgkmcnt(5)\n\t" \
    "v_mfma_f32_16x16x32_bf16 v[220:223], v[244:247], v[52:55], v[220:223]\n\t" \
    "ds_read_b128 v[240:243], %[va] offset:3072\n\t" \
    "v_mfma_f32_16x16x32_bf16 v[204:207], v[244:247], v[76:79], v[204:207]\n\t" \
    "s_waitcnt lgkmcnt(3)\n\t" \
    "v_mfma_f32_16x16x32_bf16 v[208:211], v[224:227], v[248:251], 0\n\t" \
    "ds_read_b128 v[244:247], %[va] offset:9216\n\t" \
    "v_mfma_f32_16x16x32_bf16 v[200:203], v[224:227], v[56:59], v[200:203]\n\t" \
    "v_mfma_f32_16x16x32_bf16 v[212:215], v[228:231], v[248:251], 0\n\t" \
    "ds_read_b128 v[224:227], %[va] offset:1024\n\t" \
    "v_mfma_f32_16x16x32_bf16 v[204:207], v[228:231], v[56:59], v[204:207]\n\t" \
    "v_cvt_pk_bf16_f32 v216, v216, v217\n\t" \
    "s_waitcnt lgkmcnt(4)\n\t" \
    "v_mfma_f32_16x16x32_bf16 v[208:211], v[232:235], v[80:83], v[208:211]\n\t" \
    "ds_read_b128 v[228:231], %[va] offset:7168\n\t" \
    "ds_read_b128 v[248:251], %[wl] offset:5120\n\t" \
    "v_cvt_pk_bf16_f32 v217, v218, v219\n\t" \
    "v_mfma_f32_16x16x32_bf16 v[200:203], v[232:235], v[64:67], v[200:203]\n\t" \
    "v_cvt_pk_bf16_f32 v218, v220, v221\n\t" \
    "s_waitcnt lgkmcnt(5)\n\t" \
    "v_mfma_f32_16x16x32_bf16 v[212:215], v[236:239], v[80:83], v[212:215]\n\t" \
    "ds_read_b128 v[232:235], %[va] offset:4096\n\t" \
    "v_cvt_pk_bf16_f32 v219, v222, v223\n\t" \
    "v_mfma_f32_16x16x32_bf16 v[204:207], v[236:239], v[64:67], v[204:207]\n\t" \
    "buffer_store_dwordx4 v[216:219], %[l16], %[rd], %[so] offen sc1\n\t" \
    "s_waitcnt lgkmcnt(5)\n\t" \
    "v_mfma_f32_16x16x32_bf16 v[208:211], v[240:243], v[84:87], v[208:211]\n\t" \
    "ds_read_b128 v[236:239], %[va] offset:10240\n\t" \
    "s_add_u32 %[so], %[so], 0x10000\n\t" \
    "v_mfma_f32_16x16x32_bf16 v[200:203], v[240:243], v[68:71], v[200:203]\n\t" \
    "s_waitcnt lgkmcnt(5)\n\t" \
    "v_mfma_f32_16x16x32_bf16 v[212:215], v[244:247], v[84:87], v[212:215]\n\t" \
    "ds_read_b128 v[240:243], %[va] offset:5120\n\t" \
    "v_mfma_f32_16x16x32_bf16 v[204:207], v[244:247], v[68:71], v[204:207]\n\t" \
    "s_waitcnt lgkmcnt(3)\n\t" \
    "v_mfma_f32_16x16x32_bf16 v[208:211], v[224:227], v[248:251], v[208:211]\n\t" \
    "ds_read_b128 v[244:247], %[va] offset:11264\n\t" \
    "v_mfma_f32_16x16x32_bf16 v[216:219], v[224:227], v[100:103], 0\n\t" \
    "v_mfma_f32_16x16x32_bf16 v[212:215], v[228:231], v[248:251], v[212:215]\n\t" \
    "ds_read_b128 v[224:227], %[va] offset:0\n\t" \
    "v_mfma_f32_16x16x32_bf16 v[220:223], v[228:231], v[100:103], 0\n\t" \
    "v_cvt_pk_bf16_f32 v200, v200, v201\n\t" \
    "s_waitcnt lgkmcnt(4)\n\t" \
    "v_mfma_f32_16x16x32_bf16 v[208:211], v[232:235], v[88:91], v[208:211]\n\t" \
    "ds_read_b128 v[228:231], %[va] offset:6144\n\t" \
    "ds_read_b128 v[248:251], %[wl] offset:6144\n\t" \
    "v_cvt_pk_bf16_f32 v201, v202, v203\n\t" \
    "v_mfma_f32_16x16x32_bf16 v[216:219], v[232:235], v[112:115], v[216:219]\n\t" \
    "v_cvt_pk_bf16_f32 v202, v204, v205\n\t" \
    "s_waitcnt lgkmcnt(5)\n\t" \
    "v_mfma_f32_16x16x32_bf16 v[212:215], v[236:239], v[88:91], v[212:215]\n\t" \
    "ds_read_b128 v[232:235], %[va] offset:2048\n\t" \
    "v_cvt_pk_bf16_f32 v203, v206, v207\n\t" \
    "v_mfma_f32_16x16x32_bf16 v[220:223], v[236:239], v[112:115], v[220:223]\n\t" \
    "buffer_store_dwordx4 v[200:203], %[l16], %[rd], %[so] offen sc1\n\t" \
    "s_waitcnt lgkmcnt(5)\n\t" \
    "v_mfma_f32_16x16x32_bf16 v[208:211], v[240:243], v[92:95], v[208:211]\n\t" \
    "ds_read_b128 v[236:239], %[va] offset:8192\n\t" \
    "s_add_u32 %[so], %[so], 0x10000\n\t" \
    "v_mfma_f32_16x16x32_bf16 v[216:219], v[240:243], v[116:119], v[216:219]\n\t" \
    "s_waitcnt lgkmcnt(5)\n\t" \
    "v_mfma_f32_16x16x32_bf16 v[212:215], v[244:247], v[92:95], v[212:215]\n\t" \
    "ds_read_b128 v[240:243], %[va] offset:3072\n\t" \
    "v_mfma_f32_16x16x32_bf16 v[220:223], v[244:247], v[116:119], v[220:223]\n\t" \
    "s_waitcnt lgkmcnt(3)\n\t" \
    "v_mfma_f32_16x16x32_bf16 v[200:203], v[224:227], v[248:251], 0\n\t" \
    "ds_read_b128 v[244:247], %[va] offset:9216\n\t" \
    "v_mfma_f32_16x16x32_bf16 v[216:219], v[224:227], v[96:99], v[216:219]\n\t" \
    "v_mfma_f32_16x16x32_bf16 v[204:207], v[228:231], v[248:251], 0\n\t" \
    "ds_read_b128 v[224:227], %[va] offset:1024\n\t" \
    "v_mfma_f32_16x16x32_bf16 v[220:223], v[228:231], v[96:99], v[220:223]\n\t" \
    "v_cvt_pk_bf16_f32 v208, v208, v209\n\t" \
    "s_waitcnt lgkmcnt(4)\n\t" \
    "v_mfma_f32_16x16x32_bf16 v[200:203], v[232:235], v[120:123], v[200:203]\n\t" \
    "ds_read_b128 v[228:231], %[va] offset:7168\n\t" \
    "ds_read_b128 v[248:251], %[wl] offset:7168\n\t" \
    "v_cvt_pk_bf16_f32 v209, v210, v211\n\t" \
    "v_mfma_f32_16x16x32_bf16 v[216:219], v[232:235], v[104:107], v[216:219]\n\t" \
    "v_cvt_pk_bf16_f32 v210, v212, v213\n\t" \
    "s_waitcnt lgkmcnt(5)\n\t" \
    "v_mfma_f32_16x16x32_bf16 v[204:207], v[236:239], v[120:123], v[204:207]\n\t" \
    "ds_read_b128 v[232:235], %[va] offset:4096\n\t" \
    "v_cvt_pk_bf16_f32 v211, v214, v215\n\t" \
    "v_mfma_f32_16x16x32_bf16 v[220:223], v[236:239], v[104:107], v[220:223]\n\t" \
    "buffer_store_dwordx4 v[208:211], %[l16], %[rd], %[so] offen sc1\n\t" \
    "s_waitcnt lgkmcnt(5)\n\t" \
    "v_mfma_f32_16x16x32_bf16 v[200:203], v[240:243], v[124:127], v[200:203]\n\t" \
    "ds_read_b128 v[236:239], %[va] offset:10240\n\t" \
    "s_add_u32 %[so], %[so], 0x10000\n\t" \
    "v_mfma_f32_16x16x32_bf16 v[216:219], v[240:243], v[108:111], v[216:219]\n\t" \
    "s_waitcnt lgkmcnt(5)\n\t" \
    "v_mfma_f32_16x16x32_bf16 v[204:207], v[244:247], v[124:127], v[204:207]\n\t" \
    "ds_read_b128 v[240:243], %[va] offset:5120\n\t" \
    "v_mfma_f32_16x16x32_bf16 v[220:223], v[244:247], v[108:111], v[220:223]\n\t" \
    "s_waitcnt lgkmcnt(3)\n\t" \
    "v_mfma_f32_16x16x32_bf16 v[200:203], v[224:227], v[248:251], v[200:203]\n\t" \
    "ds_read_b128 v[244:247], %[va] offset:11264\n\t" \
    "v_mfma_f32_16x16x32_bf16 v[208:211], v[224:227], v[140:143], 0\n\t" \
    "v_mfma_f32_16x16x32_bf16 v[204:207], v[228:231], v[248:251], v[204:207]\n\t" \
    "ds_read_b128 v[224:227], %[va] offset:0\n\t" \
    "v_mfma_f32_16x16x32_bf16 v[212:215], v[228:231], v[140:143], 0\n\t" \
    "v_cvt_pk_bf16_f32 v216, v216, v217\n\t" \
    "s_waitcnt lgkmcnt(4)\n\t" \
    "v_mfma_f32_16x16x32_bf16 v[200:203], v[232:235], v[128:131], v[200:203]\n\t" \
    "ds_read_b128 v[228:231], %[va] offset:6144\n\t" \
    "ds_read_b128 v[248:251], %[wl] offset:8192\n\t" \
    "v_cvt_pk_bf16_f32 v217, v218, v219\n\t" \
    "v_mfma_f32_16x16x32_bf16 v[208:211], v[232:235], v[152:155], v[208:211]\n\t" \
    "v_cvt_pk_bf16_f32 v218, v220, v221\n\t" \
    "s_waitcnt lgkmcnt(5)\n\t" \
    "v_mfma_f32_16x16x32_bf16 v[204:207], v[236:239], v[128:131], v[204:207]\n\t" \
    "ds_read_b128 v[232:235], %[va] offset:2048\n\t" \
    "v_cvt_pk_bf16_f32 v219, v222, v223\n\t" \
    "v_mfma_f32_16x16x32_bf16 v[212:215], v[236:239], v[152:155], v[212:215]\n\t" \
    "buffer_store_dwordx4 v[216:219], %[l16], %[rd], %[so] offen sc1\n\t" \
    "s_waitcnt lgkmcnt(5)\n\t" \
    "v_mfma_f32_16x16x32_bf16 v[200:203], v[240:243], v[132:135], v[200:203]\n\t" \
    "ds_read_b128 v[236:239], %[va] offset:8192\n\t" \
    "s_add_u32 %[so], %[so], 0x10000\n\t" \
    "v_mfma_f32_16x16x32_bf16 v[208:211], v[240:243], v[156:159], v[208:211]\n\t" \
    "s_waitcnt lgkmcnt(5)\n\t" \
    "v_mfma_f32_16x16x32_bf16 v[204:207], v[244:247], v[132:135], v[204:207]\n\t" \
    "ds_read_b128 v[240:243], %[va] offset:3072\n\t" \
    "v_mfma_f32_16x16x32_bf16 v[212:215], v[244:247], v[156:159], v[212:215]\n\t" \
    "s_waitcnt lgkmcnt(3)\n\t" \
    "v_mfma_f32_16x16x32_bf16 v[216:219], v[224:227], v[248:251], 0\n\t" \
    "ds_read_b128 v[244:247], %[va] offset:9216\n\t" \
    "v_mfma_f32_16x16x32_bf16 v[208:211], v[224:227], v[136:139], v[208:211]\n\t" \
    "v_mfma_f32_16x16x32_bf16 v[220:223], v[228:231], v[248:251], 0\n\t" \
    "ds_read_b128 v[224:227], %[va] offset:1024\n\t" \
    "v_mfma_f32_16x16x32_bf16 v[212:215], v[228:231], v[136:139], v[212:215]\n\t" \
    "v_cvt_pk_bf16_f32 v200, v200, v201\n\t" \
    "s_waitcnt lgkmcnt(4)\n\t" \
    "v_mfma_f32_16x16x32_bf16 v[216:219], v[232:235], v[160:163], v[216:219]\n\t" \
    "ds_read_b128 v[228:231], %[va] offset:7168\n\t" \
    "ds_read_b128 v[248:251], %[wl] offset:9216\n\t" \
    "v_cvt_pk_bf16_f32 v201, v202, v203\n\t" \
    "v_mfma_f32_16x16x32_bf16 v[208:211], v[232:235], v[144:147], v[208:211]\n\t" \
    "v_cvt_pk_bf16_f32 v202, v204, v205\n\t" \
    "s_waitcnt lgkmcnt(5)\n\t" \
    "v_mfma_f32_16x16x32_bf16 v[220:223], v[236:239], v[160:163], v[220:223]\n\t" \
    "ds_read_b128 v[232:235], %[va] offset:4096\n\t" \
    "v_cvt_pk_bf16_f32 v203, v206, v207\n\t" \
    "v_mfma_f32_16x16x32_bf16 v[212:215], v[236:239], v[144:147], v[212:215]\n\t" \
    "buffer_store_dwordx4 v[200:203], %[l16], %[rd], %[so] offen sc1\n\t" \
    "s_waitcnt lgkmcnt(5)\n\t" \
    "v_mfma_f32_16x16x32_bf16 v[216:219], v[240:243], v[164:167], v[216:219]\n\t" \
    "ds_read_b128 v[236:239], %[va] offset:10240\n\t" \
    "s_add_u32 %[so], %[so], 0x10000\n\t" \
    "v_mfma_f32_16x16x32_bf16 v[208:211], v[240:243], v[148:151], v[208:211]\n\t" \
    "s_waitcnt lgkmcnt(5)\n\t" \
    "v_mfma_f32_16x16x32_bf16 v[220:223], v[244:247], v[164:167], v[220:223]\n\t" \
    "ds_read_b128 v[240:243], %[va] offset:5120\n\t" \
    "v_mfma_f32_16x16x32_bf16 v[212:215], v[244:247], v[148:151], v[212:215]\n\t" \
    "s_waitcnt lgkmcnt(3)\n\t" \
    "v_mfma_f32_16x16x32_bf16 v[216:219], v[224:227], v[248:251], v[216:219]\n\t" \
    "ds_read_b128 v[244:247], %[va] offset:11264\n\t" \
    "v_mfma_f32_16x16x32_bf16 v[200:203], v[224:227], v[180:183], 0\n\t" \
    "v_mfma_f32_16x16x32_bf16 v[220:223], v[228:231], v[248:251], v[220:223]\n\t" \
    "ds_read_b128 v[224:227], %[va] offset:0\n\t" \
    "v_mfma_f32_16x16x32_bf16 v[204:207], v[228:231], v[180:183], 0\n\t" \
    "v_cvt_pk_bf16_f32 v208, v208, v209\n\t" \
    "s_waitcnt lgkmcnt(4)\n\t" \
    "v_mfma_f32_16x16x32_bf16 v[216:219], v[232:235], v[168:171], v[216:219]\n\t" \
    "ds_read_b128 v[228:231], %[va] offset:6144\n\t" \
    "v_cvt_pk_bf16_f32 v209, v210, v211\n\t" \
    "v_mfma_f32_16x16x32_bf16 v[200:203], v[232:235], v[192:195], v[200:203]\n\t" \
    "v_cvt_pk_bf16_f32 v210, v212, v213\n\t" \
    "s_waitcnt lgkmcnt(4)\n\t" \
    "v_mfma_f32_16x16x32_bf16 v[220:223], v[236:239], v[168:171], v[220:223]\n\t" \
    "ds_read_b128 v[232:235], %[va] offset:2048\n\t" \
    "v_cvt_pk_bf16_f32 v211, v214, v215\n\t" \
    "v_mfma_f32_16x16x32_bf16 v[204:207], v[236:239], v[192:195], v[204:207]\n\t" \
    "buffer_store_dwordx4 v[208:211], %[l16], %[rd], %[so] offen sc1\n\t" \
    "s_waitcnt lgkmcnt(4)\n\t" \
    "v_mfma_f32_16x16x32_bf16 v[216:219], v[240:243], v[172:175], v[216:219]\n\t" \
    "ds_read_b128 v[236:239], %[va] offset:8192\n\t" \
    "s_add_u32 %[so], %[so], 0x10000\n\t" \
    "v_mfma_f32_16x16x32_bf16 v[200:203], v[240:243], v[196:199], v[200:203]\n\t" \
    "s_waitcnt lgkmcnt(4)\n\t" \
    "v_mfma_f32_16x16x32_bf16 v[220:223], v[244:247], v[172:175], v[220:223]\n\t" \
    "ds_read_b128 v[240:243], %[va] offset:3072\n\t" \
    "v_mfma_f32_16x16x32_bf16 v[204:207], v[244:247], v[196:199], v[204:207]\n\t" \
    "s_waitcnt lgkmcnt(4)\n\t" \
    "v_mfma_f32_16x16x32_bf16 v[200:203], v[224:227], v[176:179], v[200:203]\n\t" \
    "ds_read_b128 v[244:247], %[va] offset:9216\n\t" \
    "s_waitcnt lgkmcnt(4)\n\t" \
    "v_mfma_f32_16x16x32_bf16 v[204:207], v[228:231], v[176:179], v[204:207]\n\t" \
    "s_waitcnt lgkmcnt(3)\n\t" \
    "v_mfma_f32_16x16x32_bf16 v[200:203], v[232:235], v[184:187], v[200:203]\n\t" \
    "s_waitcnt lgkmcnt(2)\n\t" \
    "v_mfma_f32_16x16x32_bf16 v[204:207], v[236:239], v[184:187], v[204:207]\n\t" \
    "v_cvt_pk_bf16_f32 v216, v216, v217\n\t" \
    "s_waitcnt lgkmcnt(1)\n\t" \
    "v_mfma_f32_16x16x32_bf16 v[200:203], v[240:243], v[188:191], v[200:203]\n\t" \
    "v_cvt_pk_bf16_f32 v217, v218, v219\n\t" \
    "s_waitcnt lgkmcnt(0)\n\t" \
    "v_mfma_f32_16x16x32_bf16 v[204:207], v[244:247], v[188:191], v[204:207]\n\t" \
    "v_cvt_pk_bf16_f32 v218, v220, v221\n\t" \
    "v_cvt_pk_bf16_f32 v219, v222, v223\n\t" \
    "buffer_store_dwordx4 v[216:219], %[l16], %[rd], %[so] offen sc1\n\t" \
    "s_add_u32 %[so], %[so], 0x10000\n\t" \
    "s_nop 0\n\t" \
    "v_cvt_pk_bf16_f32 v200, v200, v201\n\t" \
    "v_cvt_pk_bf16_f32 v201, v202, v203\n\t" \
    "s_nop 1\n\t" \
    "v_cvt_pk_bf16_f32 v202, v204, v205\n\t" \
    "v_cvt_pk_bf16_f32 v203, v206, v207\n\t" \
    "buffer_store_dwordx4 v[200:203], %[l16], %[rd], %[so] offen sc1\n\t" \
    "s_add_u32 %[so], %[so], 0x10000\n\t" \
    "s_nop 1\n\t"
#define ASM_CRIT2 \
    "s_waitcnt lgkmcnt(0)\n\t" \
    "s_barrier\n\t" ASM_ST \
    "ds_read_b128 v[204:207], v200 offset:11264\n\t" \
    "ds_read_b128 v[208:211], v200 offset:12288\n\t" \
    "ds_read_b128 v[200:203], v200 offset:10240\n\t" \
    "s_waitcnt lgkmcnt(2)\n\t" \
    "v_smfmac_f32_16x16x64_bf16 v[224:227], v[204:207], v[24:31], %[sidx]\n\t" \
    "v_smfmac_f32_16x16x64_bf16 v[220:223], v[204:207], v[0:7], %[sidx]\n\t" \
    "s_waitcnt lgkmcnt(1)\n\t" \
    "v_smfmac_f32_16x16x64_bf16 v[224:227], v[208:211], v[32:39], %[sidx]\n\t" \
    "v_smfmac_f32_16x16x64_bf16 v[220:223], v[208:211], v[8:15], %[sidx]\n\t" \
    "s_waitcnt lgkmcnt(0)\n\t" \
    "v_smfmac_f32_16x16x64_bf16 v[224:227], v[200:203], v[16:23], %[sidx]\n\t" \
    "v_smfmac_f32_16x16x64_bf16 v[220:223], v[200:203], v[212:219], %[sidx]\n\t" \
    "ds_read_b128 v[212:215], %[wl] offset:2048\n\t" \
    "ds_read_b128 v[216:219], %[wl] offset:3072\n\t" \
    "v_smfmac_f32_16x16x64_bf16 v[232:235], v[204:207], v[64:71], %[sidx]\n\t" \
    "v_smfmac_f32_16x16x64_bf16 v[228:231], v[204:207], v[40:47], %[sidx]\n\t" \
    "v_smfmac_f32_16x16x64_bf16 v[232:235], v[208:211], v[72:79], %[sidx]\n\t" \
    "v_permlane32_swap_b32 v220, v224\n\t" \
    "v_permlane32_swap_b32 v221, v225\n\t" \
    "v_permlane32_swap_b32 v222, v226\n\t" \
    "v_smfmac_f32_16x16x64_bf16 v[228:231], v[208:211], v[48:55], %[sidx]\n\t" \
    "v_permlane32_swap_b32 v223, v227\n\t" \
    "v_add_f32 v220, v220, v224\n\t" \
    "v_add_f32 v221, v221, v225\n\t" \
    "v_smfmac_f32_16x16x64_bf16 v[232:235], v[200:203], v[56:63], %[sidx]\n\t" \
    "v_add_f32 v222, v222, v226\n\t" \
    "v_add_f32 v223, v223, v227\n\t" \
    "v_cvt_pk_bf16_f32 v226, v220, v221\n\t" \
    "s_waitcnt lgkmcnt(0)\n\t" \
    "v_smfmac_f32_16x16x64_bf16 v[228:231], v[200:203], v[212:219], %[sidx]\n\t" \
    "ds_read_b128 v[212:215], %[wl] offset:4096\n\t" \
    "ds_read_b128 v[216:219], %[wl] offset:5120\n\t" \
    "v_cvt_pk_bf16_f32 v227, v222, v223\n\t" \
    "v_min_u32 v226, -2, v226\n\t" \
    "v_min_u32 v227, -2, v227\n\t" \
    "v_smfmac_f32_16x16x64_bf16 v[240:243], v[204:207], v[104:111], %[sidx]\n\t" \
    "v_smfmac_f32_16x16x64_bf16 v[236:239], v[204:207], v[80:87], %[sidx]\n\t" \
    "v_smfmac_f32_16x16x64_bf16 v[240:243], v[208:211], v[112:119], %[sidx]\n\t" \
    "v_permlane32_swap_b32 v228, v232\n\t" \
    "v_permlane32_swap_b32 v229, v233\n\t" \
    "v_permlane32_swap_b32 v230, v234\n\t" \
    "v_smfmac_f32_16x16x64_bf16 v[236:239], v[208:211], v[88:95], %[sidx]\n\t" \
    "v_permlane32_swap_b32 v231, v235\n\t" \
    "v_add_f32 v228, v228, v232\n\t" \
    "v_add_f32 v229, v229, v233\n\t" \
    "v_smfmac_f32_16x16x64_bf16 v[240:243], v[200:203], v[96:103], %[sidx]\n\t" \
    "v_add_f32 v230, v230, v234\n\t" \
    "v_add_f32 v231, v231, v235\n\t" \
    "v_cvt_pk_bf16_f32 v228, v228, v229\n\t" \
    "s_waitcnt lgkmcnt(0)\n\t" \
    "v_smfmac_f32_16x16x64_bf16 v[236:239], v[200:203], v[212:219], %[sidx]\n\t" \
    "ds_read_b128 v[212:215], %[wl] offset:6144\n\t" \
    "ds_read_b128 v[216:219], %[wl] offset:7168\n\t" \
    "v_cvt_pk_bf16_f32 v229, v230, v231\n\t" \
    "v_min_u32 v228, -2, v228\n\t" \
    "v_min_u32 v229, -2, v229\n\t" \
    "s_nop 1\n\t" \
    "v_permlane16_swap_b32 v226, v228\n\t" \
    "v_permlane16_swap_b32 v227, v229\n\t" \
    "buffer_store_dwordx4 v[226:229], %[vo], %[rr], %[so] offen sc1\n\t" \
    "s_add_u32 %[so], %[so], 0x10000\n\t" \
    "v_smfmac_f32_16x16x64_bf16 v[248:251], v[204:207], v[144:151], %[sidx]\n\t" \
    "v_mov_b64 v[220:221], 0\n\t" \
    "v_mov_b64 v[222:223], 0\n\t" \
    "v_smfmac_f32_16x16x64_bf16 v[244:247], v[204:207], v[120:127], %[sidx]\n\t" \
    "v_mov_b64 v[224:225], 0\n\t" \
    "v_mov_b64 v[226:227], 0\n\t" \
    "v_smfmac_f32_16x16x64_bf16 v[248:251], v[208:211], v[152:159], %[sidx]\n\t" \
    "v_permlane32_swap_b32 v236, v240\n\t" \
    "v_permlane32_swap_b32 v237, v241\n\t" \
    "v_permlane32_swap_b32 v238, v242\n\t" \
    "v_smfmac_f32_16x16x64_bf16 v[244:247], v[208:211], v[128:135], %[sidx]\n\t" \
    "v_permlane32_swap_b32 v239, v243\n\t" \
    "v_add_f32 v236, v236, v240\n\t" \
    "v_add_f32 v237, v237, v241\n\t" \
    "v_smfmac_f32_16x16x64_bf16 v[248:251], v[200:203], v[136:143], %[sidx]\n\t" \
    "v_add_f32 v238, v238, v242\n\t" \
    "v_add_f32 v239, v239, v243\n\t" \
    "v_cvt_pk_bf16_f32 v242, v236, v237\n\t" \
    "s_waitcnt lgkmcnt(0)\n\t" \
    "v_smfmac_f32_16x16x64_bf16 v[244:247], v[200:203], v[212:219], %[sidx]\n\t" \
    "ds_read_b128 v[212:215], %[wl] offset:8192\n\t" \
    "ds_read_b128 v[216:219], %[wl] offset:9216\n\t" \
    "v_cvt_pk_bf16_f32 v243, v238, v239\n\t" \
    "v_min_u32 v242, -2, v242\n\t" \
    "v_min_u32 v243, -2, v243\n\t" \
    "v_smfmac_f32_16x16x64_bf16 v[224:227], v[204:207], v[184:191], %[sidx]\n\t" \
    "v_and_b32 v232, 0x7fff, %[vo]\n\t" \
    "v_lshrrev_b32 v233, 15, %[vo]\n\t" \
    "v_smfmac_f32_16x16x64_bf16 v[220:223], v[204:207], v[160:167], %[sidx]\n\t" \
    "v_lshl_add_u32 v232, v233, 3, v232\n\t" \
    "v_permlane32_swap_b32 v244, v248\n\t" \
    "v_smfmac_f32_16x16x64_bf16 v[224:227], v[208:211], v[192:199], %[sidx]\n\t" \
    "v_permlane32_swap_b32 v245, v249\n\t" \
    "v_permlane32_swap_b32 v246, v250\n\t" \
    "v_permlane32_swap_b32 v247, v251\n\t" \
    "v_smfmac_f32_16x16x64_bf16 v[220:223], v[208:211], v[168:175], %[sidx]\n\t" \
    "v_add_f32 v244, v244, v248\n\t" \
    "v_add_f32 v245, v245, v249\n\t" \
    "v_add_f32 v246, v246, v250\n\t" \
    "v_smfmac_f32_16x16x64_bf16 v[224:227], v[200:203], v[176:183], %[sidx]\n\t" \
    "v_add_f32 v247, v247, v251\n\t" \
    "v_cvt_pk_bf16_f32 v244, v244, v245\n\t" \
    "v_cvt_pk_bf16_f32 v245, v246, v247\n\t" \
    "s_waitcnt lgkmcnt(0)\n\t" \
    "v_smfmac_f32_16x16x64_bf16 v[220:223], v[200:203], v[212:219], %[sidx]\n\t" \
    "v_min_u32 v244, -2, v244\n\t" \
    "v_min_u32 v245, -2, v245\n\t" \
    "s_nop 1\n\t" \
    "v_permlane16_swap_b32 v242, v244\n\t" \
    "v_permlane16_swap_b32 v243, v245\n\t" \
    "buffer_store_dwordx4 v[242:245], %[vo], %[rr], %[so] offen sc1\n\t" \
    "s_add_u32 %[so], %[so], 0x10000\n\t" \
    "s_nop 0\n\t" \
    "v_permlane32_swap_b32 v220, v224\n\t" \
    "v_permlane32_swap_b32 v221, v225\n\t" \
    "v_permlane32_swap_b32 v222, v226\n\t" \
    "v_permlane32_swap_b32 v223, v227\n\t" \
    "v_add_f32 v220, v220, v224\n\t" \
    "v_add_f32 v221, v221, v225\n\t" \
    "v_add_f32 v222, v222, v226\n\t" \
    "v_add_f32 v223, v223, v227\n\t" \
    "v_cvt_pk_bf16_f32 v220, v220, v221\n\t" \
    "v_cvt_pk_bf16_f32 v221, v222, v223\n\t" \
    "v_min_u32 v220, -2, v220\n\t" \
    "v_min_u32 v221, -2, v221\n\t" \
    "buffer_store_dwordx2 v[220:221], v232, %[rr], %[so] offen sc1\n\t" \
    "s_nop 1\n\t"
#define ASM_P4_0 \
    "ds_read_b128 v[200:203], %[qa] offset:0\n\t" \
    "ds_read_b128 v[204:207], %[qa] offset:1024\n\t" \
    "ds_read_b128 v[208:211], %[qa] offset:2048\n\t" \
    "ds_read_b128 v[212:215], %[qa] offset:3072\n\t" \
    "ds_read_b128 v[216:219], %[qa] offset:4096\n\t" \
    "ds_read_b128 v[220:223], %[qa] offset:5120\n\t" \
    "ds_read_b128 v[224:227], %[qa] offset:12288\n\t" \
    "ds_read_b128 v[228:231], %[qa] offset:13312\n\t" \
    "ds_read_b128 v[232:235], %[qa] offset:14336\n\t" \
    "ds_read_b128 v[236:239], %[qa] offset:15360\n\t" \
    "s_waitcnt lgkmcnt(0)\n\t" \
    "s_barrier\n\t" ASM_ST \
    "v_mfma_f32_16x16x32_bf16 v[240:243], v[224:227], v[200:203], v[240:243]\n\t" \
    "ds_read_b128 v[224:227], %[qa] offset:16384\n\t" \
    "v_mfma_f32_16x16x32_bf16 v[240:243], v[228:231], v[204:207], v[240:243]\n\t" \
    "ds_read_b128 v[228:231], %[qa] offset:17408\n\t" \
    "v_mfma_f32_16x16x32_bf16 v[240:243], v[232:235], v[208:211], v[240:243]\n\t" \
    "ds_read_b128 v[232:235], %[km] offset:0\n\t" \
    "v_mfma_f32_16x16x32_bf16 v[240:243], v[236:239], v[212:215], v[240:243]\n\t" \
    "ds_read_b128 v[236:239], %[km] offset:512\n\t" \
    "s_waitcnt lgkmcnt(3)\n\t" \
    "v_mfma_f32_16x16x32_bf16 v[240:243], v[224:227], v[216:219], v[240:243]\n\t" \
    "ds_read_b128 v[224:227], %[km] offset:1024\n\t" \
    "s_waitcnt lgkmcnt(3)\n\t" \
    "v_mfma_f32_16x16x32_bf16 v[240:243], v[228:231], v[220:223], v[240:243]\n\t" \
    "ds_read_b128 v[228:231], %[km] offset:1536\n\t" \
    "s_waitcnt lgkmcnt(3)\n\t" \
    "v_mfma_f32_16x16x32_bf16 v[248:251], v[232:235], v[200:203], 0\n\t" \
    "ds_read_b128 v[232:235], %[km] offset:2048\n\t" \
    "s_waitcnt lgkmcnt(3)\n\t" \
    "v_mfma_f32_16x16x32_bf16 v[248:251], v[236:239], v[204:207], v[248:251]\n\t" \
    "ds_read_b128 v[236:239], %[km] offset:2560\n\t" \
    "s_waitcnt lgkmcnt(3)\n\t" \
    "v_mfma_f32_16x16x32_bf16 v[248:251], v[224:227], v[208:211], v[248:251]\n\t" \
    "s_waitcnt lgkmcnt(2)\n\t" \
    "v_mfma_f32_16x16x32_bf16 v[248:251], v[228:231], v[212:215], v[248:251]\n\t" \
    "s_waitcnt lgkmcnt(1)\n\t" \
    "v_mfma_f32_16x16x32_bf16 v[248:251], v[232:235], v[216:219], v[248:251]\n\t" \
    "s_waitcnt lgkmcnt(0)\n\t" \
    "v_mfma_f32_16x16x32_bf16 v[248:251], v[236:239], v[220:223], v[248:251]\n\t" \
    "v_max3_f32 v224, v240, v241, v242\n\t" \
    "v_mov_b32 v225, v243\n\t" \
    "v_lshlrev_b32 v201, 23, %[qa]\n\t" \
    "v_and_b32 v201, 0x80000000, v201\n\t" \
    "v_and_b32 v200, 0x3ff, %[qa]\n\t" \
    "v_or_b32 v200, v201, v200\n\t" \
    "s_nop 1\n\t" \
    "v_max3_f32 v224, v224, v248, v249\n\t" \
    "v_max3_f32 v225, v225, v250, v251\n\t" \
    "v_max_f32 v224, v224, v225\n\t" \
    "v_mov_b32 v225, v224\n\t" \
    "s_nop 1\n\t" \
    "v_permlane16_swap_b32 v224, v225\n\t" \
    "v_max_f32 v224, v224, v225\n\t" \
    "v_mov_b32 v225, v224\n\t" \
    "s_nop 1\n\t" \
    "v_permlane32_swap_b32 v224, v225\n\t" \
    "v_max_f32 v224, v224, v225\n\t" \
    "v_mul_f32 v224, %[scl], v224\n\t" \
    "v_fma_f32 v240, v240, %[scl], -v224\n\t" \
    "v_fma_f32 v241, v241, %[scl], -v224\n\t" \
    "v_exp_f32 v240, v240\n\t" \
    "v_fma_f32 v242, v242, %[scl], -v224\n\t" \
    "v_exp_f32 v241, v241\n\t" \
    "v_fma_f32 v243, v243, %[scl], -v224\n\t" \
    "v_exp_f32 v242, v242\n\t" \
    "v_fma_f32 v248, v248, %[scl], -v224\n\t" \
    "v_exp_f32 v243, v243\n\t" \
    "v_fma_f32 v249, v249, %[scl], -v224\n\t" \
    "v_exp_f32 v248, v248\n\t" \
    "v_fma_f32 v250, v250, %[scl], -v224\n\t" \
    "v_exp_f32 v249, v249\n\t" \
    "v_fma_f32 v251, v251, %[scl], -v224\n\t" \
    "v_exp_f32 v250, v250\n\t" \
    "v_exp_f32 v251, v251\n\t" \
    "v_add_f32 v224, v240, v241\n\t" \
    "v_add_f32 v225, v242, v243\n\t" \
    "v_add_f32 v232, v248, v249\n\t" \
    "v_add_f32 v233, v250, v251\n\t" \
    "v_add_f32 v224, v224, v225\n\t" \
    "v_add_f32 v232, v232, v233\n\t" \
    "v_fma_f32 v224, v232, 0.5, v224\n\t" \
    "v_mov_b32 v225, v224\n\t" \
    "s_nop 1\n\t" \
    "v_permlane16_swap_b32 v224, v225\n\t" \
    "v_add_f32 v224, v224, v225\n\t" \
    "v_mov_b32 v225, v224\n\t" \
    "s_nop 1\n\t" \
    "v_permlane32_swap_b32 v224, v225\n\t" \
    "v_add_f32 v224, v224, v225\n\t" \
    "v_rcp_f32 v224, v224\n\t" \
    "s_nop 0\n\t" \
    "v_mul_f32 v240, v240, v224\n\t" \
    "v_mul_f32 v241, v241, v224\n\t" \
    "v_mul_f32 v242, v242, v224\n\t" \
    "v_mul_f32 v243, v243, v224\n\t" \
    "v_mul_f32 v248, v248, v224\n\t" \
    "v_mul_f32 v249, v249, v224\n\t" \
    "v_mul_f32 v250, v250, v224\n\t" \
    "v_mul_f32 v251, v251, v224\n\t" \
    "v_cvt_pk_bf16_f32 v228, |v240|, |v241|\n\t" \
    "v_cvt_pk_bf16_f32 v229, |v242|, |v243|\n\t" \
    "v_cvt_pk_bf16_f32 v232, |v248|, |v249|\n\t" \
    "v_cvt_pk_bf16_f32 v233, |v250|, |v251|\n\t" \
    "v_permlane16_swap_b32 v228, v230\n\t" \
    "v_permlane16_swap_b32 v229, v231\n\t" \
    "buffer_store_dwordx4 v[228:231], v200, %[rr], %[so] offen sc1\n\t" \
    "buffer_store_dwordx2 v[232:233], %[pm], %[rr], %[so] offen sc1\n\t" \
    "s_nop 1\n\t"
#define ASM_P4_1 \
    "ds_read_b128 v[200:203], %[qa] offset:0\n\t" \
    "ds_read_b128 v[204:207], %[qa] offset:1024\n\t" \
    "ds_read_b128 v[208:211], %[qa] offset:2048\n\t" \
    "ds_read_b128 v[212:215], %[qa] offset:3072\n\t" \
    "ds_read_b128 v[216:219], %[qa] offset:4096\n\t" \
    "ds_read_b128 v[220:223], %[qa] offset:5120\n\t" \
    "ds_read_b128 v[224:227], %[qa] offset:12288\n\t" \
    "ds_read_b128 v[228:231], %[qa] offset:13312\n\t" \
    "ds_read_b128 v[232:235], %[qa] offset:14336\n\t" \
    "ds_read_b128 v[236:239], %[qa] offset:15360\n\t" \
    "s_waitcnt lgkmcnt(0)\n\t" \
    "s_barrier\n\t" ASM_ST \
    "v_mfma_f32_16x16x32_bf16 v[240:243], v[224:227], v[200:203], v[240:243]\n\t" \
    "ds_read_b128 v[224:227], %[qa] offset:16384\n\t" \
    "v_mfma_f32_16x16x32_bf16 v[240:243], v[228:231], v[204:207], v[240:243]\n\t" \
    "ds_read_b128 v[228:231], %[qa] offset:17408\n\t" \
    "v_mfma_f32_16x16x32_bf16 v[240:243], v[232:235], v[208:211], v[240:243]\n\t" \
    "ds_read_b128 v[232:235], %[qa] offset:6144\n\t" \
    "v_mfma_f32_16x16x32_bf16 v[240:243], v[236:239], v[212:215], v[240:243]\n\t" \
    "ds_read_b128 v[236:239], %[qa] offset:7168\n\t" \
    "s_waitcnt lgkmcnt(3)\n\t" \
    "v_mfma_f32_16x16x32_bf16 v[240:243], v[224:227], v[216:219], v[240:243]\n\t" \
    "ds_read_b128 v[224:227], %[qa] offset:8192\n\t" \
    "s_waitcnt lgkmcnt(3)\n\t" \
    "v_mfma_f32_16x16x32_bf16 v[240:243], v[228:231], v[220:223], v[240:243]\n\t" \
    "ds_read_b128 v[228:231], %[qa] offset:9216\n\t" \
    "s_waitcnt lgkmcnt(3)\n\t" \
    "v_mfma_f32_16x16x32_bf16 v[244:247], v[232:235], v[200:203], 0\n\t" \
    "ds_read_b128 v[232:235], %[qa] offset:10240\n\t" \
    "s_waitcnt lgkmcnt(3)\n\t" \
    "v_mfma_f32_16x16x32_bf16 v[244:247], v[236:239], v[204:207], v[244:247]\n\t" \
    "ds_read_b128 v[236:239], %[qa] offset:11264\n\t" \
    "s_waitcnt lgkmcnt(3)\n\t" \
    "v_mfma_f32_16x16x32_bf16 v[244:247], v[224:227], v[208:211], v[244:247]\n\t" \
    "ds_read_b128 v[224:227], %[km] offset:0\n\t" \
    "s_waitcnt lgkmcnt(3)\n\t" \
    "v_mfma_f32_16x16x32_bf16 v[244:247], v[228:231], v[212:215], v[244:247]\n\t" \
    "ds_read_b128 v[228:231], %[km] offset:512\n\t" \
    "s_waitcnt lgkmcnt(3)\n\t" \
    "v_mfma_f32_16x16x32_bf16 v[244:247], v[232:235], v[216:219], v[244:247]\n\t" \
    "ds_read_b128 v[232:235], %[km] offset:1024\n\t" \
    "s_waitcnt lgkmcnt(3)\n\t" \
    "v_mfma_f32_16x16x32_bf16 v[244:247], v[236:239], v[220:223], v[244:247]\n\t" \
    "ds_read_b128 v[236:239], %[km] offset:1536\n\t" \
    "s_waitcnt lgkmcnt(3)\n\t" \
    "v_mfma_f32_16x16x32_bf16 v[248:251], v[224:227], v[200:203], 0\n\t" \
    "ds_read_b128 v[224:227], %[km] offset:2048\n\t" \
    "s_waitcnt lgkmcnt(3)\n\t" \
    "v_mfma_f32_16x16x32_bf16 v[248:251], v[228:231], v[204:207], v[248:251]\n\t" \
    "ds_read_b128 v[228:231], %[km] offset:2560\n\t" \
    "s_waitcnt lgkmcnt(3)\n\t" \
    "v_mfma_f32_16x16x32_bf16 v[248:251], v[232:235], v[208:211], v[248:251]\n\t" \
    "s_waitcnt lgkmcnt(2)\n\t" \
    "v_mfma_f32_16x16x32_bf16 v[248:251], v[236:239], v[212:215], v[248:251]\n\t" \
    "s_waitcnt lgkmcnt(1)\n\t" \
    "v_mfma_f32_16x16x32_bf16 v[248:251], v[224:227], v[216:219], v[248:251]\n\t" \
    "s_waitcnt lgkmcnt(0)\n\t" \
    "v_mfma_f32_16x16x32_bf16 v[248:251], v[228:231], v[220:223], v[248:251]\n\t" \
    "v_max3_f32 v224, v240, v241, v242\n\t" \
    "v_max3_f32 v225, v244, v245, v246\n\t" \
    "v_max3_f32 v224, v224, v243, v247\n\t" \
    "v_and_b32 v200, 0x3ff, %[qa]\n\t" \
    "s_nop 3\n\t" \
    "v_max3_f32 v224, v224, v248, v249\n\t" \
    "v_max3_f32 v225, v225, v250, v251\n\t" \
    "v_max_f32 v224, v224, v225\n\t" \
    "v_mov_b32 v225, v224\n\t" \
    "s_nop 1\n\t" \
    "v_permlane16_swap_b32 v224, v225\n\t" \
    "v_max_f32 v224, v224, v225\n\t" \
    "v_mov_b32 v225, v224\n\t" \
    "s_nop 1\n\t" \
    "v_permlane32_swap_b32 v224, v225\n\t" \
    "v_max_f32 v224, v224, v225\n\t" \
    "v_mul_f32 v224, %[scl], v224\n\t" \
    "v_fma_f32 v240, v240, %[scl], -v224\n\t" \
    "v_fma_f32 v241, v241, %[scl], -v224\n\t" \
    "v_exp_f32 v240, v240\n\t" \
    "v_fma_f32 v242, v242, %[scl], -v224\n\t" \
    "v_exp_f32 v241, v241\n\t" \
    "v_fma_f32 v243, v243, %[scl], -v224\n\t" \
    "v_exp_f32 v242, v242\n\t" \
    "v_fma_f32 v244, v244, %[scl], -v224\n\t" \
    "v_exp_f32 v243, v243\n\t" \
    "v_fma_f32 v245, v245, %[scl], -v224\n\t" \
    "v_exp_f32 v244, v244\n\t" \
    "v_fma_f32 v246, v246, %[scl], -v224\n\t" \
    "v_exp_f32 v245, v245\n\t" \
    "v_fma_f32 v247, v247, %[scl], -v224\n\t" \
    "v_exp_f32 v246, v246\n\t" \
    "v_fma_f32 v248, v248, %[scl], -v224\n\t" \
    "v_exp_f32 v247, v247\n\t" \
    "v_fma_f32 v249, v249, %[scl], -v224\n\t" \
    "v_exp_f32 v248, v248\n\t" \
    "v_fma_f32 v250, v250, %[scl], -v224\n\t" \
    "v_exp_f32 v249, v249\n\t" \
    "v_fma_f32 v251, v251, %[scl], -v224\n\t" \
    "v_exp_f32 v250, v250\n\t" \
    "v_exp_f32 v251, v251\n\t" \
    "v_add_f32 v224, v240, v241\n\t" \
    "v_add_f32 v225, v242, v243\n\t" \
    "v_add_f32 v226, v244, v245\n\t" \
    "v_add_f32 v227, v246, v247\n\t" \
    "v_add_f32 v232, v248, v249\n\t" \
    "v_add_f32 v233, v250, v251\n\t" \
    "v_add_f32 v224, v224, v225\n\t" \
    "v_add_f32 v226, v226, v227\n\t" \
    "v_add_f32 v232, v232, v233\n\t" \
    "v_add_f32 v224, v224, v226\n\t" \
    "v_fma_f32 v224, v232, 0.5, v224\n\t" \
    "v_mov_b32 v225, v224\n\t" \
    "s_nop 1\n\t" \
    "v_permlane16_swap_b32 v224, v225\n\t" \
    "v_add_f32 v224, v224, v225\n\t" \
    "v_mov_b32 v225, v224\n\t" \
    "s_nop 1\n\t" \
    "v_permlane32_swap_b32 v224, v225\n\t" \
    "v_add_f32 v224, v224, v225\n\t" \
    "v_rcp_f32 v224, v224\n\t" \
    "s_nop 0\n\t" \
    "v_mul_f32 v240, v240, v224\n\t" \
    "v_mul_f32 v241, v241, v224\n\t" \
    "v_mul_f32 v242, v242, v224\n\t" \
    "v_mul_f32 v243, v243, v224\n\t" \
    "v_mul_f32 v244, v244, v224\n\t" \
    "v_mul_f32 v245, v245, v224\n\t" \
    "v_mul_f32 v246, v246, v224\n\t" \
    "v_mul_f32 v247, v247, v224\n\t" \
    "v_mul_f32 v248, v248, v224\n\t" \
    "v_mul_f32 v249, v249, v224\n\t" \
    "v_mul_f32 v250, v250, v224\n\t" \
    "v_mul_f32 v251, v251, v224\n\t" \
    "v_cvt_pk_bf16_f32 v230, |v240|, |v241|\n\t" \
    "v_cvt_pk_bf16_f32 v231, |v242|, |v243|\n\t" \
    "v_cvt_pk_bf16_f32 v228, |v244|, |v245|\n\t" \
    "v_cvt_pk_bf16_f32 v229, |v246|, |v247|\n\t" \
    "v_cvt_pk_bf16_f32 v232, |v248|, |v249|\n\t" \
    "v_cvt_pk_bf16_f32 v233, |v250|, |v251|\n\t" \
    "buffer_store_dwordx4 v[228:231], v200, %[rr], %[so] offen sc1\n\t" \
    "buffer_store_dwordx2 v[232:233], %[pm], %[rr], %[so] offen sc1\n\t" \
    "s_nop 1\n\t"
#define ASM_RCRIT \
    "s_movk_i32 %[n], 0x400\n\t" \
    ".Lfa%=:\n\t" \
    "s_mov_b32 %[a], 0\n\t" \
    "v_max_u32 v196, v176, v177\n\t" \
    "v_max3_u32 v196, v178, v179, v196\n\t" \
    "v_cmp_eq_u32 vcc, -1, v196\n\t" \
    "s_cbranch_vccz .Lga0_%=\n\t" \
    "buffer_load_dwordx4 v[176:179], %[ov], %[rs], %[sv] offen sc1\n\t" \
    "s_mov_b32 %[a], 1\n\t" \
    ".Lga0_%=:\n\t" \
    "v_max_u32 v196, v180, v181\n\t" \
    "v_max3_u32 v196, v182, v183, v196\n\t" \
    "v_cmp_eq_u32 vcc, -1, v196\n\t" \
    "s_cbranch_vccz .Lga1_%=\n\t" \
    "buffer_load_dwordx4 v[180:183], %[ov], %[rs], %[sv] offen offset:1024 sc1\n\t" \
    "s_mov_b32 %[a], 1\n\t" \
    ".Lga1_%=:\n\t" \
    "s_cmp_eq_u32 %[a], 0\n\t" \
    "s_cbranch_scc1 .Lha%=\n\t" \
    "s_waitcnt vmcnt(12)\n\t" \
    "v_max_u32 v196, v176, v177\n\t" \
    "v_max3_u32 v196, v178, v179, v196\n\t" \
    "v_max3_u32 v196, v180, v181, v196\n\t" \
    "v_max3_u32 v196, v182, v183, v196\n\t" \
    "v_cmp_eq_u32 vcc, -1, v196\n\t" \
    "s_cbranch_vccz .Lha%=\n\t" \
    "s_nop 7\n\t" \
    "v_max_u32 v196, v176, v177\n\t" \
    "v_max3_u32 v196, v178, v179, v196\n\t" \
    "v_max3_u32 v196, v180, v181, v196\n\t" \
    "v_max3_u32 v196, v182, v183, v196\n\t" \
    "v_cmp_eq_u32 vcc, -1, v196\n\t" \
    "s_cbranch_vccz .Lha%=\n\t" \
    "s_nop 7\n\t" \
    "v_max_u32 v196, v176, v177\n\t" \
    "v_max3_u32 v196, v178, v179, v196\n\t" \
    "v_max3_u32 v196, v180, v181, v196\n\t" \
    "v_max3_u32 v196, v182, v183, v196\n\t" \
    "v_cmp_eq_u32 vcc, -1, v196\n\t" \
    "s_cbranch_vccz .Lha%=\n\t" \
    "s_nop 7\n\t" \
    "v_max_u32 v196, v176, v177\n\t" \
    "v_max3_u32 v196, v178, v179, v196\n\t" \
    "v_max3_u32 v196, v180, v181, v196\n\t" \
    "v_max3_u32 v196, v182, v183, v196\n\t" \
    "v_cmp_eq_u32 vcc, -1, v196\n\t" \
    "s_cbranch_vccz .Lha%=\n\t" \
    "s_nop 7\n\t" \
    "s_sub_u32 %[n], %[n], 1\n\t" \
    "s_cmp_lg_u32 %[n], 0\n\t" \
    "s_cbranch_scc1 .Lfa%=\n\t" \
    ".Lha%=:\n\t" \
    "s_cmp_eq_u32 %[n], 0\n\t" \
    "s_cbranch_scc1 .Lre%=\n\t" \
    ".Lfb%=:\n\t" \
    "s_mov_b32 %[a], 0\n\t" \
    "v_max_u32 v196, v184, v185\n\t" \
    "v_max3_u32 v196, v186, v187, v196\n\t" \
    "v_cmp_eq_u32 vcc, -1, v196\n\t" \
    "s_cbranch_vccz .Lgb0_%=\n\t" \
    "buffer_load_dwordx4 v[184:187], %[o0], %[rs], %[sp] offen sc1\n\t" \
    "s_mov_b32 %[a], 1\n\t" \
    ".Lgb0_%=:\n\t" \
    "v_max_u32 v196, v188, v189\n\t" \
    "v_max3_u32 v196, v190, v191, v196\n\t" \
    "v_cmp_eq_u32 vcc, -1, v196\n\t" \
    "s_cbranch_vccz .Lgb1_%=\n\t" \
    "buffer_load_dwordx4 v[188:191], %[o1], %[rs], %[sp] offen sc1\n\t" \
    "s_mov_b32 %[a], 1\n\t" \
    ".Lgb1_%=:\n\t" \
    "v_max_u32 v196, v192, v193\n\t" \
    "v_max3_u32 v196, v194, v195, v196\n\t" \
    "v_cmp_eq_u32 vcc, -1, v196\n\t" \
    "s_cbranch_vccz .Lgb2_%=\n\t" \
    "buffer_load_dwordx4 v[192:195], %[o2], %[rs], %[sp] offen sc1\n\t" \
    "s_mov_b32 %[a], 1\n\t" \
    ".Lgb2_%=:\n\t" \
    "s_cmp_eq_u32 %[a], 0\n\t" \
    "s_cbranch_scc1 .Lhb%=\n\t" \
    "s_waitcnt vmcnt(12)\n\t" \
    "v_max_u32 v196, v184, v185\n\t" \
    "v_max3_u32 v196, v186, v187, v196\n\t" \
    "v_max3_u32 v196, v188, v189, v196\n\t" \
    "v_max3_u32 v196, v190, v191, v196\n\t" \
    "v_max3_u32 v196, v192, v193, v196\n\t" \
    "v_max3_u32 v196, v194, v195, v196\n\t" \
    "v_cmp_eq_u32 vcc, -1, v196\n\t" \
    "s_cbranch_vccz .Lhb%=\n\t" \
    "s_nop 7\n\t" \
    "v_max_u32 v196, v184, v185\n\t" \
    "v_max3_u32 v196, v186, v187, v196\n\t" \
    "v_max3_u32 v196, v188, v189, v196\n\t" \
    "v_max3_u32 v196, v190, v191, v196\n\t" \
    "v_max3_u32 v196, v192, v193, v196\n\t" \
    "v_max3_u32 v196, v194, v195, v196\n\t" \
    "v_cmp_eq_u32 vcc, -1, v196\n\t" \
    "s_cbranch_vccz .Lhb%=\n\t" \
    "s_nop 7\n\t" \
    "v_max_u32 v196, v184, v185\n\t" \
    "v_max3_u32 v196, v186, v187, v196\n\t" \
    "v_max3_u32 v196, v188, v189, v196\n\t" \
    "v_max3_u32 v196, v190, v191, v196\n\t" \
    "v_max3_u32 v196, v192, v193, v196\n\t" \
    "v_max3_u32 v196, v194, v195, v196\n\t" \
    "v_cmp_eq_u32 vcc, -1, v196\n\t" \
    "s_cbranch_vccz .Lhb%=\n\t" \
    "s_nop 7\n\t" \
    "v_max_u32 v196, v184, v185\n\t" \
    "v_max3_u32 v196, v186, v187, v196\n\t" \
    "v_max3_u32 v196, v188, v189, v196\n\t" \
    "v_max3_u32 v196, v190, v191, v196\n\t" \
    "v_max3_u32 v196, v192, v193, v196\n\t" \
    "v_max3_u32 v196, v194, v195, v196\n\t" \
    "v_cmp_eq_u32 vcc, -1, v196\n\t" \
    "s_cbranch_vccz .Lhb%=\n\t" \
    "s_nop 7\n\t" \
    "v_max_u32 v196, v184, v185\n\t" \
    "v_max3_u32 v196, v186, v187, v196\n\t" \
    "v_max3_u32 v196, v188, v189, v196\n\t" \
    "v_max3_u32 v196, v190, v191, v196\n\t" \
    "v_max3_u32 v196, v192, v193, v196\n\t" \
    "v_max3_u32 v196, v194, v195, v196\n\t" \
    "v_cmp_eq_u32 vcc, -1, v196\n\t" \
    "s_cbranch_vccz .Lhb%=\n\t" \
    "s_nop 7\n\t" \
    "v_max_u32 v196, v184, v185\n\t" \
    "v_max3_u32 v196, v186, v187, v196\n\t" \
    "v_max3_u32 v196, v188, v189, v196\n\t" \
    "v_max3_u32 v196, v190, v191, v196\n\t" \
    "v_max3_u32 v196, v192, v193, v196\n\t" \
    "v_max3_u32 v196, v194, v195, v196\n\t" \
    "v_cmp_eq_u32 vcc, -1, v196\n\t" \
    "s_cbranch_vccz .Lhb%=\n\t" \
    "s_nop 7\n\t" \
    "v_max_u32 v196, v184, v185\n\t" \
    "v_max3_u32 v196, v186, v187, v196\n\t" \
    "v_max3_u32 v196, v188, v189, v196\n\t" \
    "v_max3_u32 v196, v190, v191, v196\n\t" \
    "v_max3_u32 v196, v192, v193, v196\n\t" \
    "v_max3_u32 v196, v194, v195, v196\n\t" \
    "v_cmp_eq_u32 vcc, -1, v196\n\t" \
    "s_cbranch_vccz .Lhb%=\n\t" \
    "s_nop 7\n\t" \
    "v_max_u32 v196, v184, v185\n\t" \
    "v_max3_u32 v196, v186, v187, v196\n\t" \
    "v_max3_u32 v196, v188, v189, v196\n\t" \
    "v_max3_u32 v196, v190, v191, v196\n\t" \
    "v_max3_u32 v196, v192, v193, v196\n\t" \
    "v_max3_u32 v196, v194, v195, v196\n\t" \
    "v_cmp_eq_u32 vcc, -1, v196\n\t" \
    "s_cbranch_vccz .Lhb%=\n\t" \
    "s_nop 7\n\t" \
    "s_sub_u32 %[n], %[n], 1\n\t" \
    "s_cmp_lg_u32 %[n], 0\n\t" \
    "s_cbranch_scc1 .Lfb%=\n\t" \
    "s_branch .Lre%=\n\t" \
    ".Lhb%=:\n\t" \
    ASM_ST2 \
    "v_mfma_f32_16x16x32_bf16 v[160:163], v[192:195], v[176:179], 0\n\t" \
    "v_mfma_f32_16x16x32_bf16 v[164:167], v[192:195], v[180:183], 0\n\t" \
    "v_smfmac_f32_16x16x64_bf16 v[160:163], v[184:187], v[128:135], %[sidx]\n\t" \
    "v_smfmac_f32_16x16x64_bf16 v[164:167], v[184:187], v[144:151], %[sidx]\n\t" \
    "v_smfmac_f32_16x16x64_bf16 v[160:163], v[188:191], v[136:143], %[sidx]\n\t" \
    "v_smfmac_f32_16x16x64_bf16 v[164:167], v[188:191], v[152:159], %[sidx]\n\t" \
    "s_cmp_eq_u32 %[pub], 0\n\t" \
    "s_nop 7\n\t" \
    "v_permlane32_swap_b32 v160, v164\n\t" \
    "v_permlane32_swap_b32 v161, v165\n\t" \
    "v_permlane32_swap_b32 v162, v166\n\t" \
    "v_permlane32_swap_b32 v163, v167\n\t" \
    "v_add_f32 v160, v160, v164\n\t" \
    "v_add_f32 v161, v161, v165\n\t" \
    "ds_write2_b32 %[rw], v160, v161 offset1:80\n\t" \
    "v_add_f32 v162, v162, v166\n\t" \
    "v_add_f32 v163, v163, v167\n\t" \
    "ds_write2_b32 %[rw], v162, v163 offset0:160 offset1:240\n\t" \
    "s_waitcnt lgkmcnt(0)\n\t" \
    "s_barrier\n\t" ASM_ST \
    "s_cbranch_scc1 .Lre%=\n\t" \
    "ds_read_b128 v[200:203], %[rd] offset:0\n\t" \
    "ds_read_b128 v[204:207], %[rd] offset:16\n\t" \
    "s_waitcnt lgkmcnt(1)\n\t" \
    "v_pk_add_f32 v[200:201], v[200:201], v[202:203]\n\t" \
    "s_waitcnt lgkmcnt(0)\n\t" \
    "v_pk_add_f32 v[204:205], v[204:205], v[206:207]\n\t" \
    "v_pk_add_f32 v[200:201], v[200:201], v[204:205]\n\t" \
    "s_nop 1\n\t" \
    "v_permlane32_swap_b32 v200, v201\n\t" \
    "v_add_f32 v202, v200, v201\n\t" \
    "v_add_f32 v203, v200, v201\n\t" \
    "s_nop 1\n\t" \
    "v_permlane32_swap_b32 v202, v203\n\t" \
    "v_cvt_pk_bf16_f32 v200, v202, v203\n\t" \
    "v_min_u32 v200, -2, v200\n\t" \
    "buffer_store_dword v200, %[oo], %[rs], %[so] offen sc1\n\t" \
    ".Lre%=:\n\t"
#define ASM_RX \
    "s_cmp_eq_u32 %[fin], 0\n\t" \
    "s_cbranch_scc1 .Lx0%=\n\t" \
    "buffer_load_dwordx2 v[250:251], %[xo], %[rx], %[sx] offen sc1\n\t" \
    ".Lx0%=:\n\t" \
    "buffer_load_dword v248, %[fo], %[rs], %[fs] offen sc1\n\t" \
    "buffer_load_dwordx4 v[200:203], %[l16], %[rs], %[s0] offen offset:0 sc1\n\t" \
    "buffer_load_dwordx4 v[204:207], %[l16], %[rs], %[s0] offen offset:1024 sc1\n\t" \
    "buffer_load_dwordx4 v[208:211], %[l16], %[rs], %[s0] offen offset:2048 sc1\n\t" \
    "buffer_load_dwordx4 v[212:215], %[l16], %[rs], %[s0] offen offset:3072 sc1\n\t" \
    "buffer_load_dwordx4 v[216:219], %[l16], %[rs], %[s1] offen offset:0 sc1\n\t" \
    "buffer_load_dwordx4 v[220:223], %[l16], %[rs], %[s1] offen offset:1024 sc1\n\t" \
    "buffer_load_dwordx4 v[224:227], %[l16], %[rs], %[s1] offen offset:2048 sc1\n\t" \
    "buffer_load_dwordx4 v[228:231], %[l16], %[rs], %[s1] offen offset:3072 sc1\n\t" \
    "v_mov_b64 v[96:97], v[128:129]\n\t" \
    "v_mov_b64 v[98:99], v[132:133]\n\t" \
    "v_mov_b64 v[100:101], v[136:137]\n\t" \
    "v_mov_b64 v[102:103], v[140:141]\n\t" \
    "v_mov_b64 v[104:105], v[144:145]\n\t" \
    "v_mov_b64 v[106:107], v[148:149]\n\t" \
    "v_mov_b64 v[108:109], v[152:153]\n\t" \
    "v_mov_b64 v[110:111], v[156:157]\n\t" \
    "s_movk_i32 %[n], 0x200\n\t" \
    "s_nop 1\n\t" \
    "v_permlane16_swap_b32 v96, v98\n\t" \
    "v_permlane16_swap_b32 v97, v99\n\t" \
    "v_permlane16_swap_b32 v100, v102\n\t" \
    "v_permlane16_swap_b32 v101, v103\n\t" \
    "v_permlane16_swap_b32 v104, v106\n\t" \
    "v_permlane16_swap_b32 v105, v107\n\t" \
    "v_permlane16_swap_b32 v108, v110\n\t" \
    "v_permlane16_swap_b32 v109, v111\n\t" \
    "s_waitcnt vmcnt(7)\n\t" \
    "v_max_u32 v196, v200, v201\n\t" \
    "v_max3_u32 v196, v202, v203, v196\n\t" \
    "s_waitcnt vmcnt(6)\n\t" \
    "v_max3_u32 v196, v204, v205, v196\n\t" \
    "v_max3_u32 v196, v206, v207, v196\n\t" \
    "s_waitcnt vmcnt(5)\n\t" \
    "v_max3_u32 v196, v208, v209, v196\n\t" \
    "v_max3_u32 v196, v210, v211, v196\n\t" \
    "s_waitcnt vmcnt(4)\n\t" \
    "v_max3_u32 v196, v212, v213, v196\n\t" \
    "v_max3_u32 v196, v214, v215, v196\n\t" \
    "s_waitcnt vmcnt(3)\n\t" \
    "v_max3_u32 v196, v216, v217, v196\n\t" \
    "v_max3_u32 v196, v218, v219, v196\n\t" \
    "s_waitcnt vmcnt(2)\n\t" \
    "v_max3_u32 v196, v220, v221, v196\n\t" \
    "v_max3_u32 v196, v222, v223, v196\n\t" \
    "s_waitcnt vmcnt(1)\n\t" \
    "v_max3_u32 v196, v224, v225, v196\n\t" \
    "v_max3_u32 v196, v226, v227, v196\n\t" \
    "s_waitcnt vmcnt(0)\n\t" \
    "v_max3_u32 v196, v228, v229, v196\n\t" \
    "v_max3_u32 v196, v230, v231, v196\n\t" \
    "v_cmp_eq_u32 vcc, -1, v196\n\t" \
    "s_cbranch_vccz .Lxd%=\n\t" \
    ".Lxl%=:\n\t" \
    "s_mov_b32 %[a], 0\n\t" \
    "v_max_u32 v196, v200, v201\n\t" \
    "v_max3_u32 v196, v202, v203, v196\n\t" \
    "v_cmp_eq_u32 vcc, -1, v196\n\t" \
    "s_cbranch_vccz .Lxs0_%=\n\t" \
    "buffer_load_dwordx4 v[200:203], %[l16], %[rs], %[s0] offen offset:0 sc1\n\t" \
    "s_mov_b32 %[a], 1\n\t" \
    ".Lxs0_%=:\n\t" \
    "v_max_u32 v196, v204, v205\n\t" \
    "v_max3_u32 v196, v206, v207, v196\n\t" \
    "v_cmp_eq_u32 vcc, -1, v196\n\t" \
    "s_cbranch_vccz .Lxs1_%=\n\t" \
    "buffer_load_dwordx4 v[204:207], %[l16], %[rs], %[s0] offen offset:1024 sc1\n\t" \
    "s_mov_b32 %[a], 1\n\t" \
    ".Lxs1_%=:\n\t" \
    "v_max_u32 v196, v208, v209\n\t" \
    "v_max3_u32 v196, v210, v211, v196\n\t" \
    "v_cmp_eq_u32 vcc, -1, v196\n\t" \
    "s_cbranch_vccz .Lxs2_%=\n\t" \
    "buffer_load_dwordx4 v[208:211], %[l16], %[rs], %[s0] offen offset:2048 sc1\n\t" \
    "s_mov_b32 %[a], 1\n\t" \
    ".Lxs2_%=:\n\t" \
    "v_max_u32 v196, v212, v213\n\t" \
    "v_max3_u32 v196, v214, v215, v196\n\t" \
    "v_cmp_eq_u32 vcc, -1, v196\n\t" \
    "s_cbranch_vccz .Lxs3_%=\n\t" \
    "buffer_load_dwordx4 v[212:215], %[l16], %[rs], %[s0] offen offset:3072 sc1\n\t" \
    "s_mov_b32 %[a], 1\n\t" \
    ".Lxs3_%=:\n\t" \
    "v_max_u32 v196, v216, v217\n\t" \
    "v_max3_u32 v196, v218, v219, v196\n\t" \
    "v_cmp_eq_u32 vcc, -1, v196\n\t" \
    "s_cbranch_vccz .Lxs4_%=\n\t" \
    "buffer_load_dwordx4 v[216:219], %[l16], %[rs], %[s1] offen offset:0 sc1\n\t" \
    "s_mov_b32 %[a], 1\n\t" \
    ".Lxs4_%=:\n\t" \
    "v_max_u32 v196, v220, v221\n\t" \
    "v_max3_u32 v196, v222, v223, v196\n\t" \
    "v_cmp_eq_u32 vcc, -1, v196\n\t" \
    "s_cbranch_vccz .Lxs5_%=\n\t" \
    "buffer_load_dwordx4 v[220:223], %[l16], %[rs], %[s1] offen offset:1024 sc1\n\t" \
    "s_mov_b32 %[a], 1\n\t" \
    ".Lxs5_%=:\n\t" \
    "v_max_u32 v196, v224, v225\n\t" \
    "v_max3_u32 v196, v226, v227, v196\n\t" \
    "v_cmp_eq_u32 vcc, -1, v196\n\t" \
    "s_cbranch_vccz .Lxs6_%=\n\t" \
    "buffer_load_dwordx4 v[224:227], %[l16], %[rs], %[s1] offen offset:2048 sc1\n\t" \
    "s_mov_b32 %[a], 1\n\t" \
    ".Lxs6_%=:\n\t" \
    "v_max_u32 v196, v228, v229\n\t" \
    "v_max3_u32 v196, v230, v231, v196\n\t" \
    "v_cmp_eq_u32 vcc, -1, v196\n\t" \
    "s_cbranch_vccz .Lxs7_%=\n\t" \
    "buffer_load_dwordx4 v[228:231], %[l16], %[rs], %[s1] offen offset:3072 sc1\n\t" \
    "s_mov_b32 %[a], 1\n\t" \
    ".Lxs7_%=:\n\t" \
    "s_cmp_eq_u32 %[a], 0\n\t" \
    "s_cbranch_scc1 .Lxd%=\n\t" \
    "s_waitcnt vmcnt(0)\n\t" \
    "s_sub_u32 %[n], %[n], 1\n\t" \
    "s_cmp_lg_u32 %[n], 0\n\t" \
    "s_cbranch_scc1 .Lxl%=\n\t" \
    "s_branch .Lxe%=\n\t" \
    ".Lxd%=:\n\t" \
    ASM_ST2 \
    "v_mfma_f32_16x16x32_bf16 v[120:123], v[128:131], v[200:203], 0\n\t" \
    "v_mfma_f32_16x16x32_bf16 v[124:127], v[144:147], v[200:203], 0\n\t" \
    "v_mfma_f32_16x16x32_bf16 v[120:123], v[132:135], v[204:207], v[120:123]\n\t" \
    "v_mfma_f32_16x16x32_bf16 v[124:127], v[148:151], v[204:207], v[124:127]\n\t" \
    "v_mfma_f32_16x16x32_bf16 v[120:123], v[136:139], v[208:211], v[120:123]\n\t" \
    "v_mfma_f32_16x16x32_bf16 v[124:127], v[152:155], v[208:211], v[124:127]\n\t" \
    "v_mfma_f32_16x16x32_bf16 v[120:123], v[140:143], v[212:215], v[120:123]\n\t" \
    "v_mfma_f32_16x16x32_bf16 v[124:127], v[156:159], v[212:215], v[124:127]\n\t" \
    "v_mfma_f32_16x16x32_bf16 v[112:115], v[96:99], v[220:223], 0\n\t" \
    "v_mfma_f32_16x16x32_bf16 v[116:119], v[104:107], v[220:223], 0\n\t" \
    "v_mfma_f32_16x16x32_bf16 v[120:123], v[176:179], v[216:219], v[120:123]\n\t" \
    "v_mfma_f32_16x16x32_bf16 v[124:127], v[180:183], v[216:219], v[124:127]\n\t" \
    "v_mfma_f32_16x16x32_bf16 v[112:115], v[100:103], v[224:227], v[112:115]\n\t" \
    "v_mfma_f32_16x16x32_bf16 v[116:119], v[108:111], v[224:227], v[116:119]\n\t" \
    "v_mfma_f32_16x16x32_bf16 v[112:115], v[176:179], v[228:231], v[112:115]\n\t" \
    "v_mfma_f32_16x16x32_bf16 v[116:119], v[180:183], v[228:231], v[116:119]\n\t" \
    "s_nop 1\n\t" \
    "ds_write_b128 %[pw], v[120:123] offset:16384\n\t" \
    "ds_write_b128 %[pw], v[124:127] offset:24576\n\t" \
    "s_nop 3\n\t" \
    "ds_write_b128 %[pw], v[112:115] offset:0\n\t" \
    "ds_write_b128 %[pw], v[116:119] offset:8192\n\t" \
    "v_cmp_ge_u32 vcc, %[tn], v248\n\t" \
    "s_mov_b32 %[a], 0\n\t" \
    "s_cbranch_vccnz .Lxv%=\n\t" \
    "buffer_load_dwordx4 v[128:131], %[l16], %[rp], %[sv0] offen offset:0 sc1\n\t" \
    "buffer_load_dwordx4 v[132:135], %[l16], %[rp], %[sv0] offen offset:1024 sc1\n\t" \
    "buffer_load_dwordx4 v[136:139], %[l16], %[rp], %[sv0] offen offset:2048 sc1\n\t" \
    "buffer_load_dwordx4 v[140:143], %[l16], %[rp], %[sv0] offen offset:3072 sc1\n\t" \
    "buffer_load_dwordx4 v[144:147], %[l16], %[rp], %[sv1] offen offset:0 sc1\n\t" \
    "buffer_load_dwordx4 v[148:151], %[l16], %[rp], %[sv1] offen offset:1024 sc1\n\t" \
    "buffer_load_dwordx4 v[152:155], %[l16], %[rp], %[sv1] offen offset:2048 sc1\n\t" \
    "buffer_load_dwordx4 v[156:159], %[l16], %[rp], %[sv1] offen offset:3072 sc1\n\t" \
    "s_mov_b32 %[a], 1\n\t" \
    ".Lxv%=:\n\t" \
    "s_cmp_eq_u32 %[fin], 0\n\t" \
    "s_waitcnt lgkmcnt(0)\n\t" \
    "s_barrier\n\t" ASM_ST \
    "s_cbranch_scc1 .Lxe%=\n\t" \
    "ds_read_b128 v[200:203], %[pr] offset:0\n\t" \
    "ds_read_b128 v[204:207], %[pr] offset:1024\n\t" \
    "ds_read_b128 v[208:211], %[pr] offset:2048\n\t" \
    "ds_read_b128 v[212:215], %[pr] offset:3072\n\t" \
    "ds_read_b128 v[216:219], %[pr] offset:4096\n\t" \
    "ds_read_b128 v[220:223], %[pr] offset:5120\n\t" \
    "ds_read_b128 v[224:227], %[pr] offset:6144\n\t" \
    "ds_read_b128 v[228:231], %[pr] offset:7168\n\t" \
    "v_lshlrev_b32 v240, 16, v250\n\t" \
    "v_and_b32 v241, 0xffff0000, v250\n\t" \
    "v_lshlrev_b32 v242, 16, v251\n\t" \
    "v_and_b32 v243, 0xffff0000, v251\n\t" \
    "s_waitcnt lgkmcnt(6)\n\t" \
    "v_pk_add_f32 v[200:201], v[200:201], v[204:205]\n\t" \
    "v_pk_add_f32 v[202:203], v[202:203], v[206:207]\n\t" \
    "s_waitcnt lgkmcnt(4)\n\t" \
    "v_pk_add_f32 v[208:209], v[208:209], v[212:213]\n\t" \
    "v_pk_add_f32 v[210:211], v[210:211], v[214:215]\n\t" \
    "v_pk_add_f32 v[200:201], v[200:201], v[208:209]\n\t" \
    "v_pk_add_f32 v[202:203], v[202:203], v[210:211]\n\t" \
    "s_waitcnt lgkmcnt(2)\n\t" \
    "v_pk_add_f32 v[216:217], v[216:217], v[220:221]\n\t" \
    "v_pk_add_f32 v[218:219], v[218:219], v[222:223]\n\t" \
    "s_waitcnt lgkmcnt(0)\n\t" \
    "v_pk_add_f32 v[224:225], v[224:225], v[228:229]\n\t" \
    "v_pk_add_f32 v[226:227], v[226:227], v[230:231]\n\t" \
    "v_pk_add_f32 v[216:217], v[216:217], v[224:225]\n\t" \
    "v_pk_add_f32 v[218:219], v[218:219], v[226:227]\n\t" \
    "v_pk_add_f32 v[200:201], v[200:201], v[216:217]\n\t" \
    "v_pk_add_f32 v[202:203], v[202:203], v[218:219]\n\t" \
    "v_pk_add_f32 v[200:201], v[200:201], v[240:241]\n\t" \
    "v_pk_add_f32 v[202:203], v[202:203], v[242:243]\n\t" \
    "v_cvt_pk_bf16_f32 v200, v200, v201\n\t" \
    "v_cvt_pk_bf16_f32 v201, v202, v203\n\t" \
    "v_min_u32 v200, -2, v200\n\t" \
    "v_min_u32 v201, -2, v201\n\t" \
    "buffer_store_dwordx2 v[200:201], %[xo], %[rd], %[sa] offen sc1\n\t" \
    ".Lxe%=:\n\t"
#define ASM_W4_M \
    "buffer_load_dwordx4 v[184:187], %[l16], %[rs], %[s1] offen offset:2048 sc1\n\t" \
    "buffer_load_dwordx4 v[180:183], %[l16], %[rs], %[s1] offen offset:1024 sc1\n\t" \
    "buffer_load_dwordx4 v[176:179], %[l16], %[rs], %[s1] offen offset:0 sc1\n\t" \
    "buffer_load_dwordx4 v[172:175], %[l16], %[rs], %[s0] offen offset:3072 sc1\n\t" \
    "buffer_load_dwordx4 v[168:171], %[l16], %[rs], %[s0] offen offset:2048 sc1\n\t" \
    "buffer_load_dwordx4 v[164:167], %[l16], %[rs], %[s0] offen offset:1024 sc1\n\t" \
    "buffer_load_dwordx4 v[160:163], %[l16], %[rs], %[s0] offen offset:0 sc1\n\t" \
    "buffer_load_dwordx4 v[220:223], %[l16], %[rs], %[s3] offen offset:3072 sc1\n\t" \
    "v_mfma_f32_16x16x32_bf16 v[224:227], v[28:31], v[188:191], 0\n\t" \
    "v_mfma_f32_16x16x32_bf16 v[228:231], v[60:63], v[188:191], 0\n\t" \
    "v_mfma_f32_16x16x32_bf16 v[232:235], v[92:95], v[188:191], 0\n\t" \
    "v_mfma_f32_16x16x32_bf16 v[236:239], v[124:127], v[188:191], 0\n\t" \
    "v_mfma_f32_16x16x32_bf16 v[240:243], v[156:159], v[188:191], 0\n\t" \
    "s_waitcnt vmcnt(7)\n\t" \
    ".Lc06_%=:\n\t" \
    "v_max_u32 v244, v184, v185\n\t" \
    "v_max3_u32 v244, v186, v187, v244\n\t" \
    "v_cmp_eq_u32 vcc, -1, v244\n\t" \
    "s_cbranch_vccnz .Ls06_%=\n\t" \
    ".Lk06_%=:\n\t" \
    "v_mfma_f32_16x16x32_bf16 v[224:227], v[24:27], v[184:187], v[224:227]\n\t" \
    "v_mfma_f32_16x16x32_bf16 v[228:231], v[56:59], v[184:187], v[228:231]\n\t" \
    "v_mfma_f32_16x16x32_bf16 v[232:235], v[88:91], v[184:187], v[232:235]\n\t" \
    "v_mfma_f32_16x16x32_bf16 v[236:239], v[120:123], v[184:187], v[236:239]\n\t" \
    "v_mfma_f32_16x16x32_bf16 v[240:243], v[152:155], v[184:187], v[240:243]\n\t" \
    "s_waitcnt vmcnt(6)\n\t" \
    ".Lc05_%=:\n\t" \
    "v_max_u32 v244, v180, v181\n\t" \
    "v_max3_u32 v244, v182, v183, v244\n\t" \
    "v_cmp_eq_u32 vcc, -1, v244\n\t" \
    "s_cbranch_vccnz .Ls05_%=\n\t" \
    ".Lk05_%=:\n\t" \
    "v_mfma_f32_16x16x32_bf16 v[224:227], v[20:23], v[180:183], v[224:227]\n\t" \
    "v_mfma_f32_16x16x32_bf16 v[228:231], v[52:55], v[180:183], v[228:231]\n\t" \
    "v_mfma_f32_16x16x32_bf16 v[232:235], v[84:87], v[180:183], v[232:235]\n\t" \
    "v_mfma_f32_16x16x32_bf16 v[236:239], v[116:119], v[180:183], v[236:239]\n\t" \
    "v_mfma_f32_16x16x32_bf16 v[240:243], v[148:151], v[180:183], v[240:243]\n\t" \
    "s_waitcnt vmcnt(5)\n\t" \
    ".Lc04_%=:\n\t" \
    "v_max_u32 v244, v176, v177\n\t" \
    "v_max3_u32 v244, v178, v179, v244\n\t" \
    "v_cmp_eq_u32 vcc, -1, v244\n\t" \
    "s_cbranch_vccnz .Ls04_%=\n\t" \
    ".Lk04_%=:\n\t" \
    "v_mfma_f32_16x16x32_bf16 v[224:227], v[16:19], v[176:179], v[224:227]\n\t" \
    "v_mfma_f32_16x16x32_bf16 v[228:231], v[48:51], v[176:179], v[228:231]\n\t" \
    "v_mfma_f32_16x16x32_bf16 v[232:235], v[80:83], v[176:179], v[232:235]\n\t" \
    "v_mfma_f32_16x16x32_bf16 v[236:239], v[112:115], v[176:179], v[236:239]\n\t" \
    "v_mfma_f32_16x16x32_bf16 v[240:243], v[144:147], v[176:179], v[240:243]\n\t" \
    "s_waitcnt vmcnt(4)\n\t" \
    ".Lc03_%=:\n\t" \
    "v_max_u32 v244, v172, v173\n\t" \
    "v_max3_u32 v244, v174, v175, v244\n\t" \
    "v_cmp_eq_u32 vcc, -1, v244\n\t" \
    "s_cbranch_vccnz .Ls03_%=\n\t" \
    ".Lk03_%=:\n\t" \
    "v_mfma_f32_16x16x32_bf16 v[224:227], v[12:15], v[172:175], v[224:227]\n\t" \
    "v_mfma_f32_16x16x32_bf16 v[228:231], v[44:47], v[172:175], v[228:231]\n\t" \
    "v_mfma_f32_16x16x32_bf16 v[232:235], v[76:79], v[172:175], v[232:235]\n\t" \
    "v_mfma_f32_16x16x32_bf16 v[236:239], v[108:111], v[172:175], v[236:239]\n\t" \
    "v_mfma_f32_16x16x32_bf16 v[240:243], v[140:143], v[172:175], v[240:243]\n\t" \
    "s_waitcnt vmcnt(3)\n\t" \
    ".Lc02_%=:\n\t" \
    "v_max_u32 v244, v168, v169\n\t" \
    "v_max3_u32 v244, v170, v171, v244\n\t" \
    "v_cmp_eq_u32 vcc, -1, v244\n\t" \
    "s_cbranch_vccnz .Ls02_%=\n\t" \
    ".Lk02_%=:\n\t" \
    "v_mfma_f32_16x16x32_bf16 v[224:227], v[8:11], v[168:171], v[224:227]\n\t" \
    "v_mfma_f32_16x16x32_bf16 v[228:231], v[40:43], v[168:171], v[228:231]\n\t" \
    "v_mfma_f32_16x16x32_bf16 v[232:235], v[72:75], v[168:171], v[232:235]\n\t" \
    "v_mfma_f32_16x16x32_bf16 v[236:239], v[104:107], v[168:171], v[236:239]\n\t" \
    "v_mfma_f32_16x16x32_bf16 v[240:243], v[136:139], v[168:171], v[240:243]\n\t" \
    "s_waitcnt vmcnt(2)\n\t" \
    ".Lc01_%=:\n\t" \
    "v_max_u32 v244, v164, v165\n\t" \
    "v_max3_u32 v244, v166, v167, v244\n\t" \
    "v_cmp_eq_u32 vcc, -1, v244\n\t" \
    "s_cbranch_vccnz .Ls01_%=\n\t" \
    ".Lk01_%=:\n\t" \
    "v_mfma_f32_16x16x32_bf16 v[224:227], v[4:7], v[164:167], v[224:227]\n\t" \
    "v_mfma_f32_16x16x32_bf16 v[228:231], v[36:39], v[164:167], v[228:231]\n\t" \
    "v_mfma_f32_16x16x32_bf16 v[232:235], v[68:71], v[164:167], v[232:235]\n\t" \
    "v_mfma_f32_16x16x32_bf16 v[236:239], v[100:103], v[164:167], v[236:239]\n\t" \
    "v_mfma_f32_16x16x32_bf16 v[240:243], v[132:135], v[164:167], v[240:243]\n\t" \
    "s_waitcnt vmcnt(1)\n\t" \
    ".Lc00_%=:\n\t" \
    "v_max_u32 v244, v160, v161\n\t" \
    "v_max3_u32 v244, v162, v163, v244\n\t" \
    "v_cmp_eq_u32 vcc, -1, v244\n\t" \
    "s_cbranch_vccnz .Ls00_%=\n\t" \
    ".Lk00_%=:\n\t" \
    "v_mfma_f32_16x16x32_bf16 v[224:227], v[0:3], v[160:163], v[224:227]\n\t" \
    "v_mfma_f32_16x16x32_bf16 v[228:231], v[32:35], v[160:163], v[228:231]\n\t" \
    "v_mfma_f32_16x16x32_bf16 v[232:235], v[64:67], v[160:163], v[232:235]\n\t" \
    "v_mfma_f32_16x16x32_bf16 v[236:239], v[96:99], v[160:163], v[236:239]\n\t" \
    "v_mfma_f32_16x16x32_bf16 v[240:243], v[128:131], v[160:163], v[240:243]\n\t" \
    "s_waitcnt vmcnt(0)\n\t" \
    ".Lc17_%=:\n\t" \
    "v_max_u32 v244, v220, v221\n\t" \
    "v_max3_u32 v244, v222, v223, v244\n\t" \
    "v_cmp_eq_u32 vcc, -1, v244\n\t" \
    "s_cbranch_vccnz .Ls17_%=\n\t" \
    ".Lk17_%=:\n\t" \
    "buffer_load_dwordx4 v[216:219], %[l16], %[rs], %[s3] offen offset:2048 sc1\n\t" \
    "buffer_load_dwordx4 v[212:215], %[l16], %[rs], %[s3] offen offset:1024 sc1\n\t" \
    "buffer_load_dwordx4 v[208:211], %[l16], %[rs], %[s3] offen offset:0 sc1\n\t" \
    "buffer_load_dwordx4 v[204:207], %[l16], %[rs], %[s2] offen offset:3072 sc1\n\t" \
    "buffer_load_dwordx4 v[200:203], %[l16], %[rs], %[s2] offen offset:2048 sc1\n\t" \
    "buffer_load_dwordx4 v[196:199], %[l16], %[rs], %[s2] offen offset:1024 sc1\n\t" \
    "buffer_load_dwordx4 v[192:195], %[l16], %[rs], %[s2] offen offset:0 sc1\n\t" \
    "v_mfma_f32_16x16x32_bf16 v[160:163], v[28:31], v[220:223], 0\n\t" \
    "ds_write_b128 %[ld], v[224:227] offset:0\n\t" \
    "v_mfma_f32_16x16x32_bf16 v[164:167], v[60:63], v[220:223], 0\n\t" \
    "ds_write_b128 %[ld], v[228:231] offset:6144\n\t" \
    "v_mfma_f32_16x16x32_bf16 v[168:171], v[92:95], v[220:223], 0\n\t" \
    "ds_write_b128 %[ld], v[232:235] offset:12288\n\t" \
    "v_mfma_f32_16x16x32_bf16 v[172:175], v[124:127], v[220:223], 0\n\t" \
    "ds_write_b128 %[ld], v[236:239] offset:18432\n\t" \
    "v_mfma_f32_16x16x32_bf16 v[176:179], v[156:159], v[220:223], 0\n\t" \
    "ds_write_b128 %[ld], v[240:243] offset:24576\n\t" \
    "s_waitcnt vmcnt(6)\n\t" \
    ".Lc16_%=:\n\t" \
    "v_max_u32 v244, v216, v217\n\t" \
    "v_max3_u32 v244, v218, v219, v244\n\t" \
    "v_cmp_eq_u32 vcc, -1, v244\n\t" \
    "s_cbranch_vccnz .Ls16_%=\n\t" \
    ".Lk16_%=:\n\t" \
    "v_mfma_f32_16x16x32_bf16 v[160:163], v[24:27], v[216:219], v[160:163]\n\t" \
    "v_mfma_f32_16x16x32_bf16 v[164:167], v[56:59], v[216:219], v[164:167]\n\t" \
    "v_mfma_f32_16x16x32_bf16 v[168:171], v[88:91], v[216:219], v[168:171]\n\t" \
    "v_mfma_f32_16x16x32_bf16 v[172:175], v[120:123], v[216:219], v[172:175]\n\t" \
    "v_mfma_f32_16x16x32_bf16 v[176:179], v[152:155], v[216:219], v[176:179]\n\t" \
    "s_waitcnt vmcnt(5)\n\t" \
    ".Lc15_%=:\n\t" \
    "v_max_u32 v244, v212, v213\n\t" \
    "v_max3_u32 v244, v214, v215, v244\n\t" \
    "v_cmp_eq_u32 vcc, -1, v244\n\t" \
    "s_cbranch_vccnz .Ls15_%=\n\t" \
    ".Lk15_%=:\n\t" \
    "v_mfma_f32_16x16x32_bf16 v[160:163], v[20:23], v[212:215], v[160:163]\n\t" \
    "v_mfma_f32_16x16x32_bf16 v[164:167], v[52:55], v[212:215], v[164:167]\n\t" \
    "v_mfma_f32_16x16x32_bf16 v[168:171], v[84:87], v[212:215], v[168:171]\n\t" \
    "v_mfma_f32_16x16x32_bf16 v[172:175], v[116:119], v[212:215], v[172:175]\n\t" \
    "v_mfma_f32_16x16x32_bf16 v[176:179], v[148:151], v[212:215], v[176:179]\n\t" \
    "s_waitcnt vmcnt(4)\n\t" \
    ".Lc14_%=:\n\t" \
    "v_max_u32 v244, v208, v209\n\t" \
    "v_max3_u32 v244, v210, v211, v244\n\t" \
    "v_cmp_eq_u32 vcc, -1, v244\n\t" \
    "s_cbranch_vccnz .Ls14_%=\n\t" \
    ".Lk14_%=:\n\t" \
    "v_mfma_f32_16x16x32_bf16 v[160:163], v[16:19], v[208:211], v[160:163]\n\t" \
    "v_mfma_f32_16x16x32_bf16 v[164:167], v[48:51], v[208:211], v[164:167]\n\t" \
    "v_mfma_f32_16x16x32_bf16 v[168:171], v[80:83], v[208:211], v[168:171]\n\t" \
    "v_mfma_f32_16x16x32_bf16 v[172:175], v[112:115], v[208:211], v[172:175]\n\t" \
    "v_mfma_f32_16x16x32_bf16 v[176:179], v[144:147], v[208:211], v[176:179]\n\t" \
    "s_waitcnt vmcnt(3)\n\t" \
    ".Lc13_%=:\n\t" \
    "v_max_u32 v244, v204, v205\n\t" \
    "v_max3_u32 v244, v206, v207, v244\n\t" \
    "v_cmp_eq_u32 vcc, -1, v244\n\t" \
    "s_cbranch_vccnz .Ls13_%=\n\t" \
    ".Lk13_%=:\n\t" \
    "v_mfma_f32_16x16x32_bf16 v[160:163], v[12:15], v[204:207], v[160:163]\n\t" \
    "v_mfma_f32_16x16x32_bf16 v[164:167], v[44:47], v[204:207], v[164:167]\n\t" \
    "v_mfma_f32_16x16x32_bf16 v[168:171], v[76:79], v[204:207], v[168:171]\n\t" \
    "v_mfma_f32_16x16x32_bf16 v[172:175], v[108:111], v[204:207], v[172:175]\n\t" \
    "v_mfma_f32_16x16x32_bf16 v[176:179], v[140:143], v[204:207], v[176:179]\n\t" \
    "s_waitcnt vmcnt(2)\n\t" \
    ".Lc12_%=:\n\t" \
    "v_max_u32 v244, v200, v201\n\t" \
    "v_max3_u32 v244, v202, v203, v244\n\t" \
    "v_cmp_eq_u32 vcc, -1, v244\n\t" \
    "s_cbranch_vccnz .Ls12_%=\n\t" \
    ".Lk12_%=:\n\t" \
    "v_mfma_f32_16x16x32_bf16 v[160:163], v[8:11], v[200:203], v[160:163]\n\t" \
    "v_mfma_f32_16x16x32_bf16 v[164:167], v[40:43], v[200:203], v[164:167]\n\t" \
    "v_mfma_f32_16x16x32_bf16 v[168:171], v[72:75], v[200:203], v[168:171]\n\t" \
    "v_mfma_f32_16x16x32_bf16 v[172:175], v[104:107], v[200:203], v[172:175]\n\t" \
    "v_mfma_f32_16x16x32_bf16 v[176:179], v[136:139], v[200:203], v[176:179]\n\t" \
    "s_waitcnt vmcnt(1)\n\t" \
    ".Lc11_%=:\n\t" \
    "v_max_u32 v244, v196, v197\n\t" \
    "v_max3_u32 v244, v198, v199, v244\n\t" \
    "v_cmp_eq_u32 vcc, -1, v244\n\t" \
    "s_cbranch_vccnz .Ls11_%=\n\t" \
    ".Lk11_%=:\n\t" \
    "v_mfma_f32_16x16x32_bf16 v[160:163], v[4:7], v[196:199], v[160:163]\n\t" \
    "v_mfma_f32_16x16x32_bf16 v[164:167], v[36:39], v[196:199], v[164:167]\n\t" \
    "v_mfma_f32_16x16x32_bf16 v[168:171], v[68:71], v[196:199], v[168:171]\n\t" \
    "v_mfma_f32_16x16x32_bf16 v[172:175], v[100:103], v[196:199], v[172:175]\n\t" \
    "v_mfma_f32_16x16x32_bf16 v[176:179], v[132:135], v[196:199], v[176:179]\n\t" \
    "s_waitcnt vmcnt(0)\n\t" \
    ".Lc10_%=:\n\t" \
    "v_max_u32 v244, v192, v193\n\t" \
    "v_max3_u32 v244, v194, v195, v244\n\t" \
    "v_cmp_eq_u32 vcc, -1, v244\n\t" \
    "s_cbranch_vccnz .Ls10_%=\n\t" \
    ".Lk10_%=:\n\t" \
    "v_mfma_f32_16x16x32_bf16 v[160:163], v[0:3], v[192:195], v[160:163]\n\t" \
    "v_mfma_f32_16x16x32_bf16 v[164:167], v[32:35], v[192:195], v[164:167]\n\t" \
    "v_mfma_f32_16x16x32_bf16 v[168:171], v[64:67], v[192:195], v[168:171]\n\t" \
    "v_mfma_f32_16x16x32_bf16 v[172:175], v[96:99], v[192:195], v[172:175]\n\t" \
    "v_mfma_f32_16x16x32_bf16 v[176:179], v[128:131], v[192:195], v[176:179]\n\t" \
    "ds_write_b128 %[ld], v[160:163] offset:30720\n\t" \
    "ds_write_b128 %[ld], v[164:167] offset:36864\n\t" \
    "ds_write_b128 %[ld], v[168:171] offset:43008\n\t" \
    "s_nop 1\n\t" \
    "ds_write_b128 %[ld], v[172:175] offset:49152\n\t" \
    "s_nop 1\n\t" \
    "ds_write_b128 %[ld], v[176:179] offset:55296\n\t" \
    "s_branch .Lme%=\n\t" \
    ".Ls06_%=:\n\t" \
    "s_cmp_eq_u32 %[n], 0\n\t" \
    "s_cbranch_scc1 .Lk06_%=\n\t" \
    "s_sub_u32 %[n], %[n], 1\n\t" \
    "buffer_load_dwordx4 v[184:187], %[l16], %[rs], %[s1] offen offset:2048 sc1\n\t" \
    "s_waitcnt vmcnt(0)\n\t" \
    "s_branch .Lc06_%=\n\t" \
    ".Ls05_%=:\n\t" \
    "s_cmp_eq_u32 %[n], 0\n\t" \
    "s_cbranch_scc1 .Lk05_%=\n\t" \
    "s_sub_u32 %[n], %[n], 1\n\t" \
    "buffer_load_dwordx4 v[180:183], %[l16], %[rs], %[s1] offen offset:1024 sc1\n\t" \
    "s_waitcnt vmcnt(0)\n\t" \
    "s_branch .Lc05_%=\n\t" \
    ".Ls04_%=:\n\t" \
    "s_cmp_eq_u32 %[n], 0\n\t" \
    "s_cbranch_scc1 .Lk04_%=\n\t" \
    "s_sub_u32 %[n], %[n], 1\n\t" \
    "buffer_load_dwordx4 v[176:179], %[l16], %[rs], %[s1] offen offset:0 sc1\n\t" \
    "s_waitcnt vmcnt(0)\n\t" \
    "s_branch .Lc04_%=\n\t" \
    ".Ls03_%=:\n\t" \
    "s_cmp_eq_u32 %[n], 0\n\t" \
    "s_cbranch_scc1 .Lk03_%=\n\t" \
    "s_sub_u32 %[n], %[n], 1\n\t" \
    "buffer_load_dwordx4 v[172:175], %[l16], %[rs], %[s0] offen offset:3072 sc1\n\t" \
    "s_waitcnt vmcnt(0)\n\t" \
    "s_branch .Lc03_%=\n\t" \
    ".Ls02_%=:\n\t" \
    "s_cmp_eq_u32 %[n], 0\n\t" \
    "s_cbranch_scc1 .Lk02_%=\n\t" \
    "s_sub_u32 %[n], %[n], 1\n\t" \
    "buffer_load_dwordx4 v[168:171], %[l16], %[rs], %[s0] offen offset:2048 sc1\n\t" \
    "s_waitcnt vmcnt(0)\n\t" \
    "s_branch .Lc02_%=\n\t" \
    ".Ls01_%=:\n\t" \
    "s_cmp_eq_u32 %[n], 0\n\t" \
    "s_cbranch_scc1 .Lk01_%=\n\t" \
    "s_sub_u32 %[n], %[n], 1\n\t" \
    "buffer_load_dwordx4 v[164:167], %[l16], %[rs], %[s0] offen offset:1024 sc1\n\t" \
    "s_waitcnt vmcnt(0)\n\t" \
    "s_branch .Lc01_%=\n\t" \
    ".Ls00_%=:\n\t" \
    "s_cmp_eq_u32 %[n], 0\n\t" \
    "s_cbranch_scc1 .Lk00_%=\n\t" \
    "s_sub_u32 %[n], %[n], 1\n\t" \
    "buffer_load_dwordx4 v[160:163], %[l16], %[rs], %[s0] offen offset:0 sc1\n\t" \
    "s_waitcnt vmcnt(0)\n\t" \
    "s_branch .Lc00_%=\n\t" \
    ".Ls17_%=:\n\t" \
    "s_cmp_eq_u32 %[n], 0\n\t" \
    "s_cbranch_scc1 .Lk17_%=\n\t" \
    "s_sub_u32 %[n], %[n], 1\n\t" \
    "buffer_load_dwordx4 v[220:223], %[l16], %[rs], %[s3] offen offset:3072 sc1\n\t" \
    "s_waitcnt vmcnt(0)\n\t" \
    "s_branch .Lc17_%=\n\t" \
    ".Ls16_%=:\n\t" \
    "s_cmp_eq_u32 %[n], 0\n\t" \
    "s_cbranch_scc1 .Lk16_%=\n\t" \
    "s_sub_u32 %[n], %[n], 1\n\t" \
    "buffer_load_dwordx4 v[216:219], %[l16], %[rs], %[s3] offen offset:2048 sc1\n\t" \
    "s_waitcnt vmcnt(0)\n\t" \
    "s_branch .Lc16_%=\n\t" \
    ".Ls15_%=:\n\t" \
    "s_cmp_eq_u32 %[n], 0\n\t" \
    "s_cbranch_scc1 .Lk15_%=\n\t" \
    "s_sub_u32 %[n], %[n], 1\n\t" \
    "buffer_load_dwordx4 v[212:215], %[l16], %[rs], %[s3] offen offset:1024 sc1\n\t" \
    "s_waitcnt vmcnt(0)\n\t" \
    "s_branch .Lc15_%=\n\t" \
    ".Ls14_%=:\n\t" \
    "s_cmp_eq_u32 %[n], 0\n\t" \
    "s_cbranch_scc1 .Lk14_%=\n\t" \
    "s_sub_u32 %[n], %[n], 1\n\t" \
    "buffer_load_dwordx4 v[208:211], %[l16], %[rs], %[s3] offen offset:0 sc1\n\t" \
    "s_waitcnt vmcnt(0)\n\t" \
    "s_branch .Lc14_%=\n\t" \
    ".Ls13_%=:\n\t" \
    "s_cmp_eq_u32 %[n], 0\n\t" \
    "s_cbranch_scc1 .Lk13_%=\n\t" \
    "s_sub_u32 %[n], %[n], 1\n\t" \
    "buffer_load_dwordx4 v[204:207], %[l16], %[rs], %[s2] offen offset:3072 sc1\n\t" \
    "s_waitcnt vmcnt(0)\n\t" \
    "s_branch .Lc13_%=\n\t" \
    ".Ls12_%=:\n\t" \
    "s_cmp_eq_u32 %[n], 0\n\t" \
    "s_cbranch_scc1 .Lk12_%=\n\t" \
    "s_sub_u32 %[n], %[n], 1\n\t" \
    "buffer_load_dwordx4 v[200:203], %[l16], %[rs], %[s2] offen offset:2048 sc1\n\t" \
    "s_waitcnt vmcnt(0)\n\t" \
    "s_branch .Lc12_%=\n\t" \
    ".Ls11_%=:\n\t" \
    "s_cmp_eq_u32 %[n], 0\n\t" \
    "s_cbranch_scc1 .Lk11_%=\n\t" \
    "s_sub_u32 %[n], %[n], 1\n\t" \
    "buffer_load_dwordx4 v[196:199], %[l16], %[rs], %[s2] offen offset:1024 sc1\n\t" \
    "s_waitcnt vmcnt(0)\n\t" \
    "s_branch .Lc11_%=\n\t" \
    ".Ls10_%=:\n\t" \
    "s_cmp_eq_u32 %[n], 0\n\t" \
    "s_cbranch_scc1 .Lk10_%=\n\t" \
    "s_sub_u32 %[n], %[n], 1\n\t" \
    "buffer_load_dwordx4 v[192:195], %[l16], %[rs], %[s2] offen offset:0 sc1\n\t" \
    "s_waitcnt vmcnt(0)\n\t" \
    "s_branch .Lc10_%=\n\t" \
    ".Lme%=:\n\t"
#define ASM_POLL5_I \
    "buffer_load_dwordx4 v[232:235], %[l16], %[rs], %[s0] offen offset:0 sc1\n\t" \
    "buffer_load_dwordx4 v[236:239], %[l16], %[rs], %[s0] offen offset:1024 sc1\n\t" \
    "buffer_load_dwordx4 v[240:243], %[l16], %[rs], %[s0] offen offset:2048 sc1\n\t" \
    "buffer_load_dwordx4 v[244:247], %[l16], %[rs], %[s0] offen offset:3072 sc1\n\t" \
    "buffer_load_dwordx4 v[248:251], %[l16], %[rs], %[s1] offen offset:0 sc1\n\t"
#define ASM_POLL5_C \
    "s_movk_i32 %[n], 0x200\n\t" \
    "v_max_u32 v252, v232, v233\n\t" \
    "v_max3_u32 v252, v234, v235, v252\n\t" \
    "v_max3_u32 v252, v236, v237, v252\n\t" \
    "v_max3_u32 v252, v238, v239, v252\n\t" \
    "v_max3_u32 v252, v240, v241, v252\n\t" \
    "v_max3_u32 v252, v242, v243, v252\n\t" \
    "v_max3_u32 v252, v244, v245, v252\n\t" \
    "v_max3_u32 v252, v246, v247, v252\n\t" \
    "v_max3_u32 v252, v248, v249, v252\n\t" \
    "v_max3_u32 v252, v250, v251, v252\n\t" \
    "v_cmp_eq_u32 vcc, -1, v252\n\t" \
    "s_cbranch_vccz .Lhw%=\n\t" \
    ".Lfw%=:\n\t" \
    "s_mov_b32 %[a], 0\n\t" \
    "v_max_u32 v252, v232, v233\n\t" \
    "v_max3_u32 v252, v234, v235, v252\n\t" \
    "v_cmp_eq_u32 vcc, -1, v252\n\t" \
    "s_cbranch_vccz .Lgw0_%=\n\t" \
    "buffer_load_dwordx4 v[232:235], %[l16], %[rs], %[s0] offen offset:0 sc1\n\t" \
    "s_mov_b32 %[a], 1\n\t" \
    ".Lgw0_%=:\n\t" \
    "v_max_u32 v252, v236, v237\n\t" \
    "v_max3_u32 v252, v238, v239, v252\n\t" \
    "v_cmp_eq_u32 vcc, -1, v252\n\t" \
    "s_cbranch_vccz .Lgw1_%=\n\t" \
    "buffer_load_dwordx4 v[236:239], %[l16], %[rs], %[s0] offen offset:1024 sc1\n\t" \
    "s_mov_b32 %[a], 1\n\t" \
    ".Lgw1_%=:\n\t" \
    "v_max_u32 v252, v240, v241\n\t" \
    "v_max3_u32 v252, v242, v243, v252\n\t" \
    "v_cmp_eq_u32 vcc, -1, v252\n\t" \
    "s_cbranch_vccz .Lgw2_%=\n\t" \
    "buffer_load_dwordx4 v[240:243], %[l16], %[rs], %[s0] offen offset:2048 sc1\n\t" \
    "s_mov_b32 %[a], 1\n\t" \
    ".Lgw2_%=:\n\t" \
    "v_max_u32 v252, v244, v245\n\t" \
    "v_max3_u32 v252, v246, v247, v252\n\t" \
    "v_cmp_eq_u32 vcc, -1, v252\n\t" \
    "s_cbranch_vccz .Lgw3_%=\n\t" \
    "buffer_load_dwordx4 v[244:247], %[l16], %[rs], %[s0] offen offset:3072 sc1\n\t" \
    "s_mov_b32 %[a], 1\n\t" \
    ".Lgw3_%=:\n\t" \
    "v_max_u32 v252, v248, v249\n\t" \
    "v_max3_u32 v252, v250, v251, v252\n\t" \
    "v_cmp_eq_u32 vcc, -1, v252\n\t" \
    "s_cbranch_vccz .Lgw4_%=\n\t" \
    "buffer_load_dwordx4 v[248:251], %[l16], %[rs], %[s1] offen offset:0 sc1\n\t" \
    "s_mov_b32 %[a], 1\n\t" \
    ".Lgw4_%=:\n\t" \
    "s_cmp_eq_u32 %[a], 0\n\t" \
    "s_cbranch_scc1 .Lhw%=\n\t" \
    "s_waitcnt vmcnt(20)\n\t" \
    "v_max_u32 v252, v232, v233\n\t" \
    "v_max3_u32 v252, v234, v235, v252\n\t" \
    "v_max3_u32 v252, v236, v237, v252\n\t" \
    "v_max3_u32 v252, v238, v239, v252\n\t" \
    "v_max3_u32 v252, v240, v241, v252\n\t" \
    "v_max3_u32 v252, v242, v243, v252\n\t" \
    "v_max3_u32 v252, v244, v245, v252\n\t" \
    "v_max3_u32 v252, v246, v247, v252\n\t" \
    "v_max3_u32 v252, v248, v249, v252\n\t" \
    "v_max3_u32 v252, v250, v251, v252\n\t" \
    "v_cmp_eq_u32 vcc, -1, v252\n\t" \
    "s_cbranch_vccz .Lhw%=\n\t" \
    "s_sleep 1\n\t" \
    "v_max_u32 v252, v232, v233\n\t" \
    "v_max3_u32 v252, v234, v235, v252\n\t" \
    "v_max3_u32 v252, v236, v237, v252\n\t" \
    "v_max3_u32 v252, v238, v239, v252\n\t" \
    "v_max3_u32 v252, v240, v241, v252\n\t" \
    "v_max3_u32 v252, v242, v243, v252\n\t" \
    "v_max3_u32 v252, v244, v245, v252\n\t" \
    "v_max3_u32 v252, v246, v247, v252\n\t" \
    "v_max3_u32 v252, v248, v249, v252\n\t" \
    "v_max3_u32 v252, v250, v251, v252\n\t" \
    "v_cmp_eq_u32 vcc, -1, v252\n\t" \
    "s_cbranch_vccz .Lhw%=\n\t" \
    "s_sleep 1\n\t" \
    "v_max_u32 v252, v232, v233\n\t" \
    "v_max3_u32 v252, v234, v235, v252\n\t" \
    "v_max3_u32 v252, v236, v237, v252\n\t" \
    "v_max3_u32 v252, v238, v239, v252\n\t" \
    "v_max3_u32 v252, v240, v241, v252\n\t" \
    "v_max3_u32 v252, v242, v243, v252\n\t" \
    "v_max3_u32 v252, v244, v245, v252\n\t" \
    "v_max3_u32 v252, v246, v247, v252\n\t" \
    "v_max3_u32 v252, v248, v249, v252\n\t" \
    "v_max3_u32 v252, v250, v251, v252\n\t" \
    "v_cmp_eq_u32 vcc, -1, v252\n\t" \
    "s_cbranch_vccz .Lhw%=\n\t" \
    "s_sleep 1\n\t" \
    "v_max_u32 v252, v232, v233\n\t" \
    "v_max3_u32 v252, v234, v235, v252\n\t" \
    "v_max3_u32 v252, v236, v237, v252\n\t" \
    "v_max3_u32 v252, v238, v239, v252\n\t" \
    "v_max3_u32 v252, v240, v241, v252\n\t" \
    "v_max3_u32 v252, v242, v243, v252\n\t" \
    "v_max3_u32 v252, v244, v245, v252\n\t" \
    "v_max3_u32 v252, v246, v247, v252\n\t" \
    "v_max3_u32 v252, v248, v249, v252\n\t" \
    "v_max3_u32 v252, v250, v251, v252\n\t" \
    "v_cmp_eq_u32 vcc, -1, v252\n\t" \
    "s_cbranch_vccz .Lhw%=\n\t" \
    "s_sleep 1\n\t" \
    "v_max_u32 v252, v232, v233\n\t" \
    "v_max3_u32 v252, v234, v235, v252\n\t" \
    "v_max3_u32 v252, v236, v237, v252\n\t" \
    "v_max3_u32 v252, v238, v239, v252\n\t" \
    "v_max3_u32 v252, v240, v241, v252\n\t" \
    "v_max3_u32 v252, v242, v243, v252\n\t" \
    "v_max3_u32 v252, v244, v245, v252\n\t" \
    "v_max3_u32 v252, v246, v247, v252\n\t" \
    "v_max3_u32 v252, v248, v249, v252\n\t" \
    "v_max3_u32 v252, v250, v251, v252\n\t" \
    "v_cmp_eq_u32 vcc, -1, v252\n\t" \
    "s_cbranch_vccz .Lhw%=\n\t" \
    "s_sleep 1\n\t" \
    "v_max_u32 v252, v232, v233\n\t" \
    "v_max3_u32 v252, v234, v235, v252\n\t" \
    "v_max3_u32 v252, v236, v237, v252\n\t" \
    "v_max3_u32 v252, v238, v239, v252\n\t" \
    "v_max3_u32 v252, v240, v241, v252\n\t" \
    "v_max3_u32 v252, v242, v243, v252\n\t" \
    "v_max3_u32 v252, v244, v245, v252\n\t" \
    "v_max3_u32 v252, v246, v247, v252\n\t" \
    "v_max3_u32 v252, v248, v249, v252\n\t" \
    "v_max3_u32 v252, v250, v251, v252\n\t" \
    "v_cmp_eq_u32 vcc, -1, v252\n\t" \
    "s_cbranch_vccz .Lhw%=\n\t" \
    "s_sleep 1\n\t" \
    "s_sub_u32 %[n], %[n], 1\n\t" \
    "s_cmp_lg_u32 %[n], 0\n\t" \
    "s_cbranch_scc1 .Lfw%=\n\t" \
    ".Lhw%=:\n\t"
#define ASM_POLLS3 \
    "buffer_load_dwordx4 v[208:211], %[l16], %[rs], %[s0] offen offset:0 sc1\n\t" \
    "buffer_load_dwordx4 v[212:215], %[l16], %[rs], %[s1] offen offset:0 sc1\n\t" \
    "buffer_load_dwordx4 v[216:219], %[l16], %[rs], %[s2] offen offset:0 sc1\n\t" \
    "s_movk_i32 %[n], 0x200\n\t" \
    "s_waitcnt vmcnt(2)\n\t" \
    "v_max_u32 v240, v208, v209\n\t" \
    "v_max3_u32 v240, v210, v211, v240\n\t" \
    "s_waitcnt vmcnt(1)\n\t" \
    "v_max3_u32 v240, v212, v213, v240\n\t" \
    "v_max3_u32 v240, v214, v215, v240\n\t" \
    "s_waitcnt vmcnt(0)\n\t" \
    "v_max3_u32 v240, v216, v217, v240\n\t" \
    "v_max3_u32 v240, v218, v219, v240\n\t" \
    "v_cmp_eq_u32 vcc, -1, v240\n\t" \
    "s_cbranch_vccz .Lpd%=\n\t" \
    ".Lpl%=:\n\t" \
    "s_mov_b32 %[a], 0\n\t" \
    "v_max_u32 v240, v208, v209\n\t" \
    "v_max3_u32 v240, v210, v211, v240\n\t" \
    "v_cmp_eq_u32 vcc, -1, v240\n\t" \
    "s_cbranch_vccz .Lps0_%=\n\t" \
    "buffer_load_dwordx4 v[208:211], %[l16], %[rs], %[s0] offen offset:0 sc1\n\t" \
    "s_mov_b32 %[a], 1\n\t" \
    ".Lps0_%=:\n\t" \
    "v_max_u32 v240, v212, v213\n\t" \
    "v_max3_u32 v240, v214, v215, v240\n\t" \
    "v_cmp_eq_u32 vcc, -1, v240\n\t" \
    "s_cbranch_vccz .Lps1_%=\n\t" \
    "buffer_load_dwordx4 v[212:215], %[l16], %[rs], %[s1] offen offset:0 sc1\n\t" \
    "s_mov_b32 %[a], 1\n\t" \
    ".Lps1_%=:\n\t" \
    "v_max_u32 v240, v216, v217\n\t" \
    "v_max3_u32 v240, v218, v219, v240\n\t" \
    "v_cmp_eq_u32 vcc, -1, v240\n\t" \
    "s_cbranch_vccz .Lps2_%=\n\t" \
    "buffer_load_dwordx4 v[216:219], %[l16], %[rs], %[s2] offen offset:0 sc1\n\t" \
    "s_mov_b32 %[a], 1\n\t" \
    ".Lps2_%=:\n\t" \
    "s_cmp_eq_u32 %[a], 0\n\t" \
    "s_cbranch_scc1 .Lpd%=\n\t" \
    "s_waitcnt vmcnt(0)\n\t" \
    "s_sub_u32 %[n], %[n], 1\n\t" \
    "s_cmp_lg_u32 %[n], 0\n\t" \
    "s_cbranch_scc1 .Lpl%=\n\t" \
    ".Lpd%=:\n\t"
#define ASM_POLLS2 \
    "buffer_load_dwordx4 v[208:211], %[l16], %[rs], %[s0] offen offset:0 sc1\n\t" \
    "buffer_load_dwordx4 v[212:215], %[l16], %[rs], %[s1] offen offset:0 sc1\n\t" \
    "s_movk_i32 %[n], 0x200\n\t" \
    "s_waitcnt vmcnt(1)\n\t" \
    "v_max_u32 v240, v208, v209\n\t" \
    "v_max3_u32 v240, v210, v211, v240\n\t" \
    "s_waitcnt vmcnt(0)\n\t" \
    "v_max3_u32 v240, v212, v213, v240\n\t" \
    "v_max3_u32 v240, v214, v215, v240\n\t" \
    "v_cmp_eq_u32 vcc, -1, v240\n\t" \
    "s_cbranch_vccz .Lpd%=\n\t" \
    ".Lpl%=:\n\t" \
    "s_mov_b32 %[a], 0\n\t" \
    "v_max_u32 v240, v208, v209\n\t" \
    "v_max3_u32 v240, v210, v211, v240\n\t" \
    "v_cmp_eq_u32 vcc, -1, v240\n\t" \
    "s_cbranch_vccz .Lps0_%=\n\t" \
    "buffer_load_dwordx4 v[208:211], %[l16], %[rs], %[s0] offen offset:0 sc1\n\t" \
    "s_mov_b32 %[a], 1\n\t" \
    ".Lps0_%=:\n\t" \
    "v_max_u32 v240, v212, v213\n\t" \
    "v_max3_u32 v240, v214, v215, v240\n\t" \
    "v_cmp_eq_u32 vcc, -1, v240\n\t" \
    "s_cbranch_vccz .Lps1_%=\n\t" \
    "buffer_load_dwordx4 v[212:215], %[l16], %[rs], %[s1] offen offset:0 sc1\n\t" \
    "s_mov_b32 %[a], 1\n\t" \
    ".Lps1_%=:\n\t" \
    "s_cmp_eq_u32 %[a], 0\n\t" \
    "s_cbranch_scc1 .Lpd%=\n\t" \
    "s_waitcnt vmcnt(0)\n\t" \
    "s_sub_u32 %[n], %[n], 1\n\t" \
    "s_cmp_lg_u32 %[n], 0\n\t" \
    "s_cbranch_scc1 .Lpl%=\n\t" \
    ".Lpd%=:\n\t"
#define ASM_POLLV3 \
    "buffer_load_dwordx4 v[200:203], %[l16], %[rs], %[s0] offen offset:0 sc1\n\t" \
    "buffer_load_dwordx4 v[204:207], %[l16], %[rs], %[s1] offen offset:0 sc1\n\t" \
    "buffer_load_dwordx4 v[208:211], %[l16], %[rs], %[s2] offen offset:0 sc1\n\t" \
    "s_movk_i32 %[n], 0x200\n\t" \
    "s_waitcnt vmcnt(2)\n\t" \
    "v_max_u32 v252, v200, v201\n\t" \
    "v_max3_u32 v252, v202, v203, v252\n\t" \
    "s_waitcnt vmcnt(1)\n\t" \
    "v_max3_u32 v252, v204, v205, v252\n\t" \
    "v_max3_u32 v252, v206, v207, v252\n\t" \
    "s_waitcnt vmcnt(0)\n\t" \
    "v_max3_u32 v252, v208, v209, v252\n\t" \
    "v_max3_u32 v252, v210, v211, v252\n\t" \
    "v_cmp_eq_u32 vcc, -1, v252\n\t" \
    "s_cbranch_vccz .Lpd%=\n\t" \
    ".Lpl%=:\n\t" \
    "s_mov_b32 %[a], 0\n\t" \
    "v_max_u32 v252, v200, v201\n\t" \
    "v_max3_u32 v252, v202, v203, v252\n\t" \
    "v_cmp_eq_u32 vcc, -1, v252\n\t" \
    "s_cbranch_vccz .Lps0_%=\n\t" \
    "buffer_load_dwordx4 v[200:203], %[l16], %[rs], %[s0] offen offset:0 sc1\n\t" \
    "s_mov_b32 %[a], 1\n\t" \
    ".Lps0_%=:\n\t" \
    "v_max_u32 v252, v204, v205\n\t" \
    "v_max3_u32 v252, v206, v207, v252\n\t" \
    "v_cmp_eq_u32 vcc, -1, v252\n\t" \
    "s_cbranch_vccz .Lps1_%=\n\t" \
    "buffer_load_dwordx4 v[204:207], %[l16], %[rs], %[s1] offen offset:0 sc1\n\t" \
    "s_mov_b32 %[a], 1\n\t" \
    ".Lps1_%=:\n\t" \
    "v_max_u32 v252, v208, v209\n\t" \
    "v_max3_u32 v252, v210, v211, v252\n\t" \
    "v_cmp_eq_u32 vcc, -1, v252\n\t" \
    "s_cbranch_vccz .Lps2_%=\n\t" \
    "buffer_load_dwordx4 v[208:211], %[l16], %[rs], %[s2] offen offset:0 sc1\n\t" \
    "s_mov_b32 %[a], 1\n\t" \
    ".Lps2_%=:\n\t" \
    "s_cmp_eq_u32 %[a], 0\n\t" \
    "s_cbranch_scc1 .Lpd%=\n\t" \
    "s_waitcnt vmcnt(0)\n\t" \
    "s_sub_u32 %[n], %[n], 1\n\t" \
    "s_cmp_lg_u32 %[n], 0\n\t" \
    "s_cbranch_scc1 .Lpl%=\n\t" \
    ".Lpd%=:\n\t"
#define ASM_POLLV2 \
    "buffer_load_dwordx4 v[200:203], %[l16], %[rs], %[s0] offen offset:0 sc1\n\t" \
    "buffer_load_dwordx4 v[204:207], %[l16], %[rs], %[s1] offen offset:0 sc1\n\t" \
    "s_movk_i32 %[n], 0x200\n\t" \
    "s_waitcnt vmcnt(1)\n\t" \
    "v_max_u32 v252, v200, v201\n\t" \
    "v_max3_u32 v252, v202, v203, v252\n\t" \
    "s_waitcnt vmcnt(0)\n\t" \
    "v_max3_u32 v252, v204, v205, v252\n\t" \
    "v_max3_u32 v252, v206, v207, v252\n\t" \
    "v_cmp_eq_u32 vcc, -1, v252\n\t" \
    "s_cbranch_vccz .Lpd%=\n\t" \
    ".Lpl%=:\n\t" \
    "s_mov_b32 %[a], 0\n\t" \
    "v_max_u32 v252, v200, v201\n\t" \
    "v_max3_u32 v252, v202, v203, v252\n\t" \
    "v_cmp_eq_u32 vcc, -1, v252\n\t" \
    "s_cbranch_vccz .Lps0_%=\n\t" \
    "buffer_load_dwordx4 v[200:203], %[l16], %[rs], %[s0] offen offset:0 sc1\n\t" \
    "s_mov_b32 %[a], 1\n\t" \
    ".Lps0_%=:\n\t" \
    "v_max_u32 v252, v204, v205\n\t" \
    "v_max3_u32 v252, v206, v207, v252\n\t" \
    "v_cmp_eq_u32 vcc, -1, v252\n\t" \
    "s_cbranch_vccz .Lps1_%=\n\t" \
    "buffer_load_dwordx4 v[204:207], %[l16], %[rs], %[s1] offen offset:0 sc1\n\t" \
    "s_mov_b32 %[a], 1\n\t" \
    ".Lps1_%=:\n\t" \
    "s_cmp_eq_u32 %[a], 0\n\t" \
    "s_cbranch_scc1 .Lpd%=\n\t" \
    "s_waitcnt vmcnt(0)\n\t" \
    "s_sub_u32 %[n], %[n], 1\n\t" \
    "s_cmp_lg_u32 %[n], 0\n\t" \
    "s_cbranch_scc1 .Lpl%=\n\t" \
    ".Lpd%=:\n\t"
__device__ __forceinline__ void poll5_issue(rsrc_t rs, unsigned l16, unsigned base, unsigned base1, u32x4 (&v)[5]) {
    asm volatile(ASM_POLL5_I : "+{v[232:235]}"(v[0]), "+{v[236:239]}"(v[1]), "+{v[240:243]}"(v[2]), "+{v[244:247]}"(v[3]), "+{v[248:251]}"(v[4]) : [l16] "v"(l16), [rs] "s"(rs), [s0] "s"(base), [s1] "s"(base1) : "memory");
}
__device__ __forceinline__ void poll5(rsrc_t rs, unsigned l16, unsigned base, unsigned base1, u32x4 (&v)[5], const Params& P) {
#pragma unroll 1
    for (int spins = 1023;; spins += 1024) {
        unsigned n, a;
        asm volatile(ASM_POLL5_C : "+{v[232:235]}"(v[0]), "+{v[236:239]}"(v[1]), "+{v[240:243]}"(v[2]), "+{v[244:247]}"(v[3]), "+{v[248:251]}"(v[4]), [n] "=&s"(n), [a] "=&s"(a) : [l16] "v"(l16), [rs] "s"(rs), [s0] "s"(base), [s1] "s"(base1) : "memory", "vcc", "scc", "v252");
        if (n || spin_fail(spins, P)) break;
    }
}
using u32x8 = __attribute__((ext_vector_type(8))) unsigned;
using u32x16 = __attribute__((ext_vector_type(16))) unsigned;
using u32x32 = __attribute__((ext_vector_type(32))) unsigned;
struct WRegs { u32x32 w[6]; u32x8 t; };
#define WREGS(W) "{v[0:31]}"(W.w[0]), "{v[32:63]}"(W.w[1]), "{v[64:95]}"(W.w[2]), "{v[96:127]}"(W.w[3]), "{v[128:159]}"(W.w[4]), "{v[160:191]}"(W.w[5]), "{v[192:199]}"(W.t)
__device__ __forceinline__ u32x32 cat32(u32x8 a, u32x8 b, u32x8 c, u32x8 d) {
    const u32x16 lo = __builtin_shufflevector(a, b, 0, 1, 2, 3, 4, 5, 6, 7, 8, 9, 10, 11, 12, 13, 14, 15);
    const u32x16 hi = __builtin_shufflevector(c, d, 0, 1, 2, 3, 4, 5, 6, 7, 8, 9, 10, 11, 12, 13, 14, 15);
    return __builtin_shufflevector(lo, hi, 0, 1, 2, 3, 4, 5, 6, 7, 8, 9, 10, 11, 12, 13, 14, 15, 16, 17, 18, 19, 20, 21, 22, 23, 24, 25, 26, 27, 28, 29, 30, 31);
}
#define WOUT(W) "=&{v[0:31]}"(W.w[0]), "=&{v[32:63]}"(W.w[1]), "=&{v[64:95]}"(W.w[2]), "=&{v[96:127]}"(W.w[3]), "=&{v[128:159]}"(W.w[4]), "=&{v[160:191]}"(W.w[5]), "=&{v[192:199]}"(W.t)
#define WTMP "memory", "scc", "v200", "v201", "v202", "v203", "v204", "v205", "v206", "v207", "v208", "v209", "v210", "v211", "v212", "v213", "v214", "v215", "v216", "v217", "v218", "v219", "v220", "v221", "v222", "v223", "v224", "v225", "v226", "v227", "v228", "v229", "v230", "v231", "v232", "v233", "v234", "v235", "v236", "v237", "v238", "v239"
__device__ __forceinline__ void load_w3(WRegs& W, const bf16_t* m, int wave, unsigned c0, unsigned ls) {
    const int ln = lane_id();
    unsigned o[3], s[3], t;
#pragma unroll
    for (int k = 0; k < 3; ++k) {
        const int row = (k * 64 + ln) / 24, sg = (k * 64 + ln) % 24;
        o[k] = row * (HD * 2) + (c0 + (sg >> 3) * ls) % (HD * 2) + (sg & 7) * 16; s[k] = wave * 13824 + row * 416 + sg * 16;
    }
    const unsigned tr = (ln >> 4) * 3456 + ((ln >> 2) & 3) * 416 + (wave & 3) * 32 + (ln & 3) * 8;
    asm volatile(ASM_WLOAD3 : WOUT(W), [t] "=&s"(t) : [rs] "s"(mkrsrc(m, DM * HD * 2)), [wx] "s"(wave), [o0] "v"(o[0]), [o1] "v"(o[1]), [o2] "v"(o[2]), [s0] "v"(s[0]), [s1] "v"(s[1]), [s2] "v"(s[2]),
                 [t0] "v"(tr + wave * 13824), [t1] "v"(tr + (wave ^ 1) * 13824), [t2] "v"(tr + (wave ^ 2) * 13824), [t3] "v"(tr + (wave ^ 3) * 13824), [t4] "v"(tr + (wave & 4) * 13824),
                 [wl] "v"(L_W + wave * 10240 + ln * 16) : WTMP);
}
__device__ __forceinline__ void load_wo(WRegs& W, const bf16_t* m, int wave, int h) {
    const int ln = lane_id();
    unsigned o[5], s[5], t;
#pragma unroll
    for (int k = 0; k < 5; ++k) {
        const int row = (k * 64 + ln) / 20, sg = (k * 64 + ln) % 20;
        o[k] = (h * DH + row) * (DM * 2) + sg * 16 + (wave & 3) * 320; s[k] = wave * 13824 + row * 352 + sg * 16;
    }
    asm volatile(ASM_WLOADO : WOUT(W), [t] "=&s"(t) : [rs] "s"(mkrsrc(m, DM * HD * 2)), [o0] "v"(o[0]), [o1] "v"(o[1]), [o2] "v"(o[2]), [o3] "v"(o[3]), [o4] "v"(o[4]),
                 [s0] "v"(s[0]), [s1] "v"(s[1]), [s2] "v"(s[2]), [s3] "v"(s[3]), [s4] "v"(s[4]), [tr] "v"(wave * 13824 + (ln >> 2) * 352 + (ln & 3) * 8), [wl] "v"(L_W + wave * 10240 + ln * 16) : WTMP);
}
using s16x4 = __attribute__((ext_vector_type(4))) short;
template <int NT, int KS, int N>
__device__ __forceinline__ void load_wn(u32x4 (&f)[NT * KS], const bf16_t* m, int rows, int wave, int c0, int k0) {
    const int ln = lane_id();
    const rsrc_t rs = mkrsrc(m, rows * N * 2);
#pragma unroll
    for (int k = 0; k < KS; ++k)
#pragma unroll
        for (int n = 0; n < NT; ++n) f[n * KS + k] = __builtin_amdgcn_raw_buffer_load_b128(rs, ((n * 64 + ln) / (2 * NT)) * (N * 2) + ((n * 64 + ln) % (2 * NT)) * 16, (k0 + k) * (N * 64) + c0 * 32, 0);
    const unsigned st = wave * 10240 + ln * 16, tr = wave * 10240 + ((ln >> 4) * 8 + ((ln >> 2) & 3)) * (NT * 32) + (ln & 3) * 8;
#pragma unroll
    for (int k = 0; k < KS; ++k) {
#pragma unroll
        for (int n = 0; n < NT; ++n) lds_st<u32x4>(st + n * 1024, f[n * KS + k]);
        asm volatile("" ::: "memory");
#pragma unroll
        for (int n = 0; n < NT; ++n) {
            const u32x2 a = __builtin_bit_cast(u32x2, __builtin_amdgcn_ds_read_tr16_b64_v4i16((LAS s16x4*)(tr + n * 32)));
            const u32x2 b = __builtin_bit_cast(u32x2, __builtin_amdgcn_ds_read_tr16_b64_v4i16((LAS s16x4*)(tr + n * 32 + NT * 128)));
            f[n * KS + k] = (u32x4){a[0], a[1], b[0], b[1]};
        }
        asm volatile("" ::: "memory");
    }
}
struct C1Regs { u32x8 lw0, lw1, z1; u32x4 z0; };
#define XG_OUT "=&{v[200:207]}"(C.lw0), "=&{v[224:227]}"(C.z0), "=&{v[228:235]}"(C.lw1), "=&{v[244:251]}"(C.z1)
#define XG_IN WREGS(W), [xa] "v"(xa), [wl] "v"(wl), [qs] "s"(qs)
#define XG_CLOB "memory", "scc", "v208", "v209", "v210", "v211", "v212", "v213", "v214", "v215", "v216", "v217", "v218", "v219", "v220", "v221", "v222", "v223", "v236", "v237", "v238", "v239", "v240", "v241", "v242", "v243", "v252"
__device__ __forceinline__ void xn_wait(const Params& P, rsrc_t rr, int seg, int cpy) {
    for (int spins = 0;; ++spins) {
        const unsigned f = __builtin_amdgcn_raw_buffer_load_b32(rr, (lane_id() & 15) * 128, RG_FX + (cpy & 3) * 2048, 16);
        if (__all(f > (unsigned)seg)) break;
        __builtin_amdgcn_s_sleep(4);
        if (spin_fail(spins, P)) break;
    }
    asm volatile("" ::: "memory");
}
__device__ __forceinline__ unsigned c1_dst(int wave, bool ktype) {
    const int ln = lane_id(), l15 = ln & 15, hf = (wave >> 1) & 1;
    const int rb16 = ((ln >> 2) & 4) * 16 + (ln >> 5) * 32;
    return (ktype ? L_KM + (l15 >> 2) * 128 + hf * 8 + (l15 & 3) * 2
                  : L_Q + (l15 >> 3) * 256 + ((l15 >> 1) & 1) * 128 + ((l15 >> 2) & 1) * 8 + hf * 4 + (l15 & 1) * 2) + (wave & 1) * 1536 + rb16;
}
template <bool VR>
__device__ __forceinline__ void mem_stage(const Params& P, rsrc_t rr, unsigned sbase, int w, unsigned abase, u32x4 (&v)[3]) {
#define PM_IN [l16] "v"(abase), [rs] "s"(rr), [s0] "s"(sbase), [s1] "s"(sbase + 4096)
#pragma unroll 1
    for (int spins = 1023;; spins += 1024) {
        unsigned n, a;
        if (w < 2) {
            if (VR) asm volatile(ASM_POLLV3 : "=&{v[200:203]}"(v[0]), "=&{v[204:207]}"(v[1]), "=&{v[208:211]}"(v[2]), [n] "=&s"(n), [a] "=&s"(a) : PM_IN, [s2] "s"(sbase + BLK) : "memory", "vcc", "scc", "v252");
            else asm volatile(ASM_POLLS3 : "=&{v[208:211]}"(v[0]), "=&{v[212:215]}"(v[1]), "=&{v[216:219]}"(v[2]), [n] "=&s"(n), [a] "=&s"(a) : PM_IN, [s2] "s"(sbase + BLK) : "memory", "vcc", "scc", "v240");
            lds_st<u32x4>(abase + 8192, v[2]);
        } else {
            if (VR) asm volatile(ASM_POLLV2 : "=&{v[200:203]}"(v[0]), "=&{v[204:207]}"(v[1]), [n] "=&s"(n), [a] "=&s"(a) : PM_IN : "memory", "vcc", "scc", "v252");
            else asm volatile(ASM_POLLS2 : "=&{v[208:211]}"(v[0]), "=&{v[212:215]}"(v[1]), [n] "=&s"(n), [a] "=&s"(a) : PM_IN : "memory", "vcc", "scc", "v240");
        }
        lds_st<u32x4>(abase, v[0]);
        lds_st<u32x4>(abase + 4096, v[1]);
        if (__builtin_amdgcn_readfirstlane(n) || spin_fail(spins, P)) break;
    }
}
template <bool POLL>
__device__ __forceinline__ void crit1(const Params& P, const WRegs& W, C1Regs& C, rsrc_t rr, unsigned sbase, int wave, unsigned wl, unsigned dsto
) {
    unsigned abase;
    asm volatile("v_and_b32 %0, 0x3ff, %1\n\tv_or_b32 %0, %2, %0" : "=&v"(abase) : "v"(wl), "s"(wave * 1024));
    ASM_ST_DECL;
#define C1_OUT "+{v[224:227]}"(C.z0), "+{v[244:251]}"(C.z1), "+{v[200:207]}"(C.lw0), "+{v[228:235]}"(C.lw1)
#define C1_IN WREGS(W), [ab] "v"(abase), [wl] "v"(wl), [dst] "v"(dsto)
    if constexpr (POLL) {
        asm volatile("" :: "v"(dsto));
        SB();
        ST(1);
        u32x4 v[3];
        mem_stage<false>(P, rr, sbase, wave, abase, v);
        ST(2);
        asm volatile(ASM_CRIT1 : C1_OUT, "+{v[208:211]}"(v[0]) ASM_ST_OUT : C1_IN : "memory", "v212", "v213", "v214", "v215", "v216", "v217", "v218", "v219", "v220", "v221", "v222", "v223", "v236", "v237", "v238", "v239", "v240", "v241");
    } else {
        asm volatile(ASM_CRIT1_N : C1_OUT ASM_ST_OUT : C1_IN : "memory", "v208", "v209", "v210", "v211", "v212", "v213", "v214", "v215", "v216", "v217", "v218", "v219", "v220", "v221", "v222", "v223", "v236", "v237", "v238", "v239", "v240", "v241");
    }
    SB();
    ASM_ST_SAVE(3);
}
#define STARG

#define SRG 5
__device__ __forceinline__ void s_role(const Params& P, int wave, int L, int par, int h, int cpy) {
    const rsrc_t rr = mkrsrc(P.ws + par * 8192, RG_L);
    const char* xn = P.ws + PG0 + PG_XN + L * PG_LX;
    auto xdma = [&](int seg, int hb) {
        const unsigned l16 = lane_id() * 16;
        const int mt = (wave >> 1) & 1;
        const char* g = xn + pgb(seg, par);
        if (hb == 0) {
            dma4(g + blin(mt * 20480 + (wave & 1) * 4096), l16, L_XA + mt * 16384 + (wave & 1) * 4096);
            dmaq<0>(g + blin(mt * 20480 + 16384) + (wave & 1) * 1024, l16, L_XB + mt * 4096 + (wave & 1) * 1024);
        } else {
            dma4(g + blin(mt * 20480 + 8192), l16, L_XA + mt * 16384 + 8192); dma4(g + blin(mt * 20480 + 12288), l16, L_XA + mt * 16384 + 12288);
            dmaq<2>(g + blin(mt * 20480 + 16384), l16, L_XB + mt * 4096); dmaq<3>(g + blin(mt * 20480 + 16384), l16, L_XB + mt * 4096);
        }
    };
    const float SCL = 0.07216878364870322f * 1.4426950408889634f;
    auto run = [&](auto Cc) {
    constexpr int CLS = decltype(Cc)::value;
    WRegs wf;
    load_w3(wf, P.w[CLS == 3], wave, h * 384, 128);
    if (CLS == 3) {
        if (L) xn_wait(P, rr, 0, cpy);
        xdma(0, 0);
        if (wave == 4 || wave == 7) xdma(0, 1);
        wait_vm<0>();
    }
    bar();
    if (CLS == 3 && wave == 7 && L) xn_wait(P, rr, 1, cpy);
    const unsigned qs = (CLS == 3 ? L_KX : L_QX) + (wave & 1) * 3072 + ((wave >> 1) & 1) * 8;
    unsigned xa, wl, dsto;
    if (CLS == 3) { xa = L_XA + wave * 2048 + lane_id() * 16; wl = L_W + wave * 10240 + lane_id() * 16; dsto = c1_dst(wave, true); }
    STDECL;
    LDECL;
    for (int t = 0; t < NSEG; ++t) {
        const unsigned slot = t & (NSLOT - 1);
        C1Regs C;
        {
            const WRegs& W = wf;
            ASM_ST_DECL;
            if constexpr (CLS == 3) {
                const int mt = (wave >> 1) & 1;
                const char* g = xn + pgb(t + 1 < NSEG ? t + 1 : t, par);
                asm volatile(ASM_XG_S_HI : XG_OUT ASM_ST_OUT : XG_IN, [slp] "s"(wave == 4 ? t : 0), [g0] "s"(g + blin(mt * 20480 + (wave & 1) * 4096)), [d0] "s"(L_XA + mt * 16384 + (wave & 1) * 4096),
                             [g1] "s"(g + blin(mt * 20480 + 16384) + (wave & 1) * 1024), [d1] "s"(L_XB + mt * 4096 + (wave & 1) * 1024) : XG_CLOB);
                wait_vm<0>();
                LB(t);
            } else {
                const unsigned l16 = lane_id() * 16;
                xa = L_XA + wave * 2048 + l16; wl = L_W + wave * 10240 + l16;
                asm volatile("s_cmp_lt_u32 %[t], 2\n\ts_cbranch_scc1 .Lq3%=\n\t" ASM_XG_S_LO2 "s_branch .Lqe%=\n\t.Lq3%=:\n\t" ASM_XG_S_LO ".Lqe%=:" : XG_OUT ASM_ST_OUT : XG_IN, [t] "s"(t) : XG_CLOB);
                if ((CLS == 4 || CLS == 2) && t >= HT0) {
                    const int ln = lane_id(), u = h / 6, f = ((h % 6) / 3) * 12 + (CLS == 2) * 3 + h % 3;
                    for (int spins = 0;; ++spins) {
                        const unsigned fl = __builtin_amdgcn_raw_buffer_load_b32(rr, (ln & 3) * 128, RG_FH + BLK + u * 512, 16);
                        if (__all(fl > (unsigned)t)) break;
                        __builtin_amdgcn_s_sleep(2);
                        if (spin_fail(spins, P)) break;
                    }
                    asm volatile("" ::: "memory");
                    const char* g = P.ws + PG0 + pgb(t, par) + PG_HV + (16 + u) * 3 * BLK;
                    dmaq<0>(g + (f >> 3) * BLK + (f & 7) * 1024, ln * 16, L_QX + (CLS == 2) * 3072);
                    dmaq<0>(g + ((f + 6) >> 3) * BLK + ((f + 6) & 7) * 1024, ln * 16, L_QX + (CLS == 2) * 3072 + 6144);
                }
                dsto = c1_dst(wave, false);
            }
            ASM_ST_SAVE(7);
            ST(6);
        }
        ST(0);
        crit1<CLS != 3>(P, wf, C, rr, RG_XM + slot * XM_SLOT + cpy * XM_COPY, wave, wl, dsto STARG);
        if constexpr (CLS == 0) {
            int ln = lane_id();
            const int sidx = ((ln & 15) < 8) ? 0x4444 : 0xEEEE;
            const unsigned l16 = ln * 16;
            bf16x16 F0, F1, F2, F3;
            F0 = lds_frag(L_KX + l16, 1024); F1 = lds_frag(L_KX + 6144 + l16, 1024); F2 = lds_frag(L_KX + 2048 + l16, 1024);
            f32x4 sc[3];
#pragma unroll
            for (int j = 0; j < 3; ++j) sc[j] = (f32x4){0.f, 0.f, 0.f, 0.f};
            unsigned kmo = L_KM + (ln >> 4) * 128 + (ln & 7) * 16;
            asm volatile("s_waitcnt lgkmcnt(0)" : "+v"(kmo), "+{v[244:247]}"(sc[0]), "+{v[252:255]}"(sc[1]), "+{v[240:243]}"(sc[2]),
                         "+v"(F0), "+v"(F1), "+v"(F2) :: "memory");
            SB();
            __builtin_amdgcn_s_barrier();
            asm volatile("" ::: "memory");
            __builtin_amdgcn_s_setprio(3);
            bf16x8 qa0 = lds_ld<bf16x8>(L_Q + l16);
            bf16x8 qa1 = lds_ld<bf16x8>(L_Q + 1024 + l16);
            F3 = lds_frag(kmo, 512);
            SB();
            SMF(qa0, F0, sc[0]);
            SB();
            F0 = lds_frag(kmo + 1024, 512);
            SMF(qa0, F1, sc[1]);
            SB();
            F1 = lds_frag(L_KX + 6144 + 2048 + l16, 1024);
            SMF(qa1, F2, sc[0]);
            SB();
            F2 = lds_frag(L_KX + 4096 + l16, 1024);
            SMF(qa0, F3, sc[2]);
            SB();
            qa0 = lds_ld<bf16x8>(L_Q + 2048 + l16);
            F3 = lds_frag(L_KX + 6144 + 4096 + l16, 1024);
            SMF(qa1, F0, sc[2]);
            SB();
            F0 = lds_frag(kmo + 2048, 512);
            SMF(qa1, F1, sc[1]);
            SB();
            ln = lane_id();
            const int l15 = ln & 15;
            const int rb16 = ((ln >> 2) & 4) * 16 + (ln >> 5) * 32;
            unsigned pb = L_Q + rb16 + (l15 & 7) * 2;
            unsigned pbx = L_Q + 128 + (l15 >> 3) * 256 + ((l15 >> 1) & 1) * 128 + rb16 + ((l15 >> 2) & 1) * 8 + (l15 & 1) * 2;
            unsigned poff = (l16 + ((ln < 8) ? h * 128 : BLK - 128 + (h >> 4) * BLK + (h & 15) * 512)) | ((ln >= 40) ? 0x40000000u : 0u);
            asm volatile("" : "+v"(pb), "+v"(pbx), "+v"(poff));
            SB();
            SMF(qa0, F2, sc[0]);
            SB();
            SMF(qa0, F3, sc[1]);
            SB();
            SMF(qa0, F0, sc[2]);
            SB();
            f32x4 mz;
            asm volatile(
                "s_nop 5\n\t"
                "v_permlane32_swap_b32 v244, v246\n\t"
                "v_permlane32_swap_b32 v245, v247\n\t"
                "v_permlane32_swap_b32 v252, v254\n\t"
                "v_permlane32_swap_b32 v253, v255\n\t"
                "v_permlane32_swap_b32 v240, v242\n\t"
                "v_permlane32_swap_b32 v241, v243\n\t"
                "v_pk_add_f32 v[244:245], v[244:245], v[246:247]\n\t"
                "v_pk_add_f32 v[246:247], v[252:253], v[254:255]\n\t"
                "v_pk_add_f32 v[240:241], v[240:241], v[242:243]\n\t"
                "s_nop 0\n\t"
                "v_max3_f32 v248, v244, v246, v240\n\t"
                "v_max3_f32 v249, v245, v247, v241\n\t"
                "s_nop 0\n\t"
                "v_max_f32_dpp v248, v248, v248 quad_perm:[1,0,3,2] row_mask:0xf bank_mask:0xf\n\t"
                "v_max_f32_dpp v249, v249, v249 quad_perm:[1,0,3,2] row_mask:0xf bank_mask:0xf\n\t"
                "s_nop 0\n\t"
                "v_max_f32_dpp v248, v248, v248 quad_perm:[2,3,0,1] row_mask:0xf bank_mask:0xf\n\t"
                "v_max_f32_dpp v249, v249, v249 quad_perm:[2,3,0,1] row_mask:0xf bank_mask:0xf\n\t"
                "s_nop 0\n\t"
                "v_max_f32_dpp v248, v248, v248 row_half_mirror row_mask:0xf bank_mask:0xf\n\t"
                "v_max_f32_dpp v249, v249, v249 row_half_mirror row_mask:0xf bank_mask:0xf\n\t"
                "s_nop 0\n\t"
                "v_max_f32_dpp v248, v248, v248 row_mirror row_mask:0xf bank_mask:0xf\n\t"
                "v_max_f32_dpp v249, v249, v249 row_mirror row_mask:0xf bank_mask:0xf\n\t"
                "v_mul_f32 v248, %[scl], v248\n\t"
                "v_mul_f32 v249, %[scl], v249\n\t"
                "v_fma_f32 v244, v244, %[scl], -v248\n\t"
                "v_fma_f32 v245, v245, %[scl], -v249\n\t"
                "v_fma_f32 v246, v246, %[scl], -v248\n\t"
                "v_fma_f32 v247, v247, %[scl], -v249\n\t"
                "v_exp_f32 v244, v244\n\t"
                "v_exp_f32 v245, v245\n\t"
                "v_fma_f32 v240, v240, %[scl], -v248\n\t"
                "v_fma_f32 v241, v241, %[scl], -v249\n\t"
                "v_exp_f32 v246, v246\n\t"
                "v_exp_f32 v247, v247\n\t"
                "v_exp_f32 v240, v240\n\t"
                "v_exp_f32 v241, v241\n\t"
                "v_add_f32 v250, v244, v246\n\t"
                "v_add_f32 v251, v245, v247\n\t"
                "v_fma_f32 v250, v240, 0.5, v250\n\t"
                "v_fma_f32 v251, v241, 0.5, v251\n\t"
                "s_nop 0\n\t"
                "v_add_f32_dpp v250, v250, v250 quad_perm:[1,0,3,2] row_mask:0xf bank_mask:0xf\n\t"
                "v_add_f32_dpp v251, v251, v251 quad_perm:[1,0,3,2] row_mask:0xf bank_mask:0xf\n\t"
                "s_nop 0\n\t"
                "v_add_f32_dpp v250, v250, v250 quad_perm:[2,3,0,1] row_mask:0xf bank_mask:0xf\n\t"
                "v_add_f32_dpp v251, v251, v251 quad_perm:[2,3,0,1] row_mask:0xf bank_mask:0xf\n\t"
                "s_nop 0\n\t"
                "v_add_f32_dpp v250, v250, v250 row_half_mirror row_mask:0xf bank_mask:0xf\n\t"
                "v_add_f32_dpp v251, v251, v251 row_half_mirror row_mask:0xf bank_mask:0xf\n\t"
                "s_nop 0\n\t"
                "v_add_f32_dpp v250, v250, v250 row_mirror row_mask:0xf bank_mask:0xf\n\t"
                "v_add_f32_dpp v251, v251, v251 row_mirror row_mask:0xf bank_mask:0xf\n\t"
                "v_rcp_f32 v250, v250\n\t"
                "v_rcp_f32 v251, v251\n\t"
                "s_nop 0\n\t"
                "v_pk_mul_f32 v[244:245], v[244:245], v[250:251]\n\t"
                "v_pk_mul_f32 v[246:247], v[246:247], v[250:251]\n\t"
                "v_pk_mul_f32 v[240:241], v[240:241], v[250:251]\n\t"
                "v_cvt_pk_bf16_f32 v244, |v244|, |v245|\n\t"
                "v_cvt_pk_bf16_f32 v246, |v246|, |v247|\n\t"
                "v_cvt_pk_bf16_f32 v240, |v240|, |v241|\n\t"
                "ds_write_b16 %[pbx], v244\n\t"
                "ds_write_b16_d16_hi %[pbx], v244 offset:16\n\t"
                "ds_write_b16 %[pbx], v246 offset:4\n\t"
                "ds_write_b16_d16_hi %[pbx], v246 offset:20\n\t"
                "ds_write_b16 %[pb], v240\n\t"
                "ds_write_b16_d16_hi %[pb], v240 offset:16\n\t"
                EWAKE
                : "+{v[240:243]}"(sc[2]), "+{v[244:247]}"(sc[0]), "=&{v[248:251]}"(mz), "+{v[252:255]}"(sc[1])
                : [pb] "v"(pb), [pbx] "v"(pbx), [scl] "s"(SCL) : "memory");
            asm volatile("s_waitcnt lgkmcnt(0)" ::: "memory");
            const u32x4 v = lds_ld<u32x4>(L_Q + l16);
#pragma unroll
            for (int c = 0; c < (int)NCP; ++c) __builtin_amdgcn_raw_buffer_store_b128(v, rr, poff, RG_XP + slot * XP_SLOT + c * XP_COPY, 16);
            asm volatile(LWAKE "s_nop 1" :: "v"(v) : "memory");
            __builtin_amdgcn_s_setprio(0);
            ST(5);
        } else if constexpr (CLS == 1 || CLS == 4) {
            const int ln = lane_id(), l15 = ln & 15, l4 = ln >> 4;
            constexpr int mt = (CLS == 4);
            const unsigned qa = L_QX + mt * 6144 + ln * 16, kmo = L_KM + l4 * 128 + (ln & 7) * 16;
            const unsigned xo = mt ? (h & 3) * 1024 : 5120 + ((h >> 1) & 1) * 1024 + (h & 1) * 256;
            const unsigned pm = l4 < 2 ? (mt ? 4096 : 7168) + (h & 3) * 256 - xo + l15 * 16 + l4 * 8 : OOB;
            const unsigned so = RG_PX + slot * PX_SLOT + (h >> 2) * BLK + xo;
            f32x4 sd;
#pragma unroll
            for (int r = 0; r < 4; ++r) sd[r] = (l4 * 4 + r > l15) ? -3.0e38f : 0.f;
            ASM_ST_DECL;
#define P4_ARGS : "+{v[240:243]}"(sd) ASM_ST_OUT : WREGS(wf), [qa] "v"(qa), [km] "v"(kmo), [pm] "v"(pm), [rr] "s"(rr), [so] "s"(so), [scl] "s"(SCL) : "memory", "v200", "v201", "v202", "v203", "v204", "v205", "v206", "v207", "v208", "v209", "v210", "v211", "v212", "v213", "v214", "v215", "v216", "v217", "v218", "v219", "v220", "v221", "v222", "v223", "v224", "v225", "v226", "v227", "v228", "v229", "v230", "v231", "v232", "v233", "v234", "v235", "v236", "v237", "v238", "v239", "v244", "v245", "v246", "v247", "v248", "v249", "v250", "v251"
            if constexpr (mt) asm volatile(ASM_P4_1 P4_ARGS); else asm volatile(ASM_P4_0 P4_ARGS);
            ASM_ST_SAVE(4);
            ST(5);
            if constexpr (mt) {
                const int ln = lane_id();
                const u32x4 s4 = sent4();
                const unsigned poff = (ln * 16 + ((ln < 8) ? h * 128 : BLK - 128 + (h >> 4) * BLK + (h & 15) * 512)) | ((ln >= 40 || t < 1) ? 0x40000000u : 0u);
#pragma unroll
                for (int c = 0; c < (int)NCP; ++c) st128(s4, rr, poff, RG_XP + ((t - 1) & (NSLOT - 1)) * XP_SLOT + c * XP_COPY);
                const unsigned pbase = RG_PX + ((t - 2) & (NSLOT - 1)) * PX_SLOT + (h >> 2) * BLK;
                const unsigned o2 = ((ln & 16) ? 5120 + ((h >> 1) & 1) * 1024 + (h & 1) * 256 + (ln >> 5) * 512 : 4096 + (h & 3) * 256 + (ln >> 5) * 3072) + (ln & 15) * 16;
                if (t >= 2) { st128(s4, rr, ln * 16, pbase + (h & 3) * 1024); st128(s4, rr, o2, pbase); }
            } else if (L && t + 2 < NSEG) xn_wait(P, rr, t + 2, cpy);
        } else {
            bar();
            ST(4);
            if (CLS == 3) LA();
            {
                if (CLS == 3 && (wave == 4 || wave == 7)) {
                    if (t + 1 < NSEG) xdma(t + 1, 1);
                }
                ST(5);
            }
        }
        STFLUSH(t);
    }
    LF();
    };
    if (wave == 0) run(std::integral_constant<int, 0>{});
    else if (wave == 1) run(std::integral_constant<int, 1>{});
    else if (wave == 2) run(std::integral_constant<int, 4>{});
    else if (wave == 3) run(std::integral_constant<int, 2>{});
    else run(std::integral_constant<int, 3>{});
}

__device__ __forceinline__ void h_role(const Params& P, int wave, int L, int par, int i, int cpy) {
    const rsrc_t rr = mkrsrc(P.ws + par * 8192, RG_L), rp = mkrsrc(P.ws + PG0, PG_L);
    const char* xn = P.ws + PG0 + PG_XN + L * PG_LX;
    auto run = [&](auto Cc) {
    constexpr bool HI = decltype(Cc)::value;
    const int mt = (wave >> 1) & 1;
    auto xdma = [&](int seg, int hb) {
        const unsigned l16 = lane_id() * 16;
        const char* g = xn + pgb(seg, par);
        dma4(g + blin(mt * 20480 + hb * 8192 + (wave & 1) * 4096), l16, L_XA + mt * 16384 + hb * 8192 + (wave & 1) * 4096);
        dmaq<0>(g + blin(mt * 20480 + 16384) + hb * 2048 + (wave & 1) * 1024, l16, L_XB + mt * 4096 + hb * 2048 + (wave & 1) * 1024);
    };
    WRegs wf;
    load_w3(wf, i < 16 ? P.w[2] : P.w[0], wave, (i < 16 ? i * 2 + HI : (i - 16) * 6 + HI * 3) * 384, i < 16 ? 128 : 384);
    const unsigned qs = (HI ? L_KX : L_QX) + (wave & 1) * 3072 + mt * 8;
    const unsigned xa = L_XA + wave * 2048 + lane_id() * 16, wl = L_W + wave * 10240 + lane_id() * 16;
    const unsigned fp = RG_FH + (i >> 4) * BLK + ((i & 15) * 4 + (wave & 3)) * 128;
    int frc;
    asm volatile("s_xor_b32 %0, %1, 1" : "=s"(frc) : "s"(L) : "scc");
    unsigned go = 0, all = 0;
    STDECL;
    for (int t = HT0; t <= NSEG; ++t) {
        ST(0);
        if constexpr (HI) {
            const unsigned l16 = lane_id() * 16;
            if (t > HT0) {
                u32x4 v[6];
                const int w4 = wave - 4;
#pragma unroll
                for (int k = 0; k < 6; ++k) v[k] = lds_ld<u32x4>(L_QX + ((k >> 1) * 8 + w4 * 2 + (k & 1)) * 1024 + l16);
                const unsigned so = pgb(t - 1, par) + PG_HV + i * 3 * BLK + w4 * 2048;
#pragma unroll
                for (int k = 0; k < 6; ++k) st128(v[k], rp, l16 + (k & 1) * 1024, so + (k >> 1) * BLK);
                if (go) xdma(t, 1);
                else {
                    wait_vm<0>();
                    if (l16 < 512) __builtin_amdgcn_raw_buffer_store_b32(t, rr, l16 >> 2, fp, 16);
                }
            }
            ST(1);
            if (t == NSEG) break;
            if (!go) { if (L) xn_wait(P, rr, t, cpy); xdma(t, 0); xdma(t, 1); wait_vm<5>(); }
            if (!all) bar();
        } else {
            if (t == NSEG) break;
            if (!all) bar();
        }
        ST(2);
        C1Regs C;
        const WRegs& W = wf;
        ASM_ST_DECL;
        if constexpr (HI) {
            const char* g = xn + pgb(t + 1 < NSEG ? t + 1 : t, par);
            unsigned fv = (lane_id() & 15) * 128;
            asm volatile(ASM_XG_H_HI : XG_OUT, [go] "+s"(go), [fv] "+v"(fv) ASM_ST_OUT : XG_IN, [rr] "s"(rr), [fs] "s"(RG_FX + (cpy & 3) * 2048), [fp] "s"(fp), [tv] "s"(t), [t1] "s"(t + 1),
                         [frc] "s"(frc), [g0] "s"(g + blin(mt * 20480 + (wave & 1) * 4096)), [d0] "s"(L_XA + mt * 16384 + (wave & 1) * 4096),
                         [g1] "s"(g + blin(mt * 20480 + 16384) + (wave & 1) * 1024), [d1] "s"(L_XB + mt * 4096 + (wave & 1) * 1024) : XG_CLOB, "vcc");
            go = __builtin_amdgcn_readfirstlane(go);
            lds_st<unsigned>(L_MEM + (wave & 3) * 4, go);
            wait_vm<0>();
        } else asm volatile(ASM_XG_S_LO : XG_OUT ASM_ST_OUT : XG_IN : XG_CLOB);
        ASM_ST_SAVE(3);
        ST(4);
        bar();
        const u32x4 gv = lds_ld<u32x4>(L_MEM);
        all = __builtin_amdgcn_readfirstlane(gv[0] & gv[1] & gv[2] & gv[3]);
        ST(5);
        STFLUSH(t);
    }
    };
    if (wave < 4) run(std::false_type{}); else run(std::true_type{});
}

__device__ __forceinline__ void v_role(const Params& P, int wave, int L, int par, int h, int cpy) {
    const rsrc_t rr = mkrsrc(P.ws + par * 8192, RG_L);
    WRegs wf;
    STDECL;
    if (wave < 4) {
        load_w3(wf, P.w[2], wave, h * 384, 128);
        const unsigned wl = L_W + wave * 10240 + lane_id() * 16, dsto = c1_dst(wave, false);
        auto vfetch = [&](int seg, int tries) {
            const int ln = lane_id();
            for (int spins = 0;; ++spins) {
                if (spins == tries) return false;
                const unsigned f = __builtin_amdgcn_raw_buffer_load_b32(rr, (ln & 3) * 128, RG_FH + (h >> 1) * 512, 16);
                if (__all(f > (unsigned)seg)) break;
                __builtin_amdgcn_s_sleep(4);
                if (spin_fail(spins, P)) break;
            }
            asm volatile("" ::: "memory");
            const char* g = P.ws + PG0 + pgb(seg, par) + PG_HV + (h >> 1) * 3 * BLK;
#pragma unroll
            for (int k = 0; k < 3; ++k) {
                const int f = (h & 1) * 12 + wave * 3 + k;
                dmaq<0>(g + (f >> 3) * BLK + (f & 7) * 1024, ln * 16, L_QX + (seg & 1) * 12288 + (wave * 3 + k) * 1024);
            }
            return true;
        };
#pragma unroll 1
        for (int t = 0; t < HT0; ++t) {
            if (L) xn_wait(P, rr, t, cpy);
            const unsigned l16 = lane_id() * 16;
            const char* g = P.ws + PG0 + PG_XN + L * PG_LX + pgb(t, par);
            dma4(g + blin(wave * 4096), l16, L_XA + wave * 4096); dma4(g + blin(20480 + wave * 4096), l16, L_XA + 16384 + wave * 4096);
            dmaq<0>(g + blin(16384) + wave * 1024, l16, L_XB + wave * 1024); dmaq<0>(g + blin(20480 + 16384) + wave * 1024, l16, L_XB + 4096 + wave * 1024);
            wait_vm<0>();
            bar();
            C1Regs C;
            const WRegs& W = wf;
            const unsigned xa = L_XA + wave * 2048 + l16, qs = L_QX + (wave & 1) * 3072 + ((wave >> 1) & 1) * 8 + t * 12288;
            asm volatile(ASM_XG_V : XG_OUT : XG_IN : XG_CLOB);
            if (t == 0) bar();
        }
        bool have = true;
        for (int t = 0; t < NSEG; ++t) {
            if (!have && t + 1 < NSEG) { vfetch(t + 1, -1); wait_vm<0>(); }
            ST(7);
            bar();
            have = t + 2 < NSEG && vfetch(t + 2, VTRY);
            C1Regs C;
            asm volatile("ds_read_b128 v[200:203], %4\n\tds_read_b128 v[204:207], %4 offset:1024\n\tds_read_b128 v[228:231], %4 offset:2048\n\tds_read_b128 v[232:235], %4 offset:3072\n\t"
                         "v_mov_b64 v[224:225], 0\n\tv_mov_b64 v[226:227], 0\n\tv_mov_b64 v[244:245], 0\n\tv_mov_b64 v[246:247], 0\n\tv_mov_b64 v[248:249], 0\n\tv_mov_b64 v[250:251], 0\n\ts_waitcnt lgkmcnt(0)"
                         : XG_OUT : "v"(wl) : "memory");
            ST(6);
            __builtin_amdgcn_s_sleep(VSL);
            crit1<true>(P, wf, C, rr, RG_XM + (t & (NSLOT - 1)) * XM_SLOT + cpy * XM_COPY, wave, wl, dsto STARG);
            bar();
            ST(4);
            STC(5);
            STFLUSH(t);
        }
    } else {
        const int w4 = wave - 4;
        const rsrc_t rd = mkrsrc(P.ws + PG0, PG_L);
        load_wo(wf, P.w[3], wave, h);
        bar();
        bar();
        for (int t = -1; t < NSEG; ++t) {
            const unsigned slot = t & (NSLOT - 1);
            bar();
            ST(7);
            if (t + 1 < NSEG) {
                const unsigned l16 = lane_id() * 16;
                const unsigned wl = L_W + wave * 10240 + l16, va = L_QX + ((t + 1) & 1) * 12288 + l16;
                unsigned so = (((t + 1) & 7) << 22) + pgb(64 + (w4 >> 1), par) + ((w4 & 1) * 40 + (h >> 3)) * BLK + (h & 7) * 1024;
                asm volatile(ASM_VOX : [so] "+s"(so) : WREGS(wf), [l16] "v"(l16), [wl] "v"(wl), [va] "v"(va), [rd] "s"(rd) : "memory", "scc", "v200", "v201", "v202", "v203", "v204", "v205", "v206", "v207", "v208", "v209", "v210", "v211", "v212", "v213", "v214", "v215", "v216", "v217", "v218", "v219", "v220", "v221", "v222", "v223", "v224", "v225", "v226", "v227", "v228", "v229", "v230", "v231", "v232", "v233", "v234", "v235", "v236", "v237", "v238", "v239", "v240", "v241", "v242", "v243", "v244", "v245", "v246", "v247", "v248", "v249", "v250", "v251");
            }
            if (t < 0) {
                wait_vm<0>();
                const unsigned l4 = lane_id() * 4;
                if (l4 < 128) __builtin_amdgcn_raw_buffer_store_b32(1, rr, l4, RG_FV + (w4 >> 1) * BLK + ((w4 & 1) * 32 + h) * 128, 16);
                continue;
            }
            ST(0);
            unsigned wl = L_W + wave * 10240 + lane_id() * 16;
            bf16x16 lw = lds_frag(wl, 1024);
            u32x32 cz;
            asm volatile("v_mov_b64 v[220:221], 0\n\tv_mov_b64 v[222:223], 0\n\tv_mov_b64 v[224:225], 0\n\tv_mov_b64 v[226:227], 0\n\tv_mov_b64 v[228:229], 0\n\tv_mov_b64 v[230:231], 0\n\tv_mov_b64 v[232:233], 0\n\tv_mov_b64 v[234:235], 0\n\tv_mov_b64 v[236:237], 0\n\tv_mov_b64 v[238:239], 0\n\tv_mov_b64 v[240:241], 0\n\tv_mov_b64 v[242:243], 0\n\tv_mov_b64 v[244:245], 0\n\tv_mov_b64 v[246:247], 0\n\tv_mov_b64 v[248:249], 0\n\tv_mov_b64 v[250:251], 0" : "={v[220:251]}"(cz));
            unsigned vo = ((wl >> 8) & 1) * 2 * BLK + ((wl >> 9) & 1) * 1024 + (wl & 255);
            asm volatile("" : "+{v[212:219]}"(lw), "+{v[220:251]}"(cz), "+v"(wl), "+v"(vo));
            wait_vm<0>();
            unsigned l16 = wl & 1023u;
            if (l16 < 512 && t + 1 < NSEG) __builtin_amdgcn_raw_buffer_store_b32(t + 2, rr, l16 >> 2, RG_FV + (w4 >> 1) * BLK + ((w4 & 1) * 32 + h) * 128, 16);
            ST(1);
            const int sidx = (wl & 128u) ? 0xEEEE : 0x4444;
            unsigned so = RG_VM + slot * VM_SLOT + (unsigned)((w4 * 10 + (h >> 4)) * BLK + ((h >> 2) & 3) * 2048 + (h & 3) * 256);
            bar();
            ST(3);
            ASM_ST_DECL;
            asm volatile(ASM_CRIT2 : [so] "+s"(so), "+{v[212:219]}"(lw), "+{v200}"(l16), "+{v[220:251]}"(cz) ASM_ST_OUT
                         : WREGS(wf), [wl] "v"(wl), [vo] "v"(vo), [sidx] "v"(sidx), [rr] "s"(rr)
                         : "memory", "scc", "v201", "v202", "v203", "v204", "v205", "v206", "v207", "v208", "v209", "v210", "v211");
            ASM_ST_SAVE(4);
            ST(5);
            STFLUSH(t);
        }
    }
}

constexpr unsigned R_RED = 0;
constexpr unsigned R_PART = 16384;
__device__ __forceinline__ void r_role(const Params& P, int wave, int L, int par, int jj, int cpy) {
    const int lane = lane_id(), l15 = lane & 15, l4 = lane >> 4;
    const unsigned l16 = lane * 16;
    const rsrc_t rr = mkrsrc(P.ws + par * 8192, RG_L), rd = mkrsrc(P.ws + PG0, PG_L);
    const int sidx = (l15 < 8) ? 0x4444 : 0xEEEE;
    unsigned offp[3];
    offp[0] = RG_XP + cpy * XP_COPY + BLK + (wave >> 2) * BLK + (wave & 3) * 2048 + l16;
    offp[1] = offp[0] + 1024;
    offp[2] = (l15 < 8) ? RG_XP + cpy * XP_COPY + wave * 512 + l4 * 128 + l15 * 16 : OOB;
    const unsigned offv = RG_VM + (jj * 2 + (wave >> 2)) * BLK + (wave & 3) * 2048 + l16;
    const int j = jj * 2 + ((wave >> 1) & 1), fl = (wave & 1) * 32 + (lane & 31);
    const unsigned offo = RG_XM + ((wave >> 2) * 2 + (lane >> 5)) * XM_COPY + j * 256 + (j >> 5) * 8192 + fl * 4;
    {
        const int row = (lane >> 2) & 7, col = 16 * j + 4 * (lane & 3) + 2 * (wave & 1);
        __builtin_amdgcn_raw_buffer_store_b32(nosent(*(const unsigned*)(P.mem0 + row * DM + col)), rr, offo, 0, 16);
    }
    auto red_addr = [](int fl) { return R_RED + fl * 80 + (fl >> 4) * 16; };
    const unsigned rw0 = (lane >> 5) * 5248 + red_addr((((l15 >> 1) & 1) * 8 + 4 * (l4 & 1)) * 4 + (l15 >> 2)) + (l15 & 1) * 4 + (wave ^ (l15 >> 3)) * 8;
    const unsigned rrd = ((wave >> 1) & 1) * 5248 + red_addr(fl) + (lane >> 5) * 32;
    u32x32 vx;
    const unsigned fo = (lane & 3) * 128, fs = RG_FV + (jj / 10) * BLK + (((jj / 5) & 1) * 32 + wave * 4) * 128, vxo = pgb(64 + jj / 10, par) + (unsigned)((jj % 10) * 8 + (wave >> 1)) * BLK + (wave & 1) * 4096;
    const rsrc_t rp = mkrsrc(P.ws + PG0, PG_L);
    const unsigned xo = (l4 >> 1) * 256 + l15 * 16 + (l4 & 1) * 8, pw = R_PART + wave * 1024 + l16, pr = R_PART + (wave & 3) * 8192 + l16;
    const int fin = wave >> 2;
    asm volatile("" : "={v[128:159]}"(vx));
    auto vx_fetch = [&](int t) {
        for (int spins = 0;; ++spins) {
            const unsigned f = __builtin_amdgcn_raw_buffer_load_b32(rr, fo, fs, 16);
            if (__all(f > (unsigned)t)) break;
            __builtin_amdgcn_s_sleep(2);
            if (spin_fail(spins, P)) break;
        }
        const unsigned sv = ((t & 7) << 22) + vxo;
        asm volatile("buffer_load_dwordx4 v[128:131], %1, %2, %3 offen sc1\n\tbuffer_load_dwordx4 v[132:135], %1, %2, %3 offen offset:1024 sc1\n\t"
                     "buffer_load_dwordx4 v[136:139], %1, %2, %3 offen offset:2048 sc1\n\tbuffer_load_dwordx4 v[140:143], %1, %2, %3 offen offset:3072 sc1\n\t"
                     "buffer_load_dwordx4 v[144:147], %1, %2, %4 offen sc1\n\tbuffer_load_dwordx4 v[148:151], %1, %2, %4 offen offset:1024 sc1\n\t"
                     "buffer_load_dwordx4 v[152:155], %1, %2, %4 offen offset:2048 sc1\n\tbuffer_load_dwordx4 v[156:159], %1, %2, %4 offen offset:3072 sc1"
                     : "+{v[128:159]}"(vx) : "v"(l16), "s"(rp), "s"(sv), "s"(sv + 4 * BLK) : "memory");
    };
    vx_fetch(0);
    STDECL;
    LDECL;
    for (int t = 0; t < NSEG; ++t) {
        const unsigned slot = t & (NSLOT - 1);
        ST(0);
        __builtin_amdgcn_s_sleep(RSL);
        u32x4 pm = sent4();
        u32x8 pv = __builtin_shufflevector(pm, pm, 0, 1, 2, 3, 0, 1, 2, 3), vm = pv;
#pragma unroll 1
        for (int spins = 1023;; spins += 1024) {
            unsigned n, a;
            ASM_ST_DECL;
            asm volatile(ASM_RCRIT : "+{v[176:183]}"(vm), "+{v[184:191]}"(pv), "+{v[192:195]}"(pm), [n] "=&s"(n), [a] "=&s"(a) ASM_ST_OUT ASM_ST2_OUT
                         : [rs] "s"(rr), [ov] "v"(offv), [sv] "s"(slot * VM_SLOT), [o0] "v"(offp[0]), [o1] "v"(offp[1]), [o2] "v"(offp[2]), [sp] "s"(slot * XP_SLOT),
                           "{v[128:159]}"(vx), [sidx] "v"(sidx), [rw] "v"(rw0), [rd] "v"(rrd), [oo] "v"(offo),
                           [so] "s"(((t + 1) & (NSLOT - 1)) * XM_SLOT), [pub] "s"(NSEG - 1 - t)
                         : "memory", "vcc", "scc", "v160", "v161", "v162", "v163", "v164", "v165", "v166", "v167", "v196", "v200", "v201", "v202", "v203", "v204", "v205", "v206", "v207", "v208", "v209", "v210", "v211", "v212", "v213", "v214", "v215");
            ASM_ST_SAVE(2); ASM_ST2_SAVE(1);
            if (__builtin_amdgcn_readfirstlane(n)) break;
            if (spin_fail(spins, P)) { bar(); break; }
        }
        ST(3);
        LA();
        __builtin_amdgcn_s_sleep(RXDEL);
        unsigned early;
#pragma unroll 1
        for (int spins = 1023;; spins += 1024) {
            unsigned n;
            const unsigned sx = pgb(t, par) + blin(((wave >> 1) & 1) * 20480 + jj * 1024) + (wave & 1) * 512;
            const unsigned pxb = RG_PX + slot * PX_SLOT + wave * BLK, sv = (((t + 1) & 7) << 22) + vxo;
            ASM_ST_DECL;
            asm volatile(ASM_RX : "+{v[128:159]}"(vx), [n] "=&s"(n), [a] "=&s"(early) ASM_ST_OUT ASM_ST2_OUT
                         : "{v[176:183]}"(vm), "{v[184:191]}"(pv), "{v[192:195]}"(pm), [l16] "v"(l16), [rs] "s"(rr), [s0] "s"(pxb), [s1] "s"(pxb + 4096), [rd] "s"(rd), [rx] "s"(rd), [xo] "v"(xo),
                           [fo] "v"(fo), [fs] "s"(fs), [tn] "s"(t + 1), [sv0] "s"(sv), [sv1] "s"(sv + 4 * BLK), [rp] "s"(rp), [sx] "s"(PG_XR + L * PG_LX + sx), [sa] "s"(PG_XA + sx),
                           [pw] "v"(pw), [pr] "v"(pr), [fin] "s"(fin)
                         : "memory", "vcc", "scc", "v96", "v97", "v98", "v99", "v100", "v101", "v102", "v103", "v104", "v105", "v106", "v107", "v108", "v109", "v110", "v111", "v112", "v113", "v114", "v115", "v116", "v117", "v118", "v119", "v120", "v121", "v122", "v123", "v124", "v125", "v126", "v127", "v196", "v200", "v201", "v202", "v203", "v204", "v205", "v206", "v207", "v208", "v209", "v210", "v211", "v212", "v213", "v214", "v215", "v216", "v217", "v218", "v219", "v220", "v221", "v222", "v223", "v224", "v225", "v226", "v227", "v228", "v229", "v230", "v231", "v240", "v241", "v242", "v243", "v248", "v250", "v251");
            ASM_ST_SAVE(4); ASM_ST2_SAVE(5);
            if (__builtin_amdgcn_readfirstlane(n)) break;
            early = 0;
            if (spin_fail(spins, P)) { bar(); break; }
        }
        ST(6);
        if (t + 1 < NSEG && !__builtin_amdgcn_readfirstlane(early)) vx_fetch(t + 1);
        st128(sent4(), rr, offv, slot * VM_SLOT); st128(sent4(), rr, offv + 1024, slot * VM_SLOT);
        __builtin_amdgcn_raw_buffer_store_b32(SENT, rr, offo, slot * XM_SLOT, 16);
        if (wave < 2 && t + AH < NSEG) st128(sent4(), rd, l16, PG_XA + pgb(t + AH, par) + blin(wave * 20480 + jj * 1024));
        ST(7);
        LB(t);
        STFLUSH(t);
    }
    LF();
}

template <int WHICH>
__device__ __forceinline__ void w_role(const Params& P, int wave, int par, int cu) {
    constexpr int NT = (WHICH == 1) ? 1 : 2;
    constexpr int KS = 20, PW = 5;
    constexpr int N = (WHICH == 3) ? FF : DM;
    constexpr unsigned W_ST = 2 * 49152;
    const int ln = lane_id(), l15 = ln & 15, l4 = ln >> 4;
    const unsigned l16 = ln * 16;
    const rsrc_t rd = mkrsrc(P.ws + PG0, PG_L);
    constexpr unsigned D_IN = WHICH == 1 ? PG_XA : WHICH == 2 ? PG_HS : PG_X1;
    constexpr unsigned D_OUT = WHICH == 1 ? PG_HS : WHICH == 2 ? PG_X1 : PG_H2;
    const unsigned pc0 = (wave >> 2) * 20480 + (wave & 3) * 4096, pc1 = (wave >> 2) * 20480 + 16384 + (wave & 3) * 1024, in0 = D_IN + blin(pc0), in1 = D_IN + blin(pc1);
    if (WHICH == 2 && cu * 8 + wave >= 20) {
        bar();
        for (int t = 0; t < NSEG; ++t) {
            u32x4 v[PW] = {};
            poll5_issue(rd, l16, pgb(t, par) + in0, pgb(t, par) + in1, v);
            wait_vm<0>();
            poll5(rd, l16, pgb(t, par) + in0, pgb(t, par) + in1, v, P);
#pragma unroll
            for (int k = 0; k < PW; ++k) lds_st<u32x4>((t & 1) * 49152u + (k < 4 ? pc0 + k * 1024 : pc1) + l16, v[k]);
            bar();
            wait_vm<0>();
            asm volatile("" :: "v"(v[0]), "v"(v[1]), "v"(v[2]), "v"(v[3]), "v"(v[4]));
        }
        return;
    }
    const int nt0 = (cu * 8 + wave) * NT;
    bf16x8 wf[NT][KS];
    {
        u32x4 f[NT * KS];
        load_wn<NT, KS, N>(f, P.w[3 + WHICH], DM, wave, nt0, 0);
#pragma unroll
        for (int k = 0; k < NT * KS; ++k) wf[k / KS][k % KS] = BF8(f[k]);
    }
    bar();
    f32x4 cs[NT];
    if (WHICH != 2) {
        const bf16x8 ones = __builtin_bit_cast(bf16x8, ((u32x4){0x3F803F80u, 0x3F803F80u, 0x3F803F80u, 0x3F803F80u}));
#pragma unroll
        for (int n = 0; n < NT; ++n) {
            cs[n] = (f32x4){0.f, 0.f, 0.f, 0.f};
#pragma unroll
            for (int k = 0; k < KS; ++k) MFMA(wf[n][k], ones, cs[n]);
        }
    }
    const int grp = nt0 / 10, sh = (WHICH == 1) ? ((grp & 1) ? -1 : 1) * ((grp >> 1) + 1) : 0;
    if (WHICH == 1) {
        const int zr = (grp & 1) ? S_ - 1 - l15 : l15;
        unsigned zv;
        asm volatile("v_mov_b32 %0, 0" : "=v"(zv));
        st128(((u32x4){zv, zv, zv, zv}), rd, (l15 <= (grp >> 1) && l4 < 2) ? toff(zr, nt0 * 16 + l4 * 8, par) : OOB, PG_HS);
    }
    u32x4 v[PW] = {};
    poll5_issue(rd, l16, pgb(0, par) + in0, pgb(0, par) + in1, v);
    wait_vm<0>();
    STDECL;
    for (int t = 0; t < NSEG; ++t) {
        const unsigned ab = (t & 1) * 49152u, pg = pgb(t, par);
        ST(0);
        {
            poll5(rd, l16, pg + in0, pg + in1, v, P);
            ST(1);
#pragma unroll
            for (int k = 0; k < PW; ++k) lds_st<u32x4>(ab + (k < 4 ? pc0 + k * 1024 : pc1) + l16, v[k]);
            if (WHICH != 2) {
                float s = 0.f, q = 0.f;
                const bf16x2 one2 = __builtin_bit_cast(bf16x2, 0x3F803F80u);
#pragma unroll
                for (int k = 0; k < PW; ++k)
#pragma unroll
                    for (int e = 0; e < 4; ++e) {
                        const unsigned u = v[k][e];
                        const bf16x2 x = __builtin_bit_cast(bf16x2, u);
                        s = __builtin_amdgcn_fdot2_f32_bf16(x, one2, s, false);
                        q = __builtin_amdgcn_fdot2_f32_bf16(x, x, q, false);
                    }
                sum2_rows(s, q);
                if (ln < 16) lds_st<f32x2>(W_ST + (t & 1) * 1024 + wave * 128 + ln * 8, (f32x2){s, q});
            }
        }
        bar();
        ST(2);
        u32x2 r1[2][NT], sv[2];
        if (WHICH == 2) {
            const unsigned ro = (l4 >> 1) * 256 + l15 * 16 + (l4 & 1) * 8;
#pragma unroll
            for (int mt = 0; mt < 2; ++mt) { r1[mt][0] = LD64(rd, ro, pg + PG_XA + blin(mt * 20480 + nt0 * 512)); r1[mt][NT - 1] = LD64(rd, ro + 512, pg + PG_XA + blin(mt * 20480 + nt0 * 512)); sv[mt] = LD64(rd, (mt * 16 + l15) * 8, pg + PG_SS); }
        }
        float mr[2], rs[2];
        f32x2 st[2][4];
        if (WHICH != 2) {
#pragma unroll
            for (int w = 0; w < 8; ++w) st[w >> 2][w & 3] = lds_ld<f32x2>(W_ST + (t & 1) * 1024 + w * 128 + l15 * 8);
        }
        auto stats = [&](int mt) {
            const f32x2 a = (st[mt][0] + st[mt][1]) + (st[mt][2] + st[mt][3]);
            const float mean = a[0] * (1.0f / DM), var = __builtin_fmaxf(a[1] * (1.0f / DM) - mean * mean, 0.f);
            const float m1 = rbf(mean), d1 = ln_den(var), r1i = rcp(d1);
            if (WHICH == 1) {
                const float m2 = rbf((mean - m1) * r1i), d2 = ln_den(var * r1i * r1i);
                rs[mt] = r1i * rcp(d2); mr[mt] = -(m1 + m2 * d1) * rs[mt];
                if (cu == 0 && wave == 0)
                    __builtin_amdgcn_raw_buffer_store_b64((u32x2){__float_as_uint(m1), __float_as_uint(r1i)}, rd, ln < 16 ? (mt * 16 + ln) * 8 : OOB, pg + PG_SS, 16);
            } else { rs[mt] = r1i; mr[mt] = -m1 * r1i; }
            asm volatile("" : "+v"(rs[mt]), "+v"(mr[mt]));
        };
        f32x4 acc[NT][2];
        unsigned o[2][NT][2];
        auto fin = [&](int mt, int n, int hf) {
            float v[2];
#pragma unroll
            for (int e = 0; e < 2; ++e) {
                if (WHICH != 2) v[e] = gelu_tanh(__builtin_fmaf(acc[n][mt][hf * 2 + e], rs[mt], cs[n][hf * 2 + e] * mr[mt]));
                else v[e] = rbf(((e ? hi2f(r1[mt][n][hf]) : lo2f(r1[mt][n][hf])) - __uint_as_float(sv[mt][0])) * __uint_as_float(sv[mt][1])) + acc[n][mt][hf * 2 + e];
            }
            o[mt][n][hf] = nosent(pack2(v[0], v[1]));
            asm volatile("" : "+v"(o[mt][n][hf]));
        };
        const unsigned o3 = (unsigned)((l4 & 1) * 512 + (l4 >> 1) * 256 + l15 * 16), c3 = D_OUT + blin((nt0 >> 1) * 1024), d3 = D_OUT + blin(N * 32 + (nt0 >> 1) * 1024);
#pragma unroll
        for (int n = 0; n < NT; ++n) { acc[n][0] = (f32x4){0.f, 0.f, 0.f, 0.f}; acc[n][1] = acc[n][0]; }
        {
            constexpr int RG = 8;
            bf16x8 a[RG];
#pragma unroll
            for (int u = 0; u < RG - 1; ++u) a[u] = lds_ld<bf16x8>(ab + l16 + u * 1024);
#pragma unroll
            for (int u = 0; u < 2 * KS; ++u) {
                if (u + RG - 1 < 2 * KS) a[(u + RG - 1) % RG] = lds_ld<bf16x8>(ab + l16 + (u + RG - 1) * 1024);
                SB();
#pragma unroll
                for (int n = 0; n < NT; ++n) MFMA(wf[n][u % KS], a[u % RG], acc[n][u / KS]);
                if (WHICH != 2 && (u == 4 || u == 12)) stats(u == 12);
                if (u == (NT == 2 ? 22 : 6)) poll5_issue(rd, l16, pgb(t + 1, par) + in0, pgb(t + 1, par) + in1, v);
                if (NT == 2 && u == 38) wait_vm<0>();
                if (u >= 24 && u % 4 == 0 && (u - 24) / 4 < 2 * NT) fin(0, (u - 24) / 8, ((u - 24) / 4) & 1);
                if (NT == 2 && u == 38) st128(merge16((u32x2){o[0][0][0], o[0][0][1]}, (u32x2){o[0][NT - 1][0], o[0][NT - 1][1]}), rd, o3, pg + c3);
                SB();
            }
        }
        ST(3);
#pragma unroll
        for (int k = 0; k < 2 * NT; ++k) fin(1, k >> 1, k & 1);
        if (NT == 2) {
            st128(merge16((u32x2){o[1][0][0], o[1][0][1]}, (u32x2){o[1][NT - 1][0], o[1][NT - 1][1]}), rd, o3, pg + d3);
            ST(4);
            if (t + AH < NSEG) { st128(sent4(), rd, l16, pgb(t + AH, par) + c3); st128(sent4(), rd, l16, pgb(t + AH, par) + d3); }
        } else {
            wait_vm<0>();
            const int orow = t * SEG + (l4 & 1) * 16 + l15 + sh;
            st128(merge16((u32x2){o[0][0][0], o[0][0][1]}, (u32x2){o[1][0][0], o[1][0][1]}), rd, (unsigned)orow < (unsigned)S_ ? toff(orow, nt0 * 16 + (l4 >> 1) * 8, par) : OOB, D_OUT);
            ST(4);
            if (t + AH < NSEG) st128(sent4(), rd, orow + AH * SEG < S_ ? toff(orow + AH * SEG, nt0 * 16 + (l4 >> 1) * 8, par) : OOB, D_OUT);
        }
        STFLUSH(t);
    }
    wait_vm<0>();
    asm volatile("" :: "v"(v[0]), "v"(v[1]), "v"(v[2]), "v"(v[3]), "v"(v[4]));
}

__device__ __forceinline__ void w4_role(const Params& P, int wave, int L, int par, int cu) {
    const int ln = lane_id(), l15 = ln & 15, l4 = ln >> 4;
    const unsigned l16 = ln * 16;
    const rsrc_t rd = mkrsrc(P.ws + PG0, PG_L);
    STDECL;
    if (wave < 6) {
        u32x32 w[5];
        {
            u32x4 g[40];
            load_wn<5, 8, DM>(g, P.w[7], FF, wave, cu * 5, wave * 8);
#pragma unroll
            for (int n = 0; n < 5; ++n) {
                u32x8 f[4];
#pragma unroll
                for (int i = 0; i < 4; ++i) f[i] = __builtin_shufflevector(g[n * 8 + 2 * i], g[n * 8 + 2 * i + 1], 0, 1, 2, 3, 4, 5, 6, 7);
                w[n] = cat32(f[0], f[1], f[2], f[3]);
            }
            asm volatile("" : "+{v[0:31]}"(w[0]), "+{v[32:63]}"(w[1]), "+{v[64:95]}"(w[2]), "+{v[96:127]}"(w[3]), "+{v[128:159]}"(w[4]));
        }
        bar();
        unsigned budget = 1u << 17;
        for (int t = 0; t < NSEG; ++t) {
            ST(0);
            const unsigned gb = PG_H2 + pgb(t, par) + wave * BLK;
            u32x4 can;
            for (int spins = 0;; ++spins) {
                can = LD128(rd, l16, gb + 7168);
                if (__all(umax4(0u, can) != SENT)) break;
                __builtin_amdgcn_s_sleep(1);
                if (spin_fail(spins, P)) { budget = 0; break; }
            }
            ST(1);
            asm volatile(ASM_W4_M : [n] "+s"(budget)
                         : "{v[188:191]}"(can), "{v[0:31]}"(w[0]), "{v[32:63]}"(w[1]), "{v[64:95]}"(w[2]), "{v[96:127]}"(w[3]), "{v[128:159]}"(w[4]),
                           [ld] "v"(l16 + wave * 1024 + (t & 1) * 61440), [l16] "v"(l16), [rs] "s"(rd), [s0] "s"(gb), [s1] "s"(gb + 4096), [s2] "s"(gb + 6 * BLK), [s3] "s"(gb + 6 * BLK + 4096)
                         : "memory", "vcc", "scc", "v160", "v161", "v162", "v163", "v164", "v165", "v166", "v167", "v168", "v169", "v170", "v171", "v172", "v173", "v174", "v175", "v176", "v177", "v178", "v179", "v180", "v181", "v182", "v183", "v184", "v185", "v186", "v187", "v192", "v193", "v194", "v195", "v196", "v197", "v198", "v199", "v200", "v201", "v202", "v203", "v204", "v205", "v206", "v207", "v208", "v209", "v210", "v211", "v212", "v213", "v214", "v215", "v216", "v217", "v218", "v219", "v220", "v221", "v222", "v223", "v224", "v225", "v226", "v227", "v228", "v229", "v230", "v231", "v232", "v233", "v234", "v235", "v236", "v237", "v238", "v239", "v240", "v241", "v242", "v243", "v244");
            ST(2);
            bar();
            ST(3);
            STFLUSH(t);
        }
    } else {
        const int mt = wave - 6;
        bar();
        const rsrc_t rr = mkrsrc(P.ws + par * 8192, RG_L), ro = mkrsrc(P.out, ACT);
        const unsigned c0 = mt * 20480 + cu * 2560, o8 = (l4 >> 1) * 256 + l15 * 16 + (l4 & 1) * 8;
        const unsigned o16a = blin(c0 + (l4 & 1) * 512) + (l4 >> 1) * 256 + l15 * 16, o16b = blin(c0 + 1024 + (l4 & 1) * 512) + (l4 >> 1) * 256 + l15 * 16, o8c = blin(c0 + 2048) + o8;
        const unsigned q8 = l15 * (DM * 2) + l4 * 8, q16 = l15 * (DM * 2) + (l4 & 1) * 32 + (l4 >> 1) * 16;
        for (int t = 0; t < NSEG; ++t) {
            const unsigned sb = pgb(t, par), sn = (sb ^ 8192) + PG_LX;
            ST(0);
            f32x4 res[5];
            for (int spins = 0;; ++spins) {
                u32x2 r1[5], r2[5];
                unsigned m = 0;
#pragma unroll
                for (int n = 0; n < 5; ++n) { r1[n] = LD64(rd, o8 + blin(c0 + n * 512), PG_X1 + sb); r2[n] = LD64(rd, o8 + blin(c0 + n * 512), PG_XA + sb); }
#pragma unroll
                for (int n = 0; n < 5; ++n) {
                    m = umax4(m, (u32x4){r1[n][0], r1[n][1], r2[n][0], r2[n][1]});
                    res[n] = (f32x4){lo2f(r1[n][0]), hi2f(r1[n][0]), lo2f(r1[n][1]), hi2f(r1[n][1])} + (f32x4){lo2f(r2[n][0]), hi2f(r2[n][0]), lo2f(r2[n][1]), hi2f(r2[n][1])};
                }
                if (__all(m != SENT)) break;
                __builtin_amdgcn_s_sleep(8);
                if (spin_fail(spins * 4 + 3, P)) break;
            }
            asm volatile("" : "+v"(res[0]), "+v"(res[1]), "+v"(res[2]), "+v"(res[3]), "+v"(res[4]));
            ST(1);
            bar();
            ST(2);
            {
                const unsigned la = (t & 1) * 61440 + mt * 30720 + l16;
                f32x4 c[30];
#pragma unroll
                for (int i = 0; i < 30; ++i) c[i] = lds_ld<f32x4>(la + ((i % 5) * 6 + i / 5) * 1024);
#pragma unroll
                for (int i = 0; i < 30; ++i) { res[i % 5] += c[i]; asm volatile("" : "+v"(res[i % 5])); }
            }
            u32x2 o[5];
#pragma unroll
            for (int n = 0; n < 5; ++n) o[n] = (u32x2){pack2(res[n][0], res[n][1]), pack2(res[n][2], res[n][3])};
            if (L) {
                const unsigned ob = (unsigned)(t * 2 + mt) * 20480 + cu * 160;
                st128_<0>(merge16(o[0], o[1]), ro, q16, ob); st128_<0>(merge16(o[2], o[3]), ro, q16, ob + 64);
                __builtin_amdgcn_raw_buffer_store_b64(o[4], ro, q8, ob + 128, 0);
            } else {
                float s = 0.f, q = 0.f;
                const bf16x2 one2 = __builtin_bit_cast(bf16x2, 0x3F803F80u);
#pragma unroll
                for (int e = 0; e < 10; ++e) {
                    const unsigned u = o[e >> 1][e & 1];
                    const bf16x2 x = __builtin_bit_cast(bf16x2, u);
                    s = __builtin_amdgcn_fdot2_f32_bf16(x, one2, s, false);
                    q = __builtin_amdgcn_fdot2_f32_bf16(x, x, q, false);
                }
                sum2_rows(s, q);
                const unsigned st = RG_ST + (unsigned)(t >> 2) * BLK + (unsigned)((t & 3) * 2 + mt) * 1024;
                __builtin_amdgcn_raw_buffer_store_b64((u32x2){nosent(__float_as_uint(s)), nosent(__float_as_uint(q))}, rr, ln < 16 ? ln * 8 : OOB, st + cu * 128, 16);
                ST(3);
                st128(merge16(o[0], o[1]), rd, o16a, PG_XR + sn); st128(merge16(o[2], o[3]), rd, o16b, PG_XR + sn);
                __builtin_amdgcn_raw_buffer_store_b64(o[4], rd, o8c, PG_XR + sn, 16);
                __builtin_amdgcn_s_sleep(W4SL);
                for (int spins = 0;; ++spins) {
                    const u32x2 a = LD64(rr, ln * 8, st), b = LD64(rr, ln * 8 + 512, st);
                    s = __uint_as_float(a[0]) + __uint_as_float(b[0]); q = __uint_as_float(a[1]) + __uint_as_float(b[1]);
                    if (__all(umax4(0u, (u32x4){a[0], a[1], b[0], b[1]}) != SENT)) break;
                    asm volatile("" ::: "memory");
                    if (spin_fail(spins, P)) break;
                }
                ST(4);
                sum2_rows(s, q);
                const float mean = s * (1.0f / DM), var = __builtin_fmaxf(q * (1.0f / DM) - mean * mean, 0.f);
                const float r = rcp(ln_den(var)), mr = -rbf(mean) * r;
#pragma unroll
                for (int n = 0; n < 5; ++n)
                    o[n] = (u32x2){pack2(__builtin_fmaf(res[n][0], r, mr), __builtin_fmaf(res[n][1], r, mr)), pack2(__builtin_fmaf(res[n][2], r, mr), __builtin_fmaf(res[n][3], r, mr))};
                st128(merge16(o[0], o[1]), rd, o16a, PG_XN + sn); st128(merge16(o[2], o[3]), rd, o16b, PG_XN + sn);
                __builtin_amdgcn_raw_buffer_store_b64(o[4], rd, o8c, PG_XN + sn, 16);
                wait_vm<0>();
                st128(((u32x4){(unsigned)t + 1, (unsigned)t + 1, (unsigned)t + 1, (unsigned)t + 1}), mkrsrc(P.ws + (par ^ 1) * 8192, RG_L), ln < 32 ? (ln >> 3) * 2048 + (ln & 7) * 16 : OOB, RG_FX + (cu * 2 + mt) * 128);
            }
            ST(5);
            STFLUSH(t);
        }
    }
}

__global__ __launch_bounds__(512, 2) void mega_kernel(Params P) {
    const int wave = __builtin_amdgcn_readfirstlane(threadIdx.x >> 6);
    if (threadIdx.x == 0) {
        const unsigned c0 = *(const unsigned*)(P.ws + FL0 + FL_BYTES);
        unsigned g = (unsigned)__builtin_amdgcn_s_getreg((3 << 11) | 20) & 7u, k;
        while ((k = __hip_atomic_fetch_add((unsigned*)(P.ws + FL0 + FL_TK + g * 128), 1u, RLX_AGENT)) >= 32u) g = (g + 1) & 7u;
        lds_st<unsigned>(0, (c0 & 1u) << 8 | g << 5 | k);
    }
    bar();
    const unsigned tk = __builtin_amdgcn_readfirstlane(lds_ld<unsigned>(0));
    bar();
    const int g4 = (tk >> 5) & 3, k = tk & 31;
#define MAP 0
    const int L = (tk >> 7) & 1;
    const int x = g4, r = k, cpy = x;
    const int par = L ^ (tk >> 8);
    {
        const int i = (r - 26) * 4 + x;
        const int d = L * DLY_L + (r < 8 ? DLY_S : r < 21 ? 0 : (i < 0 || i == 8 || i == 9) ? DLY_H : DLY_W);
        for (int k = 0; k < d; ++k) __builtin_amdgcn_s_sleep(32);
    }
    if (r < 8) s_role(P, wave, L, par, x * 8 + r, cpy);
    else if (r < 16) v_role(P, wave ^ 4, L, par, x * 8 + r - 8, cpy);
    else if (r < 21) r_role(P, wave, L, par, (r - 16) * 4 + x, cpy);
    else {
        const int i = (r - 26) * 4 + x;
        if (i < 0 || i == 8 || i == 9) h_role(P, wave, L, par, r < 25 ? x * 4 + r - 21 : i < 0 ? 16 + x : 12 + i, cpy);
        else if (i < 5) w_role<1>(P, wave, par, i);
        else if (i < 8) w_role<2>(P, wave, par, i - 5);
        else if (i < 16) w_role<3>(P, wave, par, i - 10);
        else w4_role(P, wave, L, par, i - 16);
    }
}

__device__ __forceinline__ void xtile_body(int mt, const bf16_t* __restrict__ x, char* __restrict__ ws) {
    __shared__ float red[2][16][17];
    const int r15 = threadIdx.x & 15, slot = threadIdx.x >> 4;
    u32x4 v[5];
    float s = 0.f;
#pragma unroll
    for (int c = 0; c < 5; ++c) {
        v[c] = *(const u32x4*)(x + (size_t)(mt * 16 + r15) * DM + (slot * 5 + c) * 8);
#pragma unroll
        for (int e = 0; e < 4; ++e) s += lo2f(v[c][e]) + hi2f(v[c][e]);
    }
    red[0][slot][r15] = s;
    __syncthreads();
    s = 0.f;
#pragma unroll
    for (int k = 0; k < 16; ++k) s += red[0][k][r15];
    const float mean = s * (1.0f / DM);
    float q = 0.f;
#pragma unroll
    for (int c = 0; c < 5; ++c)
#pragma unroll
        for (int e = 0; e < 4; ++e) { const float a = lo2f(v[c][e]) - mean, b = hi2f(v[c][e]) - mean; q += a * a + b * b; }
    red[1][slot][r15] = q;
    __syncthreads();
    q = 0.f;
#pragma unroll
    for (int k = 0; k < 16; ++k) q += red[1][k][r15];
    const float m1 = rbf(mean), r = 1.0f / ln_den(q * (1.0f / DM));
#pragma unroll
    for (int c = 0; c < 5; ++c) {
        const unsigned o = PG0 + toff(mt * 16 + r15, (slot * 5 + c) * 8, 0);
        *(u32x4*)(ws + PG_XR + o) = v[c]; *(u32x4*)(ws + PG_XR + (o ^ 8192)) = v[c];
        u32x4 n;
#pragma unroll
        for (int e = 0; e < 4; ++e) n[e] = pack2(rbf(lo2f(v[c][e]) - m1) * r, rbf(hi2f(v[c][e]) - m1) * r);
        *(u32x4*)(ws + PG_XN + o) = n; *(u32x4*)(ws + PG_XN + (o ^ 8192)) = n;
    }
}

struct Prep1 { const bf16_t* x; char* ws; };
__global__ __launch_bounds__(256) void prep1_kernel(Prep1 A) {
    int b = blockIdx.x;
    if (b == 0) {
        __shared__ unsigned best[4][2];
        const int w = threadIdx.x >> 6;
        const rsrc_t r = mkrsrc(A.ws, RG_SLOT);
        unsigned m[2] = {~0u, ~0u};
        for (int i = 0; i < CALN; ++i) {
            const unsigned long long t0 = __builtin_amdgcn_s_memtime();
            __builtin_amdgcn_raw_buffer_store_b32(0u, r, (96 + w * 8 + ((i >> 1) & 7)) * BLK + (i & 1) * 8192 + (i >> 1) * 768, 0, 16);
            asm volatile("s_waitcnt vmcnt(0)" ::: "memory");
            const unsigned d = (unsigned)(__builtin_amdgcn_s_memtime() - t0);
            if (i & 1) m[1] = d < m[1] ? d : m[1]; else m[0] = d < m[0] ? d : m[0];
        }
        best[w][0] = m[0]; best[w][1] = m[1];
        __syncthreads();
        if (threadIdx.x == 0) {
            const unsigned a = best[0][0] + best[1][0] + best[2][0] + best[3][0], c = best[0][1] + best[1][1] + best[2][1] + best[3][1];
            *(u32x4*)(A.ws + FL0 + FL_BYTES) = (u32x4){(a > c ? 1u : 0u) ^ (((unsigned)__builtin_amdgcn_s_getreg((3 << 11) | 20) >> 2) & 1u), a, c, 0u};
        }
        return;
    }
    b -= 1;
    if (b < 256) {
        for (unsigned i = b * 256 + threadIdx.x; i < (4 * RG_ST + 16 * BLK) / 16; i += 256 * 256) {
            const unsigned sl = i / (RG_ST / 16);
            ((u32x4*)A.ws)[sl < 4 ? i + sl * ((RG_SLOT - RG_ST) / 16) : i - 3 * (RG_ST / 16)] = (u32x4){SENT, SENT, SENT, SENT};
        }
        if (b < 20) ((u32x4*)(A.ws + RG_FV))[b * 256 + threadIdx.x] = (u32x4){0u, 0u, 0u, 0u};
        if (b == 0) for (unsigned i = threadIdx.x; i < FL_BYTES / 16; i += 256) ((u32x4*)(A.ws + FL0))[i] = (u32x4){0u, 0u, 0u, 0u};
        for (unsigned i = (b & 63) * 256 + threadIdx.x; i < PG_SS / 16; i += 64 * 256) ((u32x4*)(A.ws + PG0 + ((b >> 6) << 21)))[i] = (u32x4){SENT, SENT, SENT, SENT};
        return;
    }
    b -= 256;
    xtile_body(b, A.x, A.ws);
}

extern "C" void kernel_launch(void* const* d_in, const int* in_sizes, int n_in, void* d_out, int out_size, void* d_ws,
                              size_t ws_size, hipStream_t stream) {
    char* ws = (char*)d_ws;
    Params P;
    P.ws = ws; P.mem0 = (const bf16_t*)d_in[1]; P.out = (bf16_t*)d_out;
    for (int i = 0; i < 8; ++i) P.w[i] = (const bf16_t*)d_in[i < 4 ? 2 + i : 4 + i];
    (void)hipFuncSetAttribute((const void*)mega_kernel, hipFuncAttributeMaxDynamicSharedMemorySize, MEGA_LDS);

    Prep1 A1;
    A1.x = (const bf16_t*)d_in[0]; A1.ws = ws;
    prep1_kernel<<<1 + 256 + S_ / 16, 256, 0, stream>>>(A1);
    mega_kernel<<<256, 512, MEGA_LDS, stream>>>(P);
}
```

```cpp
#include <hip/hip_runtime.h>
#include <stdint.h>
#include <type_traits>

typedef unsigned short bf16_t;
using bf16x2 = __attribute__((ext_vector_type(2))) __bf16;
using bf16x8 = __attribute__((ext_vector_type(8))) __bf16;
using bf16x16 = __attribute__((ext_vector_type(16))) __bf16;
using f32x2 = __attribute__((ext_vector_type(2))) float;
using f32x4 = __attribute__((ext_vector_type(4))) float;
using u32x2 = __attribute__((ext_vector_type(2))) unsigned;
using u32x4 = __attribute__((ext_vector_type(4))) unsigned;
typedef __amdgpu_buffer_rsrc_t rsrc_t;

__device__ __forceinline__ unsigned pack2(float a, float b) {
    bf16x2 r = __builtin_convertvector((f32x2){a, b}, bf16x2);
    return __builtin_bit_cast(unsigned, r);
}
__device__ __forceinline__ float rbf(float f) { return __uint_as_float(pack2(f, 0.f) << 16); }
__device__ __forceinline__ float lo2f(unsigned u) { return __uint_as_float(u << 16); }
__device__ __forceinline__ float hi2f(unsigned u) { return __uint_as_float(u & 0xFFFF0000u); }
__device__ __forceinline__ rsrc_t mkrsrc(const void* p, unsigned bytes) {
    return __builtin_amdgcn_make_buffer_rsrc((void*)p, 0, bytes, 0x00020000);
}

constexpr int S_ = 2048, DM = 640, NH = 32, DH = 192, HD = NH * DH  , SEG = 32, NSEG = 64;
constexpr int FF = 1536;

#define XMAP
#define NCPY 4
constexpr unsigned NSLOT = 4, NCP = NCPY;
#define FL_TK_DEF
#define DLY_L 14
#define DLY_S 0
#define DLY_W 10
#define DLY_H 0
#define EWAKE "s_nop 0"
#define LWAKE "s_wakeup\n\t"
#define SLP4 (wave == 4 ? t : 0)
#define WPF 12
#define WPRI 1
#define WPRI2 0
#define CALN 8
#define RSL 9
#define VTRY 3
#define VSL 1
#define NRR 5
constexpr unsigned BLK = 16384, RG_SLOT = 4u << 20;
constexpr unsigned XP_COPY = 3 * BLK, XP_SLOT = RG_SLOT;
constexpr unsigned XM_COPY = 2 * BLK, XM_SLOT = RG_SLOT;
constexpr unsigned VM_SLOT = RG_SLOT;
constexpr unsigned PX_SLOT = RG_SLOT;
constexpr unsigned RG_XP = 0, RG_XM = 12 * BLK, RG_VM = 20 * BLK, RG_PX = 60 * BLK;
constexpr unsigned RG_ST = 68 * BLK, RG_FV = 84 * BLK, RG_FH = 86 * BLK, RG_FX = 88 * BLK, RG_L = NSLOT * RG_SLOT;
constexpr unsigned FL0 = 2u << 20, FL_TK = 512, FL_BYTES = FL_TK + 8 * 128;
constexpr unsigned ACT = S_ * DM * 2;
#define PXROT 5
#define W4SL 6
#define RXDEL 1
#define VXD 32
#define VXAUX 16
constexpr unsigned PG0 = RG_L, PG_L = 80u << 21;
constexpr unsigned PG_XA = 0, PG_HS = 5 * BLK, PG_X1 = 10 * BLK, PG_H2 = 15 * BLK, PG_SS = 27 * BLK, PG_XR = 28 * BLK, PG_XN = 33 * BLK, PG_LX = 10 * BLK, PG_HV = 48 * BLK;
__device__ __forceinline__ unsigned pgb(unsigned p, int par) { return p << 21 | ((p ^ par) & 1) << 13; }
__device__ __forceinline__ constexpr unsigned blin(unsigned o) { return o + (o & ~8191u); }
constexpr int HT0 = 2;
constexpr int AH = 3;
constexpr unsigned WS_END = PG0 + PG_L;

#define SB() __builtin_amdgcn_sched_barrier(0)
#define LAS __attribute__((address_space(3)))
template <class T> __device__ __forceinline__ T lds_ld(unsigned a) { return *(const LAS T*)(a); }
template <class T> __device__ __forceinline__ void lds_st(unsigned a, T v) { *(LAS T*)(a) = v; }
constexpr unsigned OOB = 0x7FFFFF00u;
template <int N> __device__ __forceinline__ void wait_vm() { asm volatile("s_waitcnt vmcnt(%0)" :: "i"(N) : "memory"); }
__device__ __forceinline__ void bar() {
    asm volatile("s_waitcnt lgkmcnt(0)" ::: "memory");
    __builtin_amdgcn_s_barrier();
    asm volatile("" ::: "memory");
}
__device__ __forceinline__ int lane_id() {
    int ln;
    asm volatile("v_mbcnt_lo_u32_b32 %0, -1, 0\n\tv_mbcnt_hi_u32_b32 %0, -1, %0" : "=v"(ln));
    return ln;
}
__device__ __forceinline__ void swap16(unsigned& a, unsigned& b) { asm volatile("s_nop 3\n\tv_permlane16_swap_b32 %0, %1\n\ts_nop 1" : "+v"(a), "+v"(b)); }
__device__ __forceinline__ void swap32(unsigned& a, unsigned& b) { asm volatile("s_nop 3\n\tv_permlane32_swap_b32 %0, %1\n\ts_nop 1" : "+v"(a), "+v"(b)); }
__device__ __forceinline__ float sum_x16(float x) { unsigned a = __float_as_uint(x), b = a; swap16(a, b); return __uint_as_float(a) + __uint_as_float(b); }
__device__ __forceinline__ float sum_x32(float x) { unsigned a = __float_as_uint(x), b = a; swap32(a, b); return __uint_as_float(a) + __uint_as_float(b); }
__device__ __forceinline__ float max_x16(float x) { unsigned a = __float_as_uint(x), b = a; swap16(a, b); return __builtin_fmaxf(__uint_as_float(a), __uint_as_float(b)); }
__device__ __forceinline__ float max_x32(float x) { unsigned a = __float_as_uint(x), b = a; swap32(a, b); return __builtin_fmaxf(__uint_as_float(a), __uint_as_float(b)); }
__device__ __forceinline__ void sum2_rows(float& s, float& q) {
    unsigned a = __float_as_uint(s), b = a, c = __float_as_uint(q), d = c;
    asm volatile("s_nop 2\n\tv_permlane16_swap_b32 %0, %1\n\tv_permlane16_swap_b32 %2, %3\n\tv_add_f32 %0, %0, %1\n\tv_add_f32 %2, %2, %3\n\tv_mov_b32 %1, %0\n\tv_mov_b32 %3, %2\n\t"
                 "s_nop 1\n\tv_permlane32_swap_b32 %0, %1\n\tv_permlane32_swap_b32 %2, %3\n\tv_add_f32 %0, %0, %1\n\tv_add_f32 %2, %2, %3" : "+v"(a), "+v"(b), "+v"(c), "+v"(d));
    s = __uint_as_float(a); q = __uint_as_float(c);
}
__device__ __forceinline__ u32x4 merge16(u32x2 a, u32x2 b) {
    unsigned a0 = a[0], a1 = a[1], b0 = b[0], b1 = b[1];
    asm volatile("s_nop 3\n\tv_permlane16_swap_b32 %0, %2\n\tv_permlane16_swap_b32 %1, %3\n\ts_nop 1" : "+v"(a0), "+v"(a1), "+v"(b0), "+v"(b1));
    return (u32x4){a0, a1, b0, b1};
}
template <int AUXV>
__device__ __forceinline__ void st128_(u32x4 v, rsrc_t r, unsigned voff, unsigned soff) {
    __builtin_amdgcn_raw_buffer_store_b128(v, r, voff, soff, AUXV);
    asm volatile("s_nop 1" :: "v"(v) : "memory");
}
#define st128(V, R, VO_, SO_) st128_<16>(V, R, VO_, SO_)
#define LD128(R, VO_, SO_) __builtin_amdgcn_raw_buffer_load_b128(R, VO_, SO_, 16)
#define LD64(R, VO_, SO_) __builtin_amdgcn_raw_buffer_load_b64(R, VO_, SO_, 16)
#define BF8(X) __builtin_bit_cast(bf16x8, X)
__device__ __forceinline__ float gelu_tanh(float x) {
    const float c0 = -2.0f * 0.7978845608028654f * 1.4426950408889634f, c1 = c0 * 0.044715f;
    const float e = __builtin_amdgcn_exp2f(x * __builtin_fmaf(x * x, c1, c0));
    return x * __builtin_amdgcn_rcpf(1.0f + e);
}
#define MFMA(A_, B_, C_) C_ = __builtin_amdgcn_mfma_f32_16x16x32_bf16(A_, B_, C_, 0, 0, 0)
#define SMF(A_, B_, C_) C_ = __builtin_amdgcn_smfmac_f32_16x16x64_bf16(A_, B_, C_, sidx, 0, 0)
__device__ __forceinline__ float ln_den(float var) { return rbf(__builtin_amdgcn_sqrtf(rbf(rbf(var) + 1e-5f))); }
__device__ __forceinline__ float rcp(float x) { return __builtin_amdgcn_rcpf(x); }

struct Params {
    char* ws;
    const bf16_t* mem0;
    const bf16_t* w[8];
    bf16_t* out;
};

#define RLX_AGENT __ATOMIC_RELAXED, __HIP_MEMORY_SCOPE_AGENT
#define SENT 0xFFFFFFFFu
constexpr int SPIN_CAP = 400000;
__device__ __forceinline__ bool spin_fail(int spins, const Params& P) {
    if ((spins & 1023) == 1023) {
        const rsrc_t rt = mkrsrc(P.ws + FL0, FL_BYTES);
        unsigned o;
        asm volatile("v_mov_b32 %0, 0x100" : "=v"(o));
        if (__builtin_amdgcn_readfirstlane(__builtin_amdgcn_raw_buffer_load_b32(rt, o, 0, 16)) != 0u) return true;
        if (spins > SPIN_CAP) { __builtin_amdgcn_raw_buffer_store_b32(o, rt, o, 0, 16); return true; }
    }
    return false;
}
__device__ __forceinline__ bool ok4(u32x4 v) { return v[0] != SENT && v[1] != SENT && v[2] != SENT && v[3] != SENT; }
__device__ __forceinline__ unsigned nosent(unsigned d) { return d < 0xFFFFFFFEu ? d : 0xFFFFFFFEu; }
__device__ __forceinline__ u32x4 nosent4(u32x4 v) { return (u32x4){nosent(v[0]), nosent(v[1]), nosent(v[2]), nosent(v[3])}; }
__device__ __forceinline__ unsigned umax3(unsigned a, unsigned b, unsigned c) { const unsigned m = a > b ? a : b; return m > c ? m : c; }
__device__ __forceinline__ unsigned umax4(unsigned m, u32x4 v) { return umax3(umax3(m, v[0], v[1]), v[2], v[3]); }
__device__ __forceinline__ u32x4 sent4() { unsigned sv; asm volatile("v_mov_b32 %0, -1" : "=v"(sv)); return (u32x4){sv, sv, sv, sv}; }
template <int N>
__device__ __forceinline__ void poll(rsrc_t rs, unsigned base, const unsigned (&off)[N], u32x4 (&v)[N], const Params& P) {
    for (int spins = 0;; ++spins) {
        unsigned m = 0;
#pragma unroll
        for (int k = 0; k < N; ++k) v[k] = LD128(rs, off[k], base);
#pragma unroll
        for (int k = 0; k < N; ++k) m = umax4(m, v[k]);
        if (__all(m != SENT)) return;
        asm volatile("" ::: "memory");
        if (spin_fail(spins, P)) return;
    }
}
__device__ __forceinline__ bf16x16 cat16(bf16x8 a, bf16x8 b) {
    return __builtin_shufflevector(a, b, 0, 1, 2, 3, 4, 5, 6, 7, 8, 9, 10, 11, 12, 13, 14, 15);
}
__device__ __forceinline__ bf16x8 half8(bf16x16 v, int h) {
    return h ? __builtin_shufflevector(v, v, 8, 9, 10, 11, 12, 13, 14, 15) : __builtin_shufflevector(v, v, 0, 1, 2, 3, 4, 5, 6, 7);
}
__device__ __forceinline__ bf16x16 lds_frag(unsigned a, unsigned step) { return cat16(lds_ld<bf16x8>(a), lds_ld<bf16x8>(a + step)); }
template <int I>
__device__ __forceinline__ void dmaq(const char* g, unsigned l16, unsigned dst) {
    asm volatile("s_mov_b32 m0, %2\n\ts_nop 0\n\tglobal_load_lds_dwordx4 %0, %1 offset:%3 sc1" :: "v"(l16), "s"(g), "s"(dst), "i"(I * 1024) : "memory");
}
__device__ __forceinline__ void dma4(const char* g, unsigned l16, unsigned dst) {
    asm volatile("s_mov_b32 m0, %2\n\ts_nop 0\n\tglobal_load_lds_dwordx4 %0, %1 sc1\n\tglobal_load_lds_dwordx4 %0, %1 offset:1024 sc1\n\t"
                 "global_load_lds_dwordx4 %0, %1 offset:2048 sc1\n\tglobal_load_lds_dwordx4 %0, %1 offset:3072 sc1" :: "v"(l16), "s"(g), "s"(dst) : "memory");
}
__device__ __forceinline__ unsigned toff(int row, int col, int par) {
    return pgb(row >> 5, par) + blin((unsigned)((row >> 4) & 1) * 20480u + (unsigned)(col >> 3) * 256u) + (unsigned)(row & 15) * 16u;
}

#define ASM_ST
#define ASM_ST_DECL
#define ASM_ST_OUT
#define ASM_ST_SAVE(k)
#define ASM_ST2
#define ASM_ST3
#define ASM_ST3_OUT
#define ASM_ST3_SAVE(k)
#define ASM_ST2_OUT
#define ASM_ST2_SAVE(k)
#define STDECL
#define ST(k)
#define STC(k)
#define STFLUSH(t)

#define LDECL
#define LA()
#define LB(t)
#define LF()
constexpr unsigned L_MEM = 0;
constexpr unsigned L_Q = 10240;
constexpr unsigned L_KM = 13312;
constexpr unsigned L_QX = 16384;
constexpr unsigned L_KX = 28672;
constexpr unsigned L_XB = 40960;
constexpr unsigned L_XA = 49152;
constexpr unsigned L_W = 81920;
constexpr int MEGA_LDS = 163840;
constexpr int wi(int i, int j) { return (i >> 1) * 5 + ((i & 1) ? 2 + j : j - 1); }

#define RPUB2
#define WNT " nt"
#define ASM_WLOAD3 \
    "s_xor_b32 %[t], %[wx], 0\n\t" \
    "s_mul_i32 %[t], %[t], 0xc0000\n\t" \
    "buffer_load_dwordx4 v[200:203], %[o0], %[rs], %[t] offen" WNT "\n\t" \
    "buffer_load_dwordx4 v[0:3], %[o1], %[rs], %[t] offen" WNT "\n\t" \
    "buffer_load_dwordx4 v[8:11], %[o2], %[rs], %[t] offen" WNT "\n\t" \
    "s_add_u32 %[t], %[t], 0x18000\n\t" \
    "buffer_load_dwordx4 v[16:19], %[o0], %[rs], %[t] offen" WNT "\n\t" \
    "buffer_load_dwordx4 v[24:27], %[o1], %[rs], %[t] offen" WNT "\n\t" \
    "buffer_load_dwordx4 v[32:35], %[o2], %[rs], %[t] offen" WNT "\n\t" \
    "s_add_u32 %[t], %[t], 0x18000\n\t" \
    "buffer_load_dwordx4 v[208:211], %[o0], %[rs], %[t] offen" WNT "\n\t" \
    "buffer_load_dwordx4 v[40:43], %[o1], %[rs], %[t] offen" WNT "\n\t" \
    "buffer_load_dwordx4 v[48:51], %[o2], %[rs], %[t] offen" WNT "\n\t" \
    "s_add_u32 %[t], %[t], 0x18000\n\t" \
    "buffer_load_dwordx4 v[56:59], %[o0], %[rs], %[t] offen" WNT "\n\t" \
    "buffer_load_dwordx4 v[64:67], %[o1], %[rs], %[t] offen" WNT "\n\t" \
    "buffer_load_dwordx4 v[72:75], %[o2], %[rs], %[t] offen" WNT "\n\t" \
    "s_add_u32 %[t], %[t], 0x18000\n\t" \
    "buffer_load_dwordx4 v[204:207], %[o0], %[rs], %[t] offen" WNT "\n\t" \
    "buffer_load_dwordx4 v[4:7], %[o1], %[rs], %[t] offen" WNT "\n\t" \
    "buffer_load_dwordx4 v[12:15], %[o2], %[rs], %[t] offen" WNT "\n\t" \
    "s_add_u32 %[t], %[t], 0x18000\n\t" \
    "buffer_load_dwordx4 v[20:23], %[o0], %[rs], %[t] offen" WNT "\n\t" \
    "buffer_load_dwordx4 v[28:31], %[o1], %[rs], %[t] offen" WNT "\n\t" \
    "buffer_load_dwordx4 v[36:39], %[o2], %[rs], %[t] offen" WNT "\n\t" \
    "s_add_u32 %[t], %[t], 0x18000\n\t" \
    "buffer_load_dwordx4 v[212:215], %[o0], %[rs], %[t] offen" WNT "\n\t" \
    "buffer_load_dwordx4 v[44:47], %[o1], %[rs], %[t] offen" WNT "\n\t" \
    "buffer_load_dwordx4 v[52:55], %[o2], %[rs], %[t] offen" WNT "\n\t" \
    "s_add_u32 %[t], %[t], 0x18000\n\t" \
    "buffer_load_dwordx4 v[60:63], %[o0], %[rs], %[t] offen" WNT "\n\t" \
    "buffer_load_dwordx4 v[68:71], %[o1], %[rs], %[t] offen" WNT "\n\t" \
    "buffer_load_dwordx4 v[76:79], %[o2], %[rs], %[t] offen" WNT "\n\t" \
    "s_add_u32 %[t], %[t], 0x18000\n\t" \
    "s_waitcnt vmcnt(21)\n\t" \
    "ds_write_b128 %[s0], v[200:203] offset:0\n\t" \
    "ds_write_b128 %[s1], v[0:3] offset:0\n\t" \
    "ds_write_b128 %[s2], v[8:11] offset:0\n\t" \
    "s_waitcnt vmcnt(18)\n\t" \
    "ds_write_b128 %[s0], v[16:19] offset:3456\n\t" \
    "ds_write_b128 %[s1], v[24:27] offset:3456\n\t" \
    "ds_write_b128 %[s2], v[32:35] offset:3456\n\t" \
    "s_waitcnt vmcnt(15)\n\t" \
    "ds_write_b128 %[s0], v[208:211] offset:6912\n\t" \
    "ds_write_b128 %[s1], v[40:43] offset:6912\n\t" \
    "ds_write_b128 %[s2], v[48:51] offset:6912\n\t" \
    "s_waitcnt vmcnt(12)\n\t" \
    "ds_write_b128 %[s0], v[56:59] offset:10368\n\t" \
    "ds_write_b128 %[s1], v[64:67] offset:10368\n\t" \
    "ds_write_b128 %[s2], v[72:75] offset:10368\n\t" \
    "s_waitcnt lgkmcnt(0)\n\t" \
    "s_barrier \n\t" \
    "ds_read_b64_tr_b16 v[200:201], %[t0] offset:0\n\t" \
    "ds_read_b64_tr_b16 v[202:203], %[t0] offset:1664\n\t" \
    "ds_read_b64_tr_b16 v[0:1], %[t0] offset:128\n\t" \
    "ds_read_b64_tr_b16 v[2:3], %[t0] offset:1792\n\t" \
    "ds_read_b64_tr_b16 v[8:9], %[t0] offset:256\n\t" \
    "ds_read_b64_tr_b16 v[10:11], %[t0] offset:1920\n\t" \
    "ds_read_b64_tr_b16 v[16:17], %[t1] offset:0\n\t" \
    "ds_read_b64_tr_b16 v[18:19], %[t1] offset:1664\n\t" \
    "ds_read_b64_tr_b16 v[24:25], %[t1] offset:128\n\t" \
    "ds_read_b64_tr_b16 v[26:27], %[t1] offset:1792\n\t" \
    "ds_read_b64_tr_b16 v[32:33], %[t1] offset:256\n\t" \
    "ds_read_b64_tr_b16 v[34:35], %[t1] offset:1920\n\t" \
    "ds_read_b64_tr_b16 v[208:209], %[t2] offset:0\n\t" \
    "ds_read_b64_tr_b16 v[210:211], %[t2] offset:1664\n\t" \
    "ds_read_b64_tr_b16 v[40:41], %[t2] offset:128\n\t" \
    "ds_read_b64_tr_b16 v[42:43], %[t2] offset:1792\n\t" \
    "ds_read_b64_tr_b16 v[48:49], %[t2] offset:256\n\t" \
    "ds_read_b64_tr_b16 v[50:51], %[t2] offset:1920\n\t" \
    "ds_read_b64_tr_b16 v[56:57], %[t3] offset:0\n\t" \
    "ds_read_b64_tr_b16 v[58:59], %[t3] offset:1664\n\t" \
    "ds_read_b64_tr_b16 v[64:65], %[t3] offset:128\n\t" \
    "ds_read_b64_tr_b16 v[66:67], %[t3] offset:1792\n\t" \
    "ds_read_b64_tr_b16 v[72:73], %[t3] offset:256\n\t" \
    "ds_read_b64_tr_b16 v[74:75], %[t3] offset:1920\n\t" \
    "s_xor_b32 %[t], %[wx], 4\n\t" \
    "s_mul_i32 %[t], %[t], 0xc0000\n\t" \
    "buffer_load_dwordx4 v[216:219], %[o0], %[rs], %[t] offen" WNT "\n\t" \
    "buffer_load_dwordx4 v[80:83], %[o1], %[rs], %[t] offen" WNT "\n\t" \
    "buffer_load_dwordx4 v[88:91], %[o2], %[rs], %[t] offen" WNT "\n\t" \
    "s_add_u32 %[t], %[t], 0x18000\n\t" \
    "buffer_load_dwordx4 v[96:99], %[o0], %[rs], %[t] offen" WNT "\n\t" \
    "buffer_load_dwordx4 v[104:107], %[o1], %[rs], %[t] offen" WNT "\n\t" \
    "buffer_load_dwordx4 v[112:115], %[o2], %[rs], %[t] offen" WNT "\n\t" \
    "s_add_u32 %[t], %[t], 0x18000\n\t" \
    "buffer_load_dwordx4 v[224:227], %[o0], %[rs], %[t] offen" WNT "\n\t" \
    "buffer_load_dwordx4 v[120:123], %[o1], %[rs], %[t] offen" WNT "\n\t" \
    "buffer_load_dwordx4 v[128:131], %[o2], %[rs], %[t] offen" WNT "\n\t" \
    "s_add_u32 %[t], %[t], 0x18000\n\t" \
    "buffer_load_dwordx4 v[136:139], %[o0], %[rs], %[t] offen" WNT "\n\t" \
    "buffer_load_dwordx4 v[144:147], %[o1], %[rs], %[t] offen" WNT "\n\t" \
    "buffer_load_dwordx4 v[152:155], %[o2], %[rs], %[t] offen" WNT "\n\t" \
    "s_add_u32 %[t], %[t], 0x18000\n\t" \
    "s_waitcnt lgkmcnt(0)\n\t" \
    "s_barrier \n\t" \
    "s_waitcnt vmcnt(21)\n\t" \
    "ds_write_b128 %[s0], v[204:207] offset:0\n\t" \
    "ds_write_b128 %[s1], v[4:7] offset:0\n\t" \
    "ds_write_b128 %[s2], v[12:15] offset:0\n\t" \
    "s_waitcnt vmcnt(18)\n\t" \
    "ds_write_b128 %[s0], v[20:23] offset:3456\n\t" \
    "ds_write_b128 %[s1], v[28:31] offset:3456\n\t" \
    "ds_write_b128 %[s2], v[36:39] offset:3456\n\t" \
    "s_waitcnt vmcnt(15)\n\t" \
    "ds_write_b128 %[s0], v[212:215] offset:6912\n\t" \
    "ds_write_b128 %[s1], v[44:47] offset:6912\n\t" \
    "ds_write_b128 %[s2], v[52:55] offset:6912\n\t" \
    "s_waitcnt vmcnt(12)\n\t" \
    "ds_write_b128 %[s0], v[60:63] offset:10368\n\t" \
    "ds_write_b128 %[s1], v[68:71] offset:10368\n\t" \
    "ds_write_b128 %[s2], v[76:79] offset:10368\n\t" \
    "s_waitcnt lgkmcnt(0)\n\t" \
    "s_barrier \n\t" \
    "ds_read_b64_tr_b16 v[204:205], %[t0] offset:0\n\t" \
    "ds_read_b64_tr_b16 v[206:207], %[t0] offset:1664\n\t" \
    "ds_read_b64_tr_b16 v[4:5], %[t0] offset:128\n\t" \
    "ds_read_b64_tr_b16 v[6:7], %[t0] offset:1792\n\t" \
    "ds_read_b64_tr_b16 v[12:13], %[t0] offset:256\n\t" \
    "ds_read_b64_tr_b16 v[14:15], %[t0] offset:1920\n\t" \
    "ds_read_b64_tr_b16 v[20:21], %[t1] offset:0\n\t" \
    "ds_read_b64_tr_b16 v[22:23], %[t1] offset:1664\n\t" \
    "ds_read_b64_tr_b16 v[28:29], %[t1] offset:128\n\t" \
    "ds_read_b64_tr_b16 v[30:31], %[t1] offset:1792\n\t" \
    "ds_read_b64_tr_b16 v[36:37], %[t1] offset:256\n\t" \
    "ds_read_b64_tr_b16 v[38:39], %[t1] offset:1920\n\t" \
    "ds_read_b64_tr_b16 v[212:213], %[t2] offset:0\n\t" \
    "ds_read_b64_tr_b16 v[214:215], %[t2] offset:1664\n\t" \
    "ds_read_b64_tr_b16 v[44:45], %[t2] offset:128\n\t" \
    "ds_read_b64_tr_b16 v[46:47], %[t2] offset:1792\n\t" \
    "ds_read_b64_tr_b16 v[52:53], %[t2] offset:256\n\t" \
    "ds_read_b64_tr_b16 v[54:55], %[t2] offset:1920\n\t" \
    "ds_read_b64_tr_b16 v[60:61], %[t3] offset:0\n\t" \
    "ds_read_b64_tr_b16 v[62:63], %[t3] offset:1664\n\t" \
    "ds_read_b64_tr_b16 v[68:69], %[t3] offset:128\n\t" \
    "ds_read_b64_tr_b16 v[70:71], %[t3] offset:1792\n\t" \
    "ds_read_b64_tr_b16 v[76:77], %[t3] offset:256\n\t" \
    "ds_read_b64_tr_b16 v[78:79], %[t3] offset:1920\n\t" \
    "buffer_load_dwordx4 v[220:223], %[o0], %[rs], %[t] offen" WNT "\n\t" \
    "buffer_load_dwordx4 v[84:87], %[o1], %[rs], %[t] offen" WNT "\n\t" \
    "buffer_load_dwordx4 v[92:95], %[o2], %[rs], %[t] offen" WNT "\n\t" \
    "s_add_u32 %[t], %[t], 0x18000\n\t" \
    "buffer_load_dwordx4 v[100:103], %[o0], %[rs], %[t] offen" WNT "\n\t" \
    "buffer_load_dwordx4 v[108:111], %[o1], %[rs], %[t] offen" WNT "\n\t" \
    "buffer_load_dwordx4 v[116:119], %[o2], %[rs], %[t] offen" WNT "\n\t" \
    "s_add_u32 %[t], %[t], 0x18000\n\t" \
    "buffer_load_dwordx4 v[228:231], %[o0], %[rs], %[t] offen" WNT "\n\t" \
    "buffer_load_dwordx4 v[124:127], %[o1], %[rs], %[t] offen" WNT "\n\t" \
    "buffer_load_dwordx4 v[132:135], %[o2], %[rs], %[t] offen" WNT "\n\t" \
    "s_add_u32 %[t], %[t], 0x18000\n\t" \
    "buffer_load_dwordx4 v[140:143], %[o0], %[rs], %[t] offen" WNT "\n\t" \
    "buffer_load_dwordx4 v[148:151], %[o1], %[rs], %[t] offen" WNT "\n\t" \
    "buffer_load_dwordx4 v[156:159], %[o2], %[rs], %[t] offen" WNT "\n\t" \
    "s_add_u32 %[t], %[t], 0x18000\n\t" \
    "s_waitcnt lgkmcnt(0)\n\t" \
    "s_barrier \n\t" \
    "s_waitcnt vmcnt(21)\n\t" \
    "ds_write_b128 %[s0], v[216:219] offset:0\n\t" \
    "ds_write_b128 %[s1], v[80:83] offset:0\n\t" \
    "ds_write_b128 %[s2], v[88:91] offset:0\n\t" \
    "s_waitcnt vmcnt(18)\n\t" \
    "ds_write_b128 %[s0], v[96:99] offset:3456\n\t" \
    "ds_write_b128 %[s1], v[104:107] offset:3456\n\t" \
    "ds_write_b128 %[s2], v[112:115] offset:3456\n\t" \
    "s_waitcnt vmcnt(15)\n\t" \
    "ds_write_b128 %[s0], v[224:227] offset:6912\n\t" \
    "ds_write_b128 %[s1], v[120:123] offset:6912\n\t" \
    "ds_write_b128 %[s2], v[128:131] offset:6912\n\t" \
    "s_waitcnt vmcnt(12)\n\t" \
    "ds_write_b128 %[s0], v[136:139] offset:10368\n\t" \
    "ds_write_b128 %[s1], v[144:147] offset:10368\n\t" \
    "ds_write_b128 %[s2], v[152:155] offset:10368\n\t" \
    "s_waitcnt lgkmcnt(0)\n\t" \
    "s_barrier \n\t" \
    "ds_read_b64_tr_b16 v[216:217], %[t0] offset:0\n\t" \
    "ds_read_b64_tr_b16 v[218:219], %[t0] offset:1664\n\t" \
    "ds_read_b64_tr_b16 v[80:81], %[t0] offset:128\n\t" \
    "ds_read_b64_tr_b16 v[82:83], %[t0] offset:1792\n\t" \
    "ds_read_b64_tr_b16 v[88:89], %[t0] offset:256\n\t" \
    "ds_read_b64_tr_b16 v[90:91], %[t0] offset:1920\n\t" \
    "ds_read_b64_tr_b16 v[96:97], %[t1] offset:0\n\t" \
    "ds_read_b64_tr_b16 v[98:99], %[t1] offset:1664\n\t" \
    "ds_read_b64_tr_b16 v[104:105], %[t1] offset:128\n\t" \
    "ds_read_b64_tr_b16 v[106:107], %[t1] offset:1792\n\t" \
    "ds_read_b64_tr_b16 v[112:113], %[t1] offset:256\n\t" \
    "ds_read_b64_tr_b16 v[114:115], %[t1] offset:1920\n\t" \
    "ds_read_b64_tr_b16 v[224:225], %[t2] offset:0\n\t" \
    "ds_read_b64_tr_b16 v[226:227], %[t2] offset:1664\n\t" \
    "ds_read_b64_tr_b16 v[120:121], %[t2] offset:128\n\t" \
    "ds_read_b64_tr_b16 v[122:123], %[t2] offset:1792\n\t" \
    "ds_read_b64_tr_b16 v[128:129], %[t2] offset:256\n\t" \
    "ds_read_b64_tr_b16 v[130:131], %[t2] offset:1920\n\t" \
    "ds_read_b64_tr_b16 v[136:137], %[t3] offset:0\n\t" \
    "ds_read_b64_tr_b16 v[138:139], %[t3] offset:1664\n\t" \
    "ds_read_b64_tr_b16 v[144:145], %[t3] offset:128\n\t" \
    "ds_read_b64_tr_b16 v[146:147], %[t3] offset:1792\n\t" \
    "ds_read_b64_tr_b16 v[152:153], %[t3] offset:256\n\t" \
    "ds_read_b64_tr_b16 v[154:155], %[t3] offset:1920\n\t" \
    "s_and_b32 %[t], %[wx], 3\n\t" \
    "s_mul_i32 %[t], %[t], 0x60000\n\t" \
    "s_add_u32 %[t], %[t], 0x600000\n\t" \
    "buffer_load_dwordx4 v[232:235], %[o0], %[rs], %[t] offen" WNT "\n\t" \
    "buffer_load_dwordx4 v[160:163], %[o1], %[rs], %[t] offen" WNT "\n\t" \
    "buffer_load_dwordx4 v[168:171], %[o2], %[rs], %[t] offen" WNT "\n\t" \
    "s_add_u32 %[t], %[t], 0x18000\n\t" \
    "buffer_load_dwordx4 v[236:239], %[o0], %[rs], %[t] offen" WNT "\n\t" \
    "buffer_load_dwordx4 v[164:167], %[o1], %[rs], %[t] offen" WNT "\n\t" \
    "buffer_load_dwordx4 v[172:175], %[o2], %[rs], %[t] offen" WNT "\n\t" \
    "s_add_u32 %[t], %[t], 0x18000\n\t" \
    "buffer_load_dwordx4 v[176:179], %[o0], %[rs], %[t] offen" WNT "\n\t" \
    "buffer_load_dwordx4 v[184:187], %[o1], %[rs], %[t] offen" WNT "\n\t" \
    "buffer_load_dwordx4 v[192:195], %[o2], %[rs], %[t] offen" WNT "\n\t" \
    "s_add_u32 %[t], %[t], 0x18000\n\t" \
    "buffer_load_dwordx4 v[180:183], %[o0], %[rs], %[t] offen" WNT "\n\t" \
    "buffer_load_dwordx4 v[188:191], %[o1], %[rs], %[t] offen" WNT "\n\t" \
    "buffer_load_dwordx4 v[196:199], %[o2], %[rs], %[t] offen" WNT "\n\t" \
    "s_add_u32 %[t], %[t], 0x18000\n\t" \
    "s_waitcnt lgkmcnt(0)\n\t" \
    "s_barrier \n\t" \
    "s_waitcnt vmcnt(21)\n\t" \
    "ds_write_b128 %[s0], v[220:223] offset:0\n\t" \
    "ds_write_b128 %[s1], v[84:87] offset:0\n\t" \
    "ds_write_b128 %[s2], v[92:95] offset:0\n\t" \
    "s_waitcnt vmcnt(18)\n\t" \
    "ds_write_b128 %[s0], v[100:103] offset:3456\n\t" \
    "ds_write_b128 %[s1], v[108:111] offset:3456\n\t" \
    "ds_write_b128 %[s2], v[116:119] offset:3456\n\t" \
    "s_waitcnt vmcnt(15)\n\t" \
    "ds_write_b128 %[s0], v[228:231] offset:6912\n\t" \
    "ds_write_b128 %[s1], v[124:127] offset:6912\n\t" \
    "ds_write_b128 %[s2], v[132:135] offset:6912\n\t" \
    "s_waitcnt vmcnt(12)\n\t" \
    "ds_write_b128 %[s0], v[140:143] offset:10368\n\t" \
    "ds_write_b128 %[s1], v[148:151] offset:10368\n\t" \
    "ds_write_b128 %[s2], v[156:159] offset:10368\n\t" \
    "s_waitcnt lgkmcnt(0)\n\t" \
    "s_barrier \n\t" \
    "ds_read_b64_tr_b16 v[220:221], %[t0] offset:0\n\t" \
    "ds_read_b64_tr_b16 v[222:223], %[t0] offset:1664\n\t" \
    "ds_read_b64_tr_b16 v[84:85], %[t0] offset:128\n\t" \
    "ds_read_b64_tr_b16 v[86:87], %[t0] offset:1792\n\t" \
    "ds_read_b64_tr_b16 v[92:93], %[t0] offset:256\n\t" \
    "ds_read_b64_tr_b16 v[94:95], %[t0] offset:1920\n\t" \
    "ds_read_b64_tr_b16 v[100:101], %[t1] offset:0\n\t" \
    "ds_read_b64_tr_b16 v[102:103], %[t1] offset:1664\n\t" \
    "ds_read_b64_tr_b16 v[108:109], %[t1] offset:128\n\t" \
    "ds_read_b64_tr_b16 v[110:111], %[t1] offset:1792\n\t" \
    "ds_read_b64_tr_b16 v[116:117], %[t1] offset:256\n\t" \
    "ds_read_b64_tr_b16 v[118:119], %[t1] offset:1920\n\t" \
    "ds_read_b64_tr_b16 v[228:229], %[t2] offset:0\n\t" \
    "ds_read_b64_tr_b16 v[230:231], %[t2] offset:1664\n\t" \
    "ds_read_b64_tr_b16 v[124:125], %[t2] offset:128\n\t" \
    "ds_read_b64_tr_b16 v[126:127], %[t2] offset:1792\n\t" \
    "ds_read_b64_tr_b16 v[132:133], %[t2] offset:256\n\t" \
    "ds_read_b64_tr_b16 v[134:135], %[t2] offset:1920\n\t" \
    "ds_read_b64_tr_b16 v[140:141], %[t3] offset:0\n\t" \
    "ds_read_b64_tr_b16 v[142:143], %[t3] offset:1664\n\t" \
    "ds_read_b64_tr_b16 v[148:149], %[t3] offset:128\n\t" \
    "ds_read_b64_tr_b16 v[150:151], %[t3] offset:1792\n\t" \
    "ds_read_b64_tr_b16 v[156:157], %[t3] offset:256\n\t" \
    "ds_read_b64_tr_b16 v[158:159], %[t3] offset:1920\n\t" \
    "s_waitcnt lgkmcnt(0)\n\t" \
    "s_barrier \n\t" \
    "s_waitcnt vmcnt(9)\n\t" \
    "ds_write_b128 %[s0], v[232:235] offset:0\n\t" \
    "ds_write_b128 %[s1], v[160:163] offset:0\n\t" \
    "ds_write_b128 %[s2], v[168:171] offset:0\n\t" \
    "s_waitcnt vmcnt(6)\n\t" \
    "ds_write_b128 %[s0], v[236:239] offset:3456\n\t" \
    "ds_write_b128 %[s1], v[164:167] offset:3456\n\t" \
    "ds_write_b128 %[s2], v[172:175] offset:3456\n\t" \
    "s_waitcnt vmcnt(3)\n\t" \
    "ds_write_b128 %[s0], v[176:179] offset:6912\n\t" \
    "ds_write_b128 %[s1], v[184:187] offset:6912\n\t" \
    "ds_write_b128 %[s2], v[192:195] offset:6912\n\t" \
    "s_waitcnt vmcnt(0)\n\t" \
    "ds_write_b128 %[s0], v[180:183] offset:10368\n\t" \
    "ds_write_b128 %[s1], v[188:191] offset:10368\n\t" \
    "ds_write_b128 %[s2], v[196:199] offset:10368\n\t" \
    "s_waitcnt lgkmcnt(0)\n\t" \
    "s_barrier \n\t" \
    "ds_read_b64_tr_b16 v[232:233], %[t4] offset:0\n\t" \
    "ds_read_b64_tr_b16 v[234:235], %[t4] offset:1664\n\t" \
    "ds_read_b64_tr_b16 v[160:161], %[t4] offset:128\n\t" \
    "ds_read_b64_tr_b16 v[162:163], %[t4] offset:1792\n\t" \
    "ds_read_b64_tr_b16 v[168:169], %[t4] offset:256\n\t" \
    "ds_read_b64_tr_b16 v[170:171], %[t4] offset:1920\n\t" \
    "ds_read_b64_tr_b16 v[236:237], %[t4] offset:13824\n\t" \
    "ds_read_b64_tr_b16 v[238:239], %[t4] offset:15488\n\t" \
    "ds_read_b64_tr_b16 v[164:165], %[t4] offset:13952\n\t" \
    "ds_read_b64_tr_b16 v[166:167], %[t4] offset:15616\n\t" \
    "ds_read_b64_tr_b16 v[172:173], %[t4] offset:14080\n\t" \
    "ds_read_b64_tr_b16 v[174:175], %[t4] offset:15744\n\t" \
    "ds_read_b64_tr_b16 v[176:177], %[t4] offset:27648\n\t" \
    "ds_read_b64_tr_b16 v[178:179], %[t4] offset:29312\n\t" \
    "ds_read_b64_tr_b16 v[184:185], %[t4] offset:27776\n\t" \
    "ds_read_b64_tr_b16 v[186:187], %[t4] offset:29440\n\t" \
    "ds_read_b64_tr_b16 v[192:193], %[t4] offset:27904\n\t" \
    "ds_read_b64_tr_b16 v[194:195], %[t4] offset:29568\n\t" \
    "ds_read_b64_tr_b16 v[180:181], %[t4] offset:41472\n\t" \
    "ds_read_b64_tr_b16 v[182:183], %[t4] offset:43136\n\t" \
    "ds_read_b64_tr_b16 v[188:189], %[t4] offset:41600\n\t" \
    "ds_read_b64_tr_b16 v[190:191], %[t4] offset:43264\n\t" \
    "ds_read_b64_tr_b16 v[196:197], %[t4] offset:41728\n\t" \
    "ds_read_b64_tr_b16 v[198:199], %[t4] offset:43392\n\t" \
    "s_waitcnt lgkmcnt(0)\n\t" \
    "s_barrier \n\t" \
    "ds_write_b128 %[wl], v[200:203] offset:0\n\t" \
    "ds_write_b128 %[wl], v[204:207] offset:1024\n\t" \
    "ds_write_b128 %[wl], v[208:211] offset:2048\n\t" \
    "ds_write_b128 %[wl], v[212:215] offset:3072\n\t" \
    "ds_write_b128 %[wl], v[216:219] offset:4096\n\t" \
    "ds_write_b128 %[wl], v[220:223] offset:5120\n\t" \
    "ds_write_b128 %[wl], v[224:227] offset:6144\n\t" \
    "ds_write_b128 %[wl], v[228:231] offset:7168\n\t" \
    "ds_write_b128 %[wl], v[232:235] offset:8192\n\t" \
    "ds_write_b128 %[wl], v[236:239] offset:9216\n\t" \
    "s_waitcnt lgkmcnt(0)\n\t"
#define ASM_WLOADO \
    "s_mov_b32 %[t], 0x0\n\t" \
    "buffer_load_dwordx4 v[200:203], %[o0], %[rs], %[t] offen" WNT "\n\t" \
    "buffer_load_dwordx4 v[16:19], %[o1], %[rs], %[t] offen" WNT "\n\t" \
    "buffer_load_dwordx4 v[208:211], %[o2], %[rs], %[t] offen" WNT "\n\t" \
    "buffer_load_dwordx4 v[56:59], %[o3], %[rs], %[t] offen" WNT "\n\t" \
    "buffer_load_dwordx4 v[216:219], %[o4], %[rs], %[t] offen" WNT "\n\t" \
    "s_mov_b32 %[t], 0xa000\n\t" \
    "buffer_load_dwordx4 v[96:99], %[o0], %[rs], %[t] offen" WNT "\n\t" \
    "buffer_load_dwordx4 v[224:227], %[o1], %[rs], %[t] offen" WNT "\n\t" \
    "buffer_load_dwordx4 v[136:139], %[o2], %[rs], %[t] offen" WNT "\n\t" \
    "buffer_load_dwordx4 v[232:235], %[o3], %[rs], %[t] offen" WNT "\n\t" \
    "buffer_load_dwordx4 v[176:179], %[o4], %[rs], %[t] offen" WNT "\n\t" \
    "s_mov_b32 %[t], 0x14000\n\t" \
    "buffer_load_dwordx4 v[204:207], %[o0], %[rs], %[t] offen" WNT "\n\t" \
    "buffer_load_dwordx4 v[20:23], %[o1], %[rs], %[t] offen" WNT "\n\t" \
    "buffer_load_dwordx4 v[212:215], %[o2], %[rs], %[t] offen" WNT "\n\t" \
    "buffer_load_dwordx4 v[60:63], %[o3], %[rs], %[t] offen" WNT "\n\t" \
    "buffer_load_dwordx4 v[220:223], %[o4], %[rs], %[t] offen" WNT "\n\t" \
    "s_mov_b32 %[t], 0x1e000\n\t" \
    "buffer_load_dwordx4 v[100:103], %[o0], %[rs], %[t] offen" WNT "\n\t" \
    "buffer_load_dwordx4 v[228:231], %[o1], %[rs], %[t] offen" WNT "\n\t" \
    "buffer_load_dwordx4 v[140:143], %[o2], %[rs], %[t] offen" WNT "\n\t" \
    "buffer_load_dwordx4 v[236:239], %[o3], %[rs], %[t] offen" WNT "\n\t" \
    "buffer_load_dwordx4 v[180:183], %[o4], %[rs], %[t] offen" WNT "\n\t" \
    "s_waitcnt vmcnt(15)\n\t" \
    "ds_write_b128 %[s0], v[200:203] offset:0\n\t" \
    "ds_write_b128 %[s1], v[16:19] offset:0\n\t" \
    "ds_write_b128 %[s2], v[208:211] offset:0\n\t" \
    "ds_write_b128 %[s3], v[56:59] offset:0\n\t" \
    "ds_write_b128 %[s4], v[216:219] offset:0\n\t" \
    "s_waitcnt vmcnt(10)\n\t" \
    "ds_write_b128 %[s0], v[96:99] offset:5632\n\t" \
    "ds_write_b128 %[s1], v[224:227] offset:5632\n\t" \
    "ds_write_b128 %[s2], v[136:139] offset:5632\n\t" \
    "ds_write_b128 %[s3], v[232:235] offset:5632\n\t" \
    "ds_write_b128 %[s4], v[176:179] offset:5632\n\t" \
    "ds_read_b64_tr_b16 v[200:201], %[tr] offset:0\n\t" \
    "ds_read_b64_tr_b16 v[202:203], %[tr] offset:5632\n\t" \
    "ds_read_b64_tr_b16 v[16:17], %[tr] offset:32\n\t" \
    "ds_read_b64_tr_b16 v[18:19], %[tr] offset:5664\n\t" \
    "ds_read_b64_tr_b16 v[208:209], %[tr] offset:64\n\t" \
    "ds_read_b64_tr_b16 v[210:211], %[tr] offset:5696\n\t" \
    "ds_read_b64_tr_b16 v[56:57], %[tr] offset:96\n\t" \
    "ds_read_b64_tr_b16 v[58:59], %[tr] offset:5728\n\t" \
    "ds_read_b64_tr_b16 v[216:217], %[tr] offset:128\n\t" \
    "ds_read_b64_tr_b16 v[218:219], %[tr] offset:5760\n\t" \
    "ds_read_b64_tr_b16 v[96:97], %[tr] offset:160\n\t" \
    "ds_read_b64_tr_b16 v[98:99], %[tr] offset:5792\n\t" \
    "ds_read_b64_tr_b16 v[224:225], %[tr] offset:192\n\t" \
    "ds_read_b64_tr_b16 v[226:227], %[tr] offset:5824\n\t" \
    "ds_read_b64_tr_b16 v[136:137], %[tr] offset:224\n\t" \
    "ds_read_b64_tr_b16 v[138:139], %[tr] offset:5856\n\t" \
    "ds_read_b64_tr_b16 v[232:233], %[tr] offset:256\n\t" \
    "ds_read_b64_tr_b16 v[234:235], %[tr] offset:5888\n\t" \
    "ds_read_b64_tr_b16 v[176:177], %[tr] offset:288\n\t" \
    "ds_read_b64_tr_b16 v[178:179], %[tr] offset:5920\n\t" \
    "s_barrier \n\t" \
    "s_mov_b32 %[t], 0x28000\n\t" \
    "buffer_load_dwordx4 v[0:3], %[o0], %[rs], %[t] offen" WNT "\n\t" \
    "buffer_load_dwordx4 v[24:27], %[o1], %[rs], %[t] offen" WNT "\n\t" \
    "buffer_load_dwordx4 v[40:43], %[o2], %[rs], %[t] offen" WNT "\n\t" \
    "buffer_load_dwordx4 v[64:67], %[o3], %[rs], %[t] offen" WNT "\n\t" \
    "buffer_load_dwordx4 v[80:83], %[o4], %[rs], %[t] offen" WNT "\n\t" \
    "s_mov_b32 %[t], 0x32000\n\t" \
    "buffer_load_dwordx4 v[104:107], %[o0], %[rs], %[t] offen" WNT "\n\t" \
    "buffer_load_dwordx4 v[120:123], %[o1], %[rs], %[t] offen" WNT "\n\t" \
    "buffer_load_dwordx4 v[144:147], %[o2], %[rs], %[t] offen" WNT "\n\t" \
    "buffer_load_dwordx4 v[160:163], %[o3], %[rs], %[t] offen" WNT "\n\t" \
    "buffer_load_dwordx4 v[184:187], %[o4], %[rs], %[t] offen" WNT "\n\t" \
    "s_waitcnt lgkmcnt(0)\n\t" \
    "s_barrier \n\t" \
    "s_waitcnt vmcnt(15)\n\t" \
    "ds_write_b128 %[s0], v[204:207] offset:0\n\t" \
    "ds_write_b128 %[s1], v[20:23] offset:0\n\t" \
    "ds_write_b128 %[s2], v[212:215] offset:0\n\t" \
    "ds_write_b128 %[s3], v[60:63] offset:0\n\t" \
    "ds_write_b128 %[s4], v[220:223] offset:0\n\t" \
    "s_waitcnt vmcnt(10)\n\t" \
    "ds_write_b128 %[s0], v[100:103] offset:5632\n\t" \
    "ds_write_b128 %[s1], v[228:231] offset:5632\n\t" \
    "ds_write_b128 %[s2], v[140:143] offset:5632\n\t" \
    "ds_write_b128 %[s3], v[236:239] offset:5632\n\t" \
    "ds_write_b128 %[s4], v[180:183] offset:5632\n\t" \
    "ds_read_b64_tr_b16 v[204:205], %[tr] offset:0\n\t" \
    "ds_read_b64_tr_b16 v[206:207], %[tr] offset:5632\n\t" \
    "ds_read_b64_tr_b16 v[20:21], %[tr] offset:32\n\t" \
    "ds_read_b64_tr_b16 v[22:23], %[tr] offset:5664\n\t" \
    "ds_read_b64_tr_b16 v[212:213], %[tr] offset:64\n\t" \
    "ds_read_b64_tr_b16 v[214:215], %[tr] offset:5696\n\t" \
    "ds_read_b64_tr_b16 v[60:61], %[tr] offset:96\n\t" \
    "ds_read_b64_tr_b16 v[62:63], %[tr] offset:5728\n\t" \
    "ds_read_b64_tr_b16 v[220:221], %[tr] offset:128\n\t" \
    "ds_read_b64_tr_b16 v[222:223], %[tr] offset:5760\n\t" \
    "ds_read_b64_tr_b16 v[100:101], %[tr] offset:160\n\t" \
    "ds_read_b64_tr_b16 v[102:103], %[tr] offset:5792\n\t" \
    "ds_read_b64_tr_b16 v[228:229], %[tr] offset:192\n\t" \
    "ds_read_b64_tr_b16 v[230:231], %[tr] offset:5824\n\t" \
    "ds_read_b64_tr_b16 v[140:141], %[tr] offset:224\n\t" \
    "ds_read_b64_tr_b16 v[142:143], %[tr] offset:5856\n\t" \
    "ds_read_b64_tr_b16 v[236:237], %[tr] offset:256\n\t" \
    "ds_read_b64_tr_b16 v[238:239], %[tr] offset:5888\n\t" \
    "ds_read_b64_tr_b16 v[180:181], %[tr] offset:288\n\t" \
    "ds_read_b64_tr_b16 v[182:183], %[tr] offset:5920\n\t" \
    "s_barrier \n\t" \
    "s_mov_b32 %[t], 0x5000\n\t" \
    "buffer_load_dwordx4 v[4:7], %[o0], %[rs], %[t] offen" WNT "\n\t" \
    "buffer_load_dwordx4 v[28:31], %[o1], %[rs], %[t] offen" WNT "\n\t" \
    "buffer_load_dwordx4 v[44:47], %[o2], %[rs], %[t] offen" WNT "\n\t" \
    "buffer_load_dwordx4 v[68:71], %[o3], %[rs], %[t] offen" WNT "\n\t" \
    "buffer_load_dwordx4 v[84:87], %[o4], %[rs], %[t] offen" WNT "\n\t" \
    "s_mov_b32 %[t], 0xf000\n\t" \
    "buffer_load_dwordx4 v[108:111], %[o0], %[rs], %[t] offen" WNT "\n\t" \
    "buffer_load_dwordx4 v[124:127], %[o1], %[rs], %[t] offen" WNT "\n\t" \
    "buffer_load_dwordx4 v[148:151], %[o2], %[rs], %[t] offen" WNT "\n\t" \
    "buffer_load_dwordx4 v[164:167], %[o3], %[rs], %[t] offen" WNT "\n\t" \
    "buffer_load_dwordx4 v[188:191], %[o4], %[rs], %[t] offen" WNT "\n\t" \
    "s_waitcnt lgkmcnt(0)\n\t" \
    "s_barrier \n\t" \
    "s_waitcnt vmcnt(15)\n\t" \
    "ds_write_b128 %[s0], v[0:3] offset:0\n\t" \
    "ds_write_b128 %[s1], v[24:27] offset:0\n\t" \
    "ds_write_b128 %[s2], v[40:43] offset:0\n\t" \
    "ds_write_b128 %[s3], v[64:67] offset:0\n\t" \
    "ds_write_b128 %[s4], v[80:83] offset:0\n\t" \
    "s_waitcnt vmcnt(10)\n\t" \
    "ds_write_b128 %[s0], v[104:107] offset:5632\n\t" \
    "ds_write_b128 %[s1], v[120:123] offset:5632\n\t" \
    "ds_write_b128 %[s2], v[144:147] offset:5632\n\t" \
    "ds_write_b128 %[s3], v[160:163] offset:5632\n\t" \
    "ds_write_b128 %[s4], v[184:187] offset:5632\n\t" \
    "ds_read_b64_tr_b16 v[0:1], %[tr] offset:0\n\t" \
    "ds_read_b64_tr_b16 v[2:3], %[tr] offset:5632\n\t" \
    "ds_read_b64_tr_b16 v[24:25], %[tr] offset:32\n\t" \
    "ds_read_b64_tr_b16 v[26:27], %[tr] offset:5664\n\t" \
    "ds_read_b64_tr_b16 v[40:41], %[tr] offset:64\n\t" \
    "ds_read_b64_tr_b16 v[42:43], %[tr] offset:5696\n\t" \
    "ds_read_b64_tr_b16 v[64:65], %[tr] offset:96\n\t" \
    "ds_read_b64_tr_b16 v[66:67], %[tr] offset:5728\n\t" \
    "ds_read_b64_tr_b16 v[80:81], %[tr] offset:128\n\t" \
    "ds_read_b64_tr_b16 v[82:83], %[tr] offset:5760\n\t" \
    "ds_read_b64_tr_b16 v[104:105], %[tr] offset:160\n\t" \
    "ds_read_b64_tr_b16 v[106:107], %[tr] offset:5792\n\t" \
    "ds_read_b64_tr_b16 v[120:121], %[tr] offset:192\n\t" \
    "ds_read_b64_tr_b16 v[122:123], %[tr] offset:5824\n\t" \
    "ds_read_b64_tr_b16 v[144:145], %[tr] offset:224\n\t" \
    "ds_read_b64_tr_b16 v[146:147], %[tr] offset:5856\n\t" \
    "ds_read_b64_tr_b16 v[160:161], %[tr] offset:256\n\t" \
    "ds_read_b64_tr_b16 v[162:163], %[tr] offset:5888\n\t" \
    "ds_read_b64_tr_b16 v[184:185], %[tr] offset:288\n\t" \
    "ds_read_b64_tr_b16 v[186:187], %[tr] offset:5920\n\t" \
    "s_barrier \n\t" \
    "s_mov_b32 %[t], 0x19000\n\t" \
    "buffer_load_dwordx4 v[8:11], %[o0], %[rs], %[t] offen" WNT "\n\t" \
    "buffer_load_dwordx4 v[32:35], %[o1], %[rs], %[t] offen" WNT "\n\t" \
    "buffer_load_dwordx4 v[48:51], %[o2], %[rs], %[t] offen" WNT "\n\t" \
    "buffer_load_dwordx4 v[72:75], %[o3], %[rs], %[t] offen" WNT "\n\t" \
    "buffer_load_dwordx4 v[88:91], %[o4], %[rs], %[t] offen" WNT "\n\t" \
    "s_mov_b32 %[t], 0x23000\n\t" \
    "buffer_load_dwordx4 v[112:115], %[o0], %[rs], %[t] offen" WNT "\n\t" \
    "buffer_load_dwordx4 v[128:131], %[o1], %[rs], %[t] offen" WNT "\n\t" \
    "buffer_load_dwordx4 v[152:155], %[o2], %[rs], %[t] offen" WNT "\n\t" \
    "buffer_load_dwordx4 v[168:171], %[o3], %[rs], %[t] offen" WNT "\n\t" \
    "buffer_load_dwordx4 v[192:195], %[o4], %[rs], %[t] offen" WNT "\n\t" \
    "s_waitcnt lgkmcnt(0)\n\t" \
    "s_barrier \n\t" \
    "s_waitcnt vmcnt(15)\n\t" \
    "ds_write_b128 %[s0], v[4:7] offset:0\n\t" \
    "ds_write_b128 %[s1], v[28:31] offset:0\n\t" \
    "ds_write_b128 %[s2], v[44:47] offset:0\n\t" \
    "ds_write_b128 %[s3], v[68:71] offset:0\n\t" \
    "ds_write_b128 %[s4], v[84:87] offset:0\n\t" \
    "s_waitcnt vmcnt(10)\n\t" \
    "ds_write_b128 %[s0], v[108:111] offset:5632\n\t" \
    "ds_write_b128 %[s1], v[124:127] offset:5632\n\t" \
    "ds_write_b128 %[s2], v[148:151] offset:5632\n\t" \
    "ds_write_b128 %[s3], v[164:167] offset:5632\n\t" \
    "ds_write_b128 %[s4], v[188:191] offset:5632\n\t" \
    "ds_read_b64_tr_b16 v[4:5], %[tr] offset:0\n\t" \
    "ds_read_b64_tr_b16 v[6:7], %[tr] offset:5632\n\t" \
    "ds_read_b64_tr_b16 v[28:29], %[tr] offset:32\n\t" \
    "ds_read_b64_tr_b16 v[30:31], %[tr] offset:5664\n\t" \
    "ds_read_b64_tr_b16 v[44:45], %[tr] offset:64\n\t" \
    "ds_read_b64_tr_b16 v[46:47], %[tr] offset:5696\n\t" \
    "ds_read_b64_tr_b16 v[68:69], %[tr] offset:96\n\t" \
    "ds_read_b64_tr_b16 v[70:71], %[tr] offset:5728\n\t" \
    "ds_read_b64_tr_b16 v[84:85], %[tr] offset:128\n\t" \
    "ds_read_b64_tr_b16 v[86:87], %[tr] offset:5760\n\t" \
    "ds_read_b64_tr_b16 v[108:109], %[tr] offset:160\n\t" \
    "ds_read_b64_tr_b16 v[110:111], %[tr] offset:5792\n\t" \
    "ds_read_b64_tr_b16 v[124:125], %[tr] offset:192\n\t" \
    "ds_read_b64_tr_b16 v[126:127], %[tr] offset:5824\n\t" \
    "ds_read_b64_tr_b16 v[148:149], %[tr] offset:224\n\t" \
    "ds_read_b64_tr_b16 v[150:151], %[tr] offset:5856\n\t" \
    "ds_read_b64_tr_b16 v[164:165], %[tr] offset:256\n\t" \
    "ds_read_b64_tr_b16 v[166:167], %[tr] offset:5888\n\t" \
    "ds_read_b64_tr_b16 v[188:189], %[tr] offset:288\n\t" \
    "ds_read_b64_tr_b16 v[190:191], %[tr] offset:5920\n\t" \
    "s_barrier \n\t" \
    "s_mov_b32 %[t], 0x2d000\n\t" \
    "buffer_load_dwordx4 v[12:15], %[o0], %[rs], %[t] offen" WNT "\n\t" \
    "buffer_load_dwordx4 v[36:39], %[o1], %[rs], %[t] offen" WNT "\n\t" \
    "buffer_load_dwordx4 v[52:55], %[o2], %[rs], %[t] offen" WNT "\n\t" \
    "buffer_load_dwordx4 v[76:79], %[o3], %[rs], %[t] offen" WNT "\n\t" \
    "buffer_load_dwordx4 v[92:95], %[o4], %[rs], %[t] offen" WNT "\n\t" \
    "s_mov_b32 %[t], 0x37000\n\t" \
    "buffer_load_dwordx4 v[116:119], %[o0], %[rs], %[t] offen" WNT "\n\t" \
    "buffer_load_dwordx4 v[132:135], %[o1], %[rs], %[t] offen" WNT "\n\t" \
    "buffer_load_dwordx4 v[156:159], %[o2], %[rs], %[t] offen" WNT "\n\t" \
    "buffer_load_dwordx4 v[172:175], %[o3], %[rs], %[t] offen" WNT "\n\t" \
    "buffer_load_dwordx4 v[196:199], %[o4], %[rs], %[t] offen" WNT "\n\t" \
    "s_waitcnt lgkmcnt(0)\n\t" \
    "s_barrier \n\t" \
    "s_waitcnt vmcnt(15)\n\t" \
    "ds_write_b128 %[s0], v[8:11] offset:0\n\t" \
    "ds_write_b128 %[s1], v[32:35] offset:0\n\t" \
    "ds_write_b128 %[s2], v[48:51] offset:0\n\t" \
    "ds_write_b128 %[s3], v[72:75] offset:0\n\t" \
    "ds_write_b128 %[s4], v[88:91] offset:0\n\t" \
    "s_waitcnt vmcnt(10)\n\t" \
    "ds_write_b128 %[s0], v[112:115] offset:5632\n\t" \
    "ds_write_b128 %[s1], v[128:131] offset:5632\n\t" \
    "ds_write_b128 %[s2], v[152:155] offset:5632\n\t" \
    "ds_write_b128 %[s3], v[168:171] offset:5632\n\t" \
    "ds_write_b128 %[s4], v[192:195] offset:5632\n\t" \
    "ds_read_b64_tr_b16 v[8:9], %[tr] offset:0\n\t" \
    "ds_read_b64_tr_b16 v[10:11], %[tr] offset:5632\n\t" \
    "ds_read_b64_tr_b16 v[32:33], %[tr] offset:32\n\t" \
    "ds_read_b64_tr_b16 v[34:35], %[tr] offset:5664\n\t" \
    "ds_read_b64_tr_b16 v[48:49], %[tr] offset:64\n\t" \
    "ds_read_b64_tr_b16 v[50:51], %[tr] offset:5696\n\t" \
    "ds_read_b64_tr_b16 v[72:73], %[tr] offset:96\n\t" \
    "ds_read_b64_tr_b16 v[74:75], %[tr] offset:5728\n\t" \
    "ds_read_b64_tr_b16 v[88:89], %[tr] offset:128\n\t" \
    "ds_read_b64_tr_b16 v[90:91], %[tr] offset:5760\n\t" \
    "ds_read_b64_tr_b16 v[112:113], %[tr] offset:160\n\t" \
    "ds_read_b64_tr_b16 v[114:115], %[tr] offset:5792\n\t" \
    "ds_read_b64_tr_b16 v[128:129], %[tr] offset:192\n\t" \
    "ds_read_b64_tr_b16 v[130:131], %[tr] offset:5824\n\t" \
    "ds_read_b64_tr_b16 v[152:153], %[tr] offset:224\n\t" \
    "ds_read_b64_tr_b16 v[154:155], %[tr] offset:5856\n\t" \
    "ds_read_b64_tr_b16 v[168:169], %[tr] offset:256\n\t" \
    "ds_read_b64_tr_b16 v[170:171], %[tr] offset:5888\n\t" \
    "ds_read_b64_tr_b16 v[192:193], %[tr] offset:288\n\t" \
    "ds_read_b64_tr_b16 v[194:195], %[tr] offset:5920\n\t" \
    "s_waitcnt lgkmcnt(0)\n\t" \
    "s_barrier \n\t" \
    "s_waitcnt vmcnt(5)\n\t" \
    "ds_write_b128 %[s0], v[12:15] offset:0\n\t" \
    "ds_write_b128 %[s1], v[36:39] offset:0\n\t" \
    "ds_write_b128 %[s2], v[52:55] offset:0\n\t" \
    "ds_write_b128 %[s3], v[76:79] offset:0\n\t" \
    "ds_write_b128 %[s4], v[92:95] offset:0\n\t" \
    "s_waitcnt vmcnt(0)\n\t" \
    "ds_write_b128 %[s0], v[116:119] offset:5632\n\t" \
    "ds_write_b128 %[s1], v[132:135] offset:5632\n\t" \
    "ds_write_b128 %[s2], v[156:159] offset:5632\n\t" \
    "ds_write_b128 %[s3], v[172:175] offset:5632\n\t" \
    "ds_write_b128 %[s4], v[196:199] offset:5632\n\t" \
    "ds_read_b64_tr_b16 v[12:13], %[tr] offset:0\n\t" \
    "ds_read_b64_tr_b16 v[14:15], %[tr] offset:5632\n\t" \
    "ds_read_b64_tr_b16 v[36:37], %[tr] offset:32\n\t" \
    "ds_read_b64_tr_b16 v[38:39], %[tr] offset:5664\n\t" \
    "ds_read_b64_tr_b16 v[52:53], %[tr] offset:64\n\t" \
    "ds_read_b64_tr_b16 v[54:55], %[tr] offset:5696\n\t" \
    "ds_read_b64_tr_b16 v[76:77], %[tr] offset:96\n\t" \
    "ds_read_b64_tr_b16 v[78:79], %[tr] offset:5728\n\t" \
    "ds_read_b64_tr_b16 v[92:93], %[tr] offset:128\n\t" \
    "ds_read_b64_tr_b16 v[94:95], %[tr] offset:5760\n\t" \
    "ds_read_b64_tr_b16 v[116:117], %[tr] offset:160\n\t" \
    "ds_read_b64_tr_b16 v[118:119], %[tr] offset:5792\n\t" \
    "ds_read_b64_tr_b16 v[132:133], %[tr] offset:192\n\t" \
    "ds_read_b64_tr_b16 v[134:135], %[tr] offset:5824\n\t" \
    "ds_read_b64_tr_b16 v[156:157], %[tr] offset:224\n\t" \
    "ds_read_b64_tr_b16 v[158:159], %[tr] offset:5856\n\t" \
    "ds_read_b64_tr_b16 v[172:173], %[tr] offset:256\n\t" \
    "ds_read_b64_tr_b16 v[174:175], %[tr] offset:5888\n\t" \
    "ds_read_b64_tr_b16 v[196:197], %[tr] offset:288\n\t" \
    "ds_read_b64_tr_b16 v[198:199], %[tr] offset:5920\n\t" \
    "s_waitcnt lgkmcnt(0)\n\t" \
    "s_barrier \n\t" \
    "ds_write_b128 %[wl], v[200:203] offset:0\n\t" \
    "ds_write_b128 %[wl], v[204:207] offset:1024\n\t" \
    "ds_write_b128 %[wl], v[208:211] offset:2048\n\t" \
    "ds_write_b128 %[wl], v[212:215] offset:3072\n\t" \
    "ds_write_b128 %[wl], v[216:219] offset:4096\n\t" \
    "ds_write_b128 %[wl], v[220:223] offset:5120\n\t" \
    "ds_write_b128 %[wl], v[224:227] offset:6144\n\t" \
    "ds_write_b128 %[wl], v[228:231] offset:7168\n\t" \
    "ds_write_b128 %[wl], v[232:235] offset:8192\n\t" \
    "ds_write_b128 %[wl], v[236:239] offset:9216\n\t" \
    "s_waitcnt lgkmcnt(0)\n\t"
#define ASM_XG_S_LO \
    "s_waitcnt lgkmcnt(0)\n\t" \
    "ds_read_b128 v[224:227], %[xa] offset:0\n\t" \
    "ds_read_b128 v[244:247], %[wl] offset:0\n\t" \
    "ds_read_b128 v[228:231], %[xa] offset:16384\n\t" \
    "ds_read_b128 v[232:235], %[xa] offset:1024\n\t" \
    "ds_read_b128 v[236:239], %[xa] offset:17408\n\t" \
    "ds_read_b128 v[248:251], %[wl] offset:1024\n\t" \
    "s_waitcnt lgkmcnt(5)\n\t" \
    "v_mfma_f32_16x16x32_bf16 v[204:207], v[0:3], v[224:227], 0\n\t" \
    "v_xor_b32 v252, 0x800, %[xa]\n\t" \
    "ds_read_b128 v[240:243], v252 offset:0\n\t" \
    "v_mfma_f32_16x16x32_bf16 v[208:211], v[8:11], v[224:227], 0\n\t" \
    "s_waitcnt lgkmcnt(5)\n\t" \
    "v_mfma_f32_16x16x32_bf16 v[200:203], v[244:247], v[224:227], 0\n\t" \
    "s_waitcnt lgkmcnt(4)\n\t" \
    "v_mfma_f32_16x16x32_bf16 v[216:219], v[0:3], v[228:231], 0\n\t" \
    "ds_read_b128 v[224:227], v252 offset:16384\n\t" \
    "v_mfma_f32_16x16x32_bf16 v[220:223], v[8:11], v[228:231], 0\n\t" \
    "v_mfma_f32_16x16x32_bf16 v[212:215], v[244:247], v[228:231], 0\n\t" \
    "s_waitcnt lgkmcnt(4)\n\t" \
    "v_mfma_f32_16x16x32_bf16 v[204:207], v[4:7], v[232:235], v[204:207]\n\t" \
    "ds_read_b128 v[228:231], v252 offset:1024\n\t" \
    "v_mfma_f32_16x16x32_bf16 v[208:211], v[12:15], v[232:235], v[208:211]\n\t" \
    "ds_read_b128 v[244:247], %[wl] offset:2048\n\t" \
    "s_waitcnt lgkmcnt(4)\n\t" \
    "v_mfma_f32_16x16x32_bf16 v[200:203], v[248:251], v[232:235], v[200:203]\n\t" \
    "v_mfma_f32_16x16x32_bf16 v[216:219], v[4:7], v[236:239], v[216:219]\n\t" \
    "ds_read_b128 v[232:235], v252 offset:17408\n\t" \
    "v_mfma_f32_16x16x32_bf16 v[220:223], v[12:15], v[236:239], v[220:223]\n\t" \
    "v_mfma_f32_16x16x32_bf16 v[212:215], v[248:251], v[236:239], v[212:215]\n\t" \
    "s_waitcnt lgkmcnt(4)\n\t" \
    "v_mfma_f32_16x16x32_bf16 v[204:207], v[24:27], v[240:243], v[204:207]\n\t" \
    "v_xor_b32 v252, 0x1000, %[xa]\n\t" \
    "ds_read_b128 v[236:239], v252 offset:0\n\t" \
    "v_mfma_f32_16x16x32_bf16 v[208:211], v[32:35], v[240:243], v[208:211]\n\t" \
    "ds_read_b128 v[248:251], %[wl] offset:3072\n\t" \
    "v_mfma_f32_16x16x32_bf16 v[200:203], v[16:19], v[240:243], v[200:203]\n\t" \
    "s_waitcnt lgkmcnt(5)\n\t" \
    "v_mfma_f32_16x16x32_bf16 v[216:219], v[24:27], v[224:227], v[216:219]\n\t" \
    "ds_read_b128 v[240:243], v252 offset:16384\n\t" \
    "v_mfma_f32_16x16x32_bf16 v[220:223], v[32:35], v[224:227], v[220:223]\n\t" \
    "v_mfma_f32_16x16x32_bf16 v[212:215], v[16:19], v[224:227], v[212:215]\n\t" \
    "s_waitcnt lgkmcnt(5)\n\t" \
    "v_mfma_f32_16x16x32_bf16 v[204:207], v[28:31], v[228:231], v[204:207]\n\t" \
    "ds_read_b128 v[224:227], v252 offset:1024\n\t" \
    "v_mfma_f32_16x16x32_bf16 v[208:211], v[36:39], v[228:231], v[208:211]\n\t" \
    "v_mfma_f32_16x16x32_bf16 v[200:203], v[20:23], v[228:231], v[200:203]\n\t" \
    "s_waitcnt lgkmcnt(4)\n\t" \
    "v_mfma_f32_16x16x32_bf16 v[216:219], v[28:31], v[232:235], v[216:219]\n\t" \
    "ds_read_b128 v[228:231], v252 offset:17408\n\t" \
    "v_mfma_f32_16x16x32_bf16 v[220:223], v[36:39], v[232:235], v[220:223]\n\t" \
    "v_mfma_f32_16x16x32_bf16 v[212:215], v[20:23], v[232:235], v[212:215]\n\t" \
    "s_waitcnt lgkmcnt(4)\n\t" \
    "v_mfma_f32_16x16x32_bf16 v[204:207], v[40:43], v[236:239], v[204:207]\n\t" \
    "v_xor_b32 v252, 0x1800, %[xa]\n\t" \
    "ds_read_b128 v[232:235], v252 offset:0\n\t" \
    "v_mfma_f32_16x16x32_bf16 v[208:211], v[48:51], v[236:239], v[208:211]\n\t" \
    "v_mfma_f32_16x16x32_bf16 v[200:203], v[244:247], v[236:239], v[200:203]\n\t" \
    "s_waitcnt lgkmcnt(3)\n\t" \
    "v_mfma_f32_16x16x32_bf16 v[216:219], v[40:43], v[240:243], v[216:219]\n\t" \
    "ds_read_b128 v[236:239], v252 offset:16384\n\t" \
    "v_mfma_f32_16x16x32_bf16 v[220:223], v[48:51], v[240:243], v[220:223]\n\t" \
    "v_mfma_f32_16x16x32_bf16 v[212:215], v[244:247], v[240:243], v[212:215]\n\t" \
    "s_waitcnt lgkmcnt(3)\n\t" \
    "v_mfma_f32_16x16x32_bf16 v[204:207], v[44:47], v[224:227], v[204:207]\n\t" \
    "ds_read_b128 v[240:243], v252 offset:1024\n\t" \
    "v_mfma_f32_16x16x32_bf16 v[208:211], v[52:55], v[224:227], v[208:211]\n\t" \
    "ds_read_b128 v[244:247], %[wl] offset:8192\n\t" \
    "v_mfma_f32_16x16x32_bf16 v[200:203], v[248:251], v[224:227], v[200:203]\n\t" \
    "s_waitcnt lgkmcnt(4)\n\t" \
    "v_mfma_f32_16x16x32_bf16 v[216:219], v[44:47], v[228:231], v[216:219]\n\t" \
    "ds_read_b128 v[224:227], v252 offset:17408\n\t" \
    "v_mfma_f32_16x16x32_bf16 v[220:223], v[52:55], v[228:231], v[220:223]\n\t" \
    "v_mfma_f32_16x16x32_bf16 v[212:215], v[248:251], v[228:231], v[212:215]\n\t" \
    "s_waitcnt lgkmcnt(4)\n\t" \
    "v_mfma_f32_16x16x32_bf16 v[204:207], v[64:67], v[232:235], v[204:207]\n\t" \
    "v_and_b32 v252, 0x3ff, %[xa]\n\t" \
    "ds_read_b128 v[228:231], v252 offset:40960\n\t" \
    "v_mfma_f32_16x16x32_bf16 v[208:211], v[72:75], v[232:235], v[208:211]\n\t" \
    "ds_read_b128 v[248:251], %[wl] offset:9216\n\t" \
    "v_mfma_f32_16x16x32_bf16 v[200:203], v[56:59], v[232:235], v[200:203]\n\t" \
    "s_waitcnt lgkmcnt(5)\n\t" \
    "v_mfma_f32_16x16x32_bf16 v[216:219], v[64:67], v[236:239], v[216:219]\n\t" \
    "ds_read_b128 v[232:235], v252 offset:45056\n\t" \
    "v_mfma_f32_16x16x32_bf16 v[220:223], v[72:75], v[236:239], v[220:223]\n\t" \
    "v_mfma_f32_16x16x32_bf16 v[212:215], v[56:59], v[236:239], v[212:215]\n\t" \
    "s_waitcnt lgkmcnt(5)\n\t" \
    "v_mfma_f32_16x16x32_bf16 v[204:207], v[68:71], v[240:243], v[204:207]\n\t" \
    "ds_read_b128 v[236:239], v252 offset:41984\n\t" \
    "v_mfma_f32_16x16x32_bf16 v[208:211], v[76:79], v[240:243], v[208:211]\n\t" \
    "v_mfma_f32_16x16x32_bf16 v[200:203], v[60:63], v[240:243], v[200:203]\n\t" \
    "s_waitcnt lgkmcnt(4)\n\t" \
    "v_mfma_f32_16x16x32_bf16 v[216:219], v[68:71], v[224:227], v[216:219]\n\t" \
    "ds_read_b128 v[240:243], v252 offset:46080\n\t" \
    "v_mfma_f32_16x16x32_bf16 v[220:223], v[76:79], v[224:227], v[220:223]\n\t" \
    "v_mfma_f32_16x16x32_bf16 v[212:215], v[60:63], v[224:227], v[212:215]\n\t" \
    "s_waitcnt lgkmcnt(0)\n\t" \
    "s_barrier\n\t" ASM_ST \
    "v_mfma_f32_16x16x32_bf16 v[204:207], v[160:163], v[228:231], v[204:207]\n\t" \
    "v_xor_b32 v252, 0x2000, %[xa]\n\t" \
    "ds_read_b128 v[224:227], v252 offset:0\n\t" \
    "v_mfma_f32_16x16x32_bf16 v[208:211], v[168:171], v[228:231], v[208:211]\n\t" \
    "v_mfma_f32_16x16x32_bf16 v[200:203], v[244:247], v[228:231], v[200:203]\n\t" \
    "v_mfma_f32_16x16x32_bf16 v[216:219], v[160:163], v[232:235], v[216:219]\n\t" \
    "ds_read_b128 v[228:231], v252 offset:16384\n\t" \
    "v_mfma_f32_16x16x32_bf16 v[220:223], v[168:171], v[232:235], v[220:223]\n\t" \
    "v_mfma_f32_16x16x32_bf16 v[212:215], v[244:247], v[232:235], v[212:215]\n\t" \
    "v_mfma_f32_16x16x32_bf16 v[204:207], v[164:167], v[236:239], v[204:207]\n\t" \
    "ds_read_b128 v[232:235], v252 offset:1024\n\t" \
    "v_mfma_f32_16x16x32_bf16 v[208:211], v[172:175], v[236:239], v[208:211]\n\t" \
    "ds_read_b128 v[244:247], %[wl] offset:4096\n\t" \
    "v_mfma_f32_16x16x32_bf16 v[200:203], v[248:251], v[236:239], v[200:203]\n\t" \
    "v_mfma_f32_16x16x32_bf16 v[216:219], v[164:167], v[240:243], v[216:219]\n\t" \
    "ds_read_b128 v[236:239], v252 offset:17408\n\t" \
    "v_mfma_f32_16x16x32_bf16 v[220:223], v[172:175], v[240:243], v[220:223]\n\t" \
    "v_mfma_f32_16x16x32_bf16 v[212:215], v[248:251], v[240:243], v[212:215]\n\t" \
    "s_waitcnt lgkmcnt(4)\n\t" \
    "v_mfma_f32_16x16x32_bf16 v[204:207], v[80:83], v[224:227], v[204:207]\n\t" \
    "v_xor_b32 v252, 0x2800, %[xa]\n\t" \
    "ds_read_b128 v[240:243], v252 offset:0\n\t" \
    "v_mfma_f32_16x16x32_bf16 v[208:211], v[88:91], v[224:227], v[208:211]\n\t" \
    "ds_read_b128 v[248:251], %[wl] offset:5120\n\t" \
    "s_waitcnt lgkmcnt(3)\n\t" \
    "v_mfma_f32_16x16x32_bf16 v[200:203], v[244:247], v[224:227], v[200:203]\n\t" \
    "v_mfma_f32_16x16x32_bf16 v[216:219], v[80:83], v[228:231], v[216:219]\n\t" \
    "ds_read_b128 v[224:227], v252 offset:16384\n\t" \
    "v_mfma_f32_16x16x32_bf16 v[220:223], v[88:91], v[228:231], v[220:223]\n\t" \
    "v_mfma_f32_16x16x32_bf16 v[212:215], v[244:247], v[228:231], v[212:215]\n\t" \
    "v_mfma_f32_16x16x32_bf16 v[204:207], v[84:87], v[232:235], v[204:207]\n\t" \
    "ds_read_b128 v[228:231], v252 offset:1024\n\t" \
    "v_mfma_f32_16x16x32_bf16 v[208:211], v[92:95], v[232:235], v[208:211]\n\t" \
    "ds_read_b128 v[244:247], %[wl] offset:6144\n\t" \
    "s_waitcnt lgkmcnt(3)\n\t" \
    "v_mfma_f32_16x16x32_bf16 v[200:203], v[248:251], v[232:235], v[200:203]\n\t" \
    "v_mfma_f32_16x16x32_bf16 v[216:219], v[84:87], v[236:239], v[216:219]\n\t" \
    "ds_read_b128 v[232:235], v252 offset:17408\n\t" \
    "v_mfma_f32_16x16x32_bf16 v[220:223], v[92:95], v[236:239], v[220:223]\n\t" \
    "v_mfma_f32_16x16x32_bf16 v[212:215], v[248:251], v[236:239], v[212:215]\n\t" \
    "v_mfma_f32_16x16x32_bf16 v[204:207], v[104:107], v[240:243], v[204:207]\n\t" \
    "v_xor_b32 v252, 0x3000, %[xa]\n\t" \
    "ds_read_b128 v[236:239], v252 offset:0\n\t" \
    "v_mfma_f32_16x16x32_bf16 v[208:211], v[112:115], v[240:243], v[208:211]\n\t" \
    "ds_read_b128 v[248:251], %[wl] offset:7168\n\t" \
    "v_mfma_f32_16x16x32_bf16 v[200:203], v[96:99], v[240:243], v[200:203]\n\t" \
    "s_waitcnt lgkmcnt(5)\n\t" \
    "v_mfma_f32_16x16x32_bf16 v[216:219], v[104:107], v[224:227], v[216:219]\n\t" \
    "ds_read_b128 v[240:243], v252 offset:16384\n\t" \
    "v_mfma_f32_16x16x32_bf16 v[220:223], v[112:115], v[224:227], v[220:223]\n\t" \
    "v_mfma_f32_16x16x32_bf16 v[212:215], v[96:99], v[224:227], v[212:215]\n\t" \
    "s_waitcnt lgkmcnt(5)\n\t" \
    "v_mfma_f32_16x16x32_bf16 v[204:207], v[108:111], v[228:231], v[204:207]\n\t" \
    "ds_read_b128 v[224:227], v252 offset:1024\n\t" \
    "v_mfma_f32_16x16x32_bf16 v[208:211], v[116:119], v[228:231], v[208:211]\n\t" \
    "v_mfma_f32_16x16x32_bf16 v[200:203], v[100:103], v[228:231], v[200:203]\n\t" \
    "s_waitcnt lgkmcnt(4)\n\t" \
    "v_mfma_f32_16x16x32_bf16 v[216:219], v[108:111], v[232:235], v[216:219]\n\t" \
    "ds_read_b128 v[228:231], v252 offset:17408\n\t" \
    "v_mfma_f32_16x16x32_bf16 v[220:223], v[116:119], v[232:235], v[220:223]\n\t" \
    "v_mfma_f32_16x16x32_bf16 v[212:215], v[100:103], v[232:235], v[212:215]\n\t" \
    "s_waitcnt lgkmcnt(4)\n\t" \
    "v_mfma_f32_16x16x32_bf16 v[204:207], v[120:123], v[236:239], v[204:207]\n\t" \
    "v_xor_b32 v252, 0x3800, %[xa]\n\t" \
    "ds_read_b128 v[232:235], v252 offset:0\n\t" \
    "v_mfma_f32_16x16x32_bf16 v[208:211], v[128:131], v[236:239], v[208:211]\n\t" \
    "v_mfma_f32_16x16x32_bf16 v[200:203], v[244:247], v[236:239], v[200:203]\n\t" \
    "s_waitcnt lgkmcnt(3)\n\t" \
    "v_mfma_f32_16x16x32_bf16 v[216:219], v[120:123], v[240:243], v[216:219]\n\t" \
    "ds_read_b128 v[236:239], v252 offset:16384\n\t" \
    "v_mfma_f32_16x16x32_bf16 v[220:223], v[128:131], v[240:243], v[220:223]\n\t" \
    "v_mfma_f32_16x16x32_bf16 v[212:215], v[244:247], v[240:243], v[212:215]\n\t" \
    "s_waitcnt lgkmcnt(3)\n\t" \
    "v_mfma_f32_16x16x32_bf16 v[204:207], v[124:127], v[224:227], v[204:207]\n\t" \
    "ds_read_b128 v[240:243], v252 offset:1024\n\t" \
    "v_mfma_f32_16x16x32_bf16 v[208:211], v[132:135], v[224:227], v[208:211]\n\t" \
    "v_mfma_f32_16x16x32_bf16 v[200:203], v[248:251], v[224:227], v[200:203]\n\t" \
    "s_waitcnt lgkmcnt(3)\n\t" \
    "v_mfma_f32_16x16x32_bf16 v[216:219], v[124:127], v[228:231], v[216:219]\n\t" \
    "ds_read_b128 v[224:227], v252 offset:17408\n\t" \
    "v_mfma_f32_16x16x32_bf16 v[220:223], v[132:135], v[228:231], v[220:223]\n\t" \
    "v_mfma_f32_16x16x32_bf16 v[212:215], v[248:251], v[228:231], v[212:215]\n\t" \
    "s_waitcnt lgkmcnt(3)\n\t" \
    "v_mfma_f32_16x16x32_bf16 v[204:207], v[144:147], v[232:235], v[204:207]\n\t" \
    "v_and_b32 v252, 0x3ff, %[xa]\n\t" \
    "ds_read_b128 v[228:231], v252 offset:43008\n\t" \
    "v_mov_b64 v[244:245], 0\n\t" \
    "v_mfma_f32_16x16x32_bf16 v[208:211], v[152:155], v[232:235], v[208:211]\n\t" \
    "v_mov_b64 v[246:247], 0\n\t" \
    "v_mfma_f32_16x16x32_bf16 v[200:203], v[136:139], v[232:235], v[200:203]\n\t" \
    "v_mov_b64 v[248:249], 0\n\t" \
    "s_waitcnt lgkmcnt(3)\n\t" \
    "v_mfma_f32_16x16x32_bf16 v[216:219], v[144:147], v[236:239], v[216:219]\n\t" \
    "ds_read_b128 v[232:235], v252 offset:44032\n\t" \
    "v_mov_b64 v[250:251], 0\n\t" \
    "v_mfma_f32_16x16x32_bf16 v[220:223], v[152:155], v[236:239], v[220:223]\n\t" \
    "v_mfma_f32_16x16x32_bf16 v[212:215], v[136:139], v[236:239], v[212:215]\n\t" \
    "s_waitcnt lgkmcnt(3)\n\t" \
    "v_mfma_f32_16x16x32_bf16 v[204:207], v[148:151], v[240:243], v[204:207]\n\t" \
    "ds_read_b128 v[236:239], v252 offset:47104\n\t" \
    "v_mfma_f32_16x16x32_bf16 v[208:211], v[156:159], v[240:243], v[208:211]\n\t" \
    "v_mfma_f32_16x16x32_bf16 v[200:203], v[140:143], v[240:243], v[200:203]\n\t" \
    "s_waitcnt lgkmcnt(3)\n\t" \
    "v_mfma_f32_16x16x32_bf16 v[216:219], v[148:151], v[224:227], v[216:219]\n\t" \
    "ds_read_b128 v[240:243], v252 offset:48128\n\t" \
    "v_mfma_f32_16x16x32_bf16 v[220:223], v[156:159], v[224:227], v[220:223]\n\t" \
    "v_mfma_f32_16x16x32_bf16 v[212:215], v[140:143], v[224:227], v[212:215]\n\t" \
    "s_waitcnt lgkmcnt(3)\n\t" \
    "v_mfma_f32_16x16x32_bf16 v[204:207], v[184:187], v[228:231], v[204:207]\n\t" \
    "v_add_u32 v252, %[qs], v252\n\t" \
    "v_mfma_f32_16x16x32_bf16 v[208:211], v[192:195], v[228:231], v[208:211]\n\t" \
    "v_mov_b64 v[224:225], 0\n\t" \
    "v_mfma_f32_16x16x32_bf16 v[200:203], v[176:179], v[228:231], v[200:203]\n\t" \
    "v_mov_b64 v[226:227], 0\n\t" \
    "s_waitcnt lgkmcnt(2)\n\t" \
    "v_mfma_f32_16x16x32_bf16 v[204:207], v[188:191], v[232:235], v[204:207]\n\t" \
    "v_mfma_f32_16x16x32_bf16 v[208:211], v[196:199], v[232:235], v[208:211]\n\t" \
    "v_mfma_f32_16x16x32_bf16 v[200:203], v[180:183], v[232:235], v[200:203]\n\t" \
    "s_waitcnt lgkmcnt(1)\n\t" \
    "v_mfma_f32_16x16x32_bf16 v[216:219], v[184:187], v[236:239], v[216:219]\n\t" \
    "ds_read_b128 v[228:231], %[wl] offset:2048\n\t" \
    "ds_read_b128 v[232:235], %[wl] offset:3072\n\t" \
    "v_mfma_f32_16x16x32_bf16 v[220:223], v[192:195], v[236:239], v[220:223]\n\t" \
    "v_cvt_pk_bf16_f32 v204, v204, v205\n\t" \
    "v_cvt_pk_bf16_f32 v205, v206, v207\n\t" \
    "v_mfma_f32_16x16x32_bf16 v[212:215], v[176:179], v[236:239], v[212:215]\n\t" \
    "ds_write_b64 v252, v[204:205] offset:1024\n\t" \
    "v_cvt_pk_bf16_f32 v208, v208, v209\n\t" \
    "s_waitcnt lgkmcnt(3)\n\t" \
    "v_mfma_f32_16x16x32_bf16 v[216:219], v[188:191], v[240:243], v[216:219]\n\t" \
    "v_cvt_pk_bf16_f32 v209, v210, v211\n\t" \
    "ds_write_b64 v252, v[208:209] offset:2048\n\t" \
    "v_mfma_f32_16x16x32_bf16 v[220:223], v[196:199], v[240:243], v[220:223]\n\t" \
    "v_cvt_pk_bf16_f32 v200, v200, v201\n\t" \
    "v_cvt_pk_bf16_f32 v201, v202, v203\n\t" \
    "v_mfma_f32_16x16x32_bf16 v[212:215], v[180:183], v[240:243], v[212:215]\n\t" \
    "ds_write_b64 v252, v[200:201] offset:0\n\t" \
    "ds_read_b128 v[200:203], %[wl] offset:0\n\t" \
    "ds_read_b128 v[204:207], %[wl] offset:1024\n\t" \
    "v_cvt_pk_bf16_f32 v216, v216, v217\n\t" \
    "v_cvt_pk_bf16_f32 v217, v218, v219\n\t" \
    "ds_write_b64 v252, v[216:217] offset:7168\n\t" \
    "v_cvt_pk_bf16_f32 v220, v220, v221\n\t" \
    "v_cvt_pk_bf16_f32 v221, v222, v223\n\t" \
    "ds_write_b64 v252, v[220:221] offset:8192\n\t" \
    "v_cvt_pk_bf16_f32 v212, v212, v213\n\t" \
    "v_cvt_pk_bf16_f32 v213, v214, v215\n\t" \
    "ds_write_b64 v252, v[212:213] offset:6144\n\t" \
    "s_waitcnt lgkmcnt(0)\n\t"
#define ASM_XG_S_HI \
    "s_waitcnt lgkmcnt(0)\n\t" \
    "v_xor_b32 v252, 0x2000, %[xa]\n\t" \
    "ds_read_b128 v[224:227], v252 offset:0\n\t" \
    "ds_read_b128 v[244:247], %[wl] offset:4096\n\t" \
    "ds_read_b128 v[228:231], v252 offset:16384\n\t" \
    "ds_read_b128 v[232:235], v252 offset:1024\n\t" \
    "ds_read_b128 v[236:239], v252 offset:17408\n\t" \
    "ds_read_b128 v[248:251], %[wl] offset:5120\n\t" \
    "s_cmp_eq_u32 %[slp], 0\n\t" \
    "s_cbranch_scc1 .Lxs%=\n\t" \
    "s_sleep 10\n\t" \
    ".Lxs%=:\n\t" \
    "s_waitcnt lgkmcnt(5)\n\t" \
    "v_mfma_f32_16x16x32_bf16 v[204:207], v[80:83], v[224:227], 0\n\t" \
    "v_xor_b32 v252, 0x2800, %[xa]\n\t" \
    "ds_read_b128 v[240:243], v252 offset:0\n\t" \
    "v_mfma_f32_16x16x32_bf16 v[208:211], v[88:91], v[224:227], 0\n\t" \
    "s_waitcnt lgkmcnt(5)\n\t" \
    "v_mfma_f32_16x16x32_bf16 v[200:203], v[244:247], v[224:227], 0\n\t" \
    "s_waitcnt lgkmcnt(4)\n\t" \
    "v_mfma_f32_16x16x32_bf16 v[216:219], v[80:83], v[228:231], 0\n\t" \
    "ds_read_b128 v[224:227], v252 offset:16384\n\t" \
    "v_mfma_f32_16x16x32_bf16 v[220:223], v[88:91], v[228:231], 0\n\t" \
    "v_mfma_f32_16x16x32_bf16 v[212:215], v[244:247], v[228:231], 0\n\t" \
    "s_waitcnt lgkmcnt(4)\n\t" \
    "v_mfma_f32_16x16x32_bf16 v[204:207], v[84:87], v[232:235], v[204:207]\n\t" \
    "ds_read_b128 v[228:231], v252 offset:1024\n\t" \
    "v_mfma_f32_16x16x32_bf16 v[208:211], v[92:95], v[232:235], v[208:211]\n\t" \
    "ds_read_b128 v[244:247], %[wl] offset:6144\n\t" \
    "s_waitcnt lgkmcnt(4)\n\t" \
    "v_mfma_f32_16x16x32_bf16 v[200:203], v[248:251], v[232:235], v[200:203]\n\t" \
    "v_mfma_f32_16x16x32_bf16 v[216:219], v[84:87], v[236:239], v[216:219]\n\t" \
    "ds_read_b128 v[232:235], v252 offset:17408\n\t" \
    "v_mfma_f32_16x16x32_bf16 v[220:223], v[92:95], v[236:239], v[220:223]\n\t" \
    "v_mfma_f32_16x16x32_bf16 v[212:215], v[248:251], v[236:239], v[212:215]\n\t" \
    "s_waitcnt lgkmcnt(4)\n\t" \
    "v_mfma_f32_16x16x32_bf16 v[204:207], v[104:107], v[240:243], v[204:207]\n\t" \
    "v_xor_b32 v252, 0x3000, %[xa]\n\t" \
    "ds_read_b128 v[236:239], v252 offset:0\n\t" \
    "v_mfma_f32_16x16x32_bf16 v[208:211], v[112:115], v[240:243], v[208:211]\n\t" \
    "ds_read_b128 v[248:251], %[wl] offset:7168\n\t" \
    "v_mfma_f32_16x16x32_bf16 v[200:203], v[96:99], v[240:243], v[200:203]\n\t" \
    "s_waitcnt lgkmcnt(5)\n\t" \
    "v_mfma_f32_16x16x32_bf16 v[216:219], v[104:107], v[224:227], v[216:219]\n\t" \
    "ds_read_b128 v[240:243], v252 offset:16384\n\t" \
    "v_mfma_f32_16x16x32_bf16 v[220:223], v[112:115], v[224:227], v[220:223]\n\t" \
    "v_mfma_f32_16x16x32_bf16 v[212:215], v[96:99], v[224:227], v[212:215]\n\t" \
    "s_waitcnt lgkmcnt(5)\n\t" \
    "v_mfma_f32_16x16x32_bf16 v[204:207], v[108:111], v[228:231], v[204:207]\n\t" \
    "ds_read_b128 v[224:227], v252 offset:1024\n\t" \
    "v_mfma_f32_16x16x32_bf16 v[208:211], v[116:119], v[228:231], v[208:211]\n\t" \
    "v_mfma_f32_16x16x32_bf16 v[200:203], v[100:103], v[228:231], v[200:203]\n\t" \
    "s_waitcnt lgkmcnt(4)\n\t" \
    "v_mfma_f32_16x16x32_bf16 v[216:219], v[108:111], v[232:235], v[216:219]\n\t" \
    "ds_read_b128 v[228:231], v252 offset:17408\n\t" \
    "v_mfma_f32_16x16x32_bf16 v[220:223], v[116:119], v[232:235], v[220:223]\n\t" \
    "v_mfma_f32_16x16x32_bf16 v[212:215], v[100:103], v[232:235], v[212:215]\n\t" \
    "s_waitcnt lgkmcnt(4)\n\t" \
    "v_mfma_f32_16x16x32_bf16 v[204:207], v[120:123], v[236:239], v[204:207]\n\t" \
    "v_xor_b32 v252, 0x3800, %[xa]\n\t" \
    "ds_read_b128 v[232:235], v252 offset:0\n\t" \
    "v_mfma_f32_16x16x32_bf16 v[208:211], v[128:131], v[236:239], v[208:211]\n\t" \
    "v_mfma_f32_16x16x32_bf16 v[200:203], v[244:247], v[236:239], v[200:203]\n\t" \
    "s_waitcnt lgkmcnt(3)\n\t" \
    "v_mfma_f32_16x16x32_bf16 v[216:219], v[120:123], v[240:243], v[216:219]\n\t" \
    "ds_read_b128 v[236:239], v252 offset:16384\n\t" \
    "v_mfma_f32_16x16x32_bf16 v[220:223], v[128:131], v[240:243], v[220:223]\n\t" \
    "v_mfma_f32_16x16x32_bf16 v[212:215], v[244:247], v[240:243], v[212:215]\n\t" \
    "s_waitcnt lgkmcnt(3)\n\t" \
    "v_mfma_f32_16x16x32_bf16 v[204:207], v[124:127], v[224:227], v[204:207]\n\t" \
    "ds_read_b128 v[240:243], v252 offset:1024\n\t" \
    "v_mfma_f32_16x16x32_bf16 v[208:211], v[132:135], v[224:227], v[208:211]\n\t" \
    "ds_read_b128 v[244:247], %[wl] offset:8192\n\t" \
    "v_mfma_f32_16x16x32_bf16 v[200:203], v[248:251], v[224:227], v[200:203]\n\t" \
    "s_waitcnt lgkmcnt(4)\n\t" \
    "v_mfma_f32_16x16x32_bf16 v[216:219], v[124:127], v[228:231], v[216:219]\n\t" \
    "ds_read_b128 v[224:227], v252 offset:17408\n\t" \
    "v_mfma_f32_16x16x32_bf16 v[220:223], v[132:135], v[228:231], v[220:223]\n\t" \
    "v_mfma_f32_16x16x32_bf16 v[212:215], v[248:251], v[228:231], v[212:215]\n\t" \
    "s_waitcnt lgkmcnt(4)\n\t" \
    "v_mfma_f32_16x16x32_bf16 v[204:207], v[144:147], v[232:235], v[204:207]\n\t" \
    "v_and_b32 v252, 0x3ff, %[xa]\n\t" \
    "ds_read_b128 v[228:231], v252 offset:40960\n\t" \
    "v_mfma_f32_16x16x32_bf16 v[208:211], v[152:155], v[232:235], v[208:211]\n\t" \
    "ds_read_b128 v[248:251], %[wl] offset:9216\n\t" \
    "v_mfma_f32_16x16x32_bf16 v[200:203], v[136:139], v[232:235], v[200:203]\n\t" \
    "s_waitcnt lgkmcnt(5)\n\t" \
    "v_mfma_f32_16x16x32_bf16 v[216:219], v[144:147], v[236:239], v[216:219]\n\t" \
    "ds_read_b128 v[232:235], v252 offset:45056\n\t" \
    "v_mfma_f32_16x16x32_bf16 v[220:223], v[152:155], v[236:239], v[220:223]\n\t" \
    "v_mfma_f32_16x16x32_bf16 v[212:215], v[136:139], v[236:239], v[212:215]\n\t" \
    "s_waitcnt lgkmcnt(5)\n\t" \
    "v_mfma_f32_16x16x32_bf16 v[204:207], v[148:151], v[240:243], v[204:207]\n\t" \
    "ds_read_b128 v[236:239], v252 offset:41984\n\t" \
    "v_mfma_f32_16x16x32_bf16 v[208:211], v[156:159], v[240:243], v[208:211]\n\t" \
    "v_mfma_f32_16x16x32_bf16 v[200:203], v[140:143], v[240:243], v[200:203]\n\t" \
    "s_waitcnt lgkmcnt(4)\n\t" \
    "v_mfma_f32_16x16x32_bf16 v[216:219], v[148:151], v[224:227], v[216:219]\n\t" \
    "ds_read_b128 v[240:243], v252 offset:46080\n\t" \
    "v_mfma_f32_16x16x32_bf16 v[220:223], v[156:159], v[224:227], v[220:223]\n\t" \
    "v_mfma_f32_16x16x32_bf16 v[212:215], v[140:143], v[224:227], v[212:215]\n\t" \
    "s_waitcnt vmcnt(0) lgkmcnt(0)\n\t" \
    "s_barrier\n\t" ASM_ST \
    "s_mov_b32 m0, %[d0]\n\t" \
    "s_nop 0\n\t" \
    "global_load_lds_dwordx4 v252, %[g0] offset:0 sc1\n\t" \
    "global_load_lds_dwordx4 v252, %[g0] offset:1024 sc1\n\t" \
    "global_load_lds_dwordx4 v252, %[g0] offset:2048 sc1\n\t" \
    "global_load_lds_dwordx4 v252, %[g0] offset:3072 sc1\n\t" \
    "s_mov_b32 m0, %[d1]\n\t" \
    "s_nop 0\n\t" \
    "global_load_lds_dwordx4 v252, %[g1] sc1\n\t" \
    "v_mfma_f32_16x16x32_bf16 v[204:207], v[160:163], v[228:231], v[204:207]\n\t" \
    "ds_read_b128 v[224:227], %[xa] offset:0\n\t" \
    "v_mfma_f32_16x16x32_bf16 v[208:211], v[168:171], v[228:231], v[208:211]\n\t" \
    "v_mfma_f32_16x16x32_bf16 v[200:203], v[244:247], v[228:231], v[200:203]\n\t" \
    "v_mfma_f32_16x16x32_bf16 v[216:219], v[160:163], v[232:235], v[216:219]\n\t" \
    "ds_read_b128 v[228:231], %[xa] offset:16384\n\t" \
    "v_mfma_f32_16x16x32_bf16 v[220:223], v[168:171], v[232:235], v[220:223]\n\t" \
    "v_mfma_f32_16x16x32_bf16 v[212:215], v[244:247], v[232:235], v[212:215]\n\t" \
    "v_mfma_f32_16x16x32_bf16 v[204:207], v[164:167], v[236:239], v[204:207]\n\t" \
    "ds_read_b128 v[232:235], %[xa] offset:1024\n\t" \
    "v_mfma_f32_16x16x32_bf16 v[208:211], v[172:175], v[236:239], v[208:211]\n\t" \
    "ds_read_b128 v[244:247], %[wl] offset:0\n\t" \
    "v_mfma_f32_16x16x32_bf16 v[200:203], v[248:251], v[236:239], v[200:203]\n\t" \
    "v_mfma_f32_16x16x32_bf16 v[216:219], v[164:167], v[240:243], v[216:219]\n\t" \
    "ds_read_b128 v[236:239], %[xa] offset:17408\n\t" \
    "v_mfma_f32_16x16x32_bf16 v[220:223], v[172:175], v[240:243], v[220:223]\n\t" \
    "v_mfma_f32_16x16x32_bf16 v[212:215], v[248:251], v[240:243], v[212:215]\n\t" \
    "s_waitcnt lgkmcnt(4)\n\t" \
    "v_mfma_f32_16x16x32_bf16 v[204:207], v[0:3], v[224:227], v[204:207]\n\t" \
    "v_xor_b32 v252, 0x800, %[xa]\n\t" \
    "ds_read_b128 v[240:243], v252 offset:0\n\t" \
    "v_mfma_f32_16x16x32_bf16 v[208:211], v[8:11], v[224:227], v[208:211]\n\t" \
    "ds_read_b128 v[248:251], %[wl] offset:1024\n\t" \
    "s_waitcnt lgkmcnt(3)\n\t" \
    "v_mfma_f32_16x16x32_bf16 v[200:203], v[244:247], v[224:227], v[200:203]\n\t" \
    "v_mfma_f32_16x16x32_bf16 v[216:219], v[0:3], v[228:231], v[216:219]\n\t" \
    "ds_read_b128 v[224:227], v252 offset:16384\n\t" \
    "v_mfma_f32_16x16x32_bf16 v[220:223], v[8:11], v[228:231], v[220:223]\n\t" \
    "v_mfma_f32_16x16x32_bf16 v[212:215], v[244:247], v[228:231], v[212:215]\n\t" \
    "v_mfma_f32_16x16x32_bf16 v[204:207], v[4:7], v[232:235], v[204:207]\n\t" \
    "ds_read_b128 v[228:231], v252 offset:1024\n\t" \
    "v_mfma_f32_16x16x32_bf16 v[208:211], v[12:15], v[232:235], v[208:211]\n\t" \
    "ds_read_b128 v[244:247], %[wl] offset:2048\n\t" \
    "s_waitcnt lgkmcnt(3)\n\t" \
    "v_mfma_f32_16x16x32_bf16 v[200:203], v[248:251], v[232:235], v[200:203]\n\t" \
    "v_mfma_f32_16x16x32_bf16 v[216:219], v[4:7], v[236:239], v[216:219]\n\t" \
    "ds_read_b128 v[232:235], v252 offset:17408\n\t" \
    "v_mfma_f32_16x16x32_bf16 v[220:223], v[12:15], v[236:239], v[220:223]\n\t" \
    "v_mfma_f32_16x16x32_bf16 v[212:215], v[248:251], v[236:239], v[212:215]\n\t" \
    "v_mfma_f32_16x16x32_bf16 v[204:207], v[24:27], v[240:243], v[204:207]\n\t" \
    "v_xor_b32 v252, 0x1000, %[xa]\n\t" \
    "ds_read_b128 v[236:239], v252 offset:0\n\t" \
    "v_mfma_f32_16x16x32_bf16 v[208:211], v[32:35], v[240:243], v[208:211]\n\t" \
    "ds_read_b128 v[248:251], %[wl] offset:3072\n\t" \
    "v_mfma_f32_16x16x32_bf16 v[200:203], v[16:19], v[240:243], v[200:203]\n\t" \
    "s_waitcnt lgkmcnt(5)\n\t" \
    "v_mfma_f32_16x16x32_bf16 v[216:219], v[24:27], v[224:227], v[216:219]\n\t" \
    "ds_read_b128 v[240:243], v252 offset:16384\n\t" \
    "v_mfma_f32_16x16x32_bf16 v[220:223], v[32:35], v[224:227], v[220:223]\n\t" \
    "v_mfma_f32_16x16x32_bf16 v[212:215], v[16:19], v[224:227], v[212:215]\n\t" \
    "s_waitcnt lgkmcnt(5)\n\t" \
    "v_mfma_f32_16x16x32_bf16 v[204:207], v[28:31], v[228:231], v[204:207]\n\t" \
    "ds_read_b128 v[224:227], v252 offset:1024\n\t" \
    "v_mfma_f32_16x16x32_bf16 v[208:211], v[36:39], v[228:231], v[208:211]\n\t" \
    "v_mfma_f32_16x16x32_bf16 v[200:203], v[20:23], v[228:231], v[200:203]\n\t" \
    "s_waitcnt lgkmcnt(4)\n\t" \
    "v_mfma_f32_16x16x32_bf16 v[216:219], v[28:31], v[232:235], v[216:219]\n\t" \
    "ds_read_b128 v[228:231], v252 offset:17408\n\t" \
    "v_mfma_f32_16x16x32_bf16 v[220:223], v[36:39], v[232:235], v[220:223]\n\t" \
    "v_mfma_f32_16x16x32_bf16 v[212:215], v[20:23], v[232:235], v[212:215]\n\t" \
    "s_waitcnt lgkmcnt(4)\n\t" \
    "v_mfma_f32_16x16x32_bf16 v[204:207], v[40:43], v[236:239], v[204:207]\n\t" \
    "v_xor_b32 v252, 0x1800, %[xa]\n\t" \
    "ds_read_b128 v[232:235], v252 offset:0\n\t" \
    "v_mfma_f32_16x16x32_bf16 v[208:211], v[48:51], v[236:239], v[208:211]\n\t" \
    "v_mfma_f32_16x16x32_bf16 v[200:203], v[244:247], v[236:239], v[200:203]\n\t" \
    "s_waitcnt lgkmcnt(3)\n\t" \
    "v_mfma_f32_16x16x32_bf16 v[216:219], v[40:43], v[240:243], v[216:219]\n\t" \
    "ds_read_b128 v[236:239], v252 offset:16384\n\t" \
    "v_mfma_f32_16x16x32_bf16 v[220:223], v[48:51], v[240:243], v[220:223]\n\t" \
    "v_mfma_f32_16x16x32_bf16 v[212:215], v[244:247], v[240:243], v[212:215]\n\t" \
    "s_waitcnt lgkmcnt(3)\n\t" \
    "v_mfma_f32_16x16x32_bf16 v[204:207], v[44:47], v[224:227], v[204:207]\n\t" \
    "ds_read_b128 v[240:243], v252 offset:1024\n\t" \
    "v_mfma_f32_16x16x32_bf16 v[208:211], v[52:55], v[224:227], v[208:211]\n\t" \
    "v_mfma_f32_16x16x32_bf16 v[200:203], v[248:251], v[224:227], v[200:203]\n\t" \
    "s_waitcnt lgkmcnt(3)\n\t" \
    "v_mfma_f32_16x16x32_bf16 v[216:219], v[44:47], v[228:231], v[216:219]\n\t" \
    "ds_read_b128 v[224:227], v252 offset:17408\n\t" \
    "v_mfma_f32_16x16x32_bf16 v[220:223], v[52:55], v[228:231], v[220:223]\n\t" \
    "v_mfma_f32_16x16x32_bf16 v[212:215], v[248:251], v[228:231], v[212:215]\n\t" \
    "s_waitcnt lgkmcnt(3)\n\t" \
    "v_mfma_f32_16x16x32_bf16 v[204:207], v[64:67], v[232:235], v[204:207]\n\t" \
    "v_and_b32 v252, 0x3ff, %[xa]\n\t" \
    "ds_read_b128 v[228:231], v252 offset:43008\n\t" \
    "v_mov_b64 v[244:245], 0\n\t" \
    "v_mfma_f32_16x16x32_bf16 v[208:211], v[72:75], v[232:235], v[208:211]\n\t" \
    "v_mov_b64 v[246:247], 0\n\t" \
    "v_mfma_f32_16x16x32_bf16 v[200:203], v[56:59], v[232:235], v[200:203]\n\t" \
    "v_mov_b64 v[248:249], 0\n\t" \
    "s_waitcnt lgkmcnt(3)\n\t" \
    "v_mfma_f32_16x16x32_bf16 v[216:219], v[64:67], v[236:239], v[216:219]\n\t" \
    "ds_read_b128 v[232:235], v252 offset:44032\n\t" \
    "v_mov_b64 v[250:251], 0\n\t" \
    "v_mfma_f32_16x16x32_bf16 v[220:223], v[72:75], v[236:239], v[220:223]\n\t" \
    "v_mfma_f32_16x16x32_bf16 v[212:215], v[56:59], v[236:239], v[212:215]\n\t" \
    "s_waitcnt lgkmcnt(3)\n\t" \
    "v_mfma_f32_16x16x32_bf16 v[204:207], v[68:71], v[240:243], v[204:207]\n\t" \
    "ds_read_b128 v[236:239], v252 offset:47104\n\t" \
    "v_mfma_f32_16x16x32_bf16 v[208:211], v[76:79], v[240:243], v[208:211]\n\t" \
    "v_mfma_f32_16x16x32_bf16 v[200:203], v[60:63], v[240:243], v[200:203]\n\t" \
    "s_waitcnt lgkmcnt(3)\n\t" \
    "v_mfma_f32_16x16x32_bf16 v[216:219], v[68:71], v[224:227], v[216:219]\n\t" \
    "ds_read_b128 v[240:243], v252 offset:48128\n\t" \
    "v_mfma_f32_16x16x32_bf16 v[220:223], v[76:79], v[224:227], v[220:223]\n\t" \
    "v_mfma_f32_16x16x32_bf16 v[212:215], v[60:63], v[224:227], v[212:215]\n\t" \
    "s_waitcnt lgkmcnt(3)\n\t" \
    "v_mfma_f32_16x16x32_bf16 v[204:207], v[184:187], v[228:231], v[204:207]\n\t" \
    "v_add_u32 v252, %[qs], v252\n\t" \
    "v_mfma_f32_16x16x32_bf16 v[208:211], v[192:195], v[228:231], v[208:211]\n\t" \
    "v_mov_b64 v[224:225], 0\n\t" \
    "v_mfma_f32_16x16x32_bf16 v[200:203], v[176:179], v[228:231], v[200:203]\n\t" \
    "v_mov_b64 v[226:227], 0\n\t" \
    "s_waitcnt lgkmcnt(2)\n\t" \
    "v_mfma_f32_16x16x32_bf16 v[204:207], v[188:191], v[232:235], v[204:207]\n\t" \
    "v_mfma_f32_16x16x32_bf16 v[208:211], v[196:199], v[232:235], v[208:211]\n\t" \
    "v_mfma_f32_16x16x32_bf16 v[200:203], v[180:183], v[232:235], v[200:203]\n\t" \
    "s_waitcnt lgkmcnt(1)\n\t" \
    "v_mfma_f32_16x16x32_bf16 v[216:219], v[184:187], v[236:239], v[216:219]\n\t" \
    "ds_read_b128 v[228:231], %[wl] offset:2048\n\t" \
    "ds_read_b128 v[232:235], %[wl] offset:3072\n\t" \
    "v_mfma_f32_16x16x32_bf16 v[220:223], v[192:195], v[236:239], v[220:223]\n\t" \
    "v_cvt_pk_bf16_f32 v204, v204, v205\n\t" \
    "v_cvt_pk_bf16_f32 v205, v206, v207\n\t" \
    "v_mfma_f32_16x16x32_bf16 v[212:215], v[176:179], v[236:239], v[212:215]\n\t" \
    "ds_write_b64 v252, v[204:205] offset:1024\n\t" \
    "v_cvt_pk_bf16_f32 v208, v208, v209\n\t" \
    "s_waitcnt lgkmcnt(3)\n\t" \
    "v_mfma_f32_16x16x32_bf16 v[216:219], v[188:191], v[240:243], v[216:219]\n\t" \
    "v_cvt_pk_bf16_f32 v209, v210, v211\n\t" \
    "ds_write_b64 v252, v[208:209] offset:2048\n\t" \
    "v_mfma_f32_16x16x32_bf16 v[220:223], v[196:199], v[240:243], v[220:223]\n\t" \
    "v_cvt_pk_bf16_f32 v200, v200, v201\n\t" \
    "v_cvt_pk_bf16_f32 v201, v202, v203\n\t" \
    "v_mfma_f32_16x16x32_bf16 v[212:215], v[180:183], v[240:243], v[212:215]\n\t" \
    "ds_write_b64 v252, v[200:201] offset:0\n\t" \
    "ds_read_b128 v[200:203], %[wl] offset:0\n\t" \
    "ds_read_b128 v[204:207], %[wl] offset:1024\n\t" \
    "v_cvt_pk_bf16_f32 v216, v216, v217\n\t" \
    "v_cvt_pk_bf16_f32 v217, v218, v219\n\t" \
    "ds_write_b64 v252, v[216:217] offset:7168\n\t" \
    "v_cvt_pk_bf16_f32 v220, v220, v221\n\t" \
    "v_cvt_pk_bf16_f32 v221, v222, v223\n\t" \
    "ds_write_b64 v252, v[220:221] offset:8192\n\t" \
    "v_cvt_pk_bf16_f32 v212, v212, v213\n\t" \
    "v_cvt_pk_bf16_f32 v213, v214, v215\n\t" \
    "ds_write_b64 v252, v[212:213] offset:6144\n\t" \
    "s_waitcnt lgkmcnt(0)\n\t"
#define ASM_XG_S_LO2 \
    "s_waitcnt lgkmcnt(0)\n\t" \
    "ds_read_b128 v[224:227], %[xa] offset:0\n\t" \
    "ds_read_b128 v[228:231], %[xa] offset:16384\n\t" \
    "ds_read_b128 v[232:235], %[xa] offset:1024\n\t" \
    "ds_read_b128 v[236:239], %[xa] offset:17408\n\t" \
    "s_waitcnt lgkmcnt(3)\n\t" \
    "v_mfma_f32_16x16x32_bf16 v[204:207], v[0:3], v[224:227], 0\n\t" \
    "v_xor_b32 v252, 0x800, %[xa]\n\t" \
    "ds_read_b128 v[240:243], v252 offset:0\n\t" \
    "v_mfma_f32_16x16x32_bf16 v[208:211], v[8:11], v[224:227], 0\n\t" \
    "s_waitcnt lgkmcnt(3)\n\t" \
    "v_mfma_f32_16x16x32_bf16 v[216:219], v[0:3], v[228:231], 0\n\t" \
    "ds_read_b128 v[224:227], v252 offset:16384\n\t" \
    "v_mfma_f32_16x16x32_bf16 v[220:223], v[8:11], v[228:231], 0\n\t" \
    "s_waitcnt lgkmcnt(3)\n\t" \
    "v_mfma_f32_16x16x32_bf16 v[204:207], v[4:7], v[232:235], v[204:207]\n\t" \
    "ds_read_b128 v[228:231], v252 offset:1024\n\t" \
    "v_mfma_f32_16x16x32_bf16 v[208:211], v[12:15], v[232:235], v[208:211]\n\t" \
    "s_waitcnt lgkmcnt(3)\n\t" \
    "v_mfma_f32_16x16x32_bf16 v[216:219], v[4:7], v[236:239], v[216:219]\n\t" \
    "ds_read_b128 v[232:235], v252 offset:17408\n\t" \
    "v_mfma_f32_16x16x32_bf16 v[220:223], v[12:15], v[236:239], v[220:223]\n\t" \
    "s_waitcnt lgkmcnt(3)\n\t" \
    "v_mfma_f32_16x16x32_bf16 v[204:207], v[24:27], v[240:243], v[204:207]\n\t" \
    "v_xor_b32 v252, 0x1000, %[xa]\n\t" \
    "ds_read_b128 v[236:239], v252 offset:0\n\t" \
    "v_mfma_f32_16x16x32_bf16 v[208:211], v[32:35], v[240:243], v[208:211]\n\t" \
    "s_waitcnt lgkmcnt(3)\n\t" \
    "v_mfma_f32_16x16x32_bf16 v[216:219], v[24:27], v[224:227], v[216:219]\n\t" \
    "ds_read_b128 v[240:243], v252 offset:16384\n\t" \
    "v_mfma_f32_16x16x32_bf16 v[220:223], v[32:35], v[224:227], v[220:223]\n\t" \
    "s_waitcnt lgkmcnt(3)\n\t" \
    "v_mfma_f32_16x16x32_bf16 v[204:207], v[28:31], v[228:231], v[204:207]\n\t" \
    "ds_read_b128 v[224:227], v252 offset:1024\n\t" \
    "v_mfma_f32_16x16x32_bf16 v[208:211], v[36:39], v[228:231], v[208:211]\n\t" \
    "s_waitcnt lgkmcnt(3)\n\t" \
    "v_mfma_f32_16x16x32_bf16 v[216:219], v[28:31], v[232:235], v[216:219]\n\t" \
    "ds_read_b128 v[228:231], v252 offset:17408\n\t" \
    "v_mfma_f32_16x16x32_bf16 v[220:223], v[36:39], v[232:235], v[220:223]\n\t" \
    "s_waitcnt lgkmcnt(3)\n\t" \
    "v_mfma_f32_16x16x32_bf16 v[204:207], v[40:43], v[236:239], v[204:207]\n\t" \
    "v_xor_b32 v252, 0x1800, %[xa]\n\t" \
    "ds_read_b128 v[232:235], v252 offset:0\n\t" \
    "v_mfma_f32_16x16x32_bf16 v[208:211], v[48:51], v[236:239], v[208:211]\n\t" \
    "s_waitcnt lgkmcnt(3)\n\t" \
    "v_mfma_f32_16x16x32_bf16 v[216:219], v[40:43], v[240:243], v[216:219]\n\t" \
    "ds_read_b128 v[236:239], v252 offset:16384\n\t" \
    "v_mfma_f32_16x16x32_bf16 v[220:223], v[48:51], v[240:243], v[220:223]\n\t" \
    "s_waitcnt lgkmcnt(3)\n\t" \
    "v_mfma_f32_16x16x32_bf16 v[204:207], v[44:47], v[224:227], v[204:207]\n\t" \
    "ds_read_b128 v[240:243], v252 offset:1024\n\t" \
    "v_mfma_f32_16x16x32_bf16 v[208:211], v[52:55], v[224:227], v[208:211]\n\t" \
    "s_waitcnt lgkmcnt(3)\n\t" \
    "v_mfma_f32_16x16x32_bf16 v[216:219], v[44:47], v[228:231], v[216:219]\n\t" \
    "ds_read_b128 v[224:227], v252 offset:17408\n\t" \
    "v_mfma_f32_16x16x32_bf16 v[220:223], v[52:55], v[228:231], v[220:223]\n\t" \
    "s_waitcnt lgkmcnt(3)\n\t" \
    "v_mfma_f32_16x16x32_bf16 v[204:207], v[64:67], v[232:235], v[204:207]\n\t" \
    "v_and_b32 v252, 0x3ff, %[xa]\n\t" \
    "ds_read_b128 v[228:231], v252 offset:40960\n\t" \
    "v_mfma_f32_16x16x32_bf16 v[208:211], v[72:75], v[232:235], v[208:211]\n\t" \
    "s_waitcnt lgkmcnt(3)\n\t" \
    "v_mfma_f32_16x16x32_bf16 v[216:219], v[64:67], v[236:239], v[216:219]\n\t" \
    "ds_read_b128 v[232:235], v252 offset:45056\n\t" \
    "v_mfma_f32_16x16x32_bf16 v[220:223], v[72:75], v[236:239], v[220:223]\n\t" \
    "s_waitcnt lgkmcnt(3)\n\t" \
    "v_mfma_f32_16x16x32_bf16 v[204:207], v[68:71], v[240:243], v[204:207]\n\t" \
    "ds_read_b128 v[236:239], v252 offset:41984\n\t" \
    "v_mfma_f32_16x16x32_bf16 v[208:211], v[76:79], v[240:243], v[208:211]\n\t" \
    "s_waitcnt lgkmcnt(3)\n\t" \
    "v_mfma_f32_16x16x32_bf16 v[216:219], v[68:71], v[224:227], v[216:219]\n\t" \
    "ds_read_b128 v[240:243], v252 offset:46080\n\t" \
    "v_mfma_f32_16x16x32_bf16 v[220:223], v[76:79], v[224:227], v[220:223]\n\t" \
    "s_waitcnt lgkmcnt(0)\n\t" \
    "s_barrier\n\t" ASM_ST \
    "v_mfma_f32_16x16x32_bf16 v[204:207], v[160:163], v[228:231], v[204:207]\n\t" \
    "v_xor_b32 v252, 0x2000, %[xa]\n\t" \
    "ds_read_b128 v[224:227], v252 offset:0\n\t" \
    "v_mfma_f32_16x16x32_bf16 v[208:211], v[168:171], v[228:231], v[208:211]\n\t" \
    "v_mfma_f32_16x16x32_bf16 v[216:219], v[160:163], v[232:235], v[216:219]\n\t" \
    "ds_read_b128 v[228:231], v252 offset:16384\n\t" \
    "v_mfma_f32_16x16x32_bf16 v[220:223], v[168:171], v[232:235], v[220:223]\n\t" \
    "v_mfma_f32_16x16x32_bf16 v[204:207], v[164:167], v[236:239], v[204:207]\n\t" \
    "ds_read_b128 v[232:235], v252 offset:1024\n\t" \
    "v_mfma_f32_16x16x32_bf16 v[208:211], v[172:175], v[236:239], v[208:211]\n\t" \
    "v_mfma_f32_16x16x32_bf16 v[216:219], v[164:167], v[240:243], v[216:219]\n\t" \
    "ds_read_b128 v[236:239], v252 offset:17408\n\t" \
    "v_mfma_f32_16x16x32_bf16 v[220:223], v[172:175], v[240:243], v[220:223]\n\t" \
    "s_waitcnt lgkmcnt(3)\n\t" \
    "v_mfma_f32_16x16x32_bf16 v[204:207], v[80:83], v[224:227], v[204:207]\n\t" \
    "v_xor_b32 v252, 0x2800, %[xa]\n\t" \
    "ds_read_b128 v[240:243], v252 offset:0\n\t" \
    "v_mfma_f32_16x16x32_bf16 v[208:211], v[88:91], v[224:227], v[208:211]\n\t" \
    "s_waitcnt lgkmcnt(3)\n\t" \
    "v_mfma_f32_16x16x32_bf16 v[216:219], v[80:83], v[228:231], v[216:219]\n\t" \
    "ds_read_b128 v[224:227], v252 offset:16384\n\t" \
    "v_mfma_f32_16x16x32_bf16 v[220:223], v[88:91], v[228:231], v[220:223]\n\t" \
    "s_waitcnt lgkmcnt(3)\n\t" \
    "v_mfma_f32_16x16x32_bf16 v[204:207], v[84:87], v[232:235], v[204:207]\n\t" \
    "ds_read_b128 v[228:231], v252 offset:1024\n\t" \
    "v_mfma_f32_16x16x32_bf16 v[208:211], v[92:95], v[232:235], v[208:211]\n\t" \
    "s_waitcnt lgkmcnt(3)\n\t" \
    "v_mfma_f32_16x16x32_bf16 v[216:219], v[84:87], v[236:239], v[216:219]\n\t" \
    "ds_read_b128 v[232:235], v252 offset:17408\n\t" \
    "v_mfma_f32_16x16x32_bf16 v[220:223], v[92:95], v[236:239], v[220:223]\n\t" \
    "s_waitcnt lgkmcnt(3)\n\t" \
    "v_mfma_f32_16x16x32_bf16 v[204:207], v[104:107], v[240:243], v[204:207]\n\t" \
    "v_xor_b32 v252, 0x3000, %[xa]\n\t" \
    "ds_read_b128 v[236:239], v252 offset:0\n\t" \
    "v_mfma_f32_16x16x32_bf16 v[208:211], v[112:115], v[240:243], v[208:211]\n\t" \
    "s_waitcnt lgkmcnt(3)\n\t" \
    "v_mfma_f32_16x16x32_bf16 v[216:219], v[104:107], v[224:227], v[216:219]\n\t" \
    "ds_read_b128 v[240:243], v252 offset:16384\n\t" \
    "v_mfma_f32_16x16x32_bf16 v[220:223], v[112:115], v[224:227], v[220:223]\n\t" \
    "s_waitcnt lgkmcnt(3)\n\t" \
    "v_mfma_f32_16x16x32_bf16 v[204:207], v[108:111], v[228:231], v[204:207]\n\t" \
    "ds_read_b128 v[224:227], v252 offset:1024\n\t" \
    "v_mfma_f32_16x16x32_bf16 v[208:211], v[116:119], v[228:231], v[208:211]\n\t" \
    "s_waitcnt lgkmcnt(3)\n\t" \
    "v_mfma_f32_16x16x32_bf16 v[216:219], v[108:111], v[232:235], v[216:219]\n\t" \
    "ds_read_b128 v[228:231], v252 offset:17408\n\t" \
    "v_mfma_f32_16x16x32_bf16 v[220:223], v[116:119], v[232:235], v[220:223]\n\t" \
    "s_waitcnt lgkmcnt(3)\n\t" \
    "v_mfma_f32_16x16x32_bf16 v[204:207], v[120:123], v[236:239], v[204:207]\n\t" \
    "v_xor_b32 v252, 0x3800, %[xa]\n\t" \
    "ds_read_b128 v[232:235], v252 offset:0\n\t" \
    "v_mfma_f32_16x16x32_bf16 v[208:211], v[128:131], v[236:239], v[208:211]\n\t" \
    "s_waitcnt lgkmcnt(3)\n\t" \
    "v_mfma_f32_16x16x32_bf16 v[216:219], v[120:123], v[240:243], v[216:219]\n\t" \
    "ds_read_b128 v[236:239], v252 offset:16384\n\t" \
    "v_mfma_f32_16x16x32_bf16 v[220:223], v[128:131], v[240:243], v[220:223]\n\t" \
    "s_waitcnt lgkmcnt(3)\n\t" \
    "v_mfma_f32_16x16x32_bf16 v[204:207], v[124:127], v[224:227], v[204:207]\n\t" \
    "ds_read_b128 v[240:243], v252 offset:1024\n\t" \
    "v_mfma_f32_16x16x32_bf16 v[208:211], v[132:135], v[224:227], v[208:211]\n\t" \
    "s_waitcnt lgkmcnt(3)\n\t" \
    "v_mfma_f32_16x16x32_bf16 v[216:219], v[124:127], v[228:231], v[216:219]\n\t" \
    "ds_read_b128 v[224:227], v252 offset:17408\n\t" \
    "v_mfma_f32_16x16x32_bf16 v[220:223], v[132:135], v[228:231], v[220:223]\n\t" \
    "s_waitcnt lgkmcnt(3)\n\t" \
    "v_mfma_f32_16x16x32_bf16 v[204:207], v[144:147], v[232:235], v[204:207]\n\t" \
    "v_and_b32 v252, 0x3ff, %[xa]\n\t" \
    "ds_read_b128 v[228:231], v252 offset:43008\n\t" \
    "v_mov_b64 v[244:245], 0\n\t" \
    "v_mfma_f32_16x16x32_bf16 v[208:211], v[152:155], v[232:235], v[208:211]\n\t" \
    "v_mov_b64 v[246:247], 0\n\t" \
    "s_waitcnt lgkmcnt(3)\n\t" \
    "v_mfma_f32_16x16x32_bf16 v[216:219], v[144:147], v[236:239], v[216:219]\n\t" \
    "ds_read_b128 v[232:235], v252 offset:44032\n\t" \
    "v_mov_b64 v[248:249], 0\n\t" \
    "v_mfma_f32_16x16x32_bf16 v[220:223], v[152:155], v[236:239], v[220:223]\n\t" \
    "v_mov_b64 v[250:251], 0\n\t" \
    "s_waitcnt lgkmcnt(3)\n\t" \
    "v_mfma_f32_16x16x32_bf16 v[204:207], v[148:151], v[240:243], v[204:207]\n\t" \
    "ds_read_b128 v[236:239], v252 offset:47104\n\t" \
    "v_mfma_f32_16x16x32_bf16 v[208:211], v[156:159], v[240:243], v[208:211]\n\t" \
    "s_waitcnt lgkmcnt(3)\n\t" \
    "v_mfma_f32_16x16x32_bf16 v[216:219], v[148:151], v[224:227], v[216:219]\n\t" \
    "ds_read_b128 v[240:243], v252 offset:48128\n\t" \
    "v_mfma_f32_16x16x32_bf16 v[220:223], v[156:159], v[224:227], v[220:223]\n\t" \
    "s_waitcnt lgkmcnt(3)\n\t" \
    "v_mfma_f32_16x16x32_bf16 v[204:207], v[184:187], v[228:231], v[204:207]\n\t" \
    "v_add_u32 v252, %[qs], v252\n\t" \
    "v_mfma_f32_16x16x32_bf16 v[208:211], v[192:195], v[228:231], v[208:211]\n\t" \
    "v_mov_b64 v[224:225], 0\n\t" \
    "s_waitcnt lgkmcnt(2)\n\t" \
    "v_mfma_f32_16x16x32_bf16 v[204:207], v[188:191], v[232:235], v[204:207]\n\t" \
    "v_mov_b64 v[226:227], 0\n\t" \
    "v_mfma_f32_16x16x32_bf16 v[208:211], v[196:199], v[232:235], v[208:211]\n\t" \
    "s_waitcnt lgkmcnt(1)\n\t" \
    "v_mfma_f32_16x16x32_bf16 v[216:219], v[184:187], v[236:239], v[216:219]\n\t" \
    "ds_read_b128 v[228:231], %[wl] offset:2048\n\t" \
    "ds_read_b128 v[232:235], %[wl] offset:3072\n\t" \
    "v_mfma_f32_16x16x32_bf16 v[220:223], v[192:195], v[236:239], v[220:223]\n\t" \
    "v_cvt_pk_bf16_f32 v204, v204, v205\n\t" \
    "v_cvt_pk_bf16_f32 v205, v206, v207\n\t" \
    "s_waitcnt lgkmcnt(2)\n\t" \
    "v_mfma_f32_16x16x32_bf16 v[216:219], v[188:191], v[240:243], v[216:219]\n\t" \
    "ds_write_b64 v252, v[204:205] offset:1024\n\t" \
    "v_cvt_pk_bf16_f32 v208, v208, v209\n\t" \
    "v_mfma_f32_16x16x32_bf16 v[220:223], v[196:199], v[240:243], v[220:223]\n\t" \
    "v_cvt_pk_bf16_f32 v209, v210, v211\n\t" \
    "ds_write_b64 v252, v[208:209] offset:2048\n\t" \
    "ds_read_b128 v[200:203], %[wl] offset:0\n\t" \
    "ds_read_b128 v[204:207], %[wl] offset:1024\n\t" \
    "v_cvt_pk_bf16_f32 v216, v216, v217\n\t" \
    "v_cvt_pk_bf16_f32 v217, v218, v219\n\t" \
    "ds_write_b64 v252, v[216:217] offset:7168\n\t" \
    "s_nop 0\n\t" \
    "v_cvt_pk_bf16_f32 v220, v220, v221\n\t" \
    "v_cvt_pk_bf16_f32 v221, v222, v223\n\t" \
    "ds_write_b64 v252, v[220:221] offset:8192\n\t" \
    "s_waitcnt lgkmcnt(0)\n\t"
#define ASM_XG_S_LO1 \
    "s_waitcnt lgkmcnt(0)\n\t" \
    "ds_read_b128 v[224:227], %[xa] offset:0\n\t" \
    "ds_read_b128 v[228:231], %[xa] offset:16384\n\t" \
    "ds_read_b128 v[232:235], %[xa] offset:1024\n\t" \
    "ds_read_b128 v[236:239], %[xa] offset:17408\n\t" \
    "s_waitcnt lgkmcnt(3)\n\t" \
    "v_mfma_f32_16x16x32_bf16 v[208:211], v[8:11], v[224:227], 0\n\t" \
    "v_xor_b32 v252, 0x800, %[xa]\n\t" \
    "ds_read_b128 v[240:243], v252 offset:0\n\t" \
    "s_waitcnt lgkmcnt(3)\n\t" \
    "v_mfma_f32_16x16x32_bf16 v[220:223], v[8:11], v[228:231], 0\n\t" \
    "ds_read_b128 v[224:227], v252 offset:16384\n\t" \
    "s_waitcnt lgkmcnt(3)\n\t" \
    "v_mfma_f32_16x16x32_bf16 v[208:211], v[12:15], v[232:235], v[208:211]\n\t" \
    "ds_read_b128 v[228:231], v252 offset:1024\n\t" \
    "s_waitcnt lgkmcnt(3)\n\t" \
    "v_mfma_f32_16x16x32_bf16 v[220:223], v[12:15], v[236:239], v[220:223]\n\t" \
    "ds_read_b128 v[232:235], v252 offset:17408\n\t" \
    "s_waitcnt lgkmcnt(3)\n\t" \
    "v_mfma_f32_16x16x32_bf16 v[208:211], v[32:35], v[240:243], v[208:211]\n\t" \
    "v_xor_b32 v252, 0x1000, %[xa]\n\t" \
    "ds_read_b128 v[236:239], v252 offset:0\n\t" \
    "s_waitcnt lgkmcnt(3)\n\t" \
    "v_mfma_f32_16x16x32_bf16 v[220:223], v[32:35], v[224:227], v[220:223]\n\t" \
    "ds_read_b128 v[240:243], v252 offset:16384\n\t" \
    "s_waitcnt lgkmcnt(3)\n\t" \
    "v_mfma_f32_16x16x32_bf16 v[208:211], v[36:39], v[228:231], v[208:211]\n\t" \
    "ds_read_b128 v[224:227], v252 offset:1024\n\t" \
    "s_waitcnt lgkmcnt(3)\n\t" \
    "v_mfma_f32_16x16x32_bf16 v[220:223], v[36:39], v[232:235], v[220:223]\n\t" \
    "ds_read_b128 v[228:231], v252 offset:17408\n\t" \
    "s_waitcnt lgkmcnt(3)\n\t" \
    "v_mfma_f32_16x16x32_bf16 v[208:211], v[48:51], v[236:239], v[208:211]\n\t" \
    "v_xor_b32 v252, 0x1800, %[xa]\n\t" \
    "ds_read_b128 v[232:235], v252 offset:0\n\t" \
    "s_waitcnt lgkmcnt(3)\n\t" \
    "v_mfma_f32_16x16x32_bf16 v[220:223], v[48:51], v[240:243], v[220:223]\n\t" \
    "ds_read_b128 v[236:239], v252 offset:16384\n\t" \
    "s_waitcnt lgkmcnt(3)\n\t" \
    "v_mfma_f32_16x16x32_bf16 v[208:211], v[52:55], v[224:227], v[208:211]\n\t" \
    "ds_read_b128 v[240:243], v252 offset:1024\n\t" \
    "s_waitcnt lgkmcnt(3)\n\t" \
    "v_mfma_f32_16x16x32_bf16 v[220:223], v[52:55], v[228:231], v[220:223]\n\t" \
    "ds_read_b128 v[224:227], v252 offset:17408\n\t" \
    "s_waitcnt lgkmcnt(3)\n\t" \
    "v_mfma_f32_16x16x32_bf16 v[208:211], v[72:75], v[232:235], v[208:211]\n\t" \
    "v_and_b32 v252, 0x3ff, %[xa]\n\t" \
    "ds_read_b128 v[228:231], v252 offset:40960\n\t" \
    "s_waitcnt lgkmcnt(3)\n\t" \
    "v_mfma_f32_16x16x32_bf16 v[220:223], v[72:75], v[236:239], v[220:223]\n\t" \
    "ds_read_b128 v[232:235], v252 offset:45056\n\t" \
    "s_waitcnt lgkmcnt(3)\n\t" \
    "v_mfma_f32_16x16x32_bf16 v[208:211], v[76:79], v[240:243], v[208:211]\n\t" \
    "ds_read_b128 v[236:239], v252 offset:41984\n\t" \
    "s_waitcnt lgkmcnt(3)\n\t" \
    "v_mfma_f32_16x16x32_bf16 v[220:223], v[76:79], v[224:227], v[220:223]\n\t" \
    "ds_read_b128 v[240:243], v252 offset:46080\n\t" \
    "s_waitcnt lgkmcnt(0)\n\t" \
    "s_barrier\n\t" ASM_ST \
    "v_mfma_f32_16x16x32_bf16 v[208:211], v[168:171], v[228:231], v[208:211]\n\t" \
    "v_xor_b32 v252, 0x2000, %[xa]\n\t" \
    "ds_read_b128 v[224:227], v252 offset:0\n\t" \
    "v_mfma_f32_16x16x32_bf16 v[220:223], v[168:171], v[232:235], v[220:223]\n\t" \
    "ds_read_b128 v[228:231], v252 offset:16384\n\t" \
    "v_mfma_f32_16x16x32_bf16 v[208:211], v[172:175], v[236:239], v[208:211]\n\t" \
    "ds_read_b128 v[232:235], v252 offset:1024\n\t" \
    "v_mfma_f32_16x16x32_bf16 v[220:223], v[172:175], v[240:243], v[220:223]\n\t" \
    "ds_read_b128 v[236:239], v252 offset:17408\n\t" \
    "s_waitcnt lgkmcnt(3)\n\t" \
    "v_mfma_f32_16x16x32_bf16 v[208:211], v[88:91], v[224:227], v[208:211]\n\t" \
    "v_xor_b32 v252, 0x2800, %[xa]\n\t" \
    "ds_read_b128 v[240:243], v252 offset:0\n\t" \
    "s_waitcnt lgkmcnt(3)\n\t" \
    "v_mfma_f32_16x16x32_bf16 v[220:223], v[88:91], v[228:231], v[220:223]\n\t" \
    "ds_read_b128 v[224:227], v252 offset:16384\n\t" \
    "s_waitcnt lgkmcnt(3)\n\t" \
    "v_mfma_f32_16x16x32_bf16 v[208:211], v[92:95], v[232:235], v[208:211]\n\t" \
    "ds_read_b128 v[228:231], v252 offset:1024\n\t" \
    "s_waitcnt lgkmcnt(3)\n\t" \
    "v_mfma_f32_16x16x32_bf16 v[220:223], v[92:95], v[236:239], v[220:223]\n\t" \
    "ds_read_b128 v[232:235], v252 offset:17408\n\t" \
    "s_waitcnt lgkmcnt(3)\n\t" \
    "v_mfma_f32_16x16x32_bf16 v[208:211], v[112:115], v[240:243], v[208:211]\n\t" \
    "v_xor_b32 v252, 0x3000, %[xa]\n\t" \
    "ds_read_b128 v[236:239], v252 offset:0\n\t" \
    "s_waitcnt lgkmcnt(3)\n\t" \
    "v_mfma_f32_16x16x32_bf16 v[220:223], v[112:115], v[224:227], v[220:223]\n\t" \
    "ds_read_b128 v[240:243], v252 offset:16384\n\t" \
    "s_waitcnt lgkmcnt(3)\n\t" \
    "v_mfma_f32_16x16x32_bf16 v[208:211], v[116:119], v[228:231], v[208:211]\n\t" \
    "ds_read_b128 v[224:227], v252 offset:1024\n\t" \
    "s_waitcnt lgkmcnt(3)\n\t" \
    "v_mfma_f32_16x16x32_bf16 v[220:223], v[116:119], v[232:235], v[220:223]\n\t" \
    "ds_read_b128 v[228:231], v252 offset:17408\n\t" \
    "s_waitcnt lgkmcnt(3)\n\t" \
    "v_mfma_f32_16x16x32_bf16 v[208:211], v[128:131], v[236:239], v[208:211]\n\t" \
    "v_xor_b32 v252, 0x3800, %[xa]\n\t" \
    "ds_read_b128 v[232:235], v252 offset:0\n\t" \
    "s_waitcnt lgkmcnt(3)\n\t" \
    "v_mfma_f32_16x16x32_bf16 v[220:223], v[128:131], v[240:243], v[220:223]\n\t" \
    "ds_read_b128 v[236:239], v252 offset:16384\n\t" \
    "s_waitcnt lgkmcnt(3)\n\t" \
    "v_mfma_f32_16x16x32_bf16 v[208:211], v[132:135], v[224:227], v[208:211]\n\t" \
    "ds_read_b128 v[240:243], v252 offset:1024\n\t" \
    "s_waitcnt lgkmcnt(3)\n\t" \
    "v_mfma_f32_16x16x32_bf16 v[220:223], v[132:135], v[228:231], v[220:223]\n\t" \
    "ds_read_b128 v[224:227], v252 offset:17408\n\t" \
    "s_waitcnt lgkmcnt(3)\n\t" \
    "v_mfma_f32_16x16x32_bf16 v[208:211], v[152:155], v[232:235], v[208:211]\n\t" \
    "v_and_b32 v252, 0x3ff, %[xa]\n\t" \
    "ds_read_b128 v[228:231], v252 offset:43008\n\t" \
    "v_mov_b64 v[244:245], 0\n\t" \
    "s_waitcnt lgkmcnt(3)\n\t" \
    "v_mfma_f32_16x16x32_bf16 v[220:223], v[152:155], v[236:239], v[220:223]\n\t" \
    "ds_read_b128 v[232:235], v252 offset:44032\n\t" \
    "v_mov_b64 v[246:247], 0\n\t" \
    "s_waitcnt lgkmcnt(3)\n\t" \
    "v_mfma_f32_16x16x32_bf16 v[208:211], v[156:159], v[240:243], v[208:211]\n\t" \
    "ds_read_b128 v[236:239], v252 offset:47104\n\t" \
    "v_mov_b64 v[248:249], 0\n\t" \
    "s_waitcnt lgkmcnt(3)\n\t" \
    "v_mfma_f32_16x16x32_bf16 v[220:223], v[156:159], v[224:227], v[220:223]\n\t" \
    "ds_read_b128 v[240:243], v252 offset:48128\n\t" \
    "v_mov_b64 v[250:251], 0\n\t" \
    "s_waitcnt lgkmcnt(3)\n\t" \
    "v_mfma_f32_16x16x32_bf16 v[208:211], v[192:195], v[228:231], v[208:211]\n\t" \
    "v_add_u32 v252, %[qs], v252\n\t" \
    "s_waitcnt lgkmcnt(2)\n\t" \
    "v_mfma_f32_16x16x32_bf16 v[208:211], v[196:199], v[232:235], v[208:211]\n\t" \
    "v_mov_b64 v[224:225], 0\n\t" \
    "s_waitcnt lgkmcnt(1)\n\t" \
    "v_mfma_f32_16x16x32_bf16 v[220:223], v[192:195], v[236:239], v[220:223]\n\t" \
    "v_mov_b64 v[226:227], 0\n\t" \
    "ds_read_b128 v[228:231], %[wl] offset:2048\n\t" \
    "s_waitcnt lgkmcnt(1)\n\t" \
    "v_mfma_f32_16x16x32_bf16 v[220:223], v[196:199], v[240:243], v[220:223]\n\t" \
    "ds_read_b128 v[232:235], %[wl] offset:3072\n\t" \
    "v_cvt_pk_bf16_f32 v208, v208, v209\n\t" \
    "v_cvt_pk_bf16_f32 v209, v210, v211\n\t" \
    "ds_write_b64 v252, v[208:209] offset:2048\n\t" \
    "ds_read_b128 v[200:203], %[wl] offset:0\n\t" \
    "ds_read_b128 v[204:207], %[wl] offset:1024\n\t" \
    "s_nop 1\n\t" \
    "v_cvt_pk_bf16_f32 v220, v220, v221\n\t" \
    "v_cvt_pk_bf16_f32 v221, v222, v223\n\t" \
    "ds_write_b64 v252, v[220:221] offset:8192\n\t" \
    "s_waitcnt lgkmcnt(0)\n\t"
#define ASM_XG_V \
    "s_waitcnt lgkmcnt(0)\n\t" \
    "ds_read_b128 v[224:227], %[xa] offset:0\n\t" \
    "ds_read_b128 v[244:247], %[wl] offset:0\n\t" \
    "ds_read_b128 v[228:231], %[xa] offset:16384\n\t" \
    "ds_read_b128 v[232:235], %[xa] offset:1024\n\t" \
    "ds_read_b128 v[236:239], %[xa] offset:17408\n\t" \
    "ds_read_b128 v[248:251], %[wl] offset:1024\n\t" \
    "s_waitcnt lgkmcnt(5)\n\t" \
    "v_mfma_f32_16x16x32_bf16 v[204:207], v[0:3], v[224:227], 0\n\t" \
    "v_xor_b32 v252, 0x800, %[xa]\n\t" \
    "ds_read_b128 v[240:243], v252 offset:0\n\t" \
    "v_mfma_f32_16x16x32_bf16 v[208:211], v[8:11], v[224:227], 0\n\t" \
    "s_waitcnt lgkmcnt(5)\n\t" \
    "v_mfma_f32_16x16x32_bf16 v[200:203], v[244:247], v[224:227], 0\n\t" \
    "s_waitcnt lgkmcnt(4)\n\t" \
    "v_mfma_f32_16x16x32_bf16 v[216:219], v[0:3], v[228:231], 0\n\t" \
    "ds_read_b128 v[224:227], v252 offset:16384\n\t" \
    "v_mfma_f32_16x16x32_bf16 v[220:223], v[8:11], v[228:231], 0\n\t" \
    "v_mfma_f32_16x16x32_bf16 v[212:215], v[244:247], v[228:231], 0\n\t" \
    "s_waitcnt lgkmcnt(4)\n\t" \
    "v_mfma_f32_16x16x32_bf16 v[204:207], v[4:7], v[232:235], v[204:207]\n\t" \
    "ds_read_b128 v[228:231], v252 offset:1024\n\t" \
    "v_mfma_f32_16x16x32_bf16 v[208:211], v[12:15], v[232:235], v[208:211]\n\t" \
    "ds_read_b128 v[244:247], %[wl] offset:2048\n\t" \
    "s_waitcnt lgkmcnt(4)\n\t" \
    "v_mfma_f32_16x16x32_bf16 v[200:203], v[248:251], v[232:235], v[200:203]\n\t" \
    "v_mfma_f32_16x16x32_bf16 v[216:219], v[4:7], v[236:239], v[216:219]\n\t" \
    "ds_read_b128 v[232:235], v252 offset:17408\n\t" \
    "v_mfma_f32_16x16x32_bf16 v[220:223], v[12:15], v[236:239], v[220:223]\n\t" \
    "v_mfma_f32_16x16x32_bf16 v[212:215], v[248:251], v[236:239], v[212:215]\n\t" \
    "s_waitcnt lgkmcnt(4)\n\t" \
    "v_mfma_f32_16x16x32_bf16 v[204:207], v[24:27], v[240:243], v[204:207]\n\t" \
    "v_xor_b32 v252, 0x1000, %[xa]\n\t" \
    "ds_read_b128 v[236:239], v252 offset:0\n\t" \
    "v_mfma_f32_16x16x32_bf16 v[208:211], v[32:35], v[240:243], v[208:211]\n\t" \
    "ds_read_b128 v[248:251], %[wl] offset:3072\n\t" \
    "v_mfma_f32_16x16x32_bf16 v[200:203], v[16:19], v[240:243], v[200:203]\n\t" \
    "s_waitcnt lgkmcnt(5)\n\t" \
    "v_mfma_f32_16x16x32_bf16 v[216:219], v[24:27], v[224:227], v[216:219]\n\t" \
    "ds_read_b128 v[240:243], v252 offset:16384\n\t" \
    "v_mfma_f32_16x16x32_bf16 v[220:223], v[32:35], v[224:227], v[220:223]\n\t" \
    "v_mfma_f32_16x16x32_bf16 v[212:215], v[16:19], v[224:227], v[212:215]\n\t" \
    "s_waitcnt lgkmcnt(5)\n\t" \
    "v_mfma_f32_16x16x32_bf16 v[204:207], v[28:31], v[228:231], v[204:207]\n\t" \
    "ds_read_b128 v[224:227], v252 offset:1024\n\t" \
    "v_mfma_f32_16x16x32_bf16 v[208:211], v[36:39], v[228:231], v[208:211]\n\t" \
    "v_mfma_f32_16x16x32_bf16 v[200:203], v[20:23], v[228:231], v[200:203]\n\t" \
    "s_waitcnt lgkmcnt(4)\n\t" \
    "v_mfma_f32_16x16x32_bf16 v[216:219], v[28:31], v[232:235], v[216:219]\n\t" \
    "ds_read_b128 v[228:231], v252 offset:17408\n\t" \
    "v_mfma_f32_16x16x32_bf16 v[220:223], v[36:39], v[232:235], v[220:223]\n\t" \
    "v_mfma_f32_16x16x32_bf16 v[212:215], v[20:23], v[232:235], v[212:215]\n\t" \
    "s_waitcnt lgkmcnt(4)\n\t" \
    "v_mfma_f32_16x16x32_bf16 v[204:207], v[40:43], v[236:239], v[204:207]\n\t" \
    "v_xor_b32 v252, 0x1800, %[xa]\n\t" \
    "ds_read_b128 v[232:235], v252 offset:0\n\t" \
    "v_mfma_f32_16x16x32_bf16 v[208:211], v[48:51], v[236:239], v[208:211]\n\t" \
    "v_mfma_f32_16x16x32_bf16 v[200:203], v[244:247], v[236:239], v[200:203]\n\t" \
    "s_waitcnt lgkmcnt(3)\n\t" \
    "v_mfma_f32_16x16x32_bf16 v[216:219], v[40:43], v[240:243], v[216:219]\n\t" \
    "ds_read_b128 v[236:239], v252 offset:16384\n\t" \
    "v_mfma_f32_16x16x32_bf16 v[220:223], v[48:51], v[240:243], v[220:223]\n\t" \
    "v_mfma_f32_16x16x32_bf16 v[212:215], v[244:247], v[240:243], v[212:215]\n\t" \
    "s_waitcnt lgkmcnt(3)\n\t" \
    "v_mfma_f32_16x16x32_bf16 v[204:207], v[44:47], v[224:227], v[204:207]\n\t" \
    "ds_read_b128 v[240:243], v252 offset:1024\n\t" \
    "v_mfma_f32_16x16x32_bf16 v[208:211], v[52:55], v[224:227], v[208:211]\n\t" \
    "ds_read_b128 v[244:247], %[wl] offset:8192\n\t" \
    "v_mfma_f32_16x16x32_bf16 v[200:203], v[248:251], v[224:227], v[200:203]\n\t" \
    "s_waitcnt lgkmcnt(4)\n\t" \
    "v_mfma_f32_16x16x32_bf16 v[216:219], v[44:47], v[228:231], v[216:219]\n\t" \
    "ds_read_b128 v[224:227], v252 offset:17408\n\t" \
    "v_mfma_f32_16x16x32_bf16 v[220:223], v[52:55], v[228:231], v[220:223]\n\t" \
    "v_mfma_f32_16x16x32_bf16 v[212:215], v[248:251], v[228:231], v[212:215]\n\t" \
    "s_waitcnt lgkmcnt(4)\n\t" \
    "v_mfma_f32_16x16x32_bf16 v[204:207], v[64:67], v[232:235], v[204:207]\n\t" \
    "v_and_b32 v252, 0x3ff, %[xa]\n\t" \
    "ds_read_b128 v[228:231], v252 offset:40960\n\t" \
    "v_mfma_f32_16x16x32_bf16 v[208:211], v[72:75], v[232:235], v[208:211]\n\t" \
    "ds_read_b128 v[248:251], %[wl] offset:9216\n\t" \
    "v_mfma_f32_16x16x32_bf16 v[200:203], v[56:59], v[232:235], v[200:203]\n\t" \
    "s_waitcnt lgkmcnt(5)\n\t" \
    "v_mfma_f32_16x16x32_bf16 v[216:219], v[64:67], v[236:239], v[216:219]\n\t" \
    "ds_read_b128 v[232:235], v252 offset:45056\n\t" \
    "v_mfma_f32_16x16x32_bf16 v[220:223], v[72:75], v[236:239], v[220:223]\n\t" \
    "v_mfma_f32_16x16x32_bf16 v[212:215], v[56:59], v[236:239], v[212:215]\n\t" \
    "s_waitcnt lgkmcnt(5)\n\t" \
    "v_mfma_f32_16x16x32_bf16 v[204:207], v[68:71], v[240:243], v[204:207]\n\t" \
    "ds_read_b128 v[236:239], v252 offset:41984\n\t" \
    "v_mfma_f32_16x16x32_bf16 v[208:211], v[76:79], v[240:243], v[208:211]\n\t" \
    "v_mfma_f32_16x16x32_bf16 v[200:203], v[60:63], v[240:243], v[200:203]\n\t" \
    "s_waitcnt lgkmcnt(4)\n\t" \
    "v_mfma_f32_16x16x32_bf16 v[216:219], v[68:71], v[224:227], v[216:219]\n\t" \
    "ds_read_b128 v[240:243], v252 offset:46080\n\t" \
    "v_mfma_f32_16x16x32_bf16 v[220:223], v[76:79], v[224:227], v[220:223]\n\t" \
    "v_mfma_f32_16x16x32_bf16 v[212:215], v[60:63], v[224:227], v[212:215]\n\t" \
    "s_waitcnt lgkmcnt(4)\n\t" \
    "v_mfma_f32_16x16x32_bf16 v[204:207], v[160:163], v[228:231], v[204:207]\n\t" \
    "v_xor_b32 v252, 0x2000, %[xa]\n\t" \
    "ds_read_b128 v[224:227], v252 offset:0\n\t" \
    "v_mfma_f32_16x16x32_bf16 v[208:211], v[168:171], v[228:231], v[208:211]\n\t" \
    "v_mfma_f32_16x16x32_bf16 v[200:203], v[244:247], v[228:231], v[200:203]\n\t" \
    "s_waitcnt lgkmcnt(3)\n\t" \
    "v_mfma_f32_16x16x32_bf16 v[216:219], v[160:163], v[232:235], v[216:219]\n\t" \
    "ds_read_b128 v[228:231], v252 offset:16384\n\t" \
    "v_mfma_f32_16x16x32_bf16 v[220:223], v[168:171], v[232:235], v[220:223]\n\t" \
    "v_mfma_f32_16x16x32_bf16 v[212:215], v[244:247], v[232:235], v[212:215]\n\t" \
    "s_waitcnt lgkmcnt(3)\n\t" \
    "v_mfma_f32_16x16x32_bf16 v[204:207], v[164:167], v[236:239], v[204:207]\n\t" \
    "ds_read_b128 v[232:235], v252 offset:1024\n\t" \
    "v_mfma_f32_16x16x32_bf16 v[208:211], v[172:175], v[236:239], v[208:211]\n\t" \
    "ds_read_b128 v[244:247], %[wl] offset:4096\n\t" \
    "v_mfma_f32_16x16x32_bf16 v[200:203], v[248:251], v[236:239], v[200:203]\n\t" \
    "s_waitcnt lgkmcnt(4)\n\t" \
    "v_mfma_f32_16x16x32_bf16 v[216:219], v[164:167], v[240:243], v[216:219]\n\t" \
    "ds_read_b128 v[236:239], v252 offset:17408\n\t" \
    "v_mfma_f32_16x16x32_bf16 v[220:223], v[172:175], v[240:243], v[220:223]\n\t" \
    "v_mfma_f32_16x16x32_bf16 v[212:215], v[248:251], v[240:243], v[212:215]\n\t" \
    "s_waitcnt lgkmcnt(4)\n\t" \
    "v_mfma_f32_16x16x32_bf16 v[204:207], v[80:83], v[224:227], v[204:207]\n\t" \
    "v_xor_b32 v252, 0x2800, %[xa]\n\t" \
    "ds_read_b128 v[240:243], v252 offset:0\n\t" \
    "v_mfma_f32_16x16x32_bf16 v[208:211], v[88:91], v[224:227], v[208:211]\n\t" \
    "ds_read_b128 v[248:251], %[wl] offset:5120\n\t" \
    "s_waitcnt lgkmcnt(3)\n\t" \
    "v_mfma_f32_16x16x32_bf16 v[200:203], v[244:247], v[224:227], v[200:203]\n\t" \
    "v_mfma_f32_16x16x32_bf16 v[216:219], v[80:83], v[228:231], v[216:219]\n\t" \
    "ds_read_b128 v[224:227], v252 offset:16384\n\t" \
    "v_mfma_f32_16x16x32_bf16 v[220:223], v[88:91], v[228:231], v[220:223]\n\t" \
    "v_mfma_f32_16x16x32_bf16 v[212:215], v[244:247], v[228:231], v[212:215]\n\t" \
    "v_mfma_f32_16x16x32_bf16 v[204:207], v[84:87], v[232:235], v[204:207]\n\t" \
    "ds_read_b128 v[228:231], v252 offset:1024\n\t" \
    "v_mfma_f32_16x16x32_bf16 v[208:211], v[92:95], v[232:235], v[208:211]\n\t" \
    "ds_read_b128 v[244:247], %[wl] offset:6144\n\t" \
    "s_waitcnt lgkmcnt(3)\n\t" \
    "v_mfma_f32_16x16x32_bf16 v[200:203], v[248:251], v[232:235], v[200:203]\n\t" \
    "v_mfma_f32_16x16x32_bf16 v[216:219], v[84:87], v[236:239], v[216:219]\n\t" \
    "ds_read_b128 v[232:235], v252 offset:17408\n\t" \
    "v_mfma_f32_16x16x32_bf16 v[220:223], v[92:95], v[236:239], v[220:223]\n\t" \
    "v_mfma_f32_16x16x32_bf16 v[212:215], v[248:251], v[236:239], v[212:215]\n\t" \
    "v_mfma_f32_16x16x32_bf16 v[204:207], v[104:107], v[240:243], v[204:207]\n\t" \
    "v_xor_b32 v252, 0x3000, %[xa]\n\t" \
    "ds_read_b128 v[236:239], v252 offset:0\n\t" \
    "v_mfma_f32_16x16x32_bf16 v[208:211], v[112:115], v[240:243], v[208:211]\n\t" \
    "ds_read_b128 v[248:251], %[wl] offset:7168\n\t" \
    "v_mfma_f32_16x16x32_bf16 v[200:203], v[96:99], v[240:243], v[200:203]\n\t" \
    "s_waitcnt lgkmcnt(5)\n\t" \
    "v_mfma_f32_16x16x32_bf16 v[216:219], v[104:107], v[224:227], v[216:219]\n\t" \
    "ds_read_b128 v[240:243], v252 offset:16384\n\t" \
    "v_mfma_f32_16x16x32_bf16 v[220:223], v[112:115], v[224:227], v[220:223]\n\t" \
    "v_mfma_f32_16x16x32_bf16 v[212:215], v[96:99], v[224:227], v[212:215]\n\t" \
    "s_waitcnt lgkmcnt(5)\n\t" \
    "v_mfma_f32_16x16x32_bf16 v[204:207], v[108:111], v[228:231], v[204:207]\n\t" \
    "ds_read_b128 v[224:227], v252 offset:1024\n\t" \
    "v_mfma_f32_16x16x32_bf16 v[208:211], v[116:119], v[228:231], v[208:211]\n\t" \
    "v_mfma_f32_16x16x32_bf16 v[200:203], v[100:103], v[228:231], v[200:203]\n\t" \
    "s_waitcnt lgkmcnt(4)\n\t" \
    "v_mfma_f32_16x16x32_bf16 v[216:219], v[108:111], v[232:235], v[216:219]\n\t" \
    "ds_read_b128 v[228:231], v252 offset:17408\n\t" \
    "v_mfma_f32_16x16x32_bf16 v[220:223], v[116:119], v[232:235], v[220:223]\n\t" \
    "v_mfma_f32_16x16x32_bf16 v[212:215], v[100:103], v[232:235], v[212:215]\n\t" \
    "s_waitcnt lgkmcnt(4)\n\t" \
    "v_mfma_f32_16x16x32_bf16 v[204:207], v[120:123], v[236:239], v[204:207]\n\t" \
    "v_xor_b32 v252, 0x3800, %[xa]\n\t" \
    "ds_read_b128 v[232:235], v252 offset:0\n\t" \
    "v_mfma_f32_16x16x32_bf16 v[208:211], v[128:131], v[236:239], v[208:211]\n\t" \
    "v_mfma_f32_16x16x32_bf16 v[200:203], v[244:247], v[236:239], v[200:203]\n\t" \
    "s_waitcnt lgkmcnt(3)\n\t" \
    "v_mfma_f32_16x16x32_bf16 v[216:219], v[120:123], v[240:243], v[216:219]\n\t" \
    "ds_read_b128 v[236:239], v252 offset:16384\n\t" \
    "v_mfma_f32_16x16x32_bf16 v[220:223], v[128:131], v[240:243], v[220:223]\n\t" \
    "v_mfma_f32_16x16x32_bf16 v[212:215], v[244:247], v[240:243], v[212:215]\n\t" \
    "s_waitcnt lgkmcnt(3)\n\t" \
    "v_mfma_f32_16x16x32_bf16 v[204:207], v[124:127], v[224:227], v[204:207]\n\t" \
    "ds_read_b128 v[240:243], v252 offset:1024\n\t" \
    "v_mfma_f32_16x16x32_bf16 v[208:211], v[132:135], v[224:227], v[208:211]\n\t" \
    "v_mfma_f32_16x16x32_bf16 v[200:203], v[248:251], v[224:227], v[200:203]\n\t" \
    "s_waitcnt lgkmcnt(3)\n\t" \
    "v_mfma_f32_16x16x32_bf16 v[216:219], v[124:127], v[228:231], v[216:219]\n\t" \
    "ds_read_b128 v[224:227], v252 offset:17408\n\t" \
    "v_mfma_f32_16x16x32_bf16 v[220:223], v[132:135], v[228:231], v[220:223]\n\t" \
    "v_mfma_f32_16x16x32_bf16 v[212:215], v[248:251], v[228:231], v[212:215]\n\t" \
    "s_waitcnt lgkmcnt(3)\n\t" \
    "v_mfma_f32_16x16x32_bf16 v[204:207], v[144:147], v[232:235], v[204:207]\n\t" \
    "v_and_b32 v252, 0x3ff, %[xa]\n\t" \
    "ds_read_b128 v[228:231], v252 offset:43008\n\t" \
    "v_mov_b64 v[244:245], 0\n\t" \
    "v_mfma_f32_16x16x32_bf16 v[208:211], v[152:155], v[232:235], v[208:211]\n\t" \
    "v_mov_b64 v[246:247], 0\n\t" \
    "v_mfma_f32_16x16x32_bf16 v[200:203], v[136:139], v[232:235], v[200:203]\n\t" \
    "v_mov_b64 v[248:249], 0\n\t" \
    "s_waitcnt lgkmcnt(3)\n\t" \
    "v_mfma_f32_16x16x32_bf16 v[216:219], v[144:147], v[236:239], v[216:219]\n\t" \
    "ds_read_b128 v[232:235], v252 offset:44032\n\t" \
    "v_mov_b64 v[250:251], 0\n\t" \
    "v_mfma_f32_16x16x32_bf16 v[220:223], v[152:155], v[236:239], v[220:223]\n\t" \
    "v_mfma_f32_16x16x32_bf16 v[212:215], v[136:139], v[236:239], v[212:215]\n\t" \
    "s_waitcnt lgkmcnt(3)\n\t" \
    "v_mfma_f32_16x16x32_bf16 v[204:207], v[148:151], v[240:243], v[204:207]\n\t" \
    "ds_read_b128 v[236:239], v252 offset:47104\n\t" \
    "v_mfma_f32_16x16x32_bf16 v[208:211], v[156:159], v[240:243], v[208:211]\n\t" \
    "v_mfma_f32_16x16x32_bf16 v[200:203], v[140:143], v[240:243], v[200:203]\n\t" \
    "s_waitcnt lgkmcnt(3)\n\t" \
    "v_mfma_f32_16x16x32_bf16 v[216:219], v[148:151], v[224:227], v[216:219]\n\t" \
    "ds_read_b128 v[240:243], v252 offset:48128\n\t" \
    "v_mfma_f32_16x16x32_bf16 v[220:223], v[156:159], v[224:227], v[220:223]\n\t" \
    "v_mfma_f32_16x16x32_bf16 v[212:215], v[140:143], v[224:227], v[212:215]\n\t" \
    "s_waitcnt lgkmcnt(3)\n\t" \
    "v_mfma_f32_16x16x32_bf16 v[204:207], v[184:187], v[228:231], v[204:207]\n\t" \
    "v_add_u32 v252, %[qs], v252\n\t" \
    "v_mfma_f32_16x16x32_bf16 v[208:211], v[192:195], v[228:231], v[208:211]\n\t" \
    "v_mov_b64 v[224:225], 0\n\t" \
    "v_mfma_f32_16x16x32_bf16 v[200:203], v[176:179], v[228:231], v[200:203]\n\t" \
    "v_mov_b64 v[226:227], 0\n\t" \
    "s_waitcnt lgkmcnt(2)\n\t" \
    "v_mfma_f32_16x16x32_bf16 v[204:207], v[188:191], v[232:235], v[204:207]\n\t" \
    "v_mfma_f32_16x16x32_bf16 v[208:211], v[196:199], v[232:235], v[208:211]\n\t" \
    "v_mfma_f32_16x16x32_bf16 v[200:203], v[180:183], v[232:235], v[200:203]\n\t" \
    "s_waitcnt lgkmcnt(1)\n\t" \
    "v_mfma_f32_16x16x32_bf16 v[216:219], v[184:187], v[236:239], v[216:219]\n\t" \
    "ds_read_b128 v[228:231], %[wl] offset:2048\n\t" \
    "ds_read_b128 v[232:235], %[wl] offset:3072\n\t" \
    "v_mfma_f32_16x16x32_bf16 v[220:223], v[192:195], v[236:239], v[220:223]\n\t" \
    "v_cvt_pk_bf16_f32 v204, v204, v205\n\t" \
    "v_cvt_pk_bf16_f32 v205, v206, v207\n\t" \
    "v_mfma_f32_16x16x32_bf16 v[212:215], v[176:179], v[236:239], v[212:215]\n\t" \
    "ds_write_b64 v252, v[204:205] offset:1024\n\t" \
    "v_cvt_pk_bf16_f32 v208, v208, v209\n\t" \
    "s_waitcnt lgkmcnt(3)\n\t" \
    "v_mfma_f32_16x16x32_bf16 v[216:219], v[188:191], v[240:243], v[216:219]\n\t" \
    "v_cvt_pk_bf16_f32 v209, v210, v211\n\t" \
    "ds_write_b64 v252, v[208:209] offset:2048\n\t" \
    "v_mfma_f32_16x16x32_bf16 v[220:223], v[196:199], v[240:243], v[220:223]\n\t" \
    "v_cvt_pk_bf16_f32 v200, v200, v201\n\t" \
    "v_cvt_pk_bf16_f32 v201, v202, v203\n\t" \
    "v_mfma_f32_16x16x32_bf16 v[212:215], v[180:183], v[240:243], v[212:215]\n\t" \
    "ds_write_b64 v252, v[200:201] offset:0\n\t" \
    "ds_read_b128 v[200:203], %[wl] offset:0\n\t" \
    "ds_read_b128 v[204:207], %[wl] offset:1024\n\t" \
    "v_cvt_pk_bf16_f32 v216, v216, v217\n\t" \
    "v_cvt_pk_bf16_f32 v217, v218, v219\n\t" \
    "ds_write_b64 v252, v[216:217] offset:7168\n\t" \
    "v_cvt_pk_bf16_f32 v220, v220, v221\n\t" \
    "v_cvt_pk_bf16_f32 v221, v222, v223\n\t" \
    "ds_write_b64 v252, v[220:221] offset:8192\n\t" \
    "v_cvt_pk_bf16_f32 v212, v212, v213\n\t" \
    "v_cvt_pk_bf16_f32 v213, v214, v215\n\t" \
    "ds_write_b64 v252, v[212:213] offset:6144\n\t" \
    "s_waitcnt lgkmcnt(0)\n\t"
#define ASM_XG_H_HI \
    "s_waitcnt lgkmcnt(0)\n\t" \
    "v_xor_b32 v252, 0x2000, %[xa]\n\t" \
    "ds_read_b128 v[224:227], v252 offset:0\n\t" \
    "ds_read_b128 v[244:247], %[wl] offset:4096\n\t" \
    "ds_read_b128 v[228:231], v252 offset:16384\n\t" \
    "ds_read_b128 v[232:235], v252 offset:1024\n\t" \
    "ds_read_b128 v[236:239], v252 offset:17408\n\t" \
    "ds_read_b128 v[248:251], %[wl] offset:5120\n\t" \
    "s_waitcnt lgkmcnt(5)\n\t" \
    "v_mfma_f32_16x16x32_bf16 v[204:207], v[80:83], v[224:227], 0\n\t" \
    "v_xor_b32 v252, 0x2800, %[xa]\n\t" \
    "ds_read_b128 v[240:243], v252 offset:0\n\t" \
    "v_mfma_f32_16x16x32_bf16 v[208:211], v[88:91], v[224:227], 0\n\t" \
    "s_waitcnt lgkmcnt(5)\n\t" \
    "v_mfma_f32_16x16x32_bf16 v[200:203], v[244:247], v[224:227], 0\n\t" \
    "s_waitcnt lgkmcnt(4)\n\t" \
    "v_mfma_f32_16x16x32_bf16 v[216:219], v[80:83], v[228:231], 0\n\t" \
    "ds_read_b128 v[224:227], v252 offset:16384\n\t" \
    "v_mfma_f32_16x16x32_bf16 v[220:223], v[88:91], v[228:231], 0\n\t" \
    "v_mfma_f32_16x16x32_bf16 v[212:215], v[244:247], v[228:231], 0\n\t" \
    "s_waitcnt lgkmcnt(4)\n\t" \
    "v_mfma_f32_16x16x32_bf16 v[204:207], v[84:87], v[232:235], v[204:207]\n\t" \
    "ds_read_b128 v[228:231], v252 offset:1024\n\t" \
    "v_mfma_f32_16x16x32_bf16 v[208:211], v[92:95], v[232:235], v[208:211]\n\t" \
    "ds_read_b128 v[244:247], %[wl] offset:6144\n\t" \
    "s_waitcnt lgkmcnt(4)\n\t" \
    "v_mfma_f32_16x16x32_bf16 v[200:203], v[248:251], v[232:235], v[200:203]\n\t" \
    "v_mfma_f32_16x16x32_bf16 v[216:219], v[84:87], v[236:239], v[216:219]\n\t" \
    "ds_read_b128 v[232:235], v252 offset:17408\n\t" \
    "v_mfma_f32_16x16x32_bf16 v[220:223], v[92:95], v[236:239], v[220:223]\n\t" \
    "v_mfma_f32_16x16x32_bf16 v[212:215], v[248:251], v[236:239], v[212:215]\n\t" \
    "s_waitcnt lgkmcnt(4)\n\t" \
    "v_mfma_f32_16x16x32_bf16 v[204:207], v[104:107], v[240:243], v[204:207]\n\t" \
    "v_xor_b32 v252, 0x3000, %[xa]\n\t" \
    "ds_read_b128 v[236:239], v252 offset:0\n\t" \
    "v_mfma_f32_16x16x32_bf16 v[208:211], v[112:115], v[240:243], v[208:211]\n\t" \
    "ds_read_b128 v[248:251], %[wl] offset:7168\n\t" \
    "v_mfma_f32_16x16x32_bf16 v[200:203], v[96:99], v[240:243], v[200:203]\n\t" \
    "s_waitcnt lgkmcnt(5)\n\t" \
    "v_mfma_f32_16x16x32_bf16 v[216:219], v[104:107], v[224:227], v[216:219]\n\t" \
    "ds_read_b128 v[240:243], v252 offset:16384\n\t" \
    "v_mfma_f32_16x16x32_bf16 v[220:223], v[112:115], v[224:227], v[220:223]\n\t" \
    "v_mfma_f32_16x16x32_bf16 v[212:215], v[96:99], v[224:227], v[212:215]\n\t" \
    "s_nop 3\n\t" \
    "v_mov_b32 v224, %[tv]\n\t" \
    "s_waitcnt vmcnt(5)\n\t" \
    "buffer_store_dword v224, %[fv], %[rr], %[fp] offen sc1\n\t" \
    "s_waitcnt lgkmcnt(5)\n\t" \
    "v_mfma_f32_16x16x32_bf16 v[204:207], v[108:111], v[228:231], v[204:207]\n\t" \
    "ds_read_b128 v[224:227], v252 offset:1024\n\t" \
    "v_mfma_f32_16x16x32_bf16 v[208:211], v[116:119], v[228:231], v[208:211]\n\t" \
    "v_mfma_f32_16x16x32_bf16 v[200:203], v[100:103], v[228:231], v[200:203]\n\t" \
    "s_waitcnt lgkmcnt(4)\n\t" \
    "v_mfma_f32_16x16x32_bf16 v[216:219], v[108:111], v[232:235], v[216:219]\n\t" \
    "ds_read_b128 v[228:231], v252 offset:17408\n\t" \
    "v_mfma_f32_16x16x32_bf16 v[220:223], v[116:119], v[232:235], v[220:223]\n\t" \
    "v_mfma_f32_16x16x32_bf16 v[212:215], v[100:103], v[232:235], v[212:215]\n\t" \
    "s_waitcnt lgkmcnt(4)\n\t" \
    "v_mfma_f32_16x16x32_bf16 v[204:207], v[120:123], v[236:239], v[204:207]\n\t" \
    "v_xor_b32 v252, 0x3800, %[xa]\n\t" \
    "ds_read_b128 v[232:235], v252 offset:0\n\t" \
    "v_mfma_f32_16x16x32_bf16 v[208:211], v[128:131], v[236:239], v[208:211]\n\t" \
    "v_mfma_f32_16x16x32_bf16 v[200:203], v[244:247], v[236:239], v[200:203]\n\t" \
    "s_waitcnt lgkmcnt(3)\n\t" \
    "v_mfma_f32_16x16x32_bf16 v[216:219], v[120:123], v[240:243], v[216:219]\n\t" \
    "ds_read_b128 v[236:239], v252 offset:16384\n\t" \
    "v_mfma_f32_16x16x32_bf16 v[220:223], v[128:131], v[240:243], v[220:223]\n\t" \
    "v_mfma_f32_16x16x32_bf16 v[212:215], v[244:247], v[240:243], v[212:215]\n\t" \
    "s_waitcnt lgkmcnt(3)\n\t" \
    "v_mfma_f32_16x16x32_bf16 v[204:207], v[124:127], v[224:227], v[204:207]\n\t" \
    "ds_read_b128 v[240:243], v252 offset:1024\n\t" \
    "v_mfma_f32_16x16x32_bf16 v[208:211], v[132:135], v[224:227], v[208:211]\n\t" \
    "ds_read_b128 v[244:247], %[wl] offset:8192\n\t" \
    "v_mfma_f32_16x16x32_bf16 v[200:203], v[248:251], v[224:227], v[200:203]\n\t" \
    "s_waitcnt lgkmcnt(4)\n\t" \
    "v_mfma_f32_16x16x32_bf16 v[216:219], v[124:127], v[228:231], v[216:219]\n\t" \
    "ds_read_b128 v[224:227], v252 offset:17408\n\t" \
    "v_mfma_f32_16x16x32_bf16 v[220:223], v[132:135], v[228:231], v[220:223]\n\t" \
    "v_mfma_f32_16x16x32_bf16 v[212:215], v[248:251], v[228:231], v[212:215]\n\t" \
    "s_waitcnt lgkmcnt(4)\n\t" \
    "v_mfma_f32_16x16x32_bf16 v[204:207], v[144:147], v[232:235], v[204:207]\n\t" \
    "v_and_b32 v252, 0x3ff, %[xa]\n\t" \
    "ds_read_b128 v[228:231], v252 offset:40960\n\t" \
    "v_mfma_f32_16x16x32_bf16 v[208:211], v[152:155], v[232:235], v[208:211]\n\t" \
    "ds_read_b128 v[248:251], %[wl] offset:9216\n\t" \
    "v_mfma_f32_16x16x32_bf16 v[200:203], v[136:139], v[232:235], v[200:203]\n\t" \
    "s_waitcnt lgkmcnt(5)\n\t" \
    "v_mfma_f32_16x16x32_bf16 v[216:219], v[144:147], v[236:239], v[216:219]\n\t" \
    "ds_read_b128 v[232:235], v252 offset:45056\n\t" \
    "v_mfma_f32_16x16x32_bf16 v[220:223], v[152:155], v[236:239], v[220:223]\n\t" \
    "v_mfma_f32_16x16x32_bf16 v[212:215], v[136:139], v[236:239], v[212:215]\n\t" \
    "s_waitcnt lgkmcnt(5)\n\t" \
    "v_mfma_f32_16x16x32_bf16 v[204:207], v[148:151], v[240:243], v[204:207]\n\t" \
    "ds_read_b128 v[236:239], v252 offset:41984\n\t" \
    "v_mfma_f32_16x16x32_bf16 v[208:211], v[156:159], v[240:243], v[208:211]\n\t" \
    "v_mfma_f32_16x16x32_bf16 v[200:203], v[140:143], v[240:243], v[200:203]\n\t" \
    "s_waitcnt lgkmcnt(4)\n\t" \
    "v_mfma_f32_16x16x32_bf16 v[216:219], v[148:151], v[224:227], v[216:219]\n\t" \
    "ds_read_b128 v[240:243], v252 offset:46080\n\t" \
    "v_mfma_f32_16x16x32_bf16 v[220:223], v[156:159], v[224:227], v[220:223]\n\t" \
    "v_mfma_f32_16x16x32_bf16 v[212:215], v[140:143], v[224:227], v[212:215]\n\t" \
    "s_waitcnt vmcnt(1) lgkmcnt(0)\n\t" \
    "s_barrier\n\t" ASM_ST \
    "v_mfma_f32_16x16x32_bf16 v[204:207], v[160:163], v[228:231], v[204:207]\n\t" \
    "ds_read_b128 v[224:227], %[xa] offset:0\n\t" \
    "v_mfma_f32_16x16x32_bf16 v[208:211], v[168:171], v[228:231], v[208:211]\n\t" \
    "v_mfma_f32_16x16x32_bf16 v[200:203], v[244:247], v[228:231], v[200:203]\n\t" \
    "v_mfma_f32_16x16x32_bf16 v[216:219], v[160:163], v[232:235], v[216:219]\n\t" \
    "ds_read_b128 v[228:231], %[xa] offset:16384\n\t" \
    "v_mfma_f32_16x16x32_bf16 v[220:223], v[168:171], v[232:235], v[220:223]\n\t" \
    "v_mfma_f32_16x16x32_bf16 v[212:215], v[244:247], v[232:235], v[212:215]\n\t" \
    "v_mfma_f32_16x16x32_bf16 v[204:207], v[164:167], v[236:239], v[204:207]\n\t" \
    "ds_read_b128 v[232:235], %[xa] offset:1024\n\t" \
    "v_mfma_f32_16x16x32_bf16 v[208:211], v[172:175], v[236:239], v[208:211]\n\t" \
    "ds_read_b128 v[244:247], %[wl] offset:0\n\t" \
    "v_mfma_f32_16x16x32_bf16 v[200:203], v[248:251], v[236:239], v[200:203]\n\t" \
    "v_mfma_f32_16x16x32_bf16 v[216:219], v[164:167], v[240:243], v[216:219]\n\t" \
    "ds_read_b128 v[236:239], %[xa] offset:17408\n\t" \
    "v_mfma_f32_16x16x32_bf16 v[220:223], v[172:175], v[240:243], v[220:223]\n\t" \
    "v_mfma_f32_16x16x32_bf16 v[212:215], v[248:251], v[240:243], v[212:215]\n\t" \
    "s_waitcnt lgkmcnt(4)\n\t" \
    "v_mfma_f32_16x16x32_bf16 v[204:207], v[0:3], v[224:227], v[204:207]\n\t" \
    "v_xor_b32 v252, 0x800, %[xa]\n\t" \
    "ds_read_b128 v[240:243], v252 offset:0\n\t" \
    "v_mfma_f32_16x16x32_bf16 v[208:211], v[8:11], v[224:227], v[208:211]\n\t" \
    "ds_read_b128 v[248:251], %[wl] offset:1024\n\t" \
    "s_waitcnt lgkmcnt(3)\n\t" \
    "v_mfma_f32_16x16x32_bf16 v[200:203], v[244:247], v[224:227], v[200:203]\n\t" \
    "v_mfma_f32_16x16x32_bf16 v[216:219], v[0:3], v[228:231], v[216:219]\n\t" \
    "ds_read_b128 v[224:227], v252 offset:16384\n\t" \
    "v_mfma_f32_16x16x32_bf16 v[220:223], v[8:11], v[228:231], v[220:223]\n\t" \
    "v_mfma_f32_16x16x32_bf16 v[212:215], v[244:247], v[228:231], v[212:215]\n\t" \
    "v_mfma_f32_16x16x32_bf16 v[204:207], v[4:7], v[232:235], v[204:207]\n\t" \
    "ds_read_b128 v[228:231], v252 offset:1024\n\t" \
    "v_mfma_f32_16x16x32_bf16 v[208:211], v[12:15], v[232:235], v[208:211]\n\t" \
    "ds_read_b128 v[244:247], %[wl] offset:2048\n\t" \
    "s_waitcnt lgkmcnt(3)\n\t" \
    "v_mfma_f32_16x16x32_bf16 v[200:203], v[248:251], v[232:235], v[200:203]\n\t" \
    "v_mfma_f32_16x16x32_bf16 v[216:219], v[4:7], v[236:239], v[216:219]\n\t" \
    "ds_read_b128 v[232:235], v252 offset:17408\n\t" \
    "v_mfma_f32_16x16x32_bf16 v[220:223], v[12:15], v[236:239], v[220:223]\n\t" \
    "v_mfma_f32_16x16x32_bf16 v[212:215], v[248:251], v[236:239], v[212:215]\n\t" \
    "v_mfma_f32_16x16x32_bf16 v[204:207], v[24:27], v[240:243], v[204:207]\n\t" \
    "v_xor_b32 v252, 0x1000, %[xa]\n\t" \
    "ds_read_b128 v[236:239], v252 offset:0\n\t" \
    "v_mfma_f32_16x16x32_bf16 v[208:211], v[32:35], v[240:243], v[208:211]\n\t" \
    "ds_read_b128 v[248:251], %[wl] offset:3072\n\t" \
    "v_mfma_f32_16x16x32_bf16 v[200:203], v[16:19], v[240:243], v[200:203]\n\t" \
    "s_waitcnt lgkmcnt(5)\n\t" \
    "v_mfma_f32_16x16x32_bf16 v[216:219], v[24:27], v[224:227], v[216:219]\n\t" \
    "ds_read_b128 v[240:243], v252 offset:16384\n\t" \
    "v_mfma_f32_16x16x32_bf16 v[220:223], v[32:35], v[224:227], v[220:223]\n\t" \
    "v_mfma_f32_16x16x32_bf16 v[212:215], v[16:19], v[224:227], v[212:215]\n\t" \
    "s_waitcnt lgkmcnt(5)\n\t" \
    "v_mfma_f32_16x16x32_bf16 v[204:207], v[28:31], v[228:231], v[204:207]\n\t" \
    "ds_read_b128 v[224:227], v252 offset:1024\n\t" \
    "v_mfma_f32_16x16x32_bf16 v[208:211], v[36:39], v[228:231], v[208:211]\n\t" \
    "v_mfma_f32_16x16x32_bf16 v[200:203], v[20:23], v[228:231], v[200:203]\n\t" \
    "s_waitcnt lgkmcnt(4)\n\t" \
    "v_mfma_f32_16x16x32_bf16 v[216:219], v[28:31], v[232:235], v[216:219]\n\t" \
    "ds_read_b128 v[228:231], v252 offset:17408\n\t" \
    "v_mfma_f32_16x16x32_bf16 v[220:223], v[36:39], v[232:235], v[220:223]\n\t" \
    "v_mfma_f32_16x16x32_bf16 v[212:215], v[20:23], v[232:235], v[212:215]\n\t" \
    "s_waitcnt lgkmcnt(4)\n\t" \
    "v_mfma_f32_16x16x32_bf16 v[204:207], v[40:43], v[236:239], v[204:207]\n\t" \
    "v_xor_b32 v252, 0x1800, %[xa]\n\t" \
    "ds_read_b128 v[232:235], v252 offset:0\n\t" \
    "v_mfma_f32_16x16x32_bf16 v[208:211], v[48:51], v[236:239], v[208:211]\n\t" \
    "v_mfma_f32_16x16x32_bf16 v[200:203], v[244:247], v[236:239], v[200:203]\n\t" \
    "s_waitcnt lgkmcnt(3)\n\t" \
    "v_mfma_f32_16x16x32_bf16 v[216:219], v[40:43], v[240:243], v[216:219]\n\t" \
    "ds_read_b128 v[236:239], v252 offset:16384\n\t" \
    "v_mfma_f32_16x16x32_bf16 v[220:223], v[48:51], v[240:243], v[220:223]\n\t" \
    "v_mfma_f32_16x16x32_bf16 v[212:215], v[244:247], v[240:243], v[212:215]\n\t" \
    "s_waitcnt lgkmcnt(3)\n\t" \
    "v_mfma_f32_16x16x32_bf16 v[204:207], v[44:47], v[224:227], v[204:207]\n\t" \
    "ds_read_b128 v[240:243], v252 offset:1024\n\t" \
    "v_mfma_f32_16x16x32_bf16 v[208:211], v[52:55], v[224:227], v[208:211]\n\t" \
    "v_mfma_f32_16x16x32_bf16 v[200:203], v[248:251], v[224:227], v[200:203]\n\t" \
    "s_waitcnt lgkmcnt(3)\n\t" \
    "v_mfma_f32_16x16x32_bf16 v[216:219], v[44:47], v[228:231], v[216:219]\n\t" \
    "ds_read_b128 v[224:227], v252 offset:17408\n\t" \
    "v_mfma_f32_16x16x32_bf16 v[220:223], v[52:55], v[228:231], v[220:223]\n\t" \
    "v_mfma_f32_16x16x32_bf16 v[212:215], v[248:251], v[228:231], v[212:215]\n\t" \
    "s_waitcnt lgkmcnt(3)\n\t" \
    "v_mfma_f32_16x16x32_bf16 v[204:207], v[64:67], v[232:235], v[204:207]\n\t" \
    "v_and_b32 v252, 0x3ff, %[xa]\n\t" \
    "ds_read_b128 v[228:231], v252 offset:43008\n\t" \
    "v_mov_b64 v[244:245], 0\n\t" \
    "v_mfma_f32_16x16x32_bf16 v[208:211], v[72:75], v[232:235], v[208:211]\n\t" \
    "v_mov_b64 v[246:247], 0\n\t" \
    "v_mfma_f32_16x16x32_bf16 v[200:203], v[56:59], v[232:235], v[200:203]\n\t" \
    "v_mov_b64 v[248:249], 0\n\t" \
    "s_waitcnt lgkmcnt(3)\n\t" \
    "v_mfma_f32_16x16x32_bf16 v[216:219], v[64:67], v[236:239], v[216:219]\n\t" \
    "ds_read_b128 v[232:235], v252 offset:44032\n\t" \
    "v_mov_b64 v[250:251], 0\n\t" \
    "v_mfma_f32_16x16x32_bf16 v[220:223], v[72:75], v[236:239], v[220:223]\n\t" \
    "v_mfma_f32_16x16x32_bf16 v[212:215], v[56:59], v[236:239], v[212:215]\n\t" \
    "s_waitcnt lgkmcnt(3)\n\t" \
    "v_mfma_f32_16x16x32_bf16 v[204:207], v[68:71], v[240:243], v[204:207]\n\t" \
    "ds_read_b128 v[236:239], v252 offset:47104\n\t" \
    "v_mfma_f32_16x16x32_bf16 v[208:211], v[76:79], v[240:243], v[208:211]\n\t" \
    "v_mfma_f32_16x16x32_bf16 v[200:203], v[60:63], v[240:243], v[200:203]\n\t" \
    "s_waitcnt lgkmcnt(3)\n\t" \
    "v_mfma_f32_16x16x32_bf16 v[216:219], v[68:71], v[224:227], v[216:219]\n\t" \
    "ds_read_b128 v[240:243], v252 offset:48128\n\t" \
    "v_mfma_f32_16x16x32_bf16 v[220:223], v[76:79], v[224:227], v[220:223]\n\t" \
    "v_mfma_f32_16x16x32_bf16 v[212:215], v[60:63], v[224:227], v[212:215]\n\t" \
    "s_waitcnt lgkmcnt(3)\n\t" \
    "v_mfma_f32_16x16x32_bf16 v[204:207], v[184:187], v[228:231], v[204:207]\n\t" \
    "v_add_u32 v252, %[qs], v252\n\t" \
    "v_mfma_f32_16x16x32_bf16 v[208:211], v[192:195], v[228:231], v[208:211]\n\t" \
    "v_mov_b64 v[224:225], 0\n\t" \
    "v_mfma_f32_16x16x32_bf16 v[200:203], v[176:179], v[228:231], v[200:203]\n\t" \
    "v_mov_b64 v[226:227], 0\n\t" \
    "s_waitcnt lgkmcnt(2)\n\t" \
    "v_mfma_f32_16x16x32_bf16 v[204:207], v[188:191], v[232:235], v[204:207]\n\t" \
    "v_mfma_f32_16x16x32_bf16 v[208:211], v[196:199], v[232:235], v[208:211]\n\t" \
    "v_mfma_f32_16x16x32_bf16 v[200:203], v[180:183], v[232:235], v[200:203]\n\t" \
    "s_waitcnt lgkmcnt(1)\n\t" \
    "v_mfma_f32_16x16x32_bf16 v[216:219], v[184:187], v[236:239], v[216:219]\n\t" \
    "ds_read_b128 v[228:231], %[wl] offset:2048\n\t" \
    "ds_read_b128 v[232:235], %[wl] offset:3072\n\t" \
    "v_mfma_f32_16x16x32_bf16 v[220:223], v[192:195], v[236:239], v[220:223]\n\t" \
    "v_cvt_pk_bf16_f32 v204, v204, v205\n\t" \
    "v_cvt_pk_bf16_f32 v205, v206, v207\n\t" \
    "v_mfma_f32_16x16x32_bf16 v[212:215], v[176:179], v[236:239], v[212:215]\n\t" \
    "ds_write_b64 v252, v[204:205] offset:1024\n\t" \
    "v_cvt_pk_bf16_f32 v208, v208, v209\n\t" \
    "s_waitcnt lgkmcnt(3)\n\t" \
    "v_mfma_f32_16x16x32_bf16 v[216:219], v[188:191], v[240:243], v[216:219]\n\t" \
    "v_cvt_pk_bf16_f32 v209, v210, v211\n\t" \
    "ds_write_b64 v252, v[208:209] offset:2048\n\t" \
    "v_mfma_f32_16x16x32_bf16 v[220:223], v[196:199], v[240:243], v[220:223]\n\t" \
    "v_cvt_pk_bf16_f32 v200, v200, v201\n\t" \
    "v_cvt_pk_bf16_f32 v201, v202, v203\n\t" \
    "v_mfma_f32_16x16x32_bf16 v[212:215], v[180:183], v[240:243], v[212:215]\n\t" \
    "ds_write_b64 v252, v[200:201] offset:0\n\t" \
    "ds_read_b128 v[200:203], %[wl] offset:0\n\t" \
    "ds_read_b128 v[204:207], %[wl] offset:1024\n\t" \
    "v_cvt_pk_bf16_f32 v216, v216, v217\n\t" \
    "v_cvt_pk_bf16_f32 v217, v218, v219\n\t" \
    "ds_write_b64 v252, v[216:217] offset:7168\n\t" \
    "v_cvt_pk_bf16_f32 v220, v220, v221\n\t" \
    "v_cvt_pk_bf16_f32 v221, v222, v223\n\t" \
    "ds_write_b64 v252, v[220:221] offset:8192\n\t" \
    "v_cvt_pk_bf16_f32 v212, v212, v213\n\t" \
    "v_cvt_pk_bf16_f32 v213, v214, v215\n\t" \
    "ds_write_b64 v252, v[212:213] offset:6144\n\t" \
    "s_waitcnt lgkmcnt(0)\n\t"
#define ASM_CRIT1_3 \
    "s_branch .Lc0%=\n\t" \
    ".Lcr%=:\n\t" \
    "s_cmp_eq_u32 %[n], 0\n\t" \
    "s_cbranch_scc1 .Lcb%=\n\t" \
    "s_sub_u32 %[n], %[n], 1\n\t" \
    "v_max_u32 v243, v208, v209\n\t" \
    "v_max3_u32 v243, v210, v211, v243\n\t" \
    "v_cmp_eq_u32 vcc, -1, v243\n\t" \
    "s_cbranch_vccz .Lcs0_%=\n\t" \
    "buffer_load_dwordx4 v[208:211], %[ab], %[rs], %[s0] offen sc1\n\t" \
    ".Lcs0_%=:\n\t" \
    "v_max_u32 v243, v236, v237\n\t" \
    "v_max3_u32 v243, v238, v239, v243\n\t" \
    "v_cmp_eq_u32 vcc, -1, v243\n\t" \
    "s_cbranch_vccz .Lcs1_%=\n\t" \
    "buffer_load_dwordx4 v[236:239], %[ab], %[rs], %[s1] offen sc1\n\t" \
    ".Lcs1_%=:\n\t" \
    "v_max_u32 v243, v220, v221\n\t" \
    "v_max3_u32 v243, v222, v223, v243\n\t" \
    "v_cmp_eq_u32 vcc, -1, v243\n\t" \
    "s_cbranch_vccz .Lca%=\n\t" \
    "buffer_load_dwordx4 v[220:223], %[ab], %[rs], %[s2] offen sc1\n\t" \
    "s_branch .Lca%=\n\t" \
    ".Lc0%=:\n\t" \
    "buffer_load_dwordx4 v[208:211], %[ab], %[rs], %[s0] offen sc1\n\t" \
    "buffer_load_dwordx4 v[236:239], %[ab], %[rs], %[s1] offen sc1\n\t" \
    "buffer_load_dwordx4 v[220:223], %[ab], %[rs], %[s2] offen sc1\n\t" \
    "v_bfe_i32 v241, %[ab], 7, 1\n\t" \
    "v_and_b32 v241, 0xaaaa, v241\n\t" \
    "v_or_b32 v241, 0x4444, v241\n\t" \
    "v_xor_b32 v212, 0x400, %[ab]\n\t" \
    "v_xor_b32 v216, 0x800, %[ab]\n\t" \
    "v_xor_b32 v242, 0xc00, %[ab]\n\t" \
    "v_xor_b32 v240, 0x1400, %[ab]\n\t" \
    ".Lca%=:\n\t" \
    "s_waitcnt vmcnt(0)\n\t" \
    "ds_write_b128 %[ab], v[208:211] offset:0\n\t" \
    "ds_write_b128 %[ab], v[236:239] offset:4096\n\t" \
    "ds_write_b128 %[ab], v[220:223] offset:8192\n\t" \
    "v_max_u32 v243, v208, v209\n\t" \
    "v_max3_u32 v243, v210, v211, v243\n\t" \
    "v_max3_u32 v243, v236, v237, v243\n\t" \
    "v_max3_u32 v243, v238, v239, v243\n\t" \
    "v_max3_u32 v243, v220, v221, v243\n\t" \
    "v_max3_u32 v243, v222, v223, v243\n\t" \
    "v_cmp_eq_u32 vcc, -1, v243\n\t" \
    "s_cbranch_vccnz .Lcr%=\n\t" \
    ".Lcb%=:\n\t" \
    ASM_ST2 \
    "v_smfmac_f32_16x16x64_bf16 v[224:227], v[208:211], v[200:207], v241\n\t" \
    "ds_read_b128 v[200:203], %[wl] offset:4096\n\t" \
    "ds_read_b128 v[204:207], %[wl] offset:5120\n\t" \
    "v_smfmac_f32_16x16x64_bf16 v[244:247], v[208:211], v[0:7], v241\n\t" \
    "v_smfmac_f32_16x16x64_bf16 v[248:251], v[208:211], v[8:15], v241\n\t" \
    "s_waitcnt lgkmcnt(2)\n\t" \
    "s_barrier\n\t" ASM_ST \
    "ds_read_b128 v[212:215], v212 offset:0\n\t" \
    "ds_read_b128 v[216:219], v216 offset:0\n\t" \
    "ds_read_b128 v[220:223], v242 offset:0\n\t" \
    "ds_read_b128 v[208:211], v240 offset:0\n\t" \
    "v_smfmac_f32_16x16x64_bf16 v[244:247], v[236:239], v[80:87], v241\n\t" \
    "v_xor_b32 v240, 0x1800, %[ab]\n\t" \
    "v_smfmac_f32_16x16x64_bf16 v[248:251], v[236:239], v[88:95], v241\n\t" \
    "s_waitcnt lgkmcnt(4)\n\t" \
    "v_smfmac_f32_16x16x64_bf16 v[224:227], v[236:239], v[200:207], v241\n\t" \
    "ds_read_b128 v[200:203], %[wl] offset:6144\n\t" \
    "ds_read_b128 v[204:207], %[wl] offset:7168\n\t" \
    "ds_read_b128 v[236:239], v240 offset:0\n\t" \
    "s_waitcnt lgkmcnt(6)\n\t" \
    "v_smfmac_f32_16x16x64_bf16 v[224:227], v[212:215], v[16:23], v241\n\t" \
    "v_smfmac_f32_16x16x64_bf16 v[244:247], v[212:215], v[24:31], v241\n\t" \
    "v_xor_b32 v240, 0x1c00, %[ab]\n\t" \
    "v_smfmac_f32_16x16x64_bf16 v[248:251], v[212:215], v[32:39], v241\n\t" \
    "ds_read_b128 v[212:215], v240 offset:0\n\t" \
    "s_waitcnt lgkmcnt(6)\n\t" \
    "v_smfmac_f32_16x16x64_bf16 v[224:227], v[216:219], v[228:235], v241\n\t" \
    "ds_read_b128 v[228:231], %[wl] offset:8192\n\t" \
    "ds_read_b128 v[232:235], %[wl] offset:9216\n\t" \
    "v_smfmac_f32_16x16x64_bf16 v[244:247], v[216:219], v[40:47], v241\n\t" \
    "v_and_b32 v240, 0x3ff, %[ab]\n\t" \
    "v_smfmac_f32_16x16x64_bf16 v[248:251], v[216:219], v[48:55], v241\n\t" \
    "ds_read_b128 v[216:219], v240 offset:8192\n\t" \
    "s_waitcnt lgkmcnt(8)\n\t" \
    "v_smfmac_f32_16x16x64_bf16 v[224:227], v[220:223], v[56:63], v241\n\t" \
    "v_smfmac_f32_16x16x64_bf16 v[244:247], v[220:223], v[64:71], v241\n\t" \
    "v_smfmac_f32_16x16x64_bf16 v[248:251], v[220:223], v[72:79], v241\n\t" \
    "ds_read_b128 v[220:223], v240 offset:9216\n\t" \
    "s_waitcnt lgkmcnt(8)\n\t" \
    "v_smfmac_f32_16x16x64_bf16 v[244:247], v[208:211], v[104:111], v241\n\t" \
    "s_waitcnt lgkmcnt(5)\n\t" \
    "v_smfmac_f32_16x16x64_bf16 v[244:247], v[236:239], v[120:127], v241\n\t" \
    "s_waitcnt lgkmcnt(4)\n\t" \
    "v_smfmac_f32_16x16x64_bf16 v[244:247], v[212:215], v[144:151], v241\n\t" \
    "s_waitcnt lgkmcnt(1)\n\t" \
    "v_smfmac_f32_16x16x64_bf16 v[244:247], v[216:219], v[160:167], v241\n\t" \
    "s_waitcnt lgkmcnt(0)\n\t" \
    "v_smfmac_f32_16x16x64_bf16 v[244:247], v[220:223], v[184:191], v241\n\t" \
    "v_smfmac_f32_16x16x64_bf16 v[248:251], v[208:211], v[112:119], v241\n\t" \
    "v_smfmac_f32_16x16x64_bf16 v[248:251], v[236:239], v[128:135], v241\n\t" \
    "v_smfmac_f32_16x16x64_bf16 v[248:251], v[212:215], v[152:159], v241\n\t" \
    "v_permlane32_swap_b32 v244, v246\n\t" \
    "v_permlane32_swap_b32 v245, v247\n\t" \
    "v_smfmac_f32_16x16x64_bf16 v[248:251], v[216:219], v[168:175], v241\n\t" \
    "v_add_f32 v244, v244, v246\n\t" \
    "v_add_f32 v245, v245, v247\n\t" \
    "v_smfmac_f32_16x16x64_bf16 v[248:251], v[220:223], v[192:199], v241\n\t" \
    "v_cvt_pk_bf16_f32 v244, v244, v245\n\t" \
    "ds_write_b16 %[dst], v244 offset:512\n\t" \
    "ds_write_b16_d16_hi %[dst], v244 offset:528\n\t" \
    "v_smfmac_f32_16x16x64_bf16 v[224:227], v[208:211], v[96:103], v241\n\t" \
    "v_smfmac_f32_16x16x64_bf16 v[224:227], v[236:239], v[200:207], v241\n\t" \
    "v_smfmac_f32_16x16x64_bf16 v[224:227], v[212:215], v[136:143], v241\n\t" \
    "v_permlane32_swap_b32 v248, v250\n\t" \
    "v_permlane32_swap_b32 v249, v251\n\t" \
    "v_smfmac_f32_16x16x64_bf16 v[224:227], v[216:219], v[228:235], v241\n\t" \
    "v_add_f32 v248, v248, v250\n\t" \
    "v_add_f32 v249, v249, v251\n\t" \
    "v_smfmac_f32_16x16x64_bf16 v[224:227], v[220:223], v[176:183], v241\n\t" \
    "v_cvt_pk_bf16_f32 v248, v248, v249\n\t" \
    "ds_write_b16 %[dst], v248 offset:1024\n\t" \
    "ds_write_b16_d16_hi %[dst], v248 offset:1040\n\t" \
    "s_nop 4\n\t" \
    "v_permlane32_swap_b32 v224, v226\n\t" \
    "v_permlane32_swap_b32 v225, v227\n\t" \
    "v_add_f32 v224, v224, v226\n\t" \
    "v_add_f32 v225, v225, v227\n\t" \
    "v_cvt_pk_bf16_f32 v224, v224, v225\n\t" \
    "ds_write_b16 %[dst], v224 offset:0\n\t" \
    "ds_write_b16_d16_hi %[dst], v224 offset:16\n\t"
#define ASM_CRIT1_2 \
    "s_branch .Lc0%=\n\t" \
    ".Lcr%=:\n\t" \
    "s_cmp_eq_u32 %[n], 0\n\t" \
    "s_cbranch_scc1 .Lcb%=\n\t" \
    "s_sub_u32 %[n], %[n], 1\n\t" \
    "v_max_u32 v243, v208, v209\n\t" \
    "v_max3_u32 v243, v210, v211, v243\n\t" \
    "v_cmp_eq_u32 vcc, -1, v243\n\t" \
    "s_cbranch_vccz .Lcs0_%=\n\t" \
    "buffer_load_dwordx4 v[208:211], %[ab], %[rs], %[s0] offen sc1\n\t" \
    ".Lcs0_%=:\n\t" \
    "v_max_u32 v243, v236, v237\n\t" \
    "v_max3_u32 v243, v238, v239, v243\n\t" \
    "v_cmp_eq_u32 vcc, -1, v243\n\t" \
    "s_cbranch_vccz .Lca%=\n\t" \
    "buffer_load_dwordx4 v[236:239], %[ab], %[rs], %[s1] offen sc1\n\t" \
    "s_branch .Lca%=\n\t" \
    ".Lc0%=:\n\t" \
    "buffer_load_dwordx4 v[208:211], %[ab], %[rs], %[s0] offen sc1\n\t" \
    "buffer_load_dwordx4 v[236:239], %[ab], %[rs], %[s1] offen sc1\n\t" \
    "v_bfe_i32 v241, %[ab], 7, 1\n\t" \
    "v_and_b32 v241, 0xaaaa, v241\n\t" \
    "v_or_b32 v241, 0x4444, v241\n\t" \
    "v_xor_b32 v212, 0x400, %[ab]\n\t" \
    "v_xor_b32 v216, 0x800, %[ab]\n\t" \
    "v_xor_b32 v242, 0xc00, %[ab]\n\t" \
    "v_xor_b32 v240, 0x1400, %[ab]\n\t" \
    ".Lca%=:\n\t" \
    "s_waitcnt vmcnt(0)\n\t" \
    "ds_write_b128 %[ab], v[208:211] offset:0\n\t" \
    "ds_write_b128 %[ab], v[236:239] offset:4096\n\t" \
    "v_max_u32 v243, v208, v209\n\t" \
    "v_max3_u32 v243, v210, v211, v243\n\t" \
    "v_max3_u32 v243, v236, v237, v243\n\t" \
    "v_max3_u32 v243, v238, v239, v243\n\t" \
    "v_cmp_eq_u32 vcc, -1, v243\n\t" \
    "s_cbranch_vccnz .Lcr%=\n\t" \
    ".Lcb%=:\n\t" \
    ASM_ST2 \
    "v_smfmac_f32_16x16x64_bf16 v[224:227], v[208:211], v[200:207], v241\n\t" \
    "ds_read_b128 v[200:203], %[wl] offset:4096\n\t" \
    "ds_read_b128 v[204:207], %[wl] offset:5120\n\t" \
    "v_smfmac_f32_16x16x64_bf16 v[244:247], v[208:211], v[0:7], v241\n\t" \
    "v_smfmac_f32_16x16x64_bf16 v[248:251], v[208:211], v[8:15], v241\n\t" \
    "s_waitcnt lgkmcnt(2)\n\t" \
    "s_barrier\n\t" ASM_ST \
    "ds_read_b128 v[212:215], v212 offset:0\n\t" \
    "ds_read_b128 v[216:219], v216 offset:0\n\t" \
    "ds_read_b128 v[220:223], v242 offset:0\n\t" \
    "ds_read_b128 v[208:211], v240 offset:0\n\t" \
    "v_smfmac_f32_16x16x64_bf16 v[244:247], v[236:239], v[80:87], v241\n\t" \
    "v_xor_b32 v240, 0x1800, %[ab]\n\t" \
    "v_smfmac_f32_16x16x64_bf16 v[248:251], v[236:239], v[88:95], v241\n\t" \
    "s_waitcnt lgkmcnt(4)\n\t" \
    "v_smfmac_f32_16x16x64_bf16 v[224:227], v[236:239], v[200:207], v241\n\t" \
    "ds_read_b128 v[200:203], %[wl] offset:6144\n\t" \
    "ds_read_b128 v[204:207], %[wl] offset:7168\n\t" \
    "ds_read_b128 v[236:239], v240 offset:0\n\t" \
    "s_waitcnt lgkmcnt(6)\n\t" \
    "v_smfmac_f32_16x16x64_bf16 v[224:227], v[212:215], v[16:23], v241\n\t" \
    "v_smfmac_f32_16x16x64_bf16 v[244:247], v[212:215], v[24:31], v241\n\t" \
    "v_xor_b32 v240, 0x1c00, %[ab]\n\t" \
    "v_smfmac_f32_16x16x64_bf16 v[248:251], v[212:215], v[32:39], v241\n\t" \
    "ds_read_b128 v[212:215], v240 offset:0\n\t" \
    "s_waitcnt lgkmcnt(6)\n\t" \
    "v_smfmac_f32_16x16x64_bf16 v[224:227], v[216:219], v[228:235], v241\n\t" \
    "ds_read_b128 v[228:231], %[wl] offset:8192\n\t" \
    "ds_read_b128 v[232:235], %[wl] offset:9216\n\t" \
    "v_smfmac_f32_16x16x64_bf16 v[244:247], v[216:219], v[40:47], v241\n\t" \
    "v_and_b32 v240, 0x3ff, %[ab]\n\t" \
    "v_smfmac_f32_16x16x64_bf16 v[248:251], v[216:219], v[48:55], v241\n\t" \
    "ds_read_b128 v[216:219], v240 offset:8192\n\t" \
    "s_waitcnt lgkmcnt(8)\n\t" \
    "v_smfmac_f32_16x16x64_bf16 v[224:227], v[220:223], v[56:63], v241\n\t" \
    "v_smfmac_f32_16x16x64_bf16 v[244:247], v[220:223], v[64:71], v241\n\t" \
    "v_smfmac_f32_16x16x64_bf16 v[248:251], v[220:223], v[72:79], v241\n\t" \
    "ds_read_b128 v[220:223], v240 offset:9216\n\t" \
    "s_waitcnt lgkmcnt(8)\n\t" \
    "v_smfmac_f32_16x16x64_bf16 v[244:247], v[208:211], v[104:111], v241\n\t" \
    "s_waitcnt lgkmcnt(5)\n\t" \
    "v_smfmac_f32_16x16x64_bf16 v[244:247], v[236:239], v[120:127], v241\n\t" \
    "s_waitcnt lgkmcnt(4)\n\t" \
    "v_smfmac_f32_16x16x64_bf16 v[244:247], v[212:215], v[144:151], v241\n\t" \
    "s_waitcnt lgkmcnt(1)\n\t" \
    "v_smfmac_f32_16x16x64_bf16 v[244:247], v[216:219], v[160:167], v241\n\t" \
    "s_waitcnt lgkmcnt(0)\n\t" \
    "v_smfmac_f32_16x16x64_bf16 v[244:247], v[220:223], v[184:191], v241\n\t" \
    "v_smfmac_f32_16x16x64_bf16 v[248:251], v[208:211], v[112:119], v241\n\t" \
    "v_smfmac_f32_16x16x64_bf16 v[248:251], v[236:239], v[128:135], v241\n\t" \
    "v_smfmac_f32_16x16x64_bf16 v[248:251], v[212:215], v[152:159], v241\n\t" \
    "v_permlane32_swap_b32 v244, v246\n\t" \
    "v_permlane32_swap_b32 v245, v247\n\t" \
    "v_smfmac_f32_16x16x64_bf16 v[248:251], v[216:219], v[168:175], v241\n\t" \
    "v_add_f32 v244, v244, v246\n\t" \
    "v_add_f32 v245, v245, v247\n\t" \
    "v_smfmac_f32_16x16x64_bf16 v[248:251], v[220:223], v[192:199], v241\n\t" \
    "v_cvt_pk_bf16_f32 v244, v244, v245\n\t" \
    "ds_write_b16 %[dst], v244 offset:512\n\t" \
    "ds_write_b16_d16_hi %[dst], v244 offset:528\n\t" \
    "v_smfmac_f32_16x16x64_bf16 v[224:227], v[208:211], v[96:103], v241\n\t" \
    "v_smfmac_f32_16x16x64_bf16 v[224:227], v[236:239], v[200:207], v241\n\t" \
    "v_smfmac_f32_16x16x64_bf16 v[224:227], v[212:215], v[136:143], v241\n\t" \
    "v_permlane32_swap_b32 v248, v250\n\t" \
    "v_permlane32_swap_b32 v249, v251\n\t" \
    "v_smfmac_f32_16x16x64_bf16 v[224:227], v[216:219], v[228:235], v241\n\t" \
    "v_add_f32 v248, v248, v250\n\t" \
    "v_add_f32 v249, v249, v251\n\t" \
    "v_smfmac_f32_16x16x64_bf16 v[224:227], v[220:223], v[176:183], v241\n\t" \
    "v_cvt_pk_bf16_f32 v248, v248, v249\n\t" \
    "ds_write_b16 %[dst], v248 offset:1024\n\t" \
    "ds_write_b16_d16_hi %[dst], v248 offset:1040\n\t" \
    "s_nop 4\n\t" \
    "v_permlane32_swap_b32 v224, v226\n\t" \
    "v_permlane32_swap_b32 v225, v227\n\t" \
    "v_add_f32 v224, v224, v226\n\t" \
    "v_add_f32 v225, v225, v227\n\t" \
    "v_cvt_pk_bf16_f32 v224, v224, v225\n\t" \
    "ds_write_b16 %[dst], v224 offset:0\n\t" \
    "ds_write_b16_d16_hi %[dst], v224 offset:16\n\t"
#define ASM_CRIT1_N \
    "v_bfe_i32 v241, %[ab], 7, 1\n\t" \
    "v_and_b32 v241, 0xaaaa, v241\n\t" \
    "v_or_b32 v241, 0x4444, v241\n\t" \
    "v_xor_b32 v212, 0x400, %[ab]\n\t" \
    "v_xor_b32 v216, 0x800, %[ab]\n\t" \
    "v_xor_b32 v242, 0xc00, %[ab]\n\t" \
    "v_xor_b32 v240, 0x1400, %[ab]\n\t" \
    "v_xor_b32 v236, 0x1000, %[ab]\n\t" \
    "s_waitcnt lgkmcnt(0)\n\t" \
    "s_barrier\n\t" ASM_ST \
    "ds_read_b128 v[208:211], %[ab] offset:0\n\t" \
    "ds_read_b128 v[212:215], v212 offset:0\n\t" \
    "ds_read_b128 v[216:219], v216 offset:0\n\t" \
    "ds_read_b128 v[220:223], v242 offset:0\n\t" \
    "ds_read_b128 v[236:239], v236 offset:0\n\t" \
    "s_waitcnt lgkmcnt(4)\n\t" \
    "v_smfmac_f32_16x16x64_bf16 v[224:227], v[208:211], v[200:207], v241\n\t" \
    "ds_read_b128 v[200:203], %[wl] offset:4096\n\t" \
    "ds_read_b128 v[204:207], %[wl] offset:5120\n\t" \
    "v_smfmac_f32_16x16x64_bf16 v[244:247], v[208:211], v[0:7], v241\n\t" \
    "v_smfmac_f32_16x16x64_bf16 v[248:251], v[208:211], v[8:15], v241\n\t" \
    "ds_read_b128 v[208:211], v240 offset:0\n\t" \
    "s_waitcnt lgkmcnt(6)\n\t" \
    "v_smfmac_f32_16x16x64_bf16 v[224:227], v[212:215], v[16:23], v241\n\t" \
    "v_smfmac_f32_16x16x64_bf16 v[244:247], v[212:215], v[24:31], v241\n\t" \
    "v_xor_b32 v240, 0x1800, %[ab]\n\t" \
    "v_smfmac_f32_16x16x64_bf16 v[248:251], v[212:215], v[32:39], v241\n\t" \
    "ds_read_b128 v[212:215], v240 offset:0\n\t" \
    "s_waitcnt lgkmcnt(6)\n\t" \
    "v_smfmac_f32_16x16x64_bf16 v[224:227], v[216:219], v[228:235], v241\n\t" \
    "ds_read_b128 v[228:231], %[wl] offset:6144\n\t" \
    "ds_read_b128 v[232:235], %[wl] offset:7168\n\t" \
    "v_smfmac_f32_16x16x64_bf16 v[244:247], v[216:219], v[40:47], v241\n\t" \
    "v_xor_b32 v240, 0x1c00, %[ab]\n\t" \
    "v_smfmac_f32_16x16x64_bf16 v[248:251], v[216:219], v[48:55], v241\n\t" \
    "ds_read_b128 v[216:219], v240 offset:0\n\t" \
    "s_waitcnt lgkmcnt(8)\n\t" \
    "v_smfmac_f32_16x16x64_bf16 v[224:227], v[220:223], v[56:63], v241\n\t" \
    "v_smfmac_f32_16x16x64_bf16 v[244:247], v[220:223], v[64:71], v241\n\t" \
    "v_and_b32 v240, 0x3ff, %[ab]\n\t" \
    "v_smfmac_f32_16x16x64_bf16 v[248:251], v[220:223], v[72:79], v241\n\t" \
    "ds_read_b128 v[220:223], v240 offset:8192\n\t" \
    "s_waitcnt lgkmcnt(6)\n\t" \
    "v_smfmac_f32_16x16x64_bf16 v[224:227], v[236:239], v[200:207], v241\n\t" \
    "ds_read_b128 v[200:203], %[wl] offset:8192\n\t" \
    "ds_read_b128 v[204:207], %[wl] offset:9216\n\t" \
    "v_smfmac_f32_16x16x64_bf16 v[244:247], v[236:239], v[80:87], v241\n\t" \
    "v_smfmac_f32_16x16x64_bf16 v[248:251], v[236:239], v[88:95], v241\n\t" \
    "ds_read_b128 v[236:239], v240 offset:9216\n\t" \
    "s_waitcnt lgkmcnt(8)\n\t" \
    "v_smfmac_f32_16x16x64_bf16 v[244:247], v[208:211], v[104:111], v241\n\t" \
    "s_waitcnt lgkmcnt(7)\n\t" \
    "v_smfmac_f32_16x16x64_bf16 v[244:247], v[212:215], v[120:127], v241\n\t" \
    "s_waitcnt lgkmcnt(4)\n\t" \
    "v_smfmac_f32_16x16x64_bf16 v[244:247], v[216:219], v[144:151], v241\n\t" \
    "s_waitcnt lgkmcnt(3)\n\t" \
    "v_smfmac_f32_16x16x64_bf16 v[244:247], v[220:223], v[160:167], v241\n\t" \
    "s_waitcnt lgkmcnt(0)\n\t" \
    "v_smfmac_f32_16x16x64_bf16 v[244:247], v[236:239], v[184:191], v241\n\t" \
    "v_smfmac_f32_16x16x64_bf16 v[248:251], v[208:211], v[112:119], v241\n\t" \
    "v_smfmac_f32_16x16x64_bf16 v[248:251], v[212:215], v[128:135], v241\n\t" \
    "v_smfmac_f32_16x16x64_bf16 v[248:251], v[216:219], v[152:159], v241\n\t" \
    "v_permlane32_swap_b32 v244, v246\n\t" \
    "v_permlane32_swap_b32 v245, v247\n\t" \
    "v_smfmac_f32_16x16x64_bf16 v[248:251], v[220:223], v[168:175], v241\n\t" \
    "v_add_f32 v244, v244, v246\n\t" \
    "v_add_f32 v245, v245, v247\n\t" \
    "v_smfmac_f32_16x16x64_bf16 v[248:251], v[236:239], v[192:199], v241\n\t" \
    "v_cvt_pk_bf16_f32 v244, v244, v245\n\t" \
    "ds_write_b16 %[dst], v244 offset:512\n\t" \
    "ds_write_b16_d16_hi %[dst], v244 offset:528\n\t" \
    "v_smfmac_f32_16x16x64_bf16 v[224:227], v[208:211], v[96:103], v241\n\t" \
    "v_smfmac_f32_16x16x64_bf16 v[224:227], v[212:215], v[228:235], v241\n\t" \
    "v_smfmac_f32_16x16x64_bf16 v[224:227], v[216:219], v[136:143], v241\n\t" \
    "v_permlane32_swap_b32 v248, v250\n\t" \
    "v_permlane32_swap_b32 v249, v251\n\t" \
    "v_smfmac_f32_16x16x64_bf16 v[224:227], v[220:223], v[200:207], v241\n\t" \
    "v_add_f32 v248, v248, v250\n\t" \
    "v_add_f32 v249, v249, v251\n\t" \
    "v_smfmac_f32_16x16x64_bf16 v[224:227], v[236:239], v[176:183], v241\n\t" \
    "v_cvt_pk_bf16_f32 v248, v248, v249\n\t" \
    "ds_write_b16 %[dst], v248 offset:1024\n\t" \
    "ds_write_b16_d16_hi %[dst], v248 offset:1040\n\t" \
    "s_nop 4\n\t" \
    "v_permlane32_swap_b32 v224, v226\n\t" \
    "v_permlane32_swap_b32 v225, v227\n\t" \
    "v_add_f32 v224, v224, v226\n\t" \
    "v_add_f32 v225, v225, v227\n\t" \
    "v_cvt_pk_bf16_f32 v224, v224, v225\n\t" \
    "ds_write_b16 %[dst], v224 offset:0\n\t" \
    "ds_write_b16_d16_hi %[dst], v224 offset:16\n\t"
#define ASM_VOX \
    "s_waitcnt lgkmcnt(0)\n\t" \
    "ds_read_b128 v[224:227], %[va] offset:0\n\t" \
    "ds_read_b128 v[248:251], %[wl] offset:0\n\t" \
    "ds_read_b128 v[228:231], %[va] offset:6144\n\t" \
    "ds_read_b128 v[232:235], %[va] offset:2048\n\t" \
    "ds_read_b128 v[236:239], %[va] offset:8192\n\t" \
    "ds_read_b128 v[240:243], %[va] offset:3072\n\t" \
    "s_waitcnt lgkmcnt(4)\n\t" \
    "v_mfma_f32_16x16x32_bf16 v[200:203], v[224:227], v[248:251], 0\n\t" \
    "ds_read_b128 v[244:247], %[va] offset:9216\n\t" \
    "s_waitcnt lgkmcnt(4)\n\t" \
    "v_mfma_f32_16x16x32_bf16 v[204:207], v[228:231], v[248:251], 0\n\t" \
    "ds_read_b128 v[224:227], %[va] offset:1024\n\t" \
    "s_waitcnt lgkmcnt(4)\n\t" \
    "v_mfma_f32_16x16x32_bf16 v[200:203], v[232:235], v[0:3], v[200:203]\n\t" \
    "ds_read_b128 v[228:231], %[va] offset:7168\n\t" \
    "ds_read_b128 v[248:251], %[wl] offset:1024\n\t" \
    "s_waitcnt lgkmcnt(5)\n\t" \
    "v_mfma_f32_16x16x32_bf16 v[204:207], v[236:239], v[0:3], v[204:207]\n\t" \
    "ds_read_b128 v[232:235], %[va] offset:4096\n\t" \
    "s_waitcnt lgkmcnt(5)\n\t" \
    "v_mfma_f32_16x16x32_bf16 v[200:203], v[240:243], v[4:7], v[200:203]\n\t" \
    "ds_read_b128 v[236:239], %[va] offset:10240\n\t" \
    "s_waitcnt lgkmcnt(5)\n\t" \
    "v_mfma_f32_16x16x32_bf16 v[204:207], v[244:247], v[4:7], v[204:207]\n\t" \
    "ds_read_b128 v[240:243], %[va] offset:5120\n\t" \
    "s_waitcnt lgkmcnt(3)\n\t" \
    "v_mfma_f32_16x16x32_bf16 v[200:203], v[224:227], v[248:251], v[200:203]\n\t" \
    "ds_read_b128 v[244:247], %[va] offset:11264\n\t" \
    "v_mfma_f32_16x16x32_bf16 v[208:211], v[224:227], v[20:23], 0\n\t" \
    "v_mfma_f32_16x16x32_bf16 v[204:207], v[228:231], v[248:251], v[204:207]\n\t" \
    "ds_read_b128 v[224:227], %[va] offset:0\n\t" \
    "v_mfma_f32_16x16x32_bf16 v[212:215], v[228:231], v[20:23], 0\n\t" \
    "s_waitcnt lgkmcnt(4)\n\t" \
    "v_mfma_f32_16x16x32_bf16 v[200:203], v[232:235], v[8:11], v[200:203]\n\t" \
    "ds_read_b128 v[228:231], %[va] offset:6144\n\t" \
    "ds_read_b128 v[248:251], %[wl] offset:2048\n\t" \
    "v_mfma_f32_16x16x32_bf16 v[208:211], v[232:235], v[32:35], v[208:211]\n\t" \
    "s_waitcnt lgkmcnt(5)\n\t" \
    "v_mfma_f32_16x16x32_bf16 v[204:207], v[236:239], v[8:11], v[204:207]\n\t" \
    "ds_read_b128 v[232:235], %[va] offset:2048\n\t" \
    "v_mfma_f32_16x16x32_bf16 v[212:215], v[236:239], v[32:35], v[212:215]\n\t" \
    "s_waitcnt lgkmcnt(5)\n\t" \
    "v_mfma_f32_16x16x32_bf16 v[200:203], v[240:243], v[12:15], v[200:203]\n\t" \
    "ds_read_b128 v[236:239], %[va] offset:8192\n\t" \
    "v_mfma_f32_16x16x32_bf16 v[208:211], v[240:243], v[36:39], v[208:211]\n\t" \
    "s_waitcnt lgkmcnt(5)\n\t" \
    "v_mfma_f32_16x16x32_bf16 v[204:207], v[244:247], v[12:15], v[204:207]\n\t" \
    "ds_read_b128 v[240:243], %[va] offset:3072\n\t" \
    "v_mfma_f32_16x16x32_bf16 v[212:215], v[244:247], v[36:39], v[212:215]\n\t" \
    "s_waitcnt lgkmcnt(3)\n\t" \
    "v_mfma_f32_16x16x32_bf16 v[216:219], v[224:227], v[248:251], 0\n\t" \
    "ds_read_b128 v[244:247], %[va] offset:9216\n\t" \
    "v_mfma_f32_16x16x32_bf16 v[208:211], v[224:227], v[16:19], v[208:211]\n\t" \
    "v_mfma_f32_16x16x32_bf16 v[220:223], v[228:231], v[248:251], 0\n\t" \
    "ds_read_b128 v[224:227], %[va] offset:1024\n\t" \
    "v_mfma_f32_16x16x32_bf16 v[212:215], v[228:231], v[16:19], v[212:215]\n\t" \
    "v_cvt_pk_bf16_f32 v200, v200, v201\n\t" \
    "s_waitcnt lgkmcnt(4)\n\t" \
    "v_mfma_f32_16x16x32_bf16 v[216:219], v[232:235], v[40:43], v[216:219]\n\t" \
    "ds_read_b128 v[228:231], %[va] offset:7168\n\t" \
    "ds_read_b128 v[248:251], %[wl] offset:3072\n\t" \
    "v_cvt_pk_bf16_f32 v201, v202, v203\n\t" \
    "v_mfma_f32_16x16x32_bf16 v[208:211], v[232:235], v[24:27], v[208:211]\n\t" \
    "v_cvt_pk_bf16_f32 v202, v204, v205\n\t" \
    "s_waitcnt lgkmcnt(5)\n\t" \
    "v_mfma_f32_16x16x32_bf16 v[220:223], v[236:239], v[40:43], v[220:223]\n\t" \
    "ds_read_b128 v[232:235], %[va] offset:4096\n\t" \
    "v_cvt_pk_bf16_f32 v203, v206, v207\n\t" \
    "v_mfma_f32_16x16x32_bf16 v[212:215], v[236:239], v[24:27], v[212:215]\n\t" \
    "buffer_store_dwordx4 v[200:203], %[l16], %[rd], %[so] offen sc1\n\t" \
    "s_waitcnt lgkmcnt(5)\n\t" \
    "v_mfma_f32_16x16x32_bf16 v[216:219], v[240:243], v[44:47], v[216:219]\n\t" \
    "ds_read_b128 v[236:239], %[va] offset:10240\n\t" \
    "s_add_u32 %[so], %[so], 0x10000\n\t" \
    "v_mfma_f32_16x16x32_bf16 v[208:211], v[240:243], v[28:31], v[208:211]\n\t" \
    "s_waitcnt lgkmcnt(5)\n\t" \
    "v_mfma_f32_16x16x32_bf16 v[220:223], v[244:247], v[44:47], v[220:223]\n\t" \
    "ds_read_b128 v[240:243], %[va] offset:5120\n\t" \
    "v_mfma_f32_16x16x32_bf16 v[212:215], v[244:247], v[28:31], v[212:215]\n\t" \
    "s_waitcnt lgkmcnt(3)\n\t" \
    "v_mfma_f32_16x16x32_bf16 v[216:219], v[224:227], v[248:251], v[216:219]\n\t" \
    "ds_read_b128 v[244:247], %[va] offset:11264\n\t" \
    "v_mfma_f32_16x16x32_bf16 v[200:203], v[224:227], v[60:63], 0\n\t" \
    "v_mfma_f32_16x16x32_bf16 v[220:223], v[228:231], v[248:251], v[220:223]\n\t" \
    "ds_read_b128 v[224:227], %[va] offset:0\n\t" \
    "v_mfma_f32_16x16x32_bf16 v[204:207], v[228:231], v[60:63], 0\n\t" \
    "v_cvt_pk_bf16_f32 v208, v208, v209\n\t" \
    "s_waitcnt lgkmcnt(4)\n\t" \
    "v_mfma_f32_16x16x32_bf16 v[216:219], v[232:235], v[48:51], v[216:219]\n\t" \
    "ds_read_b128 v[228:231], %[va] offset:6144\n\t" \
    "ds_read_b128 v[248:251], %[wl] offset:4096\n\t" \
    "v_cvt_pk_bf16_f32 v209, v210, v211\n\t" \
    "v_mfma_f32_16x16x32_bf16 v[200:203], v[232:235], v[72:75], v[200:203]\n\t" \
    "v_cvt_pk_bf16_f32 v210, v212, v213\n\t" \
    "s_waitcnt lgkmcnt(5)\n\t" \
    "v_mfma_f32_16x16x32_bf16 v[220:223], v[236:239], v[48:51], v[220:223]\n\t" \
    "ds_read_b128 v[232:235], %[va] offset:2048\n\t" \
    "v_cvt_pk_bf16_f32 v211, v214, v215\n\t" \
    "v_mfma_f32_16x16x32_bf16 v[204:207], v[236:239], v[72:75], v[204:207]\n\t" \
    "buffer_store_dwordx4 v[208:211], %[l16], %[rd], %[so] offen sc1\n\t" \
    "s_waitcnt lgkmcnt(5)\n\t" \
    "v_mfma_f32_16x16x32_bf16 v[216:219], v[240:243], v[52:55], v[216:219]\n\t" \
    "ds_read_b128 v[236:239], %[va] offset:8192\n\t" \
    "s_add_u32 %[so], %[so], 0x10000\n\t" \
    "v_mfma_f32_16x16x32_bf16 v[200:203], v[240:243], v[76:79], v[200:203]\n\t" \
    "s_waitcnt lgkmcnt(5)\n\t" \
    "v_mfma_f32_16x16x32_bf16 v[220:223], v[244:247], v[52:55], v[220:223]\n\t" \
    "ds_read_b128 v[240:243], %[va] offset:3072\n\t" \
    "v_mfma_f32_16x16x32_bf16 v[204:207], v[244:247], v[76:79], v[204:207]\n\t" \
    "s_waitcnt lgkmcnt(3)\n\t" \
    "v_mfma_f32_16x16x32_bf16 v[208:211], v[224:227], v[248:251], 0\n\t" \
    "ds_read_b128 v[244:247], %[va] offset:9216\n\t" \
    "v_mfma_f32_16x16x32_bf16 v[200:203], v[224:227], v[56:59], v[200:203]\n\t" \
    "v_mfma_f32_16x16x32_bf16 v[212:215], v[228:231], v[248:251], 0\n\t" \
    "ds_read_b128 v[224:227], %[va] offset:1024\n\t" \
    "v_mfma_f32_16x16x32_bf16 v[204:207], v[228:231], v[56:59], v[204:207]\n\t" \
    "v_cvt_pk_bf16_f32 v216, v216, v217\n\t" \
    "s_waitcnt lgkmcnt(4)\n\t" \
    "v_mfma_f32_16x16x32_bf16 v[208:211], v[232:235], v[80:83], v[208:211]\n\t" \
    "ds_read_b128 v[228:231], %[va] offset:7168\n\t" \
    "ds_read_b128 v[248:251], %[wl] offset:5120\n\t" \
    "v_cvt_pk_bf16_f32 v217, v218, v219\n\t" \
    "v_mfma_f32_16x16x32_bf16 v[200:203], v[232:235], v[64:67], v[200:203]\n\t" \
    "v_cvt_pk_bf16_f32 v218, v220, v221\n\t" \
    "s_waitcnt lgkmcnt(5)\n\t" \
    "v_mfma_f32_16x16x32_bf16 v[212:215], v[236:239], v[80:83], v[212:215]\n\t" \
    "ds_read_b128 v[232:235], %[va] offset:4096\n\t" \
    "v_cvt_pk_bf16_f32 v219, v222, v223\n\t" \
    "v_mfma_f32_16x16x32_bf16 v[204:207], v[236:239], v[64:67], v[204:207]\n\t" \
    "buffer_store_dwordx4 v[216:219], %[l16], %[rd], %[so] offen sc1\n\t" \
    "s_waitcnt lgkmcnt(5)\n\t" \
    "v_mfma_f32_16x16x32_bf16 v[208:211], v[240:243], v[84:87], v[208:211]\n\t" \
    "ds_read_b128 v[236:239], %[va] offset:10240\n\t" \
    "s_add_u32 %[so], %[so], 0x10000\n\t" \
    "v_mfma_f32_16x16x32_bf16 v[200:203], v[240:243], v[68:71], v[200:203]\n\t" \
    "s_waitcnt lgkmcnt(5)\n\t" \
    "v_mfma_f32_16x16x32_bf16 v[212:215], v[244:247], v[84:87], v[212:215]\n\t" \
    "ds_read_b128 v[240:243], %[va] offset:5120\n\t" \
    "v_mfma_f32_16x16x32_bf16 v[204:207], v[244:247], v[68:71], v[204:207]\n\t" \
    "s_waitcnt lgkmcnt(3)\n\t" \
    "v_mfma_f32_16x16x32_bf16 v[208:211], v[224:227], v[248:251], v[208:211]\n\t" \
    "ds_read_b128 v[244:247], %[va] offset:11264\n\t" \
    "v_mfma_f32_16x16x32_bf16 v[216:219], v[224:227], v[100:103], 0\n\t" \
    "v_mfma_f32_16x16x32_bf16 v[212:215], v[228:231], v[248:251], v[212:215]\n\t" \
    "ds_read_b128 v[224:227], %[va] offset:0\n\t" \
    "v_mfma_f32_16x16x32_bf16 v[220:223], v[228:231], v[100:103], 0\n\t" \
    "v_cvt_pk_bf16_f32 v200, v200, v201\n\t" \
    "s_waitcnt lgkmcnt(4)\n\t" \
    "v_mfma_f32_16x16x32_bf16 v[208:211], v[232:235], v[88:91], v[208:211]\n\t" \
    "ds_read_b128 v[228:231], %[va] offset:6144\n\t" \
    "ds_read_b128 v[248:251], %[wl] offset:6144\n\t" \
    "v_cvt_pk_bf16_f32 v201, v202, v203\n\t" \
    "v_mfma_f32_16x16x32_bf16 v[216:219], v[232:235], v[112:115], v[216:219]\n\t" \
    "v_cvt_pk_bf16_f32 v202, v204, v205\n\t" \
    "s_waitcnt lgkmcnt(5)\n\t" \
    "v_mfma_f32_16x16x32_bf16 v[212:215], v[236:239], v[88:91], v[212:215]\n\t" \
    "ds_read_b128 v[232:235], %[va] offset:2048\n\t" \
    "v_cvt_pk_bf16_f32 v203, v206, v207\n\t" \
    "v_mfma_f32_16x16x32_bf16 v[220:223], v[236:239], v[112:115], v[220:223]\n\t" \
    "buffer_store_dwordx4 v[200:203], %[l16], %[rd], %[so] offen sc1\n\t" \
    "s_waitcnt lgkmcnt(5)\n\t" \
    "v_mfma_f32_16x16x32_bf16 v[208:211], v[240:243], v[92:95], v[208:211]\n\t" \
    "ds_read_b128 v[236:239], %[va] offset:8192\n\t" \
    "s_add_u32 %[so], %[so], 0x10000\n\t" \
    "v_mfma_f32_16x16x32_bf16 v[216:219], v[240:243], v[116:119], v[216:219]\n\t" \
    "s_waitcnt lgkmcnt(5)\n\t" \
    "v_mfma_f32_16x16x32_bf16 v[212:215], v[244:247], v[92:95], v[212:215]\n\t" \
    "ds_read_b128 v[240:243], %[va] offset:3072\n\t" \
    "v_mfma_f32_16x16x32_bf16 v[220:223], v[244:247], v[116:119], v[220:223]\n\t" \
    "s_waitcnt lgkmcnt(3)\n\t" \
    "v_mfma_f32_16x16x32_bf16 v[200:203], v[224:227], v[248:251], 0\n\t" \
    "ds_read_b128 v[244:247], %[va] offset:9216\n\t" \
    "v_mfma_f32_16x16x32_bf16 v[216:219], v[224:227], v[96:99], v[216:219]\n\t" \
    "v_mfma_f32_16x16x32_bf16 v[204:207], v[228:231], v[248:251], 0\n\t" \
    "ds_read_b128 v[224:227], %[va] offset:1024\n\t" \
    "v_mfma_f32_16x16x32_bf16 v[220:223], v[228:231], v[96:99], v[220:223]\n\t" \
    "v_cvt_pk_bf16_f32 v208, v208, v209\n\t" \
    "s_waitcnt lgkmcnt(4)\n\t" \
    "v_mfma_f32_16x16x32_bf16 v[200:203], v[232:235], v[120:123], v[200:203]\n\t" \
    "ds_read_b128 v[228:231], %[va] offset:7168\n\t" \
    "ds_read_b128 v[248:251], %[wl] offset:7168\n\t" \
    "v_cvt_pk_bf16_f32 v209, v210, v211\n\t" \
    "v_mfma_f32_16x16x32_bf16 v[216:219], v[232:235], v[104:107], v[216:219]\n\t" \
    "v_cvt_pk_bf16_f32 v210, v212, v213\n\t" \
    "s_waitcnt lgkmcnt(5)\n\t" \
    "v_mfma_f32_16x16x32_bf16 v[204:207], v[236:239], v[120:123], v[204:207]\n\t" \
    "ds_read_b128 v[232:235], %[va] offset:4096\n\t" \
    "v_cvt_pk_bf16_f32 v211, v214, v215\n\t" \
    "v_mfma_f32_16x16x32_bf16 v[220:223], v[236:239], v[104:107], v[220:223]\n\t" \
    "buffer_store_dwordx4 v[208:211], %[l16], %[rd], %[so] offen sc1\n\t" \
    "s_waitcnt lgkmcnt(5)\n\t" \
    "v_mfma_f32_16x16x32_bf16 v[200:203], v[240:243], v[124:127], v[200:203]\n\t" \
    "ds_read_b128 v[236:239], %[va] offset:10240\n\t" \
    "s_add_u32 %[so], %[so], 0x10000\n\t" \
    "v_mfma_f32_16x16x32_bf16 v[216:219], v[240:243], v[108:111], v[216:219]\n\t" \
    "s_waitcnt lgkmcnt(5)\n\t" \
    "v_mfma_f32_16x16x32_bf16 v[204:207], v[244:247], v[124:127], v[204:207]\n\t" \
    "ds_read_b128 v[240:243], %[va] offset:5120\n\t" \
    "v_mfma_f32_16x16x32_bf16 v[220:223], v[244:247], v[108:111], v[220:223]\n\t" \
    "s_waitcnt lgkmcnt(3)\n\t" \
    "v_mfma_f32_16x16x32_bf16 v[200:203], v[224:227], v[248:251], v[200:203]\n\t" \
    "ds_read_b128 v[244:247], %[va] offset:11264\n\t" \
    "v_mfma_f32_16x16x32_bf16 v[208:211], v[224:227], v[140:143], 0\n\t" \
    "v_mfma_f32_16x16x32_bf16 v[204:207], v[228:231], v[248:251], v[204:207]\n\t" \
    "ds_read_b128 v[224:227], %[va] offset:0\n\t" \
    "v_mfma_f32_16x16x32_bf16 v[212:215], v[228:231], v[140:143], 0\n\t" \
    "v_cvt_pk_bf16_f32 v216, v216, v217\n\t" \
    "s_waitcnt lgkmcnt(4)\n\t" \
    "v_mfma_f32_16x16x32_bf16 v[200:203], v[232:235], v[128:131], v[200:203]\n\t" \
    "ds_read_b128 v[228:231], %[va] offset:6144\n\t" \
    "ds_read_b128 v[248:251], %[wl] offset:8192\n\t" \
    "v_cvt_pk_bf16_f32 v217, v218, v219\n\t" \
    "v_mfma_f32_16x16x32_bf16 v[208:211], v[232:235], v[152:155], v[208:211]\n\t" \
    "v_cvt_pk_bf16_f32 v218, v220, v221\n\t" \
    "s_waitcnt lgkmcnt(5)\n\t" \
    "v_mfma_f32_16x16x32_bf16 v[204:207], v[236:239], v[128:131], v[204:207]\n\t" \
    "ds_read_b128 v[232:235], %[va] offset:2048\n\t" \
    "v_cvt_pk_bf16_f32 v219, v222, v223\n\t" \
    "v_mfma_f32_16x16x32_bf16 v[212:215], v[236:239], v[152:155], v[212:215]\n\t" \
    "buffer_store_dwordx4 v[216:219], %[l16], %[rd], %[so] offen sc1\n\t" \
    "s_waitcnt lgkmcnt(5)\n\t" \
    "v_mfma_f32_16x16x32_bf16 v[200:203], v[240:243], v[132:135], v[200:203]\n\t" \
    "ds_read_b128 v[236:239], %[va] offset:8192\n\t" \
    "s_add_u32 %[so], %[so], 0x10000\n\t" \
    "v_mfma_f32_16x16x32_bf16 v[208:211], v[240:243], v[156:159], v[208:211]\n\t" \
    "s_waitcnt lgkmcnt(5)\n\t" \
    "v_mfma_f32_16x16x32_bf16 v[204:207], v[244:247], v[132:135], v[204:207]\n\t" \
    "ds_read_b128 v[240:243], %[va] offset:3072\n\t" \
    "v_mfma_f32_16x16x32_bf16 v[212:215], v[244:247], v[156:159], v[212:215]\n\t" \
    "s_waitcnt lgkmcnt(3)\n\t" \
    "v_mfma_f32_16x16x32_bf16 v[216:219], v[224:227], v[248:251], 0\n\t" \
    "ds_read_b128 v[244:247], %[va] offset:9216\n\t" \
    "v_mfma_f32_16x16x32_bf16 v[208:211], v[224:227], v[136:139], v[208:211]\n\t" \
    "v_mfma_f32_16x16x32_bf16 v[220:223], v[228:231], v[248:251], 0\n\t" \
    "ds_read_b128 v[224:227], %[va] offset:1024\n\t" \
    "v_mfma_f32_16x16x32_bf16 v[212:215], v[228:231], v[136:139], v[212:215]\n\t" \
    "v_cvt_pk_bf16_f32 v200, v200, v201\n\t" \
    "s_waitcnt lgkmcnt(4)\n\t" \
    "v_mfma_f32_16x16x32_bf16 v[216:219], v[232:235], v[160:163], v[216:219]\n\t" \
    "ds_read_b128 v[228:231], %[va] offset:7168\n\t" \
    "ds_read_b128 v[248:251], %[wl] offset:9216\n\t" \
    "v_cvt_pk_bf16_f32 v201, v202, v203\n\t" \
    "v_mfma_f32_16x16x32_bf16 v[208:211], v[232:235], v[144:147], v[208:211]\n\t" \
    "v_cvt_pk_bf16_f32 v202, v204, v205\n\t" \
    "s_waitcnt lgkmcnt(5)\n\t" \
    "v_mfma_f32_16x16x32_bf16 v[220:223], v[236:239], v[160:163], v[220:223]\n\t" \
    "ds_read_b128 v[232:235], %[va] offset:4096\n\t" \
    "v_cvt_pk_bf16_f32 v203, v206, v207\n\t" \
    "v_mfma_f32_16x16x32_bf16 v[212:215], v[236:239], v[144:147], v[212:215]\n\t" \
    "buffer_store_dwordx4 v[200:203], %[l16], %[rd], %[so] offen sc1\n\t" \
    "s_waitcnt lgkmcnt(5)\n\t" \
    "v_mfma_f32_16x16x32_bf16 v[216:219], v[240:243], v[164:167], v[216:219]\n\t" \
    "ds_read_b128 v[236:239], %[va] offset:10240\n\t" \
    "s_add_u32 %[so], %[so], 0x10000\n\t" \
    "v_mfma_f32_16x16x32_bf16 v[208:211], v[240:243], v[148:151], v[208:211]\n\t" \
    "s_waitcnt lgkmcnt(5)\n\t" \
    "v_mfma_f32_16x16x32_bf16 v[220:223], v[244:247], v[164:167], v[220:223]\n\t" \
    "ds_read_b128 v[240:243], %[va] offset:5120\n\t" \
    "v_mfma_f32_16x16x32_bf16 v[212:215], v[244:247], v[148:151], v[212:215]\n\t" \
    "s_waitcnt lgkmcnt(3)\n\t" \
    "v_mfma_f32_16x16x32_bf16 v[216:219], v[224:227], v[248:251], v[216:219]\n\t" \
    "ds_read_b128 v[244:247], %[va] offset:11264\n\t" \
    "v_mfma_f32_16x16x32_bf16 v[200:203], v[224:227], v[180:183], 0\n\t" \
    "v_mfma_f32_16x16x32_bf16 v[220:223], v[228:231], v[248:251], v[220:223]\n\t" \
    "ds_read_b128 v[224:227], %[va] offset:0\n\t" \
    "v_mfma_f32_16x16x32_bf16 v[204:207], v[228:231], v[180:183], 0\n\t" \
    "v_cvt_pk_bf16_f32 v208, v208, v209\n\t" \
    "s_waitcnt lgkmcnt(4)\n\t" \
    "v_mfma_f32_16x16x32_bf16 v[216:219], v[232:235], v[168:171], v[216:219]\n\t" \
    "ds_read_b128 v[228:231], %[va] offset:6144\n\t" \
    "v_cvt_pk_bf16_f32 v209, v210, v211\n\t" \
    "v_mfma_f32_16x16x32_bf16 v[200:203], v[232:235], v[192:195], v[200:203]\n\t" \
    "v_cvt_pk_bf16_f32 v210, v212, v213\n\t" \
    "s_waitcnt lgkmcnt(4)\n\t" \
    "v_mfma_f32_16x16x32_bf16 v[220:223], v[236:239], v[168:171], v[220:223]\n\t" \
    "ds_read_b128 v[232:235], %[va] offset:2048\n\t" \
    "v_cvt_pk_bf16_f32 v211, v214, v215\n\t" \
    "v_mfma_f32_16x16x32_bf16 v[204:207], v[236:239], v[192:195], v[204:207]\n\t" \
    "buffer_store_dwordx4 v[208:211], %[l16], %[rd], %[so] offen sc1\n\t" \
    "s_waitcnt lgkmcnt(4)\n\t" \
    "v_mfma_f32_16x16x32_bf16 v[216:219], v[240:243], v[172:175], v[216:219]\n\t" \
    "ds_read_b128 v[236:239], %[va] offset:8192\n\t" \
    "s_add_u32 %[so], %[so], 0x10000\n\t" \
    "v_mfma_f32_16x16x32_bf16 v[200:203], v[240:243], v[196:199], v[200:203]\n\t" \
    "s_waitcnt lgkmcnt(4)\n\t" \
    "v_mfma_f32_16x16x32_bf16 v[220:223], v[244:247], v[172:175], v[220:223]\n\t" \
    "ds_read_b128 v[240:243], %[va] offset:3072\n\t" \
    "v_mfma_f32_16x16x32_bf16 v[204:207], v[244:247], v[196:199], v[204:207]\n\t" \
    "s_waitcnt lgkmcnt(4)\n\t" \
    "v_mfma_f32_16x16x32_bf16 v[200:203], v[224:227], v[176:179], v[200:203]\n\t" \
    "ds_read_b128 v[244:247], %[va] offset:9216\n\t" \
    "s_waitcnt lgkmcnt(4)\n\t" \
    "v_mfma_f32_16x16x32_bf16 v[204:207], v[228:231], v[176:179], v[204:207]\n\t" \
    "s_waitcnt lgkmcnt(3)\n\t" \
    "v_mfma_f32_16x16x32_bf16 v[200:203], v[232:235], v[184:187], v[200:203]\n\t" \
    "s_waitcnt lgkmcnt(2)\n\t" \
    "v_mfma_f32_16x16x32_bf16 v[204:207], v[236:239], v[184:187], v[204:207]\n\t" \
    "v_cvt_pk_bf16_f32 v216, v216, v217\n\t" \
    "s_waitcnt lgkmcnt(1)\n\t" \
    "v_mfma_f32_16x16x32_bf16 v[200:203], v[240:243], v[188:191], v[200:203]\n\t" \
    "v_cvt_pk_bf16_f32 v217, v218, v219\n\t" \
    "s_waitcnt lgkmcnt(0)\n\t" \
    "v_mfma_f32_16x16x32_bf16 v[204:207], v[244:247], v[188:191], v[204:207]\n\t" \
    "v_cvt_pk_bf16_f32 v218, v220, v221\n\t" \
    "v_cvt_pk_bf16_f32 v219, v222, v223\n\t" \
    "buffer_store_dwordx4 v[216:219], %[l16], %[rd], %[so] offen sc1\n\t" \
    "s_add_u32 %[so], %[so], 0x10000\n\t" \
    "s_nop 0\n\t" \
    "v_cvt_pk_bf16_f32 v200, v200, v201\n\t" \
    "v_cvt_pk_bf16_f32 v201, v202, v203\n\t" \
    "s_nop 1\n\t" \
    "v_cvt_pk_bf16_f32 v202, v204, v205\n\t" \
    "v_cvt_pk_bf16_f32 v203, v206, v207\n\t" \
    "buffer_store_dwordx4 v[200:203], %[l16], %[rd], %[so] offen sc1\n\t" \
    "s_add_u32 %[so], %[so], 0x10000\n\t" \
    "s_nop 1\n\t"
#define ASM_CRIT2 \
    "s_waitcnt lgkmcnt(0)\n\t" \
    "s_barrier\n\t" ASM_ST \
    "ds_read_b128 v[204:207], v200 offset:11264\n\t" \
    "ds_read_b128 v[208:211], v200 offset:12288\n\t" \
    "ds_read_b128 v[200:203], v200 offset:10240\n\t" \
    "s_waitcnt lgkmcnt(2)\n\t" \
    "v_smfmac_f32_16x16x64_bf16 v[224:227], v[204:207], v[24:31], %[sidx]\n\t" \
    "v_smfmac_f32_16x16x64_bf16 v[220:223], v[204:207], v[0:7], %[sidx]\n\t" \
    "s_waitcnt lgkmcnt(1)\n\t" \
    "v_smfmac_f32_16x16x64_bf16 v[224:227], v[208:211], v[32:39], %[sidx]\n\t" \
    "v_smfmac_f32_16x16x64_bf16 v[220:223], v[208:211], v[8:15], %[sidx]\n\t" \
    "s_waitcnt lgkmcnt(0)\n\t" \
    "v_smfmac_f32_16x16x64_bf16 v[224:227], v[200:203], v[16:23], %[sidx]\n\t" \
    "v_smfmac_f32_16x16x64_bf16 v[220:223], v[200:203], v[212:219], %[sidx]\n\t" \
    "ds_read_b128 v[212:215], %[wl] offset:2048\n\t" \
    "ds_read_b128 v[216:219], %[wl] offset:3072\n\t" \
    "v_smfmac_f32_16x16x64_bf16 v[232:235], v[204:207], v[64:71], %[sidx]\n\t" \
    "v_smfmac_f32_16x16x64_bf16 v[228:231], v[204:207], v[40:47], %[sidx]\n\t" \
    "v_smfmac_f32_16x16x64_bf16 v[232:235], v[208:211], v[72:79], %[sidx]\n\t" \
    "v_permlane32_swap_b32 v220, v224\n\t" \
    "v_permlane32_swap_b32 v221, v225\n\t" \
    "v_permlane32_swap_b32 v222, v226\n\t" \
    "v_smfmac_f32_16x16x64_bf16 v[228:231], v[208:211], v[48:55], %[sidx]\n\t" \
    "v_permlane32_swap_b32 v223, v227\n\t" \
    "v_add_f32 v220, v220, v224\n\t" \
    "v_add_f32 v221, v221, v225\n\t" \
    "v_smfmac_f32_16x16x64_bf16 v[232:235], v[200:203], v[56:63], %[sidx]\n\t" \
    "v_add_f32 v222, v222, v226\n\t" \
    "v_add_f32 v223, v223, v227\n\t" \
    "v_cvt_pk_bf16_f32 v226, v220, v221\n\t" \
    "s_waitcnt lgkmcnt(0)\n\t" \
    "v_smfmac_f32_16x16x64_bf16 v[228:231], v[200:203], v[212:219], %[sidx]\n\t" \
    "ds_read_b128 v[212:215], %[wl] offset:4096\n\t" \
    "ds_read_b128 v[216:219], %[wl] offset:5120\n\t" \
    "v_cvt_pk_bf16_f32 v227, v222, v223\n\t" \
    "v_min_u32 v226, -2, v226\n\t" \
    "v_min_u32 v227, -2, v227\n\t" \
    "v_smfmac_f32_16x16x64_bf16 v[240:243], v[204:207], v[104:111], %[sidx]\n\t" \
    "v_smfmac_f32_16x16x64_bf16 v[236:239], v[204:207], v[80:87], %[sidx]\n\t" \
    "v_smfmac_f32_16x16x64_bf16 v[240:243], v[208:211], v[112:119], %[sidx]\n\t" \
    "v_permlane32_swap_b32 v228, v232\n\t" \
    "v_permlane32_swap_b32 v229, v233\n\t" \
    "v_permlane32_swap_b32 v230, v234\n\t" \
    "v_smfmac_f32_16x16x64_bf16 v[236:239], v[208:211], v[88:95], %[sidx]\n\t" \
    "v_permlane32_swap_b32 v231, v235\n\t" \
    "v_add_f32 v228, v228, v232\n\t" \
    "v_add_f32 v229, v229, v233\n\t" \
    "v_smfmac_f32_16x16x64_bf16 v[240:243], v[200:203], v[96:103], %[sidx]\n\t" \
    "v_add_f32 v230, v230, v234\n\t" \
    "v_add_f32 v231, v231, v235\n\t" \
    "v_cvt_pk_bf16_f32 v228, v228, v229\n\t" \
    "s_waitcnt lgkmcnt(0)\n\t" \
    "v_smfmac_f32_16x16x64_bf16 v[236:239], v[200:203], v[212:219], %[sidx]\n\t" \
    "ds_read_b128 v[212:215], %[wl] offset:6144\n\t" \
    "ds_read_b128 v[216:219], %[wl] offset:7168\n\t" \
    "v_cvt_pk_bf16_f32 v229, v230, v231\n\t" \
    "v_min_u32 v228, -2, v228\n\t" \
    "v_min_u32 v229, -2, v229\n\t" \
    "s_nop 1\n\t" \
    "v_permlane16_swap_b32 v226, v228\n\t" \
    "v_permlane16_swap_b32 v227, v229\n\t" \
    "buffer_store_dwordx4 v[226:229], %[vo], %[rr], %[so] offen sc1\n\t" \
    "s_add_u32 %[so], %[so], 0x10000\n\t" \
    "v_smfmac_f32_16x16x64_bf16 v[248:251], v[204:207], v[144:151], %[sidx]\n\t" \
    "v_mov_b64 v[220:221], 0\n\t" \
    "v_mov_b64 v[222:223], 0\n\t" \
    "v_smfmac_f32_16x16x64_bf16 v[244:247], v[204:207], v[120:127], %[sidx]\n\t" \
    "v_mov_b64 v[224:225], 0\n\t" \
    "v_mov_b64 v[226:227], 0\n\t" \
    "v_smfmac_f32_16x16x64_bf16 v[248:251], v[208:211], v[152:159], %[sidx]\n\t" \
    "v_permlane32_swap_b32 v236, v240\n\t" \
    "v_permlane32_swap_b32 v237, v241\n\t" \
    "v_permlane32_swap_b32 v238, v242\n\t" \
    "v_smfmac_f32_16x16x64_bf16 v[244:247], v[208:211], v[128:135], %[sidx]\n\t" \
    "v_permlane32_swap_b32 v239, v243\n\t" \
    "v_add_f32 v236, v236, v240\n\t" \
    "v_add_f32 v237, v237, v241\n\t" \
    "v_smfmac_f32_16x16x64_bf16 v[248:251], v[200:203], v[136:143], %[sidx]\n\t" \
    "v_add_f32 v238, v238, v242\n\t" \
    "v_add_f32 v239, v239, v243\n\t" \
    "v_cvt_pk_bf16_f32 v242, v236, v237\n\t" \
    "s_waitcnt lgkmcnt(0)\n\t" \
    "v_smfmac_f32_16x16x64_bf16 v[244:247], v[200:203], v[212:219], %[sidx]\n\t" \
    "ds_read_b128 v[212:215], %[wl] offset:8192\n\t" \
    "ds_read_b128 v[216:219], %[wl] offset:9216\n\t" \
    "v_cvt_pk_bf16_f32 v243, v238, v239\n\t" \
    "v_min_u32 v242, -2, v242\n\t" \
    "v_min_u32 v243, -2, v243\n\t" \
    "v_smfmac_f32_16x16x64_bf16 v[224:227], v[204:207], v[184:191], %[sidx]\n\t" \
    "v_and_b32 v232, 0x7fff, %[vo]\n\t" \
    "v_lshrrev_b32 v233, 15, %[vo]\n\t" \
    "v_smfmac_f32_16x16x64_bf16 v[220:223], v[204:207], v[160:167], %[sidx]\n\t" \
    "v_lshl_add_u32 v232, v233, 3, v232\n\t" \
    "v_permlane32_swap_b32 v244, v248\n\t" \
    "v_smfmac_f32_16x16x64_bf16 v[224:227], v[208:211], v[192:199], %[sidx]\n\t" \
    "v_permlane32_swap_b32 v245, v249\n\t" \
    "v_permlane32_swap_b32 v246, v250\n\t" \
    "v_permlane32_swap_b32 v247, v251\n\t" \
    "v_smfmac_f32_16x16x64_bf16 v[220:223], v[208:211], v[168:175], %[sidx]\n\t" \
    "v_add_f32 v244, v244, v248\n\t" \
    "v_add_f32 v245, v245, v249\n\t" \
    "v_add_f32 v246, v246, v250\n\t" \
    "v_smfmac_f32_16x16x64_bf16 v[224:227], v[200:203], v[176:183], %[sidx]\n\t" \
    "v_add_f32 v247, v247, v251\n\t" \
    "v_cvt_pk_bf16_f32 v244, v244, v245\n\t" \
    "v_cvt_pk_bf16_f32 v245, v246, v247\n\t" \
    "s_waitcnt lgkmcnt(0)\n\t" \
    "v_smfmac_f32_16x16x64_bf16 v[220:223], v[200:203], v[212:219], %[sidx]\n\t" \
    "v_min_u32 v244, -2, v244\n\t" \
    "v_min_u32 v245, -2, v245\n\t" \
    "s_nop 1\n\t" \
    "v_permlane16_swap_b32 v242, v244\n\t" \
    "v_permlane16_swap_b32 v243, v245\n\t" \
    "buffer_store_dwordx4 v[242:245], %[vo], %[rr], %[so] offen sc1\n\t" \
    "s_add_u32 %[so], %[so], 0x10000\n\t" \
    "s_nop 0\n\t" \
    "v_permlane32_swap_b32 v220, v224\n\t" \
    "v_permlane32_swap_b32 v221, v225\n\t" \
    "v_permlane32_swap_b32 v222, v226\n\t" \
    "v_permlane32_swap_b32 v223, v227\n\t" \
    "v_add_f32 v220, v220, v224\n\t" \
    "v_add_f32 v221, v221, v225\n\t" \
    "v_add_f32 v222, v222, v226\n\t" \
    "v_add_f32 v223, v223, v227\n\t" \
    "v_cvt_pk_bf16_f32 v220, v220, v221\n\t" \
    "v_cvt_pk_bf16_f32 v221, v222, v223\n\t" \
    "v_min_u32 v220, -2, v220\n\t" \
    "v_min_u32 v221, -2, v221\n\t" \
    "buffer_store_dwordx2 v[220:221], v232, %[rr], %[so] offen sc1\n\t" \
    "s_nop 1\n\t"
#define ASM_P4_0 \
    "ds_read_b128 v[200:203], %[qa] offset:0\n\t" \
    "ds_read_b128 v[204:207], %[qa] offset:1024\n\t" \
    "ds_read_b128 v[208:211], %[qa] offset:2048\n\t" \
    "ds_read_b128 v[212:215], %[qa] offset:3072\n\t" \
    "ds_read_b128 v[216:219], %[qa] offset:4096\n\t" \
    "ds_read_b128 v[220:223], %[qa] offset:5120\n\t" \
    "ds_read_b128 v[224:227], %[qa] offset:12288\n\t" \
    "ds_read_b128 v[228:231], %[qa] offset:13312\n\t" \
    "ds_read_b128 v[232:235], %[qa] offset:14336\n\t" \
    "ds_read_b128 v[236:239], %[qa] offset:15360\n\t" \
    "s_waitcnt lgkmcnt(0)\n\t" \
    "s_barrier\n\t" ASM_ST \
    "v_mfma_f32_16x16x32_bf16 v[240:243], v[224:227], v[200:203], v[240:243]\n\t" \
    "ds_read_b128 v[224:227], %[qa] offset:16384\n\t" \
    "v_mfma_f32_16x16x32_bf16 v[240:243], v[228:231], v[204:207], v[240:243]\n\t" \
    "ds_read_b128 v[228:231], %[qa] offset:17408\n\t" \
    "v_mfma_f32_16x16x32_bf16 v[240:243], v[232:235], v[208:211], v[240:243]\n\t" \
    "ds_read_b128 v[232:235], %[km] offset:0\n\t" \
    "v_mfma_f32_16x16x32_bf16 v[240:243], v[236:239], v[212:215], v[240:243]\n\t" \
    "ds_read_b128 v[236:239], %[km] offset:512\n\t" \
    "s_waitcnt lgkmcnt(3)\n\t" \
    "v_mfma_f32_16x16x32_bf16 v[240:243], v[224:227], v[216:219], v[240:243]\n\t" \
    "ds_read_b128 v[224:227], %[km] offset:1024\n\t" \
    "s_waitcnt lgkmcnt(3)\n\t" \
    "v_mfma_f32_16x16x32_bf16 v[240:243], v[228:231], v[220:223], v[240:243]\n\t" \
    "ds_read_b128 v[228:231], %[km] offset:1536\n\t" \
    "s_waitcnt lgkmcnt(3)\n\t" \
    "v_mfma_f32_16x16x32_bf16 v[248:251], v[232:235], v[200:203], 0\n\t" \
    "ds_read_b128 v[232:235], %[km] offset:2048\n\t" \
    "s_waitcnt lgkmcnt(3)\n\t" \
    "v_mfma_f32_16x16x32_bf16 v[248:251], v[236:239], v[204:207], v[248:251]\n\t" \
    "ds_read_b128 v[236:239], %[km] offset:2560\n\t" \
    "s_waitcnt lgkmcnt(3)\n\t" \
    "v_mfma_f32_16x16x32_bf16 v[248:251], v[224:227], v[208:211], v[248:251]\n\t" \
    "s_waitcnt lgkmcnt(2)\n\t" \
    "v_mfma_f32_16x16x32_bf16 v[248:251], v[228:231], v[212:215], v[248:251]\n\t" \
    "s_waitcnt lgkmcnt(1)\n\t" \
    "v_mfma_f32_16x16x32_bf16 v[248:251], v[232:235], v[216:219], v[248:251]\n\t" \
    "s_waitcnt lgkmcnt(0)\n\t" \
    "v_mfma_f32_16x16x32_bf16 v[248:251], v[236:239], v[220:223], v[248:251]\n\t" \
    "v_max3_f32 v224, v240, v241, v242\n\t" \
    "v_mov_b32 v225, v243\n\t" \
    "v_lshlrev_b32 v201, 23, %[qa]\n\t" \
    "v_and_b32 v201, 0x80000000, v201\n\t" \
    "v_and_b32 v200, 0x3ff, %[qa]\n\t" \
    "v_or_b32 v200, v201, v200\n\t" \
    "s_nop 1\n\t" \
    "v_max3_f32 v224, v224, v248, v249\n\t" \
    "v_max3_f32 v225, v225, v250, v251\n\t" \
    "v_max_f32 v224, v224, v225\n\t" \
    "v_mov_b32 v225, v224\n\t" \
    "s_nop 1\n\t" \
    "v_permlane16_swap_b32 v224, v225\n\t" \
    "v_max_f32 v224, v224, v225\n\t" \
    "v_mov_b32 v225, v224\n\t" \
    "s_nop 1\n\t" \
    "v_permlane32_swap_b32 v224, v225\n\t" \
    "v_max_f32 v224, v224, v225\n\t" \
    "v_mul_f32 v224, %[scl], v224\n\t" \
    "v_fma_f32 v240, v240, %[scl], -v224\n\t" \
    "v_fma_f32 v241, v241, %[scl], -v224\n\t" \
    "v_exp_f32 v240, v240\n\t" \
    "v_fma_f32 v242, v242, %[scl], -v224\n\t" \
    "v_exp_f32 v241, v241\n\t" \
    "v_fma_f32 v243, v243, %[scl], -v224\n\t" \
    "v_exp_f32 v242, v242\n\t" \
    "v_fma_f32 v248, v248, %[scl], -v224\n\t" \
    "v_exp_f32 v243, v243\n\t" \
    "v_fma_f32 v249, v249, %[scl], -v224\n\t" \
    "v_exp_f32 v248, v248\n\t" \
    "v_fma_f32 v250, v250, %[scl], -v224\n\t" \
    "v_exp_f32 v249, v249\n\t" \
    "v_fma_f32 v251, v251, %[scl], -v224\n\t" \
    "v_exp_f32 v250, v250\n\t" \
    "v_exp_f32 v251, v251\n\t" \
    "v_add_f32 v224, v240, v241\n\t" \
    "v_add_f32 v225, v242, v243\n\t" \
    "v_add_f32 v232, v248, v249\n\t" \
    "v_add_f32 v233, v250, v251\n\t" \
    "v_add_f32 v224, v224, v225\n\t" \
    "v_add_f32 v232, v232, v233\n\t" \
    "v_fma_f32 v224, v232, 0.5, v224\n\t" \
    "v_mov_b32 v225, v224\n\t" \
    "s_nop 1\n\t" \
    "v_permlane16_swap_b32 v224, v225\n\t" \
    "v_add_f32 v224, v224, v225\n\t" \
    "v_mov_b32 v225, v224\n\t" \
    "s_nop 1\n\t" \
    "v_permlane32_swap_b32 v224, v225\n\t" \
    "v_add_f32 v224, v224, v225\n\t" \
    "v_rcp_f32 v224, v224\n\t" \
    "s_nop 0\n\t" \
    "v_mul_f32 v240, v240, v224\n\t" \
    "v_mul_f32 v241, v241, v224\n\t" \
    "v_mul_f32 v242, v242, v224\n\t" \
    "v_mul_f32 v243, v243, v224\n\t" \
    "v_mul_f32 v248, v248, v224\n\t" \
    "v_mul_f32 v249, v249, v224\n\t" \
    "v_mul_f32 v250, v250, v224\n\t" \
    "v_mul_f32 v251, v251, v224\n\t" \
    "v_cvt_pk_bf16_f32 v228, |v240|, |v241|\n\t" \
    "v_cvt_pk_bf16_f32 v229, |v242|, |v243|\n\t" \
    "v_cvt_pk_bf16_f32 v232, |v248|, |v249|\n\t" \
    "v_cvt_pk_bf16_f32 v233, |v250|, |v251|\n\t" \
    "v_permlane16_swap_b32 v228, v230\n\t" \
    "v_permlane16_swap_b32 v229, v231\n\t" \
    "buffer_store_dwordx4 v[228:231], v200, %[rr], %[so] offen sc1\n\t" \
    "buffer_store_dwordx2 v[232:233], %[pm], %[rr], %[so] offen sc1\n\t" \
    "s_nop 1\n\t"
#define ASM_P4_1 \
    "ds_read_b128 v[200:203], %[qa] offset:0\n\t" \
    "ds_read_b128 v[204:207], %[qa] offset:1024\n\t" \
    "ds_read_b128 v[208:211], %[qa] offset:2048\n\t" \
    "ds_read_b128 v[212:215], %[qa] offset:3072\n\t" \
    "ds_read_b128 v[216:219], %[qa] offset:4096\n\t" \
    "ds_read_b128 v[220:223], %[qa] offset:5120\n\t" \
    "ds_read_b128 v[224:227], %[qa] offset:12288\n\t" \
    "ds_read_b128 v[228:231], %[qa] offset:13312\n\t" \
    "ds_read_b128 v[232:235], %[qa] offset:14336\n\t" \
    "ds_read_b128 v[236:239], %[qa] offset:15360\n\t" \
    "s_waitcnt lgkmcnt(0)\n\t" \
    "s_barrier\n\t" ASM_ST \
    "v_mfma_f32_16x16x32_bf16 v[240:243], v[224:227], v[200:203], v[240:243]\n\t" \
    "ds_read_b128 v[224:227], %[qa] offset:16384\n\t" \
    "v_mfma_f32_16x16x32_bf16 v[240:243], v[228:231], v[204:207], v[240:243]\n\t" \
    "ds_read_b128 v[228:231], %[qa] offset:17408\n\t" \
    "v_mfma_f32_16x16x32_bf16 v[240:243], v[232:235], v[208:211], v[240:243]\n\t" \
    "ds_read_b128 v[232:235], %[qa] offset:6144\n\t" \
    "v_mfma_f32_16x16x32_bf16 v[240:243], v[236:239], v[212:215], v[240:243]\n\t" \
    "ds_read_b128 v[236:239], %[qa] offset:7168\n\t" \
    "s_waitcnt lgkmcnt(3)\n\t" \
    "v_mfma_f32_16x16x32_bf16 v[240:243], v[224:227], v[216:219], v[240:243]\n\t" \
    "ds_read_b128 v[224:227], %[qa] offset:8192\n\t" \
    "s_waitcnt lgkmcnt(3)\n\t" \
    "v_mfma_f32_16x16x32_bf16 v[240:243], v[228:231], v[220:223], v[240:243]\n\t" \
    "ds_read_b128 v[228:231], %[qa] offset:9216\n\t" \
    "s_waitcnt lgkmcnt(3)\n\t" \
    "v_mfma_f32_16x16x32_bf16 v[244:247], v[232:235], v[200:203], 0\n\t" \
    "ds_read_b128 v[232:235], %[qa] offset:10240\n\t" \
    "s_waitcnt lgkmcnt(3)\n\t" \
    "v_mfma_f32_16x16x32_bf16 v[244:247], v[236:239], v[204:207], v[244:247]\n\t" \
    "ds_read_b128 v[236:239], %[qa] offset:11264\n\t" \
    "s_waitcnt lgkmcnt(3)\n\t" \
    "v_mfma_f32_16x16x32_bf16 v[244:247], v[224:227], v[208:211], v[244:247]\n\t" \
    "ds_read_b128 v[224:227], %[km] offset:0\n\t" \
    "s_waitcnt lgkmcnt(3)\n\t" \
    "v_mfma_f32_16x16x32_bf16 v[244:247], v[228:231], v[212:215], v[244:247]\n\t" \
    "ds_read_b128 v[228:231], %[km] offset:512\n\t" \
    "s_waitcnt lgkmcnt(3)\n\t" \
    "v_mfma_f32_16x16x32_bf16 v[244:247], v[232:235], v[216:219], v[244:247]\n\t" \
    "ds_read_b128 v[232:235], %[km] offset:1024\n\t" \
    "s_waitcnt lgkmcnt(3)\n\t" \
    "v_mfma_f32_16x16x32_bf16 v[244:247], v[236:239], v[220:223], v[244:247]\n\t" \
    "ds_read_b128 v[236:239], %[km] offset:1536\n\t" \
    "s_waitcnt lgkmcnt(3)\n\t" \
    "v_mfma_f32_16x16x32_bf16 v[248:251], v[224:227], v[200:203], 0\n\t" \
    "ds_read_b128 v[224:227], %[km] offset:2048\n\t" \
    "s_waitcnt lgkmcnt(3)\n\t" \
    "v_mfma_f32_16x16x32_bf16 v[248:251], v[228:231], v[204:207], v[248:251]\n\t" \
    "ds_read_b128 v[228:231], %[km] offset:2560\n\t" \
    "s_waitcnt lgkmcnt(3)\n\t" \
    "v_mfma_f32_16x16x32_bf16 v[248:251], v[232:235], v[208:211], v[248:251]\n\t" \
    "s_waitcnt lgkmcnt(2)\n\t" \
    "v_mfma_f32_16x16x32_bf16 v[248:251], v[236:239], v[212:215], v[248:251]\n\t" \
    "s_waitcnt lgkmcnt(1)\n\t" \
    "v_mfma_f32_16x16x32_bf16 v[248:251], v[224:227], v[216:219], v[248:251]\n\t" \
    "s_waitcnt lgkmcnt(0)\n\t" \
    "v_mfma_f32_16x16x32_bf16 v[248:251], v[228:231], v[220:223], v[248:251]\n\t" \
    "v_max3_f32 v224, v240, v241, v242\n\t" \
    "v_max3_f32 v225, v244, v245, v246\n\t" \
    "v_max3_f32 v224, v224, v243, v247\n\t" \
    "v_and_b32 v200, 0x3ff, %[qa]\n\t" \
    "s_nop 3\n\t" \
    "v_max3_f32 v224, v224, v248, v249\n\t" \
    "v_max3_f32 v225, v225, v250, v251\n\t" \
    "v_max_f32 v224, v224, v225\n\t" \
    "v_mov_b32 v225, v224\n\t" \
    "s_nop 1\n\t" \
    "v_permlane16_swap_b32 v224, v225\n\t" \
    "v_max_f32 v224, v224, v225\n\t" \
    "v_mov_b32 v225, v224\n\t" \
    "s_nop 1\n\t" \
    "v_permlane32_swap_b32 v224, v225\n\t" \
    "v_max_f32 v224, v224, v225\n\t" \
    "v_mul_f32 v224, %[scl], v224\n\t" \
    "v_fma_f32 v240, v240, %[scl], -v224\n\t" \
    "v_fma_f32 v241, v241, %[scl], -v224\n\t" \
    "v_exp_f32 v240, v240\n\t" \
    "v_fma_f32 v242, v242, %[scl], -v224\n\t" \
    "v_exp_f32 v241, v241\n\t" \
    "v_fma_f32 v243, v243, %[scl], -v224\n\t" \
    "v_exp_f32 v242, v242\n\t" \
    "v_fma_f32 v244, v244, %[scl], -v224\n\t" \
    "v_exp_f32 v243, v243\n\t" \
    "v_fma_f32 v245, v245, %[scl], -v224\n\t" \
    "v_exp_f32 v244, v244\n\t" \
    "v_fma_f32 v246, v246, %[scl], -v224\n\t" \
    "v_exp_f32 v245, v245\n\t" \
    "v_fma_f32 v247, v247, %[scl], -v224\n\t" \
    "v_exp_f32 v246, v246\n\t" \
    "v_fma_f32 v248, v248, %[scl], -v224\n\t" \
    "v_exp_f32 v247, v247\n\t" \
    "v_fma_f32 v249, v249, %[scl], -v224\n\t" \
    "v_exp_f32 v248, v248\n\t" \
    "v_fma_f32 v250, v250, %[scl], -v224\n\t" \
    "v_exp_f32 v249, v249\n\t" \
    "v_fma_f32 v251, v251, %[scl], -v224\n\t" \
    "v_exp_f32 v250, v250\n\t" \
    "v_exp_f32 v251, v251\n\t" \
    "v_add_f32 v224, v240, v241\n\t" \
    "v_add_f32 v225, v242, v243\n\t" \
    "v_add_f32 v226, v244, v245\n\t" \
    "v_add_f32 v227, v246, v247\n\t" \
    "v_add_f32 v232, v248, v249\n\t" \
    "v_add_f32 v233, v250, v251\n\t" \
    "v_add_f32 v224, v224, v225\n\t" \
    "v_add_f32 v226, v226, v227\n\t" \
    "v_add_f32 v232, v232, v233\n\t" \
    "v_add_f32 v224, v224, v226\n\t" \
    "v_fma_f32 v224, v232, 0.5, v224\n\t" \
    "v_mov_b32 v225, v224\n\t" \
    "s_nop 1\n\t" \
    "v_permlane16_swap_b32 v224, v225\n\t" \
    "v_add_f32 v224, v224, v225\n\t" \
    "v_mov_b32 v225, v224\n\t" \
    "s_nop 1\n\t" \
    "v_permlane32_swap_b32 v224, v225\n\t" \
    "v_add_f32 v224, v224, v225\n\t" \
    "v_rcp_f32 v224, v224\n\t" \
    "s_nop 0\n\t" \
    "v_mul_f32 v240, v240, v224\n\t" \
    "v_mul_f32 v241, v241, v224\n\t" \
    "v_mul_f32 v242, v242, v224\n\t" \
    "v_mul_f32 v243, v243, v224\n\t" \
    "v_mul_f32 v244, v244, v224\n\t" \
    "v_mul_f32 v245, v245, v224\n\t" \
    "v_mul_f32 v246, v246, v224\n\t" \
    "v_mul_f32 v247, v247, v224\n\t" \
    "v_mul_f32 v248, v248, v224\n\t" \
    "v_mul_f32 v249, v249, v224\n\t" \
    "v_mul_f32 v250, v250, v224\n\t" \
    "v_mul_f32 v251, v251, v224\n\t" \
    "v_cvt_pk_bf16_f32 v230, |v240|, |v241|\n\t" \
    "v_cvt_pk_bf16_f32 v231, |v242|, |v243|\n\t" \
    "v_cvt_pk_bf16_f32 v228, |v244|, |v245|\n\t" \
    "v_cvt_pk_bf16_f32 v229, |v246|, |v247|\n\t" \
    "v_cvt_pk_bf16_f32 v232, |v248|, |v249|\n\t" \
    "v_cvt_pk_bf16_f32 v233, |v250|, |v251|\n\t" \
    "buffer_store_dwordx4 v[228:231], v200, %[rr], %[so] offen sc1\n\t" \
    "buffer_store_dwordx2 v[232:233], %[pm], %[rr], %[so] offen sc1\n\t" \
    "s_nop 1\n\t"
#define ASM_RCRIT \
    "v_mov_b64 v[176:177], -1\n\t" \
    "v_mov_b64 v[178:179], -1\n\t" \
    "v_mov_b64 v[180:181], -1\n\t" \
    "v_mov_b64 v[182:183], -1\n\t" \
    "v_mov_b64 v[184:185], -1\n\t" \
    "v_mov_b64 v[186:187], -1\n\t" \
    "v_mov_b64 v[188:189], -1\n\t" \
    "v_mov_b64 v[190:191], -1\n\t" \
    "v_mov_b64 v[192:193], -1\n\t" \
    "v_mov_b64 v[194:195], -1\n\t" \
    ".Lfa%=:\n\t" \
    "s_mov_b32 %[a], 0\n\t" \
    "v_max_u32 v196, v176, v177\n\t" \
    "v_max3_u32 v196, v178, v179, v196\n\t" \
    "v_cmp_eq_u32 vcc, -1, v196\n\t" \
    "s_cbranch_vccz .Lga0_%=\n\t" \
    "buffer_load_dwordx4 v[176:179], %[ov], %[rs], %[sv] offen sc1\n\t" \
    "s_mov_b32 %[a], 1\n\t" \
    ".Lga0_%=:\n\t" \
    "v_max_u32 v196, v180, v181\n\t" \
    "v_max3_u32 v196, v182, v183, v196\n\t" \
    "v_cmp_eq_u32 vcc, -1, v196\n\t" \
    "s_cbranch_vccz .Lga1_%=\n\t" \
    "buffer_load_dwordx4 v[180:183], %[ov], %[rs], %[sv] offen offset:1024 sc1\n\t" \
    "s_mov_b32 %[a], 1\n\t" \
    ".Lga1_%=:\n\t" \
    "s_cmp_eq_u32 %[a], 0\n\t" \
    "s_cbranch_scc1 .Lha%=\n\t" \
    "s_waitcnt vmcnt(12)\n\t" \
    ".Lia%=:\n\t" \
    "v_max_u32 v196, v176, v177\n\t" \
    "v_max3_u32 v196, v178, v179, v196\n\t" \
    "v_max3_u32 v196, v180, v181, v196\n\t" \
    "v_max3_u32 v196, v182, v183, v196\n\t" \
    "v_cmp_eq_u32 vcc, -1, v196\n\t" \
    "s_cbranch_vccz .Lha%=\n\t" \
    "s_nop 7\n\t" \
    "v_max_u32 v196, v176, v177\n\t" \
    "v_max3_u32 v196, v178, v179, v196\n\t" \
    "v_max3_u32 v196, v180, v181, v196\n\t" \
    "v_max3_u32 v196, v182, v183, v196\n\t" \
    "v_cmp_eq_u32 vcc, -1, v196\n\t" \
    "s_cbranch_vccz .Lha%=\n\t" \
    "s_nop 7\n\t" \
    "v_max_u32 v196, v176, v177\n\t" \
    "v_max3_u32 v196, v178, v179, v196\n\t" \
    "v_max3_u32 v196, v180, v181, v196\n\t" \
    "v_max3_u32 v196, v182, v183, v196\n\t" \
    "v_cmp_eq_u32 vcc, -1, v196\n\t" \
    "s_cbranch_vccz .Lha%=\n\t" \
    "s_nop 7\n\t" \
    "v_max_u32 v196, v176, v177\n\t" \
    "v_max3_u32 v196, v178, v179, v196\n\t" \
    "v_max3_u32 v196, v180, v181, v196\n\t" \
    "v_max3_u32 v196, v182, v183, v196\n\t" \
    "v_cmp_eq_u32 vcc, -1, v196\n\t" \
    "s_cbranch_vccz .Lha%=\n\t" \
    "s_nop 7\n\t" \
    "s_sub_u32 %[n], %[n], 1\n\t" \
    "s_cmp_gt_i32 %[n], 0\n\t" \
    "s_cbranch_scc1 .Lfa%=\n\t" \
    ".Lha%=:\n\t" \
    ASM_ST3 \
    ".Lfb%=:\n\t" \
    "s_mov_b32 %[a], 0\n\t" \
    "v_max_u32 v196, v184, v185\n\t" \
    "v_max3_u32 v196, v186, v187, v196\n\t" \
    "v_cmp_eq_u32 vcc, -1, v196\n\t" \
    "s_cbranch_vccz .Lgb0_%=\n\t" \
    "buffer_load_dwordx4 v[184:187], %[o0], %[rs], %[sp] offen sc1\n\t" \
    "s_mov_b32 %[a], 1\n\t" \
    ".Lgb0_%=:\n\t" \
    "v_max_u32 v196, v188, v189\n\t" \
    "v_max3_u32 v196, v190, v191, v196\n\t" \
    "v_cmp_eq_u32 vcc, -1, v196\n\t" \
    "s_cbranch_vccz .Lgb1_%=\n\t" \
    "buffer_load_dwordx4 v[188:191], %[o0], %[rs], %[sp] offen offset:1024 sc1\n\t" \
    "s_mov_b32 %[a], 1\n\t" \
    ".Lgb1_%=:\n\t" \
    "v_max_u32 v196, v192, v193\n\t" \
    "v_max3_u32 v196, v194, v195, v196\n\t" \
    "v_cmp_eq_u32 vcc, -1, v196\n\t" \
    "s_cbranch_vccz .Lgb2_%=\n\t" \
    "buffer_load_dwordx4 v[192:195], %[o2], %[rs], %[sp] offen sc1\n\t" \
    "s_mov_b32 %[a], 1\n\t" \
    ".Lgb2_%=:\n\t" \
    "s_cmp_eq_u32 %[a], 0\n\t" \
    "s_cbranch_scc1 .Lhb%=\n\t" \
    "s_waitcnt vmcnt(12)\n\t" \
    ".Lib%=:\n\t" \
    "v_max_u32 v196, v184, v185\n\t" \
    "v_max3_u32 v196, v186, v187, v196\n\t" \
    "v_max3_u32 v196, v188, v189, v196\n\t" \
    "v_max3_u32 v196, v190, v191, v196\n\t" \
    "v_max3_u32 v196, v192, v193, v196\n\t" \
    "v_max3_u32 v196, v194, v195, v196\n\t" \
    "v_cmp_eq_u32 vcc, -1, v196\n\t" \
    "s_cbranch_vccz .Lhb%=\n\t" \
    "s_nop 7\n\t" \
    "v_max_u32 v196, v184, v185\n\t" \
    "v_max3_u32 v196, v186, v187, v196\n\t" \
    "v_max3_u32 v196, v188, v189, v196\n\t" \
    "v_max3_u32 v196, v190, v191, v196\n\t" \
    "v_max3_u32 v196, v192, v193, v196\n\t" \
    "v_max3_u32 v196, v194, v195, v196\n\t" \
    "v_cmp_eq_u32 vcc, -1, v196\n\t" \
    "s_cbranch_vccz .Lhb%=\n\t" \
    "s_nop 7\n\t" \
    "v_max_u32 v196, v184, v185\n\t" \
    "v_max3_u32 v196, v186, v187, v196\n\t" \
    "v_max3_u32 v196, v188, v189, v196\n\t" \
    "v_max3_u32 v196, v190, v191, v196\n\t" \
    "v_max3_u32 v196, v192, v193, v196\n\t" \
    "v_max3_u32 v196, v194, v195, v196\n\t" \
    "v_cmp_eq_u32 vcc, -1, v196\n\t" \
    "s_cbranch_vccz .Lhb%=\n\t" \
    "s_nop 7\n\t" \
    "v_max_u32 v196, v184, v185\n\t" \
    "v_max3_u32 v196, v186, v187, v196\n\t" \
    "v_max3_u32 v196, v188, v189, v196\n\t" \
    "v_max3_u32 v196, v190, v191, v196\n\t" \
    "v_max3_u32 v196, v192, v193, v196\n\t" \
    "v_max3_u32 v196, v194, v195, v196\n\t" \
    "v_cmp_eq_u32 vcc, -1, v196\n\t" \
    "s_cbranch_vccz .Lhb%=\n\t" \
    "s_nop 7\n\t" \
    "v_max_u32 v196, v184, v185\n\t" \
    "v_max3_u32 v196, v186, v187, v196\n\t" \
    "v_max3_u32 v196, v188, v189, v196\n\t" \
    "v_max3_u32 v196, v190, v191, v196\n\t" \
    "v_max3_u32 v196, v192, v193, v196\n\t" \
    "v_max3_u32 v196, v194, v195, v196\n\t" \
    "v_cmp_eq_u32 vcc, -1, v196\n\t" \
    "s_cbranch_vccz .Lhb%=\n\t" \
    "s_nop 7\n\t" \
    "v_max_u32 v196, v184, v185\n\t" \
    "v_max3_u32 v196, v186, v187, v196\n\t" \
    "v_max3_u32 v196, v188, v189, v196\n\t" \
    "v_max3_u32 v196, v190, v191, v196\n\t" \
    "v_max3_u32 v196, v192, v193, v196\n\t" \
    "v_max3_u32 v196, v194, v195, v196\n\t" \
    "v_cmp_eq_u32 vcc, -1, v196\n\t" \
    "s_cbranch_vccz .Lhb%=\n\t" \
    "s_nop 7\n\t" \
    "v_max_u32 v196, v184, v185\n\t" \
    "v_max3_u32 v196, v186, v187, v196\n\t" \
    "v_max3_u32 v196, v188, v189, v196\n\t" \
    "v_max3_u32 v196, v190, v191, v196\n\t" \
    "v_max3_u32 v196, v192, v193, v196\n\t" \
    "v_max3_u32 v196, v194, v195, v196\n\t" \
    "v_cmp_eq_u32 vcc, -1, v196\n\t" \
    "s_cbranch_vccz .Lhb%=\n\t" \
    "s_nop 7\n\t" \
    "v_max_u32 v196, v184, v185\n\t" \
    "v_max3_u32 v196, v186, v187, v196\n\t" \
    "v_max3_u32 v196, v188, v189, v196\n\t" \
    "v_max3_u32 v196, v190, v191, v196\n\t" \
    "v_max3_u32 v196, v192, v193, v196\n\t" \
    "v_max3_u32 v196, v194, v195, v196\n\t" \
    "v_cmp_eq_u32 vcc, -1, v196\n\t" \
    "s_cbranch_vccz .Lhb%=\n\t" \
    "s_nop 7\n\t" \
    "s_sub_u32 %[n], %[n], 1\n\t" \
    "s_cmp_gt_i32 %[n], 0\n\t" \
    "s_cbranch_scc1 .Lfb%=\n\t" \
    ".Lhb%=:\n\t" \
    ASM_ST2 \
    "v_mfma_f32_16x16x32_bf16 v[160:163], v[192:195], v[176:179], 0\n\t" \
    "v_mfma_f32_16x16x32_bf16 v[164:167], v[192:195], v[180:183], 0\n\t" \
    "v_smfmac_f32_16x16x64_bf16 v[160:163], v[184:187], v[128:135], %[sidx]\n\t" \
    "v_smfmac_f32_16x16x64_bf16 v[164:167], v[184:187], v[144:151], %[sidx]\n\t" \
    "v_smfmac_f32_16x16x64_bf16 v[160:163], v[188:191], v[136:143], %[sidx]\n\t" \
    "v_smfmac_f32_16x16x64_bf16 v[164:167], v[188:191], v[152:159], %[sidx]\n\t" \
    "s_cmp_eq_u32 %[pub], 0\n\t" \
    "s_nop 7\n\t" \
    "v_permlane32_swap_b32 v160, v164\n\t" \
    "v_permlane32_swap_b32 v161, v165\n\t" \
    "v_permlane32_swap_b32 v162, v166\n\t" \
    "v_permlane32_swap_b32 v163, v167\n\t" \
    "v_add_f32 v160, v160, v164\n\t" \
    "v_add_f32 v161, v161, v165\n\t" \
    "ds_write2_b32 %[rw], v160, v161 offset1:80\n\t" \
    "v_add_f32 v162, v162, v166\n\t" \
    "v_add_f32 v163, v163, v167\n\t" \
    "ds_write2_b32 %[rw], v162, v163 offset0:160 offset1:240\n\t" \
    "s_waitcnt lgkmcnt(0)\n\t" \
    "s_barrier\n\t" ASM_ST \
    "s_cbranch_scc1 .Lre%=\n\t" \
    "ds_read_b128 v[200:203], %[rd] offset:0\n\t" \
    "ds_read_b128 v[204:207], %[rd] offset:16\n\t" \
    "s_waitcnt lgkmcnt(1)\n\t" \
    "v_pk_add_f32 v[200:201], v[200:201], v[202:203]\n\t" \
    "s_waitcnt lgkmcnt(0)\n\t" \
    "v_pk_add_f32 v[204:205], v[204:205], v[206:207]\n\t" \
    "v_pk_add_f32 v[200:201], v[200:201], v[204:205]\n\t" \
    "s_nop 1\n\t" \
    "v_permlane32_swap_b32 v200, v201\n\t" \
    "v_add_f32 v202, v200, v201\n\t" \
    "v_add_f32 v203, v200, v201\n\t" \
    "s_nop 1\n\t" \
    "v_permlane32_swap_b32 v202, v203\n\t" \
    "v_cvt_pk_bf16_f32 v200, v202, v203\n\t" \
    "v_min_u32 v200, -2, v200\n\t" \
    "buffer_store_dword v200, %[oo], %[rs], %[so] offen sc1\n\t" \
    ".Lre%=:\n\t"
#define ASM_RX \
    "s_cmp_eq_u32 %[fin], 0\n\t" \
    "s_cbranch_scc1 .Lx0%=\n\t" \
    "buffer_load_dwordx2 v[250:251], %[xo], %[rx], %[sx] offen sc1\n\t" \
    ".Lx0%=:\n\t" \
    "buffer_load_dword v248, %[fo], %[rs], %[fs] offen sc1\n\t" \
    "buffer_load_dwordx4 v[200:203], %[l16], %[rs], %[s0] offen offset:0 sc1\n\t" \
    "buffer_load_dwordx4 v[204:207], %[l16], %[rs], %[s0] offen offset:1024 sc1\n\t" \
    "buffer_load_dwordx4 v[208:211], %[l16], %[rs], %[s0] offen offset:2048 sc1\n\t" \
    "buffer_load_dwordx4 v[212:215], %[l16], %[rs], %[s0] offen offset:3072 sc1\n\t" \
    "buffer_load_dwordx4 v[216:219], %[l16], %[rs], %[s1] offen offset:0 sc1\n\t" \
    "buffer_load_dwordx4 v[220:223], %[l16], %[rs], %[s1] offen offset:1024 sc1\n\t" \
    "buffer_load_dwordx4 v[224:227], %[l16], %[rs], %[s1] offen offset:2048 sc1\n\t" \
    "buffer_load_dwordx4 v[228:231], %[l16], %[rs], %[s1] offen offset:3072 sc1\n\t" \
    "v_mov_b64 v[96:97], v[128:129]\n\t" \
    "v_mov_b64 v[98:99], v[132:133]\n\t" \
    "v_mov_b64 v[100:101], v[136:137]\n\t" \
    "v_mov_b64 v[102:103], v[140:141]\n\t" \
    "v_mov_b64 v[104:105], v[144:145]\n\t" \
    "v_mov_b64 v[106:107], v[148:149]\n\t" \
    "v_mov_b64 v[108:109], v[152:153]\n\t" \
    "v_mov_b64 v[110:111], v[156:157]\n\t" \
    "s_movk_i32 %[n], 0x200\n\t" \
    "s_nop 1\n\t" \
    "v_permlane16_swap_b32 v96, v98\n\t" \
    "v_permlane16_swap_b32 v97, v99\n\t" \
    "v_permlane16_swap_b32 v100, v102\n\t" \
    "v_permlane16_swap_b32 v101, v103\n\t" \
    "v_permlane16_swap_b32 v104, v106\n\t" \
    "v_permlane16_swap_b32 v105, v107\n\t" \
    "v_permlane16_swap_b32 v108, v110\n\t" \
    "v_permlane16_swap_b32 v109, v111\n\t" \
    "s_waitcnt vmcnt(7)\n\t" \
    "v_max_u32 v196, v200, v201\n\t" \
    "v_max3_u32 v196, v202, v203, v196\n\t" \
    "s_waitcnt vmcnt(6)\n\t" \
    "v_max3_u32 v196, v204, v205, v196\n\t" \
    "v_max3_u32 v196, v206, v207, v196\n\t" \
    "s_waitcnt vmcnt(5)\n\t" \
    "v_max3_u32 v196, v208, v209, v196\n\t" \
    "v_max3_u32 v196, v210, v211, v196\n\t" \
    "s_waitcnt vmcnt(4)\n\t" \
    "v_max3_u32 v196, v212, v213, v196\n\t" \
    "v_max3_u32 v196, v214, v215, v196\n\t" \
    "s_waitcnt vmcnt(3)\n\t" \
    "v_max3_u32 v196, v216, v217, v196\n\t" \
    "v_max3_u32 v196, v218, v219, v196\n\t" \
    "s_waitcnt vmcnt(2)\n\t" \
    "v_max3_u32 v196, v220, v221, v196\n\t" \
    "v_max3_u32 v196, v222, v223, v196\n\t" \
    "s_waitcnt vmcnt(1)\n\t" \
    "v_max3_u32 v196, v224, v225, v196\n\t" \
    "v_max3_u32 v196, v226, v227, v196\n\t" \
    "s_waitcnt vmcnt(0)\n\t" \
    "v_max3_u32 v196, v228, v229, v196\n\t" \
    "v_max3_u32 v196, v230, v231, v196\n\t" \
    "v_cmp_eq_u32 vcc, -1, v196\n\t" \
    "s_cbranch_vccz .Lxd%=\n\t" \
    ".Lxl%=:\n\t" \
    "s_mov_b32 %[a], 0\n\t" \
    "v_max_u32 v196, v200, v201\n\t" \
    "v_max3_u32 v196, v202, v203, v196\n\t" \
    "v_cmp_eq_u32 vcc, -1, v196\n\t" \
    "s_cbranch_vccz .Lxs0_%=\n\t" \
    "buffer_load_dwordx4 v[200:203], %[l16], %[rs], %[s0] offen offset:0 sc1\n\t" \
    "s_mov_b32 %[a], 1\n\t" \
    ".Lxs0_%=:\n\t" \
    "v_max_u32 v196, v204, v205\n\t" \
    "v_max3_u32 v196, v206, v207, v196\n\t" \
    "v_cmp_eq_u32 vcc, -1, v196\n\t" \
    "s_cbranch_vccz .Lxs1_%=\n\t" \
    "buffer_load_dwordx4 v[204:207], %[l16], %[rs], %[s0] offen offset:1024 sc1\n\t" \
    "s_mov_b32 %[a], 1\n\t" \
    ".Lxs1_%=:\n\t" \
    "v_max_u32 v196, v208, v209\n\t" \
    "v_max3_u32 v196, v210, v211, v196\n\t" \
    "v_cmp_eq_u32 vcc, -1, v196\n\t" \
    "s_cbranch_vccz .Lxs2_%=\n\t" \
    "buffer_load_dwordx4 v[208:211], %[l16], %[rs], %[s0] offen offset:2048 sc1\n\t" \
    "s_mov_b32 %[a], 1\n\t" \
    ".Lxs2_%=:\n\t" \
    "v_max_u32 v196, v212, v213\n\t" \
    "v_max3_u32 v196, v214, v215, v196\n\t" \
    "v_cmp_eq_u32 vcc, -1, v196\n\t" \
    "s_cbranch_vccz .Lxs3_%=\n\t" \
    "buffer_load_dwordx4 v[212:215], %[l16], %[rs], %[s0] offen offset:3072 sc1\n\t" \
    "s_mov_b32 %[a], 1\n\t" \
    ".Lxs3_%=:\n\t" \
    "v_max_u32 v196, v216, v217\n\t" \
    "v_max3_u32 v196, v218, v219, v196\n\t" \
    "v_cmp_eq_u32 vcc, -1, v196\n\t" \
    "s_cbranch_vccz .Lxs4_%=\n\t" \
    "buffer_load_dwordx4 v[216:219], %[l16], %[rs], %[s1] offen offset:0 sc1\n\t" \
    "s_mov_b32 %[a], 1\n\t" \
    ".Lxs4_%=:\n\t" \
    "v_max_u32 v196, v220, v221\n\t" \
    "v_max3_u32 v196, v222, v223, v196\n\t" \
    "v_cmp_eq_u32 vcc, -1, v196\n\t" \
    "s_cbranch_vccz .Lxs5_%=\n\t" \
    "buffer_load_dwordx4 v[220:223], %[l16], %[rs], %[s1] offen offset:1024 sc1\n\t" \
    "s_mov_b32 %[a], 1\n\t" \
    ".Lxs5_%=:\n\t" \
    "v_max_u32 v196, v224, v225\n\t" \
    "v_max3_u32 v196, v226, v227, v196\n\t" \
    "v_cmp_eq_u32 vcc, -1, v196\n\t" \
    "s_cbranch_vccz .Lxs6_%=\n\t" \
    "buffer_load_dwordx4 v[224:227], %[l16], %[rs], %[s1] offen offset:2048 sc1\n\t" \
    "s_mov_b32 %[a], 1\n\t" \
    ".Lxs6_%=:\n\t" \
    "v_max_u32 v196, v228, v229\n\t" \
    "v_max3_u32 v196, v230, v231, v196\n\t" \
    "v_cmp_eq_u32 vcc, -1, v196\n\t" \
    "s_cbranch_vccz .Lxs7_%=\n\t" \
    "buffer_load_dwordx4 v[228:231], %[l16], %[rs], %[s1] offen offset:3072 sc1\n\t" \
    "s_mov_b32 %[a], 1\n\t" \
    ".Lxs7_%=:\n\t" \
    "s_cmp_eq_u32 %[a], 0\n\t" \
    "s_cbranch_scc1 .Lxd%=\n\t" \
    "s_waitcnt vmcnt(0)\n\t" \
    "s_sub_u32 %[n], %[n], 1\n\t" \
    "s_cmp_lg_u32 %[n], 0\n\t" \
    "s_cbranch_scc1 .Lxl%=\n\t" \
    "s_branch .Lxe%=\n\t" \
    ".Lxd%=:\n\t" \
    ASM_ST2 \
    "v_mfma_f32_16x16x32_bf16 v[120:123], v[128:131], v[200:203], 0\n\t" \
    "v_mfma_f32_16x16x32_bf16 v[124:127], v[144:147], v[200:203], 0\n\t" \
    "v_mfma_f32_16x16x32_bf16 v[120:123], v[132:135], v[204:207], v[120:123]\n\t" \
    "v_mfma_f32_16x16x32_bf16 v[124:127], v[148:151], v[204:207], v[124:127]\n\t" \
    "v_mfma_f32_16x16x32_bf16 v[120:123], v[136:139], v[208:211], v[120:123]\n\t" \
    "v_mfma_f32_16x16x32_bf16 v[124:127], v[152:155], v[208:211], v[124:127]\n\t" \
    "v_mfma_f32_16x16x32_bf16 v[120:123], v[140:143], v[212:215], v[120:123]\n\t" \
    "v_mfma_f32_16x16x32_bf16 v[124:127], v[156:159], v[212:215], v[124:127]\n\t" \
    "v_mfma_f32_16x16x32_bf16 v[112:115], v[96:99], v[220:223], 0\n\t" \
    "v_mfma_f32_16x16x32_bf16 v[116:119], v[104:107], v[220:223], 0\n\t" \
    "v_mfma_f32_16x16x32_bf16 v[120:123], v[176:179], v[216:219], v[120:123]\n\t" \
    "v_mfma_f32_16x16x32_bf16 v[124:127], v[180:183], v[216:219], v[124:127]\n\t" \
    "v_mfma_f32_16x16x32_bf16 v[112:115], v[100:103], v[224:227], v[112:115]\n\t" \
    "v_mfma_f32_16x16x32_bf16 v[116:119], v[108:111], v[224:227], v[116:119]\n\t" \
    "v_mfma_f32_16x16x32_bf16 v[112:115], v[176:179], v[228:231], v[112:115]\n\t" \
    "v_mfma_f32_16x16x32_bf16 v[116:119], v[180:183], v[228:231], v[116:119]\n\t" \
    "s_nop 1\n\t" \
    "ds_write_b128 %[pw], v[120:123] offset:16384\n\t" \
    "ds_write_b128 %[pw], v[124:127] offset:24576\n\t" \
    "s_nop 3\n\t" \
    "ds_write_b128 %[pw], v[112:115] offset:0\n\t" \
    "ds_write_b128 %[pw], v[116:119] offset:8192\n\t" \
    "v_cmp_ge_u32 vcc, %[tn], v248\n\t" \
    "s_mov_b32 %[a], 0\n\t" \
    "s_cbranch_vccnz .Lxv%=\n\t" \
    "buffer_load_dwordx4 v[128:131], %[l16], %[rp], %[sv0] offen offset:0 sc1\n\t" \
    "buffer_load_dwordx4 v[132:135], %[l16], %[rp], %[sv0] offen offset:1024 sc1\n\t" \
    "buffer_load_dwordx4 v[136:139], %[l16], %[rp], %[sv0] offen offset:2048 sc1\n\t" \
    "buffer_load_dwordx4 v[140:143], %[l16], %[rp], %[sv0] offen offset:3072 sc1\n\t" \
    "buffer_load_dwordx4 v[144:147], %[l16], %[rp], %[sv1] offen offset:0 sc1\n\t" \
    "buffer_load_dwordx4 v[148:151], %[l16], %[rp], %[sv1] offen offset:1024 sc1\n\t" \
    "buffer_load_dwordx4 v[152:155], %[l16], %[rp], %[sv1] offen offset:2048 sc1\n\t" \
    "buffer_load_dwordx4 v[156:159], %[l16], %[rp], %[sv1] offen offset:3072 sc1\n\t" \
    "s_mov_b32 %[a], 1\n\t" \
    ".Lxv%=:\n\t" \
    "s_cmp_eq_u32 %[fin], 0\n\t" \
    "s_waitcnt lgkmcnt(0)\n\t" \
    "s_barrier\n\t" ASM_ST \
    "s_cbranch_scc1 .Lxe%=\n\t" \
    "ds_read_b128 v[200:203], %[pr] offset:0\n\t" \
    "ds_read_b128 v[204:207], %[pr] offset:1024\n\t" \
    "ds_read_b128 v[208:211], %[pr] offset:2048\n\t" \
    "ds_read_b128 v[212:215], %[pr] offset:3072\n\t" \
    "ds_read_b128 v[216:219], %[pr] offset:4096\n\t" \
    "ds_read_b128 v[220:223], %[pr] offset:5120\n\t" \
    "ds_read_b128 v[224:227], %[pr] offset:6144\n\t" \
    "ds_read_b128 v[228:231], %[pr] offset:7168\n\t" \
    "v_lshlrev_b32 v240, 16, v250\n\t" \
    "v_and_b32 v241, 0xffff0000, v250\n\t" \
    "v_lshlrev_b32 v242, 16, v251\n\t" \
    "v_and_b32 v243, 0xffff0000, v251\n\t" \
    "s_waitcnt lgkmcnt(6)\n\t" \
    "v_pk_add_f32 v[200:201], v[200:201], v[204:205]\n\t" \
    "v_pk_add_f32 v[202:203], v[202:203], v[206:207]\n\t" \
    "s_waitcnt lgkmcnt(4)\n\t" \
    "v_pk_add_f32 v[208:209], v[208:209], v[212:213]\n\t" \
    "v_pk_add_f32 v[210:211], v[210:211], v[214:215]\n\t" \
    "v_pk_add_f32 v[200:201], v[200:201], v[208:209]\n\t" \
    "v_pk_add_f32 v[202:203], v[202:203], v[210:211]\n\t" \
    "s_waitcnt lgkmcnt(2)\n\t" \
    "v_pk_add_f32 v[216:217], v[216:217], v[220:221]\n\t" \
    "v_pk_add_f32 v[218:219], v[218:219], v[222:223]\n\t" \
    "s_waitcnt lgkmcnt(0)\n\t" \
    "v_pk_add_f32 v[224:225], v[224:225], v[228:229]\n\t" \
    "v_pk_add_f32 v[226:227], v[226:227], v[230:231]\n\t" \
    "v_pk_add_f32 v[216:217], v[216:217], v[224:225]\n\t" \
    "v_pk_add_f32 v[218:219], v[218:219], v[226:227]\n\t" \
    "v_pk_add_f32 v[200:201], v[200:201], v[216:217]\n\t" \
    "v_pk_add_f32 v[202:203], v[202:203], v[218:219]\n\t" \
    "v_pk_add_f32 v[200:201], v[200:201], v[240:241]\n\t" \
    "v_pk_add_f32 v[202:203], v[202:203], v[242:243]\n\t" \
    "v_cvt_pk_bf16_f32 v200, v200, v201\n\t" \
    "v_cvt_pk_bf16_f32 v201, v202, v203\n\t" \
    "v_min_u32 v200, -2, v200\n\t" \
    "v_min_u32 v201, -2, v201\n\t" \
    "buffer_store_dwordx2 v[200:201], %[xo], %[rd], %[sa] offen sc1\n\t" \
    ".Lxe%=:\n\t"
#define ASM_W4_M \
    "buffer_load_dwordx4 v[188:191], %[l16], %[rs], %[s1] offen offset:3072 sc1\n\t" \
    ".Lc7_%=:\n\t" \
    "s_waitcnt vmcnt(0)\n\t" \
    "v_max_u32 v212, v188, v189\n\t" \
    "v_max3_u32 v212, v190, v191, v212\n\t" \
    "v_cmp_eq_u32 vcc, -1, v212\n\t" \
    "s_cbranch_vccnz .Ls7_%=\n\t" \
    ".Lk7_%=:\n\t" \
    ASM_ST2 \
    "buffer_load_dwordx4 v[184:187], %[l16], %[rs], %[s1] offen offset:2048 sc1\n\t" \
    "buffer_load_dwordx4 v[180:183], %[l16], %[rs], %[s1] offen offset:1024 sc1\n\t" \
    "buffer_load_dwordx4 v[176:179], %[l16], %[rs], %[s1] offen offset:0 sc1\n\t" \
    "buffer_load_dwordx4 v[172:175], %[l16], %[rs], %[s0] offen offset:3072 sc1\n\t" \
    "buffer_load_dwordx4 v[168:171], %[l16], %[rs], %[s0] offen offset:2048 sc1\n\t" \
    "buffer_load_dwordx4 v[164:167], %[l16], %[rs], %[s0] offen offset:1024 sc1\n\t" \
    "buffer_load_dwordx4 v[160:163], %[l16], %[rs], %[s0] offen offset:0 sc1\n\t" \
    "v_mfma_f32_16x16x32_bf16 v[192:195], v[28:31], v[188:191], 0\n\t" \
    "v_mfma_f32_16x16x32_bf16 v[196:199], v[60:63], v[188:191], 0\n\t" \
    "v_mfma_f32_16x16x32_bf16 v[200:203], v[92:95], v[188:191], 0\n\t" \
    "v_mfma_f32_16x16x32_bf16 v[204:207], v[124:127], v[188:191], 0\n\t" \
    "v_mfma_f32_16x16x32_bf16 v[208:211], v[156:159], v[188:191], 0\n\t" \
    ".Lc6_%=:\n\t" \
    "s_waitcnt vmcnt(6)\n\t" \
    "v_max_u32 v212, v184, v185\n\t" \
    "v_max3_u32 v212, v186, v187, v212\n\t" \
    "v_cmp_eq_u32 vcc, -1, v212\n\t" \
    "s_cbranch_vccnz .Ls6_%=\n\t" \
    ".Lk6_%=:\n\t" \
    "v_mfma_f32_16x16x32_bf16 v[192:195], v[24:27], v[184:187], v[192:195]\n\t" \
    "v_mfma_f32_16x16x32_bf16 v[196:199], v[56:59], v[184:187], v[196:199]\n\t" \
    "v_mfma_f32_16x16x32_bf16 v[200:203], v[88:91], v[184:187], v[200:203]\n\t" \
    "v_mfma_f32_16x16x32_bf16 v[204:207], v[120:123], v[184:187], v[204:207]\n\t" \
    "v_mfma_f32_16x16x32_bf16 v[208:211], v[152:155], v[184:187], v[208:211]\n\t" \
    ".Lc5_%=:\n\t" \
    "s_waitcnt vmcnt(5)\n\t" \
    "v_max_u32 v212, v180, v181\n\t" \
    "v_max3_u32 v212, v182, v183, v212\n\t" \
    "v_cmp_eq_u32 vcc, -1, v212\n\t" \
    "s_cbranch_vccnz .Ls5_%=\n\t" \
    ".Lk5_%=:\n\t" \
    "v_mfma_f32_16x16x32_bf16 v[192:195], v[20:23], v[180:183], v[192:195]\n\t" \
    "v_mfma_f32_16x16x32_bf16 v[196:199], v[52:55], v[180:183], v[196:199]\n\t" \
    "v_mfma_f32_16x16x32_bf16 v[200:203], v[84:87], v[180:183], v[200:203]\n\t" \
    "v_mfma_f32_16x16x32_bf16 v[204:207], v[116:119], v[180:183], v[204:207]\n\t" \
    "v_mfma_f32_16x16x32_bf16 v[208:211], v[148:151], v[180:183], v[208:211]\n\t" \
    ".Lc4_%=:\n\t" \
    "s_waitcnt vmcnt(4)\n\t" \
    "v_max_u32 v212, v176, v177\n\t" \
    "v_max3_u32 v212, v178, v179, v212\n\t" \
    "v_cmp_eq_u32 vcc, -1, v212\n\t" \
    "s_cbranch_vccnz .Ls4_%=\n\t" \
    ".Lk4_%=:\n\t" \
    "v_mfma_f32_16x16x32_bf16 v[192:195], v[16:19], v[176:179], v[192:195]\n\t" \
    "v_mfma_f32_16x16x32_bf16 v[196:199], v[48:51], v[176:179], v[196:199]\n\t" \
    "v_mfma_f32_16x16x32_bf16 v[200:203], v[80:83], v[176:179], v[200:203]\n\t" \
    "v_mfma_f32_16x16x32_bf16 v[204:207], v[112:115], v[176:179], v[204:207]\n\t" \
    "v_mfma_f32_16x16x32_bf16 v[208:211], v[144:147], v[176:179], v[208:211]\n\t" \
    ".Lc3_%=:\n\t" \
    "s_waitcnt vmcnt(3)\n\t" \
    "v_max_u32 v212, v172, v173\n\t" \
    "v_max3_u32 v212, v174, v175, v212\n\t" \
    "v_cmp_eq_u32 vcc, -1, v212\n\t" \
    "s_cbranch_vccnz .Ls3_%=\n\t" \
    ".Lk3_%=:\n\t" \
    "v_mfma_f32_16x16x32_bf16 v[192:195], v[12:15], v[172:175], v[192:195]\n\t" \
    "v_mfma_f32_16x16x32_bf16 v[196:199], v[44:47], v[172:175], v[196:199]\n\t" \
    "v_mfma_f32_16x16x32_bf16 v[200:203], v[76:79], v[172:175], v[200:203]\n\t" \
    "v_mfma_f32_16x16x32_bf16 v[204:207], v[108:111], v[172:175], v[204:207]\n\t" \
    "v_mfma_f32_16x16x32_bf16 v[208:211], v[140:143], v[172:175], v[208:211]\n\t" \
    ".Lc2_%=:\n\t" \
    "s_waitcnt vmcnt(2)\n\t" \
    "v_max_u32 v212, v168, v169\n\t" \
    "v_max3_u32 v212, v170, v171, v212\n\t" \
    "v_cmp_eq_u32 vcc, -1, v212\n\t" \
    "s_cbranch_vccnz .Ls2_%=\n\t" \
    ".Lk2_%=:\n\t" \
    "v_mfma_f32_16x16x32_bf16 v[192:195], v[8:11], v[168:171], v[192:195]\n\t" \
    "v_mfma_f32_16x16x32_bf16 v[196:199], v[40:43], v[168:171], v[196:199]\n\t" \
    "v_mfma_f32_16x16x32_bf16 v[200:203], v[72:75], v[168:171], v[200:203]\n\t" \
    "v_mfma_f32_16x16x32_bf16 v[204:207], v[104:107], v[168:171], v[204:207]\n\t" \
    "v_mfma_f32_16x16x32_bf16 v[208:211], v[136:139], v[168:171], v[208:211]\n\t" \
    ".Lc1_%=:\n\t" \
    "s_waitcnt vmcnt(1)\n\t" \
    "v_max_u32 v212, v164, v165\n\t" \
    "v_max3_u32 v212, v166, v167, v212\n\t" \
    "v_cmp_eq_u32 vcc, -1, v212\n\t" \
    "s_cbranch_vccnz .Ls1_%=\n\t" \
    ".Lk1_%=:\n\t" \
    "v_mfma_f32_16x16x32_bf16 v[192:195], v[4:7], v[164:167], v[192:195]\n\t" \
    "v_mfma_f32_16x16x32_bf16 v[196:199], v[36:39], v[164:167], v[196:199]\n\t" \
    "v_mfma_f32_16x16x32_bf16 v[200:203], v[68:71], v[164:167], v[200:203]\n\t" \
    "v_mfma_f32_16x16x32_bf16 v[204:207], v[100:103], v[164:167], v[204:207]\n\t" \
    "v_mfma_f32_16x16x32_bf16 v[208:211], v[132:135], v[164:167], v[208:211]\n\t" \
    ".Lc0_%=:\n\t" \
    "s_waitcnt vmcnt(0)\n\t" \
    "v_max_u32 v212, v160, v161\n\t" \
    "v_max3_u32 v212, v162, v163, v212\n\t" \
    "v_cmp_eq_u32 vcc, -1, v212\n\t" \
    "s_cbranch_vccnz .Ls0_%=\n\t" \
    ".Lk0_%=:\n\t" \
    "v_mfma_f32_16x16x32_bf16 v[192:195], v[0:3], v[160:163], v[192:195]\n\t" \
    "v_mfma_f32_16x16x32_bf16 v[196:199], v[32:35], v[160:163], v[196:199]\n\t" \
    "v_mfma_f32_16x16x32_bf16 v[200:203], v[64:67], v[160:163], v[200:203]\n\t" \
    "v_mfma_f32_16x16x32_bf16 v[204:207], v[96:99], v[160:163], v[204:207]\n\t" \
    "v_mfma_f32_16x16x32_bf16 v[208:211], v[128:131], v[160:163], v[208:211]\n\t" \
    "v_mov_b32 v212, %[un]\n\t" \
    "ds_write_b128 %[ld], v[192:195] offset:0\n\t" \
    "ds_write_b128 %[ld], v[196:199] offset:6144\n\t" \
    "ds_write_b128 %[ld], v[200:203] offset:12288\n\t" \
    "s_nop 0\n\t" \
    "ds_write_b128 %[ld], v[204:207] offset:18432\n\t" \
    "s_nop 1\n\t" \
    "ds_write_b128 %[ld], v[208:211] offset:24576\n\t" \
    "ds_write_b32 %[fl], v212\n\t" \
    "s_branch .Lme%=\n\t" \
    ".Ls7_%=:\n\t" \
    "s_cmp_eq_u32 %[n], 0\n\t" \
    "s_cbranch_scc1 .Lk7_%=\n\t" \
    "s_sub_u32 %[n], %[n], 1\n\t" \
    "s_sleep 1\n\t" \
    "buffer_load_dwordx4 v[188:191], %[l16], %[rs], %[s1] offen offset:3072 sc1\n\t" \
    "s_branch .Lc7_%=\n\t" \
    ".Ls6_%=:\n\t" \
    "s_cmp_eq_u32 %[n], 0\n\t" \
    "s_cbranch_scc1 .Lk6_%=\n\t" \
    "s_sub_u32 %[n], %[n], 1\n\t" \
    "buffer_load_dwordx4 v[184:187], %[l16], %[rs], %[s1] offen offset:2048 sc1\n\t" \
    "s_waitcnt vmcnt(0)\n\t" \
    "s_branch .Lc6_%=\n\t" \
    ".Ls5_%=:\n\t" \
    "s_cmp_eq_u32 %[n], 0\n\t" \
    "s_cbranch_scc1 .Lk5_%=\n\t" \
    "s_sub_u32 %[n], %[n], 1\n\t" \
    "buffer_load_dwordx4 v[180:183], %[l16], %[rs], %[s1] offen offset:1024 sc1\n\t" \
    "s_waitcnt vmcnt(0)\n\t" \
    "s_branch .Lc5_%=\n\t" \
    ".Ls4_%=:\n\t" \
    "s_cmp_eq_u32 %[n], 0\n\t" \
    "s_cbranch_scc1 .Lk4_%=\n\t" \
    "s_sub_u32 %[n], %[n], 1\n\t" \
    "buffer_load_dwordx4 v[176:179], %[l16], %[rs], %[s1] offen offset:0 sc1\n\t" \
    "s_waitcnt vmcnt(0)\n\t" \
    "s_branch .Lc4_%=\n\t" \
    ".Ls3_%=:\n\t" \
    "s_cmp_eq_u32 %[n], 0\n\t" \
    "s_cbranch_scc1 .Lk3_%=\n\t" \
    "s_sub_u32 %[n], %[n], 1\n\t" \
    "buffer_load_dwordx4 v[172:175], %[l16], %[rs], %[s0] offen offset:3072 sc1\n\t" \
    "s_waitcnt vmcnt(0)\n\t" \
    "s_branch .Lc3_%=\n\t" \
    ".Ls2_%=:\n\t" \
    "s_cmp_eq_u32 %[n], 0\n\t" \
    "s_cbranch_scc1 .Lk2_%=\n\t" \
    "s_sub_u32 %[n], %[n], 1\n\t" \
    "buffer_load_dwordx4 v[168:171], %[l16], %[rs], %[s0] offen offset:2048 sc1\n\t" \
    "s_waitcnt vmcnt(0)\n\t" \
    "s_branch .Lc2_%=\n\t" \
    ".Ls1_%=:\n\t" \
    "s_cmp_eq_u32 %[n], 0\n\t" \
    "s_cbranch_scc1 .Lk1_%=\n\t" \
    "s_sub_u32 %[n], %[n], 1\n\t" \
    "buffer_load_dwordx4 v[164:167], %[l16], %[rs], %[s0] offen offset:1024 sc1\n\t" \
    "s_waitcnt vmcnt(0)\n\t" \
    "s_branch .Lc1_%=\n\t" \
    ".Ls0_%=:\n\t" \
    "s_cmp_eq_u32 %[n], 0\n\t" \
    "s_cbranch_scc1 .Lk0_%=\n\t" \
    "s_sub_u32 %[n], %[n], 1\n\t" \
    "buffer_load_dwordx4 v[160:163], %[l16], %[rs], %[s0] offen offset:0 sc1\n\t" \
    "s_branch .Lc0_%=\n\t" \
    ".Lme%=:\n\t"
#define ASM_WCS \
    "v_mfma_f32_16x16x32_bf16 v[196:199], v[0:3], v[216:219], 0\n\t" \
    "v_mfma_f32_16x16x32_bf16 v[200:203], v[80:83], v[216:219], 0\n\t" \
    "v_mfma_f32_16x16x32_bf16 v[196:199], v[4:7], v[216:219], v[196:199]\n\t" \
    "v_mfma_f32_16x16x32_bf16 v[200:203], v[84:87], v[216:219], v[200:203]\n\t" \
    "v_mfma_f32_16x16x32_bf16 v[196:199], v[8:11], v[216:219], v[196:199]\n\t" \
    "v_mfma_f32_16x16x32_bf16 v[200:203], v[88:91], v[216:219], v[200:203]\n\t" \
    "v_mfma_f32_16x16x32_bf16 v[196:199], v[12:15], v[216:219], v[196:199]\n\t" \
    "v_mfma_f32_16x16x32_bf16 v[200:203], v[92:95], v[216:219], v[200:203]\n\t" \
    "v_mfma_f32_16x16x32_bf16 v[196:199], v[16:19], v[216:219], v[196:199]\n\t" \
    "v_mfma_f32_16x16x32_bf16 v[200:203], v[96:99], v[216:219], v[200:203]\n\t" \
    "v_mfma_f32_16x16x32_bf16 v[196:199], v[20:23], v[216:219], v[196:199]\n\t" \
    "v_mfma_f32_16x16x32_bf16 v[200:203], v[100:103], v[216:219], v[200:203]\n\t" \
    "v_mfma_f32_16x16x32_bf16 v[196:199], v[24:27], v[216:219], v[196:199]\n\t" \
    "v_mfma_f32_16x16x32_bf16 v[200:203], v[104:107], v[216:219], v[200:203]\n\t" \
    "v_mfma_f32_16x16x32_bf16 v[196:199], v[28:31], v[216:219], v[196:199]\n\t" \
    "v_mfma_f32_16x16x32_bf16 v[200:203], v[108:111], v[216:219], v[200:203]\n\t" \
    "v_mfma_f32_16x16x32_bf16 v[196:199], v[32:35], v[216:219], v[196:199]\n\t" \
    "v_mfma_f32_16x16x32_bf16 v[200:203], v[112:115], v[216:219], v[200:203]\n\t" \
    "v_mfma_f32_16x16x32_bf16 v[196:199], v[36:39], v[216:219], v[196:199]\n\t" \
    "v_mfma_f32_16x16x32_bf16 v[200:203], v[116:119], v[216:219], v[200:203]\n\t" \
    "v_mfma_f32_16x16x32_bf16 v[196:199], v[40:43], v[216:219], v[196:199]\n\t" \
    "v_mfma_f32_16x16x32_bf16 v[200:203], v[120:123], v[216:219], v[200:203]\n\t" \
    "v_mfma_f32_16x16x32_bf16 v[196:199], v[44:47], v[216:219], v[196:199]\n\t" \
    "v_mfma_f32_16x16x32_bf16 v[200:203], v[124:127], v[216:219], v[200:203]\n\t" \
    "v_mfma_f32_16x16x32_bf16 v[196:199], v[48:51], v[216:219], v[196:199]\n\t" \
    "v_mfma_f32_16x16x32_bf16 v[200:203], v[128:131], v[216:219], v[200:203]\n\t" \
    "v_mfma_f32_16x16x32_bf16 v[196:199], v[52:55], v[216:219], v[196:199]\n\t" \
    "v_mfma_f32_16x16x32_bf16 v[200:203], v[132:135], v[216:219], v[200:203]\n\t" \
    "v_mfma_f32_16x16x32_bf16 v[196:199], v[56:59], v[216:219], v[196:199]\n\t" \
    "v_mfma_f32_16x16x32_bf16 v[200:203], v[136:139], v[216:219], v[200:203]\n\t" \
    "v_mfma_f32_16x16x32_bf16 v[196:199], v[60:63], v[216:219], v[196:199]\n\t" \
    "v_mfma_f32_16x16x32_bf16 v[200:203], v[140:143], v[216:219], v[200:203]\n\t" \
    "v_mfma_f32_16x16x32_bf16 v[196:199], v[64:67], v[216:219], v[196:199]\n\t" \
    "v_mfma_f32_16x16x32_bf16 v[200:203], v[144:147], v[216:219], v[200:203]\n\t" \
    "v_mfma_f32_16x16x32_bf16 v[196:199], v[68:71], v[216:219], v[196:199]\n\t" \
    "v_mfma_f32_16x16x32_bf16 v[200:203], v[148:151], v[216:219], v[200:203]\n\t" \
    "v_mfma_f32_16x16x32_bf16 v[196:199], v[72:75], v[216:219], v[196:199]\n\t" \
    "v_mfma_f32_16x16x32_bf16 v[200:203], v[152:155], v[216:219], v[200:203]\n\t" \
    "v_mfma_f32_16x16x32_bf16 v[196:199], v[76:79], v[216:219], v[196:199]\n\t" \
    "v_mfma_f32_16x16x32_bf16 v[200:203], v[156:159], v[216:219], v[200:203]\n\t" \
    "s_nop 7\n\t"
#define ASM_WT1_LO \
    "s_branch .Lt0%=\n\t" \
    ".Ltr%=:\n\t" \
    "s_cmp_gt_i32 %[n], 0\n\t" \
    "s_cbranch_scc0 .Ltg%=\n\t" \
    ".Lfw%=:\n\t" \
    "s_mov_b32 %[a], 0\n\t" \
    "v_max_u32 v252, v232, v233\n\t" \
    "v_max3_u32 v252, v234, v235, v252\n\t" \
    "v_cmp_eq_u32 vcc, -1, v252\n\t" \
    "s_cbranch_vccz .Lgw0_%=\n\t" \
    "buffer_load_dwordx4 v[232:235], %[l16], %[rs], %[i0] offen offset:0 sc1\n\t" \
    "s_mov_b32 %[a], 1\n\t" \
    ".Lgw0_%=:\n\t" \
    "v_max_u32 v252, v236, v237\n\t" \
    "v_max3_u32 v252, v238, v239, v252\n\t" \
    "v_cmp_eq_u32 vcc, -1, v252\n\t" \
    "s_cbranch_vccz .Lgw1_%=\n\t" \
    "buffer_load_dwordx4 v[236:239], %[l16], %[rs], %[i0] offen offset:1024 sc1\n\t" \
    "s_mov_b32 %[a], 1\n\t" \
    ".Lgw1_%=:\n\t" \
    "v_max_u32 v252, v240, v241\n\t" \
    "v_max3_u32 v252, v242, v243, v252\n\t" \
    "v_cmp_eq_u32 vcc, -1, v252\n\t" \
    "s_cbranch_vccz .Lgw2_%=\n\t" \
    "buffer_load_dwordx4 v[240:243], %[l16], %[rs], %[i0] offen offset:2048 sc1\n\t" \
    "s_mov_b32 %[a], 1\n\t" \
    ".Lgw2_%=:\n\t" \
    "v_max_u32 v252, v244, v245\n\t" \
    "v_max3_u32 v252, v246, v247, v252\n\t" \
    "v_cmp_eq_u32 vcc, -1, v252\n\t" \
    "s_cbranch_vccz .Lgw3_%=\n\t" \
    "buffer_load_dwordx4 v[244:247], %[l16], %[rs], %[i0] offen offset:3072 sc1\n\t" \
    "s_mov_b32 %[a], 1\n\t" \
    ".Lgw3_%=:\n\t" \
    "v_max_u32 v252, v248, v249\n\t" \
    "v_max3_u32 v252, v250, v251, v252\n\t" \
    "v_cmp_eq_u32 vcc, -1, v252\n\t" \
    "s_cbranch_vccz .Lgw4_%=\n\t" \
    "buffer_load_dwordx4 v[248:251], %[l16], %[rs], %[i1] offen offset:0 sc1\n\t" \
    "s_mov_b32 %[a], 1\n\t" \
    ".Lgw4_%=:\n\t" \
    "s_cmp_eq_u32 %[a], 0\n\t" \
    "s_cbranch_scc1 .Lhw%=\n\t" \
    "s_waitcnt vmcnt(20)\n\t" \
    ".Liw%=:\n\t" \
    "v_max_u32 v252, v232, v233\n\t" \
    "v_max3_u32 v252, v234, v235, v252\n\t" \
    "v_max3_u32 v252, v236, v237, v252\n\t" \
    "v_max3_u32 v252, v238, v239, v252\n\t" \
    "v_max3_u32 v252, v240, v241, v252\n\t" \
    "v_max3_u32 v252, v242, v243, v252\n\t" \
    "v_max3_u32 v252, v244, v245, v252\n\t" \
    "v_max3_u32 v252, v246, v247, v252\n\t" \
    "v_max3_u32 v252, v248, v249, v252\n\t" \
    "v_max3_u32 v252, v250, v251, v252\n\t" \
    "v_cmp_eq_u32 vcc, -1, v252\n\t" \
    "s_cbranch_vccz .Lhw%=\n\t" \
    "s_sleep 1\n\t" \
    "v_max_u32 v252, v232, v233\n\t" \
    "v_max3_u32 v252, v234, v235, v252\n\t" \
    "v_max3_u32 v252, v236, v237, v252\n\t" \
    "v_max3_u32 v252, v238, v239, v252\n\t" \
    "v_max3_u32 v252, v240, v241, v252\n\t" \
    "v_max3_u32 v252, v242, v243, v252\n\t" \
    "v_max3_u32 v252, v244, v245, v252\n\t" \
    "v_max3_u32 v252, v246, v247, v252\n\t" \
    "v_max3_u32 v252, v248, v249, v252\n\t" \
    "v_max3_u32 v252, v250, v251, v252\n\t" \
    "v_cmp_eq_u32 vcc, -1, v252\n\t" \
    "s_cbranch_vccz .Lhw%=\n\t" \
    "s_sleep 1\n\t" \
    "v_max_u32 v252, v232, v233\n\t" \
    "v_max3_u32 v252, v234, v235, v252\n\t" \
    "v_max3_u32 v252, v236, v237, v252\n\t" \
    "v_max3_u32 v252, v238, v239, v252\n\t" \
    "v_max3_u32 v252, v240, v241, v252\n\t" \
    "v_max3_u32 v252, v242, v243, v252\n\t" \
    "v_max3_u32 v252, v244, v245, v252\n\t" \
    "v_max3_u32 v252, v246, v247, v252\n\t" \
    "v_max3_u32 v252, v248, v249, v252\n\t" \
    "v_max3_u32 v252, v250, v251, v252\n\t" \
    "v_cmp_eq_u32 vcc, -1, v252\n\t" \
    "s_cbranch_vccz .Lhw%=\n\t" \
    "s_sleep 1\n\t" \
    "v_max_u32 v252, v232, v233\n\t" \
    "v_max3_u32 v252, v234, v235, v252\n\t" \
    "v_max3_u32 v252, v236, v237, v252\n\t" \
    "v_max3_u32 v252, v238, v239, v252\n\t" \
    "v_max3_u32 v252, v240, v241, v252\n\t" \
    "v_max3_u32 v252, v242, v243, v252\n\t" \
    "v_max3_u32 v252, v244, v245, v252\n\t" \
    "v_max3_u32 v252, v246, v247, v252\n\t" \
    "v_max3_u32 v252, v248, v249, v252\n\t" \
    "v_max3_u32 v252, v250, v251, v252\n\t" \
    "v_cmp_eq_u32 vcc, -1, v252\n\t" \
    "s_cbranch_vccz .Lhw%=\n\t" \
    "s_sleep 1\n\t" \
    "v_max_u32 v252, v232, v233\n\t" \
    "v_max3_u32 v252, v234, v235, v252\n\t" \
    "v_max3_u32 v252, v236, v237, v252\n\t" \
    "v_max3_u32 v252, v238, v239, v252\n\t" \
    "v_max3_u32 v252, v240, v241, v252\n\t" \
    "v_max3_u32 v252, v242, v243, v252\n\t" \
    "v_max3_u32 v252, v244, v245, v252\n\t" \
    "v_max3_u32 v252, v246, v247, v252\n\t" \
    "v_max3_u32 v252, v248, v249, v252\n\t" \
    "v_max3_u32 v252, v250, v251, v252\n\t" \
    "v_cmp_eq_u32 vcc, -1, v252\n\t" \
    "s_cbranch_vccz .Lhw%=\n\t" \
    "s_sleep 1\n\t" \
    "v_max_u32 v252, v232, v233\n\t" \
    "v_max3_u32 v252, v234, v235, v252\n\t" \
    "v_max3_u32 v252, v236, v237, v252\n\t" \
    "v_max3_u32 v252, v238, v239, v252\n\t" \
    "v_max3_u32 v252, v240, v241, v252\n\t" \
    "v_max3_u32 v252, v242, v243, v252\n\t" \
    "v_max3_u32 v252, v244, v245, v252\n\t" \
    "v_max3_u32 v252, v246, v247, v252\n\t" \
    "v_max3_u32 v252, v248, v249, v252\n\t" \
    "v_max3_u32 v252, v250, v251, v252\n\t" \
    "v_cmp_eq_u32 vcc, -1, v252\n\t" \
    "s_cbranch_vccz .Lhw%=\n\t" \
    "s_sleep 1\n\t" \
    "s_sub_u32 %[n], %[n], 1\n\t" \
    "s_cmp_gt_i32 %[n], 0\n\t" \
    "s_cbranch_scc1 .Lfw%=\n\t" \
    ".Lhw%=:\n\t" \
    "s_branch .Lta%=\n\t" \
    ".Lt0%=:\n\t" \
    "s_waitcnt vmcnt(1)\n\t" \
    ".Lta%=:\n\t" \
    "ds_write_b128 %[lw], v[232:235] offset:0\n\t" \
    "v_max_u32 v252, v232, v233\n\t" \
    "v_max3_u32 v252, v234, v235, v252\n\t" \
    "v_mfma_f32_16x16x32_bf16 v[188:191], v[232:235], v[232:235], 0\n\t" \
    "v_mfma_f32_16x16x32_bf16 v[192:195], v[216:219], v[232:235], 0\n\t" \
    "ds_write_b128 %[lw], v[236:239] offset:1024\n\t" \
    "v_max3_u32 v252, v236, v237, v252\n\t" \
    "v_max3_u32 v252, v238, v239, v252\n\t" \
    "v_mfma_f32_16x16x32_bf16 v[188:191], v[236:239], v[236:239], v[188:191]\n\t" \
    "v_mfma_f32_16x16x32_bf16 v[192:195], v[216:219], v[236:239], v[192:195]\n\t" \
    "ds_write_b128 %[lw], v[240:243] offset:2048\n\t" \
    "v_max3_u32 v252, v240, v241, v252\n\t" \
    "v_max3_u32 v252, v242, v243, v252\n\t" \
    "v_mfma_f32_16x16x32_bf16 v[188:191], v[240:243], v[240:243], v[188:191]\n\t" \
    "v_mfma_f32_16x16x32_bf16 v[192:195], v[216:219], v[240:243], v[192:195]\n\t" \
    "ds_write_b128 %[lw], v[244:247] offset:3072\n\t" \
    "v_max3_u32 v252, v244, v245, v252\n\t" \
    "v_max3_u32 v252, v246, v247, v252\n\t" \
    "v_mfma_f32_16x16x32_bf16 v[188:191], v[244:247], v[244:247], v[188:191]\n\t" \
    "v_mfma_f32_16x16x32_bf16 v[192:195], v[216:219], v[244:247], v[192:195]\n\t" \
    "ds_write_b128 %[lw], v[248:251] offset:4096\n\t" \
    "v_max3_u32 v252, v248, v249, v252\n\t" \
    "v_max3_u32 v252, v250, v251, v252\n\t" \
    "v_mfma_f32_16x16x32_bf16 v[188:191], v[248:251], v[248:251], v[188:191]\n\t" \
    "v_mfma_f32_16x16x32_bf16 v[192:195], v[216:219], v[248:251], v[192:195]\n\t" \
    "v_cmp_eq_u32 vcc, -1, v252\n\t" \
    "s_cbranch_vccnz .Ltr%=\n\t" \
    ".Ltg%=:\n\t" \
    ASM_ST2 \
    "s_nop 5\n\t" \
    "ds_write_b128 %[lw], v[188:191] offset:5120\n\t" \
    "ds_write_b128 %[lw], v[192:195] offset:6144\n\t" \
    "s_waitcnt lgkmcnt(0)\n\t" \
    "s_setprio 2\n\t" \
    "s_barrier\n\t" ASM_ST \
    "ds_read_b128 v[160:163], %[la] offset:0\n\t" \
    "ds_read_b128 v[164:167], %[la] offset:1024\n\t" \
    "ds_read_b128 v[168:171], %[la] offset:2048\n\t" \
    "ds_read2st64_b32 v[204:205], %[sr] offset0:20 offset1:24\n\t" \
    "ds_read_b128 v[172:175], %[la] offset:3072\n\t" \
    "ds_read_b128 v[176:179], %[la] offset:8192\n\t" \
    "ds_read_b128 v[180:183], %[la] offset:9216\n\t" \
    "s_waitcnt lgkmcnt(6)\n\t" \
    "v_mfma_f32_16x16x32_bf16 v[188:191], v[0:3], v[160:163], 0\n\t" \
    "ds_read_b128 v[184:187], %[la] offset:10240\n\t" \
    "v_mfma_f32_16x16x32_bf16 v[192:195], v[80:83], v[160:163], 0\n\t" \
    "s_waitcnt lgkmcnt(6)\n\t" \
    "v_mfma_f32_16x16x32_bf16 v[188:191], v[4:7], v[164:167], v[188:191]\n\t" \
    "ds_read_b128 v[160:163], %[la] offset:11264\n\t" \
    "v_mfma_f32_16x16x32_bf16 v[192:195], v[84:87], v[164:167], v[192:195]\n\t" \
    "s_waitcnt lgkmcnt(6)\n\t" \
    "v_mfma_f32_16x16x32_bf16 v[188:191], v[8:11], v[168:171], v[188:191]\n\t" \
    "ds_read_b128 v[164:167], %[la] offset:16384\n\t" \
    "s_waitcnt lgkmcnt(6)\n\t" \
    "v_mfma_f32_16x16x32_bf16 v[192:195], v[88:91], v[168:171], v[192:195]\n\t" \
    "v_permlane16_swap_b32 v204, v205\n\t" \
    "s_waitcnt lgkmcnt(5)\n\t" \
    "v_mfma_f32_16x16x32_bf16 v[188:191], v[12:15], v[172:175], v[188:191]\n\t" \
    "ds_read_b128 v[168:171], %[la] offset:17408\n\t" \
    "v_mfma_f32_16x16x32_bf16 v[192:195], v[92:95], v[172:175], v[192:195]\n\t" \
    "v_add_f32 v206, v204, v205\n\t" \
    "s_waitcnt lgkmcnt(5)\n\t" \
    "v_mfma_f32_16x16x32_bf16 v[188:191], v[16:19], v[176:179], v[188:191]\n\t" \
    "ds_read_b128 v[172:175], %[la] offset:18432\n\t" \
    "v_add_f32 v207, v204, v205\n\t" \
    "v_mfma_f32_16x16x32_bf16 v[192:195], v[96:99], v[176:179], v[192:195]\n\t" \
    "s_waitcnt lgkmcnt(5)\n\t" \
    "v_mfma_f32_16x16x32_bf16 v[188:191], v[20:23], v[180:183], v[188:191]\n\t" \
    "ds_read_b128 v[176:179], %[la] offset:19456\n\t" \
    "v_permlane32_swap_b32 v206, v207\n\t" \
    "v_mfma_f32_16x16x32_bf16 v[192:195], v[100:103], v[180:183], v[192:195]\n\t" \
    "s_waitcnt lgkmcnt(5)\n\t" \
    "v_mfma_f32_16x16x32_bf16 v[188:191], v[24:27], v[184:187], v[188:191]\n\t" \
    "ds_read_b128 v[180:183], %[la] offset:24576\n\t" \
    "v_add_f32 v204, v206, v207\n\t" \
    "v_mfma_f32_16x16x32_bf16 v[192:195], v[104:107], v[184:187], v[192:195]\n\t" \
    "v_add_f32 v205, v206, v207\n\t" \
    "s_waitcnt lgkmcnt(5)\n\t" \
    "v_mfma_f32_16x16x32_bf16 v[188:191], v[28:31], v[160:163], v[188:191]\n\t" \
    "ds_read_b128 v[184:187], %[la] offset:25600\n\t" \
    "v_mfma_f32_16x16x32_bf16 v[192:195], v[108:111], v[160:163], v[192:195]\n\t" \
    "v_permlane16_swap_b32 v204, v205\n\t" \
    "s_waitcnt lgkmcnt(5)\n\t" \
    "v_mfma_f32_16x16x32_bf16 v[188:191], v[32:35], v[164:167], v[188:191]\n\t" \
    "ds_read_b128 v[160:163], %[la] offset:26624\n\t" \
    "v_mfma_f32_16x16x32_bf16 v[192:195], v[112:115], v[164:167], v[192:195]\n\t" \
    "v_mul_f32 v205, %[kd], v205\n\t" \
    "s_waitcnt lgkmcnt(5)\n\t" \
    "v_mfma_f32_16x16x32_bf16 v[188:191], v[36:39], v[168:171], v[188:191]\n\t" \
    "ds_read_b128 v[164:167], %[la] offset:27648\n\t" \
    "v_mul_f32 v204, %[kd], v204\n\t" \
    "v_mfma_f32_16x16x32_bf16 v[192:195], v[116:119], v[168:171], v[192:195]\n\t" \
    "v_fma_f32 v204, -v205, v205, v204\n\t" \
    "s_waitcnt lgkmcnt(5)\n\t" \
    "v_mfma_f32_16x16x32_bf16 v[188:191], v[40:43], v[172:175], v[188:191]\n\t" \
    "ds_read_b128 v[168:171], %[la] offset:4096\n\t" \
    "v_max_f32 v204, 0, v204\n\t" \
    "v_mfma_f32_16x16x32_bf16 v[192:195], v[120:123], v[172:175], v[192:195]\n\t" \
    "v_add_f32 v206, %[ke], v204\n\t" \
    "s_waitcnt lgkmcnt(5)\n\t" \
    "v_mfma_f32_16x16x32_bf16 v[188:191], v[44:47], v[176:179], v[188:191]\n\t" \
    "ds_read_b128 v[172:175], %[la] offset:12288\n\t" \
    "v_rsq_f32 v206, v206\n\t" \
    "v_mfma_f32_16x16x32_bf16 v[192:195], v[124:127], v[176:179], v[192:195]\n\t" \
    "s_waitcnt lgkmcnt(5)\n\t" \
    "v_mfma_f32_16x16x32_bf16 v[188:191], v[48:51], v[180:183], v[188:191]\n\t" \
    "ds_read_b128 v[176:179], %[la] offset:20480\n\t" \
    "v_mul_f32 v207, v204, v206\n\t" \
    "v_mfma_f32_16x16x32_bf16 v[192:195], v[128:131], v[180:183], v[192:195]\n\t" \
    "v_fma_f32 v207, v207, v206, %[ke]\n\t" \
    "s_waitcnt lgkmcnt(5)\n\t" \
    "v_mfma_f32_16x16x32_bf16 v[188:191], v[52:55], v[184:187], v[188:191]\n\t" \
    "ds_read_b128 v[180:183], %[la] offset:28672\n\t" \
    "v_rsq_f32 v207, v207\n\t" \
    "v_mfma_f32_16x16x32_bf16 v[192:195], v[132:135], v[184:187], v[192:195]\n\t" \
    "v_mul_f32 v206, v206, v207\n\t" \
    "v_mul_f32 v207, -v205, v206\n\t" \
    "s_waitcnt lgkmcnt(5)\n\t" \
    "v_mfma_f32_16x16x32_bf16 v[188:191], v[56:59], v[160:163], v[188:191]\n\t" \
    "v_mul_f32 v208, v196, v207\n\t" \
    "v_mul_f32 v209, v197, v207\n\t" \
    "v_mfma_f32_16x16x32_bf16 v[192:195], v[136:139], v[160:163], v[192:195]\n\t" \
    "v_mul_f32 v210, v198, v207\n\t" \
    "v_mul_f32 v211, v199, v207\n\t" \
    "s_waitcnt lgkmcnt(4)\n\t" \
    "v_mfma_f32_16x16x32_bf16 v[188:191], v[60:63], v[164:167], v[188:191]\n\t" \
    "v_mul_f32 v212, v200, v207\n\t" \
    "v_mul_f32 v213, v201, v207\n\t" \
    "v_mfma_f32_16x16x32_bf16 v[192:195], v[140:143], v[164:167], v[192:195]\n\t" \
    "v_mul_f32 v214, v202, v207\n\t" \
    "v_mul_f32 v215, v203, v207\n\t" \
    "s_waitcnt lgkmcnt(3)\n\t" \
    "v_mfma_f32_16x16x32_bf16 v[188:191], v[64:67], v[168:171], v[188:191]\n\t" \
    "s_cmp_lg_u32 %[sw], 0\n\t" \
    "s_cbranch_scc1 .Lts%=\n\t" \
    "v_cvt_pk_bf16_f32 v217, v204, v204\n\t" \
    "v_lshlrev_b32 v217, 16, v217\n\t" \
    "v_add_f32 v217, %[ke], v217\n\t" \
    "v_cvt_pk_bf16_f32 v217, v217, v217\n\t" \
    "v_lshlrev_b32 v217, 16, v217\n\t" \
    "v_sqrt_f32 v217, v217\n\t" \
    "v_cvt_pk_bf16_f32 v216, v205, v205\n\t" \
    "v_lshlrev_b32 v216, 16, v216\n\t" \
    "v_cvt_pk_bf16_f32 v217, v217, v217\n\t" \
    "v_lshlrev_b32 v217, 16, v217\n\t" \
    "v_rcp_f32 v217, v217\n\t" \
    "v_lshrrev_b32 v218, 1, %[l16]\n\t" \
    "v_and_b32 v218, 0x78, v218\n\t" \
    "buffer_store_dwordx2 v[216:217], v218, %[rs], %[ss] offen sc1\n\t" \
    ".Lts%=:\n\t" \
    "v_mov_b64 v[216:217], -1\n\t" \
    "v_mfma_f32_16x16x32_bf16 v[192:195], v[144:147], v[168:171], v[192:195]\n\t" \
    "v_mov_b64 v[218:219], -1\n\t" \
    "buffer_store_dwordx4 v[216:219], %[ob], %[rs], %[sa] offen sc1\n\t" \
    "s_waitcnt lgkmcnt(2)\n\t" \
    "v_mfma_f32_16x16x32_bf16 v[188:191], v[68:71], v[172:175], v[188:191]\n\t" \
    "v_mfma_f32_16x16x32_bf16 v[192:195], v[148:151], v[172:175], v[192:195]\n\t" \
    "s_waitcnt lgkmcnt(1)\n\t" \
    "v_mfma_f32_16x16x32_bf16 v[188:191], v[72:75], v[176:179], v[188:191]\n\t" \
    "v_mfma_f32_16x16x32_bf16 v[192:195], v[152:155], v[176:179], v[192:195]\n\t" \
    "s_waitcnt lgkmcnt(0)\n\t" \
    "v_mfma_f32_16x16x32_bf16 v[188:191], v[76:79], v[180:183], v[188:191]\n\t" \
    "v_mfma_f32_16x16x32_bf16 v[192:195], v[156:159], v[180:183], v[192:195]\n\t" \
    "s_setprio 2\n\t" \
    "buffer_load_dwordx4 v[232:235], %[l16], %[rs], %[n0] offen offset:0 sc1\n\t" \
    "buffer_load_dwordx4 v[236:239], %[l16], %[rs], %[n0] offen offset:1024 sc1\n\t" \
    "buffer_load_dwordx4 v[240:243], %[l16], %[rs], %[n0] offen offset:2048 sc1\n\t" \
    "buffer_load_dwordx4 v[244:247], %[l16], %[rs], %[n0] offen offset:3072 sc1\n\t" \
    "buffer_load_dwordx4 v[248:251], %[l16], %[rs], %[n1] offen offset:0 sc1\n\t" \
    ASM_ST3 \
    "v_fma_f32 v188, v188, v206, v208\n\t" \
    "v_fma_f32 v189, v189, v206, v209\n\t" \
    "v_fma_f32 v190, v190, v206, v210\n\t" \
    "v_fma_f32 v191, v191, v206, v211\n\t" \
    "v_mul_f32 v160, v188, v188\n\t" \
    "v_mul_f32 v161, v189, v189\n\t" \
    "v_mul_f32 v162, v190, v190\n\t" \
    "v_mul_f32 v163, v191, v191\n\t" \
    "v_fma_f32 v160, v160, %[k1], v220\n\t" \
    "v_fma_f32 v161, v161, %[k1], v220\n\t" \
    "v_fma_f32 v162, v162, %[k1], v220\n\t" \
    "v_fma_f32 v163, v163, %[k1], v220\n\t" \
    "v_mul_f32 v160, v188, v160\n\t" \
    "v_mul_f32 v161, v189, v161\n\t" \
    "v_mul_f32 v162, v190, v162\n\t" \
    "v_mul_f32 v163, v191, v163\n\t" \
    "v_fma_f32 v192, v192, v206, v212\n\t" \
    "v_fma_f32 v193, v193, v206, v213\n\t" \
    "v_fma_f32 v194, v194, v206, v214\n\t" \
    "v_fma_f32 v195, v195, v206, v215\n\t" \
    "v_mul_f32 v164, v192, v192\n\t" \
    "v_mul_f32 v165, v193, v193\n\t" \
    "v_mul_f32 v166, v194, v194\n\t" \
    "v_mul_f32 v167, v195, v195\n\t" \
    "v_fma_f32 v164, v164, %[k1], v220\n\t" \
    "v_fma_f32 v165, v165, %[k1], v220\n\t" \
    "v_fma_f32 v166, v166, %[k1], v220\n\t" \
    "v_fma_f32 v167, v167, %[k1], v220\n\t" \
    "v_mul_f32 v164, v192, v164\n\t" \
    "v_mul_f32 v165, v193, v165\n\t" \
    "v_mul_f32 v166, v194, v166\n\t" \
    "v_mul_f32 v167, v195, v167\n\t" \
    "v_exp_f32 v160, v160\n\t" \
    "v_exp_f32 v161, v161\n\t" \
    "v_exp_f32 v162, v162\n\t" \
    "v_exp_f32 v163, v163\n\t" \
    "v_exp_f32 v164, v164\n\t" \
    "v_exp_f32 v165, v165\n\t" \
    "v_exp_f32 v166, v166\n\t" \
    "v_exp_f32 v167, v167\n\t" \
    "v_add_f32 v160, 1.0, v160\n\t" \
    "v_add_f32 v161, 1.0, v161\n\t" \
    "v_add_f32 v162, 1.0, v162\n\t" \
    "v_add_f32 v163, 1.0, v163\n\t" \
    "v_add_f32 v164, 1.0, v164\n\t" \
    "v_add_f32 v165, 1.0, v165\n\t" \
    "v_add_f32 v166, 1.0, v166\n\t" \
    "v_add_f32 v167, 1.0, v167\n\t" \
    "v_rcp_f32 v160, v160\n\t" \
    "v_rcp_f32 v161, v161\n\t" \
    "v_rcp_f32 v162, v162\n\t" \
    "v_rcp_f32 v163, v163\n\t" \
    "v_rcp_f32 v164, v164\n\t" \
    "v_rcp_f32 v165, v165\n\t" \
    "v_rcp_f32 v166, v166\n\t" \
    "v_rcp_f32 v167, v167\n\t" \
    "v_mul_f32 v188, v188, v160\n\t" \
    "v_mul_f32 v189, v189, v161\n\t" \
    "v_mul_f32 v190, v190, v162\n\t" \
    "v_mul_f32 v191, v191, v163\n\t" \
    "v_mul_f32 v192, v192, v164\n\t" \
    "v_mul_f32 v193, v193, v165\n\t" \
    "v_mul_f32 v194, v194, v166\n\t" \
    "v_mul_f32 v195, v195, v167\n\t" \
    "v_cvt_pk_bf16_f32 v188, v188, v189\n\t" \
    "v_cvt_pk_bf16_f32 v189, v190, v191\n\t" \
    "v_cvt_pk_bf16_f32 v190, v192, v193\n\t" \
    "v_cvt_pk_bf16_f32 v191, v194, v195\n\t" \
    "v_min_u32 v188, -2, v188\n\t" \
    "v_min_u32 v189, -2, v189\n\t" \
    "v_min_u32 v190, -2, v190\n\t" \
    "v_min_u32 v191, -2, v191\n\t" \
    "v_mov_b32 v216, 0x3f803f80\n\t" \
    "v_mov_b32 v217, 0x3f803f80\n\t" \
    "v_mov_b32 v218, 0x3f803f80\n\t" \
    "v_mov_b32 v219, 0x3f803f80\n\t" \
    "v_permlane16_swap_b32 v188, v190\n\t" \
    "v_permlane16_swap_b32 v189, v191\n\t" \
    "s_waitcnt vmcnt(6)\n\t" \
    "buffer_store_dwordx4 v[188:191], %[oa], %[rs], %[so] offen sc1\n\t" \
    "s_nop 1\n\t" \
    "s_setprio 0\n\t"
#define ASM_WT1_HI \
    "s_setprio 1\n\t" \
    "s_barrier\n\t" ASM_ST \
    "ds_read_b128 v[160:163], %[la] offset:0\n\t" \
    "ds_read_b128 v[164:167], %[la] offset:1024\n\t" \
    "ds_read_b128 v[168:171], %[la] offset:2048\n\t" \
    "ds_read2st64_b32 v[204:205], %[sr] offset0:20 offset1:24\n\t" \
    "ds_read_b128 v[172:175], %[la] offset:3072\n\t" \
    "ds_read_b128 v[176:179], %[la] offset:8192\n\t" \
    "ds_read_b128 v[180:183], %[la] offset:9216\n\t" \
    "s_waitcnt lgkmcnt(6)\n\t" \
    "v_mfma_f32_16x16x32_bf16 v[188:191], v[0:3], v[160:163], 0\n\t" \
    "ds_read_b128 v[184:187], %[la] offset:10240\n\t" \
    "v_mfma_f32_16x16x32_bf16 v[192:195], v[80:83], v[160:163], 0\n\t" \
    "s_waitcnt lgkmcnt(6)\n\t" \
    "v_mfma_f32_16x16x32_bf16 v[188:191], v[4:7], v[164:167], v[188:191]\n\t" \
    "ds_read_b128 v[160:163], %[la] offset:11264\n\t" \
    "v_mfma_f32_16x16x32_bf16 v[192:195], v[84:87], v[164:167], v[192:195]\n\t" \
    "s_waitcnt lgkmcnt(6)\n\t" \
    "v_mfma_f32_16x16x32_bf16 v[188:191], v[8:11], v[168:171], v[188:191]\n\t" \
    "ds_read_b128 v[164:167], %[la] offset:16384\n\t" \
    "s_waitcnt lgkmcnt(6)\n\t" \
    "v_mfma_f32_16x16x32_bf16 v[192:195], v[88:91], v[168:171], v[192:195]\n\t" \
    "v_permlane16_swap_b32 v204, v205\n\t" \
    "s_waitcnt lgkmcnt(5)\n\t" \
    "v_mfma_f32_16x16x32_bf16 v[188:191], v[12:15], v[172:175], v[188:191]\n\t" \
    "ds_read_b128 v[168:171], %[la] offset:17408\n\t" \
    "v_mfma_f32_16x16x32_bf16 v[192:195], v[92:95], v[172:175], v[192:195]\n\t" \
    "v_add_f32 v206, v204, v205\n\t" \
    "s_waitcnt lgkmcnt(5)\n\t" \
    "v_mfma_f32_16x16x32_bf16 v[188:191], v[16:19], v[176:179], v[188:191]\n\t" \
    "ds_read_b128 v[172:175], %[la] offset:18432\n\t" \
    "v_add_f32 v207, v204, v205\n\t" \
    "v_mfma_f32_16x16x32_bf16 v[192:195], v[96:99], v[176:179], v[192:195]\n\t" \
    "s_waitcnt lgkmcnt(5)\n\t" \
    "v_mfma_f32_16x16x32_bf16 v[188:191], v[20:23], v[180:183], v[188:191]\n\t" \
    "ds_read_b128 v[176:179], %[la] offset:19456\n\t" \
    "v_permlane32_swap_b32 v206, v207\n\t" \
    "v_mfma_f32_16x16x32_bf16 v[192:195], v[100:103], v[180:183], v[192:195]\n\t" \
    "s_waitcnt lgkmcnt(5)\n\t" \
    "v_mfma_f32_16x16x32_bf16 v[188:191], v[24:27], v[184:187], v[188:191]\n\t" \
    "ds_read_b128 v[180:183], %[la] offset:24576\n\t" \
    "v_add_f32 v204, v206, v207\n\t" \
    "v_mfma_f32_16x16x32_bf16 v[192:195], v[104:107], v[184:187], v[192:195]\n\t" \
    "v_add_f32 v205, v206, v207\n\t" \
    "s_waitcnt lgkmcnt(5)\n\t" \
    "v_mfma_f32_16x16x32_bf16 v[188:191], v[28:31], v[160:163], v[188:191]\n\t" \
    "ds_read_b128 v[184:187], %[la] offset:25600\n\t" \
    "v_mfma_f32_16x16x32_bf16 v[192:195], v[108:111], v[160:163], v[192:195]\n\t" \
    "v_permlane16_swap_b32 v204, v205\n\t" \
    "s_waitcnt lgkmcnt(5)\n\t" \
    "v_mfma_f32_16x16x32_bf16 v[188:191], v[32:35], v[164:167], v[188:191]\n\t" \
    "ds_read_b128 v[160:163], %[la] offset:26624\n\t" \
    "v_mfma_f32_16x16x32_bf16 v[192:195], v[112:115], v[164:167], v[192:195]\n\t" \
    "v_mul_f32 v205, %[kd], v205\n\t" \
    "s_waitcnt lgkmcnt(5)\n\t" \
    "v_mfma_f32_16x16x32_bf16 v[188:191], v[36:39], v[168:171], v[188:191]\n\t" \
    "ds_read_b128 v[164:167], %[la] offset:27648\n\t" \
    "v_mul_f32 v204, %[kd], v204\n\t" \
    "v_mfma_f32_16x16x32_bf16 v[192:195], v[116:119], v[168:171], v[192:195]\n\t" \
    "v_fma_f32 v204, -v205, v205, v204\n\t" \
    "s_waitcnt lgkmcnt(5)\n\t" \
    "v_mfma_f32_16x16x32_bf16 v[188:191], v[40:43], v[172:175], v[188:191]\n\t" \
    "ds_read_b128 v[168:171], %[la] offset:4096\n\t" \
    "v_max_f32 v204, 0, v204\n\t" \
    "v_mfma_f32_16x16x32_bf16 v[192:195], v[120:123], v[172:175], v[192:195]\n\t" \
    "v_add_f32 v206, %[ke], v204\n\t" \
    "s_waitcnt lgkmcnt(5)\n\t" \
    "v_mfma_f32_16x16x32_bf16 v[188:191], v[44:47], v[176:179], v[188:191]\n\t" \
    "ds_read_b128 v[172:175], %[la] offset:12288\n\t" \
    "v_rsq_f32 v206, v206\n\t" \
    "v_mfma_f32_16x16x32_bf16 v[192:195], v[124:127], v[176:179], v[192:195]\n\t" \
    "s_waitcnt lgkmcnt(5)\n\t" \
    "v_mfma_f32_16x16x32_bf16 v[188:191], v[48:51], v[180:183], v[188:191]\n\t" \
    "ds_read_b128 v[176:179], %[la] offset:20480\n\t" \
    "v_mul_f32 v207, v204, v206\n\t" \
    "v_mfma_f32_16x16x32_bf16 v[192:195], v[128:131], v[180:183], v[192:195]\n\t" \
    "v_fma_f32 v207, v207, v206, %[ke]\n\t" \
    "s_waitcnt lgkmcnt(5)\n\t" \
    "v_mfma_f32_16x16x32_bf16 v[188:191], v[52:55], v[184:187], v[188:191]\n\t" \
    "ds_read_b128 v[180:183], %[la] offset:28672\n\t" \
    "v_rsq_f32 v207, v207\n\t" \
    "v_mfma_f32_16x16x32_bf16 v[192:195], v[132:135], v[184:187], v[192:195]\n\t" \
    "v_mul_f32 v206, v206, v207\n\t" \
    "v_mul_f32 v207, -v205, v206\n\t" \
    "s_waitcnt lgkmcnt(5)\n\t" \
    "v_mfma_f32_16x16x32_bf16 v[188:191], v[56:59], v[160:163], v[188:191]\n\t" \
    "v_mul_f32 v208, v196, v207\n\t" \
    "v_mul_f32 v209, v197, v207\n\t" \
    "v_mfma_f32_16x16x32_bf16 v[192:195], v[136:139], v[160:163], v[192:195]\n\t" \
    "v_mul_f32 v210, v198, v207\n\t" \
    "v_mul_f32 v211, v199, v207\n\t" \
    "s_waitcnt lgkmcnt(4)\n\t" \
    "v_mfma_f32_16x16x32_bf16 v[188:191], v[60:63], v[164:167], v[188:191]\n\t" \
    "v_mul_f32 v212, v200, v207\n\t" \
    "v_mul_f32 v213, v201, v207\n\t" \
    "v_mfma_f32_16x16x32_bf16 v[192:195], v[140:143], v[164:167], v[192:195]\n\t" \
    "v_mul_f32 v214, v202, v207\n\t" \
    "v_mul_f32 v215, v203, v207\n\t" \
    "s_waitcnt lgkmcnt(3)\n\t" \
    "v_mfma_f32_16x16x32_bf16 v[188:191], v[64:67], v[168:171], v[188:191]\n\t" \
    "v_mov_b64 v[216:217], -1\n\t" \
    "v_mov_b64 v[218:219], -1\n\t" \
    "v_mfma_f32_16x16x32_bf16 v[192:195], v[144:147], v[168:171], v[192:195]\n\t" \
    "buffer_store_dwordx4 v[216:219], %[ob], %[rs], %[sa] offen sc1\n\t" \
    "s_waitcnt lgkmcnt(2)\n\t" \
    "v_mfma_f32_16x16x32_bf16 v[188:191], v[68:71], v[172:175], v[188:191]\n\t" \
    "v_mfma_f32_16x16x32_bf16 v[192:195], v[148:151], v[172:175], v[192:195]\n\t" \
    "s_waitcnt lgkmcnt(1)\n\t" \
    "v_mfma_f32_16x16x32_bf16 v[188:191], v[72:75], v[176:179], v[188:191]\n\t" \
    "v_mfma_f32_16x16x32_bf16 v[192:195], v[152:155], v[176:179], v[192:195]\n\t" \
    "s_waitcnt lgkmcnt(0)\n\t" \
    "v_mfma_f32_16x16x32_bf16 v[188:191], v[76:79], v[180:183], v[188:191]\n\t" \
    "v_mfma_f32_16x16x32_bf16 v[192:195], v[156:159], v[180:183], v[192:195]\n\t" \
    ASM_ST3 \
    "s_nop 4\n\t" \
    "v_fma_f32 v188, v188, v206, v208\n\t" \
    "v_fma_f32 v189, v189, v206, v209\n\t" \
    "v_fma_f32 v190, v190, v206, v210\n\t" \
    "v_fma_f32 v191, v191, v206, v211\n\t" \
    "v_mul_f32 v160, v188, v188\n\t" \
    "v_mul_f32 v161, v189, v189\n\t" \
    "v_mul_f32 v162, v190, v190\n\t" \
    "v_mul_f32 v163, v191, v191\n\t" \
    "v_fma_f32 v160, v160, %[k1], v220\n\t" \
    "v_fma_f32 v161, v161, %[k1], v220\n\t" \
    "v_fma_f32 v162, v162, %[k1], v220\n\t" \
    "v_fma_f32 v163, v163, %[k1], v220\n\t" \
    "v_mul_f32 v160, v188, v160\n\t" \
    "v_mul_f32 v161, v189, v161\n\t" \
    "v_mul_f32 v162, v190, v162\n\t" \
    "v_mul_f32 v163, v191, v163\n\t" \
    "v_fma_f32 v192, v192, v206, v212\n\t" \
    "v_fma_f32 v193, v193, v206, v213\n\t" \
    "v_fma_f32 v194, v194, v206, v214\n\t" \
    "v_fma_f32 v195, v195, v206, v215\n\t" \
    "v_mul_f32 v164, v192, v192\n\t" \
    "v_mul_f32 v165, v193, v193\n\t" \
    "v_mul_f32 v166, v194, v194\n\t" \
    "v_mul_f32 v167, v195, v195\n\t" \
    "v_fma_f32 v164, v164, %[k1], v220\n\t" \
    "v_fma_f32 v165, v165, %[k1], v220\n\t" \
    "v_fma_f32 v166, v166, %[k1], v220\n\t" \
    "v_fma_f32 v167, v167, %[k1], v220\n\t" \
    "v_mul_f32 v164, v192, v164\n\t" \
    "v_mul_f32 v165, v193, v165\n\t" \
    "v_mul_f32 v166, v194, v166\n\t" \
    "v_mul_f32 v167, v195, v167\n\t" \
    "v_exp_f32 v160, v160\n\t" \
    "v_exp_f32 v161, v161\n\t" \
    "v_exp_f32 v162, v162\n\t" \
    "v_exp_f32 v163, v163\n\t" \
    "v_exp_f32 v164, v164\n\t" \
    "v_exp_f32 v165, v165\n\t" \
    "v_exp_f32 v166, v166\n\t" \
    "v_exp_f32 v167, v167\n\t" \
    "v_add_f32 v160, 1.0, v160\n\t" \
    "v_add_f32 v161, 1.0, v161\n\t" \
    "v_add_f32 v162, 1.0, v162\n\t" \
    "v_add_f32 v163, 1.0, v163\n\t" \
    "v_add_f32 v164, 1.0, v164\n\t" \
    "v_add_f32 v165, 1.0, v165\n\t" \
    "v_add_f32 v166, 1.0, v166\n\t" \
    "v_add_f32 v167, 1.0, v167\n\t" \
    "v_rcp_f32 v160, v160\n\t" \
    "v_rcp_f32 v161, v161\n\t" \
    "v_rcp_f32 v162, v162\n\t" \
    "v_rcp_f32 v163, v163\n\t" \
    "v_rcp_f32 v164, v164\n\t" \
    "v_rcp_f32 v165, v165\n\t" \
    "v_rcp_f32 v166, v166\n\t" \
    "v_rcp_f32 v167, v167\n\t" \
    "v_mul_f32 v188, v188, v160\n\t" \
    "v_mul_f32 v189, v189, v161\n\t" \
    "v_mul_f32 v190, v190, v162\n\t" \
    "v_mul_f32 v191, v191, v163\n\t" \
    "v_mul_f32 v192, v192, v164\n\t" \
    "v_mul_f32 v193, v193, v165\n\t" \
    "v_mul_f32 v194, v194, v166\n\t" \
    "v_mul_f32 v195, v195, v167\n\t" \
    "v_cvt_pk_bf16_f32 v188, v188, v189\n\t" \
    "v_cvt_pk_bf16_f32 v189, v190, v191\n\t" \
    "v_cvt_pk_bf16_f32 v190, v192, v193\n\t" \
    "v_cvt_pk_bf16_f32 v191, v194, v195\n\t" \
    "v_min_u32 v188, -2, v188\n\t" \
    "v_min_u32 v189, -2, v189\n\t" \
    "v_min_u32 v190, -2, v190\n\t" \
    "v_min_u32 v191, -2, v191\n\t" \
    "s_nop 1\n\t" \
    "v_permlane16_swap_b32 v188, v190\n\t" \
    "v_permlane16_swap_b32 v189, v191\n\t" \
    "s_waitcnt vmcnt(1)\n\t" \
    "buffer_store_dwordx4 v[188:191], %[oa], %[rs], %[so] offen sc1\n\t" \
    "s_nop 1\n\t"
#define ASM_WT2_LO \
    "s_branch .Lt0%=\n\t" \
    ".Ltr%=:\n\t" \
    "s_cmp_gt_i32 %[n], 0\n\t" \
    "s_cbranch_scc0 .Ltg%=\n\t" \
    ".Lfw%=:\n\t" \
    "s_mov_b32 %[a], 0\n\t" \
    "v_max_u32 v252, v232, v233\n\t" \
    "v_max3_u32 v252, v234, v235, v252\n\t" \
    "v_cmp_eq_u32 vcc, -1, v252\n\t" \
    "s_cbranch_vccz .Lgw0_%=\n\t" \
    "buffer_load_dwordx4 v[232:235], %[l16], %[rs], %[i0] offen offset:0 sc1\n\t" \
    "s_mov_b32 %[a], 1\n\t" \
    ".Lgw0_%=:\n\t" \
    "v_max_u32 v252, v236, v237\n\t" \
    "v_max3_u32 v252, v238, v239, v252\n\t" \
    "v_cmp_eq_u32 vcc, -1, v252\n\t" \
    "s_cbranch_vccz .Lgw1_%=\n\t" \
    "buffer_load_dwordx4 v[236:239], %[l16], %[rs], %[i0] offen offset:1024 sc1\n\t" \
    "s_mov_b32 %[a], 1\n\t" \
    ".Lgw1_%=:\n\t" \
    "v_max_u32 v252, v240, v241\n\t" \
    "v_max3_u32 v252, v242, v243, v252\n\t" \
    "v_cmp_eq_u32 vcc, -1, v252\n\t" \
    "s_cbranch_vccz .Lgw2_%=\n\t" \
    "buffer_load_dwordx4 v[240:243], %[l16], %[rs], %[i0] offen offset:2048 sc1\n\t" \
    "s_mov_b32 %[a], 1\n\t" \
    ".Lgw2_%=:\n\t" \
    "v_max_u32 v252, v244, v245\n\t" \
    "v_max3_u32 v252, v246, v247, v252\n\t" \
    "v_cmp_eq_u32 vcc, -1, v252\n\t" \
    "s_cbranch_vccz .Lgw3_%=\n\t" \
    "buffer_load_dwordx4 v[244:247], %[l16], %[rs], %[i0] offen offset:3072 sc1\n\t" \
    "s_mov_b32 %[a], 1\n\t" \
    ".Lgw3_%=:\n\t" \
    "v_max_u32 v252, v248, v249\n\t" \
    "v_max3_u32 v252, v250, v251, v252\n\t" \
    "v_cmp_eq_u32 vcc, -1, v252\n\t" \
    "s_cbranch_vccz .Lgw4_%=\n\t" \
    "buffer_load_dwordx4 v[248:251], %[l16], %[rs], %[i1] offen offset:0 sc1\n\t" \
    "s_mov_b32 %[a], 1\n\t" \
    ".Lgw4_%=:\n\t" \
    "s_cmp_eq_u32 %[a], 0\n\t" \
    "s_cbranch_scc1 .Lhw%=\n\t" \
    "s_waitcnt vmcnt(20)\n\t" \
    ".Liw%=:\n\t" \
    "v_max_u32 v252, v232, v233\n\t" \
    "v_max3_u32 v252, v234, v235, v252\n\t" \
    "v_max3_u32 v252, v236, v237, v252\n\t" \
    "v_max3_u32 v252, v238, v239, v252\n\t" \
    "v_max3_u32 v252, v240, v241, v252\n\t" \
    "v_max3_u32 v252, v242, v243, v252\n\t" \
    "v_max3_u32 v252, v244, v245, v252\n\t" \
    "v_max3_u32 v252, v246, v247, v252\n\t" \
    "v_max3_u32 v252, v248, v249, v252\n\t" \
    "v_max3_u32 v252, v250, v251, v252\n\t" \
    "v_cmp_eq_u32 vcc, -1, v252\n\t" \
    "s_cbranch_vccz .Lhw%=\n\t" \
    "s_sleep 1\n\t" \
    "v_max_u32 v252, v232, v233\n\t" \
    "v_max3_u32 v252, v234, v235, v252\n\t" \
    "v_max3_u32 v252, v236, v237, v252\n\t" \
    "v_max3_u32 v252, v238, v239, v252\n\t" \
    "v_max3_u32 v252, v240, v241, v252\n\t" \
    "v_max3_u32 v252, v242, v243, v252\n\t" \
    "v_max3_u32 v252, v244, v245, v252\n\t" \
    "v_max3_u32 v252, v246, v247, v252\n\t" \
    "v_max3_u32 v252, v248, v249, v252\n\t" \
    "v_max3_u32 v252, v250, v251, v252\n\t" \
    "v_cmp_eq_u32 vcc, -1, v252\n\t" \
    "s_cbranch_vccz .Lhw%=\n\t" \
    "s_sleep 1\n\t" \
    "v_max_u32 v252, v232, v233\n\t" \
    "v_max3_u32 v252, v234, v235, v252\n\t" \
    "v_max3_u32 v252, v236, v237, v252\n\t" \
    "v_max3_u32 v252, v238, v239, v252\n\t" \
    "v_max3_u32 v252, v240, v241, v252\n\t" \
    "v_max3_u32 v252, v242, v243, v252\n\t" \
    "v_max3_u32 v252, v244, v245, v252\n\t" \
    "v_max3_u32 v252, v246, v247, v252\n\t" \
    "v_max3_u32 v252, v248, v249, v252\n\t" \
    "v_max3_u32 v252, v250, v251, v252\n\t" \
    "v_cmp_eq_u32 vcc, -1, v252\n\t" \
    "s_cbranch_vccz .Lhw%=\n\t" \
    "s_sleep 1\n\t" \
    "v_max_u32 v252, v232, v233\n\t" \
    "v_max3_u32 v252, v234, v235, v252\n\t" \
    "v_max3_u32 v252, v236, v237, v252\n\t" \
    "v_max3_u32 v252, v238, v239, v252\n\t" \
    "v_max3_u32 v252, v240, v241, v252\n\t" \
    "v_max3_u32 v252, v242, v243, v252\n\t" \
    "v_max3_u32 v252, v244, v245, v252\n\t" \
    "v_max3_u32 v252, v246, v247, v252\n\t" \
    "v_max3_u32 v252, v248, v249, v252\n\t" \
    "v_max3_u32 v252, v250, v251, v252\n\t" \
    "v_cmp_eq_u32 vcc, -1, v252\n\t" \
    "s_cbranch_vccz .Lhw%=\n\t" \
    "s_sleep 1\n\t" \
    "v_max_u32 v252, v232, v233\n\t" \
    "v_max3_u32 v252, v234, v235, v252\n\t" \
    "v_max3_u32 v252, v236, v237, v252\n\t" \
    "v_max3_u32 v252, v238, v239, v252\n\t" \
    "v_max3_u32 v252, v240, v241, v252\n\t" \
    "v_max3_u32 v252, v242, v243, v252\n\t" \
    "v_max3_u32 v252, v244, v245, v252\n\t" \
    "v_max3_u32 v252, v246, v247, v252\n\t" \
    "v_max3_u32 v252, v248, v249, v252\n\t" \
    "v_max3_u32 v252, v250, v251, v252\n\t" \
    "v_cmp_eq_u32 vcc, -1, v252\n\t" \
    "s_cbranch_vccz .Lhw%=\n\t" \
    "s_sleep 1\n\t" \
    "v_max_u32 v252, v232, v233\n\t" \
    "v_max3_u32 v252, v234, v235, v252\n\t" \
    "v_max3_u32 v252, v236, v237, v252\n\t" \
    "v_max3_u32 v252, v238, v239, v252\n\t" \
    "v_max3_u32 v252, v240, v241, v252\n\t" \
    "v_max3_u32 v252, v242, v243, v252\n\t" \
    "v_max3_u32 v252, v244, v245, v252\n\t" \
    "v_max3_u32 v252, v246, v247, v252\n\t" \
    "v_max3_u32 v252, v248, v249, v252\n\t" \
    "v_max3_u32 v252, v250, v251, v252\n\t" \
    "v_cmp_eq_u32 vcc, -1, v252\n\t" \
    "s_cbranch_vccz .Lhw%=\n\t" \
    "s_sleep 1\n\t" \
    "s_sub_u32 %[n], %[n], 1\n\t" \
    "s_cmp_gt_i32 %[n], 0\n\t" \
    "s_cbranch_scc1 .Lfw%=\n\t" \
    ".Lhw%=:\n\t" \
    "s_branch .Lta%=\n\t" \
    ".Lt0%=:\n\t" \
    "s_waitcnt vmcnt(1)\n\t" \
    ".Lta%=:\n\t" \
    "ds_write_b128 %[lw], v[232:235] offset:0\n\t" \
    "v_max_u32 v252, v232, v233\n\t" \
    "v_max3_u32 v252, v234, v235, v252\n\t" \
    "ds_write_b128 %[lw], v[236:239] offset:1024\n\t" \
    "v_max3_u32 v252, v236, v237, v252\n\t" \
    "v_max3_u32 v252, v238, v239, v252\n\t" \
    "ds_write_b128 %[lw], v[240:243] offset:2048\n\t" \
    "v_max3_u32 v252, v240, v241, v252\n\t" \
    "v_max3_u32 v252, v242, v243, v252\n\t" \
    "ds_write_b128 %[lw], v[244:247] offset:3072\n\t" \
    "v_max3_u32 v252, v244, v245, v252\n\t" \
    "v_max3_u32 v252, v246, v247, v252\n\t" \
    "ds_write_b128 %[lw], v[248:251] offset:4096\n\t" \
    "v_max3_u32 v252, v248, v249, v252\n\t" \
    "v_max3_u32 v252, v250, v251, v252\n\t" \
    "v_cmp_eq_u32 vcc, -1, v252\n\t" \
    "s_cbranch_vccnz .Ltr%=\n\t" \
    ".Ltg%=:\n\t" \
    ASM_ST2 \
    "s_waitcnt lgkmcnt(0)\n\t" \
    "s_setprio 2\n\t" \
    "s_barrier\n\t" ASM_ST \
    "ds_read_b128 v[160:163], %[la] offset:0\n\t" \
    "ds_read_b128 v[164:167], %[la] offset:1024\n\t" \
    "ds_read_b128 v[168:171], %[la] offset:2048\n\t" \
    "ds_read_b128 v[172:175], %[la] offset:3072\n\t" \
    "ds_read_b128 v[176:179], %[la] offset:8192\n\t" \
    "ds_read_b128 v[180:183], %[la] offset:9216\n\t" \
    "s_waitcnt lgkmcnt(5)\n\t" \
    "v_mfma_f32_16x16x32_bf16 v[188:191], v[0:3], v[160:163], 0\n\t" \
    "ds_read_b128 v[184:187], %[la] offset:10240\n\t" \
    "buffer_load_dwordx2 v[196:197], %[ro], %[rs], %[sx] offen sc1\n\t" \
    "v_mfma_f32_16x16x32_bf16 v[192:195], v[80:83], v[160:163], 0\n\t" \
    "buffer_load_dwordx2 v[198:199], %[ro], %[rs], %[sx] offen offset:512 sc1\n\t" \
    "s_waitcnt lgkmcnt(5)\n\t" \
    "v_mfma_f32_16x16x32_bf16 v[188:191], v[4:7], v[164:167], v[188:191]\n\t" \
    "ds_read_b128 v[160:163], %[la] offset:11264\n\t" \
    "v_lshrrev_b32 v204, 1, %[l16]\n\t" \
    "v_mfma_f32_16x16x32_bf16 v[192:195], v[84:87], v[164:167], v[192:195]\n\t" \
    "v_and_b32 v204, 0x78, v204\n\t" \
    "s_waitcnt lgkmcnt(5)\n\t" \
    "v_mfma_f32_16x16x32_bf16 v[188:191], v[8:11], v[168:171], v[188:191]\n\t" \
    "ds_read_b128 v[164:167], %[la] offset:16384\n\t" \
    "buffer_load_dwordx2 v[200:201], v204, %[rs], %[ss] offen sc1\n\t" \
    "v_mfma_f32_16x16x32_bf16 v[192:195], v[88:91], v[168:171], v[192:195]\n\t" \
    "v_mov_b64 v[216:217], -1\n\t" \
    "s_waitcnt lgkmcnt(5)\n\t" \
    "v_mfma_f32_16x16x32_bf16 v[188:191], v[12:15], v[172:175], v[188:191]\n\t" \
    "ds_read_b128 v[168:171], %[la] offset:17408\n\t" \
    "v_mov_b64 v[218:219], -1\n\t" \
    "v_mfma_f32_16x16x32_bf16 v[192:195], v[92:95], v[172:175], v[192:195]\n\t" \
    "buffer_store_dwordx4 v[216:219], %[l16], %[rs], %[sa] offen sc1\n\t" \
    "s_waitcnt lgkmcnt(5)\n\t" \
    "v_mfma_f32_16x16x32_bf16 v[188:191], v[16:19], v[176:179], v[188:191]\n\t" \
    "ds_read_b128 v[172:175], %[la] offset:18432\n\t" \
    "v_mfma_f32_16x16x32_bf16 v[192:195], v[96:99], v[176:179], v[192:195]\n\t" \
    "s_waitcnt lgkmcnt(5)\n\t" \
    "v_mfma_f32_16x16x32_bf16 v[188:191], v[20:23], v[180:183], v[188:191]\n\t" \
    "ds_read_b128 v[176:179], %[la] offset:19456\n\t" \
    "v_mfma_f32_16x16x32_bf16 v[192:195], v[100:103], v[180:183], v[192:195]\n\t" \
    "s_waitcnt lgkmcnt(5)\n\t" \
    "v_mfma_f32_16x16x32_bf16 v[188:191], v[24:27], v[184:187], v[188:191]\n\t" \
    "ds_read_b128 v[180:183], %[la] offset:24576\n\t" \
    "v_mfma_f32_16x16x32_bf16 v[192:195], v[104:107], v[184:187], v[192:195]\n\t" \
    "s_waitcnt lgkmcnt(5)\n\t" \
    "v_mfma_f32_16x16x32_bf16 v[188:191], v[28:31], v[160:163], v[188:191]\n\t" \
    "ds_read_b128 v[184:187], %[la] offset:25600\n\t" \
    "v_mfma_f32_16x16x32_bf16 v[192:195], v[108:111], v[160:163], v[192:195]\n\t" \
    "s_waitcnt lgkmcnt(5)\n\t" \
    "v_mfma_f32_16x16x32_bf16 v[188:191], v[32:35], v[164:167], v[188:191]\n\t" \
    "ds_read_b128 v[160:163], %[la] offset:26624\n\t" \
    "v_mfma_f32_16x16x32_bf16 v[192:195], v[112:115], v[164:167], v[192:195]\n\t" \
    "s_waitcnt lgkmcnt(5)\n\t" \
    "v_mfma_f32_16x16x32_bf16 v[188:191], v[36:39], v[168:171], v[188:191]\n\t" \
    "ds_read_b128 v[164:167], %[la] offset:27648\n\t" \
    "v_mfma_f32_16x16x32_bf16 v[192:195], v[116:119], v[168:171], v[192:195]\n\t" \
    "s_waitcnt lgkmcnt(5)\n\t" \
    "v_mfma_f32_16x16x32_bf16 v[188:191], v[40:43], v[172:175], v[188:191]\n\t" \
    "ds_read_b128 v[168:171], %[la] offset:4096\n\t" \
    "v_mfma_f32_16x16x32_bf16 v[192:195], v[120:123], v[172:175], v[192:195]\n\t" \
    "s_waitcnt lgkmcnt(5)\n\t" \
    "v_mfma_f32_16x16x32_bf16 v[188:191], v[44:47], v[176:179], v[188:191]\n\t" \
    "ds_read_b128 v[172:175], %[la] offset:12288\n\t" \
    "v_mfma_f32_16x16x32_bf16 v[192:195], v[124:127], v[176:179], v[192:195]\n\t" \
    "s_waitcnt lgkmcnt(5)\n\t" \
    "v_mfma_f32_16x16x32_bf16 v[188:191], v[48:51], v[180:183], v[188:191]\n\t" \
    "ds_read_b128 v[176:179], %[la] offset:20480\n\t" \
    "v_mfma_f32_16x16x32_bf16 v[192:195], v[128:131], v[180:183], v[192:195]\n\t" \
    "s_waitcnt lgkmcnt(5)\n\t" \
    "v_mfma_f32_16x16x32_bf16 v[188:191], v[52:55], v[184:187], v[188:191]\n\t" \
    "ds_read_b128 v[180:183], %[la] offset:28672\n\t" \
    "v_mfma_f32_16x16x32_bf16 v[192:195], v[132:135], v[184:187], v[192:195]\n\t" \
    "s_waitcnt lgkmcnt(5)\n\t" \
    "v_mfma_f32_16x16x32_bf16 v[188:191], v[56:59], v[160:163], v[188:191]\n\t" \
    "v_mfma_f32_16x16x32_bf16 v[192:195], v[136:139], v[160:163], v[192:195]\n\t" \
    "s_waitcnt lgkmcnt(4)\n\t" \
    "v_mfma_f32_16x16x32_bf16 v[188:191], v[60:63], v[164:167], v[188:191]\n\t" \
    "v_mfma_f32_16x16x32_bf16 v[192:195], v[140:143], v[164:167], v[192:195]\n\t" \
    "s_waitcnt lgkmcnt(3)\n\t" \
    "v_mfma_f32_16x16x32_bf16 v[188:191], v[64:67], v[168:171], v[188:191]\n\t" \
    "v_mfma_f32_16x16x32_bf16 v[192:195], v[144:147], v[168:171], v[192:195]\n\t" \
    "s_waitcnt lgkmcnt(2)\n\t" \
    "v_mfma_f32_16x16x32_bf16 v[188:191], v[68:71], v[172:175], v[188:191]\n\t" \
    "v_mfma_f32_16x16x32_bf16 v[192:195], v[148:151], v[172:175], v[192:195]\n\t" \
    "s_waitcnt lgkmcnt(1)\n\t" \
    "v_mfma_f32_16x16x32_bf16 v[188:191], v[72:75], v[176:179], v[188:191]\n\t" \
    "v_mfma_f32_16x16x32_bf16 v[192:195], v[152:155], v[176:179], v[192:195]\n\t" \
    "s_waitcnt lgkmcnt(0)\n\t" \
    "v_mfma_f32_16x16x32_bf16 v[188:191], v[76:79], v[180:183], v[188:191]\n\t" \
    "v_mfma_f32_16x16x32_bf16 v[192:195], v[156:159], v[180:183], v[192:195]\n\t" \
    "s_setprio 2\n\t" \
    "buffer_load_dwordx4 v[232:235], %[l16], %[rs], %[n0] offen offset:0 sc1\n\t" \
    "buffer_load_dwordx4 v[236:239], %[l16], %[rs], %[n0] offen offset:1024 sc1\n\t" \
    "buffer_load_dwordx4 v[240:243], %[l16], %[rs], %[n0] offen offset:2048 sc1\n\t" \
    "buffer_load_dwordx4 v[244:247], %[l16], %[rs], %[n0] offen offset:3072 sc1\n\t" \
    "buffer_load_dwordx4 v[248:251], %[l16], %[rs], %[n1] offen offset:0 sc1\n\t" \
    ASM_ST3 \
    "s_waitcnt vmcnt(6)\n\t" \
    "v_lshlrev_b32 v160, 16, v196\n\t" \
    "v_and_b32 v161, 0xffff0000, v196\n\t" \
    "v_lshlrev_b32 v162, 16, v197\n\t" \
    "v_and_b32 v163, 0xffff0000, v197\n\t" \
    "v_lshlrev_b32 v164, 16, v198\n\t" \
    "v_and_b32 v165, 0xffff0000, v198\n\t" \
    "v_lshlrev_b32 v166, 16, v199\n\t" \
    "v_and_b32 v167, 0xffff0000, v199\n\t" \
    "v_sub_f32 v160, v160, v200\n\t" \
    "v_sub_f32 v161, v161, v200\n\t" \
    "v_sub_f32 v162, v162, v200\n\t" \
    "v_sub_f32 v163, v163, v200\n\t" \
    "v_sub_f32 v164, v164, v200\n\t" \
    "v_sub_f32 v165, v165, v200\n\t" \
    "v_sub_f32 v166, v166, v200\n\t" \
    "v_sub_f32 v167, v167, v200\n\t" \
    "v_mul_f32 v160, v160, v201\n\t" \
    "v_mul_f32 v161, v161, v201\n\t" \
    "v_mul_f32 v162, v162, v201\n\t" \
    "v_mul_f32 v163, v163, v201\n\t" \
    "v_mul_f32 v164, v164, v201\n\t" \
    "v_mul_f32 v165, v165, v201\n\t" \
    "v_mul_f32 v166, v166, v201\n\t" \
    "v_mul_f32 v167, v167, v201\n\t" \
    "v_cvt_pk_bf16_f32 v160, v160, v161\n\t" \
    "v_cvt_pk_bf16_f32 v162, v162, v163\n\t" \
    "v_cvt_pk_bf16_f32 v164, v164, v165\n\t" \
    "v_cvt_pk_bf16_f32 v166, v166, v167\n\t" \
    "v_and_b32 v161, 0xffff0000, v160\n\t" \
    "v_lshlrev_b32 v160, 16, v160\n\t" \
    "v_and_b32 v163, 0xffff0000, v162\n\t" \
    "v_lshlrev_b32 v162, 16, v162\n\t" \
    "v_and_b32 v165, 0xffff0000, v164\n\t" \
    "v_lshlrev_b32 v164, 16, v164\n\t" \
    "v_and_b32 v167, 0xffff0000, v166\n\t" \
    "v_lshlrev_b32 v166, 16, v166\n\t" \
    "v_add_f32 v188, v188, v160\n\t" \
    "v_add_f32 v189, v189, v161\n\t" \
    "v_add_f32 v190, v190, v162\n\t" \
    "v_add_f32 v191, v191, v163\n\t" \
    "v_add_f32 v192, v192, v164\n\t" \
    "v_add_f32 v193, v193, v165\n\t" \
    "v_add_f32 v194, v194, v166\n\t" \
    "v_add_f32 v195, v195, v167\n\t" \
    "v_cvt_pk_bf16_f32 v188, v188, v189\n\t" \
    "v_cvt_pk_bf16_f32 v189, v190, v191\n\t" \
    "v_cvt_pk_bf16_f32 v190, v192, v193\n\t" \
    "v_cvt_pk_bf16_f32 v191, v194, v195\n\t" \
    "v_min_u32 v188, -2, v188\n\t" \
    "v_min_u32 v189, -2, v189\n\t" \
    "v_min_u32 v190, -2, v190\n\t" \
    "v_min_u32 v191, -2, v191\n\t" \
    "s_nop 1\n\t" \
    "v_permlane16_swap_b32 v188, v190\n\t" \
    "v_permlane16_swap_b32 v189, v191\n\t" \
    "s_waitcnt vmcnt(6)\n\t" \
    "buffer_store_dwordx4 v[188:191], %[oa], %[rs], %[so] offen sc1\n\t" \
    "s_nop 1\n\t" \
    "s_setprio 0\n\t"
#define ASM_WT2_HI \
    "s_setprio 1\n\t" \
    "s_barrier\n\t" ASM_ST \
    "ds_read_b128 v[160:163], %[la] offset:0\n\t" \
    "ds_read_b128 v[164:167], %[la] offset:1024\n\t" \
    "ds_read_b128 v[168:171], %[la] offset:2048\n\t" \
    "ds_read_b128 v[172:175], %[la] offset:3072\n\t" \
    "ds_read_b128 v[176:179], %[la] offset:8192\n\t" \
    "ds_read_b128 v[180:183], %[la] offset:9216\n\t" \
    "s_waitcnt lgkmcnt(5)\n\t" \
    "v_mfma_f32_16x16x32_bf16 v[188:191], v[0:3], v[160:163], 0\n\t" \
    "ds_read_b128 v[184:187], %[la] offset:10240\n\t" \
    "buffer_load_dwordx2 v[196:197], %[ro], %[rs], %[sx] offen sc1\n\t" \
    "v_mfma_f32_16x16x32_bf16 v[192:195], v[80:83], v[160:163], 0\n\t" \
    "buffer_load_dwordx2 v[198:199], %[ro], %[rs], %[sx] offen offset:512 sc1\n\t" \
    "s_waitcnt lgkmcnt(5)\n\t" \
    "v_mfma_f32_16x16x32_bf16 v[188:191], v[4:7], v[164:167], v[188:191]\n\t" \
    "ds_read_b128 v[160:163], %[la] offset:11264\n\t" \
    "v_lshrrev_b32 v204, 1, %[l16]\n\t" \
    "v_mfma_f32_16x16x32_bf16 v[192:195], v[84:87], v[164:167], v[192:195]\n\t" \
    "v_and_b32 v204, 0x78, v204\n\t" \
    "s_waitcnt lgkmcnt(5)\n\t" \
    "v_mfma_f32_16x16x32_bf16 v[188:191], v[8:11], v[168:171], v[188:191]\n\t" \
    "ds_read_b128 v[164:167], %[la] offset:16384\n\t" \
    "buffer_load_dwordx2 v[200:201], v204, %[rs], %[ss] offen sc1\n\t" \
    "v_mfma_f32_16x16x32_bf16 v[192:195], v[88:91], v[168:171], v[192:195]\n\t" \
    "v_mov_b64 v[216:217], -1\n\t" \
    "s_waitcnt lgkmcnt(5)\n\t" \
    "v_mfma_f32_16x16x32_bf16 v[188:191], v[12:15], v[172:175], v[188:191]\n\t" \
    "ds_read_b128 v[168:171], %[la] offset:17408\n\t" \
    "v_mov_b64 v[218:219], -1\n\t" \
    "v_mfma_f32_16x16x32_bf16 v[192:195], v[92:95], v[172:175], v[192:195]\n\t" \
    "buffer_store_dwordx4 v[216:219], %[l16], %[rs], %[sa] offen sc1\n\t" \
    "s_waitcnt lgkmcnt(5)\n\t" \
    "v_mfma_f32_16x16x32_bf16 v[188:191], v[16:19], v[176:179], v[188:191]\n\t" \
    "ds_read_b128 v[172:175], %[la] offset:18432\n\t" \
    "v_mfma_f32_16x16x32_bf16 v[192:195], v[96:99], v[176:179], v[192:195]\n\t" \
    "s_waitcnt lgkmcnt(5)\n\t" \
    "v_mfma_f32_16x16x32_bf16 v[188:191], v[20:23], v[180:183], v[188:191]\n\t" \
    "ds_read_b128 v[176:179], %[la] offset:19456\n\t" \
    "v_mfma_f32_16x16x32_bf16 v[192:195], v[100:103], v[180:183], v[192:195]\n\t" \
    "s_waitcnt lgkmcnt(5)\n\t" \
    "v_mfma_f32_16x16x32_bf16 v[188:191], v[24:27], v[184:187], v[188:191]\n\t" \
    "ds_read_b128 v[180:183], %[la] offset:24576\n\t" \
    "v_mfma_f32_16x16x32_bf16 v[192:195], v[104:107], v[184:187], v[192:195]\n\t" \
    "s_waitcnt lgkmcnt(5)\n\t" \
    "v_mfma_f32_16x16x32_bf16 v[188:191], v[28:31], v[160:163], v[188:191]\n\t" \
    "ds_read_b128 v[184:187], %[la] offset:25600\n\t" \
    "v_mfma_f32_16x16x32_bf16 v[192:195], v[108:111], v[160:163], v[192:195]\n\t" \
    "s_waitcnt lgkmcnt(5)\n\t" \
    "v_mfma_f32_16x16x32_bf16 v[188:191], v[32:35], v[164:167], v[188:191]\n\t" \
    "ds_read_b128 v[160:163], %[la] offset:26624\n\t" \
    "v_mfma_f32_16x16x32_bf16 v[192:195], v[112:115], v[164:167], v[192:195]\n\t" \
    "s_waitcnt lgkmcnt(5)\n\t" \
    "v_mfma_f32_16x16x32_bf16 v[188:191], v[36:39], v[168:171], v[188:191]\n\t" \
    "ds_read_b128 v[164:167], %[la] offset:27648\n\t" \
    "v_mfma_f32_16x16x32_bf16 v[192:195], v[116:119], v[168:171], v[192:195]\n\t" \
    "s_waitcnt lgkmcnt(5)\n\t" \
    "v_mfma_f32_16x16x32_bf16 v[188:191], v[40:43], v[172:175], v[188:191]\n\t" \
    "ds_read_b128 v[168:171], %[la] offset:4096\n\t" \
    "v_mfma_f32_16x16x32_bf16 v[192:195], v[120:123], v[172:175], v[192:195]\n\t" \
    "s_waitcnt lgkmcnt(5)\n\t" \
    "v_mfma_f32_16x16x32_bf16 v[188:191], v[44:47], v[176:179], v[188:191]\n\t" \
    "ds_read_b128 v[172:175], %[la] offset:12288\n\t" \
    "v_mfma_f32_16x16x32_bf16 v[192:195], v[124:127], v[176:179], v[192:195]\n\t" \
    "s_waitcnt lgkmcnt(5)\n\t" \
    "v_mfma_f32_16x16x32_bf16 v[188:191], v[48:51], v[180:183], v[188:191]\n\t" \
    "ds_read_b128 v[176:179], %[la] offset:20480\n\t" \
    "v_mfma_f32_16x16x32_bf16 v[192:195], v[128:131], v[180:183], v[192:195]\n\t" \
    "s_waitcnt lgkmcnt(5)\n\t" \
    "v_mfma_f32_16x16x32_bf16 v[188:191], v[52:55], v[184:187], v[188:191]\n\t" \
    "ds_read_b128 v[180:183], %[la] offset:28672\n\t" \
    "v_mfma_f32_16x16x32_bf16 v[192:195], v[132:135], v[184:187], v[192:195]\n\t" \
    "s_waitcnt lgkmcnt(5)\n\t" \
    "v_mfma_f32_16x16x32_bf16 v[188:191], v[56:59], v[160:163], v[188:191]\n\t" \
    "v_mfma_f32_16x16x32_bf16 v[192:195], v[136:139], v[160:163], v[192:195]\n\t" \
    "s_waitcnt lgkmcnt(4)\n\t" \
    "v_mfma_f32_16x16x32_bf16 v[188:191], v[60:63], v[164:167], v[188:191]\n\t" \
    "v_mfma_f32_16x16x32_bf16 v[192:195], v[140:143], v[164:167], v[192:195]\n\t" \
    "s_waitcnt lgkmcnt(3)\n\t" \
    "v_mfma_f32_16x16x32_bf16 v[188:191], v[64:67], v[168:171], v[188:191]\n\t" \
    "v_mfma_f32_16x16x32_bf16 v[192:195], v[144:147], v[168:171], v[192:195]\n\t" \
    "s_waitcnt lgkmcnt(2)\n\t" \
    "v_mfma_f32_16x16x32_bf16 v[188:191], v[68:71], v[172:175], v[188:191]\n\t" \
    "v_mfma_f32_16x16x32_bf16 v[192:195], v[148:151], v[172:175], v[192:195]\n\t" \
    "s_waitcnt lgkmcnt(1)\n\t" \
    "v_mfma_f32_16x16x32_bf16 v[188:191], v[72:75], v[176:179], v[188:191]\n\t" \
    "v_mfma_f32_16x16x32_bf16 v[192:195], v[152:155], v[176:179], v[192:195]\n\t" \
    "s_waitcnt lgkmcnt(0)\n\t" \
    "v_mfma_f32_16x16x32_bf16 v[188:191], v[76:79], v[180:183], v[188:191]\n\t" \
    "v_mfma_f32_16x16x32_bf16 v[192:195], v[156:159], v[180:183], v[192:195]\n\t" \
    ASM_ST3 \
    "s_waitcnt vmcnt(1)\n\t" \
    "v_lshlrev_b32 v160, 16, v196\n\t" \
    "v_and_b32 v161, 0xffff0000, v196\n\t" \
    "v_lshlrev_b32 v162, 16, v197\n\t" \
    "v_and_b32 v163, 0xffff0000, v197\n\t" \
    "v_lshlrev_b32 v164, 16, v198\n\t" \
    "v_and_b32 v165, 0xffff0000, v198\n\t" \
    "v_lshlrev_b32 v166, 16, v199\n\t" \
    "v_and_b32 v167, 0xffff0000, v199\n\t" \
    "v_sub_f32 v160, v160, v200\n\t" \
    "v_sub_f32 v161, v161, v200\n\t" \
    "v_sub_f32 v162, v162, v200\n\t" \
    "v_sub_f32 v163, v163, v200\n\t" \
    "v_sub_f32 v164, v164, v200\n\t" \
    "v_sub_f32 v165, v165, v200\n\t" \
    "v_sub_f32 v166, v166, v200\n\t" \
    "v_sub_f32 v167, v167, v200\n\t" \
    "v_mul_f32 v160, v160, v201\n\t" \
    "v_mul_f32 v161, v161, v201\n\t" \
    "v_mul_f32 v162, v162, v201\n\t" \
    "v_mul_f32 v163, v163, v201\n\t" \
    "v_mul_f32 v164, v164, v201\n\t" \
    "v_mul_f32 v165, v165, v201\n\t" \
    "v_mul_f32 v166, v166, v201\n\t" \
    "v_mul_f32 v167, v167, v201\n\t" \
    "v_cvt_pk_bf16_f32 v160, v160, v161\n\t" \
    "v_cvt_pk_bf16_f32 v162, v162, v163\n\t" \
    "v_cvt_pk_bf16_f32 v164, v164, v165\n\t" \
    "v_cvt_pk_bf16_f32 v166, v166, v167\n\t" \
    "v_and_b32 v161, 0xffff0000, v160\n\t" \
    "v_lshlrev_b32 v160, 16, v160\n\t" \
    "v_and_b32 v163, 0xffff0000, v162\n\t" \
    "v_lshlrev_b32 v162, 16, v162\n\t" \
    "v_and_b32 v165, 0xffff0000, v164\n\t" \
    "v_lshlrev_b32 v164, 16, v164\n\t" \
    "v_and_b32 v167, 0xffff0000, v166\n\t" \
    "v_lshlrev_b32 v166, 16, v166\n\t" \
    "v_add_f32 v188, v188, v160\n\t" \
    "v_add_f32 v189, v189, v161\n\t" \
    "v_add_f32 v190, v190, v162\n\t" \
    "v_add_f32 v191, v191, v163\n\t" \
    "v_add_f32 v192, v192, v164\n\t" \
    "v_add_f32 v193, v193, v165\n\t" \
    "v_add_f32 v194, v194, v166\n\t" \
    "v_add_f32 v195, v195, v167\n\t" \
    "v_cvt_pk_bf16_f32 v188, v188, v189\n\t" \
    "v_cvt_pk_bf16_f32 v189, v190, v191\n\t" \
    "v_cvt_pk_bf16_f32 v190, v192, v193\n\t" \
    "v_cvt_pk_bf16_f32 v191, v194, v195\n\t" \
    "v_min_u32 v188, -2, v188\n\t" \
    "v_min_u32 v189, -2, v189\n\t" \
    "v_min_u32 v190, -2, v190\n\t" \
    "v_min_u32 v191, -2, v191\n\t" \
    "s_nop 1\n\t" \
    "v_permlane16_swap_b32 v188, v190\n\t" \
    "v_permlane16_swap_b32 v189, v191\n\t" \
    "s_waitcnt vmcnt(1)\n\t" \
    "buffer_store_dwordx4 v[188:191], %[oa], %[rs], %[so] offen sc1\n\t" \
    "s_nop 1\n\t"
#define ASM_WT3_LO \
    "s_branch .Lt0%=\n\t" \
    ".Ltr%=:\n\t" \
    "s_cmp_gt_i32 %[n], 0\n\t" \
    "s_cbranch_scc0 .Ltg%=\n\t" \
    ".Lfw%=:\n\t" \
    "s_mov_b32 %[a], 0\n\t" \
    "v_max_u32 v252, v232, v233\n\t" \
    "v_max3_u32 v252, v234, v235, v252\n\t" \
    "v_cmp_eq_u32 vcc, -1, v252\n\t" \
    "s_cbranch_vccz .Lgw0_%=\n\t" \
    "buffer_load_dwordx4 v[232:235], %[l16], %[rs], %[i0] offen offset:0 sc1\n\t" \
    "s_mov_b32 %[a], 1\n\t" \
    ".Lgw0_%=:\n\t" \
    "v_max_u32 v252, v236, v237\n\t" \
    "v_max3_u32 v252, v238, v239, v252\n\t" \
    "v_cmp_eq_u32 vcc, -1, v252\n\t" \
    "s_cbranch_vccz .Lgw1_%=\n\t" \
    "buffer_load_dwordx4 v[236:239], %[l16], %[rs], %[i0] offen offset:1024 sc1\n\t" \
    "s_mov_b32 %[a], 1\n\t" \
    ".Lgw1_%=:\n\t" \
    "v_max_u32 v252, v240, v241\n\t" \
    "v_max3_u32 v252, v242, v243, v252\n\t" \
    "v_cmp_eq_u32 vcc, -1, v252\n\t" \
    "s_cbranch_vccz .Lgw2_%=\n\t" \
    "buffer_load_dwordx4 v[240:243], %[l16], %[rs], %[i0] offen offset:2048 sc1\n\t" \
    "s_mov_b32 %[a], 1\n\t" \
    ".Lgw2_%=:\n\t" \
    "v_max_u32 v252, v244, v245\n\t" \
    "v_max3_u32 v252, v246, v247, v252\n\t" \
    "v_cmp_eq_u32 vcc, -1, v252\n\t" \
    "s_cbranch_vccz .Lgw3_%=\n\t" \
    "buffer_load_dwordx4 v[244:247], %[l16], %[rs], %[i0] offen offset:3072 sc1\n\t" \
    "s_mov_b32 %[a], 1\n\t" \
    ".Lgw3_%=:\n\t" \
    "v_max_u32 v252, v248, v249\n\t" \
    "v_max3_u32 v252, v250, v251, v252\n\t" \
    "v_cmp_eq_u32 vcc, -1, v252\n\t" \
    "s_cbranch_vccz .Lgw4_%=\n\t" \
    "buffer_load_dwordx4 v[248:251], %[l16], %[rs], %[i1] offen offset:0 sc1\n\t" \
    "s_mov_b32 %[a], 1\n\t" \
    ".Lgw4_%=:\n\t" \
    "s_cmp_eq_u32 %[a], 0\n\t" \
    "s_cbranch_scc1 .Lhw%=\n\t" \
    "s_waitcnt vmcnt(20)\n\t" \
    ".Liw%=:\n\t" \
    "v_max_u32 v252, v232, v233\n\t" \
    "v_max3_u32 v252, v234, v235, v252\n\t" \
    "v_max3_u32 v252, v236, v237, v252\n\t" \
    "v_max3_u32 v252, v238, v239, v252\n\t" \
    "v_max3_u32 v252, v240, v241, v252\n\t" \
    "v_max3_u32 v252, v242, v243, v252\n\t" \
    "v_max3_u32 v252, v244, v245, v252\n\t" \
    "v_max3_u32 v252, v246, v247, v252\n\t" \
    "v_max3_u32 v252, v248, v249, v252\n\t" \
    "v_max3_u32 v252, v250, v251, v252\n\t" \
    "v_cmp_eq_u32 vcc, -1, v252\n\t" \
    "s_cbranch_vccz .Lhw%=\n\t" \
    "s_sleep 1\n\t" \
    "v_max_u32 v252, v232, v233\n\t" \
    "v_max3_u32 v252, v234, v235, v252\n\t" \
    "v_max3_u32 v252, v236, v237, v252\n\t" \
    "v_max3_u32 v252, v238, v239, v252\n\t" \
    "v_max3_u32 v252, v240, v241, v252\n\t" \
    "v_max3_u32 v252, v242, v243, v252\n\t" \
    "v_max3_u32 v252, v244, v245, v252\n\t" \
    "v_max3_u32 v252, v246, v247, v252\n\t" \
    "v_max3_u32 v252, v248, v249, v252\n\t" \
    "v_max3_u32 v252, v250, v251, v252\n\t" \
    "v_cmp_eq_u32 vcc, -1, v252\n\t" \
    "s_cbranch_vccz .Lhw%=\n\t" \
    "s_sleep 1\n\t" \
    "v_max_u32 v252, v232, v233\n\t" \
    "v_max3_u32 v252, v234, v235, v252\n\t" \
    "v_max3_u32 v252, v236, v237, v252\n\t" \
    "v_max3_u32 v252, v238, v239, v252\n\t" \
    "v_max3_u32 v252, v240, v241, v252\n\t" \
    "v_max3_u32 v252, v242, v243, v252\n\t" \
    "v_max3_u32 v252, v244, v245, v252\n\t" \
    "v_max3_u32 v252, v246, v247, v252\n\t" \
    "v_max3_u32 v252, v248, v249, v252\n\t" \
    "v_max3_u32 v252, v250, v251, v252\n\t" \
    "v_cmp_eq_u32 vcc, -1, v252\n\t" \
    "s_cbranch_vccz .Lhw%=\n\t" \
    "s_sleep 1\n\t" \
    "v_max_u32 v252, v232, v233\n\t" \
    "v_max3_u32 v252, v234, v235, v252\n\t" \
    "v_max3_u32 v252, v236, v237, v252\n\t" \
    "v_max3_u32 v252, v238, v239, v252\n\t" \
    "v_max3_u32 v252, v240, v241, v252\n\t" \
    "v_max3_u32 v252, v242, v243, v252\n\t" \
    "v_max3_u32 v252, v244, v245, v252\n\t" \
    "v_max3_u32 v252, v246, v247, v252\n\t" \
    "v_max3_u32 v252, v248, v249, v252\n\t" \
    "v_max3_u32 v252, v250, v251, v252\n\t" \
    "v_cmp_eq_u32 vcc, -1, v252\n\t" \
    "s_cbranch_vccz .Lhw%=\n\t" \
    "s_sleep 1\n\t" \
    "v_max_u32 v252, v232, v233\n\t" \
    "v_max3_u32 v252, v234, v235, v252\n\t" \
    "v_max3_u32 v252, v236, v237, v252\n\t" \
    "v_max3_u32 v252, v238, v239, v252\n\t" \
    "v_max3_u32 v252, v240, v241, v252\n\t" \
    "v_max3_u32 v252, v242, v243, v252\n\t" \
    "v_max3_u32 v252, v244, v245, v252\n\t" \
    "v_max3_u32 v252, v246, v247, v252\n\t" \
    "v_max3_u32 v252, v248, v249, v252\n\t" \
    "v_max3_u32 v252, v250, v251, v252\n\t" \
    "v_cmp_eq_u32 vcc, -1, v252\n\t" \
    "s_cbranch_vccz .Lhw%=\n\t" \
    "s_sleep 1\n\t" \
    "v_max_u32 v252, v232, v233\n\t" \
    "v_max3_u32 v252, v234, v235, v252\n\t" \
    "v_max3_u32 v252, v236, v237, v252\n\t" \
    "v_max3_u32 v252, v238, v239, v252\n\t" \
    "v_max3_u32 v252, v240, v241, v252\n\t" \
    "v_max3_u32 v252, v242, v243, v252\n\t" \
    "v_max3_u32 v252, v244, v245, v252\n\t" \
    "v_max3_u32 v252, v246, v247, v252\n\t" \
    "v_max3_u32 v252, v248, v249, v252\n\t" \
    "v_max3_u32 v252, v250, v251, v252\n\t" \
    "v_cmp_eq_u32 vcc, -1, v252\n\t" \
    "s_cbranch_vccz .Lhw%=\n\t" \
    "s_sleep 1\n\t" \
    "s_sub_u32 %[n], %[n], 1\n\t" \
    "s_cmp_gt_i32 %[n], 0\n\t" \
    "s_cbranch_scc1 .Lfw%=\n\t" \
    ".Lhw%=:\n\t" \
    "s_branch .Lta%=\n\t" \
    ".Lt0%=:\n\t" \
    "s_waitcnt vmcnt(1)\n\t" \
    ".Lta%=:\n\t" \
    "ds_write_b128 %[lw], v[232:235] offset:0\n\t" \
    "v_max_u32 v252, v232, v233\n\t" \
    "v_max3_u32 v252, v234, v235, v252\n\t" \
    "v_mfma_f32_16x16x32_bf16 v[188:191], v[232:235], v[232:235], 0\n\t" \
    "v_mfma_f32_16x16x32_bf16 v[192:195], v[216:219], v[232:235], 0\n\t" \
    "ds_write_b128 %[lw], v[236:239] offset:1024\n\t" \
    "v_max3_u32 v252, v236, v237, v252\n\t" \
    "v_max3_u32 v252, v238, v239, v252\n\t" \
    "v_mfma_f32_16x16x32_bf16 v[188:191], v[236:239], v[236:239], v[188:191]\n\t" \
    "v_mfma_f32_16x16x32_bf16 v[192:195], v[216:219], v[236:239], v[192:195]\n\t" \
    "ds_write_b128 %[lw], v[240:243] offset:2048\n\t" \
    "v_max3_u32 v252, v240, v241, v252\n\t" \
    "v_max3_u32 v252, v242, v243, v252\n\t" \
    "v_mfma_f32_16x16x32_bf16 v[188:191], v[240:243], v[240:243], v[188:191]\n\t" \
    "v_mfma_f32_16x16x32_bf16 v[192:195], v[216:219], v[240:243], v[192:195]\n\t" \
    "ds_write_b128 %[lw], v[244:247] offset:3072\n\t" \
    "v_max3_u32 v252, v244, v245, v252\n\t" \
    "v_max3_u32 v252, v246, v247, v252\n\t" \
    "v_mfma_f32_16x16x32_bf16 v[188:191], v[244:247], v[244:247], v[188:191]\n\t" \
    "v_mfma_f32_16x16x32_bf16 v[192:195], v[216:219], v[244:247], v[192:195]\n\t" \
    "ds_write_b128 %[lw], v[248:251] offset:4096\n\t" \
    "v_max3_u32 v252, v248, v249, v252\n\t" \
    "v_max3_u32 v252, v250, v251, v252\n\t" \
    "v_mfma_f32_16x16x32_bf16 v[188:191], v[248:251], v[248:251], v[188:191]\n\t" \
    "v_mfma_f32_16x16x32_bf16 v[192:195], v[216:219], v[248:251], v[192:195]\n\t" \
    "v_cmp_eq_u32 vcc, -1, v252\n\t" \
    "s_cbranch_vccnz .Ltr%=\n\t" \
    ".Ltg%=:\n\t" \
    ASM_ST2 \
    "s_nop 5\n\t" \
    "ds_write_b128 %[lw], v[188:191] offset:5120\n\t" \
    "ds_write_b128 %[lw], v[192:195] offset:6144\n\t" \
    "s_waitcnt lgkmcnt(0)\n\t" \
    "s_setprio 2\n\t" \
    "s_barrier\n\t" ASM_ST \
    "ds_read_b128 v[160:163], %[la] offset:0\n\t" \
    "ds_read_b128 v[164:167], %[la] offset:1024\n\t" \
    "ds_read_b128 v[168:171], %[la] offset:2048\n\t" \
    "ds_read2st64_b32 v[204:205], %[sr] offset0:20 offset1:24\n\t" \
    "ds_read_b128 v[172:175], %[la] offset:3072\n\t" \
    "ds_read_b128 v[176:179], %[la] offset:8192\n\t" \
    "ds_read_b128 v[180:183], %[la] offset:9216\n\t" \
    "s_waitcnt lgkmcnt(6)\n\t" \
    "v_mfma_f32_16x16x32_bf16 v[188:191], v[0:3], v[160:163], 0\n\t" \
    "ds_read_b128 v[184:187], %[la] offset:10240\n\t" \
    "v_mfma_f32_16x16x32_bf16 v[192:195], v[80:83], v[160:163], 0\n\t" \
    "s_waitcnt lgkmcnt(6)\n\t" \
    "v_mfma_f32_16x16x32_bf16 v[188:191], v[4:7], v[164:167], v[188:191]\n\t" \
    "ds_read_b128 v[160:163], %[la] offset:11264\n\t" \
    "v_mfma_f32_16x16x32_bf16 v[192:195], v[84:87], v[164:167], v[192:195]\n\t" \
    "s_waitcnt lgkmcnt(6)\n\t" \
    "v_mfma_f32_16x16x32_bf16 v[188:191], v[8:11], v[168:171], v[188:191]\n\t" \
    "ds_read_b128 v[164:167], %[la] offset:16384\n\t" \
    "s_waitcnt lgkmcnt(6)\n\t" \
    "v_mfma_f32_16x16x32_bf16 v[192:195], v[88:91], v[168:171], v[192:195]\n\t" \
    "v_permlane16_swap_b32 v204, v205\n\t" \
    "s_waitcnt lgkmcnt(5)\n\t" \
    "v_mfma_f32_16x16x32_bf16 v[188:191], v[12:15], v[172:175], v[188:191]\n\t" \
    "ds_read_b128 v[168:171], %[la] offset:17408\n\t" \
    "v_mfma_f32_16x16x32_bf16 v[192:195], v[92:95], v[172:175], v[192:195]\n\t" \
    "v_add_f32 v206, v204, v205\n\t" \
    "s_waitcnt lgkmcnt(5)\n\t" \
    "v_mfma_f32_16x16x32_bf16 v[188:191], v[16:19], v[176:179], v[188:191]\n\t" \
    "ds_read_b128 v[172:175], %[la] offset:18432\n\t" \
    "v_add_f32 v207, v204, v205\n\t" \
    "v_mfma_f32_16x16x32_bf16 v[192:195], v[96:99], v[176:179], v[192:195]\n\t" \
    "s_waitcnt lgkmcnt(5)\n\t" \
    "v_mfma_f32_16x16x32_bf16 v[188:191], v[20:23], v[180:183], v[188:191]\n\t" \
    "ds_read_b128 v[176:179], %[la] offset:19456\n\t" \
    "v_permlane32_swap_b32 v206, v207\n\t" \
    "v_mfma_f32_16x16x32_bf16 v[192:195], v[100:103], v[180:183], v[192:195]\n\t" \
    "s_waitcnt lgkmcnt(5)\n\t" \
    "v_mfma_f32_16x16x32_bf16 v[188:191], v[24:27], v[184:187], v[188:191]\n\t" \
    "ds_read_b128 v[180:183], %[la] offset:24576\n\t" \
    "v_add_f32 v204, v206, v207\n\t" \
    "v_mfma_f32_16x16x32_bf16 v[192:195], v[104:107], v[184:187], v[192:195]\n\t" \
    "v_add_f32 v205, v206, v207\n\t" \
    "s_waitcnt lgkmcnt(5)\n\t" \
    "v_mfma_f32_16x16x32_bf16 v[188:191], v[28:31], v[160:163], v[188:191]\n\t" \
    "ds_read_b128 v[184:187], %[la] offset:25600\n\t" \
    "v_mfma_f32_16x16x32_bf16 v[192:195], v[108:111], v[160:163], v[192:195]\n\t" \
    "v_permlane16_swap_b32 v204, v205\n\t" \
    "s_waitcnt lgkmcnt(5)\n\t" \
    "v_mfma_f32_16x16x32_bf16 v[188:191], v[32:35], v[164:167], v[188:191]\n\t" \
    "ds_read_b128 v[160:163], %[la] offset:26624\n\t" \
    "v_mfma_f32_16x16x32_bf16 v[192:195], v[112:115], v[164:167], v[192:195]\n\t" \
    "v_mul_f32 v205, %[kd], v205\n\t" \
    "s_waitcnt lgkmcnt(5)\n\t" \
    "v_mfma_f32_16x16x32_bf16 v[188:191], v[36:39], v[168:171], v[188:191]\n\t" \
    "ds_read_b128 v[164:167], %[la] offset:27648\n\t" \
    "v_mul_f32 v204, %[kd], v204\n\t" \
    "v_mfma_f32_16x16x32_bf16 v[192:195], v[116:119], v[168:171], v[192:195]\n\t" \
    "v_fma_f32 v204, -v205, v205, v204\n\t" \
    "s_waitcnt lgkmcnt(5)\n\t" \
    "v_mfma_f32_16x16x32_bf16 v[188:191], v[40:43], v[172:175], v[188:191]\n\t" \
    "ds_read_b128 v[168:171], %[la] offset:4096\n\t" \
    "v_max_f32 v204, 0, v204\n\t" \
    "v_mfma_f32_16x16x32_bf16 v[192:195], v[120:123], v[172:175], v[192:195]\n\t" \
    "v_add_f32 v206, %[ke], v204\n\t" \
    "s_waitcnt lgkmcnt(5)\n\t" \
    "v_mfma_f32_16x16x32_bf16 v[188:191], v[44:47], v[176:179], v[188:191]\n\t" \
    "ds_read_b128 v[172:175], %[la] offset:12288\n\t" \
    "v_rsq_f32 v206, v206\n\t" \
    "v_mfma_f32_16x16x32_bf16 v[192:195], v[124:127], v[176:179], v[192:195]\n\t" \
    "s_waitcnt lgkmcnt(5)\n\t" \
    "v_mfma_f32_16x16x32_bf16 v[188:191], v[48:51], v[180:183], v[188:191]\n\t" \
    "ds_read_b128 v[176:179], %[la] offset:20480\n\t" \
    "v_mul_f32 v207, -v205, v206\n\t" \
    "v_mfma_f32_16x16x32_bf16 v[192:195], v[128:131], v[180:183], v[192:195]\n\t" \
    "v_mul_f32 v208, v196, v207\n\t" \
    "s_waitcnt lgkmcnt(5)\n\t" \
    "v_mfma_f32_16x16x32_bf16 v[188:191], v[52:55], v[184:187], v[188:191]\n\t" \
    "ds_read_b128 v[180:183], %[la] offset:28672\n\t" \
    "v_mul_f32 v209, v197, v207\n\t" \
    "v_mul_f32 v210, v198, v207\n\t" \
    "v_mfma_f32_16x16x32_bf16 v[192:195], v[132:135], v[184:187], v[192:195]\n\t" \
    "v_mul_f32 v211, v199, v207\n\t" \
    "v_mul_f32 v212, v200, v207\n\t" \
    "s_waitcnt lgkmcnt(5)\n\t" \
    "v_mfma_f32_16x16x32_bf16 v[188:191], v[56:59], v[160:163], v[188:191]\n\t" \
    "v_mul_f32 v213, v201, v207\n\t" \
    "v_mul_f32 v214, v202, v207\n\t" \
    "v_mfma_f32_16x16x32_bf16 v[192:195], v[136:139], v[160:163], v[192:195]\n\t" \
    "v_mul_f32 v215, v203, v207\n\t" \
    "v_mov_b64 v[216:217], -1\n\t" \
    "s_waitcnt lgkmcnt(4)\n\t" \
    "v_mfma_f32_16x16x32_bf16 v[188:191], v[60:63], v[164:167], v[188:191]\n\t" \
    "v_mov_b64 v[218:219], -1\n\t" \
    "buffer_store_dwordx4 v[216:219], %[l16], %[rs], %[sa] offen sc1\n\t" \
    "v_mfma_f32_16x16x32_bf16 v[192:195], v[140:143], v[164:167], v[192:195]\n\t" \
    "s_waitcnt lgkmcnt(3)\n\t" \
    "v_mfma_f32_16x16x32_bf16 v[188:191], v[64:67], v[168:171], v[188:191]\n\t" \
    "v_mfma_f32_16x16x32_bf16 v[192:195], v[144:147], v[168:171], v[192:195]\n\t" \
    "s_waitcnt lgkmcnt(2)\n\t" \
    "v_mfma_f32_16x16x32_bf16 v[188:191], v[68:71], v[172:175], v[188:191]\n\t" \
    "v_mfma_f32_16x16x32_bf16 v[192:195], v[148:151], v[172:175], v[192:195]\n\t" \
    "s_waitcnt lgkmcnt(1)\n\t" \
    "v_mfma_f32_16x16x32_bf16 v[188:191], v[72:75], v[176:179], v[188:191]\n\t" \
    "v_mfma_f32_16x16x32_bf16 v[192:195], v[152:155], v[176:179], v[192:195]\n\t" \
    "s_waitcnt lgkmcnt(0)\n\t" \
    "v_mfma_f32_16x16x32_bf16 v[188:191], v[76:79], v[180:183], v[188:191]\n\t" \
    "v_mfma_f32_16x16x32_bf16 v[192:195], v[156:159], v[180:183], v[192:195]\n\t" \
    "s_setprio 2\n\t" \
    "buffer_load_dwordx4 v[232:235], %[l16], %[rs], %[n0] offen offset:0 sc1\n\t" \
    "buffer_load_dwordx4 v[236:239], %[l16], %[rs], %[n0] offen offset:1024 sc1\n\t" \
    "buffer_load_dwordx4 v[240:243], %[l16], %[rs], %[n0] offen offset:2048 sc1\n\t" \
    "buffer_load_dwordx4 v[244:247], %[l16], %[rs], %[n0] offen offset:3072 sc1\n\t" \
    "buffer_load_dwordx4 v[248:251], %[l16], %[rs], %[n1] offen offset:0 sc1\n\t" \
    ASM_ST3 \
    "v_fma_f32 v188, v188, v206, v208\n\t" \
    "v_fma_f32 v189, v189, v206, v209\n\t" \
    "v_fma_f32 v190, v190, v206, v210\n\t" \
    "v_fma_f32 v191, v191, v206, v211\n\t" \
    "v_mul_f32 v160, v188, v188\n\t" \
    "v_mul_f32 v161, v189, v189\n\t" \
    "v_mul_f32 v162, v190, v190\n\t" \
    "v_mul_f32 v163, v191, v191\n\t" \
    "v_fma_f32 v160, v160, %[k1], v220\n\t" \
    "v_fma_f32 v161, v161, %[k1], v220\n\t" \
    "v_fma_f32 v162, v162, %[k1], v220\n\t" \
    "v_fma_f32 v163, v163, %[k1], v220\n\t" \
    "v_mul_f32 v160, v188, v160\n\t" \
    "v_mul_f32 v161, v189, v161\n\t" \
    "v_mul_f32 v162, v190, v162\n\t" \
    "v_mul_f32 v163, v191, v163\n\t" \
    "v_fma_f32 v192, v192, v206, v212\n\t" \
    "v_fma_f32 v193, v193, v206, v213\n\t" \
    "v_fma_f32 v194, v194, v206, v214\n\t" \
    "v_fma_f32 v195, v195, v206, v215\n\t" \
    "v_mul_f32 v164, v192, v192\n\t" \
    "v_mul_f32 v165, v193, v193\n\t" \
    "v_mul_f32 v166, v194, v194\n\t" \
    "v_mul_f32 v167, v195, v195\n\t" \
    "v_fma_f32 v164, v164, %[k1], v220\n\t" \
    "v_fma_f32 v165, v165, %[k1], v220\n\t" \
    "v_fma_f32 v166, v166, %[k1], v220\n\t" \
    "v_fma_f32 v167, v167, %[k1], v220\n\t" \
    "v_mul_f32 v164, v192, v164\n\t" \
    "v_mul_f32 v165, v193, v165\n\t" \
    "v_mul_f32 v166, v194, v166\n\t" \
    "v_mul_f32 v167, v195, v167\n\t" \
    "v_exp_f32 v160, v160\n\t" \
    "v_exp_f32 v161, v161\n\t" \
    "v_exp_f32 v162, v162\n\t" \
    "v_exp_f32 v163, v163\n\t" \
    "v_exp_f32 v164, v164\n\t" \
    "v_exp_f32 v165, v165\n\t" \
    "v_exp_f32 v166, v166\n\t" \
    "v_exp_f32 v167, v167\n\t" \
    "v_add_f32 v160, 1.0, v160\n\t" \
    "v_add_f32 v161, 1.0, v161\n\t" \
    "v_add_f32 v162, 1.0, v162\n\t" \
    "v_add_f32 v163, 1.0, v163\n\t" \
    "v_add_f32 v164, 1.0, v164\n\t" \
    "v_add_f32 v165, 1.0, v165\n\t" \
    "v_add_f32 v166, 1.0, v166\n\t" \
    "v_add_f32 v167, 1.0, v167\n\t" \
    "v_rcp_f32 v160, v160\n\t" \
    "v_rcp_f32 v161, v161\n\t" \
    "v_rcp_f32 v162, v162\n\t" \
    "v_rcp_f32 v163, v163\n\t" \
    "v_rcp_f32 v164, v164\n\t" \
    "v_rcp_f32 v165, v165\n\t" \
    "v_rcp_f32 v166, v166\n\t" \
    "v_rcp_f32 v167, v167\n\t" \
    "v_mul_f32 v188, v188, v160\n\t" \
    "v_mul_f32 v189, v189, v161\n\t" \
    "v_mul_f32 v190, v190, v162\n\t" \
    "v_mul_f32 v191, v191, v163\n\t" \
    "v_mul_f32 v192, v192, v164\n\t" \
    "v_mul_f32 v193, v193, v165\n\t" \
    "v_mul_f32 v194, v194, v166\n\t" \
    "v_mul_f32 v195, v195, v167\n\t" \
    "v_cvt_pk_bf16_f32 v188, v188, v189\n\t" \
    "v_cvt_pk_bf16_f32 v189, v190, v191\n\t" \
    "v_cvt_pk_bf16_f32 v190, v192, v193\n\t" \
    "v_cvt_pk_bf16_f32 v191, v194, v195\n\t" \
    "v_min_u32 v188, -2, v188\n\t" \
    "v_min_u32 v189, -2, v189\n\t" \
    "v_min_u32 v190, -2, v190\n\t" \
    "v_min_u32 v191, -2, v191\n\t" \
    "v_mov_b32 v216, 0x3f803f80\n\t" \
    "v_mov_b32 v217, 0x3f803f80\n\t" \
    "v_mov_b32 v218, 0x3f803f80\n\t" \
    "v_mov_b32 v219, 0x3f803f80\n\t" \
    "v_permlane16_swap_b32 v188, v190\n\t" \
    "v_permlane16_swap_b32 v189, v191\n\t" \
    "s_waitcnt vmcnt(6)\n\t" \
    "buffer_store_dwordx4 v[188:191], %[oa], %[rs], %[so] offen sc1\n\t" \
    "s_nop 1\n\t" \
    "s_setprio 0\n\t"
#define ASM_WT3_HI \
    "s_setprio 1\n\t" \
    "s_barrier\n\t" ASM_ST \
    "ds_read_b128 v[160:163], %[la] offset:0\n\t" \
    "ds_read_b128 v[164:167], %[la] offset:1024\n\t" \
    "ds_read_b128 v[168:171], %[la] offset:2048\n\t" \
    "ds_read2st64_b32 v[204:205], %[sr] offset0:20 offset1:24\n\t" \
    "ds_read_b128 v[172:175], %[la] offset:3072\n\t" \
    "ds_read_b128 v[176:179], %[la] offset:8192\n\t" \
    "ds_read_b128 v[180:183], %[la] offset:9216\n\t" \
    "s_waitcnt lgkmcnt(6)\n\t" \
    "v_mfma_f32_16x16x32_bf16 v[188:191], v[0:3], v[160:163], 0\n\t" \
    "ds_read_b128 v[184:187], %[la] offset:10240\n\t" \
    "v_mfma_f32_16x16x32_bf16 v[192:195], v[80:83], v[160:163], 0\n\t" \
    "s_waitcnt lgkmcnt(6)\n\t" \
    "v_mfma_f32_16x16x32_bf16 v[188:191], v[4:7], v[164:167], v[188:191]\n\t" \
    "ds_read_b128 v[160:163], %[la] offset:11264\n\t" \
    "v_mfma_f32_16x16x32_bf16 v[192:195], v[84:87], v[164:167], v[192:195]\n\t" \
    "s_waitcnt lgkmcnt(6)\n\t" \
    "v_mfma_f32_16x16x32_bf16 v[188:191], v[8:11], v[168:171], v[188:191]\n\t" \
    "ds_read_b128 v[164:167], %[la] offset:16384\n\t" \
    "s_waitcnt lgkmcnt(6)\n\t" \
    "v_mfma_f32_16x16x32_bf16 v[192:195], v[88:91], v[168:171], v[192:195]\n\t" \
    "v_permlane16_swap_b32 v204, v205\n\t" \
    "s_waitcnt lgkmcnt(5)\n\t" \
    "v_mfma_f32_16x16x32_bf16 v[188:191], v[12:15], v[172:175], v[188:191]\n\t" \
    "ds_read_b128 v[168:171], %[la] offset:17408\n\t" \
    "v_mfma_f32_16x16x32_bf16 v[192:195], v[92:95], v[172:175], v[192:195]\n\t" \
    "v_add_f32 v206, v204, v205\n\t" \
    "s_waitcnt lgkmcnt(5)\n\t" \
    "v_mfma_f32_16x16x32_bf16 v[188:191], v[16:19], v[176:179], v[188:191]\n\t" \
    "ds_read_b128 v[172:175], %[la] offset:18432\n\t" \
    "v_add_f32 v207, v204, v205\n\t" \
    "v_mfma_f32_16x16x32_bf16 v[192:195], v[96:99], v[176:179], v[192:195]\n\t" \
    "s_waitcnt lgkmcnt(5)\n\t" \
    "v_mfma_f32_16x16x32_bf16 v[188:191], v[20:23], v[180:183], v[188:191]\n\t" \
    "ds_read_b128 v[176:179], %[la] offset:19456\n\t" \
    "v_permlane32_swap_b32 v206, v207\n\t" \
    "v_mfma_f32_16x16x32_bf16 v[192:195], v[100:103], v[180:183], v[192:195]\n\t" \
    "s_waitcnt lgkmcnt(5)\n\t" \
    "v_mfma_f32_16x16x32_bf16 v[188:191], v[24:27], v[184:187], v[188:191]\n\t" \
    "ds_read_b128 v[180:183], %[la] offset:24576\n\t" \
    "v_add_f32 v204, v206, v207\n\t" \
    "v_mfma_f32_16x16x32_bf16 v[192:195], v[104:107], v[184:187], v[192:195]\n\t" \
    "v_add_f32 v205, v206, v207\n\t" \
    "s_waitcnt lgkmcnt(5)\n\t" \
    "v_mfma_f32_16x16x32_bf16 v[188:191], v[28:31], v[160:163], v[188:191]\n\t" \
    "ds_read_b128 v[184:187], %[la] offset:25600\n\t" \
    "v_mfma_f32_16x16x32_bf16 v[192:195], v[108:111], v[160:163], v[192:195]\n\t" \
    "v_permlane16_swap_b32 v204, v205\n\t" \
    "s_waitcnt lgkmcnt(5)\n\t" \
    "v_mfma_f32_16x16x32_bf16 v[188:191], v[32:35], v[164:167], v[188:191]\n\t" \
    "ds_read_b128 v[160:163], %[la] offset:26624\n\t" \
    "v_mfma_f32_16x16x32_bf16 v[192:195], v[112:115], v[164:167], v[192:195]\n\t" \
    "v_mul_f32 v205, %[kd], v205\n\t" \
    "s_waitcnt lgkmcnt(5)\n\t" \
    "v_mfma_f32_16x16x32_bf16 v[188:191], v[36:39], v[168:171], v[188:191]\n\t" \
    "ds_read_b128 v[164:167], %[la] offset:27648\n\t" \
    "v_mul_f32 v204, %[kd], v204\n\t" \
    "v_mfma_f32_16x16x32_bf16 v[192:195], v[116:119], v[168:171], v[192:195]\n\t" \
    "v_fma_f32 v204, -v205, v205, v204\n\t" \
    "s_waitcnt lgkmcnt(5)\n\t" \
    "v_mfma_f32_16x16x32_bf16 v[188:191], v[40:43], v[172:175], v[188:191]\n\t" \
    "ds_read_b128 v[168:171], %[la] offset:4096\n\t" \
    "v_max_f32 v204, 0, v204\n\t" \
    "v_mfma_f32_16x16x32_bf16 v[192:195], v[120:123], v[172:175], v[192:195]\n\t" \
    "v_add_f32 v206, %[ke], v204\n\t" \
    "s_waitcnt lgkmcnt(5)\n\t" \
    "v_mfma_f32_16x16x32_bf16 v[188:191], v[44:47], v[176:179], v[188:191]\n\t" \
    "ds_read_b128 v[172:175], %[la] offset:12288\n\t" \
    "v_rsq_f32 v206, v206\n\t" \
    "v_mfma_f32_16x16x32_bf16 v[192:195], v[124:127], v[176:179], v[192:195]\n\t" \
    "s_waitcnt lgkmcnt(5)\n\t" \
    "v_mfma_f32_16x16x32_bf16 v[188:191], v[48:51], v[180:183], v[188:191]\n\t" \
    "ds_read_b128 v[176:179], %[la] offset:20480\n\t" \
    "v_mul_f32 v207, -v205, v206\n\t" \
    "v_mfma_f32_16x16x32_bf16 v[192:195], v[128:131], v[180:183], v[192:195]\n\t" \
    "v_mul_f32 v208, v196, v207\n\t" \
    "s_waitcnt lgkmcnt(5)\n\t" \
    "v_mfma_f32_16x16x32_bf16 v[188:191], v[52:55], v[184:187], v[188:191]\n\t" \
    "ds_read_b128 v[180:183], %[la] offset:28672\n\t" \
    "v_mul_f32 v209, v197, v207\n\t" \
    "v_mul_f32 v210, v198, v207\n\t" \
    "v_mfma_f32_16x16x32_bf16 v[192:195], v[132:135], v[184:187], v[192:195]\n\t" \
    "v_mul_f32 v211, v199, v207\n\t" \
    "v_mul_f32 v212, v200, v207\n\t" \
    "s_waitcnt lgkmcnt(5)\n\t" \
    "v_mfma_f32_16x16x32_bf16 v[188:191], v[56:59], v[160:163], v[188:191]\n\t" \
    "v_mul_f32 v213, v201, v207\n\t" \
    "v_mul_f32 v214, v202, v207\n\t" \
    "v_mfma_f32_16x16x32_bf16 v[192:195], v[136:139], v[160:163], v[192:195]\n\t" \
    "v_mul_f32 v215, v203, v207\n\t" \
    "v_mov_b64 v[216:217], -1\n\t" \
    "s_waitcnt lgkmcnt(4)\n\t" \
    "v_mfma_f32_16x16x32_bf16 v[188:191], v[60:63], v[164:167], v[188:191]\n\t" \
    "v_mov_b64 v[218:219], -1\n\t" \
    "buffer_store_dwordx4 v[216:219], %[l16], %[rs], %[sa] offen sc1\n\t" \
    "v_mfma_f32_16x16x32_bf16 v[192:195], v[140:143], v[164:167], v[192:195]\n\t" \
    "s_waitcnt lgkmcnt(3)\n\t" \
    "v_mfma_f32_16x16x32_bf16 v[188:191], v[64:67], v[168:171], v[188:191]\n\t" \
    "v_mfma_f32_16x16x32_bf16 v[192:195], v[144:147], v[168:171], v[192:195]\n\t" \
    "s_waitcnt lgkmcnt(2)\n\t" \
    "v_mfma_f32_16x16x32_bf16 v[188:191], v[68:71], v[172:175], v[188:191]\n\t" \
    "v_mfma_f32_16x16x32_bf16 v[192:195], v[148:151], v[172:175], v[192:195]\n\t" \
    "s_waitcnt lgkmcnt(1)\n\t" \
    "v_mfma_f32_16x16x32_bf16 v[188:191], v[72:75], v[176:179], v[188:191]\n\t" \
    "v_mfma_f32_16x16x32_bf16 v[192:195], v[152:155], v[176:179], v[192:195]\n\t" \
    "s_waitcnt lgkmcnt(0)\n\t" \
    "v_mfma_f32_16x16x32_bf16 v[188:191], v[76:79], v[180:183], v[188:191]\n\t" \
    "v_mfma_f32_16x16x32_bf16 v[192:195], v[156:159], v[180:183], v[192:195]\n\t" \
    ASM_ST3 \
    "s_nop 4\n\t" \
    "v_fma_f32 v188, v188, v206, v208\n\t" \
    "v_fma_f32 v189, v189, v206, v209\n\t" \
    "v_fma_f32 v190, v190, v206, v210\n\t" \
    "v_fma_f32 v191, v191, v206, v211\n\t" \
    "v_mul_f32 v160, v188, v188\n\t" \
    "v_mul_f32 v161, v189, v189\n\t" \
    "v_mul_f32 v162, v190, v190\n\t" \
    "v_mul_f32 v163, v191, v191\n\t" \
    "v_fma_f32 v160, v160, %[k1], v220\n\t" \
    "v_fma_f32 v161, v161, %[k1], v220\n\t" \
    "v_fma_f32 v162, v162, %[k1], v220\n\t" \
    "v_fma_f32 v163, v163, %[k1], v220\n\t" \
    "v_mul_f32 v160, v188, v160\n\t" \
    "v_mul_f32 v161, v189, v161\n\t" \
    "v_mul_f32 v162, v190, v162\n\t" \
    "v_mul_f32 v163, v191, v163\n\t" \
    "v_fma_f32 v192, v192, v206, v212\n\t" \
    "v_fma_f32 v193, v193, v206, v213\n\t" \
    "v_fma_f32 v194, v194, v206, v214\n\t" \
    "v_fma_f32 v195, v195, v206, v215\n\t" \
    "v_mul_f32 v164, v192, v192\n\t" \
    "v_mul_f32 v165, v193, v193\n\t" \
    "v_mul_f32 v166, v194, v194\n\t" \
    "v_mul_f32 v167, v195, v195\n\t" \
    "v_fma_f32 v164, v164, %[k1], v220\n\t" \
    "v_fma_f32 v165, v165, %[k1], v220\n\t" \
    "v_fma_f32 v166, v166, %[k1], v220\n\t" \
    "v_fma_f32 v167, v167, %[k1], v220\n\t" \
    "v_mul_f32 v164, v192, v164\n\t" \
    "v_mul_f32 v165, v193, v165\n\t" \
    "v_mul_f32 v166, v194, v166\n\t" \
    "v_mul_f32 v167, v195, v167\n\t" \
    "v_exp_f32 v160, v160\n\t" \
    "v_exp_f32 v161, v161\n\t" \
    "v_exp_f32 v162, v162\n\t" \
    "v_exp_f32 v163, v163\n\t" \
    "v_exp_f32 v164, v164\n\t" \
    "v_exp_f32 v165, v165\n\t" \
    "v_exp_f32 v166, v166\n\t" \
    "v_exp_f32 v167, v167\n\t" \
    "v_add_f32 v160, 1.0, v160\n\t" \
    "v_add_f32 v161, 1.0, v161\n\t" \
    "v_add_f32 v162, 1.0, v162\n\t" \
    "v_add_f32 v163, 1.0, v163\n\t" \
    "v_add_f32 v164, 1.0, v164\n\t" \
    "v_add_f32 v165, 1.0, v165\n\t" \
    "v_add_f32 v166, 1.0, v166\n\t" \
    "v_add_f32 v167, 1.0, v167\n\t" \
    "v_rcp_f32 v160, v160\n\t" \
    "v_rcp_f32 v161, v161\n\t" \
    "v_rcp_f32 v162, v162\n\t" \
    "v_rcp_f32 v163, v163\n\t" \
    "v_rcp_f32 v164, v164\n\t" \
    "v_rcp_f32 v165, v165\n\t" \
    "v_rcp_f32 v166, v166\n\t" \
    "v_rcp_f32 v167, v167\n\t" \
    "v_mul_f32 v188, v188, v160\n\t" \
    "v_mul_f32 v189, v189, v161\n\t" \
    "v_mul_f32 v190, v190, v162\n\t" \
    "v_mul_f32 v191, v191, v163\n\t" \
    "v_mul_f32 v192, v192, v164\n\t" \
    "v_mul_f32 v193, v193, v165\n\t" \
    "v_mul_f32 v194, v194, v166\n\t" \
    "v_mul_f32 v195, v195, v167\n\t" \
    "v_cvt_pk_bf16_f32 v188, v188, v189\n\t" \
    "v_cvt_pk_bf16_f32 v189, v190, v191\n\t" \
    "v_cvt_pk_bf16_f32 v190, v192, v193\n\t" \
    "v_cvt_pk_bf16_f32 v191, v194, v195\n\t" \
    "v_min_u32 v188, -2, v188\n\t" \
    "v_min_u32 v189, -2, v189\n\t" \
    "v_min_u32 v190, -2, v190\n\t" \
    "v_min_u32 v191, -2, v191\n\t" \
    "s_nop 1\n\t" \
    "v_permlane16_swap_b32 v188, v190\n\t" \
    "v_permlane16_swap_b32 v189, v191\n\t" \
    "s_waitcnt vmcnt(1)\n\t" \
    "buffer_store_dwordx4 v[188:191], %[oa], %[rs], %[so] offen sc1\n\t" \
    "s_nop 1\n\t"
#define ASM_POLL5_I \
    "buffer_load_dwordx4 v[232:235], %[l16], %[rs], %[s0] offen offset:0 sc1\n\t" \
    "buffer_load_dwordx4 v[236:239], %[l16], %[rs], %[s0] offen offset:1024 sc1\n\t" \
    "buffer_load_dwordx4 v[240:243], %[l16], %[rs], %[s0] offen offset:2048 sc1\n\t" \
    "buffer_load_dwordx4 v[244:247], %[l16], %[rs], %[s0] offen offset:3072 sc1\n\t" \
    "buffer_load_dwordx4 v[248:251], %[l16], %[rs], %[s1] offen offset:0 sc1\n\t"
using u32x8 = __attribute__((ext_vector_type(8))) unsigned;
using u32x16 = __attribute__((ext_vector_type(16))) unsigned;
using u32x32 = __attribute__((ext_vector_type(32))) unsigned;
struct WRegs { u32x32 w[6]; u32x8 t; };
#define WREGS(W) "{v[0:31]}"(W.w[0]), "{v[32:63]}"(W.w[1]), "{v[64:95]}"(W.w[2]), "{v[96:127]}"(W.w[3]), "{v[128:159]}"(W.w[4]), "{v[160:191]}"(W.w[5]), "{v[192:199]}"(W.t)
__device__ __forceinline__ u32x32 cat32(u32x8 a, u32x8 b, u32x8 c, u32x8 d) {
    const u32x16 lo = __builtin_shufflevector(a, b, 0, 1, 2, 3, 4, 5, 6, 7, 8, 9, 10, 11, 12, 13, 14, 15);
    const u32x16 hi = __builtin_shufflevector(c, d, 0, 1, 2, 3, 4, 5, 6, 7, 8, 9, 10, 11, 12, 13, 14, 15);
    return __builtin_shufflevector(lo, hi, 0, 1, 2, 3, 4, 5, 6, 7, 8, 9, 10, 11, 12, 13, 14, 15, 16, 17, 18, 19, 20, 21, 22, 23, 24, 25, 26, 27, 28, 29, 30, 31);
}
#define WOUT(W) "=&{v[0:31]}"(W.w[0]), "=&{v[32:63]}"(W.w[1]), "=&{v[64:95]}"(W.w[2]), "=&{v[96:127]}"(W.w[3]), "=&{v[128:159]}"(W.w[4]), "=&{v[160:191]}"(W.w[5]), "=&{v[192:199]}"(W.t)
#define WTMP "memory", "scc", "v200", "v201", "v202", "v203", "v204", "v205", "v206", "v207", "v208", "v209", "v210", "v211", "v212", "v213", "v214", "v215", "v216", "v217", "v218", "v219", "v220", "v221", "v222", "v223", "v224", "v225", "v226", "v227", "v228", "v229", "v230", "v231", "v232", "v233", "v234", "v235", "v236", "v237", "v238", "v239"
__device__ __forceinline__ void load_w3(WRegs& W, const bf16_t* m, int wave, unsigned c0, bool hq) {
    const int ln = lane_id();
    unsigned o[3], s[3], t;
#pragma unroll
    for (int k = 0; k < 3; ++k) {
        const int row = (k * 64 + ln) / 24, sg = (k * 64 + ln) % 24;
        o[k] = row * (HD * 2) + c0 + (hq ? ((sg >> 1) & 1) * 384 + ((0x140 >> ((sg >> 3) * 4)) & 15) * 32 + (sg & 5) * 16 : sg * 16); s[k] = wave * 13824 + row * 416 + sg * 16;
    }
    const unsigned tr = (ln >> 4) * 3456 + ((ln >> 2) & 3) * 416 + (wave & 3) * 32 + (ln & 3) * 8;
    asm volatile(ASM_WLOAD3 : WOUT(W), [t] "=&s"(t) : [rs] "s"(mkrsrc(m, DM * HD * 2)), [wx] "s"(wave), [o0] "v"(o[0]), [o1] "v"(o[1]), [o2] "v"(o[2]), [s0] "v"(s[0]), [s1] "v"(s[1]), [s2] "v"(s[2]),
                 [t0] "v"(tr + wave * 13824), [t1] "v"(tr + (wave ^ 1) * 13824), [t2] "v"(tr + (wave ^ 2) * 13824), [t3] "v"(tr + (wave ^ 3) * 13824), [t4] "v"(tr + (wave & 4) * 13824),
                 [wl] "v"(L_W + wave * 10240 + ln * 16) : WTMP);
}
__device__ __forceinline__ void load_wo(WRegs& W, const bf16_t* m, int wave, int h) {
    const int ln = lane_id();
    unsigned o[5], s[5], t;
#pragma unroll
    for (int k = 0; k < 5; ++k) {
        const int row = (k * 64 + ln) / 20, sg = (k * 64 + ln) % 20;
        o[k] = (h * DH + row) * (DM * 2) + sg * 16 + (wave & 3) * 320; s[k] = wave * 13824 + row * 352 + sg * 16;
    }
    asm volatile(ASM_WLOADO : WOUT(W), [t] "=&s"(t) : [rs] "s"(mkrsrc(m, DM * HD * 2)), [o0] "v"(o[0]), [o1] "v"(o[1]), [o2] "v"(o[2]), [o3] "v"(o[3]), [o4] "v"(o[4]),
                 [s0] "v"(s[0]), [s1] "v"(s[1]), [s2] "v"(s[2]), [s3] "v"(s[3]), [s4] "v"(s[4]), [tr] "v"(wave * 13824 + (ln >> 2) * 352 + (ln & 3) * 8), [wl] "v"(L_W + wave * 10240 + ln * 16) : WTMP);
}
using s16x4 = __attribute__((ext_vector_type(4))) short;
template <int NT, int KS, int N>
__device__ __forceinline__ void load_wn(u32x4 (&f)[NT * KS], const bf16_t* m, int rows, int wave, int c0, int k0) {
    const int ln = lane_id();
    const rsrc_t rs = mkrsrc(m, rows * N * 2);
#pragma unroll
    for (int k = 0; k < KS; ++k)
#pragma unroll
        for (int n = 0; n < NT; ++n) f[n * KS + k] = __builtin_amdgcn_raw_buffer_load_b128(rs, ((n * 64 + ln) / (2 * NT)) * (N * 2) + ((n * 64 + ln) % (2 * NT)) * 16, (k0 + k) * (N * 64) + c0 * 32, 0);
    const unsigned st = wave * 10240 + ln * 16, tr = wave * 10240 + ((ln >> 4) * 8 + ((ln >> 2) & 3)) * (NT * 32) + (ln & 3) * 8;
#pragma unroll
    for (int k = 0; k < KS; ++k) {
#pragma unroll
        for (int n = 0; n < NT; ++n) lds_st<u32x4>(st + n * 1024, f[n * KS + k]);
        asm volatile("" ::: "memory");
#pragma unroll
        for (int n = 0; n < NT; ++n) {
            const u32x2 a = __builtin_bit_cast(u32x2, __builtin_amdgcn_ds_read_tr16_b64_v4i16((LAS s16x4*)(tr + n * 32)));
            const u32x2 b = __builtin_bit_cast(u32x2, __builtin_amdgcn_ds_read_tr16_b64_v4i16((LAS s16x4*)(tr + n * 32 + NT * 128)));
            f[n * KS + k] = (u32x4){a[0], a[1], b[0], b[1]};
        }
        asm volatile("" ::: "memory");
    }
}
struct C1Regs { u32x8 lw0, lw1, z1; u32x4 z0; };
#define XG_OUT "=&{v[200:207]}"(C.lw0), "=&{v[224:227]}"(C.z0), "=&{v[228:235]}"(C.lw1), "=&{v[244:251]}"(C.z1)
#define XG_IN WREGS(W), [xa] "v"(xa), [wl] "v"(wl), [qs] "s"(qs)
#define XG_CLOB "memory", "scc", "v208", "v209", "v210", "v211", "v212", "v213", "v214", "v215", "v216", "v217", "v218", "v219", "v220", "v221", "v222", "v223", "v236", "v237", "v238", "v239", "v240", "v241", "v242", "v243", "v252"
__device__ __forceinline__ void xn_wait(const Params& P, rsrc_t rr, int seg, int cpy) {
    for (int spins = 0;; ++spins) {
        const unsigned f = __builtin_amdgcn_raw_buffer_load_b32(rr, (lane_id() & 15) * 128, RG_FX + (cpy & 3) * 2048, 16);
        if (__all(f > (unsigned)seg)) break;
        __builtin_amdgcn_s_sleep(4);
        if (spin_fail(spins, P)) break;
    }
    asm volatile("" ::: "memory");
}
__device__ __forceinline__ unsigned c1_dst(int wave, bool ktype) {
    const int ln = lane_id(), l15 = ln & 15, hf = (wave >> 1) & 1;
    const int rb16 = ((ln >> 2) & 4) * 16 + (ln >> 5) * 32;
    return (ktype ? L_KM + (l15 >> 2) * 128 + hf * 8 + (l15 & 3) * 2
                  : L_Q + (l15 >> 3) * 256 + ((l15 >> 1) & 1) * 128 + ((l15 >> 2) & 1) * 8 + hf * 4 + (l15 & 1) * 2) + (wave & 1) * 1536 + rb16;
}
template <int NP>
__device__ __forceinline__ void crit1(const WRegs& W, C1Regs& C, rsrc_t rr, unsigned sbase, int wave, unsigned wl, unsigned dsto, unsigned& budget
) {
    unsigned abase;
    asm volatile("v_and_b32 %0, 0x3ff, %1\n\tv_or_b32 %0, %2, %0" : "=&v"(abase) : "v"(wl), "s"(wave * 1024));
    ASM_ST_DECL;
#define C1_OUT "+{v[224:227]}"(C.z0), "+{v[244:251]}"(C.z1), "+{v[200:207]}"(C.lw0), "+{v[228:235]}"(C.lw1)
#define C1_IN WREGS(W), [ab] "v"(abase), [wl] "v"(wl), [dst] "v"(dsto)
#define C1_CLOB "memory", "vcc", "scc", "v208", "v209", "v210", "v211", "v212", "v213", "v214", "v215", "v216", "v217", "v218", "v219", "v220", "v221", "v222", "v223", "v236", "v237", "v238", "v239", "v240", "v241", "v242", "v243"
#define C1_POLL [rs] "s"(rr), [s0] "s"(sbase), [s1] "s"(sbase + 4096), [s2] "s"(sbase + BLK)
    ST(1);
    if constexpr (NP == 3) asm volatile(ASM_CRIT1_3 : C1_OUT, [n] "+s"(budget) ASM_ST_OUT ASM_ST2_OUT : C1_IN, C1_POLL : C1_CLOB);
    else if constexpr (NP == 2) asm volatile(ASM_CRIT1_2 : C1_OUT, [n] "+s"(budget) ASM_ST_OUT ASM_ST2_OUT : C1_IN, C1_POLL : C1_CLOB);
    else asm volatile(ASM_CRIT1_N : C1_OUT ASM_ST_OUT : C1_IN : C1_CLOB);
    SB();
    ASM_ST_SAVE(3);
    if constexpr (NP != 0) { ASM_ST2_SAVE(2); }
}
#define STARG

#define SRG 5
__device__ __forceinline__ void s_role(const Params& P, int wave, int L, int par, int h, int cpy) {
    const rsrc_t rr = mkrsrc(P.ws + par * 8192, RG_L);
    const char* xn = P.ws + PG0 + PG_XN + L * PG_LX;
    auto xdma = [&](int seg, int hb) {
        const unsigned l16 = lane_id() * 16;
        const int mt = (wave >> 1) & 1;
        const char* g = xn + pgb(seg, par);
        if (hb == 0) {
            dma4(g + blin(mt * 20480 + (wave & 1) * 4096), l16, L_XA + mt * 16384 + (wave & 1) * 4096);
            dmaq<0>(g + blin(mt * 20480 + 16384) + (wave & 1) * 1024, l16, L_XB + mt * 4096 + (wave & 1) * 1024);
        } else {
            dma4(g + blin(mt * 20480 + 8192), l16, L_XA + mt * 16384 + 8192); dma4(g + blin(mt * 20480 + 12288), l16, L_XA + mt * 16384 + 12288);
            dmaq<2>(g + blin(mt * 20480 + 16384), l16, L_XB + mt * 4096); dmaq<3>(g + blin(mt * 20480 + 16384), l16, L_XB + mt * 4096);
        }
    };
    const float SCL = 0.07216878364870322f * 1.4426950408889634f;
    auto run = [&](auto Cc) {
    constexpr int CLS = decltype(Cc)::value;
    WRegs wf;
    load_w3(wf, P.w[CLS == 3], wave, h * 384, false);
    if (CLS == 3) {
        if (L) xn_wait(P, rr, 0, cpy);
        xdma(0, 0);
        if (wave == 4 || wave == 7) xdma(0, 1);
        wait_vm<0>();
    }
    bar();
    if (CLS == 3 && wave == 7 && L) xn_wait(P, rr, 1, cpy);
    const unsigned qs = (CLS == 3 ? L_KX : L_QX) + (wave & 1) * 3072 + ((wave >> 1) & 1) * 8;
    unsigned xa, wl, dsto;
    if (CLS == 3) { xa = L_XA + wave * 2048 + lane_id() * 16; wl = L_W + wave * 10240 + lane_id() * 16; dsto = c1_dst(wave, true); }
    unsigned budget = 1u << 18;
    STDECL;
    LDECL;
    for (int t = 0; t < NSEG; ++t) {
        const unsigned slot = t & (NSLOT - 1);
        C1Regs C;
        {
            const WRegs& W = wf;
            ASM_ST_DECL;
            if constexpr (CLS == 3) {
                const int mt = (wave >> 1) & 1;
                const char* g = xn + pgb(t + 1 < NSEG ? t + 1 : t, par);
                asm volatile(ASM_XG_S_HI : XG_OUT ASM_ST_OUT : XG_IN, [slp] "s"(SLP4), [g0] "s"(g + blin(mt * 20480 + (wave & 1) * 4096)), [d0] "s"(L_XA + mt * 16384 + (wave & 1) * 4096),
                             [g1] "s"(g + blin(mt * 20480 + 16384) + (wave & 1) * 1024), [d1] "s"(L_XB + mt * 4096 + (wave & 1) * 1024) : XG_CLOB);
                wait_vm<0>();
                LB(t);
            } else {
                const unsigned l16 = lane_id() * 16;
                xa = L_XA + wave * 2048 + l16; wl = L_W + wave * 10240 + l16;
#define XG_Q(A) asm volatile("s_cmp_lt_u32 %[t], 2\n\ts_cbranch_scc1 .Lq3%=\n\t" A "s_branch .Lqe%=\n\t.Lq3%=:\n\t" ASM_XG_S_LO ".Lqe%=:" : XG_OUT ASM_ST_OUT : XG_IN, [t] "s"(t) : XG_CLOB)
                if constexpr (CLS == 0 || CLS == 4) XG_Q(ASM_XG_S_LO1); else XG_Q(ASM_XG_S_LO2);
                if (CLS == 4 && t >= HT0) {
                    const int ln = lane_id(), u = h >> 2;
                    for (int spins = 0;; ++spins) {
                        const unsigned fl = __builtin_amdgcn_raw_buffer_load_b32(rr, (ln & 3) * 128, RG_FH + BLK + u * 512, 16);
                        if (__all(fl > (unsigned)t)) break;
                        __builtin_amdgcn_s_sleep(2);
                        if (spin_fail(spins, P)) break;
                    }
                    asm volatile("" ::: "memory");
                    const char* g = P.ws + PG0 + pgb(t, par) + PG_HV + (16 + u) * 3 * BLK;
#pragma unroll
                    for (int k = 0; k < 6; ++k)
                        dmaq<0>(g + blin((((h >> 1) & 1) * 12 + (h & 1) * 3 + (k / 3) * 6 + k % 3) * 1024), ln * 16, L_QX + (k / 3) * 6144 + (k % 3 == 2 ? 3 : k % 3) * 1024);
                }
                dsto = c1_dst(wave, false);
            }
            ASM_ST_SAVE(7);
            ST(6);
        }
        ST(0);
        crit1<CLS == 3 ? 0 : CLS < 2 ? 3 : 2>(wf, C, rr, RG_XM + slot * XM_SLOT + cpy * XM_COPY, wave, wl, dsto, budget STARG);
        if constexpr (CLS == 0) {
            int ln = lane_id();
            const int sidx = ((ln & 15) < 8) ? 0x4444 : 0xEEEE;
            const unsigned l16 = ln * 16;
            bf16x16 F0, F1, F2, F3;
            F0 = lds_frag(L_KX + l16, 1024); F1 = lds_frag(L_KX + 6144 + l16, 1024); F2 = lds_frag(L_KX + 2048 + l16, 1024);
            f32x4 sc[3];
#pragma unroll
            for (int j = 0; j < 3; ++j) sc[j] = (f32x4){0.f, 0.f, 0.f, 0.f};
            unsigned kmo = L_KM + (ln >> 4) * 128 + (ln & 7) * 16;
            asm volatile("s_waitcnt lgkmcnt(0)" : "+v"(kmo), "+{v[244:247]}"(sc[0]), "+{v[252:255]}"(sc[1]), "+{v[240:243]}"(sc[2]),
                         "+v"(F0), "+v"(F1), "+v"(F2) :: "memory");
            SB();
            __builtin_amdgcn_s_barrier();
            asm volatile("" ::: "memory");
            __builtin_amdgcn_s_setprio(3);
            bf16x8 qa0 = lds_ld<bf16x8>(L_Q + l16);
            bf16x8 qa1 = lds_ld<bf16x8>(L_Q + 1024 + l16);
            F3 = lds_frag(kmo, 512);
            SB();
            SMF(qa0, F0, sc[0]);
            SB();
            F0 = lds_frag(kmo + 1024, 512);
            SMF(qa0, F1, sc[1]);
            SB();
            F1 = lds_frag(L_KX + 6144 + 2048 + l16, 1024);
            SMF(qa1, F2, sc[0]);
            SB();
            F2 = lds_frag(L_KX + 4096 + l16, 1024);
            SMF(qa0, F3, sc[2]);
            SB();
            qa0 = lds_ld<bf16x8>(L_Q + 2048 + l16);
            F3 = lds_frag(L_KX + 6144 + 4096 + l16, 1024);
            SMF(qa1, F0, sc[2]);
            SB();
            F0 = lds_frag(kmo + 2048, 512);
            SMF(qa1, F1, sc[1]);
            SB();
            ln = lane_id();
            const int l15 = ln & 15;
            const int rb16 = ((ln >> 2) & 4) * 16 + (ln >> 5) * 32;
            unsigned pb = L_Q + rb16 + (l15 & 7) * 2;
            unsigned pbx = L_Q + 128 + (l15 >> 3) * 256 + ((l15 >> 1) & 1) * 128 + rb16 + ((l15 >> 2) & 1) * 8 + (l15 & 1) * 2;
            unsigned poff = (l16 + ((ln < 8) ? h * 128 : BLK - 128 + (h >> 4) * BLK + (h & 15) * 512)) | ((ln >= 40) ? 0x40000000u : 0u);
            asm volatile("" : "+v"(pb), "+v"(pbx), "+v"(poff));
            SB();
            SMF(qa0, F2, sc[0]);
            SB();
            SMF(qa0, F3, sc[1]);
            SB();
            SMF(qa0, F0, sc[2]);
            SB();
            f32x4 mz;
            asm volatile(
                "s_nop 5\n\t"
                "v_permlane32_swap_b32 v244, v246\n\t"
                "v_permlane32_swap_b32 v245, v247\n\t"
                "v_permlane32_swap_b32 v252, v254\n\t"
                "v_permlane32_swap_b32 v253, v255\n\t"
                "v_permlane32_swap_b32 v240, v242\n\t"
                "v_permlane32_swap_b32 v241, v243\n\t"
                "v_pk_add_f32 v[244:245], v[244:245], v[246:247]\n\t"
                "v_pk_add_f32 v[246:247], v[252:253], v[254:255]\n\t"
                "v_pk_add_f32 v[240:241], v[240:241], v[242:243]\n\t"
                "s_nop 0\n\t"
                "v_max3_f32 v248, v244, v246, v240\n\t"
                "v_max3_f32 v249, v245, v247, v241\n\t"
                "s_nop 0\n\t"
                "v_max_f32_dpp v248, v248, v248 quad_perm:[1,0,3,2] row_mask:0xf bank_mask:0xf\n\t"
                "v_max_f32_dpp v249, v249, v249 quad_perm:[1,0,3,2] row_mask:0xf bank_mask:0xf\n\t"
                "s_nop 0\n\t"
                "v_max_f32_dpp v248, v248, v248 quad_perm:[2,3,0,1] row_mask:0xf bank_mask:0xf\n\t"
                "v_max_f32_dpp v249, v249, v249 quad_perm:[2,3,0,1] row_mask:0xf bank_mask:0xf\n\t"
                "s_nop 0\n\t"
                "v_max_f32_dpp v248, v248, v248 row_half_mirror row_mask:0xf bank_mask:0xf\n\t"
                "v_max_f32_dpp v249, v249, v249 row_half_mirror row_mask:0xf bank_mask:0xf\n\t"
                "s_nop 0\n\t"
                "v_max_f32_dpp v248, v248, v248 row_mirror row_mask:0xf bank_mask:0xf\n\t"
                "v_max_f32_dpp v249, v249, v249 row_mirror row_mask:0xf bank_mask:0xf\n\t"
                "v_mul_f32 v248, %[scl], v248\n\t"
                "v_mul_f32 v249, %[scl], v249\n\t"
                "v_fma_f32 v244, v244, %[scl], -v248\n\t"
                "v_fma_f32 v245, v245, %[scl], -v249\n\t"
                "v_fma_f32 v246, v246, %[scl], -v248\n\t"
                "v_fma_f32 v247, v247, %[scl], -v249\n\t"
                "v_exp_f32 v244, v244\n\t"
                "v_exp_f32 v245, v245\n\t"
                "v_fma_f32 v240, v240, %[scl], -v248\n\t"
                "v_fma_f32 v241, v241, %[scl], -v249\n\t"
                "v_exp_f32 v246, v246\n\t"
                "v_exp_f32 v247, v247\n\t"
                "v_exp_f32 v240, v240\n\t"
                "v_exp_f32 v241, v241\n\t"
                "v_add_f32 v250, v244, v246\n\t"
                "v_add_f32 v251, v245, v247\n\t"
                "v_fma_f32 v250, v240, 0.5, v250\n\t"
                "v_fma_f32 v251, v241, 0.5, v251\n\t"
                "s_nop 0\n\t"
                "v_add_f32_dpp v250, v250, v250 quad_perm:[1,0,3,2] row_mask:0xf bank_mask:0xf\n\t"
                "v_add_f32_dpp v251, v251, v251 quad_perm:[1,0,3,2] row_mask:0xf bank_mask:0xf\n\t"
                "s_nop 0\n\t"
                "v_add_f32_dpp v250, v250, v250 quad_perm:[2,3,0,1] row_mask:0xf bank_mask:0xf\n\t"
                "v_add_f32_dpp v251, v251, v251 quad_perm:[2,3,0,1] row_mask:0xf bank_mask:0xf\n\t"
                "s_nop 0\n\t"
                "v_add_f32_dpp v250, v250, v250 row_half_mirror row_mask:0xf bank_mask:0xf\n\t"
                "v_add_f32_dpp v251, v251, v251 row_half_mirror row_mask:0xf bank_mask:0xf\n\t"
                "s_nop 0\n\t"
                "v_add_f32_dpp v250, v250, v250 row_mirror row_mask:0xf bank_mask:0xf\n\t"
                "v_add_f32_dpp v251, v251, v251 row_mirror row_mask:0xf bank_mask:0xf\n\t"
                "v_rcp_f32 v250, v250\n\t"
                "v_rcp_f32 v251, v251\n\t"
                "s_nop 0\n\t"
                "v_pk_mul_f32 v[244:245], v[244:245], v[250:251]\n\t"
                "v_pk_mul_f32 v[246:247], v[246:247], v[250:251]\n\t"
                "v_pk_mul_f32 v[240:241], v[240:241], v[250:251]\n\t"
                "v_cvt_pk_bf16_f32 v244, |v244|, |v245|\n\t"
                "v_cvt_pk_bf16_f32 v246, |v246|, |v247|\n\t"
                "v_cvt_pk_bf16_f32 v240, |v240|, |v241|\n\t"
                "ds_write_b16 %[pbx], v244\n\t"
                "ds_write_b16_d16_hi %[pbx], v244 offset:16\n\t"
                "ds_write_b16 %[pbx], v246 offset:4\n\t"
                "ds_write_b16_d16_hi %[pbx], v246 offset:20\n\t"
                "ds_write_b16 %[pb], v240\n\t"
                "ds_write_b16_d16_hi %[pb], v240 offset:16\n\t"
                EWAKE
                : "+{v[240:243]}"(sc[2]), "+{v[244:247]}"(sc[0]), "=&{v[248:251]}"(mz), "+{v[252:255]}"(sc[1])
                : [pb] "v"(pb), [pbx] "v"(pbx), [scl] "s"(SCL) : "memory");
            asm volatile("s_waitcnt lgkmcnt(0)" ::: "memory");
            const u32x4 v = lds_ld<u32x4>(L_Q + l16);
#pragma unroll
            for (int c = 0; c < (int)NCP; ++c) __builtin_amdgcn_raw_buffer_store_b128(v, rr, poff, RG_XP + slot * XP_SLOT + c * XP_COPY, 16);
            asm volatile(LWAKE "s_nop 1" :: "v"(v) : "memory");
            __builtin_amdgcn_s_setprio(0);
            ST(5);
        } else if constexpr (CLS == 1 || CLS == 4) {
            const int ln = lane_id(), l15 = ln & 15, l4 = ln >> 4;
            constexpr int mt = (CLS == 4);
            const unsigned qa = L_QX + mt * 6144 + ln * 16, kmo = L_KM + l4 * 128 + (ln & 7) * 16;
            const unsigned xo = mt ? (h & 3) * 1024 : 5120 + ((h >> 1) & 1) * 1024 + (h & 1) * 256;
            const unsigned pm = l4 < 2 ? (mt ? 4096 : 7168) + (h & 3) * 256 - xo + l15 * 16 + l4 * 8 : OOB;
            const unsigned so = RG_PX + slot * PX_SLOT + (h >> 2) * BLK + xo;
            f32x4 sd;
#pragma unroll
            for (int r = 0; r < 4; ++r) sd[r] = (l4 * 4 + r > l15) ? -3.0e38f : 0.f;
            ASM_ST_DECL;
#define P4_ARGS : "+{v[240:243]}"(sd) ASM_ST_OUT : WREGS(wf), [qa] "v"(qa), [km] "v"(kmo), [pm] "v"(pm), [rr] "s"(rr), [so] "s"(so), [scl] "s"(SCL) : "memory", "v200", "v201", "v202", "v203", "v204", "v205", "v206", "v207", "v208", "v209", "v210", "v211", "v212", "v213", "v214", "v215", "v216", "v217", "v218", "v219", "v220", "v221", "v222", "v223", "v224", "v225", "v226", "v227", "v228", "v229", "v230", "v231", "v232", "v233", "v234", "v235", "v236", "v237", "v238", "v239", "v244", "v245", "v246", "v247", "v248", "v249", "v250", "v251"
            if constexpr (mt) asm volatile(ASM_P4_1 P4_ARGS); else asm volatile(ASM_P4_0 P4_ARGS);
            ASM_ST_SAVE(4);
            ST(5);
            if constexpr (mt) {
                const int ln = lane_id();
                const u32x4 s4 = sent4();
                const unsigned poff = (ln * 16 + ((ln < 8) ? h * 128 : BLK - 128 + (h >> 4) * BLK + (h & 15) * 512)) | ((ln >= 40 || t < 1) ? 0x40000000u : 0u);
#pragma unroll
                for (int c = 0; c < (int)NCP; ++c) st128(s4, rr, poff, RG_XP + ((t - 1) & (NSLOT - 1)) * XP_SLOT + c * XP_COPY);
                const unsigned pbase = RG_PX + ((t - 2) & (NSLOT - 1)) * PX_SLOT + (h >> 2) * BLK;
                const unsigned o2 = ((ln & 16) ? 5120 + ((h >> 1) & 1) * 1024 + (h & 1) * 256 + (ln >> 5) * 512 : 4096 + (h & 3) * 256 + (ln >> 5) * 3072) + (ln & 15) * 16;
                if (t >= 2) { st128(s4, rr, ln * 16, pbase + (h & 3) * 1024); st128(s4, rr, o2, pbase); }
            } else if (L && t + 2 < NSEG) xn_wait(P, rr, t + 2, cpy);
        } else {
            bar();
            ST(4);
            if (CLS == 3) LA();
            {
                if (CLS == 3 && (wave == 4 || wave == 7)) {
                    if (t + 1 < NSEG) xdma(t + 1, 1);
                }
                ST(5);
            }
        }
        STFLUSH(t);
    }
    LF();
    };
    if (wave == 0) run(std::integral_constant<int, 0>{});
    else if (wave == 1) run(std::integral_constant<int, 1>{});
    else if (wave == 2) run(std::integral_constant<int, 4>{});
    else if (wave == 3) run(std::integral_constant<int, 2>{});
    else run(std::integral_constant<int, 3>{});
}

__device__ __forceinline__ void h_role(const Params& P, int wave, int L, int par, int i, int cpy) {
    const rsrc_t rr = mkrsrc(P.ws + par * 8192, RG_L), rp = mkrsrc(P.ws + PG0, PG_L);
    const char* xn = P.ws + PG0 + PG_XN + L * PG_LX;
    auto run = [&](auto Cc) {
    constexpr bool HI = decltype(Cc)::value;
    const int mt = (wave >> 1) & 1;
    auto xdma = [&](int seg, int hb) {
        const unsigned l16 = lane_id() * 16;
        const char* g = xn + pgb(seg, par);
        dma4(g + blin(mt * 20480 + hb * 8192 + (wave & 1) * 4096), l16, L_XA + mt * 16384 + hb * 8192 + (wave & 1) * 4096);
        dmaq<0>(g + blin(mt * 20480 + 16384) + hb * 2048 + (wave & 1) * 1024, l16, L_XB + mt * 4096 + hb * 2048 + (wave & 1) * 1024);
    };
    WRegs wf;
    load_w3(wf, i < 16 ? P.w[2] : P.w[0], wave, (i < 16 ? i * 2 + HI : (i - 16) * 4 + HI * 2) * 384, i >= 16);
    const unsigned qs = (HI ? L_KX : L_QX) + (wave & 1) * 3072 + mt * 8;
    const unsigned xa = L_XA + wave * 2048 + lane_id() * 16, wl = L_W + wave * 10240 + lane_id() * 16;
    const unsigned fp = RG_FH + (i >> 4) * BLK + ((i & 15) * 4 + (wave & 3)) * 128;
    unsigned go = 0, all = 0;
    STDECL;
    LDECL;
    for (int t = HT0; t <= NSEG; ++t) {
        ST(0);
        if constexpr (HI) {
            const unsigned l16 = lane_id() * 16;
            if (t > HT0) {
                u32x4 v[6];
                const int w4 = wave - 4;
#pragma unroll
                for (int k = 0; k < 6; ++k) v[k] = lds_ld<u32x4>(L_QX + ((k >> 1) * 8 + w4 * 2 + (k & 1)) * 1024 + l16);
                const unsigned so = pgb(t - 1, par) + PG_HV + i * 3 * BLK + w4 * 2048;
#pragma unroll
                for (int k = 0; k < 6; ++k) st128(v[k], rp, l16 + (k & 1) * 1024, so + (k >> 1) * BLK);
                if (!all) {
                    wait_vm<0>();
                    if (l16 < 512) __builtin_amdgcn_raw_buffer_store_b32(t, rr, l16 >> 2, fp, 16);
                }
            }
            ST(1);
            if (t == NSEG) break;
            if (!all) bar();
            xdma(t, 1);
        } else {
            if (t == NSEG) break;
            if (!go) { if (L) xn_wait(P, rr, t, cpy); xdma(t, 0); wait_vm<0>(); }
            if (!all) bar();
        }
        ST(2);
        C1Regs C;
        const WRegs& W = wf;
        ASM_ST_DECL;
        if constexpr (HI) {
            const unsigned l4 = lane_id() * 4;
            asm volatile(ASM_XG_H_HI : XG_OUT ASM_ST_OUT : XG_IN, [rr] "s"(rr), [fp] "s"(fp), [tv] "s"(t), [fv] "v"((all && l4 < 128) ? l4 : OOB) : XG_CLOB);
        } else {
            asm volatile(ASM_XG_S_LO : XG_OUT ASM_ST_OUT : XG_IN : XG_CLOB);
            unsigned fv = NSEG;
            if (L) fv = __builtin_amdgcn_raw_buffer_load_b32(rr, (lane_id() & 15) * 128, RG_FX + (cpy & 3) * 2048, 16);
            go = __builtin_amdgcn_readfirstlane((t + 1 < NSEG && __all(fv > (unsigned)t + 1)) ? 1u : 0u);
            if (go) xdma(t + 1, 0);
            lds_st<unsigned>(L_MEM + wave * 4, go);
            wait_vm<0>();
        }
        ASM_ST_SAVE(3);
        ST(4);
        if (!HI) { LB(t); LA(); }
        bar();
        const u32x4 gv = lds_ld<u32x4>(L_MEM);
        all = __builtin_amdgcn_readfirstlane(gv[0] & gv[1] & gv[2] & gv[3]);
        ST(5);
        STFLUSH(t);
    }
    LF();
    };
    if (wave < 4) run(std::false_type{}); else run(std::true_type{});
}

__device__ __forceinline__ void v_role(const Params& P, int wave, int L, int par, int h, int cpy) {
    const rsrc_t rr = mkrsrc(P.ws + par * 8192, RG_L);
    WRegs wf;
    STDECL;
    auto runv = [&](auto Cc) {
        load_w3(wf, P.w[2], wave, h * 384, false);
        unsigned budget = 1u << 18;
        const unsigned wl = L_W + wave * 10240 + lane_id() * 16, dsto = c1_dst(wave, false);
        auto vfetch = [&](int seg, int tries) {
            const int ln = lane_id();
            for (int spins = 0;; ++spins) {
                if (spins == tries) return false;
                const unsigned f = __builtin_amdgcn_raw_buffer_load_b32(rr, (ln & 3) * 128, RG_FH + (h >> 1) * 512, 16);
                if (__all(f > (unsigned)seg)) break;
                __builtin_amdgcn_s_sleep(4);
                if (spin_fail(spins, P)) break;
            }
            asm volatile("" ::: "memory");
            const char* g = P.ws + PG0 + pgb(seg, par) + PG_HV + (h >> 1) * 3 * BLK;
#pragma unroll
            for (int k = 0; k < 3; ++k) {
                const int f = (h & 1) * 12 + wave * 3 + k;
                dmaq<0>(g + (f >> 3) * BLK + (f & 7) * 1024, ln * 16, L_QX + (seg & 1) * 12288 + (wave * 3 + k) * 1024);
            }
            return true;
        };
#pragma unroll 1
        for (int t = 0; t < HT0; ++t) {
            if (L) xn_wait(P, rr, t, cpy);
            const unsigned l16 = lane_id() * 16;
            const char* g = P.ws + PG0 + PG_XN + L * PG_LX + pgb(t, par);
            dma4(g + blin(wave * 4096), l16, L_XA + wave * 4096); dma4(g + blin(20480 + wave * 4096), l16, L_XA + 16384 + wave * 4096);
            dmaq<0>(g + blin(16384) + wave * 1024, l16, L_XB + wave * 1024); dmaq<0>(g + blin(20480 + 16384) + wave * 1024, l16, L_XB + 4096 + wave * 1024);
            wait_vm<0>();
            bar();
            C1Regs C;
            const WRegs& W = wf;
            const unsigned xa = L_XA + wave * 2048 + l16, qs = L_QX + (wave & 1) * 3072 + ((wave >> 1) & 1) * 8 + t * 12288;
            asm volatile(ASM_XG_V : XG_OUT : XG_IN : XG_CLOB);
            if (t == 0) bar();
        }
        bool have = true;
        for (int t = 0; t < NSEG; ++t) {
            if (!have && t + 1 < NSEG) { vfetch(t + 1, -1); wait_vm<0>(); }
            ST(7);
            bar();
            have = t + 2 < NSEG && vfetch(t + 2, VTRY);
            C1Regs C;
            asm volatile("ds_read_b128 v[200:203], %4\n\tds_read_b128 v[204:207], %4 offset:1024\n\tds_read_b128 v[228:231], %4 offset:2048\n\tds_read_b128 v[232:235], %4 offset:3072\n\t"
                         "v_mov_b64 v[224:225], 0\n\tv_mov_b64 v[226:227], 0\n\tv_mov_b64 v[244:245], 0\n\tv_mov_b64 v[246:247], 0\n\tv_mov_b64 v[248:249], 0\n\tv_mov_b64 v[250:251], 0\n\ts_waitcnt lgkmcnt(0)"
                         : XG_OUT : "v"(wl) : "memory");
            ST(6);
            __builtin_amdgcn_s_sleep(VSL);
            crit1<decltype(Cc)::value>(wf, C, rr, RG_XM + (t & (NSLOT - 1)) * XM_SLOT + cpy * XM_COPY, wave, wl, dsto, budget STARG);
            bar();
            ST(4);
            STC(5);
            STFLUSH(t);
        }
    };
    if (wave < 2) runv(std::integral_constant<int, 3>{});
    else if (wave < 4) runv(std::integral_constant<int, 2>{});
    else {
        const int w4 = wave - 4;
        const rsrc_t rd = mkrsrc(P.ws + PG0, PG_L);
        load_wo(wf, P.w[3], wave, h);
        bar();
        bar();
        for (int t = -1; t < NSEG; ++t) {
            const unsigned slot = t & (NSLOT - 1);
            bar();
            ST(7);
            if (t + 1 < NSEG) {
                const unsigned l16 = lane_id() * 16;
                const unsigned wl = L_W + wave * 10240 + l16, va = L_QX + ((t + 1) & 1) * 12288 + l16;
                unsigned so = (((t + 1) & 7) << 22) + pgb(64 + (w4 >> 1), par) + ((w4 & 1) * 40 + (h >> 3)) * BLK + (h & 7) * 1024;
                asm volatile(ASM_VOX : [so] "+s"(so) : WREGS(wf), [l16] "v"(l16), [wl] "v"(wl), [va] "v"(va), [rd] "s"(rd) : "memory", "scc", "v200", "v201", "v202", "v203", "v204", "v205", "v206", "v207", "v208", "v209", "v210", "v211", "v212", "v213", "v214", "v215", "v216", "v217", "v218", "v219", "v220", "v221", "v222", "v223", "v224", "v225", "v226", "v227", "v228", "v229", "v230", "v231", "v232", "v233", "v234", "v235", "v236", "v237", "v238", "v239", "v240", "v241", "v242", "v243", "v244", "v245", "v246", "v247", "v248", "v249", "v250", "v251");
            }
            if (t < 0) {
                wait_vm<0>();
                const unsigned l4 = lane_id() * 4;
                if (l4 < 128) __builtin_amdgcn_raw_buffer_store_b32(1, rr, l4, RG_FV + (w4 >> 1) * BLK + ((w4 & 1) * 32 + h) * 128, 16);
                continue;
            }
            ST(0);
            unsigned wl = L_W + wave * 10240 + lane_id() * 16;
            bf16x16 lw = lds_frag(wl, 1024);
            u32x32 cz;
            asm volatile("v_mov_b64 v[220:221], 0\n\tv_mov_b64 v[222:223], 0\n\tv_mov_b64 v[224:225], 0\n\tv_mov_b64 v[226:227], 0\n\tv_mov_b64 v[228:229], 0\n\tv_mov_b64 v[230:231], 0\n\tv_mov_b64 v[232:233], 0\n\tv_mov_b64 v[234:235], 0\n\tv_mov_b64 v[236:237], 0\n\tv_mov_b64 v[238:239], 0\n\tv_mov_b64 v[240:241], 0\n\tv_mov_b64 v[242:243], 0\n\tv_mov_b64 v[244:245], 0\n\tv_mov_b64 v[246:247], 0\n\tv_mov_b64 v[248:249], 0\n\tv_mov_b64 v[250:251], 0" : "={v[220:251]}"(cz));
            unsigned vo = ((wl >> 8) & 1) * 2 * BLK + ((wl >> 9) & 1) * 1024 + (wl & 255);
            asm volatile("" : "+{v[212:219]}"(lw), "+{v[220:251]}"(cz), "+v"(wl), "+v"(vo));
            unsigned l16 = wl & 1023u;
            bar();
            wait_vm<0>();
            if (l16 < 512 && t + 1 < NSEG) __builtin_amdgcn_raw_buffer_store_b32(t + 2, rr, l16 >> 2, RG_FV + (w4 >> 1) * BLK + ((w4 & 1) * 32 + h) * 128, 16);
            ST(1);
            const int sidx = (wl & 128u) ? 0xEEEE : 0x4444;
            unsigned so = RG_VM + slot * VM_SLOT + (unsigned)((w4 * 10 + (h >> 4)) * BLK + ((h >> 2) & 3) * 2048 + (h & 3) * 256);
            ST(3);
            ASM_ST_DECL;
            asm volatile(ASM_CRIT2 : [so] "+s"(so), "+{v[212:219]}"(lw), "+{v200}"(l16), "+{v[220:251]}"(cz) ASM_ST_OUT
                         : WREGS(wf), [wl] "v"(wl), [vo] "v"(vo), [sidx] "v"(sidx), [rr] "s"(rr)
                         : "memory", "scc", "v201", "v202", "v203", "v204", "v205", "v206", "v207", "v208", "v209", "v210", "v211");
            ASM_ST_SAVE(4);
            ST(5);
            STFLUSH(t);
        }
    }
}

constexpr unsigned R_RED = 0;
constexpr unsigned R_PART = 16384;
__device__ __forceinline__ void r_role(const Params& P, int wave, int L, int par, int jj, int cpy) {
    const int lane = lane_id(), l15 = lane & 15, l4 = lane >> 4;
    const unsigned l16 = lane * 16;
    const rsrc_t rr = mkrsrc(P.ws + par * 8192, RG_L), rd = mkrsrc(P.ws + PG0, PG_L);
    const int sidx = (l15 < 8) ? 0x4444 : 0xEEEE;
    unsigned offp[3];
    offp[0] = RG_XP + cpy * XP_COPY + BLK + (wave >> 2) * BLK + (wave & 3) * 2048 + l16;
    offp[2] = (l15 < 8) ? RG_XP + cpy * XP_COPY + wave * 512 + l4 * 128 + l15 * 16 : OOB;
    const unsigned offv = RG_VM + (jj * 2 + (wave >> 2)) * BLK + (wave & 3) * 2048 + l16;
    const int j = jj * 2 + ((wave >> 1) & 1), fl = (wave & 1) * 32 + (lane & 31);
    const unsigned offo = RG_XM + ((wave >> 2) * 2 + (lane >> 5)) * XM_COPY + j * 256 + (j >> 5) * 8192 + fl * 4;
    {
        const int row = (lane >> 2) & 7, col = 16 * j + 4 * (lane & 3) + 2 * (wave & 1);
        __builtin_amdgcn_raw_buffer_store_b32(nosent(*(const unsigned*)(P.mem0 + row * DM + col)), rr, offo, 0, 16);
    }
    auto red_addr = [](int fl) { return R_RED + fl * 80 + (fl >> 4) * 16; };
    const unsigned rw0 = (lane >> 5) * 5248 + red_addr((((l15 >> 1) & 1) * 8 + 4 * (l4 & 1)) * 4 + (l15 >> 2)) + (l15 & 1) * 4 + (wave ^ (l15 >> 3)) * 8;
    const unsigned rrd = ((wave >> 1) & 1) * 5248 + red_addr(fl) + (lane >> 5) * 32;
    u32x32 vx;
    const unsigned fo = (lane & 3) * 128, fs = RG_FV + (jj / 10) * BLK + (((jj / 5) & 1) * 32 + wave * 4) * 128, vxo = pgb(64 + jj / 10, par) + (unsigned)((jj % 10) * 8 + (wave >> 1)) * BLK + (wave & 1) * 4096;
    const rsrc_t rp = mkrsrc(P.ws + PG0, PG_L);
    const unsigned xo = (l4 >> 1) * 256 + l15 * 16 + (l4 & 1) * 8, pw = R_PART + wave * 1024 + l16, pr = R_PART + (wave & 3) * 8192 + l16;
    const int fin = wave >> 2;
    asm volatile("" : "={v[128:159]}"(vx));
    auto vx_fetch = [&](int t) {
        for (int spins = 0;; ++spins) {
            const unsigned f = __builtin_amdgcn_raw_buffer_load_b32(rr, fo, fs, 16);
            if (__all(f > (unsigned)t)) break;
            __builtin_amdgcn_s_sleep(2);
            if (spin_fail(spins, P)) break;
        }
        const unsigned sv = ((t & 7) << 22) + vxo;
        asm volatile("buffer_load_dwordx4 v[128:131], %1, %2, %3 offen sc1\n\tbuffer_load_dwordx4 v[132:135], %1, %2, %3 offen offset:1024 sc1\n\t"
                     "buffer_load_dwordx4 v[136:139], %1, %2, %3 offen offset:2048 sc1\n\tbuffer_load_dwordx4 v[140:143], %1, %2, %3 offen offset:3072 sc1\n\t"
                     "buffer_load_dwordx4 v[144:147], %1, %2, %4 offen sc1\n\tbuffer_load_dwordx4 v[148:151], %1, %2, %4 offen offset:1024 sc1\n\t"
                     "buffer_load_dwordx4 v[152:155], %1, %2, %4 offen offset:2048 sc1\n\tbuffer_load_dwordx4 v[156:159], %1, %2, %4 offen offset:3072 sc1"
                     : "+{v[128:159]}"(vx) : "v"(l16), "s"(rp), "s"(sv), "s"(sv + 4 * BLK) : "memory");
    };
    vx_fetch(0);
    unsigned budget = 1u << 18;
    STDECL;
    LDECL;
    for (int t = 0; t < NSEG; ++t) {
        const unsigned slot = t & (NSLOT - 1);
        ST(0);
        __builtin_amdgcn_s_sleep(RSL);
        u32x8 vm, pv;
        u32x4 pm;
        {
            unsigned a;
            ASM_ST_DECL;
            asm volatile(ASM_RCRIT : "=&{v[176:183]}"(vm), "=&{v[184:191]}"(pv), "=&{v[192:195]}"(pm), [a] "=&s"(a), [n] "+s"(budget) ASM_ST_OUT ASM_ST2_OUT ASM_ST3_OUT
                         : [rs] "s"(rr), [ov] "v"(offv), [sv] "s"(slot * VM_SLOT), [o0] "v"(offp[0]), [o2] "v"(offp[2]), [sp] "s"(slot * XP_SLOT),
                           "{v[128:159]}"(vx), [sidx] "v"(sidx), [rw] "v"(rw0), [rd] "v"(rrd), [oo] "v"(offo),
                           [so] "s"(((t + 1) & (NSLOT - 1)) * XM_SLOT), [pub] "s"(NSEG - 1 - t)
                         : "memory", "vcc", "scc", "v160", "v161", "v162", "v163", "v164", "v165", "v166", "v167", "v196", "v200", "v201", "v202", "v203", "v204", "v205", "v206", "v207");
            ASM_ST_SAVE(2); ASM_ST2_SAVE(1); ASM_ST3_SAVE(6);
        }
        ST(3);
        LA();
        __builtin_amdgcn_s_sleep(RXDEL);
        unsigned early;
#pragma unroll 1
        for (int spins = 1023;; spins += 1024) {
            unsigned n;
            const unsigned sx = pgb(t, par) + blin(((wave >> 1) & 1) * 20480 + jj * 1024) + (wave & 1) * 512;
            const unsigned pxb = RG_PX + slot * PX_SLOT + wave * BLK, sv = (((t + 1) & 7) << 22) + vxo;
            ASM_ST_DECL;
            asm volatile(ASM_RX : "+{v[128:159]}"(vx), [n] "=&s"(n), [a] "=&s"(early) ASM_ST_OUT ASM_ST2_OUT
                         : "{v[176:183]}"(vm), "{v[184:191]}"(pv), "{v[192:195]}"(pm), [l16] "v"(l16), [rs] "s"(rr), [s0] "s"(pxb), [s1] "s"(pxb + 4096), [rd] "s"(rd), [rx] "s"(rd), [xo] "v"(xo),
                           [fo] "v"(fo), [fs] "s"(fs), [tn] "s"(t + 1), [sv0] "s"(sv), [sv1] "s"(sv + 4 * BLK), [rp] "s"(rp), [sx] "s"(PG_XR + L * PG_LX + sx), [sa] "s"(PG_XA + sx),
                           [pw] "v"(pw), [pr] "v"(pr), [fin] "s"(fin)
                         : "memory", "vcc", "scc", "v96", "v97", "v98", "v99", "v100", "v101", "v102", "v103", "v104", "v105", "v106", "v107", "v108", "v109", "v110", "v111", "v112", "v113", "v114", "v115", "v116", "v117", "v118", "v119", "v120", "v121", "v122", "v123", "v124", "v125", "v126", "v127", "v196", "v200", "v201", "v202", "v203", "v204", "v205", "v206", "v207", "v208", "v209", "v210", "v211", "v212", "v213", "v214", "v215", "v216", "v217", "v218", "v219", "v220", "v221", "v222", "v223", "v224", "v225", "v226", "v227", "v228", "v229", "v230", "v231", "v240", "v241", "v242", "v243", "v248", "v250", "v251");
            ASM_ST_SAVE(4); ASM_ST2_SAVE(5);
            if (__builtin_amdgcn_readfirstlane(n)) break;
            early = 0;
            if (spin_fail(spins, P)) { bar(); break; }
        }
        if (t + 1 < NSEG && !__builtin_amdgcn_readfirstlane(early)) vx_fetch(t + 1);
        st128(sent4(), rr, offv, slot * VM_SLOT); st128(sent4(), rr, offv + 1024, slot * VM_SLOT);
        __builtin_amdgcn_raw_buffer_store_b32(SENT, rr, offo, slot * XM_SLOT, 16);
        if (wave < 2 && t + AH < NSEG) st128(sent4(), rd, l16, PG_XA + pgb(t + AH, par) + blin(wave * 20480 + jj * 1024));
        ST(7);
        LB(t);
        STFLUSH(t);
    }
    LF();
}

template <int WHICH>
__device__ __forceinline__ void w_role(const Params& P, int wave, int par, int cu) {
    constexpr int KS = 20, N = (WHICH == 3) ? FF : DM, NU = 2 * NSEG;
    constexpr unsigned D_IN = WHICH == 1 ? PG_XA : WHICH == 2 ? PG_HS : PG_X1;
    constexpr unsigned D_OUT = WHICH == 1 ? PG_HS : WHICH == 2 ? PG_X1 : PG_H2;
    if (WHICH != 3 && cu * 8 + wave >= 20) {
        for (int u = 0; u <= NU; ++u) bar();
        return;
    }
    auto run = [&](auto Cc) {
    constexpr bool LO = decltype(Cc)::value;
    const int ln = lane_id(), l15 = ln & 15, l4 = ln >> 4;
    const unsigned l16 = ln * 16;
    const rsrc_t rd = mkrsrc(P.ws + PG0, PG_L);
    const int nt0 = (cu * 8 + wave) * 2;
    u32x32 w[5];
    {
        u32x4 f[2 * KS];
        load_wn<2, KS, N>(f, P.w[3 + WHICH], DM, wave, nt0, 0);
#pragma unroll
        for (int n = 0; n < 5; ++n) {
            u32x8 q[4];
#pragma unroll
            for (int i = 0; i < 4; ++i) q[i] = __builtin_shufflevector(f[n * 8 + 2 * i], f[n * 8 + 2 * i + 1], 0, 1, 2, 3, 4, 5, 6, 7);
            w[n] = cat32(q[0], q[1], q[2], q[3]);
        }
        asm volatile("" : "+{v[0:31]}"(w[0]), "+{v[32:63]}"(w[1]), "+{v[64:95]}"(w[2]), "+{v[96:127]}"(w[3]), "+{v[128:159]}"(w[4]));
    }
    bar();
    const int ocol = nt0 * 16 + (l4 & 1) * 16 + (l4 >> 1) * 8;
    const unsigned o3 = (unsigned)((l4 & 1) * 512 + (l4 >> 1) * 256 + l15 * 16), ro = (l4 >> 1) * 256 + l15 * 16 + (l4 & 1) * 8;
    const int grp = nt0 / 10, sh = ((grp & 1) ? -1 : 1) * ((grp >> 1) + 1);
    if (WHICH == 1) {
        unsigned zv;
        asm volatile("v_mov_b32 %0, 0" : "=v"(zv));
        st128(((u32x4){zv, zv, zv, zv}), rd, l15 <= (grp >> 1) ? toff((grp & 1) ? S_ - 1 - l15 : l15, ocol, par) : OOB, PG_HS);
    }
    auto in0 = [&](int u) { return D_IN + pgb(u >> 1, par) + blin((u & 1) * 20480 + wave * 4096); };
    auto in1 = [&](int u) { return D_IN + pgb(u >> 1, par) + blin((u & 1) * 20480 + 16384) + wave * 1024; };
    u32x16 va;
    u32x4 vb;
    u32x8 cs, k8;
#define WT_W "{v[0:31]}"(w[0]), "{v[32:63]}"(w[1]), "{v[64:95]}"(w[2]), "{v[96:127]}"(w[3]), "{v[128:159]}"(w[4])
    asm volatile("v_mov_b32 v216, 0x3f803f80\n\tv_mov_b32 v217, v216\n\tv_mov_b32 v218, v216\n\tv_mov_b32 v219, v216\n\tv_mov_b32 v220, %[c0]\n\tv_mov_b32 v232, -1\n\tv_mov_b32 v236, -1\n\t"
                 "v_mov_b32 v240, -1\n\tv_mov_b32 v244, -1\n\tv_mov_b32 v248, -1\n\t" ASM_WCS
                 : "=&{v[196:203]}"(cs), "=&{v[216:223]}"(k8), "=&{v[232:247]}"(va), "=&{v[248:251]}"(vb) : WT_W, [c0] "s"(-2.0f * 0.7978845608028654f * 1.4426950408889634f));
    unsigned la = l16, lw = wave * 8192 + l16, sr = l4 * 8192 + ((l15 >> 2) * 16 + l15) * 16 + (l15 & 3) * 4, budget = 1u << 18;
    wait_vm<0>();
    STDECL;
    LDECL;
#pragma unroll 1
    for (int u = 0; u < NU; ++u) {
        const int mt = u & 1;
        const unsigned pg = pgb(u >> 1, par), c3 = D_OUT + blin(mt * (N * 32) + (nt0 >> 1) * 1024);
        unsigned oa = o3, ob = 0, so = pg + c3, sa = u + 2 * AH < NU ? pgb((u >> 1) + AH, par) + c3 : pg + PG_SS + 4096, a;
        if (WHICH == 1) {
            const int orow = u * 16 + l15 + sh;
            ob = toff(orow + AH * SEG, ocol, par);
            oa = (unsigned)orow < (unsigned)S_ ? (ob ^ 8192u) - (AH << 21) : OOB;
            if (orow + AH * SEG >= S_ + (sh < 0 ? sh : 0)) ob = OOB;
            so = sa = D_OUT;
        }
        if (mt) { ST(0); LB(u >> 1); } else LA();
        ASM_ST_DECL;
#define WT_A WT_W, [la] "v"(la), [l16] "v"(l16), [oa] "v"(oa), [rs] "s"(rd), [so] "s"(so), [sa] "s"(sa)
#define WT_G [sr] "v"(sr), [kd] "s"(1.0f / DM), [ke] "s"(1e-5f), [k1] "s"(-2.0f * 0.7978845608028654f * 1.4426950408889634f * 0.044715f)
#define WT_1 WT_G, [ob] "v"(ob), [ss] "s"(pg + PG_SS + mt * 128), [sw] "s"(nt0)
#define WT_2 [ro] "v"(ro), [sx] "s"(pg + PG_XA + blin(mt * 20480 + nt0 * 512)), [ss] "s"(pg + PG_SS + mt * 128)
#define WT_C "memory", "vcc", "scc", "v160", "v161", "v162", "v163", "v164", "v165", "v166", "v167", "v168", "v169", "v170", "v171", "v172", "v173", "v174", "v175", "v176", "v177", "v178", "v179", "v180", "v181", "v182", "v183", "v184", "v185", "v186", "v187", "v188", "v189", "v190", "v191", "v192", "v193", "v194", "v195", "v204", "v205", "v206", "v207", "v208", "v209", "v210", "v211", "v212", "v213", "v214", "v215", "v252"
#define WT_LO(A, ...) asm volatile(A : "+{v[196:203]}"(cs), "+{v[216:223]}"(k8), "+{v[232:247]}"(va), "+{v[248:251]}"(vb), [n] "+s"(budget), [a] "=&s"(a) ASM_ST_OUT ASM_ST2_OUT ASM_ST3_OUT \
                              : WT_A, __VA_ARGS__, [lw] "v"(lw), [i0] "s"(in0(u)), [i1] "s"(in1(u)), [n0] "s"(in0(u + 1)), [n1] "s"(in1(u + 1)) : WT_C)
#define WT_HI(A, ...) asm volatile(A : "+{v[196:203]}"(cs), "+{v[216:223]}"(k8) ASM_ST_OUT ASM_ST3_OUT : WT_A, __VA_ARGS__ : WT_C)
        if constexpr (LO) { if constexpr (WHICH == 1) WT_LO(ASM_WT1_LO, WT_1); else if constexpr (WHICH == 2) WT_LO(ASM_WT2_LO, WT_2); else WT_LO(ASM_WT3_LO, WT_G); }
        else { if constexpr (WHICH == 1) WT_HI(ASM_WT1_HI, WT_1); else if constexpr (WHICH == 2) WT_HI(ASM_WT2_HI, WT_2); else WT_HI(ASM_WT3_HI, WT_G); }
        la ^= 32768u; lw ^= 32768u; sr ^= 32768u;
    }
    wait_vm<0>();
    if constexpr (LO) asm volatile("" :: "{v[232:247]}"(va), "{v[248:251]}"(vb));
    LF();
    };
    if (wave < 4) run(std::true_type{}); else run(std::false_type{});
}

__device__ __forceinline__ void w4_role(const Params& P, int wave, int L, int par, int cu) {
    constexpr unsigned W_FL = 122880;
    const int ln = lane_id(), l15 = ln & 15, l4 = ln >> 4;
    const unsigned l16 = ln * 16;
    const rsrc_t rd = mkrsrc(P.ws + PG0, PG_L);
    lds_st<unsigned>(W_FL + wave * 4, 0u);
    STDECL;
    if (wave < 6) {
        u32x32 w[5];
        {
            u32x4 g[40];
            load_wn<5, 8, DM>(g, P.w[7], FF, wave, cu * 5, wave * 8);
#pragma unroll
            for (int n = 0; n < 5; ++n) {
                u32x8 f[4];
#pragma unroll
                for (int i = 0; i < 4; ++i) f[i] = __builtin_shufflevector(g[n * 8 + 2 * i], g[n * 8 + 2 * i + 1], 0, 1, 2, 3, 4, 5, 6, 7);
                w[n] = cat32(f[0], f[1], f[2], f[3]);
            }
            asm volatile("" : "+{v[0:31]}"(w[0]), "+{v[32:63]}"(w[1]), "+{v[64:95]}"(w[2]), "+{v[96:127]}"(w[3]), "+{v[128:159]}"(w[4]));
        }
        bar();
        unsigned budget = 1u << 18;
        for (int u = 0; u < 2 * NSEG; ++u) {
            if (u & 1) ST(0);
            for (int spins = 0;; ++spins) {
                asm volatile("" ::: "memory");
                if ((int)__builtin_amdgcn_readfirstlane(lds_ld<unsigned>(W_FL + 24 + (u & 1) * 4)) + 3 >= u) break;
                __builtin_amdgcn_s_sleep(2);
                if (spin_fail(spins, P)) break;
            }
            const unsigned gb = PG_H2 + pgb(u >> 1, par) + ((u & 1) * 6 + wave) * BLK;
            ASM_ST_DECL;
            asm volatile(ASM_W4_M : [n] "+s"(budget) ASM_ST2_OUT
                         : "{v[0:31]}"(w[0]), "{v[32:63]}"(w[1]), "{v[64:95]}"(w[2]), "{v[96:127]}"(w[3]), "{v[128:159]}"(w[4]),
                           [ld] "v"(l16 + wave * 1024 + (u & 3) * 30720), [l16] "v"(l16), [rs] "s"(rd), [s0] "s"(gb), [s1] "s"(gb + 4096), [un] "s"(u + 1), [fl] "v"(W_FL + wave * 4)
                         : "memory", "vcc", "scc", "v160", "v161", "v162", "v163", "v164", "v165", "v166", "v167", "v168", "v169", "v170", "v171", "v172", "v173", "v174", "v175", "v176", "v177", "v178", "v179", "v180", "v181", "v182", "v183", "v184", "v185", "v186", "v187", "v188", "v189", "v190", "v191", "v192", "v193", "v194", "v195", "v196", "v197", "v198", "v199", "v200", "v201", "v202", "v203", "v204", "v205", "v206", "v207", "v208", "v209", "v210", "v211", "v212");
            if (u & 1) { ASM_ST2_SAVE(1); ST(2); STFLUSH(u >> 1); }
        }
    } else {
        const int mt = wave - 6;
        bar();
        const rsrc_t rr = mkrsrc(P.ws + par * 8192, RG_L), ro = mkrsrc(P.out, ACT);
        const unsigned c0 = mt * 20480 + cu * 2560, o8 = (l4 >> 1) * 256 + l15 * 16 + (l4 & 1) * 8;
        const unsigned o16a = blin(c0 + (l4 & 1) * 512) + (l4 >> 1) * 256 + l15 * 16, o16b = blin(c0 + 1024 + (l4 & 1) * 512) + (l4 >> 1) * 256 + l15 * 16, o8c = blin(c0 + 2048) + o8;
        const unsigned q8 = l15 * (DM * 2) + l4 * 8, q16 = l15 * (DM * 2) + (l4 & 1) * 32 + (l4 >> 1) * 16;
        for (int t = 0; t < NSEG; ++t) {
            const unsigned sb = pgb(t, par), sn = (sb ^ 8192) + PG_LX;
            ST(0);
            f32x4 res[5];
            for (int spins = 0;; ++spins) {
                u32x2 r1[5], r2[5];
                unsigned m = 0;
#pragma unroll
                for (int n = 0; n < 5; ++n) { r1[n] = LD64(rd, o8 + blin(c0 + n * 512), PG_X1 + sb); r2[n] = LD64(rd, o8 + blin(c0 + n * 512), PG_XA + sb); }
#pragma unroll
                for (int n = 0; n < 5; ++n) {
                    m = umax4(m, (u32x4){r1[n][0], r1[n][1], r2[n][0], r2[n][1]});
                    res[n] = (f32x4){lo2f(r1[n][0]), hi2f(r1[n][0]), lo2f(r1[n][1]), hi2f(r1[n][1])} + (f32x4){lo2f(r2[n][0]), hi2f(r2[n][0]), lo2f(r2[n][1]), hi2f(r2[n][1])};
                }
                if (__all(m != SENT)) break;
                __builtin_amdgcn_s_sleep(8);
                if (spin_fail(spins * 4 + 3, P)) break;
            }
            asm volatile("" : "+v"(res[0]), "+v"(res[1]), "+v"(res[2]), "+v"(res[3]), "+v"(res[4]));
            ST(1);
            const unsigned u = t * 2 + mt;
            for (int spins = 0;; ++spins) {
                asm volatile("" ::: "memory");
                if (__all(lds_ld<unsigned>(W_FL + (ln < 6 ? ln : 0) * 4) > u)) break;
                __builtin_amdgcn_s_sleep(1);
                if (spin_fail(spins, P)) break;
            }
            asm volatile("" ::: "memory");
            ST(2);
            {
                const unsigned la = (u & 3) * 30720 + l16;
                f32x4 c[30];
#pragma unroll
                for (int i = 0; i < 30; ++i) c[i] = lds_ld<f32x4>(la + ((i % 5) * 6 + i / 5) * 1024);
#pragma unroll
                for (int i = 0; i < 30; ++i) { res[i % 5] += c[i]; asm volatile("" : "+v"(res[i % 5])); }
                asm volatile("" ::: "memory");
                lds_st<unsigned>(W_FL + wave * 4, u + 1);
            }
            u32x2 o[5];
#pragma unroll
            for (int n = 0; n < 5; ++n) o[n] = (u32x2){pack2(res[n][0], res[n][1]), pack2(res[n][2], res[n][3])};
            if (L) {
                const unsigned ob = (unsigned)(t * 2 + mt) * 20480 + cu * 160;
                st128_<0>(merge16(o[0], o[1]), ro, q16, ob); st128_<0>(merge16(o[2], o[3]), ro, q16, ob + 64);
                __builtin_amdgcn_raw_buffer_store_b64(o[4], ro, q8, ob + 128, 0);
            } else {
                float s = 0.f, q = 0.f;
                const bf16x2 one2 = __builtin_bit_cast(bf16x2, 0x3F803F80u);
#pragma unroll
                for (int e = 0; e < 10; ++e) {
                    const unsigned u = o[e >> 1][e & 1];
                    const bf16x2 x = __builtin_bit_cast(bf16x2, u);
                    s = __builtin_amdgcn_fdot2_f32_bf16(x, one2, s, false);
                    q = __builtin_amdgcn_fdot2_f32_bf16(x, x, q, false);
                }
                sum2_rows(s, q);
                const unsigned st = RG_ST + (unsigned)(t >> 2) * BLK + (unsigned)((t & 3) * 2 + mt) * 1024;
                __builtin_amdgcn_raw_buffer_store_b64((u32x2){nosent(__float_as_uint(s)), nosent(__float_as_uint(q))}, rr, ln < 16 ? ln * 8 : OOB, st + cu * 128, 16);
                ST(3);
                st128(merge16(o[0], o[1]), rd, o16a, PG_XR + sn); st128(merge16(o[2], o[3]), rd, o16b, PG_XR + sn);
                __builtin_amdgcn_raw_buffer_store_b64(o[4], rd, o8c, PG_XR + sn, 16);
                __builtin_amdgcn_s_sleep(W4SL);
                for (int spins = 0;; ++spins) {
                    const u32x2 a = LD64(rr, ln * 8, st), b = LD64(rr, ln * 8 + 512, st);
                    s = __uint_as_float(a[0]) + __uint_as_float(b[0]); q = __uint_as_float(a[1]) + __uint_as_float(b[1]);
                    if (__all(umax4(0u, (u32x4){a[0], a[1], b[0], b[1]}) != SENT)) break;
                    asm volatile("" ::: "memory");
                    if (spin_fail(spins, P)) break;
                }
                ST(4);
                sum2_rows(s, q);
                const float mean = s * (1.0f / DM), var = __builtin_fmaxf(q * (1.0f / DM) - mean * mean, 0.f);
                const float r = rcp(ln_den(var)), mr = -rbf(mean) * r;
#pragma unroll
                for (int n = 0; n < 5; ++n)
                    o[n] = (u32x2){pack2(__builtin_fmaf(res[n][0], r, mr), __builtin_fmaf(res[n][1], r, mr)), pack2(__builtin_fmaf(res[n][2], r, mr), __builtin_fmaf(res[n][3], r, mr))};
                st128(merge16(o[0], o[1]), rd, o16a, PG_XN + sn); st128(merge16(o[2], o[3]), rd, o16b, PG_XN + sn);
                __builtin_amdgcn_raw_buffer_store_b64(o[4], rd, o8c, PG_XN + sn, 16);
                wait_vm<0>();
                st128(((u32x4){(unsigned)t + 1, (unsigned)t + 1, (unsigned)t + 1, (unsigned)t + 1}), mkrsrc(P.ws + (par ^ 1) * 8192, RG_L), ln < 32 ? (ln >> 3) * 2048 + (ln & 7) * 16 : OOB, RG_FX + (cu * 2 + mt) * 128);
            }
            ST(5);
            STFLUSH(t);
        }
    }
}

__global__ __launch_bounds__(512, 2) void mega_kernel(Params P) {
    const int wave = __builtin_amdgcn_readfirstlane(threadIdx.x >> 6);
    if (threadIdx.x == 0) {
        const unsigned c0 = *(const unsigned*)(P.ws + FL0 + FL_BYTES);
        unsigned g = (unsigned)__builtin_amdgcn_s_getreg((3 << 11) | 20) & 7u, k;
        while ((k = __hip_atomic_fetch_add((unsigned*)(P.ws + FL0 + FL_TK + g * 128), 1u, RLX_AGENT)) >= 32u) g = (g + 1) & 7u;
        lds_st<unsigned>(0, (c0 & 1u) << 8 | g << 5 | k);
    }
    bar();
    const unsigned tk = __builtin_amdgcn_readfirstlane(lds_ld<unsigned>(0));
    bar();
    const int g4 = (tk >> 5) & 3, k = tk & 31;
#define MAP 0
    const int L = (tk >> 7) & 1;
    const int x = g4, r = k, cpy = x;
    const int par = L ^ (tk >> 8);
    {
        const int i = (r - 26) * 4 + x;
        const int d = L * DLY_L + (r < 8 ? DLY_S : r < 21 ? 0 : i < 4 ? DLY_H : DLY_W);
        for (int k = 0; k < d; ++k) __builtin_amdgcn_s_sleep(32);
    }
    if (r < 8) s_role(P, wave, L, par, x * 8 + r, cpy);
    else if (r < 16) v_role(P, wave ^ 4, L, par, x * 8 + r - 8, cpy);
    else if (r < 21) r_role(P, wave, L, par, (r - 16) * 4 + x, cpy);
    else {
        const int i = (r - 26) * 4 + x;
        if (i < 4) h_role(P, wave, L, par, r < 25 ? x * 4 + r - 21 : 16 + x * 2 + r - 25, cpy);
        else if (i < 7) w_role<1>(P, wave, par, i - 4);
        else if (i < 10) w_role<2>(P, wave, par, i - 7);
        else if (i < 16) w_role<3>(P, wave, par, i - 10);
        else w4_role(P, wave, L, par, i - 16);
    }
}

__device__ __forceinline__ void xtile_body(int mt, const bf16_t* __restrict__ x, char* __restrict__ ws) {
    __shared__ float red[2][16][17];
    const int r15 = threadIdx.x & 15, slot = threadIdx.x >> 4;
    u32x4 v[5];
    float s = 0.f;
#pragma unroll
    for (int c = 0; c < 5; ++c) {
        v[c] = *(const u32x4*)(x + (size_t)(mt * 16 + r15) * DM + (slot * 5 + c) * 8);
#pragma unroll
        for (int e = 0; e < 4; ++e) s += lo2f(v[c][e]) + hi2f(v[c][e]);
    }
    red[0][slot][r15] = s;
    __syncthreads();
    s = 0.f;
#pragma unroll
    for (int k = 0; k < 16; ++k) s += red[0][k][r15];
    const float mean = s * (1.0f / DM);
    float q = 0.f;
#pragma unroll
    for (int c = 0; c < 5; ++c)
#pragma unroll
        for (int e = 0; e < 4; ++e) { const float a = lo2f(v[c][e]) - mean, b = hi2f(v[c][e]) - mean; q += a * a + b * b; }
    red[1][slot][r15] = q;
    __syncthreads();
    q = 0.f;
#pragma unroll
    for (int k = 0; k < 16; ++k) q += red[1][k][r15];
    const float m1 = rbf(mean), r = 1.0f / ln_den(q * (1.0f / DM));
#pragma unroll
    for (int c = 0; c < 5; ++c) {
        const unsigned o = PG0 + toff(mt * 16 + r15, (slot * 5 + c) * 8, 0);
        *(u32x4*)(ws + PG_XR + o) = v[c]; *(u32x4*)(ws + PG_XR + (o ^ 8192)) = v[c];
        u32x4 n;
#pragma unroll
        for (int e = 0; e < 4; ++e) n[e] = pack2(rbf(lo2f(v[c][e]) - m1) * r, rbf(hi2f(v[c][e]) - m1) * r);
        *(u32x4*)(ws + PG_XN + o) = n; *(u32x4*)(ws + PG_XN + (o ^ 8192)) = n;
    }
}

struct Prep1 { const bf16_t* x; char* ws; };
__global__ __launch_bounds__(256) void prep1_kernel(Prep1 A) {
    int b = blockIdx.x;
    if (b == 0) {
        __shared__ unsigned best[4][2];
        const int w = threadIdx.x >> 6;
        const rsrc_t r = mkrsrc(A.ws, RG_SLOT);
        unsigned m[2] = {~0u, ~0u};
        for (int i = 0; i < CALN; ++i) {
            const unsigned long long t0 = __builtin_amdgcn_s_memtime();
            __builtin_amdgcn_raw_buffer_store_b32(0u, r, (96 + w * 8 + ((i >> 1) & 7)) * BLK + (i & 1) * 8192 + (i >> 1) * 768, 0, 16);
            asm volatile("s_waitcnt vmcnt(0)" ::: "memory");
            const unsigned d = (unsigned)(__builtin_amdgcn_s_memtime() - t0);
            if (i & 1) m[1] = d < m[1] ? d : m[1]; else m[0] = d < m[0] ? d : m[0];
        }
        best[w][0] = m[0]; best[w][1] = m[1];
        __syncthreads();
        if (threadIdx.x == 0) {
            const unsigned a = best[0][0] + best[1][0] + best[2][0] + best[3][0], c = best[0][1] + best[1][1] + best[2][1] + best[3][1];
            *(u32x4*)(A.ws + FL0 + FL_BYTES) = (u32x4){(a > c ? 1u : 0u) ^ (((unsigned)__builtin_amdgcn_s_getreg((3 << 11) | 20) >> 2) & 1u), a, c, 0u};
        }
        return;
    }
    b -= 1;
    if (b < 256) {
        for (unsigned i = b * 256 + threadIdx.x; i < (4 * RG_ST + 16 * BLK) / 16; i += 256 * 256) {
            const unsigned sl = i / (RG_ST / 16);
            ((u32x4*)A.ws)[sl < 4 ? i + sl * ((RG_SLOT - RG_ST) / 16) : i - 3 * (RG_ST / 16)] = (u32x4){SENT, SENT, SENT, SENT};
        }
        if (b < 20) ((u32x4*)(A.ws + RG_FV))[b * 256 + threadIdx.x] = (u32x4){0u, 0u, 0u, 0u};
        if (b == 0) for (unsigned i = threadIdx.x; i < FL_BYTES / 16; i += 256) ((u32x4*)(A.ws + FL0))[i] = (u32x4){0u, 0u, 0u, 0u};
        for (unsigned i = (b & 63) * 256 + threadIdx.x; i < PG_SS / 16; i += 64 * 256) ((u32x4*)(A.ws + PG0 + ((b >> 6) << 21)))[i] = (u32x4){SENT, SENT, SENT, SENT};
        return;
    }
    b -= 256;
    xtile_body(b, A.x, A.ws);
}

extern "C" void kernel_launch(void* const* d_in, const int* in_sizes, int n_in, void* d_out, int out_size, void* d_ws,
                              size_t ws_size, hipStream_t stream) {
    char* ws = (char*)d_ws;
    Params P;
    P.ws = ws; P.mem0 = (const bf16_t*)d_in[1]; P.out = (bf16_t*)d_out;
    for (int i = 0; i < 8; ++i) P.w[i] = (const bf16_t*)d_in[i < 4 ? 2 + i : 4 + i];
    (void)hipFuncSetAttribute((const void*)mega_kernel, hipFuncAttributeMaxDynamicSharedMemorySize, MEGA_LDS);

    Prep1 A1;
    A1.x = (const bf16_t*)d_in[0]; A1.ws = ws;
    prep1_kernel<<<1 + 256 + S_ / 16, 256, 0, stream>>>(A1);
    mega_kernel<<<256, 512, MEGA_LDS, stream>>>(P);
}
```

```cpp
#include <hip/hip_runtime.h>
#include <stdint.h>
#include <type_traits>

typedef unsigned short bf16_t;
using bf16x2 = __attribute__((ext_vector_type(2))) __bf16;
using bf16x8 = __attribute__((ext_vector_type(8))) __bf16;
using bf16x16 = __attribute__((ext_vector_type(16))) __bf16;
using f32x2 = __attribute__((ext_vector_type(2))) float;
using f32x4 = __attribute__((ext_vector_type(4))) float;
using u32x2 = __attribute__((ext_vector_type(2))) unsigned;
using u32x4 = __attribute__((ext_vector_type(4))) unsigned;
typedef __amdgpu_buffer_rsrc_t rsrc_t;

__device__ __forceinline__ unsigned pack2(float a, float b) {
    bf16x2 r = __builtin_convertvector((f32x2){a, b}, bf16x2);
    return __builtin_bit_cast(unsigned, r);
}
__device__ __forceinline__ float rbf(float f) { return __uint_as_float(pack2(f, 0.f) << 16); }
__device__ __forceinline__ float lo2f(unsigned u) { return __uint_as_float(u << 16); }
__device__ __forceinline__ float hi2f(unsigned u) { return __uint_as_float(u & 0xFFFF0000u); }
__device__ __forceinline__ rsrc_t mkrsrc(const void* p, unsigned bytes) {
    return __builtin_amdgcn_make_buffer_rsrc((void*)p, 0, bytes, 0x00020000);
}

constexpr int S_ = 2048, DM = 640, NH = 32, DH = 192, HD = NH * DH  , SEG = 32, NSEG = 64;
constexpr int FF = 1536;

#define XMAP
#define NCPY 4
constexpr unsigned NSLOT = 4, NCP = NCPY;
#define FL_TK_DEF
#define DLY_L 12
#define DLY_S 1
#define DLY_W 10
#define DLY_H 2
#define EWAKE "s_nop 0"
#define LWAKE "s_wakeup\n\t"
#define SLP4 (wave == 4 ? t : 0)
#define WPF 12
#define WPRI 1
#define WPRI2 0
#define RTD 3500
#define VTD 4000
#define CALN 12
#define RSL 9
#define VTRY 1
#define VSL 1
#define NRR 5
constexpr unsigned BLK = 16384, RG_SLOT = 4u << 20;
constexpr unsigned XP_PAIR = 6 * BLK, XP_SLOT = RG_SLOT;
constexpr unsigned XM_PAIR = 4 * BLK, XM_SLOT = RG_SLOT;
constexpr unsigned VM_SLOT = RG_SLOT;
constexpr unsigned PX_SLOT = RG_SLOT;
constexpr unsigned RG_XP = 0, RG_XM = 12 * BLK, RG_VM = 20 * BLK, RG_PX = 60 * BLK;
constexpr unsigned RG_ST = 68 * BLK, RG_FV = 84 * BLK, RG_FH = 86 * BLK, RG_FX = 88 * BLK, RG_SG = 89 * BLK, RG_L = NSLOT * RG_SLOT;
constexpr unsigned FL0 = 2u << 20, FL_TK = 512, FL_IN = FL_TK + 8 * 128, FL_BYTES = FL_IN + 16 * 128;
constexpr unsigned ACT = S_ * DM * 2;
#define PXROT 5
#define W4SL 6
#define RXDEL 1
#define VXD 32
#define VXAUX 16
constexpr unsigned PG0 = RG_L, PG_L = 80u << 21;
constexpr unsigned PG_XA = 0, PG_HS = 5 * BLK, PG_X1 = 10 * BLK, PG_H2 = 15 * BLK, PG_SS = 27 * BLK, PG_XR = 28 * BLK, PG_XN = 33 * BLK, PG_LX = 10 * BLK, PG_HV = 48 * BLK;
__device__ __forceinline__ unsigned pgb(unsigned p, int par) { return p << 21 | ((p ^ par) & 1) << 13; }
__device__ __forceinline__ constexpr unsigned blin(unsigned o) { return o + (o & ~8191u); }
constexpr int HT0 = 2;
constexpr int AH = 3;
constexpr unsigned WS_END = PG0 + PG_L;

#define SB() __builtin_amdgcn_sched_barrier(0)
#define LAS __attribute__((address_space(3)))
template <class T> __device__ __forceinline__ T lds_ld(unsigned a) { return *(const LAS T*)(a); }
template <class T> __device__ __forceinline__ void lds_st(unsigned a, T v) { *(LAS T*)(a) = v; }
constexpr unsigned OOB = 0x7FFFFF00u;
template <int N> __device__ __forceinline__ void wait_vm() { asm volatile("s_waitcnt vmcnt(%0)" :: "i"(N) : "memory"); }
__device__ __forceinline__ void bar() {
    asm volatile("s_waitcnt lgkmcnt(0)" ::: "memory");
    __builtin_amdgcn_s_barrier();
    asm volatile("" ::: "memory");
}
__device__ __forceinline__ int lane_id() {
    int ln;
    asm volatile("v_mbcnt_lo_u32_b32 %0, -1, 0\n\tv_mbcnt_hi_u32_b32 %0, -1, %0" : "=v"(ln));
    return ln;
}
__device__ __forceinline__ void swap16(unsigned& a, unsigned& b) { asm volatile("s_nop 3\n\tv_permlane16_swap_b32 %0, %1\n\ts_nop 1" : "+v"(a), "+v"(b)); }
__device__ __forceinline__ void swap32(unsigned& a, unsigned& b) { asm volatile("s_nop 3\n\tv_permlane32_swap_b32 %0, %1\n\ts_nop 1" : "+v"(a), "+v"(b)); }
__device__ __forceinline__ float sum_x16(float x) { unsigned a = __float_as_uint(x), b = a; swap16(a, b); return __uint_as_float(a) + __uint_as_float(b); }
__device__ __forceinline__ float sum_x32(float x) { unsigned a = __float_as_uint(x), b = a; swap32(a, b); return __uint_as_float(a) + __uint_as_float(b); }
__device__ __forceinline__ float max_x16(float x) { unsigned a = __float_as_uint(x), b = a; swap16(a, b); return __builtin_fmaxf(__uint_as_float(a), __uint_as_float(b)); }
__device__ __forceinline__ float max_x32(float x) { unsigned a = __float_as_uint(x), b = a; swap32(a, b); return __builtin_fmaxf(__uint_as_float(a), __uint_as_float(b)); }
__device__ __forceinline__ void sum2_rows(float& s, float& q) {
    unsigned a = __float_as_uint(s), b = a, c = __float_as_uint(q), d = c;
    asm volatile("s_nop 2\n\tv_permlane16_swap_b32 %0, %1\n\tv_permlane16_swap_b32 %2, %3\n\tv_add_f32 %0, %0, %1\n\tv_add_f32 %2, %2, %3\n\tv_mov_b32 %1, %0\n\tv_mov_b32 %3, %2\n\t"
                 "s_nop 1\n\tv_permlane32_swap_b32 %0, %1\n\tv_permlane32_swap_b32 %2, %3\n\tv_add_f32 %0, %0, %1\n\tv_add_f32 %2, %2, %3" : "+v"(a), "+v"(b), "+v"(c), "+v"(d));
    s = __uint_as_float(a); q = __uint_as_float(c);
}
__device__ __forceinline__ u32x4 merge16(u32x2 a, u32x2 b) {
    unsigned a0 = a[0], a1 = a[1], b0 = b[0], b1 = b[1];
    asm volatile("s_nop 3\n\tv_permlane16_swap_b32 %0, %2\n\tv_permlane16_swap_b32 %1, %3\n\ts_nop 1" : "+v"(a0), "+v"(a1), "+v"(b0), "+v"(b1));
    return (u32x4){a0, a1, b0, b1};
}
template <int AUXV>
__device__ __forceinline__ void st128_(u32x4 v, rsrc_t r, unsigned voff, unsigned soff) {
    __builtin_amdgcn_raw_buffer_store_b128(v, r, voff, soff, AUXV);
    asm volatile("s_nop 1" :: "v"(v) : "memory");
}
#define st128(V, R, VO_, SO_) st128_<16>(V, R, VO_, SO_)
#define LD128(R, VO_, SO_) __builtin_amdgcn_raw_buffer_load_b128(R, VO_, SO_, 16)
#define LD64(R, VO_, SO_) __builtin_amdgcn_raw_buffer_load_b64(R, VO_, SO_, 16)
#define BF8(X) __builtin_bit_cast(bf16x8, X)
__device__ __forceinline__ float gelu_tanh(float x) {
    const float c0 = -2.0f * 0.7978845608028654f * 1.4426950408889634f, c1 = c0 * 0.044715f;
    const float e = __builtin_amdgcn_exp2f(x * __builtin_fmaf(x * x, c1, c0));
    return x * __builtin_amdgcn_rcpf(1.0f + e);
}
#define MFMA(A_, B_, C_) C_ = __builtin_amdgcn_mfma_f32_16x16x32_bf16(A_, B_, C_, 0, 0, 0)
#define SMF(A_, B_, C_) C_ = __builtin_amdgcn_smfmac_f32_16x16x64_bf16(A_, B_, C_, sidx, 0, 0)
__device__ __forceinline__ float ln_den(float var) { return rbf(__builtin_amdgcn_sqrtf(rbf(rbf(var) + 1e-5f))); }
__device__ __forceinline__ float rcp(float x) { return __builtin_amdgcn_rcpf(x); }

struct Params {
    char* ws;
    const bf16_t* x;
    const bf16_t* mem0;
    const bf16_t* w[8];
    bf16_t* out;
};

#define RLX_AGENT __ATOMIC_RELAXED, __HIP_MEMORY_SCOPE_AGENT
#define SENT 0xFFFFFFFFu
constexpr int SPIN_CAP = 400000;
__device__ __forceinline__ bool spin_fail(int spins, const Params& P) {
    if ((spins & 1023) == 1023) {
        const rsrc_t rt = mkrsrc(P.ws + FL0, FL_BYTES);
        unsigned o;
        asm volatile("v_mov_b32 %0, 0x100" : "=v"(o));
        if (__builtin_amdgcn_readfirstlane(__builtin_amdgcn_raw_buffer_load_b32(rt, o, 0, 16)) != 0u) return true;
        if (spins > SPIN_CAP) { __builtin_amdgcn_raw_buffer_store_b32(o, rt, o, 0, 16); return true; }
    }
    return false;
}
__device__ __forceinline__ bool ok4(u32x4 v) { return v[0] != SENT && v[1] != SENT && v[2] != SENT && v[3] != SENT; }
__device__ __forceinline__ unsigned nosent(unsigned d) { return d < 0xFFFFFFFEu ? d : 0xFFFFFFFEu; }
__device__ __forceinline__ u32x4 nosent4(u32x4 v) { return (u32x4){nosent(v[0]), nosent(v[1]), nosent(v[2]), nosent(v[3])}; }
__device__ __forceinline__ unsigned umax3(unsigned a, unsigned b, unsigned c) { const unsigned m = a > b ? a : b; return m > c ? m : c; }
__device__ __forceinline__ unsigned umax4(unsigned m, u32x4 v) { return umax3(umax3(m, v[0], v[1]), v[2], v[3]); }
__device__ __forceinline__ u32x4 sent4() { unsigned sv; asm volatile("v_mov_b32 %0, -1" : "=v"(sv)); return (u32x4){sv, sv, sv, sv}; }
template <int N>
__device__ __forceinline__ void poll(rsrc_t rs, unsigned base, const unsigned (&off)[N], u32x4 (&v)[N], const Params& P) {
    for (int spins = 0;; ++spins) {
        unsigned m = 0;
#pragma unroll
        for (int k = 0; k < N; ++k) v[k] = LD128(rs, off[k], base);
#pragma unroll
        for (int k = 0; k < N; ++k) m = umax4(m, v[k]);
        if (__all(m != SENT)) return;
        asm volatile("" ::: "memory");
        if (spin_fail(spins, P)) return;
    }
}
__device__ __forceinline__ bf16x16 cat16(bf16x8 a, bf16x8 b) {
    return __builtin_shufflevector(a, b, 0, 1, 2, 3, 4, 5, 6, 7, 8, 9, 10, 11, 12, 13, 14, 15);
}
__device__ __forceinline__ bf16x8 half8(bf16x16 v, int h) {
    return h ? __builtin_shufflevector(v, v, 8, 9, 10, 11, 12, 13, 14, 15) : __builtin_shufflevector(v, v, 0, 1, 2, 3, 4, 5, 6, 7);
}
__device__ __forceinline__ bf16x16 lds_frag(unsigned a, unsigned step) { return cat16(lds_ld<bf16x8>(a), lds_ld<bf16x8>(a + step)); }
template <int I>
__device__ __forceinline__ void dmaq(const char* g, unsigned l16, unsigned dst) {
    asm volatile("s_mov_b32 m0, %2\n\ts_nop 0\n\tglobal_load_lds_dwordx4 %0, %1 offset:%3 sc1" :: "v"(l16), "s"(g), "s"(dst), "i"(I * 1024) : "memory");
}
__device__ __forceinline__ void dma4(const char* g, unsigned l16, unsigned dst) {
    asm volatile("s_mov_b32 m0, %2\n\ts_nop 0\n\tglobal_load_lds_dwordx4 %0, %1 sc1\n\tglobal_load_lds_dwordx4 %0, %1 offset:1024 sc1\n\t"
                 "global_load_lds_dwordx4 %0, %1 offset:2048 sc1\n\tglobal_load_lds_dwordx4 %0, %1 offset:3072 sc1" :: "v"(l16), "s"(g), "s"(dst) : "memory");
}
__device__ __forceinline__ unsigned toff(int row, int col, int par) {
    return pgb(row >> 5, par) + blin((unsigned)((row >> 4) & 1) * 20480u + (unsigned)(col >> 3) * 256u) + (unsigned)(row & 15) * 16u;
}

#define ASM_ST
#define ASM_ST_DECL
#define ASM_ST_OUT
#define ASM_ST_SAVE(k)
#define ASM_ST2
#define ASM_ST3
#define ASM_ST3_OUT
#define ASM_ST3_SAVE(k)
#define ASM_ST2_OUT
#define ASM_ST2_SAVE(k)
#define STDECL
#define ST(k)
#define STC(k)
#define STFLUSH(t)

#define MARK(c, k)
#define MARKX(c, k)
#define LDECL
#define LA()
#define LB(t)
#define LF()
constexpr unsigned L_MEM = 0;
constexpr unsigned L_Q = 10240;
constexpr unsigned L_KM = 13312;
constexpr unsigned L_QX = 16384;
constexpr unsigned L_KX = 28672;
constexpr unsigned L_XB = 40960;
constexpr unsigned L_XA = 49152;
constexpr unsigned L_W = 81920;
constexpr int MEGA_LDS = 163840;
constexpr int wi(int i, int j) { return (i >> 1) * 5 + ((i & 1) ? 2 + j : j - 1); }

#define RPUB2
#define WNT ""
#define PLAY 0
#define ASM_WLOAD3 \
    "s_xor_b32 %[t], %[wx], 0\n\t" \
    "s_mul_i32 %[t], %[t], 0xc0000\n\t" \
    "buffer_load_dwordx4 v[200:203], %[o0], %[rs], %[t] offen" WNT "\n\t" \
    "buffer_load_dwordx4 v[0:3], %[o1], %[rs], %[t] offen" WNT "\n\t" \
    "buffer_load_dwordx4 v[8:11], %[o2], %[rs], %[t] offen" WNT "\n\t" \
    "s_add_u32 %[t], %[t], 0x18000\n\t" \
    "buffer_load_dwordx4 v[16:19], %[o0], %[rs], %[t] offen" WNT "\n\t" \
    "buffer_load_dwordx4 v[24:27], %[o1], %[rs], %[t] offen" WNT "\n\t" \
    "buffer_load_dwordx4 v[32:35], %[o2], %[rs], %[t] offen" WNT "\n\t" \
    "s_add_u32 %[t], %[t], 0x18000\n\t" \
    "buffer_load_dwordx4 v[208:211], %[o0], %[rs], %[t] offen" WNT "\n\t" \
    "buffer_load_dwordx4 v[40:43], %[o1], %[rs], %[t] offen" WNT "\n\t" \
    "buffer_load_dwordx4 v[48:51], %[o2], %[rs], %[t] offen" WNT "\n\t" \
    "s_add_u32 %[t], %[t], 0x18000\n\t" \
    "buffer_load_dwordx4 v[56:59], %[o0], %[rs], %[t] offen" WNT "\n\t" \
    "buffer_load_dwordx4 v[64:67], %[o1], %[rs], %[t] offen" WNT "\n\t" \
    "buffer_load_dwordx4 v[72:75], %[o2], %[rs], %[t] offen" WNT "\n\t" \
    "s_add_u32 %[t], %[t], 0x18000\n\t" \
    "buffer_load_dwordx4 v[204:207], %[o0], %[rs], %[t] offen" WNT "\n\t" \
    "buffer_load_dwordx4 v[4:7], %[o1], %[rs], %[t] offen" WNT "\n\t" \
    "buffer_load_dwordx4 v[12:15], %[o2], %[rs], %[t] offen" WNT "\n\t" \
    "s_add_u32 %[t], %[t], 0x18000\n\t" \
    "buffer_load_dwordx4 v[20:23], %[o0], %[rs], %[t] offen" WNT "\n\t" \
    "buffer_load_dwordx4 v[28:31], %[o1], %[rs], %[t] offen" WNT "\n\t" \
    "buffer_load_dwordx4 v[36:39], %[o2], %[rs], %[t] offen" WNT "\n\t" \
    "s_add_u32 %[t], %[t], 0x18000\n\t" \
    "buffer_load_dwordx4 v[212:215], %[o0], %[rs], %[t] offen" WNT "\n\t" \
    "buffer_load_dwordx4 v[44:47], %[o1], %[rs], %[t] offen" WNT "\n\t" \
    "buffer_load_dwordx4 v[52:55], %[o2], %[rs], %[t] offen" WNT "\n\t" \
    "s_add_u32 %[t], %[t], 0x18000\n\t" \
    "buffer_load_dwordx4 v[60:63], %[o0], %[rs], %[t] offen" WNT "\n\t" \
    "buffer_load_dwordx4 v[68:71], %[o1], %[rs], %[t] offen" WNT "\n\t" \
    "buffer_load_dwordx4 v[76:79], %[o2], %[rs], %[t] offen" WNT "\n\t" \
    "s_add_u32 %[t], %[t], 0x18000\n\t" \
    "s_waitcnt vmcnt(21)\n\t" \
    "ds_write_b128 %[s0], v[200:203] offset:0\n\t" \
    "ds_write_b128 %[s1], v[0:3] offset:0\n\t" \
    "ds_write_b128 %[s2], v[8:11] offset:0\n\t" \
    "s_waitcnt vmcnt(18)\n\t" \
    "ds_write_b128 %[s0], v[16:19] offset:3456\n\t" \
    "ds_write_b128 %[s1], v[24:27] offset:3456\n\t" \
    "ds_write_b128 %[s2], v[32:35] offset:3456\n\t" \
    "s_waitcnt vmcnt(15)\n\t" \
    "ds_write_b128 %[s0], v[208:211] offset:6912\n\t" \
    "ds_write_b128 %[s1], v[40:43] offset:6912\n\t" \
    "ds_write_b128 %[s2], v[48:51] offset:6912\n\t" \
    "s_waitcnt vmcnt(12)\n\t" \
    "ds_write_b128 %[s0], v[56:59] offset:10368\n\t" \
    "ds_write_b128 %[s1], v[64:67] offset:10368\n\t" \
    "ds_write_b128 %[s2], v[72:75] offset:10368\n\t" \
    "s_waitcnt lgkmcnt(0)\n\t" \
    "s_barrier \n\t" \
    "ds_read_b64_tr_b16 v[200:201], %[t0] offset:0\n\t" \
    "ds_read_b64_tr_b16 v[202:203], %[t0] offset:1664\n\t" \
    "ds_read_b64_tr_b16 v[0:1], %[t0] offset:128\n\t" \
    "ds_read_b64_tr_b16 v[2:3], %[t0] offset:1792\n\t" \
    "ds_read_b64_tr_b16 v[8:9], %[t0] offset:256\n\t" \
    "ds_read_b64_tr_b16 v[10:11], %[t0] offset:1920\n\t" \
    "ds_read_b64_tr_b16 v[16:17], %[t1] offset:0\n\t" \
    "ds_read_b64_tr_b16 v[18:19], %[t1] offset:1664\n\t" \
    "ds_read_b64_tr_b16 v[24:25], %[t1] offset:128\n\t" \
    "ds_read_b64_tr_b16 v[26:27], %[t1] offset:1792\n\t" \
    "ds_read_b64_tr_b16 v[32:33], %[t1] offset:256\n\t" \
    "ds_read_b64_tr_b16 v[34:35], %[t1] offset:1920\n\t" \
    "ds_read_b64_tr_b16 v[208:209], %[t2] offset:0\n\t" \
    "ds_read_b64_tr_b16 v[210:211], %[t2] offset:1664\n\t" \
    "ds_read_b64_tr_b16 v[40:41], %[t2] offset:128\n\t" \
    "ds_read_b64_tr_b16 v[42:43], %[t2] offset:1792\n\t" \
    "ds_read_b64_tr_b16 v[48:49], %[t2] offset:256\n\t" \
    "ds_read_b64_tr_b16 v[50:51], %[t2] offset:1920\n\t" \
    "ds_read_b64_tr_b16 v[56:57], %[t3] offset:0\n\t" \
    "ds_read_b64_tr_b16 v[58:59], %[t3] offset:1664\n\t" \
    "ds_read_b64_tr_b16 v[64:65], %[t3] offset:128\n\t" \
    "ds_read_b64_tr_b16 v[66:67], %[t3] offset:1792\n\t" \
    "ds_read_b64_tr_b16 v[72:73], %[t3] offset:256\n\t" \
    "ds_read_b64_tr_b16 v[74:75], %[t3] offset:1920\n\t" \
    "s_xor_b32 %[t], %[wx], 4\n\t" \
    "s_mul_i32 %[t], %[t], 0xc0000\n\t" \
    "buffer_load_dwordx4 v[216:219], %[o0], %[rs], %[t] offen" WNT "\n\t" \
    "buffer_load_dwordx4 v[80:83], %[o1], %[rs], %[t] offen" WNT "\n\t" \
    "buffer_load_dwordx4 v[88:91], %[o2], %[rs], %[t] offen" WNT "\n\t" \
    "s_add_u32 %[t], %[t], 0x18000\n\t" \
    "buffer_load_dwordx4 v[96:99], %[o0], %[rs], %[t] offen" WNT "\n\t" \
    "buffer_load_dwordx4 v[104:107], %[o1], %[rs], %[t] offen" WNT "\n\t" \
    "buffer_load_dwordx4 v[112:115], %[o2], %[rs], %[t] offen" WNT "\n\t" \
    "s_add_u32 %[t], %[t], 0x18000\n\t" \
    "buffer_load_dwordx4 v[224:227], %[o0], %[rs], %[t] offen" WNT "\n\t" \
    "buffer_load_dwordx4 v[120:123], %[o1], %[rs], %[t] offen" WNT "\n\t" \
    "buffer_load_dwordx4 v[128:131], %[o2], %[rs], %[t] offen" WNT "\n\t" \
    "s_add_u32 %[t], %[t], 0x18000\n\t" \
    "buffer_load_dwordx4 v[136:139], %[o0], %[rs], %[t] offen" WNT "\n\t" \
    "buffer_load_dwordx4 v[144:147], %[o1], %[rs], %[t] offen" WNT "\n\t" \
    "buffer_load_dwordx4 v[152:155], %[o2], %[rs], %[t] offen" WNT "\n\t" \
    "s_add_u32 %[t], %[t], 0x18000\n\t" \
    "s_waitcnt lgkmcnt(0)\n\t" \
    "s_barrier \n\t" \
    "s_waitcnt vmcnt(21)\n\t" \
    "ds_write_b128 %[s0], v[204:207] offset:0\n\t" \
    "ds_write_b128 %[s1], v[4:7] offset:0\n\t" \
    "ds_write_b128 %[s2], v[12:15] offset:0\n\t" \
    "s_waitcnt vmcnt(18)\n\t" \
    "ds_write_b128 %[s0], v[20:23] offset:3456\n\t" \
    "ds_write_b128 %[s1], v[28:31] offset:3456\n\t" \
    "ds_write_b128 %[s2], v[36:39] offset:3456\n\t" \
    "s_waitcnt vmcnt(15)\n\t" \
    "ds_write_b128 %[s0], v[212:215] offset:6912\n\t" \
    "ds_write_b128 %[s1], v[44:47] offset:6912\n\t" \
    "ds_write_b128 %[s2], v[52:55] offset:6912\n\t" \
    "s_waitcnt vmcnt(12)\n\t" \
    "ds_write_b128 %[s0], v[60:63] offset:10368\n\t" \
    "ds_write_b128 %[s1], v[68:71] offset:10368\n\t" \
    "ds_write_b128 %[s2], v[76:79] offset:10368\n\t" \
    "s_waitcnt lgkmcnt(0)\n\t" \
    "s_barrier \n\t" \
    "ds_read_b64_tr_b16 v[204:205], %[t0] offset:0\n\t" \
    "ds_read_b64_tr_b16 v[206:207], %[t0] offset:1664\n\t" \
    "ds_read_b64_tr_b16 v[4:5], %[t0] offset:128\n\t" \
    "ds_read_b64_tr_b16 v[6:7], %[t0] offset:1792\n\t" \
    "ds_read_b64_tr_b16 v[12:13], %[t0] offset:256\n\t" \
    "ds_read_b64_tr_b16 v[14:15], %[t0] offset:1920\n\t" \
    "ds_read_b64_tr_b16 v[20:21], %[t1] offset:0\n\t" \
    "ds_read_b64_tr_b16 v[22:23], %[t1] offset:1664\n\t" \
    "ds_read_b64_tr_b16 v[28:29], %[t1] offset:128\n\t" \
    "ds_read_b64_tr_b16 v[30:31], %[t1] offset:1792\n\t" \
    "ds_read_b64_tr_b16 v[36:37], %[t1] offset:256\n\t" \
    "ds_read_b64_tr_b16 v[38:39], %[t1] offset:1920\n\t" \
    "ds_read_b64_tr_b16 v[212:213], %[t2] offset:0\n\t" \
    "ds_read_b64_tr_b16 v[214:215], %[t2] offset:1664\n\t" \
    "ds_read_b64_tr_b16 v[44:45], %[t2] offset:128\n\t" \
    "ds_read_b64_tr_b16 v[46:47], %[t2] offset:1792\n\t" \
    "ds_read_b64_tr_b16 v[52:53], %[t2] offset:256\n\t" \
    "ds_read_b64_tr_b16 v[54:55], %[t2] offset:1920\n\t" \
    "ds_read_b64_tr_b16 v[60:61], %[t3] offset:0\n\t" \
    "ds_read_b64_tr_b16 v[62:63], %[t3] offset:1664\n\t" \
    "ds_read_b64_tr_b16 v[68:69], %[t3] offset:128\n\t" \
    "ds_read_b64_tr_b16 v[70:71], %[t3] offset:1792\n\t" \
    "ds_read_b64_tr_b16 v[76:77], %[t3] offset:256\n\t" \
    "ds_read_b64_tr_b16 v[78:79], %[t3] offset:1920\n\t" \
    "buffer_load_dwordx4 v[220:223], %[o0], %[rs], %[t] offen" WNT "\n\t" \
    "buffer_load_dwordx4 v[84:87], %[o1], %[rs], %[t] offen" WNT "\n\t" \
    "buffer_load_dwordx4 v[92:95], %[o2], %[rs], %[t] offen" WNT "\n\t" \
    "s_add_u32 %[t], %[t], 0x18000\n\t" \
    "buffer_load_dwordx4 v[100:103], %[o0], %[rs], %[t] offen" WNT "\n\t" \
    "buffer_load_dwordx4 v[108:111], %[o1], %[rs], %[t] offen" WNT "\n\t" \
    "buffer_load_dwordx4 v[116:119], %[o2], %[rs], %[t] offen" WNT "\n\t" \
    "s_add_u32 %[t], %[t], 0x18000\n\t" \
    "buffer_load_dwordx4 v[228:231], %[o0], %[rs], %[t] offen" WNT "\n\t" \
    "buffer_load_dwordx4 v[124:127], %[o1], %[rs], %[t] offen" WNT "\n\t" \
    "buffer_load_dwordx4 v[132:135], %[o2], %[rs], %[t] offen" WNT "\n\t" \
    "s_add_u32 %[t], %[t], 0x18000\n\t" \
    "buffer_load_dwordx4 v[140:143], %[o0], %[rs], %[t] offen" WNT "\n\t" \
    "buffer_load_dwordx4 v[148:151], %[o1], %[rs], %[t] offen" WNT "\n\t" \
    "buffer_load_dwordx4 v[156:159], %[o2], %[rs], %[t] offen" WNT "\n\t" \
    "s_add_u32 %[t], %[t], 0x18000\n\t" \
    "s_waitcnt lgkmcnt(0)\n\t" \
    "s_barrier \n\t" \
    "s_waitcnt vmcnt(21)\n\t" \
    "ds_write_b128 %[s0], v[216:219] offset:0\n\t" \
    "ds_write_b128 %[s1], v[80:83] offset:0\n\t" \
    "ds_write_b128 %[s2], v[88:91] offset:0\n\t" \
    "s_waitcnt vmcnt(18)\n\t" \
    "ds_write_b128 %[s0], v[96:99] offset:3456\n\t" \
    "ds_write_b128 %[s1], v[104:107] offset:3456\n\t" \
    "ds_write_b128 %[s2], v[112:115] offset:3456\n\t" \
    "s_waitcnt vmcnt(15)\n\t" \
    "ds_write_b128 %[s0], v[224:227] offset:6912\n\t" \
    "ds_write_b128 %[s1], v[120:123] offset:6912\n\t" \
    "ds_write_b128 %[s2], v[128:131] offset:6912\n\t" \
    "s_waitcnt vmcnt(12)\n\t" \
    "ds_write_b128 %[s0], v[136:139] offset:10368\n\t" \
    "ds_write_b128 %[s1], v[144:147] offset:10368\n\t" \
    "ds_write_b128 %[s2], v[152:155] offset:10368\n\t" \
    "s_waitcnt lgkmcnt(0)\n\t" \
    "s_barrier \n\t" \
    "ds_read_b64_tr_b16 v[216:217], %[t0] offset:0\n\t" \
    "ds_read_b64_tr_b16 v[218:219], %[t0] offset:1664\n\t" \
    "ds_read_b64_tr_b16 v[80:81], %[t0] offset:128\n\t" \
    "ds_read_b64_tr_b16 v[82:83], %[t0] offset:1792\n\t" \
    "ds_read_b64_tr_b16 v[88:89], %[t0] offset:256\n\t" \
    "ds_read_b64_tr_b16 v[90:91], %[t0] offset:1920\n\t" \
    "ds_read_b64_tr_b16 v[96:97], %[t1] offset:0\n\t" \
    "ds_read_b64_tr_b16 v[98:99], %[t1] offset:1664\n\t" \
    "ds_read_b64_tr_b16 v[104:105], %[t1] offset:128\n\t" \
    "ds_read_b64_tr_b16 v[106:107], %[t1] offset:1792\n\t" \
    "ds_read_b64_tr_b16 v[112:113], %[t1] offset:256\n\t" \
    "ds_read_b64_tr_b16 v[114:115], %[t1] offset:1920\n\t" \
    "ds_read_b64_tr_b16 v[224:225], %[t2] offset:0\n\t" \
    "ds_read_b64_tr_b16 v[226:227], %[t2] offset:1664\n\t" \
    "ds_read_b64_tr_b16 v[120:121], %[t2] offset:128\n\t" \
    "ds_read_b64_tr_b16 v[122:123], %[t2] offset:1792\n\t" \
    "ds_read_b64_tr_b16 v[128:129], %[t2] offset:256\n\t" \
    "ds_read_b64_tr_b16 v[130:131], %[t2] offset:1920\n\t" \
    "ds_read_b64_tr_b16 v[136:137], %[t3] offset:0\n\t" \
    "ds_read_b64_tr_b16 v[138:139], %[t3] offset:1664\n\t" \
    "ds_read_b64_tr_b16 v[144:145], %[t3] offset:128\n\t" \
    "ds_read_b64_tr_b16 v[146:147], %[t3] offset:1792\n\t" \
    "ds_read_b64_tr_b16 v[152:153], %[t3] offset:256\n\t" \
    "ds_read_b64_tr_b16 v[154:155], %[t3] offset:1920\n\t" \
    "s_and_b32 %[t], %[wx], 3\n\t" \
    "s_mul_i32 %[t], %[t], 0x60000\n\t" \
    "s_add_u32 %[t], %[t], 0x600000\n\t" \
    "buffer_load_dwordx4 v[232:235], %[o0], %[rs], %[t] offen" WNT "\n\t" \
    "buffer_load_dwordx4 v[160:163], %[o1], %[rs], %[t] offen" WNT "\n\t" \
    "buffer_load_dwordx4 v[168:171], %[o2], %[rs], %[t] offen" WNT "\n\t" \
    "s_add_u32 %[t], %[t], 0x18000\n\t" \
    "buffer_load_dwordx4 v[236:239], %[o0], %[rs], %[t] offen" WNT "\n\t" \
    "buffer_load_dwordx4 v[164:167], %[o1], %[rs], %[t] offen" WNT "\n\t" \
    "buffer_load_dwordx4 v[172:175], %[o2], %[rs], %[t] offen" WNT "\n\t" \
    "s_add_u32 %[t], %[t], 0x18000\n\t" \
    "buffer_load_dwordx4 v[176:179], %[o0], %[rs], %[t] offen" WNT "\n\t" \
    "buffer_load_dwordx4 v[184:187], %[o1], %[rs], %[t] offen" WNT "\n\t" \
    "buffer_load_dwordx4 v[192:195], %[o2], %[rs], %[t] offen" WNT "\n\t" \
    "s_add_u32 %[t], %[t], 0x18000\n\t" \
    "buffer_load_dwordx4 v[180:183], %[o0], %[rs], %[t] offen" WNT "\n\t" \
    "buffer_load_dwordx4 v[188:191], %[o1], %[rs], %[t] offen" WNT "\n\t" \
    "buffer_load_dwordx4 v[196:199], %[o2], %[rs], %[t] offen" WNT "\n\t" \
    "s_add_u32 %[t], %[t], 0x18000\n\t" \
    "s_waitcnt lgkmcnt(0)\n\t" \
    "s_barrier \n\t" \
    "s_waitcnt vmcnt(21)\n\t" \
    "ds_write_b128 %[s0], v[220:223] offset:0\n\t" \
    "ds_write_b128 %[s1], v[84:87] offset:0\n\t" \
    "ds_write_b128 %[s2], v[92:95] offset:0\n\t" \
    "s_waitcnt vmcnt(18)\n\t" \
    "ds_write_b128 %[s0], v[100:103] offset:3456\n\t" \
    "ds_write_b128 %[s1], v[108:111] offset:3456\n\t" \
    "ds_write_b128 %[s2], v[116:119] offset:3456\n\t" \
    "s_waitcnt vmcnt(15)\n\t" \
    "ds_write_b128 %[s0], v[228:231] offset:6912\n\t" \
    "ds_write_b128 %[s1], v[124:127] offset:6912\n\t" \
    "ds_write_b128 %[s2], v[132:135] offset:6912\n\t" \
    "s_waitcnt vmcnt(12)\n\t" \
    "ds_write_b128 %[s0], v[140:143] offset:10368\n\t" \
    "ds_write_b128 %[s1], v[148:151] offset:10368\n\t" \
    "ds_write_b128 %[s2], v[156:159] offset:10368\n\t" \
    "s_waitcnt lgkmcnt(0)\n\t" \
    "s_barrier \n\t" \
    "ds_read_b64_tr_b16 v[220:221], %[t0] offset:0\n\t" \
    "ds_read_b64_tr_b16 v[222:223], %[t0] offset:1664\n\t" \
    "ds_read_b64_tr_b16 v[84:85], %[t0] offset:128\n\t" \
    "ds_read_b64_tr_b16 v[86:87], %[t0] offset:1792\n\t" \
    "ds_read_b64_tr_b16 v[92:93], %[t0] offset:256\n\t" \
    "ds_read_b64_tr_b16 v[94:95], %[t0] offset:1920\n\t" \
    "ds_read_b64_tr_b16 v[100:101], %[t1] offset:0\n\t" \
    "ds_read_b64_tr_b16 v[102:103], %[t1] offset:1664\n\t" \
    "ds_read_b64_tr_b16 v[108:109], %[t1] offset:128\n\t" \
    "ds_read_b64_tr_b16 v[110:111], %[t1] offset:1792\n\t" \
    "ds_read_b64_tr_b16 v[116:117], %[t1] offset:256\n\t" \
    "ds_read_b64_tr_b16 v[118:119], %[t1] offset:1920\n\t" \
    "ds_read_b64_tr_b16 v[228:229], %[t2] offset:0\n\t" \
    "ds_read_b64_tr_b16 v[230:231], %[t2] offset:1664\n\t" \
    "ds_read_b64_tr_b16 v[124:125], %[t2] offset:128\n\t" \
    "ds_read_b64_tr_b16 v[126:127], %[t2] offset:1792\n\t" \
    "ds_read_b64_tr_b16 v[132:133], %[t2] offset:256\n\t" \
    "ds_read_b64_tr_b16 v[134:135], %[t2] offset:1920\n\t" \
    "ds_read_b64_tr_b16 v[140:141], %[t3] offset:0\n\t" \
    "ds_read_b64_tr_b16 v[142:143], %[t3] offset:1664\n\t" \
    "ds_read_b64_tr_b16 v[148:149], %[t3] offset:128\n\t" \
    "ds_read_b64_tr_b16 v[150:151], %[t3] offset:1792\n\t" \
    "ds_read_b64_tr_b16 v[156:157], %[t3] offset:256\n\t" \
    "ds_read_b64_tr_b16 v[158:159], %[t3] offset:1920\n\t" \
    "s_waitcnt lgkmcnt(0)\n\t" \
    "s_barrier \n\t" \
    "s_waitcnt vmcnt(9)\n\t" \
    "ds_write_b128 %[s0], v[232:235] offset:0\n\t" \
    "ds_write_b128 %[s1], v[160:163] offset:0\n\t" \
    "ds_write_b128 %[s2], v[168:171] offset:0\n\t" \
    "s_waitcnt vmcnt(6)\n\t" \
    "ds_write_b128 %[s0], v[236:239] offset:3456\n\t" \
    "ds_write_b128 %[s1], v[164:167] offset:3456\n\t" \
    "ds_write_b128 %[s2], v[172:175] offset:3456\n\t" \
    "s_waitcnt vmcnt(3)\n\t" \
    "ds_write_b128 %[s0], v[176:179] offset:6912\n\t" \
    "ds_write_b128 %[s1], v[184:187] offset:6912\n\t" \
    "ds_write_b128 %[s2], v[192:195] offset:6912\n\t" \
    "s_waitcnt vmcnt(0)\n\t" \
    "ds_write_b128 %[s0], v[180:183] offset:10368\n\t" \
    "ds_write_b128 %[s1], v[188:191] offset:10368\n\t" \
    "ds_write_b128 %[s2], v[196:199] offset:10368\n\t" \
    "s_waitcnt lgkmcnt(0)\n\t" \
    "s_barrier \n\t" \
    "ds_read_b64_tr_b16 v[232:233], %[t4] offset:0\n\t" \
    "ds_read_b64_tr_b16 v[234:235], %[t4] offset:1664\n\t" \
    "ds_read_b64_tr_b16 v[160:161], %[t4] offset:128\n\t" \
    "ds_read_b64_tr_b16 v[162:163], %[t4] offset:1792\n\t" \
    "ds_read_b64_tr_b16 v[168:169], %[t4] offset:256\n\t" \
    "ds_read_b64_tr_b16 v[170:171], %[t4] offset:1920\n\t" \
    "ds_read_b64_tr_b16 v[236:237], %[t4] offset:13824\n\t" \
    "ds_read_b64_tr_b16 v[238:239], %[t4] offset:15488\n\t" \
    "ds_read_b64_tr_b16 v[164:165], %[t4] offset:13952\n\t" \
    "ds_read_b64_tr_b16 v[166:167], %[t4] offset:15616\n\t" \
    "ds_read_b64_tr_b16 v[172:173], %[t4] offset:14080\n\t" \
    "ds_read_b64_tr_b16 v[174:175], %[t4] offset:15744\n\t" \
    "ds_read_b64_tr_b16 v[176:177], %[t4] offset:27648\n\t" \
    "ds_read_b64_tr_b16 v[178:179], %[t4] offset:29312\n\t" \
    "ds_read_b64_tr_b16 v[184:185], %[t4] offset:27776\n\t" \
    "ds_read_b64_tr_b16 v[186:187], %[t4] offset:29440\n\t" \
    "ds_read_b64_tr_b16 v[192:193], %[t4] offset:27904\n\t" \
    "ds_read_b64_tr_b16 v[194:195], %[t4] offset:29568\n\t" \
    "ds_read_b64_tr_b16 v[180:181], %[t4] offset:41472\n\t" \
    "ds_read_b64_tr_b16 v[182:183], %[t4] offset:43136\n\t" \
    "ds_read_b64_tr_b16 v[188:189], %[t4] offset:41600\n\t" \
    "ds_read_b64_tr_b16 v[190:191], %[t4] offset:43264\n\t" \
    "ds_read_b64_tr_b16 v[196:197], %[t4] offset:41728\n\t" \
    "ds_read_b64_tr_b16 v[198:199], %[t4] offset:43392\n\t" \
    "s_waitcnt lgkmcnt(0)\n\t" \
    "s_barrier \n\t" \
    "ds_write_b128 %[wl], v[200:203] offset:0\n\t" \
    "ds_write_b128 %[wl], v[204:207] offset:1024\n\t" \
    "ds_write_b128 %[wl], v[208:211] offset:2048\n\t" \
    "ds_write_b128 %[wl], v[212:215] offset:3072\n\t" \
    "ds_write_b128 %[wl], v[216:219] offset:4096\n\t" \
    "ds_write_b128 %[wl], v[220:223] offset:5120\n\t" \
    "ds_write_b128 %[wl], v[224:227] offset:6144\n\t" \
    "ds_write_b128 %[wl], v[228:231] offset:7168\n\t" \
    "ds_write_b128 %[wl], v[232:235] offset:8192\n\t" \
    "ds_write_b128 %[wl], v[236:239] offset:9216\n\t" \
    "s_waitcnt lgkmcnt(0)\n\t"
#define ASM_WLOADO \
    "s_mov_b32 %[t], 0x0\n\t" \
    "buffer_load_dwordx4 v[200:203], %[o0], %[rs], %[t] offen" WNT "\n\t" \
    "buffer_load_dwordx4 v[16:19], %[o1], %[rs], %[t] offen" WNT "\n\t" \
    "buffer_load_dwordx4 v[208:211], %[o2], %[rs], %[t] offen" WNT "\n\t" \
    "buffer_load_dwordx4 v[56:59], %[o3], %[rs], %[t] offen" WNT "\n\t" \
    "buffer_load_dwordx4 v[216:219], %[o4], %[rs], %[t] offen" WNT "\n\t" \
    "s_mov_b32 %[t], 0xa000\n\t" \
    "buffer_load_dwordx4 v[96:99], %[o0], %[rs], %[t] offen" WNT "\n\t" \
    "buffer_load_dwordx4 v[224:227], %[o1], %[rs], %[t] offen" WNT "\n\t" \
    "buffer_load_dwordx4 v[136:139], %[o2], %[rs], %[t] offen" WNT "\n\t" \
    "buffer_load_dwordx4 v[232:235], %[o3], %[rs], %[t] offen" WNT "\n\t" \
    "buffer_load_dwordx4 v[176:179], %[o4], %[rs], %[t] offen" WNT "\n\t" \
    "s_mov_b32 %[t], 0x14000\n\t" \
    "buffer_load_dwordx4 v[204:207], %[o0], %[rs], %[t] offen" WNT "\n\t" \
    "buffer_load_dwordx4 v[20:23], %[o1], %[rs], %[t] offen" WNT "\n\t" \
    "buffer_load_dwordx4 v[212:215], %[o2], %[rs], %[t] offen" WNT "\n\t" \
    "buffer_load_dwordx4 v[60:63], %[o3], %[rs], %[t] offen" WNT "\n\t" \
    "buffer_load_dwordx4 v[220:223], %[o4], %[rs], %[t] offen" WNT "\n\t" \
    "s_mov_b32 %[t], 0x1e000\n\t" \
    "buffer_load_dwordx4 v[100:103], %[o0], %[rs], %[t] offen" WNT "\n\t" \
    "buffer_load_dwordx4 v[228:231], %[o1], %[rs], %[t] offen" WNT "\n\t" \
    "buffer_load_dwordx4 v[140:143], %[o2], %[rs], %[t] offen" WNT "\n\t" \
    "buffer_load_dwordx4 v[236:239], %[o3], %[rs], %[t] offen" WNT "\n\t" \
    "buffer_load_dwordx4 v[180:183], %[o4], %[rs], %[t] offen" WNT "\n\t" \
    "s_waitcnt vmcnt(15)\n\t" \
    "ds_write_b128 %[s0], v[200:203] offset:0\n\t" \
    "ds_write_b128 %[s1], v[16:19] offset:0\n\t" \
    "ds_write_b128 %[s2], v[208:211] offset:0\n\t" \
    "ds_write_b128 %[s3], v[56:59] offset:0\n\t" \
    "ds_write_b128 %[s4], v[216:219] offset:0\n\t" \
    "s_waitcnt vmcnt(10)\n\t" \
    "ds_write_b128 %[s0], v[96:99] offset:5632\n\t" \
    "ds_write_b128 %[s1], v[224:227] offset:5632\n\t" \
    "ds_write_b128 %[s2], v[136:139] offset:5632\n\t" \
    "ds_write_b128 %[s3], v[232:235] offset:5632\n\t" \
    "ds_write_b128 %[s4], v[176:179] offset:5632\n\t" \
    "ds_read_b64_tr_b16 v[200:201], %[tr] offset:0\n\t" \
    "ds_read_b64_tr_b16 v[202:203], %[tr] offset:5632\n\t" \
    "ds_read_b64_tr_b16 v[16:17], %[tr] offset:32\n\t" \
    "ds_read_b64_tr_b16 v[18:19], %[tr] offset:5664\n\t" \
    "ds_read_b64_tr_b16 v[208:209], %[tr] offset:64\n\t" \
    "ds_read_b64_tr_b16 v[210:211], %[tr] offset:5696\n\t" \
    "ds_read_b64_tr_b16 v[56:57], %[tr] offset:96\n\t" \
    "ds_read_b64_tr_b16 v[58:59], %[tr] offset:5728\n\t" \
    "ds_read_b64_tr_b16 v[216:217], %[tr] offset:128\n\t" \
    "ds_read_b64_tr_b16 v[218:219], %[tr] offset:5760\n\t" \
    "ds_read_b64_tr_b16 v[96:97], %[tr] offset:160\n\t" \
    "ds_read_b64_tr_b16 v[98:99], %[tr] offset:5792\n\t" \
    "ds_read_b64_tr_b16 v[224:225], %[tr] offset:192\n\t" \
    "ds_read_b64_tr_b16 v[226:227], %[tr] offset:5824\n\t" \
    "ds_read_b64_tr_b16 v[136:137], %[tr] offset:224\n\t" \
    "ds_read_b64_tr_b16 v[138:139], %[tr] offset:5856\n\t" \
    "ds_read_b64_tr_b16 v[232:233], %[tr] offset:256\n\t" \
    "ds_read_b64_tr_b16 v[234:235], %[tr] offset:5888\n\t" \
    "ds_read_b64_tr_b16 v[176:177], %[tr] offset:288\n\t" \
    "ds_read_b64_tr_b16 v[178:179], %[tr] offset:5920\n\t" \
    "s_barrier \n\t" \
    "s_mov_b32 %[t], 0x28000\n\t" \
    "buffer_load_dwordx4 v[0:3], %[o0], %[rs], %[t] offen" WNT "\n\t" \
    "buffer_load_dwordx4 v[24:27], %[o1], %[rs], %[t] offen" WNT "\n\t" \
    "buffer_load_dwordx4 v[40:43], %[o2], %[rs], %[t] offen" WNT "\n\t" \
    "buffer_load_dwordx4 v[64:67], %[o3], %[rs], %[t] offen" WNT "\n\t" \
    "buffer_load_dwordx4 v[80:83], %[o4], %[rs], %[t] offen" WNT "\n\t" \
    "s_mov_b32 %[t], 0x32000\n\t" \
    "buffer_load_dwordx4 v[104:107], %[o0], %[rs], %[t] offen" WNT "\n\t" \
    "buffer_load_dwordx4 v[120:123], %[o1], %[rs], %[t] offen" WNT "\n\t" \
    "buffer_load_dwordx4 v[144:147], %[o2], %[rs], %[t] offen" WNT "\n\t" \
    "buffer_load_dwordx4 v[160:163], %[o3], %[rs], %[t] offen" WNT "\n\t" \
    "buffer_load_dwordx4 v[184:187], %[o4], %[rs], %[t] offen" WNT "\n\t" \
    "s_waitcnt lgkmcnt(0)\n\t" \
    "s_barrier \n\t" \
    "s_waitcnt vmcnt(15)\n\t" \
    "ds_write_b128 %[s0], v[204:207] offset:0\n\t" \
    "ds_write_b128 %[s1], v[20:23] offset:0\n\t" \
    "ds_write_b128 %[s2], v[212:215] offset:0\n\t" \
    "ds_write_b128 %[s3], v[60:63] offset:0\n\t" \
    "ds_write_b128 %[s4], v[220:223] offset:0\n\t" \
    "s_waitcnt vmcnt(10)\n\t" \
    "ds_write_b128 %[s0], v[100:103] offset:5632\n\t" \
    "ds_write_b128 %[s1], v[228:231] offset:5632\n\t" \
    "ds_write_b128 %[s2], v[140:143] offset:5632\n\t" \
    "ds_write_b128 %[s3], v[236:239] offset:5632\n\t" \
    "ds_write_b128 %[s4], v[180:183] offset:5632\n\t" \
    "ds_read_b64_tr_b16 v[204:205], %[tr] offset:0\n\t" \
    "ds_read_b64_tr_b16 v[206:207], %[tr] offset:5632\n\t" \
    "ds_read_b64_tr_b16 v[20:21], %[tr] offset:32\n\t" \
    "ds_read_b64_tr_b16 v[22:23], %[tr] offset:5664\n\t" \
    "ds_read_b64_tr_b16 v[212:213], %[tr] offset:64\n\t" \
    "ds_read_b64_tr_b16 v[214:215], %[tr] offset:5696\n\t" \
    "ds_read_b64_tr_b16 v[60:61], %[tr] offset:96\n\t" \
    "ds_read_b64_tr_b16 v[62:63], %[tr] offset:5728\n\t" \
    "ds_read_b64_tr_b16 v[220:221], %[tr] offset:128\n\t" \
    "ds_read_b64_tr_b16 v[222:223], %[tr] offset:5760\n\t" \
    "ds_read_b64_tr_b16 v[100:101], %[tr] offset:160\n\t" \
    "ds_read_b64_tr_b16 v[102:103], %[tr] offset:5792\n\t" \
    "ds_read_b64_tr_b16 v[228:229], %[tr] offset:192\n\t" \
    "ds_read_b64_tr_b16 v[230:231], %[tr] offset:5824\n\t" \
    "ds_read_b64_tr_b16 v[140:141], %[tr] offset:224\n\t" \
    "ds_read_b64_tr_b16 v[142:143], %[tr] offset:5856\n\t" \
    "ds_read_b64_tr_b16 v[236:237], %[tr] offset:256\n\t" \
    "ds_read_b64_tr_b16 v[238:239], %[tr] offset:5888\n\t" \
    "ds_read_b64_tr_b16 v[180:181], %[tr] offset:288\n\t" \
    "ds_read_b64_tr_b16 v[182:183], %[tr] offset:5920\n\t" \
    "s_barrier \n\t" \
    "s_mov_b32 %[t], 0x5000\n\t" \
    "buffer_load_dwordx4 v[4:7], %[o0], %[rs], %[t] offen" WNT "\n\t" \
    "buffer_load_dwordx4 v[28:31], %[o1], %[rs], %[t] offen" WNT "\n\t" \
    "buffer_load_dwordx4 v[44:47], %[o2], %[rs], %[t] offen" WNT "\n\t" \
    "buffer_load_dwordx4 v[68:71], %[o3], %[rs], %[t] offen" WNT "\n\t" \
    "buffer_load_dwordx4 v[84:87], %[o4], %[rs], %[t] offen" WNT "\n\t" \
    "s_mov_b32 %[t], 0xf000\n\t" \
    "buffer_load_dwordx4 v[108:111], %[o0], %[rs], %[t] offen" WNT "\n\t" \
    "buffer_load_dwordx4 v[124:127], %[o1], %[rs], %[t] offen" WNT "\n\t" \
    "buffer_load_dwordx4 v[148:151], %[o2], %[rs], %[t] offen" WNT "\n\t" \
    "buffer_load_dwordx4 v[164:167], %[o3], %[rs], %[t] offen" WNT "\n\t" \
    "buffer_load_dwordx4 v[188:191], %[o4], %[rs], %[t] offen" WNT "\n\t" \
    "s_waitcnt lgkmcnt(0)\n\t" \
    "s_barrier \n\t" \
    "s_waitcnt vmcnt(15)\n\t" \
    "ds_write_b128 %[s0], v[0:3] offset:0\n\t" \
    "ds_write_b128 %[s1], v[24:27] offset:0\n\t" \
    "ds_write_b128 %[s2], v[40:43] offset:0\n\t" \
    "ds_write_b128 %[s3], v[64:67] offset:0\n\t" \
    "ds_write_b128 %[s4], v[80:83] offset:0\n\t" \
    "s_waitcnt vmcnt(10)\n\t" \
    "ds_write_b128 %[s0], v[104:107] offset:5632\n\t" \
    "ds_write_b128 %[s1], v[120:123] offset:5632\n\t" \
    "ds_write_b128 %[s2], v[144:147] offset:5632\n\t" \
    "ds_write_b128 %[s3], v[160:163] offset:5632\n\t" \
    "ds_write_b128 %[s4], v[184:187] offset:5632\n\t" \
    "ds_read_b64_tr_b16 v[0:1], %[tr] offset:0\n\t" \
    "ds_read_b64_tr_b16 v[2:3], %[tr] offset:5632\n\t" \
    "ds_read_b64_tr_b16 v[24:25], %[tr] offset:32\n\t" \
    "ds_read_b64_tr_b16 v[26:27], %[tr] offset:5664\n\t" \
    "ds_read_b64_tr_b16 v[40:41], %[tr] offset:64\n\t" \
    "ds_read_b64_tr_b16 v[42:43], %[tr] offset:5696\n\t" \
    "ds_read_b64_tr_b16 v[64:65], %[tr] offset:96\n\t" \
    "ds_read_b64_tr_b16 v[66:67], %[tr] offset:5728\n\t" \
    "ds_read_b64_tr_b16 v[80:81], %[tr] offset:128\n\t" \
    "ds_read_b64_tr_b16 v[82:83], %[tr] offset:5760\n\t" \
    "ds_read_b64_tr_b16 v[104:105], %[tr] offset:160\n\t" \
    "ds_read_b64_tr_b16 v[106:107], %[tr] offset:5792\n\t" \
    "ds_read_b64_tr_b16 v[120:121], %[tr] offset:192\n\t" \
    "ds_read_b64_tr_b16 v[122:123], %[tr] offset:5824\n\t" \
    "ds_read_b64_tr_b16 v[144:145], %[tr] offset:224\n\t" \
    "ds_read_b64_tr_b16 v[146:147], %[tr] offset:5856\n\t" \
    "ds_read_b64_tr_b16 v[160:161], %[tr] offset:256\n\t" \
    "ds_read_b64_tr_b16 v[162:163], %[tr] offset:5888\n\t" \
    "ds_read_b64_tr_b16 v[184:185], %[tr] offset:288\n\t" \
    "ds_read_b64_tr_b16 v[186:187], %[tr] offset:5920\n\t" \
    "s_barrier \n\t" \
    "s_mov_b32 %[t], 0x19000\n\t" \
    "buffer_load_dwordx4 v[8:11], %[o0], %[rs], %[t] offen" WNT "\n\t" \
    "buffer_load_dwordx4 v[32:35], %[o1], %[rs], %[t] offen" WNT "\n\t" \
    "buffer_load_dwordx4 v[48:51], %[o2], %[rs], %[t] offen" WNT "\n\t" \
    "buffer_load_dwordx4 v[72:75], %[o3], %[rs], %[t] offen" WNT "\n\t" \
    "buffer_load_dwordx4 v[88:91], %[o4], %[rs], %[t] offen" WNT "\n\t" \
    "s_mov_b32 %[t], 0x23000\n\t" \
    "buffer_load_dwordx4 v[112:115], %[o0], %[rs], %[t] offen" WNT "\n\t" \
    "buffer_load_dwordx4 v[128:131], %[o1], %[rs], %[t] offen" WNT "\n\t" \
    "buffer_load_dwordx4 v[152:155], %[o2], %[rs], %[t] offen" WNT "\n\t" \
    "buffer_load_dwordx4 v[168:171], %[o3], %[rs], %[t] offen" WNT "\n\t" \
    "buffer_load_dwordx4 v[192:195], %[o4], %[rs], %[t] offen" WNT "\n\t" \
    "s_waitcnt lgkmcnt(0)\n\t" \
    "s_barrier \n\t" \
    "s_waitcnt vmcnt(15)\n\t" \
    "ds_write_b128 %[s0], v[4:7] offset:0\n\t" \
    "ds_write_b128 %[s1], v[28:31] offset:0\n\t" \
    "ds_write_b128 %[s2], v[44:47] offset:0\n\t" \
    "ds_write_b128 %[s3], v[68:71] offset:0\n\t" \
    "ds_write_b128 %[s4], v[84:87] offset:0\n\t" \
    "s_waitcnt vmcnt(10)\n\t" \
    "ds_write_b128 %[s0], v[108:111] offset:5632\n\t" \
    "ds_write_b128 %[s1], v[124:127] offset:5632\n\t" \
    "ds_write_b128 %[s2], v[148:151] offset:5632\n\t" \
    "ds_write_b128 %[s3], v[164:167] offset:5632\n\t" \
    "ds_write_b128 %[s4], v[188:191] offset:5632\n\t" \
    "ds_read_b64_tr_b16 v[4:5], %[tr] offset:0\n\t" \
    "ds_read_b64_tr_b16 v[6:7], %[tr] offset:5632\n\t" \
    "ds_read_b64_tr_b16 v[28:29], %[tr] offset:32\n\t" \
    "ds_read_b64_tr_b16 v[30:31], %[tr] offset:5664\n\t" \
    "ds_read_b64_tr_b16 v[44:45], %[tr] offset:64\n\t" \
    "ds_read_b64_tr_b16 v[46:47], %[tr] offset:5696\n\t" \
    "ds_read_b64_tr_b16 v[68:69], %[tr] offset:96\n\t" \
    "ds_read_b64_tr_b16 v[70:71], %[tr] offset:5728\n\t" \
    "ds_read_b64_tr_b16 v[84:85], %[tr] offset:128\n\t" \
    "ds_read_b64_tr_b16 v[86:87], %[tr] offset:5760\n\t" \
    "ds_read_b64_tr_b16 v[108:109], %[tr] offset:160\n\t" \
    "ds_read_b64_tr_b16 v[110:111], %[tr] offset:5792\n\t" \
    "ds_read_b64_tr_b16 v[124:125], %[tr] offset:192\n\t" \
    "ds_read_b64_tr_b16 v[126:127], %[tr] offset:5824\n\t" \
    "ds_read_b64_tr_b16 v[148:149], %[tr] offset:224\n\t" \
    "ds_read_b64_tr_b16 v[150:151], %[tr] offset:5856\n\t" \
    "ds_read_b64_tr_b16 v[164:165], %[tr] offset:256\n\t" \
    "ds_read_b64_tr_b16 v[166:167], %[tr] offset:5888\n\t" \
    "ds_read_b64_tr_b16 v[188:189], %[tr] offset:288\n\t" \
    "ds_read_b64_tr_b16 v[190:191], %[tr] offset:5920\n\t" \
    "s_barrier \n\t" \
    "s_mov_b32 %[t], 0x2d000\n\t" \
    "buffer_load_dwordx4 v[12:15], %[o0], %[rs], %[t] offen" WNT "\n\t" \
    "buffer_load_dwordx4 v[36:39], %[o1], %[rs], %[t] offen" WNT "\n\t" \
    "buffer_load_dwordx4 v[52:55], %[o2], %[rs], %[t] offen" WNT "\n\t" \
    "buffer_load_dwordx4 v[76:79], %[o3], %[rs], %[t] offen" WNT "\n\t" \
    "buffer_load_dwordx4 v[92:95], %[o4], %[rs], %[t] offen" WNT "\n\t" \
    "s_mov_b32 %[t], 0x37000\n\t" \
    "buffer_load_dwordx4 v[116:119], %[o0], %[rs], %[t] offen" WNT "\n\t" \
    "buffer_load_dwordx4 v[132:135], %[o1], %[rs], %[t] offen" WNT "\n\t" \
    "buffer_load_dwordx4 v[156:159], %[o2], %[rs], %[t] offen" WNT "\n\t" \
    "buffer_load_dwordx4 v[172:175], %[o3], %[rs], %[t] offen" WNT "\n\t" \
    "buffer_load_dwordx4 v[196:199], %[o4], %[rs], %[t] offen" WNT "\n\t" \
    "s_waitcnt lgkmcnt(0)\n\t" \
    "s_barrier \n\t" \
    "s_waitcnt vmcnt(15)\n\t" \
    "ds_write_b128 %[s0], v[8:11] offset:0\n\t" \
    "ds_write_b128 %[s1], v[32:35] offset:0\n\t" \
    "ds_write_b128 %[s2], v[48:51] offset:0\n\t" \
    "ds_write_b128 %[s3], v[72:75] offset:0\n\t" \
    "ds_write_b128 %[s4], v[88:91] offset:0\n\t" \
    "s_waitcnt vmcnt(10)\n\t" \
    "ds_write_b128 %[s0], v[112:115] offset:5632\n\t" \
    "ds_write_b128 %[s1], v[128:131] offset:5632\n\t" \
    "ds_write_b128 %[s2], v[152:155] offset:5632\n\t" \
    "ds_write_b128 %[s3], v[168:171] offset:5632\n\t" \
    "ds_write_b128 %[s4], v[192:195] offset:5632\n\t" \
    "ds_read_b64_tr_b16 v[8:9], %[tr] offset:0\n\t" \
    "ds_read_b64_tr_b16 v[10:11], %[tr] offset:5632\n\t" \
    "ds_read_b64_tr_b16 v[32:33], %[tr] offset:32\n\t" \
    "ds_read_b64_tr_b16 v[34:35], %[tr] offset:5664\n\t" \
    "ds_read_b64_tr_b16 v[48:49], %[tr] offset:64\n\t" \
    "ds_read_b64_tr_b16 v[50:51], %[tr] offset:5696\n\t" \
    "ds_read_b64_tr_b16 v[72:73], %[tr] offset:96\n\t" \
    "ds_read_b64_tr_b16 v[74:75], %[tr] offset:5728\n\t" \
    "ds_read_b64_tr_b16 v[88:89], %[tr] offset:128\n\t" \
    "ds_read_b64_tr_b16 v[90:91], %[tr] offset:5760\n\t" \
    "ds_read_b64_tr_b16 v[112:113], %[tr] offset:160\n\t" \
    "ds_read_b64_tr_b16 v[114:115], %[tr] offset:5792\n\t" \
    "ds_read_b64_tr_b16 v[128:129], %[tr] offset:192\n\t" \
    "ds_read_b64_tr_b16 v[130:131], %[tr] offset:5824\n\t" \
    "ds_read_b64_tr_b16 v[152:153], %[tr] offset:224\n\t" \
    "ds_read_b64_tr_b16 v[154:155], %[tr] offset:5856\n\t" \
    "ds_read_b64_tr_b16 v[168:169], %[tr] offset:256\n\t" \
    "ds_read_b64_tr_b16 v[170:171], %[tr] offset:5888\n\t" \
    "ds_read_b64_tr_b16 v[192:193], %[tr] offset:288\n\t" \
    "ds_read_b64_tr_b16 v[194:195], %[tr] offset:5920\n\t" \
    "s_waitcnt lgkmcnt(0)\n\t" \
    "s_barrier \n\t" \
    "s_waitcnt vmcnt(5)\n\t" \
    "ds_write_b128 %[s0], v[12:15] offset:0\n\t" \
    "ds_write_b128 %[s1], v[36:39] offset:0\n\t" \
    "ds_write_b128 %[s2], v[52:55] offset:0\n\t" \
    "ds_write_b128 %[s3], v[76:79] offset:0\n\t" \
    "ds_write_b128 %[s4], v[92:95] offset:0\n\t" \
    "s_waitcnt vmcnt(0)\n\t" \
    "ds_write_b128 %[s0], v[116:119] offset:5632\n\t" \
    "ds_write_b128 %[s1], v[132:135] offset:5632\n\t" \
    "ds_write_b128 %[s2], v[156:159] offset:5632\n\t" \
    "ds_write_b128 %[s3], v[172:175] offset:5632\n\t" \
    "ds_write_b128 %[s4], v[196:199] offset:5632\n\t" \
    "ds_read_b64_tr_b16 v[12:13], %[tr] offset:0\n\t" \
    "ds_read_b64_tr_b16 v[14:15], %[tr] offset:5632\n\t" \
    "ds_read_b64_tr_b16 v[36:37], %[tr] offset:32\n\t" \
    "ds_read_b64_tr_b16 v[38:39], %[tr] offset:5664\n\t" \
    "ds_read_b64_tr_b16 v[52:53], %[tr] offset:64\n\t" \
    "ds_read_b64_tr_b16 v[54:55], %[tr] offset:5696\n\t" \
    "ds_read_b64_tr_b16 v[76:77], %[tr] offset:96\n\t" \
    "ds_read_b64_tr_b16 v[78:79], %[tr] offset:5728\n\t" \
    "ds_read_b64_tr_b16 v[92:93], %[tr] offset:128\n\t" \
    "ds_read_b64_tr_b16 v[94:95], %[tr] offset:5760\n\t" \
    "ds_read_b64_tr_b16 v[116:117], %[tr] offset:160\n\t" \
    "ds_read_b64_tr_b16 v[118:119], %[tr] offset:5792\n\t" \
    "ds_read_b64_tr_b16 v[132:133], %[tr] offset:192\n\t" \
    "ds_read_b64_tr_b16 v[134:135], %[tr] offset:5824\n\t" \
    "ds_read_b64_tr_b16 v[156:157], %[tr] offset:224\n\t" \
    "ds_read_b64_tr_b16 v[158:159], %[tr] offset:5856\n\t" \
    "ds_read_b64_tr_b16 v[172:173], %[tr] offset:256\n\t" \
    "ds_read_b64_tr_b16 v[174:175], %[tr] offset:5888\n\t" \
    "ds_read_b64_tr_b16 v[196:197], %[tr] offset:288\n\t" \
    "ds_read_b64_tr_b16 v[198:199], %[tr] offset:5920\n\t" \
    "s_waitcnt lgkmcnt(0)\n\t" \
    "s_barrier \n\t" \
    "ds_write_b128 %[wl], v[200:203] offset:0\n\t" \
    "ds_write_b128 %[wl], v[204:207] offset:1024\n\t" \
    "ds_write_b128 %[wl], v[208:211] offset:2048\n\t" \
    "ds_write_b128 %[wl], v[212:215] offset:3072\n\t" \
    "ds_write_b128 %[wl], v[216:219] offset:4096\n\t" \
    "ds_write_b128 %[wl], v[220:223] offset:5120\n\t" \
    "ds_write_b128 %[wl], v[224:227] offset:6144\n\t" \
    "ds_write_b128 %[wl], v[228:231] offset:7168\n\t" \
    "ds_write_b128 %[wl], v[232:235] offset:8192\n\t" \
    "ds_write_b128 %[wl], v[236:239] offset:9216\n\t" \
    "s_waitcnt lgkmcnt(0)\n\t"
#define ASM_XG_S_LO \
    "s_waitcnt lgkmcnt(0)\n\t" \
    "ds_read_b128 v[224:227], %[xa] offset:0\n\t" \
    "ds_read_b128 v[244:247], %[wl] offset:0\n\t" \
    "ds_read_b128 v[228:231], %[xa] offset:16384\n\t" \
    "ds_read_b128 v[232:235], %[xa] offset:1024\n\t" \
    "ds_read_b128 v[236:239], %[xa] offset:17408\n\t" \
    "ds_read_b128 v[248:251], %[wl] offset:1024\n\t" \
    "s_waitcnt lgkmcnt(5)\n\t" \
    "v_mfma_f32_16x16x32_bf16 v[204:207], v[0:3], v[224:227], 0\n\t" \
    "v_xor_b32 v252, 0x800, %[xa]\n\t" \
    "ds_read_b128 v[240:243], v252 offset:0\n\t" \
    "v_mfma_f32_16x16x32_bf16 v[208:211], v[8:11], v[224:227], 0\n\t" \
    "s_waitcnt lgkmcnt(5)\n\t" \
    "v_mfma_f32_16x16x32_bf16 v[200:203], v[244:247], v[224:227], 0\n\t" \
    "s_waitcnt lgkmcnt(4)\n\t" \
    "v_mfma_f32_16x16x32_bf16 v[216:219], v[0:3], v[228:231], 0\n\t" \
    "ds_read_b128 v[224:227], v252 offset:16384\n\t" \
    "v_mfma_f32_16x16x32_bf16 v[220:223], v[8:11], v[228:231], 0\n\t" \
    "v_mfma_f32_16x16x32_bf16 v[212:215], v[244:247], v[228:231], 0\n\t" \
    "s_waitcnt lgkmcnt(4)\n\t" \
    "v_mfma_f32_16x16x32_bf16 v[204:207], v[4:7], v[232:235], v[204:207]\n\t" \
    "ds_read_b128 v[228:231], v252 offset:1024\n\t" \
    "v_mfma_f32_16x16x32_bf16 v[208:211], v[12:15], v[232:235], v[208:211]\n\t" \
    "ds_read_b128 v[244:247], %[wl] offset:2048\n\t" \
    "s_waitcnt lgkmcnt(4)\n\t" \
    "v_mfma_f32_16x16x32_bf16 v[200:203], v[248:251], v[232:235], v[200:203]\n\t" \
    "v_mfma_f32_16x16x32_bf16 v[216:219], v[4:7], v[236:239], v[216:219]\n\t" \
    "ds_read_b128 v[232:235], v252 offset:17408\n\t" \
    "v_mfma_f32_16x16x32_bf16 v[220:223], v[12:15], v[236:239], v[220:223]\n\t" \
    "v_mfma_f32_16x16x32_bf16 v[212:215], v[248:251], v[236:239], v[212:215]\n\t" \
    "s_waitcnt lgkmcnt(4)\n\t" \
    "v_mfma_f32_16x16x32_bf16 v[204:207], v[24:27], v[240:243], v[204:207]\n\t" \
    "v_xor_b32 v252, 0x1000, %[xa]\n\t" \
    "ds_read_b128 v[236:239], v252 offset:0\n\t" \
    "v_mfma_f32_16x16x32_bf16 v[208:211], v[32:35], v[240:243], v[208:211]\n\t" \
    "ds_read_b128 v[248:251], %[wl] offset:3072\n\t" \
    "v_mfma_f32_16x16x32_bf16 v[200:203], v[16:19], v[240:243], v[200:203]\n\t" \
    "s_waitcnt lgkmcnt(5)\n\t" \
    "v_mfma_f32_16x16x32_bf16 v[216:219], v[24:27], v[224:227], v[216:219]\n\t" \
    "ds_read_b128 v[240:243], v252 offset:16384\n\t" \
    "v_mfma_f32_16x16x32_bf16 v[220:223], v[32:35], v[224:227], v[220:223]\n\t" \
    "v_mfma_f32_16x16x32_bf16 v[212:215], v[16:19], v[224:227], v[212:215]\n\t" \
    "s_waitcnt lgkmcnt(5)\n\t" \
    "v_mfma_f32_16x16x32_bf16 v[204:207], v[28:31], v[228:231], v[204:207]\n\t" \
    "ds_read_b128 v[224:227], v252 offset:1024\n\t" \
    "v_mfma_f32_16x16x32_bf16 v[208:211], v[36:39], v[228:231], v[208:211]\n\t" \
    "v_mfma_f32_16x16x32_bf16 v[200:203], v[20:23], v[228:231], v[200:203]\n\t" \
    "s_waitcnt lgkmcnt(4)\n\t" \
    "v_mfma_f32_16x16x32_bf16 v[216:219], v[28:31], v[232:235], v[216:219]\n\t" \
    "ds_read_b128 v[228:231], v252 offset:17408\n\t" \
    "v_mfma_f32_16x16x32_bf16 v[220:223], v[36:39], v[232:235], v[220:223]\n\t" \
    "v_mfma_f32_16x16x32_bf16 v[212:215], v[20:23], v[232:235], v[212:215]\n\t" \
    "s_waitcnt lgkmcnt(4)\n\t" \
    "v_mfma_f32_16x16x32_bf16 v[204:207], v[40:43], v[236:239], v[204:207]\n\t" \
    "v_xor_b32 v252, 0x1800, %[xa]\n\t" \
    "ds_read_b128 v[232:235], v252 offset:0\n\t" \
    "v_mfma_f32_16x16x32_bf16 v[208:211], v[48:51], v[236:239], v[208:211]\n\t" \
    "v_mfma_f32_16x16x32_bf16 v[200:203], v[244:247], v[236:239], v[200:203]\n\t" \
    "s_waitcnt lgkmcnt(3)\n\t" \
    "v_mfma_f32_16x16x32_bf16 v[216:219], v[40:43], v[240:243], v[216:219]\n\t" \
    "ds_read_b128 v[236:239], v252 offset:16384\n\t" \
    "v_mfma_f32_16x16x32_bf16 v[220:223], v[48:51], v[240:243], v[220:223]\n\t" \
    "v_mfma_f32_16x16x32_bf16 v[212:215], v[244:247], v[240:243], v[212:215]\n\t" \
    "s_waitcnt lgkmcnt(3)\n\t" \
    "v_mfma_f32_16x16x32_bf16 v[204:207], v[44:47], v[224:227], v[204:207]\n\t" \
    "ds_read_b128 v[240:243], v252 offset:1024\n\t" \
    "v_mfma_f32_16x16x32_bf16 v[208:211], v[52:55], v[224:227], v[208:211]\n\t" \
    "ds_read_b128 v[244:247], %[wl] offset:8192\n\t" \
    "v_mfma_f32_16x16x32_bf16 v[200:203], v[248:251], v[224:227], v[200:203]\n\t" \
    "s_waitcnt lgkmcnt(4)\n\t" \
    "v_mfma_f32_16x16x32_bf16 v[216:219], v[44:47], v[228:231], v[216:219]\n\t" \
    "ds_read_b128 v[224:227], v252 offset:17408\n\t" \
    "v_mfma_f32_16x16x32_bf16 v[220:223], v[52:55], v[228:231], v[220:223]\n\t" \
    "v_mfma_f32_16x16x32_bf16 v[212:215], v[248:251], v[228:231], v[212:215]\n\t" \
    "s_waitcnt lgkmcnt(4)\n\t" \
    "v_mfma_f32_16x16x32_bf16 v[204:207], v[64:67], v[232:235], v[204:207]\n\t" \
    "v_and_b32 v252, 0x3ff, %[xa]\n\t" \
    "ds_read_b128 v[228:231], v252 offset:40960\n\t" \
    "v_mfma_f32_16x16x32_bf16 v[208:211], v[72:75], v[232:235], v[208:211]\n\t" \
    "ds_read_b128 v[248:251], %[wl] offset:9216\n\t" \
    "v_mfma_f32_16x16x32_bf16 v[200:203], v[56:59], v[232:235], v[200:203]\n\t" \
    "s_waitcnt lgkmcnt(5)\n\t" \
    "v_mfma_f32_16x16x32_bf16 v[216:219], v[64:67], v[236:239], v[216:219]\n\t" \
    "ds_read_b128 v[232:235], v252 offset:45056\n\t" \
    "v_mfma_f32_16x16x32_bf16 v[220:223], v[72:75], v[236:239], v[220:223]\n\t" \
    "v_mfma_f32_16x16x32_bf16 v[212:215], v[56:59], v[236:239], v[212:215]\n\t" \
    "s_waitcnt lgkmcnt(5)\n\t" \
    "v_mfma_f32_16x16x32_bf16 v[204:207], v[68:71], v[240:243], v[204:207]\n\t" \
    "ds_read_b128 v[236:239], v252 offset:41984\n\t" \
    "v_mfma_f32_16x16x32_bf16 v[208:211], v[76:79], v[240:243], v[208:211]\n\t" \
    "v_mfma_f32_16x16x32_bf16 v[200:203], v[60:63], v[240:243], v[200:203]\n\t" \
    "s_waitcnt lgkmcnt(4)\n\t" \
    "v_mfma_f32_16x16x32_bf16 v[216:219], v[68:71], v[224:227], v[216:219]\n\t" \
    "ds_read_b128 v[240:243], v252 offset:46080\n\t" \
    "v_mfma_f32_16x16x32_bf16 v[220:223], v[76:79], v[224:227], v[220:223]\n\t" \
    "v_mfma_f32_16x16x32_bf16 v[212:215], v[60:63], v[224:227], v[212:215]\n\t" \
    "s_waitcnt lgkmcnt(0)\n\t" \
    "s_barrier\n\t" ASM_ST \
    "v_mfma_f32_16x16x32_bf16 v[204:207], v[160:163], v[228:231], v[204:207]\n\t" \
    "v_xor_b32 v252, 0x2000, %[xa]\n\t" \
    "ds_read_b128 v[224:227], v252 offset:0\n\t" \
    "v_mfma_f32_16x16x32_bf16 v[208:211], v[168:171], v[228:231], v[208:211]\n\t" \
    "v_mfma_f32_16x16x32_bf16 v[200:203], v[244:247], v[228:231], v[200:203]\n\t" \
    "v_mfma_f32_16x16x32_bf16 v[216:219], v[160:163], v[232:235], v[216:219]\n\t" \
    "ds_read_b128 v[228:231], v252 offset:16384\n\t" \
    "v_mfma_f32_16x16x32_bf16 v[220:223], v[168:171], v[232:235], v[220:223]\n\t" \
    "v_mfma_f32_16x16x32_bf16 v[212:215], v[244:247], v[232:235], v[212:215]\n\t" \
    "v_mfma_f32_16x16x32_bf16 v[204:207], v[164:167], v[236:239], v[204:207]\n\t" \
    "ds_read_b128 v[232:235], v252 offset:1024\n\t" \
    "v_mfma_f32_16x16x32_bf16 v[208:211], v[172:175], v[236:239], v[208:211]\n\t" \
    "ds_read_b128 v[244:247], %[wl] offset:4096\n\t" \
    "v_mfma_f32_16x16x32_bf16 v[200:203], v[248:251], v[236:239], v[200:203]\n\t" \
    "v_mfma_f32_16x16x32_bf16 v[216:219], v[164:167], v[240:243], v[216:219]\n\t" \
    "ds_read_b128 v[236:239], v252 offset:17408\n\t" \
    "v_mfma_f32_16x16x32_bf16 v[220:223], v[172:175], v[240:243], v[220:223]\n\t" \
    "v_mfma_f32_16x16x32_bf16 v[212:215], v[248:251], v[240:243], v[212:215]\n\t" \
    "s_waitcnt lgkmcnt(4)\n\t" \
    "v_mfma_f32_16x16x32_bf16 v[204:207], v[80:83], v[224:227], v[204:207]\n\t" \
    "v_xor_b32 v252, 0x2800, %[xa]\n\t" \
    "ds_read_b128 v[240:243], v252 offset:0\n\t" \
    "v_mfma_f32_16x16x32_bf16 v[208:211], v[88:91], v[224:227], v[208:211]\n\t" \
    "ds_read_b128 v[248:251], %[wl] offset:5120\n\t" \
    "s_waitcnt lgkmcnt(3)\n\t" \
    "v_mfma_f32_16x16x32_bf16 v[200:203], v[244:247], v[224:227], v[200:203]\n\t" \
    "v_mfma_f32_16x16x32_bf16 v[216:219], v[80:83], v[228:231], v[216:219]\n\t" \
    "ds_read_b128 v[224:227], v252 offset:16384\n\t" \
    "v_mfma_f32_16x16x32_bf16 v[220:223], v[88:91], v[228:231], v[220:223]\n\t" \
    "v_mfma_f32_16x16x32_bf16 v[212:215], v[244:247], v[228:231], v[212:215]\n\t" \
    "v_mfma_f32_16x16x32_bf16 v[204:207], v[84:87], v[232:235], v[204:207]\n\t" \
    "ds_read_b128 v[228:231], v252 offset:1024\n\t" \
    "v_mfma_f32_16x16x32_bf16 v[208:211], v[92:95], v[232:235], v[208:211]\n\t" \
    "ds_read_b128 v[244:247], %[wl] offset:6144\n\t" \
    "s_waitcnt lgkmcnt(3)\n\t" \
    "v_mfma_f32_16x16x32_bf16 v[200:203], v[248:251], v[232:235], v[200:203]\n\t" \
    "v_mfma_f32_16x16x32_bf16 v[216:219], v[84:87], v[236:239], v[216:219]\n\t" \
    "ds_read_b128 v[232:235], v252 offset:17408\n\t" \
    "v_mfma_f32_16x16x32_bf16 v[220:223], v[92:95], v[236:239], v[220:223]\n\t" \
    "v_mfma_f32_16x16x32_bf16 v[212:215], v[248:251], v[236:239], v[212:215]\n\t" \
    "v_mfma_f32_16x16x32_bf16 v[204:207], v[104:107], v[240:243], v[204:207]\n\t" \
    "v_xor_b32 v252, 0x3000, %[xa]\n\t" \
    "ds_read_b128 v[236:239], v252 offset:0\n\t" \
    "v_mfma_f32_16x16x32_bf16 v[208:211], v[112:115], v[240:243], v[208:211]\n\t" \
    "ds_read_b128 v[248:251], %[wl] offset:7168\n\t" \
    "v_mfma_f32_16x16x32_bf16 v[200:203], v[96:99], v[240:243], v[200:203]\n\t" \
    "s_waitcnt lgkmcnt(5)\n\t" \
    "v_mfma_f32_16x16x32_bf16 v[216:219], v[104:107], v[224:227], v[216:219]\n\t" \
    "ds_read_b128 v[240:243], v252 offset:16384\n\t" \
    "v_mfma_f32_16x16x32_bf16 v[220:223], v[112:115], v[224:227], v[220:223]\n\t" \
    "v_mfma_f32_16x16x32_bf16 v[212:215], v[96:99], v[224:227], v[212:215]\n\t" \
    "s_waitcnt lgkmcnt(5)\n\t" \
    "v_mfma_f32_16x16x32_bf16 v[204:207], v[108:111], v[228:231], v[204:207]\n\t" \
    "ds_read_b128 v[224:227], v252 offset:1024\n\t" \
    "v_mfma_f32_16x16x32_bf16 v[208:211], v[116:119], v[228:231], v[208:211]\n\t" \
    "v_mfma_f32_16x16x32_bf16 v[200:203], v[100:103], v[228:231], v[200:203]\n\t" \
    "s_waitcnt lgkmcnt(4)\n\t" \
    "v_mfma_f32_16x16x32_bf16 v[216:219], v[108:111], v[232:235], v[216:219]\n\t" \
    "ds_read_b128 v[228:231], v252 offset:17408\n\t" \
    "v_mfma_f32_16x16x32_bf16 v[220:223], v[116:119], v[232:235], v[220:223]\n\t" \
    "v_mfma_f32_16x16x32_bf16 v[212:215], v[100:103], v[232:235], v[212:215]\n\t" \
    "s_waitcnt lgkmcnt(4)\n\t" \
    "v_mfma_f32_16x16x32_bf16 v[204:207], v[120:123], v[236:239], v[204:207]\n\t" \
    "v_xor_b32 v252, 0x3800, %[xa]\n\t" \
    "ds_read_b128 v[232:235], v252 offset:0\n\t" \
    "v_mfma_f32_16x16x32_bf16 v[208:211], v[128:131], v[236:239], v[208:211]\n\t" \
    "v_mfma_f32_16x16x32_bf16 v[200:203], v[244:247], v[236:239], v[200:203]\n\t" \
    "s_waitcnt lgkmcnt(3)\n\t" \
    "v_mfma_f32_16x16x32_bf16 v[216:219], v[120:123], v[240:243], v[216:219]\n\t" \
    "ds_read_b128 v[236:239], v252 offset:16384\n\t" \
    "v_mfma_f32_16x16x32_bf16 v[220:223], v[128:131], v[240:243], v[220:223]\n\t" \
    "v_mfma_f32_16x16x32_bf16 v[212:215], v[244:247], v[240:243], v[212:215]\n\t" \
    "s_waitcnt lgkmcnt(3)\n\t" \
    "v_mfma_f32_16x16x32_bf16 v[204:207], v[124:127], v[224:227], v[204:207]\n\t" \
    "ds_read_b128 v[240:243], v252 offset:1024\n\t" \
    "v_mfma_f32_16x16x32_bf16 v[208:211], v[132:135], v[224:227], v[208:211]\n\t" \
    "v_mfma_f32_16x16x32_bf16 v[200:203], v[248:251], v[224:227], v[200:203]\n\t" \
    "s_waitcnt lgkmcnt(3)\n\t" \
    "v_mfma_f32_16x16x32_bf16 v[216:219], v[124:127], v[228:231], v[216:219]\n\t" \
    "ds_read_b128 v[224:227], v252 offset:17408\n\t" \
    "v_mfma_f32_16x16x32_bf16 v[220:223], v[132:135], v[228:231], v[220:223]\n\t" \
    "v_mfma_f32_16x16x32_bf16 v[212:215], v[248:251], v[228:231], v[212:215]\n\t" \
    "s_waitcnt lgkmcnt(3)\n\t" \
    "v_mfma_f32_16x16x32_bf16 v[204:207], v[144:147], v[232:235], v[204:207]\n\t" \
    "v_and_b32 v252, 0x3ff, %[xa]\n\t" \
    "ds_read_b128 v[228:231], v252 offset:43008\n\t" \
    "v_mov_b64 v[244:245], 0\n\t" \
    "v_mfma_f32_16x16x32_bf16 v[208:211], v[152:155], v[232:235], v[208:211]\n\t" \
    "v_mov_b64 v[246:247], 0\n\t" \
    "v_mfma_f32_16x16x32_bf16 v[200:203], v[136:139], v[232:235], v[200:203]\n\t" \
    "v_mov_b64 v[248:249], 0\n\t" \
    "s_waitcnt lgkmcnt(3)\n\t" \
    "v_mfma_f32_16x16x32_bf16 v[216:219], v[144:147], v[236:239], v[216:219]\n\t" \
    "ds_read_b128 v[232:235], v252 offset:44032\n\t" \
    "v_mov_b64 v[250:251], 0\n\t" \
    "v_mfma_f32_16x16x32_bf16 v[220:223], v[152:155], v[236:239], v[220:223]\n\t" \
    "v_mfma_f32_16x16x32_bf16 v[212:215], v[136:139], v[236:239], v[212:215]\n\t" \
    "s_waitcnt lgkmcnt(3)\n\t" \
    "v_mfma_f32_16x16x32_bf16 v[204:207], v[148:151], v[240:243], v[204:207]\n\t" \
    "ds_read_b128 v[236:239], v252 offset:47104\n\t" \
    "v_mfma_f32_16x16x32_bf16 v[208:211], v[156:159], v[240:243], v[208:211]\n\t" \
    "v_mfma_f32_16x16x32_bf16 v[200:203], v[140:143], v[240:243], v[200:203]\n\t" \
    "s_waitcnt lgkmcnt(3)\n\t" \
    "v_mfma_f32_16x16x32_bf16 v[216:219], v[148:151], v[224:227], v[216:219]\n\t" \
    "ds_read_b128 v[240:243], v252 offset:48128\n\t" \
    "v_mfma_f32_16x16x32_bf16 v[220:223], v[156:159], v[224:227], v[220:223]\n\t" \
    "v_mfma_f32_16x16x32_bf16 v[212:215], v[140:143], v[224:227], v[212:215]\n\t" \
    "s_waitcnt lgkmcnt(3)\n\t" \
    "v_mfma_f32_16x16x32_bf16 v[204:207], v[184:187], v[228:231], v[204:207]\n\t" \
    "v_add_u32 v252, %[qs], v252\n\t" \
    "v_mfma_f32_16x16x32_bf16 v[208:211], v[192:195], v[228:231], v[208:211]\n\t" \
    "v_mov_b64 v[224:225], 0\n\t" \
    "v_mfma_f32_16x16x32_bf16 v[200:203], v[176:179], v[228:231], v[200:203]\n\t" \
    "v_mov_b64 v[226:227], 0\n\t" \
    "s_waitcnt lgkmcnt(2)\n\t" \
    "v_mfma_f32_16x16x32_bf16 v[204:207], v[188:191], v[232:235], v[204:207]\n\t" \
    "v_mfma_f32_16x16x32_bf16 v[208:211], v[196:199], v[232:235], v[208:211]\n\t" \
    "v_mfma_f32_16x16x32_bf16 v[200:203], v[180:183], v[232:235], v[200:203]\n\t" \
    "s_waitcnt lgkmcnt(1)\n\t" \
    "v_mfma_f32_16x16x32_bf16 v[216:219], v[184:187], v[236:239], v[216:219]\n\t" \
    "ds_read_b128 v[228:231], %[wl] offset:2048\n\t" \
    "ds_read_b128 v[232:235], %[wl] offset:3072\n\t" \
    "v_mfma_f32_16x16x32_bf16 v[220:223], v[192:195], v[236:239], v[220:223]\n\t" \
    "v_cvt_pk_bf16_f32 v204, v204, v205\n\t" \
    "v_cvt_pk_bf16_f32 v205, v206, v207\n\t" \
    "v_mfma_f32_16x16x32_bf16 v[212:215], v[176:179], v[236:239], v[212:215]\n\t" \
    "ds_write_b64 v252, v[204:205] offset:1024\n\t" \
    "v_cvt_pk_bf16_f32 v208, v208, v209\n\t" \
    "s_waitcnt lgkmcnt(3)\n\t" \
    "v_mfma_f32_16x16x32_bf16 v[216:219], v[188:191], v[240:243], v[216:219]\n\t" \
    "v_cvt_pk_bf16_f32 v209, v210, v211\n\t" \
    "ds_write_b64 v252, v[208:209] offset:2048\n\t" \
    "v_mfma_f32_16x16x32_bf16 v[220:223], v[196:199], v[240:243], v[220:223]\n\t" \
    "v_cvt_pk_bf16_f32 v200, v200, v201\n\t" \
    "v_cvt_pk_bf16_f32 v201, v202, v203\n\t" \
    "v_mfma_f32_16x16x32_bf16 v[212:215], v[180:183], v[240:243], v[212:215]\n\t" \
    "ds_write_b64 v252, v[200:201] offset:0\n\t" \
    "ds_read_b128 v[200:203], %[wl] offset:0\n\t" \
    "ds_read_b128 v[204:207], %[wl] offset:1024\n\t" \
    "v_cvt_pk_bf16_f32 v216, v216, v217\n\t" \
    "v_cvt_pk_bf16_f32 v217, v218, v219\n\t" \
    "ds_write_b64 v252, v[216:217] offset:7168\n\t" \
    "v_cvt_pk_bf16_f32 v220, v220, v221\n\t" \
    "v_cvt_pk_bf16_f32 v221, v222, v223\n\t" \
    "ds_write_b64 v252, v[220:221] offset:8192\n\t" \
    "v_cvt_pk_bf16_f32 v212, v212, v213\n\t" \
    "v_cvt_pk_bf16_f32 v213, v214, v215\n\t" \
    "ds_write_b64 v252, v[212:213] offset:6144\n\t" \
    "s_waitcnt lgkmcnt(0)\n\t"
#define ASM_XG_S_HI \
    "s_waitcnt lgkmcnt(0)\n\t" \
    "v_xor_b32 v252, 0x2000, %[xa]\n\t" \
    "ds_read_b128 v[224:227], v252 offset:0\n\t" \
    "ds_read_b128 v[244:247], %[wl] offset:4096\n\t" \
    "ds_read_b128 v[228:231], v252 offset:16384\n\t" \
    "ds_read_b128 v[232:235], v252 offset:1024\n\t" \
    "ds_read_b128 v[236:239], v252 offset:17408\n\t" \
    "ds_read_b128 v[248:251], %[wl] offset:5120\n\t" \
    "s_cmp_eq_u32 %[slp], 0\n\t" \
    "s_cbranch_scc1 .Lxs%=\n\t" \
    "s_sleep 10\n\t" \
    ".Lxs%=:\n\t" \
    "s_waitcnt lgkmcnt(5)\n\t" \
    "v_mfma_f32_16x16x32_bf16 v[204:207], v[80:83], v[224:227], 0\n\t" \
    "v_xor_b32 v252, 0x2800, %[xa]\n\t" \
    "ds_read_b128 v[240:243], v252 offset:0\n\t" \
    "v_mfma_f32_16x16x32_bf16 v[208:211], v[88:91], v[224:227], 0\n\t" \
    "s_waitcnt lgkmcnt(5)\n\t" \
    "v_mfma_f32_16x16x32_bf16 v[200:203], v[244:247], v[224:227], 0\n\t" \
    "s_waitcnt lgkmcnt(4)\n\t" \
    "v_mfma_f32_16x16x32_bf16 v[216:219], v[80:83], v[228:231], 0\n\t" \
    "ds_read_b128 v[224:227], v252 offset:16384\n\t" \
    "v_mfma_f32_16x16x32_bf16 v[220:223], v[88:91], v[228:231], 0\n\t" \
    "v_mfma_f32_16x16x32_bf16 v[212:215], v[244:247], v[228:231], 0\n\t" \
    "s_waitcnt lgkmcnt(4)\n\t" \
    "v_mfma_f32_16x16x32_bf16 v[204:207], v[84:87], v[232:235], v[204:207]\n\t" \
    "ds_read_b128 v[228:231], v252 offset:1024\n\t" \
    "v_mfma_f32_16x16x32_bf16 v[208:211], v[92:95], v[232:235], v[208:211]\n\t" \
    "ds_read_b128 v[244:247], %[wl] offset:6144\n\t" \
    "s_waitcnt lgkmcnt(4)\n\t" \
    "v_mfma_f32_16x16x32_bf16 v[200:203], v[248:251], v[232:235], v[200:203]\n\t" \
    "v_mfma_f32_16x16x32_bf16 v[216:219], v[84:87], v[236:239], v[216:219]\n\t" \
    "ds_read_b128 v[232:235], v252 offset:17408\n\t" \
    "v_mfma_f32_16x16x32_bf16 v[220:223], v[92:95], v[236:239], v[220:223]\n\t" \
    "v_mfma_f32_16x16x32_bf16 v[212:215], v[248:251], v[236:239], v[212:215]\n\t" \
    "s_waitcnt lgkmcnt(4)\n\t" \
    "v_mfma_f32_16x16x32_bf16 v[204:207], v[104:107], v[240:243], v[204:207]\n\t" \
    "v_xor_b32 v252, 0x3000, %[xa]\n\t" \
    "ds_read_b128 v[236:239], v252 offset:0\n\t" \
    "v_mfma_f32_16x16x32_bf16 v[208:211], v[112:115], v[240:243], v[208:211]\n\t" \
    "ds_read_b128 v[248:251], %[wl] offset:7168\n\t" \
    "v_mfma_f32_16x16x32_bf16 v[200:203], v[96:99], v[240:243], v[200:203]\n\t" \
    "s_waitcnt lgkmcnt(5)\n\t" \
    "v_mfma_f32_16x16x32_bf16 v[216:219], v[104:107], v[224:227], v[216:219]\n\t" \
    "ds_read_b128 v[240:243], v252 offset:16384\n\t" \
    "v_mfma_f32_16x16x32_bf16 v[220:223], v[112:115], v[224:227], v[220:223]\n\t" \
    "v_mfma_f32_16x16x32_bf16 v[212:215], v[96:99], v[224:227], v[212:215]\n\t" \
    "s_waitcnt lgkmcnt(5)\n\t" \
    "v_mfma_f32_16x16x32_bf16 v[204:207], v[108:111], v[228:231], v[204:207]\n\t" \
    "ds_read_b128 v[224:227], v252 offset:1024\n\t" \
    "v_mfma_f32_16x16x32_bf16 v[208:211], v[116:119], v[228:231], v[208:211]\n\t" \
    "v_mfma_f32_16x16x32_bf16 v[200:203], v[100:103], v[228:231], v[200:203]\n\t" \
    "s_waitcnt lgkmcnt(4)\n\t" \
    "v_mfma_f32_16x16x32_bf16 v[216:219], v[108:111], v[232:235], v[216:219]\n\t" \
    "ds_read_b128 v[228:231], v252 offset:17408\n\t" \
    "v_mfma_f32_16x16x32_bf16 v[220:223], v[116:119], v[232:235], v[220:223]\n\t" \
    "v_mfma_f32_16x16x32_bf16 v[212:215], v[100:103], v[232:235], v[212:215]\n\t" \
    "s_waitcnt lgkmcnt(4)\n\t" \
    "v_mfma_f32_16x16x32_bf16 v[204:207], v[120:123], v[236:239], v[204:207]\n\t" \
    "v_xor_b32 v252, 0x3800, %[xa]\n\t" \
    "ds_read_b128 v[232:235], v252 offset:0\n\t" \
    "v_mfma_f32_16x16x32_bf16 v[208:211], v[128:131], v[236:239], v[208:211]\n\t" \
    "v_mfma_f32_16x16x32_bf16 v[200:203], v[244:247], v[236:239], v[200:203]\n\t" \
    "s_waitcnt lgkmcnt(3)\n\t" \
    "v_mfma_f32_16x16x32_bf16 v[216:219], v[120:123], v[240:243], v[216:219]\n\t" \
    "ds_read_b128 v[236:239], v252 offset:16384\n\t" \
    "v_mfma_f32_16x16x32_bf16 v[220:223], v[128:131], v[240:243], v[220:223]\n\t" \
    "v_mfma_f32_16x16x32_bf16 v[212:215], v[244:247], v[240:243], v[212:215]\n\t" \
    "s_waitcnt lgkmcnt(3)\n\t" \
    "v_mfma_f32_16x16x32_bf16 v[204:207], v[124:127], v[224:227], v[204:207]\n\t" \
    "ds_read_b128 v[240:243], v252 offset:1024\n\t" \
    "v_mfma_f32_16x16x32_bf16 v[208:211], v[132:135], v[224:227], v[208:211]\n\t" \
    "ds_read_b128 v[244:247], %[wl] offset:8192\n\t" \
    "v_mfma_f32_16x16x32_bf16 v[200:203], v[248:251], v[224:227], v[200:203]\n\t" \
    "s_waitcnt lgkmcnt(4)\n\t" \
    "v_mfma_f32_16x16x32_bf16 v[216:219], v[124:127], v[228:231], v[216:219]\n\t" \
    "ds_read_b128 v[224:227], v252 offset:17408\n\t" \
    "v_mfma_f32_16x16x32_bf16 v[220:223], v[132:135], v[228:231], v[220:223]\n\t" \
    "v_mfma_f32_16x16x32_bf16 v[212:215], v[248:251], v[228:231], v[212:215]\n\t" \
    "s_waitcnt lgkmcnt(4)\n\t" \
    "v_mfma_f32_16x16x32_bf16 v[204:207], v[144:147], v[232:235], v[204:207]\n\t" \
    "v_and_b32 v252, 0x3ff, %[xa]\n\t" \
    "ds_read_b128 v[228:231], v252 offset:40960\n\t" \
    "v_mfma_f32_16x16x32_bf16 v[208:211], v[152:155], v[232:235], v[208:211]\n\t" \
    "ds_read_b128 v[248:251], %[wl] offset:9216\n\t" \
    "v_mfma_f32_16x16x32_bf16 v[200:203], v[136:139], v[232:235], v[200:203]\n\t" \
    "s_waitcnt lgkmcnt(5)\n\t" \
    "v_mfma_f32_16x16x32_bf16 v[216:219], v[144:147], v[236:239], v[216:219]\n\t" \
    "ds_read_b128 v[232:235], v252 offset:45056\n\t" \
    "v_mfma_f32_16x16x32_bf16 v[220:223], v[152:155], v[236:239], v[220:223]\n\t" \
    "v_mfma_f32_16x16x32_bf16 v[212:215], v[136:139], v[236:239], v[212:215]\n\t" \
    "s_waitcnt lgkmcnt(5)\n\t" \
    "v_mfma_f32_16x16x32_bf16 v[204:207], v[148:151], v[240:243], v[204:207]\n\t" \
    "ds_read_b128 v[236:239], v252 offset:41984\n\t" \
    "v_mfma_f32_16x16x32_bf16 v[208:211], v[156:159], v[240:243], v[208:211]\n\t" \
    "v_mfma_f32_16x16x32_bf16 v[200:203], v[140:143], v[240:243], v[200:203]\n\t" \
    "s_waitcnt lgkmcnt(4)\n\t" \
    "v_mfma_f32_16x16x32_bf16 v[216:219], v[148:151], v[224:227], v[216:219]\n\t" \
    "ds_read_b128 v[240:243], v252 offset:46080\n\t" \
    "v_mfma_f32_16x16x32_bf16 v[220:223], v[156:159], v[224:227], v[220:223]\n\t" \
    "v_mfma_f32_16x16x32_bf16 v[212:215], v[140:143], v[224:227], v[212:215]\n\t" \
    "s_waitcnt vmcnt(0) lgkmcnt(0)\n\t" \
    "s_barrier\n\t" ASM_ST \
    "s_mov_b32 m0, %[d0]\n\t" \
    "s_nop 0\n\t" \
    "global_load_lds_dwordx4 v252, %[g0] offset:0 sc1\n\t" \
    "global_load_lds_dwordx4 v252, %[g0] offset:1024 sc1\n\t" \
    "global_load_lds_dwordx4 v252, %[g0] offset:2048 sc1\n\t" \
    "global_load_lds_dwordx4 v252, %[g0] offset:3072 sc1\n\t" \
    "s_mov_b32 m0, %[d1]\n\t" \
    "s_nop 0\n\t" \
    "global_load_lds_dwordx4 v252, %[g1] sc1\n\t" \
    "v_mfma_f32_16x16x32_bf16 v[204:207], v[160:163], v[228:231], v[204:207]\n\t" \
    "ds_read_b128 v[224:227], %[xa] offset:0\n\t" \
    "v_mfma_f32_16x16x32_bf16 v[208:211], v[168:171], v[228:231], v[208:211]\n\t" \
    "v_mfma_f32_16x16x32_bf16 v[200:203], v[244:247], v[228:231], v[200:203]\n\t" \
    "v_mfma_f32_16x16x32_bf16 v[216:219], v[160:163], v[232:235], v[216:219]\n\t" \
    "ds_read_b128 v[228:231], %[xa] offset:16384\n\t" \
    "v_mfma_f32_16x16x32_bf16 v[220:223], v[168:171], v[232:235], v[220:223]\n\t" \
    "v_mfma_f32_16x16x32_bf16 v[212:215], v[244:247], v[232:235], v[212:215]\n\t" \
    "v_mfma_f32_16x16x32_bf16 v[204:207], v[164:167], v[236:239], v[204:207]\n\t" \
    "ds_read_b128 v[232:235], %[xa] offset:1024\n\t" \
    "v_mfma_f32_16x16x32_bf16 v[208:211], v[172:175], v[236:239], v[208:211]\n\t" \
    "ds_read_b128 v[244:247], %[wl] offset:0\n\t" \
    "v_mfma_f32_16x16x32_bf16 v[200:203], v[248:251], v[236:239], v[200:203]\n\t" \
    "v_mfma_f32_16x16x32_bf16 v[216:219], v[164:167], v[240:243], v[216:219]\n\t" \
    "ds_read_b128 v[236:239], %[xa] offset:17408\n\t" \
    "v_mfma_f32_16x16x32_bf16 v[220:223], v[172:175], v[240:243], v[220:223]\n\t" \
    "v_mfma_f32_16x16x32_bf16 v[212:215], v[248:251], v[240:243], v[212:215]\n\t" \
    "s_waitcnt lgkmcnt(4)\n\t" \
    "v_mfma_f32_16x16x32_bf16 v[204:207], v[0:3], v[224:227], v[204:207]\n\t" \
    "v_xor_b32 v252, 0x800, %[xa]\n\t" \
    "ds_read_b128 v[240:243], v252 offset:0\n\t" \
    "v_mfma_f32_16x16x32_bf16 v[208:211], v[8:11], v[224:227], v[208:211]\n\t" \
    "ds_read_b128 v[248:251], %[wl] offset:1024\n\t" \
    "s_waitcnt lgkmcnt(3)\n\t" \
    "v_mfma_f32_16x16x32_bf16 v[200:203], v[244:247], v[224:227], v[200:203]\n\t" \
    "v_mfma_f32_16x16x32_bf16 v[216:219], v[0:3], v[228:231], v[216:219]\n\t" \
    "ds_read_b128 v[224:227], v252 offset:16384\n\t" \
    "v_mfma_f32_16x16x32_bf16 v[220:223], v[8:11], v[228:231], v[220:223]\n\t" \
    "v_mfma_f32_16x16x32_bf16 v[212:215], v[244:247], v[228:231], v[212:215]\n\t" \
    "v_mfma_f32_16x16x32_bf16 v[204:207], v[4:7], v[232:235], v[204:207]\n\t" \
    "ds_read_b128 v[228:231], v252 offset:1024\n\t" \
    "v_mfma_f32_16x16x32_bf16 v[208:211], v[12:15], v[232:235], v[208:211]\n\t" \
    "ds_read_b128 v[244:247], %[wl] offset:2048\n\t" \
    "s_waitcnt lgkmcnt(3)\n\t" \
    "v_mfma_f32_16x16x32_bf16 v[200:203], v[248:251], v[232:235], v[200:203]\n\t" \
    "v_mfma_f32_16x16x32_bf16 v[216:219], v[4:7], v[236:239], v[216:219]\n\t" \
    "ds_read_b128 v[232:235], v252 offset:17408\n\t" \
    "v_mfma_f32_16x16x32_bf16 v[220:223], v[12:15], v[236:239], v[220:223]\n\t" \
    "v_mfma_f32_16x16x32_bf16 v[212:215], v[248:251], v[236:239], v[212:215]\n\t" \
    "v_mfma_f32_16x16x32_bf16 v[204:207], v[24:27], v[240:243], v[204:207]\n\t" \
    "v_xor_b32 v252, 0x1000, %[xa]\n\t" \
    "ds_read_b128 v[236:239], v252 offset:0\n\t" \
    "v_mfma_f32_16x16x32_bf16 v[208:211], v[32:35], v[240:243], v[208:211]\n\t" \
    "ds_read_b128 v[248:251], %[wl] offset:3072\n\t" \
    "v_mfma_f32_16x16x32_bf16 v[200:203], v[16:19], v[240:243], v[200:203]\n\t" \
    "s_waitcnt lgkmcnt(5)\n\t" \
    "v_mfma_f32_16x16x32_bf16 v[216:219], v[24:27], v[224:227], v[216:219]\n\t" \
    "ds_read_b128 v[240:243], v252 offset:16384\n\t" \
    "v_mfma_f32_16x16x32_bf16 v[220:223], v[32:35], v[224:227], v[220:223]\n\t" \
    "v_mfma_f32_16x16x32_bf16 v[212:215], v[16:19], v[224:227], v[212:215]\n\t" \
    "s_waitcnt lgkmcnt(5)\n\t" \
    "v_mfma_f32_16x16x32_bf16 v[204:207], v[28:31], v[228:231], v[204:207]\n\t" \
    "ds_read_b128 v[224:227], v252 offset:1024\n\t" \
    "v_mfma_f32_16x16x32_bf16 v[208:211], v[36:39], v[228:231], v[208:211]\n\t" \
    "v_mfma_f32_16x16x32_bf16 v[200:203], v[20:23], v[228:231], v[200:203]\n\t" \
    "s_waitcnt lgkmcnt(4)\n\t" \
    "v_mfma_f32_16x16x32_bf16 v[216:219], v[28:31], v[232:235], v[216:219]\n\t" \
    "ds_read_b128 v[228:231], v252 offset:17408\n\t" \
    "v_mfma_f32_16x16x32_bf16 v[220:223], v[36:39], v[232:235], v[220:223]\n\t" \
    "v_mfma_f32_16x16x32_bf16 v[212:215], v[20:23], v[232:235], v[212:215]\n\t" \
    "s_waitcnt lgkmcnt(4)\n\t" \
    "v_mfma_f32_16x16x32_bf16 v[204:207], v[40:43], v[236:239], v[204:207]\n\t" \
    "v_xor_b32 v252, 0x1800, %[xa]\n\t" \
    "ds_read_b128 v[232:235], v252 offset:0\n\t" \
    "v_mfma_f32_16x16x32_bf16 v[208:211], v[48:51], v[236:239], v[208:211]\n\t" \
    "v_mfma_f32_16x16x32_bf16 v[200:203], v[244:247], v[236:239], v[200:203]\n\t" \
    "s_waitcnt lgkmcnt(3)\n\t" \
    "v_mfma_f32_16x16x32_bf16 v[216:219], v[40:43], v[240:243], v[216:219]\n\t" \
    "ds_read_b128 v[236:239], v252 offset:16384\n\t" \
    "v_mfma_f32_16x16x32_bf16 v[220:223], v[48:51], v[240:243], v[220:223]\n\t" \
    "v_mfma_f32_16x16x32_bf16 v[212:215], v[244:247], v[240:243], v[212:215]\n\t" \
    "s_waitcnt lgkmcnt(3)\n\t" \
    "v_mfma_f32_16x16x32_bf16 v[204:207], v[44:47], v[224:227], v[204:207]\n\t" \
    "ds_read_b128 v[240:243], v252 offset:1024\n\t" \
    "v_mfma_f32_16x16x32_bf16 v[208:211], v[52:55], v[224:227], v[208:211]\n\t" \
    "v_mfma_f32_16x16x32_bf16 v[200:203], v[248:251], v[224:227], v[200:203]\n\t" \
    "s_waitcnt lgkmcnt(3)\n\t" \
    "v_mfma_f32_16x16x32_bf16 v[216:219], v[44:47], v[228:231], v[216:219]\n\t" \
    "ds_read_b128 v[224:227], v252 offset:17408\n\t" \
    "v_mfma_f32_16x16x32_bf16 v[220:223], v[52:55], v[228:231], v[220:223]\n\t" \
    "v_mfma_f32_16x16x32_bf16 v[212:215], v[248:251], v[228:231], v[212:215]\n\t" \
    "s_waitcnt lgkmcnt(3)\n\t" \
    "v_mfma_f32_16x16x32_bf16 v[204:207], v[64:67], v[232:235], v[204:207]\n\t" \
    "v_and_b32 v252, 0x3ff, %[xa]\n\t" \
    "ds_read_b128 v[228:231], v252 offset:43008\n\t" \
    "v_mov_b64 v[244:245], 0\n\t" \
    "v_mfma_f32_16x16x32_bf16 v[208:211], v[72:75], v[232:235], v[208:211]\n\t" \
    "v_mov_b64 v[246:247], 0\n\t" \
    "v_mfma_f32_16x16x32_bf16 v[200:203], v[56:59], v[232:235], v[200:203]\n\t" \
    "v_mov_b64 v[248:249], 0\n\t" \
    "s_waitcnt lgkmcnt(3)\n\t" \
    "v_mfma_f32_16x16x32_bf16 v[216:219], v[64:67], v[236:239], v[216:219]\n\t" \
    "ds_read_b128 v[232:235], v252 offset:44032\n\t" \
    "v_mov_b64 v[250:251], 0\n\t" \
    "v_mfma_f32_16x16x32_bf16 v[220:223], v[72:75], v[236:239], v[220:223]\n\t" \
    "v_mfma_f32_16x16x32_bf16 v[212:215], v[56:59], v[236:239], v[212:215]\n\t" \
    "s_waitcnt lgkmcnt(3)\n\t" \
    "v_mfma_f32_16x16x32_bf16 v[204:207], v[68:71], v[240:243], v[204:207]\n\t" \
    "ds_read_b128 v[236:239], v252 offset:47104\n\t" \
    "v_mfma_f32_16x16x32_bf16 v[208:211], v[76:79], v[240:243], v[208:211]\n\t" \
    "v_mfma_f32_16x16x32_bf16 v[200:203], v[60:63], v[240:243], v[200:203]\n\t" \
    "s_waitcnt lgkmcnt(3)\n\t" \
    "v_mfma_f32_16x16x32_bf16 v[216:219], v[68:71], v[224:227], v[216:219]\n\t" \
    "ds_read_b128 v[240:243], v252 offset:48128\n\t" \
    "v_mfma_f32_16x16x32_bf16 v[220:223], v[76:79], v[224:227], v[220:223]\n\t" \
    "v_mfma_f32_16x16x32_bf16 v[212:215], v[60:63], v[224:227], v[212:215]\n\t" \
    "s_waitcnt lgkmcnt(3)\n\t" \
    "v_mfma_f32_16x16x32_bf16 v[204:207], v[184:187], v[228:231], v[204:207]\n\t" \
    "v_add_u32 v252, %[qs], v252\n\t" \
    "v_mfma_f32_16x16x32_bf16 v[208:211], v[192:195], v[228:231], v[208:211]\n\t" \
    "v_mov_b64 v[224:225], 0\n\t" \
    "v_mfma_f32_16x16x32_bf16 v[200:203], v[176:179], v[228:231], v[200:203]\n\t" \
    "v_mov_b64 v[226:227], 0\n\t" \
    "s_waitcnt lgkmcnt(2)\n\t" \
    "v_mfma_f32_16x16x32_bf16 v[204:207], v[188:191], v[232:235], v[204:207]\n\t" \
    "v_mfma_f32_16x16x32_bf16 v[208:211], v[196:199], v[232:235], v[208:211]\n\t" \
    "v_mfma_f32_16x16x32_bf16 v[200:203], v[180:183], v[232:235], v[200:203]\n\t" \
    "s_waitcnt lgkmcnt(1)\n\t" \
    "v_mfma_f32_16x16x32_bf16 v[216:219], v[184:187], v[236:239], v[216:219]\n\t" \
    "ds_read_b128 v[228:231], %[wl] offset:2048\n\t" \
    "ds_read_b128 v[232:235], %[wl] offset:3072\n\t" \
    "v_mfma_f32_16x16x32_bf16 v[220:223], v[192:195], v[236:239], v[220:223]\n\t" \
    "v_cvt_pk_bf16_f32 v204, v204, v205\n\t" \
    "v_cvt_pk_bf16_f32 v205, v206, v207\n\t" \
    "v_mfma_f32_16x16x32_bf16 v[212:215], v[176:179], v[236:239], v[212:215]\n\t" \
    "ds_write_b64 v252, v[204:205] offset:1024\n\t" \
    "v_cvt_pk_bf16_f32 v208, v208, v209\n\t" \
    "s_waitcnt lgkmcnt(3)\n\t" \
    "v_mfma_f32_16x16x32_bf16 v[216:219], v[188:191], v[240:243], v[216:219]\n\t" \
    "v_cvt_pk_bf16_f32 v209, v210, v211\n\t" \
    "ds_write_b64 v252, v[208:209] offset:2048\n\t" \
    "v_mfma_f32_16x16x32_bf16 v[220:223], v[196:199], v[240:243], v[220:223]\n\t" \
    "v_cvt_pk_bf16_f32 v200, v200, v201\n\t" \
    "v_cvt_pk_bf16_f32 v201, v202, v203\n\t" \
    "v_mfma_f32_16x16x32_bf16 v[212:215], v[180:183], v[240:243], v[212:215]\n\t" \
    "ds_write_b64 v252, v[200:201] offset:0\n\t" \
    "ds_read_b128 v[200:203], %[wl] offset:0\n\t" \
    "ds_read_b128 v[204:207], %[wl] offset:1024\n\t" \
    "v_cvt_pk_bf16_f32 v216, v216, v217\n\t" \
    "v_cvt_pk_bf16_f32 v217, v218, v219\n\t" \
    "ds_write_b64 v252, v[216:217] offset:7168\n\t" \
    "v_cvt_pk_bf16_f32 v220, v220, v221\n\t" \
    "v_cvt_pk_bf16_f32 v221, v222, v223\n\t" \
    "ds_write_b64 v252, v[220:221] offset:8192\n\t" \
    "v_cvt_pk_bf16_f32 v212, v212, v213\n\t" \
    "v_cvt_pk_bf16_f32 v213, v214, v215\n\t" \
    "ds_write_b64 v252, v[212:213] offset:6144\n\t" \
    "s_waitcnt lgkmcnt(0)\n\t"
#define ASM_XG_S_LO2 \
    "s_waitcnt lgkmcnt(0)\n\t" \
    "ds_read_b128 v[224:227], %[xa] offset:0\n\t" \
    "ds_read_b128 v[228:231], %[xa] offset:16384\n\t" \
    "ds_read_b128 v[232:235], %[xa] offset:1024\n\t" \
    "ds_read_b128 v[236:239], %[xa] offset:17408\n\t" \
    "s_waitcnt lgkmcnt(3)\n\t" \
    "v_mfma_f32_16x16x32_bf16 v[204:207], v[0:3], v[224:227], 0\n\t" \
    "v_xor_b32 v252, 0x800, %[xa]\n\t" \
    "ds_read_b128 v[240:243], v252 offset:0\n\t" \
    "v_mfma_f32_16x16x32_bf16 v[208:211], v[8:11], v[224:227], 0\n\t" \
    "s_waitcnt lgkmcnt(3)\n\t" \
    "v_mfma_f32_16x16x32_bf16 v[216:219], v[0:3], v[228:231], 0\n\t" \
    "ds_read_b128 v[224:227], v252 offset:16384\n\t" \
    "v_mfma_f32_16x16x32_bf16 v[220:223], v[8:11], v[228:231], 0\n\t" \
    "s_waitcnt lgkmcnt(3)\n\t" \
    "v_mfma_f32_16x16x32_bf16 v[204:207], v[4:7], v[232:235], v[204:207]\n\t" \
    "ds_read_b128 v[228:231], v252 offset:1024\n\t" \
    "v_mfma_f32_16x16x32_bf16 v[208:211], v[12:15], v[232:235], v[208:211]\n\t" \
    "s_waitcnt lgkmcnt(3)\n\t" \
    "v_mfma_f32_16x16x32_bf16 v[216:219], v[4:7], v[236:239], v[216:219]\n\t" \
    "ds_read_b128 v[232:235], v252 offset:17408\n\t" \
    "v_mfma_f32_16x16x32_bf16 v[220:223], v[12:15], v[236:239], v[220:223]\n\t" \
    "s_waitcnt lgkmcnt(3)\n\t" \
    "v_mfma_f32_16x16x32_bf16 v[204:207], v[24:27], v[240:243], v[204:207]\n\t" \
    "v_xor_b32 v252, 0x1000, %[xa]\n\t" \
    "ds_read_b128 v[236:239], v252 offset:0\n\t" \
    "v_mfma_f32_16x16x32_bf16 v[208:211], v[32:35], v[240:243], v[208:211]\n\t" \
    "s_waitcnt lgkmcnt(3)\n\t" \
    "v_mfma_f32_16x16x32_bf16 v[216:219], v[24:27], v[224:227], v[216:219]\n\t" \
    "ds_read_b128 v[240:243], v252 offset:16384\n\t" \
    "v_mfma_f32_16x16x32_bf16 v[220:223], v[32:35], v[224:227], v[220:223]\n\t" \
    "s_waitcnt lgkmcnt(3)\n\t" \
    "v_mfma_f32_16x16x32_bf16 v[204:207], v[28:31], v[228:231], v[204:207]\n\t" \
    "ds_read_b128 v[224:227], v252 offset:1024\n\t" \
    "v_mfma_f32_16x16x32_bf16 v[208:211], v[36:39], v[228:231], v[208:211]\n\t" \
    "s_waitcnt lgkmcnt(3)\n\t" \
    "v_mfma_f32_16x16x32_bf16 v[216:219], v[28:31], v[232:235], v[216:219]\n\t" \
    "ds_read_b128 v[228:231], v252 offset:17408\n\t" \
    "v_mfma_f32_16x16x32_bf16 v[220:223], v[36:39], v[232:235], v[220:223]\n\t" \
    "s_waitcnt lgkmcnt(3)\n\t" \
    "v_mfma_f32_16x16x32_bf16 v[204:207], v[40:43], v[236:239], v[204:207]\n\t" \
    "v_xor_b32 v252, 0x1800, %[xa]\n\t" \
    "ds_read_b128 v[232:235], v252 offset:0\n\t" \
    "v_mfma_f32_16x16x32_bf16 v[208:211], v[48:51], v[236:239], v[208:211]\n\t" \
    "s_waitcnt lgkmcnt(3)\n\t" \
    "v_mfma_f32_16x16x32_bf16 v[216:219], v[40:43], v[240:243], v[216:219]\n\t" \
    "ds_read_b128 v[236:239], v252 offset:16384\n\t" \
    "v_mfma_f32_16x16x32_bf16 v[220:223], v[48:51], v[240:243], v[220:223]\n\t" \
    "s_waitcnt lgkmcnt(3)\n\t" \
    "v_mfma_f32_16x16x32_bf16 v[204:207], v[44:47], v[224:227], v[204:207]\n\t" \
    "ds_read_b128 v[240:243], v252 offset:1024\n\t" \
    "v_mfma_f32_16x16x32_bf16 v[208:211], v[52:55], v[224:227], v[208:211]\n\t" \
    "s_waitcnt lgkmcnt(3)\n\t" \
    "v_mfma_f32_16x16x32_bf16 v[216:219], v[44:47], v[228:231], v[216:219]\n\t" \
    "ds_read_b128 v[224:227], v252 offset:17408\n\t" \
    "v_mfma_f32_16x16x32_bf16 v[220:223], v[52:55], v[228:231], v[220:223]\n\t" \
    "s_waitcnt lgkmcnt(3)\n\t" \
    "v_mfma_f32_16x16x32_bf16 v[204:207], v[64:67], v[232:235], v[204:207]\n\t" \
    "v_and_b32 v252, 0x3ff, %[xa]\n\t" \
    "ds_read_b128 v[228:231], v252 offset:40960\n\t" \
    "v_mfma_f32_16x16x32_bf16 v[208:211], v[72:75], v[232:235], v[208:211]\n\t" \
    "s_waitcnt lgkmcnt(3)\n\t" \
    "v_mfma_f32_16x16x32_bf16 v[216:219], v[64:67], v[236:239], v[216:219]\n\t" \
    "ds_read_b128 v[232:235], v252 offset:45056\n\t" \
    "v_mfma_f32_16x16x32_bf16 v[220:223], v[72:75], v[236:239], v[220:223]\n\t" \
    "s_waitcnt lgkmcnt(3)\n\t" \
    "v_mfma_f32_16x16x32_bf16 v[204:207], v[68:71], v[240:243], v[204:207]\n\t" \
    "ds_read_b128 v[236:239], v252 offset:41984\n\t" \
    "v_mfma_f32_16x16x32_bf16 v[208:211], v[76:79], v[240:243], v[208:211]\n\t" \
    "s_waitcnt lgkmcnt(3)\n\t" \
    "v_mfma_f32_16x16x32_bf16 v[216:219], v[68:71], v[224:227], v[216:219]\n\t" \
    "ds_read_b128 v[240:243], v252 offset:46080\n\t" \
    "v_mfma_f32_16x16x32_bf16 v[220:223], v[76:79], v[224:227], v[220:223]\n\t" \
    "s_waitcnt lgkmcnt(0)\n\t" \
    "s_barrier\n\t" ASM_ST \
    "v_mfma_f32_16x16x32_bf16 v[204:207], v[160:163], v[228:231], v[204:207]\n\t" \
    "v_xor_b32 v252, 0x2000, %[xa]\n\t" \
    "ds_read_b128 v[224:227], v252 offset:0\n\t" \
    "v_mfma_f32_16x16x32_bf16 v[208:211], v[168:171], v[228:231], v[208:211]\n\t" \
    "v_mfma_f32_16x16x32_bf16 v[216:219], v[160:163], v[232:235], v[216:219]\n\t" \
    "ds_read_b128 v[228:231], v252 offset:16384\n\t" \
    "v_mfma_f32_16x16x32_bf16 v[220:223], v[168:171], v[232:235], v[220:223]\n\t" \
    "v_mfma_f32_16x16x32_bf16 v[204:207], v[164:167], v[236:239], v[204:207]\n\t" \
    "ds_read_b128 v[232:235], v252 offset:1024\n\t" \
    "v_mfma_f32_16x16x32_bf16 v[208:211], v[172:175], v[236:239], v[208:211]\n\t" \
    "v_mfma_f32_16x16x32_bf16 v[216:219], v[164:167], v[240:243], v[216:219]\n\t" \
    "ds_read_b128 v[236:239], v252 offset:17408\n\t" \
    "v_mfma_f32_16x16x32_bf16 v[220:223], v[172:175], v[240:243], v[220:223]\n\t" \
    "s_waitcnt lgkmcnt(3)\n\t" \
    "v_mfma_f32_16x16x32_bf16 v[204:207], v[80:83], v[224:227], v[204:207]\n\t" \
    "v_xor_b32 v252, 0x2800, %[xa]\n\t" \
    "ds_read_b128 v[240:243], v252 offset:0\n\t" \
    "v_mfma_f32_16x16x32_bf16 v[208:211], v[88:91], v[224:227], v[208:211]\n\t" \
    "s_waitcnt lgkmcnt(3)\n\t" \
    "v_mfma_f32_16x16x32_bf16 v[216:219], v[80:83], v[228:231], v[216:219]\n\t" \
    "ds_read_b128 v[224:227], v252 offset:16384\n\t" \
    "v_mfma_f32_16x16x32_bf16 v[220:223], v[88:91], v[228:231], v[220:223]\n\t" \
    "s_waitcnt lgkmcnt(3)\n\t" \
    "v_mfma_f32_16x16x32_bf16 v[204:207], v[84:87], v[232:235], v[204:207]\n\t" \
    "ds_read_b128 v[228:231], v252 offset:1024\n\t" \
    "v_mfma_f32_16x16x32_bf16 v[208:211], v[92:95], v[232:235], v[208:211]\n\t" \
    "s_waitcnt lgkmcnt(3)\n\t" \
    "v_mfma_f32_16x16x32_bf16 v[216:219], v[84:87], v[236:239], v[216:219]\n\t" \
    "ds_read_b128 v[232:235], v252 offset:17408\n\t" \
    "v_mfma_f32_16x16x32_bf16 v[220:223], v[92:95], v[236:239], v[220:223]\n\t" \
    "s_waitcnt lgkmcnt(3)\n\t" \
    "v_mfma_f32_16x16x32_bf16 v[204:207], v[104:107], v[240:243], v[204:207]\n\t" \
    "v_xor_b32 v252, 0x3000, %[xa]\n\t" \
    "ds_read_b128 v[236:239], v252 offset:0\n\t" \
    "v_mfma_f32_16x16x32_bf16 v[208:211], v[112:115], v[240:243], v[208:211]\n\t" \
    "s_waitcnt lgkmcnt(3)\n\t" \
    "v_mfma_f32_16x16x32_bf16 v[216:219], v[104:107], v[224:227], v[216:219]\n\t" \
    "ds_read_b128 v[240:243], v252 offset:16384\n\t" \
    "v_mfma_f32_16x16x32_bf16 v[220:223], v[112:115], v[224:227], v[220:223]\n\t" \
    "s_waitcnt lgkmcnt(3)\n\t" \
    "v_mfma_f32_16x16x32_bf16 v[204:207], v[108:111], v[228:231], v[204:207]\n\t" \
    "ds_read_b128 v[224:227], v252 offset:1024\n\t" \
    "v_mfma_f32_16x16x32_bf16 v[208:211], v[116:119], v[228:231], v[208:211]\n\t" \
    "s_waitcnt lgkmcnt(3)\n\t" \
    "v_mfma_f32_16x16x32_bf16 v[216:219], v[108:111], v[232:235], v[216:219]\n\t" \
    "ds_read_b128 v[228:231], v252 offset:17408\n\t" \
    "v_mfma_f32_16x16x32_bf16 v[220:223], v[116:119], v[232:235], v[220:223]\n\t" \
    "s_waitcnt lgkmcnt(3)\n\t" \
    "v_mfma_f32_16x16x32_bf16 v[204:207], v[120:123], v[236:239], v[204:207]\n\t" \
    "v_xor_b32 v252, 0x3800, %[xa]\n\t" \
    "ds_read_b128 v[232:235], v252 offset:0\n\t" \
    "v_mfma_f32_16x16x32_bf16 v[208:211], v[128:131], v[236:239], v[208:211]\n\t" \
    "s_waitcnt lgkmcnt(3)\n\t" \
    "v_mfma_f32_16x16x32_bf16 v[216:219], v[120:123], v[240:243], v[216:219]\n\t" \
    "ds_read_b128 v[236:239], v252 offset:16384\n\t" \
    "v_mfma_f32_16x16x32_bf16 v[220:223], v[128:131], v[240:243], v[220:223]\n\t" \
    "s_waitcnt lgkmcnt(3)\n\t" \
    "v_mfma_f32_16x16x32_bf16 v[204:207], v[124:127], v[224:227], v[204:207]\n\t" \
    "ds_read_b128 v[240:243], v252 offset:1024\n\t" \
    "v_mfma_f32_16x16x32_bf16 v[208:211], v[132:135], v[224:227], v[208:211]\n\t" \
    "s_waitcnt lgkmcnt(3)\n\t" \
    "v_mfma_f32_16x16x32_bf16 v[216:219], v[124:127], v[228:231], v[216:219]\n\t" \
    "ds_read_b128 v[224:227], v252 offset:17408\n\t" \
    "v_mfma_f32_16x16x32_bf16 v[220:223], v[132:135], v[228:231], v[220:223]\n\t" \
    "s_waitcnt lgkmcnt(3)\n\t" \
    "v_mfma_f32_16x16x32_bf16 v[204:207], v[144:147], v[232:235], v[204:207]\n\t" \
    "v_and_b32 v252, 0x3ff, %[xa]\n\t" \
    "ds_read_b128 v[228:231], v252 offset:43008\n\t" \
    "v_mov_b64 v[244:245], 0\n\t" \
    "v_mfma_f32_16x16x32_bf16 v[208:211], v[152:155], v[232:235], v[208:211]\n\t" \
    "v_mov_b64 v[246:247], 0\n\t" \
    "s_waitcnt lgkmcnt(3)\n\t" \
    "v_mfma_f32_16x16x32_bf16 v[216:219], v[144:147], v[236:239], v[216:219]\n\t" \
    "ds_read_b128 v[232:235], v252 offset:44032\n\t" \
    "v_mov_b64 v[248:249], 0\n\t" \
    "v_mfma_f32_16x16x32_bf16 v[220:223], v[152:155], v[236:239], v[220:223]\n\t" \
    "v_mov_b64 v[250:251], 0\n\t" \
    "s_waitcnt lgkmcnt(3)\n\t" \
    "v_mfma_f32_16x16x32_bf16 v[204:207], v[148:151], v[240:243], v[204:207]\n\t" \
    "ds_read_b128 v[236:239], v252 offset:47104\n\t" \
    "v_mfma_f32_16x16x32_bf16 v[208:211], v[156:159], v[240:243], v[208:211]\n\t" \
    "s_waitcnt lgkmcnt(3)\n\t" \
    "v_mfma_f32_16x16x32_bf16 v[216:219], v[148:151], v[224:227], v[216:219]\n\t" \
    "ds_read_b128 v[240:243], v252 offset:48128\n\t" \
    "v_mfma_f32_16x16x32_bf16 v[220:223], v[156:159], v[224:227], v[220:223]\n\t" \
    "s_waitcnt lgkmcnt(3)\n\t" \
    "v_mfma_f32_16x16x32_bf16 v[204:207], v[184:187], v[228:231], v[204:207]\n\t" \
    "v_add_u32 v252, %[qs], v252\n\t" \
    "v_mfma_f32_16x16x32_bf16 v[208:211], v[192:195], v[228:231], v[208:211]\n\t" \
    "v_mov_b64 v[224:225], 0\n\t" \
    "s_waitcnt lgkmcnt(2)\n\t" \
    "v_mfma_f32_16x16x32_bf16 v[204:207], v[188:191], v[232:235], v[204:207]\n\t" \
    "v_mov_b64 v[226:227], 0\n\t" \
    "v_mfma_f32_16x16x32_bf16 v[208:211], v[196:199], v[232:235], v[208:211]\n\t" \
    "s_waitcnt lgkmcnt(1)\n\t" \
    "v_mfma_f32_16x16x32_bf16 v[216:219], v[184:187], v[236:239], v[216:219]\n\t" \
    "ds_read_b128 v[228:231], %[wl] offset:2048\n\t" \
    "ds_read_b128 v[232:235], %[wl] offset:3072\n\t" \
    "v_mfma_f32_16x16x32_bf16 v[220:223], v[192:195], v[236:239], v[220:223]\n\t" \
    "v_cvt_pk_bf16_f32 v204, v204, v205\n\t" \
    "v_cvt_pk_bf16_f32 v205, v206, v207\n\t" \
    "s_waitcnt lgkmcnt(2)\n\t" \
    "v_mfma_f32_16x16x32_bf16 v[216:219], v[188:191], v[240:243], v[216:219]\n\t" \
    "ds_write_b64 v252, v[204:205] offset:1024\n\t" \
    "v_cvt_pk_bf16_f32 v208, v208, v209\n\t" \
    "v_mfma_f32_16x16x32_bf16 v[220:223], v[196:199], v[240:243], v[220:223]\n\t" \
    "v_cvt_pk_bf16_f32 v209, v210, v211\n\t" \
    "ds_write_b64 v252, v[208:209] offset:2048\n\t" \
    "ds_read_b128 v[200:203], %[wl] offset:0\n\t" \
    "ds_read_b128 v[204:207], %[wl] offset:1024\n\t" \
    "v_cvt_pk_bf16_f32 v216, v216, v217\n\t" \
    "v_cvt_pk_bf16_f32 v217, v218, v219\n\t" \
    "ds_write_b64 v252, v[216:217] offset:7168\n\t" \
    "s_nop 0\n\t" \
    "v_cvt_pk_bf16_f32 v220, v220, v221\n\t" \
    "v_cvt_pk_bf16_f32 v221, v222, v223\n\t" \
    "ds_write_b64 v252, v[220:221] offset:8192\n\t" \
    "s_waitcnt lgkmcnt(0)\n\t"
#define ASM_XG_S_LO1 \
    "s_waitcnt lgkmcnt(0)\n\t" \
    "ds_read_b128 v[224:227], %[xa] offset:0\n\t" \
    "ds_read_b128 v[228:231], %[xa] offset:16384\n\t" \
    "ds_read_b128 v[232:235], %[xa] offset:1024\n\t" \
    "ds_read_b128 v[236:239], %[xa] offset:17408\n\t" \
    "s_waitcnt lgkmcnt(3)\n\t" \
    "v_mfma_f32_16x16x32_bf16 v[208:211], v[8:11], v[224:227], 0\n\t" \
    "v_xor_b32 v252, 0x800, %[xa]\n\t" \
    "ds_read_b128 v[240:243], v252 offset:0\n\t" \
    "s_waitcnt lgkmcnt(3)\n\t" \
    "v_mfma_f32_16x16x32_bf16 v[220:223], v[8:11], v[228:231], 0\n\t" \
    "ds_read_b128 v[224:227], v252 offset:16384\n\t" \
    "s_waitcnt lgkmcnt(3)\n\t" \
    "v_mfma_f32_16x16x32_bf16 v[208:211], v[12:15], v[232:235], v[208:211]\n\t" \
    "ds_read_b128 v[228:231], v252 offset:1024\n\t" \
    "s_waitcnt lgkmcnt(3)\n\t" \
    "v_mfma_f32_16x16x32_bf16 v[220:223], v[12:15], v[236:239], v[220:223]\n\t" \
    "ds_read_b128 v[232:235], v252 offset:17408\n\t" \
    "s_waitcnt lgkmcnt(3)\n\t" \
    "v_mfma_f32_16x16x32_bf16 v[208:211], v[32:35], v[240:243], v[208:211]\n\t" \
    "v_xor_b32 v252, 0x1000, %[xa]\n\t" \
    "ds_read_b128 v[236:239], v252 offset:0\n\t" \
    "s_waitcnt lgkmcnt(3)\n\t" \
    "v_mfma_f32_16x16x32_bf16 v[220:223], v[32:35], v[224:227], v[220:223]\n\t" \
    "ds_read_b128 v[240:243], v252 offset:16384\n\t" \
    "s_waitcnt lgkmcnt(3)\n\t" \
    "v_mfma_f32_16x16x32_bf16 v[208:211], v[36:39], v[228:231], v[208:211]\n\t" \
    "ds_read_b128 v[224:227], v252 offset:1024\n\t" \
    "s_waitcnt lgkmcnt(3)\n\t" \
    "v_mfma_f32_16x16x32_bf16 v[220:223], v[36:39], v[232:235], v[220:223]\n\t" \
    "ds_read_b128 v[228:231], v252 offset:17408\n\t" \
    "s_waitcnt lgkmcnt(3)\n\t" \
    "v_mfma_f32_16x16x32_bf16 v[208:211], v[48:51], v[236:239], v[208:211]\n\t" \
    "v_xor_b32 v252, 0x1800, %[xa]\n\t" \
    "ds_read_b128 v[232:235], v252 offset:0\n\t" \
    "s_waitcnt lgkmcnt(3)\n\t" \
    "v_mfma_f32_16x16x32_bf16 v[220:223], v[48:51], v[240:243], v[220:223]\n\t" \
    "ds_read_b128 v[236:239], v252 offset:16384\n\t" \
    "s_waitcnt lgkmcnt(3)\n\t" \
    "v_mfma_f32_16x16x32_bf16 v[208:211], v[52:55], v[224:227], v[208:211]\n\t" \
    "ds_read_b128 v[240:243], v252 offset:1024\n\t" \
    "s_waitcnt lgkmcnt(3)\n\t" \
    "v_mfma_f32_16x16x32_bf16 v[220:223], v[52:55], v[228:231], v[220:223]\n\t" \
    "ds_read_b128 v[224:227], v252 offset:17408\n\t" \
    "s_waitcnt lgkmcnt(3)\n\t" \
    "v_mfma_f32_16x16x32_bf16 v[208:211], v[72:75], v[232:235], v[208:211]\n\t" \
    "v_and_b32 v252, 0x3ff, %[xa]\n\t" \
    "ds_read_b128 v[228:231], v252 offset:40960\n\t" \
    "s_waitcnt lgkmcnt(3)\n\t" \
    "v_mfma_f32_16x16x32_bf16 v[220:223], v[72:75], v[236:239], v[220:223]\n\t" \
    "ds_read_b128 v[232:235], v252 offset:45056\n\t" \
    "s_waitcnt lgkmcnt(3)\n\t" \
    "v_mfma_f32_16x16x32_bf16 v[208:211], v[76:79], v[240:243], v[208:211]\n\t" \
    "ds_read_b128 v[236:239], v252 offset:41984\n\t" \
    "s_waitcnt lgkmcnt(3)\n\t" \
    "v_mfma_f32_16x16x32_bf16 v[220:223], v[76:79], v[224:227], v[220:223]\n\t" \
    "ds_read_b128 v[240:243], v252 offset:46080\n\t" \
    "s_waitcnt lgkmcnt(0)\n\t" \
    "s_barrier\n\t" ASM_ST \
    "v_mfma_f32_16x16x32_bf16 v[208:211], v[168:171], v[228:231], v[208:211]\n\t" \
    "v_xor_b32 v252, 0x2000, %[xa]\n\t" \
    "ds_read_b128 v[224:227], v252 offset:0\n\t" \
    "v_mfma_f32_16x16x32_bf16 v[220:223], v[168:171], v[232:235], v[220:223]\n\t" \
    "ds_read_b128 v[228:231], v252 offset:16384\n\t" \
    "v_mfma_f32_16x16x32_bf16 v[208:211], v[172:175], v[236:239], v[208:211]\n\t" \
    "ds_read_b128 v[232:235], v252 offset:1024\n\t" \
    "v_mfma_f32_16x16x32_bf16 v[220:223], v[172:175], v[240:243], v[220:223]\n\t" \
    "ds_read_b128 v[236:239], v252 offset:17408\n\t" \
    "s_waitcnt lgkmcnt(3)\n\t" \
    "v_mfma_f32_16x16x32_bf16 v[208:211], v[88:91], v[224:227], v[208:211]\n\t" \
    "v_xor_b32 v252, 0x2800, %[xa]\n\t" \
    "ds_read_b128 v[240:243], v252 offset:0\n\t" \
    "s_waitcnt lgkmcnt(3)\n\t" \
    "v_mfma_f32_16x16x32_bf16 v[220:223], v[88:91], v[228:231], v[220:223]\n\t" \
    "ds_read_b128 v[224:227], v252 offset:16384\n\t" \
    "s_waitcnt lgkmcnt(3)\n\t" \
    "v_mfma_f32_16x16x32_bf16 v[208:211], v[92:95], v[232:235], v[208:211]\n\t" \
    "ds_read_b128 v[228:231], v252 offset:1024\n\t" \
    "s_waitcnt lgkmcnt(3)\n\t" \
    "v_mfma_f32_16x16x32_bf16 v[220:223], v[92:95], v[236:239], v[220:223]\n\t" \
    "ds_read_b128 v[232:235], v252 offset:17408\n\t" \
    "s_waitcnt lgkmcnt(3)\n\t" \
    "v_mfma_f32_16x16x32_bf16 v[208:211], v[112:115], v[240:243], v[208:211]\n\t" \
    "v_xor_b32 v252, 0x3000, %[xa]\n\t" \
    "ds_read_b128 v[236:239], v252 offset:0\n\t" \
    "s_waitcnt lgkmcnt(3)\n\t" \
    "v_mfma_f32_16x16x32_bf16 v[220:223], v[112:115], v[224:227], v[220:223]\n\t" \
    "ds_read_b128 v[240:243], v252 offset:16384\n\t" \
    "s_waitcnt lgkmcnt(3)\n\t" \
    "v_mfma_f32_16x16x32_bf16 v[208:211], v[116:119], v[228:231], v[208:211]\n\t" \
    "ds_read_b128 v[224:227], v252 offset:1024\n\t" \
    "s_waitcnt lgkmcnt(3)\n\t" \
    "v_mfma_f32_16x16x32_bf16 v[220:223], v[116:119], v[232:235], v[220:223]\n\t" \
    "ds_read_b128 v[228:231], v252 offset:17408\n\t" \
    "s_waitcnt lgkmcnt(3)\n\t" \
    "v_mfma_f32_16x16x32_bf16 v[208:211], v[128:131], v[236:239], v[208:211]\n\t" \
    "v_xor_b32 v252, 0x3800, %[xa]\n\t" \
    "ds_read_b128 v[232:235], v252 offset:0\n\t" \
    "s_waitcnt lgkmcnt(3)\n\t" \
    "v_mfma_f32_16x16x32_bf16 v[220:223], v[128:131], v[240:243], v[220:223]\n\t" \
    "ds_read_b128 v[236:239], v252 offset:16384\n\t" \
    "s_waitcnt lgkmcnt(3)\n\t" \
    "v_mfma_f32_16x16x32_bf16 v[208:211], v[132:135], v[224:227], v[208:211]\n\t" \
    "ds_read_b128 v[240:243], v252 offset:1024\n\t" \
    "s_waitcnt lgkmcnt(3)\n\t" \
    "v_mfma_f32_16x16x32_bf16 v[220:223], v[132:135], v[228:231], v[220:223]\n\t" \
    "ds_read_b128 v[224:227], v252 offset:17408\n\t" \
    "s_waitcnt lgkmcnt(3)\n\t" \
    "v_mfma_f32_16x16x32_bf16 v[208:211], v[152:155], v[232:235], v[208:211]\n\t" \
    "v_and_b32 v252, 0x3ff, %[xa]\n\t" \
    "ds_read_b128 v[228:231], v252 offset:43008\n\t" \
    "v_mov_b64 v[244:245], 0\n\t" \
    "s_waitcnt lgkmcnt(3)\n\t" \
    "v_mfma_f32_16x16x32_bf16 v[220:223], v[152:155], v[236:239], v[220:223]\n\t" \
    "ds_read_b128 v[232:235], v252 offset:44032\n\t" \
    "v_mov_b64 v[246:247], 0\n\t" \
    "s_waitcnt lgkmcnt(3)\n\t" \
    "v_mfma_f32_16x16x32_bf16 v[208:211], v[156:159], v[240:243], v[208:211]\n\t" \
    "ds_read_b128 v[236:239], v252 offset:47104\n\t" \
    "v_mov_b64 v[248:249], 0\n\t" \
    "s_waitcnt lgkmcnt(3)\n\t" \
    "v_mfma_f32_16x16x32_bf16 v[220:223], v[156:159], v[224:227], v[220:223]\n\t" \
    "ds_read_b128 v[240:243], v252 offset:48128\n\t" \
    "v_mov_b64 v[250:251], 0\n\t" \
    "s_waitcnt lgkmcnt(3)\n\t" \
    "v_mfma_f32_16x16x32_bf16 v[208:211], v[192:195], v[228:231], v[208:211]\n\t" \
    "v_add_u32 v252, %[qs], v252\n\t" \
    "s_waitcnt lgkmcnt(2)\n\t" \
    "v_mfma_f32_16x16x32_bf16 v[208:211], v[196:199], v[232:235], v[208:211]\n\t" \
    "v_mov_b64 v[224:225], 0\n\t" \
    "s_waitcnt lgkmcnt(1)\n\t" \
    "v_mfma_f32_16x16x32_bf16 v[220:223], v[192:195], v[236:239], v[220:223]\n\t" \
    "v_mov_b64 v[226:227], 0\n\t" \
    "ds_read_b128 v[228:231], %[wl] offset:2048\n\t" \
    "s_waitcnt lgkmcnt(1)\n\t" \
    "v_mfma_f32_16x16x32_bf16 v[220:223], v[196:199], v[240:243], v[220:223]\n\t" \
    "ds_read_b128 v[232:235], %[wl] offset:3072\n\t" \
    "v_cvt_pk_bf16_f32 v208, v208, v209\n\t" \
    "v_cvt_pk_bf16_f32 v209, v210, v211\n\t" \
    "ds_write_b64 v252, v[208:209] offset:2048\n\t" \
    "ds_read_b128 v[200:203], %[wl] offset:0\n\t" \
    "ds_read_b128 v[204:207], %[wl] offset:1024\n\t" \
    "s_nop 1\n\t" \
    "v_cvt_pk_bf16_f32 v220, v220, v221\n\t" \
    "v_cvt_pk_bf16_f32 v221, v222, v223\n\t" \
    "ds_write_b64 v252, v[220:221] offset:8192\n\t" \
    "s_waitcnt lgkmcnt(0)\n\t"
#define ASM_XG_V \
    "s_waitcnt lgkmcnt(0)\n\t" \
    "ds_read_b128 v[224:227], %[xa] offset:0\n\t" \
    "ds_read_b128 v[244:247], %[wl] offset:0\n\t" \
    "ds_read_b128 v[228:231], %[xa] offset:16384\n\t" \
    "ds_read_b128 v[232:235], %[xa] offset:1024\n\t" \
    "ds_read_b128 v[236:239], %[xa] offset:17408\n\t" \
    "ds_read_b128 v[248:251], %[wl] offset:1024\n\t" \
    "s_waitcnt lgkmcnt(5)\n\t" \
    "v_mfma_f32_16x16x32_bf16 v[204:207], v[0:3], v[224:227], 0\n\t" \
    "v_xor_b32 v252, 0x800, %[xa]\n\t" \
    "ds_read_b128 v[240:243], v252 offset:0\n\t" \
    "v_mfma_f32_16x16x32_bf16 v[208:211], v[8:11], v[224:227], 0\n\t" \
    "s_waitcnt lgkmcnt(5)\n\t" \
    "v_mfma_f32_16x16x32_bf16 v[200:203], v[244:247], v[224:227], 0\n\t" \
    "s_waitcnt lgkmcnt(4)\n\t" \
    "v_mfma_f32_16x16x32_bf16 v[216:219], v[0:3], v[228:231], 0\n\t" \
    "ds_read_b128 v[224:227], v252 offset:16384\n\t" \
    "v_mfma_f32_16x16x32_bf16 v[220:223], v[8:11], v[228:231], 0\n\t" \
    "v_mfma_f32_16x16x32_bf16 v[212:215], v[244:247], v[228:231], 0\n\t" \
    "s_waitcnt lgkmcnt(4)\n\t" \
    "v_mfma_f32_16x16x32_bf16 v[204:207], v[4:7], v[232:235], v[204:207]\n\t" \
    "ds_read_b128 v[228:231], v252 offset:1024\n\t" \
    "v_mfma_f32_16x16x32_bf16 v[208:211], v[12:15], v[232:235], v[208:211]\n\t" \
    "ds_read_b128 v[244:247], %[wl] offset:2048\n\t" \
    "s_waitcnt lgkmcnt(4)\n\t" \
    "v_mfma_f32_16x16x32_bf16 v[200:203], v[248:251], v[232:235], v[200:203]\n\t" \
    "v_mfma_f32_16x16x32_bf16 v[216:219], v[4:7], v[236:239], v[216:219]\n\t" \
    "ds_read_b128 v[232:235], v252 offset:17408\n\t" \
    "v_mfma_f32_16x16x32_bf16 v[220:223], v[12:15], v[236:239], v[220:223]\n\t" \
    "v_mfma_f32_16x16x32_bf16 v[212:215], v[248:251], v[236:239], v[212:215]\n\t" \
    "s_waitcnt lgkmcnt(4)\n\t" \
    "v_mfma_f32_16x16x32_bf16 v[204:207], v[24:27], v[240:243], v[204:207]\n\t" \
    "v_xor_b32 v252, 0x1000, %[xa]\n\t" \
    "ds_read_b128 v[236:239], v252 offset:0\n\t" \
    "v_mfma_f32_16x16x32_bf16 v[208:211], v[32:35], v[240:243], v[208:211]\n\t" \
    "ds_read_b128 v[248:251], %[wl] offset:3072\n\t" \
    "v_mfma_f32_16x16x32_bf16 v[200:203], v[16:19], v[240:243], v[200:203]\n\t" \
    "s_waitcnt lgkmcnt(5)\n\t" \
    "v_mfma_f32_16x16x32_bf16 v[216:219], v[24:27], v[224:227], v[216:219]\n\t" \
    "ds_read_b128 v[240:243], v252 offset:16384\n\t" \
    "v_mfma_f32_16x16x32_bf16 v[220:223], v[32:35], v[224:227], v[220:223]\n\t" \
    "v_mfma_f32_16x16x32_bf16 v[212:215], v[16:19], v[224:227], v[212:215]\n\t" \
    "s_waitcnt lgkmcnt(5)\n\t" \
    "v_mfma_f32_16x16x32_bf16 v[204:207], v[28:31], v[228:231], v[204:207]\n\t" \
    "ds_read_b128 v[224:227], v252 offset:1024\n\t" \
    "v_mfma_f32_16x16x32_bf16 v[208:211], v[36:39], v[228:231], v[208:211]\n\t" \
    "v_mfma_f32_16x16x32_bf16 v[200:203], v[20:23], v[228:231], v[200:203]\n\t" \
    "s_waitcnt lgkmcnt(4)\n\t" \
    "v_mfma_f32_16x16x32_bf16 v[216:219], v[28:31], v[232:235], v[216:219]\n\t" \
    "ds_read_b128 v[228:231], v252 offset:17408\n\t" \
    "v_mfma_f32_16x16x32_bf16 v[220:223], v[36:39], v[232:235], v[220:223]\n\t" \
    "v_mfma_f32_16x16x32_bf16 v[212:215], v[20:23], v[232:235], v[212:215]\n\t" \
    "s_waitcnt lgkmcnt(4)\n\t" \
    "v_mfma_f32_16x16x32_bf16 v[204:207], v[40:43], v[236:239], v[204:207]\n\t" \
    "v_xor_b32 v252, 0x1800, %[xa]\n\t" \
    "ds_read_b128 v[232:235], v252 offset:0\n\t" \
    "v_mfma_f32_16x16x32_bf16 v[208:211], v[48:51], v[236:239], v[208:211]\n\t" \
    "v_mfma_f32_16x16x32_bf16 v[200:203], v[244:247], v[236:239], v[200:203]\n\t" \
    "s_waitcnt lgkmcnt(3)\n\t" \
    "v_mfma_f32_16x16x32_bf16 v[216:219], v[40:43], v[240:243], v[216:219]\n\t" \
    "ds_read_b128 v[236:239], v252 offset:16384\n\t" \
    "v_mfma_f32_16x16x32_bf16 v[220:223], v[48:51], v[240:243], v[220:223]\n\t" \
    "v_mfma_f32_16x16x32_bf16 v[212:215], v[244:247], v[240:243], v[212:215]\n\t" \
    "s_waitcnt lgkmcnt(3)\n\t" \
    "v_mfma_f32_16x16x32_bf16 v[204:207], v[44:47], v[224:227], v[204:207]\n\t" \
    "ds_read_b128 v[240:243], v252 offset:1024\n\t" \
    "v_mfma_f32_16x16x32_bf16 v[208:211], v[52:55], v[224:227], v[208:211]\n\t" \
    "ds_read_b128 v[244:247], %[wl] offset:8192\n\t" \
    "v_mfma_f32_16x16x32_bf16 v[200:203], v[248:251], v[224:227], v[200:203]\n\t" \
    "s_waitcnt lgkmcnt(4)\n\t" \
    "v_mfma_f32_16x16x32_bf16 v[216:219], v[44:47], v[228:231], v[216:219]\n\t" \
    "ds_read_b128 v[224:227], v252 offset:17408\n\t" \
    "v_mfma_f32_16x16x32_bf16 v[220:223], v[52:55], v[228:231], v[220:223]\n\t" \
    "v_mfma_f32_16x16x32_bf16 v[212:215], v[248:251], v[228:231], v[212:215]\n\t" \
    "s_waitcnt lgkmcnt(4)\n\t" \
    "v_mfma_f32_16x16x32_bf16 v[204:207], v[64:67], v[232:235], v[204:207]\n\t" \
    "v_and_b32 v252, 0x3ff, %[xa]\n\t" \
    "ds_read_b128 v[228:231], v252 offset:40960\n\t" \
    "v_mfma_f32_16x16x32_bf16 v[208:211], v[72:75], v[232:235], v[208:211]\n\t" \
    "ds_read_b128 v[248:251], %[wl] offset:9216\n\t" \
    "v_mfma_f32_16x16x32_bf16 v[200:203], v[56:59], v[232:235], v[200:203]\n\t" \
    "s_waitcnt lgkmcnt(5)\n\t" \
    "v_mfma_f32_16x16x32_bf16 v[216:219], v[64:67], v[236:239], v[216:219]\n\t" \
    "ds_read_b128 v[232:235], v252 offset:45056\n\t" \
    "v_mfma_f32_16x16x32_bf16 v[220:223], v[72:75], v[236:239], v[220:223]\n\t" \
    "v_mfma_f32_16x16x32_bf16 v[212:215], v[56:59], v[236:239], v[212:215]\n\t" \
    "s_waitcnt lgkmcnt(5)\n\t" \
    "v_mfma_f32_16x16x32_bf16 v[204:207], v[68:71], v[240:243], v[204:207]\n\t" \
    "ds_read_b128 v[236:239], v252 offset:41984\n\t" \
    "v_mfma_f32_16x16x32_bf16 v[208:211], v[76:79], v[240:243], v[208:211]\n\t" \
    "v_mfma_f32_16x16x32_bf16 v[200:203], v[60:63], v[240:243], v[200:203]\n\t" \
    "s_waitcnt lgkmcnt(4)\n\t" \
    "v_mfma_f32_16x16x32_bf16 v[216:219], v[68:71], v[224:227], v[216:219]\n\t" \
    "ds_read_b128 v[240:243], v252 offset:46080\n\t" \
    "v_mfma_f32_16x16x32_bf16 v[220:223], v[76:79], v[224:227], v[220:223]\n\t" \
    "v_mfma_f32_16x16x32_bf16 v[212:215], v[60:63], v[224:227], v[212:215]\n\t" \
    "s_waitcnt lgkmcnt(4)\n\t" \
    "v_mfma_f32_16x16x32_bf16 v[204:207], v[160:163], v[228:231], v[204:207]\n\t" \
    "v_xor_b32 v252, 0x2000, %[xa]\n\t" \
    "ds_read_b128 v[224:227], v252 offset:0\n\t" \
    "v_mfma_f32_16x16x32_bf16 v[208:211], v[168:171], v[228:231], v[208:211]\n\t" \
    "v_mfma_f32_16x16x32_bf16 v[200:203], v[244:247], v[228:231], v[200:203]\n\t" \
    "s_waitcnt lgkmcnt(3)\n\t" \
    "v_mfma_f32_16x16x32_bf16 v[216:219], v[160:163], v[232:235], v[216:219]\n\t" \
    "ds_read_b128 v[228:231], v252 offset:16384\n\t" \
    "v_mfma_f32_16x16x32_bf16 v[220:223], v[168:171], v[232:235], v[220:223]\n\t" \
    "v_mfma_f32_16x16x32_bf16 v[212:215], v[244:247], v[232:235], v[212:215]\n\t" \
    "s_waitcnt lgkmcnt(3)\n\t" \
    "v_mfma_f32_16x16x32_bf16 v[204:207], v[164:167], v[236:239], v[204:207]\n\t" \
    "ds_read_b128 v[232:235], v252 offset:1024\n\t" \
    "v_mfma_f32_16x16x32_bf16 v[208:211], v[172:175], v[236:239], v[208:211]\n\t" \
    "ds_read_b128 v[244:247], %[wl] offset:4096\n\t" \
    "v_mfma_f32_16x16x32_bf16 v[200:203], v[248:251], v[236:239], v[200:203]\n\t" \
    "s_waitcnt lgkmcnt(4)\n\t" \
    "v_mfma_f32_16x16x32_bf16 v[216:219], v[164:167], v[240:243], v[216:219]\n\t" \
    "ds_read_b128 v[236:239], v252 offset:17408\n\t" \
    "v_mfma_f32_16x16x32_bf16 v[220:223], v[172:175], v[240:243], v[220:223]\n\t" \
    "v_mfma_f32_16x16x32_bf16 v[212:215], v[248:251], v[240:243], v[212:215]\n\t" \
    "s_waitcnt lgkmcnt(4)\n\t" \
    "v_mfma_f32_16x16x32_bf16 v[204:207], v[80:83], v[224:227], v[204:207]\n\t" \
    "v_xor_b32 v252, 0x2800, %[xa]\n\t" \
    "ds_read_b128 v[240:243], v252 offset:0\n\t" \
    "v_mfma_f32_16x16x32_bf16 v[208:211], v[88:91], v[224:227], v[208:211]\n\t" \
    "ds_read_b128 v[248:251], %[wl] offset:5120\n\t" \
    "s_waitcnt lgkmcnt(3)\n\t" \
    "v_mfma_f32_16x16x32_bf16 v[200:203], v[244:247], v[224:227], v[200:203]\n\t" \
    "v_mfma_f32_16x16x32_bf16 v[216:219], v[80:83], v[228:231], v[216:219]\n\t" \
    "ds_read_b128 v[224:227], v252 offset:16384\n\t" \
    "v_mfma_f32_16x16x32_bf16 v[220:223], v[88:91], v[228:231], v[220:223]\n\t" \
    "v_mfma_f32_16x16x32_bf16 v[212:215], v[244:247], v[228:231], v[212:215]\n\t" \
    "v_mfma_f32_16x16x32_bf16 v[204:207], v[84:87], v[232:235], v[204:207]\n\t" \
    "ds_read_b128 v[228:231], v252 offset:1024\n\t" \
    "v_mfma_f32_16x16x32_bf16 v[208:211], v[92:95], v[232:235], v[208:211]\n\t" \
    "ds_read_b128 v[244:247], %[wl] offset:6144\n\t" \
    "s_waitcnt lgkmcnt(3)\n\t" \
    "v_mfma_f32_16x16x32_bf16 v[200:203], v[248:251], v[232:235], v[200:203]\n\t" \
    "v_mfma_f32_16x16x32_bf16 v[216:219], v[84:87], v[236:239], v[216:219]\n\t" \
    "ds_read_b128 v[232:235], v252 offset:17408\n\t" \
    "v_mfma_f32_16x16x32_bf16 v[220:223], v[92:95], v[236:239], v[220:223]\n\t" \
    "v_mfma_f32_16x16x32_bf16 v[212:215], v[248:251], v[236:239], v[212:215]\n\t" \
    "v_mfma_f32_16x16x32_bf16 v[204:207], v[104:107], v[240:243], v[204:207]\n\t" \
    "v_xor_b32 v252, 0x3000, %[xa]\n\t" \
    "ds_read_b128 v[236:239], v252 offset:0\n\t" \
    "v_mfma_f32_16x16x32_bf16 v[208:211], v[112:115], v[240:243], v[208:211]\n\t" \
    "ds_read_b128 v[248:251], %[wl] offset:7168\n\t" \
    "v_mfma_f32_16x16x32_bf16 v[200:203], v[96:99], v[240:243], v[200:203]\n\t" \
    "s_waitcnt lgkmcnt(5)\n\t" \
    "v_mfma_f32_16x16x32_bf16 v[216:219], v[104:107], v[224:227], v[216:219]\n\t" \
    "ds_read_b128 v[240:243], v252 offset:16384\n\t" \
    "v_mfma_f32_16x16x32_bf16 v[220:223], v[112:115], v[224:227], v[220:223]\n\t" \
    "v_mfma_f32_16x16x32_bf16 v[212:215], v[96:99], v[224:227], v[212:215]\n\t" \
    "s_waitcnt lgkmcnt(5)\n\t" \
    "v_mfma_f32_16x16x32_bf16 v[204:207], v[108:111], v[228:231], v[204:207]\n\t" \
    "ds_read_b128 v[224:227], v252 offset:1024\n\t" \
    "v_mfma_f32_16x16x32_bf16 v[208:211], v[116:119], v[228:231], v[208:211]\n\t" \
    "v_mfma_f32_16x16x32_bf16 v[200:203], v[100:103], v[228:231], v[200:203]\n\t" \
    "s_waitcnt lgkmcnt(4)\n\t" \
    "v_mfma_f32_16x16x32_bf16 v[216:219], v[108:111], v[232:235], v[216:219]\n\t" \
    "ds_read_b128 v[228:231], v252 offset:17408\n\t" \
    "v_mfma_f32_16x16x32_bf16 v[220:223], v[116:119], v[232:235], v[220:223]\n\t" \
    "v_mfma_f32_16x16x32_bf16 v[212:215], v[100:103], v[232:235], v[212:215]\n\t" \
    "s_waitcnt lgkmcnt(4)\n\t" \
    "v_mfma_f32_16x16x32_bf16 v[204:207], v[120:123], v[236:239], v[204:207]\n\t" \
    "v_xor_b32 v252, 0x3800, %[xa]\n\t" \
    "ds_read_b128 v[232:235], v252 offset:0\n\t" \
    "v_mfma_f32_16x16x32_bf16 v[208:211], v[128:131], v[236:239], v[208:211]\n\t" \
    "v_mfma_f32_16x16x32_bf16 v[200:203], v[244:247], v[236:239], v[200:203]\n\t" \
    "s_waitcnt lgkmcnt(3)\n\t" \
    "v_mfma_f32_16x16x32_bf16 v[216:219], v[120:123], v[240:243], v[216:219]\n\t" \
    "ds_read_b128 v[236:239], v252 offset:16384\n\t" \
    "v_mfma_f32_16x16x32_bf16 v[220:223], v[128:131], v[240:243], v[220:223]\n\t" \
    "v_mfma_f32_16x16x32_bf16 v[212:215], v[244:247], v[240:243], v[212:215]\n\t" \
    "s_waitcnt lgkmcnt(3)\n\t" \
    "v_mfma_f32_16x16x32_bf16 v[204:207], v[124:127], v[224:227], v[204:207]\n\t" \
    "ds_read_b128 v[240:243], v252 offset:1024\n\t" \
    "v_mfma_f32_16x16x32_bf16 v[208:211], v[132:135], v[224:227], v[208:211]\n\t" \
    "v_mfma_f32_16x16x32_bf16 v[200:203], v[248:251], v[224:227], v[200:203]\n\t" \
    "s_waitcnt lgkmcnt(3)\n\t" \
    "v_mfma_f32_16x16x32_bf16 v[216:219], v[124:127], v[228:231], v[216:219]\n\t" \
    "ds_read_b128 v[224:227], v252 offset:17408\n\t" \
    "v_mfma_f32_16x16x32_bf16 v[220:223], v[132:135], v[228:231], v[220:223]\n\t" \
    "v_mfma_f32_16x16x32_bf16 v[212:215], v[248:251], v[228:231], v[212:215]\n\t" \
    "s_waitcnt lgkmcnt(3)\n\t" \
    "v_mfma_f32_16x16x32_bf16 v[204:207], v[144:147], v[232:235], v[204:207]\n\t" \
    "v_and_b32 v252, 0x3ff, %[xa]\n\t" \
    "ds_read_b128 v[228:231], v252 offset:43008\n\t" \
    "v_mov_b64 v[244:245], 0\n\t" \
    "v_mfma_f32_16x16x32_bf16 v[208:211], v[152:155], v[232:235], v[208:211]\n\t" \
    "v_mov_b64 v[246:247], 0\n\t" \
    "v_mfma_f32_16x16x32_bf16 v[200:203], v[136:139], v[232:235], v[200:203]\n\t" \
    "v_mov_b64 v[248:249], 0\n\t" \
    "s_waitcnt lgkmcnt(3)\n\t" \
    "v_mfma_f32_16x16x32_bf16 v[216:219], v[144:147], v[236:239], v[216:219]\n\t" \
    "ds_read_b128 v[232:235], v252 offset:44032\n\t" \
    "v_mov_b64 v[250:251], 0\n\t" \
    "v_mfma_f32_16x16x32_bf16 v[220:223], v[152:155], v[236:239], v[220:223]\n\t" \
    "v_mfma_f32_16x16x32_bf16 v[212:215], v[136:139], v[236:239], v[212:215]\n\t" \
    "s_waitcnt lgkmcnt(3)\n\t" \
    "v_mfma_f32_16x16x32_bf16 v[204:207], v[148:151], v[240:243], v[204:207]\n\t" \
    "ds_read_b128 v[236:239], v252 offset:47104\n\t" \
    "v_mfma_f32_16x16x32_bf16 v[208:211], v[156:159], v[240:243], v[208:211]\n\t" \
    "v_mfma_f32_16x16x32_bf16 v[200:203], v[140:143], v[240:243], v[200:203]\n\t" \
    "s_waitcnt lgkmcnt(3)\n\t" \
    "v_mfma_f32_16x16x32_bf16 v[216:219], v[148:151], v[224:227], v[216:219]\n\t" \
    "ds_read_b128 v[240:243], v252 offset:48128\n\t" \
    "v_mfma_f32_16x16x32_bf16 v[220:223], v[156:159], v[224:227], v[220:223]\n\t" \
    "v_mfma_f32_16x16x32_bf16 v[212:215], v[140:143], v[224:227], v[212:215]\n\t" \
    "s_waitcnt lgkmcnt(3)\n\t" \
    "v_mfma_f32_16x16x32_bf16 v[204:207], v[184:187], v[228:231], v[204:207]\n\t" \
    "v_add_u32 v252, %[qs], v252\n\t" \
    "v_mfma_f32_16x16x32_bf16 v[208:211], v[192:195], v[228:231], v[208:211]\n\t" \
    "v_mov_b64 v[224:225], 0\n\t" \
    "v_mfma_f32_16x16x32_bf16 v[200:203], v[176:179], v[228:231], v[200:203]\n\t" \
    "v_mov_b64 v[226:227], 0\n\t" \
    "s_waitcnt lgkmcnt(2)\n\t" \
    "v_mfma_f32_16x16x32_bf16 v[204:207], v[188:191], v[232:235], v[204:207]\n\t" \
    "v_mfma_f32_16x16x32_bf16 v[208:211], v[196:199], v[232:235], v[208:211]\n\t" \
    "v_mfma_f32_16x16x32_bf16 v[200:203], v[180:183], v[232:235], v[200:203]\n\t" \
    "s_waitcnt lgkmcnt(1)\n\t" \
    "v_mfma_f32_16x16x32_bf16 v[216:219], v[184:187], v[236:239], v[216:219]\n\t" \
    "ds_read_b128 v[228:231], %[wl] offset:2048\n\t" \
    "ds_read_b128 v[232:235], %[wl] offset:3072\n\t" \
    "v_mfma_f32_16x16x32_bf16 v[220:223], v[192:195], v[236:239], v[220:223]\n\t" \
    "v_cvt_pk_bf16_f32 v204, v204, v205\n\t" \
    "v_cvt_pk_bf16_f32 v205, v206, v207\n\t" \
    "v_mfma_f32_16x16x32_bf16 v[212:215], v[176:179], v[236:239], v[212:215]\n\t" \
    "ds_write_b64 v252, v[204:205] offset:1024\n\t" \
    "v_cvt_pk_bf16_f32 v208, v208, v209\n\t" \
    "s_waitcnt lgkmcnt(3)\n\t" \
    "v_mfma_f32_16x16x32_bf16 v[216:219], v[188:191], v[240:243], v[216:219]\n\t" \
    "v_cvt_pk_bf16_f32 v209, v210, v211\n\t" \
    "ds_write_b64 v252, v[208:209] offset:2048\n\t" \
    "v_mfma_f32_16x16x32_bf16 v[220:223], v[196:199], v[240:243], v[220:223]\n\t" \
    "v_cvt_pk_bf16_f32 v200, v200, v201\n\t" \
    "v_cvt_pk_bf16_f32 v201, v202, v203\n\t" \
    "v_mfma_f32_16x16x32_bf16 v[212:215], v[180:183], v[240:243], v[212:215]\n\t" \
    "ds_write_b64 v252, v[200:201] offset:0\n\t" \
    "ds_read_b128 v[200:203], %[wl] offset:0\n\t" \
    "ds_read_b128 v[204:207], %[wl] offset:1024\n\t" \
    "v_cvt_pk_bf16_f32 v216, v216, v217\n\t" \
    "v_cvt_pk_bf16_f32 v217, v218, v219\n\t" \
    "ds_write_b64 v252, v[216:217] offset:7168\n\t" \
    "v_cvt_pk_bf16_f32 v220, v220, v221\n\t" \
    "v_cvt_pk_bf16_f32 v221, v222, v223\n\t" \
    "ds_write_b64 v252, v[220:221] offset:8192\n\t" \
    "v_cvt_pk_bf16_f32 v212, v212, v213\n\t" \
    "v_cvt_pk_bf16_f32 v213, v214, v215\n\t" \
    "ds_write_b64 v252, v[212:213] offset:6144\n\t" \
    "s_waitcnt lgkmcnt(0)\n\t"
#define ASM_XG_H_HI \
    "s_waitcnt lgkmcnt(0)\n\t" \
    "v_xor_b32 v252, 0x2000, %[xa]\n\t" \
    "ds_read_b128 v[224:227], v252 offset:0\n\t" \
    "ds_read_b128 v[244:247], %[wl] offset:4096\n\t" \
    "ds_read_b128 v[228:231], v252 offset:16384\n\t" \
    "ds_read_b128 v[232:235], v252 offset:1024\n\t" \
    "ds_read_b128 v[236:239], v252 offset:17408\n\t" \
    "ds_read_b128 v[248:251], %[wl] offset:5120\n\t" \
    "s_waitcnt lgkmcnt(5)\n\t" \
    "v_mfma_f32_16x16x32_bf16 v[204:207], v[80:83], v[224:227], 0\n\t" \
    "v_xor_b32 v252, 0x2800, %[xa]\n\t" \
    "ds_read_b128 v[240:243], v252 offset:0\n\t" \
    "v_mfma_f32_16x16x32_bf16 v[208:211], v[88:91], v[224:227], 0\n\t" \
    "s_waitcnt lgkmcnt(5)\n\t" \
    "v_mfma_f32_16x16x32_bf16 v[200:203], v[244:247], v[224:227], 0\n\t" \
    "s_waitcnt lgkmcnt(4)\n\t" \
    "v_mfma_f32_16x16x32_bf16 v[216:219], v[80:83], v[228:231], 0\n\t" \
    "ds_read_b128 v[224:227], v252 offset:16384\n\t" \
    "v_mfma_f32_16x16x32_bf16 v[220:223], v[88:91], v[228:231], 0\n\t" \
    "v_mfma_f32_16x16x32_bf16 v[212:215], v[244:247], v[228:231], 0\n\t" \
    "s_waitcnt lgkmcnt(4)\n\t" \
    "v_mfma_f32_16x16x32_bf16 v[204:207], v[84:87], v[232:235], v[204:207]\n\t" \
    "ds_read_b128 v[228:231], v252 offset:1024\n\t" \
    "v_mfma_f32_16x16x32_bf16 v[208:211], v[92:95], v[232:235], v[208:211]\n\t" \
    "ds_read_b128 v[244:247], %[wl] offset:6144\n\t" \
    "s_waitcnt lgkmcnt(4)\n\t" \
    "v_mfma_f32_16x16x32_bf16 v[200:203], v[248:251], v[232:235], v[200:203]\n\t" \
    "v_mfma_f32_16x16x32_bf16 v[216:219], v[84:87], v[236:239], v[216:219]\n\t" \
    "ds_read_b128 v[232:235], v252 offset:17408\n\t" \
    "v_mfma_f32_16x16x32_bf16 v[220:223], v[92:95], v[236:239], v[220:223]\n\t" \
    "v_mfma_f32_16x16x32_bf16 v[212:215], v[248:251], v[236:239], v[212:215]\n\t" \
    "s_waitcnt lgkmcnt(4)\n\t" \
    "v_mfma_f32_16x16x32_bf16 v[204:207], v[104:107], v[240:243], v[204:207]\n\t" \
    "v_xor_b32 v252, 0x3000, %[xa]\n\t" \
    "ds_read_b128 v[236:239], v252 offset:0\n\t" \
    "v_mfma_f32_16x16x32_bf16 v[208:211], v[112:115], v[240:243], v[208:211]\n\t" \
    "ds_read_b128 v[248:251], %[wl] offset:7168\n\t" \
    "v_mfma_f32_16x16x32_bf16 v[200:203], v[96:99], v[240:243], v[200:203]\n\t" \
    "s_waitcnt lgkmcnt(5)\n\t" \
    "v_mfma_f32_16x16x32_bf16 v[216:219], v[104:107], v[224:227], v[216:219]\n\t" \
    "ds_read_b128 v[240:243], v252 offset:16384\n\t" \
    "v_mfma_f32_16x16x32_bf16 v[220:223], v[112:115], v[224:227], v[220:223]\n\t" \
    "v_mfma_f32_16x16x32_bf16 v[212:215], v[96:99], v[224:227], v[212:215]\n\t" \
    "s_nop 3\n\t" \
    "v_mov_b32 v224, %[tv]\n\t" \
    "s_waitcnt vmcnt(5)\n\t" \
    "buffer_store_dword v224, %[fv], %[rr], %[fp] offen sc1\n\t" \
    "s_waitcnt lgkmcnt(5)\n\t" \
    "v_mfma_f32_16x16x32_bf16 v[204:207], v[108:111], v[228:231], v[204:207]\n\t" \
    "ds_read_b128 v[224:227], v252 offset:1024\n\t" \
    "v_mfma_f32_16x16x32_bf16 v[208:211], v[116:119], v[228:231], v[208:211]\n\t" \
    "v_mfma_f32_16x16x32_bf16 v[200:203], v[100:103], v[228:231], v[200:203]\n\t" \
    "s_waitcnt lgkmcnt(4)\n\t" \
    "v_mfma_f32_16x16x32_bf16 v[216:219], v[108:111], v[232:235], v[216:219]\n\t" \
    "ds_read_b128 v[228:231], v252 offset:17408\n\t" \
    "v_mfma_f32_16x16x32_bf16 v[220:223], v[116:119], v[232:235], v[220:223]\n\t" \
    "v_mfma_f32_16x16x32_bf16 v[212:215], v[100:103], v[232:235], v[212:215]\n\t" \
    "s_waitcnt lgkmcnt(4)\n\t" \
    "v_mfma_f32_16x16x32_bf16 v[204:207], v[120:123], v[236:239], v[204:207]\n\t" \
    "v_xor_b32 v252, 0x3800, %[xa]\n\t" \
    "ds_read_b128 v[232:235], v252 offset:0\n\t" \
    "v_mfma_f32_16x16x32_bf16 v[208:211], v[128:131], v[236:239], v[208:211]\n\t" \
    "v_mfma_f32_16x16x32_bf16 v[200:203], v[244:247], v[236:239], v[200:203]\n\t" \
    "s_waitcnt lgkmcnt(3)\n\t" \
    "v_mfma_f32_16x16x32_bf16 v[216:219], v[120:123], v[240:243], v[216:219]\n\t" \
    "ds_read_b128 v[236:239], v252 offset:16384\n\t" \
    "v_mfma_f32_16x16x32_bf16 v[220:223], v[128:131], v[240:243], v[220:223]\n\t" \
    "v_mfma_f32_16x16x32_bf16 v[212:215], v[244:247], v[240:243], v[212:215]\n\t" \
    "s_waitcnt lgkmcnt(3)\n\t" \
    "v_mfma_f32_16x16x32_bf16 v[204:207], v[124:127], v[224:227], v[204:207]\n\t" \
    "ds_read_b128 v[240:243], v252 offset:1024\n\t" \
    "v_mfma_f32_16x16x32_bf16 v[208:211], v[132:135], v[224:227], v[208:211]\n\t" \
    "ds_read_b128 v[244:247], %[wl] offset:8192\n\t" \
    "v_mfma_f32_16x16x32_bf16 v[200:203], v[248:251], v[224:227], v[200:203]\n\t" \
    "s_waitcnt lgkmcnt(4)\n\t" \
    "v_mfma_f32_16x16x32_bf16 v[216:219], v[124:127], v[228:231], v[216:219]\n\t" \
    "ds_read_b128 v[224:227], v252 offset:17408\n\t" \
    "v_mfma_f32_16x16x32_bf16 v[220:223], v[132:135], v[228:231], v[220:223]\n\t" \
    "v_mfma_f32_16x16x32_bf16 v[212:215], v[248:251], v[228:231], v[212:215]\n\t" \
    "s_waitcnt lgkmcnt(4)\n\t" \
    "v_mfma_f32_16x16x32_bf16 v[204:207], v[144:147], v[232:235], v[204:207]\n\t" \
    "v_and_b32 v252, 0x3ff, %[xa]\n\t" \
    "ds_read_b128 v[228:231], v252 offset:40960\n\t" \
    "v_mfma_f32_16x16x32_bf16 v[208:211], v[152:155], v[232:235], v[208:211]\n\t" \
    "ds_read_b128 v[248:251], %[wl] offset:9216\n\t" \
    "v_mfma_f32_16x16x32_bf16 v[200:203], v[136:139], v[232:235], v[200:203]\n\t" \
    "s_waitcnt lgkmcnt(5)\n\t" \
    "v_mfma_f32_16x16x32_bf16 v[216:219], v[144:147], v[236:239], v[216:219]\n\t" \
    "ds_read_b128 v[232:235], v252 offset:45056\n\t" \
    "v_mfma_f32_16x16x32_bf16 v[220:223], v[152:155], v[236:239], v[220:223]\n\t" \
    "v_mfma_f32_16x16x32_bf16 v[212:215], v[136:139], v[236:239], v[212:215]\n\t" \
    "s_waitcnt lgkmcnt(5)\n\t" \
    "v_mfma_f32_16x16x32_bf16 v[204:207], v[148:151], v[240:243], v[204:207]\n\t" \
    "ds_read_b128 v[236:239], v252 offset:41984\n\t" \
    "v_mfma_f32_16x16x32_bf16 v[208:211], v[156:159], v[240:243], v[208:211]\n\t" \
    "v_mfma_f32_16x16x32_bf16 v[200:203], v[140:143], v[240:243], v[200:203]\n\t" \
    "s_waitcnt lgkmcnt(4)\n\t" \
    "v_mfma_f32_16x16x32_bf16 v[216:219], v[148:151], v[224:227], v[216:219]\n\t" \
    "ds_read_b128 v[240:243], v252 offset:46080\n\t" \
    "v_mfma_f32_16x16x32_bf16 v[220:223], v[156:159], v[224:227], v[220:223]\n\t" \
    "v_mfma_f32_16x16x32_bf16 v[212:215], v[140:143], v[224:227], v[212:215]\n\t" \
    "s_waitcnt vmcnt(1) lgkmcnt(0)\n\t" \
    "s_barrier\n\t" ASM_ST \
    "v_mfma_f32_16x16x32_bf16 v[204:207], v[160:163], v[228:231], v[204:207]\n\t" \
    "ds_read_b128 v[224:227], %[xa] offset:0\n\t" \
    "v_mfma_f32_16x16x32_bf16 v[208:211], v[168:171], v[228:231], v[208:211]\n\t" \
    "v_mfma_f32_16x16x32_bf16 v[200:203], v[244:247], v[228:231], v[200:203]\n\t" \
    "v_mfma_f32_16x16x32_bf16 v[216:219], v[160:163], v[232:235], v[216:219]\n\t" \
    "ds_read_b128 v[228:231], %[xa] offset:16384\n\t" \
    "v_mfma_f32_16x16x32_bf16 v[220:223], v[168:171], v[232:235], v[220:223]\n\t" \
    "v_mfma_f32_16x16x32_bf16 v[212:215], v[244:247], v[232:235], v[212:215]\n\t" \
    "v_mfma_f32_16x16x32_bf16 v[204:207], v[164:167], v[236:239], v[204:207]\n\t" \
    "ds_read_b128 v[232:235], %[xa] offset:1024\n\t" \
    "v_mfma_f32_16x16x32_bf16 v[208:211], v[172:175], v[236:239], v[208:211]\n\t" \
    "ds_read_b128 v[244:247], %[wl] offset:0\n\t" \
    "v_mfma_f32_16x16x32_bf16 v[200:203], v[248:251], v[236:239], v[200:203]\n\t" \
    "v_mfma_f32_16x16x32_bf16 v[216:219], v[164:167], v[240:243], v[216:219]\n\t" \
    "ds_read_b128 v[236:239], %[xa] offset:17408\n\t" \
    "v_mfma_f32_16x16x32_bf16 v[220:223], v[172:175], v[240:243], v[220:223]\n\t" \
    "v_mfma_f32_16x16x32_bf16 v[212:215], v[248:251], v[240:243], v[212:215]\n\t" \
    "s_waitcnt lgkmcnt(4)\n\t" \
    "v_mfma_f32_16x16x32_bf16 v[204:207], v[0:3], v[224:227], v[204:207]\n\t" \
    "v_xor_b32 v252, 0x800, %[xa]\n\t" \
    "ds_read_b128 v[240:243], v252 offset:0\n\t" \
    "v_mfma_f32_16x16x32_bf16 v[208:211], v[8:11], v[224:227], v[208:211]\n\t" \
    "ds_read_b128 v[248:251], %[wl] offset:1024\n\t" \
    "s_waitcnt lgkmcnt(3)\n\t" \
    "v_mfma_f32_16x16x32_bf16 v[200:203], v[244:247], v[224:227], v[200:203]\n\t" \
    "v_mfma_f32_16x16x32_bf16 v[216:219], v[0:3], v[228:231], v[216:219]\n\t" \
    "ds_read_b128 v[224:227], v252 offset:16384\n\t" \
    "v_mfma_f32_16x16x32_bf16 v[220:223], v[8:11], v[228:231], v[220:223]\n\t" \
    "v_mfma_f32_16x16x32_bf16 v[212:215], v[244:247], v[228:231], v[212:215]\n\t" \
    "v_mfma_f32_16x16x32_bf16 v[204:207], v[4:7], v[232:235], v[204:207]\n\t" \
    "ds_read_b128 v[228:231], v252 offset:1024\n\t" \
    "v_mfma_f32_16x16x32_bf16 v[208:211], v[12:15], v[232:235], v[208:211]\n\t" \
    "ds_read_b128 v[244:247], %[wl] offset:2048\n\t" \
    "s_waitcnt lgkmcnt(3)\n\t" \
    "v_mfma_f32_16x16x32_bf16 v[200:203], v[248:251], v[232:235], v[200:203]\n\t" \
    "v_mfma_f32_16x16x32_bf16 v[216:219], v[4:7], v[236:239], v[216:219]\n\t" \
    "ds_read_b128 v[232:235], v252 offset:17408\n\t" \
    "v_mfma_f32_16x16x32_bf16 v[220:223], v[12:15], v[236:239], v[220:223]\n\t" \
    "v_mfma_f32_16x16x32_bf16 v[212:215], v[248:251], v[236:239], v[212:215]\n\t" \
    "v_mfma_f32_16x16x32_bf16 v[204:207], v[24:27], v[240:243], v[204:207]\n\t" \
    "v_xor_b32 v252, 0x1000, %[xa]\n\t" \
    "ds_read_b128 v[236:239], v252 offset:0\n\t" \
    "v_mfma_f32_16x16x32_bf16 v[208:211], v[32:35], v[240:243], v[208:211]\n\t" \
    "ds_read_b128 v[248:251], %[wl] offset:3072\n\t" \
    "v_mfma_f32_16x16x32_bf16 v[200:203], v[16:19], v[240:243], v[200:203]\n\t" \
    "s_waitcnt lgkmcnt(5)\n\t" \
    "v_mfma_f32_16x16x32_bf16 v[216:219], v[24:27], v[224:227], v[216:219]\n\t" \
    "ds_read_b128 v[240:243], v252 offset:16384\n\t" \
    "v_mfma_f32_16x16x32_bf16 v[220:223], v[32:35], v[224:227], v[220:223]\n\t" \
    "v_mfma_f32_16x16x32_bf16 v[212:215], v[16:19], v[224:227], v[212:215]\n\t" \
    "s_waitcnt lgkmcnt(5)\n\t" \
    "v_mfma_f32_16x16x32_bf16 v[204:207], v[28:31], v[228:231], v[204:207]\n\t" \
    "ds_read_b128 v[224:227], v252 offset:1024\n\t" \
    "v_mfma_f32_16x16x32_bf16 v[208:211], v[36:39], v[228:231], v[208:211]\n\t" \
    "v_mfma_f32_16x16x32_bf16 v[200:203], v[20:23], v[228:231], v[200:203]\n\t" \
    "s_waitcnt lgkmcnt(4)\n\t" \
    "v_mfma_f32_16x16x32_bf16 v[216:219], v[28:31], v[232:235], v[216:219]\n\t" \
    "ds_read_b128 v[228:231], v252 offset:17408\n\t" \
    "v_mfma_f32_16x16x32_bf16 v[220:223], v[36:39], v[232:235], v[220:223]\n\t" \
    "v_mfma_f32_16x16x32_bf16 v[212:215], v[20:23], v[232:235], v[212:215]\n\t" \
    "s_waitcnt lgkmcnt(4)\n\t" \
    "v_mfma_f32_16x16x32_bf16 v[204:207], v[40:43], v[236:239], v[204:207]\n\t" \
    "v_xor_b32 v252, 0x1800, %[xa]\n\t" \
    "ds_read_b128 v[232:235], v252 offset:0\n\t" \
    "v_mfma_f32_16x16x32_bf16 v[208:211], v[48:51], v[236:239], v[208:211]\n\t" \
    "v_mfma_f32_16x16x32_bf16 v[200:203], v[244:247], v[236:239], v[200:203]\n\t" \
    "s_waitcnt lgkmcnt(3)\n\t" \
    "v_mfma_f32_16x16x32_bf16 v[216:219], v[40:43], v[240:243], v[216:219]\n\t" \
    "ds_read_b128 v[236:239], v252 offset:16384\n\t" \
    "v_mfma_f32_16x16x32_bf16 v[220:223], v[48:51], v[240:243], v[220:223]\n\t" \
    "v_mfma_f32_16x16x32_bf16 v[212:215], v[244:247], v[240:243], v[212:215]\n\t" \
    "s_waitcnt lgkmcnt(3)\n\t" \
    "v_mfma_f32_16x16x32_bf16 v[204:207], v[44:47], v[224:227], v[204:207]\n\t" \
    "ds_read_b128 v[240:243], v252 offset:1024\n\t" \
    "v_mfma_f32_16x16x32_bf16 v[208:211], v[52:55], v[224:227], v[208:211]\n\t" \
    "v_mfma_f32_16x16x32_bf16 v[200:203], v[248:251], v[224:227], v[200:203]\n\t" \
    "s_waitcnt lgkmcnt(3)\n\t" \
    "v_mfma_f32_16x16x32_bf16 v[216:219], v[44:47], v[228:231], v[216:219]\n\t" \
    "ds_read_b128 v[224:227], v252 offset:17408\n\t" \
    "v_mfma_f32_16x16x32_bf16 v[220:223], v[52:55], v[228:231], v[220:223]\n\t" \
    "v_mfma_f32_16x16x32_bf16 v[212:215], v[248:251], v[228:231], v[212:215]\n\t" \
    "s_waitcnt lgkmcnt(3)\n\t" \
    "v_mfma_f32_16x16x32_bf16 v[204:207], v[64:67], v[232:235], v[204:207]\n\t" \
    "v_and_b32 v252, 0x3ff, %[xa]\n\t" \
    "ds_read_b128 v[228:231], v252 offset:43008\n\t" \
    "v_mov_b64 v[244:245], 0\n\t" \
    "v_mfma_f32_16x16x32_bf16 v[208:211], v[72:75], v[232:235], v[208:211]\n\t" \
    "v_mov_b64 v[246:247], 0\n\t" \
    "v_mfma_f32_16x16x32_bf16 v[200:203], v[56:59], v[232:235], v[200:203]\n\t" \
    "v_mov_b64 v[248:249], 0\n\t" \
    "s_waitcnt lgkmcnt(3)\n\t" \
    "v_mfma_f32_16x16x32_bf16 v[216:219], v[64:67], v[236:239], v[216:219]\n\t" \
    "ds_read_b128 v[232:235], v252 offset:44032\n\t" \
    "v_mov_b64 v[250:251], 0\n\t" \
    "v_mfma_f32_16x16x32_bf16 v[220:223], v[72:75], v[236:239], v[220:223]\n\t" \
    "v_mfma_f32_16x16x32_bf16 v[212:215], v[56:59], v[236:239], v[212:215]\n\t" \
    "s_waitcnt lgkmcnt(3)\n\t" \
    "v_mfma_f32_16x16x32_bf16 v[204:207], v[68:71], v[240:243], v[204:207]\n\t" \
    "ds_read_b128 v[236:239], v252 offset:47104\n\t" \
    "v_mfma_f32_16x16x32_bf16 v[208:211], v[76:79], v[240:243], v[208:211]\n\t" \
    "v_mfma_f32_16x16x32_bf16 v[200:203], v[60:63], v[240:243], v[200:203]\n\t" \
    "s_waitcnt lgkmcnt(3)\n\t" \
    "v_mfma_f32_16x16x32_bf16 v[216:219], v[68:71], v[224:227], v[216:219]\n\t" \
    "ds_read_b128 v[240:243], v252 offset:48128\n\t" \
    "v_mfma_f32_16x16x32_bf16 v[220:223], v[76:79], v[224:227], v[220:223]\n\t" \
    "v_mfma_f32_16x16x32_bf16 v[212:215], v[60:63], v[224:227], v[212:215]\n\t" \
    "s_waitcnt lgkmcnt(3)\n\t" \
    "v_mfma_f32_16x16x32_bf16 v[204:207], v[184:187], v[228:231], v[204:207]\n\t" \
    "v_add_u32 v252, %[qs], v252\n\t" \
    "v_mfma_f32_16x16x32_bf16 v[208:211], v[192:195], v[228:231], v[208:211]\n\t" \
    "v_mov_b64 v[224:225], 0\n\t" \
    "v_mfma_f32_16x16x32_bf16 v[200:203], v[176:179], v[228:231], v[200:203]\n\t" \
    "v_mov_b64 v[226:227], 0\n\t" \
    "s_waitcnt lgkmcnt(2)\n\t" \
    "v_mfma_f32_16x16x32_bf16 v[204:207], v[188:191], v[232:235], v[204:207]\n\t" \
    "v_mfma_f32_16x16x32_bf16 v[208:211], v[196:199], v[232:235], v[208:211]\n\t" \
    "v_mfma_f32_16x16x32_bf16 v[200:203], v[180:183], v[232:235], v[200:203]\n\t" \
    "s_waitcnt lgkmcnt(1)\n\t" \
    "v_mfma_f32_16x16x32_bf16 v[216:219], v[184:187], v[236:239], v[216:219]\n\t" \
    "ds_read_b128 v[228:231], %[wl] offset:2048\n\t" \
    "ds_read_b128 v[232:235], %[wl] offset:3072\n\t" \
    "v_mfma_f32_16x16x32_bf16 v[220:223], v[192:195], v[236:239], v[220:223]\n\t" \
    "v_cvt_pk_bf16_f32 v204, v204, v205\n\t" \
    "v_cvt_pk_bf16_f32 v205, v206, v207\n\t" \
    "v_mfma_f32_16x16x32_bf16 v[212:215], v[176:179], v[236:239], v[212:215]\n\t" \
    "ds_write_b64 v252, v[204:205] offset:1024\n\t" \
    "v_cvt_pk_bf16_f32 v208, v208, v209\n\t" \
    "s_waitcnt lgkmcnt(3)\n\t" \
    "v_mfma_f32_16x16x32_bf16 v[216:219], v[188:191], v[240:243], v[216:219]\n\t" \
    "v_cvt_pk_bf16_f32 v209, v210, v211\n\t" \
    "ds_write_b64 v252, v[208:209] offset:2048\n\t" \
    "v_mfma_f32_16x16x32_bf16 v[220:223], v[196:199], v[240:243], v[220:223]\n\t" \
    "v_cvt_pk_bf16_f32 v200, v200, v201\n\t" \
    "v_cvt_pk_bf16_f32 v201, v202, v203\n\t" \
    "v_mfma_f32_16x16x32_bf16 v[212:215], v[180:183], v[240:243], v[212:215]\n\t" \
    "ds_write_b64 v252, v[200:201] offset:0\n\t" \
    "ds_read_b128 v[200:203], %[wl] offset:0\n\t" \
    "ds_read_b128 v[204:207], %[wl] offset:1024\n\t" \
    "v_cvt_pk_bf16_f32 v216, v216, v217\n\t" \
    "v_cvt_pk_bf16_f32 v217, v218, v219\n\t" \
    "ds_write_b64 v252, v[216:217] offset:7168\n\t" \
    "v_cvt_pk_bf16_f32 v220, v220, v221\n\t" \
    "v_cvt_pk_bf16_f32 v221, v222, v223\n\t" \
    "ds_write_b64 v252, v[220:221] offset:8192\n\t" \
    "v_cvt_pk_bf16_f32 v212, v212, v213\n\t" \
    "v_cvt_pk_bf16_f32 v213, v214, v215\n\t" \
    "ds_write_b64 v252, v[212:213] offset:6144\n\t" \
    "s_waitcnt lgkmcnt(0)\n\t"
#define ASM_CRIT1_3 \
    "s_branch .Lc0%=\n\t" \
    ".Lcr%=:\n\t" \
    "s_cmp_eq_u32 %[n], 0\n\t" \
    "s_cbranch_scc1 .Lcb%=\n\t" \
    "s_sub_u32 %[n], %[n], 1\n\t" \
    "v_max_u32 v243, v208, v209\n\t" \
    "v_max3_u32 v243, v210, v211, v243\n\t" \
    "v_cmp_eq_u32 vcc, -1, v243\n\t" \
    "s_cbranch_vccz .Lcs0_%=\n\t" \
    "buffer_load_dwordx4 v[208:211], %[ab], %[rs], %[s0] offen sc1\n\t" \
    ".Lcs0_%=:\n\t" \
    "v_max_u32 v243, v236, v237\n\t" \
    "v_max3_u32 v243, v238, v239, v243\n\t" \
    "v_cmp_eq_u32 vcc, -1, v243\n\t" \
    "s_cbranch_vccz .Lcs1_%=\n\t" \
    "buffer_load_dwordx4 v[236:239], %[ab], %[rs], %[s1] offen sc1\n\t" \
    ".Lcs1_%=:\n\t" \
    "v_max_u32 v243, v220, v221\n\t" \
    "v_max3_u32 v243, v222, v223, v243\n\t" \
    "v_cmp_eq_u32 vcc, -1, v243\n\t" \
    "s_cbranch_vccz .Lca%=\n\t" \
    "buffer_load_dwordx4 v[220:223], %[ab], %[rs], %[s2] offen sc1\n\t" \
    "s_branch .Lca%=\n\t" \
    ".Lc0%=:\n\t" \
    "buffer_load_dwordx4 v[208:211], %[ab], %[rs], %[s0] offen sc1\n\t" \
    "buffer_load_dwordx4 v[236:239], %[ab], %[rs], %[s1] offen sc1\n\t" \
    "buffer_load_dwordx4 v[220:223], %[ab], %[rs], %[s2] offen sc1\n\t" \
    "v_bfe_i32 v241, %[ab], 7, 1\n\t" \
    "v_and_b32 v241, 0xaaaa, v241\n\t" \
    "v_or_b32 v241, 0x4444, v241\n\t" \
    "v_xor_b32 v212, 0x400, %[ab]\n\t" \
    "v_xor_b32 v216, 0x800, %[ab]\n\t" \
    "v_xor_b32 v242, 0xc00, %[ab]\n\t" \
    "v_xor_b32 v240, 0x1400, %[ab]\n\t" \
    ".Lca%=:\n\t" \
    "s_waitcnt vmcnt(0)\n\t" \
    "ds_write_b128 %[ab], v[208:211] offset:0\n\t" \
    "ds_write_b128 %[ab], v[236:239] offset:4096\n\t" \
    "ds_write_b128 %[ab], v[220:223] offset:8192\n\t" \
    "v_max_u32 v243, v208, v209\n\t" \
    "v_max3_u32 v243, v210, v211, v243\n\t" \
    "v_max3_u32 v243, v236, v237, v243\n\t" \
    "v_max3_u32 v243, v238, v239, v243\n\t" \
    "v_max3_u32 v243, v220, v221, v243\n\t" \
    "v_max3_u32 v243, v222, v223, v243\n\t" \
    "v_cmp_eq_u32 vcc, -1, v243\n\t" \
    "s_cbranch_vccnz .Lcr%=\n\t" \
    ".Lcb%=:\n\t" \
    ASM_ST2 \
    "v_smfmac_f32_16x16x64_bf16 v[224:227], v[208:211], v[200:207], v241\n\t" \
    "ds_read_b128 v[200:203], %[wl] offset:4096\n\t" \
    "ds_read_b128 v[204:207], %[wl] offset:5120\n\t" \
    "v_smfmac_f32_16x16x64_bf16 v[244:247], v[208:211], v[0:7], v241\n\t" \
    "v_smfmac_f32_16x16x64_bf16 v[248:251], v[208:211], v[8:15], v241\n\t" \
    "s_waitcnt lgkmcnt(2)\n\t" \
    "s_barrier\n\t" ASM_ST \
    "ds_read_b128 v[212:215], v212 offset:0\n\t" \
    "ds_read_b128 v[216:219], v216 offset:0\n\t" \
    "ds_read_b128 v[220:223], v242 offset:0\n\t" \
    "ds_read_b128 v[208:211], v240 offset:0\n\t" \
    "v_smfmac_f32_16x16x64_bf16 v[244:247], v[236:239], v[80:87], v241\n\t" \
    "v_xor_b32 v240, 0x1800, %[ab]\n\t" \
    "v_smfmac_f32_16x16x64_bf16 v[248:251], v[236:239], v[88:95], v241\n\t" \
    "s_waitcnt lgkmcnt(4)\n\t" \
    "v_smfmac_f32_16x16x64_bf16 v[224:227], v[236:239], v[200:207], v241\n\t" \
    "ds_read_b128 v[200:203], %[wl] offset:6144\n\t" \
    "ds_read_b128 v[204:207], %[wl] offset:7168\n\t" \
    "ds_read_b128 v[236:239], v240 offset:0\n\t" \
    "s_waitcnt lgkmcnt(6)\n\t" \
    "v_smfmac_f32_16x16x64_bf16 v[224:227], v[212:215], v[16:23], v241\n\t" \
    "v_smfmac_f32_16x16x64_bf16 v[244:247], v[212:215], v[24:31], v241\n\t" \
    "v_xor_b32 v240, 0x1c00, %[ab]\n\t" \
    "v_smfmac_f32_16x16x64_bf16 v[248:251], v[212:215], v[32:39], v241\n\t" \
    "ds_read_b128 v[212:215], v240 offset:0\n\t" \
    "s_waitcnt lgkmcnt(6)\n\t" \
    "v_smfmac_f32_16x16x64_bf16 v[224:227], v[216:219], v[228:235], v241\n\t" \
    "ds_read_b128 v[228:231], %[wl] offset:8192\n\t" \
    "ds_read_b128 v[232:235], %[wl] offset:9216\n\t" \
    "v_smfmac_f32_16x16x64_bf16 v[244:247], v[216:219], v[40:47], v241\n\t" \
    "v_and_b32 v240, 0x3ff, %[ab]\n\t" \
    "v_smfmac_f32_16x16x64_bf16 v[248:251], v[216:219], v[48:55], v241\n\t" \
    "ds_read_b128 v[216:219], v240 offset:8192\n\t" \
    "s_waitcnt lgkmcnt(8)\n\t" \
    "v_smfmac_f32_16x16x64_bf16 v[224:227], v[220:223], v[56:63], v241\n\t" \
    "v_smfmac_f32_16x16x64_bf16 v[244:247], v[220:223], v[64:71], v241\n\t" \
    "v_smfmac_f32_16x16x64_bf16 v[248:251], v[220:223], v[72:79], v241\n\t" \
    "ds_read_b128 v[220:223], v240 offset:9216\n\t" \
    "s_waitcnt lgkmcnt(8)\n\t" \
    "v_smfmac_f32_16x16x64_bf16 v[244:247], v[208:211], v[104:111], v241\n\t" \
    "s_waitcnt lgkmcnt(5)\n\t" \
    "v_smfmac_f32_16x16x64_bf16 v[244:247], v[236:239], v[120:127], v241\n\t" \
    "s_waitcnt lgkmcnt(4)\n\t" \
    "v_smfmac_f32_16x16x64_bf16 v[244:247], v[212:215], v[144:151], v241\n\t" \
    "s_waitcnt lgkmcnt(1)\n\t" \
    "v_smfmac_f32_16x16x64_bf16 v[244:247], v[216:219], v[160:167], v241\n\t" \
    "s_waitcnt lgkmcnt(0)\n\t" \
    "v_smfmac_f32_16x16x64_bf16 v[244:247], v[220:223], v[184:191], v241\n\t" \
    "v_smfmac_f32_16x16x64_bf16 v[248:251], v[208:211], v[112:119], v241\n\t" \
    "v_smfmac_f32_16x16x64_bf16 v[248:251], v[236:239], v[128:135], v241\n\t" \
    "v_smfmac_f32_16x16x64_bf16 v[248:251], v[212:215], v[152:159], v241\n\t" \
    "v_permlane32_swap_b32 v244, v246\n\t" \
    "v_permlane32_swap_b32 v245, v247\n\t" \
    "v_smfmac_f32_16x16x64_bf16 v[248:251], v[216:219], v[168:175], v241\n\t" \
    "v_add_f32 v244, v244, v246\n\t" \
    "v_add_f32 v245, v245, v247\n\t" \
    "v_smfmac_f32_16x16x64_bf16 v[248:251], v[220:223], v[192:199], v241\n\t" \
    "v_cvt_pk_bf16_f32 v244, v244, v245\n\t" \
    "ds_write_b16 %[dst], v244 offset:512\n\t" \
    "ds_write_b16_d16_hi %[dst], v244 offset:528\n\t" \
    "v_smfmac_f32_16x16x64_bf16 v[224:227], v[208:211], v[96:103], v241\n\t" \
    "v_smfmac_f32_16x16x64_bf16 v[224:227], v[236:239], v[200:207], v241\n\t" \
    "v_smfmac_f32_16x16x64_bf16 v[224:227], v[212:215], v[136:143], v241\n\t" \
    "v_permlane32_swap_b32 v248, v250\n\t" \
    "v_permlane32_swap_b32 v249, v251\n\t" \
    "v_smfmac_f32_16x16x64_bf16 v[224:227], v[216:219], v[228:235], v241\n\t" \
    "v_add_f32 v248, v248, v250\n\t" \
    "v_add_f32 v249, v249, v251\n\t" \
    "v_smfmac_f32_16x16x64_bf16 v[224:227], v[220:223], v[176:183], v241\n\t" \
    "v_cvt_pk_bf16_f32 v248, v248, v249\n\t" \
    "ds_write_b16 %[dst], v248 offset:1024\n\t" \
    "ds_write_b16_d16_hi %[dst], v248 offset:1040\n\t" \
    "s_nop 4\n\t" \
    "v_permlane32_swap_b32 v224, v226\n\t" \
    "v_permlane32_swap_b32 v225, v227\n\t" \
    "v_add_f32 v224, v224, v226\n\t" \
    "v_add_f32 v225, v225, v227\n\t" \
    "v_cvt_pk_bf16_f32 v224, v224, v225\n\t" \
    "ds_write_b16 %[dst], v224 offset:0\n\t" \
    "ds_write_b16_d16_hi %[dst], v224 offset:16\n\t"
#define ASM_CRIT1_2 \
    "s_branch .Lc0%=\n\t" \
    ".Lcr%=:\n\t" \
    "s_cmp_eq_u32 %[n], 0\n\t" \
    "s_cbranch_scc1 .Lcb%=\n\t" \
    "s_sub_u32 %[n], %[n], 1\n\t" \
    "v_max_u32 v243, v208, v209\n\t" \
    "v_max3_u32 v243, v210, v211, v243\n\t" \
    "v_cmp_eq_u32 vcc, -1, v243\n\t" \
    "s_cbranch_vccz .Lcs0_%=\n\t" \
    "buffer_load_dwordx4 v[208:211], %[ab], %[rs], %[s0] offen sc1\n\t" \
    ".Lcs0_%=:\n\t" \
    "v_max_u32 v243, v236, v237\n\t" \
    "v_max3_u32 v243, v238, v239, v243\n\t" \
    "v_cmp_eq_u32 vcc, -1, v243\n\t" \
    "s_cbranch_vccz .Lca%=\n\t" \
    "buffer_load_dwordx4 v[236:239], %[ab], %[rs], %[s1] offen sc1\n\t" \
    "s_branch .Lca%=\n\t" \
    ".Lc0%=:\n\t" \
    "buffer_load_dwordx4 v[208:211], %[ab], %[rs], %[s0] offen sc1\n\t" \
    "buffer_load_dwordx4 v[236:239], %[ab], %[rs], %[s1] offen sc1\n\t" \
    "v_bfe_i32 v241, %[ab], 7, 1\n\t" \
    "v_and_b32 v241, 0xaaaa, v241\n\t" \
    "v_or_b32 v241, 0x4444, v241\n\t" \
    "v_xor_b32 v212, 0x400, %[ab]\n\t" \
    "v_xor_b32 v216, 0x800, %[ab]\n\t" \
    "v_xor_b32 v242, 0xc00, %[ab]\n\t" \
    "v_xor_b32 v240, 0x1400, %[ab]\n\t" \
    ".Lca%=:\n\t" \
    "s_waitcnt vmcnt(0)\n\t" \
    "ds_write_b128 %[ab], v[208:211] offset:0\n\t" \
    "ds_write_b128 %[ab], v[236:239] offset:4096\n\t" \
    "v_max_u32 v243, v208, v209\n\t" \
    "v_max3_u32 v243, v210, v211, v243\n\t" \
    "v_max3_u32 v243, v236, v237, v243\n\t" \
    "v_max3_u32 v243, v238, v239, v243\n\t" \
    "v_cmp_eq_u32 vcc, -1, v243\n\t" \
    "s_cbranch_vccnz .Lcr%=\n\t" \
    ".Lcb%=:\n\t" \
    ASM_ST2 \
    "v_smfmac_f32_16x16x64_bf16 v[224:227], v[208:211], v[200:207], v241\n\t" \
    "ds_read_b128 v[200:203], %[wl] offset:4096\n\t" \
    "ds_read_b128 v[204:207], %[wl] offset:5120\n\t" \
    "v_smfmac_f32_16x16x64_bf16 v[244:247], v[208:211], v[0:7], v241\n\t" \
    "v_smfmac_f32_16x16x64_bf16 v[248:251], v[208:211], v[8:15], v241\n\t" \
    "s_waitcnt lgkmcnt(2)\n\t" \
    "s_barrier\n\t" ASM_ST \
    "ds_read_b128 v[212:215], v212 offset:0\n\t" \
    "ds_read_b128 v[216:219], v216 offset:0\n\t" \
    "ds_read_b128 v[220:223], v242 offset:0\n\t" \
    "ds_read_b128 v[208:211], v240 offset:0\n\t" \
    "v_smfmac_f32_16x16x64_bf16 v[244:247], v[236:239], v[80:87], v241\n\t" \
    "v_xor_b32 v240, 0x1800, %[ab]\n\t" \
    "v_smfmac_f32_16x16x64_bf16 v[248:251], v[236:239], v[88:95], v241\n\t" \
    "s_waitcnt lgkmcnt(4)\n\t" \
    "v_smfmac_f32_16x16x64_bf16 v[224:227], v[236:239], v[200:207], v241\n\t" \
    "ds_read_b128 v[200:203], %[wl] offset:6144\n\t" \
    "ds_read_b128 v[204:207], %[wl] offset:7168\n\t" \
    "ds_read_b128 v[236:239], v240 offset:0\n\t" \
    "s_waitcnt lgkmcnt(6)\n\t" \
    "v_smfmac_f32_16x16x64_bf16 v[224:227], v[212:215], v[16:23], v241\n\t" \
    "v_smfmac_f32_16x16x64_bf16 v[244:247], v[212:215], v[24:31], v241\n\t" \
    "v_xor_b32 v240, 0x1c00, %[ab]\n\t" \
    "v_smfmac_f32_16x16x64_bf16 v[248:251], v[212:215], v[32:39], v241\n\t" \
    "ds_read_b128 v[212:215], v240 offset:0\n\t" \
    "s_waitcnt lgkmcnt(6)\n\t" \
    "v_smfmac_f32_16x16x64_bf16 v[224:227], v[216:219], v[228:235], v241\n\t" \
    "ds_read_b128 v[228:231], %[wl] offset:8192\n\t" \
    "ds_read_b128 v[232:235], %[wl] offset:9216\n\t" \
    "v_smfmac_f32_16x16x64_bf16 v[244:247], v[216:219], v[40:47], v241\n\t" \
    "v_and_b32 v240, 0x3ff, %[ab]\n\t" \
    "v_smfmac_f32_16x16x64_bf16 v[248:251], v[216:219], v[48:55], v241\n\t" \
    "ds_read_b128 v[216:219], v240 offset:8192\n\t" \
    "s_waitcnt lgkmcnt(8)\n\t" \
    "v_smfmac_f32_16x16x64_bf16 v[224:227], v[220:223], v[56:63], v241\n\t" \
    "v_smfmac_f32_16x16x64_bf16 v[244:247], v[220:223], v[64:71], v241\n\t" \
    "v_smfmac_f32_16x16x64_bf16 v[248:251], v[220:223], v[72:79], v241\n\t" \
    "ds_read_b128 v[220:223], v240 offset:9216\n\t" \
    "s_waitcnt lgkmcnt(8)\n\t" \
    "v_smfmac_f32_16x16x64_bf16 v[244:247], v[208:211], v[104:111], v241\n\t" \
    "s_waitcnt lgkmcnt(5)\n\t" \
    "v_smfmac_f32_16x16x64_bf16 v[244:247], v[236:239], v[120:127], v241\n\t" \
    "s_waitcnt lgkmcnt(4)\n\t" \
    "v_smfmac_f32_16x16x64_bf16 v[244:247], v[212:215], v[144:151], v241\n\t" \
    "s_waitcnt lgkmcnt(1)\n\t" \
    "v_smfmac_f32_16x16x64_bf16 v[244:247], v[216:219], v[160:167], v241\n\t" \
    "s_waitcnt lgkmcnt(0)\n\t" \
    "v_smfmac_f32_16x16x64_bf16 v[244:247], v[220:223], v[184:191], v241\n\t" \
    "v_smfmac_f32_16x16x64_bf16 v[248:251], v[208:211], v[112:119], v241\n\t" \
    "v_smfmac_f32_16x16x64_bf16 v[248:251], v[236:239], v[128:135], v241\n\t" \
    "v_smfmac_f32_16x16x64_bf16 v[248:251], v[212:215], v[152:159], v241\n\t" \
    "v_permlane32_swap_b32 v244, v246\n\t" \
    "v_permlane32_swap_b32 v245, v247\n\t" \
    "v_smfmac_f32_16x16x64_bf16 v[248:251], v[216:219], v[168:175], v241\n\t" \
    "v_add_f32 v244, v244, v246\n\t" \
    "v_add_f32 v245, v245, v247\n\t" \
    "v_smfmac_f32_16x16x64_bf16 v[248:251], v[220:223], v[192:199], v241\n\t" \
    "v_cvt_pk_bf16_f32 v244, v244, v245\n\t" \
    "ds_write_b16 %[dst], v244 offset:512\n\t" \
    "ds_write_b16_d16_hi %[dst], v244 offset:528\n\t" \
    "v_smfmac_f32_16x16x64_bf16 v[224:227], v[208:211], v[96:103], v241\n\t" \
    "v_smfmac_f32_16x16x64_bf16 v[224:227], v[236:239], v[200:207], v241\n\t" \
    "v_smfmac_f32_16x16x64_bf16 v[224:227], v[212:215], v[136:143], v241\n\t" \
    "v_permlane32_swap_b32 v248, v250\n\t" \
    "v_permlane32_swap_b32 v249, v251\n\t" \
    "v_smfmac_f32_16x16x64_bf16 v[224:227], v[216:219], v[228:235], v241\n\t" \
    "v_add_f32 v248, v248, v250\n\t" \
    "v_add_f32 v249, v249, v251\n\t" \
    "v_smfmac_f32_16x16x64_bf16 v[224:227], v[220:223], v[176:183], v241\n\t" \
    "v_cvt_pk_bf16_f32 v248, v248, v249\n\t" \
    "ds_write_b16 %[dst], v248 offset:1024\n\t" \
    "ds_write_b16_d16_hi %[dst], v248 offset:1040\n\t" \
    "s_nop 4\n\t" \
    "v_permlane32_swap_b32 v224, v226\n\t" \
    "v_permlane32_swap_b32 v225, v227\n\t" \
    "v_add_f32 v224, v224, v226\n\t" \
    "v_add_f32 v225, v225, v227\n\t" \
    "v_cvt_pk_bf16_f32 v224, v224, v225\n\t" \
    "ds_write_b16 %[dst], v224 offset:0\n\t" \
    "ds_write_b16_d16_hi %[dst], v224 offset:16\n\t"
#define ASM_CRIT1_N \
    "v_bfe_i32 v241, %[ab], 7, 1\n\t" \
    "v_and_b32 v241, 0xaaaa, v241\n\t" \
    "v_or_b32 v241, 0x4444, v241\n\t" \
    "v_xor_b32 v212, 0x400, %[ab]\n\t" \
    "v_xor_b32 v216, 0x800, %[ab]\n\t" \
    "v_xor_b32 v242, 0xc00, %[ab]\n\t" \
    "v_xor_b32 v240, 0x1400, %[ab]\n\t" \
    "v_xor_b32 v236, 0x1000, %[ab]\n\t" \
    "s_waitcnt lgkmcnt(0)\n\t" \
    "s_barrier\n\t" ASM_ST \
    "ds_read_b128 v[208:211], %[ab] offset:0\n\t" \
    "ds_read_b128 v[212:215], v212 offset:0\n\t" \
    "ds_read_b128 v[216:219], v216 offset:0\n\t" \
    "ds_read_b128 v[220:223], v242 offset:0\n\t" \
    "ds_read_b128 v[236:239], v236 offset:0\n\t" \
    "s_waitcnt lgkmcnt(4)\n\t" \
    "v_smfmac_f32_16x16x64_bf16 v[224:227], v[208:211], v[200:207], v241\n\t" \
    "ds_read_b128 v[200:203], %[wl] offset:4096\n\t" \
    "ds_read_b128 v[204:207], %[wl] offset:5120\n\t" \
    "v_smfmac_f32_16x16x64_bf16 v[244:247], v[208:211], v[0:7], v241\n\t" \
    "v_smfmac_f32_16x16x64_bf16 v[248:251], v[208:211], v[8:15], v241\n\t" \
    "ds_read_b128 v[208:211], v240 offset:0\n\t" \
    "s_waitcnt lgkmcnt(6)\n\t" \
    "v_smfmac_f32_16x16x64_bf16 v[224:227], v[212:215], v[16:23], v241\n\t" \
    "v_smfmac_f32_16x16x64_bf16 v[244:247], v[212:215], v[24:31], v241\n\t" \
    "v_xor_b32 v240, 0x1800, %[ab]\n\t" \
    "v_smfmac_f32_16x16x64_bf16 v[248:251], v[212:215], v[32:39], v241\n\t" \
    "ds_read_b128 v[212:215], v240 offset:0\n\t" \
    "s_waitcnt lgkmcnt(6)\n\t" \
    "v_smfmac_f32_16x16x64_bf16 v[224:227], v[216:219], v[228:235], v241\n\t" \
    "ds_read_b128 v[228:231], %[wl] offset:6144\n\t" \
    "ds_read_b128 v[232:235], %[wl] offset:7168\n\t" \
    "v_smfmac_f32_16x16x64_bf16 v[244:247], v[216:219], v[40:47], v241\n\t" \
    "v_xor_b32 v240, 0x1c00, %[ab]\n\t" \
    "v_smfmac_f32_16x16x64_bf16 v[248:251], v[216:219], v[48:55], v241\n\t" \
    "ds_read_b128 v[216:219], v240 offset:0\n\t" \
    "s_waitcnt lgkmcnt(8)\n\t" \
    "v_smfmac_f32_16x16x64_bf16 v[224:227], v[220:223], v[56:63], v241\n\t" \
    "v_smfmac_f32_16x16x64_bf16 v[244:247], v[220:223], v[64:71], v241\n\t" \
    "v_and_b32 v240, 0x3ff, %[ab]\n\t" \
    "v_smfmac_f32_16x16x64_bf16 v[248:251], v[220:223], v[72:79], v241\n\t" \
    "ds_read_b128 v[220:223], v240 offset:8192\n\t" \
    "s_waitcnt lgkmcnt(6)\n\t" \
    "v_smfmac_f32_16x16x64_bf16 v[224:227], v[236:239], v[200:207], v241\n\t" \
    "ds_read_b128 v[200:203], %[wl] offset:8192\n\t" \
    "ds_read_b128 v[204:207], %[wl] offset:9216\n\t" \
    "v_smfmac_f32_16x16x64_bf16 v[244:247], v[236:239], v[80:87], v241\n\t" \
    "v_smfmac_f32_16x16x64_bf16 v[248:251], v[236:239], v[88:95], v241\n\t" \
    "ds_read_b128 v[236:239], v240 offset:9216\n\t" \
    "s_waitcnt lgkmcnt(8)\n\t" \
    "v_smfmac_f32_16x16x64_bf16 v[244:247], v[208:211], v[104:111], v241\n\t" \
    "s_waitcnt lgkmcnt(7)\n\t" \
    "v_smfmac_f32_16x16x64_bf16 v[244:247], v[212:215], v[120:127], v241\n\t" \
    "s_waitcnt lgkmcnt(4)\n\t" \
    "v_smfmac_f32_16x16x64_bf16 v[244:247], v[216:219], v[144:151], v241\n\t" \
    "s_waitcnt lgkmcnt(3)\n\t" \
    "v_smfmac_f32_16x16x64_bf16 v[244:247], v[220:223], v[160:167], v241\n\t" \
    "s_waitcnt lgkmcnt(0)\n\t" \
    "v_smfmac_f32_16x16x64_bf16 v[244:247], v[236:239], v[184:191], v241\n\t" \
    "v_smfmac_f32_16x16x64_bf16 v[248:251], v[208:211], v[112:119], v241\n\t" \
    "v_smfmac_f32_16x16x64_bf16 v[248:251], v[212:215], v[128:135], v241\n\t" \
    "v_smfmac_f32_16x16x64_bf16 v[248:251], v[216:219], v[152:159], v241\n\t" \
    "v_permlane32_swap_b32 v244, v246\n\t" \
    "v_permlane32_swap_b32 v245, v247\n\t" \
    "v_smfmac_f32_16x16x64_bf16 v[248:251], v[220:223], v[168:175], v241\n\t" \
    "v_add_f32 v244, v244, v246\n\t" \
    "v_add_f32 v245, v245, v247\n\t" \
    "v_smfmac_f32_16x16x64_bf16 v[248:251], v[236:239], v[192:199], v241\n\t" \
    "v_cvt_pk_bf16_f32 v244, v244, v245\n\t" \
    "ds_write_b16 %[dst], v244 offset:512\n\t" \
    "ds_write_b16_d16_hi %[dst], v244 offset:528\n\t" \
    "v_smfmac_f32_16x16x64_bf16 v[224:227], v[208:211], v[96:103], v241\n\t" \
    "v_smfmac_f32_16x16x64_bf16 v[224:227], v[212:215], v[228:235], v241\n\t" \
    "v_smfmac_f32_16x16x64_bf16 v[224:227], v[216:219], v[136:143], v241\n\t" \
    "v_permlane32_swap_b32 v248, v250\n\t" \
    "v_permlane32_swap_b32 v249, v251\n\t" \
    "v_smfmac_f32_16x16x64_bf16 v[224:227], v[220:223], v[200:207], v241\n\t" \
    "v_add_f32 v248, v248, v250\n\t" \
    "v_add_f32 v249, v249, v251\n\t" \
    "v_smfmac_f32_16x16x64_bf16 v[224:227], v[236:239], v[176:183], v241\n\t" \
    "v_cvt_pk_bf16_f32 v248, v248, v249\n\t" \
    "ds_write_b16 %[dst], v248 offset:1024\n\t" \
    "ds_write_b16_d16_hi %[dst], v248 offset:1040\n\t" \
    "s_nop 4\n\t" \
    "v_permlane32_swap_b32 v224, v226\n\t" \
    "v_permlane32_swap_b32 v225, v227\n\t" \
    "v_add_f32 v224, v224, v226\n\t" \
    "v_add_f32 v225, v225, v227\n\t" \
    "v_cvt_pk_bf16_f32 v224, v224, v225\n\t" \
    "ds_write_b16 %[dst], v224 offset:0\n\t" \
    "ds_write_b16_d16_hi %[dst], v224 offset:16\n\t"
#define ASM_VOX \
    "s_waitcnt lgkmcnt(0)\n\t" \
    "ds_read_b128 v[224:227], %[va] offset:0\n\t" \
    "ds_read_b128 v[248:251], %[wl] offset:0\n\t" \
    "ds_read_b128 v[228:231], %[va] offset:6144\n\t" \
    "ds_read_b128 v[232:235], %[va] offset:2048\n\t" \
    "ds_read_b128 v[236:239], %[va] offset:8192\n\t" \
    "ds_read_b128 v[240:243], %[va] offset:3072\n\t" \
    "s_waitcnt lgkmcnt(4)\n\t" \
    "v_mfma_f32_16x16x32_bf16 v[200:203], v[224:227], v[248:251], 0\n\t" \
    "ds_read_b128 v[244:247], %[va] offset:9216\n\t" \
    "s_waitcnt lgkmcnt(4)\n\t" \
    "v_mfma_f32_16x16x32_bf16 v[204:207], v[228:231], v[248:251], 0\n\t" \
    "ds_read_b128 v[224:227], %[va] offset:1024\n\t" \
    "s_waitcnt lgkmcnt(4)\n\t" \
    "v_mfma_f32_16x16x32_bf16 v[200:203], v[232:235], v[0:3], v[200:203]\n\t" \
    "ds_read_b128 v[228:231], %[va] offset:7168\n\t" \
    "ds_read_b128 v[248:251], %[wl] offset:1024\n\t" \
    "s_waitcnt lgkmcnt(5)\n\t" \
    "v_mfma_f32_16x16x32_bf16 v[204:207], v[236:239], v[0:3], v[204:207]\n\t" \
    "ds_read_b128 v[232:235], %[va] offset:4096\n\t" \
    "s_waitcnt lgkmcnt(5)\n\t" \
    "v_mfma_f32_16x16x32_bf16 v[200:203], v[240:243], v[4:7], v[200:203]\n\t" \
    "ds_read_b128 v[236:239], %[va] offset:10240\n\t" \
    "s_waitcnt lgkmcnt(5)\n\t" \
    "v_mfma_f32_16x16x32_bf16 v[204:207], v[244:247], v[4:7], v[204:207]\n\t" \
    "ds_read_b128 v[240:243], %[va] offset:5120\n\t" \
    "s_waitcnt lgkmcnt(3)\n\t" \
    "v_mfma_f32_16x16x32_bf16 v[200:203], v[224:227], v[248:251], v[200:203]\n\t" \
    "ds_read_b128 v[244:247], %[va] offset:11264\n\t" \
    "v_mfma_f32_16x16x32_bf16 v[208:211], v[224:227], v[20:23], 0\n\t" \
    "v_mfma_f32_16x16x32_bf16 v[204:207], v[228:231], v[248:251], v[204:207]\n\t" \
    "ds_read_b128 v[224:227], %[va] offset:0\n\t" \
    "v_mfma_f32_16x16x32_bf16 v[212:215], v[228:231], v[20:23], 0\n\t" \
    "s_waitcnt lgkmcnt(4)\n\t" \
    "v_mfma_f32_16x16x32_bf16 v[200:203], v[232:235], v[8:11], v[200:203]\n\t" \
    "ds_read_b128 v[228:231], %[va] offset:6144\n\t" \
    "ds_read_b128 v[248:251], %[wl] offset:2048\n\t" \
    "v_mfma_f32_16x16x32_bf16 v[208:211], v[232:235], v[32:35], v[208:211]\n\t" \
    "s_waitcnt lgkmcnt(5)\n\t" \
    "v_mfma_f32_16x16x32_bf16 v[204:207], v[236:239], v[8:11], v[204:207]\n\t" \
    "ds_read_b128 v[232:235], %[va] offset:2048\n\t" \
    "v_mfma_f32_16x16x32_bf16 v[212:215], v[236:239], v[32:35], v[212:215]\n\t" \
    "s_waitcnt lgkmcnt(5)\n\t" \
    "v_mfma_f32_16x16x32_bf16 v[200:203], v[240:243], v[12:15], v[200:203]\n\t" \
    "ds_read_b128 v[236:239], %[va] offset:8192\n\t" \
    "v_mfma_f32_16x16x32_bf16 v[208:211], v[240:243], v[36:39], v[208:211]\n\t" \
    "s_waitcnt lgkmcnt(5)\n\t" \
    "v_mfma_f32_16x16x32_bf16 v[204:207], v[244:247], v[12:15], v[204:207]\n\t" \
    "ds_read_b128 v[240:243], %[va] offset:3072\n\t" \
    "v_mfma_f32_16x16x32_bf16 v[212:215], v[244:247], v[36:39], v[212:215]\n\t" \
    "s_waitcnt lgkmcnt(3)\n\t" \
    "v_mfma_f32_16x16x32_bf16 v[216:219], v[224:227], v[248:251], 0\n\t" \
    "ds_read_b128 v[244:247], %[va] offset:9216\n\t" \
    "v_mfma_f32_16x16x32_bf16 v[208:211], v[224:227], v[16:19], v[208:211]\n\t" \
    "v_mfma_f32_16x16x32_bf16 v[220:223], v[228:231], v[248:251], 0\n\t" \
    "ds_read_b128 v[224:227], %[va] offset:1024\n\t" \
    "v_mfma_f32_16x16x32_bf16 v[212:215], v[228:231], v[16:19], v[212:215]\n\t" \
    "v_cvt_pk_bf16_f32 v200, v200, v201\n\t" \
    "s_waitcnt lgkmcnt(4)\n\t" \
    "v_mfma_f32_16x16x32_bf16 v[216:219], v[232:235], v[40:43], v[216:219]\n\t" \
    "ds_read_b128 v[228:231], %[va] offset:7168\n\t" \
    "ds_read_b128 v[248:251], %[wl] offset:3072\n\t" \
    "v_cvt_pk_bf16_f32 v201, v202, v203\n\t" \
    "v_mfma_f32_16x16x32_bf16 v[208:211], v[232:235], v[24:27], v[208:211]\n\t" \
    "v_cvt_pk_bf16_f32 v202, v204, v205\n\t" \
    "s_waitcnt lgkmcnt(5)\n\t" \
    "v_mfma_f32_16x16x32_bf16 v[220:223], v[236:239], v[40:43], v[220:223]\n\t" \
    "ds_read_b128 v[232:235], %[va] offset:4096\n\t" \
    "v_cvt_pk_bf16_f32 v203, v206, v207\n\t" \
    "v_mfma_f32_16x16x32_bf16 v[212:215], v[236:239], v[24:27], v[212:215]\n\t" \
    "buffer_store_dwordx4 v[200:203], %[l16], %[rd], %[so] offen sc1\n\t" \
    "s_waitcnt lgkmcnt(5)\n\t" \
    "v_mfma_f32_16x16x32_bf16 v[216:219], v[240:243], v[44:47], v[216:219]\n\t" \
    "ds_read_b128 v[236:239], %[va] offset:10240\n\t" \
    "s_add_u32 %[so], %[so], 0x10000\n\t" \
    "v_mfma_f32_16x16x32_bf16 v[208:211], v[240:243], v[28:31], v[208:211]\n\t" \
    "s_waitcnt lgkmcnt(5)\n\t" \
    "v_mfma_f32_16x16x32_bf16 v[220:223], v[244:247], v[44:47], v[220:223]\n\t" \
    "ds_read_b128 v[240:243], %[va] offset:5120\n\t" \
    "v_mfma_f32_16x16x32_bf16 v[212:215], v[244:247], v[28:31], v[212:215]\n\t" \
    "s_waitcnt lgkmcnt(3)\n\t" \
    "v_mfma_f32_16x16x32_bf16 v[216:219], v[224:227], v[248:251], v[216:219]\n\t" \
    "ds_read_b128 v[244:247], %[va] offset:11264\n\t" \
    "v_mfma_f32_16x16x32_bf16 v[200:203], v[224:227], v[60:63], 0\n\t" \
    "v_mfma_f32_16x16x32_bf16 v[220:223], v[228:231], v[248:251], v[220:223]\n\t" \
    "ds_read_b128 v[224:227], %[va] offset:0\n\t" \
    "v_mfma_f32_16x16x32_bf16 v[204:207], v[228:231], v[60:63], 0\n\t" \
    "v_cvt_pk_bf16_f32 v208, v208, v209\n\t" \
    "s_waitcnt lgkmcnt(4)\n\t" \
    "v_mfma_f32_16x16x32_bf16 v[216:219], v[232:235], v[48:51], v[216:219]\n\t" \
    "ds_read_b128 v[228:231], %[va] offset:6144\n\t" \
    "ds_read_b128 v[248:251], %[wl] offset:4096\n\t" \
    "v_cvt_pk_bf16_f32 v209, v210, v211\n\t" \
    "v_mfma_f32_16x16x32_bf16 v[200:203], v[232:235], v[72:75], v[200:203]\n\t" \
    "v_cvt_pk_bf16_f32 v210, v212, v213\n\t" \
    "s_waitcnt lgkmcnt(5)\n\t" \
    "v_mfma_f32_16x16x32_bf16 v[220:223], v[236:239], v[48:51], v[220:223]\n\t" \
    "ds_read_b128 v[232:235], %[va] offset:2048\n\t" \
    "v_cvt_pk_bf16_f32 v211, v214, v215\n\t" \
    "v_mfma_f32_16x16x32_bf16 v[204:207], v[236:239], v[72:75], v[204:207]\n\t" \
    "buffer_store_dwordx4 v[208:211], %[l16], %[rd], %[so] offen sc1\n\t" \
    "s_waitcnt lgkmcnt(5)\n\t" \
    "v_mfma_f32_16x16x32_bf16 v[216:219], v[240:243], v[52:55], v[216:219]\n\t" \
    "ds_read_b128 v[236:239], %[va] offset:8192\n\t" \
    "s_add_u32 %[so], %[so], 0x10000\n\t" \
    "v_mfma_f32_16x16x32_bf16 v[200:203], v[240:243], v[76:79], v[200:203]\n\t" \
    "s_waitcnt lgkmcnt(5)\n\t" \
    "v_mfma_f32_16x16x32_bf16 v[220:223], v[244:247], v[52:55], v[220:223]\n\t" \
    "ds_read_b128 v[240:243], %[va] offset:3072\n\t" \
    "v_mfma_f32_16x16x32_bf16 v[204:207], v[244:247], v[76:79], v[204:207]\n\t" \
    "s_waitcnt lgkmcnt(3)\n\t" \
    "v_mfma_f32_16x16x32_bf16 v[208:211], v[224:227], v[248:251], 0\n\t" \
    "ds_read_b128 v[244:247], %[va] offset:9216\n\t" \
    "v_mfma_f32_16x16x32_bf16 v[200:203], v[224:227], v[56:59], v[200:203]\n\t" \
    "v_mfma_f32_16x16x32_bf16 v[212:215], v[228:231], v[248:251], 0\n\t" \
    "ds_read_b128 v[224:227], %[va] offset:1024\n\t" \
    "v_mfma_f32_16x16x32_bf16 v[204:207], v[228:231], v[56:59], v[204:207]\n\t" \
    "v_cvt_pk_bf16_f32 v216, v216, v217\n\t" \
    "s_waitcnt lgkmcnt(4)\n\t" \
    "v_mfma_f32_16x16x32_bf16 v[208:211], v[232:235], v[80:83], v[208:211]\n\t" \
    "ds_read_b128 v[228:231], %[va] offset:7168\n\t" \
    "ds_read_b128 v[248:251], %[wl] offset:5120\n\t" \
    "v_cvt_pk_bf16_f32 v217, v218, v219\n\t" \
    "v_mfma_f32_16x16x32_bf16 v[200:203], v[232:235], v[64:67], v[200:203]\n\t" \
    "v_cvt_pk_bf16_f32 v218, v220, v221\n\t" \
    "s_waitcnt lgkmcnt(5)\n\t" \
    "v_mfma_f32_16x16x32_bf16 v[212:215], v[236:239], v[80:83], v[212:215]\n\t" \
    "ds_read_b128 v[232:235], %[va] offset:4096\n\t" \
    "v_cvt_pk_bf16_f32 v219, v222, v223\n\t" \
    "v_mfma_f32_16x16x32_bf16 v[204:207], v[236:239], v[64:67], v[204:207]\n\t" \
    "buffer_store_dwordx4 v[216:219], %[l16], %[rd], %[so] offen sc1\n\t" \
    "s_waitcnt lgkmcnt(5)\n\t" \
    "v_mfma_f32_16x16x32_bf16 v[208:211], v[240:243], v[84:87], v[208:211]\n\t" \
    "ds_read_b128 v[236:239], %[va] offset:10240\n\t" \
    "s_add_u32 %[so], %[so], 0x10000\n\t" \
    "v_mfma_f32_16x16x32_bf16 v[200:203], v[240:243], v[68:71], v[200:203]\n\t" \
    "s_waitcnt lgkmcnt(5)\n\t" \
    "v_mfma_f32_16x16x32_bf16 v[212:215], v[244:247], v[84:87], v[212:215]\n\t" \
    "ds_read_b128 v[240:243], %[va] offset:5120\n\t" \
    "v_mfma_f32_16x16x32_bf16 v[204:207], v[244:247], v[68:71], v[204:207]\n\t" \
    "s_waitcnt lgkmcnt(3)\n\t" \
    "v_mfma_f32_16x16x32_bf16 v[208:211], v[224:227], v[248:251], v[208:211]\n\t" \
    "ds_read_b128 v[244:247], %[va] offset:11264\n\t" \
    "v_mfma_f32_16x16x32_bf16 v[216:219], v[224:227], v[100:103], 0\n\t" \
    "v_mfma_f32_16x16x32_bf16 v[212:215], v[228:231], v[248:251], v[212:215]\n\t" \
    "ds_read_b128 v[224:227], %[va] offset:0\n\t" \
    "v_mfma_f32_16x16x32_bf16 v[220:223], v[228:231], v[100:103], 0\n\t" \
    "v_cvt_pk_bf16_f32 v200, v200, v201\n\t" \
    "s_waitcnt lgkmcnt(4)\n\t" \
    "v_mfma_f32_16x16x32_bf16 v[208:211], v[232:235], v[88:91], v[208:211]\n\t" \
    "ds_read_b128 v[228:231], %[va] offset:6144\n\t" \
    "ds_read_b128 v[248:251], %[wl] offset:6144\n\t" \
    "v_cvt_pk_bf16_f32 v201, v202, v203\n\t" \
    "v_mfma_f32_16x16x32_bf16 v[216:219], v[232:235], v[112:115], v[216:219]\n\t" \
    "v_cvt_pk_bf16_f32 v202, v204, v205\n\t" \
    "s_waitcnt lgkmcnt(5)\n\t" \
    "v_mfma_f32_16x16x32_bf16 v[212:215], v[236:239], v[88:91], v[212:215]\n\t" \
    "ds_read_b128 v[232:235], %[va] offset:2048\n\t" \
    "v_cvt_pk_bf16_f32 v203, v206, v207\n\t" \
    "v_mfma_f32_16x16x32_bf16 v[220:223], v[236:239], v[112:115], v[220:223]\n\t" \
    "buffer_store_dwordx4 v[200:203], %[l16], %[rd], %[so] offen sc1\n\t" \
    "s_waitcnt lgkmcnt(5)\n\t" \
    "v_mfma_f32_16x16x32_bf16 v[208:211], v[240:243], v[92:95], v[208:211]\n\t" \
    "ds_read_b128 v[236:239], %[va] offset:8192\n\t" \
    "s_add_u32 %[so], %[so], 0x10000\n\t" \
    "v_mfma_f32_16x16x32_bf16 v[216:219], v[240:243], v[116:119], v[216:219]\n\t" \
    "s_waitcnt lgkmcnt(5)\n\t" \
    "v_mfma_f32_16x16x32_bf16 v[212:215], v[244:247], v[92:95], v[212:215]\n\t" \
    "ds_read_b128 v[240:243], %[va] offset:3072\n\t" \
    "v_mfma_f32_16x16x32_bf16 v[220:223], v[244:247], v[116:119], v[220:223]\n\t" \
    "s_waitcnt lgkmcnt(3)\n\t" \
    "v_mfma_f32_16x16x32_bf16 v[200:203], v[224:227], v[248:251], 0\n\t" \
    "ds_read_b128 v[244:247], %[va] offset:9216\n\t" \
    "v_mfma_f32_16x16x32_bf16 v[216:219], v[224:227], v[96:99], v[216:219]\n\t" \
    "v_mfma_f32_16x16x32_bf16 v[204:207], v[228:231], v[248:251], 0\n\t" \
    "ds_read_b128 v[224:227], %[va] offset:1024\n\t" \
    "v_mfma_f32_16x16x32_bf16 v[220:223], v[228:231], v[96:99], v[220:223]\n\t" \
    "v_cvt_pk_bf16_f32 v208, v208, v209\n\t" \
    "s_waitcnt lgkmcnt(4)\n\t" \
    "v_mfma_f32_16x16x32_bf16 v[200:203], v[232:235], v[120:123], v[200:203]\n\t" \
    "ds_read_b128 v[228:231], %[va] offset:7168\n\t" \
    "ds_read_b128 v[248:251], %[wl] offset:7168\n\t" \
    "v_cvt_pk_bf16_f32 v209, v210, v211\n\t" \
    "v_mfma_f32_16x16x32_bf16 v[216:219], v[232:235], v[104:107], v[216:219]\n\t" \
    "v_cvt_pk_bf16_f32 v210, v212, v213\n\t" \
    "s_waitcnt lgkmcnt(5)\n\t" \
    "v_mfma_f32_16x16x32_bf16 v[204:207], v[236:239], v[120:123], v[204:207]\n\t" \
    "ds_read_b128 v[232:235], %[va] offset:4096\n\t" \
    "v_cvt_pk_bf16_f32 v211, v214, v215\n\t" \
    "v_mfma_f32_16x16x32_bf16 v[220:223], v[236:239], v[104:107], v[220:223]\n\t" \
    "buffer_store_dwordx4 v[208:211], %[l16], %[rd], %[so] offen sc1\n\t" \
    "s_waitcnt lgkmcnt(5)\n\t" \
    "v_mfma_f32_16x16x32_bf16 v[200:203], v[240:243], v[124:127], v[200:203]\n\t" \
    "ds_read_b128 v[236:239], %[va] offset:10240\n\t" \
    "s_add_u32 %[so], %[so], 0x10000\n\t" \
    "v_mfma_f32_16x16x32_bf16 v[216:219], v[240:243], v[108:111], v[216:219]\n\t" \
    "s_waitcnt lgkmcnt(5)\n\t" \
    "v_mfma_f32_16x16x32_bf16 v[204:207], v[244:247], v[124:127], v[204:207]\n\t" \
    "ds_read_b128 v[240:243], %[va] offset:5120\n\t" \
    "v_mfma_f32_16x16x32_bf16 v[220:223], v[244:247], v[108:111], v[220:223]\n\t" \
    "s_waitcnt lgkmcnt(3)\n\t" \
    "v_mfma_f32_16x16x32_bf16 v[200:203], v[224:227], v[248:251], v[200:203]\n\t" \
    "ds_read_b128 v[244:247], %[va] offset:11264\n\t" \
    "v_mfma_f32_16x16x32_bf16 v[208:211], v[224:227], v[140:143], 0\n\t" \
    "v_mfma_f32_16x16x32_bf16 v[204:207], v[228:231], v[248:251], v[204:207]\n\t" \
    "ds_read_b128 v[224:227], %[va] offset:0\n\t" \
    "v_mfma_f32_16x16x32_bf16 v[212:215], v[228:231], v[140:143], 0\n\t" \
    "v_cvt_pk_bf16_f32 v216, v216, v217\n\t" \
    "s_waitcnt lgkmcnt(4)\n\t" \
    "v_mfma_f32_16x16x32_bf16 v[200:203], v[232:235], v[128:131], v[200:203]\n\t" \
    "ds_read_b128 v[228:231], %[va] offset:6144\n\t" \
    "ds_read_b128 v[248:251], %[wl] offset:8192\n\t" \
    "v_cvt_pk_bf16_f32 v217, v218, v219\n\t" \
    "v_mfma_f32_16x16x32_bf16 v[208:211], v[232:235], v[152:155], v[208:211]\n\t" \
    "v_cvt_pk_bf16_f32 v218, v220, v221\n\t" \
    "s_waitcnt lgkmcnt(5)\n\t" \
    "v_mfma_f32_16x16x32_bf16 v[204:207], v[236:239], v[128:131], v[204:207]\n\t" \
    "ds_read_b128 v[232:235], %[va] offset:2048\n\t" \
    "v_cvt_pk_bf16_f32 v219, v222, v223\n\t" \
    "v_mfma_f32_16x16x32_bf16 v[212:215], v[236:239], v[152:155], v[212:215]\n\t" \
    "buffer_store_dwordx4 v[216:219], %[l16], %[rd], %[so] offen sc1\n\t" \
    "s_waitcnt lgkmcnt(5)\n\t" \
    "v_mfma_f32_16x16x32_bf16 v[200:203], v[240:243], v[132:135], v[200:203]\n\t" \
    "ds_read_b128 v[236:239], %[va] offset:8192\n\t" \
    "s_add_u32 %[so], %[so], 0x10000\n\t" \
    "v_mfma_f32_16x16x32_bf16 v[208:211], v[240:243], v[156:159], v[208:211]\n\t" \
    "s_waitcnt lgkmcnt(5)\n\t" \
    "v_mfma_f32_16x16x32_bf16 v[204:207], v[244:247], v[132:135], v[204:207]\n\t" \
    "ds_read_b128 v[240:243], %[va] offset:3072\n\t" \
    "v_mfma_f32_16x16x32_bf16 v[212:215], v[244:247], v[156:159], v[212:215]\n\t" \
    "s_waitcnt lgkmcnt(3)\n\t" \
    "v_mfma_f32_16x16x32_bf16 v[216:219], v[224:227], v[248:251], 0\n\t" \
    "ds_read_b128 v[244:247], %[va] offset:9216\n\t" \
    "v_mfma_f32_16x16x32_bf16 v[208:211], v[224:227], v[136:139], v[208:211]\n\t" \
    "v_mfma_f32_16x16x32_bf16 v[220:223], v[228:231], v[248:251], 0\n\t" \
    "ds_read_b128 v[224:227], %[va] offset:1024\n\t" \
    "v_mfma_f32_16x16x32_bf16 v[212:215], v[228:231], v[136:139], v[212:215]\n\t" \
    "v_cvt_pk_bf16_f32 v200, v200, v201\n\t" \
    "s_waitcnt lgkmcnt(4)\n\t" \
    "v_mfma_f32_16x16x32_bf16 v[216:219], v[232:235], v[160:163], v[216:219]\n\t" \
    "ds_read_b128 v[228:231], %[va] offset:7168\n\t" \
    "ds_read_b128 v[248:251], %[wl] offset:9216\n\t" \
    "v_cvt_pk_bf16_f32 v201, v202, v203\n\t" \
    "v_mfma_f32_16x16x32_bf16 v[208:211], v[232:235], v[144:147], v[208:211]\n\t" \
    "v_cvt_pk_bf16_f32 v202, v204, v205\n\t" \
    "s_waitcnt lgkmcnt(5)\n\t" \
    "v_mfma_f32_16x16x32_bf16 v[220:223], v[236:239], v[160:163], v[220:223]\n\t" \
    "ds_read_b128 v[232:235], %[va] offset:4096\n\t" \
    "v_cvt_pk_bf16_f32 v203, v206, v207\n\t" \
    "v_mfma_f32_16x16x32_bf16 v[212:215], v[236:239], v[144:147], v[212:215]\n\t" \
    "buffer_store_dwordx4 v[200:203], %[l16], %[rd], %[so] offen sc1\n\t" \
    "s_waitcnt lgkmcnt(5)\n\t" \
    "v_mfma_f32_16x16x32_bf16 v[216:219], v[240:243], v[164:167], v[216:219]\n\t" \
    "ds_read_b128 v[236:239], %[va] offset:10240\n\t" \
    "s_add_u32 %[so], %[so], 0x10000\n\t" \
    "v_mfma_f32_16x16x32_bf16 v[208:211], v[240:243], v[148:151], v[208:211]\n\t" \
    "s_waitcnt lgkmcnt(5)\n\t" \
    "v_mfma_f32_16x16x32_bf16 v[220:223], v[244:247], v[164:167], v[220:223]\n\t" \
    "ds_read_b128 v[240:243], %[va] offset:5120\n\t" \
    "v_mfma_f32_16x16x32_bf16 v[212:215], v[244:247], v[148:151], v[212:215]\n\t" \
    "s_waitcnt lgkmcnt(3)\n\t" \
    "v_mfma_f32_16x16x32_bf16 v[216:219], v[224:227], v[248:251], v[216:219]\n\t" \
    "ds_read_b128 v[244:247], %[va] offset:11264\n\t" \
    "v_mfma_f32_16x16x32_bf16 v[200:203], v[224:227], v[180:183], 0\n\t" \
    "v_mfma_f32_16x16x32_bf16 v[220:223], v[228:231], v[248:251], v[220:223]\n\t" \
    "ds_read_b128 v[224:227], %[va] offset:0\n\t" \
    "v_mfma_f32_16x16x32_bf16 v[204:207], v[228:231], v[180:183], 0\n\t" \
    "v_cvt_pk_bf16_f32 v208, v208, v209\n\t" \
    "s_waitcnt lgkmcnt(4)\n\t" \
    "v_mfma_f32_16x16x32_bf16 v[216:219], v[232:235], v[168:171], v[216:219]\n\t" \
    "ds_read_b128 v[228:231], %[va] offset:6144\n\t" \
    "v_cvt_pk_bf16_f32 v209, v210, v211\n\t" \
    "v_mfma_f32_16x16x32_bf16 v[200:203], v[232:235], v[192:195], v[200:203]\n\t" \
    "v_cvt_pk_bf16_f32 v210, v212, v213\n\t" \
    "s_waitcnt lgkmcnt(4)\n\t" \
    "v_mfma_f32_16x16x32_bf16 v[220:223], v[236:239], v[168:171], v[220:223]\n\t" \
    "ds_read_b128 v[232:235], %[va] offset:2048\n\t" \
    "v_cvt_pk_bf16_f32 v211, v214, v215\n\t" \
    "v_mfma_f32_16x16x32_bf16 v[204:207], v[236:239], v[192:195], v[204:207]\n\t" \
    "buffer_store_dwordx4 v[208:211], %[l16], %[rd], %[so] offen sc1\n\t" \
    "s_waitcnt lgkmcnt(4)\n\t" \
    "v_mfma_f32_16x16x32_bf16 v[216:219], v[240:243], v[172:175], v[216:219]\n\t" \
    "ds_read_b128 v[236:239], %[va] offset:8192\n\t" \
    "s_add_u32 %[so], %[so], 0x10000\n\t" \
    "v_mfma_f32_16x16x32_bf16 v[200:203], v[240:243], v[196:199], v[200:203]\n\t" \
    "s_waitcnt lgkmcnt(4)\n\t" \
    "v_mfma_f32_16x16x32_bf16 v[220:223], v[244:247], v[172:175], v[220:223]\n\t" \
    "ds_read_b128 v[240:243], %[va] offset:3072\n\t" \
    "v_mfma_f32_16x16x32_bf16 v[204:207], v[244:247], v[196:199], v[204:207]\n\t" \
    "s_waitcnt lgkmcnt(4)\n\t" \
    "v_mfma_f32_16x16x32_bf16 v[200:203], v[224:227], v[176:179], v[200:203]\n\t" \
    "ds_read_b128 v[244:247], %[va] offset:9216\n\t" \
    "s_waitcnt lgkmcnt(4)\n\t" \
    "v_mfma_f32_16x16x32_bf16 v[204:207], v[228:231], v[176:179], v[204:207]\n\t" \
    "s_waitcnt lgkmcnt(3)\n\t" \
    "v_mfma_f32_16x16x32_bf16 v[200:203], v[232:235], v[184:187], v[200:203]\n\t" \
    "s_waitcnt lgkmcnt(2)\n\t" \
    "v_mfma_f32_16x16x32_bf16 v[204:207], v[236:239], v[184:187], v[204:207]\n\t" \
    "v_cvt_pk_bf16_f32 v216, v216, v217\n\t" \
    "s_waitcnt lgkmcnt(1)\n\t" \
    "v_mfma_f32_16x16x32_bf16 v[200:203], v[240:243], v[188:191], v[200:203]\n\t" \
    "v_cvt_pk_bf16_f32 v217, v218, v219\n\t" \
    "s_waitcnt lgkmcnt(0)\n\t" \
    "v_mfma_f32_16x16x32_bf16 v[204:207], v[244:247], v[188:191], v[204:207]\n\t" \
    "v_cvt_pk_bf16_f32 v218, v220, v221\n\t" \
    "v_cvt_pk_bf16_f32 v219, v222, v223\n\t" \
    "buffer_store_dwordx4 v[216:219], %[l16], %[rd], %[so] offen sc1\n\t" \
    "s_add_u32 %[so], %[so], 0x10000\n\t" \
    "s_nop 0\n\t" \
    "v_cvt_pk_bf16_f32 v200, v200, v201\n\t" \
    "v_cvt_pk_bf16_f32 v201, v202, v203\n\t" \
    "s_nop 1\n\t" \
    "v_cvt_pk_bf16_f32 v202, v204, v205\n\t" \
    "v_cvt_pk_bf16_f32 v203, v206, v207\n\t" \
    "buffer_store_dwordx4 v[200:203], %[l16], %[rd], %[so] offen sc1\n\t" \
    "s_add_u32 %[so], %[so], 0x10000\n\t" \
    "s_nop 1\n\t"
#define ASM_CRIT2 \
    "s_waitcnt lgkmcnt(0)\n\t" \
    "s_barrier\n\t" ASM_ST \
    "ds_read_b128 v[204:207], v200 offset:11264\n\t" \
    "ds_read_b128 v[208:211], v200 offset:12288\n\t" \
    "ds_read_b128 v[200:203], v200 offset:10240\n\t" \
    "s_waitcnt lgkmcnt(2)\n\t" \
    "v_smfmac_f32_16x16x64_bf16 v[224:227], v[204:207], v[24:31], %[sidx]\n\t" \
    "v_smfmac_f32_16x16x64_bf16 v[220:223], v[204:207], v[0:7], %[sidx]\n\t" \
    "s_waitcnt lgkmcnt(1)\n\t" \
    "v_smfmac_f32_16x16x64_bf16 v[224:227], v[208:211], v[32:39], %[sidx]\n\t" \
    "v_smfmac_f32_16x16x64_bf16 v[220:223], v[208:211], v[8:15], %[sidx]\n\t" \
    "s_waitcnt lgkmcnt(0)\n\t" \
    "v_smfmac_f32_16x16x64_bf16 v[224:227], v[200:203], v[16:23], %[sidx]\n\t" \
    "v_smfmac_f32_16x16x64_bf16 v[220:223], v[200:203], v[212:219], %[sidx]\n\t" \
    "ds_read_b128 v[212:215], %[wl] offset:2048\n\t" \
    "ds_read_b128 v[216:219], %[wl] offset:3072\n\t" \
    "v_smfmac_f32_16x16x64_bf16 v[232:235], v[204:207], v[64:71], %[sidx]\n\t" \
    "v_smfmac_f32_16x16x64_bf16 v[228:231], v[204:207], v[40:47], %[sidx]\n\t" \
    "v_smfmac_f32_16x16x64_bf16 v[232:235], v[208:211], v[72:79], %[sidx]\n\t" \
    "v_permlane32_swap_b32 v220, v224\n\t" \
    "v_permlane32_swap_b32 v221, v225\n\t" \
    "v_permlane32_swap_b32 v222, v226\n\t" \
    "v_smfmac_f32_16x16x64_bf16 v[228:231], v[208:211], v[48:55], %[sidx]\n\t" \
    "v_permlane32_swap_b32 v223, v227\n\t" \
    "v_add_f32 v220, v220, v224\n\t" \
    "v_add_f32 v221, v221, v225\n\t" \
    "v_smfmac_f32_16x16x64_bf16 v[232:235], v[200:203], v[56:63], %[sidx]\n\t" \
    "v_add_f32 v222, v222, v226\n\t" \
    "v_add_f32 v223, v223, v227\n\t" \
    "v_cvt_pk_bf16_f32 v226, v220, v221\n\t" \
    "s_waitcnt lgkmcnt(0)\n\t" \
    "v_smfmac_f32_16x16x64_bf16 v[228:231], v[200:203], v[212:219], %[sidx]\n\t" \
    "ds_read_b128 v[212:215], %[wl] offset:4096\n\t" \
    "ds_read_b128 v[216:219], %[wl] offset:5120\n\t" \
    "v_cvt_pk_bf16_f32 v227, v222, v223\n\t" \
    "v_min_u32 v226, -2, v226\n\t" \
    "v_min_u32 v227, -2, v227\n\t" \
    "v_smfmac_f32_16x16x64_bf16 v[240:243], v[204:207], v[104:111], %[sidx]\n\t" \
    "v_smfmac_f32_16x16x64_bf16 v[236:239], v[204:207], v[80:87], %[sidx]\n\t" \
    "v_smfmac_f32_16x16x64_bf16 v[240:243], v[208:211], v[112:119], %[sidx]\n\t" \
    "v_permlane32_swap_b32 v228, v232\n\t" \
    "v_permlane32_swap_b32 v229, v233\n\t" \
    "v_permlane32_swap_b32 v230, v234\n\t" \
    "v_smfmac_f32_16x16x64_bf16 v[236:239], v[208:211], v[88:95], %[sidx]\n\t" \
    "v_permlane32_swap_b32 v231, v235\n\t" \
    "v_add_f32 v228, v228, v232\n\t" \
    "v_add_f32 v229, v229, v233\n\t" \
    "v_smfmac_f32_16x16x64_bf16 v[240:243], v[200:203], v[96:103], %[sidx]\n\t" \
    "v_add_f32 v230, v230, v234\n\t" \
    "v_add_f32 v231, v231, v235\n\t" \
    "v_cvt_pk_bf16_f32 v228, v228, v229\n\t" \
    "s_waitcnt lgkmcnt(0)\n\t" \
    "v_smfmac_f32_16x16x64_bf16 v[236:239], v[200:203], v[212:219], %[sidx]\n\t" \
    "ds_read_b128 v[212:215], %[wl] offset:6144\n\t" \
    "ds_read_b128 v[216:219], %[wl] offset:7168\n\t" \
    "v_cvt_pk_bf16_f32 v229, v230, v231\n\t" \
    "v_min_u32 v228, -2, v228\n\t" \
    "v_min_u32 v229, -2, v229\n\t" \
    "s_nop 1\n\t" \
    "v_permlane16_swap_b32 v226, v228\n\t" \
    "v_permlane16_swap_b32 v227, v229\n\t" \
    "buffer_store_dwordx4 v[226:229], %[vo], %[rr], %[so] offen sc1\n\t" \
    "s_add_u32 %[so], %[so], 0x10000\n\t" \
    "v_smfmac_f32_16x16x64_bf16 v[248:251], v[204:207], v[144:151], %[sidx]\n\t" \
    "v_mov_b64 v[220:221], 0\n\t" \
    "v_mov_b64 v[222:223], 0\n\t" \
    "v_smfmac_f32_16x16x64_bf16 v[244:247], v[204:207], v[120:127], %[sidx]\n\t" \
    "v_mov_b64 v[224:225], 0\n\t" \
    "v_mov_b64 v[226:227], 0\n\t" \
    "v_smfmac_f32_16x16x64_bf16 v[248:251], v[208:211], v[152:159], %[sidx]\n\t" \
    "v_permlane32_swap_b32 v236, v240\n\t" \
    "v_permlane32_swap_b32 v237, v241\n\t" \
    "v_permlane32_swap_b32 v238, v242\n\t" \
    "v_smfmac_f32_16x16x64_bf16 v[244:247], v[208:211], v[128:135], %[sidx]\n\t" \
    "v_permlane32_swap_b32 v239, v243\n\t" \
    "v_add_f32 v236, v236, v240\n\t" \
    "v_add_f32 v237, v237, v241\n\t" \
    "v_smfmac_f32_16x16x64_bf16 v[248:251], v[200:203], v[136:143], %[sidx]\n\t" \
    "v_add_f32 v238, v238, v242\n\t" \
    "v_add_f32 v239, v239, v243\n\t" \
    "v_cvt_pk_bf16_f32 v242, v236, v237\n\t" \
    "s_waitcnt lgkmcnt(0)\n\t" \
    "v_smfmac_f32_16x16x64_bf16 v[244:247], v[200:203], v[212:219], %[sidx]\n\t" \
    "ds_read_b128 v[212:215], %[wl] offset:8192\n\t" \
    "ds_read_b128 v[216:219], %[wl] offset:9216\n\t" \
    "v_cvt_pk_bf16_f32 v243, v238, v239\n\t" \
    "v_min_u32 v242, -2, v242\n\t" \
    "v_min_u32 v243, -2, v243\n\t" \
    "v_smfmac_f32_16x16x64_bf16 v[224:227], v[204:207], v[184:191], %[sidx]\n\t" \
    "v_and_b32 v232, 0x3fff, %[vo]\n\t" \
    "v_bfe_u32 v233, %[vo], 14, 1\n\t" \
    "v_smfmac_f32_16x16x64_bf16 v[220:223], v[204:207], v[160:167], %[sidx]\n\t" \
    "v_lshl_add_u32 v232, v233, 3, v232\n\t" \
    "v_permlane32_swap_b32 v244, v248\n\t" \
    "v_smfmac_f32_16x16x64_bf16 v[224:227], v[208:211], v[192:199], %[sidx]\n\t" \
    "v_permlane32_swap_b32 v245, v249\n\t" \
    "v_permlane32_swap_b32 v246, v250\n\t" \
    "v_permlane32_swap_b32 v247, v251\n\t" \
    "v_smfmac_f32_16x16x64_bf16 v[220:223], v[208:211], v[168:175], %[sidx]\n\t" \
    "v_add_f32 v244, v244, v248\n\t" \
    "v_add_f32 v245, v245, v249\n\t" \
    "v_add_f32 v246, v246, v250\n\t" \
    "v_smfmac_f32_16x16x64_bf16 v[224:227], v[200:203], v[176:183], %[sidx]\n\t" \
    "v_add_f32 v247, v247, v251\n\t" \
    "v_cvt_pk_bf16_f32 v244, v244, v245\n\t" \
    "v_cvt_pk_bf16_f32 v245, v246, v247\n\t" \
    "s_waitcnt lgkmcnt(0)\n\t" \
    "v_smfmac_f32_16x16x64_bf16 v[220:223], v[200:203], v[212:219], %[sidx]\n\t" \
    "v_min_u32 v244, -2, v244\n\t" \
    "v_min_u32 v245, -2, v245\n\t" \
    "s_nop 1\n\t" \
    "v_permlane16_swap_b32 v242, v244\n\t" \
    "v_permlane16_swap_b32 v243, v245\n\t" \
    "buffer_store_dwordx4 v[242:245], %[vo], %[rr], %[so] offen sc1\n\t" \
    "s_add_u32 %[so], %[so], 0x10000\n\t" \
    "s_nop 0\n\t" \
    "v_permlane32_swap_b32 v220, v224\n\t" \
    "v_permlane32_swap_b32 v221, v225\n\t" \
    "v_permlane32_swap_b32 v222, v226\n\t" \
    "v_permlane32_swap_b32 v223, v227\n\t" \
    "v_add_f32 v220, v220, v224\n\t" \
    "v_add_f32 v221, v221, v225\n\t" \
    "v_add_f32 v222, v222, v226\n\t" \
    "v_add_f32 v223, v223, v227\n\t" \
    "v_cvt_pk_bf16_f32 v220, v220, v221\n\t" \
    "v_cvt_pk_bf16_f32 v221, v222, v223\n\t" \
    "v_min_u32 v220, -2, v220\n\t" \
    "v_min_u32 v221, -2, v221\n\t" \
    "buffer_store_dwordx2 v[220:221], v232, %[rr], %[so] offen sc1\n\t" \
    "s_nop 1\n\t"
#define ASM_SM \
    "v_mbcnt_lo_u32_b32 v252, -1, 0\n\t" \
    "v_mbcnt_hi_u32_b32 v252, -1, v252\n\t" \
    "v_lshlrev_b32 v252, 4, v252\n\t" \
    "v_lshrrev_b32 v253, 1, v252\n\t" \
    "v_xor_b32 v253, v253, v252\n\t" \
    "v_and_b32 v253, 0x20, v253\n\t" \
    "v_mul_u32_u24 v253, 3, v253\n\t" \
    "v_xor_b32 v253, v253, v252\n\t" \
    "ds_read_b128 v[200:203], v253 offset:28672\n\t" \
    "ds_read_b128 v[204:207], v253 offset:29696\n\t" \
    "ds_read_b128 v[208:211], v253 offset:34816\n\t" \
    "ds_read_b128 v[212:215], v253 offset:35840\n\t" \
    "ds_read_b128 v[216:219], v253 offset:30720\n\t" \
    "ds_read_b128 v[220:223], v253 offset:31744\n\t" \
    "ds_read_b128 v[224:227], v253 offset:36864\n\t" \
    "ds_read_b128 v[228:231], v253 offset:37888\n\t" \
    "v_lshrrev_b32 v254, 1, v252\n\t" \
    "v_and_b32 v254, 0x180, v254\n\t" \
    "v_and_or_b32 v254, v252, %[c70], v254\n\t" \
    "v_bfe_i32 v255, v252, 7, 1\n\t" \
    "v_and_b32 v255, 0xaaaa, v255\n\t" \
    "v_or_b32 v255, 0x4444, v255\n\t" \
    "v_mov_b64 v[232:233], 0\n\t" \
    "v_mov_b64 v[234:235], 0\n\t" \
    "v_mov_b64 v[236:237], 0\n\t" \
    "v_mov_b64 v[238:239], 0\n\t" \
    "s_waitcnt lgkmcnt(0)\n\t" \
    "s_barrier\n\t" ASM_ST \
    "s_setprio 3\n\t" \
    "ds_read_b128 v[240:243], v252 offset:10240\n\t" \
    "ds_read_b128 v[244:247], v252 offset:11264\n\t" \
    "ds_read_b128 v[248:251], v252 offset:12288\n\t" \
    "s_waitcnt lgkmcnt(2)\n\t" \
    "v_smfmac_f32_16x16x64_bf16 v[232:235], v[240:243], v[200:207], v255\n\t" \
    "ds_read_b128 v[200:203], v253 offset:32768\n\t" \
    "ds_read_b128 v[204:207], v253 offset:33792\n\t" \
    "v_smfmac_f32_16x16x64_bf16 v[236:239], v[240:243], v[208:215], v255\n\t" \
    "ds_read_b128 v[208:211], v253 offset:38912\n\t" \
    "ds_read_b128 v[212:215], v253 offset:39936\n\t" \
    "s_waitcnt lgkmcnt(5)\n\t" \
    "v_smfmac_f32_16x16x64_bf16 v[232:235], v[244:247], v[216:223], v255\n\t" \
    "ds_read_b128 v[216:219], v254 offset:13312\n\t" \
    "ds_read_b128 v[220:223], v254 offset:13824\n\t" \
    "v_smfmac_f32_16x16x64_bf16 v[236:239], v[244:247], v[224:231], v255\n\t" \
    "ds_read_b128 v[224:227], v254 offset:14336\n\t" \
    "ds_read_b128 v[228:231], v254 offset:14848\n\t" \
    "s_waitcnt lgkmcnt(6)\n\t" \
    "v_smfmac_f32_16x16x64_bf16 v[232:235], v[248:251], v[200:207], v255\n\t" \
    "s_waitcnt lgkmcnt(4)\n\t" \
    "v_smfmac_f32_16x16x64_bf16 v[236:239], v[248:251], v[208:215], v255\n\t" \
    "ds_read_b128 v[208:211], v254 offset:15360\n\t" \
    "ds_read_b128 v[212:215], v254 offset:15872\n\t" \
    "v_mov_b64 v[204:205], 0\n\t" \
    "v_mov_b64 v[206:207], 0\n\t" \
    "s_nop 1\n\t" \
    "s_waitcnt lgkmcnt(4)\n\t" \
    "v_smfmac_f32_16x16x64_bf16 v[204:207], v[240:243], v[216:223], v255\n\t" \
    "s_waitcnt lgkmcnt(2)\n\t" \
    "v_smfmac_f32_16x16x64_bf16 v[204:207], v[244:247], v[224:231], v255\n\t" \
    "v_bfe_u32 v253, v252, 5, 1\n\t" \
    "v_bfe_i32 v202, v252, 4, 1\n\t" \
    "v_lshlrev_b32 v200, 1, v252\n\t" \
    "v_and_b32 v200, 0x180, v200\n\t" \
    "v_permlane32_swap_b32 v232, v234\n\t" \
    "v_permlane32_swap_b32 v233, v235\n\t" \
    "v_permlane32_swap_b32 v236, v238\n\t" \
    "v_permlane32_swap_b32 v237, v239\n\t" \
    "v_lshrrev_b32 v201, 2, v252\n\t" \
    "v_lshrrev_b32 v203, 4, v252\n\t" \
    "v_and_b32 v201, 0x40, v201\n\t" \
    "v_and_or_b32 v201, v203, 32, v201\n\t" \
    "v_and_or_b32 v201, v252, 16, v201\n\t" \
    "v_pk_add_f32 v[232:233], v[232:233], v[234:235]\n\t" \
    "v_pk_add_f32 v[234:235], v[236:237], v[238:239]\n\t" \
    "v_or_b32 v200, v200, v201\n\t" \
    "v_mad_u32_u24 v200, v253, %[cs], v200\n\t" \
    "v_mad_u32_u24 v201, v253, %[cs], v201\n\t" \
    "v_lshrrev_b32 v203, 3, v252\n\t" \
    "v_bfe_u32 v253, v252, 7, 1\n\t" \
    "s_waitcnt lgkmcnt(0)\n\t" \
    "v_smfmac_f32_16x16x64_bf16 v[204:207], v[248:251], v[208:215], v255\n\t" \
    "v_and_or_b32 v201, v203, 8, v201\n\t" \
    "v_and_b32 v202, 0x06060606, v202\n\t" \
    "v_mad_u32_u24 v201, v253, %[cp], v201\n\t" \
    "v_xor_b32 v202, 0x01000504, v202\n\t" \
    "v_max_f32 v236, v232, v234\n\t" \
    "v_max_f32 v237, v233, v235\n\t" \
    "s_nop 1\n\t" \
    "v_permlane32_swap_b32 v204, v206\n\t" \
    "v_permlane32_swap_b32 v205, v207\n\t" \
    "v_add_f32 v204, v204, v206\n\t" \
    "v_add_f32 v205, v205, v207\n\t" \
    "v_max_f32 v236, v236, v204\n\t" \
    "v_max_f32 v237, v237, v205\n\t" \
    "s_nop 0\n\t" \
    "v_max_f32_dpp v236, v236, v236 quad_perm:[1,0,3,2] row_mask:0xf bank_mask:0xf\n\t" \
    "v_max_f32_dpp v237, v237, v237 quad_perm:[1,0,3,2] row_mask:0xf bank_mask:0xf\n\t" \
    "s_nop 0\n\t" \
    "v_max_f32_dpp v236, v236, v236 quad_perm:[2,3,0,1] row_mask:0xf bank_mask:0xf\n\t" \
    "v_max_f32_dpp v237, v237, v237 quad_perm:[2,3,0,1] row_mask:0xf bank_mask:0xf\n\t" \
    "s_nop 0\n\t" \
    "v_max_f32_dpp v236, v236, v236 row_half_mirror row_mask:0xf bank_mask:0xf\n\t" \
    "v_max_f32_dpp v237, v237, v237 row_half_mirror row_mask:0xf bank_mask:0xf\n\t" \
    "s_nop 0\n\t" \
    "v_max_f32_dpp v236, v236, v236 row_mirror row_mask:0xf bank_mask:0xf\n\t" \
    "v_max_f32_dpp v237, v237, v237 row_mirror row_mask:0xf bank_mask:0xf\n\t" \
    "v_mul_f32 v236, %[scl], v236\n\t" \
    "v_mul_f32 v237, %[scl], v237\n\t" \
    "v_fma_f32 v232, v232, %[scl], -v236\n\t" \
    "v_fma_f32 v233, v233, %[scl], -v237\n\t" \
    "v_fma_f32 v234, v234, %[scl], -v236\n\t" \
    "v_fma_f32 v235, v235, %[scl], -v237\n\t" \
    "v_exp_f32 v232, v232\n\t" \
    "v_exp_f32 v233, v233\n\t" \
    "v_fma_f32 v204, v204, %[scl], -v236\n\t" \
    "v_fma_f32 v205, v205, %[scl], -v237\n\t" \
    "v_exp_f32 v234, v234\n\t" \
    "v_exp_f32 v235, v235\n\t" \
    "v_exp_f32 v204, v204\n\t" \
    "v_exp_f32 v205, v205\n\t" \
    "v_add_f32 v238, v232, v234\n\t" \
    "v_add_f32 v239, v233, v235\n\t" \
    "v_fma_f32 v238, v204, 0.5, v238\n\t" \
    "v_fma_f32 v239, v205, 0.5, v239\n\t" \
    "s_nop 0\n\t" \
    "v_add_f32_dpp v238, v238, v238 quad_perm:[1,0,3,2] row_mask:0xf bank_mask:0xf\n\t" \
    "v_add_f32_dpp v239, v239, v239 quad_perm:[1,0,3,2] row_mask:0xf bank_mask:0xf\n\t" \
    "s_nop 0\n\t" \
    "v_add_f32_dpp v238, v238, v238 quad_perm:[2,3,0,1] row_mask:0xf bank_mask:0xf\n\t" \
    "v_add_f32_dpp v239, v239, v239 quad_perm:[2,3,0,1] row_mask:0xf bank_mask:0xf\n\t" \
    "s_nop 0\n\t" \
    "v_add_f32_dpp v238, v238, v238 row_half_mirror row_mask:0xf bank_mask:0xf\n\t" \
    "v_add_f32_dpp v239, v239, v239 row_half_mirror row_mask:0xf bank_mask:0xf\n\t" \
    "s_nop 0\n\t" \
    "v_add_f32_dpp v238, v238, v238 row_mirror row_mask:0xf bank_mask:0xf\n\t" \
    "v_add_f32_dpp v239, v239, v239 row_mirror row_mask:0xf bank_mask:0xf\n\t" \
    "v_rcp_f32 v238, v238\n\t" \
    "v_rcp_f32 v239, v239\n\t" \
    "s_nop 0\n\t" \
    "v_pk_mul_f32 v[232:233], v[232:233], v[238:239]\n\t" \
    "v_pk_mul_f32 v[234:235], v[234:235], v[238:239]\n\t" \
    "v_pk_mul_f32 v[204:205], v[204:205], v[238:239]\n\t" \
    "v_cvt_pk_bf16_f32 v232, |v232|, |v233|\n\t" \
    "v_cvt_pk_bf16_f32 v234, |v234|, |v235|\n\t" \
    "v_cvt_pk_bf16_f32 v204, |v204|, |v205|\n\t" \
    "v_mov_b32_dpp v233, v232 quad_perm:[1,0,3,2] row_mask:0xf bank_mask:0xf\n\t" \
    "v_mov_b32_dpp v235, v234 quad_perm:[1,0,3,2] row_mask:0xf bank_mask:0xf\n\t" \
    "v_mov_b32_dpp v205, v204 quad_perm:[1,0,3,2] row_mask:0xf bank_mask:0xf\n\t" \
    "v_perm_b32 v232, v232, v233, v202\n\t" \
    "v_perm_b32 v234, v234, v235, v202\n\t" \
    "v_perm_b32 v204, v204, v205, v202\n\t" \
    "v_mov_b32_dpp v240, v232 quad_perm:[0,1,0,1] row_mask:0xf bank_mask:0xf\n\t" \
    "v_mov_b32_dpp v241, v234 quad_perm:[0,1,0,1] row_mask:0xf bank_mask:0xf\n\t" \
    "v_mov_b32_dpp v242, v232 quad_perm:[2,3,2,3] row_mask:0xf bank_mask:0xf\n\t" \
    "v_mov_b32_dpp v243, v234 quad_perm:[2,3,2,3] row_mask:0xf bank_mask:0xf\n\t" \
    "buffer_store_dwordx4 v[240:243], v200, %[rr], %[sx] offen sc1\n\t" \
    "v_mov_b32_dpp v244, v204 quad_perm:[0,1,0,1] row_mask:0xf bank_mask:0xf\n\t" \
    "v_mov_b32_dpp v245, v204 quad_perm:[2,3,2,3] row_mask:0xf bank_mask:0xf\n\t" \
    "buffer_store_dwordx4 v[240:243], v200, %[rr], %[sy] offen sc1\n\t" \
    "buffer_store_dwordx2 v[244:245], v201, %[rr], %[sm] offen sc1\n\t" \
    "s_wakeup\n\t" \
    "s_setprio 0\n\t"
#define ASM_P4_0 \
    "ds_read_b128 v[200:203], %[qa] offset:0\n\t" \
    "ds_read_b128 v[204:207], %[qa] offset:1024\n\t" \
    "ds_read_b128 v[208:211], %[qa] offset:2048\n\t" \
    "ds_read_b128 v[212:215], %[qa] offset:3072\n\t" \
    "ds_read_b128 v[216:219], %[qa] offset:4096\n\t" \
    "ds_read_b128 v[220:223], %[qa] offset:5120\n\t" \
    "ds_read_b128 v[224:227], %[qa] offset:12288\n\t" \
    "ds_read_b128 v[228:231], %[qa] offset:13312\n\t" \
    "ds_read_b128 v[232:235], %[qa] offset:14336\n\t" \
    "ds_read_b128 v[236:239], %[qa] offset:15360\n\t" \
    "s_waitcnt lgkmcnt(0)\n\t" \
    "s_barrier\n\t" ASM_ST \
    "v_mfma_f32_16x16x32_bf16 v[240:243], v[224:227], v[200:203], v[240:243]\n\t" \
    "ds_read_b128 v[224:227], %[qa] offset:16384\n\t" \
    "v_mfma_f32_16x16x32_bf16 v[240:243], v[228:231], v[204:207], v[240:243]\n\t" \
    "ds_read_b128 v[228:231], %[qa] offset:17408\n\t" \
    "v_mfma_f32_16x16x32_bf16 v[240:243], v[232:235], v[208:211], v[240:243]\n\t" \
    "ds_read_b128 v[232:235], %[km] offset:0\n\t" \
    "v_mfma_f32_16x16x32_bf16 v[240:243], v[236:239], v[212:215], v[240:243]\n\t" \
    "ds_read_b128 v[236:239], %[km] offset:512\n\t" \
    "s_waitcnt lgkmcnt(3)\n\t" \
    "v_mfma_f32_16x16x32_bf16 v[240:243], v[224:227], v[216:219], v[240:243]\n\t" \
    "ds_read_b128 v[224:227], %[km] offset:1024\n\t" \
    "s_waitcnt lgkmcnt(3)\n\t" \
    "v_mfma_f32_16x16x32_bf16 v[240:243], v[228:231], v[220:223], v[240:243]\n\t" \
    "ds_read_b128 v[228:231], %[km] offset:1536\n\t" \
    "s_waitcnt lgkmcnt(3)\n\t" \
    "v_mfma_f32_16x16x32_bf16 v[248:251], v[232:235], v[200:203], 0\n\t" \
    "ds_read_b128 v[232:235], %[km] offset:2048\n\t" \
    "s_waitcnt lgkmcnt(3)\n\t" \
    "v_mfma_f32_16x16x32_bf16 v[248:251], v[236:239], v[204:207], v[248:251]\n\t" \
    "ds_read_b128 v[236:239], %[km] offset:2560\n\t" \
    "s_waitcnt lgkmcnt(3)\n\t" \
    "v_mfma_f32_16x16x32_bf16 v[248:251], v[224:227], v[208:211], v[248:251]\n\t" \
    "s_waitcnt lgkmcnt(2)\n\t" \
    "v_mfma_f32_16x16x32_bf16 v[248:251], v[228:231], v[212:215], v[248:251]\n\t" \
    "s_waitcnt lgkmcnt(1)\n\t" \
    "v_mfma_f32_16x16x32_bf16 v[248:251], v[232:235], v[216:219], v[248:251]\n\t" \
    "s_waitcnt lgkmcnt(0)\n\t" \
    "v_mfma_f32_16x16x32_bf16 v[248:251], v[236:239], v[220:223], v[248:251]\n\t" \
    "v_max3_f32 v224, v240, v241, v242\n\t" \
    "v_mov_b32 v225, v243\n\t" \
    "v_lshlrev_b32 v201, 23, %[qa]\n\t" \
    "v_and_b32 v201, 0x80000000, v201\n\t" \
    "v_and_b32 v200, 0x3ff, %[qa]\n\t" \
    "v_or_b32 v200, v201, v200\n\t" \
    "s_nop 1\n\t" \
    "v_max3_f32 v224, v224, v248, v249\n\t" \
    "v_max3_f32 v225, v225, v250, v251\n\t" \
    "v_max_f32 v224, v224, v225\n\t" \
    "v_mov_b32 v225, v224\n\t" \
    "s_nop 1\n\t" \
    "v_permlane16_swap_b32 v224, v225\n\t" \
    "v_max_f32 v224, v224, v225\n\t" \
    "v_mov_b32 v225, v224\n\t" \
    "s_nop 1\n\t" \
    "v_permlane32_swap_b32 v224, v225\n\t" \
    "v_max_f32 v224, v224, v225\n\t" \
    "v_mul_f32 v224, %[scl], v224\n\t" \
    "v_fma_f32 v240, v240, %[scl], -v224\n\t" \
    "v_fma_f32 v241, v241, %[scl], -v224\n\t" \
    "v_exp_f32 v240, v240\n\t" \
    "v_fma_f32 v242, v242, %[scl], -v224\n\t" \
    "v_exp_f32 v241, v241\n\t" \
    "v_fma_f32 v243, v243, %[scl], -v224\n\t" \
    "v_exp_f32 v242, v242\n\t" \
    "v_fma_f32 v248, v248, %[scl], -v224\n\t" \
    "v_exp_f32 v243, v243\n\t" \
    "v_fma_f32 v249, v249, %[scl], -v224\n\t" \
    "v_exp_f32 v248, v248\n\t" \
    "v_fma_f32 v250, v250, %[scl], -v224\n\t" \
    "v_exp_f32 v249, v249\n\t" \
    "v_fma_f32 v251, v251, %[scl], -v224\n\t" \
    "v_exp_f32 v250, v250\n\t" \
    "v_exp_f32 v251, v251\n\t" \
    "v_add_f32 v224, v240, v241\n\t" \
    "v_add_f32 v225, v242, v243\n\t" \
    "v_add_f32 v232, v248, v249\n\t" \
    "v_add_f32 v233, v250, v251\n\t" \
    "v_add_f32 v224, v224, v225\n\t" \
    "v_add_f32 v232, v232, v233\n\t" \
    "v_fma_f32 v224, v232, 0.5, v224\n\t" \
    "v_mov_b32 v225, v224\n\t" \
    "s_nop 1\n\t" \
    "v_permlane16_swap_b32 v224, v225\n\t" \
    "v_add_f32 v224, v224, v225\n\t" \
    "v_mov_b32 v225, v224\n\t" \
    "s_nop 1\n\t" \
    "v_permlane32_swap_b32 v224, v225\n\t" \
    "v_add_f32 v224, v224, v225\n\t" \
    "v_rcp_f32 v224, v224\n\t" \
    "s_nop 0\n\t" \
    "v_mul_f32 v240, v240, v224\n\t" \
    "v_mul_f32 v241, v241, v224\n\t" \
    "v_mul_f32 v242, v242, v224\n\t" \
    "v_mul_f32 v243, v243, v224\n\t" \
    "v_mul_f32 v248, v248, v224\n\t" \
    "v_mul_f32 v249, v249, v224\n\t" \
    "v_mul_f32 v250, v250, v224\n\t" \
    "v_mul_f32 v251, v251, v224\n\t" \
    "v_cvt_pk_bf16_f32 v228, |v240|, |v241|\n\t" \
    "v_cvt_pk_bf16_f32 v229, |v242|, |v243|\n\t" \
    "v_cvt_pk_bf16_f32 v232, |v248|, |v249|\n\t" \
    "v_cvt_pk_bf16_f32 v233, |v250|, |v251|\n\t" \
    "v_permlane16_swap_b32 v228, v230\n\t" \
    "v_permlane16_swap_b32 v229, v231\n\t" \
    "buffer_store_dwordx4 v[228:231], v200, %[rr], %[so] offen sc1\n\t" \
    "buffer_store_dwordx2 v[232:233], %[pm], %[rr], %[so] offen sc1\n\t" \
    "s_nop 1\n\t"
#define ASM_P4_1 \
    "ds_read_b128 v[200:203], %[qa] offset:0\n\t" \
    "ds_read_b128 v[204:207], %[qa] offset:1024\n\t" \
    "ds_read_b128 v[208:211], %[qa] offset:2048\n\t" \
    "ds_read_b128 v[212:215], %[qa] offset:3072\n\t" \
    "ds_read_b128 v[216:219], %[qa] offset:4096\n\t" \
    "ds_read_b128 v[220:223], %[qa] offset:5120\n\t" \
    "ds_read_b128 v[224:227], %[qa] offset:12288\n\t" \
    "ds_read_b128 v[228:231], %[qa] offset:13312\n\t" \
    "ds_read_b128 v[232:235], %[qa] offset:14336\n\t" \
    "ds_read_b128 v[236:239], %[qa] offset:15360\n\t" \
    "s_waitcnt lgkmcnt(0)\n\t" \
    "s_barrier\n\t" ASM_ST \
    "v_mfma_f32_16x16x32_bf16 v[240:243], v[224:227], v[200:203], v[240:243]\n\t" \
    "ds_read_b128 v[224:227], %[qa] offset:16384\n\t" \
    "v_mfma_f32_16x16x32_bf16 v[240:243], v[228:231], v[204:207], v[240:243]\n\t" \
    "ds_read_b128 v[228:231], %[qa] offset:17408\n\t" \
    "v_mfma_f32_16x16x32_bf16 v[240:243], v[232:235], v[208:211], v[240:243]\n\t" \
    "ds_read_b128 v[232:235], %[qa] offset:6144\n\t" \
    "v_mfma_f32_16x16x32_bf16 v[240:243], v[236:239], v[212:215], v[240:243]\n\t" \
    "ds_read_b128 v[236:239], %[qa] offset:7168\n\t" \
    "s_waitcnt lgkmcnt(3)\n\t" \
    "v_mfma_f32_16x16x32_bf16 v[240:243], v[224:227], v[216:219], v[240:243]\n\t" \
    "ds_read_b128 v[224:227], %[qa] offset:8192\n\t" \
    "s_waitcnt lgkmcnt(3)\n\t" \
    "v_mfma_f32_16x16x32_bf16 v[240:243], v[228:231], v[220:223], v[240:243]\n\t" \
    "ds_read_b128 v[228:231], %[qa] offset:9216\n\t" \
    "s_waitcnt lgkmcnt(3)\n\t" \
    "v_mfma_f32_16x16x32_bf16 v[244:247], v[232:235], v[200:203], 0\n\t" \
    "ds_read_b128 v[232:235], %[qa] offset:10240\n\t" \
    "s_waitcnt lgkmcnt(3)\n\t" \
    "v_mfma_f32_16x16x32_bf16 v[244:247], v[236:239], v[204:207], v[244:247]\n\t" \
    "ds_read_b128 v[236:239], %[qa] offset:11264\n\t" \
    "s_waitcnt lgkmcnt(3)\n\t" \
    "v_mfma_f32_16x16x32_bf16 v[244:247], v[224:227], v[208:211], v[244:247]\n\t" \
    "ds_read_b128 v[224:227], %[km] offset:0\n\t" \
    "s_waitcnt lgkmcnt(3)\n\t" \
    "v_mfma_f32_16x16x32_bf16 v[244:247], v[228:231], v[212:215], v[244:247]\n\t" \
    "ds_read_b128 v[228:231], %[km] offset:512\n\t" \
    "s_waitcnt lgkmcnt(3)\n\t" \
    "v_mfma_f32_16x16x32_bf16 v[244:247], v[232:235], v[216:219], v[244:247]\n\t" \
    "ds_read_b128 v[232:235], %[km] offset:1024\n\t" \
    "s_waitcnt lgkmcnt(3)\n\t" \
    "v_mfma_f32_16x16x32_bf16 v[244:247], v[236:239], v[220:223], v[244:247]\n\t" \
    "ds_read_b128 v[236:239], %[km] offset:1536\n\t" \
    "s_waitcnt lgkmcnt(3)\n\t" \
    "v_mfma_f32_16x16x32_bf16 v[248:251], v[224:227], v[200:203], 0\n\t" \
    "ds_read_b128 v[224:227], %[km] offset:2048\n\t" \
    "s_waitcnt lgkmcnt(3)\n\t" \
    "v_mfma_f32_16x16x32_bf16 v[248:251], v[228:231], v[204:207], v[248:251]\n\t" \
    "ds_read_b128 v[228:231], %[km] offset:2560\n\t" \
    "s_waitcnt lgkmcnt(3)\n\t" \
    "v_mfma_f32_16x16x32_bf16 v[248:251], v[232:235], v[208:211], v[248:251]\n\t" \
    "s_waitcnt lgkmcnt(2)\n\t" \
    "v_mfma_f32_16x16x32_bf16 v[248:251], v[236:239], v[212:215], v[248:251]\n\t" \
    "s_waitcnt lgkmcnt(1)\n\t" \
    "v_mfma_f32_16x16x32_bf16 v[248:251], v[224:227], v[216:219], v[248:251]\n\t" \
    "s_waitcnt lgkmcnt(0)\n\t" \
    "v_mfma_f32_16x16x32_bf16 v[248:251], v[228:231], v[220:223], v[248:251]\n\t" \
    "v_max3_f32 v224, v240, v241, v242\n\t" \
    "v_max3_f32 v225, v244, v245, v246\n\t" \
    "v_max3_f32 v224, v224, v243, v247\n\t" \
    "v_and_b32 v200, 0x3ff, %[qa]\n\t" \
    "s_nop 3\n\t" \
    "v_max3_f32 v224, v224, v248, v249\n\t" \
    "v_max3_f32 v225, v225, v250, v251\n\t" \
    "v_max_f32 v224, v224, v225\n\t" \
    "v_mov_b32 v225, v224\n\t" \
    "s_nop 1\n\t" \
    "v_permlane16_swap_b32 v224, v225\n\t" \
    "v_max_f32 v224, v224, v225\n\t" \
    "v_mov_b32 v225, v224\n\t" \
    "s_nop 1\n\t" \
    "v_permlane32_swap_b32 v224, v225\n\t" \
    "v_max_f32 v224, v224, v225\n\t" \
    "v_mul_f32 v224, %[scl], v224\n\t" \
    "v_fma_f32 v240, v240, %[scl], -v224\n\t" \
    "v_fma_f32 v241, v241, %[scl], -v224\n\t" \
    "v_exp_f32 v240, v240\n\t" \
    "v_fma_f32 v242, v242, %[scl], -v224\n\t" \
    "v_exp_f32 v241, v241\n\t" \
    "v_fma_f32 v243, v243, %[scl], -v224\n\t" \
    "v_exp_f32 v242, v242\n\t" \
    "v_fma_f32 v244, v244, %[scl], -v224\n\t" \
    "v_exp_f32 v243, v243\n\t" \
    "v_fma_f32 v245, v245, %[scl], -v224\n\t" \
    "v_exp_f32 v244, v244\n\t" \
    "v_fma_f32 v246, v246, %[scl], -v224\n\t" \
    "v_exp_f32 v245, v245\n\t" \
    "v_fma_f32 v247, v247, %[scl], -v224\n\t" \
    "v_exp_f32 v246, v246\n\t" \
    "v_fma_f32 v248, v248, %[scl], -v224\n\t" \
    "v_exp_f32 v247, v247\n\t" \
    "v_fma_f32 v249, v249, %[scl], -v224\n\t" \
    "v_exp_f32 v248, v248\n\t" \
    "v_fma_f32 v250, v250, %[scl], -v224\n\t" \
    "v_exp_f32 v249, v249\n\t" \
    "v_fma_f32 v251, v251, %[scl], -v224\n\t" \
    "v_exp_f32 v250, v250\n\t" \
    "v_exp_f32 v251, v251\n\t" \
    "v_add_f32 v224, v240, v241\n\t" \
    "v_add_f32 v225, v242, v243\n\t" \
    "v_add_f32 v226, v244, v245\n\t" \
    "v_add_f32 v227, v246, v247\n\t" \
    "v_add_f32 v232, v248, v249\n\t" \
    "v_add_f32 v233, v250, v251\n\t" \
    "v_add_f32 v224, v224, v225\n\t" \
    "v_add_f32 v226, v226, v227\n\t" \
    "v_add_f32 v232, v232, v233\n\t" \
    "v_add_f32 v224, v224, v226\n\t" \
    "v_fma_f32 v224, v232, 0.5, v224\n\t" \
    "v_mov_b32 v225, v224\n\t" \
    "s_nop 1\n\t" \
    "v_permlane16_swap_b32 v224, v225\n\t" \
    "v_add_f32 v224, v224, v225\n\t" \
    "v_mov_b32 v225, v224\n\t" \
    "s_nop 1\n\t" \
    "v_permlane32_swap_b32 v224, v225\n\t" \
    "v_add_f32 v224, v224, v225\n\t" \
    "v_rcp_f32 v224, v224\n\t" \
    "s_nop 0\n\t" \
    "v_mul_f32 v240, v240, v224\n\t" \
    "v_mul_f32 v241, v241, v224\n\t" \
    "v_mul_f32 v242, v242, v224\n\t" \
    "v_mul_f32 v243, v243, v224\n\t" \
    "v_mul_f32 v244, v244, v224\n\t" \
    "v_mul_f32 v245, v245, v224\n\t" \
    "v_mul_f32 v246, v246, v224\n\t" \
    "v_mul_f32 v247, v247, v224\n\t" \
    "v_mul_f32 v248, v248, v224\n\t" \
    "v_mul_f32 v249, v249, v224\n\t" \
    "v_mul_f32 v250, v250, v224\n\t" \
    "v_mul_f32 v251, v251, v224\n\t" \
    "v_cvt_pk_bf16_f32 v230, |v240|, |v241|\n\t" \
    "v_cvt_pk_bf16_f32 v231, |v242|, |v243|\n\t" \
    "v_cvt_pk_bf16_f32 v228, |v244|, |v245|\n\t" \
    "v_cvt_pk_bf16_f32 v229, |v246|, |v247|\n\t" \
    "v_cvt_pk_bf16_f32 v232, |v248|, |v249|\n\t" \
    "v_cvt_pk_bf16_f32 v233, |v250|, |v251|\n\t" \
    "buffer_store_dwordx4 v[228:231], v200, %[rr], %[so] offen sc1\n\t" \
    "buffer_store_dwordx2 v[232:233], %[pm], %[rr], %[so] offen sc1\n\t" \
    "s_nop 1\n\t"
#define ASM_RCRIT \
    "v_mov_b64 v[176:177], -1\n\t" \
    "v_mov_b64 v[178:179], -1\n\t" \
    "v_mov_b64 v[180:181], -1\n\t" \
    "v_mov_b64 v[182:183], -1\n\t" \
    "v_mov_b64 v[184:185], -1\n\t" \
    "v_mov_b64 v[186:187], -1\n\t" \
    "v_mov_b64 v[188:189], -1\n\t" \
    "v_mov_b64 v[190:191], -1\n\t" \
    "v_mov_b64 v[192:193], -1\n\t" \
    "v_mov_b64 v[194:195], -1\n\t" \
    ".Lfa%=:\n\t" \
    "s_mov_b32 %[a], 0\n\t" \
    "v_max_u32 v196, v176, v177\n\t" \
    "v_max3_u32 v196, v178, v179, v196\n\t" \
    "v_cmp_eq_u32 vcc, -1, v196\n\t" \
    "s_cbranch_vccz .Lga0_%=\n\t" \
    "buffer_load_dwordx4 v[176:179], %[ov], %[rs], %[sv] offen sc1\n\t" \
    "s_mov_b32 %[a], 1\n\t" \
    ".Lga0_%=:\n\t" \
    "v_max_u32 v196, v180, v181\n\t" \
    "v_max3_u32 v196, v182, v183, v196\n\t" \
    "v_cmp_eq_u32 vcc, -1, v196\n\t" \
    "s_cbranch_vccz .Lga1_%=\n\t" \
    "buffer_load_dwordx4 v[180:183], %[ov], %[rs], %[sv] offen offset:1024 sc1\n\t" \
    "s_mov_b32 %[a], 1\n\t" \
    ".Lga1_%=:\n\t" \
    "s_cmp_eq_u32 %[a], 0\n\t" \
    "s_cbranch_scc1 .Lha%=\n\t" \
    "s_waitcnt vmcnt(12)\n\t" \
    ".Lia%=:\n\t" \
    "v_max_u32 v196, v176, v177\n\t" \
    "v_max3_u32 v196, v178, v179, v196\n\t" \
    "v_max3_u32 v196, v180, v181, v196\n\t" \
    "v_max3_u32 v196, v182, v183, v196\n\t" \
    "v_cmp_eq_u32 vcc, -1, v196\n\t" \
    "s_cbranch_vccz .Lha%=\n\t" \
    "s_nop 7\n\t" \
    "v_max_u32 v196, v176, v177\n\t" \
    "v_max3_u32 v196, v178, v179, v196\n\t" \
    "v_max3_u32 v196, v180, v181, v196\n\t" \
    "v_max3_u32 v196, v182, v183, v196\n\t" \
    "v_cmp_eq_u32 vcc, -1, v196\n\t" \
    "s_cbranch_vccz .Lha%=\n\t" \
    "s_nop 7\n\t" \
    "v_max_u32 v196, v176, v177\n\t" \
    "v_max3_u32 v196, v178, v179, v196\n\t" \
    "v_max3_u32 v196, v180, v181, v196\n\t" \
    "v_max3_u32 v196, v182, v183, v196\n\t" \
    "v_cmp_eq_u32 vcc, -1, v196\n\t" \
    "s_cbranch_vccz .Lha%=\n\t" \
    "s_nop 7\n\t" \
    "v_max_u32 v196, v176, v177\n\t" \
    "v_max3_u32 v196, v178, v179, v196\n\t" \
    "v_max3_u32 v196, v180, v181, v196\n\t" \
    "v_max3_u32 v196, v182, v183, v196\n\t" \
    "v_cmp_eq_u32 vcc, -1, v196\n\t" \
    "s_cbranch_vccz .Lha%=\n\t" \
    "s_nop 7\n\t" \
    "s_sub_u32 %[n], %[n], 1\n\t" \
    "s_cmp_gt_i32 %[n], 0\n\t" \
    "s_cbranch_scc1 .Lfa%=\n\t" \
    ".Lha%=:\n\t" \
    ASM_ST3 \
    "buffer_load_dwordx4 v[184:187], %[o0], %[rs], %[sp] offen sc1\n\t" \
    "buffer_load_dwordx4 v[188:191], %[o0], %[rs], %[sp] offen offset:1024 sc1\n\t" \
    "buffer_load_dwordx4 v[192:195], %[o2], %[rs], %[sp] offen sc1\n\t" \
    "s_waitcnt vmcnt(0)\n\t" \
    "v_max_u32 v196, v184, v185\n\t" \
    "v_max3_u32 v196, v186, v187, v196\n\t" \
    "v_max3_u32 v196, v188, v189, v196\n\t" \
    "v_max3_u32 v196, v190, v191, v196\n\t" \
    "v_max3_u32 v196, v192, v193, v196\n\t" \
    "v_max3_u32 v196, v194, v195, v196\n\t" \
    "v_cmp_eq_u32 vcc, -1, v196\n\t" \
    "s_cbranch_vccz .Lhb%=\n\t" \
    ".Lfb%=:\n\t" \
    "s_mov_b32 %[a], 0\n\t" \
    "v_max_u32 v196, v184, v185\n\t" \
    "v_max3_u32 v196, v186, v187, v196\n\t" \
    "v_cmp_eq_u32 vcc, -1, v196\n\t" \
    "s_cbranch_vccz .Lgb0_%=\n\t" \
    "buffer_load_dwordx4 v[184:187], %[o0], %[rs], %[sp] offen sc1\n\t" \
    "s_mov_b32 %[a], 1\n\t" \
    ".Lgb0_%=:\n\t" \
    "v_max_u32 v196, v188, v189\n\t" \
    "v_max3_u32 v196, v190, v191, v196\n\t" \
    "v_cmp_eq_u32 vcc, -1, v196\n\t" \
    "s_cbranch_vccz .Lgb1_%=\n\t" \
    "buffer_load_dwordx4 v[188:191], %[o0], %[rs], %[sp] offen offset:1024 sc1\n\t" \
    "s_mov_b32 %[a], 1\n\t" \
    ".Lgb1_%=:\n\t" \
    "v_max_u32 v196, v192, v193\n\t" \
    "v_max3_u32 v196, v194, v195, v196\n\t" \
    "v_cmp_eq_u32 vcc, -1, v196\n\t" \
    "s_cbranch_vccz .Lgb2_%=\n\t" \
    "buffer_load_dwordx4 v[192:195], %[o2], %[rs], %[sp] offen sc1\n\t" \
    "s_mov_b32 %[a], 1\n\t" \
    ".Lgb2_%=:\n\t" \
    "s_cmp_eq_u32 %[a], 0\n\t" \
    "s_cbranch_scc1 .Lhb%=\n\t" \
    "s_waitcnt vmcnt(12)\n\t" \
    ".Lib%=:\n\t" \
    "v_max_u32 v196, v184, v185\n\t" \
    "v_max3_u32 v196, v186, v187, v196\n\t" \
    "v_max3_u32 v196, v188, v189, v196\n\t" \
    "v_max3_u32 v196, v190, v191, v196\n\t" \
    "v_max3_u32 v196, v192, v193, v196\n\t" \
    "v_max3_u32 v196, v194, v195, v196\n\t" \
    "v_cmp_eq_u32 vcc, -1, v196\n\t" \
    "s_cbranch_vccz .Lhb%=\n\t" \
    "s_nop 7\n\t" \
    "v_max_u32 v196, v184, v185\n\t" \
    "v_max3_u32 v196, v186, v187, v196\n\t" \
    "v_max3_u32 v196, v188, v189, v196\n\t" \
    "v_max3_u32 v196, v190, v191, v196\n\t" \
    "v_max3_u32 v196, v192, v193, v196\n\t" \
    "v_max3_u32 v196, v194, v195, v196\n\t" \
    "v_cmp_eq_u32 vcc, -1, v196\n\t" \
    "s_cbranch_vccz .Lhb%=\n\t" \
    "s_nop 7\n\t" \
    "v_max_u32 v196, v184, v185\n\t" \
    "v_max3_u32 v196, v186, v187, v196\n\t" \
    "v_max3_u32 v196, v188, v189, v196\n\t" \
    "v_max3_u32 v196, v190, v191, v196\n\t" \
    "v_max3_u32 v196, v192, v193, v196\n\t" \
    "v_max3_u32 v196, v194, v195, v196\n\t" \
    "v_cmp_eq_u32 vcc, -1, v196\n\t" \
    "s_cbranch_vccz .Lhb%=\n\t" \
    "s_nop 7\n\t" \
    "v_max_u32 v196, v184, v185\n\t" \
    "v_max3_u32 v196, v186, v187, v196\n\t" \
    "v_max3_u32 v196, v188, v189, v196\n\t" \
    "v_max3_u32 v196, v190, v191, v196\n\t" \
    "v_max3_u32 v196, v192, v193, v196\n\t" \
    "v_max3_u32 v196, v194, v195, v196\n\t" \
    "v_cmp_eq_u32 vcc, -1, v196\n\t" \
    "s_cbranch_vccz .Lhb%=\n\t" \
    "s_nop 7\n\t" \
    "s_sub_u32 %[n], %[n], 1\n\t" \
    "s_cmp_gt_i32 %[n], 0\n\t" \
    "s_cbranch_scc1 .Lfb%=\n\t" \
    ".Lhb%=:\n\t" \
    ASM_ST2 \
    "v_mfma_f32_16x16x32_bf16 v[160:163], v[192:195], v[176:179], 0\n\t" \
    "v_mfma_f32_16x16x32_bf16 v[164:167], v[192:195], v[180:183], 0\n\t" \
    "v_smfmac_f32_16x16x64_bf16 v[160:163], v[184:187], v[128:135], %[sidx]\n\t" \
    "v_smfmac_f32_16x16x64_bf16 v[164:167], v[184:187], v[144:151], %[sidx]\n\t" \
    "v_smfmac_f32_16x16x64_bf16 v[160:163], v[188:191], v[136:143], %[sidx]\n\t" \
    "v_smfmac_f32_16x16x64_bf16 v[164:167], v[188:191], v[152:159], %[sidx]\n\t" \
    "s_cmp_eq_u32 %[pub], 0\n\t" \
    "s_nop 7\n\t" \
    "v_permlane32_swap_b32 v160, v164\n\t" \
    "v_permlane32_swap_b32 v161, v165\n\t" \
    "v_permlane32_swap_b32 v162, v166\n\t" \
    "v_permlane32_swap_b32 v163, v167\n\t" \
    "v_add_f32 v160, v160, v164\n\t" \
    "v_add_f32 v161, v161, v165\n\t" \
    "ds_write2_b32 %[rw], v160, v161 offset1:80\n\t" \
    "v_add_f32 v162, v162, v166\n\t" \
    "v_add_f32 v163, v163, v167\n\t" \
    "ds_write2_b32 %[rw], v162, v163 offset0:160 offset1:240\n\t" \
    "s_waitcnt lgkmcnt(0)\n\t" \
    "s_barrier\n\t" ASM_ST \
    "s_cbranch_scc1 .Lre%=\n\t" \
    "ds_read_b128 v[200:203], %[rd] offset:0\n\t" \
    "ds_read_b128 v[204:207], %[rd] offset:16\n\t" \
    "s_waitcnt lgkmcnt(1)\n\t" \
    "v_pk_add_f32 v[200:201], v[200:201], v[202:203]\n\t" \
    "s_waitcnt lgkmcnt(0)\n\t" \
    "v_pk_add_f32 v[204:205], v[204:205], v[206:207]\n\t" \
    "v_pk_add_f32 v[200:201], v[200:201], v[204:205]\n\t" \
    "s_nop 1\n\t" \
    "v_permlane32_swap_b32 v200, v201\n\t" \
    "v_add_f32 v202, v200, v201\n\t" \
    "v_add_f32 v203, v200, v201\n\t" \
    "s_nop 1\n\t" \
    "v_permlane32_swap_b32 v202, v203\n\t" \
    "v_cvt_pk_bf16_f32 v200, v202, v203\n\t" \
    "v_min_u32 v200, -2, v200\n\t" \
    "buffer_store_dword v200, %[oo], %[rs], %[so] offen sc1\n\t" \
    ".Lre%=:\n\t"
#define ASM_RX \
    "s_cmp_eq_u32 %[fin], 0\n\t" \
    "s_cbranch_scc1 .Lx0%=\n\t" \
    "buffer_load_dwordx2 v[250:251], %[xo], %[rx], %[sx] offen sc1\n\t" \
    ".Lx0%=:\n\t" \
    "buffer_load_dword v248, %[fo], %[rs], %[fs] offen sc1\n\t" \
    "buffer_load_dwordx4 v[200:203], %[l16], %[rs], %[s0] offen offset:0 sc1\n\t" \
    "buffer_load_dwordx4 v[204:207], %[l16], %[rs], %[s0] offen offset:1024 sc1\n\t" \
    "buffer_load_dwordx4 v[208:211], %[l16], %[rs], %[s0] offen offset:2048 sc1\n\t" \
    "buffer_load_dwordx4 v[212:215], %[l16], %[rs], %[s0] offen offset:3072 sc1\n\t" \
    "buffer_load_dwordx4 v[216:219], %[l16], %[rs], %[s1] offen offset:0 sc1\n\t" \
    "buffer_load_dwordx4 v[220:223], %[l16], %[rs], %[s1] offen offset:1024 sc1\n\t" \
    "buffer_load_dwordx4 v[224:227], %[l16], %[rs], %[s1] offen offset:2048 sc1\n\t" \
    "buffer_load_dwordx4 v[228:231], %[l16], %[rs], %[s1] offen offset:3072 sc1\n\t" \
    "v_mov_b64 v[96:97], v[128:129]\n\t" \
    "v_mov_b64 v[98:99], v[132:133]\n\t" \
    "v_mov_b64 v[100:101], v[136:137]\n\t" \
    "v_mov_b64 v[102:103], v[140:141]\n\t" \
    "v_mov_b64 v[104:105], v[144:145]\n\t" \
    "v_mov_b64 v[106:107], v[148:149]\n\t" \
    "v_mov_b64 v[108:109], v[152:153]\n\t" \
    "v_mov_b64 v[110:111], v[156:157]\n\t" \
    "s_movk_i32 %[n], 0x200\n\t" \
    "s_nop 1\n\t" \
    "v_permlane16_swap_b32 v96, v98\n\t" \
    "v_permlane16_swap_b32 v97, v99\n\t" \
    "v_permlane16_swap_b32 v100, v102\n\t" \
    "v_permlane16_swap_b32 v101, v103\n\t" \
    "v_permlane16_swap_b32 v104, v106\n\t" \
    "v_permlane16_swap_b32 v105, v107\n\t" \
    "v_permlane16_swap_b32 v108, v110\n\t" \
    "v_permlane16_swap_b32 v109, v111\n\t" \
    "s_waitcnt vmcnt(7)\n\t" \
    "v_max_u32 v196, v200, v201\n\t" \
    "v_max3_u32 v196, v202, v203, v196\n\t" \
    "s_waitcnt vmcnt(6)\n\t" \
    "v_max3_u32 v196, v204, v205, v196\n\t" \
    "v_max3_u32 v196, v206, v207, v196\n\t" \
    "s_waitcnt vmcnt(5)\n\t" \
    "v_max3_u32 v196, v208, v209, v196\n\t" \
    "v_max3_u32 v196, v210, v211, v196\n\t" \
    "s_waitcnt vmcnt(4)\n\t" \
    "v_max3_u32 v196, v212, v213, v196\n\t" \
    "v_max3_u32 v196, v214, v215, v196\n\t" \
    "s_waitcnt vmcnt(3)\n\t" \
    "v_max3_u32 v196, v216, v217, v196\n\t" \
    "v_max3_u32 v196, v218, v219, v196\n\t" \
    "s_waitcnt vmcnt(2)\n\t" \
    "v_max3_u32 v196, v220, v221, v196\n\t" \
    "v_max3_u32 v196, v222, v223, v196\n\t" \
    "s_waitcnt vmcnt(1)\n\t" \
    "v_max3_u32 v196, v224, v225, v196\n\t" \
    "v_max3_u32 v196, v226, v227, v196\n\t" \
    "s_waitcnt vmcnt(0)\n\t" \
    "v_max3_u32 v196, v228, v229, v196\n\t" \
    "v_max3_u32 v196, v230, v231, v196\n\t" \
    "v_cmp_eq_u32 vcc, -1, v196\n\t" \
    "s_cbranch_vccz .Lxd%=\n\t" \
    ".Lxl%=:\n\t" \
    "s_mov_b32 %[a], 0\n\t" \
    "v_max_u32 v196, v200, v201\n\t" \
    "v_max3_u32 v196, v202, v203, v196\n\t" \
    "v_cmp_eq_u32 vcc, -1, v196\n\t" \
    "s_cbranch_vccz .Lxs0_%=\n\t" \
    "buffer_load_dwordx4 v[200:203], %[l16], %[rs], %[s0] offen offset:0 sc1\n\t" \
    "s_mov_b32 %[a], 1\n\t" \
    ".Lxs0_%=:\n\t" \
    "v_max_u32 v196, v204, v205\n\t" \
    "v_max3_u32 v196, v206, v207, v196\n\t" \
    "v_cmp_eq_u32 vcc, -1, v196\n\t" \
    "s_cbranch_vccz .Lxs1_%=\n\t" \
    "buffer_load_dwordx4 v[204:207], %[l16], %[rs], %[s0] offen offset:1024 sc1\n\t" \
    "s_mov_b32 %[a], 1\n\t" \
    ".Lxs1_%=:\n\t" \
    "v_max_u32 v196, v208, v209\n\t" \
    "v_max3_u32 v196, v210, v211, v196\n\t" \
    "v_cmp_eq_u32 vcc, -1, v196\n\t" \
    "s_cbranch_vccz .Lxs2_%=\n\t" \
    "buffer_load_dwordx4 v[208:211], %[l16], %[rs], %[s0] offen offset:2048 sc1\n\t" \
    "s_mov_b32 %[a], 1\n\t" \
    ".Lxs2_%=:\n\t" \
    "v_max_u32 v196, v212, v213\n\t" \
    "v_max3_u32 v196, v214, v215, v196\n\t" \
    "v_cmp_eq_u32 vcc, -1, v196\n\t" \
    "s_cbranch_vccz .Lxs3_%=\n\t" \
    "buffer_load_dwordx4 v[212:215], %[l16], %[rs], %[s0] offen offset:3072 sc1\n\t" \
    "s_mov_b32 %[a], 1\n\t" \
    ".Lxs3_%=:\n\t" \
    "v_max_u32 v196, v216, v217\n\t" \
    "v_max3_u32 v196, v218, v219, v196\n\t" \
    "v_cmp_eq_u32 vcc, -1, v196\n\t" \
    "s_cbranch_vccz .Lxs4_%=\n\t" \
    "buffer_load_dwordx4 v[216:219], %[l16], %[rs], %[s1] offen offset:0 sc1\n\t" \
    "s_mov_b32 %[a], 1\n\t" \
    ".Lxs4_%=:\n\t" \
    "v_max_u32 v196, v220, v221\n\t" \
    "v_max3_u32 v196, v222, v223, v196\n\t" \
    "v_cmp_eq_u32 vcc, -1, v196\n\t" \
    "s_cbranch_vccz .Lxs5_%=\n\t" \
    "buffer_load_dwordx4 v[220:223], %[l16], %[rs], %[s1] offen offset:1024 sc1\n\t" \
    "s_mov_b32 %[a], 1\n\t" \
    ".Lxs5_%=:\n\t" \
    "v_max_u32 v196, v224, v225\n\t" \
    "v_max3_u32 v196, v226, v227, v196\n\t" \
    "v_cmp_eq_u32 vcc, -1, v196\n\t" \
    "s_cbranch_vccz .Lxs6_%=\n\t" \
    "buffer_load_dwordx4 v[224:227], %[l16], %[rs], %[s1] offen offset:2048 sc1\n\t" \
    "s_mov_b32 %[a], 1\n\t" \
    ".Lxs6_%=:\n\t" \
    "v_max_u32 v196, v228, v229\n\t" \
    "v_max3_u32 v196, v230, v231, v196\n\t" \
    "v_cmp_eq_u32 vcc, -1, v196\n\t" \
    "s_cbranch_vccz .Lxs7_%=\n\t" \
    "buffer_load_dwordx4 v[228:231], %[l16], %[rs], %[s1] offen offset:3072 sc1\n\t" \
    "s_mov_b32 %[a], 1\n\t" \
    ".Lxs7_%=:\n\t" \
    "s_cmp_eq_u32 %[a], 0\n\t" \
    "s_cbranch_scc1 .Lxd%=\n\t" \
    "s_waitcnt vmcnt(0)\n\t" \
    "s_sub_u32 %[n], %[n], 1\n\t" \
    "s_cmp_lg_u32 %[n], 0\n\t" \
    "s_cbranch_scc1 .Lxl%=\n\t" \
    "s_branch .Lxe%=\n\t" \
    ".Lxd%=:\n\t" \
    ASM_ST2 \
    "v_mfma_f32_16x16x32_bf16 v[120:123], v[128:131], v[200:203], 0\n\t" \
    "v_mfma_f32_16x16x32_bf16 v[124:127], v[144:147], v[200:203], 0\n\t" \
    "v_mfma_f32_16x16x32_bf16 v[120:123], v[132:135], v[204:207], v[120:123]\n\t" \
    "v_mfma_f32_16x16x32_bf16 v[124:127], v[148:151], v[204:207], v[124:127]\n\t" \
    "v_mfma_f32_16x16x32_bf16 v[120:123], v[136:139], v[208:211], v[120:123]\n\t" \
    "v_mfma_f32_16x16x32_bf16 v[124:127], v[152:155], v[208:211], v[124:127]\n\t" \
    "v_mfma_f32_16x16x32_bf16 v[120:123], v[140:143], v[212:215], v[120:123]\n\t" \
    "v_mfma_f32_16x16x32_bf16 v[124:127], v[156:159], v[212:215], v[124:127]\n\t" \
    "v_mfma_f32_16x16x32_bf16 v[112:115], v[96:99], v[220:223], 0\n\t" \
    "v_mfma_f32_16x16x32_bf16 v[116:119], v[104:107], v[220:223], 0\n\t" \
    "v_mfma_f32_16x16x32_bf16 v[120:123], v[176:179], v[216:219], v[120:123]\n\t" \
    "v_mfma_f32_16x16x32_bf16 v[124:127], v[180:183], v[216:219], v[124:127]\n\t" \
    "v_mfma_f32_16x16x32_bf16 v[112:115], v[100:103], v[224:227], v[112:115]\n\t" \
    "v_mfma_f32_16x16x32_bf16 v[116:119], v[108:111], v[224:227], v[116:119]\n\t" \
    "v_mfma_f32_16x16x32_bf16 v[112:115], v[176:179], v[228:231], v[112:115]\n\t" \
    "v_mfma_f32_16x16x32_bf16 v[116:119], v[180:183], v[228:231], v[116:119]\n\t" \
    "s_nop 1\n\t" \
    "ds_write_b128 %[pw], v[120:123] offset:16384\n\t" \
    "ds_write_b128 %[pw], v[124:127] offset:24576\n\t" \
    "s_nop 3\n\t" \
    "ds_write_b128 %[pw], v[112:115] offset:0\n\t" \
    "ds_write_b128 %[pw], v[116:119] offset:8192\n\t" \
    "v_cmp_ge_u32 vcc, %[tn], v248\n\t" \
    "s_mov_b32 %[a], 0\n\t" \
    "s_cbranch_vccnz .Lxv%=\n\t" \
    "buffer_load_dwordx4 v[128:131], %[l16], %[rp], %[sv0] offen offset:0 sc1\n\t" \
    "buffer_load_dwordx4 v[132:135], %[l16], %[rp], %[sv0] offen offset:1024 sc1\n\t" \
    "buffer_load_dwordx4 v[136:139], %[l16], %[rp], %[sv0] offen offset:2048 sc1\n\t" \
    "buffer_load_dwordx4 v[140:143], %[l16], %[rp], %[sv0] offen offset:3072 sc1\n\t" \
    "buffer_load_dwordx4 v[144:147], %[l16], %[rp], %[sv1] offen offset:0 sc1\n\t" \
    "buffer_load_dwordx4 v[148:151], %[l16], %[rp], %[sv1] offen offset:1024 sc1\n\t" \
    "buffer_load_dwordx4 v[152:155], %[l16], %[rp], %[sv1] offen offset:2048 sc1\n\t" \
    "buffer_load_dwordx4 v[156:159], %[l16], %[rp], %[sv1] offen offset:3072 sc1\n\t" \
    "s_mov_b32 %[a], 1\n\t" \
    ".Lxv%=:\n\t" \
    "s_cmp_eq_u32 %[fin], 0\n\t" \
    "s_waitcnt lgkmcnt(0)\n\t" \
    "s_barrier\n\t" ASM_ST \
    "s_cbranch_scc1 .Lxe%=\n\t" \
    "ds_read_b128 v[200:203], %[pr] offset:0\n\t" \
    "ds_read_b128 v[204:207], %[pr] offset:1024\n\t" \
    "ds_read_b128 v[208:211], %[pr] offset:2048\n\t" \
    "ds_read_b128 v[212:215], %[pr] offset:3072\n\t" \
    "ds_read_b128 v[216:219], %[pr] offset:4096\n\t" \
    "ds_read_b128 v[220:223], %[pr] offset:5120\n\t" \
    "ds_read_b128 v[224:227], %[pr] offset:6144\n\t" \
    "ds_read_b128 v[228:231], %[pr] offset:7168\n\t" \
    "v_lshlrev_b32 v240, 16, v250\n\t" \
    "v_and_b32 v241, 0xffff0000, v250\n\t" \
    "v_lshlrev_b32 v242, 16, v251\n\t" \
    "v_and_b32 v243, 0xffff0000, v251\n\t" \
    "s_waitcnt lgkmcnt(6)\n\t" \
    "v_pk_add_f32 v[200:201], v[200:201], v[204:205]\n\t" \
    "v_pk_add_f32 v[202:203], v[202:203], v[206:207]\n\t" \
    "s_waitcnt lgkmcnt(4)\n\t" \
    "v_pk_add_f32 v[208:209], v[208:209], v[212:213]\n\t" \
    "v_pk_add_f32 v[210:211], v[210:211], v[214:215]\n\t" \
    "v_pk_add_f32 v[200:201], v[200:201], v[208:209]\n\t" \
    "v_pk_add_f32 v[202:203], v[202:203], v[210:211]\n\t" \
    "s_waitcnt lgkmcnt(2)\n\t" \
    "v_pk_add_f32 v[216:217], v[216:217], v[220:221]\n\t" \
    "v_pk_add_f32 v[218:219], v[218:219], v[222:223]\n\t" \
    "s_waitcnt lgkmcnt(0)\n\t" \
    "v_pk_add_f32 v[224:225], v[224:225], v[228:229]\n\t" \
    "v_pk_add_f32 v[226:227], v[226:227], v[230:231]\n\t" \
    "v_pk_add_f32 v[216:217], v[216:217], v[224:225]\n\t" \
    "v_pk_add_f32 v[218:219], v[218:219], v[226:227]\n\t" \
    "v_pk_add_f32 v[200:201], v[200:201], v[216:217]\n\t" \
    "v_pk_add_f32 v[202:203], v[202:203], v[218:219]\n\t" \
    "v_pk_add_f32 v[200:201], v[200:201], v[240:241]\n\t" \
    "v_pk_add_f32 v[202:203], v[202:203], v[242:243]\n\t" \
    "v_cvt_pk_bf16_f32 v200, v200, v201\n\t" \
    "v_cvt_pk_bf16_f32 v201, v202, v203\n\t" \
    "v_min_u32 v200, -2, v200\n\t" \
    "v_min_u32 v201, -2, v201\n\t" \
    "buffer_store_dwordx2 v[200:201], %[xo], %[rd], %[sa] offen sc1\n\t" \
    ".Lxe%=:\n\t"
#define ASM_W4_M \
    "buffer_load_dwordx4 v[188:191], %[l16], %[rs], %[s1] offen offset:3072 sc1\n\t" \
    ".Lc7_%=:\n\t" \
    "s_waitcnt vmcnt(0)\n\t" \
    "v_max_u32 v212, v188, v189\n\t" \
    "v_max3_u32 v212, v190, v191, v212\n\t" \
    "v_cmp_eq_u32 vcc, -1, v212\n\t" \
    "s_cbranch_vccnz .Ls7_%=\n\t" \
    ".Lk7_%=:\n\t" \
    ASM_ST2 \
    "buffer_load_dwordx4 v[184:187], %[l16], %[rs], %[s1] offen offset:2048 sc1\n\t" \
    "buffer_load_dwordx4 v[180:183], %[l16], %[rs], %[s1] offen offset:1024 sc1\n\t" \
    "buffer_load_dwordx4 v[176:179], %[l16], %[rs], %[s1] offen offset:0 sc1\n\t" \
    "buffer_load_dwordx4 v[172:175], %[l16], %[rs], %[s0] offen offset:3072 sc1\n\t" \
    "buffer_load_dwordx4 v[168:171], %[l16], %[rs], %[s0] offen offset:2048 sc1\n\t" \
    "buffer_load_dwordx4 v[164:167], %[l16], %[rs], %[s0] offen offset:1024 sc1\n\t" \
    "buffer_load_dwordx4 v[160:163], %[l16], %[rs], %[s0] offen offset:0 sc1\n\t" \
    "v_mfma_f32_16x16x32_bf16 v[192:195], v[28:31], v[188:191], 0\n\t" \
    "v_mfma_f32_16x16x32_bf16 v[196:199], v[60:63], v[188:191], 0\n\t" \
    "v_mfma_f32_16x16x32_bf16 v[200:203], v[92:95], v[188:191], 0\n\t" \
    "v_mfma_f32_16x16x32_bf16 v[204:207], v[124:127], v[188:191], 0\n\t" \
    "v_mfma_f32_16x16x32_bf16 v[208:211], v[156:159], v[188:191], 0\n\t" \
    ".Lc6_%=:\n\t" \
    "s_waitcnt vmcnt(6)\n\t" \
    "v_max_u32 v212, v184, v185\n\t" \
    "v_max3_u32 v212, v186, v187, v212\n\t" \
    "v_cmp_eq_u32 vcc, -1, v212\n\t" \
    "s_cbranch_vccnz .Ls6_%=\n\t" \
    ".Lk6_%=:\n\t" \
    "v_mfma_f32_16x16x32_bf16 v[192:195], v[24:27], v[184:187], v[192:195]\n\t" \
    "v_mfma_f32_16x16x32_bf16 v[196:199], v[56:59], v[184:187], v[196:199]\n\t" \
    "v_mfma_f32_16x16x32_bf16 v[200:203], v[88:91], v[184:187], v[200:203]\n\t" \
    "v_mfma_f32_16x16x32_bf16 v[204:207], v[120:123], v[184:187], v[204:207]\n\t" \
    "v_mfma_f32_16x16x32_bf16 v[208:211], v[152:155], v[184:187], v[208:211]\n\t" \
    ".Lc5_%=:\n\t" \
    "s_waitcnt vmcnt(5)\n\t" \
    "v_max_u32 v212, v180, v181\n\t" \
    "v_max3_u32 v212, v182, v183, v212\n\t" \
    "v_cmp_eq_u32 vcc, -1, v212\n\t" \
    "s_cbranch_vccnz .Ls5_%=\n\t" \
    ".Lk5_%=:\n\t" \
    "v_mfma_f32_16x16x32_bf16 v[192:195], v[20:23], v[180:183], v[192:195]\n\t" \
    "v_mfma_f32_16x16x32_bf16 v[196:199], v[52:55], v[180:183], v[196:199]\n\t" \
    "v_mfma_f32_16x16x32_bf16 v[200:203], v[84:87], v[180:183], v[200:203]\n\t" \
    "v_mfma_f32_16x16x32_bf16 v[204:207], v[116:119], v[180:183], v[204:207]\n\t" \
    "v_mfma_f32_16x16x32_bf16 v[208:211], v[148:151], v[180:183], v[208:211]\n\t" \
    ".Lc4_%=:\n\t" \
    "s_waitcnt vmcnt(4)\n\t" \
    "v_max_u32 v212, v176, v177\n\t" \
    "v_max3_u32 v212, v178, v179, v212\n\t" \
    "v_cmp_eq_u32 vcc, -1, v212\n\t" \
    "s_cbranch_vccnz .Ls4_%=\n\t" \
    ".Lk4_%=:\n\t" \
    "v_mfma_f32_16x16x32_bf16 v[192:195], v[16:19], v[176:179], v[192:195]\n\t" \
    "v_mfma_f32_16x16x32_bf16 v[196:199], v[48:51], v[176:179], v[196:199]\n\t" \
    "v_mfma_f32_16x16x32_bf16 v[200:203], v[80:83], v[176:179], v[200:203]\n\t" \
    "v_mfma_f32_16x16x32_bf16 v[204:207], v[112:115], v[176:179], v[204:207]\n\t" \
    "v_mfma_f32_16x16x32_bf16 v[208:211], v[144:147], v[176:179], v[208:211]\n\t" \
    ".Lc3_%=:\n\t" \
    "s_waitcnt vmcnt(3)\n\t" \
    "v_max_u32 v212, v172, v173\n\t" \
    "v_max3_u32 v212, v174, v175, v212\n\t" \
    "v_cmp_eq_u32 vcc, -1, v212\n\t" \
    "s_cbranch_vccnz .Ls3_%=\n\t" \
    ".Lk3_%=:\n\t" \
    "v_mfma_f32_16x16x32_bf16 v[192:195], v[12:15], v[172:175], v[192:195]\n\t" \
    "v_mfma_f32_16x16x32_bf16 v[196:199], v[44:47], v[172:175], v[196:199]\n\t" \
    "v_mfma_f32_16x16x32_bf16 v[200:203], v[76:79], v[172:175], v[200:203]\n\t" \
    "v_mfma_f32_16x16x32_bf16 v[204:207], v[108:111], v[172:175], v[204:207]\n\t" \
    "v_mfma_f32_16x16x32_bf16 v[208:211], v[140:143], v[172:175], v[208:211]\n\t" \
    ".Lc2_%=:\n\t" \
    "s_waitcnt vmcnt(2)\n\t" \
    "v_max_u32 v212, v168, v169\n\t" \
    "v_max3_u32 v212, v170, v171, v212\n\t" \
    "v_cmp_eq_u32 vcc, -1, v212\n\t" \
    "s_cbranch_vccnz .Ls2_%=\n\t" \
    ".Lk2_%=:\n\t" \
    "v_mfma_f32_16x16x32_bf16 v[192:195], v[8:11], v[168:171], v[192:195]\n\t" \
    "v_mfma_f32_16x16x32_bf16 v[196:199], v[40:43], v[168:171], v[196:199]\n\t" \
    "v_mfma_f32_16x16x32_bf16 v[200:203], v[72:75], v[168:171], v[200:203]\n\t" \
    "v_mfma_f32_16x16x32_bf16 v[204:207], v[104:107], v[168:171], v[204:207]\n\t" \
    "v_mfma_f32_16x16x32_bf16 v[208:211], v[136:139], v[168:171], v[208:211]\n\t" \
    ".Lc1_%=:\n\t" \
    "s_waitcnt vmcnt(1)\n\t" \
    "v_max_u32 v212, v164, v165\n\t" \
    "v_max3_u32 v212, v166, v167, v212\n\t" \
    "v_cmp_eq_u32 vcc, -1, v212\n\t" \
    "s_cbranch_vccnz .Ls1_%=\n\t" \
    ".Lk1_%=:\n\t" \
    "v_mfma_f32_16x16x32_bf16 v[192:195], v[4:7], v[164:167], v[192:195]\n\t" \
    "v_mfma_f32_16x16x32_bf16 v[196:199], v[36:39], v[164:167], v[196:199]\n\t" \
    "v_mfma_f32_16x16x32_bf16 v[200:203], v[68:71], v[164:167], v[200:203]\n\t" \
    "v_mfma_f32_16x16x32_bf16 v[204:207], v[100:103], v[164:167], v[204:207]\n\t" \
    "v_mfma_f32_16x16x32_bf16 v[208:211], v[132:135], v[164:167], v[208:211]\n\t" \
    ".Lc0_%=:\n\t" \
    "s_waitcnt vmcnt(0)\n\t" \
    "v_max_u32 v212, v160, v161\n\t" \
    "v_max3_u32 v212, v162, v163, v212\n\t" \
    "v_cmp_eq_u32 vcc, -1, v212\n\t" \
    "s_cbranch_vccnz .Ls0_%=\n\t" \
    ".Lk0_%=:\n\t" \
    "v_mfma_f32_16x16x32_bf16 v[192:195], v[0:3], v[160:163], v[192:195]\n\t" \
    "v_mfma_f32_16x16x32_bf16 v[196:199], v[32:35], v[160:163], v[196:199]\n\t" \
    "v_mfma_f32_16x16x32_bf16 v[200:203], v[64:67], v[160:163], v[200:203]\n\t" \
    "v_mfma_f32_16x16x32_bf16 v[204:207], v[96:99], v[160:163], v[204:207]\n\t" \
    "v_mfma_f32_16x16x32_bf16 v[208:211], v[128:131], v[160:163], v[208:211]\n\t" \
    "v_mov_b32 v212, %[un]\n\t" \
    "ds_write_b128 %[ld], v[192:195] offset:0\n\t" \
    "ds_write_b128 %[ld], v[196:199] offset:6144\n\t" \
    "ds_write_b128 %[ld], v[200:203] offset:12288\n\t" \
    "s_nop 0\n\t" \
    "ds_write_b128 %[ld], v[204:207] offset:18432\n\t" \
    "s_nop 1\n\t" \
    "ds_write_b128 %[ld], v[208:211] offset:24576\n\t" \
    "ds_write_b32 %[fl], v212\n\t" \
    "s_branch .Lme%=\n\t" \
    ".Ls7_%=:\n\t" \
    "s_cmp_eq_u32 %[n], 0\n\t" \
    "s_cbranch_scc1 .Lk7_%=\n\t" \
    "s_sub_u32 %[n], %[n], 1\n\t" \
    "s_sleep 1\n\t" \
    "buffer_load_dwordx4 v[188:191], %[l16], %[rs], %[s1] offen offset:3072 sc1\n\t" \
    "s_branch .Lc7_%=\n\t" \
    ".Ls6_%=:\n\t" \
    "s_cmp_eq_u32 %[n], 0\n\t" \
    "s_cbranch_scc1 .Lk6_%=\n\t" \
    "s_sub_u32 %[n], %[n], 1\n\t" \
    "buffer_load_dwordx4 v[184:187], %[l16], %[rs], %[s1] offen offset:2048 sc1\n\t" \
    "s_waitcnt vmcnt(0)\n\t" \
    "s_branch .Lc6_%=\n\t" \
    ".Ls5_%=:\n\t" \
    "s_cmp_eq_u32 %[n], 0\n\t" \
    "s_cbranch_scc1 .Lk5_%=\n\t" \
    "s_sub_u32 %[n], %[n], 1\n\t" \
    "buffer_load_dwordx4 v[180:183], %[l16], %[rs], %[s1] offen offset:1024 sc1\n\t" \
    "s_waitcnt vmcnt(0)\n\t" \
    "s_branch .Lc5_%=\n\t" \
    ".Ls4_%=:\n\t" \
    "s_cmp_eq_u32 %[n], 0\n\t" \
    "s_cbranch_scc1 .Lk4_%=\n\t" \
    "s_sub_u32 %[n], %[n], 1\n\t" \
    "buffer_load_dwordx4 v[176:179], %[l16], %[rs], %[s1] offen offset:0 sc1\n\t" \
    "s_waitcnt vmcnt(0)\n\t" \
    "s_branch .Lc4_%=\n\t" \
    ".Ls3_%=:\n\t" \
    "s_cmp_eq_u32 %[n], 0\n\t" \
    "s_cbranch_scc1 .Lk3_%=\n\t" \
    "s_sub_u32 %[n], %[n], 1\n\t" \
    "buffer_load_dwordx4 v[172:175], %[l16], %[rs], %[s0] offen offset:3072 sc1\n\t" \
    "s_waitcnt vmcnt(0)\n\t" \
    "s_branch .Lc3_%=\n\t" \
    ".Ls2_%=:\n\t" \
    "s_cmp_eq_u32 %[n], 0\n\t" \
    "s_cbranch_scc1 .Lk2_%=\n\t" \
    "s_sub_u32 %[n], %[n], 1\n\t" \
    "buffer_load_dwordx4 v[168:171], %[l16], %[rs], %[s0] offen offset:2048 sc1\n\t" \
    "s_waitcnt vmcnt(0)\n\t" \
    "s_branch .Lc2_%=\n\t" \
    ".Ls1_%=:\n\t" \
    "s_cmp_eq_u32 %[n], 0\n\t" \
    "s_cbranch_scc1 .Lk1_%=\n\t" \
    "s_sub_u32 %[n], %[n], 1\n\t" \
    "buffer_load_dwordx4 v[164:167], %[l16], %[rs], %[s0] offen offset:1024 sc1\n\t" \
    "s_waitcnt vmcnt(0)\n\t" \
    "s_branch .Lc1_%=\n\t" \
    ".Ls0_%=:\n\t" \
    "s_cmp_eq_u32 %[n], 0\n\t" \
    "s_cbranch_scc1 .Lk0_%=\n\t" \
    "s_sub_u32 %[n], %[n], 1\n\t" \
    "buffer_load_dwordx4 v[160:163], %[l16], %[rs], %[s0] offen offset:0 sc1\n\t" \
    "s_branch .Lc0_%=\n\t" \
    ".Lme%=:\n\t"
#define ASM_WCS \
    "v_mfma_f32_16x16x32_bf16 v[196:199], v[0:3], v[216:219], 0\n\t" \
    "v_mfma_f32_16x16x32_bf16 v[200:203], v[80:83], v[216:219], 0\n\t" \
    "v_mfma_f32_16x16x32_bf16 v[196:199], v[4:7], v[216:219], v[196:199]\n\t" \
    "v_mfma_f32_16x16x32_bf16 v[200:203], v[84:87], v[216:219], v[200:203]\n\t" \
    "v_mfma_f32_16x16x32_bf16 v[196:199], v[8:11], v[216:219], v[196:199]\n\t" \
    "v_mfma_f32_16x16x32_bf16 v[200:203], v[88:91], v[216:219], v[200:203]\n\t" \
    "v_mfma_f32_16x16x32_bf16 v[196:199], v[12:15], v[216:219], v[196:199]\n\t" \
    "v_mfma_f32_16x16x32_bf16 v[200:203], v[92:95], v[216:219], v[200:203]\n\t" \
    "v_mfma_f32_16x16x32_bf16 v[196:199], v[16:19], v[216:219], v[196:199]\n\t" \
    "v_mfma_f32_16x16x32_bf16 v[200:203], v[96:99], v[216:219], v[200:203]\n\t" \
    "v_mfma_f32_16x16x32_bf16 v[196:199], v[20:23], v[216:219], v[196:199]\n\t" \
    "v_mfma_f32_16x16x32_bf16 v[200:203], v[100:103], v[216:219], v[200:203]\n\t" \
    "v_mfma_f32_16x16x32_bf16 v[196:199], v[24:27], v[216:219], v[196:199]\n\t" \
    "v_mfma_f32_16x16x32_bf16 v[200:203], v[104:107], v[216:219], v[200:203]\n\t" \
    "v_mfma_f32_16x16x32_bf16 v[196:199], v[28:31], v[216:219], v[196:199]\n\t" \
    "v_mfma_f32_16x16x32_bf16 v[200:203], v[108:111], v[216:219], v[200:203]\n\t" \
    "v_mfma_f32_16x16x32_bf16 v[196:199], v[32:35], v[216:219], v[196:199]\n\t" \
    "v_mfma_f32_16x16x32_bf16 v[200:203], v[112:115], v[216:219], v[200:203]\n\t" \
    "v_mfma_f32_16x16x32_bf16 v[196:199], v[36:39], v[216:219], v[196:199]\n\t" \
    "v_mfma_f32_16x16x32_bf16 v[200:203], v[116:119], v[216:219], v[200:203]\n\t" \
    "v_mfma_f32_16x16x32_bf16 v[196:199], v[40:43], v[216:219], v[196:199]\n\t" \
    "v_mfma_f32_16x16x32_bf16 v[200:203], v[120:123], v[216:219], v[200:203]\n\t" \
    "v_mfma_f32_16x16x32_bf16 v[196:199], v[44:47], v[216:219], v[196:199]\n\t" \
    "v_mfma_f32_16x16x32_bf16 v[200:203], v[124:127], v[216:219], v[200:203]\n\t" \
    "v_mfma_f32_16x16x32_bf16 v[196:199], v[48:51], v[216:219], v[196:199]\n\t" \
    "v_mfma_f32_16x16x32_bf16 v[200:203], v[128:131], v[216:219], v[200:203]\n\t" \
    "v_mfma_f32_16x16x32_bf16 v[196:199], v[52:55], v[216:219], v[196:199]\n\t" \
    "v_mfma_f32_16x16x32_bf16 v[200:203], v[132:135], v[216:219], v[200:203]\n\t" \
    "v_mfma_f32_16x16x32_bf16 v[196:199], v[56:59], v[216:219], v[196:199]\n\t" \
    "v_mfma_f32_16x16x32_bf16 v[200:203], v[136:139], v[216:219], v[200:203]\n\t" \
    "v_mfma_f32_16x16x32_bf16 v[196:199], v[60:63], v[216:219], v[196:199]\n\t" \
    "v_mfma_f32_16x16x32_bf16 v[200:203], v[140:143], v[216:219], v[200:203]\n\t" \
    "v_mfma_f32_16x16x32_bf16 v[196:199], v[64:67], v[216:219], v[196:199]\n\t" \
    "v_mfma_f32_16x16x32_bf16 v[200:203], v[144:147], v[216:219], v[200:203]\n\t" \
    "v_mfma_f32_16x16x32_bf16 v[196:199], v[68:71], v[216:219], v[196:199]\n\t" \
    "v_mfma_f32_16x16x32_bf16 v[200:203], v[148:151], v[216:219], v[200:203]\n\t" \
    "v_mfma_f32_16x16x32_bf16 v[196:199], v[72:75], v[216:219], v[196:199]\n\t" \
    "v_mfma_f32_16x16x32_bf16 v[200:203], v[152:155], v[216:219], v[200:203]\n\t" \
    "v_mfma_f32_16x16x32_bf16 v[196:199], v[76:79], v[216:219], v[196:199]\n\t" \
    "v_mfma_f32_16x16x32_bf16 v[200:203], v[156:159], v[216:219], v[200:203]\n\t" \
    "s_nop 7\n\t"
#define ASM_WT1_LO \
    "s_branch .Lt0%=\n\t" \
    ".Ltr%=:\n\t" \
    "s_cmp_gt_i32 %[n], 0\n\t" \
    "s_cbranch_scc0 .Ltg%=\n\t" \
    ".Lfw%=:\n\t" \
    "s_mov_b32 %[a], 0\n\t" \
    "v_max_u32 v252, v232, v233\n\t" \
    "v_max3_u32 v252, v234, v235, v252\n\t" \
    "v_cmp_eq_u32 vcc, -1, v252\n\t" \
    "s_cbranch_vccz .Lgw0_%=\n\t" \
    "buffer_load_dwordx4 v[232:235], %[l16], %[rs], %[i0] offen offset:0 sc1\n\t" \
    "s_mov_b32 %[a], 1\n\t" \
    ".Lgw0_%=:\n\t" \
    "v_max_u32 v252, v236, v237\n\t" \
    "v_max3_u32 v252, v238, v239, v252\n\t" \
    "v_cmp_eq_u32 vcc, -1, v252\n\t" \
    "s_cbranch_vccz .Lgw1_%=\n\t" \
    "buffer_load_dwordx4 v[236:239], %[l16], %[rs], %[i0] offen offset:1024 sc1\n\t" \
    "s_mov_b32 %[a], 1\n\t" \
    ".Lgw1_%=:\n\t" \
    "v_max_u32 v252, v240, v241\n\t" \
    "v_max3_u32 v252, v242, v243, v252\n\t" \
    "v_cmp_eq_u32 vcc, -1, v252\n\t" \
    "s_cbranch_vccz .Lgw2_%=\n\t" \
    "buffer_load_dwordx4 v[240:243], %[l16], %[rs], %[i0] offen offset:2048 sc1\n\t" \
    "s_mov_b32 %[a], 1\n\t" \
    ".Lgw2_%=:\n\t" \
    "v_max_u32 v252, v244, v245\n\t" \
    "v_max3_u32 v252, v246, v247, v252\n\t" \
    "v_cmp_eq_u32 vcc, -1, v252\n\t" \
    "s_cbranch_vccz .Lgw3_%=\n\t" \
    "buffer_load_dwordx4 v[244:247], %[l16], %[rs], %[i0] offen offset:3072 sc1\n\t" \
    "s_mov_b32 %[a], 1\n\t" \
    ".Lgw3_%=:\n\t" \
    "v_max_u32 v252, v248, v249\n\t" \
    "v_max3_u32 v252, v250, v251, v252\n\t" \
    "v_cmp_eq_u32 vcc, -1, v252\n\t" \
    "s_cbranch_vccz .Lgw4_%=\n\t" \
    "buffer_load_dwordx4 v[248:251], %[l16], %[rs], %[i1] offen offset:0 sc1\n\t" \
    "s_mov_b32 %[a], 1\n\t" \
    ".Lgw4_%=:\n\t" \
    "s_cmp_eq_u32 %[a], 0\n\t" \
    "s_cbranch_scc1 .Lhw%=\n\t" \
    "s_waitcnt vmcnt(20)\n\t" \
    ".Liw%=:\n\t" \
    "v_max_u32 v252, v232, v233\n\t" \
    "v_max3_u32 v252, v234, v235, v252\n\t" \
    "v_max3_u32 v252, v236, v237, v252\n\t" \
    "v_max3_u32 v252, v238, v239, v252\n\t" \
    "v_max3_u32 v252, v240, v241, v252\n\t" \
    "v_max3_u32 v252, v242, v243, v252\n\t" \
    "v_max3_u32 v252, v244, v245, v252\n\t" \
    "v_max3_u32 v252, v246, v247, v252\n\t" \
    "v_max3_u32 v252, v248, v249, v252\n\t" \
    "v_max3_u32 v252, v250, v251, v252\n\t" \
    "v_cmp_eq_u32 vcc, -1, v252\n\t" \
    "s_cbranch_vccz .Lhw%=\n\t" \
    "s_sleep 1\n\t" \
    "v_max_u32 v252, v232, v233\n\t" \
    "v_max3_u32 v252, v234, v235, v252\n\t" \
    "v_max3_u32 v252, v236, v237, v252\n\t" \
    "v_max3_u32 v252, v238, v239, v252\n\t" \
    "v_max3_u32 v252, v240, v241, v252\n\t" \
    "v_max3_u32 v252, v242, v243, v252\n\t" \
    "v_max3_u32 v252, v244, v245, v252\n\t" \
    "v_max3_u32 v252, v246, v247, v252\n\t" \
    "v_max3_u32 v252, v248, v249, v252\n\t" \
    "v_max3_u32 v252, v250, v251, v252\n\t" \
    "v_cmp_eq_u32 vcc, -1, v252\n\t" \
    "s_cbranch_vccz .Lhw%=\n\t" \
    "s_sleep 1\n\t" \
    "v_max_u32 v252, v232, v233\n\t" \
    "v_max3_u32 v252, v234, v235, v252\n\t" \
    "v_max3_u32 v252, v236, v237, v252\n\t" \
    "v_max3_u32 v252, v238, v239, v252\n\t" \
    "v_max3_u32 v252, v240, v241, v252\n\t" \
    "v_max3_u32 v252, v242, v243, v252\n\t" \
    "v_max3_u32 v252, v244, v245, v252\n\t" \
    "v_max3_u32 v252, v246, v247, v252\n\t" \
    "v_max3_u32 v252, v248, v249, v252\n\t" \
    "v_max3_u32 v252, v250, v251, v252\n\t" \
    "v_cmp_eq_u32 vcc, -1, v252\n\t" \
    "s_cbranch_vccz .Lhw%=\n\t" \
    "s_sleep 1\n\t" \
    "v_max_u32 v252, v232, v233\n\t" \
    "v_max3_u32 v252, v234, v235, v252\n\t" \
    "v_max3_u32 v252, v236, v237, v252\n\t" \
    "v_max3_u32 v252, v238, v239, v252\n\t" \
    "v_max3_u32 v252, v240, v241, v252\n\t" \
    "v_max3_u32 v252, v242, v243, v252\n\t" \
    "v_max3_u32 v252, v244, v245, v252\n\t" \
    "v_max3_u32 v252, v246, v247, v252\n\t" \
    "v_max3_u32 v252, v248, v249, v252\n\t" \
    "v_max3_u32 v252, v250, v251, v252\n\t" \
    "v_cmp_eq_u32 vcc, -1, v252\n\t" \
    "s_cbranch_vccz .Lhw%=\n\t" \
    "s_sleep 1\n\t" \
    "v_max_u32 v252, v232, v233\n\t" \
    "v_max3_u32 v252, v234, v235, v252\n\t" \
    "v_max3_u32 v252, v236, v237, v252\n\t" \
    "v_max3_u32 v252, v238, v239, v252\n\t" \
    "v_max3_u32 v252, v240, v241, v252\n\t" \
    "v_max3_u32 v252, v242, v243, v252\n\t" \
    "v_max3_u32 v252, v244, v245, v252\n\t" \
    "v_max3_u32 v252, v246, v247, v252\n\t" \
    "v_max3_u32 v252, v248, v249, v252\n\t" \
    "v_max3_u32 v252, v250, v251, v252\n\t" \
    "v_cmp_eq_u32 vcc, -1, v252\n\t" \
    "s_cbranch_vccz .Lhw%=\n\t" \
    "s_sleep 1\n\t" \
    "v_max_u32 v252, v232, v233\n\t" \
    "v_max3_u32 v252, v234, v235, v252\n\t" \
    "v_max3_u32 v252, v236, v237, v252\n\t" \
    "v_max3_u32 v252, v238, v239, v252\n\t" \
    "v_max3_u32 v252, v240, v241, v252\n\t" \
    "v_max3_u32 v252, v242, v243, v252\n\t" \
    "v_max3_u32 v252, v244, v245, v252\n\t" \
    "v_max3_u32 v252, v246, v247, v252\n\t" \
    "v_max3_u32 v252, v248, v249, v252\n\t" \
    "v_max3_u32 v252, v250, v251, v252\n\t" \
    "v_cmp_eq_u32 vcc, -1, v252\n\t" \
    "s_cbranch_vccz .Lhw%=\n\t" \
    "s_sleep 1\n\t" \
    "s_sub_u32 %[n], %[n], 1\n\t" \
    "s_cmp_gt_i32 %[n], 0\n\t" \
    "s_cbranch_scc1 .Lfw%=\n\t" \
    ".Lhw%=:\n\t" \
    "s_branch .Lta%=\n\t" \
    ".Lt0%=:\n\t" \
    "s_waitcnt vmcnt(1)\n\t" \
    ".Lta%=:\n\t" \
    "ds_write_b128 %[lw], v[232:235] offset:0\n\t" \
    "v_max_u32 v252, v232, v233\n\t" \
    "v_max3_u32 v252, v234, v235, v252\n\t" \
    "v_mfma_f32_16x16x32_bf16 v[188:191], v[232:235], v[232:235], 0\n\t" \
    "v_mfma_f32_16x16x32_bf16 v[192:195], v[216:219], v[232:235], 0\n\t" \
    "ds_write_b128 %[lw], v[236:239] offset:1024\n\t" \
    "v_max3_u32 v252, v236, v237, v252\n\t" \
    "v_max3_u32 v252, v238, v239, v252\n\t" \
    "v_mfma_f32_16x16x32_bf16 v[188:191], v[236:239], v[236:239], v[188:191]\n\t" \
    "v_mfma_f32_16x16x32_bf16 v[192:195], v[216:219], v[236:239], v[192:195]\n\t" \
    "ds_write_b128 %[lw], v[240:243] offset:2048\n\t" \
    "v_max3_u32 v252, v240, v241, v252\n\t" \
    "v_max3_u32 v252, v242, v243, v252\n\t" \
    "v_mfma_f32_16x16x32_bf16 v[188:191], v[240:243], v[240:243], v[188:191]\n\t" \
    "v_mfma_f32_16x16x32_bf16 v[192:195], v[216:219], v[240:243], v[192:195]\n\t" \
    "ds_write_b128 %[lw], v[244:247] offset:3072\n\t" \
    "v_max3_u32 v252, v244, v245, v252\n\t" \
    "v_max3_u32 v252, v246, v247, v252\n\t" \
    "v_mfma_f32_16x16x32_bf16 v[188:191], v[244:247], v[244:247], v[188:191]\n\t" \
    "v_mfma_f32_16x16x32_bf16 v[192:195], v[216:219], v[244:247], v[192:195]\n\t" \
    "ds_write_b128 %[lw], v[248:251] offset:4096\n\t" \
    "v_max3_u32 v252, v248, v249, v252\n\t" \
    "v_max3_u32 v252, v250, v251, v252\n\t" \
    "v_mfma_f32_16x16x32_bf16 v[188:191], v[248:251], v[248:251], v[188:191]\n\t" \
    "v_mfma_f32_16x16x32_bf16 v[192:195], v[216:219], v[248:251], v[192:195]\n\t" \
    "v_cmp_eq_u32 vcc, -1, v252\n\t" \
    "s_cbranch_vccnz .Ltr%=\n\t" \
    ".Ltg%=:\n\t" \
    ASM_ST2 \
    "s_nop 5\n\t" \
    "ds_write_b128 %[lw], v[188:191] offset:5120\n\t" \
    "ds_write_b128 %[lw], v[192:195] offset:6144\n\t" \
    "s_waitcnt lgkmcnt(0)\n\t" \
    "s_setprio 2\n\t" \
    "s_barrier\n\t" ASM_ST \
    "ds_read_b128 v[160:163], %[la] offset:0\n\t" \
    "ds_read_b128 v[164:167], %[la] offset:1024\n\t" \
    "ds_read_b128 v[168:171], %[la] offset:2048\n\t" \
    "ds_read2st64_b32 v[204:205], %[sr] offset0:20 offset1:24\n\t" \
    "ds_read_b128 v[172:175], %[la] offset:3072\n\t" \
    "ds_read_b128 v[176:179], %[la] offset:8192\n\t" \
    "ds_read_b128 v[180:183], %[la] offset:9216\n\t" \
    "s_waitcnt lgkmcnt(6)\n\t" \
    "v_mfma_f32_16x16x32_bf16 v[188:191], v[0:3], v[160:163], 0\n\t" \
    "ds_read_b128 v[184:187], %[la] offset:10240\n\t" \
    "v_mfma_f32_16x16x32_bf16 v[192:195], v[80:83], v[160:163], 0\n\t" \
    "s_waitcnt lgkmcnt(6)\n\t" \
    "v_mfma_f32_16x16x32_bf16 v[188:191], v[4:7], v[164:167], v[188:191]\n\t" \
    "ds_read_b128 v[160:163], %[la] offset:11264\n\t" \
    "v_mfma_f32_16x16x32_bf16 v[192:195], v[84:87], v[164:167], v[192:195]\n\t" \
    "s_waitcnt lgkmcnt(6)\n\t" \
    "v_mfma_f32_16x16x32_bf16 v[188:191], v[8:11], v[168:171], v[188:191]\n\t" \
    "ds_read_b128 v[164:167], %[la] offset:16384\n\t" \
    "s_waitcnt lgkmcnt(6)\n\t" \
    "v_mfma_f32_16x16x32_bf16 v[192:195], v[88:91], v[168:171], v[192:195]\n\t" \
    "v_permlane16_swap_b32 v204, v205\n\t" \
    "s_waitcnt lgkmcnt(5)\n\t" \
    "v_mfma_f32_16x16x32_bf16 v[188:191], v[12:15], v[172:175], v[188:191]\n\t" \
    "ds_read_b128 v[168:171], %[la] offset:17408\n\t" \
    "v_mfma_f32_16x16x32_bf16 v[192:195], v[92:95], v[172:175], v[192:195]\n\t" \
    "v_add_f32 v206, v204, v205\n\t" \
    "s_waitcnt lgkmcnt(5)\n\t" \
    "v_mfma_f32_16x16x32_bf16 v[188:191], v[16:19], v[176:179], v[188:191]\n\t" \
    "ds_read_b128 v[172:175], %[la] offset:18432\n\t" \
    "v_add_f32 v207, v204, v205\n\t" \
    "v_mfma_f32_16x16x32_bf16 v[192:195], v[96:99], v[176:179], v[192:195]\n\t" \
    "s_waitcnt lgkmcnt(5)\n\t" \
    "v_mfma_f32_16x16x32_bf16 v[188:191], v[20:23], v[180:183], v[188:191]\n\t" \
    "ds_read_b128 v[176:179], %[la] offset:19456\n\t" \
    "v_permlane32_swap_b32 v206, v207\n\t" \
    "v_mfma_f32_16x16x32_bf16 v[192:195], v[100:103], v[180:183], v[192:195]\n\t" \
    "s_waitcnt lgkmcnt(5)\n\t" \
    "v_mfma_f32_16x16x32_bf16 v[188:191], v[24:27], v[184:187], v[188:191]\n\t" \
    "ds_read_b128 v[180:183], %[la] offset:24576\n\t" \
    "v_add_f32 v204, v206, v207\n\t" \
    "v_mfma_f32_16x16x32_bf16 v[192:195], v[104:107], v[184:187], v[192:195]\n\t" \
    "v_add_f32 v205, v206, v207\n\t" \
    "s_waitcnt lgkmcnt(5)\n\t" \
    "v_mfma_f32_16x16x32_bf16 v[188:191], v[28:31], v[160:163], v[188:191]\n\t" \
    "ds_read_b128 v[184:187], %[la] offset:25600\n\t" \
    "v_mfma_f32_16x16x32_bf16 v[192:195], v[108:111], v[160:163], v[192:195]\n\t" \
    "v_permlane16_swap_b32 v204, v205\n\t" \
    "s_waitcnt lgkmcnt(5)\n\t" \
    "v_mfma_f32_16x16x32_bf16 v[188:191], v[32:35], v[164:167], v[188:191]\n\t" \
    "ds_read_b128 v[160:163], %[la] offset:26624\n\t" \
    "v_mfma_f32_16x16x32_bf16 v[192:195], v[112:115], v[164:167], v[192:195]\n\t" \
    "v_mul_f32 v205, %[kd], v205\n\t" \
    "s_waitcnt lgkmcnt(5)\n\t" \
    "v_mfma_f32_16x16x32_bf16 v[188:191], v[36:39], v[168:171], v[188:191]\n\t" \
    "ds_read_b128 v[164:167], %[la] offset:27648\n\t" \
    "v_mul_f32 v204, %[kd], v204\n\t" \
    "v_mfma_f32_16x16x32_bf16 v[192:195], v[116:119], v[168:171], v[192:195]\n\t" \
    "v_fma_f32 v204, -v205, v205, v204\n\t" \
    "s_waitcnt lgkmcnt(5)\n\t" \
    "v_mfma_f32_16x16x32_bf16 v[188:191], v[40:43], v[172:175], v[188:191]\n\t" \
    "ds_read_b128 v[168:171], %[la] offset:4096\n\t" \
    "v_max_f32 v204, 0, v204\n\t" \
    "v_mfma_f32_16x16x32_bf16 v[192:195], v[120:123], v[172:175], v[192:195]\n\t" \
    "v_add_f32 v206, %[ke], v204\n\t" \
    "s_waitcnt lgkmcnt(5)\n\t" \
    "v_mfma_f32_16x16x32_bf16 v[188:191], v[44:47], v[176:179], v[188:191]\n\t" \
    "ds_read_b128 v[172:175], %[la] offset:12288\n\t" \
    "v_rsq_f32 v206, v206\n\t" \
    "v_mfma_f32_16x16x32_bf16 v[192:195], v[124:127], v[176:179], v[192:195]\n\t" \
    "s_waitcnt lgkmcnt(5)\n\t" \
    "v_mfma_f32_16x16x32_bf16 v[188:191], v[48:51], v[180:183], v[188:191]\n\t" \
    "ds_read_b128 v[176:179], %[la] offset:20480\n\t" \
    "v_mul_f32 v207, v204, v206\n\t" \
    "v_mfma_f32_16x16x32_bf16 v[192:195], v[128:131], v[180:183], v[192:195]\n\t" \
    "v_fma_f32 v207, v207, v206, %[ke]\n\t" \
    "s_waitcnt lgkmcnt(5)\n\t" \
    "v_mfma_f32_16x16x32_bf16 v[188:191], v[52:55], v[184:187], v[188:191]\n\t" \
    "ds_read_b128 v[180:183], %[la] offset:28672\n\t" \
    "v_rsq_f32 v207, v207\n\t" \
    "v_mfma_f32_16x16x32_bf16 v[192:195], v[132:135], v[184:187], v[192:195]\n\t" \
    "v_mul_f32 v206, v206, v207\n\t" \
    "v_mul_f32 v207, -v205, v206\n\t" \
    "s_waitcnt lgkmcnt(5)\n\t" \
    "v_mfma_f32_16x16x32_bf16 v[188:191], v[56:59], v[160:163], v[188:191]\n\t" \
    "v_mul_f32 v208, v196, v207\n\t" \
    "v_mul_f32 v209, v197, v207\n\t" \
    "v_mfma_f32_16x16x32_bf16 v[192:195], v[136:139], v[160:163], v[192:195]\n\t" \
    "v_mul_f32 v210, v198, v207\n\t" \
    "v_mul_f32 v211, v199, v207\n\t" \
    "s_waitcnt lgkmcnt(4)\n\t" \
    "v_mfma_f32_16x16x32_bf16 v[188:191], v[60:63], v[164:167], v[188:191]\n\t" \
    "v_mul_f32 v212, v200, v207\n\t" \
    "v_mul_f32 v213, v201, v207\n\t" \
    "v_mfma_f32_16x16x32_bf16 v[192:195], v[140:143], v[164:167], v[192:195]\n\t" \
    "v_mul_f32 v214, v202, v207\n\t" \
    "v_mul_f32 v215, v203, v207\n\t" \
    "s_waitcnt lgkmcnt(3)\n\t" \
    "v_mfma_f32_16x16x32_bf16 v[188:191], v[64:67], v[168:171], v[188:191]\n\t" \
    "s_cmp_lg_u32 %[sw], 0\n\t" \
    "s_cbranch_scc1 .Lts%=\n\t" \
    "v_cvt_pk_bf16_f32 v217, v204, v204\n\t" \
    "v_lshlrev_b32 v217, 16, v217\n\t" \
    "v_add_f32 v217, %[ke], v217\n\t" \
    "v_cvt_pk_bf16_f32 v217, v217, v217\n\t" \
    "v_lshlrev_b32 v217, 16, v217\n\t" \
    "v_sqrt_f32 v217, v217\n\t" \
    "v_cvt_pk_bf16_f32 v216, v205, v205\n\t" \
    "v_lshlrev_b32 v216, 16, v216\n\t" \
    "v_cvt_pk_bf16_f32 v217, v217, v217\n\t" \
    "v_lshlrev_b32 v217, 16, v217\n\t" \
    "v_rcp_f32 v217, v217\n\t" \
    "v_lshrrev_b32 v218, 1, %[l16]\n\t" \
    "v_and_b32 v218, 0x78, v218\n\t" \
    "buffer_store_dwordx2 v[216:217], v218, %[rs], %[ss] offen sc1\n\t" \
    ".Lts%=:\n\t" \
    "v_mov_b64 v[216:217], -1\n\t" \
    "v_mfma_f32_16x16x32_bf16 v[192:195], v[144:147], v[168:171], v[192:195]\n\t" \
    "v_mov_b64 v[218:219], -1\n\t" \
    "buffer_store_dwordx4 v[216:219], %[ob], %[rs], %[sa] offen sc1\n\t" \
    "s_waitcnt lgkmcnt(2)\n\t" \
    "v_mfma_f32_16x16x32_bf16 v[188:191], v[68:71], v[172:175], v[188:191]\n\t" \
    "v_mfma_f32_16x16x32_bf16 v[192:195], v[148:151], v[172:175], v[192:195]\n\t" \
    "s_waitcnt lgkmcnt(1)\n\t" \
    "v_mfma_f32_16x16x32_bf16 v[188:191], v[72:75], v[176:179], v[188:191]\n\t" \
    "v_mfma_f32_16x16x32_bf16 v[192:195], v[152:155], v[176:179], v[192:195]\n\t" \
    "s_waitcnt lgkmcnt(0)\n\t" \
    "v_mfma_f32_16x16x32_bf16 v[188:191], v[76:79], v[180:183], v[188:191]\n\t" \
    "v_mfma_f32_16x16x32_bf16 v[192:195], v[156:159], v[180:183], v[192:195]\n\t" \
    "s_setprio 2\n\t" \
    "buffer_load_dwordx4 v[232:235], %[l16], %[rs], %[n0] offen offset:0 sc1\n\t" \
    "buffer_load_dwordx4 v[236:239], %[l16], %[rs], %[n0] offen offset:1024 sc1\n\t" \
    "buffer_load_dwordx4 v[240:243], %[l16], %[rs], %[n0] offen offset:2048 sc1\n\t" \
    "buffer_load_dwordx4 v[244:247], %[l16], %[rs], %[n0] offen offset:3072 sc1\n\t" \
    "buffer_load_dwordx4 v[248:251], %[l16], %[rs], %[n1] offen offset:0 sc1\n\t" \
    ASM_ST3 \
    "v_fma_f32 v188, v188, v206, v208\n\t" \
    "v_fma_f32 v189, v189, v206, v209\n\t" \
    "v_fma_f32 v190, v190, v206, v210\n\t" \
    "v_fma_f32 v191, v191, v206, v211\n\t" \
    "v_mul_f32 v160, v188, v188\n\t" \
    "v_mul_f32 v161, v189, v189\n\t" \
    "v_mul_f32 v162, v190, v190\n\t" \
    "v_mul_f32 v163, v191, v191\n\t" \
    "v_fma_f32 v160, v160, %[k1], v220\n\t" \
    "v_fma_f32 v161, v161, %[k1], v220\n\t" \
    "v_fma_f32 v162, v162, %[k1], v220\n\t" \
    "v_fma_f32 v163, v163, %[k1], v220\n\t" \
    "v_mul_f32 v160, v188, v160\n\t" \
    "v_mul_f32 v161, v189, v161\n\t" \
    "v_mul_f32 v162, v190, v162\n\t" \
    "v_mul_f32 v163, v191, v163\n\t" \
    "v_fma_f32 v192, v192, v206, v212\n\t" \
    "v_fma_f32 v193, v193, v206, v213\n\t" \
    "v_fma_f32 v194, v194, v206, v214\n\t" \
    "v_fma_f32 v195, v195, v206, v215\n\t" \
    "v_mul_f32 v164, v192, v192\n\t" \
    "v_mul_f32 v165, v193, v193\n\t" \
    "v_mul_f32 v166, v194, v194\n\t" \
    "v_mul_f32 v167, v195, v195\n\t" \
    "v_fma_f32 v164, v164, %[k1], v220\n\t" \
    "v_fma_f32 v165, v165, %[k1], v220\n\t" \
    "v_fma_f32 v166, v166, %[k1], v220\n\t" \
    "v_fma_f32 v167, v167, %[k1], v220\n\t" \
    "v_mul_f32 v164, v192, v164\n\t" \
    "v_mul_f32 v165, v193, v165\n\t" \
    "v_mul_f32 v166, v194, v166\n\t" \
    "v_mul_f32 v167, v195, v167\n\t" \
    "v_exp_f32 v160, v160\n\t" \
    "v_exp_f32 v161, v161\n\t" \
    "v_exp_f32 v162, v162\n\t" \
    "v_exp_f32 v163, v163\n\t" \
    "v_exp_f32 v164, v164\n\t" \
    "v_exp_f32 v165, v165\n\t" \
    "v_exp_f32 v166, v166\n\t" \
    "v_exp_f32 v167, v167\n\t" \
    "v_add_f32 v160, 1.0, v160\n\t" \
    "v_add_f32 v161, 1.0, v161\n\t" \
    "v_add_f32 v162, 1.0, v162\n\t" \
    "v_add_f32 v163, 1.0, v163\n\t" \
    "v_add_f32 v164, 1.0, v164\n\t" \
    "v_add_f32 v165, 1.0, v165\n\t" \
    "v_add_f32 v166, 1.0, v166\n\t" \
    "v_add_f32 v167, 1.0, v167\n\t" \
    "v_rcp_f32 v160, v160\n\t" \
    "v_rcp_f32 v161, v161\n\t" \
    "v_rcp_f32 v162, v162\n\t" \
    "v_rcp_f32 v163, v163\n\t" \
    "v_rcp_f32 v164, v164\n\t" \
    "v_rcp_f32 v165, v165\n\t" \
    "v_rcp_f32 v166, v166\n\t" \
    "v_rcp_f32 v167, v167\n\t" \
    "v_mul_f32 v188, v188, v160\n\t" \
    "v_mul_f32 v189, v189, v161\n\t" \
    "v_mul_f32 v190, v190, v162\n\t" \
    "v_mul_f32 v191, v191, v163\n\t" \
    "v_mul_f32 v192, v192, v164\n\t" \
    "v_mul_f32 v193, v193, v165\n\t" \
    "v_mul_f32 v194, v194, v166\n\t" \
    "v_mul_f32 v195, v195, v167\n\t" \
    "v_cvt_pk_bf16_f32 v188, v188, v189\n\t" \
    "v_cvt_pk_bf16_f32 v189, v190, v191\n\t" \
    "v_cvt_pk_bf16_f32 v190, v192, v193\n\t" \
    "v_cvt_pk_bf16_f32 v191, v194, v195\n\t" \
    "v_min_u32 v188, -2, v188\n\t" \
    "v_min_u32 v189, -2, v189\n\t" \
    "v_min_u32 v190, -2, v190\n\t" \
    "v_min_u32 v191, -2, v191\n\t" \
    "v_mov_b32 v216, 0x3f803f80\n\t" \
    "v_mov_b32 v217, 0x3f803f80\n\t" \
    "v_mov_b32 v218, 0x3f803f80\n\t" \
    "v_mov_b32 v219, 0x3f803f80\n\t" \
    "v_permlane16_swap_b32 v188, v190\n\t" \
    "v_permlane16_swap_b32 v189, v191\n\t" \
    "s_waitcnt vmcnt(6)\n\t" \
    "buffer_store_dwordx4 v[188:191], %[oa], %[rs], %[so] offen sc1\n\t" \
    "s_nop 1\n\t" \
    "s_setprio 0\n\t"
#define ASM_WT1_HI \
    "s_setprio 1\n\t" \
    "s_barrier\n\t" ASM_ST \
    "ds_read_b128 v[160:163], %[la] offset:0\n\t" \
    "ds_read_b128 v[164:167], %[la] offset:1024\n\t" \
    "ds_read_b128 v[168:171], %[la] offset:2048\n\t" \
    "ds_read2st64_b32 v[204:205], %[sr] offset0:20 offset1:24\n\t" \
    "ds_read_b128 v[172:175], %[la] offset:3072\n\t" \
    "ds_read_b128 v[176:179], %[la] offset:8192\n\t" \
    "ds_read_b128 v[180:183], %[la] offset:9216\n\t" \
    "s_waitcnt lgkmcnt(6)\n\t" \
    "v_mfma_f32_16x16x32_bf16 v[188:191], v[0:3], v[160:163], 0\n\t" \
    "ds_read_b128 v[184:187], %[la] offset:10240\n\t" \
    "v_mfma_f32_16x16x32_bf16 v[192:195], v[80:83], v[160:163], 0\n\t" \
    "s_waitcnt lgkmcnt(6)\n\t" \
    "v_mfma_f32_16x16x32_bf16 v[188:191], v[4:7], v[164:167], v[188:191]\n\t" \
    "ds_read_b128 v[160:163], %[la] offset:11264\n\t" \
    "v_mfma_f32_16x16x32_bf16 v[192:195], v[84:87], v[164:167], v[192:195]\n\t" \
    "s_waitcnt lgkmcnt(6)\n\t" \
    "v_mfma_f32_16x16x32_bf16 v[188:191], v[8:11], v[168:171], v[188:191]\n\t" \
    "ds_read_b128 v[164:167], %[la] offset:16384\n\t" \
    "s_waitcnt lgkmcnt(6)\n\t" \
    "v_mfma_f32_16x16x32_bf16 v[192:195], v[88:91], v[168:171], v[192:195]\n\t" \
    "v_permlane16_swap_b32 v204, v205\n\t" \
    "s_waitcnt lgkmcnt(5)\n\t" \
    "v_mfma_f32_16x16x32_bf16 v[188:191], v[12:15], v[172:175], v[188:191]\n\t" \
    "ds_read_b128 v[168:171], %[la] offset:17408\n\t" \
    "v_mfma_f32_16x16x32_bf16 v[192:195], v[92:95], v[172:175], v[192:195]\n\t" \
    "v_add_f32 v206, v204, v205\n\t" \
    "s_waitcnt lgkmcnt(5)\n\t" \
    "v_mfma_f32_16x16x32_bf16 v[188:191], v[16:19], v[176:179], v[188:191]\n\t" \
    "ds_read_b128 v[172:175], %[la] offset:18432\n\t" \
    "v_add_f32 v207, v204, v205\n\t" \
    "v_mfma_f32_16x16x32_bf16 v[192:195], v[96:99], v[176:179], v[192:195]\n\t" \
    "s_waitcnt lgkmcnt(5)\n\t" \
    "v_mfma_f32_16x16x32_bf16 v[188:191], v[20:23], v[180:183], v[188:191]\n\t" \
    "ds_read_b128 v[176:179], %[la] offset:19456\n\t" \
    "v_permlane32_swap_b32 v206, v207\n\t" \
    "v_mfma_f32_16x16x32_bf16 v[192:195], v[100:103], v[180:183], v[192:195]\n\t" \
    "s_waitcnt lgkmcnt(5)\n\t" \
    "v_mfma_f32_16x16x32_bf16 v[188:191], v[24:27], v[184:187], v[188:191]\n\t" \
    "ds_read_b128 v[180:183], %[la] offset:24576\n\t" \
    "v_add_f32 v204, v206, v207\n\t" \
    "v_mfma_f32_16x16x32_bf16 v[192:195], v[104:107], v[184:187], v[192:195]\n\t" \
    "v_add_f32 v205, v206, v207\n\t" \
    "s_waitcnt lgkmcnt(5)\n\t" \
    "v_mfma_f32_16x16x32_bf16 v[188:191], v[28:31], v[160:163], v[188:191]\n\t" \
    "ds_read_b128 v[184:187], %[la] offset:25600\n\t" \
    "v_mfma_f32_16x16x32_bf16 v[192:195], v[108:111], v[160:163], v[192:195]\n\t" \
    "v_permlane16_swap_b32 v204, v205\n\t" \
    "s_waitcnt lgkmcnt(5)\n\t" \
    "v_mfma_f32_16x16x32_bf16 v[188:191], v[32:35], v[164:167], v[188:191]\n\t" \
    "ds_read_b128 v[160:163], %[la] offset:26624\n\t" \
    "v_mfma_f32_16x16x32_bf16 v[192:195], v[112:115], v[164:167], v[192:195]\n\t" \
    "v_mul_f32 v205, %[kd], v205\n\t" \
    "s_waitcnt lgkmcnt(5)\n\t" \
    "v_mfma_f32_16x16x32_bf16 v[188:191], v[36:39], v[168:171], v[188:191]\n\t" \
    "ds_read_b128 v[164:167], %[la] offset:27648\n\t" \
    "v_mul_f32 v204, %[kd], v204\n\t" \
    "v_mfma_f32_16x16x32_bf16 v[192:195], v[116:119], v[168:171], v[192:195]\n\t" \
    "v_fma_f32 v204, -v205, v205, v204\n\t" \
    "s_waitcnt lgkmcnt(5)\n\t" \
    "v_mfma_f32_16x16x32_bf16 v[188:191], v[40:43], v[172:175], v[188:191]\n\t" \
    "ds_read_b128 v[168:171], %[la] offset:4096\n\t" \
    "v_max_f32 v204, 0, v204\n\t" \
    "v_mfma_f32_16x16x32_bf16 v[192:195], v[120:123], v[172:175], v[192:195]\n\t" \
    "v_add_f32 v206, %[ke], v204\n\t" \
    "s_waitcnt lgkmcnt(5)\n\t" \
    "v_mfma_f32_16x16x32_bf16 v[188:191], v[44:47], v[176:179], v[188:191]\n\t" \
    "ds_read_b128 v[172:175], %[la] offset:12288\n\t" \
    "v_rsq_f32 v206, v206\n\t" \
    "v_mfma_f32_16x16x32_bf16 v[192:195], v[124:127], v[176:179], v[192:195]\n\t" \
    "s_waitcnt lgkmcnt(5)\n\t" \
    "v_mfma_f32_16x16x32_bf16 v[188:191], v[48:51], v[180:183], v[188:191]\n\t" \
    "ds_read_b128 v[176:179], %[la] offset:20480\n\t" \
    "v_mul_f32 v207, v204, v206\n\t" \
    "v_mfma_f32_16x16x32_bf16 v[192:195], v[128:131], v[180:183], v[192:195]\n\t" \
    "v_fma_f32 v207, v207, v206, %[ke]\n\t" \
    "s_waitcnt lgkmcnt(5)\n\t" \
    "v_mfma_f32_16x16x32_bf16 v[188:191], v[52:55], v[184:187], v[188:191]\n\t" \
    "ds_read_b128 v[180:183], %[la] offset:28672\n\t" \
    "v_rsq_f32 v207, v207\n\t" \
    "v_mfma_f32_16x16x32_bf16 v[192:195], v[132:135], v[184:187], v[192:195]\n\t" \
    "v_mul_f32 v206, v206, v207\n\t" \
    "v_mul_f32 v207, -v205, v206\n\t" \
    "s_waitcnt lgkmcnt(5)\n\t" \
    "v_mfma_f32_16x16x32_bf16 v[188:191], v[56:59], v[160:163], v[188:191]\n\t" \
    "v_mul_f32 v208, v196, v207\n\t" \
    "v_mul_f32 v209, v197, v207\n\t" \
    "v_mfma_f32_16x16x32_bf16 v[192:195], v[136:139], v[160:163], v[192:195]\n\t" \
    "v_mul_f32 v210, v198, v207\n\t" \
    "v_mul_f32 v211, v199, v207\n\t" \
    "s_waitcnt lgkmcnt(4)\n\t" \
    "v_mfma_f32_16x16x32_bf16 v[188:191], v[60:63], v[164:167], v[188:191]\n\t" \
    "v_mul_f32 v212, v200, v207\n\t" \
    "v_mul_f32 v213, v201, v207\n\t" \
    "v_mfma_f32_16x16x32_bf16 v[192:195], v[140:143], v[164:167], v[192:195]\n\t" \
    "v_mul_f32 v214, v202, v207\n\t" \
    "v_mul_f32 v215, v203, v207\n\t" \
    "s_waitcnt lgkmcnt(3)\n\t" \
    "v_mfma_f32_16x16x32_bf16 v[188:191], v[64:67], v[168:171], v[188:191]\n\t" \
    "v_mov_b64 v[216:217], -1\n\t" \
    "v_mov_b64 v[218:219], -1\n\t" \
    "v_mfma_f32_16x16x32_bf16 v[192:195], v[144:147], v[168:171], v[192:195]\n\t" \
    "buffer_store_dwordx4 v[216:219], %[ob], %[rs], %[sa] offen sc1\n\t" \
    "s_waitcnt lgkmcnt(2)\n\t" \
    "v_mfma_f32_16x16x32_bf16 v[188:191], v[68:71], v[172:175], v[188:191]\n\t" \
    "v_mfma_f32_16x16x32_bf16 v[192:195], v[148:151], v[172:175], v[192:195]\n\t" \
    "s_waitcnt lgkmcnt(1)\n\t" \
    "v_mfma_f32_16x16x32_bf16 v[188:191], v[72:75], v[176:179], v[188:191]\n\t" \
    "v_mfma_f32_16x16x32_bf16 v[192:195], v[152:155], v[176:179], v[192:195]\n\t" \
    "s_waitcnt lgkmcnt(0)\n\t" \
    "v_mfma_f32_16x16x32_bf16 v[188:191], v[76:79], v[180:183], v[188:191]\n\t" \
    "v_mfma_f32_16x16x32_bf16 v[192:195], v[156:159], v[180:183], v[192:195]\n\t" \
    ASM_ST3 \
    "s_nop 4\n\t" \
    "v_fma_f32 v188, v188, v206, v208\n\t" \
    "v_fma_f32 v189, v189, v206, v209\n\t" \
    "v_fma_f32 v190, v190, v206, v210\n\t" \
    "v_fma_f32 v191, v191, v206, v211\n\t" \
    "v_mul_f32 v160, v188, v188\n\t" \
    "v_mul_f32 v161, v189, v189\n\t" \
    "v_mul_f32 v162, v190, v190\n\t" \
    "v_mul_f32 v163, v191, v191\n\t" \
    "v_fma_f32 v160, v160, %[k1], v220\n\t" \
    "v_fma_f32 v161, v161, %[k1], v220\n\t" \
    "v_fma_f32 v162, v162, %[k1], v220\n\t" \
    "v_fma_f32 v163, v163, %[k1], v220\n\t" \
    "v_mul_f32 v160, v188, v160\n\t" \
    "v_mul_f32 v161, v189, v161\n\t" \
    "v_mul_f32 v162, v190, v162\n\t" \
    "v_mul_f32 v163, v191, v163\n\t" \
    "v_fma_f32 v192, v192, v206, v212\n\t" \
    "v_fma_f32 v193, v193, v206, v213\n\t" \
    "v_fma_f32 v194, v194, v206, v214\n\t" \
    "v_fma_f32 v195, v195, v206, v215\n\t" \
    "v_mul_f32 v164, v192, v192\n\t" \
    "v_mul_f32 v165, v193, v193\n\t" \
    "v_mul_f32 v166, v194, v194\n\t" \
    "v_mul_f32 v167, v195, v195\n\t" \
    "v_fma_f32 v164, v164, %[k1], v220\n\t" \
    "v_fma_f32 v165, v165, %[k1], v220\n\t" \
    "v_fma_f32 v166, v166, %[k1], v220\n\t" \
    "v_fma_f32 v167, v167, %[k1], v220\n\t" \
    "v_mul_f32 v164, v192, v164\n\t" \
    "v_mul_f32 v165, v193, v165\n\t" \
    "v_mul_f32 v166, v194, v166\n\t" \
    "v_mul_f32 v167, v195, v167\n\t" \
    "v_exp_f32 v160, v160\n\t" \
    "v_exp_f32 v161, v161\n\t" \
    "v_exp_f32 v162, v162\n\t" \
    "v_exp_f32 v163, v163\n\t" \
    "v_exp_f32 v164, v164\n\t" \
    "v_exp_f32 v165, v165\n\t" \
    "v_exp_f32 v166, v166\n\t" \
    "v_exp_f32 v167, v167\n\t" \
    "v_add_f32 v160, 1.0, v160\n\t" \
    "v_add_f32 v161, 1.0, v161\n\t" \
    "v_add_f32 v162, 1.0, v162\n\t" \
    "v_add_f32 v163, 1.0, v163\n\t" \
    "v_add_f32 v164, 1.0, v164\n\t" \
    "v_add_f32 v165, 1.0, v165\n\t" \
    "v_add_f32 v166, 1.0, v166\n\t" \
    "v_add_f32 v167, 1.0, v167\n\t" \
    "v_rcp_f32 v160, v160\n\t" \
    "v_rcp_f32 v161, v161\n\t" \
    "v_rcp_f32 v162, v162\n\t" \
    "v_rcp_f32 v163, v163\n\t" \
    "v_rcp_f32 v164, v164\n\t" \
    "v_rcp_f32 v165, v165\n\t" \
    "v_rcp_f32 v166, v166\n\t" \
    "v_rcp_f32 v167, v167\n\t" \
    "v_mul_f32 v188, v188, v160\n\t" \
    "v_mul_f32 v189, v189, v161\n\t" \
    "v_mul_f32 v190, v190, v162\n\t" \
    "v_mul_f32 v191, v191, v163\n\t" \
    "v_mul_f32 v192, v192, v164\n\t" \
    "v_mul_f32 v193, v193, v165\n\t" \
    "v_mul_f32 v194, v194, v166\n\t" \
    "v_mul_f32 v195, v195, v167\n\t" \
    "v_cvt_pk_bf16_f32 v188, v188, v189\n\t" \
    "v_cvt_pk_bf16_f32 v189, v190, v191\n\t" \
    "v_cvt_pk_bf16_f32 v190, v192, v193\n\t" \
    "v_cvt_pk_bf16_f32 v191, v194, v195\n\t" \
    "v_min_u32 v188, -2, v188\n\t" \
    "v_min_u32 v189, -2, v189\n\t" \
    "v_min_u32 v190, -2, v190\n\t" \
    "v_min_u32 v191, -2, v191\n\t" \
    "s_nop 1\n\t" \
    "v_permlane16_swap_b32 v188, v190\n\t" \
    "v_permlane16_swap_b32 v189, v191\n\t" \
    "s_waitcnt vmcnt(1)\n\t" \
    "buffer_store_dwordx4 v[188:191], %[oa], %[rs], %[so] offen sc1\n\t" \
    "s_nop 1\n\t"
#define ASM_WT2_LO \
    "s_branch .Lt0%=\n\t" \
    ".Ltr%=:\n\t" \
    "s_cmp_gt_i32 %[n], 0\n\t" \
    "s_cbranch_scc0 .Ltg%=\n\t" \
    ".Lfw%=:\n\t" \
    "s_mov_b32 %[a], 0\n\t" \
    "v_max_u32 v252, v232, v233\n\t" \
    "v_max3_u32 v252, v234, v235, v252\n\t" \
    "v_cmp_eq_u32 vcc, -1, v252\n\t" \
    "s_cbranch_vccz .Lgw0_%=\n\t" \
    "buffer_load_dwordx4 v[232:235], %[l16], %[rs], %[i0] offen offset:0 sc1\n\t" \
    "s_mov_b32 %[a], 1\n\t" \
    ".Lgw0_%=:\n\t" \
    "v_max_u32 v252, v236, v237\n\t" \
    "v_max3_u32 v252, v238, v239, v252\n\t" \
    "v_cmp_eq_u32 vcc, -1, v252\n\t" \
    "s_cbranch_vccz .Lgw1_%=\n\t" \
    "buffer_load_dwordx4 v[236:239], %[l16], %[rs], %[i0] offen offset:1024 sc1\n\t" \
    "s_mov_b32 %[a], 1\n\t" \
    ".Lgw1_%=:\n\t" \
    "v_max_u32 v252, v240, v241\n\t" \
    "v_max3_u32 v252, v242, v243, v252\n\t" \
    "v_cmp_eq_u32 vcc, -1, v252\n\t" \
    "s_cbranch_vccz .Lgw2_%=\n\t" \
    "buffer_load_dwordx4 v[240:243], %[l16], %[rs], %[i0] offen offset:2048 sc1\n\t" \
    "s_mov_b32 %[a], 1\n\t" \
    ".Lgw2_%=:\n\t" \
    "v_max_u32 v252, v244, v245\n\t" \
    "v_max3_u32 v252, v246, v247, v252\n\t" \
    "v_cmp_eq_u32 vcc, -1, v252\n\t" \
    "s_cbranch_vccz .Lgw3_%=\n\t" \
    "buffer_load_dwordx4 v[244:247], %[l16], %[rs], %[i0] offen offset:3072 sc1\n\t" \
    "s_mov_b32 %[a], 1\n\t" \
    ".Lgw3_%=:\n\t" \
    "v_max_u32 v252, v248, v249\n\t" \
    "v_max3_u32 v252, v250, v251, v252\n\t" \
    "v_cmp_eq_u32 vcc, -1, v252\n\t" \
    "s_cbranch_vccz .Lgw4_%=\n\t" \
    "buffer_load_dwordx4 v[248:251], %[l16], %[rs], %[i1] offen offset:0 sc1\n\t" \
    "s_mov_b32 %[a], 1\n\t" \
    ".Lgw4_%=:\n\t" \
    "s_cmp_eq_u32 %[a], 0\n\t" \
    "s_cbranch_scc1 .Lhw%=\n\t" \
    "s_waitcnt vmcnt(20)\n\t" \
    ".Liw%=:\n\t" \
    "v_max_u32 v252, v232, v233\n\t" \
    "v_max3_u32 v252, v234, v235, v252\n\t" \
    "v_max3_u32 v252, v236, v237, v252\n\t" \
    "v_max3_u32 v252, v238, v239, v252\n\t" \
    "v_max3_u32 v252, v240, v241, v252\n\t" \
    "v_max3_u32 v252, v242, v243, v252\n\t" \
    "v_max3_u32 v252, v244, v245, v252\n\t" \
    "v_max3_u32 v252, v246, v247, v252\n\t" \
    "v_max3_u32 v252, v248, v249, v252\n\t" \
    "v_max3_u32 v252, v250, v251, v252\n\t" \
    "v_cmp_eq_u32 vcc, -1, v252\n\t" \
    "s_cbranch_vccz .Lhw%=\n\t" \
    "s_sleep 1\n\t" \
    "v_max_u32 v252, v232, v233\n\t" \
    "v_max3_u32 v252, v234, v235, v252\n\t" \
    "v_max3_u32 v252, v236, v237, v252\n\t" \
    "v_max3_u32 v252, v238, v239, v252\n\t" \
    "v_max3_u32 v252, v240, v241, v252\n\t" \
    "v_max3_u32 v252, v242, v243, v252\n\t" \
    "v_max3_u32 v252, v244, v245, v252\n\t" \
    "v_max3_u32 v252, v246, v247, v252\n\t" \
    "v_max3_u32 v252, v248, v249, v252\n\t" \
    "v_max3_u32 v252, v250, v251, v252\n\t" \
    "v_cmp_eq_u32 vcc, -1, v252\n\t" \
    "s_cbranch_vccz .Lhw%=\n\t" \
    "s_sleep 1\n\t" \
    "v_max_u32 v252, v232, v233\n\t" \
    "v_max3_u32 v252, v234, v235, v252\n\t" \
    "v_max3_u32 v252, v236, v237, v252\n\t" \
    "v_max3_u32 v252, v238, v239, v252\n\t" \
    "v_max3_u32 v252, v240, v241, v252\n\t" \
    "v_max3_u32 v252, v242, v243, v252\n\t" \
    "v_max3_u32 v252, v244, v245, v252\n\t" \
    "v_max3_u32 v252, v246, v247, v252\n\t" \
    "v_max3_u32 v252, v248, v249, v252\n\t" \
    "v_max3_u32 v252, v250, v251, v252\n\t" \
    "v_cmp_eq_u32 vcc, -1, v252\n\t" \
    "s_cbranch_vccz .Lhw%=\n\t" \
    "s_sleep 1\n\t" \
    "v_max_u32 v252, v232, v233\n\t" \
    "v_max3_u32 v252, v234, v235, v252\n\t" \
    "v_max3_u32 v252, v236, v237, v252\n\t" \
    "v_max3_u32 v252, v238, v239, v252\n\t" \
    "v_max3_u32 v252, v240, v241, v252\n\t" \
    "v_max3_u32 v252, v242, v243, v252\n\t" \
    "v_max3_u32 v252, v244, v245, v252\n\t" \
    "v_max3_u32 v252, v246, v247, v252\n\t" \
    "v_max3_u32 v252, v248, v249, v252\n\t" \
    "v_max3_u32 v252, v250, v251, v252\n\t" \
    "v_cmp_eq_u32 vcc, -1, v252\n\t" \
    "s_cbranch_vccz .Lhw%=\n\t" \
    "s_sleep 1\n\t" \
    "v_max_u32 v252, v232, v233\n\t" \
    "v_max3_u32 v252, v234, v235, v252\n\t" \
    "v_max3_u32 v252, v236, v237, v252\n\t" \
    "v_max3_u32 v252, v238, v239, v252\n\t" \
    "v_max3_u32 v252, v240, v241, v252\n\t" \
    "v_max3_u32 v252, v242, v243, v252\n\t" \
    "v_max3_u32 v252, v244, v245, v252\n\t" \
    "v_max3_u32 v252, v246, v247, v252\n\t" \
    "v_max3_u32 v252, v248, v249, v252\n\t" \
    "v_max3_u32 v252, v250, v251, v252\n\t" \
    "v_cmp_eq_u32 vcc, -1, v252\n\t" \
    "s_cbranch_vccz .Lhw%=\n\t" \
    "s_sleep 1\n\t" \
    "v_max_u32 v252, v232, v233\n\t" \
    "v_max3_u32 v252, v234, v235, v252\n\t" \
    "v_max3_u32 v252, v236, v237, v252\n\t" \
    "v_max3_u32 v252, v238, v239, v252\n\t" \
    "v_max3_u32 v252, v240, v241, v252\n\t" \
    "v_max3_u32 v252, v242, v243, v252\n\t" \
    "v_max3_u32 v252, v244, v245, v252\n\t" \
    "v_max3_u32 v252, v246, v247, v252\n\t" \
    "v_max3_u32 v252, v248, v249, v252\n\t" \
    "v_max3_u32 v252, v250, v251, v252\n\t" \
    "v_cmp_eq_u32 vcc, -1, v252\n\t" \
    "s_cbranch_vccz .Lhw%=\n\t" \
    "s_sleep 1\n\t" \
    "s_sub_u32 %[n], %[n], 1\n\t" \
    "s_cmp_gt_i32 %[n], 0\n\t" \
    "s_cbranch_scc1 .Lfw%=\n\t" \
    ".Lhw%=:\n\t" \
    "s_branch .Lta%=\n\t" \
    ".Lt0%=:\n\t" \
    "s_waitcnt vmcnt(1)\n\t" \
    ".Lta%=:\n\t" \
    "ds_write_b128 %[lw], v[232:235] offset:0\n\t" \
    "v_max_u32 v252, v232, v233\n\t" \
    "v_max3_u32 v252, v234, v235, v252\n\t" \
    "ds_write_b128 %[lw], v[236:239] offset:1024\n\t" \
    "v_max3_u32 v252, v236, v237, v252\n\t" \
    "v_max3_u32 v252, v238, v239, v252\n\t" \
    "ds_write_b128 %[lw], v[240:243] offset:2048\n\t" \
    "v_max3_u32 v252, v240, v241, v252\n\t" \
    "v_max3_u32 v252, v242, v243, v252\n\t" \
    "ds_write_b128 %[lw], v[244:247] offset:3072\n\t" \
    "v_max3_u32 v252, v244, v245, v252\n\t" \
    "v_max3_u32 v252, v246, v247, v252\n\t" \
    "ds_write_b128 %[lw], v[248:251] offset:4096\n\t" \
    "v_max3_u32 v252, v248, v249, v252\n\t" \
    "v_max3_u32 v252, v250, v251, v252\n\t" \
    "v_cmp_eq_u32 vcc, -1, v252\n\t" \
    "s_cbranch_vccnz .Ltr%=\n\t" \
    ".Ltg%=:\n\t" \
    ASM_ST2 \
    "s_waitcnt lgkmcnt(0)\n\t" \
    "s_setprio 2\n\t" \
    "s_barrier\n\t" ASM_ST \
    "ds_read_b128 v[160:163], %[la] offset:0\n\t" \
    "ds_read_b128 v[164:167], %[la] offset:1024\n\t" \
    "ds_read_b128 v[168:171], %[la] offset:2048\n\t" \
    "ds_read_b128 v[172:175], %[la] offset:3072\n\t" \
    "ds_read_b128 v[176:179], %[la] offset:8192\n\t" \
    "ds_read_b128 v[180:183], %[la] offset:9216\n\t" \
    "s_waitcnt lgkmcnt(5)\n\t" \
    "v_mfma_f32_16x16x32_bf16 v[188:191], v[0:3], v[160:163], 0\n\t" \
    "ds_read_b128 v[184:187], %[la] offset:10240\n\t" \
    "buffer_load_dwordx2 v[196:197], %[ro], %[rs], %[sx] offen sc1\n\t" \
    "v_mfma_f32_16x16x32_bf16 v[192:195], v[80:83], v[160:163], 0\n\t" \
    "buffer_load_dwordx2 v[198:199], %[ro], %[rs], %[sx] offen offset:512 sc1\n\t" \
    "s_waitcnt lgkmcnt(5)\n\t" \
    "v_mfma_f32_16x16x32_bf16 v[188:191], v[4:7], v[164:167], v[188:191]\n\t" \
    "ds_read_b128 v[160:163], %[la] offset:11264\n\t" \
    "v_lshrrev_b32 v204, 1, %[l16]\n\t" \
    "v_mfma_f32_16x16x32_bf16 v[192:195], v[84:87], v[164:167], v[192:195]\n\t" \
    "v_and_b32 v204, 0x78, v204\n\t" \
    "s_waitcnt lgkmcnt(5)\n\t" \
    "v_mfma_f32_16x16x32_bf16 v[188:191], v[8:11], v[168:171], v[188:191]\n\t" \
    "ds_read_b128 v[164:167], %[la] offset:16384\n\t" \
    "buffer_load_dwordx2 v[200:201], v204, %[rs], %[ss] offen sc1\n\t" \
    "v_mfma_f32_16x16x32_bf16 v[192:195], v[88:91], v[168:171], v[192:195]\n\t" \
    "v_mov_b64 v[216:217], -1\n\t" \
    "s_waitcnt lgkmcnt(5)\n\t" \
    "v_mfma_f32_16x16x32_bf16 v[188:191], v[12:15], v[172:175], v[188:191]\n\t" \
    "ds_read_b128 v[168:171], %[la] offset:17408\n\t" \
    "v_mov_b64 v[218:219], -1\n\t" \
    "v_mfma_f32_16x16x32_bf16 v[192:195], v[92:95], v[172:175], v[192:195]\n\t" \
    "buffer_store_dwordx4 v[216:219], %[l16], %[rs], %[sa] offen sc1\n\t" \
    "s_waitcnt lgkmcnt(5)\n\t" \
    "v_mfma_f32_16x16x32_bf16 v[188:191], v[16:19], v[176:179], v[188:191]\n\t" \
    "ds_read_b128 v[172:175], %[la] offset:18432\n\t" \
    "v_mfma_f32_16x16x32_bf16 v[192:195], v[96:99], v[176:179], v[192:195]\n\t" \
    "s_waitcnt lgkmcnt(5)\n\t" \
    "v_mfma_f32_16x16x32_bf16 v[188:191], v[20:23], v[180:183], v[188:191]\n\t" \
    "ds_read_b128 v[176:179], %[la] offset:19456\n\t" \
    "v_mfma_f32_16x16x32_bf16 v[192:195], v[100:103], v[180:183], v[192:195]\n\t" \
    "s_waitcnt lgkmcnt(5)\n\t" \
    "v_mfma_f32_16x16x32_bf16 v[188:191], v[24:27], v[184:187], v[188:191]\n\t" \
    "ds_read_b128 v[180:183], %[la] offset:24576\n\t" \
    "v_mfma_f32_16x16x32_bf16 v[192:195], v[104:107], v[184:187], v[192:195]\n\t" \
    "s_waitcnt lgkmcnt(5)\n\t" \
    "v_mfma_f32_16x16x32_bf16 v[188:191], v[28:31], v[160:163], v[188:191]\n\t" \
    "ds_read_b128 v[184:187], %[la] offset:25600\n\t" \
    "v_mfma_f32_16x16x32_bf16 v[192:195], v[108:111], v[160:163], v[192:195]\n\t" \
    "s_waitcnt lgkmcnt(5)\n\t" \
    "v_mfma_f32_16x16x32_bf16 v[188:191], v[32:35], v[164:167], v[188:191]\n\t" \
    "ds_read_b128 v[160:163], %[la] offset:26624\n\t" \
    "v_mfma_f32_16x16x32_bf16 v[192:195], v[112:115], v[164:167], v[192:195]\n\t" \
    "s_waitcnt lgkmcnt(5)\n\t" \
    "v_mfma_f32_16x16x32_bf16 v[188:191], v[36:39], v[168:171], v[188:191]\n\t" \
    "ds_read_b128 v[164:167], %[la] offset:27648\n\t" \
    "v_mfma_f32_16x16x32_bf16 v[192:195], v[116:119], v[168:171], v[192:195]\n\t" \
    "s_waitcnt lgkmcnt(5)\n\t" \
    "v_mfma_f32_16x16x32_bf16 v[188:191], v[40:43], v[172:175], v[188:191]\n\t" \
    "ds_read_b128 v[168:171], %[la] offset:4096\n\t" \
    "v_mfma_f32_16x16x32_bf16 v[192:195], v[120:123], v[172:175], v[192:195]\n\t" \
    "s_waitcnt lgkmcnt(5)\n\t" \
    "v_mfma_f32_16x16x32_bf16 v[188:191], v[44:47], v[176:179], v[188:191]\n\t" \
    "ds_read_b128 v[172:175], %[la] offset:12288\n\t" \
    "v_mfma_f32_16x16x32_bf16 v[192:195], v[124:127], v[176:179], v[192:195]\n\t" \
    "s_waitcnt lgkmcnt(5)\n\t" \
    "v_mfma_f32_16x16x32_bf16 v[188:191], v[48:51], v[180:183], v[188:191]\n\t" \
    "ds_read_b128 v[176:179], %[la] offset:20480\n\t" \
    "v_mfma_f32_16x16x32_bf16 v[192:195], v[128:131], v[180:183], v[192:195]\n\t" \
    "s_waitcnt lgkmcnt(5)\n\t" \
    "v_mfma_f32_16x16x32_bf16 v[188:191], v[52:55], v[184:187], v[188:191]\n\t" \
    "ds_read_b128 v[180:183], %[la] offset:28672\n\t" \
    "v_mfma_f32_16x16x32_bf16 v[192:195], v[132:135], v[184:187], v[192:195]\n\t" \
    "s_waitcnt lgkmcnt(5)\n\t" \
    "v_mfma_f32_16x16x32_bf16 v[188:191], v[56:59], v[160:163], v[188:191]\n\t" \
    "v_mfma_f32_16x16x32_bf16 v[192:195], v[136:139], v[160:163], v[192:195]\n\t" \
    "s_waitcnt lgkmcnt(4)\n\t" \
    "v_mfma_f32_16x16x32_bf16 v[188:191], v[60:63], v[164:167], v[188:191]\n\t" \
    "v_mfma_f32_16x16x32_bf16 v[192:195], v[140:143], v[164:167], v[192:195]\n\t" \
    "s_waitcnt lgkmcnt(3)\n\t" \
    "v_mfma_f32_16x16x32_bf16 v[188:191], v[64:67], v[168:171], v[188:191]\n\t" \
    "v_mfma_f32_16x16x32_bf16 v[192:195], v[144:147], v[168:171], v[192:195]\n\t" \
    "s_waitcnt lgkmcnt(2)\n\t" \
    "v_mfma_f32_16x16x32_bf16 v[188:191], v[68:71], v[172:175], v[188:191]\n\t" \
    "v_mfma_f32_16x16x32_bf16 v[192:195], v[148:151], v[172:175], v[192:195]\n\t" \
    "s_waitcnt lgkmcnt(1)\n\t" \
    "v_mfma_f32_16x16x32_bf16 v[188:191], v[72:75], v[176:179], v[188:191]\n\t" \
    "v_mfma_f32_16x16x32_bf16 v[192:195], v[152:155], v[176:179], v[192:195]\n\t" \
    "s_waitcnt lgkmcnt(0)\n\t" \
    "v_mfma_f32_16x16x32_bf16 v[188:191], v[76:79], v[180:183], v[188:191]\n\t" \
    "v_mfma_f32_16x16x32_bf16 v[192:195], v[156:159], v[180:183], v[192:195]\n\t" \
    "s_setprio 2\n\t" \
    "buffer_load_dwordx4 v[232:235], %[l16], %[rs], %[n0] offen offset:0 sc1\n\t" \
    "buffer_load_dwordx4 v[236:239], %[l16], %[rs], %[n0] offen offset:1024 sc1\n\t" \
    "buffer_load_dwordx4 v[240:243], %[l16], %[rs], %[n0] offen offset:2048 sc1\n\t" \
    "buffer_load_dwordx4 v[244:247], %[l16], %[rs], %[n0] offen offset:3072 sc1\n\t" \
    "buffer_load_dwordx4 v[248:251], %[l16], %[rs], %[n1] offen offset:0 sc1\n\t" \
    ASM_ST3 \
    "s_waitcnt vmcnt(6)\n\t" \
    "v_lshlrev_b32 v160, 16, v196\n\t" \
    "v_and_b32 v161, 0xffff0000, v196\n\t" \
    "v_lshlrev_b32 v162, 16, v197\n\t" \
    "v_and_b32 v163, 0xffff0000, v197\n\t" \
    "v_lshlrev_b32 v164, 16, v198\n\t" \
    "v_and_b32 v165, 0xffff0000, v198\n\t" \
    "v_lshlrev_b32 v166, 16, v199\n\t" \
    "v_and_b32 v167, 0xffff0000, v199\n\t" \
    "v_sub_f32 v160, v160, v200\n\t" \
    "v_sub_f32 v161, v161, v200\n\t" \
    "v_sub_f32 v162, v162, v200\n\t" \
    "v_sub_f32 v163, v163, v200\n\t" \
    "v_sub_f32 v164, v164, v200\n\t" \
    "v_sub_f32 v165, v165, v200\n\t" \
    "v_sub_f32 v166, v166, v200\n\t" \
    "v_sub_f32 v167, v167, v200\n\t" \
    "v_mul_f32 v160, v160, v201\n\t" \
    "v_mul_f32 v161, v161, v201\n\t" \
    "v_mul_f32 v162, v162, v201\n\t" \
    "v_mul_f32 v163, v163, v201\n\t" \
    "v_mul_f32 v164, v164, v201\n\t" \
    "v_mul_f32 v165, v165, v201\n\t" \
    "v_mul_f32 v166, v166, v201\n\t" \
    "v_mul_f32 v167, v167, v201\n\t" \
    "v_cvt_pk_bf16_f32 v160, v160, v161\n\t" \
    "v_cvt_pk_bf16_f32 v162, v162, v163\n\t" \
    "v_cvt_pk_bf16_f32 v164, v164, v165\n\t" \
    "v_cvt_pk_bf16_f32 v166, v166, v167\n\t" \
    "v_and_b32 v161, 0xffff0000, v160\n\t" \
    "v_lshlrev_b32 v160, 16, v160\n\t" \
    "v_and_b32 v163, 0xffff0000, v162\n\t" \
    "v_lshlrev_b32 v162, 16, v162\n\t" \
    "v_and_b32 v165, 0xffff0000, v164\n\t" \
    "v_lshlrev_b32 v164, 16, v164\n\t" \
    "v_and_b32 v167, 0xffff0000, v166\n\t" \
    "v_lshlrev_b32 v166, 16, v166\n\t" \
    "v_add_f32 v188, v188, v160\n\t" \
    "v_add_f32 v189, v189, v161\n\t" \
    "v_add_f32 v190, v190, v162\n\t" \
    "v_add_f32 v191, v191, v163\n\t" \
    "v_add_f32 v192, v192, v164\n\t" \
    "v_add_f32 v193, v193, v165\n\t" \
    "v_add_f32 v194, v194, v166\n\t" \
    "v_add_f32 v195, v195, v167\n\t" \
    "v_cvt_pk_bf16_f32 v188, v188, v189\n\t" \
    "v_cvt_pk_bf16_f32 v189, v190, v191\n\t" \
    "v_cvt_pk_bf16_f32 v190, v192, v193\n\t" \
    "v_cvt_pk_bf16_f32 v191, v194, v195\n\t" \
    "v_min_u32 v188, -2, v188\n\t" \
    "v_min_u32 v189, -2, v189\n\t" \
    "v_min_u32 v190, -2, v190\n\t" \
    "v_min_u32 v191, -2, v191\n\t" \
    "s_nop 1\n\t" \
    "v_permlane16_swap_b32 v188, v190\n\t" \
    "v_permlane16_swap_b32 v189, v191\n\t" \
    "s_waitcnt vmcnt(6)\n\t" \
    "buffer_store_dwordx4 v[188:191], %[oa], %[rs], %[so] offen sc1\n\t" \
    "s_nop 1\n\t" \
    "s_setprio 0\n\t"
#define ASM_WT2_HI \
    "s_setprio 1\n\t" \
    "s_barrier\n\t" ASM_ST \
    "ds_read_b128 v[160:163], %[la] offset:0\n\t" \
    "ds_read_b128 v[164:167], %[la] offset:1024\n\t" \
    "ds_read_b128 v[168:171], %[la] offset:2048\n\t" \
    "ds_read_b128 v[172:175], %[la] offset:3072\n\t" \
    "ds_read_b128 v[176:179], %[la] offset:8192\n\t" \
    "ds_read_b128 v[180:183], %[la] offset:9216\n\t" \
    "s_waitcnt lgkmcnt(5)\n\t" \
    "v_mfma_f32_16x16x32_bf16 v[188:191], v[0:3], v[160:163], 0\n\t" \
    "ds_read_b128 v[184:187], %[la] offset:10240\n\t" \
    "buffer_load_dwordx2 v[196:197], %[ro], %[rs], %[sx] offen sc1\n\t" \
    "v_mfma_f32_16x16x32_bf16 v[192:195], v[80:83], v[160:163], 0\n\t" \
    "buffer_load_dwordx2 v[198:199], %[ro], %[rs], %[sx] offen offset:512 sc1\n\t" \
    "s_waitcnt lgkmcnt(5)\n\t" \
    "v_mfma_f32_16x16x32_bf16 v[188:191], v[4:7], v[164:167], v[188:191]\n\t" \
    "ds_read_b128 v[160:163], %[la] offset:11264\n\t" \
    "v_lshrrev_b32 v204, 1, %[l16]\n\t" \
    "v_mfma_f32_16x16x32_bf16 v[192:195], v[84:87], v[164:167], v[192:195]\n\t" \
    "v_and_b32 v204, 0x78, v204\n\t" \
    "s_waitcnt lgkmcnt(5)\n\t" \
    "v_mfma_f32_16x16x32_bf16 v[188:191], v[8:11], v[168:171], v[188:191]\n\t" \
    "ds_read_b128 v[164:167], %[la] offset:16384\n\t" \
    "buffer_load_dwordx2 v[200:201], v204, %[rs], %[ss] offen sc1\n\t" \
    "v_mfma_f32_16x16x32_bf16 v[192:195], v[88:91], v[168:171], v[192:195]\n\t" \
    "v_mov_b64 v[216:217], -1\n\t" \
    "s_waitcnt lgkmcnt(5)\n\t" \
    "v_mfma_f32_16x16x32_bf16 v[188:191], v[12:15], v[172:175], v[188:191]\n\t" \
    "ds_read_b128 v[168:171], %[la] offset:17408\n\t" \
    "v_mov_b64 v[218:219], -1\n\t" \
    "v_mfma_f32_16x16x32_bf16 v[192:195], v[92:95], v[172:175], v[192:195]\n\t" \
    "buffer_store_dwordx4 v[216:219], %[l16], %[rs], %[sa] offen sc1\n\t" \
    "s_waitcnt lgkmcnt(5)\n\t" \
    "v_mfma_f32_16x16x32_bf16 v[188:191], v[16:19], v[176:179], v[188:191]\n\t" \
    "ds_read_b128 v[172:175], %[la] offset:18432\n\t" \
    "v_mfma_f32_16x16x32_bf16 v[192:195], v[96:99], v[176:179], v[192:195]\n\t" \
    "s_waitcnt lgkmcnt(5)\n\t" \
    "v_mfma_f32_16x16x32_bf16 v[188:191], v[20:23], v[180:183], v[188:191]\n\t" \
    "ds_read_b128 v[176:179], %[la] offset:19456\n\t" \
    "v_mfma_f32_16x16x32_bf16 v[192:195], v[100:103], v[180:183], v[192:195]\n\t" \
    "s_waitcnt lgkmcnt(5)\n\t" \
    "v_mfma_f32_16x16x32_bf16 v[188:191], v[24:27], v[184:187], v[188:191]\n\t" \
    "ds_read_b128 v[180:183], %[la] offset:24576\n\t" \
    "v_mfma_f32_16x16x32_bf16 v[192:195], v[104:107], v[184:187], v[192:195]\n\t" \
    "s_waitcnt lgkmcnt(5)\n\t" \
    "v_mfma_f32_16x16x32_bf16 v[188:191], v[28:31], v[160:163], v[188:191]\n\t" \
    "ds_read_b128 v[184:187], %[la] offset:25600\n\t" \
    "v_mfma_f32_16x16x32_bf16 v[192:195], v[108:111], v[160:163], v[192:195]\n\t" \
    "s_waitcnt lgkmcnt(5)\n\t" \
    "v_mfma_f32_16x16x32_bf16 v[188:191], v[32:35], v[164:167], v[188:191]\n\t" \
    "ds_read_b128 v[160:163], %[la] offset:26624\n\t" \
    "v_mfma_f32_16x16x32_bf16 v[192:195], v[112:115], v[164:167], v[192:195]\n\t" \
    "s_waitcnt lgkmcnt(5)\n\t" \
    "v_mfma_f32_16x16x32_bf16 v[188:191], v[36:39], v[168:171], v[188:191]\n\t" \
    "ds_read_b128 v[164:167], %[la] offset:27648\n\t" \
    "v_mfma_f32_16x16x32_bf16 v[192:195], v[116:119], v[168:171], v[192:195]\n\t" \
    "s_waitcnt lgkmcnt(5)\n\t" \
    "v_mfma_f32_16x16x32_bf16 v[188:191], v[40:43], v[172:175], v[188:191]\n\t" \
    "ds_read_b128 v[168:171], %[la] offset:4096\n\t" \
    "v_mfma_f32_16x16x32_bf16 v[192:195], v[120:123], v[172:175], v[192:195]\n\t" \
    "s_waitcnt lgkmcnt(5)\n\t" \
    "v_mfma_f32_16x16x32_bf16 v[188:191], v[44:47], v[176:179], v[188:191]\n\t" \
    "ds_read_b128 v[172:175], %[la] offset:12288\n\t" \
    "v_mfma_f32_16x16x32_bf16 v[192:195], v[124:127], v[176:179], v[192:195]\n\t" \
    "s_waitcnt lgkmcnt(5)\n\t" \
    "v_mfma_f32_16x16x32_bf16 v[188:191], v[48:51], v[180:183], v[188:191]\n\t" \
    "ds_read_b128 v[176:179], %[la] offset:20480\n\t" \
    "v_mfma_f32_16x16x32_bf16 v[192:195], v[128:131], v[180:183], v[192:195]\n\t" \
    "s_waitcnt lgkmcnt(5)\n\t" \
    "v_mfma_f32_16x16x32_bf16 v[188:191], v[52:55], v[184:187], v[188:191]\n\t" \
    "ds_read_b128 v[180:183], %[la] offset:28672\n\t" \
    "v_mfma_f32_16x16x32_bf16 v[192:195], v[132:135], v[184:187], v[192:195]\n\t" \
    "s_waitcnt lgkmcnt(5)\n\t" \
    "v_mfma_f32_16x16x32_bf16 v[188:191], v[56:59], v[160:163], v[188:191]\n\t" \
    "v_mfma_f32_16x16x32_bf16 v[192:195], v[136:139], v[160:163], v[192:195]\n\t" \
    "s_waitcnt lgkmcnt(4)\n\t" \
    "v_mfma_f32_16x16x32_bf16 v[188:191], v[60:63], v[164:167], v[188:191]\n\t" \
    "v_mfma_f32_16x16x32_bf16 v[192:195], v[140:143], v[164:167], v[192:195]\n\t" \
    "s_waitcnt lgkmcnt(3)\n\t" \
    "v_mfma_f32_16x16x32_bf16 v[188:191], v[64:67], v[168:171], v[188:191]\n\t" \
    "v_mfma_f32_16x16x32_bf16 v[192:195], v[144:147], v[168:171], v[192:195]\n\t" \
    "s_waitcnt lgkmcnt(2)\n\t" \
    "v_mfma_f32_16x16x32_bf16 v[188:191], v[68:71], v[172:175], v[188:191]\n\t" \
    "v_mfma_f32_16x16x32_bf16 v[192:195], v[148:151], v[172:175], v[192:195]\n\t" \
    "s_waitcnt lgkmcnt(1)\n\t" \
    "v_mfma_f32_16x16x32_bf16 v[188:191], v[72:75], v[176:179], v[188:191]\n\t" \
    "v_mfma_f32_16x16x32_bf16 v[192:195], v[152:155], v[176:179], v[192:195]\n\t" \
    "s_waitcnt lgkmcnt(0)\n\t" \
    "v_mfma_f32_16x16x32_bf16 v[188:191], v[76:79], v[180:183], v[188:191]\n\t" \
    "v_mfma_f32_16x16x32_bf16 v[192:195], v[156:159], v[180:183], v[192:195]\n\t" \
    ASM_ST3 \
    "s_waitcnt vmcnt(1)\n\t" \
    "v_lshlrev_b32 v160, 16, v196\n\t" \
    "v_and_b32 v161, 0xffff0000, v196\n\t" \
    "v_lshlrev_b32 v162, 16, v197\n\t" \
    "v_and_b32 v163, 0xffff0000, v197\n\t" \
    "v_lshlrev_b32 v164, 16, v198\n\t" \
    "v_and_b32 v165, 0xffff0000, v198\n\t" \
    "v_lshlrev_b32 v166, 16, v199\n\t" \
    "v_and_b32 v167, 0xffff0000, v199\n\t" \
    "v_sub_f32 v160, v160, v200\n\t" \
    "v_sub_f32 v161, v161, v200\n\t" \
    "v_sub_f32 v162, v162, v200\n\t" \
    "v_sub_f32 v163, v163, v200\n\t" \
    "v_sub_f32 v164, v164, v200\n\t" \
    "v_sub_f32 v165, v165, v200\n\t" \
    "v_sub_f32 v166, v166, v200\n\t" \
    "v_sub_f32 v167, v167, v200\n\t" \
    "v_mul_f32 v160, v160, v201\n\t" \
    "v_mul_f32 v161, v161, v201\n\t" \
    "v_mul_f32 v162, v162, v201\n\t" \
    "v_mul_f32 v163, v163, v201\n\t" \
    "v_mul_f32 v164, v164, v201\n\t" \
    "v_mul_f32 v165, v165, v201\n\t" \
    "v_mul_f32 v166, v166, v201\n\t" \
    "v_mul_f32 v167, v167, v201\n\t" \
    "v_cvt_pk_bf16_f32 v160, v160, v161\n\t" \
    "v_cvt_pk_bf16_f32 v162, v162, v163\n\t" \
    "v_cvt_pk_bf16_f32 v164, v164, v165\n\t" \
    "v_cvt_pk_bf16_f32 v166, v166, v167\n\t" \
    "v_and_b32 v161, 0xffff0000, v160\n\t" \
    "v_lshlrev_b32 v160, 16, v160\n\t" \
    "v_and_b32 v163, 0xffff0000, v162\n\t" \
    "v_lshlrev_b32 v162, 16, v162\n\t" \
    "v_and_b32 v165, 0xffff0000, v164\n\t" \
    "v_lshlrev_b32 v164, 16, v164\n\t" \
    "v_and_b32 v167, 0xffff0000, v166\n\t" \
    "v_lshlrev_b32 v166, 16, v166\n\t" \
    "v_add_f32 v188, v188, v160\n\t" \
    "v_add_f32 v189, v189, v161\n\t" \
    "v_add_f32 v190, v190, v162\n\t" \
    "v_add_f32 v191, v191, v163\n\t" \
    "v_add_f32 v192, v192, v164\n\t" \
    "v_add_f32 v193, v193, v165\n\t" \
    "v_add_f32 v194, v194, v166\n\t" \
    "v_add_f32 v195, v195, v167\n\t" \
    "v_cvt_pk_bf16_f32 v188, v188, v189\n\t" \
    "v_cvt_pk_bf16_f32 v189, v190, v191\n\t" \
    "v_cvt_pk_bf16_f32 v190, v192, v193\n\t" \
    "v_cvt_pk_bf16_f32 v191, v194, v195\n\t" \
    "v_min_u32 v188, -2, v188\n\t" \
    "v_min_u32 v189, -2, v189\n\t" \
    "v_min_u32 v190, -2, v190\n\t" \
    "v_min_u32 v191, -2, v191\n\t" \
    "s_nop 1\n\t" \
    "v_permlane16_swap_b32 v188, v190\n\t" \
    "v_permlane16_swap_b32 v189, v191\n\t" \
    "s_waitcnt vmcnt(1)\n\t" \
    "buffer_store_dwordx4 v[188:191], %[oa], %[rs], %[so] offen sc1\n\t" \
    "s_nop 1\n\t"
#define ASM_WT3_LO \
    "s_branch .Lt0%=\n\t" \
    ".Ltr%=:\n\t" \
    "s_cmp_gt_i32 %[n], 0\n\t" \
    "s_cbranch_scc0 .Ltg%=\n\t" \
    ".Lfw%=:\n\t" \
    "s_mov_b32 %[a], 0\n\t" \
    "v_max_u32 v252, v232, v233\n\t" \
    "v_max3_u32 v252, v234, v235, v252\n\t" \
    "v_cmp_eq_u32 vcc, -1, v252\n\t" \
    "s_cbranch_vccz .Lgw0_%=\n\t" \
    "buffer_load_dwordx4 v[232:235], %[l16], %[rs], %[i0] offen offset:0 sc1\n\t" \
    "s_mov_b32 %[a], 1\n\t" \
    ".Lgw0_%=:\n\t" \
    "v_max_u32 v252, v236, v237\n\t" \
    "v_max3_u32 v252, v238, v239, v252\n\t" \
    "v_cmp_eq_u32 vcc, -1, v252\n\t" \
    "s_cbranch_vccz .Lgw1_%=\n\t" \
    "buffer_load_dwordx4 v[236:239], %[l16], %[rs], %[i0] offen offset:1024 sc1\n\t" \
    "s_mov_b32 %[a], 1\n\t" \
    ".Lgw1_%=:\n\t" \
    "v_max_u32 v252, v240, v241\n\t" \
    "v_max3_u32 v252, v242, v243, v252\n\t" \
    "v_cmp_eq_u32 vcc, -1, v252\n\t" \
    "s_cbranch_vccz .Lgw2_%=\n\t" \
    "buffer_load_dwordx4 v[240:243], %[l16], %[rs], %[i0] offen offset:2048 sc1\n\t" \
    "s_mov_b32 %[a], 1\n\t" \
    ".Lgw2_%=:\n\t" \
    "v_max_u32 v252, v244, v245\n\t" \
    "v_max3_u32 v252, v246, v247, v252\n\t" \
    "v_cmp_eq_u32 vcc, -1, v252\n\t" \
    "s_cbranch_vccz .Lgw3_%=\n\t" \
    "buffer_load_dwordx4 v[244:247], %[l16], %[rs], %[i0] offen offset:3072 sc1\n\t" \
    "s_mov_b32 %[a], 1\n\t" \
    ".Lgw3_%=:\n\t" \
    "v_max_u32 v252, v248, v249\n\t" \
    "v_max3_u32 v252, v250, v251, v252\n\t" \
    "v_cmp_eq_u32 vcc, -1, v252\n\t" \
    "s_cbranch_vccz .Lgw4_%=\n\t" \
    "buffer_load_dwordx4 v[248:251], %[l16], %[rs], %[i1] offen offset:0 sc1\n\t" \
    "s_mov_b32 %[a], 1\n\t" \
    ".Lgw4_%=:\n\t" \
    "s_cmp_eq_u32 %[a], 0\n\t" \
    "s_cbranch_scc1 .Lhw%=\n\t" \
    "s_waitcnt vmcnt(20)\n\t" \
    ".Liw%=:\n\t" \
    "v_max_u32 v252, v232, v233\n\t" \
    "v_max3_u32 v252, v234, v235, v252\n\t" \
    "v_max3_u32 v252, v236, v237, v252\n\t" \
    "v_max3_u32 v252, v238, v239, v252\n\t" \
    "v_max3_u32 v252, v240, v241, v252\n\t" \
    "v_max3_u32 v252, v242, v243, v252\n\t" \
    "v_max3_u32 v252, v244, v245, v252\n\t" \
    "v_max3_u32 v252, v246, v247, v252\n\t" \
    "v_max3_u32 v252, v248, v249, v252\n\t" \
    "v_max3_u32 v252, v250, v251, v252\n\t" \
    "v_cmp_eq_u32 vcc, -1, v252\n\t" \
    "s_cbranch_vccz .Lhw%=\n\t" \
    "s_sleep 1\n\t" \
    "v_max_u32 v252, v232, v233\n\t" \
    "v_max3_u32 v252, v234, v235, v252\n\t" \
    "v_max3_u32 v252, v236, v237, v252\n\t" \
    "v_max3_u32 v252, v238, v239, v252\n\t" \
    "v_max3_u32 v252, v240, v241, v252\n\t" \
    "v_max3_u32 v252, v242, v243, v252\n\t" \
    "v_max3_u32 v252, v244, v245, v252\n\t" \
    "v_max3_u32 v252, v246, v247, v252\n\t" \
    "v_max3_u32 v252, v248, v249, v252\n\t" \
    "v_max3_u32 v252, v250, v251, v252\n\t" \
    "v_cmp_eq_u32 vcc, -1, v252\n\t" \
    "s_cbranch_vccz .Lhw%=\n\t" \
    "s_sleep 1\n\t" \
    "v_max_u32 v252, v232, v233\n\t" \
    "v_max3_u32 v252, v234, v235, v252\n\t" \
    "v_max3_u32 v252, v236, v237, v252\n\t" \
    "v_max3_u32 v252, v238, v239, v252\n\t" \
    "v_max3_u32 v252, v240, v241, v252\n\t" \
    "v_max3_u32 v252, v242, v243, v252\n\t" \
    "v_max3_u32 v252, v244, v245, v252\n\t" \
    "v_max3_u32 v252, v246, v247, v252\n\t" \
    "v_max3_u32 v252, v248, v249, v252\n\t" \
    "v_max3_u32 v252, v250, v251, v252\n\t" \
    "v_cmp_eq_u32 vcc, -1, v252\n\t" \
    "s_cbranch_vccz .Lhw%=\n\t" \
    "s_sleep 1\n\t" \
    "v_max_u32 v252, v232, v233\n\t" \
    "v_max3_u32 v252, v234, v235, v252\n\t" \
    "v_max3_u32 v252, v236, v237, v252\n\t" \
    "v_max3_u32 v252, v238, v239, v252\n\t" \
    "v_max3_u32 v252, v240, v241, v252\n\t" \
    "v_max3_u32 v252, v242, v243, v252\n\t" \
    "v_max3_u32 v252, v244, v245, v252\n\t" \
    "v_max3_u32 v252, v246, v247, v252\n\t" \
    "v_max3_u32 v252, v248, v249, v252\n\t" \
    "v_max3_u32 v252, v250, v251, v252\n\t" \
    "v_cmp_eq_u32 vcc, -1, v252\n\t" \
    "s_cbranch_vccz .Lhw%=\n\t" \
    "s_sleep 1\n\t" \
    "v_max_u32 v252, v232, v233\n\t" \
    "v_max3_u32 v252, v234, v235, v252\n\t" \
    "v_max3_u32 v252, v236, v237, v252\n\t" \
    "v_max3_u32 v252, v238, v239, v252\n\t" \
    "v_max3_u32 v252, v240, v241, v252\n\t" \
    "v_max3_u32 v252, v242, v243, v252\n\t" \
    "v_max3_u32 v252, v244, v245, v252\n\t" \
    "v_max3_u32 v252, v246, v247, v252\n\t" \
    "v_max3_u32 v252, v248, v249, v252\n\t" \
    "v_max3_u32 v252, v250, v251, v252\n\t" \
    "v_cmp_eq_u32 vcc, -1, v252\n\t" \
    "s_cbranch_vccz .Lhw%=\n\t" \
    "s_sleep 1\n\t" \
    "v_max_u32 v252, v232, v233\n\t" \
    "v_max3_u32 v252, v234, v235, v252\n\t" \
    "v_max3_u32 v252, v236, v237, v252\n\t" \
    "v_max3_u32 v252, v238, v239, v252\n\t" \
    "v_max3_u32 v252, v240, v241, v252\n\t" \
    "v_max3_u32 v252, v242, v243, v252\n\t" \
    "v_max3_u32 v252, v244, v245, v252\n\t" \
    "v_max3_u32 v252, v246, v247, v252\n\t" \
    "v_max3_u32 v252, v248, v249, v252\n\t" \
    "v_max3_u32 v252, v250, v251, v252\n\t" \
    "v_cmp_eq_u32 vcc, -1, v252\n\t" \
    "s_cbranch_vccz .Lhw%=\n\t" \
    "s_sleep 1\n\t" \
    "s_sub_u32 %[n], %[n], 1\n\t" \
    "s_cmp_gt_i32 %[n], 0\n\t" \
    "s_cbranch_scc1 .Lfw%=\n\t" \
    ".Lhw%=:\n\t" \
    "s_branch .Lta%=\n\t" \
    ".Lt0%=:\n\t" \
    "s_waitcnt vmcnt(1)\n\t" \
    ".Lta%=:\n\t" \
    "ds_write_b128 %[lw], v[232:235] offset:0\n\t" \
    "v_max_u32 v252, v232, v233\n\t" \
    "v_max3_u32 v252, v234, v235, v252\n\t" \
    "v_mfma_f32_16x16x32_bf16 v[188:191], v[232:235], v[232:235], 0\n\t" \
    "v_mfma_f32_16x16x32_bf16 v[192:195], v[216:219], v[232:235], 0\n\t" \
    "ds_write_b128 %[lw], v[236:239] offset:1024\n\t" \
    "v_max3_u32 v252, v236, v237, v252\n\t" \
    "v_max3_u32 v252, v238, v239, v252\n\t" \
    "v_mfma_f32_16x16x32_bf16 v[188:191], v[236:239], v[236:239], v[188:191]\n\t" \
    "v_mfma_f32_16x16x32_bf16 v[192:195], v[216:219], v[236:239], v[192:195]\n\t" \
    "ds_write_b128 %[lw], v[240:243] offset:2048\n\t" \
    "v_max3_u32 v252, v240, v241, v252\n\t" \
    "v_max3_u32 v252, v242, v243, v252\n\t" \
    "v_mfma_f32_16x16x32_bf16 v[188:191], v[240:243], v[240:243], v[188:191]\n\t" \
    "v_mfma_f32_16x16x32_bf16 v[192:195], v[216:219], v[240:243], v[192:195]\n\t" \
    "ds_write_b128 %[lw], v[244:247] offset:3072\n\t" \
    "v_max3_u32 v252, v244, v245, v252\n\t" \
    "v_max3_u32 v252, v246, v247, v252\n\t" \
    "v_mfma_f32_16x16x32_bf16 v[188:191], v[244:247], v[244:247], v[188:191]\n\t" \
    "v_mfma_f32_16x16x32_bf16 v[192:195], v[216:219], v[244:247], v[192:195]\n\t" \
    "ds_write_b128 %[lw], v[248:251] offset:4096\n\t" \
    "v_max3_u32 v252, v248, v249, v252\n\t" \
    "v_max3_u32 v252, v250, v251, v252\n\t" \
    "v_mfma_f32_16x16x32_bf16 v[188:191], v[248:251], v[248:251], v[188:191]\n\t" \
    "v_mfma_f32_16x16x32_bf16 v[192:195], v[216:219], v[248:251], v[192:195]\n\t" \
    "v_cmp_eq_u32 vcc, -1, v252\n\t" \
    "s_cbranch_vccnz .Ltr%=\n\t" \
    ".Ltg%=:\n\t" \
    ASM_ST2 \
    "s_nop 5\n\t" \
    "ds_write_b128 %[lw], v[188:191] offset:5120\n\t" \
    "ds_write_b128 %[lw], v[192:195] offset:6144\n\t" \
    "s_waitcnt lgkmcnt(0)\n\t" \
    "s_setprio 2\n\t" \
    "s_barrier\n\t" ASM_ST \
    "ds_read_b128 v[160:163], %[la] offset:0\n\t" \
    "ds_read_b128 v[164:167], %[la] offset:1024\n\t" \
    "ds_read_b128 v[168:171], %[la] offset:2048\n\t" \
    "ds_read2st64_b32 v[204:205], %[sr] offset0:20 offset1:24\n\t" \
    "ds_read_b128 v[172:175], %[la] offset:3072\n\t" \
    "ds_read_b128 v[176:179], %[la] offset:8192\n\t" \
    "ds_read_b128 v[180:183], %[la] offset:9216\n\t" \
    "s_waitcnt lgkmcnt(6)\n\t" \
    "v_mfma_f32_16x16x32_bf16 v[188:191], v[0:3], v[160:163], 0\n\t" \
    "ds_read_b128 v[184:187], %[la] offset:10240\n\t" \
    "v_mfma_f32_16x16x32_bf16 v[192:195], v[80:83], v[160:163], 0\n\t" \
    "s_waitcnt lgkmcnt(6)\n\t" \
    "v_mfma_f32_16x16x32_bf16 v[188:191], v[4:7], v[164:167], v[188:191]\n\t" \
    "ds_read_b128 v[160:163], %[la] offset:11264\n\t" \
    "v_mfma_f32_16x16x32_bf16 v[192:195], v[84:87], v[164:167], v[192:195]\n\t" \
    "s_waitcnt lgkmcnt(6)\n\t" \
    "v_mfma_f32_16x16x32_bf16 v[188:191], v[8:11], v[168:171], v[188:191]\n\t" \
    "ds_read_b128 v[164:167], %[la] offset:16384\n\t" \
    "s_waitcnt lgkmcnt(6)\n\t" \
    "v_mfma_f32_16x16x32_bf16 v[192:195], v[88:91], v[168:171], v[192:195]\n\t" \
    "v_permlane16_swap_b32 v204, v205\n\t" \
    "s_waitcnt lgkmcnt(5)\n\t" \
    "v_mfma_f32_16x16x32_bf16 v[188:191], v[12:15], v[172:175], v[188:191]\n\t" \
    "ds_read_b128 v[168:171], %[la] offset:17408\n\t" \
    "v_mfma_f32_16x16x32_bf16 v[192:195], v[92:95], v[172:175], v[192:195]\n\t" \
    "v_add_f32 v206, v204, v205\n\t" \
    "s_waitcnt lgkmcnt(5)\n\t" \
    "v_mfma_f32_16x16x32_bf16 v[188:191], v[16:19], v[176:179], v[188:191]\n\t" \
    "ds_read_b128 v[172:175], %[la] offset:18432\n\t" \
    "v_add_f32 v207, v204, v205\n\t" \
    "v_mfma_f32_16x16x32_bf16 v[192:195], v[96:99], v[176:179], v[192:195]\n\t" \
    "s_waitcnt lgkmcnt(5)\n\t" \
    "v_mfma_f32_16x16x32_bf16 v[188:191], v[20:23], v[180:183], v[188:191]\n\t" \
    "ds_read_b128 v[176:179], %[la] offset:19456\n\t" \
    "v_permlane32_swap_b32 v206, v207\n\t" \
    "v_mfma_f32_16x16x32_bf16 v[192:195], v[100:103], v[180:183], v[192:195]\n\t" \
    "s_waitcnt lgkmcnt(5)\n\t" \
    "v_mfma_f32_16x16x32_bf16 v[188:191], v[24:27], v[184:187], v[188:191]\n\t" \
    "ds_read_b128 v[180:183], %[la] offset:24576\n\t" \
    "v_add_f32 v204, v206, v207\n\t" \
    "v_mfma_f32_16x16x32_bf16 v[192:195], v[104:107], v[184:187], v[192:195]\n\t" \
    "v_add_f32 v205, v206, v207\n\t" \
    "s_waitcnt lgkmcnt(5)\n\t" \
    "v_mfma_f32_16x16x32_bf16 v[188:191], v[28:31], v[160:163], v[188:191]\n\t" \
    "ds_read_b128 v[184:187], %[la] offset:25600\n\t" \
    "v_mfma_f32_16x16x32_bf16 v[192:195], v[108:111], v[160:163], v[192:195]\n\t" \
    "v_permlane16_swap_b32 v204, v205\n\t" \
    "s_waitcnt lgkmcnt(5)\n\t" \
    "v_mfma_f32_16x16x32_bf16 v[188:191], v[32:35], v[164:167], v[188:191]\n\t" \
    "ds_read_b128 v[160:163], %[la] offset:26624\n\t" \
    "v_mfma_f32_16x16x32_bf16 v[192:195], v[112:115], v[164:167], v[192:195]\n\t" \
    "v_mul_f32 v205, %[kd], v205\n\t" \
    "s_waitcnt lgkmcnt(5)\n\t" \
    "v_mfma_f32_16x16x32_bf16 v[188:191], v[36:39], v[168:171], v[188:191]\n\t" \
    "ds_read_b128 v[164:167], %[la] offset:27648\n\t" \
    "v_mul_f32 v204, %[kd], v204\n\t" \
    "v_mfma_f32_16x16x32_bf16 v[192:195], v[116:119], v[168:171], v[192:195]\n\t" \
    "v_fma_f32 v204, -v205, v205, v204\n\t" \
    "s_waitcnt lgkmcnt(5)\n\t" \
    "v_mfma_f32_16x16x32_bf16 v[188:191], v[40:43], v[172:175], v[188:191]\n\t" \
    "ds_read_b128 v[168:171], %[la] offset:4096\n\t" \
    "v_max_f32 v204, 0, v204\n\t" \
    "v_mfma_f32_16x16x32_bf16 v[192:195], v[120:123], v[172:175], v[192:195]\n\t" \
    "v_add_f32 v206, %[ke], v204\n\t" \
    "s_waitcnt lgkmcnt(5)\n\t" \
    "v_mfma_f32_16x16x32_bf16 v[188:191], v[44:47], v[176:179], v[188:191]\n\t" \
    "ds_read_b128 v[172:175], %[la] offset:12288\n\t" \
    "v_rsq_f32 v206, v206\n\t" \
    "v_mfma_f32_16x16x32_bf16 v[192:195], v[124:127], v[176:179], v[192:195]\n\t" \
    "s_waitcnt lgkmcnt(5)\n\t" \
    "v_mfma_f32_16x16x32_bf16 v[188:191], v[48:51], v[180:183], v[188:191]\n\t" \
    "ds_read_b128 v[176:179], %[la] offset:20480\n\t" \
    "v_mul_f32 v207, -v205, v206\n\t" \
    "v_mfma_f32_16x16x32_bf16 v[192:195], v[128:131], v[180:183], v[192:195]\n\t" \
    "v_mul_f32 v208, v196, v207\n\t" \
    "s_waitcnt lgkmcnt(5)\n\t" \
    "v_mfma_f32_16x16x32_bf16 v[188:191], v[52:55], v[184:187], v[188:191]\n\t" \
    "ds_read_b128 v[180:183], %[la] offset:28672\n\t" \
    "v_mul_f32 v209, v197, v207\n\t" \
    "v_mul_f32 v210, v198, v207\n\t" \
    "v_mfma_f32_16x16x32_bf16 v[192:195], v[132:135], v[184:187], v[192:195]\n\t" \
    "v_mul_f32 v211, v199, v207\n\t" \
    "v_mul_f32 v212, v200, v207\n\t" \
    "s_waitcnt lgkmcnt(5)\n\t" \
    "v_mfma_f32_16x16x32_bf16 v[188:191], v[56:59], v[160:163], v[188:191]\n\t" \
    "v_mul_f32 v213, v201, v207\n\t" \
    "v_mul_f32 v214, v202, v207\n\t" \
    "v_mfma_f32_16x16x32_bf16 v[192:195], v[136:139], v[160:163], v[192:195]\n\t" \
    "v_mul_f32 v215, v203, v207\n\t" \
    "v_mov_b64 v[216:217], -1\n\t" \
    "s_waitcnt lgkmcnt(4)\n\t" \
    "v_mfma_f32_16x16x32_bf16 v[188:191], v[60:63], v[164:167], v[188:191]\n\t" \
    "v_mov_b64 v[218:219], -1\n\t" \
    "buffer_store_dwordx4 v[216:219], %[l16], %[rs], %[sa] offen sc1\n\t" \
    "v_mfma_f32_16x16x32_bf16 v[192:195], v[140:143], v[164:167], v[192:195]\n\t" \
    "s_waitcnt lgkmcnt(3)\n\t" \
    "v_mfma_f32_16x16x32_bf16 v[188:191], v[64:67], v[168:171], v[188:191]\n\t" \
    "v_mfma_f32_16x16x32_bf16 v[192:195], v[144:147], v[168:171], v[192:195]\n\t" \
    "s_waitcnt lgkmcnt(2)\n\t" \
    "v_mfma_f32_16x16x32_bf16 v[188:191], v[68:71], v[172:175], v[188:191]\n\t" \
    "v_mfma_f32_16x16x32_bf16 v[192:195], v[148:151], v[172:175], v[192:195]\n\t" \
    "s_waitcnt lgkmcnt(1)\n\t" \
    "v_mfma_f32_16x16x32_bf16 v[188:191], v[72:75], v[176:179], v[188:191]\n\t" \
    "v_mfma_f32_16x16x32_bf16 v[192:195], v[152:155], v[176:179], v[192:195]\n\t" \
    "s_waitcnt lgkmcnt(0)\n\t" \
    "v_mfma_f32_16x16x32_bf16 v[188:191], v[76:79], v[180:183], v[188:191]\n\t" \
    "v_mfma_f32_16x16x32_bf16 v[192:195], v[156:159], v[180:183], v[192:195]\n\t" \
    "s_setprio 2\n\t" \
    "buffer_load_dwordx4 v[232:235], %[l16], %[rs], %[n0] offen offset:0 sc1\n\t" \
    "buffer_load_dwordx4 v[236:239], %[l16], %[rs], %[n0] offen offset:1024 sc1\n\t" \
    "buffer_load_dwordx4 v[240:243], %[l16], %[rs], %[n0] offen offset:2048 sc1\n\t" \
    "buffer_load_dwordx4 v[244:247], %[l16], %[rs], %[n0] offen offset:3072 sc1\n\t" \
    "buffer_load_dwordx4 v[248:251], %[l16], %[rs], %[n1] offen offset:0 sc1\n\t" \
    ASM_ST3 \
    "v_fma_f32 v188, v188, v206, v208\n\t" \
    "v_fma_f32 v189, v189, v206, v209\n\t" \
    "v_fma_f32 v190, v190, v206, v210\n\t" \
    "v_fma_f32 v191, v191, v206, v211\n\t" \
    "v_mul_f32 v160, v188, v188\n\t" \
    "v_mul_f32 v161, v189, v189\n\t" \
    "v_mul_f32 v162, v190, v190\n\t" \
    "v_mul_f32 v163, v191, v191\n\t" \
    "v_fma_f32 v160, v160, %[k1], v220\n\t" \
    "v_fma_f32 v161, v161, %[k1], v220\n\t" \
    "v_fma_f32 v162, v162, %[k1], v220\n\t" \
    "v_fma_f32 v163, v163, %[k1], v220\n\t" \
    "v_mul_f32 v160, v188, v160\n\t" \
    "v_mul_f32 v161, v189, v161\n\t" \
    "v_mul_f32 v162, v190, v162\n\t" \
    "v_mul_f32 v163, v191, v163\n\t" \
    "v_fma_f32 v192, v192, v206, v212\n\t" \
    "v_fma_f32 v193, v193, v206, v213\n\t" \
    "v_fma_f32 v194, v194, v206, v214\n\t" \
    "v_fma_f32 v195, v195, v206, v215\n\t" \
    "v_mul_f32 v164, v192, v192\n\t" \
    "v_mul_f32 v165, v193, v193\n\t" \
    "v_mul_f32 v166, v194, v194\n\t" \
    "v_mul_f32 v167, v195, v195\n\t" \
    "v_fma_f32 v164, v164, %[k1], v220\n\t" \
    "v_fma_f32 v165, v165, %[k1], v220\n\t" \
    "v_fma_f32 v166, v166, %[k1], v220\n\t" \
    "v_fma_f32 v167, v167, %[k1], v220\n\t" \
    "v_mul_f32 v164, v192, v164\n\t" \
    "v_mul_f32 v165, v193, v165\n\t" \
    "v_mul_f32 v166, v194, v166\n\t" \
    "v_mul_f32 v167, v195, v167\n\t" \
    "v_exp_f32 v160, v160\n\t" \
    "v_exp_f32 v161, v161\n\t" \
    "v_exp_f32 v162, v162\n\t" \
    "v_exp_f32 v163, v163\n\t" \
    "v_exp_f32 v164, v164\n\t" \
    "v_exp_f32 v165, v165\n\t" \
    "v_exp_f32 v166, v166\n\t" \
    "v_exp_f32 v167, v167\n\t" \
    "v_add_f32 v160, 1.0, v160\n\t" \
    "v_add_f32 v161, 1.0, v161\n\t" \
    "v_add_f32 v162, 1.0, v162\n\t" \
    "v_add_f32 v163, 1.0, v163\n\t" \
    "v_add_f32 v164, 1.0, v164\n\t" \
    "v_add_f32 v165, 1.0, v165\n\t" \
    "v_add_f32 v166, 1.0, v166\n\t" \
    "v_add_f32 v167, 1.0, v167\n\t" \
    "v_rcp_f32 v160, v160\n\t" \
    "v_rcp_f32 v161, v161\n\t" \
    "v_rcp_f32 v162, v162\n\t" \
    "v_rcp_f32 v163, v163\n\t" \
    "v_rcp_f32 v164, v164\n\t" \
    "v_rcp_f32 v165, v165\n\t" \
    "v_rcp_f32 v166, v166\n\t" \
    "v_rcp_f32 v167, v167\n\t" \
    "v_mul_f32 v188, v188, v160\n\t" \
    "v_mul_f32 v189, v189, v161\n\t" \
    "v_mul_f32 v190, v190, v162\n\t" \
    "v_mul_f32 v191, v191, v163\n\t" \
    "v_mul_f32 v192, v192, v164\n\t" \
    "v_mul_f32 v193, v193, v165\n\t" \
    "v_mul_f32 v194, v194, v166\n\t" \
    "v_mul_f32 v195, v195, v167\n\t" \
    "v_cvt_pk_bf16_f32 v188, v188, v189\n\t" \
    "v_cvt_pk_bf16_f32 v189, v190, v191\n\t" \
    "v_cvt_pk_bf16_f32 v190, v192, v193\n\t" \
    "v_cvt_pk_bf16_f32 v191, v194, v195\n\t" \
    "v_min_u32 v188, -2, v188\n\t" \
    "v_min_u32 v189, -2, v189\n\t" \
    "v_min_u32 v190, -2, v190\n\t" \
    "v_min_u32 v191, -2, v191\n\t" \
    "v_mov_b32 v216, 0x3f803f80\n\t" \
    "v_mov_b32 v217, 0x3f803f80\n\t" \
    "v_mov_b32 v218, 0x3f803f80\n\t" \
    "v_mov_b32 v219, 0x3f803f80\n\t" \
    "v_permlane16_swap_b32 v188, v190\n\t" \
    "v_permlane16_swap_b32 v189, v191\n\t" \
    "s_waitcnt vmcnt(6)\n\t" \
    "buffer_store_dwordx4 v[188:191], %[oa], %[rs], %[so] offen sc1\n\t" \
    "s_nop 1\n\t" \
    "s_setprio 0\n\t"
#define ASM_WT3_HI \
    "s_setprio 1\n\t" \
    "s_barrier\n\t" ASM_ST \
    "ds_read_b128 v[160:163], %[la] offset:0\n\t" \
    "ds_read_b128 v[164:167], %[la] offset:1024\n\t" \
    "ds_read_b128 v[168:171], %[la] offset:2048\n\t" \
    "ds_read2st64_b32 v[204:205], %[sr] offset0:20 offset1:24\n\t" \
    "ds_read_b128 v[172:175], %[la] offset:3072\n\t" \
    "ds_read_b128 v[176:179], %[la] offset:8192\n\t" \
    "ds_read_b128 v[180:183], %[la] offset:9216\n\t" \
    "s_waitcnt lgkmcnt(6)\n\t" \
    "v_mfma_f32_16x16x32_bf16 v[188:191], v[0:3], v[160:163], 0\n\t" \
    "ds_read_b128 v[184:187], %[la] offset:10240\n\t" \
    "v_mfma_f32_16x16x32_bf16 v[192:195], v[80:83], v[160:163], 0\n\t" \
    "s_waitcnt lgkmcnt(6)\n\t" \
    "v_mfma_f32_16x16x32_bf16 v[188:191], v[4:7], v[164:167], v[188:191]\n\t" \
    "ds_read_b128 v[160:163], %[la] offset:11264\n\t" \
    "v_mfma_f32_16x16x32_bf16 v[192:195], v[84:87], v[164:167], v[192:195]\n\t" \
    "s_waitcnt lgkmcnt(6)\n\t" \
    "v_mfma_f32_16x16x32_bf16 v[188:191], v[8:11], v[168:171], v[188:191]\n\t" \
    "ds_read_b128 v[164:167], %[la] offset:16384\n\t" \
    "s_waitcnt lgkmcnt(6)\n\t" \
    "v_mfma_f32_16x16x32_bf16 v[192:195], v[88:91], v[168:171], v[192:195]\n\t" \
    "v_permlane16_swap_b32 v204, v205\n\t" \
    "s_waitcnt lgkmcnt(5)\n\t" \
    "v_mfma_f32_16x16x32_bf16 v[188:191], v[12:15], v[172:175], v[188:191]\n\t" \
    "ds_read_b128 v[168:171], %[la] offset:17408\n\t" \
    "v_mfma_f32_16x16x32_bf16 v[192:195], v[92:95], v[172:175], v[192:195]\n\t" \
    "v_add_f32 v206, v204, v205\n\t" \
    "s_waitcnt lgkmcnt(5)\n\t" \
    "v_mfma_f32_16x16x32_bf16 v[188:191], v[16:19], v[176:179], v[188:191]\n\t" \
    "ds_read_b128 v[172:175], %[la] offset:18432\n\t" \
    "v_add_f32 v207, v204, v205\n\t" \
    "v_mfma_f32_16x16x32_bf16 v[192:195], v[96:99], v[176:179], v[192:195]\n\t" \
    "s_waitcnt lgkmcnt(5)\n\t" \
    "v_mfma_f32_16x16x32_bf16 v[188:191], v[20:23], v[180:183], v[188:191]\n\t" \
    "ds_read_b128 v[176:179], %[la] offset:19456\n\t" \
    "v_permlane32_swap_b32 v206, v207\n\t" \
    "v_mfma_f32_16x16x32_bf16 v[192:195], v[100:103], v[180:183], v[192:195]\n\t" \
    "s_waitcnt lgkmcnt(5)\n\t" \
    "v_mfma_f32_16x16x32_bf16 v[188:191], v[24:27], v[184:187], v[188:191]\n\t" \
    "ds_read_b128 v[180:183], %[la] offset:24576\n\t" \
    "v_add_f32 v204, v206, v207\n\t" \
    "v_mfma_f32_16x16x32_bf16 v[192:195], v[104:107], v[184:187], v[192:195]\n\t" \
    "v_add_f32 v205, v206, v207\n\t" \
    "s_waitcnt lgkmcnt(5)\n\t" \
    "v_mfma_f32_16x16x32_bf16 v[188:191], v[28:31], v[160:163], v[188:191]\n\t" \
    "ds_read_b128 v[184:187], %[la] offset:25600\n\t" \
    "v_mfma_f32_16x16x32_bf16 v[192:195], v[108:111], v[160:163], v[192:195]\n\t" \
    "v_permlane16_swap_b32 v204, v205\n\t" \
    "s_waitcnt lgkmcnt(5)\n\t" \
    "v_mfma_f32_16x16x32_bf16 v[188:191], v[32:35], v[164:167], v[188:191]\n\t" \
    "ds_read_b128 v[160:163], %[la] offset:26624\n\t" \
    "v_mfma_f32_16x16x32_bf16 v[192:195], v[112:115], v[164:167], v[192:195]\n\t" \
    "v_mul_f32 v205, %[kd], v205\n\t" \
    "s_waitcnt lgkmcnt(5)\n\t" \
    "v_mfma_f32_16x16x32_bf16 v[188:191], v[36:39], v[168:171], v[188:191]\n\t" \
    "ds_read_b128 v[164:167], %[la] offset:27648\n\t" \
    "v_mul_f32 v204, %[kd], v204\n\t" \
    "v_mfma_f32_16x16x32_bf16 v[192:195], v[116:119], v[168:171], v[192:195]\n\t" \
    "v_fma_f32 v204, -v205, v205, v204\n\t" \
    "s_waitcnt lgkmcnt(5)\n\t" \
    "v_mfma_f32_16x16x32_bf16 v[188:191], v[40:43], v[172:175], v[188:191]\n\t" \
    "ds_read_b128 v[168:171], %[la] offset:4096\n\t" \
    "v_max_f32 v204, 0, v204\n\t" \
    "v_mfma_f32_16x16x32_bf16 v[192:195], v[120:123], v[172:175], v[192:195]\n\t" \
    "v_add_f32 v206, %[ke], v204\n\t" \
    "s_waitcnt lgkmcnt(5)\n\t" \
    "v_mfma_f32_16x16x32_bf16 v[188:191], v[44:47], v[176:179], v[188:191]\n\t" \
    "ds_read_b128 v[172:175], %[la] offset:12288\n\t" \
    "v_rsq_f32 v206, v206\n\t" \
    "v_mfma_f32_16x16x32_bf16 v[192:195], v[124:127], v[176:179], v[192:195]\n\t" \
    "s_waitcnt lgkmcnt(5)\n\t" \
    "v_mfma_f32_16x16x32_bf16 v[188:191], v[48:51], v[180:183], v[188:191]\n\t" \
    "ds_read_b128 v[176:179], %[la] offset:20480\n\t" \
    "v_mul_f32 v207, -v205, v206\n\t" \
    "v_mfma_f32_16x16x32_bf16 v[192:195], v[128:131], v[180:183], v[192:195]\n\t" \
    "v_mul_f32 v208, v196, v207\n\t" \
    "s_waitcnt lgkmcnt(5)\n\t" \
    "v_mfma_f32_16x16x32_bf16 v[188:191], v[52:55], v[184:187], v[188:191]\n\t" \
    "ds_read_b128 v[180:183], %[la] offset:28672\n\t" \
    "v_mul_f32 v209, v197, v207\n\t" \
    "v_mul_f32 v210, v198, v207\n\t" \
    "v_mfma_f32_16x16x32_bf16 v[192:195], v[132:135], v[184:187], v[192:195]\n\t" \
    "v_mul_f32 v211, v199, v207\n\t" \
    "v_mul_f32 v212, v200, v207\n\t" \
    "s_waitcnt lgkmcnt(5)\n\t" \
    "v_mfma_f32_16x16x32_bf16 v[188:191], v[56:59], v[160:163], v[188:191]\n\t" \
    "v_mul_f32 v213, v201, v207\n\t" \
    "v_mul_f32 v214, v202, v207\n\t" \
    "v_mfma_f32_16x16x32_bf16 v[192:195], v[136:139], v[160:163], v[192:195]\n\t" \
    "v_mul_f32 v215, v203, v207\n\t" \
    "v_mov_b64 v[216:217], -1\n\t" \
    "s_waitcnt lgkmcnt(4)\n\t" \
    "v_mfma_f32_16x16x32_bf16 v[188:191], v[60:63], v[164:167], v[188:191]\n\t" \
    "v_mov_b64 v[218:219], -1\n\t" \
    "buffer_store_dwordx4 v[216:219], %[l16], %[rs], %[sa] offen sc1\n\t" \
    "v_mfma_f32_16x16x32_bf16 v[192:195], v[140:143], v[164:167], v[192:195]\n\t" \
    "s_waitcnt lgkmcnt(3)\n\t" \
    "v_mfma_f32_16x16x32_bf16 v[188:191], v[64:67], v[168:171], v[188:191]\n\t" \
    "v_mfma_f32_16x16x32_bf16 v[192:195], v[144:147], v[168:171], v[192:195]\n\t" \
    "s_waitcnt lgkmcnt(2)\n\t" \
    "v_mfma_f32_16x16x32_bf16 v[188:191], v[68:71], v[172:175], v[188:191]\n\t" \
    "v_mfma_f32_16x16x32_bf16 v[192:195], v[148:151], v[172:175], v[192:195]\n\t" \
    "s_waitcnt lgkmcnt(1)\n\t" \
    "v_mfma_f32_16x16x32_bf16 v[188:191], v[72:75], v[176:179], v[188:191]\n\t" \
    "v_mfma_f32_16x16x32_bf16 v[192:195], v[152:155], v[176:179], v[192:195]\n\t" \
    "s_waitcnt lgkmcnt(0)\n\t" \
    "v_mfma_f32_16x16x32_bf16 v[188:191], v[76:79], v[180:183], v[188:191]\n\t" \
    "v_mfma_f32_16x16x32_bf16 v[192:195], v[156:159], v[180:183], v[192:195]\n\t" \
    ASM_ST3 \
    "s_nop 4\n\t" \
    "v_fma_f32 v188, v188, v206, v208\n\t" \
    "v_fma_f32 v189, v189, v206, v209\n\t" \
    "v_fma_f32 v190, v190, v206, v210\n\t" \
    "v_fma_f32 v191, v191, v206, v211\n\t" \
    "v_mul_f32 v160, v188, v188\n\t" \
    "v_mul_f32 v161, v189, v189\n\t" \
    "v_mul_f32 v162, v190, v190\n\t" \
    "v_mul_f32 v163, v191, v191\n\t" \
    "v_fma_f32 v160, v160, %[k1], v220\n\t" \
    "v_fma_f32 v161, v161, %[k1], v220\n\t" \
    "v_fma_f32 v162, v162, %[k1], v220\n\t" \
    "v_fma_f32 v163, v163, %[k1], v220\n\t" \
    "v_mul_f32 v160, v188, v160\n\t" \
    "v_mul_f32 v161, v189, v161\n\t" \
    "v_mul_f32 v162, v190, v162\n\t" \
    "v_mul_f32 v163, v191, v163\n\t" \
    "v_fma_f32 v192, v192, v206, v212\n\t" \
    "v_fma_f32 v193, v193, v206, v213\n\t" \
    "v_fma_f32 v194, v194, v206, v214\n\t" \
    "v_fma_f32 v195, v195, v206, v215\n\t" \
    "v_mul_f32 v164, v192, v192\n\t" \
    "v_mul_f32 v165, v193, v193\n\t" \
    "v_mul_f32 v166, v194, v194\n\t" \
    "v_mul_f32 v167, v195, v195\n\t" \
    "v_fma_f32 v164, v164, %[k1], v220\n\t" \
    "v_fma_f32 v165, v165, %[k1], v220\n\t" \
    "v_fma_f32 v166, v166, %[k1], v220\n\t" \
    "v_fma_f32 v167, v167, %[k1], v220\n\t" \
    "v_mul_f32 v164, v192, v164\n\t" \
    "v_mul_f32 v165, v193, v165\n\t" \
    "v_mul_f32 v166, v194, v166\n\t" \
    "v_mul_f32 v167, v195, v167\n\t" \
    "v_exp_f32 v160, v160\n\t" \
    "v_exp_f32 v161, v161\n\t" \
    "v_exp_f32 v162, v162\n\t" \
    "v_exp_f32 v163, v163\n\t" \
    "v_exp_f32 v164, v164\n\t" \
    "v_exp_f32 v165, v165\n\t" \
    "v_exp_f32 v166, v166\n\t" \
    "v_exp_f32 v167, v167\n\t" \
    "v_add_f32 v160, 1.0, v160\n\t" \
    "v_add_f32 v161, 1.0, v161\n\t" \
    "v_add_f32 v162, 1.0, v162\n\t" \
    "v_add_f32 v163, 1.0, v163\n\t" \
    "v_add_f32 v164, 1.0, v164\n\t" \
    "v_add_f32 v165, 1.0, v165\n\t" \
    "v_add_f32 v166, 1.0, v166\n\t" \
    "v_add_f32 v167, 1.0, v167\n\t" \
    "v_rcp_f32 v160, v160\n\t" \
    "v_rcp_f32 v161, v161\n\t" \
    "v_rcp_f32 v162, v162\n\t" \
    "v_rcp_f32 v163, v163\n\t" \
    "v_rcp_f32 v164, v164\n\t" \
    "v_rcp_f32 v165, v165\n\t" \
    "v_rcp_f32 v166, v166\n\t" \
    "v_rcp_f32 v167, v167\n\t" \
    "v_mul_f32 v188, v188, v160\n\t" \
    "v_mul_f32 v189, v189, v161\n\t" \
    "v_mul_f32 v190, v190, v162\n\t" \
    "v_mul_f32 v191, v191, v163\n\t" \
    "v_mul_f32 v192, v192, v164\n\t" \
    "v_mul_f32 v193, v193, v165\n\t" \
    "v_mul_f32 v194, v194, v166\n\t" \
    "v_mul_f32 v195, v195, v167\n\t" \
    "v_cvt_pk_bf16_f32 v188, v188, v189\n\t" \
    "v_cvt_pk_bf16_f32 v189, v190, v191\n\t" \
    "v_cvt_pk_bf16_f32 v190, v192, v193\n\t" \
    "v_cvt_pk_bf16_f32 v191, v194, v195\n\t" \
    "v_min_u32 v188, -2, v188\n\t" \
    "v_min_u32 v189, -2, v189\n\t" \
    "v_min_u32 v190, -2, v190\n\t" \
    "v_min_u32 v191, -2, v191\n\t" \
    "s_nop 1\n\t" \
    "v_permlane16_swap_b32 v188, v190\n\t" \
    "v_permlane16_swap_b32 v189, v191\n\t" \
    "s_waitcnt vmcnt(1)\n\t" \
    "buffer_store_dwordx4 v[188:191], %[oa], %[rs], %[so] offen sc1\n\t" \
    "s_nop 1\n\t"
#define ASM_POLL5_I \
    "buffer_load_dwordx4 v[232:235], %[l16], %[rs], %[s0] offen offset:0 sc1\n\t" \
    "buffer_load_dwordx4 v[236:239], %[l16], %[rs], %[s0] offen offset:1024 sc1\n\t" \
    "buffer_load_dwordx4 v[240:243], %[l16], %[rs], %[s0] offen offset:2048 sc1\n\t" \
    "buffer_load_dwordx4 v[244:247], %[l16], %[rs], %[s0] offen offset:3072 sc1\n\t" \
    "buffer_load_dwordx4 v[248:251], %[l16], %[rs], %[s1] offen offset:0 sc1\n\t"
using u32x8 = __attribute__((ext_vector_type(8))) unsigned;
using u32x16 = __attribute__((ext_vector_type(16))) unsigned;
using u32x32 = __attribute__((ext_vector_type(32))) unsigned;
struct WRegs { u32x32 w[6]; u32x8 t; };
#define WREGS(W) "{v[0:31]}"(W.w[0]), "{v[32:63]}"(W.w[1]), "{v[64:95]}"(W.w[2]), "{v[96:127]}"(W.w[3]), "{v[128:159]}"(W.w[4]), "{v[160:191]}"(W.w[5]), "{v[192:199]}"(W.t)
__device__ __forceinline__ u32x32 cat32(u32x8 a, u32x8 b, u32x8 c, u32x8 d) {
    const u32x16 lo = __builtin_shufflevector(a, b, 0, 1, 2, 3, 4, 5, 6, 7, 8, 9, 10, 11, 12, 13, 14, 15);
    const u32x16 hi = __builtin_shufflevector(c, d, 0, 1, 2, 3, 4, 5, 6, 7, 8, 9, 10, 11, 12, 13, 14, 15);
    return __builtin_shufflevector(lo, hi, 0, 1, 2, 3, 4, 5, 6, 7, 8, 9, 10, 11, 12, 13, 14, 15, 16, 17, 18, 19, 20, 21, 22, 23, 24, 25, 26, 27, 28, 29, 30, 31);
}
#define WOUT(W) "=&{v[0:31]}"(W.w[0]), "=&{v[32:63]}"(W.w[1]), "=&{v[64:95]}"(W.w[2]), "=&{v[96:127]}"(W.w[3]), "=&{v[128:159]}"(W.w[4]), "=&{v[160:191]}"(W.w[5]), "=&{v[192:199]}"(W.t)
#define WTMP "memory", "scc", "v200", "v201", "v202", "v203", "v204", "v205", "v206", "v207", "v208", "v209", "v210", "v211", "v212", "v213", "v214", "v215", "v216", "v217", "v218", "v219", "v220", "v221", "v222", "v223", "v224", "v225", "v226", "v227", "v228", "v229", "v230", "v231", "v232", "v233", "v234", "v235", "v236", "v237", "v238", "v239"
__device__ __forceinline__ void load_w3(WRegs& W, const bf16_t* m, int wave, unsigned c0, bool hq) {
    const int ln = lane_id();
    unsigned o[3], s[3], t;
#pragma unroll
    for (int k = 0; k < 3; ++k) {
        const int row = (k * 64 + ln) / 24, sg = (k * 64 + ln) % 24;
        o[k] = row * (HD * 2) + c0 + (hq ? ((sg >> 1) & 1) * 384 + ((0x140 >> ((sg >> 3) * 4)) & 15) * 32 + (sg & 5) * 16 : sg * 16); s[k] = wave * 13824 + row * 416 + sg * 16;
    }
    const unsigned tr = (ln >> 4) * 3456 + ((ln >> 2) & 3) * 416 + (wave & 3) * 32 + (ln & 3) * 8;
    asm volatile(ASM_WLOAD3 : WOUT(W), [t] "=&s"(t) : [rs] "s"(mkrsrc(m, DM * HD * 2)), [wx] "s"(wave), [o0] "v"(o[0]), [o1] "v"(o[1]), [o2] "v"(o[2]), [s0] "v"(s[0]), [s1] "v"(s[1]), [s2] "v"(s[2]),
                 [t0] "v"(tr + wave * 13824), [t1] "v"(tr + (wave ^ 1) * 13824), [t2] "v"(tr + (wave ^ 2) * 13824), [t3] "v"(tr + (wave ^ 3) * 13824), [t4] "v"(tr + (wave & 4) * 13824),
                 [wl] "v"(L_W + wave * 10240 + ln * 16) : WTMP);
}
__device__ __forceinline__ void load_wo(WRegs& W, const bf16_t* m, int wave, int h) {
    const int ln = lane_id();
    unsigned o[5], s[5], t;
#pragma unroll
    for (int k = 0; k < 5; ++k) {
        const int row = (k * 64 + ln) / 20, sg = (k * 64 + ln) % 20;
        o[k] = (h * DH + row) * (DM * 2) + sg * 16 + (wave & 3) * 320; s[k] = wave * 13824 + row * 352 + sg * 16;
    }
    asm volatile(ASM_WLOADO : WOUT(W), [t] "=&s"(t) : [rs] "s"(mkrsrc(m, DM * HD * 2)), [o0] "v"(o[0]), [o1] "v"(o[1]), [o2] "v"(o[2]), [o3] "v"(o[3]), [o4] "v"(o[4]),
                 [s0] "v"(s[0]), [s1] "v"(s[1]), [s2] "v"(s[2]), [s3] "v"(s[3]), [s4] "v"(s[4]), [tr] "v"(wave * 13824 + (ln >> 2) * 352 + (ln & 3) * 8), [wl] "v"(L_W + wave * 10240 + ln * 16) : WTMP);
}
using s16x4 = __attribute__((ext_vector_type(4))) short;
template <int NT, int KS, int N>
__device__ __forceinline__ void load_wn(u32x4 (&f)[NT * KS], const bf16_t* m, int rows, int wave, int c0, int k0) {
    const int ln = lane_id();
    const rsrc_t rs = mkrsrc(m, rows * N * 2);
#pragma unroll
    for (int k = 0; k < KS; ++k)
#pragma unroll
        for (int n = 0; n < NT; ++n) f[n * KS + k] = __builtin_amdgcn_raw_buffer_load_b128(rs, ((n * 64 + ln) / (2 * NT)) * (N * 2) + ((n * 64 + ln) % (2 * NT)) * 16, (k0 + k) * (N * 64) + c0 * 32, 0);
    const unsigned st = wave * 10240 + ln * 16, tr = wave * 10240 + ((ln >> 4) * 8 + ((ln >> 2) & 3)) * (NT * 32) + (ln & 3) * 8;
#pragma unroll
    for (int k = 0; k < KS; ++k) {
#pragma unroll
        for (int n = 0; n < NT; ++n) lds_st<u32x4>(st + n * 1024, f[n * KS + k]);
        asm volatile("" ::: "memory");
#pragma unroll
        for (int n = 0; n < NT; ++n) {
            const u32x2 a = __builtin_bit_cast(u32x2, __builtin_amdgcn_ds_read_tr16_b64_v4i16((LAS s16x4*)(tr + n * 32)));
            const u32x2 b = __builtin_bit_cast(u32x2, __builtin_amdgcn_ds_read_tr16_b64_v4i16((LAS s16x4*)(tr + n * 32 + NT * 128)));
            f[n * KS + k] = (u32x4){a[0], a[1], b[0], b[1]};
        }
        asm volatile("" ::: "memory");
    }
}
struct C1Regs { u32x8 lw0, lw1, z1; u32x4 z0; };
#define XG_OUT "=&{v[200:207]}"(C.lw0), "=&{v[224:227]}"(C.z0), "=&{v[228:235]}"(C.lw1), "=&{v[244:251]}"(C.z1)
#define XG_IN WREGS(W), [xa] "v"(xa), [wl] "v"(wl), [qs] "s"(qs)
#define XG_CLOB "memory", "scc", "v208", "v209", "v210", "v211", "v212", "v213", "v214", "v215", "v216", "v217", "v218", "v219", "v220", "v221", "v222", "v223", "v236", "v237", "v238", "v239", "v240", "v241", "v242", "v243", "v252"
constexpr int XS0 = 8;
__device__ __forceinline__ void init_wait(const Params& P, int c, int ph) {
    for (int spins = 0;; ++spins) {
        unsigned o;
        asm volatile("v_mov_b32 %0, 0" : "=v"(o));
        if (__builtin_amdgcn_readfirstlane(__builtin_amdgcn_raw_buffer_load_b32(mkrsrc(P.ws + FL0, FL_BYTES), o, FL_IN + (ph * 8 + (c & 7)) * 128, 16)) >= 128u) break;
        __builtin_amdgcn_s_sleep(8);
        if (spin_fail(spins, P)) break;
    }
    asm volatile("" ::: "memory");
}
__device__ __forceinline__ void xn_wait(const Params& P, rsrc_t rr, int seg, int cpy) {
    for (int spins = 0;; ++spins) {
        const unsigned f = __builtin_amdgcn_raw_buffer_load_b32(rr, (lane_id() & 15) * 128, RG_FX + (cpy & 3) * 2048, 16);
        if (__all(f > (unsigned)seg)) break;
        __builtin_amdgcn_s_sleep(4);
        if (spin_fail(spins, P)) break;
    }
    asm volatile("" ::: "memory");
}
__device__ __forceinline__ unsigned c1_dst(int wave, bool ktype) {
    const int ln = lane_id(), l15 = ln & 15, hf = (wave >> 1) & 1;
    const int rb16 = ((ln >> 2) & 4) * 16 + (ln >> 5) * 32;
    return (ktype ? L_KM + (l15 >> 2) * 128 + hf * 8 + (l15 & 3) * 2
                  : L_Q + (l15 >> 3) * 256 + ((l15 >> 1) & 1) * 128 + ((l15 >> 2) & 1) * 8 + hf * 4 + (l15 & 1) * 2) + (wave & 1) * 1536 + rb16;
}
template <int NP>
__device__ __forceinline__ void crit1(const WRegs& W, C1Regs& C, rsrc_t rr, unsigned sbase, int wave, unsigned wl, unsigned dsto, unsigned& budget
) {
    unsigned abase;
    asm volatile("v_and_b32 %0, 0x3ff, %1\n\tv_or_b32 %0, %2, %0" : "=&v"(abase) : "v"(wl), "s"(wave * 1024));
    ASM_ST_DECL;
#define C1_OUT "+{v[224:227]}"(C.z0), "+{v[244:251]}"(C.z1), "+{v[200:207]}"(C.lw0), "+{v[228:235]}"(C.lw1)
#define C1_IN WREGS(W), [ab] "v"(abase), [wl] "v"(wl), [dst] "v"(dsto)
#define C1_CLOB "memory", "vcc", "scc", "v208", "v209", "v210", "v211", "v212", "v213", "v214", "v215", "v216", "v217", "v218", "v219", "v220", "v221", "v222", "v223", "v236", "v237", "v238", "v239", "v240", "v241", "v242", "v243"
#define C1_POLL [rs] "s"(rr), [s0] "s"(sbase), [s1] "s"(sbase + 4096), [s2] "s"(sbase + 2 * BLK)
    ST(1);
    if constexpr (NP == 3) asm volatile(ASM_CRIT1_3 : C1_OUT, [n] "+s"(budget) ASM_ST_OUT ASM_ST2_OUT : C1_IN, C1_POLL : C1_CLOB);
    else if constexpr (NP == 2) asm volatile(ASM_CRIT1_2 : C1_OUT, [n] "+s"(budget) ASM_ST_OUT ASM_ST2_OUT : C1_IN, C1_POLL : C1_CLOB);
    else asm volatile(ASM_CRIT1_N : C1_OUT ASM_ST_OUT : C1_IN : C1_CLOB);
    SB();
    ASM_ST_SAVE(3);
    if constexpr (NP != 0) { ASM_ST2_SAVE(2); }
}
#define STARG

#define SRG 5
__device__ __forceinline__ void s_role(const Params& P, int wave, int L, int par, int h, int cpy) {
    const rsrc_t rr = mkrsrc(P.ws + par * 8192, RG_L);
    const char* xn = P.ws + PG0 + PG_XN + L * PG_LX;
    auto xdma = [&](int seg, int hb) {
        const unsigned l16 = lane_id() * 16;
        const int mt = (wave >> 1) & 1;
        const char* g = xn + pgb(seg, par);
        if (hb == 0) {
            dma4(g + blin(mt * 20480 + (wave & 1) * 4096), l16, L_XA + mt * 16384 + (wave & 1) * 4096);
            dmaq<0>(g + blin(mt * 20480 + 16384) + (wave & 1) * 1024, l16, L_XB + mt * 4096 + (wave & 1) * 1024);
        } else {
            dma4(g + blin(mt * 20480 + 8192), l16, L_XA + mt * 16384 + 8192); dma4(g + blin(mt * 20480 + 12288), l16, L_XA + mt * 16384 + 12288);
            dmaq<2>(g + blin(mt * 20480 + 16384), l16, L_XB + mt * 4096); dmaq<3>(g + blin(mt * 20480 + 16384), l16, L_XB + mt * 4096);
        }
    };
    const float SCL = 0.07216878364870322f * 1.4426950408889634f;
    auto run = [&](auto Cc) {
    constexpr int CLS = decltype(Cc)::value;
    WRegs wf;
    MARK(!L && h == 0 && wave == 0, 0);
    load_w3(wf, P.w[CLS == 3], wave, h * 384, false);
    MARKX(!L && wave == 0, 1);
    init_wait(P, wave, L);
    if (CLS == 3) {
        if (L) xn_wait(P, rr, 0, cpy);
        xdma(0, 0);
        if (wave == 4 || wave == 7) xdma(0, 1);
        wait_vm<0>();
    }
    bar();
    if (CLS == 3 && wave == 7 && L) xn_wait(P, rr, 1, cpy);
    const unsigned qs = (CLS == 3 ? L_KX : L_QX) + (wave & 1) * 3072 + ((wave >> 1) & 1) * 8;
    unsigned xa, wl, dsto;
    if (CLS == 3) { xa = L_XA + wave * 2048 + lane_id() * 16; wl = L_W + wave * 10240 + lane_id() * 16; dsto = c1_dst(wave, true); }
    unsigned budget = 1u << 18;
    STDECL;
    LDECL;
    for (int t = 0; t < NSEG; ++t) {
        const unsigned slot = t & (NSLOT - 1);
        C1Regs C;
        {
            const WRegs& W = wf;
            ASM_ST_DECL;
            if constexpr (CLS == 3) {
                const int mt = (wave >> 1) & 1;
                const char* g = xn + pgb(t + 1 < NSEG ? t + 1 : t, par);
                asm volatile(ASM_XG_S_HI : XG_OUT ASM_ST_OUT : XG_IN, [slp] "s"(SLP4), [g0] "s"(g + blin(mt * 20480 + (wave & 1) * 4096)), [d0] "s"(L_XA + mt * 16384 + (wave & 1) * 4096),
                             [g1] "s"(g + blin(mt * 20480 + 16384) + (wave & 1) * 1024), [d1] "s"(L_XB + mt * 4096 + (wave & 1) * 1024) : XG_CLOB);
                wait_vm<0>();
                LB(t);
            } else {
                const unsigned l16 = lane_id() * 16;
                xa = L_XA + wave * 2048 + l16; wl = L_W + wave * 10240 + l16;
#define XG_Q(A) asm volatile("s_cmp_lt_u32 %[t], 2\n\ts_cbranch_scc1 .Lq3%=\n\t" A "s_branch .Lqe%=\n\t.Lq3%=:\n\t" ASM_XG_S_LO ".Lqe%=:" : XG_OUT ASM_ST_OUT : XG_IN, [t] "s"(t) : XG_CLOB)
                if constexpr (CLS == 0 || CLS == 4) XG_Q(ASM_XG_S_LO1); else XG_Q(ASM_XG_S_LO2);
                if (CLS == 4 && t >= HT0) {
                    const int ln = lane_id(), u = h >> 2;
                    for (int spins = 0;; ++spins) {
                        const unsigned fl = __builtin_amdgcn_raw_buffer_load_b32(rr, (ln & 3) * 128, RG_FH + BLK + u * 512, 16);
                        if (__all(fl > (unsigned)t)) break;
                        __builtin_amdgcn_s_sleep(2);
                        if (spin_fail(spins, P)) break;
                    }
                    asm volatile("" ::: "memory");
                    const char* g = P.ws + PG0 + pgb(t, par) + PG_HV + (16 + u) * 3 * BLK;
#pragma unroll
                    for (int k = 0; k < 6; ++k)
                        dmaq<0>(g + blin((((h >> 1) & 1) * 12 + (h & 1) * 3 + (k / 3) * 6 + k % 3) * 1024), ln * 16, L_QX + (k / 3) * 6144 + (k % 3 == 2 ? 3 : k % 3) * 1024);
                }
                dsto = c1_dst(wave, false);
            }
            ASM_ST_SAVE(7);
            ST(6);
        }
        ST(0);
        crit1<CLS == 3 ? 0 : CLS < 2 ? 3 : 2>(wf, C, rr, RG_XM + slot * XM_SLOT + (cpy >> 1) * XM_PAIR + (cpy & 1) * BLK, wave, wl, dsto, budget STARG);
        if constexpr (CLS == 0) {
            const unsigned sp = RG_XP + slot * XP_SLOT, sx = sp + (1 + (h >> 4)) * 2 * BLK + (h & 15) * 512;
            ASM_ST_DECL;
            asm volatile(ASM_SM : [c0] "+s"(budget) ASM_ST_OUT : WREGS(wf), [rr] "s"(rr), [sx] "s"(sx), [sy] "s"(sx + XP_PAIR), [sm] "s"(sp + h * 128), [cs] "s"(BLK), [cp] "s"(XP_PAIR), [c70] "s"(0x70), [scl] "s"(SCL)
                         : "memory", "v200", "v201", "v202", "v203", "v204", "v205", "v206", "v207", "v208", "v209", "v210", "v211", "v212", "v213", "v214", "v215", "v216", "v217", "v218", "v219", "v220", "v221", "v222", "v223", "v224", "v225", "v226", "v227", "v228", "v229", "v230", "v231", "v232", "v233", "v234", "v235", "v236", "v237", "v238", "v239", "v240", "v241", "v242", "v243", "v244", "v245", "v246", "v247", "v248", "v249", "v250", "v251", "v252", "v253", "v254", "v255");
            ASM_ST_SAVE(4);
            ST(5);
        } else if constexpr (CLS == 1 || CLS == 4) {
            const int ln = lane_id(), l15 = ln & 15, l4 = ln >> 4;
            constexpr int mt = (CLS == 4);
            const unsigned qa = L_QX + mt * 6144 + ln * 16, kmo = L_KM + l4 * 128 + (ln & 7) * 16;
            const unsigned xo = mt ? (h & 3) * 1024 : 5120 + ((h >> 1) & 1) * 1024 + (h & 1) * 256;
            const unsigned pm = l4 < 2 ? (mt ? 4096 : 7168) + (h & 3) * 256 - xo + l15 * 16 + l4 * 8 : OOB;
            const unsigned so = RG_PX + slot * PX_SLOT + (h >> 2) * BLK + xo;
            f32x4 sd;
#pragma unroll
            for (int r = 0; r < 4; ++r) sd[r] = (l4 * 4 + r > l15) ? -3.0e38f : 0.f;
            ASM_ST_DECL;
#define P4_ARGS : "+{v[240:243]}"(sd) ASM_ST_OUT : WREGS(wf), [qa] "v"(qa), [km] "v"(kmo), [pm] "v"(pm), [rr] "s"(rr), [so] "s"(so), [scl] "s"(SCL) : "memory", "v200", "v201", "v202", "v203", "v204", "v205", "v206", "v207", "v208", "v209", "v210", "v211", "v212", "v213", "v214", "v215", "v216", "v217", "v218", "v219", "v220", "v221", "v222", "v223", "v224", "v225", "v226", "v227", "v228", "v229", "v230", "v231", "v232", "v233", "v234", "v235", "v236", "v237", "v238", "v239", "v244", "v245", "v246", "v247", "v248", "v249", "v250", "v251"
            if constexpr (mt) asm volatile(ASM_P4_1 P4_ARGS); else asm volatile(ASM_P4_0 P4_ARGS);
            ASM_ST_SAVE(4);
            ST(5);
            if constexpr (mt) {
                const int ln = lane_id();
                const u32x4 s4 = sent4();
                const unsigned poff = (ln * 16 + ((ln < 8) ? h * 128 : (1 + (h >> 4)) * 2 * BLK - 128 + (h & 15) * 512)) | ((ln >= 40 || t < 1) ? 0x40000000u : 0u);
#pragma unroll
                for (int c = 0; c < (int)NCP; ++c) st128(s4, rr, poff, RG_XP + ((t - 1) & (NSLOT - 1)) * XP_SLOT + (c >> 1) * XP_PAIR + (c & 1) * BLK);
                const unsigned pbase = RG_PX + ((t - 2) & (NSLOT - 1)) * PX_SLOT + (h >> 2) * BLK;
                const unsigned o2 = ((ln & 16) ? 5120 + ((h >> 1) & 1) * 1024 + (h & 1) * 256 + (ln >> 5) * 512 : 4096 + (h & 3) * 256 + (ln >> 5) * 3072) + (ln & 15) * 16;
                if (t >= 2) { st128(s4, rr, ln * 16, pbase + (h & 3) * 1024); st128(s4, rr, o2, pbase); }
            }
        } else {
            bar();
            ST(4);
            if (CLS == 2 && L && t + 2 < NSEG) xn_wait(P, rr, t + 2, cpy);
            if (CLS == 2 && t + 2 == XS0) init_wait(P, h, 1);
            if (CLS == 3) LA();
            {
                if (CLS == 3 && (wave == 4 || wave == 7)) {
                    if (t + 1 < NSEG) xdma(t + 1, 1);
                }
                ST(5);
            }
        }
        STFLUSH(t);
    }
    LF();
    };
    if (wave == 0) run(std::integral_constant<int, 0>{});
    else if (wave == 1) run(std::integral_constant<int, 1>{});
    else if (wave == 2) run(std::integral_constant<int, 4>{});
    else if (wave == 3) run(std::integral_constant<int, 2>{});
    else run(std::integral_constant<int, 3>{});
}

__device__ __forceinline__ void h_role(const Params& P, int wave, int L, int par, int i, int cpy) {
    const rsrc_t rr = mkrsrc(P.ws + par * 8192, RG_L), rp = mkrsrc(P.ws + PG0, PG_L);
    const char* xn = P.ws + PG0 + PG_XN + L * PG_LX;
    auto run = [&](auto Cc) {
    constexpr bool HI = decltype(Cc)::value;
    const int mt = (wave >> 1) & 1;
    auto xdma = [&](int seg, int hb) {
        const unsigned l16 = lane_id() * 16;
        const char* g = xn + pgb(seg, par);
        dma4(g + blin(mt * 20480 + hb * 8192 + (wave & 1) * 4096), l16, L_XA + mt * 16384 + hb * 8192 + (wave & 1) * 4096);
        dmaq<0>(g + blin(mt * 20480 + 16384) + hb * 2048 + (wave & 1) * 1024, l16, L_XB + mt * 4096 + hb * 2048 + (wave & 1) * 1024);
    };
    WRegs wf;
    load_w3(wf, i < 16 ? P.w[2] : P.w[0], wave, (i < 16 ? i * 2 + HI : (i - 16) * 4 + HI * 2) * 384, i >= 16);
    init_wait(P, wave, L);
    const unsigned qs = (HI ? L_KX : L_QX) + (wave & 1) * 3072 + mt * 8;
    const unsigned xa = L_XA + wave * 2048 + lane_id() * 16, wl = L_W + wave * 10240 + lane_id() * 16;
    const unsigned fp = RG_FH + (i >> 4) * BLK + ((i & 15) * 4 + (wave & 3)) * 128;
    unsigned go = 0, all = 0;
    STDECL;
    LDECL;
    for (int t = HT0; t <= NSEG; ++t) {
        ST(0);
        if constexpr (HI) {
            const unsigned l16 = lane_id() * 16;
            if (t > HT0) {
                u32x4 v[6];
                const int w4 = wave - 4;
#pragma unroll
                for (int k = 0; k < 6; ++k) v[k] = lds_ld<u32x4>(L_QX + ((k >> 1) * 8 + w4 * 2 + (k & 1)) * 1024 + l16);
                const unsigned so = pgb(t - 1, par) + PG_HV + i * 3 * BLK + w4 * 2048;
#pragma unroll
                for (int k = 0; k < 6; ++k) st128(v[k], rp, l16 + (k & 1) * 1024, so + (k >> 1) * BLK);
                if (!all) {
                    wait_vm<0>();
                    if (l16 < 512) __builtin_amdgcn_raw_buffer_store_b32(t, rr, l16 >> 2, fp, 16);
                }
            }
            ST(1);
            if (t == NSEG) break;
            if (!all) bar();
            xdma(t, 1);
        } else {
            if (t == NSEG) break;
            if (!go) { if (L) xn_wait(P, rr, t, cpy); xdma(t, 0); wait_vm<0>(); }
            if (!all) bar();
        }
        ST(2);
        C1Regs C;
        const WRegs& W = wf;
        ASM_ST_DECL;
        if constexpr (HI) {
            const unsigned l4 = lane_id() * 4;
            asm volatile(ASM_XG_H_HI : XG_OUT ASM_ST_OUT : XG_IN, [rr] "s"(rr), [fp] "s"(fp), [tv] "s"(t), [fv] "v"((all && l4 < 128) ? l4 : OOB) : XG_CLOB);
        } else {
            asm volatile(ASM_XG_S_LO : XG_OUT ASM_ST_OUT : XG_IN : XG_CLOB);
            unsigned fv = NSEG;
            if (t + 1 == XS0) init_wait(P, wave, 1);
            if (L) fv = __builtin_amdgcn_raw_buffer_load_b32(rr, (lane_id() & 15) * 128, RG_FX + (cpy & 3) * 2048, 16);
            go = __builtin_amdgcn_readfirstlane((t + 1 < NSEG && __all(fv > (unsigned)t + 1)) ? 1u : 0u);
            if (go) xdma(t + 1, 0);
            lds_st<unsigned>(L_MEM + wave * 4, go);
            wait_vm<0>();
        }
        ASM_ST_SAVE(3);
        ST(4);
        if (!HI) { LB(t); LA(); }
        bar();
        const u32x4 gv = lds_ld<u32x4>(L_MEM);
        all = __builtin_amdgcn_readfirstlane(gv[0] & gv[1] & gv[2] & gv[3]);
        ST(5);
        STFLUSH(t);
    }
    LF();
    };
    if (wave < 4) run(std::false_type{}); else run(std::true_type{});
}

__device__ __forceinline__ void v_role(const Params& P, int wave, int L, int par, int h, int cpy) {
    const rsrc_t rr = mkrsrc(P.ws + par * 8192, RG_L);
    WRegs wf;
    STDECL;
    auto runv = [&](auto Cc) {
        load_w3(wf, P.w[2], wave, h * 384, false);
        MARKX(!L && wave == 0, 6);
        init_wait(P, wave, L);
        unsigned budget = 1u << 18;
        const unsigned wl = L_W + wave * 10240 + lane_id() * 16, dsto = c1_dst(wave, false);
        auto vfetch = [&](int seg, int tries) {
            const int ln = lane_id();
            for (int spins = 0;; ++spins) {
                if (spins == tries) return false;
                const unsigned f = __builtin_amdgcn_raw_buffer_load_b32(rr, (ln & 3) * 128, RG_FH + (h >> 1) * 512, 16);
                if (__all(f > (unsigned)seg)) break;
                __builtin_amdgcn_s_sleep(4);
                if (spin_fail(spins, P)) break;
            }
            asm volatile("" ::: "memory");
            const char* g = P.ws + PG0 + pgb(seg, par) + PG_HV + (h >> 1) * 3 * BLK;
#pragma unroll
            for (int k = 0; k < 3; ++k) {
                const int f = (h & 1) * 12 + wave * 3 + k;
                dmaq<0>(g + (f >> 3) * BLK + (f & 7) * 1024, ln * 16, L_QX + (seg & 1) * 12288 + (wave * 3 + k) * 1024);
            }
            return true;
        };
#pragma unroll 1
        for (int t = 0; t < HT0; ++t) {
            if (L) xn_wait(P, rr, t, cpy);
            const unsigned l16 = lane_id() * 16;
            const char* g = P.ws + PG0 + PG_XN + L * PG_LX + pgb(t, par);
            dma4(g + blin(wave * 4096), l16, L_XA + wave * 4096); dma4(g + blin(20480 + wave * 4096), l16, L_XA + 16384 + wave * 4096);
            dmaq<0>(g + blin(16384) + wave * 1024, l16, L_XB + wave * 1024); dmaq<0>(g + blin(20480 + 16384) + wave * 1024, l16, L_XB + 4096 + wave * 1024);
            wait_vm<0>();
            bar();
            C1Regs C;
            const WRegs& W = wf;
            const unsigned xa = L_XA + wave * 2048 + l16, qs = L_QX + (wave & 1) * 3072 + ((wave >> 1) & 1) * 8 + t * 12288;
            asm volatile(ASM_XG_V : XG_OUT : XG_IN : XG_CLOB);
            if (t == 0) bar();
        }
        bool have = true;
        unsigned tb2 = 0;
        for (int t = 0; t < NSEG; ++t) {
            if (!have && t + 1 < NSEG) { vfetch(t + 1, -1); wait_vm<0>(); }
            ST(7);
            bar();
            have = t != 1 && t + 2 < NSEG && vfetch(t + 2, VTRY);
            C1Regs C;
            asm volatile("ds_read_b128 v[200:203], %4\n\tds_read_b128 v[204:207], %4 offset:1024\n\tds_read_b128 v[228:231], %4 offset:2048\n\tds_read_b128 v[232:235], %4 offset:3072\n\t"
                         "v_mov_b64 v[224:225], 0\n\tv_mov_b64 v[226:227], 0\n\tv_mov_b64 v[244:245], 0\n\tv_mov_b64 v[246:247], 0\n\tv_mov_b64 v[248:249], 0\n\tv_mov_b64 v[250:251], 0\n\ts_waitcnt lgkmcnt(0)"
                         : XG_OUT : "v"(wl) : "memory");
            ST(6);
            while ((unsigned)__builtin_amdgcn_s_memtime() - tb2 < VTD) __builtin_amdgcn_s_sleep(1);
            crit1<decltype(Cc)::value>(wf, C, rr, RG_XM + (t & (NSLOT - 1)) * XM_SLOT + (cpy >> 1) * XM_PAIR + (cpy & 1) * BLK, wave, wl, dsto, budget STARG);
            bar();
            tb2 = (unsigned)__builtin_amdgcn_s_memtime();
            ST(4);
            STC(5);
            STFLUSH(t);
        }
    };
    if (wave < 2) runv(std::integral_constant<int, 3>{});
    else if (wave < 4) runv(std::integral_constant<int, 2>{});
    else {
        const int w4 = wave - 4;
        const rsrc_t rd = mkrsrc(P.ws + PG0, PG_L);
        load_wo(wf, P.w[3], wave, h);
        bar();
        bar();
        for (int t = -1; t < NSEG; ++t) {
            const unsigned slot = t & (NSLOT - 1);
            bar();
            ST(7);
#pragma unroll 1
            for (int n = t + (t != 1) + (t == 0); n <= t + 1 && n < NSEG; ++n) {
                const unsigned l16 = lane_id() * 16;
                const unsigned wl = L_W + wave * 10240 + l16, va = L_QX + (n & 1) * 12288 + l16;
                unsigned so = ((n & 7) << 22) + pgb(64 + (w4 >> 1), par) + ((w4 & 1) * 40 + (h >> 3)) * BLK + (h & 7) * 1024;
                asm volatile(ASM_VOX : [so] "+s"(so) : WREGS(wf), [l16] "v"(l16), [wl] "v"(wl), [va] "v"(va), [rd] "s"(rd) : "memory", "scc", "v200", "v201", "v202", "v203", "v204", "v205", "v206", "v207", "v208", "v209", "v210", "v211", "v212", "v213", "v214", "v215", "v216", "v217", "v218", "v219", "v220", "v221", "v222", "v223", "v224", "v225", "v226", "v227", "v228", "v229", "v230", "v231", "v232", "v233", "v234", "v235", "v236", "v237", "v238", "v239", "v240", "v241", "v242", "v243", "v244", "v245", "v246", "v247", "v248", "v249", "v250", "v251");
                if (n < 2) {
                    wait_vm<0>();
                    const unsigned l4 = lane_id() * 4;
                    if (l4 < 128) __builtin_amdgcn_raw_buffer_store_b32(n + 1, rr, l4, RG_FV + (w4 >> 1) * BLK + ((w4 & 1) * 32 + h) * 128, 16);
                }
            }
            if (t < 0) continue;
            ST(0);
            unsigned wl = L_W + wave * 10240 + lane_id() * 16;
            bf16x16 lw = lds_frag(wl, 1024);
            u32x32 cz;
            asm volatile("v_mov_b64 v[220:221], 0\n\tv_mov_b64 v[222:223], 0\n\tv_mov_b64 v[224:225], 0\n\tv_mov_b64 v[226:227], 0\n\tv_mov_b64 v[228:229], 0\n\tv_mov_b64 v[230:231], 0\n\tv_mov_b64 v[232:233], 0\n\tv_mov_b64 v[234:235], 0\n\tv_mov_b64 v[236:237], 0\n\tv_mov_b64 v[238:239], 0\n\tv_mov_b64 v[240:241], 0\n\tv_mov_b64 v[242:243], 0\n\tv_mov_b64 v[244:245], 0\n\tv_mov_b64 v[246:247], 0\n\tv_mov_b64 v[248:249], 0\n\tv_mov_b64 v[250:251], 0" : "={v[220:251]}"(cz));
            unsigned vo = ((wl >> 8) & 1) * (1 + (w4 & 1) * 2) * BLK + ((wl >> 9) & 1) * 1024 + (wl & 255);
            asm volatile("" : "+{v[212:219]}"(lw), "+{v[220:251]}"(cz), "+v"(wl), "+v"(vo));
            unsigned l16 = wl & 1023u;
            bar();
            wait_vm<0>();
            if (l16 < 512 && t && t + 1 < NSEG) __builtin_amdgcn_raw_buffer_store_b32(t + 2, rr, l16 >> 2, RG_FV + (w4 >> 1) * BLK + ((w4 & 1) * 32 + h) * 128, 16);
            ST(1);
            const int sidx = (wl & 128u) ? 0xEEEE : 0x4444;
            unsigned so = RG_VM + slot * VM_SLOT + (unsigned)((w4 * 10 - (w4 & 1) + (h >> 4) * 2) * BLK + ((h >> 2) & 3) * 2048 + (h & 3) * 256);
            ST(3);
            ASM_ST_DECL;
            asm volatile(ASM_CRIT2 : [so] "+s"(so), "+{v[212:219]}"(lw), "+{v200}"(l16), "+{v[220:251]}"(cz) ASM_ST_OUT
                         : WREGS(wf), [wl] "v"(wl), [vo] "v"(vo), [sidx] "v"(sidx), [rr] "s"(rr)
                         : "memory", "scc", "v201", "v202", "v203", "v204", "v205", "v206", "v207", "v208", "v209", "v210", "v211");
            ASM_ST_SAVE(4);
            ST(5);
            STFLUSH(t);
        }
    }
}

constexpr unsigned R_RED = 0;
constexpr unsigned R_PART = 16384;
__device__ __forceinline__ void r_role(const Params& P, int wave, int L, int par, int jj, int cpy) {
    const int lane = lane_id(), l15 = lane & 15, l4 = lane >> 4;
    const unsigned l16 = lane * 16;
    const rsrc_t rr = mkrsrc(P.ws + par * 8192, RG_L), rd = mkrsrc(P.ws + PG0, PG_L);
    const int sidx = (l15 < 8) ? 0x4444 : 0xEEEE;
    unsigned offp[3];
    offp[0] = RG_XP + (cpy >> 1) * XP_PAIR + (cpy & 1) * BLK + (1 + (wave >> 2)) * 2 * BLK + (wave & 3) * 2048 + (PLAY ? (l4 >> 1) * 512 + (((l15 >> 2) & 1) + (l15 & 2)) * 128 + (l4 & 1) * 64 + (l15 >> 3) * 32 + (l15 & 1) * 16 : l16);
    offp[2] = (l15 < 8) ? RG_XP + (cpy >> 1) * XP_PAIR + (cpy & 1) * BLK + wave * 512 + l4 * 128 + l15 * 16 : OOB;
    const unsigned offv = RG_VM + ((jj >> 1) * 4 + (wave >> 2) * 2 + (jj & 1)) * BLK + (wave & 3) * 2048 + l16;
    const int j = jj * 2 + ((wave >> 1) & 1), fl = (wave & 1) * 32 + (lane & 31);
    const unsigned offo = RG_XM + (wave >> 2) * XM_PAIR + (lane >> 5) * BLK + (j & 31) * 256 + (j >> 5) * 2 * BLK + fl * 4;
    init_wait(P, wave, L);
    MARK(!L && jj == 0 && wave == 0, 5);
    {
        const int row = (lane >> 2) & 7, col = 16 * j + 4 * (lane & 3) + 2 * (wave & 1);
        __builtin_amdgcn_raw_buffer_store_b32(nosent(*(const unsigned*)(P.mem0 + row * DM + col)), rr, offo, 0, 16);
    }
    auto red_addr = [](int fl) { return R_RED + fl * 80 + (fl >> 4) * 16; };
    const unsigned rw0 = (lane >> 5) * 5248 + red_addr((((l15 >> 1) & 1) * 8 + 4 * (l4 & 1)) * 4 + (l15 >> 2)) + (l15 & 1) * 4 + (wave ^ (l15 >> 3)) * 8;
    const unsigned rrd = ((wave >> 1) & 1) * 5248 + red_addr(fl) + (lane >> 5) * 32;
    u32x32 vx;
    const unsigned fo = (lane & 3) * 128, fs = RG_FV + (jj / 10) * BLK + (((jj / 5) & 1) * 32 + wave * 4) * 128, vxo = pgb(64 + jj / 10, par) + (unsigned)((jj % 10) * 8 + (wave >> 1)) * BLK + (wave & 1) * 4096;
    const rsrc_t rp = mkrsrc(P.ws + PG0, PG_L);
    const unsigned xo = (l4 >> 1) * 256 + l15 * 16 + (l4 & 1) * 8, pw = R_PART + wave * 1024 + l16, pr = R_PART + (wave & 3) * 8192 + l16;
    const int fin = wave >> 2;
    asm volatile("" : "={v[128:159]}"(vx));
    auto vx_fetch = [&](int t) {
        for (int spins = 0;; ++spins) {
            const unsigned f = __builtin_amdgcn_raw_buffer_load_b32(rr, fo, fs, 16);
            if (__all(f > (unsigned)t)) break;
            __builtin_amdgcn_s_sleep(2);
            if (spin_fail(spins, P)) break;
        }
        const unsigned sv = ((t & 7) << 22) + vxo;
        asm volatile("buffer_load_dwordx4 v[128:131], %1, %2, %3 offen sc1\n\tbuffer_load_dwordx4 v[132:135], %1, %2, %3 offen offset:1024 sc1\n\t"
                     "buffer_load_dwordx4 v[136:139], %1, %2, %3 offen offset:2048 sc1\n\tbuffer_load_dwordx4 v[140:143], %1, %2, %3 offen offset:3072 sc1\n\t"
                     "buffer_load_dwordx4 v[144:147], %1, %2, %4 offen sc1\n\tbuffer_load_dwordx4 v[148:151], %1, %2, %4 offen offset:1024 sc1\n\t"
                     "buffer_load_dwordx4 v[152:155], %1, %2, %4 offen offset:2048 sc1\n\tbuffer_load_dwordx4 v[156:159], %1, %2, %4 offen offset:3072 sc1"
                     : "+{v[128:159]}"(vx) : "v"(l16), "s"(rp), "s"(sv), "s"(sv + 4 * BLK) : "memory");
    };
    vx_fetch(0);
    unsigned budget = 1u << 18, tpub = 0;
    STDECL;
    LDECL;
    for (int t = 0; t < NSEG; ++t) {
        const unsigned slot = t & (NSLOT - 1);
        ST(0);
        while ((unsigned)__builtin_amdgcn_s_memtime() - tpub < RTD) __builtin_amdgcn_s_sleep(1);
        u32x8 vm, pv;
        u32x4 pm;
        {
            unsigned a;
            ASM_ST_DECL;
            asm volatile(ASM_RCRIT : "=&{v[176:183]}"(vm), "=&{v[184:191]}"(pv), "=&{v[192:195]}"(pm), [a] "=&s"(a), [n] "+s"(budget) ASM_ST_OUT ASM_ST2_OUT ASM_ST3_OUT
                         : [rs] "s"(rr), [ov] "v"(offv), [sv] "s"(slot * VM_SLOT), [o0] "v"(offp[0]), [o2] "v"(offp[2]), [sp] "s"(slot * XP_SLOT), [fo] "v"(fo), [sg] "s"(RG_SG + (cpy >> 1) * BLK + (cpy & 1) * 4096 + wave * 512), [tt] "s"(t),
                           "{v[128:159]}"(vx), [sidx] "v"(sidx), [rw] "v"(rw0), [rd] "v"(rrd), [oo] "v"(offo),
                           [so] "s"(((t + 1) & (NSLOT - 1)) * XM_SLOT), [pub] "s"(NSEG - 1 - t)
                         : "memory", "vcc", "scc", "v160", "v161", "v162", "v163", "v164", "v165", "v166", "v167", "v196", "v197", "v200", "v201", "v202", "v203", "v204", "v205", "v206", "v207");
            ASM_ST_SAVE(2); ASM_ST2_SAVE(1); ASM_ST3_SAVE(6);
        }
        ST(3);
        LA();
        MARK(!L && jj == 0 && wave == 0 && t < 4, 10 + t);
        tpub = (unsigned)__builtin_amdgcn_s_memtime();
        __builtin_amdgcn_s_sleep(RXDEL);
        unsigned early;
#pragma unroll 1
        for (int spins = 1023;; spins += 1024) {
            unsigned n;
            const unsigned sx = pgb(t, par) + blin(((wave >> 1) & 1) * 20480 + jj * 1024) + (wave & 1) * 512;
            const unsigned pxb = RG_PX + slot * PX_SLOT + wave * BLK, sv = (((t + 1) & 7) << 22) + vxo;
            ASM_ST_DECL;
            asm volatile(ASM_RX : "+{v[128:159]}"(vx), [n] "=&s"(n), [a] "=&s"(early) ASM_ST_OUT ASM_ST2_OUT
                         : "{v[176:183]}"(vm), "{v[184:191]}"(pv), "{v[192:195]}"(pm), [l16] "v"(l16), [rs] "s"(rr), [s0] "s"(pxb), [s1] "s"(pxb + 4096), [rd] "s"(rd), [rx] "s"(rd), [xo] "v"(xo),
                           [fo] "v"(fo), [fs] "s"(fs), [tn] "s"(t + 1), [sv0] "s"(sv), [sv1] "s"(sv + 4 * BLK), [rp] "s"(rp), [sx] "s"(PG_XR + L * PG_LX + sx), [sa] "s"(PG_XA + sx),
                           [pw] "v"(pw), [pr] "v"(pr), [fin] "s"(fin)
                         : "memory", "vcc", "scc", "v96", "v97", "v98", "v99", "v100", "v101", "v102", "v103", "v104", "v105", "v106", "v107", "v108", "v109", "v110", "v111", "v112", "v113", "v114", "v115", "v116", "v117", "v118", "v119", "v120", "v121", "v122", "v123", "v124", "v125", "v126", "v127", "v196", "v200", "v201", "v202", "v203", "v204", "v205", "v206", "v207", "v208", "v209", "v210", "v211", "v212", "v213", "v214", "v215", "v216", "v217", "v218", "v219", "v220", "v221", "v222", "v223", "v224", "v225", "v226", "v227", "v228", "v229", "v230", "v231", "v240", "v241", "v242", "v243", "v248", "v250", "v251");
            ASM_ST_SAVE(4); ASM_ST2_SAVE(5);
            if (__builtin_amdgcn_readfirstlane(n)) break;
            early = 0;
            if (spin_fail(spins, P)) { bar(); break; }
        }
        if (t + 1 < NSEG && !__builtin_amdgcn_readfirstlane(early)) vx_fetch(t + 1);
        st128(sent4(), rr, offv, slot * VM_SLOT); st128(sent4(), rr, offv + 1024, slot * VM_SLOT);
        __builtin_amdgcn_raw_buffer_store_b32(SENT, rr, offo, slot * XM_SLOT, 16);
        if (wave < 2 && t + AH < NSEG) st128(sent4(), rd, l16, PG_XA + pgb(t + AH, par) + blin(wave * 20480 + jj * 1024));
        ST(7);
        LB(t);
        STFLUSH(t);
    }
    LF();
}

template <int WHICH>
__device__ __forceinline__ void w_role(const Params& P, int wave, int L, int par, int cu) {
    constexpr int KS = 20, N = (WHICH == 3) ? FF : DM, NU = 2 * NSEG;
    constexpr unsigned D_IN = WHICH == 1 ? PG_XA : WHICH == 2 ? PG_HS : PG_X1;
    constexpr unsigned D_OUT = WHICH == 1 ? PG_HS : WHICH == 2 ? PG_X1 : PG_H2;
    if (WHICH != 3 && cu * 8 + wave >= 20) {
        for (int u = 0; u <= NU; ++u) bar();
        return;
    }
    auto run = [&](auto Cc) {
    constexpr bool LO = decltype(Cc)::value;
    const int ln = lane_id(), l15 = ln & 15, l4 = ln >> 4;
    const unsigned l16 = ln * 16;
    const rsrc_t rd = mkrsrc(P.ws + PG0, PG_L);
    const int nt0 = (cu * 8 + wave) * 2;
    u32x32 w[5];
    {
        u32x4 f[2 * KS];
        load_wn<2, KS, N>(f, P.w[3 + WHICH], DM, wave, nt0, 0);
#pragma unroll
        for (int n = 0; n < 5; ++n) {
            u32x8 q[4];
#pragma unroll
            for (int i = 0; i < 4; ++i) q[i] = __builtin_shufflevector(f[n * 8 + 2 * i], f[n * 8 + 2 * i + 1], 0, 1, 2, 3, 4, 5, 6, 7);
            w[n] = cat32(q[0], q[1], q[2], q[3]);
        }
        asm volatile("" : "+{v[0:31]}"(w[0]), "+{v[32:63]}"(w[1]), "+{v[64:95]}"(w[2]), "+{v[96:127]}"(w[3]), "+{v[128:159]}"(w[4]));
    }
    init_wait(P, wave, L);
    bar();
    const int ocol = nt0 * 16 + (l4 & 1) * 16 + (l4 >> 1) * 8;
    const unsigned o3 = (unsigned)((l4 & 1) * 512 + (l4 >> 1) * 256 + l15 * 16), ro = (l4 >> 1) * 256 + l15 * 16 + (l4 & 1) * 8;
    const int grp = nt0 / 10, sh = ((grp & 1) ? -1 : 1) * ((grp >> 1) + 1);
    if (WHICH == 1) {
        unsigned zv;
        asm volatile("v_mov_b32 %0, 0" : "=v"(zv));
        st128(((u32x4){zv, zv, zv, zv}), rd, l15 <= (grp >> 1) ? toff((grp & 1) ? S_ - 1 - l15 : l15, ocol, par) : OOB, PG_HS);
    }
    auto in0 = [&](int u) { return D_IN + pgb(u >> 1, par) + blin((u & 1) * 20480 + wave * 4096); };
    auto in1 = [&](int u) { return D_IN + pgb(u >> 1, par) + blin((u & 1) * 20480 + 16384) + wave * 1024; };
    u32x16 va;
    u32x4 vb;
    u32x8 cs, k8;
#define WT_W "{v[0:31]}"(w[0]), "{v[32:63]}"(w[1]), "{v[64:95]}"(w[2]), "{v[96:127]}"(w[3]), "{v[128:159]}"(w[4])
    asm volatile("v_mov_b32 v216, 0x3f803f80\n\tv_mov_b32 v217, v216\n\tv_mov_b32 v218, v216\n\tv_mov_b32 v219, v216\n\tv_mov_b32 v220, %[c0]\n\tv_mov_b32 v232, -1\n\tv_mov_b32 v236, -1\n\t"
                 "v_mov_b32 v240, -1\n\tv_mov_b32 v244, -1\n\tv_mov_b32 v248, -1\n\t" ASM_WCS
                 : "=&{v[196:203]}"(cs), "=&{v[216:223]}"(k8), "=&{v[232:247]}"(va), "=&{v[248:251]}"(vb) : WT_W, [c0] "s"(-2.0f * 0.7978845608028654f * 1.4426950408889634f));
    unsigned la = l16, lw = wave * 8192 + l16, sr = l4 * 8192 + ((l15 >> 2) * 16 + l15) * 16 + (l15 & 3) * 4, budget = 1u << 18;
    wait_vm<0>();
    STDECL;
    LDECL;
#pragma unroll 1
    for (int u = 0; u < NU; ++u) {
        const int mt = u & 1;
        const unsigned pg = pgb(u >> 1, par), c3 = D_OUT + blin(mt * (N * 32) + (nt0 >> 1) * 1024);
        unsigned oa = o3, ob = 0, so = pg + c3, sa = u + 2 * AH < NU ? pgb((u >> 1) + AH, par) + c3 : pg + PG_SS + 4096, a;
        if (WHICH == 1) {
            const int orow = u * 16 + l15 + sh;
            ob = toff(orow + AH * SEG, ocol, par);
            oa = (unsigned)orow < (unsigned)S_ ? (ob ^ 8192u) - (AH << 21) : OOB;
            if (orow + AH * SEG >= S_ + (sh < 0 ? sh : 0)) ob = OOB;
            so = sa = D_OUT;
        }
        if (mt) { ST(0); LB(u >> 1); } else LA();
        ASM_ST_DECL;
#define WT_A WT_W, [la] "v"(la), [l16] "v"(l16), [oa] "v"(oa), [rs] "s"(rd), [so] "s"(so), [sa] "s"(sa)
#define WT_G [sr] "v"(sr), [kd] "s"(1.0f / DM), [ke] "s"(1e-5f), [k1] "s"(-2.0f * 0.7978845608028654f * 1.4426950408889634f * 0.044715f)
#define WT_1 WT_G, [ob] "v"(ob), [ss] "s"(pg + PG_SS + mt * 128), [sw] "s"(nt0)
#define WT_2 [ro] "v"(ro), [sx] "s"(pg + PG_XA + blin(mt * 20480 + nt0 * 512)), [ss] "s"(pg + PG_SS + mt * 128)
#define WT_C "memory", "vcc", "scc", "v160", "v161", "v162", "v163", "v164", "v165", "v166", "v167", "v168", "v169", "v170", "v171", "v172", "v173", "v174", "v175", "v176", "v177", "v178", "v179", "v180", "v181", "v182", "v183", "v184", "v185", "v186", "v187", "v188", "v189", "v190", "v191", "v192", "v193", "v194", "v195", "v204", "v205", "v206", "v207", "v208", "v209", "v210", "v211", "v212", "v213", "v214", "v215", "v252"
#define WT_LO(A, ...) asm volatile(A : "+{v[196:203]}"(cs), "+{v[216:223]}"(k8), "+{v[232:247]}"(va), "+{v[248:251]}"(vb), [n] "+s"(budget), [a] "=&s"(a) ASM_ST_OUT ASM_ST2_OUT ASM_ST3_OUT \
                              : WT_A, __VA_ARGS__, [lw] "v"(lw), [i0] "s"(in0(u)), [i1] "s"(in1(u)), [n0] "s"(in0(u + 1)), [n1] "s"(in1(u + 1)) : WT_C)
#define WT_HI(A, ...) asm volatile(A : "+{v[196:203]}"(cs), "+{v[216:223]}"(k8) ASM_ST_OUT ASM_ST3_OUT : WT_A, __VA_ARGS__ : WT_C)
        if constexpr (LO) { if constexpr (WHICH == 1) WT_LO(ASM_WT1_LO, WT_1); else if constexpr (WHICH == 2) WT_LO(ASM_WT2_LO, WT_2); else WT_LO(ASM_WT3_LO, WT_G); }
        else { if constexpr (WHICH == 1) WT_HI(ASM_WT1_HI, WT_1); else if constexpr (WHICH == 2) WT_HI(ASM_WT2_HI, WT_2); else WT_HI(ASM_WT3_HI, WT_G); }
        la ^= 32768u; lw ^= 32768u; sr ^= 32768u;
    }
    wait_vm<0>();
    if constexpr (LO) asm volatile("" :: "{v[232:247]}"(va), "{v[248:251]}"(vb));
    LF();
    };
    if (wave < 4) run(std::true_type{}); else run(std::false_type{});
}

__device__ __forceinline__ void w4_role(const Params& P, int wave, int L, int par, int cu) {
    constexpr unsigned W_FL = 122880;
    const int ln = lane_id(), l15 = ln & 15, l4 = ln >> 4;
    const unsigned l16 = ln * 16;
    const rsrc_t rd = mkrsrc(P.ws + PG0, PG_L);
    lds_st<unsigned>(W_FL + wave * 4, 0u);
    STDECL;
    if (wave < 6) {
        u32x32 w[5];
        {
            u32x4 g[40];
            load_wn<5, 8, DM>(g, P.w[7], FF, wave, cu * 5, wave * 8);
#pragma unroll
            for (int n = 0; n < 5; ++n) {
                u32x8 f[4];
#pragma unroll
                for (int i = 0; i < 4; ++i) f[i] = __builtin_shufflevector(g[n * 8 + 2 * i], g[n * 8 + 2 * i + 1], 0, 1, 2, 3, 4, 5, 6, 7);
                w[n] = cat32(f[0], f[1], f[2], f[3]);
            }
            asm volatile("" : "+{v[0:31]}"(w[0]), "+{v[32:63]}"(w[1]), "+{v[64:95]}"(w[2]), "+{v[96:127]}"(w[3]), "+{v[128:159]}"(w[4]));
        }
        init_wait(P, wave, L);
        bar();
        unsigned budget = 1u << 18;
        for (int u = 0; u < 2 * NSEG; ++u) {
            if (u & 1) ST(0);
            for (int spins = 0;; ++spins) {
                asm volatile("" ::: "memory");
                if ((int)__builtin_amdgcn_readfirstlane(lds_ld<unsigned>(W_FL + 24 + (u & 1) * 4)) + 3 >= u) break;
                __builtin_amdgcn_s_sleep(2);
                if (spin_fail(spins, P)) break;
            }
            const unsigned gb = PG_H2 + pgb(u >> 1, par) + ((u & 1) * 6 + wave) * BLK;
            ASM_ST_DECL;
            asm volatile(ASM_W4_M : [n] "+s"(budget) ASM_ST2_OUT
                         : "{v[0:31]}"(w[0]), "{v[32:63]}"(w[1]), "{v[64:95]}"(w[2]), "{v[96:127]}"(w[3]), "{v[128:159]}"(w[4]),
                           [ld] "v"(l16 + wave * 1024 + (u & 3) * 30720), [l16] "v"(l16), [rs] "s"(rd), [s0] "s"(gb), [s1] "s"(gb + 4096), [un] "s"(u + 1), [fl] "v"(W_FL + wave * 4)
                         : "memory", "vcc", "scc", "v160", "v161", "v162", "v163", "v164", "v165", "v166", "v167", "v168", "v169", "v170", "v171", "v172", "v173", "v174", "v175", "v176", "v177", "v178", "v179", "v180", "v181", "v182", "v183", "v184", "v185", "v186", "v187", "v188", "v189", "v190", "v191", "v192", "v193", "v194", "v195", "v196", "v197", "v198", "v199", "v200", "v201", "v202", "v203", "v204", "v205", "v206", "v207", "v208", "v209", "v210", "v211", "v212");
            if (u & 1) { ASM_ST2_SAVE(1); ST(2); STFLUSH(u >> 1); }
        }
    } else {
        const int mt = wave - 6;
        bar();
        const rsrc_t rr = mkrsrc(P.ws + par * 8192, RG_L), ro = mkrsrc(P.out, ACT);
        const unsigned c0 = mt * 20480 + cu * 2560, o8 = (l4 >> 1) * 256 + l15 * 16 + (l4 & 1) * 8;
        const unsigned o16a = blin(c0 + (l4 & 1) * 512) + (l4 >> 1) * 256 + l15 * 16, o16b = blin(c0 + 1024 + (l4 & 1) * 512) + (l4 >> 1) * 256 + l15 * 16, o8c = blin(c0 + 2048) + o8;
        const unsigned q8 = l15 * (DM * 2) + l4 * 8, q16 = l15 * (DM * 2) + (l4 & 1) * 32 + (l4 >> 1) * 16;
        for (int t = 0; t < NSEG; ++t) {
            const unsigned sb = pgb(t, par), sn = (sb ^ 8192) + PG_LX;
            ST(0);
            f32x4 res[5];
            for (int spins = 0;; ++spins) {
                u32x2 r1[5], r2[5];
                unsigned m = 0;
#pragma unroll
                for (int n = 0; n < 5; ++n) { r1[n] = LD64(rd, o8 + blin(c0 + n * 512), PG_X1 + sb); r2[n] = LD64(rd, o8 + blin(c0 + n * 512), PG_XA + sb); }
#pragma unroll
                for (int n = 0; n < 5; ++n) {
                    m = umax4(m, (u32x4){r1[n][0], r1[n][1], r2[n][0], r2[n][1]});
                    res[n] = (f32x4){lo2f(r1[n][0]), hi2f(r1[n][0]), lo2f(r1[n][1]), hi2f(r1[n][1])} + (f32x4){lo2f(r2[n][0]), hi2f(r2[n][0]), lo2f(r2[n][1]), hi2f(r2[n][1])};
                }
                if (__all(m != SENT)) break;
                __builtin_amdgcn_s_sleep(8);
                if (spin_fail(spins * 4 + 3, P)) break;
            }
            asm volatile("" : "+v"(res[0]), "+v"(res[1]), "+v"(res[2]), "+v"(res[3]), "+v"(res[4]));
            ST(1);
            const unsigned u = t * 2 + mt;
            for (int spins = 0;; ++spins) {
                asm volatile("" ::: "memory");
                if (__all(lds_ld<unsigned>(W_FL + (ln < 6 ? ln : 0) * 4) > u)) break;
                __builtin_amdgcn_s_sleep(1);
                if (spin_fail(spins, P)) break;
            }
            asm volatile("" ::: "memory");
            ST(2);
            {
                const unsigned la = (u & 3) * 30720 + l16;
                f32x4 c[30];
#pragma unroll
                for (int i = 0; i < 30; ++i) c[i] = lds_ld<f32x4>(la + ((i % 5) * 6 + i / 5) * 1024);
#pragma unroll
                for (int i = 0; i < 30; ++i) { res[i % 5] += c[i]; asm volatile("" : "+v"(res[i % 5])); }
                asm volatile("" ::: "memory");
                lds_st<unsigned>(W_FL + wave * 4, u + 1);
            }
            u32x2 o[5];
#pragma unroll
            for (int n = 0; n < 5; ++n) o[n] = (u32x2){pack2(res[n][0], res[n][1]), pack2(res[n][2], res[n][3])};
            if (L) {
                const unsigned ob = (unsigned)(t * 2 + mt) * 20480 + cu * 160;
                st128_<0>(merge16(o[0], o[1]), ro, q16, ob); st128_<0>(merge16(o[2], o[3]), ro, q16, ob + 64);
                __builtin_amdgcn_raw_buffer_store_b64(o[4], ro, q8, ob + 128, 0);
            } else {
                float s = 0.f, q = 0.f;
                const bf16x2 one2 = __builtin_bit_cast(bf16x2, 0x3F803F80u);
#pragma unroll
                for (int e = 0; e < 10; ++e) {
                    const unsigned u = o[e >> 1][e & 1];
                    const bf16x2 x = __builtin_bit_cast(bf16x2, u);
                    s = __builtin_amdgcn_fdot2_f32_bf16(x, one2, s, false);
                    q = __builtin_amdgcn_fdot2_f32_bf16(x, x, q, false);
                }
                sum2_rows(s, q);
                const unsigned st = RG_ST + (unsigned)(t >> 2) * BLK + (unsigned)((t & 3) * 2 + mt) * 1024;
                __builtin_amdgcn_raw_buffer_store_b64((u32x2){nosent(__float_as_uint(s)), nosent(__float_as_uint(q))}, rr, ln < 16 ? ln * 8 : OOB, st + cu * 128, 16);
                ST(3);
                st128(merge16(o[0], o[1]), rd, o16a, PG_XR + sn); st128(merge16(o[2], o[3]), rd, o16b, PG_XR + sn);
                __builtin_amdgcn_raw_buffer_store_b64(o[4], rd, o8c, PG_XR + sn, 16);
                __builtin_amdgcn_s_sleep(W4SL);
                for (int spins = 0;; ++spins) {
                    const u32x2 a = LD64(rr, ln * 8, st), b = LD64(rr, ln * 8 + 512, st);
                    s = __uint_as_float(a[0]) + __uint_as_float(b[0]); q = __uint_as_float(a[1]) + __uint_as_float(b[1]);
                    if (__all(umax4(0u, (u32x4){a[0], a[1], b[0], b[1]}) != SENT)) break;
                    asm volatile("" ::: "memory");
                    if (spin_fail(spins, P)) break;
                }
                ST(4);
                sum2_rows(s, q);
                const float mean = s * (1.0f / DM), var = __builtin_fmaxf(q * (1.0f / DM) - mean * mean, 0.f);
                const float r = rcp(ln_den(var)), mr = -rbf(mean) * r;
#pragma unroll
                for (int n = 0; n < 5; ++n)
                    o[n] = (u32x2){pack2(__builtin_fmaf(res[n][0], r, mr), __builtin_fmaf(res[n][1], r, mr)), pack2(__builtin_fmaf(res[n][2], r, mr), __builtin_fmaf(res[n][3], r, mr))};
                st128(merge16(o[0], o[1]), rd, o16a, PG_XN + sn); st128(merge16(o[2], o[3]), rd, o16b, PG_XN + sn);
                __builtin_amdgcn_raw_buffer_store_b64(o[4], rd, o8c, PG_XN + sn, 16);
                wait_vm<0>();
                st128(((u32x4){(unsigned)t + 1, (unsigned)t + 1, (unsigned)t + 1, (unsigned)t + 1}), mkrsrc(P.ws + (par ^ 1) * 8192, RG_L), ln < 32 ? (ln >> 3) * 2048 + (ln & 7) * 16 : OOB, RG_FX + (cu * 2 + mt) * 128);
            }
            ST(5);
            STFLUSH(t);
        }
    }
}

__device__ __forceinline__ void setup(const Params& P, int wave, int mt, int par0, int ph) {
    const bool xw = wave < 4 && (mt >= 2 * XS0) == (ph != 0);
    const int tid = threadIdx.x & 255, r15 = tid & 15, slot = tid >> 4;
    const rsrc_t rw = mkrsrc(P.ws, WS_END);
    u32x4 v[5];
    float s = 0.f, q = 0.f;
    if (xw) {
#pragma unroll
        for (int c = 0; c < 5; ++c) v[c] = *(const u32x4*)(P.x + (size_t)(mt * 16 + r15) * DM + (slot * 5 + c) * 8);
#pragma unroll
        for (int c = 0; c < 5; ++c)
#pragma unroll
            for (int e = 0; e < 4; ++e) s += lo2f(v[c][e]) + hi2f(v[c][e]);
        lds_st<float>((slot * 17 + r15) * 4, s);
    } else if (wave >= 4) {
        constexpr unsigned N0 = RG_ST / BLK, N1 = 4 * N0 + 16, N2 = PG_SS / BLK;
        const u32x4 s4 = sent4();
        for (unsigned k = mt; k < 2 * (N1 + 4 * N2); k += 128) {
            const unsigned i = k >> 1;
            const unsigned o = i < N1 ? (i < 4 * N0 ? (i / N0) * RG_SLOT + (i % N0) * BLK : (i - 3 * N0) * BLK) + (par0 ^ ph) * 8192 : PG0 + pgb((i - N1) / N2, par0 ^ ph) + ((i - N1) % N2) * BLK;
            st128(s4, rw, tid * 16, o + (k & 1) * 4096);
        }
    }
    bar();
    float mean = 0.f;
    if (xw) {
        s = 0.f;
#pragma unroll
        for (int k = 0; k < 16; ++k) s += lds_ld<float>((k * 17 + r15) * 4);
        mean = s * (1.0f / DM);
#pragma unroll
        for (int c = 0; c < 5; ++c)
#pragma unroll
            for (int e = 0; e < 4; ++e) { const float a = lo2f(v[c][e]) - mean, b = hi2f(v[c][e]) - mean; q += a * a + b * b; }
        lds_st<float>(2048 + (slot * 17 + r15) * 4, q);
    }
    bar();
    if (xw) {
        q = 0.f;
#pragma unroll
        for (int k = 0; k < 16; ++k) q += lds_ld<float>(2048 + (k * 17 + r15) * 4);
        const float m1 = rbf(mean), r = 1.0f / ln_den(q * (1.0f / DM));
#pragma unroll
        for (int c = 0; c < 5; ++c) {
            const unsigned o = toff(mt * 16 + r15, (slot * 5 + c) * 8, par0);
            st128(v[c], rw, o, PG0 + PG_XR);
            u32x4 n;
#pragma unroll
            for (int e = 0; e < 4; ++e) n[e] = pack2(rbf(lo2f(v[c][e]) - m1) * r, rbf(hi2f(v[c][e]) - m1) * r);
            st128(n, rw, o, PG0 + PG_XN);
        }
    }
    wait_vm<0>();
    bar();
    if (threadIdx.x < 8) __hip_atomic_fetch_add((unsigned*)(P.ws + FL0 + FL_IN + (ph * 8 + threadIdx.x) * 128), 1u, RLX_AGENT);
}

__global__ __launch_bounds__(512, 2) void mega_kernel(Params P) {
    const int wave = __builtin_amdgcn_readfirstlane(threadIdx.x >> 6);
    u32x4 cpf;
    {
        unsigned long long pc;
        unsigned len;
        asm volatile("s_getpc_b64 %0\n\ts_mov_b32 %1, _Z12prep1_kernel5Prep1-_Z11mega_kernel6Params" : "=s"(pc), "=s"(len));
        cpf = __builtin_amdgcn_raw_buffer_load_b128(mkrsrc((const void*)(pc & ~255ull), len), (blockIdx.x >> 3) * 8192 + threadIdx.x * 16, 0, 0);
    }
    if (threadIdx.x == 0) {
        const unsigned c0 = *(const unsigned*)(P.ws + FL0 + FL_BYTES);
        unsigned g = (unsigned)__builtin_amdgcn_s_getreg((3 << 11) | 20) & 7u, k;
        while ((k = __hip_atomic_fetch_add((unsigned*)(P.ws + FL0 + FL_TK + g * 128), 1u, RLX_AGENT)) >= 32u) g = (g + 1) & 7u;
        lds_st<unsigned>(0, (c0 & 3u) << 8 | g << 5 | k);
    }
    bar();
    const unsigned tk = __builtin_amdgcn_readfirstlane(lds_ld<unsigned>(0));
    asm volatile("" :: "v"(cpf));
    bar();
    const int g4 = (tk >> 5) & 3, k = tk & 31;
#define MAP 0
    const int L = (tk >> 7) & 1;
    const int c14 = tk >> 9;
    const int x = g4, r = k, cpy = x ^ c14;
    const int par = L ^ ((tk >> 8) & 1);
    if (L) {
#pragma unroll 1
        for (int ph = 0; ph < 2; ++ph) {
            setup(P, wave, (tk & 31) * 4 + g4, par ^ 1, ph);
            for (int k = ph * DLY_L; k < DLY_L; ++k) __builtin_amdgcn_s_sleep(32);
        }
    }
    {
        const int i = (r - 26) * 4 + x;
        const int d = (r < 8 ? DLY_S : r < 21 ? 0 : i < 4 ? DLY_H : DLY_W);
        for (int k = 0; k < d; ++k) __builtin_amdgcn_s_sleep(32);
    }
    if (r < 8) s_role(P, wave, L, par, x * 8 + r, cpy);
    else if (r < 16) v_role(P, wave ^ 4, L, par, x * 8 + r - 8, cpy);
    else if (r < 21) r_role(P, wave, L, par, ((r - 16) * 4 + cpy) ^ ((r - 16) * 4 + x >= 6 ? 1 : 0), cpy);
    else {
        const int i = (r - 26) * 4 + x;
        if (i < 4) h_role(P, wave, L, par, r < 25 ? x * 4 + r - 21 : 16 + x * 2 + r - 25, cpy);
        else if (i < 7) w_role<1>(P, wave, L, par, i - 4);
        else if (i < 10) w_role<2>(P, wave, L, par, i - 7);
        else if (i < 16) w_role<3>(P, wave, L, par, i - 10);
        else w4_role(P, wave, L, par, i - 16);
    }
}

struct Prep1 { char* ws; };
__global__ __launch_bounds__(256) void prep1_kernel(Prep1 A) {
    int b = blockIdx.x;
    if (b == 0) {
        if (threadIdx.x >= 64) return;
        const rsrc_t r = mkrsrc(A.ws, RG_SLOT);
        auto probe = [&](unsigned base, unsigned bit, int n, unsigned& m0, unsigned& m1) {
            m0 = m1 = ~0u;
            for (int i = 0; i < n; ++i) {
                const unsigned long long t0 = __builtin_amdgcn_s_memtime();
                __builtin_amdgcn_raw_buffer_store_b32(0u, r, base + (i & 1) * bit + (i >> 2) * (4096 + 768), 0, 16);
                asm volatile("s_waitcnt vmcnt(0)" ::: "memory");
                const unsigned d = (unsigned)(__builtin_amdgcn_s_memtime() - t0);
                if (i & 1) m1 = d < m1 ? d : m1; else m0 = d < m0 ? d : m0;
            }
        };
        unsigned a0, a1, b0, b1;
        probe(96 * BLK, 8192, 6, a0, a1);
        const unsigned xc = (unsigned)__builtin_amdgcn_s_getreg((3 << 11) | 20), h = a0 > a1 ? 1u : 0u;
        probe(96 * BLK + h * 8192, BLK, 8, b0, b1);
        if (threadIdx.x == 0) *(u32x4*)(A.ws + FL0 + FL_BYTES) = (u32x4){(h ^ ((xc >> 2) & 1u)) | ((b0 > b1 ? 0u : 2u) ^ ((xc & 1u) << 1)), a0 | a1 << 16, b0 | b1 << 16, xc};
        return;
    }
    b -= 1;
    ((u32x4*)(A.ws + RG_FV))[b * 256 + threadIdx.x] = (u32x4){0u, 0u, 0u, 0u};
    if (b == 0 && threadIdx.x < FL_BYTES / 16) ((u32x4*)(A.ws + FL0))[threadIdx.x] = (u32x4){0u, 0u, 0u, 0u};
}

extern "C" void kernel_launch(void* const* d_in, const int* in_sizes, int n_in, void* d_out, int out_size, void* d_ws,
                              size_t ws_size, hipStream_t stream) {
    char* ws = (char*)d_ws;
    Params P;
    P.ws = ws; P.x = (const bf16_t*)d_in[0]; P.mem0 = (const bf16_t*)d_in[1]; P.out = (bf16_t*)d_out;
    for (int i = 0; i < 8; ++i) P.w[i] = (const bf16_t*)d_in[i < 4 ? 2 + i : 4 + i];
    (void)hipFuncSetAttribute((const void*)mega_kernel, hipFuncAttributeMaxDynamicSharedMemorySize, MEGA_LDS);

    Prep1 A1;
    A1.ws = ws;
    prep1_kernel<<<1 + 20, 256, 0, stream>>>(A1);
    mega_kernel<<<256, 512, MEGA_LDS, stream>>>(P);
}
```

```cpp
#include <hip/hip_runtime.h>
#include <stdint.h>
#include <type_traits>

typedef unsigned short bf16_t;
using bf16x2 = __attribute__((ext_vector_type(2))) __bf16;
using bf16x8 = __attribute__((ext_vector_type(8))) __bf16;
using bf16x16 = __attribute__((ext_vector_type(16))) __bf16;
using f32x2 = __attribute__((ext_vector_type(2))) float;
using f32x4 = __attribute__((ext_vector_type(4))) float;
using u32x2 = __attribute__((ext_vector_type(2))) unsigned;
using u32x4 = __attribute__((ext_vector_type(4))) unsigned;
typedef __amdgpu_buffer_rsrc_t rsrc_t;

__device__ __forceinline__ unsigned pack2(float a, float b) {
    bf16x2 r = __builtin_convertvector((f32x2){a, b}, bf16x2);
    return __builtin_bit_cast(unsigned, r);
}
__device__ __forceinline__ float rbf(float f) { return __uint_as_float(pack2(f, 0.f) << 16); }
__device__ __forceinline__ float lo2f(unsigned u) { return __uint_as_float(u << 16); }
__device__ __forceinline__ float hi2f(unsigned u) { return __uint_as_float(u & 0xFFFF0000u); }
__device__ __forceinline__ rsrc_t mkrsrc(const void* p, unsigned bytes) {
    return __builtin_amdgcn_make_buffer_rsrc((void*)p, 0, bytes, 0x00020000);
}

constexpr int S_ = 2048, DM = 640, NH = 32, DH = 192, HD = NH * DH  , SEG = 32, NSEG = 64;
constexpr int FF = 1536;

#define XMAP
#define NCPY 4
constexpr unsigned NSLOT = 4, NCP = NCPY;
#define FL_TK_DEF
#define DLY_L 12
#define DLY_S 1
#define DLY_W 10
#define DLY_H 2
#define EWAKE "s_nop 0"
#define LWAKE "s_wakeup\n\t"
#define SLP4 (wave == 4 ? t : 0)
#define WPF 12
#define WPRI 1
#define WPRI2 0
#define RTD 3500
#define VTD 4000
#define HAH 100
#define WEAK 0
#define BAL 0
#define HSL 8
#define OAUX 2
#define W4P 3
#define CALN 12
#define RSL 9
#define VTRY 0
#define VSL 1
#define NRR 5
constexpr unsigned BLK = 16384, RG_SLOT = 4u << 20;
constexpr unsigned XP_PAIR = 6 * BLK, XP_SLOT = RG_SLOT;
constexpr unsigned XM_PAIR = 4 * BLK, XM_SLOT = RG_SLOT;
constexpr unsigned VM_SLOT = RG_SLOT;
constexpr unsigned PX_SLOT = RG_SLOT;
constexpr unsigned RG_XP = 0, RG_XM = 12 * BLK, RG_VM = 20 * BLK, RG_PX = 60 * BLK;
constexpr unsigned RG_ST = 68 * BLK, RG_FV = 84 * BLK, RG_FH = 86 * BLK, RG_FX = 88 * BLK, RG_SG = 89 * BLK, RG_L = NSLOT * RG_SLOT;
constexpr unsigned FL0 = 2u << 20, FL_TK = 512, FL_IN = FL_TK + 8 * 128, FL_BYTES = FL_IN + 16 * 128;
constexpr unsigned ACT = S_ * DM * 2;
#define PXROT 5
#define W4SL 5
#define RXDEL 1
#define VXD 32
#define VXAUX 16
constexpr unsigned PG0 = RG_L, PG_L = 80u << 21;
constexpr unsigned PG_XA = 0, PG_HS = 5 * BLK, PG_X1 = 10 * BLK, PG_H2 = 15 * BLK, PG_SS = 27 * BLK, PG_XR = 28 * BLK, PG_XN = 33 * BLK, PG_LX = 10 * BLK, PG_HV = 48 * BLK;
__device__ __forceinline__ unsigned pgb(unsigned p, int par) { return p << 21 | ((p ^ par) & 1) << 13; }
__device__ __forceinline__ constexpr unsigned blin(unsigned o) { return o + (o & ~8191u); }
constexpr int HT0 = 2;
constexpr int AH = 3;
constexpr unsigned WS_END = PG0 + PG_L;

#define SB() __builtin_amdgcn_sched_barrier(0)
#define LAS __attribute__((address_space(3)))
template <class T> __device__ __forceinline__ T lds_ld(unsigned a) { return *(const LAS T*)(a); }
template <class T> __device__ __forceinline__ void lds_st(unsigned a, T v) { *(LAS T*)(a) = v; }
constexpr unsigned OOB = 0x7FFFFF00u;
template <int N> __device__ __forceinline__ void wait_vm() { asm volatile("s_waitcnt vmcnt(%0)" :: "i"(N) : "memory"); }
__device__ __forceinline__ void bar() {
    asm volatile("s_waitcnt lgkmcnt(0)" ::: "memory");
    __builtin_amdgcn_s_barrier();
    asm volatile("" ::: "memory");
}
__device__ __forceinline__ int lane_id() {
    int ln;
    asm volatile("v_mbcnt_lo_u32_b32 %0, -1, 0\n\tv_mbcnt_hi_u32_b32 %0, -1, %0" : "=v"(ln));
    return ln;
}
__device__ __forceinline__ void swap16(unsigned& a, unsigned& b) { asm volatile("s_nop 3\n\tv_permlane16_swap_b32 %0, %1\n\ts_nop 1" : "+v"(a), "+v"(b)); }
__device__ __forceinline__ void swap32(unsigned& a, unsigned& b) { asm volatile("s_nop 3\n\tv_permlane32_swap_b32 %0, %1\n\ts_nop 1" : "+v"(a), "+v"(b)); }
__device__ __forceinline__ float sum_x16(float x) { unsigned a = __float_as_uint(x), b = a; swap16(a, b); return __uint_as_float(a) + __uint_as_float(b); }
__device__ __forceinline__ float sum_x32(float x) { unsigned a = __float_as_uint(x), b = a; swap32(a, b); return __uint_as_float(a) + __uint_as_float(b); }
__device__ __forceinline__ float max_x16(float x) { unsigned a = __float_as_uint(x), b = a; swap16(a, b); return __builtin_fmaxf(__uint_as_float(a), __uint_as_float(b)); }
__device__ __forceinline__ float max_x32(float x) { unsigned a = __float_as_uint(x), b = a; swap32(a, b); return __builtin_fmaxf(__uint_as_float(a), __uint_as_float(b)); }
__device__ __forceinline__ void sum2_rows(float& s, float& q) {
    unsigned a = __float_as_uint(s), b = a, c = __float_as_uint(q), d = c;
    asm volatile("s_nop 2\n\tv_permlane16_swap_b32 %0, %1\n\tv_permlane16_swap_b32 %2, %3\n\tv_add_f32 %0, %0, %1\n\tv_add_f32 %2, %2, %3\n\tv_mov_b32 %1, %0\n\tv_mov_b32 %3, %2\n\t"
                 "s_nop 1\n\tv_permlane32_swap_b32 %0, %1\n\tv_permlane32_swap_b32 %2, %3\n\tv_add_f32 %0, %0, %1\n\tv_add_f32 %2, %2, %3" : "+v"(a), "+v"(b), "+v"(c), "+v"(d));
    s = __uint_as_float(a); q = __uint_as_float(c);
}
__device__ __forceinline__ u32x4 merge16(u32x2 a, u32x2 b) {
    unsigned a0 = a[0], a1 = a[1], b0 = b[0], b1 = b[1];
    asm volatile("s_nop 3\n\tv_permlane16_swap_b32 %0, %2\n\tv_permlane16_swap_b32 %1, %3\n\ts_nop 1" : "+v"(a0), "+v"(a1), "+v"(b0), "+v"(b1));
    return (u32x4){a0, a1, b0, b1};
}
template <int AUXV>
__device__ __forceinline__ void st128_(u32x4 v, rsrc_t r, unsigned voff, unsigned soff) {
    __builtin_amdgcn_raw_buffer_store_b128(v, r, voff, soff, AUXV);
    asm volatile("s_nop 1" :: "v"(v) : "memory");
}
#define st128(V, R, VO_, SO_) st128_<16>(V, R, VO_, SO_)
#define LD128(R, VO_, SO_) __builtin_amdgcn_raw_buffer_load_b128(R, VO_, SO_, 16)
#define LD64(R, VO_, SO_) __builtin_amdgcn_raw_buffer_load_b64(R, VO_, SO_, 16)
#define BF8(X) __builtin_bit_cast(bf16x8, X)
__device__ __forceinline__ float gelu_tanh(float x) {
    const float c0 = -2.0f * 0.7978845608028654f * 1.4426950408889634f, c1 = c0 * 0.044715f;
    const float e = __builtin_amdgcn_exp2f(x * __builtin_fmaf(x * x, c1, c0));
    return x * __builtin_amdgcn_rcpf(1.0f + e);
}
#define MFMA(A_, B_, C_) C_ = __builtin_amdgcn_mfma_f32_16x16x32_bf16(A_, B_, C_, 0, 0, 0)
#define SMF(A_, B_, C_) C_ = __builtin_amdgcn_smfmac_f32_16x16x64_bf16(A_, B_, C_, sidx, 0, 0)
__device__ __forceinline__ float ln_den(float var) { return rbf(__builtin_amdgcn_sqrtf(rbf(rbf(var) + 1e-5f))); }
__device__ __forceinline__ float rcp(float x) { return __builtin_amdgcn_rcpf(x); }

struct Params {
    char* ws;
    const bf16_t* x;
    const bf16_t* mem0;
    const bf16_t* w[8];
    bf16_t* out;
};

#define RLX_AGENT __ATOMIC_RELAXED, __HIP_MEMORY_SCOPE_AGENT
#define SENT 0xFFFFFFFFu
constexpr int SPIN_CAP = 400000;
__device__ __forceinline__ bool spin_fail(int spins, const Params& P) {
    if ((spins & 1023) == 1023) {
        const rsrc_t rt = mkrsrc(P.ws + FL0, FL_BYTES);
        unsigned o;
        asm volatile("v_mov_b32 %0, 0x100" : "=v"(o));
        if (__builtin_amdgcn_readfirstlane(__builtin_amdgcn_raw_buffer_load_b32(rt, o, 0, 16)) != 0u) return true;
        if (spins > SPIN_CAP) { __builtin_amdgcn_raw_buffer_store_b32(o, rt, o, 0, 16); return true; }
    }
    return false;
}
__device__ __forceinline__ bool ok4(u32x4 v) { return v[0] != SENT && v[1] != SENT && v[2] != SENT && v[3] != SENT; }
__device__ __forceinline__ unsigned nosent(unsigned d) { return d < 0xFFFFFFFEu ? d : 0xFFFFFFFEu; }
__device__ __forceinline__ u32x4 nosent4(u32x4 v) { return (u32x4){nosent(v[0]), nosent(v[1]), nosent(v[2]), nosent(v[3])}; }
__device__ __forceinline__ unsigned umax3(unsigned a, unsigned b, unsigned c) { const unsigned m = a > b ? a : b; return m > c ? m : c; }
__device__ __forceinline__ unsigned umax4(unsigned m, u32x4 v) { return umax3(umax3(m, v[0], v[1]), v[2], v[3]); }
__device__ __forceinline__ u32x4 sent4() { unsigned sv; asm volatile("v_mov_b32 %0, -1" : "=v"(sv)); return (u32x4){sv, sv, sv, sv}; }
template <int N>
__device__ __forceinline__ void poll(rsrc_t rs, unsigned base, const unsigned (&off)[N], u32x4 (&v)[N], const Params& P) {
    for (int spins = 0;; ++spins) {
        unsigned m = 0;
#pragma unroll
        for (int k = 0; k < N; ++k) v[k] = LD128(rs, off[k], base);
#pragma unroll
        for (int k = 0; k < N; ++k) m = umax4(m, v[k]);
        if (__all(m != SENT)) return;
        asm volatile("" ::: "memory");
        if (spin_fail(spins, P)) return;
    }
}
__device__ __forceinline__ bf16x16 cat16(bf16x8 a, bf16x8 b) {
    return __builtin_shufflevector(a, b, 0, 1, 2, 3, 4, 5, 6, 7, 8, 9, 10, 11, 12, 13, 14, 15);
}
__device__ __forceinline__ bf16x8 half8(bf16x16 v, int h) {
    return h ? __builtin_shufflevector(v, v, 8, 9, 10, 11, 12, 13, 14, 15) : __builtin_shufflevector(v, v, 0, 1, 2, 3, 4, 5, 6, 7);
}
__device__ __forceinline__ bf16x16 lds_frag(unsigned a, unsigned step) { return cat16(lds_ld<bf16x8>(a), lds_ld<bf16x8>(a + step)); }
template <int I>
__device__ __forceinline__ void dmaq(const char* g, unsigned l16, unsigned dst) {
    asm volatile("s_mov_b32 m0, %2\n\ts_nop 0\n\tglobal_load_lds_dwordx4 %0, %1 offset:%3 sc1" :: "v"(l16), "s"(g), "s"(dst), "i"(I * 1024) : "memory");
}
__device__ __forceinline__ void dma4(const char* g, unsigned l16, unsigned dst) {
    asm volatile("s_mov_b32 m0, %2\n\ts_nop 0\n\tglobal_load_lds_dwordx4 %0, %1 sc1\n\tglobal_load_lds_dwordx4 %0, %1 offset:1024 sc1\n\t"
                 "global_load_lds_dwordx4 %0, %1 offset:2048 sc1\n\tglobal_load_lds_dwordx4 %0, %1 offset:3072 sc1" :: "v"(l16), "s"(g), "s"(dst) : "memory");
}
__device__ __forceinline__ unsigned toff(int row, int col, int par) {
    return pgb(row >> 5, par) + blin((unsigned)((row >> 4) & 1) * 20480u + (unsigned)(col >> 3) * 256u) + (unsigned)(row & 15) * 16u;
}

#define ASM_ST
#define ASM_ST_DECL
#define ASM_ST_OUT
#define ASM_ST_SAVE(k)
#define ASM_ST2
#define ASM_ST3
#define ASM_ST3_OUT
#define ASM_ST3_SAVE(k)
#define ASM_ST2_OUT
#define ASM_ST2_SAVE(k)
#define STDECL
#define ST(k)
#define STC(k)
#define STFLUSH(t)

#define MARK(c, k)
#define MARKX(c, k)
#define PMARK(c, st, seg)
#define LDECL
#define LA()
#define LB(t)
#define LF()
#define STROLE 3
constexpr unsigned L_MEM = 0;
constexpr unsigned L_Q = 10240;
constexpr unsigned L_KM = 13312;
constexpr unsigned L_QX = 16384;
constexpr unsigned L_KX = 28672;
constexpr unsigned L_XB = 40960;
constexpr unsigned L_XA = 49152;
constexpr unsigned L_W = 81920;
constexpr int MEGA_LDS = 163840;
constexpr int wi(int i, int j) { return (i >> 1) * 5 + ((i & 1) ? 2 + j : j - 1); }

#define RPUB2
#define WNT ""
#define PRIUP "s_setprio 2\n\t"
#define PRISM PRIUP
#define STR2(x) #x
#define STRX(x) STR2(x)
#define W4SLEEP "s_sleep " STRX(W4SL) "\n\t"
#define PLAY 0
#define V1A 0
#define ASM_WLOAD3 \
    "s_xor_b32 %[t], %[wx], 0\n\t" \
    "s_mul_i32 %[t], %[t], 0xc0000\n\t" \
    "buffer_load_dwordx4 v[200:203], %[o0], %[rs], %[t] offen" WNT "\n\t" \
    "buffer_load_dwordx4 v[0:3], %[o1], %[rs], %[t] offen" WNT "\n\t" \
    "buffer_load_dwordx4 v[8:11], %[o2], %[rs], %[t] offen" WNT "\n\t" \
    "s_add_u32 %[t], %[t], 0x18000\n\t" \
    "buffer_load_dwordx4 v[16:19], %[o0], %[rs], %[t] offen" WNT "\n\t" \
    "buffer_load_dwordx4 v[24:27], %[o1], %[rs], %[t] offen" WNT "\n\t" \
    "buffer_load_dwordx4 v[32:35], %[o2], %[rs], %[t] offen" WNT "\n\t" \
    "s_add_u32 %[t], %[t], 0x18000\n\t" \
    "buffer_load_dwordx4 v[208:211], %[o0], %[rs], %[t] offen" WNT "\n\t" \
    "buffer_load_dwordx4 v[40:43], %[o1], %[rs], %[t] offen" WNT "\n\t" \
    "buffer_load_dwordx4 v[48:51], %[o2], %[rs], %[t] offen" WNT "\n\t" \
    "s_add_u32 %[t], %[t], 0x18000\n\t" \
    "buffer_load_dwordx4 v[56:59], %[o0], %[rs], %[t] offen" WNT "\n\t" \
    "buffer_load_dwordx4 v[64:67], %[o1], %[rs], %[t] offen" WNT "\n\t" \
    "buffer_load_dwordx4 v[72:75], %[o2], %[rs], %[t] offen" WNT "\n\t" \
    "s_add_u32 %[t], %[t], 0x18000\n\t" \
    "buffer_load_dwordx4 v[204:207], %[o0], %[rs], %[t] offen" WNT "\n\t" \
    "buffer_load_dwordx4 v[4:7], %[o1], %[rs], %[t] offen" WNT "\n\t" \
    "buffer_load_dwordx4 v[12:15], %[o2], %[rs], %[t] offen" WNT "\n\t" \
    "s_add_u32 %[t], %[t], 0x18000\n\t" \
    "buffer_load_dwordx4 v[20:23], %[o0], %[rs], %[t] offen" WNT "\n\t" \
    "buffer_load_dwordx4 v[28:31], %[o1], %[rs], %[t] offen" WNT "\n\t" \
    "buffer_load_dwordx4 v[36:39], %[o2], %[rs], %[t] offen" WNT "\n\t" \
    "s_add_u32 %[t], %[t], 0x18000\n\t" \
    "buffer_load_dwordx4 v[212:215], %[o0], %[rs], %[t] offen" WNT "\n\t" \
    "buffer_load_dwordx4 v[44:47], %[o1], %[rs], %[t] offen" WNT "\n\t" \
    "buffer_load_dwordx4 v[52:55], %[o2], %[rs], %[t] offen" WNT "\n\t" \
    "s_add_u32 %[t], %[t], 0x18000\n\t" \
    "buffer_load_dwordx4 v[60:63], %[o0], %[rs], %[t] offen" WNT "\n\t" \
    "buffer_load_dwordx4 v[68:71], %[o1], %[rs], %[t] offen" WNT "\n\t" \
    "buffer_load_dwordx4 v[76:79], %[o2], %[rs], %[t] offen" WNT "\n\t" \
    "s_add_u32 %[t], %[t], 0x18000\n\t" \
    "s_waitcnt vmcnt(21)\n\t" \
    "ds_write_b128 %[s0], v[200:203] offset:0\n\t" \
    "ds_write_b128 %[s1], v[0:3] offset:0\n\t" \
    "ds_write_b128 %[s2], v[8:11] offset:0\n\t" \
    "s_waitcnt vmcnt(18)\n\t" \
    "ds_write_b128 %[s0], v[16:19] offset:3456\n\t" \
    "ds_write_b128 %[s1], v[24:27] offset:3456\n\t" \
    "ds_write_b128 %[s2], v[32:35] offset:3456\n\t" \
    "s_waitcnt vmcnt(15)\n\t" \
    "ds_write_b128 %[s0], v[208:211] offset:6912\n\t" \
    "ds_write_b128 %[s1], v[40:43] offset:6912\n\t" \
    "ds_write_b128 %[s2], v[48:51] offset:6912\n\t" \
    "s_waitcnt vmcnt(12)\n\t" \
    "ds_write_b128 %[s0], v[56:59] offset:10368\n\t" \
    "ds_write_b128 %[s1], v[64:67] offset:10368\n\t" \
    "ds_write_b128 %[s2], v[72:75] offset:10368\n\t" \
    "s_waitcnt lgkmcnt(0)\n\t" \
    "s_barrier \n\t" \
    "ds_read_b64_tr_b16 v[200:201], %[t0] offset:0\n\t" \
    "ds_read_b64_tr_b16 v[202:203], %[t0] offset:1664\n\t" \
    "ds_read_b64_tr_b16 v[0:1], %[t0] offset:128\n\t" \
    "ds_read_b64_tr_b16 v[2:3], %[t0] offset:1792\n\t" \
    "ds_read_b64_tr_b16 v[8:9], %[t0] offset:256\n\t" \
    "ds_read_b64_tr_b16 v[10:11], %[t0] offset:1920\n\t" \
    "ds_read_b64_tr_b16 v[16:17], %[t1] offset:0\n\t" \
    "ds_read_b64_tr_b16 v[18:19], %[t1] offset:1664\n\t" \
    "ds_read_b64_tr_b16 v[24:25], %[t1] offset:128\n\t" \
    "ds_read_b64_tr_b16 v[26:27], %[t1] offset:1792\n\t" \
    "ds_read_b64_tr_b16 v[32:33], %[t1] offset:256\n\t" \
    "ds_read_b64_tr_b16 v[34:35], %[t1] offset:1920\n\t" \
    "ds_read_b64_tr_b16 v[208:209], %[t2] offset:0\n\t" \
    "ds_read_b64_tr_b16 v[210:211], %[t2] offset:1664\n\t" \
    "ds_read_b64_tr_b16 v[40:41], %[t2] offset:128\n\t" \
    "ds_read_b64_tr_b16 v[42:43], %[t2] offset:1792\n\t" \
    "ds_read_b64_tr_b16 v[48:49], %[t2] offset:256\n\t" \
    "ds_read_b64_tr_b16 v[50:51], %[t2] offset:1920\n\t" \
    "ds_read_b64_tr_b16 v[56:57], %[t3] offset:0\n\t" \
    "ds_read_b64_tr_b16 v[58:59], %[t3] offset:1664\n\t" \
    "ds_read_b64_tr_b16 v[64:65], %[t3] offset:128\n\t" \
    "ds_read_b64_tr_b16 v[66:67], %[t3] offset:1792\n\t" \
    "ds_read_b64_tr_b16 v[72:73], %[t3] offset:256\n\t" \
    "ds_read_b64_tr_b16 v[74:75], %[t3] offset:1920\n\t" \
    "s_xor_b32 %[t], %[wx], 4\n\t" \
    "s_mul_i32 %[t], %[t], 0xc0000\n\t" \
    "buffer_load_dwordx4 v[216:219], %[o0], %[rs], %[t] offen" WNT "\n\t" \
    "buffer_load_dwordx4 v[80:83], %[o1], %[rs], %[t] offen" WNT "\n\t" \
    "buffer_load_dwordx4 v[88:91], %[o2], %[rs], %[t] offen" WNT "\n\t" \
    "s_add_u32 %[t], %[t], 0x18000\n\t" \
    "buffer_load_dwordx4 v[96:99], %[o0], %[rs], %[t] offen" WNT "\n\t" \
    "buffer_load_dwordx4 v[104:107], %[o1], %[rs], %[t] offen" WNT "\n\t" \
    "buffer_load_dwordx4 v[112:115], %[o2], %[rs], %[t] offen" WNT "\n\t" \
    "s_add_u32 %[t], %[t], 0x18000\n\t" \
    "buffer_load_dwordx4 v[224:227], %[o0], %[rs], %[t] offen" WNT "\n\t" \
    "buffer_load_dwordx4 v[120:123], %[o1], %[rs], %[t] offen" WNT "\n\t" \
    "buffer_load_dwordx4 v[128:131], %[o2], %[rs], %[t] offen" WNT "\n\t" \
    "s_add_u32 %[t], %[t], 0x18000\n\t" \
    "buffer_load_dwordx4 v[136:139], %[o0], %[rs], %[t] offen" WNT "\n\t" \
    "buffer_load_dwordx4 v[144:147], %[o1], %[rs], %[t] offen" WNT "\n\t" \
    "buffer_load_dwordx4 v[152:155], %[o2], %[rs], %[t] offen" WNT "\n\t" \
    "s_add_u32 %[t], %[t], 0x18000\n\t" \
    "s_waitcnt lgkmcnt(0)\n\t" \
    "s_barrier \n\t" \
    "s_waitcnt vmcnt(21)\n\t" \
    "ds_write_b128 %[s0], v[204:207] offset:0\n\t" \
    "ds_write_b128 %[s1], v[4:7] offset:0\n\t" \
    "ds_write_b128 %[s2], v[12:15] offset:0\n\t" \
    "s_waitcnt vmcnt(18)\n\t" \
    "ds_write_b128 %[s0], v[20:23] offset:3456\n\t" \
    "ds_write_b128 %[s1], v[28:31] offset:3456\n\t" \
    "ds_write_b128 %[s2], v[36:39] offset:3456\n\t" \
    "s_waitcnt vmcnt(15)\n\t" \
    "ds_write_b128 %[s0], v[212:215] offset:6912\n\t" \
    "ds_write_b128 %[s1], v[44:47] offset:6912\n\t" \
    "ds_write_b128 %[s2], v[52:55] offset:6912\n\t" \
    "s_waitcnt vmcnt(12)\n\t" \
    "ds_write_b128 %[s0], v[60:63] offset:10368\n\t" \
    "ds_write_b128 %[s1], v[68:71] offset:10368\n\t" \
    "ds_write_b128 %[s2], v[76:79] offset:10368\n\t" \
    "s_waitcnt lgkmcnt(0)\n\t" \
    "s_barrier \n\t" \
    "ds_read_b64_tr_b16 v[204:205], %[t0] offset:0\n\t" \
    "ds_read_b64_tr_b16 v[206:207], %[t0] offset:1664\n\t" \
    "ds_read_b64_tr_b16 v[4:5], %[t0] offset:128\n\t" \
    "ds_read_b64_tr_b16 v[6:7], %[t0] offset:1792\n\t" \
    "ds_read_b64_tr_b16 v[12:13], %[t0] offset:256\n\t" \
    "ds_read_b64_tr_b16 v[14:15], %[t0] offset:1920\n\t" \
    "ds_read_b64_tr_b16 v[20:21], %[t1] offset:0\n\t" \
    "ds_read_b64_tr_b16 v[22:23], %[t1] offset:1664\n\t" \
    "ds_read_b64_tr_b16 v[28:29], %[t1] offset:128\n\t" \
    "ds_read_b64_tr_b16 v[30:31], %[t1] offset:1792\n\t" \
    "ds_read_b64_tr_b16 v[36:37], %[t1] offset:256\n\t" \
    "ds_read_b64_tr_b16 v[38:39], %[t1] offset:1920\n\t" \
    "ds_read_b64_tr_b16 v[212:213], %[t2] offset:0\n\t" \
    "ds_read_b64_tr_b16 v[214:215], %[t2] offset:1664\n\t" \
    "ds_read_b64_tr_b16 v[44:45], %[t2] offset:128\n\t" \
    "ds_read_b64_tr_b16 v[46:47], %[t2] offset:1792\n\t" \
    "ds_read_b64_tr_b16 v[52:53], %[t2] offset:256\n\t" \
    "ds_read_b64_tr_b16 v[54:55], %[t2] offset:1920\n\t" \
    "ds_read_b64_tr_b16 v[60:61], %[t3] offset:0\n\t" \
    "ds_read_b64_tr_b16 v[62:63], %[t3] offset:1664\n\t" \
    "ds_read_b64_tr_b16 v[68:69], %[t3] offset:128\n\t" \
    "ds_read_b64_tr_b16 v[70:71], %[t3] offset:1792\n\t" \
    "ds_read_b64_tr_b16 v[76:77], %[t3] offset:256\n\t" \
    "ds_read_b64_tr_b16 v[78:79], %[t3] offset:1920\n\t" \
    "buffer_load_dwordx4 v[220:223], %[o0], %[rs], %[t] offen" WNT "\n\t" \
    "buffer_load_dwordx4 v[84:87], %[o1], %[rs], %[t] offen" WNT "\n\t" \
    "buffer_load_dwordx4 v[92:95], %[o2], %[rs], %[t] offen" WNT "\n\t" \
    "s_add_u32 %[t], %[t], 0x18000\n\t" \
    "buffer_load_dwordx4 v[100:103], %[o0], %[rs], %[t] offen" WNT "\n\t" \
    "buffer_load_dwordx4 v[108:111], %[o1], %[rs], %[t] offen" WNT "\n\t" \
    "buffer_load_dwordx4 v[116:119], %[o2], %[rs], %[t] offen" WNT "\n\t" \
    "s_add_u32 %[t], %[t], 0x18000\n\t" \
    "buffer_load_dwordx4 v[228:231], %[o0], %[rs], %[t] offen" WNT "\n\t" \
    "buffer_load_dwordx4 v[124:127], %[o1], %[rs], %[t] offen" WNT "\n\t" \
    "buffer_load_dwordx4 v[132:135], %[o2], %[rs], %[t] offen" WNT "\n\t" \
    "s_add_u32 %[t], %[t], 0x18000\n\t" \
    "buffer_load_dwordx4 v[140:143], %[o0], %[rs], %[t] offen" WNT "\n\t" \
    "buffer_load_dwordx4 v[148:151], %[o1], %[rs], %[t] offen" WNT "\n\t" \
    "buffer_load_dwordx4 v[156:159], %[o2], %[rs], %[t] offen" WNT "\n\t" \
    "s_add_u32 %[t], %[t], 0x18000\n\t" \
    "s_waitcnt lgkmcnt(0)\n\t" \
    "s_barrier \n\t" \
    "s_waitcnt vmcnt(21)\n\t" \
    "ds_write_b128 %[s0], v[216:219] offset:0\n\t" \
    "ds_write_b128 %[s1], v[80:83] offset:0\n\t" \
    "ds_write_b128 %[s2], v[88:91] offset:0\n\t" \
    "s_waitcnt vmcnt(18)\n\t" \
    "ds_write_b128 %[s0], v[96:99] offset:3456\n\t" \
    "ds_write_b128 %[s1], v[104:107] offset:3456\n\t" \
    "ds_write_b128 %[s2], v[112:115] offset:3456\n\t" \
    "s_waitcnt vmcnt(15)\n\t" \
    "ds_write_b128 %[s0], v[224:227] offset:6912\n\t" \
    "ds_write_b128 %[s1], v[120:123] offset:6912\n\t" \
    "ds_write_b128 %[s2], v[128:131] offset:6912\n\t" \
    "s_waitcnt vmcnt(12)\n\t" \
    "ds_write_b128 %[s0], v[136:139] offset:10368\n\t" \
    "ds_write_b128 %[s1], v[144:147] offset:10368\n\t" \
    "ds_write_b128 %[s2], v[152:155] offset:10368\n\t" \
    "s_waitcnt lgkmcnt(0)\n\t" \
    "s_barrier \n\t" \
    "ds_read_b64_tr_b16 v[216:217], %[t0] offset:0\n\t" \
    "ds_read_b64_tr_b16 v[218:219], %[t0] offset:1664\n\t" \
    "ds_read_b64_tr_b16 v[80:81], %[t0] offset:128\n\t" \
    "ds_read_b64_tr_b16 v[82:83], %[t0] offset:1792\n\t" \
    "ds_read_b64_tr_b16 v[88:89], %[t0] offset:256\n\t" \
    "ds_read_b64_tr_b16 v[90:91], %[t0] offset:1920\n\t" \
    "ds_read_b64_tr_b16 v[96:97], %[t1] offset:0\n\t" \
    "ds_read_b64_tr_b16 v[98:99], %[t1] offset:1664\n\t" \
    "ds_read_b64_tr_b16 v[104:105], %[t1] offset:128\n\t" \
    "ds_read_b64_tr_b16 v[106:107], %[t1] offset:1792\n\t" \
    "ds_read_b64_tr_b16 v[112:113], %[t1] offset:256\n\t" \
    "ds_read_b64_tr_b16 v[114:115], %[t1] offset:1920\n\t" \
    "ds_read_b64_tr_b16 v[224:225], %[t2] offset:0\n\t" \
    "ds_read_b64_tr_b16 v[226:227], %[t2] offset:1664\n\t" \
    "ds_read_b64_tr_b16 v[120:121], %[t2] offset:128\n\t" \
    "ds_read_b64_tr_b16 v[122:123], %[t2] offset:1792\n\t" \
    "ds_read_b64_tr_b16 v[128:129], %[t2] offset:256\n\t" \
    "ds_read_b64_tr_b16 v[130:131], %[t2] offset:1920\n\t" \
    "ds_read_b64_tr_b16 v[136:137], %[t3] offset:0\n\t" \
    "ds_read_b64_tr_b16 v[138:139], %[t3] offset:1664\n\t" \
    "ds_read_b64_tr_b16 v[144:145], %[t3] offset:128\n\t" \
    "ds_read_b64_tr_b16 v[146:147], %[t3] offset:1792\n\t" \
    "ds_read_b64_tr_b16 v[152:153], %[t3] offset:256\n\t" \
    "ds_read_b64_tr_b16 v[154:155], %[t3] offset:1920\n\t" \
    "s_and_b32 %[t], %[wx], 3\n\t" \
    "s_mul_i32 %[t], %[t], 0x60000\n\t" \
    "s_add_u32 %[t], %[t], 0x600000\n\t" \
    "buffer_load_dwordx4 v[232:235], %[o0], %[rs], %[t] offen" WNT "\n\t" \
    "buffer_load_dwordx4 v[160:163], %[o1], %[rs], %[t] offen" WNT "\n\t" \
    "buffer_load_dwordx4 v[168:171], %[o2], %[rs], %[t] offen" WNT "\n\t" \
    "s_add_u32 %[t], %[t], 0x18000\n\t" \
    "buffer_load_dwordx4 v[236:239], %[o0], %[rs], %[t] offen" WNT "\n\t" \
    "buffer_load_dwordx4 v[164:167], %[o1], %[rs], %[t] offen" WNT "\n\t" \
    "buffer_load_dwordx4 v[172:175], %[o2], %[rs], %[t] offen" WNT "\n\t" \
    "s_add_u32 %[t], %[t], 0x18000\n\t" \
    "buffer_load_dwordx4 v[176:179], %[o0], %[rs], %[t] offen" WNT "\n\t" \
    "buffer_load_dwordx4 v[184:187], %[o1], %[rs], %[t] offen" WNT "\n\t" \
    "buffer_load_dwordx4 v[192:195], %[o2], %[rs], %[t] offen" WNT "\n\t" \
    "s_add_u32 %[t], %[t], 0x18000\n\t" \
    "buffer_load_dwordx4 v[180:183], %[o0], %[rs], %[t] offen" WNT "\n\t" \
    "buffer_load_dwordx4 v[188:191], %[o1], %[rs], %[t] offen" WNT "\n\t" \
    "buffer_load_dwordx4 v[196:199], %[o2], %[rs], %[t] offen" WNT "\n\t" \
    "s_add_u32 %[t], %[t], 0x18000\n\t" \
    "s_waitcnt lgkmcnt(0)\n\t" \
    "s_barrier \n\t" \
    "s_waitcnt vmcnt(21)\n\t" \
    "ds_write_b128 %[s0], v[220:223] offset:0\n\t" \
    "ds_write_b128 %[s1], v[84:87] offset:0\n\t" \
    "ds_write_b128 %[s2], v[92:95] offset:0\n\t" \
    "s_waitcnt vmcnt(18)\n\t" \
    "ds_write_b128 %[s0], v[100:103] offset:3456\n\t" \
    "ds_write_b128 %[s1], v[108:111] offset:3456\n\t" \
    "ds_write_b128 %[s2], v[116:119] offset:3456\n\t" \
    "s_waitcnt vmcnt(15)\n\t" \
    "ds_write_b128 %[s0], v[228:231] offset:6912\n\t" \
    "ds_write_b128 %[s1], v[124:127] offset:6912\n\t" \
    "ds_write_b128 %[s2], v[132:135] offset:6912\n\t" \
    "s_waitcnt vmcnt(12)\n\t" \
    "ds_write_b128 %[s0], v[140:143] offset:10368\n\t" \
    "ds_write_b128 %[s1], v[148:151] offset:10368\n\t" \
    "ds_write_b128 %[s2], v[156:159] offset:10368\n\t" \
    "s_waitcnt lgkmcnt(0)\n\t" \
    "s_barrier \n\t" \
    "ds_read_b64_tr_b16 v[220:221], %[t0] offset:0\n\t" \
    "ds_read_b64_tr_b16 v[222:223], %[t0] offset:1664\n\t" \
    "ds_read_b64_tr_b16 v[84:85], %[t0] offset:128\n\t" \
    "ds_read_b64_tr_b16 v[86:87], %[t0] offset:1792\n\t" \
    "ds_read_b64_tr_b16 v[92:93], %[t0] offset:256\n\t" \
    "ds_read_b64_tr_b16 v[94:95], %[t0] offset:1920\n\t" \
    "ds_read_b64_tr_b16 v[100:101], %[t1] offset:0\n\t" \
    "ds_read_b64_tr_b16 v[102:103], %[t1] offset:1664\n\t" \
    "ds_read_b64_tr_b16 v[108:109], %[t1] offset:128\n\t" \
    "ds_read_b64_tr_b16 v[110:111], %[t1] offset:1792\n\t" \
    "ds_read_b64_tr_b16 v[116:117], %[t1] offset:256\n\t" \
    "ds_read_b64_tr_b16 v[118:119], %[t1] offset:1920\n\t" \
    "ds_read_b64_tr_b16 v[228:229], %[t2] offset:0\n\t" \
    "ds_read_b64_tr_b16 v[230:231], %[t2] offset:1664\n\t" \
    "ds_read_b64_tr_b16 v[124:125], %[t2] offset:128\n\t" \
    "ds_read_b64_tr_b16 v[126:127], %[t2] offset:1792\n\t" \
    "ds_read_b64_tr_b16 v[132:133], %[t2] offset:256\n\t" \
    "ds_read_b64_tr_b16 v[134:135], %[t2] offset:1920\n\t" \
    "ds_read_b64_tr_b16 v[140:141], %[t3] offset:0\n\t" \
    "ds_read_b64_tr_b16 v[142:143], %[t3] offset:1664\n\t" \
    "ds_read_b64_tr_b16 v[148:149], %[t3] offset:128\n\t" \
    "ds_read_b64_tr_b16 v[150:151], %[t3] offset:1792\n\t" \
    "ds_read_b64_tr_b16 v[156:157], %[t3] offset:256\n\t" \
    "ds_read_b64_tr_b16 v[158:159], %[t3] offset:1920\n\t" \
    "s_waitcnt lgkmcnt(0)\n\t" \
    "s_barrier \n\t" \
    "s_waitcnt vmcnt(9)\n\t" \
    "ds_write_b128 %[s0], v[232:235] offset:0\n\t" \
    "ds_write_b128 %[s1], v[160:163] offset:0\n\t" \
    "ds_write_b128 %[s2], v[168:171] offset:0\n\t" \
    "s_waitcnt vmcnt(6)\n\t" \
    "ds_write_b128 %[s0], v[236:239] offset:3456\n\t" \
    "ds_write_b128 %[s1], v[164:167] offset:3456\n\t" \
    "ds_write_b128 %[s2], v[172:175] offset:3456\n\t" \
    "s_waitcnt vmcnt(3)\n\t" \
    "ds_write_b128 %[s0], v[176:179] offset:6912\n\t" \
    "ds_write_b128 %[s1], v[184:187] offset:6912\n\t" \
    "ds_write_b128 %[s2], v[192:195] offset:6912\n\t" \
    "s_waitcnt vmcnt(0)\n\t" \
    "ds_write_b128 %[s0], v[180:183] offset:10368\n\t" \
    "ds_write_b128 %[s1], v[188:191] offset:10368\n\t" \
    "ds_write_b128 %[s2], v[196:199] offset:10368\n\t" \
    "s_waitcnt lgkmcnt(0)\n\t" \
    "s_barrier \n\t" \
    "ds_read_b64_tr_b16 v[232:233], %[t4] offset:0\n\t" \
    "ds_read_b64_tr_b16 v[234:235], %[t4] offset:1664\n\t" \
    "ds_read_b64_tr_b16 v[160:161], %[t4] offset:128\n\t" \
    "ds_read_b64_tr_b16 v[162:163], %[t4] offset:1792\n\t" \
    "ds_read_b64_tr_b16 v[168:169], %[t4] offset:256\n\t" \
    "ds_read_b64_tr_b16 v[170:171], %[t4] offset:1920\n\t" \
    "ds_read_b64_tr_b16 v[236:237], %[t4] offset:13824\n\t" \
    "ds_read_b64_tr_b16 v[238:239], %[t4] offset:15488\n\t" \
    "ds_read_b64_tr_b16 v[164:165], %[t4] offset:13952\n\t" \
    "ds_read_b64_tr_b16 v[166:167], %[t4] offset:15616\n\t" \
    "ds_read_b64_tr_b16 v[172:173], %[t4] offset:14080\n\t" \
    "ds_read_b64_tr_b16 v[174:175], %[t4] offset:15744\n\t" \
    "ds_read_b64_tr_b16 v[176:177], %[t4] offset:27648\n\t" \
    "ds_read_b64_tr_b16 v[178:179], %[t4] offset:29312\n\t" \
    "ds_read_b64_tr_b16 v[184:185], %[t4] offset:27776\n\t" \
    "ds_read_b64_tr_b16 v[186:187], %[t4] offset:29440\n\t" \
    "ds_read_b64_tr_b16 v[192:193], %[t4] offset:27904\n\t" \
    "ds_read_b64_tr_b16 v[194:195], %[t4] offset:29568\n\t" \
    "ds_read_b64_tr_b16 v[180:181], %[t4] offset:41472\n\t" \
    "ds_read_b64_tr_b16 v[182:183], %[t4] offset:43136\n\t" \
    "ds_read_b64_tr_b16 v[188:189], %[t4] offset:41600\n\t" \
    "ds_read_b64_tr_b16 v[190:191], %[t4] offset:43264\n\t" \
    "ds_read_b64_tr_b16 v[196:197], %[t4] offset:41728\n\t" \
    "ds_read_b64_tr_b16 v[198:199], %[t4] offset:43392\n\t" \
    "s_waitcnt lgkmcnt(0)\n\t" \
    "s_barrier \n\t" \
    "ds_write_b128 %[wl], v[200:203] offset:0\n\t" \
    "ds_write_b128 %[wl], v[204:207] offset:1024\n\t" \
    "ds_write_b128 %[wl], v[208:211] offset:2048\n\t" \
    "ds_write_b128 %[wl], v[212:215] offset:3072\n\t" \
    "ds_write_b128 %[wl], v[216:219] offset:4096\n\t" \
    "ds_write_b128 %[wl], v[220:223] offset:5120\n\t" \
    "ds_write_b128 %[wl], v[224:227] offset:6144\n\t" \
    "ds_write_b128 %[wl], v[228:231] offset:7168\n\t" \
    "ds_write_b128 %[wl], v[232:235] offset:8192\n\t" \
    "ds_write_b128 %[wl], v[236:239] offset:9216\n\t" \
    "s_waitcnt lgkmcnt(0)\n\t"
#define ASM_WLOADO \
    "s_mov_b32 %[t], 0x0\n\t" \
    "buffer_load_dwordx4 v[200:203], %[o0], %[rs], %[t] offen" WNT "\n\t" \
    "buffer_load_dwordx4 v[16:19], %[o1], %[rs], %[t] offen" WNT "\n\t" \
    "buffer_load_dwordx4 v[208:211], %[o2], %[rs], %[t] offen" WNT "\n\t" \
    "buffer_load_dwordx4 v[56:59], %[o3], %[rs], %[t] offen" WNT "\n\t" \
    "buffer_load_dwordx4 v[216:219], %[o4], %[rs], %[t] offen" WNT "\n\t" \
    "s_mov_b32 %[t], 0xa000\n\t" \
    "buffer_load_dwordx4 v[96:99], %[o0], %[rs], %[t] offen" WNT "\n\t" \
    "buffer_load_dwordx4 v[224:227], %[o1], %[rs], %[t] offen" WNT "\n\t" \
    "buffer_load_dwordx4 v[136:139], %[o2], %[rs], %[t] offen" WNT "\n\t" \
    "buffer_load_dwordx4 v[232:235], %[o3], %[rs], %[t] offen" WNT "\n\t" \
    "buffer_load_dwordx4 v[176:179], %[o4], %[rs], %[t] offen" WNT "\n\t" \
    "s_mov_b32 %[t], 0x14000\n\t" \
    "buffer_load_dwordx4 v[204:207], %[o0], %[rs], %[t] offen" WNT "\n\t" \
    "buffer_load_dwordx4 v[20:23], %[o1], %[rs], %[t] offen" WNT "\n\t" \
    "buffer_load_dwordx4 v[212:215], %[o2], %[rs], %[t] offen" WNT "\n\t" \
    "buffer_load_dwordx4 v[60:63], %[o3], %[rs], %[t] offen" WNT "\n\t" \
    "buffer_load_dwordx4 v[220:223], %[o4], %[rs], %[t] offen" WNT "\n\t" \
    "s_mov_b32 %[t], 0x1e000\n\t" \
    "buffer_load_dwordx4 v[100:103], %[o0], %[rs], %[t] offen" WNT "\n\t" \
    "buffer_load_dwordx4 v[228:231], %[o1], %[rs], %[t] offen" WNT "\n\t" \
    "buffer_load_dwordx4 v[140:143], %[o2], %[rs], %[t] offen" WNT "\n\t" \
    "buffer_load_dwordx4 v[236:239], %[o3], %[rs], %[t] offen" WNT "\n\t" \
    "buffer_load_dwordx4 v[180:183], %[o4], %[rs], %[t] offen" WNT "\n\t" \
    "s_waitcnt vmcnt(15)\n\t" \
    "ds_write_b128 %[s0], v[200:203] offset:0\n\t" \
    "ds_write_b128 %[s1], v[16:19] offset:0\n\t" \
    "ds_write_b128 %[s2], v[208:211] offset:0\n\t" \
    "ds_write_b128 %[s3], v[56:59] offset:0\n\t" \
    "ds_write_b128 %[s4], v[216:219] offset:0\n\t" \
    "s_waitcnt vmcnt(10)\n\t" \
    "ds_write_b128 %[s0], v[96:99] offset:5632\n\t" \
    "ds_write_b128 %[s1], v[224:227] offset:5632\n\t" \
    "ds_write_b128 %[s2], v[136:139] offset:5632\n\t" \
    "ds_write_b128 %[s3], v[232:235] offset:5632\n\t" \
    "ds_write_b128 %[s4], v[176:179] offset:5632\n\t" \
    "ds_read_b64_tr_b16 v[200:201], %[tr] offset:0\n\t" \
    "ds_read_b64_tr_b16 v[202:203], %[tr] offset:5632\n\t" \
    "ds_read_b64_tr_b16 v[16:17], %[tr] offset:32\n\t" \
    "ds_read_b64_tr_b16 v[18:19], %[tr] offset:5664\n\t" \
    "ds_read_b64_tr_b16 v[208:209], %[tr] offset:64\n\t" \
    "ds_read_b64_tr_b16 v[210:211], %[tr] offset:5696\n\t" \
    "ds_read_b64_tr_b16 v[56:57], %[tr] offset:96\n\t" \
    "ds_read_b64_tr_b16 v[58:59], %[tr] offset:5728\n\t" \
    "ds_read_b64_tr_b16 v[216:217], %[tr] offset:128\n\t" \
    "ds_read_b64_tr_b16 v[218:219], %[tr] offset:5760\n\t" \
    "ds_read_b64_tr_b16 v[96:97], %[tr] offset:160\n\t" \
    "ds_read_b64_tr_b16 v[98:99], %[tr] offset:5792\n\t" \
    "ds_read_b64_tr_b16 v[224:225], %[tr] offset:192\n\t" \
    "ds_read_b64_tr_b16 v[226:227], %[tr] offset:5824\n\t" \
    "ds_read_b64_tr_b16 v[136:137], %[tr] offset:224\n\t" \
    "ds_read_b64_tr_b16 v[138:139], %[tr] offset:5856\n\t" \
    "ds_read_b64_tr_b16 v[232:233], %[tr] offset:256\n\t" \
    "ds_read_b64_tr_b16 v[234:235], %[tr] offset:5888\n\t" \
    "ds_read_b64_tr_b16 v[176:177], %[tr] offset:288\n\t" \
    "ds_read_b64_tr_b16 v[178:179], %[tr] offset:5920\n\t" \
    "s_barrier \n\t" \
    "s_mov_b32 %[t], 0x28000\n\t" \
    "buffer_load_dwordx4 v[0:3], %[o0], %[rs], %[t] offen" WNT "\n\t" \
    "buffer_load_dwordx4 v[24:27], %[o1], %[rs], %[t] offen" WNT "\n\t" \
    "buffer_load_dwordx4 v[40:43], %[o2], %[rs], %[t] offen" WNT "\n\t" \
    "buffer_load_dwordx4 v[64:67], %[o3], %[rs], %[t] offen" WNT "\n\t" \
    "buffer_load_dwordx4 v[80:83], %[o4], %[rs], %[t] offen" WNT "\n\t" \
    "s_mov_b32 %[t], 0x32000\n\t" \
    "buffer_load_dwordx4 v[104:107], %[o0], %[rs], %[t] offen" WNT "\n\t" \
    "buffer_load_dwordx4 v[120:123], %[o1], %[rs], %[t] offen" WNT "\n\t" \
    "buffer_load_dwordx4 v[144:147], %[o2], %[rs], %[t] offen" WNT "\n\t" \
    "buffer_load_dwordx4 v[160:163], %[o3], %[rs], %[t] offen" WNT "\n\t" \
    "buffer_load_dwordx4 v[184:187], %[o4], %[rs], %[t] offen" WNT "\n\t" \
    "s_waitcnt lgkmcnt(0)\n\t" \
    "s_barrier \n\t" \
    "s_waitcnt vmcnt(15)\n\t" \
    "ds_write_b128 %[s0], v[204:207] offset:0\n\t" \
    "ds_write_b128 %[s1], v[20:23] offset:0\n\t" \
    "ds_write_b128 %[s2], v[212:215] offset:0\n\t" \
    "ds_write_b128 %[s3], v[60:63] offset:0\n\t" \
    "ds_write_b128 %[s4], v[220:223] offset:0\n\t" \
    "s_waitcnt vmcnt(10)\n\t" \
    "ds_write_b128 %[s0], v[100:103] offset:5632\n\t" \
    "ds_write_b128 %[s1], v[228:231] offset:5632\n\t" \
    "ds_write_b128 %[s2], v[140:143] offset:5632\n\t" \
    "ds_write_b128 %[s3], v[236:239] offset:5632\n\t" \
    "ds_write_b128 %[s4], v[180:183] offset:5632\n\t" \
    "ds_read_b64_tr_b16 v[204:205], %[tr] offset:0\n\t" \
    "ds_read_b64_tr_b16 v[206:207], %[tr] offset:5632\n\t" \
    "ds_read_b64_tr_b16 v[20:21], %[tr] offset:32\n\t" \
    "ds_read_b64_tr_b16 v[22:23], %[tr] offset:5664\n\t" \
    "ds_read_b64_tr_b16 v[212:213], %[tr] offset:64\n\t" \
    "ds_read_b64_tr_b16 v[214:215], %[tr] offset:5696\n\t" \
    "ds_read_b64_tr_b16 v[60:61], %[tr] offset:96\n\t" \
    "ds_read_b64_tr_b16 v[62:63], %[tr] offset:5728\n\t" \
    "ds_read_b64_tr_b16 v[220:221], %[tr] offset:128\n\t" \
    "ds_read_b64_tr_b16 v[222:223], %[tr] offset:5760\n\t" \
    "ds_read_b64_tr_b16 v[100:101], %[tr] offset:160\n\t" \
    "ds_read_b64_tr_b16 v[102:103], %[tr] offset:5792\n\t" \
    "ds_read_b64_tr_b16 v[228:229], %[tr] offset:192\n\t" \
    "ds_read_b64_tr_b16 v[230:231], %[tr] offset:5824\n\t" \
    "ds_read_b64_tr_b16 v[140:141], %[tr] offset:224\n\t" \
    "ds_read_b64_tr_b16 v[142:143], %[tr] offset:5856\n\t" \
    "ds_read_b64_tr_b16 v[236:237], %[tr] offset:256\n\t" \
    "ds_read_b64_tr_b16 v[238:239], %[tr] offset:5888\n\t" \
    "ds_read_b64_tr_b16 v[180:181], %[tr] offset:288\n\t" \
    "ds_read_b64_tr_b16 v[182:183], %[tr] offset:5920\n\t" \
    "s_barrier \n\t" \
    "s_mov_b32 %[t], 0x5000\n\t" \
    "buffer_load_dwordx4 v[4:7], %[o0], %[rs], %[t] offen" WNT "\n\t" \
    "buffer_load_dwordx4 v[28:31], %[o1], %[rs], %[t] offen" WNT "\n\t" \
    "buffer_load_dwordx4 v[44:47], %[o2], %[rs], %[t] offen" WNT "\n\t" \
    "buffer_load_dwordx4 v[68:71], %[o3], %[rs], %[t] offen" WNT "\n\t" \
    "buffer_load_dwordx4 v[84:87], %[o4], %[rs], %[t] offen" WNT "\n\t" \
    "s_mov_b32 %[t], 0xf000\n\t" \
    "buffer_load_dwordx4 v[108:111], %[o0], %[rs], %[t] offen" WNT "\n\t" \
    "buffer_load_dwordx4 v[124:127], %[o1], %[rs], %[t] offen" WNT "\n\t" \
    "buffer_load_dwordx4 v[148:151], %[o2], %[rs], %[t] offen" WNT "\n\t" \
    "buffer_load_dwordx4 v[164:167], %[o3], %[rs], %[t] offen" WNT "\n\t" \
    "buffer_load_dwordx4 v[188:191], %[o4], %[rs], %[t] offen" WNT "\n\t" \
    "s_waitcnt lgkmcnt(0)\n\t" \
    "s_barrier \n\t" \
    "s_waitcnt vmcnt(15)\n\t" \
    "ds_write_b128 %[s0], v[0:3] offset:0\n\t" \
    "ds_write_b128 %[s1], v[24:27] offset:0\n\t" \
    "ds_write_b128 %[s2], v[40:43] offset:0\n\t" \
    "ds_write_b128 %[s3], v[64:67] offset:0\n\t" \
    "ds_write_b128 %[s4], v[80:83] offset:0\n\t" \
    "s_waitcnt vmcnt(10)\n\t" \
    "ds_write_b128 %[s0], v[104:107] offset:5632\n\t" \
    "ds_write_b128 %[s1], v[120:123] offset:5632\n\t" \
    "ds_write_b128 %[s2], v[144:147] offset:5632\n\t" \
    "ds_write_b128 %[s3], v[160:163] offset:5632\n\t" \
    "ds_write_b128 %[s4], v[184:187] offset:5632\n\t" \
    "ds_read_b64_tr_b16 v[0:1], %[tr] offset:0\n\t" \
    "ds_read_b64_tr_b16 v[2:3], %[tr] offset:5632\n\t" \
    "ds_read_b64_tr_b16 v[24:25], %[tr] offset:32\n\t" \
    "ds_read_b64_tr_b16 v[26:27], %[tr] offset:5664\n\t" \
    "ds_read_b64_tr_b16 v[40:41], %[tr] offset:64\n\t" \
    "ds_read_b64_tr_b16 v[42:43], %[tr] offset:5696\n\t" \
    "ds_read_b64_tr_b16 v[64:65], %[tr] offset:96\n\t" \
    "ds_read_b64_tr_b16 v[66:67], %[tr] offset:5728\n\t" \
    "ds_read_b64_tr_b16 v[80:81], %[tr] offset:128\n\t" \
    "ds_read_b64_tr_b16 v[82:83], %[tr] offset:5760\n\t" \
    "ds_read_b64_tr_b16 v[104:105], %[tr] offset:160\n\t" \
    "ds_read_b64_tr_b16 v[106:107], %[tr] offset:5792\n\t" \
    "ds_read_b64_tr_b16 v[120:121], %[tr] offset:192\n\t" \
    "ds_read_b64_tr_b16 v[122:123], %[tr] offset:5824\n\t" \
    "ds_read_b64_tr_b16 v[144:145], %[tr] offset:224\n\t" \
    "ds_read_b64_tr_b16 v[146:147], %[tr] offset:5856\n\t" \
    "ds_read_b64_tr_b16 v[160:161], %[tr] offset:256\n\t" \
    "ds_read_b64_tr_b16 v[162:163], %[tr] offset:5888\n\t" \
    "ds_read_b64_tr_b16 v[184:185], %[tr] offset:288\n\t" \
    "ds_read_b64_tr_b16 v[186:187], %[tr] offset:5920\n\t" \
    "s_barrier \n\t" \
    "s_mov_b32 %[t], 0x19000\n\t" \
    "buffer_load_dwordx4 v[8:11], %[o0], %[rs], %[t] offen" WNT "\n\t" \
    "buffer_load_dwordx4 v[32:35], %[o1], %[rs], %[t] offen" WNT "\n\t" \
    "buffer_load_dwordx4 v[48:51], %[o2], %[rs], %[t] offen" WNT "\n\t" \
    "buffer_load_dwordx4 v[72:75], %[o3], %[rs], %[t] offen" WNT "\n\t" \
    "buffer_load_dwordx4 v[88:91], %[o4], %[rs], %[t] offen" WNT "\n\t" \
    "s_mov_b32 %[t], 0x23000\n\t" \
    "buffer_load_dwordx4 v[112:115], %[o0], %[rs], %[t] offen" WNT "\n\t" \
    "buffer_load_dwordx4 v[128:131], %[o1], %[rs], %[t] offen" WNT "\n\t" \
    "buffer_load_dwordx4 v[152:155], %[o2], %[rs], %[t] offen" WNT "\n\t" \
    "buffer_load_dwordx4 v[168:171], %[o3], %[rs], %[t] offen" WNT "\n\t" \
    "buffer_load_dwordx4 v[192:195], %[o4], %[rs], %[t] offen" WNT "\n\t" \
    "s_waitcnt lgkmcnt(0)\n\t" \
    "s_barrier \n\t" \
    "s_waitcnt vmcnt(15)\n\t" \
    "ds_write_b128 %[s0], v[4:7] offset:0\n\t" \
    "ds_write_b128 %[s1], v[28:31] offset:0\n\t" \
    "ds_write_b128 %[s2], v[44:47] offset:0\n\t" \
    "ds_write_b128 %[s3], v[68:71] offset:0\n\t" \
    "ds_write_b128 %[s4], v[84:87] offset:0\n\t" \
    "s_waitcnt vmcnt(10)\n\t" \
    "ds_write_b128 %[s0], v[108:111] offset:5632\n\t" \
    "ds_write_b128 %[s1], v[124:127] offset:5632\n\t" \
    "ds_write_b128 %[s2], v[148:151] offset:5632\n\t" \
    "ds_write_b128 %[s3], v[164:167] offset:5632\n\t" \
    "ds_write_b128 %[s4], v[188:191] offset:5632\n\t" \
    "ds_read_b64_tr_b16 v[4:5], %[tr] offset:0\n\t" \
    "ds_read_b64_tr_b16 v[6:7], %[tr] offset:5632\n\t" \
    "ds_read_b64_tr_b16 v[28:29], %[tr] offset:32\n\t" \
    "ds_read_b64_tr_b16 v[30:31], %[tr] offset:5664\n\t" \
    "ds_read_b64_tr_b16 v[44:45], %[tr] offset:64\n\t" \
    "ds_read_b64_tr_b16 v[46:47], %[tr] offset:5696\n\t" \
    "ds_read_b64_tr_b16 v[68:69], %[tr] offset:96\n\t" \
    "ds_read_b64_tr_b16 v[70:71], %[tr] offset:5728\n\t" \
    "ds_read_b64_tr_b16 v[84:85], %[tr] offset:128\n\t" \
    "ds_read_b64_tr_b16 v[86:87], %[tr] offset:5760\n\t" \
    "ds_read_b64_tr_b16 v[108:109], %[tr] offset:160\n\t" \
    "ds_read_b64_tr_b16 v[110:111], %[tr] offset:5792\n\t" \
    "ds_read_b64_tr_b16 v[124:125], %[tr] offset:192\n\t" \
    "ds_read_b64_tr_b16 v[126:127], %[tr] offset:5824\n\t" \
    "ds_read_b64_tr_b16 v[148:149], %[tr] offset:224\n\t" \
    "ds_read_b64_tr_b16 v[150:151], %[tr] offset:5856\n\t" \
    "ds_read_b64_tr_b16 v[164:165], %[tr] offset:256\n\t" \
    "ds_read_b64_tr_b16 v[166:167], %[tr] offset:5888\n\t" \
    "ds_read_b64_tr_b16 v[188:189], %[tr] offset:288\n\t" \
    "ds_read_b64_tr_b16 v[190:191], %[tr] offset:5920\n\t" \
    "s_barrier \n\t" \
    "s_mov_b32 %[t], 0x2d000\n\t" \
    "buffer_load_dwordx4 v[12:15], %[o0], %[rs], %[t] offen" WNT "\n\t" \
    "buffer_load_dwordx4 v[36:39], %[o1], %[rs], %[t] offen" WNT "\n\t" \
    "buffer_load_dwordx4 v[52:55], %[o2], %[rs], %[t] offen" WNT "\n\t" \
    "buffer_load_dwordx4 v[76:79], %[o3], %[rs], %[t] offen" WNT "\n\t" \
    "buffer_load_dwordx4 v[92:95], %[o4], %[rs], %[t] offen" WNT "\n\t" \
    "s_mov_b32 %[t], 0x37000\n\t" \
    "buffer_load_dwordx4 v[116:119], %[o0], %[rs], %[t] offen" WNT "\n\t" \
    "buffer_load_dwordx4 v[132:135], %[o1], %[rs], %[t] offen" WNT "\n\t" \
    "buffer_load_dwordx4 v[156:159], %[o2], %[rs], %[t] offen" WNT "\n\t" \
    "buffer_load_dwordx4 v[172:175], %[o3], %[rs], %[t] offen" WNT "\n\t" \
    "buffer_load_dwordx4 v[196:199], %[o4], %[rs], %[t] offen" WNT "\n\t" \
    "s_waitcnt lgkmcnt(0)\n\t" \
    "s_barrier \n\t" \
    "s_waitcnt vmcnt(15)\n\t" \
    "ds_write_b128 %[s0], v[8:11] offset:0\n\t" \
    "ds_write_b128 %[s1], v[32:35] offset:0\n\t" \
    "ds_write_b128 %[s2], v[48:51] offset:0\n\t" \
    "ds_write_b128 %[s3], v[72:75] offset:0\n\t" \
    "ds_write_b128 %[s4], v[88:91] offset:0\n\t" \
    "s_waitcnt vmcnt(10)\n\t" \
    "ds_write_b128 %[s0], v[112:115] offset:5632\n\t" \
    "ds_write_b128 %[s1], v[128:131] offset:5632\n\t" \
    "ds_write_b128 %[s2], v[152:155] offset:5632\n\t" \
    "ds_write_b128 %[s3], v[168:171] offset:5632\n\t" \
    "ds_write_b128 %[s4], v[192:195] offset:5632\n\t" \
    "ds_read_b64_tr_b16 v[8:9], %[tr] offset:0\n\t" \
    "ds_read_b64_tr_b16 v[10:11], %[tr] offset:5632\n\t" \
    "ds_read_b64_tr_b16 v[32:33], %[tr] offset:32\n\t" \
    "ds_read_b64_tr_b16 v[34:35], %[tr] offset:5664\n\t" \
    "ds_read_b64_tr_b16 v[48:49], %[tr] offset:64\n\t" \
    "ds_read_b64_tr_b16 v[50:51], %[tr] offset:5696\n\t" \
    "ds_read_b64_tr_b16 v[72:73], %[tr] offset:96\n\t" \
    "ds_read_b64_tr_b16 v[74:75], %[tr] offset:5728\n\t" \
    "ds_read_b64_tr_b16 v[88:89], %[tr] offset:128\n\t" \
    "ds_read_b64_tr_b16 v[90:91], %[tr] offset:5760\n\t" \
    "ds_read_b64_tr_b16 v[112:113], %[tr] offset:160\n\t" \
    "ds_read_b64_tr_b16 v[114:115], %[tr] offset:5792\n\t" \
    "ds_read_b64_tr_b16 v[128:129], %[tr] offset:192\n\t" \
    "ds_read_b64_tr_b16 v[130:131], %[tr] offset:5824\n\t" \
    "ds_read_b64_tr_b16 v[152:153], %[tr] offset:224\n\t" \
    "ds_read_b64_tr_b16 v[154:155], %[tr] offset:5856\n\t" \
    "ds_read_b64_tr_b16 v[168:169], %[tr] offset:256\n\t" \
    "ds_read_b64_tr_b16 v[170:171], %[tr] offset:5888\n\t" \
    "ds_read_b64_tr_b16 v[192:193], %[tr] offset:288\n\t" \
    "ds_read_b64_tr_b16 v[194:195], %[tr] offset:5920\n\t" \
    "s_waitcnt lgkmcnt(0)\n\t" \
    "s_barrier \n\t" \
    "s_waitcnt vmcnt(5)\n\t" \
    "ds_write_b128 %[s0], v[12:15] offset:0\n\t" \
    "ds_write_b128 %[s1], v[36:39] offset:0\n\t" \
    "ds_write_b128 %[s2], v[52:55] offset:0\n\t" \
    "ds_write_b128 %[s3], v[76:79] offset:0\n\t" \
    "ds_write_b128 %[s4], v[92:95] offset:0\n\t" \
    "s_waitcnt vmcnt(0)\n\t" \
    "ds_write_b128 %[s0], v[116:119] offset:5632\n\t" \
    "ds_write_b128 %[s1], v[132:135] offset:5632\n\t" \
    "ds_write_b128 %[s2], v[156:159] offset:5632\n\t" \
    "ds_write_b128 %[s3], v[172:175] offset:5632\n\t" \
    "ds_write_b128 %[s4], v[196:199] offset:5632\n\t" \
    "ds_read_b64_tr_b16 v[12:13], %[tr] offset:0\n\t" \
    "ds_read_b64_tr_b16 v[14:15], %[tr] offset:5632\n\t" \
    "ds_read_b64_tr_b16 v[36:37], %[tr] offset:32\n\t" \
    "ds_read_b64_tr_b16 v[38:39], %[tr] offset:5664\n\t" \
    "ds_read_b64_tr_b16 v[52:53], %[tr] offset:64\n\t" \
    "ds_read_b64_tr_b16 v[54:55], %[tr] offset:5696\n\t" \
    "ds_read_b64_tr_b16 v[76:77], %[tr] offset:96\n\t" \
    "ds_read_b64_tr_b16 v[78:79], %[tr] offset:5728\n\t" \
    "ds_read_b64_tr_b16 v[92:93], %[tr] offset:128\n\t" \
    "ds_read_b64_tr_b16 v[94:95], %[tr] offset:5760\n\t" \
    "ds_read_b64_tr_b16 v[116:117], %[tr] offset:160\n\t" \
    "ds_read_b64_tr_b16 v[118:119], %[tr] offset:5792\n\t" \
    "ds_read_b64_tr_b16 v[132:133], %[tr] offset:192\n\t" \
    "ds_read_b64_tr_b16 v[134:135], %[tr] offset:5824\n\t" \
    "ds_read_b64_tr_b16 v[156:157], %[tr] offset:224\n\t" \
    "ds_read_b64_tr_b16 v[158:159], %[tr] offset:5856\n\t" \
    "ds_read_b64_tr_b16 v[172:173], %[tr] offset:256\n\t" \
    "ds_read_b64_tr_b16 v[174:175], %[tr] offset:5888\n\t" \
    "ds_read_b64_tr_b16 v[196:197], %[tr] offset:288\n\t" \
    "ds_read_b64_tr_b16 v[198:199], %[tr] offset:5920\n\t" \
    "s_waitcnt lgkmcnt(0)\n\t" \
    "s_barrier \n\t" \
    "ds_write_b128 %[wl], v[200:203] offset:0\n\t" \
    "ds_write_b128 %[wl], v[204:207] offset:1024\n\t" \
    "ds_write_b128 %[wl], v[208:211] offset:2048\n\t" \
    "ds_write_b128 %[wl], v[212:215] offset:3072\n\t" \
    "ds_write_b128 %[wl], v[216:219] offset:4096\n\t" \
    "ds_write_b128 %[wl], v[220:223] offset:5120\n\t" \
    "ds_write_b128 %[wl], v[224:227] offset:6144\n\t" \
    "ds_write_b128 %[wl], v[228:231] offset:7168\n\t" \
    "ds_write_b128 %[wl], v[232:235] offset:8192\n\t" \
    "ds_write_b128 %[wl], v[236:239] offset:9216\n\t" \
    "s_waitcnt lgkmcnt(0)\n\t"
#define ASM_XG_S_LO \
    "s_waitcnt lgkmcnt(0)\n\t" \
    "ds_read_b128 v[224:227], %[xa] offset:0\n\t" \
    "ds_read_b128 v[244:247], %[wl] offset:0\n\t" \
    "ds_read_b128 v[228:231], %[xa] offset:16384\n\t" \
    "ds_read_b128 v[232:235], %[xa] offset:1024\n\t" \
    "ds_read_b128 v[236:239], %[xa] offset:17408\n\t" \
    "ds_read_b128 v[248:251], %[wl] offset:1024\n\t" \
    "s_waitcnt lgkmcnt(5)\n\t" \
    "v_mfma_f32_16x16x32_bf16 v[204:207], v[0:3], v[224:227], 0\n\t" \
    "v_xor_b32 v252, 0x800, %[xa]\n\t" \
    "ds_read_b128 v[240:243], v252 offset:0\n\t" \
    "v_mfma_f32_16x16x32_bf16 v[208:211], v[8:11], v[224:227], 0\n\t" \
    "s_waitcnt lgkmcnt(5)\n\t" \
    "v_mfma_f32_16x16x32_bf16 v[200:203], v[244:247], v[224:227], 0\n\t" \
    "s_waitcnt lgkmcnt(4)\n\t" \
    "v_mfma_f32_16x16x32_bf16 v[216:219], v[0:3], v[228:231], 0\n\t" \
    "ds_read_b128 v[224:227], v252 offset:16384\n\t" \
    "v_mfma_f32_16x16x32_bf16 v[220:223], v[8:11], v[228:231], 0\n\t" \
    "v_mfma_f32_16x16x32_bf16 v[212:215], v[244:247], v[228:231], 0\n\t" \
    "s_waitcnt lgkmcnt(4)\n\t" \
    "v_mfma_f32_16x16x32_bf16 v[204:207], v[4:7], v[232:235], v[204:207]\n\t" \
    "ds_read_b128 v[228:231], v252 offset:1024\n\t" \
    "v_mfma_f32_16x16x32_bf16 v[208:211], v[12:15], v[232:235], v[208:211]\n\t" \
    "ds_read_b128 v[244:247], %[wl] offset:2048\n\t" \
    "s_waitcnt lgkmcnt(4)\n\t" \
    "v_mfma_f32_16x16x32_bf16 v[200:203], v[248:251], v[232:235], v[200:203]\n\t" \
    "v_mfma_f32_16x16x32_bf16 v[216:219], v[4:7], v[236:239], v[216:219]\n\t" \
    "ds_read_b128 v[232:235], v252 offset:17408\n\t" \
    "v_mfma_f32_16x16x32_bf16 v[220:223], v[12:15], v[236:239], v[220:223]\n\t" \
    "v_mfma_f32_16x16x32_bf16 v[212:215], v[248:251], v[236:239], v[212:215]\n\t" \
    "s_waitcnt lgkmcnt(4)\n\t" \
    "v_mfma_f32_16x16x32_bf16 v[204:207], v[24:27], v[240:243], v[204:207]\n\t" \
    "v_xor_b32 v252, 0x1000, %[xa]\n\t" \
    "ds_read_b128 v[236:239], v252 offset:0\n\t" \
    "v_mfma_f32_16x16x32_bf16 v[208:211], v[32:35], v[240:243], v[208:211]\n\t" \
    "ds_read_b128 v[248:251], %[wl] offset:3072\n\t" \
    "v_mfma_f32_16x16x32_bf16 v[200:203], v[16:19], v[240:243], v[200:203]\n\t" \
    "s_waitcnt lgkmcnt(5)\n\t" \
    "v_mfma_f32_16x16x32_bf16 v[216:219], v[24:27], v[224:227], v[216:219]\n\t" \
    "ds_read_b128 v[240:243], v252 offset:16384\n\t" \
    "v_mfma_f32_16x16x32_bf16 v[220:223], v[32:35], v[224:227], v[220:223]\n\t" \
    "v_mfma_f32_16x16x32_bf16 v[212:215], v[16:19], v[224:227], v[212:215]\n\t" \
    "s_waitcnt lgkmcnt(5)\n\t" \
    "v_mfma_f32_16x16x32_bf16 v[204:207], v[28:31], v[228:231], v[204:207]\n\t" \
    "ds_read_b128 v[224:227], v252 offset:1024\n\t" \
    "v_mfma_f32_16x16x32_bf16 v[208:211], v[36:39], v[228:231], v[208:211]\n\t" \
    "v_mfma_f32_16x16x32_bf16 v[200:203], v[20:23], v[228:231], v[200:203]\n\t" \
    "s_waitcnt lgkmcnt(4)\n\t" \
    "v_mfma_f32_16x16x32_bf16 v[216:219], v[28:31], v[232:235], v[216:219]\n\t" \
    "ds_read_b128 v[228:231], v252 offset:17408\n\t" \
    "v_mfma_f32_16x16x32_bf16 v[220:223], v[36:39], v[232:235], v[220:223]\n\t" \
    "v_mfma_f32_16x16x32_bf16 v[212:215], v[20:23], v[232:235], v[212:215]\n\t" \
    "s_waitcnt lgkmcnt(4)\n\t" \
    "v_mfma_f32_16x16x32_bf16 v[204:207], v[40:43], v[236:239], v[204:207]\n\t" \
    "v_xor_b32 v252, 0x1800, %[xa]\n\t" \
    "ds_read_b128 v[232:235], v252 offset:0\n\t" \
    "v_mfma_f32_16x16x32_bf16 v[208:211], v[48:51], v[236:239], v[208:211]\n\t" \
    "v_mfma_f32_16x16x32_bf16 v[200:203], v[244:247], v[236:239], v[200:203]\n\t" \
    "s_waitcnt lgkmcnt(3)\n\t" \
    "v_mfma_f32_16x16x32_bf16 v[216:219], v[40:43], v[240:243], v[216:219]\n\t" \
    "ds_read_b128 v[236:239], v252 offset:16384\n\t" \
    "v_mfma_f32_16x16x32_bf16 v[220:223], v[48:51], v[240:243], v[220:223]\n\t" \
    "v_mfma_f32_16x16x32_bf16 v[212:215], v[244:247], v[240:243], v[212:215]\n\t" \
    "s_waitcnt lgkmcnt(3)\n\t" \
    "v_mfma_f32_16x16x32_bf16 v[204:207], v[44:47], v[224:227], v[204:207]\n\t" \
    "ds_read_b128 v[240:243], v252 offset:1024\n\t" \
    "v_mfma_f32_16x16x32_bf16 v[208:211], v[52:55], v[224:227], v[208:211]\n\t" \
    "ds_read_b128 v[244:247], %[wl] offset:8192\n\t" \
    "v_mfma_f32_16x16x32_bf16 v[200:203], v[248:251], v[224:227], v[200:203]\n\t" \
    "s_waitcnt lgkmcnt(4)\n\t" \
    "v_mfma_f32_16x16x32_bf16 v[216:219], v[44:47], v[228:231], v[216:219]\n\t" \
    "ds_read_b128 v[224:227], v252 offset:17408\n\t" \
    "v_mfma_f32_16x16x32_bf16 v[220:223], v[52:55], v[228:231], v[220:223]\n\t" \
    "v_mfma_f32_16x16x32_bf16 v[212:215], v[248:251], v[228:231], v[212:215]\n\t" \
    "s_waitcnt lgkmcnt(4)\n\t" \
    "v_mfma_f32_16x16x32_bf16 v[204:207], v[64:67], v[232:235], v[204:207]\n\t" \
    "v_and_b32 v252, 0x3ff, %[xa]\n\t" \
    "ds_read_b128 v[228:231], v252 offset:40960\n\t" \
    "v_mfma_f32_16x16x32_bf16 v[208:211], v[72:75], v[232:235], v[208:211]\n\t" \
    "ds_read_b128 v[248:251], %[wl] offset:9216\n\t" \
    "v_mfma_f32_16x16x32_bf16 v[200:203], v[56:59], v[232:235], v[200:203]\n\t" \
    "s_waitcnt lgkmcnt(5)\n\t" \
    "v_mfma_f32_16x16x32_bf16 v[216:219], v[64:67], v[236:239], v[216:219]\n\t" \
    "ds_read_b128 v[232:235], v252 offset:45056\n\t" \
    "v_mfma_f32_16x16x32_bf16 v[220:223], v[72:75], v[236:239], v[220:223]\n\t" \
    "v_mfma_f32_16x16x32_bf16 v[212:215], v[56:59], v[236:239], v[212:215]\n\t" \
    "s_waitcnt lgkmcnt(5)\n\t" \
    "v_mfma_f32_16x16x32_bf16 v[204:207], v[68:71], v[240:243], v[204:207]\n\t" \
    "ds_read_b128 v[236:239], v252 offset:41984\n\t" \
    "v_mfma_f32_16x16x32_bf16 v[208:211], v[76:79], v[240:243], v[208:211]\n\t" \
    "v_mfma_f32_16x16x32_bf16 v[200:203], v[60:63], v[240:243], v[200:203]\n\t" \
    "s_waitcnt lgkmcnt(4)\n\t" \
    "v_mfma_f32_16x16x32_bf16 v[216:219], v[68:71], v[224:227], v[216:219]\n\t" \
    "ds_read_b128 v[240:243], v252 offset:46080\n\t" \
    "v_mfma_f32_16x16x32_bf16 v[220:223], v[76:79], v[224:227], v[220:223]\n\t" \
    "v_mfma_f32_16x16x32_bf16 v[212:215], v[60:63], v[224:227], v[212:215]\n\t" \
    "s_waitcnt lgkmcnt(0)\n\t" \
    "s_barrier\n\t" ASM_ST \
    "v_mfma_f32_16x16x32_bf16 v[204:207], v[160:163], v[228:231], v[204:207]\n\t" \
    "v_xor_b32 v252, 0x2000, %[xa]\n\t" \
    "ds_read_b128 v[224:227], v252 offset:0\n\t" \
    "v_mfma_f32_16x16x32_bf16 v[208:211], v[168:171], v[228:231], v[208:211]\n\t" \
    "v_mfma_f32_16x16x32_bf16 v[200:203], v[244:247], v[228:231], v[200:203]\n\t" \
    "v_mfma_f32_16x16x32_bf16 v[216:219], v[160:163], v[232:235], v[216:219]\n\t" \
    "ds_read_b128 v[228:231], v252 offset:16384\n\t" \
    "v_mfma_f32_16x16x32_bf16 v[220:223], v[168:171], v[232:235], v[220:223]\n\t" \
    "v_mfma_f32_16x16x32_bf16 v[212:215], v[244:247], v[232:235], v[212:215]\n\t" \
    "v_mfma_f32_16x16x32_bf16 v[204:207], v[164:167], v[236:239], v[204:207]\n\t" \
    "ds_read_b128 v[232:235], v252 offset:1024\n\t" \
    "v_mfma_f32_16x16x32_bf16 v[208:211], v[172:175], v[236:239], v[208:211]\n\t" \
    "ds_read_b128 v[244:247], %[wl] offset:4096\n\t" \
    "v_mfma_f32_16x16x32_bf16 v[200:203], v[248:251], v[236:239], v[200:203]\n\t" \
    "v_mfma_f32_16x16x32_bf16 v[216:219], v[164:167], v[240:243], v[216:219]\n\t" \
    "ds_read_b128 v[236:239], v252 offset:17408\n\t" \
    "v_mfma_f32_16x16x32_bf16 v[220:223], v[172:175], v[240:243], v[220:223]\n\t" \
    "v_mfma_f32_16x16x32_bf16 v[212:215], v[248:251], v[240:243], v[212:215]\n\t" \
    "s_waitcnt lgkmcnt(4)\n\t" \
    "v_mfma_f32_16x16x32_bf16 v[204:207], v[80:83], v[224:227], v[204:207]\n\t" \
    "v_xor_b32 v252, 0x2800, %[xa]\n\t" \
    "ds_read_b128 v[240:243], v252 offset:0\n\t" \
    "v_mfma_f32_16x16x32_bf16 v[208:211], v[88:91], v[224:227], v[208:211]\n\t" \
    "ds_read_b128 v[248:251], %[wl] offset:5120\n\t" \
    "s_waitcnt lgkmcnt(3)\n\t" \
    "v_mfma_f32_16x16x32_bf16 v[200:203], v[244:247], v[224:227], v[200:203]\n\t" \
    "v_mfma_f32_16x16x32_bf16 v[216:219], v[80:83], v[228:231], v[216:219]\n\t" \
    "ds_read_b128 v[224:227], v252 offset:16384\n\t" \
    "v_mfma_f32_16x16x32_bf16 v[220:223], v[88:91], v[228:231], v[220:223]\n\t" \
    "v_mfma_f32_16x16x32_bf16 v[212:215], v[244:247], v[228:231], v[212:215]\n\t" \
    "v_mfma_f32_16x16x32_bf16 v[204:207], v[84:87], v[232:235], v[204:207]\n\t" \
    "ds_read_b128 v[228:231], v252 offset:1024\n\t" \
    "v_mfma_f32_16x16x32_bf16 v[208:211], v[92:95], v[232:235], v[208:211]\n\t" \
    "ds_read_b128 v[244:247], %[wl] offset:6144\n\t" \
    "s_waitcnt lgkmcnt(3)\n\t" \
    "v_mfma_f32_16x16x32_bf16 v[200:203], v[248:251], v[232:235], v[200:203]\n\t" \
    "v_mfma_f32_16x16x32_bf16 v[216:219], v[84:87], v[236:239], v[216:219]\n\t" \
    "ds_read_b128 v[232:235], v252 offset:17408\n\t" \
    "v_mfma_f32_16x16x32_bf16 v[220:223], v[92:95], v[236:239], v[220:223]\n\t" \
    "v_mfma_f32_16x16x32_bf16 v[212:215], v[248:251], v[236:239], v[212:215]\n\t" \
    "v_mfma_f32_16x16x32_bf16 v[204:207], v[104:107], v[240:243], v[204:207]\n\t" \
    "v_xor_b32 v252, 0x3000, %[xa]\n\t" \
    "ds_read_b128 v[236:239], v252 offset:0\n\t" \
    "v_mfma_f32_16x16x32_bf16 v[208:211], v[112:115], v[240:243], v[208:211]\n\t" \
    "ds_read_b128 v[248:251], %[wl] offset:7168\n\t" \
    "v_mfma_f32_16x16x32_bf16 v[200:203], v[96:99], v[240:243], v[200:203]\n\t" \
    "s_waitcnt lgkmcnt(5)\n\t" \
    "v_mfma_f32_16x16x32_bf16 v[216:219], v[104:107], v[224:227], v[216:219]\n\t" \
    "ds_read_b128 v[240:243], v252 offset:16384\n\t" \
    "v_mfma_f32_16x16x32_bf16 v[220:223], v[112:115], v[224:227], v[220:223]\n\t" \
    "v_mfma_f32_16x16x32_bf16 v[212:215], v[96:99], v[224:227], v[212:215]\n\t" \
    "s_waitcnt lgkmcnt(5)\n\t" \
    "v_mfma_f32_16x16x32_bf16 v[204:207], v[108:111], v[228:231], v[204:207]\n\t" \
    "ds_read_b128 v[224:227], v252 offset:1024\n\t" \
    "v_mfma_f32_16x16x32_bf16 v[208:211], v[116:119], v[228:231], v[208:211]\n\t" \
    "v_mfma_f32_16x16x32_bf16 v[200:203], v[100:103], v[228:231], v[200:203]\n\t" \
    "s_waitcnt lgkmcnt(4)\n\t" \
    "v_mfma_f32_16x16x32_bf16 v[216:219], v[108:111], v[232:235], v[216:219]\n\t" \
    "ds_read_b128 v[228:231], v252 offset:17408\n\t" \
    "v_mfma_f32_16x16x32_bf16 v[220:223], v[116:119], v[232:235], v[220:223]\n\t" \
    "v_mfma_f32_16x16x32_bf16 v[212:215], v[100:103], v[232:235], v[212:215]\n\t" \
    "s_waitcnt lgkmcnt(4)\n\t" \
    "v_mfma_f32_16x16x32_bf16 v[204:207], v[120:123], v[236:239], v[204:207]\n\t" \
    "v_xor_b32 v252, 0x3800, %[xa]\n\t" \
    "ds_read_b128 v[232:235], v252 offset:0\n\t" \
    "v_mfma_f32_16x16x32_bf16 v[208:211], v[128:131], v[236:239], v[208:211]\n\t" \
    "v_mfma_f32_16x16x32_bf16 v[200:203], v[244:247], v[236:239], v[200:203]\n\t" \
    "s_waitcnt lgkmcnt(3)\n\t" \
    "v_mfma_f32_16x16x32_bf16 v[216:219], v[120:123], v[240:243], v[216:219]\n\t" \
    "ds_read_b128 v[236:239], v252 offset:16384\n\t" \
    "v_mfma_f32_16x16x32_bf16 v[220:223], v[128:131], v[240:243], v[220:223]\n\t" \
    "v_mfma_f32_16x16x32_bf16 v[212:215], v[244:247], v[240:243], v[212:215]\n\t" \
    "s_waitcnt lgkmcnt(3)\n\t" \
    "v_mfma_f32_16x16x32_bf16 v[204:207], v[124:127], v[224:227], v[204:207]\n\t" \
    "ds_read_b128 v[240:243], v252 offset:1024\n\t" \
    "v_mfma_f32_16x16x32_bf16 v[208:211], v[132:135], v[224:227], v[208:211]\n\t" \
    "v_mfma_f32_16x16x32_bf16 v[200:203], v[248:251], v[224:227], v[200:203]\n\t" \
    "s_waitcnt lgkmcnt(3)\n\t" \
    "v_mfma_f32_16x16x32_bf16 v[216:219], v[124:127], v[228:231], v[216:219]\n\t" \
    "ds_read_b128 v[224:227], v252 offset:17408\n\t" \
    "v_mfma_f32_16x16x32_bf16 v[220:223], v[132:135], v[228:231], v[220:223]\n\t" \
    "v_mfma_f32_16x16x32_bf16 v[212:215], v[248:251], v[228:231], v[212:215]\n\t" \
    "s_waitcnt lgkmcnt(3)\n\t" \
    "v_mfma_f32_16x16x32_bf16 v[204:207], v[144:147], v[232:235], v[204:207]\n\t" \
    "v_and_b32 v252, 0x3ff, %[xa]\n\t" \
    "ds_read_b128 v[228:231], v252 offset:43008\n\t" \
    "v_mov_b64 v[244:245], 0\n\t" \
    "v_mfma_f32_16x16x32_bf16 v[208:211], v[152:155], v[232:235], v[208:211]\n\t" \
    "v_mov_b64 v[246:247], 0\n\t" \
    "v_mfma_f32_16x16x32_bf16 v[200:203], v[136:139], v[232:235], v[200:203]\n\t" \
    "v_mov_b64 v[248:249], 0\n\t" \
    "s_waitcnt lgkmcnt(3)\n\t" \
    "v_mfma_f32_16x16x32_bf16 v[216:219], v[144:147], v[236:239], v[216:219]\n\t" \
    "ds_read_b128 v[232:235], v252 offset:44032\n\t" \
    "v_mov_b64 v[250:251], 0\n\t" \
    "v_mfma_f32_16x16x32_bf16 v[220:223], v[152:155], v[236:239], v[220:223]\n\t" \
    "v_mfma_f32_16x16x32_bf16 v[212:215], v[136:139], v[236:239], v[212:215]\n\t" \
    "s_waitcnt lgkmcnt(3)\n\t" \
    "v_mfma_f32_16x16x32_bf16 v[204:207], v[148:151], v[240:243], v[204:207]\n\t" \
    "ds_read_b128 v[236:239], v252 offset:47104\n\t" \
    "v_mfma_f32_16x16x32_bf16 v[208:211], v[156:159], v[240:243], v[208:211]\n\t" \
    "v_mfma_f32_16x16x32_bf16 v[200:203], v[140:143], v[240:243], v[200:203]\n\t" \
    "s_waitcnt lgkmcnt(3)\n\t" \
    "v_mfma_f32_16x16x32_bf16 v[216:219], v[148:151], v[224:227], v[216:219]\n\t" \
    "ds_read_b128 v[240:243], v252 offset:48128\n\t" \
    "v_mfma_f32_16x16x32_bf16 v[220:223], v[156:159], v[224:227], v[220:223]\n\t" \
    "v_mfma_f32_16x16x32_bf16 v[212:215], v[140:143], v[224:227], v[212:215]\n\t" \
    "s_waitcnt lgkmcnt(3)\n\t" \
    "v_mfma_f32_16x16x32_bf16 v[204:207], v[184:187], v[228:231], v[204:207]\n\t" \
    "v_add_u32 v252, %[qs], v252\n\t" \
    "v_mfma_f32_16x16x32_bf16 v[208:211], v[192:195], v[228:231], v[208:211]\n\t" \
    "v_mov_b64 v[224:225], 0\n\t" \
    "v_mfma_f32_16x16x32_bf16 v[200:203], v[176:179], v[228:231], v[200:203]\n\t" \
    "v_mov_b64 v[226:227], 0\n\t" \
    "s_waitcnt lgkmcnt(2)\n\t" \
    "v_mfma_f32_16x16x32_bf16 v[204:207], v[188:191], v[232:235], v[204:207]\n\t" \
    "v_mfma_f32_16x16x32_bf16 v[208:211], v[196:199], v[232:235], v[208:211]\n\t" \
    "v_mfma_f32_16x16x32_bf16 v[200:203], v[180:183], v[232:235], v[200:203]\n\t" \
    "s_waitcnt lgkmcnt(1)\n\t" \
    "v_mfma_f32_16x16x32_bf16 v[216:219], v[184:187], v[236:239], v[216:219]\n\t" \
    "ds_read_b128 v[228:231], %[wl] offset:2048\n\t" \
    "ds_read_b128 v[232:235], %[wl] offset:3072\n\t" \
    "v_mfma_f32_16x16x32_bf16 v[220:223], v[192:195], v[236:239], v[220:223]\n\t" \
    "v_cvt_pk_bf16_f32 v204, v204, v205\n\t" \
    "v_cvt_pk_bf16_f32 v205, v206, v207\n\t" \
    "v_mfma_f32_16x16x32_bf16 v[212:215], v[176:179], v[236:239], v[212:215]\n\t" \
    "ds_write_b64 v252, v[204:205] offset:1024\n\t" \
    "v_cvt_pk_bf16_f32 v208, v208, v209\n\t" \
    "s_waitcnt lgkmcnt(3)\n\t" \
    "v_mfma_f32_16x16x32_bf16 v[216:219], v[188:191], v[240:243], v[216:219]\n\t" \
    "v_cvt_pk_bf16_f32 v209, v210, v211\n\t" \
    "ds_write_b64 v252, v[208:209] offset:2048\n\t" \
    "v_mfma_f32_16x16x32_bf16 v[220:223], v[196:199], v[240:243], v[220:223]\n\t" \
    "v_cvt_pk_bf16_f32 v200, v200, v201\n\t" \
    "v_cvt_pk_bf16_f32 v201, v202, v203\n\t" \
    "v_mfma_f32_16x16x32_bf16 v[212:215], v[180:183], v[240:243], v[212:215]\n\t" \
    "ds_write_b64 v252, v[200:201] offset:0\n\t" \
    "ds_read_b128 v[200:203], %[wl] offset:0\n\t" \
    "ds_read_b128 v[204:207], %[wl] offset:1024\n\t" \
    "v_cvt_pk_bf16_f32 v216, v216, v217\n\t" \
    "v_cvt_pk_bf16_f32 v217, v218, v219\n\t" \
    "ds_write_b64 v252, v[216:217] offset:7168\n\t" \
    "v_cvt_pk_bf16_f32 v220, v220, v221\n\t" \
    "v_cvt_pk_bf16_f32 v221, v222, v223\n\t" \
    "ds_write_b64 v252, v[220:221] offset:8192\n\t" \
    "v_cvt_pk_bf16_f32 v212, v212, v213\n\t" \
    "v_cvt_pk_bf16_f32 v213, v214, v215\n\t" \
    "ds_write_b64 v252, v[212:213] offset:6144\n\t" \
    "s_waitcnt lgkmcnt(0)\n\t"
#define ASM_XG_S_HI \
    "s_waitcnt lgkmcnt(0)\n\t" \
    "v_xor_b32 v252, 0x2000, %[xa]\n\t" \
    "ds_read_b128 v[224:227], v252 offset:0\n\t" \
    "ds_read_b128 v[244:247], %[wl] offset:4096\n\t" \
    "ds_read_b128 v[228:231], v252 offset:16384\n\t" \
    "ds_read_b128 v[232:235], v252 offset:1024\n\t" \
    "ds_read_b128 v[236:239], v252 offset:17408\n\t" \
    "ds_read_b128 v[248:251], %[wl] offset:5120\n\t" \
    "s_cmp_eq_u32 %[slp], 0\n\t" \
    "s_cbranch_scc1 .Lxs%=\n\t" \
    "s_sleep 10\n\t" \
    ".Lxs%=:\n\t" \
    "s_waitcnt lgkmcnt(5)\n\t" \
    "v_mfma_f32_16x16x32_bf16 v[204:207], v[80:83], v[224:227], 0\n\t" \
    "v_xor_b32 v252, 0x2800, %[xa]\n\t" \
    "ds_read_b128 v[240:243], v252 offset:0\n\t" \
    "v_mfma_f32_16x16x32_bf16 v[208:211], v[88:91], v[224:227], 0\n\t" \
    "s_waitcnt lgkmcnt(5)\n\t" \
    "v_mfma_f32_16x16x32_bf16 v[200:203], v[244:247], v[224:227], 0\n\t" \
    "s_waitcnt lgkmcnt(4)\n\t" \
    "v_mfma_f32_16x16x32_bf16 v[216:219], v[80:83], v[228:231], 0\n\t" \
    "ds_read_b128 v[224:227], v252 offset:16384\n\t" \
    "v_mfma_f32_16x16x32_bf16 v[220:223], v[88:91], v[228:231], 0\n\t" \
    "v_mfma_f32_16x16x32_bf16 v[212:215], v[244:247], v[228:231], 0\n\t" \
    "s_waitcnt lgkmcnt(4)\n\t" \
    "v_mfma_f32_16x16x32_bf16 v[204:207], v[84:87], v[232:235], v[204:207]\n\t" \
    "ds_read_b128 v[228:231], v252 offset:1024\n\t" \
    "v_mfma_f32_16x16x32_bf16 v[208:211], v[92:95], v[232:235], v[208:211]\n\t" \
    "ds_read_b128 v[244:247], %[wl] offset:6144\n\t" \
    "s_waitcnt lgkmcnt(4)\n\t" \
    "v_mfma_f32_16x16x32_bf16 v[200:203], v[248:251], v[232:235], v[200:203]\n\t" \
    "v_mfma_f32_16x16x32_bf16 v[216:219], v[84:87], v[236:239], v[216:219]\n\t" \
    "ds_read_b128 v[232:235], v252 offset:17408\n\t" \
    "v_mfma_f32_16x16x32_bf16 v[220:223], v[92:95], v[236:239], v[220:223]\n\t" \
    "v_mfma_f32_16x16x32_bf16 v[212:215], v[248:251], v[236:239], v[212:215]\n\t" \
    "s_waitcnt lgkmcnt(4)\n\t" \
    "v_mfma_f32_16x16x32_bf16 v[204:207], v[104:107], v[240:243], v[204:207]\n\t" \
    "v_xor_b32 v252, 0x3000, %[xa]\n\t" \
    "ds_read_b128 v[236:239], v252 offset:0\n\t" \
    "v_mfma_f32_16x16x32_bf16 v[208:211], v[112:115], v[240:243], v[208:211]\n\t" \
    "ds_read_b128 v[248:251], %[wl] offset:7168\n\t" \
    "v_mfma_f32_16x16x32_bf16 v[200:203], v[96:99], v[240:243], v[200:203]\n\t" \
    "s_waitcnt lgkmcnt(5)\n\t" \
    "v_mfma_f32_16x16x32_bf16 v[216:219], v[104:107], v[224:227], v[216:219]\n\t" \
    "ds_read_b128 v[240:243], v252 offset:16384\n\t" \
    "v_mfma_f32_16x16x32_bf16 v[220:223], v[112:115], v[224:227], v[220:223]\n\t" \
    "v_mfma_f32_16x16x32_bf16 v[212:215], v[96:99], v[224:227], v[212:215]\n\t" \
    "s_waitcnt lgkmcnt(5)\n\t" \
    "v_mfma_f32_16x16x32_bf16 v[204:207], v[108:111], v[228:231], v[204:207]\n\t" \
    "ds_read_b128 v[224:227], v252 offset:1024\n\t" \
    "v_mfma_f32_16x16x32_bf16 v[208:211], v[116:119], v[228:231], v[208:211]\n\t" \
    "v_mfma_f32_16x16x32_bf16 v[200:203], v[100:103], v[228:231], v[200:203]\n\t" \
    "s_waitcnt lgkmcnt(4)\n\t" \
    "v_mfma_f32_16x16x32_bf16 v[216:219], v[108:111], v[232:235], v[216:219]\n\t" \
    "ds_read_b128 v[228:231], v252 offset:17408\n\t" \
    "v_mfma_f32_16x16x32_bf16 v[220:223], v[116:119], v[232:235], v[220:223]\n\t" \
    "v_mfma_f32_16x16x32_bf16 v[212:215], v[100:103], v[232:235], v[212:215]\n\t" \
    "s_waitcnt lgkmcnt(4)\n\t" \
    "v_mfma_f32_16x16x32_bf16 v[204:207], v[120:123], v[236:239], v[204:207]\n\t" \
    "v_xor_b32 v252, 0x3800, %[xa]\n\t" \
    "ds_read_b128 v[232:235], v252 offset:0\n\t" \
    "v_mfma_f32_16x16x32_bf16 v[208:211], v[128:131], v[236:239], v[208:211]\n\t" \
    "v_mfma_f32_16x16x32_bf16 v[200:203], v[244:247], v[236:239], v[200:203]\n\t" \
    "s_waitcnt lgkmcnt(3)\n\t" \
    "v_mfma_f32_16x16x32_bf16 v[216:219], v[120:123], v[240:243], v[216:219]\n\t" \
    "ds_read_b128 v[236:239], v252 offset:16384\n\t" \
    "v_mfma_f32_16x16x32_bf16 v[220:223], v[128:131], v[240:243], v[220:223]\n\t" \
    "v_mfma_f32_16x16x32_bf16 v[212:215], v[244:247], v[240:243], v[212:215]\n\t" \
    "s_waitcnt lgkmcnt(3)\n\t" \
    "v_mfma_f32_16x16x32_bf16 v[204:207], v[124:127], v[224:227], v[204:207]\n\t" \
    "ds_read_b128 v[240:243], v252 offset:1024\n\t" \
    "v_mfma_f32_16x16x32_bf16 v[208:211], v[132:135], v[224:227], v[208:211]\n\t" \
    "ds_read_b128 v[244:247], %[wl] offset:8192\n\t" \
    "v_mfma_f32_16x16x32_bf16 v[200:203], v[248:251], v[224:227], v[200:203]\n\t" \
    "s_waitcnt lgkmcnt(4)\n\t" \
    "v_mfma_f32_16x16x32_bf16 v[216:219], v[124:127], v[228:231], v[216:219]\n\t" \
    "ds_read_b128 v[224:227], v252 offset:17408\n\t" \
    "v_mfma_f32_16x16x32_bf16 v[220:223], v[132:135], v[228:231], v[220:223]\n\t" \
    "v_mfma_f32_16x16x32_bf16 v[212:215], v[248:251], v[228:231], v[212:215]\n\t" \
    "s_waitcnt lgkmcnt(4)\n\t" \
    "v_mfma_f32_16x16x32_bf16 v[204:207], v[144:147], v[232:235], v[204:207]\n\t" \
    "v_and_b32 v252, 0x3ff, %[xa]\n\t" \
    "ds_read_b128 v[228:231], v252 offset:40960\n\t" \
    "v_mfma_f32_16x16x32_bf16 v[208:211], v[152:155], v[232:235], v[208:211]\n\t" \
    "ds_read_b128 v[248:251], %[wl] offset:9216\n\t" \
    "v_mfma_f32_16x16x32_bf16 v[200:203], v[136:139], v[232:235], v[200:203]\n\t" \
    "s_waitcnt lgkmcnt(5)\n\t" \
    "v_mfma_f32_16x16x32_bf16 v[216:219], v[144:147], v[236:239], v[216:219]\n\t" \
    "ds_read_b128 v[232:235], v252 offset:45056\n\t" \
    "v_mfma_f32_16x16x32_bf16 v[220:223], v[152:155], v[236:239], v[220:223]\n\t" \
    "v_mfma_f32_16x16x32_bf16 v[212:215], v[136:139], v[236:239], v[212:215]\n\t" \
    "s_waitcnt lgkmcnt(5)\n\t" \
    "v_mfma_f32_16x16x32_bf16 v[204:207], v[148:151], v[240:243], v[204:207]\n\t" \
    "ds_read_b128 v[236:239], v252 offset:41984\n\t" \
    "v_mfma_f32_16x16x32_bf16 v[208:211], v[156:159], v[240:243], v[208:211]\n\t" \
    "v_mfma_f32_16x16x32_bf16 v[200:203], v[140:143], v[240:243], v[200:203]\n\t" \
    "s_waitcnt lgkmcnt(4)\n\t" \
    "v_mfma_f32_16x16x32_bf16 v[216:219], v[148:151], v[224:227], v[216:219]\n\t" \
    "ds_read_b128 v[240:243], v252 offset:46080\n\t" \
    "v_mfma_f32_16x16x32_bf16 v[220:223], v[156:159], v[224:227], v[220:223]\n\t" \
    "v_mfma_f32_16x16x32_bf16 v[212:215], v[140:143], v[224:227], v[212:215]\n\t" \
    "s_waitcnt vmcnt(0) lgkmcnt(0)\n\t" \
    "s_barrier\n\t" ASM_ST \
    "s_mov_b32 m0, %[d0]\n\t" \
    "s_nop 0\n\t" \
    "global_load_lds_dwordx4 v252, %[g0] offset:0 sc1\n\t" \
    "global_load_lds_dwordx4 v252, %[g0] offset:1024 sc1\n\t" \
    "global_load_lds_dwordx4 v252, %[g0] offset:2048 sc1\n\t" \
    "global_load_lds_dwordx4 v252, %[g0] offset:3072 sc1\n\t" \
    "s_mov_b32 m0, %[d1]\n\t" \
    "s_nop 0\n\t" \
    "global_load_lds_dwordx4 v252, %[g1] sc1\n\t" \
    "v_mfma_f32_16x16x32_bf16 v[204:207], v[160:163], v[228:231], v[204:207]\n\t" \
    "ds_read_b128 v[224:227], %[xa] offset:0\n\t" \
    "v_mfma_f32_16x16x32_bf16 v[208:211], v[168:171], v[228:231], v[208:211]\n\t" \
    "v_mfma_f32_16x16x32_bf16 v[200:203], v[244:247], v[228:231], v[200:203]\n\t" \
    "v_mfma_f32_16x16x32_bf16 v[216:219], v[160:163], v[232:235], v[216:219]\n\t" \
    "ds_read_b128 v[228:231], %[xa] offset:16384\n\t" \
    "v_mfma_f32_16x16x32_bf16 v[220:223], v[168:171], v[232:235], v[220:223]\n\t" \
    "v_mfma_f32_16x16x32_bf16 v[212:215], v[244:247], v[232:235], v[212:215]\n\t" \
    "v_mfma_f32_16x16x32_bf16 v[204:207], v[164:167], v[236:239], v[204:207]\n\t" \
    "ds_read_b128 v[232:235], %[xa] offset:1024\n\t" \
    "v_mfma_f32_16x16x32_bf16 v[208:211], v[172:175], v[236:239], v[208:211]\n\t" \
    "ds_read_b128 v[244:247], %[wl] offset:0\n\t" \
    "v_mfma_f32_16x16x32_bf16 v[200:203], v[248:251], v[236:239], v[200:203]\n\t" \
    "v_mfma_f32_16x16x32_bf16 v[216:219], v[164:167], v[240:243], v[216:219]\n\t" \
    "ds_read_b128 v[236:239], %[xa] offset:17408\n\t" \
    "v_mfma_f32_16x16x32_bf16 v[220:223], v[172:175], v[240:243], v[220:223]\n\t" \
    "v_mfma_f32_16x16x32_bf16 v[212:215], v[248:251], v[240:243], v[212:215]\n\t" \
    "s_waitcnt lgkmcnt(4)\n\t" \
    "v_mfma_f32_16x16x32_bf16 v[204:207], v[0:3], v[224:227], v[204:207]\n\t" \
    "v_xor_b32 v252, 0x800, %[xa]\n\t" \
    "ds_read_b128 v[240:243], v252 offset:0\n\t" \
    "v_mfma_f32_16x16x32_bf16 v[208:211], v[8:11], v[224:227], v[208:211]\n\t" \
    "ds_read_b128 v[248:251], %[wl] offset:1024\n\t" \
    "s_waitcnt lgkmcnt(3)\n\t" \
    "v_mfma_f32_16x16x32_bf16 v[200:203], v[244:247], v[224:227], v[200:203]\n\t" \
    "v_mfma_f32_16x16x32_bf16 v[216:219], v[0:3], v[228:231], v[216:219]\n\t" \
    "ds_read_b128 v[224:227], v252 offset:16384\n\t" \
    "v_mfma_f32_16x16x32_bf16 v[220:223], v[8:11], v[228:231], v[220:223]\n\t" \
    "v_mfma_f32_16x16x32_bf16 v[212:215], v[244:247], v[228:231], v[212:215]\n\t" \
    "v_mfma_f32_16x16x32_bf16 v[204:207], v[4:7], v[232:235], v[204:207]\n\t" \
    "ds_read_b128 v[228:231], v252 offset:1024\n\t" \
    "v_mfma_f32_16x16x32_bf16 v[208:211], v[12:15], v[232:235], v[208:211]\n\t" \
    "ds_read_b128 v[244:247], %[wl] offset:2048\n\t" \
    "s_waitcnt lgkmcnt(3)\n\t" \
    "v_mfma_f32_16x16x32_bf16 v[200:203], v[248:251], v[232:235], v[200:203]\n\t" \
    "v_mfma_f32_16x16x32_bf16 v[216:219], v[4:7], v[236:239], v[216:219]\n\t" \
    "ds_read_b128 v[232:235], v252 offset:17408\n\t" \
    "v_mfma_f32_16x16x32_bf16 v[220:223], v[12:15], v[236:239], v[220:223]\n\t" \
    "v_mfma_f32_16x16x32_bf16 v[212:215], v[248:251], v[236:239], v[212:215]\n\t" \
    "v_mfma_f32_16x16x32_bf16 v[204:207], v[24:27], v[240:243], v[204:207]\n\t" \
    "v_xor_b32 v252, 0x1000, %[xa]\n\t" \
    "ds_read_b128 v[236:239], v252 offset:0\n\t" \
    "v_mfma_f32_16x16x32_bf16 v[208:211], v[32:35], v[240:243], v[208:211]\n\t" \
    "ds_read_b128 v[248:251], %[wl] offset:3072\n\t" \
    "v_mfma_f32_16x16x32_bf16 v[200:203], v[16:19], v[240:243], v[200:203]\n\t" \
    "s_waitcnt lgkmcnt(5)\n\t" \
    "v_mfma_f32_16x16x32_bf16 v[216:219], v[24:27], v[224:227], v[216:219]\n\t" \
    "ds_read_b128 v[240:243], v252 offset:16384\n\t" \
    "v_mfma_f32_16x16x32_bf16 v[220:223], v[32:35], v[224:227], v[220:223]\n\t" \
    "v_mfma_f32_16x16x32_bf16 v[212:215], v[16:19], v[224:227], v[212:215]\n\t" \
    "s_waitcnt lgkmcnt(5)\n\t" \
    "v_mfma_f32_16x16x32_bf16 v[204:207], v[28:31], v[228:231], v[204:207]\n\t" \
    "ds_read_b128 v[224:227], v252 offset:1024\n\t" \
    "v_mfma_f32_16x16x32_bf16 v[208:211], v[36:39], v[228:231], v[208:211]\n\t" \
    "v_mfma_f32_16x16x32_bf16 v[200:203], v[20:23], v[228:231], v[200:203]\n\t" \
    "s_waitcnt lgkmcnt(4)\n\t" \
    "v_mfma_f32_16x16x32_bf16 v[216:219], v[28:31], v[232:235], v[216:219]\n\t" \
    "ds_read_b128 v[228:231], v252 offset:17408\n\t" \
    "v_mfma_f32_16x16x32_bf16 v[220:223], v[36:39], v[232:235], v[220:223]\n\t" \
    "v_mfma_f32_16x16x32_bf16 v[212:215], v[20:23], v[232:235], v[212:215]\n\t" \
    "s_waitcnt lgkmcnt(4)\n\t" \
    "v_mfma_f32_16x16x32_bf16 v[204:207], v[40:43], v[236:239], v[204:207]\n\t" \
    "v_xor_b32 v252, 0x1800, %[xa]\n\t" \
    "ds_read_b128 v[232:235], v252 offset:0\n\t" \
    "v_mfma_f32_16x16x32_bf16 v[208:211], v[48:51], v[236:239], v[208:211]\n\t" \
    "v_mfma_f32_16x16x32_bf16 v[200:203], v[244:247], v[236:239], v[200:203]\n\t" \
    "s_waitcnt lgkmcnt(3)\n\t" \
    "v_mfma_f32_16x16x32_bf16 v[216:219], v[40:43], v[240:243], v[216:219]\n\t" \
    "ds_read_b128 v[236:239], v252 offset:16384\n\t" \
    "v_mfma_f32_16x16x32_bf16 v[220:223], v[48:51], v[240:243], v[220:223]\n\t" \
    "v_mfma_f32_16x16x32_bf16 v[212:215], v[244:247], v[240:243], v[212:215]\n\t" \
    "s_waitcnt lgkmcnt(3)\n\t" \
    "v_mfma_f32_16x16x32_bf16 v[204:207], v[44:47], v[224:227], v[204:207]\n\t" \
    "ds_read_b128 v[240:243], v252 offset:1024\n\t" \
    "v_mfma_f32_16x16x32_bf16 v[208:211], v[52:55], v[224:227], v[208:211]\n\t" \
    "v_mfma_f32_16x16x32_bf16 v[200:203], v[248:251], v[224:227], v[200:203]\n\t" \
    "s_waitcnt lgkmcnt(3)\n\t" \
    "v_mfma_f32_16x16x32_bf16 v[216:219], v[44:47], v[228:231], v[216:219]\n\t" \
    "ds_read_b128 v[224:227], v252 offset:17408\n\t" \
    "v_mfma_f32_16x16x32_bf16 v[220:223], v[52:55], v[228:231], v[220:223]\n\t" \
    "v_mfma_f32_16x16x32_bf16 v[212:215], v[248:251], v[228:231], v[212:215]\n\t" \
    "s_waitcnt lgkmcnt(3)\n\t" \
    "v_mfma_f32_16x16x32_bf16 v[204:207], v[64:67], v[232:235], v[204:207]\n\t" \
    "v_and_b32 v252, 0x3ff, %[xa]\n\t" \
    "ds_read_b128 v[228:231], v252 offset:43008\n\t" \
    "v_mov_b64 v[244:245], 0\n\t" \
    "v_mfma_f32_16x16x32_bf16 v[208:211], v[72:75], v[232:235], v[208:211]\n\t" \
    "v_mov_b64 v[246:247], 0\n\t" \
    "v_mfma_f32_16x16x32_bf16 v[200:203], v[56:59], v[232:235], v[200:203]\n\t" \
    "v_mov_b64 v[248:249], 0\n\t" \
    "s_waitcnt lgkmcnt(3)\n\t" \
    "v_mfma_f32_16x16x32_bf16 v[216:219], v[64:67], v[236:239], v[216:219]\n\t" \
    "ds_read_b128 v[232:235], v252 offset:44032\n\t" \
    "v_mov_b64 v[250:251], 0\n\t" \
    "v_mfma_f32_16x16x32_bf16 v[220:223], v[72:75], v[236:239], v[220:223]\n\t" \
    "v_mfma_f32_16x16x32_bf16 v[212:215], v[56:59], v[236:239], v[212:215]\n\t" \
    "s_waitcnt lgkmcnt(3)\n\t" \
    "v_mfma_f32_16x16x32_bf16 v[204:207], v[68:71], v[240:243], v[204:207]\n\t" \
    "ds_read_b128 v[236:239], v252 offset:47104\n\t" \
    "v_mfma_f32_16x16x32_bf16 v[208:211], v[76:79], v[240:243], v[208:211]\n\t" \
    "v_mfma_f32_16x16x32_bf16 v[200:203], v[60:63], v[240:243], v[200:203]\n\t" \
    "s_waitcnt lgkmcnt(3)\n\t" \
    "v_mfma_f32_16x16x32_bf16 v[216:219], v[68:71], v[224:227], v[216:219]\n\t" \
    "ds_read_b128 v[240:243], v252 offset:48128\n\t" \
    "v_mfma_f32_16x16x32_bf16 v[220:223], v[76:79], v[224:227], v[220:223]\n\t" \
    "v_mfma_f32_16x16x32_bf16 v[212:215], v[60:63], v[224:227], v[212:215]\n\t" \
    "s_waitcnt lgkmcnt(3)\n\t" \
    "v_mfma_f32_16x16x32_bf16 v[204:207], v[184:187], v[228:231], v[204:207]\n\t" \
    "v_add_u32 v252, %[qs], v252\n\t" \
    "v_mfma_f32_16x16x32_bf16 v[208:211], v[192:195], v[228:231], v[208:211]\n\t" \
    "v_mov_b64 v[224:225], 0\n\t" \
    "v_mfma_f32_16x16x32_bf16 v[200:203], v[176:179], v[228:231], v[200:203]\n\t" \
    "v_mov_b64 v[226:227], 0\n\t" \
    "s_waitcnt lgkmcnt(2)\n\t" \
    "v_mfma_f32_16x16x32_bf16 v[204:207], v[188:191], v[232:235], v[204:207]\n\t" \
    "v_mfma_f32_16x16x32_bf16 v[208:211], v[196:199], v[232:235], v[208:211]\n\t" \
    "v_mfma_f32_16x16x32_bf16 v[200:203], v[180:183], v[232:235], v[200:203]\n\t" \
    "s_waitcnt lgkmcnt(1)\n\t" \
    "v_mfma_f32_16x16x32_bf16 v[216:219], v[184:187], v[236:239], v[216:219]\n\t" \
    "ds_read_b128 v[228:231], %[wl] offset:2048\n\t" \
    "ds_read_b128 v[232:235], %[wl] offset:3072\n\t" \
    "v_mfma_f32_16x16x32_bf16 v[220:223], v[192:195], v[236:239], v[220:223]\n\t" \
    "v_cvt_pk_bf16_f32 v204, v204, v205\n\t" \
    "v_cvt_pk_bf16_f32 v205, v206, v207\n\t" \
    "v_mfma_f32_16x16x32_bf16 v[212:215], v[176:179], v[236:239], v[212:215]\n\t" \
    "ds_write_b64 v252, v[204:205] offset:1024\n\t" \
    "v_cvt_pk_bf16_f32 v208, v208, v209\n\t" \
    "s_waitcnt lgkmcnt(3)\n\t" \
    "v_mfma_f32_16x16x32_bf16 v[216:219], v[188:191], v[240:243], v[216:219]\n\t" \
    "v_cvt_pk_bf16_f32 v209, v210, v211\n\t" \
    "ds_write_b64 v252, v[208:209] offset:2048\n\t" \
    "v_mfma_f32_16x16x32_bf16 v[220:223], v[196:199], v[240:243], v[220:223]\n\t" \
    "v_cvt_pk_bf16_f32 v200, v200, v201\n\t" \
    "v_cvt_pk_bf16_f32 v201, v202, v203\n\t" \
    "v_mfma_f32_16x16x32_bf16 v[212:215], v[180:183], v[240:243], v[212:215]\n\t" \
    "ds_write_b64 v252, v[200:201] offset:0\n\t" \
    "ds_read_b128 v[200:203], %[wl] offset:0\n\t" \
    "ds_read_b128 v[204:207], %[wl] offset:1024\n\t" \
    "v_cvt_pk_bf16_f32 v216, v216, v217\n\t" \
    "v_cvt_pk_bf16_f32 v217, v218, v219\n\t" \
    "ds_write_b64 v252, v[216:217] offset:7168\n\t" \
    "v_cvt_pk_bf16_f32 v220, v220, v221\n\t" \
    "v_cvt_pk_bf16_f32 v221, v222, v223\n\t" \
    "ds_write_b64 v252, v[220:221] offset:8192\n\t" \
    "v_cvt_pk_bf16_f32 v212, v212, v213\n\t" \
    "v_cvt_pk_bf16_f32 v213, v214, v215\n\t" \
    "ds_write_b64 v252, v[212:213] offset:6144\n\t" \
    "s_waitcnt lgkmcnt(0)\n\t"
#define ASM_XG_S_LO2 \
    "s_waitcnt lgkmcnt(0)\n\t" \
    "ds_read_b128 v[224:227], %[xa] offset:0\n\t" \
    "ds_read_b128 v[228:231], %[xa] offset:16384\n\t" \
    "ds_read_b128 v[232:235], %[xa] offset:1024\n\t" \
    "ds_read_b128 v[236:239], %[xa] offset:17408\n\t" \
    "s_waitcnt lgkmcnt(3)\n\t" \
    "v_mfma_f32_16x16x32_bf16 v[204:207], v[0:3], v[224:227], 0\n\t" \
    "v_xor_b32 v252, 0x800, %[xa]\n\t" \
    "ds_read_b128 v[240:243], v252 offset:0\n\t" \
    "v_mfma_f32_16x16x32_bf16 v[208:211], v[8:11], v[224:227], 0\n\t" \
    "s_waitcnt lgkmcnt(3)\n\t" \
    "v_mfma_f32_16x16x32_bf16 v[216:219], v[0:3], v[228:231], 0\n\t" \
    "ds_read_b128 v[224:227], v252 offset:16384\n\t" \
    "v_mfma_f32_16x16x32_bf16 v[220:223], v[8:11], v[228:231], 0\n\t" \
    "s_waitcnt lgkmcnt(3)\n\t" \
    "v_mfma_f32_16x16x32_bf16 v[204:207], v[4:7], v[232:235], v[204:207]\n\t" \
    "ds_read_b128 v[228:231], v252 offset:1024\n\t" \
    "v_mfma_f32_16x16x32_bf16 v[208:211], v[12:15], v[232:235], v[208:211]\n\t" \
    "s_waitcnt lgkmcnt(3)\n\t" \
    "v_mfma_f32_16x16x32_bf16 v[216:219], v[4:7], v[236:239], v[216:219]\n\t" \
    "ds_read_b128 v[232:235], v252 offset:17408\n\t" \
    "v_mfma_f32_16x16x32_bf16 v[220:223], v[12:15], v[236:239], v[220:223]\n\t" \
    "s_waitcnt lgkmcnt(3)\n\t" \
    "v_mfma_f32_16x16x32_bf16 v[204:207], v[24:27], v[240:243], v[204:207]\n\t" \
    "v_xor_b32 v252, 0x1000, %[xa]\n\t" \
    "ds_read_b128 v[236:239], v252 offset:0\n\t" \
    "v_mfma_f32_16x16x32_bf16 v[208:211], v[32:35], v[240:243], v[208:211]\n\t" \
    "s_waitcnt lgkmcnt(3)\n\t" \
    "v_mfma_f32_16x16x32_bf16 v[216:219], v[24:27], v[224:227], v[216:219]\n\t" \
    "ds_read_b128 v[240:243], v252 offset:16384\n\t" \
    "v_mfma_f32_16x16x32_bf16 v[220:223], v[32:35], v[224:227], v[220:223]\n\t" \
    "s_waitcnt lgkmcnt(3)\n\t" \
    "v_mfma_f32_16x16x32_bf16 v[204:207], v[28:31], v[228:231], v[204:207]\n\t" \
    "ds_read_b128 v[224:227], v252 offset:1024\n\t" \
    "v_mfma_f32_16x16x32_bf16 v[208:211], v[36:39], v[228:231], v[208:211]\n\t" \
    "s_waitcnt lgkmcnt(3)\n\t" \
    "v_mfma_f32_16x16x32_bf16 v[216:219], v[28:31], v[232:235], v[216:219]\n\t" \
    "ds_read_b128 v[228:231], v252 offset:17408\n\t" \
    "v_mfma_f32_16x16x32_bf16 v[220:223], v[36:39], v[232:235], v[220:223]\n\t" \
    "s_waitcnt lgkmcnt(3)\n\t" \
    "v_mfma_f32_16x16x32_bf16 v[204:207], v[40:43], v[236:239], v[204:207]\n\t" \
    "v_xor_b32 v252, 0x1800, %[xa]\n\t" \
    "ds_read_b128 v[232:235], v252 offset:0\n\t" \
    "v_mfma_f32_16x16x32_bf16 v[208:211], v[48:51], v[236:239], v[208:211]\n\t" \
    "s_waitcnt lgkmcnt(3)\n\t" \
    "v_mfma_f32_16x16x32_bf16 v[216:219], v[40:43], v[240:243], v[216:219]\n\t" \
    "ds_read_b128 v[236:239], v252 offset:16384\n\t" \
    "v_mfma_f32_16x16x32_bf16 v[220:223], v[48:51], v[240:243], v[220:223]\n\t" \
    "s_waitcnt lgkmcnt(3)\n\t" \
    "v_mfma_f32_16x16x32_bf16 v[204:207], v[44:47], v[224:227], v[204:207]\n\t" \
    "ds_read_b128 v[240:243], v252 offset:1024\n\t" \
    "v_mfma_f32_16x16x32_bf16 v[208:211], v[52:55], v[224:227], v[208:211]\n\t" \
    "s_waitcnt lgkmcnt(3)\n\t" \
    "v_mfma_f32_16x16x32_bf16 v[216:219], v[44:47], v[228:231], v[216:219]\n\t" \
    "ds_read_b128 v[224:227], v252 offset:17408\n\t" \
    "v_mfma_f32_16x16x32_bf16 v[220:223], v[52:55], v[228:231], v[220:223]\n\t" \
    "s_waitcnt lgkmcnt(3)\n\t" \
    "v_mfma_f32_16x16x32_bf16 v[204:207], v[64:67], v[232:235], v[204:207]\n\t" \
    "v_and_b32 v252, 0x3ff, %[xa]\n\t" \
    "ds_read_b128 v[228:231], v252 offset:40960\n\t" \
    "v_mfma_f32_16x16x32_bf16 v[208:211], v[72:75], v[232:235], v[208:211]\n\t" \
    "s_waitcnt lgkmcnt(3)\n\t" \
    "v_mfma_f32_16x16x32_bf16 v[216:219], v[64:67], v[236:239], v[216:219]\n\t" \
    "ds_read_b128 v[232:235], v252 offset:45056\n\t" \
    "v_mfma_f32_16x16x32_bf16 v[220:223], v[72:75], v[236:239], v[220:223]\n\t" \
    "s_waitcnt lgkmcnt(3)\n\t" \
    "v_mfma_f32_16x16x32_bf16 v[204:207], v[68:71], v[240:243], v[204:207]\n\t" \
    "ds_read_b128 v[236:239], v252 offset:41984\n\t" \
    "v_mfma_f32_16x16x32_bf16 v[208:211], v[76:79], v[240:243], v[208:211]\n\t" \
    "s_waitcnt lgkmcnt(3)\n\t" \
    "v_mfma_f32_16x16x32_bf16 v[216:219], v[68:71], v[224:227], v[216:219]\n\t" \
    "ds_read_b128 v[240:243], v252 offset:46080\n\t" \
    "v_mfma_f32_16x16x32_bf16 v[220:223], v[76:79], v[224:227], v[220:223]\n\t" \
    "s_waitcnt lgkmcnt(0)\n\t" \
    "s_barrier\n\t" ASM_ST \
    "v_mfma_f32_16x16x32_bf16 v[204:207], v[160:163], v[228:231], v[204:207]\n\t" \
    "v_xor_b32 v252, 0x2000, %[xa]\n\t" \
    "ds_read_b128 v[224:227], v252 offset:0\n\t" \
    "v_mfma_f32_16x16x32_bf16 v[208:211], v[168:171], v[228:231], v[208:211]\n\t" \
    "v_mfma_f32_16x16x32_bf16 v[216:219], v[160:163], v[232:235], v[216:219]\n\t" \
    "ds_read_b128 v[228:231], v252 offset:16384\n\t" \
    "v_mfma_f32_16x16x32_bf16 v[220:223], v[168:171], v[232:235], v[220:223]\n\t" \
    "v_mfma_f32_16x16x32_bf16 v[204:207], v[164:167], v[236:239], v[204:207]\n\t" \
    "ds_read_b128 v[232:235], v252 offset:1024\n\t" \
    "v_mfma_f32_16x16x32_bf16 v[208:211], v[172:175], v[236:239], v[208:211]\n\t" \
    "v_mfma_f32_16x16x32_bf16 v[216:219], v[164:167], v[240:243], v[216:219]\n\t" \
    "ds_read_b128 v[236:239], v252 offset:17408\n\t" \
    "v_mfma_f32_16x16x32_bf16 v[220:223], v[172:175], v[240:243], v[220:223]\n\t" \
    "s_waitcnt lgkmcnt(3)\n\t" \
    "v_mfma_f32_16x16x32_bf16 v[204:207], v[80:83], v[224:227], v[204:207]\n\t" \
    "v_xor_b32 v252, 0x2800, %[xa]\n\t" \
    "ds_read_b128 v[240:243], v252 offset:0\n\t" \
    "v_mfma_f32_16x16x32_bf16 v[208:211], v[88:91], v[224:227], v[208:211]\n\t" \
    "s_waitcnt lgkmcnt(3)\n\t" \
    "v_mfma_f32_16x16x32_bf16 v[216:219], v[80:83], v[228:231], v[216:219]\n\t" \
    "ds_read_b128 v[224:227], v252 offset:16384\n\t" \
    "v_mfma_f32_16x16x32_bf16 v[220:223], v[88:91], v[228:231], v[220:223]\n\t" \
    "s_waitcnt lgkmcnt(3)\n\t" \
    "v_mfma_f32_16x16x32_bf16 v[204:207], v[84:87], v[232:235], v[204:207]\n\t" \
    "ds_read_b128 v[228:231], v252 offset:1024\n\t" \
    "v_mfma_f32_16x16x32_bf16 v[208:211], v[92:95], v[232:235], v[208:211]\n\t" \
    "s_waitcnt lgkmcnt(3)\n\t" \
    "v_mfma_f32_16x16x32_bf16 v[216:219], v[84:87], v[236:239], v[216:219]\n\t" \
    "ds_read_b128 v[232:235], v252 offset:17408\n\t" \
    "v_mfma_f32_16x16x32_bf16 v[220:223], v[92:95], v[236:239], v[220:223]\n\t" \
    "s_waitcnt lgkmcnt(3)\n\t" \
    "v_mfma_f32_16x16x32_bf16 v[204:207], v[104:107], v[240:243], v[204:207]\n\t" \
    "v_xor_b32 v252, 0x3000, %[xa]\n\t" \
    "ds_read_b128 v[236:239], v252 offset:0\n\t" \
    "v_mfma_f32_16x16x32_bf16 v[208:211], v[112:115], v[240:243], v[208:211]\n\t" \
    "s_waitcnt lgkmcnt(3)\n\t" \
    "v_mfma_f32_16x16x32_bf16 v[216:219], v[104:107], v[224:227], v[216:219]\n\t" \
    "ds_read_b128 v[240:243], v252 offset:16384\n\t" \
    "v_mfma_f32_16x16x32_bf16 v[220:223], v[112:115], v[224:227], v[220:223]\n\t" \
    "s_waitcnt lgkmcnt(3)\n\t" \
    "v_mfma_f32_16x16x32_bf16 v[204:207], v[108:111], v[228:231], v[204:207]\n\t" \
    "ds_read_b128 v[224:227], v252 offset:1024\n\t" \
    "v_mfma_f32_16x16x32_bf16 v[208:211], v[116:119], v[228:231], v[208:211]\n\t" \
    "s_waitcnt lgkmcnt(3)\n\t" \
    "v_mfma_f32_16x16x32_bf16 v[216:219], v[108:111], v[232:235], v[216:219]\n\t" \
    "ds_read_b128 v[228:231], v252 offset:17408\n\t" \
    "v_mfma_f32_16x16x32_bf16 v[220:223], v[116:119], v[232:235], v[220:223]\n\t" \
    "s_waitcnt lgkmcnt(3)\n\t" \
    "v_mfma_f32_16x16x32_bf16 v[204:207], v[120:123], v[236:239], v[204:207]\n\t" \
    "v_xor_b32 v252, 0x3800, %[xa]\n\t" \
    "ds_read_b128 v[232:235], v252 offset:0\n\t" \
    "v_mfma_f32_16x16x32_bf16 v[208:211], v[128:131], v[236:239], v[208:211]\n\t" \
    "s_waitcnt lgkmcnt(3)\n\t" \
    "v_mfma_f32_16x16x32_bf16 v[216:219], v[120:123], v[240:243], v[216:219]\n\t" \
    "ds_read_b128 v[236:239], v252 offset:16384\n\t" \
    "v_mfma_f32_16x16x32_bf16 v[220:223], v[128:131], v[240:243], v[220:223]\n\t" \
    "s_waitcnt lgkmcnt(3)\n\t" \
    "v_mfma_f32_16x16x32_bf16 v[204:207], v[124:127], v[224:227], v[204:207]\n\t" \
    "ds_read_b128 v[240:243], v252 offset:1024\n\t" \
    "v_mfma_f32_16x16x32_bf16 v[208:211], v[132:135], v[224:227], v[208:211]\n\t" \
    "s_waitcnt lgkmcnt(3)\n\t" \
    "v_mfma_f32_16x16x32_bf16 v[216:219], v[124:127], v[228:231], v[216:219]\n\t" \
    "ds_read_b128 v[224:227], v252 offset:17408\n\t" \
    "v_mfma_f32_16x16x32_bf16 v[220:223], v[132:135], v[228:231], v[220:223]\n\t" \
    "s_waitcnt lgkmcnt(3)\n\t" \
    "v_mfma_f32_16x16x32_bf16 v[204:207], v[144:147], v[232:235], v[204:207]\n\t" \
    "v_and_b32 v252, 0x3ff, %[xa]\n\t" \
    "ds_read_b128 v[228:231], v252 offset:43008\n\t" \
    "v_mov_b64 v[244:245], 0\n\t" \
    "v_mfma_f32_16x16x32_bf16 v[208:211], v[152:155], v[232:235], v[208:211]\n\t" \
    "v_mov_b64 v[246:247], 0\n\t" \
    "s_waitcnt lgkmcnt(3)\n\t" \
    "v_mfma_f32_16x16x32_bf16 v[216:219], v[144:147], v[236:239], v[216:219]\n\t" \
    "ds_read_b128 v[232:235], v252 offset:44032\n\t" \
    "v_mov_b64 v[248:249], 0\n\t" \
    "v_mfma_f32_16x16x32_bf16 v[220:223], v[152:155], v[236:239], v[220:223]\n\t" \
    "v_mov_b64 v[250:251], 0\n\t" \
    "s_waitcnt lgkmcnt(3)\n\t" \
    "v_mfma_f32_16x16x32_bf16 v[204:207], v[148:151], v[240:243], v[204:207]\n\t" \
    "ds_read_b128 v[236:239], v252 offset:47104\n\t" \
    "v_mfma_f32_16x16x32_bf16 v[208:211], v[156:159], v[240:243], v[208:211]\n\t" \
    "s_waitcnt lgkmcnt(3)\n\t" \
    "v_mfma_f32_16x16x32_bf16 v[216:219], v[148:151], v[224:227], v[216:219]\n\t" \
    "ds_read_b128 v[240:243], v252 offset:48128\n\t" \
    "v_mfma_f32_16x16x32_bf16 v[220:223], v[156:159], v[224:227], v[220:223]\n\t" \
    "s_waitcnt lgkmcnt(3)\n\t" \
    "v_mfma_f32_16x16x32_bf16 v[204:207], v[184:187], v[228:231], v[204:207]\n\t" \
    "v_add_u32 v252, %[qs], v252\n\t" \
    "v_mfma_f32_16x16x32_bf16 v[208:211], v[192:195], v[228:231], v[208:211]\n\t" \
    "v_mov_b64 v[224:225], 0\n\t" \
    "s_waitcnt lgkmcnt(2)\n\t" \
    "v_mfma_f32_16x16x32_bf16 v[204:207], v[188:191], v[232:235], v[204:207]\n\t" \
    "v_mov_b64 v[226:227], 0\n\t" \
    "v_mfma_f32_16x16x32_bf16 v[208:211], v[196:199], v[232:235], v[208:211]\n\t" \
    "s_waitcnt lgkmcnt(1)\n\t" \
    "v_mfma_f32_16x16x32_bf16 v[216:219], v[184:187], v[236:239], v[216:219]\n\t" \
    "ds_read_b128 v[228:231], %[wl] offset:2048\n\t" \
    "ds_read_b128 v[232:235], %[wl] offset:3072\n\t" \
    "v_mfma_f32_16x16x32_bf16 v[220:223], v[192:195], v[236:239], v[220:223]\n\t" \
    "v_cvt_pk_bf16_f32 v204, v204, v205\n\t" \
    "v_cvt_pk_bf16_f32 v205, v206, v207\n\t" \
    "s_waitcnt lgkmcnt(2)\n\t" \
    "v_mfma_f32_16x16x32_bf16 v[216:219], v[188:191], v[240:243], v[216:219]\n\t" \
    "ds_write_b64 v252, v[204:205] offset:1024\n\t" \
    "v_cvt_pk_bf16_f32 v208, v208, v209\n\t" \
    "v_mfma_f32_16x16x32_bf16 v[220:223], v[196:199], v[240:243], v[220:223]\n\t" \
    "v_cvt_pk_bf16_f32 v209, v210, v211\n\t" \
    "ds_write_b64 v252, v[208:209] offset:2048\n\t" \
    "ds_read_b128 v[200:203], %[wl] offset:0\n\t" \
    "ds_read_b128 v[204:207], %[wl] offset:1024\n\t" \
    "v_cvt_pk_bf16_f32 v216, v216, v217\n\t" \
    "v_cvt_pk_bf16_f32 v217, v218, v219\n\t" \
    "ds_write_b64 v252, v[216:217] offset:7168\n\t" \
    "s_nop 0\n\t" \
    "v_cvt_pk_bf16_f32 v220, v220, v221\n\t" \
    "v_cvt_pk_bf16_f32 v221, v222, v223\n\t" \
    "ds_write_b64 v252, v[220:221] offset:8192\n\t" \
    "s_waitcnt lgkmcnt(0)\n\t"
#define ASM_XG_S_LO1 \
    "s_waitcnt lgkmcnt(0)\n\t" \
    "ds_read_b128 v[224:227], %[xa] offset:0\n\t" \
    "ds_read_b128 v[228:231], %[xa] offset:16384\n\t" \
    "ds_read_b128 v[232:235], %[xa] offset:1024\n\t" \
    "ds_read_b128 v[236:239], %[xa] offset:17408\n\t" \
    "s_waitcnt lgkmcnt(3)\n\t" \
    "v_mfma_f32_16x16x32_bf16 v[208:211], v[8:11], v[224:227], 0\n\t" \
    "v_xor_b32 v252, 0x800, %[xa]\n\t" \
    "ds_read_b128 v[240:243], v252 offset:0\n\t" \
    "s_waitcnt lgkmcnt(3)\n\t" \
    "v_mfma_f32_16x16x32_bf16 v[220:223], v[8:11], v[228:231], 0\n\t" \
    "ds_read_b128 v[224:227], v252 offset:16384\n\t" \
    "s_waitcnt lgkmcnt(3)\n\t" \
    "v_mfma_f32_16x16x32_bf16 v[208:211], v[12:15], v[232:235], v[208:211]\n\t" \
    "ds_read_b128 v[228:231], v252 offset:1024\n\t" \
    "s_waitcnt lgkmcnt(3)\n\t" \
    "v_mfma_f32_16x16x32_bf16 v[220:223], v[12:15], v[236:239], v[220:223]\n\t" \
    "ds_read_b128 v[232:235], v252 offset:17408\n\t" \
    "s_waitcnt lgkmcnt(3)\n\t" \
    "v_mfma_f32_16x16x32_bf16 v[208:211], v[32:35], v[240:243], v[208:211]\n\t" \
    "v_xor_b32 v252, 0x1000, %[xa]\n\t" \
    "ds_read_b128 v[236:239], v252 offset:0\n\t" \
    "s_waitcnt lgkmcnt(3)\n\t" \
    "v_mfma_f32_16x16x32_bf16 v[220:223], v[32:35], v[224:227], v[220:223]\n\t" \
    "ds_read_b128 v[240:243], v252 offset:16384\n\t" \
    "s_waitcnt lgkmcnt(3)\n\t" \
    "v_mfma_f32_16x16x32_bf16 v[208:211], v[36:39], v[228:231], v[208:211]\n\t" \
    "ds_read_b128 v[224:227], v252 offset:1024\n\t" \
    "s_waitcnt lgkmcnt(3)\n\t" \
    "v_mfma_f32_16x16x32_bf16 v[220:223], v[36:39], v[232:235], v[220:223]\n\t" \
    "ds_read_b128 v[228:231], v252 offset:17408\n\t" \
    "s_waitcnt lgkmcnt(3)\n\t" \
    "v_mfma_f32_16x16x32_bf16 v[208:211], v[48:51], v[236:239], v[208:211]\n\t" \
    "v_xor_b32 v252, 0x1800, %[xa]\n\t" \
    "ds_read_b128 v[232:235], v252 offset:0\n\t" \
    "s_waitcnt lgkmcnt(3)\n\t" \
    "v_mfma_f32_16x16x32_bf16 v[220:223], v[48:51], v[240:243], v[220:223]\n\t" \
    "ds_read_b128 v[236:239], v252 offset:16384\n\t" \
    "s_waitcnt lgkmcnt(3)\n\t" \
    "v_mfma_f32_16x16x32_bf16 v[208:211], v[52:55], v[224:227], v[208:211]\n\t" \
    "ds_read_b128 v[240:243], v252 offset:1024\n\t" \
    "s_waitcnt lgkmcnt(3)\n\t" \
    "v_mfma_f32_16x16x32_bf16 v[220:223], v[52:55], v[228:231], v[220:223]\n\t" \
    "ds_read_b128 v[224:227], v252 offset:17408\n\t" \
    "s_waitcnt lgkmcnt(3)\n\t" \
    "v_mfma_f32_16x16x32_bf16 v[208:211], v[72:75], v[232:235], v[208:211]\n\t" \
    "v_and_b32 v252, 0x3ff, %[xa]\n\t" \
    "ds_read_b128 v[228:231], v252 offset:40960\n\t" \
    "s_waitcnt lgkmcnt(3)\n\t" \
    "v_mfma_f32_16x16x32_bf16 v[220:223], v[72:75], v[236:239], v[220:223]\n\t" \
    "ds_read_b128 v[232:235], v252 offset:45056\n\t" \
    "s_waitcnt lgkmcnt(3)\n\t" \
    "v_mfma_f32_16x16x32_bf16 v[208:211], v[76:79], v[240:243], v[208:211]\n\t" \
    "ds_read_b128 v[236:239], v252 offset:41984\n\t" \
    "s_waitcnt lgkmcnt(3)\n\t" \
    "v_mfma_f32_16x16x32_bf16 v[220:223], v[76:79], v[224:227], v[220:223]\n\t" \
    "ds_read_b128 v[240:243], v252 offset:46080\n\t" \
    "s_waitcnt lgkmcnt(0)\n\t" \
    "s_barrier\n\t" ASM_ST \
    "v_mfma_f32_16x16x32_bf16 v[208:211], v[168:171], v[228:231], v[208:211]\n\t" \
    "v_xor_b32 v252, 0x2000, %[xa]\n\t" \
    "ds_read_b128 v[224:227], v252 offset:0\n\t" \
    "v_mfma_f32_16x16x32_bf16 v[220:223], v[168:171], v[232:235], v[220:223]\n\t" \
    "ds_read_b128 v[228:231], v252 offset:16384\n\t" \
    "v_mfma_f32_16x16x32_bf16 v[208:211], v[172:175], v[236:239], v[208:211]\n\t" \
    "ds_read_b128 v[232:235], v252 offset:1024\n\t" \
    "v_mfma_f32_16x16x32_bf16 v[220:223], v[172:175], v[240:243], v[220:223]\n\t" \
    "ds_read_b128 v[236:239], v252 offset:17408\n\t" \
    "s_waitcnt lgkmcnt(3)\n\t" \
    "v_mfma_f32_16x16x32_bf16 v[208:211], v[88:91], v[224:227], v[208:211]\n\t" \
    "v_xor_b32 v252, 0x2800, %[xa]\n\t" \
    "ds_read_b128 v[240:243], v252 offset:0\n\t" \
    "s_waitcnt lgkmcnt(3)\n\t" \
    "v_mfma_f32_16x16x32_bf16 v[220:223], v[88:91], v[228:231], v[220:223]\n\t" \
    "ds_read_b128 v[224:227], v252 offset:16384\n\t" \
    "s_waitcnt lgkmcnt(3)\n\t" \
    "v_mfma_f32_16x16x32_bf16 v[208:211], v[92:95], v[232:235], v[208:211]\n\t" \
    "ds_read_b128 v[228:231], v252 offset:1024\n\t" \
    "s_waitcnt lgkmcnt(3)\n\t" \
    "v_mfma_f32_16x16x32_bf16 v[220:223], v[92:95], v[236:239], v[220:223]\n\t" \
    "ds_read_b128 v[232:235], v252 offset:17408\n\t" \
    "s_waitcnt lgkmcnt(3)\n\t" \
    "v_mfma_f32_16x16x32_bf16 v[208:211], v[112:115], v[240:243], v[208:211]\n\t" \
    "v_xor_b32 v252, 0x3000, %[xa]\n\t" \
    "ds_read_b128 v[236:239], v252 offset:0\n\t" \
    "s_waitcnt lgkmcnt(3)\n\t" \
    "v_mfma_f32_16x16x32_bf16 v[220:223], v[112:115], v[224:227], v[220:223]\n\t" \
    "ds_read_b128 v[240:243], v252 offset:16384\n\t" \
    "s_waitcnt lgkmcnt(3)\n\t" \
    "v_mfma_f32_16x16x32_bf16 v[208:211], v[116:119], v[228:231], v[208:211]\n\t" \
    "ds_read_b128 v[224:227], v252 offset:1024\n\t" \
    "s_waitcnt lgkmcnt(3)\n\t" \
    "v_mfma_f32_16x16x32_bf16 v[220:223], v[116:119], v[232:235], v[220:223]\n\t" \
    "ds_read_b128 v[228:231], v252 offset:17408\n\t" \
    "s_waitcnt lgkmcnt(3)\n\t" \
    "v_mfma_f32_16x16x32_bf16 v[208:211], v[128:131], v[236:239], v[208:211]\n\t" \
    "v_xor_b32 v252, 0x3800, %[xa]\n\t" \
    "ds_read_b128 v[232:235], v252 offset:0\n\t" \
    "s_waitcnt lgkmcnt(3)\n\t" \
    "v_mfma_f32_16x16x32_bf16 v[220:223], v[128:131], v[240:243], v[220:223]\n\t" \
    "ds_read_b128 v[236:239], v252 offset:16384\n\t" \
    "s_waitcnt lgkmcnt(3)\n\t" \
    "v_mfma_f32_16x16x32_bf16 v[208:211], v[132:135], v[224:227], v[208:211]\n\t" \
    "ds_read_b128 v[240:243], v252 offset:1024\n\t" \
    "s_waitcnt lgkmcnt(3)\n\t" \
    "v_mfma_f32_16x16x32_bf16 v[220:223], v[132:135], v[228:231], v[220:223]\n\t" \
    "ds_read_b128 v[224:227], v252 offset:17408\n\t" \
    "s_waitcnt lgkmcnt(3)\n\t" \
    "v_mfma_f32_16x16x32_bf16 v[208:211], v[152:155], v[232:235], v[208:211]\n\t" \
    "v_and_b32 v252, 0x3ff, %[xa]\n\t" \
    "ds_read_b128 v[228:231], v252 offset:43008\n\t" \
    "v_mov_b64 v[244:245], 0\n\t" \
    "s_waitcnt lgkmcnt(3)\n\t" \
    "v_mfma_f32_16x16x32_bf16 v[220:223], v[152:155], v[236:239], v[220:223]\n\t" \
    "ds_read_b128 v[232:235], v252 offset:44032\n\t" \
    "v_mov_b64 v[246:247], 0\n\t" \
    "s_waitcnt lgkmcnt(3)\n\t" \
    "v_mfma_f32_16x16x32_bf16 v[208:211], v[156:159], v[240:243], v[208:211]\n\t" \
    "ds_read_b128 v[236:239], v252 offset:47104\n\t" \
    "v_mov_b64 v[248:249], 0\n\t" \
    "s_waitcnt lgkmcnt(3)\n\t" \
    "v_mfma_f32_16x16x32_bf16 v[220:223], v[156:159], v[224:227], v[220:223]\n\t" \
    "ds_read_b128 v[240:243], v252 offset:48128\n\t" \
    "v_mov_b64 v[250:251], 0\n\t" \
    "s_waitcnt lgkmcnt(3)\n\t" \
    "v_mfma_f32_16x16x32_bf16 v[208:211], v[192:195], v[228:231], v[208:211]\n\t" \
    "v_add_u32 v252, %[qs], v252\n\t" \
    "s_waitcnt lgkmcnt(2)\n\t" \
    "v_mfma_f32_16x16x32_bf16 v[208:211], v[196:199], v[232:235], v[208:211]\n\t" \
    "v_mov_b64 v[224:225], 0\n\t" \
    "s_waitcnt lgkmcnt(1)\n\t" \
    "v_mfma_f32_16x16x32_bf16 v[220:223], v[192:195], v[236:239], v[220:223]\n\t" \
    "v_mov_b64 v[226:227], 0\n\t" \
    "ds_read_b128 v[228:231], %[wl] offset:2048\n\t" \
    "s_waitcnt lgkmcnt(1)\n\t" \
    "v_mfma_f32_16x16x32_bf16 v[220:223], v[196:199], v[240:243], v[220:223]\n\t" \
    "ds_read_b128 v[232:235], %[wl] offset:3072\n\t" \
    "v_cvt_pk_bf16_f32 v208, v208, v209\n\t" \
    "v_cvt_pk_bf16_f32 v209, v210, v211\n\t" \
    "ds_write_b64 v252, v[208:209] offset:2048\n\t" \
    "ds_read_b128 v[200:203], %[wl] offset:0\n\t" \
    "ds_read_b128 v[204:207], %[wl] offset:1024\n\t" \
    "s_nop 1\n\t" \
    "v_cvt_pk_bf16_f32 v220, v220, v221\n\t" \
    "v_cvt_pk_bf16_f32 v221, v222, v223\n\t" \
    "ds_write_b64 v252, v[220:221] offset:8192\n\t" \
    "s_waitcnt lgkmcnt(0)\n\t"
#define ASM_XG_V \
    "s_waitcnt lgkmcnt(0)\n\t" \
    "ds_read_b128 v[224:227], %[xa] offset:0\n\t" \
    "ds_read_b128 v[244:247], %[wl] offset:0\n\t" \
    "ds_read_b128 v[228:231], %[xa] offset:16384\n\t" \
    "ds_read_b128 v[232:235], %[xa] offset:1024\n\t" \
    "ds_read_b128 v[236:239], %[xa] offset:17408\n\t" \
    "ds_read_b128 v[248:251], %[wl] offset:1024\n\t" \
    "s_waitcnt lgkmcnt(5)\n\t" \
    "v_mfma_f32_16x16x32_bf16 v[204:207], v[0:3], v[224:227], 0\n\t" \
    "v_xor_b32 v252, 0x800, %[xa]\n\t" \
    "ds_read_b128 v[240:243], v252 offset:0\n\t" \
    "v_mfma_f32_16x16x32_bf16 v[208:211], v[8:11], v[224:227], 0\n\t" \
    "s_waitcnt lgkmcnt(5)\n\t" \
    "v_mfma_f32_16x16x32_bf16 v[200:203], v[244:247], v[224:227], 0\n\t" \
    "s_waitcnt lgkmcnt(4)\n\t" \
    "v_mfma_f32_16x16x32_bf16 v[216:219], v[0:3], v[228:231], 0\n\t" \
    "ds_read_b128 v[224:227], v252 offset:16384\n\t" \
    "v_mfma_f32_16x16x32_bf16 v[220:223], v[8:11], v[228:231], 0\n\t" \
    "v_mfma_f32_16x16x32_bf16 v[212:215], v[244:247], v[228:231], 0\n\t" \
    "s_waitcnt lgkmcnt(4)\n\t" \
    "v_mfma_f32_16x16x32_bf16 v[204:207], v[4:7], v[232:235], v[204:207]\n\t" \
    "ds_read_b128 v[228:231], v252 offset:1024\n\t" \
    "v_mfma_f32_16x16x32_bf16 v[208:211], v[12:15], v[232:235], v[208:211]\n\t" \
    "ds_read_b128 v[244:247], %[wl] offset:2048\n\t" \
    "s_waitcnt lgkmcnt(4)\n\t" \
    "v_mfma_f32_16x16x32_bf16 v[200:203], v[248:251], v[232:235], v[200:203]\n\t" \
    "v_mfma_f32_16x16x32_bf16 v[216:219], v[4:7], v[236:239], v[216:219]\n\t" \
    "ds_read_b128 v[232:235], v252 offset:17408\n\t" \
    "v_mfma_f32_16x16x32_bf16 v[220:223], v[12:15], v[236:239], v[220:223]\n\t" \
    "v_mfma_f32_16x16x32_bf16 v[212:215], v[248:251], v[236:239], v[212:215]\n\t" \
    "s_waitcnt lgkmcnt(4)\n\t" \
    "v_mfma_f32_16x16x32_bf16 v[204:207], v[24:27], v[240:243], v[204:207]\n\t" \
    "v_xor_b32 v252, 0x1000, %[xa]\n\t" \
    "ds_read_b128 v[236:239], v252 offset:0\n\t" \
    "v_mfma_f32_16x16x32_bf16 v[208:211], v[32:35], v[240:243], v[208:211]\n\t" \
    "ds_read_b128 v[248:251], %[wl] offset:3072\n\t" \
    "v_mfma_f32_16x16x32_bf16 v[200:203], v[16:19], v[240:243], v[200:203]\n\t" \
    "s_waitcnt lgkmcnt(5)\n\t" \
    "v_mfma_f32_16x16x32_bf16 v[216:219], v[24:27], v[224:227], v[216:219]\n\t" \
    "ds_read_b128 v[240:243], v252 offset:16384\n\t" \
    "v_mfma_f32_16x16x32_bf16 v[220:223], v[32:35], v[224:227], v[220:223]\n\t" \
    "v_mfma_f32_16x16x32_bf16 v[212:215], v[16:19], v[224:227], v[212:215]\n\t" \
    "s_waitcnt lgkmcnt(5)\n\t" \
    "v_mfma_f32_16x16x32_bf16 v[204:207], v[28:31], v[228:231], v[204:207]\n\t" \
    "ds_read_b128 v[224:227], v252 offset:1024\n\t" \
    "v_mfma_f32_16x16x32_bf16 v[208:211], v[36:39], v[228:231], v[208:211]\n\t" \
    "v_mfma_f32_16x16x32_bf16 v[200:203], v[20:23], v[228:231], v[200:203]\n\t" \
    "s_waitcnt lgkmcnt(4)\n\t" \
    "v_mfma_f32_16x16x32_bf16 v[216:219], v[28:31], v[232:235], v[216:219]\n\t" \
    "ds_read_b128 v[228:231], v252 offset:17408\n\t" \
    "v_mfma_f32_16x16x32_bf16 v[220:223], v[36:39], v[232:235], v[220:223]\n\t" \
    "v_mfma_f32_16x16x32_bf16 v[212:215], v[20:23], v[232:235], v[212:215]\n\t" \
    "s_waitcnt lgkmcnt(4)\n\t" \
    "v_mfma_f32_16x16x32_bf16 v[204:207], v[40:43], v[236:239], v[204:207]\n\t" \
    "v_xor_b32 v252, 0x1800, %[xa]\n\t" \
    "ds_read_b128 v[232:235], v252 offset:0\n\t" \
    "v_mfma_f32_16x16x32_bf16 v[208:211], v[48:51], v[236:239], v[208:211]\n\t" \
    "v_mfma_f32_16x16x32_bf16 v[200:203], v[244:247], v[236:239], v[200:203]\n\t" \
    "s_waitcnt lgkmcnt(3)\n\t" \
    "v_mfma_f32_16x16x32_bf16 v[216:219], v[40:43], v[240:243], v[216:219]\n\t" \
    "ds_read_b128 v[236:239], v252 offset:16384\n\t" \
    "v_mfma_f32_16x16x32_bf16 v[220:223], v[48:51], v[240:243], v[220:223]\n\t" \
    "v_mfma_f32_16x16x32_bf16 v[212:215], v[244:247], v[240:243], v[212:215]\n\t" \
    "s_waitcnt lgkmcnt(3)\n\t" \
    "v_mfma_f32_16x16x32_bf16 v[204:207], v[44:47], v[224:227], v[204:207]\n\t" \
    "ds_read_b128 v[240:243], v252 offset:1024\n\t" \
    "v_mfma_f32_16x16x32_bf16 v[208:211], v[52:55], v[224:227], v[208:211]\n\t" \
    "ds_read_b128 v[244:247], %[wl] offset:8192\n\t" \
    "v_mfma_f32_16x16x32_bf16 v[200:203], v[248:251], v[224:227], v[200:203]\n\t" \
    "s_waitcnt lgkmcnt(4)\n\t" \
    "v_mfma_f32_16x16x32_bf16 v[216:219], v[44:47], v[228:231], v[216:219]\n\t" \
    "ds_read_b128 v[224:227], v252 offset:17408\n\t" \
    "v_mfma_f32_16x16x32_bf16 v[220:223], v[52:55], v[228:231], v[220:223]\n\t" \
    "v_mfma_f32_16x16x32_bf16 v[212:215], v[248:251], v[228:231], v[212:215]\n\t" \
    "s_waitcnt lgkmcnt(4)\n\t" \
    "v_mfma_f32_16x16x32_bf16 v[204:207], v[64:67], v[232:235], v[204:207]\n\t" \
    "v_and_b32 v252, 0x3ff, %[xa]\n\t" \
    "ds_read_b128 v[228:231], v252 offset:40960\n\t" \
    "v_mfma_f32_16x16x32_bf16 v[208:211], v[72:75], v[232:235], v[208:211]\n\t" \
    "ds_read_b128 v[248:251], %[wl] offset:9216\n\t" \
    "v_mfma_f32_16x16x32_bf16 v[200:203], v[56:59], v[232:235], v[200:203]\n\t" \
    "s_waitcnt lgkmcnt(5)\n\t" \
    "v_mfma_f32_16x16x32_bf16 v[216:219], v[64:67], v[236:239], v[216:219]\n\t" \
    "ds_read_b128 v[232:235], v252 offset:45056\n\t" \
    "v_mfma_f32_16x16x32_bf16 v[220:223], v[72:75], v[236:239], v[220:223]\n\t" \
    "v_mfma_f32_16x16x32_bf16 v[212:215], v[56:59], v[236:239], v[212:215]\n\t" \
    "s_waitcnt lgkmcnt(5)\n\t" \
    "v_mfma_f32_16x16x32_bf16 v[204:207], v[68:71], v[240:243], v[204:207]\n\t" \
    "ds_read_b128 v[236:239], v252 offset:41984\n\t" \
    "v_mfma_f32_16x16x32_bf16 v[208:211], v[76:79], v[240:243], v[208:211]\n\t" \
    "v_mfma_f32_16x16x32_bf16 v[200:203], v[60:63], v[240:243], v[200:203]\n\t" \
    "s_waitcnt lgkmcnt(4)\n\t" \
    "v_mfma_f32_16x16x32_bf16 v[216:219], v[68:71], v[224:227], v[216:219]\n\t" \
    "ds_read_b128 v[240:243], v252 offset:46080\n\t" \
    "v_mfma_f32_16x16x32_bf16 v[220:223], v[76:79], v[224:227], v[220:223]\n\t" \
    "v_mfma_f32_16x16x32_bf16 v[212:215], v[60:63], v[224:227], v[212:215]\n\t" \
    "s_waitcnt lgkmcnt(4)\n\t" \
    "v_mfma_f32_16x16x32_bf16 v[204:207], v[160:163], v[228:231], v[204:207]\n\t" \
    "v_xor_b32 v252, 0x2000, %[xa]\n\t" \
    "ds_read_b128 v[224:227], v252 offset:0\n\t" \
    "v_mfma_f32_16x16x32_bf16 v[208:211], v[168:171], v[228:231], v[208:211]\n\t" \
    "v_mfma_f32_16x16x32_bf16 v[200:203], v[244:247], v[228:231], v[200:203]\n\t" \
    "s_waitcnt lgkmcnt(3)\n\t" \
    "v_mfma_f32_16x16x32_bf16 v[216:219], v[160:163], v[232:235], v[216:219]\n\t" \
    "ds_read_b128 v[228:231], v252 offset:16384\n\t" \
    "v_mfma_f32_16x16x32_bf16 v[220:223], v[168:171], v[232:235], v[220:223]\n\t" \
    "v_mfma_f32_16x16x32_bf16 v[212:215], v[244:247], v[232:235], v[212:215]\n\t" \
    "s_waitcnt lgkmcnt(3)\n\t" \
    "v_mfma_f32_16x16x32_bf16 v[204:207], v[164:167], v[236:239], v[204:207]\n\t" \
    "ds_read_b128 v[232:235], v252 offset:1024\n\t" \
    "v_mfma_f32_16x16x32_bf16 v[208:211], v[172:175], v[236:239], v[208:211]\n\t" \
    "ds_read_b128 v[244:247], %[wl] offset:4096\n\t" \
    "v_mfma_f32_16x16x32_bf16 v[200:203], v[248:251], v[236:239], v[200:203]\n\t" \
    "s_waitcnt lgkmcnt(4)\n\t" \
    "v_mfma_f32_16x16x32_bf16 v[216:219], v[164:167], v[240:243], v[216:219]\n\t" \
    "ds_read_b128 v[236:239], v252 offset:17408\n\t" \
    "v_mfma_f32_16x16x32_bf16 v[220:223], v[172:175], v[240:243], v[220:223]\n\t" \
    "v_mfma_f32_16x16x32_bf16 v[212:215], v[248:251], v[240:243], v[212:215]\n\t" \
    "s_waitcnt lgkmcnt(4)\n\t" \
    "v_mfma_f32_16x16x32_bf16 v[204:207], v[80:83], v[224:227], v[204:207]\n\t" \
    "v_xor_b32 v252, 0x2800, %[xa]\n\t" \
    "ds_read_b128 v[240:243], v252 offset:0\n\t" \
    "v_mfma_f32_16x16x32_bf16 v[208:211], v[88:91], v[224:227], v[208:211]\n\t" \
    "ds_read_b128 v[248:251], %[wl] offset:5120\n\t" \
    "s_waitcnt lgkmcnt(3)\n\t" \
    "v_mfma_f32_16x16x32_bf16 v[200:203], v[244:247], v[224:227], v[200:203]\n\t" \
    "v_mfma_f32_16x16x32_bf16 v[216:219], v[80:83], v[228:231], v[216:219]\n\t" \
    "ds_read_b128 v[224:227], v252 offset:16384\n\t" \
    "v_mfma_f32_16x16x32_bf16 v[220:223], v[88:91], v[228:231], v[220:223]\n\t" \
    "v_mfma_f32_16x16x32_bf16 v[212:215], v[244:247], v[228:231], v[212:215]\n\t" \
    "v_mfma_f32_16x16x32_bf16 v[204:207], v[84:87], v[232:235], v[204:207]\n\t" \
    "ds_read_b128 v[228:231], v252 offset:1024\n\t" \
    "v_mfma_f32_16x16x32_bf16 v[208:211], v[92:95], v[232:235], v[208:211]\n\t" \
    "ds_read_b128 v[244:247], %[wl] offset:6144\n\t" \
    "s_waitcnt lgkmcnt(3)\n\t" \
    "v_mfma_f32_16x16x32_bf16 v[200:203], v[248:251], v[232:235], v[200:203]\n\t" \
    "v_mfma_f32_16x16x32_bf16 v[216:219], v[84:87], v[236:239], v[216:219]\n\t" \
    "ds_read_b128 v[232:235], v252 offset:17408\n\t" \
    "v_mfma_f32_16x16x32_bf16 v[220:223], v[92:95], v[236:239], v[220:223]\n\t" \
    "v_mfma_f32_16x16x32_bf16 v[212:215], v[248:251], v[236:239], v[212:215]\n\t" \
    "v_mfma_f32_16x16x32_bf16 v[204:207], v[104:107], v[240:243], v[204:207]\n\t" \
    "v_xor_b32 v252, 0x3000, %[xa]\n\t" \
    "ds_read_b128 v[236:239], v252 offset:0\n\t" \
    "v_mfma_f32_16x16x32_bf16 v[208:211], v[112:115], v[240:243], v[208:211]\n\t" \
    "ds_read_b128 v[248:251], %[wl] offset:7168\n\t" \
    "v_mfma_f32_16x16x32_bf16 v[200:203], v[96:99], v[240:243], v[200:203]\n\t" \
    "s_waitcnt lgkmcnt(5)\n\t" \
    "v_mfma_f32_16x16x32_bf16 v[216:219], v[104:107], v[224:227], v[216:219]\n\t" \
    "ds_read_b128 v[240:243], v252 offset:16384\n\t" \
    "v_mfma_f32_16x16x32_bf16 v[220:223], v[112:115], v[224:227], v[220:223]\n\t" \
    "v_mfma_f32_16x16x32_bf16 v[212:215], v[96:99], v[224:227], v[212:215]\n\t" \
    "s_waitcnt lgkmcnt(5)\n\t" \
    "v_mfma_f32_16x16x32_bf16 v[204:207], v[108:111], v[228:231], v[204:207]\n\t" \
    "ds_read_b128 v[224:227], v252 offset:1024\n\t" \
    "v_mfma_f32_16x16x32_bf16 v[208:211], v[116:119], v[228:231], v[208:211]\n\t" \
    "v_mfma_f32_16x16x32_bf16 v[200:203], v[100:103], v[228:231], v[200:203]\n\t" \
    "s_waitcnt lgkmcnt(4)\n\t" \
    "v_mfma_f32_16x16x32_bf16 v[216:219], v[108:111], v[232:235], v[216:219]\n\t" \
    "ds_read_b128 v[228:231], v252 offset:17408\n\t" \
    "v_mfma_f32_16x16x32_bf16 v[220:223], v[116:119], v[232:235], v[220:223]\n\t" \
    "v_mfma_f32_16x16x32_bf16 v[212:215], v[100:103], v[232:235], v[212:215]\n\t" \
    "s_waitcnt lgkmcnt(4)\n\t" \
    "v_mfma_f32_16x16x32_bf16 v[204:207], v[120:123], v[236:239], v[204:207]\n\t" \
    "v_xor_b32 v252, 0x3800, %[xa]\n\t" \
    "ds_read_b128 v[232:235], v252 offset:0\n\t" \
    "v_mfma_f32_16x16x32_bf16 v[208:211], v[128:131], v[236:239], v[208:211]\n\t" \
    "v_mfma_f32_16x16x32_bf16 v[200:203], v[244:247], v[236:239], v[200:203]\n\t" \
    "s_waitcnt lgkmcnt(3)\n\t" \
    "v_mfma_f32_16x16x32_bf16 v[216:219], v[120:123], v[240:243], v[216:219]\n\t" \
    "ds_read_b128 v[236:239], v252 offset:16384\n\t" \
    "v_mfma_f32_16x16x32_bf16 v[220:223], v[128:131], v[240:243], v[220:223]\n\t" \
    "v_mfma_f32_16x16x32_bf16 v[212:215], v[244:247], v[240:243], v[212:215]\n\t" \
    "s_waitcnt lgkmcnt(3)\n\t" \
    "v_mfma_f32_16x16x32_bf16 v[204:207], v[124:127], v[224:227], v[204:207]\n\t" \
    "ds_read_b128 v[240:243], v252 offset:1024\n\t" \
    "v_mfma_f32_16x16x32_bf16 v[208:211], v[132:135], v[224:227], v[208:211]\n\t" \
    "v_mfma_f32_16x16x32_bf16 v[200:203], v[248:251], v[224:227], v[200:203]\n\t" \
    "s_waitcnt lgkmcnt(3)\n\t" \
    "v_mfma_f32_16x16x32_bf16 v[216:219], v[124:127], v[228:231], v[216:219]\n\t" \
    "ds_read_b128 v[224:227], v252 offset:17408\n\t" \
    "v_mfma_f32_16x16x32_bf16 v[220:223], v[132:135], v[228:231], v[220:223]\n\t" \
    "v_mfma_f32_16x16x32_bf16 v[212:215], v[248:251], v[228:231], v[212:215]\n\t" \
    "s_waitcnt lgkmcnt(3)\n\t" \
    "v_mfma_f32_16x16x32_bf16 v[204:207], v[144:147], v[232:235], v[204:207]\n\t" \
    "v_and_b32 v252, 0x3ff, %[xa]\n\t" \
    "ds_read_b128 v[228:231], v252 offset:43008\n\t" \
    "v_mov_b64 v[244:245], 0\n\t" \
    "v_mfma_f32_16x16x32_bf16 v[208:211], v[152:155], v[232:235], v[208:211]\n\t" \
    "v_mov_b64 v[246:247], 0\n\t" \
    "v_mfma_f32_16x16x32_bf16 v[200:203], v[136:139], v[232:235], v[200:203]\n\t" \
    "v_mov_b64 v[248:249], 0\n\t" \
    "s_waitcnt lgkmcnt(3)\n\t" \
    "v_mfma_f32_16x16x32_bf16 v[216:219], v[144:147], v[236:239], v[216:219]\n\t" \
    "ds_read_b128 v[232:235], v252 offset:44032\n\t" \
    "v_mov_b64 v[250:251], 0\n\t" \
    "v_mfma_f32_16x16x32_bf16 v[220:223], v[152:155], v[236:239], v[220:223]\n\t" \
    "v_mfma_f32_16x16x32_bf16 v[212:215], v[136:139], v[236:239], v[212:215]\n\t" \
    "s_waitcnt lgkmcnt(3)\n\t" \
    "v_mfma_f32_16x16x32_bf16 v[204:207], v[148:151], v[240:243], v[204:207]\n\t" \
    "ds_read_b128 v[236:239], v252 offset:47104\n\t" \
    "v_mfma_f32_16x16x32_bf16 v[208:211], v[156:159], v[240:243], v[208:211]\n\t" \
    "v_mfma_f32_16x16x32_bf16 v[200:203], v[140:143], v[240:243], v[200:203]\n\t" \
    "s_waitcnt lgkmcnt(3)\n\t" \
    "v_mfma_f32_16x16x32_bf16 v[216:219], v[148:151], v[224:227], v[216:219]\n\t" \
    "ds_read_b128 v[240:243], v252 offset:48128\n\t" \
    "v_mfma_f32_16x16x32_bf16 v[220:223], v[156:159], v[224:227], v[220:223]\n\t" \
    "v_mfma_f32_16x16x32_bf16 v[212:215], v[140:143], v[224:227], v[212:215]\n\t" \
    "s_waitcnt lgkmcnt(3)\n\t" \
    "v_mfma_f32_16x16x32_bf16 v[204:207], v[184:187], v[228:231], v[204:207]\n\t" \
    "v_add_u32 v252, %[qs], v252\n\t" \
    "v_mfma_f32_16x16x32_bf16 v[208:211], v[192:195], v[228:231], v[208:211]\n\t" \
    "v_mov_b64 v[224:225], 0\n\t" \
    "v_mfma_f32_16x16x32_bf16 v[200:203], v[176:179], v[228:231], v[200:203]\n\t" \
    "v_mov_b64 v[226:227], 0\n\t" \
    "s_waitcnt lgkmcnt(2)\n\t" \
    "v_mfma_f32_16x16x32_bf16 v[204:207], v[188:191], v[232:235], v[204:207]\n\t" \
    "v_mfma_f32_16x16x32_bf16 v[208:211], v[196:199], v[232:235], v[208:211]\n\t" \
    "v_mfma_f32_16x16x32_bf16 v[200:203], v[180:183], v[232:235], v[200:203]\n\t" \
    "s_waitcnt lgkmcnt(1)\n\t" \
    "v_mfma_f32_16x16x32_bf16 v[216:219], v[184:187], v[236:239], v[216:219]\n\t" \
    "ds_read_b128 v[228:231], %[wl] offset:2048\n\t" \
    "ds_read_b128 v[232:235], %[wl] offset:3072\n\t" \
    "v_mfma_f32_16x16x32_bf16 v[220:223], v[192:195], v[236:239], v[220:223]\n\t" \
    "v_cvt_pk_bf16_f32 v204, v204, v205\n\t" \
    "v_cvt_pk_bf16_f32 v205, v206, v207\n\t" \
    "v_mfma_f32_16x16x32_bf16 v[212:215], v[176:179], v[236:239], v[212:215]\n\t" \
    "ds_write_b64 v252, v[204:205] offset:1024\n\t" \
    "v_cvt_pk_bf16_f32 v208, v208, v209\n\t" \
    "s_waitcnt lgkmcnt(3)\n\t" \
    "v_mfma_f32_16x16x32_bf16 v[216:219], v[188:191], v[240:243], v[216:219]\n\t" \
    "v_cvt_pk_bf16_f32 v209, v210, v211\n\t" \
    "ds_write_b64 v252, v[208:209] offset:2048\n\t" \
    "v_mfma_f32_16x16x32_bf16 v[220:223], v[196:199], v[240:243], v[220:223]\n\t" \
    "v_cvt_pk_bf16_f32 v200, v200, v201\n\t" \
    "v_cvt_pk_bf16_f32 v201, v202, v203\n\t" \
    "v_mfma_f32_16x16x32_bf16 v[212:215], v[180:183], v[240:243], v[212:215]\n\t" \
    "ds_write_b64 v252, v[200:201] offset:0\n\t" \
    "ds_read_b128 v[200:203], %[wl] offset:0\n\t" \
    "ds_read_b128 v[204:207], %[wl] offset:1024\n\t" \
    "v_cvt_pk_bf16_f32 v216, v216, v217\n\t" \
    "v_cvt_pk_bf16_f32 v217, v218, v219\n\t" \
    "ds_write_b64 v252, v[216:217] offset:7168\n\t" \
    "v_cvt_pk_bf16_f32 v220, v220, v221\n\t" \
    "v_cvt_pk_bf16_f32 v221, v222, v223\n\t" \
    "ds_write_b64 v252, v[220:221] offset:8192\n\t" \
    "v_cvt_pk_bf16_f32 v212, v212, v213\n\t" \
    "v_cvt_pk_bf16_f32 v213, v214, v215\n\t" \
    "ds_write_b64 v252, v[212:213] offset:6144\n\t" \
    "s_waitcnt lgkmcnt(0)\n\t"
#define ASM_XG_H_HI \
    "s_waitcnt lgkmcnt(0)\n\t" \
    "v_xor_b32 v252, 0x2000, %[xa]\n\t" \
    "ds_read_b128 v[224:227], v252 offset:0\n\t" \
    "ds_read_b128 v[244:247], %[wl] offset:4096\n\t" \
    "ds_read_b128 v[228:231], v252 offset:16384\n\t" \
    "ds_read_b128 v[232:235], v252 offset:1024\n\t" \
    "ds_read_b128 v[236:239], v252 offset:17408\n\t" \
    "ds_read_b128 v[248:251], %[wl] offset:5120\n\t" \
    "s_waitcnt lgkmcnt(5)\n\t" \
    "v_mfma_f32_16x16x32_bf16 v[204:207], v[80:83], v[224:227], 0\n\t" \
    "v_xor_b32 v252, 0x2800, %[xa]\n\t" \
    "ds_read_b128 v[240:243], v252 offset:0\n\t" \
    "v_mfma_f32_16x16x32_bf16 v[208:211], v[88:91], v[224:227], 0\n\t" \
    "s_waitcnt lgkmcnt(5)\n\t" \
    "v_mfma_f32_16x16x32_bf16 v[200:203], v[244:247], v[224:227], 0\n\t" \
    "s_waitcnt lgkmcnt(4)\n\t" \
    "v_mfma_f32_16x16x32_bf16 v[216:219], v[80:83], v[228:231], 0\n\t" \
    "ds_read_b128 v[224:227], v252 offset:16384\n\t" \
    "v_mfma_f32_16x16x32_bf16 v[220:223], v[88:91], v[228:231], 0\n\t" \
    "v_mfma_f32_16x16x32_bf16 v[212:215], v[244:247], v[228:231], 0\n\t" \
    "s_waitcnt lgkmcnt(4)\n\t" \
    "v_mfma_f32_16x16x32_bf16 v[204:207], v[84:87], v[232:235], v[204:207]\n\t" \
    "ds_read_b128 v[228:231], v252 offset:1024\n\t" \
    "v_mfma_f32_16x16x32_bf16 v[208:211], v[92:95], v[232:235], v[208:211]\n\t" \
    "ds_read_b128 v[244:247], %[wl] offset:6144\n\t" \
    "s_waitcnt lgkmcnt(4)\n\t" \
    "v_mfma_f32_16x16x32_bf16 v[200:203], v[248:251], v[232:235], v[200:203]\n\t" \
    "v_mfma_f32_16x16x32_bf16 v[216:219], v[84:87], v[236:239], v[216:219]\n\t" \
    "ds_read_b128 v[232:235], v252 offset:17408\n\t" \
    "v_mfma_f32_16x16x32_bf16 v[220:223], v[92:95], v[236:239], v[220:223]\n\t" \
    "v_mfma_f32_16x16x32_bf16 v[212:215], v[248:251], v[236:239], v[212:215]\n\t" \
    "s_waitcnt lgkmcnt(4)\n\t" \
    "v_mfma_f32_16x16x32_bf16 v[204:207], v[104:107], v[240:243], v[204:207]\n\t" \
    "v_xor_b32 v252, 0x3000, %[xa]\n\t" \
    "ds_read_b128 v[236:239], v252 offset:0\n\t" \
    "v_mfma_f32_16x16x32_bf16 v[208:211], v[112:115], v[240:243], v[208:211]\n\t" \
    "ds_read_b128 v[248:251], %[wl] offset:7168\n\t" \
    "v_mfma_f32_16x16x32_bf16 v[200:203], v[96:99], v[240:243], v[200:203]\n\t" \
    "s_waitcnt lgkmcnt(5)\n\t" \
    "v_mfma_f32_16x16x32_bf16 v[216:219], v[104:107], v[224:227], v[216:219]\n\t" \
    "ds_read_b128 v[240:243], v252 offset:16384\n\t" \
    "v_mfma_f32_16x16x32_bf16 v[220:223], v[112:115], v[224:227], v[220:223]\n\t" \
    "v_mfma_f32_16x16x32_bf16 v[212:215], v[96:99], v[224:227], v[212:215]\n\t" \
    "s_nop 3\n\t" \
    "v_mov_b32 v224, %[tv]\n\t" \
    "s_waitcnt vmcnt(5)\n\t" \
    "buffer_store_dword v224, %[fv], %[rr], %[fp] offen sc1\n\t" \
    "s_waitcnt lgkmcnt(5)\n\t" \
    "v_mfma_f32_16x16x32_bf16 v[204:207], v[108:111], v[228:231], v[204:207]\n\t" \
    "ds_read_b128 v[224:227], v252 offset:1024\n\t" \
    "v_mfma_f32_16x16x32_bf16 v[208:211], v[116:119], v[228:231], v[208:211]\n\t" \
    "v_mfma_f32_16x16x32_bf16 v[200:203], v[100:103], v[228:231], v[200:203]\n\t" \
    "s_waitcnt lgkmcnt(4)\n\t" \
    "v_mfma_f32_16x16x32_bf16 v[216:219], v[108:111], v[232:235], v[216:219]\n\t" \
    "ds_read_b128 v[228:231], v252 offset:17408\n\t" \
    "v_mfma_f32_16x16x32_bf16 v[220:223], v[116:119], v[232:235], v[220:223]\n\t" \
    "v_mfma_f32_16x16x32_bf16 v[212:215], v[100:103], v[232:235], v[212:215]\n\t" \
    "s_waitcnt lgkmcnt(4)\n\t" \
    "v_mfma_f32_16x16x32_bf16 v[204:207], v[120:123], v[236:239], v[204:207]\n\t" \
    "v_xor_b32 v252, 0x3800, %[xa]\n\t" \
    "ds_read_b128 v[232:235], v252 offset:0\n\t" \
    "v_mfma_f32_16x16x32_bf16 v[208:211], v[128:131], v[236:239], v[208:211]\n\t" \
    "v_mfma_f32_16x16x32_bf16 v[200:203], v[244:247], v[236:239], v[200:203]\n\t" \
    "s_waitcnt lgkmcnt(3)\n\t" \
    "v_mfma_f32_16x16x32_bf16 v[216:219], v[120:123], v[240:243], v[216:219]\n\t" \
    "ds_read_b128 v[236:239], v252 offset:16384\n\t" \
    "v_mfma_f32_16x16x32_bf16 v[220:223], v[128:131], v[240:243], v[220:223]\n\t" \
    "v_mfma_f32_16x16x32_bf16 v[212:215], v[244:247], v[240:243], v[212:215]\n\t" \
    "s_waitcnt lgkmcnt(3)\n\t" \
    "v_mfma_f32_16x16x32_bf16 v[204:207], v[124:127], v[224:227], v[204:207]\n\t" \
    "ds_read_b128 v[240:243], v252 offset:1024\n\t" \
    "v_mfma_f32_16x16x32_bf16 v[208:211], v[132:135], v[224:227], v[208:211]\n\t" \
    "ds_read_b128 v[244:247], %[wl] offset:8192\n\t" \
    "v_mfma_f32_16x16x32_bf16 v[200:203], v[248:251], v[224:227], v[200:203]\n\t" \
    "s_waitcnt lgkmcnt(4)\n\t" \
    "v_mfma_f32_16x16x32_bf16 v[216:219], v[124:127], v[228:231], v[216:219]\n\t" \
    "ds_read_b128 v[224:227], v252 offset:17408\n\t" \
    "v_mfma_f32_16x16x32_bf16 v[220:223], v[132:135], v[228:231], v[220:223]\n\t" \
    "v_mfma_f32_16x16x32_bf16 v[212:215], v[248:251], v[228:231], v[212:215]\n\t" \
    "s_waitcnt lgkmcnt(4)\n\t" \
    "v_mfma_f32_16x16x32_bf16 v[204:207], v[144:147], v[232:235], v[204:207]\n\t" \
    "v_and_b32 v252, 0x3ff, %[xa]\n\t" \
    "ds_read_b128 v[228:231], v252 offset:40960\n\t" \
    "v_mfma_f32_16x16x32_bf16 v[208:211], v[152:155], v[232:235], v[208:211]\n\t" \
    "ds_read_b128 v[248:251], %[wl] offset:9216\n\t" \
    "v_mfma_f32_16x16x32_bf16 v[200:203], v[136:139], v[232:235], v[200:203]\n\t" \
    "s_waitcnt lgkmcnt(5)\n\t" \
    "v_mfma_f32_16x16x32_bf16 v[216:219], v[144:147], v[236:239], v[216:219]\n\t" \
    "ds_read_b128 v[232:235], v252 offset:45056\n\t" \
    "v_mfma_f32_16x16x32_bf16 v[220:223], v[152:155], v[236:239], v[220:223]\n\t" \
    "v_mfma_f32_16x16x32_bf16 v[212:215], v[136:139], v[236:239], v[212:215]\n\t" \
    "s_waitcnt lgkmcnt(5)\n\t" \
    "v_mfma_f32_16x16x32_bf16 v[204:207], v[148:151], v[240:243], v[204:207]\n\t" \
    "ds_read_b128 v[236:239], v252 offset:41984\n\t" \
    "v_mfma_f32_16x16x32_bf16 v[208:211], v[156:159], v[240:243], v[208:211]\n\t" \
    "v_mfma_f32_16x16x32_bf16 v[200:203], v[140:143], v[240:243], v[200:203]\n\t" \
    "s_waitcnt lgkmcnt(4)\n\t" \
    "v_mfma_f32_16x16x32_bf16 v[216:219], v[148:151], v[224:227], v[216:219]\n\t" \
    "ds_read_b128 v[240:243], v252 offset:46080\n\t" \
    "v_mfma_f32_16x16x32_bf16 v[220:223], v[156:159], v[224:227], v[220:223]\n\t" \
    "v_mfma_f32_16x16x32_bf16 v[212:215], v[140:143], v[224:227], v[212:215]\n\t" \
    "s_waitcnt vmcnt(1) lgkmcnt(0)\n\t" \
    "s_barrier\n\t" ASM_ST \
    "v_mfma_f32_16x16x32_bf16 v[204:207], v[160:163], v[228:231], v[204:207]\n\t" \
    "ds_read_b128 v[224:227], %[xa] offset:0\n\t" \
    "v_mfma_f32_16x16x32_bf16 v[208:211], v[168:171], v[228:231], v[208:211]\n\t" \
    "v_mfma_f32_16x16x32_bf16 v[200:203], v[244:247], v[228:231], v[200:203]\n\t" \
    "v_mfma_f32_16x16x32_bf16 v[216:219], v[160:163], v[232:235], v[216:219]\n\t" \
    "ds_read_b128 v[228:231], %[xa] offset:16384\n\t" \
    "v_mfma_f32_16x16x32_bf16 v[220:223], v[168:171], v[232:235], v[220:223]\n\t" \
    "v_mfma_f32_16x16x32_bf16 v[212:215], v[244:247], v[232:235], v[212:215]\n\t" \
    "v_mfma_f32_16x16x32_bf16 v[204:207], v[164:167], v[236:239], v[204:207]\n\t" \
    "ds_read_b128 v[232:235], %[xa] offset:1024\n\t" \
    "v_mfma_f32_16x16x32_bf16 v[208:211], v[172:175], v[236:239], v[208:211]\n\t" \
    "ds_read_b128 v[244:247], %[wl] offset:0\n\t" \
    "v_mfma_f32_16x16x32_bf16 v[200:203], v[248:251], v[236:239], v[200:203]\n\t" \
    "v_mfma_f32_16x16x32_bf16 v[216:219], v[164:167], v[240:243], v[216:219]\n\t" \
    "ds_read_b128 v[236:239], %[xa] offset:17408\n\t" \
    "v_mfma_f32_16x16x32_bf16 v[220:223], v[172:175], v[240:243], v[220:223]\n\t" \
    "v_mfma_f32_16x16x32_bf16 v[212:215], v[248:251], v[240:243], v[212:215]\n\t" \
    "s_waitcnt lgkmcnt(4)\n\t" \
    "v_mfma_f32_16x16x32_bf16 v[204:207], v[0:3], v[224:227], v[204:207]\n\t" \
    "v_xor_b32 v252, 0x800, %[xa]\n\t" \
    "ds_read_b128 v[240:243], v252 offset:0\n\t" \
    "v_mfma_f32_16x16x32_bf16 v[208:211], v[8:11], v[224:227], v[208:211]\n\t" \
    "ds_read_b128 v[248:251], %[wl] offset:1024\n\t" \
    "s_waitcnt lgkmcnt(3)\n\t" \
    "v_mfma_f32_16x16x32_bf16 v[200:203], v[244:247], v[224:227], v[200:203]\n\t" \
    "v_mfma_f32_16x16x32_bf16 v[216:219], v[0:3], v[228:231], v[216:219]\n\t" \
    "ds_read_b128 v[224:227], v252 offset:16384\n\t" \
    "v_mfma_f32_16x16x32_bf16 v[220:223], v[8:11], v[228:231], v[220:223]\n\t" \
    "v_mfma_f32_16x16x32_bf16 v[212:215], v[244:247], v[228:231], v[212:215]\n\t" \
    "v_mfma_f32_16x16x32_bf16 v[204:207], v[4:7], v[232:235], v[204:207]\n\t" \
    "ds_read_b128 v[228:231], v252 offset:1024\n\t" \
    "v_mfma_f32_16x16x32_bf16 v[208:211], v[12:15], v[232:235], v[208:211]\n\t" \
    "ds_read_b128 v[244:247], %[wl] offset:2048\n\t" \
    "s_waitcnt lgkmcnt(3)\n\t" \
    "v_mfma_f32_16x16x32_bf16 v[200:203], v[248:251], v[232:235], v[200:203]\n\t" \
    "v_mfma_f32_16x16x32_bf16 v[216:219], v[4:7], v[236:239], v[216:219]\n\t" \
    "ds_read_b128 v[232:235], v252 offset:17408\n\t" \
    "v_mfma_f32_16x16x32_bf16 v[220:223], v[12:15], v[236:239], v[220:223]\n\t" \
    "v_mfma_f32_16x16x32_bf16 v[212:215], v[248:251], v[236:239], v[212:215]\n\t" \
    "v_mfma_f32_16x16x32_bf16 v[204:207], v[24:27], v[240:243], v[204:207]\n\t" \
    "v_xor_b32 v252, 0x1000, %[xa]\n\t" \
    "ds_read_b128 v[236:239], v252 offset:0\n\t" \
    "v_mfma_f32_16x16x32_bf16 v[208:211], v[32:35], v[240:243], v[208:211]\n\t" \
    "ds_read_b128 v[248:251], %[wl] offset:3072\n\t" \
    "v_mfma_f32_16x16x32_bf16 v[200:203], v[16:19], v[240:243], v[200:203]\n\t" \
    "s_waitcnt lgkmcnt(5)\n\t" \
    "v_mfma_f32_16x16x32_bf16 v[216:219], v[24:27], v[224:227], v[216:219]\n\t" \
    "ds_read_b128 v[240:243], v252 offset:16384\n\t" \
    "v_mfma_f32_16x16x32_bf16 v[220:223], v[32:35], v[224:227], v[220:223]\n\t" \
    "v_mfma_f32_16x16x32_bf16 v[212:215], v[16:19], v[224:227], v[212:215]\n\t" \
    "s_waitcnt lgkmcnt(5)\n\t" \
    "v_mfma_f32_16x16x32_bf16 v[204:207], v[28:31], v[228:231], v[204:207]\n\t" \
    "ds_read_b128 v[224:227], v252 offset:1024\n\t" \
    "v_mfma_f32_16x16x32_bf16 v[208:211], v[36:39], v[228:231], v[208:211]\n\t" \
    "v_mfma_f32_16x16x32_bf16 v[200:203], v[20:23], v[228:231], v[200:203]\n\t" \
    "s_waitcnt lgkmcnt(4)\n\t" \
    "v_mfma_f32_16x16x32_bf16 v[216:219], v[28:31], v[232:235], v[216:219]\n\t" \
    "ds_read_b128 v[228:231], v252 offset:17408\n\t" \
    "v_mfma_f32_16x16x32_bf16 v[220:223], v[36:39], v[232:235], v[220:223]\n\t" \
    "v_mfma_f32_16x16x32_bf16 v[212:215], v[20:23], v[232:235], v[212:215]\n\t" \
    "s_waitcnt lgkmcnt(4)\n\t" \
    "v_mfma_f32_16x16x32_bf16 v[204:207], v[40:43], v[236:239], v[204:207]\n\t" \
    "v_xor_b32 v252, 0x1800, %[xa]\n\t" \
    "ds_read_b128 v[232:235], v252 offset:0\n\t" \
    "v_mfma_f32_16x16x32_bf16 v[208:211], v[48:51], v[236:239], v[208:211]\n\t" \
    "v_mfma_f32_16x16x32_bf16 v[200:203], v[244:247], v[236:239], v[200:203]\n\t" \
    "s_waitcnt lgkmcnt(3)\n\t" \
    "v_mfma_f32_16x16x32_bf16 v[216:219], v[40:43], v[240:243], v[216:219]\n\t" \
    "ds_read_b128 v[236:239], v252 offset:16384\n\t" \
    "v_mfma_f32_16x16x32_bf16 v[220:223], v[48:51], v[240:243], v[220:223]\n\t" \
    "v_mfma_f32_16x16x32_bf16 v[212:215], v[244:247], v[240:243], v[212:215]\n\t" \
    "s_waitcnt lgkmcnt(3)\n\t" \
    "v_mfma_f32_16x16x32_bf16 v[204:207], v[44:47], v[224:227], v[204:207]\n\t" \
    "ds_read_b128 v[240:243], v252 offset:1024\n\t" \
    "v_mfma_f32_16x16x32_bf16 v[208:211], v[52:55], v[224:227], v[208:211]\n\t" \
    "v_mfma_f32_16x16x32_bf16 v[200:203], v[248:251], v[224:227], v[200:203]\n\t" \
    "s_waitcnt lgkmcnt(3)\n\t" \
    "v_mfma_f32_16x16x32_bf16 v[216:219], v[44:47], v[228:231], v[216:219]\n\t" \
    "ds_read_b128 v[224:227], v252 offset:17408\n\t" \
    "v_mfma_f32_16x16x32_bf16 v[220:223], v[52:55], v[228:231], v[220:223]\n\t" \
    "v_mfma_f32_16x16x32_bf16 v[212:215], v[248:251], v[228:231], v[212:215]\n\t" \
    "s_waitcnt lgkmcnt(3)\n\t" \
    "v_mfma_f32_16x16x32_bf16 v[204:207], v[64:67], v[232:235], v[204:207]\n\t" \
    "v_and_b32 v252, 0x3ff, %[xa]\n\t" \
    "ds_read_b128 v[228:231], v252 offset:43008\n\t" \
    "v_mov_b64 v[244:245], 0\n\t" \
    "v_mfma_f32_16x16x32_bf16 v[208:211], v[72:75], v[232:235], v[208:211]\n\t" \
    "v_mov_b64 v[246:247], 0\n\t" \
    "v_mfma_f32_16x16x32_bf16 v[200:203], v[56:59], v[232:235], v[200:203]\n\t" \
    "v_mov_b64 v[248:249], 0\n\t" \
    "s_waitcnt lgkmcnt(3)\n\t" \
    "v_mfma_f32_16x16x32_bf16 v[216:219], v[64:67], v[236:239], v[216:219]\n\t" \
    "ds_read_b128 v[232:235], v252 offset:44032\n\t" \
    "v_mov_b64 v[250:251], 0\n\t" \
    "v_mfma_f32_16x16x32_bf16 v[220:223], v[72:75], v[236:239], v[220:223]\n\t" \
    "v_mfma_f32_16x16x32_bf16 v[212:215], v[56:59], v[236:239], v[212:215]\n\t" \
    "s_waitcnt lgkmcnt(3)\n\t" \
    "v_mfma_f32_16x16x32_bf16 v[204:207], v[68:71], v[240:243], v[204:207]\n\t" \
    "ds_read_b128 v[236:239], v252 offset:47104\n\t" \
    "v_mfma_f32_16x16x32_bf16 v[208:211], v[76:79], v[240:243], v[208:211]\n\t" \
    "v_mfma_f32_16x16x32_bf16 v[200:203], v[60:63], v[240:243], v[200:203]\n\t" \
    "s_waitcnt lgkmcnt(3)\n\t" \
    "v_mfma_f32_16x16x32_bf16 v[216:219], v[68:71], v[224:227], v[216:219]\n\t" \
    "ds_read_b128 v[240:243], v252 offset:48128\n\t" \
    "v_mfma_f32_16x16x32_bf16 v[220:223], v[76:79], v[224:227], v[220:223]\n\t" \
    "v_mfma_f32_16x16x32_bf16 v[212:215], v[60:63], v[224:227], v[212:215]\n\t" \
    "s_waitcnt lgkmcnt(3)\n\t" \
    "v_mfma_f32_16x16x32_bf16 v[204:207], v[184:187], v[228:231], v[204:207]\n\t" \
    "v_add_u32 v252, %[qs], v252\n\t" \
    "v_mfma_f32_16x16x32_bf16 v[208:211], v[192:195], v[228:231], v[208:211]\n\t" \
    "v_mov_b64 v[224:225], 0\n\t" \
    "v_mfma_f32_16x16x32_bf16 v[200:203], v[176:179], v[228:231], v[200:203]\n\t" \
    "v_mov_b64 v[226:227], 0\n\t" \
    "s_waitcnt lgkmcnt(2)\n\t" \
    "v_mfma_f32_16x16x32_bf16 v[204:207], v[188:191], v[232:235], v[204:207]\n\t" \
    "v_mfma_f32_16x16x32_bf16 v[208:211], v[196:199], v[232:235], v[208:211]\n\t" \
    "v_mfma_f32_16x16x32_bf16 v[200:203], v[180:183], v[232:235], v[200:203]\n\t" \
    "s_waitcnt lgkmcnt(1)\n\t" \
    "v_mfma_f32_16x16x32_bf16 v[216:219], v[184:187], v[236:239], v[216:219]\n\t" \
    "ds_read_b128 v[228:231], %[wl] offset:2048\n\t" \
    "ds_read_b128 v[232:235], %[wl] offset:3072\n\t" \
    "v_mfma_f32_16x16x32_bf16 v[220:223], v[192:195], v[236:239], v[220:223]\n\t" \
    "v_cvt_pk_bf16_f32 v204, v204, v205\n\t" \
    "v_cvt_pk_bf16_f32 v205, v206, v207\n\t" \
    "v_mfma_f32_16x16x32_bf16 v[212:215], v[176:179], v[236:239], v[212:215]\n\t" \
    "ds_write_b64 v252, v[204:205] offset:1024\n\t" \
    "v_cvt_pk_bf16_f32 v208, v208, v209\n\t" \
    "s_waitcnt lgkmcnt(3)\n\t" \
    "v_mfma_f32_16x16x32_bf16 v[216:219], v[188:191], v[240:243], v[216:219]\n\t" \
    "v_cvt_pk_bf16_f32 v209, v210, v211\n\t" \
    "ds_write_b64 v252, v[208:209] offset:2048\n\t" \
    "v_mfma_f32_16x16x32_bf16 v[220:223], v[196:199], v[240:243], v[220:223]\n\t" \
    "v_cvt_pk_bf16_f32 v200, v200, v201\n\t" \
    "v_cvt_pk_bf16_f32 v201, v202, v203\n\t" \
    "v_mfma_f32_16x16x32_bf16 v[212:215], v[180:183], v[240:243], v[212:215]\n\t" \
    "ds_write_b64 v252, v[200:201] offset:0\n\t" \
    "ds_read_b128 v[200:203], %[wl] offset:0\n\t" \
    "ds_read_b128 v[204:207], %[wl] offset:1024\n\t" \
    "v_cvt_pk_bf16_f32 v216, v216, v217\n\t" \
    "v_cvt_pk_bf16_f32 v217, v218, v219\n\t" \
    "ds_write_b64 v252, v[216:217] offset:7168\n\t" \
    "v_cvt_pk_bf16_f32 v220, v220, v221\n\t" \
    "v_cvt_pk_bf16_f32 v221, v222, v223\n\t" \
    "ds_write_b64 v252, v[220:221] offset:8192\n\t" \
    "v_cvt_pk_bf16_f32 v212, v212, v213\n\t" \
    "v_cvt_pk_bf16_f32 v213, v214, v215\n\t" \
    "ds_write_b64 v252, v[212:213] offset:6144\n\t" \
    "s_waitcnt lgkmcnt(0)\n\t"
#define ASM_CRIT1_3 \
    "s_branch .Lc0%=\n\t" \
    ".Lcr%=:\n\t" \
    "s_cmp_eq_u32 %[n], 0\n\t" \
    "s_cbranch_scc1 .Lcb%=\n\t" \
    "s_sub_u32 %[n], %[n], 1\n\t" \
    "v_max_u32 v243, v208, v209\n\t" \
    "v_max3_u32 v243, v210, v211, v243\n\t" \
    "v_cmp_eq_u32 vcc, -1, v243\n\t" \
    "s_cbranch_vccz .Lcs0_%=\n\t" \
    "buffer_load_dwordx4 v[208:211], %[ab], %[rs], %[s0] offen sc1\n\t" \
    ".Lcs0_%=:\n\t" \
    "v_max_u32 v243, v236, v237\n\t" \
    "v_max3_u32 v243, v238, v239, v243\n\t" \
    "v_cmp_eq_u32 vcc, -1, v243\n\t" \
    "s_cbranch_vccz .Lcs1_%=\n\t" \
    "buffer_load_dwordx4 v[236:239], %[ab], %[rs], %[s1] offen sc1\n\t" \
    ".Lcs1_%=:\n\t" \
    "v_max_u32 v243, v220, v221\n\t" \
    "v_max3_u32 v243, v222, v223, v243\n\t" \
    "v_cmp_eq_u32 vcc, -1, v243\n\t" \
    "s_cbranch_vccz .Lca%=\n\t" \
    "buffer_load_dwordx4 v[220:223], %[ab], %[rs], %[s2] offen sc1\n\t" \
    "s_branch .Lca%=\n\t" \
    ".Lc0%=:\n\t" \
    "buffer_load_dwordx4 v[208:211], %[ab], %[rs], %[s0] offen sc1\n\t" \
    "buffer_load_dwordx4 v[236:239], %[ab], %[rs], %[s1] offen sc1\n\t" \
    "buffer_load_dwordx4 v[220:223], %[ab], %[rs], %[s2] offen sc1\n\t" \
    "v_bfe_i32 v241, %[ab], 7, 1\n\t" \
    "v_and_b32 v241, 0xaaaa, v241\n\t" \
    "v_or_b32 v241, 0x4444, v241\n\t" \
    "v_xor_b32 v212, 0x400, %[ab]\n\t" \
    "v_xor_b32 v216, 0x800, %[ab]\n\t" \
    "v_xor_b32 v242, 0xc00, %[ab]\n\t" \
    "v_xor_b32 v240, 0x1400, %[ab]\n\t" \
    ".Lca%=:\n\t" \
    "s_waitcnt vmcnt(0)\n\t" \
    "ds_write_b128 %[ab], v[208:211] offset:0\n\t" \
    "ds_write_b128 %[ab], v[236:239] offset:4096\n\t" \
    "ds_write_b128 %[ab], v[220:223] offset:8192\n\t" \
    "v_max_u32 v243, v208, v209\n\t" \
    "v_max3_u32 v243, v210, v211, v243\n\t" \
    "v_max3_u32 v243, v236, v237, v243\n\t" \
    "v_max3_u32 v243, v238, v239, v243\n\t" \
    "v_max3_u32 v243, v220, v221, v243\n\t" \
    "v_max3_u32 v243, v222, v223, v243\n\t" \
    "v_cmp_eq_u32 vcc, -1, v243\n\t" \
    "s_cbranch_vccnz .Lcr%=\n\t" \
    ".Lcb%=:\n\t" \
    PRIUP \
    ASM_ST2 \
    "v_smfmac_f32_16x16x64_bf16 v[224:227], v[208:211], v[200:207], v241\n\t" \
    "ds_read_b128 v[200:203], %[wl] offset:4096\n\t" \
    "ds_read_b128 v[204:207], %[wl] offset:5120\n\t" \
    "v_smfmac_f32_16x16x64_bf16 v[244:247], v[208:211], v[0:7], v241\n\t" \
    "v_smfmac_f32_16x16x64_bf16 v[248:251], v[208:211], v[8:15], v241\n\t" \
    "s_waitcnt lgkmcnt(2)\n\t" \
    "s_barrier\n\t" ASM_ST \
    "ds_read_b128 v[212:215], v212 offset:0\n\t" \
    "ds_read_b128 v[216:219], v216 offset:0\n\t" \
    "ds_read_b128 v[220:223], v242 offset:0\n\t" \
    "ds_read_b128 v[208:211], v240 offset:0\n\t" \
    "v_smfmac_f32_16x16x64_bf16 v[244:247], v[236:239], v[80:87], v241\n\t" \
    "v_xor_b32 v240, 0x1800, %[ab]\n\t" \
    "v_smfmac_f32_16x16x64_bf16 v[248:251], v[236:239], v[88:95], v241\n\t" \
    "s_waitcnt lgkmcnt(4)\n\t" \
    "v_smfmac_f32_16x16x64_bf16 v[224:227], v[236:239], v[200:207], v241\n\t" \
    "ds_read_b128 v[200:203], %[wl] offset:6144\n\t" \
    "ds_read_b128 v[204:207], %[wl] offset:7168\n\t" \
    "ds_read_b128 v[236:239], v240 offset:0\n\t" \
    "s_waitcnt lgkmcnt(6)\n\t" \
    "v_smfmac_f32_16x16x64_bf16 v[224:227], v[212:215], v[16:23], v241\n\t" \
    "v_smfmac_f32_16x16x64_bf16 v[244:247], v[212:215], v[24:31], v241\n\t" \
    "v_xor_b32 v240, 0x1c00, %[ab]\n\t" \
    "v_smfmac_f32_16x16x64_bf16 v[248:251], v[212:215], v[32:39], v241\n\t" \
    "ds_read_b128 v[212:215], v240 offset:0\n\t" \
    "s_waitcnt lgkmcnt(6)\n\t" \
    "v_smfmac_f32_16x16x64_bf16 v[224:227], v[216:219], v[228:235], v241\n\t" \
    "ds_read_b128 v[228:231], %[wl] offset:8192\n\t" \
    "ds_read_b128 v[232:235], %[wl] offset:9216\n\t" \
    "v_smfmac_f32_16x16x64_bf16 v[244:247], v[216:219], v[40:47], v241\n\t" \
    "v_and_b32 v240, 0x3ff, %[ab]\n\t" \
    "v_smfmac_f32_16x16x64_bf16 v[248:251], v[216:219], v[48:55], v241\n\t" \
    "ds_read_b128 v[216:219], v240 offset:8192\n\t" \
    "s_waitcnt lgkmcnt(8)\n\t" \
    "v_smfmac_f32_16x16x64_bf16 v[224:227], v[220:223], v[56:63], v241\n\t" \
    "v_smfmac_f32_16x16x64_bf16 v[244:247], v[220:223], v[64:71], v241\n\t" \
    "v_smfmac_f32_16x16x64_bf16 v[248:251], v[220:223], v[72:79], v241\n\t" \
    "ds_read_b128 v[220:223], v240 offset:9216\n\t" \
    "s_waitcnt lgkmcnt(8)\n\t" \
    "v_smfmac_f32_16x16x64_bf16 v[244:247], v[208:211], v[104:111], v241\n\t" \
    "s_waitcnt lgkmcnt(5)\n\t" \
    "v_smfmac_f32_16x16x64_bf16 v[244:247], v[236:239], v[120:127], v241\n\t" \
    "s_waitcnt lgkmcnt(4)\n\t" \
    "v_smfmac_f32_16x16x64_bf16 v[244:247], v[212:215], v[144:151], v241\n\t" \
    "s_waitcnt lgkmcnt(1)\n\t" \
    "v_smfmac_f32_16x16x64_bf16 v[244:247], v[216:219], v[160:167], v241\n\t" \
    "s_waitcnt lgkmcnt(0)\n\t" \
    "v_smfmac_f32_16x16x64_bf16 v[244:247], v[220:223], v[184:191], v241\n\t" \
    "v_smfmac_f32_16x16x64_bf16 v[248:251], v[208:211], v[112:119], v241\n\t" \
    "v_smfmac_f32_16x16x64_bf16 v[248:251], v[236:239], v[128:135], v241\n\t" \
    "v_smfmac_f32_16x16x64_bf16 v[248:251], v[212:215], v[152:159], v241\n\t" \
    "v_permlane32_swap_b32 v244, v246\n\t" \
    "v_permlane32_swap_b32 v245, v247\n\t" \
    "v_smfmac_f32_16x16x64_bf16 v[248:251], v[216:219], v[168:175], v241\n\t" \
    "v_add_f32 v244, v244, v246\n\t" \
    "v_add_f32 v245, v245, v247\n\t" \
    "v_smfmac_f32_16x16x64_bf16 v[248:251], v[220:223], v[192:199], v241\n\t" \
    "v_cvt_pk_bf16_f32 v244, v244, v245\n\t" \
    "ds_write_b16 %[dst], v244 offset:512\n\t" \
    "ds_write_b16_d16_hi %[dst], v244 offset:528\n\t" \
    "v_smfmac_f32_16x16x64_bf16 v[224:227], v[208:211], v[96:103], v241\n\t" \
    "v_smfmac_f32_16x16x64_bf16 v[224:227], v[236:239], v[200:207], v241\n\t" \
    "v_smfmac_f32_16x16x64_bf16 v[224:227], v[212:215], v[136:143], v241\n\t" \
    "v_permlane32_swap_b32 v248, v250\n\t" \
    "v_permlane32_swap_b32 v249, v251\n\t" \
    "v_smfmac_f32_16x16x64_bf16 v[224:227], v[216:219], v[228:235], v241\n\t" \
    "v_add_f32 v248, v248, v250\n\t" \
    "v_add_f32 v249, v249, v251\n\t" \
    "v_smfmac_f32_16x16x64_bf16 v[224:227], v[220:223], v[176:183], v241\n\t" \
    "v_cvt_pk_bf16_f32 v248, v248, v249\n\t" \
    "ds_write_b16 %[dst], v248 offset:1024\n\t" \
    "ds_write_b16_d16_hi %[dst], v248 offset:1040\n\t" \
    "s_nop 4\n\t" \
    "v_permlane32_swap_b32 v224, v226\n\t" \
    "v_permlane32_swap_b32 v225, v227\n\t" \
    "v_add_f32 v224, v224, v226\n\t" \
    "v_add_f32 v225, v225, v227\n\t" \
    "v_cvt_pk_bf16_f32 v224, v224, v225\n\t" \
    "ds_write_b16 %[dst], v224 offset:0\n\t" \
    "ds_write_b16_d16_hi %[dst], v224 offset:16\n\t"
#define ASM_CRIT1_2 \
    "s_branch .Lc0%=\n\t" \
    ".Lcr%=:\n\t" \
    "s_cmp_eq_u32 %[n], 0\n\t" \
    "s_cbranch_scc1 .Lcb%=\n\t" \
    "s_sub_u32 %[n], %[n], 1\n\t" \
    "v_max_u32 v243, v208, v209\n\t" \
    "v_max3_u32 v243, v210, v211, v243\n\t" \
    "v_cmp_eq_u32 vcc, -1, v243\n\t" \
    "s_cbranch_vccz .Lcs0_%=\n\t" \
    "buffer_load_dwordx4 v[208:211], %[ab], %[rs], %[s0] offen sc1\n\t" \
    ".Lcs0_%=:\n\t" \
    "v_max_u32 v243, v236, v237\n\t" \
    "v_max3_u32 v243, v238, v239, v243\n\t" \
    "v_cmp_eq_u32 vcc, -1, v243\n\t" \
    "s_cbranch_vccz .Lca%=\n\t" \
    "buffer_load_dwordx4 v[236:239], %[ab], %[rs], %[s1] offen sc1\n\t" \
    "s_branch .Lca%=\n\t" \
    ".Lc0%=:\n\t" \
    "buffer_load_dwordx4 v[208:211], %[ab], %[rs], %[s0] offen sc1\n\t" \
    "buffer_load_dwordx4 v[236:239], %[ab], %[rs], %[s1] offen sc1\n\t" \
    "v_bfe_i32 v241, %[ab], 7, 1\n\t" \
    "v_and_b32 v241, 0xaaaa, v241\n\t" \
    "v_or_b32 v241, 0x4444, v241\n\t" \
    "v_xor_b32 v212, 0x400, %[ab]\n\t" \
    "v_xor_b32 v216, 0x800, %[ab]\n\t" \
    "v_xor_b32 v242, 0xc00, %[ab]\n\t" \
    "v_xor_b32 v240, 0x1400, %[ab]\n\t" \
    ".Lca%=:\n\t" \
    "s_waitcnt vmcnt(0)\n\t" \
    "ds_write_b128 %[ab], v[208:211] offset:0\n\t" \
    "ds_write_b128 %[ab], v[236:239] offset:4096\n\t" \
    "v_max_u32 v243, v208, v209\n\t" \
    "v_max3_u32 v243, v210, v211, v243\n\t" \
    "v_max3_u32 v243, v236, v237, v243\n\t" \
    "v_max3_u32 v243, v238, v239, v243\n\t" \
    "v_cmp_eq_u32 vcc, -1, v243\n\t" \
    "s_cbranch_vccnz .Lcr%=\n\t" \
    ".Lcb%=:\n\t" \
    PRIUP \
    ASM_ST2 \
    "v_smfmac_f32_16x16x64_bf16 v[224:227], v[208:211], v[200:207], v241\n\t" \
    "ds_read_b128 v[200:203], %[wl] offset:4096\n\t" \
    "ds_read_b128 v[204:207], %[wl] offset:5120\n\t" \
    "v_smfmac_f32_16x16x64_bf16 v[244:247], v[208:211], v[0:7], v241\n\t" \
    "v_smfmac_f32_16x16x64_bf16 v[248:251], v[208:211], v[8:15], v241\n\t" \
    "s_waitcnt lgkmcnt(2)\n\t" \
    "s_barrier\n\t" ASM_ST \
    "ds_read_b128 v[212:215], v212 offset:0\n\t" \
    "ds_read_b128 v[216:219], v216 offset:0\n\t" \
    "ds_read_b128 v[220:223], v242 offset:0\n\t" \
    "ds_read_b128 v[208:211], v240 offset:0\n\t" \
    "v_smfmac_f32_16x16x64_bf16 v[244:247], v[236:239], v[80:87], v241\n\t" \
    "v_xor_b32 v240, 0x1800, %[ab]\n\t" \
    "v_smfmac_f32_16x16x64_bf16 v[248:251], v[236:239], v[88:95], v241\n\t" \
    "s_waitcnt lgkmcnt(4)\n\t" \
    "v_smfmac_f32_16x16x64_bf16 v[224:227], v[236:239], v[200:207], v241\n\t" \
    "ds_read_b128 v[200:203], %[wl] offset:6144\n\t" \
    "ds_read_b128 v[204:207], %[wl] offset:7168\n\t" \
    "ds_read_b128 v[236:239], v240 offset:0\n\t" \
    "s_waitcnt lgkmcnt(6)\n\t" \
    "v_smfmac_f32_16x16x64_bf16 v[224:227], v[212:215], v[16:23], v241\n\t" \
    "v_smfmac_f32_16x16x64_bf16 v[244:247], v[212:215], v[24:31], v241\n\t" \
    "v_xor_b32 v240, 0x1c00, %[ab]\n\t" \
    "v_smfmac_f32_16x16x64_bf16 v[248:251], v[212:215], v[32:39], v241\n\t" \
    "ds_read_b128 v[212:215], v240 offset:0\n\t" \
    "s_waitcnt lgkmcnt(6)\n\t" \
    "v_smfmac_f32_16x16x64_bf16 v[224:227], v[216:219], v[228:235], v241\n\t" \
    "ds_read_b128 v[228:231], %[wl] offset:8192\n\t" \
    "ds_read_b128 v[232:235], %[wl] offset:9216\n\t" \
    "v_smfmac_f32_16x16x64_bf16 v[244:247], v[216:219], v[40:47], v241\n\t" \
    "v_and_b32 v240, 0x3ff, %[ab]\n\t" \
    "v_smfmac_f32_16x16x64_bf16 v[248:251], v[216:219], v[48:55], v241\n\t" \
    "ds_read_b128 v[216:219], v240 offset:8192\n\t" \
    "s_waitcnt lgkmcnt(8)\n\t" \
    "v_smfmac_f32_16x16x64_bf16 v[224:227], v[220:223], v[56:63], v241\n\t" \
    "v_smfmac_f32_16x16x64_bf16 v[244:247], v[220:223], v[64:71], v241\n\t" \
    "v_smfmac_f32_16x16x64_bf16 v[248:251], v[220:223], v[72:79], v241\n\t" \
    "ds_read_b128 v[220:223], v240 offset:9216\n\t" \
    "s_waitcnt lgkmcnt(8)\n\t" \
    "v_smfmac_f32_16x16x64_bf16 v[244:247], v[208:211], v[104:111], v241\n\t" \
    "s_waitcnt lgkmcnt(5)\n\t" \
    "v_smfmac_f32_16x16x64_bf16 v[244:247], v[236:239], v[120:127], v241\n\t" \
    "s_waitcnt lgkmcnt(4)\n\t" \
    "v_smfmac_f32_16x16x64_bf16 v[244:247], v[212:215], v[144:151], v241\n\t" \
    "s_waitcnt lgkmcnt(1)\n\t" \
    "v_smfmac_f32_16x16x64_bf16 v[244:247], v[216:219], v[160:167], v241\n\t" \
    "s_waitcnt lgkmcnt(0)\n\t" \
    "v_smfmac_f32_16x16x64_bf16 v[244:247], v[220:223], v[184:191], v241\n\t" \
    "v_smfmac_f32_16x16x64_bf16 v[248:251], v[208:211], v[112:119], v241\n\t" \
    "v_smfmac_f32_16x16x64_bf16 v[248:251], v[236:239], v[128:135], v241\n\t" \
    "v_smfmac_f32_16x16x64_bf16 v[248:251], v[212:215], v[152:159], v241\n\t" \
    "v_permlane32_swap_b32 v244, v246\n\t" \
    "v_permlane32_swap_b32 v245, v247\n\t" \
    "v_smfmac_f32_16x16x64_bf16 v[248:251], v[216:219], v[168:175], v241\n\t" \
    "v_add_f32 v244, v244, v246\n\t" \
    "v_add_f32 v245, v245, v247\n\t" \
    "v_smfmac_f32_16x16x64_bf16 v[248:251], v[220:223], v[192:199], v241\n\t" \
    "v_cvt_pk_bf16_f32 v244, v244, v245\n\t" \
    "ds_write_b16 %[dst], v244 offset:512\n\t" \
    "ds_write_b16_d16_hi %[dst], v244 offset:528\n\t" \
    "v_smfmac_f32_16x16x64_bf16 v[224:227], v[208:211], v[96:103], v241\n\t" \
    "v_smfmac_f32_16x16x64_bf16 v[224:227], v[236:239], v[200:207], v241\n\t" \
    "v_smfmac_f32_16x16x64_bf16 v[224:227], v[212:215], v[136:143], v241\n\t" \
    "v_permlane32_swap_b32 v248, v250\n\t" \
    "v_permlane32_swap_b32 v249, v251\n\t" \
    "v_smfmac_f32_16x16x64_bf16 v[224:227], v[216:219], v[228:235], v241\n\t" \
    "v_add_f32 v248, v248, v250\n\t" \
    "v_add_f32 v249, v249, v251\n\t" \
    "v_smfmac_f32_16x16x64_bf16 v[224:227], v[220:223], v[176:183], v241\n\t" \
    "v_cvt_pk_bf16_f32 v248, v248, v249\n\t" \
    "ds_write_b16 %[dst], v248 offset:1024\n\t" \
    "ds_write_b16_d16_hi %[dst], v248 offset:1040\n\t" \
    "s_nop 4\n\t" \
    "v_permlane32_swap_b32 v224, v226\n\t" \
    "v_permlane32_swap_b32 v225, v227\n\t" \
    "v_add_f32 v224, v224, v226\n\t" \
    "v_add_f32 v225, v225, v227\n\t" \
    "v_cvt_pk_bf16_f32 v224, v224, v225\n\t" \
    "ds_write_b16 %[dst], v224 offset:0\n\t" \
    "ds_write_b16_d16_hi %[dst], v224 offset:16\n\t"
#define ASM_CRIT1_N \
    "v_bfe_i32 v241, %[ab], 7, 1\n\t" \
    "v_and_b32 v241, 0xaaaa, v241\n\t" \
    "v_or_b32 v241, 0x4444, v241\n\t" \
    "v_xor_b32 v212, 0x400, %[ab]\n\t" \
    "v_xor_b32 v216, 0x800, %[ab]\n\t" \
    "v_xor_b32 v242, 0xc00, %[ab]\n\t" \
    "v_xor_b32 v240, 0x1400, %[ab]\n\t" \
    "v_xor_b32 v236, 0x1000, %[ab]\n\t" \
    "s_waitcnt lgkmcnt(0)\n\t" \
    "s_barrier\n\t" ASM_ST \
    "ds_read_b128 v[208:211], %[ab] offset:0\n\t" \
    "ds_read_b128 v[212:215], v212 offset:0\n\t" \
    "ds_read_b128 v[216:219], v216 offset:0\n\t" \
    "ds_read_b128 v[220:223], v242 offset:0\n\t" \
    "ds_read_b128 v[236:239], v236 offset:0\n\t" \
    "s_waitcnt lgkmcnt(4)\n\t" \
    "v_smfmac_f32_16x16x64_bf16 v[224:227], v[208:211], v[200:207], v241\n\t" \
    "ds_read_b128 v[200:203], %[wl] offset:4096\n\t" \
    "ds_read_b128 v[204:207], %[wl] offset:5120\n\t" \
    "v_smfmac_f32_16x16x64_bf16 v[244:247], v[208:211], v[0:7], v241\n\t" \
    "v_smfmac_f32_16x16x64_bf16 v[248:251], v[208:211], v[8:15], v241\n\t" \
    "ds_read_b128 v[208:211], v240 offset:0\n\t" \
    "s_waitcnt lgkmcnt(6)\n\t" \
    "v_smfmac_f32_16x16x64_bf16 v[224:227], v[212:215], v[16:23], v241\n\t" \
    "v_smfmac_f32_16x16x64_bf16 v[244:247], v[212:215], v[24:31], v241\n\t" \
    "v_xor_b32 v240, 0x1800, %[ab]\n\t" \
    "v_smfmac_f32_16x16x64_bf16 v[248:251], v[212:215], v[32:39], v241\n\t" \
    "ds_read_b128 v[212:215], v240 offset:0\n\t" \
    "s_waitcnt lgkmcnt(6)\n\t" \
    "v_smfmac_f32_16x16x64_bf16 v[224:227], v[216:219], v[228:235], v241\n\t" \
    "ds_read_b128 v[228:231], %[wl] offset:6144\n\t" \
    "ds_read_b128 v[232:235], %[wl] offset:7168\n\t" \
    "v_smfmac_f32_16x16x64_bf16 v[244:247], v[216:219], v[40:47], v241\n\t" \
    "v_xor_b32 v240, 0x1c00, %[ab]\n\t" \
    "v_smfmac_f32_16x16x64_bf16 v[248:251], v[216:219], v[48:55], v241\n\t" \
    "ds_read_b128 v[216:219], v240 offset:0\n\t" \
    "s_waitcnt lgkmcnt(8)\n\t" \
    "v_smfmac_f32_16x16x64_bf16 v[224:227], v[220:223], v[56:63], v241\n\t" \
    "v_smfmac_f32_16x16x64_bf16 v[244:247], v[220:223], v[64:71], v241\n\t" \
    "v_and_b32 v240, 0x3ff, %[ab]\n\t" \
    "v_smfmac_f32_16x16x64_bf16 v[248:251], v[220:223], v[72:79], v241\n\t" \
    "ds_read_b128 v[220:223], v240 offset:8192\n\t" \
    "s_waitcnt lgkmcnt(6)\n\t" \
    "v_smfmac_f32_16x16x64_bf16 v[224:227], v[236:239], v[200:207], v241\n\t" \
    "ds_read_b128 v[200:203], %[wl] offset:8192\n\t" \
    "ds_read_b128 v[204:207], %[wl] offset:9216\n\t" \
    "v_smfmac_f32_16x16x64_bf16 v[244:247], v[236:239], v[80:87], v241\n\t" \
    "v_smfmac_f32_16x16x64_bf16 v[248:251], v[236:239], v[88:95], v241\n\t" \
    "ds_read_b128 v[236:239], v240 offset:9216\n\t" \
    "s_waitcnt lgkmcnt(8)\n\t" \
    "v_smfmac_f32_16x16x64_bf16 v[244:247], v[208:211], v[104:111], v241\n\t" \
    "s_waitcnt lgkmcnt(7)\n\t" \
    "v_smfmac_f32_16x16x64_bf16 v[244:247], v[212:215], v[120:127], v241\n\t" \
    "s_waitcnt lgkmcnt(4)\n\t" \
    "v_smfmac_f32_16x16x64_bf16 v[244:247], v[216:219], v[144:151], v241\n\t" \
    "s_waitcnt lgkmcnt(3)\n\t" \
    "v_smfmac_f32_16x16x64_bf16 v[244:247], v[220:223], v[160:167], v241\n\t" \
    "s_waitcnt lgkmcnt(0)\n\t" \
    "v_smfmac_f32_16x16x64_bf16 v[244:247], v[236:239], v[184:191], v241\n\t" \
    "v_smfmac_f32_16x16x64_bf16 v[248:251], v[208:211], v[112:119], v241\n\t" \
    "v_smfmac_f32_16x16x64_bf16 v[248:251], v[212:215], v[128:135], v241\n\t" \
    "v_smfmac_f32_16x16x64_bf16 v[248:251], v[216:219], v[152:159], v241\n\t" \
    "v_permlane32_swap_b32 v244, v246\n\t" \
    "v_permlane32_swap_b32 v245, v247\n\t" \
    "v_smfmac_f32_16x16x64_bf16 v[248:251], v[220:223], v[168:175], v241\n\t" \
    "v_add_f32 v244, v244, v246\n\t" \
    "v_add_f32 v245, v245, v247\n\t" \
    "v_smfmac_f32_16x16x64_bf16 v[248:251], v[236:239], v[192:199], v241\n\t" \
    "v_cvt_pk_bf16_f32 v244, v244, v245\n\t" \
    "ds_write_b16 %[dst], v244 offset:512\n\t" \
    "ds_write_b16_d16_hi %[dst], v244 offset:528\n\t" \
    "v_smfmac_f32_16x16x64_bf16 v[224:227], v[208:211], v[96:103], v241\n\t" \
    "v_smfmac_f32_16x16x64_bf16 v[224:227], v[212:215], v[228:235], v241\n\t" \
    "v_smfmac_f32_16x16x64_bf16 v[224:227], v[216:219], v[136:143], v241\n\t" \
    "v_permlane32_swap_b32 v248, v250\n\t" \
    "v_permlane32_swap_b32 v249, v251\n\t" \
    "v_smfmac_f32_16x16x64_bf16 v[224:227], v[220:223], v[200:207], v241\n\t" \
    "v_add_f32 v248, v248, v250\n\t" \
    "v_add_f32 v249, v249, v251\n\t" \
    "v_smfmac_f32_16x16x64_bf16 v[224:227], v[236:239], v[176:183], v241\n\t" \
    "v_cvt_pk_bf16_f32 v248, v248, v249\n\t" \
    "ds_write_b16 %[dst], v248 offset:1024\n\t" \
    "ds_write_b16_d16_hi %[dst], v248 offset:1040\n\t" \
    "s_nop 4\n\t" \
    "v_permlane32_swap_b32 v224, v226\n\t" \
    "v_permlane32_swap_b32 v225, v227\n\t" \
    "v_add_f32 v224, v224, v226\n\t" \
    "v_add_f32 v225, v225, v227\n\t" \
    "v_cvt_pk_bf16_f32 v224, v224, v225\n\t" \
    "ds_write_b16 %[dst], v224 offset:0\n\t" \
    "ds_write_b16_d16_hi %[dst], v224 offset:16\n\t"
#define ASM_CRIT1_V3 \
    "s_branch .Lc0%=\n\t" \
    ".Lcr%=:\n\t" \
    "s_cmp_eq_u32 %[n], 0\n\t" \
    "s_cbranch_scc1 .Lcb%=\n\t" \
    "s_sub_u32 %[n], %[n], 1\n\t" \
    "v_max_u32 v243, v208, v209\n\t" \
    "v_max3_u32 v243, v210, v211, v243\n\t" \
    "v_cmp_eq_u32 vcc, -1, v243\n\t" \
    "s_cbranch_vccz .Lcs0_%=\n\t" \
    "buffer_load_dwordx4 v[208:211], %[ab], %[rs], %[s0] offen sc1\n\t" \
    ".Lcs0_%=:\n\t" \
    "v_max_u32 v243, v236, v237\n\t" \
    "v_max3_u32 v243, v238, v239, v243\n\t" \
    "v_cmp_eq_u32 vcc, -1, v243\n\t" \
    "s_cbranch_vccz .Lcs1_%=\n\t" \
    "buffer_load_dwordx4 v[236:239], %[ab], %[rs], %[s1] offen sc1\n\t" \
    ".Lcs1_%=:\n\t" \
    "v_max_u32 v243, v220, v221\n\t" \
    "v_max3_u32 v243, v222, v223, v243\n\t" \
    "v_cmp_eq_u32 vcc, -1, v243\n\t" \
    "s_cbranch_vccz .Lca%=\n\t" \
    "buffer_load_dwordx4 v[220:223], %[ab], %[rs], %[s2] offen sc1\n\t" \
    "s_branch .Lca%=\n\t" \
    ".Lc0%=:\n\t" \
    "buffer_load_dwordx4 v[208:211], %[ab], %[rs], %[s0] offen sc1\n\t" \
    "buffer_load_dwordx4 v[236:239], %[ab], %[rs], %[s1] offen sc1\n\t" \
    "buffer_load_dwordx4 v[220:223], %[ab], %[rs], %[s2] offen sc1\n\t" \
    "v_bfe_i32 v241, %[ab], 7, 1\n\t" \
    "v_and_b32 v241, 0xaaaa, v241\n\t" \
    "v_or_b32 v241, 0x4444, v241\n\t" \
    "v_xor_b32 v212, 0x400, %[ab]\n\t" \
    "v_xor_b32 v216, 0x800, %[ab]\n\t" \
    "v_xor_b32 v242, 0xc00, %[ab]\n\t" \
    "v_xor_b32 v240, 0x1400, %[ab]\n\t" \
    ".Lca%=:\n\t" \
    "s_waitcnt vmcnt(0)\n\t" \
    "ds_write_b128 %[ab], v[208:211] offset:0\n\t" \
    "ds_write_b128 %[ab], v[236:239] offset:4096\n\t" \
    "ds_write_b128 %[ab], v[220:223] offset:8192\n\t" \
    "v_max_u32 v243, v208, v209\n\t" \
    "v_max3_u32 v243, v210, v211, v243\n\t" \
    "v_max3_u32 v243, v236, v237, v243\n\t" \
    "v_max3_u32 v243, v238, v239, v243\n\t" \
    "v_max3_u32 v243, v220, v221, v243\n\t" \
    "v_max3_u32 v243, v222, v223, v243\n\t" \
    "v_cmp_eq_u32 vcc, -1, v243\n\t" \
    "s_cbranch_vccnz .Lcr%=\n\t" \
    ".Lcb%=:\n\t" \
    PRIUP \
    ASM_ST2 \
    "v_smfmac_f32_16x16x64_bf16 v[224:227], v[208:211], v[200:207], v241\n\t" \
    "ds_read_b128 v[200:203], %[wl] offset:4096\n\t" \
    "ds_read_b128 v[204:207], %[wl] offset:5120\n\t" \
    "v_smfmac_f32_16x16x64_bf16 v[244:247], v[208:211], v[0:7], v241\n\t" \
    "v_smfmac_f32_16x16x64_bf16 v[248:251], v[208:211], v[8:15], v241\n\t" \
    "s_waitcnt lgkmcnt(2)\n\t" \
    "buffer_load_dword %[fv], %[fv], %[rs], %[fs] offen sc1\n\t" \
    "s_barrier\n\t" ASM_ST \
    "ds_read_b128 v[212:215], v212 offset:0\n\t" \
    "ds_read_b128 v[216:219], v216 offset:0\n\t" \
    "ds_read_b128 v[220:223], v242 offset:0\n\t" \
    "ds_read_b128 v[208:211], v240 offset:0\n\t" \
    "v_smfmac_f32_16x16x64_bf16 v[244:247], v[236:239], v[80:87], v241\n\t" \
    "v_xor_b32 v240, 0x1800, %[ab]\n\t" \
    "v_smfmac_f32_16x16x64_bf16 v[248:251], v[236:239], v[88:95], v241\n\t" \
    "s_waitcnt lgkmcnt(4)\n\t" \
    "v_smfmac_f32_16x16x64_bf16 v[224:227], v[236:239], v[200:207], v241\n\t" \
    "ds_read_b128 v[200:203], %[wl] offset:6144\n\t" \
    "ds_read_b128 v[204:207], %[wl] offset:7168\n\t" \
    "ds_read_b128 v[236:239], v240 offset:0\n\t" \
    "s_waitcnt lgkmcnt(6)\n\t" \
    "v_smfmac_f32_16x16x64_bf16 v[224:227], v[212:215], v[16:23], v241\n\t" \
    "v_smfmac_f32_16x16x64_bf16 v[244:247], v[212:215], v[24:31], v241\n\t" \
    "v_xor_b32 v240, 0x1c00, %[ab]\n\t" \
    "v_smfmac_f32_16x16x64_bf16 v[248:251], v[212:215], v[32:39], v241\n\t" \
    "ds_read_b128 v[212:215], v240 offset:0\n\t" \
    "s_waitcnt lgkmcnt(6)\n\t" \
    "v_smfmac_f32_16x16x64_bf16 v[224:227], v[216:219], v[228:235], v241\n\t" \
    "ds_read_b128 v[228:231], %[wl] offset:8192\n\t" \
    "ds_read_b128 v[232:235], %[wl] offset:9216\n\t" \
    "v_smfmac_f32_16x16x64_bf16 v[244:247], v[216:219], v[40:47], v241\n\t" \
    "v_and_b32 v240, 0x3ff, %[ab]\n\t" \
    "v_smfmac_f32_16x16x64_bf16 v[248:251], v[216:219], v[48:55], v241\n\t" \
    "ds_read_b128 v[216:219], v240 offset:8192\n\t" \
    "s_waitcnt lgkmcnt(8)\n\t" \
    "v_smfmac_f32_16x16x64_bf16 v[224:227], v[220:223], v[56:63], v241\n\t" \
    "v_smfmac_f32_16x16x64_bf16 v[244:247], v[220:223], v[64:71], v241\n\t" \
    "v_smfmac_f32_16x16x64_bf16 v[248:251], v[220:223], v[72:79], v241\n\t" \
    "ds_read_b128 v[220:223], v240 offset:9216\n\t" \
    "s_waitcnt lgkmcnt(8)\n\t" \
    "v_smfmac_f32_16x16x64_bf16 v[244:247], v[208:211], v[104:111], v241\n\t" \
    "s_waitcnt lgkmcnt(5)\n\t" \
    "v_smfmac_f32_16x16x64_bf16 v[244:247], v[236:239], v[120:127], v241\n\t" \
    "s_waitcnt lgkmcnt(4)\n\t" \
    "v_smfmac_f32_16x16x64_bf16 v[244:247], v[212:215], v[144:151], v241\n\t" \
    "s_waitcnt lgkmcnt(1)\n\t" \
    "v_smfmac_f32_16x16x64_bf16 v[244:247], v[216:219], v[160:167], v241\n\t" \
    "s_waitcnt lgkmcnt(0)\n\t" \
    "v_smfmac_f32_16x16x64_bf16 v[244:247], v[220:223], v[184:191], v241\n\t" \
    "v_smfmac_f32_16x16x64_bf16 v[248:251], v[208:211], v[112:119], v241\n\t" \
    "v_smfmac_f32_16x16x64_bf16 v[248:251], v[236:239], v[128:135], v241\n\t" \
    "v_smfmac_f32_16x16x64_bf16 v[248:251], v[212:215], v[152:159], v241\n\t" \
    "v_permlane32_swap_b32 v244, v246\n\t" \
    "v_permlane32_swap_b32 v245, v247\n\t" \
    "v_smfmac_f32_16x16x64_bf16 v[248:251], v[216:219], v[168:175], v241\n\t" \
    "v_add_f32 v244, v244, v246\n\t" \
    "v_add_f32 v245, v245, v247\n\t" \
    "v_smfmac_f32_16x16x64_bf16 v[248:251], v[220:223], v[192:199], v241\n\t" \
    "v_cvt_pk_bf16_f32 v244, v244, v245\n\t" \
    "ds_write_b16 %[dst], v244 offset:512\n\t" \
    "ds_write_b16_d16_hi %[dst], v244 offset:528\n\t" \
    "v_smfmac_f32_16x16x64_bf16 v[224:227], v[208:211], v[96:103], v241\n\t" \
    "v_smfmac_f32_16x16x64_bf16 v[224:227], v[236:239], v[200:207], v241\n\t" \
    "v_smfmac_f32_16x16x64_bf16 v[224:227], v[212:215], v[136:143], v241\n\t" \
    "v_permlane32_swap_b32 v248, v250\n\t" \
    "v_permlane32_swap_b32 v249, v251\n\t" \
    "v_smfmac_f32_16x16x64_bf16 v[224:227], v[216:219], v[228:235], v241\n\t" \
    "v_add_f32 v248, v248, v250\n\t" \
    "v_add_f32 v249, v249, v251\n\t" \
    "v_smfmac_f32_16x16x64_bf16 v[224:227], v[220:223], v[176:183], v241\n\t" \
    "v_cvt_pk_bf16_f32 v248, v248, v249\n\t" \
    "ds_write_b16 %[dst], v248 offset:1024\n\t" \
    "ds_write_b16_d16_hi %[dst], v248 offset:1040\n\t" \
    "s_nop 4\n\t" \
    "v_permlane32_swap_b32 v224, v226\n\t" \
    "v_permlane32_swap_b32 v225, v227\n\t" \
    "v_add_f32 v224, v224, v226\n\t" \
    "v_add_f32 v225, v225, v227\n\t" \
    "v_cvt_pk_bf16_f32 v224, v224, v225\n\t" \
    "ds_write_b16 %[dst], v224 offset:0\n\t" \
    "ds_write_b16_d16_hi %[dst], v224 offset:16\n\t"
#define ASM_CRIT1_V2 \
    "s_branch .Lc0%=\n\t" \
    ".Lcr%=:\n\t" \
    "s_cmp_eq_u32 %[n], 0\n\t" \
    "s_cbranch_scc1 .Lcb%=\n\t" \
    "s_sub_u32 %[n], %[n], 1\n\t" \
    "v_max_u32 v243, v208, v209\n\t" \
    "v_max3_u32 v243, v210, v211, v243\n\t" \
    "v_cmp_eq_u32 vcc, -1, v243\n\t" \
    "s_cbranch_vccz .Lcs0_%=\n\t" \
    "buffer_load_dwordx4 v[208:211], %[ab], %[rs], %[s0] offen sc1\n\t" \
    ".Lcs0_%=:\n\t" \
    "v_max_u32 v243, v236, v237\n\t" \
    "v_max3_u32 v243, v238, v239, v243\n\t" \
    "v_cmp_eq_u32 vcc, -1, v243\n\t" \
    "s_cbranch_vccz .Lca%=\n\t" \
    "buffer_load_dwordx4 v[236:239], %[ab], %[rs], %[s1] offen sc1\n\t" \
    "s_branch .Lca%=\n\t" \
    ".Lc0%=:\n\t" \
    "buffer_load_dwordx4 v[208:211], %[ab], %[rs], %[s0] offen sc1\n\t" \
    "buffer_load_dwordx4 v[236:239], %[ab], %[rs], %[s1] offen sc1\n\t" \
    "v_bfe_i32 v241, %[ab], 7, 1\n\t" \
    "v_and_b32 v241, 0xaaaa, v241\n\t" \
    "v_or_b32 v241, 0x4444, v241\n\t" \
    "v_xor_b32 v212, 0x400, %[ab]\n\t" \
    "v_xor_b32 v216, 0x800, %[ab]\n\t" \
    "v_xor_b32 v242, 0xc00, %[ab]\n\t" \
    "v_xor_b32 v240, 0x1400, %[ab]\n\t" \
    ".Lca%=:\n\t" \
    "s_waitcnt vmcnt(0)\n\t" \
    "ds_write_b128 %[ab], v[208:211] offset:0\n\t" \
    "ds_write_b128 %[ab], v[236:239] offset:4096\n\t" \
    "v_max_u32 v243, v208, v209\n\t" \
    "v_max3_u32 v243, v210, v211, v243\n\t" \
    "v_max3_u32 v243, v236, v237, v243\n\t" \
    "v_max3_u32 v243, v238, v239, v243\n\t" \
    "v_cmp_eq_u32 vcc, -1, v243\n\t" \
    "s_cbranch_vccnz .Lcr%=\n\t" \
    ".Lcb%=:\n\t" \
    PRIUP \
    ASM_ST2 \
    "v_smfmac_f32_16x16x64_bf16 v[224:227], v[208:211], v[200:207], v241\n\t" \
    "ds_read_b128 v[200:203], %[wl] offset:4096\n\t" \
    "ds_read_b128 v[204:207], %[wl] offset:5120\n\t" \
    "v_smfmac_f32_16x16x64_bf16 v[244:247], v[208:211], v[0:7], v241\n\t" \
    "v_smfmac_f32_16x16x64_bf16 v[248:251], v[208:211], v[8:15], v241\n\t" \
    "s_waitcnt lgkmcnt(2)\n\t" \
    "buffer_load_dword %[fv], %[fv], %[rs], %[fs] offen sc1\n\t" \
    "s_barrier\n\t" ASM_ST \
    "ds_read_b128 v[212:215], v212 offset:0\n\t" \
    "ds_read_b128 v[216:219], v216 offset:0\n\t" \
    "ds_read_b128 v[220:223], v242 offset:0\n\t" \
    "ds_read_b128 v[208:211], v240 offset:0\n\t" \
    "v_smfmac_f32_16x16x64_bf16 v[244:247], v[236:239], v[80:87], v241\n\t" \
    "v_xor_b32 v240, 0x1800, %[ab]\n\t" \
    "v_smfmac_f32_16x16x64_bf16 v[248:251], v[236:239], v[88:95], v241\n\t" \
    "s_waitcnt lgkmcnt(4)\n\t" \
    "v_smfmac_f32_16x16x64_bf16 v[224:227], v[236:239], v[200:207], v241\n\t" \
    "ds_read_b128 v[200:203], %[wl] offset:6144\n\t" \
    "ds_read_b128 v[204:207], %[wl] offset:7168\n\t" \
    "ds_read_b128 v[236:239], v240 offset:0\n\t" \
    "s_waitcnt lgkmcnt(6)\n\t" \
    "v_smfmac_f32_16x16x64_bf16 v[224:227], v[212:215], v[16:23], v241\n\t" \
    "v_smfmac_f32_16x16x64_bf16 v[244:247], v[212:215], v[24:31], v241\n\t" \
    "v_xor_b32 v240, 0x1c00, %[ab]\n\t" \
    "v_smfmac_f32_16x16x64_bf16 v[248:251], v[212:215], v[32:39], v241\n\t" \
    "ds_read_b128 v[212:215], v240 offset:0\n\t" \
    "s_waitcnt lgkmcnt(6)\n\t" \
    "v_smfmac_f32_16x16x64_bf16 v[224:227], v[216:219], v[228:235], v241\n\t" \
    "ds_read_b128 v[228:231], %[wl] offset:8192\n\t" \
    "ds_read_b128 v[232:235], %[wl] offset:9216\n\t" \
    "v_smfmac_f32_16x16x64_bf16 v[244:247], v[216:219], v[40:47], v241\n\t" \
    "v_and_b32 v240, 0x3ff, %[ab]\n\t" \
    "v_smfmac_f32_16x16x64_bf16 v[248:251], v[216:219], v[48:55], v241\n\t" \
    "ds_read_b128 v[216:219], v240 offset:8192\n\t" \
    "s_waitcnt lgkmcnt(8)\n\t" \
    "v_smfmac_f32_16x16x64_bf16 v[224:227], v[220:223], v[56:63], v241\n\t" \
    "v_smfmac_f32_16x16x64_bf16 v[244:247], v[220:223], v[64:71], v241\n\t" \
    "v_smfmac_f32_16x16x64_bf16 v[248:251], v[220:223], v[72:79], v241\n\t" \
    "ds_read_b128 v[220:223], v240 offset:9216\n\t" \
    "s_waitcnt lgkmcnt(8)\n\t" \
    "v_smfmac_f32_16x16x64_bf16 v[244:247], v[208:211], v[104:111], v241\n\t" \
    "s_waitcnt lgkmcnt(5)\n\t" \
    "v_smfmac_f32_16x16x64_bf16 v[244:247], v[236:239], v[120:127], v241\n\t" \
    "s_waitcnt lgkmcnt(4)\n\t" \
    "v_smfmac_f32_16x16x64_bf16 v[244:247], v[212:215], v[144:151], v241\n\t" \
    "s_waitcnt lgkmcnt(1)\n\t" \
    "v_smfmac_f32_16x16x64_bf16 v[244:247], v[216:219], v[160:167], v241\n\t" \
    "s_waitcnt lgkmcnt(0)\n\t" \
    "v_smfmac_f32_16x16x64_bf16 v[244:247], v[220:223], v[184:191], v241\n\t" \
    "v_smfmac_f32_16x16x64_bf16 v[248:251], v[208:211], v[112:119], v241\n\t" \
    "v_smfmac_f32_16x16x64_bf16 v[248:251], v[236:239], v[128:135], v241\n\t" \
    "v_smfmac_f32_16x16x64_bf16 v[248:251], v[212:215], v[152:159], v241\n\t" \
    "v_permlane32_swap_b32 v244, v246\n\t" \
    "v_permlane32_swap_b32 v245, v247\n\t" \
    "v_smfmac_f32_16x16x64_bf16 v[248:251], v[216:219], v[168:175], v241\n\t" \
    "v_add_f32 v244, v244, v246\n\t" \
    "v_add_f32 v245, v245, v247\n\t" \
    "v_smfmac_f32_16x16x64_bf16 v[248:251], v[220:223], v[192:199], v241\n\t" \
    "v_cvt_pk_bf16_f32 v244, v244, v245\n\t" \
    "ds_write_b16 %[dst], v244 offset:512\n\t" \
    "ds_write_b16_d16_hi %[dst], v244 offset:528\n\t" \
    "v_smfmac_f32_16x16x64_bf16 v[224:227], v[208:211], v[96:103], v241\n\t" \
    "v_smfmac_f32_16x16x64_bf16 v[224:227], v[236:239], v[200:207], v241\n\t" \
    "v_smfmac_f32_16x16x64_bf16 v[224:227], v[212:215], v[136:143], v241\n\t" \
    "v_permlane32_swap_b32 v248, v250\n\t" \
    "v_permlane32_swap_b32 v249, v251\n\t" \
    "v_smfmac_f32_16x16x64_bf16 v[224:227], v[216:219], v[228:235], v241\n\t" \
    "v_add_f32 v248, v248, v250\n\t" \
    "v_add_f32 v249, v249, v251\n\t" \
    "v_smfmac_f32_16x16x64_bf16 v[224:227], v[220:223], v[176:183], v241\n\t" \
    "v_cvt_pk_bf16_f32 v248, v248, v249\n\t" \
    "ds_write_b16 %[dst], v248 offset:1024\n\t" \
    "ds_write_b16_d16_hi %[dst], v248 offset:1040\n\t" \
    "s_nop 4\n\t" \
    "v_permlane32_swap_b32 v224, v226\n\t" \
    "v_permlane32_swap_b32 v225, v227\n\t" \
    "v_add_f32 v224, v224, v226\n\t" \
    "v_add_f32 v225, v225, v227\n\t" \
    "v_cvt_pk_bf16_f32 v224, v224, v225\n\t" \
    "ds_write_b16 %[dst], v224 offset:0\n\t" \
    "ds_write_b16_d16_hi %[dst], v224 offset:16\n\t"
#define ASM_VOX \
    "s_waitcnt lgkmcnt(0)\n\t" \
    "ds_read_b128 v[224:227], %[va] offset:0\n\t" \
    "ds_read_b128 v[248:251], %[wl] offset:0\n\t" \
    "ds_read_b128 v[228:231], %[va] offset:6144\n\t" \
    "ds_read_b128 v[232:235], %[va] offset:2048\n\t" \
    "ds_read_b128 v[236:239], %[va] offset:8192\n\t" \
    "ds_read_b128 v[240:243], %[va] offset:3072\n\t" \
    "s_waitcnt lgkmcnt(4)\n\t" \
    "v_mfma_f32_16x16x32_bf16 v[200:203], v[224:227], v[248:251], 0\n\t" \
    "ds_read_b128 v[244:247], %[va] offset:9216\n\t" \
    "s_waitcnt lgkmcnt(4)\n\t" \
    "v_mfma_f32_16x16x32_bf16 v[204:207], v[228:231], v[248:251], 0\n\t" \
    "ds_read_b128 v[224:227], %[va] offset:1024\n\t" \
    "s_waitcnt lgkmcnt(4)\n\t" \
    "v_mfma_f32_16x16x32_bf16 v[200:203], v[232:235], v[0:3], v[200:203]\n\t" \
    "ds_read_b128 v[228:231], %[va] offset:7168\n\t" \
    "ds_read_b128 v[248:251], %[wl] offset:1024\n\t" \
    "s_waitcnt lgkmcnt(5)\n\t" \
    "v_mfma_f32_16x16x32_bf16 v[204:207], v[236:239], v[0:3], v[204:207]\n\t" \
    "ds_read_b128 v[232:235], %[va] offset:4096\n\t" \
    "s_waitcnt lgkmcnt(5)\n\t" \
    "v_mfma_f32_16x16x32_bf16 v[200:203], v[240:243], v[4:7], v[200:203]\n\t" \
    "ds_read_b128 v[236:239], %[va] offset:10240\n\t" \
    "s_waitcnt lgkmcnt(5)\n\t" \
    "v_mfma_f32_16x16x32_bf16 v[204:207], v[244:247], v[4:7], v[204:207]\n\t" \
    "ds_read_b128 v[240:243], %[va] offset:5120\n\t" \
    "s_waitcnt lgkmcnt(3)\n\t" \
    "v_mfma_f32_16x16x32_bf16 v[200:203], v[224:227], v[248:251], v[200:203]\n\t" \
    "ds_read_b128 v[244:247], %[va] offset:11264\n\t" \
    "v_mfma_f32_16x16x32_bf16 v[208:211], v[224:227], v[20:23], 0\n\t" \
    "v_mfma_f32_16x16x32_bf16 v[204:207], v[228:231], v[248:251], v[204:207]\n\t" \
    "ds_read_b128 v[224:227], %[va] offset:0\n\t" \
    "v_mfma_f32_16x16x32_bf16 v[212:215], v[228:231], v[20:23], 0\n\t" \
    "s_waitcnt lgkmcnt(4)\n\t" \
    "v_mfma_f32_16x16x32_bf16 v[200:203], v[232:235], v[8:11], v[200:203]\n\t" \
    "ds_read_b128 v[228:231], %[va] offset:6144\n\t" \
    "ds_read_b128 v[248:251], %[wl] offset:2048\n\t" \
    "v_mfma_f32_16x16x32_bf16 v[208:211], v[232:235], v[32:35], v[208:211]\n\t" \
    "s_waitcnt lgkmcnt(5)\n\t" \
    "v_mfma_f32_16x16x32_bf16 v[204:207], v[236:239], v[8:11], v[204:207]\n\t" \
    "ds_read_b128 v[232:235], %[va] offset:2048\n\t" \
    "v_mfma_f32_16x16x32_bf16 v[212:215], v[236:239], v[32:35], v[212:215]\n\t" \
    "s_waitcnt lgkmcnt(5)\n\t" \
    "v_mfma_f32_16x16x32_bf16 v[200:203], v[240:243], v[12:15], v[200:203]\n\t" \
    "ds_read_b128 v[236:239], %[va] offset:8192\n\t" \
    "v_mfma_f32_16x16x32_bf16 v[208:211], v[240:243], v[36:39], v[208:211]\n\t" \
    "s_waitcnt lgkmcnt(5)\n\t" \
    "v_mfma_f32_16x16x32_bf16 v[204:207], v[244:247], v[12:15], v[204:207]\n\t" \
    "ds_read_b128 v[240:243], %[va] offset:3072\n\t" \
    "v_mfma_f32_16x16x32_bf16 v[212:215], v[244:247], v[36:39], v[212:215]\n\t" \
    "s_waitcnt lgkmcnt(3)\n\t" \
    "v_mfma_f32_16x16x32_bf16 v[216:219], v[224:227], v[248:251], 0\n\t" \
    "ds_read_b128 v[244:247], %[va] offset:9216\n\t" \
    "v_mfma_f32_16x16x32_bf16 v[208:211], v[224:227], v[16:19], v[208:211]\n\t" \
    "v_mfma_f32_16x16x32_bf16 v[220:223], v[228:231], v[248:251], 0\n\t" \
    "ds_read_b128 v[224:227], %[va] offset:1024\n\t" \
    "v_mfma_f32_16x16x32_bf16 v[212:215], v[228:231], v[16:19], v[212:215]\n\t" \
    "v_cvt_pk_bf16_f32 v200, v200, v201\n\t" \
    "s_waitcnt lgkmcnt(4)\n\t" \
    "v_mfma_f32_16x16x32_bf16 v[216:219], v[232:235], v[40:43], v[216:219]\n\t" \
    "ds_read_b128 v[228:231], %[va] offset:7168\n\t" \
    "ds_read_b128 v[248:251], %[wl] offset:3072\n\t" \
    "v_cvt_pk_bf16_f32 v201, v202, v203\n\t" \
    "v_mfma_f32_16x16x32_bf16 v[208:211], v[232:235], v[24:27], v[208:211]\n\t" \
    "v_cvt_pk_bf16_f32 v202, v204, v205\n\t" \
    "s_waitcnt lgkmcnt(5)\n\t" \
    "v_mfma_f32_16x16x32_bf16 v[220:223], v[236:239], v[40:43], v[220:223]\n\t" \
    "ds_read_b128 v[232:235], %[va] offset:4096\n\t" \
    "v_cvt_pk_bf16_f32 v203, v206, v207\n\t" \
    "v_mfma_f32_16x16x32_bf16 v[212:215], v[236:239], v[24:27], v[212:215]\n\t" \
    "buffer_store_dwordx4 v[200:203], %[l16], %[rd], %[so] offen sc1\n\t" \
    "s_waitcnt lgkmcnt(5)\n\t" \
    "v_mfma_f32_16x16x32_bf16 v[216:219], v[240:243], v[44:47], v[216:219]\n\t" \
    "ds_read_b128 v[236:239], %[va] offset:10240\n\t" \
    "s_add_u32 %[so], %[so], 0x10000\n\t" \
    "v_mfma_f32_16x16x32_bf16 v[208:211], v[240:243], v[28:31], v[208:211]\n\t" \
    "s_waitcnt lgkmcnt(5)\n\t" \
    "v_mfma_f32_16x16x32_bf16 v[220:223], v[244:247], v[44:47], v[220:223]\n\t" \
    "ds_read_b128 v[240:243], %[va] offset:5120\n\t" \
    "v_mfma_f32_16x16x32_bf16 v[212:215], v[244:247], v[28:31], v[212:215]\n\t" \
    "s_waitcnt lgkmcnt(3)\n\t" \
    "v_mfma_f32_16x16x32_bf16 v[216:219], v[224:227], v[248:251], v[216:219]\n\t" \
    "ds_read_b128 v[244:247], %[va] offset:11264\n\t" \
    "v_mfma_f32_16x16x32_bf16 v[200:203], v[224:227], v[60:63], 0\n\t" \
    "v_mfma_f32_16x16x32_bf16 v[220:223], v[228:231], v[248:251], v[220:223]\n\t" \
    "ds_read_b128 v[224:227], %[va] offset:0\n\t" \
    "v_mfma_f32_16x16x32_bf16 v[204:207], v[228:231], v[60:63], 0\n\t" \
    "v_cvt_pk_bf16_f32 v208, v208, v209\n\t" \
    "s_waitcnt lgkmcnt(4)\n\t" \
    "v_mfma_f32_16x16x32_bf16 v[216:219], v[232:235], v[48:51], v[216:219]\n\t" \
    "ds_read_b128 v[228:231], %[va] offset:6144\n\t" \
    "ds_read_b128 v[248:251], %[wl] offset:4096\n\t" \
    "v_cvt_pk_bf16_f32 v209, v210, v211\n\t" \
    "v_mfma_f32_16x16x32_bf16 v[200:203], v[232:235], v[72:75], v[200:203]\n\t" \
    "v_cvt_pk_bf16_f32 v210, v212, v213\n\t" \
    "s_waitcnt lgkmcnt(5)\n\t" \
    "v_mfma_f32_16x16x32_bf16 v[220:223], v[236:239], v[48:51], v[220:223]\n\t" \
    "ds_read_b128 v[232:235], %[va] offset:2048\n\t" \
    "v_cvt_pk_bf16_f32 v211, v214, v215\n\t" \
    "v_mfma_f32_16x16x32_bf16 v[204:207], v[236:239], v[72:75], v[204:207]\n\t" \
    "buffer_store_dwordx4 v[208:211], %[l16], %[rd], %[so] offen sc1\n\t" \
    "s_waitcnt lgkmcnt(5)\n\t" \
    "v_mfma_f32_16x16x32_bf16 v[216:219], v[240:243], v[52:55], v[216:219]\n\t" \
    "ds_read_b128 v[236:239], %[va] offset:8192\n\t" \
    "s_add_u32 %[so], %[so], 0x10000\n\t" \
    "v_mfma_f32_16x16x32_bf16 v[200:203], v[240:243], v[76:79], v[200:203]\n\t" \
    "s_waitcnt lgkmcnt(5)\n\t" \
    "v_mfma_f32_16x16x32_bf16 v[220:223], v[244:247], v[52:55], v[220:223]\n\t" \
    "ds_read_b128 v[240:243], %[va] offset:3072\n\t" \
    "v_mfma_f32_16x16x32_bf16 v[204:207], v[244:247], v[76:79], v[204:207]\n\t" \
    "s_waitcnt lgkmcnt(3)\n\t" \
    "v_mfma_f32_16x16x32_bf16 v[208:211], v[224:227], v[248:251], 0\n\t" \
    "ds_read_b128 v[244:247], %[va] offset:9216\n\t" \
    "v_mfma_f32_16x16x32_bf16 v[200:203], v[224:227], v[56:59], v[200:203]\n\t" \
    "v_mfma_f32_16x16x32_bf16 v[212:215], v[228:231], v[248:251], 0\n\t" \
    "ds_read_b128 v[224:227], %[va] offset:1024\n\t" \
    "v_mfma_f32_16x16x32_bf16 v[204:207], v[228:231], v[56:59], v[204:207]\n\t" \
    "v_cvt_pk_bf16_f32 v216, v216, v217\n\t" \
    "s_waitcnt lgkmcnt(4)\n\t" \
    "v_mfma_f32_16x16x32_bf16 v[208:211], v[232:235], v[80:83], v[208:211]\n\t" \
    "ds_read_b128 v[228:231], %[va] offset:7168\n\t" \
    "ds_read_b128 v[248:251], %[wl] offset:5120\n\t" \
    "v_cvt_pk_bf16_f32 v217, v218, v219\n\t" \
    "v_mfma_f32_16x16x32_bf16 v[200:203], v[232:235], v[64:67], v[200:203]\n\t" \
    "v_cvt_pk_bf16_f32 v218, v220, v221\n\t" \
    "s_waitcnt lgkmcnt(5)\n\t" \
    "v_mfma_f32_16x16x32_bf16 v[212:215], v[236:239], v[80:83], v[212:215]\n\t" \
    "ds_read_b128 v[232:235], %[va] offset:4096\n\t" \
    "v_cvt_pk_bf16_f32 v219, v222, v223\n\t" \
    "v_mfma_f32_16x16x32_bf16 v[204:207], v[236:239], v[64:67], v[204:207]\n\t" \
    "buffer_store_dwordx4 v[216:219], %[l16], %[rd], %[so] offen sc1\n\t" \
    "s_waitcnt lgkmcnt(5)\n\t" \
    "v_mfma_f32_16x16x32_bf16 v[208:211], v[240:243], v[84:87], v[208:211]\n\t" \
    "ds_read_b128 v[236:239], %[va] offset:10240\n\t" \
    "s_add_u32 %[so], %[so], 0x10000\n\t" \
    "v_mfma_f32_16x16x32_bf16 v[200:203], v[240:243], v[68:71], v[200:203]\n\t" \
    "s_waitcnt lgkmcnt(5)\n\t" \
    "v_mfma_f32_16x16x32_bf16 v[212:215], v[244:247], v[84:87], v[212:215]\n\t" \
    "ds_read_b128 v[240:243], %[va] offset:5120\n\t" \
    "v_mfma_f32_16x16x32_bf16 v[204:207], v[244:247], v[68:71], v[204:207]\n\t" \
    "s_waitcnt lgkmcnt(3)\n\t" \
    "v_mfma_f32_16x16x32_bf16 v[208:211], v[224:227], v[248:251], v[208:211]\n\t" \
    "ds_read_b128 v[244:247], %[va] offset:11264\n\t" \
    "v_mfma_f32_16x16x32_bf16 v[216:219], v[224:227], v[100:103], 0\n\t" \
    "v_mfma_f32_16x16x32_bf16 v[212:215], v[228:231], v[248:251], v[212:215]\n\t" \
    "ds_read_b128 v[224:227], %[va] offset:0\n\t" \
    "v_mfma_f32_16x16x32_bf16 v[220:223], v[228:231], v[100:103], 0\n\t" \
    "v_cvt_pk_bf16_f32 v200, v200, v201\n\t" \
    "s_waitcnt lgkmcnt(4)\n\t" \
    "v_mfma_f32_16x16x32_bf16 v[208:211], v[232:235], v[88:91], v[208:211]\n\t" \
    "ds_read_b128 v[228:231], %[va] offset:6144\n\t" \
    "ds_read_b128 v[248:251], %[wl] offset:6144\n\t" \
    "v_cvt_pk_bf16_f32 v201, v202, v203\n\t" \
    "v_mfma_f32_16x16x32_bf16 v[216:219], v[232:235], v[112:115], v[216:219]\n\t" \
    "v_cvt_pk_bf16_f32 v202, v204, v205\n\t" \
    "s_waitcnt lgkmcnt(5)\n\t" \
    "v_mfma_f32_16x16x32_bf16 v[212:215], v[236:239], v[88:91], v[212:215]\n\t" \
    "ds_read_b128 v[232:235], %[va] offset:2048\n\t" \
    "v_cvt_pk_bf16_f32 v203, v206, v207\n\t" \
    "v_mfma_f32_16x16x32_bf16 v[220:223], v[236:239], v[112:115], v[220:223]\n\t" \
    "buffer_store_dwordx4 v[200:203], %[l16], %[rd], %[so] offen sc1\n\t" \
    "s_waitcnt lgkmcnt(5)\n\t" \
    "v_mfma_f32_16x16x32_bf16 v[208:211], v[240:243], v[92:95], v[208:211]\n\t" \
    "ds_read_b128 v[236:239], %[va] offset:8192\n\t" \
    "s_add_u32 %[so], %[so], 0x10000\n\t" \
    "v_mfma_f32_16x16x32_bf16 v[216:219], v[240:243], v[116:119], v[216:219]\n\t" \
    "s_waitcnt lgkmcnt(5)\n\t" \
    "v_mfma_f32_16x16x32_bf16 v[212:215], v[244:247], v[92:95], v[212:215]\n\t" \
    "ds_read_b128 v[240:243], %[va] offset:3072\n\t" \
    "v_mfma_f32_16x16x32_bf16 v[220:223], v[244:247], v[116:119], v[220:223]\n\t" \
    "s_waitcnt lgkmcnt(3)\n\t" \
    "v_mfma_f32_16x16x32_bf16 v[200:203], v[224:227], v[248:251], 0\n\t" \
    "ds_read_b128 v[244:247], %[va] offset:9216\n\t" \
    "v_mfma_f32_16x16x32_bf16 v[216:219], v[224:227], v[96:99], v[216:219]\n\t" \
    "v_mfma_f32_16x16x32_bf16 v[204:207], v[228:231], v[248:251], 0\n\t" \
    "ds_read_b128 v[224:227], %[va] offset:1024\n\t" \
    "v_mfma_f32_16x16x32_bf16 v[220:223], v[228:231], v[96:99], v[220:223]\n\t" \
    "v_cvt_pk_bf16_f32 v208, v208, v209\n\t" \
    "s_waitcnt lgkmcnt(4)\n\t" \
    "v_mfma_f32_16x16x32_bf16 v[200:203], v[232:235], v[120:123], v[200:203]\n\t" \
    "ds_read_b128 v[228:231], %[va] offset:7168\n\t" \
    "ds_read_b128 v[248:251], %[wl] offset:7168\n\t" \
    "v_cvt_pk_bf16_f32 v209, v210, v211\n\t" \
    "v_mfma_f32_16x16x32_bf16 v[216:219], v[232:235], v[104:107], v[216:219]\n\t" \
    "v_cvt_pk_bf16_f32 v210, v212, v213\n\t" \
    "s_waitcnt lgkmcnt(5)\n\t" \
    "v_mfma_f32_16x16x32_bf16 v[204:207], v[236:239], v[120:123], v[204:207]\n\t" \
    "ds_read_b128 v[232:235], %[va] offset:4096\n\t" \
    "v_cvt_pk_bf16_f32 v211, v214, v215\n\t" \
    "v_mfma_f32_16x16x32_bf16 v[220:223], v[236:239], v[104:107], v[220:223]\n\t" \
    "buffer_store_dwordx4 v[208:211], %[l16], %[rd], %[so] offen sc1\n\t" \
    "s_waitcnt lgkmcnt(5)\n\t" \
    "v_mfma_f32_16x16x32_bf16 v[200:203], v[240:243], v[124:127], v[200:203]\n\t" \
    "ds_read_b128 v[236:239], %[va] offset:10240\n\t" \
    "s_add_u32 %[so], %[so], 0x10000\n\t" \
    "v_mfma_f32_16x16x32_bf16 v[216:219], v[240:243], v[108:111], v[216:219]\n\t" \
    "s_waitcnt lgkmcnt(5)\n\t" \
    "v_mfma_f32_16x16x32_bf16 v[204:207], v[244:247], v[124:127], v[204:207]\n\t" \
    "ds_read_b128 v[240:243], %[va] offset:5120\n\t" \
    "v_mfma_f32_16x16x32_bf16 v[220:223], v[244:247], v[108:111], v[220:223]\n\t" \
    "s_waitcnt lgkmcnt(3)\n\t" \
    "v_mfma_f32_16x16x32_bf16 v[200:203], v[224:227], v[248:251], v[200:203]\n\t" \
    "ds_read_b128 v[244:247], %[va] offset:11264\n\t" \
    "v_mfma_f32_16x16x32_bf16 v[208:211], v[224:227], v[140:143], 0\n\t" \
    "v_mfma_f32_16x16x32_bf16 v[204:207], v[228:231], v[248:251], v[204:207]\n\t" \
    "ds_read_b128 v[224:227], %[va] offset:0\n\t" \
    "v_mfma_f32_16x16x32_bf16 v[212:215], v[228:231], v[140:143], 0\n\t" \
    "v_cvt_pk_bf16_f32 v216, v216, v217\n\t" \
    "s_waitcnt lgkmcnt(4)\n\t" \
    "v_mfma_f32_16x16x32_bf16 v[200:203], v[232:235], v[128:131], v[200:203]\n\t" \
    "ds_read_b128 v[228:231], %[va] offset:6144\n\t" \
    "ds_read_b128 v[248:251], %[wl] offset:8192\n\t" \
    "v_cvt_pk_bf16_f32 v217, v218, v219\n\t" \
    "v_mfma_f32_16x16x32_bf16 v[208:211], v[232:235], v[152:155], v[208:211]\n\t" \
    "v_cvt_pk_bf16_f32 v218, v220, v221\n\t" \
    "s_waitcnt lgkmcnt(5)\n\t" \
    "v_mfma_f32_16x16x32_bf16 v[204:207], v[236:239], v[128:131], v[204:207]\n\t" \
    "ds_read_b128 v[232:235], %[va] offset:2048\n\t" \
    "v_cvt_pk_bf16_f32 v219, v222, v223\n\t" \
    "v_mfma_f32_16x16x32_bf16 v[212:215], v[236:239], v[152:155], v[212:215]\n\t" \
    "buffer_store_dwordx4 v[216:219], %[l16], %[rd], %[so] offen sc1\n\t" \
    "s_waitcnt lgkmcnt(5)\n\t" \
    "v_mfma_f32_16x16x32_bf16 v[200:203], v[240:243], v[132:135], v[200:203]\n\t" \
    "ds_read_b128 v[236:239], %[va] offset:8192\n\t" \
    "s_add_u32 %[so], %[so], 0x10000\n\t" \
    "v_mfma_f32_16x16x32_bf16 v[208:211], v[240:243], v[156:159], v[208:211]\n\t" \
    "s_waitcnt lgkmcnt(5)\n\t" \
    "v_mfma_f32_16x16x32_bf16 v[204:207], v[244:247], v[132:135], v[204:207]\n\t" \
    "ds_read_b128 v[240:243], %[va] offset:3072\n\t" \
    "v_mfma_f32_16x16x32_bf16 v[212:215], v[244:247], v[156:159], v[212:215]\n\t" \
    "s_waitcnt lgkmcnt(3)\n\t" \
    "v_mfma_f32_16x16x32_bf16 v[216:219], v[224:227], v[248:251], 0\n\t" \
    "ds_read_b128 v[244:247], %[va] offset:9216\n\t" \
    "v_mfma_f32_16x16x32_bf16 v[208:211], v[224:227], v[136:139], v[208:211]\n\t" \
    "v_mfma_f32_16x16x32_bf16 v[220:223], v[228:231], v[248:251], 0\n\t" \
    "ds_read_b128 v[224:227], %[va] offset:1024\n\t" \
    "v_mfma_f32_16x16x32_bf16 v[212:215], v[228:231], v[136:139], v[212:215]\n\t" \
    "v_cvt_pk_bf16_f32 v200, v200, v201\n\t" \
    "s_waitcnt lgkmcnt(4)\n\t" \
    "v_mfma_f32_16x16x32_bf16 v[216:219], v[232:235], v[160:163], v[216:219]\n\t" \
    "ds_read_b128 v[228:231], %[va] offset:7168\n\t" \
    "ds_read_b128 v[248:251], %[wl] offset:9216\n\t" \
    "v_cvt_pk_bf16_f32 v201, v202, v203\n\t" \
    "v_mfma_f32_16x16x32_bf16 v[208:211], v[232:235], v[144:147], v[208:211]\n\t" \
    "v_cvt_pk_bf16_f32 v202, v204, v205\n\t" \
    "s_waitcnt lgkmcnt(5)\n\t" \
    "v_mfma_f32_16x16x32_bf16 v[220:223], v[236:239], v[160:163], v[220:223]\n\t" \
    "ds_read_b128 v[232:235], %[va] offset:4096\n\t" \
    "v_cvt_pk_bf16_f32 v203, v206, v207\n\t" \
    "v_mfma_f32_16x16x32_bf16 v[212:215], v[236:239], v[144:147], v[212:215]\n\t" \
    "buffer_store_dwordx4 v[200:203], %[l16], %[rd], %[so] offen sc1\n\t" \
    "s_waitcnt lgkmcnt(5)\n\t" \
    "v_mfma_f32_16x16x32_bf16 v[216:219], v[240:243], v[164:167], v[216:219]\n\t" \
    "ds_read_b128 v[236:239], %[va] offset:10240\n\t" \
    "s_add_u32 %[so], %[so], 0x10000\n\t" \
    "v_mfma_f32_16x16x32_bf16 v[208:211], v[240:243], v[148:151], v[208:211]\n\t" \
    "s_waitcnt lgkmcnt(5)\n\t" \
    "v_mfma_f32_16x16x32_bf16 v[220:223], v[244:247], v[164:167], v[220:223]\n\t" \
    "ds_read_b128 v[240:243], %[va] offset:5120\n\t" \
    "v_mfma_f32_16x16x32_bf16 v[212:215], v[244:247], v[148:151], v[212:215]\n\t" \
    "s_waitcnt lgkmcnt(3)\n\t" \
    "v_mfma_f32_16x16x32_bf16 v[216:219], v[224:227], v[248:251], v[216:219]\n\t" \
    "ds_read_b128 v[244:247], %[va] offset:11264\n\t" \
    "v_mfma_f32_16x16x32_bf16 v[200:203], v[224:227], v[180:183], 0\n\t" \
    "v_mfma_f32_16x16x32_bf16 v[220:223], v[228:231], v[248:251], v[220:223]\n\t" \
    "ds_read_b128 v[224:227], %[va] offset:0\n\t" \
    "v_mfma_f32_16x16x32_bf16 v[204:207], v[228:231], v[180:183], 0\n\t" \
    "v_cvt_pk_bf16_f32 v208, v208, v209\n\t" \
    "s_waitcnt lgkmcnt(4)\n\t" \
    "v_mfma_f32_16x16x32_bf16 v[216:219], v[232:235], v[168:171], v[216:219]\n\t" \
    "ds_read_b128 v[228:231], %[va] offset:6144\n\t" \
    "v_cvt_pk_bf16_f32 v209, v210, v211\n\t" \
    "v_mfma_f32_16x16x32_bf16 v[200:203], v[232:235], v[192:195], v[200:203]\n\t" \
    "v_cvt_pk_bf16_f32 v210, v212, v213\n\t" \
    "s_waitcnt lgkmcnt(4)\n\t" \
    "v_mfma_f32_16x16x32_bf16 v[220:223], v[236:239], v[168:171], v[220:223]\n\t" \
    "ds_read_b128 v[232:235], %[va] offset:2048\n\t" \
    "v_cvt_pk_bf16_f32 v211, v214, v215\n\t" \
    "v_mfma_f32_16x16x32_bf16 v[204:207], v[236:239], v[192:195], v[204:207]\n\t" \
    "buffer_store_dwordx4 v[208:211], %[l16], %[rd], %[so] offen sc1\n\t" \
    "s_waitcnt lgkmcnt(4)\n\t" \
    "v_mfma_f32_16x16x32_bf16 v[216:219], v[240:243], v[172:175], v[216:219]\n\t" \
    "ds_read_b128 v[236:239], %[va] offset:8192\n\t" \
    "s_add_u32 %[so], %[so], 0x10000\n\t" \
    "v_mfma_f32_16x16x32_bf16 v[200:203], v[240:243], v[196:199], v[200:203]\n\t" \
    "s_waitcnt lgkmcnt(4)\n\t" \
    "v_mfma_f32_16x16x32_bf16 v[220:223], v[244:247], v[172:175], v[220:223]\n\t" \
    "ds_read_b128 v[240:243], %[va] offset:3072\n\t" \
    "v_mfma_f32_16x16x32_bf16 v[204:207], v[244:247], v[196:199], v[204:207]\n\t" \
    "s_waitcnt lgkmcnt(4)\n\t" \
    "v_mfma_f32_16x16x32_bf16 v[200:203], v[224:227], v[176:179], v[200:203]\n\t" \
    "ds_read_b128 v[244:247], %[va] offset:9216\n\t" \
    "s_waitcnt lgkmcnt(4)\n\t" \
    "v_mfma_f32_16x16x32_bf16 v[204:207], v[228:231], v[176:179], v[204:207]\n\t" \
    "s_waitcnt lgkmcnt(3)\n\t" \
    "v_mfma_f32_16x16x32_bf16 v[200:203], v[232:235], v[184:187], v[200:203]\n\t" \
    "s_waitcnt lgkmcnt(2)\n\t" \
    "v_mfma_f32_16x16x32_bf16 v[204:207], v[236:239], v[184:187], v[204:207]\n\t" \
    "v_cvt_pk_bf16_f32 v216, v216, v217\n\t" \
    "s_waitcnt lgkmcnt(1)\n\t" \
    "v_mfma_f32_16x16x32_bf16 v[200:203], v[240:243], v[188:191], v[200:203]\n\t" \
    "v_cvt_pk_bf16_f32 v217, v218, v219\n\t" \
    "s_waitcnt lgkmcnt(0)\n\t" \
    "v_mfma_f32_16x16x32_bf16 v[204:207], v[244:247], v[188:191], v[204:207]\n\t" \
    "v_cvt_pk_bf16_f32 v218, v220, v221\n\t" \
    "v_cvt_pk_bf16_f32 v219, v222, v223\n\t" \
    "buffer_store_dwordx4 v[216:219], %[l16], %[rd], %[so] offen sc1\n\t" \
    "s_add_u32 %[so], %[so], 0x10000\n\t" \
    "s_nop 0\n\t" \
    "v_cvt_pk_bf16_f32 v200, v200, v201\n\t" \
    "v_cvt_pk_bf16_f32 v201, v202, v203\n\t" \
    "s_nop 1\n\t" \
    "v_cvt_pk_bf16_f32 v202, v204, v205\n\t" \
    "v_cvt_pk_bf16_f32 v203, v206, v207\n\t" \
    "buffer_store_dwordx4 v[200:203], %[l16], %[rd], %[so] offen sc1\n\t" \
    "s_add_u32 %[so], %[so], 0x10000\n\t" \
    "s_nop 1\n\t"
#define ASM_CRIT2 \
    "s_waitcnt lgkmcnt(0)\n\t" \
    "s_barrier\n\t" ASM_ST \
    "ds_read_b128 v[204:207], v200 offset:11264\n\t" \
    "ds_read_b128 v[208:211], v200 offset:12288\n\t" \
    "ds_read_b128 v[200:203], v200 offset:10240\n\t" \
    "s_waitcnt lgkmcnt(2)\n\t" \
    "v_smfmac_f32_16x16x64_bf16 v[224:227], v[204:207], v[24:31], %[sidx]\n\t" \
    "v_smfmac_f32_16x16x64_bf16 v[220:223], v[204:207], v[0:7], %[sidx]\n\t" \
    "s_waitcnt lgkmcnt(1)\n\t" \
    "v_smfmac_f32_16x16x64_bf16 v[224:227], v[208:211], v[32:39], %[sidx]\n\t" \
    "v_smfmac_f32_16x16x64_bf16 v[220:223], v[208:211], v[8:15], %[sidx]\n\t" \
    "s_waitcnt lgkmcnt(0)\n\t" \
    "v_smfmac_f32_16x16x64_bf16 v[224:227], v[200:203], v[16:23], %[sidx]\n\t" \
    "v_smfmac_f32_16x16x64_bf16 v[220:223], v[200:203], v[212:219], %[sidx]\n\t" \
    "ds_read_b128 v[212:215], %[wl] offset:2048\n\t" \
    "ds_read_b128 v[216:219], %[wl] offset:3072\n\t" \
    "v_smfmac_f32_16x16x64_bf16 v[232:235], v[204:207], v[64:71], %[sidx]\n\t" \
    "v_smfmac_f32_16x16x64_bf16 v[228:231], v[204:207], v[40:47], %[sidx]\n\t" \
    "v_smfmac_f32_16x16x64_bf16 v[232:235], v[208:211], v[72:79], %[sidx]\n\t" \
    "v_permlane32_swap_b32 v220, v224\n\t" \
    "v_permlane32_swap_b32 v221, v225\n\t" \
    "v_permlane32_swap_b32 v222, v226\n\t" \
    "v_smfmac_f32_16x16x64_bf16 v[228:231], v[208:211], v[48:55], %[sidx]\n\t" \
    "v_permlane32_swap_b32 v223, v227\n\t" \
    "v_add_f32 v220, v220, v224\n\t" \
    "v_add_f32 v221, v221, v225\n\t" \
    "v_smfmac_f32_16x16x64_bf16 v[232:235], v[200:203], v[56:63], %[sidx]\n\t" \
    "v_add_f32 v222, v222, v226\n\t" \
    "v_add_f32 v223, v223, v227\n\t" \
    "v_cvt_pk_bf16_f32 v226, v220, v221\n\t" \
    "s_waitcnt lgkmcnt(0)\n\t" \
    "v_smfmac_f32_16x16x64_bf16 v[228:231], v[200:203], v[212:219], %[sidx]\n\t" \
    "ds_read_b128 v[212:215], %[wl] offset:4096\n\t" \
    "ds_read_b128 v[216:219], %[wl] offset:5120\n\t" \
    "v_cvt_pk_bf16_f32 v227, v222, v223\n\t" \
    "v_min_u32 v226, -2, v226\n\t" \
    "v_min_u32 v227, -2, v227\n\t" \
    "v_smfmac_f32_16x16x64_bf16 v[240:243], v[204:207], v[104:111], %[sidx]\n\t" \
    "v_smfmac_f32_16x16x64_bf16 v[236:239], v[204:207], v[80:87], %[sidx]\n\t" \
    "v_smfmac_f32_16x16x64_bf16 v[240:243], v[208:211], v[112:119], %[sidx]\n\t" \
    "v_permlane32_swap_b32 v228, v232\n\t" \
    "v_permlane32_swap_b32 v229, v233\n\t" \
    "v_permlane32_swap_b32 v230, v234\n\t" \
    "v_smfmac_f32_16x16x64_bf16 v[236:239], v[208:211], v[88:95], %[sidx]\n\t" \
    "v_permlane32_swap_b32 v231, v235\n\t" \
    "v_add_f32 v228, v228, v232\n\t" \
    "v_add_f32 v229, v229, v233\n\t" \
    "v_smfmac_f32_16x16x64_bf16 v[240:243], v[200:203], v[96:103], %[sidx]\n\t" \
    "v_add_f32 v230, v230, v234\n\t" \
    "v_add_f32 v231, v231, v235\n\t" \
    "v_cvt_pk_bf16_f32 v228, v228, v229\n\t" \
    "s_waitcnt lgkmcnt(0)\n\t" \
    "v_smfmac_f32_16x16x64_bf16 v[236:239], v[200:203], v[212:219], %[sidx]\n\t" \
    "ds_read_b128 v[212:215], %[wl] offset:6144\n\t" \
    "ds_read_b128 v[216:219], %[wl] offset:7168\n\t" \
    "v_cvt_pk_bf16_f32 v229, v230, v231\n\t" \
    "v_min_u32 v228, -2, v228\n\t" \
    "v_min_u32 v229, -2, v229\n\t" \
    "s_nop 1\n\t" \
    "v_permlane16_swap_b32 v226, v228\n\t" \
    "v_permlane16_swap_b32 v227, v229\n\t" \
    "buffer_store_dwordx4 v[226:229], %[vo], %[rr], %[so] offen sc1\n\t" \
    "s_add_u32 %[so], %[so], 0x10000\n\t" \
    "v_smfmac_f32_16x16x64_bf16 v[248:251], v[204:207], v[144:151], %[sidx]\n\t" \
    "v_mov_b64 v[220:221], 0\n\t" \
    "v_mov_b64 v[222:223], 0\n\t" \
    "v_smfmac_f32_16x16x64_bf16 v[244:247], v[204:207], v[120:127], %[sidx]\n\t" \
    "v_mov_b64 v[224:225], 0\n\t" \
    "v_mov_b64 v[226:227], 0\n\t" \
    "v_smfmac_f32_16x16x64_bf16 v[248:251], v[208:211], v[152:159], %[sidx]\n\t" \
    "v_permlane32_swap_b32 v236, v240\n\t" \
    "v_permlane32_swap_b32 v237, v241\n\t" \
    "v_permlane32_swap_b32 v238, v242\n\t" \
    "v_smfmac_f32_16x16x64_bf16 v[244:247], v[208:211], v[128:135], %[sidx]\n\t" \
    "v_permlane32_swap_b32 v239, v243\n\t" \
    "v_add_f32 v236, v236, v240\n\t" \
    "v_add_f32 v237, v237, v241\n\t" \
    "v_smfmac_f32_16x16x64_bf16 v[248:251], v[200:203], v[136:143], %[sidx]\n\t" \
    "v_add_f32 v238, v238, v242\n\t" \
    "v_add_f32 v239, v239, v243\n\t" \
    "v_cvt_pk_bf16_f32 v242, v236, v237\n\t" \
    "s_waitcnt lgkmcnt(0)\n\t" \
    "v_smfmac_f32_16x16x64_bf16 v[244:247], v[200:203], v[212:219], %[sidx]\n\t" \
    "ds_read_b128 v[212:215], %[wl] offset:8192\n\t" \
    "ds_read_b128 v[216:219], %[wl] offset:9216\n\t" \
    "v_cvt_pk_bf16_f32 v243, v238, v239\n\t" \
    "v_min_u32 v242, -2, v242\n\t" \
    "v_min_u32 v243, -2, v243\n\t" \
    "v_smfmac_f32_16x16x64_bf16 v[224:227], v[204:207], v[184:191], %[sidx]\n\t" \
    "v_and_b32 v232, 0x3fff, %[vo]\n\t" \
    "v_bfe_u32 v233, %[vo], 14, 1\n\t" \
    "v_smfmac_f32_16x16x64_bf16 v[220:223], v[204:207], v[160:167], %[sidx]\n\t" \
    "v_lshl_add_u32 v232, v233, 3, v232\n\t" \
    "v_permlane32_swap_b32 v244, v248\n\t" \
    "v_smfmac_f32_16x16x64_bf16 v[224:227], v[208:211], v[192:199], %[sidx]\n\t" \
    "v_permlane32_swap_b32 v245, v249\n\t" \
    "v_permlane32_swap_b32 v246, v250\n\t" \
    "v_permlane32_swap_b32 v247, v251\n\t" \
    "v_smfmac_f32_16x16x64_bf16 v[220:223], v[208:211], v[168:175], %[sidx]\n\t" \
    "v_add_f32 v244, v244, v248\n\t" \
    "v_add_f32 v245, v245, v249\n\t" \
    "v_add_f32 v246, v246, v250\n\t" \
    "v_smfmac_f32_16x16x64_bf16 v[224:227], v[200:203], v[176:183], %[sidx]\n\t" \
    "v_add_f32 v247, v247, v251\n\t" \
    "v_cvt_pk_bf16_f32 v244, v244, v245\n\t" \
    "v_cvt_pk_bf16_f32 v245, v246, v247\n\t" \
    "s_waitcnt lgkmcnt(0)\n\t" \
    "v_smfmac_f32_16x16x64_bf16 v[220:223], v[200:203], v[212:219], %[sidx]\n\t" \
    "v_min_u32 v244, -2, v244\n\t" \
    "v_min_u32 v245, -2, v245\n\t" \
    "s_nop 1\n\t" \
    "v_permlane16_swap_b32 v242, v244\n\t" \
    "v_permlane16_swap_b32 v243, v245\n\t" \
    "buffer_store_dwordx4 v[242:245], %[vo], %[rr], %[so] offen sc1\n\t" \
    "s_add_u32 %[so], %[so], 0x10000\n\t" \
    "s_nop 0\n\t" \
    "v_permlane32_swap_b32 v220, v224\n\t" \
    "v_permlane32_swap_b32 v221, v225\n\t" \
    "v_permlane32_swap_b32 v222, v226\n\t" \
    "v_permlane32_swap_b32 v223, v227\n\t" \
    "v_add_f32 v220, v220, v224\n\t" \
    "v_add_f32 v221, v221, v225\n\t" \
    "v_add_f32 v222, v222, v226\n\t" \
    "v_add_f32 v223, v223, v227\n\t" \
    "v_cvt_pk_bf16_f32 v220, v220, v221\n\t" \
    "v_cvt_pk_bf16_f32 v221, v222, v223\n\t" \
    "v_min_u32 v220, -2, v220\n\t" \
    "v_min_u32 v221, -2, v221\n\t" \
    "buffer_store_dwordx2 v[220:221], v232, %[rr], %[so] offen sc1\n\t" \
    "s_nop 1\n\t"
#define ASM_SM \
    "v_mbcnt_lo_u32_b32 v252, -1, 0\n\t" \
    "v_mbcnt_hi_u32_b32 v252, -1, v252\n\t" \
    "v_lshlrev_b32 v252, 4, v252\n\t" \
    "v_lshrrev_b32 v253, 1, v252\n\t" \
    "v_xor_b32 v253, v253, v252\n\t" \
    "v_and_b32 v253, 0x20, v253\n\t" \
    "v_mul_u32_u24 v253, 3, v253\n\t" \
    "v_xor_b32 v253, v253, v252\n\t" \
    "ds_read_b128 v[200:203], v253 offset:28672\n\t" \
    "ds_read_b128 v[204:207], v253 offset:29696\n\t" \
    "ds_read_b128 v[208:211], v253 offset:34816\n\t" \
    "ds_read_b128 v[212:215], v253 offset:35840\n\t" \
    "ds_read_b128 v[216:219], v253 offset:30720\n\t" \
    "ds_read_b128 v[220:223], v253 offset:31744\n\t" \
    "ds_read_b128 v[224:227], v253 offset:36864\n\t" \
    "ds_read_b128 v[228:231], v253 offset:37888\n\t" \
    "v_lshrrev_b32 v254, 1, v252\n\t" \
    "v_and_b32 v254, 0x180, v254\n\t" \
    "v_and_or_b32 v254, v252, %[c70], v254\n\t" \
    "v_bfe_i32 v255, v252, 7, 1\n\t" \
    "v_and_b32 v255, 0xaaaa, v255\n\t" \
    "v_or_b32 v255, 0x4444, v255\n\t" \
    "v_mov_b64 v[232:233], 0\n\t" \
    "v_mov_b64 v[234:235], 0\n\t" \
    "v_mov_b64 v[236:237], 0\n\t" \
    "v_mov_b64 v[238:239], 0\n\t" \
    "s_waitcnt lgkmcnt(0)\n\t" \
    "s_barrier\n\t" ASM_ST \
    "s_setprio 3\n\t" \
    "ds_read_b128 v[240:243], v252 offset:10240\n\t" \
    "ds_read_b128 v[244:247], v252 offset:11264\n\t" \
    "ds_read_b128 v[248:251], v252 offset:12288\n\t" \
    "s_waitcnt lgkmcnt(2)\n\t" \
    "v_smfmac_f32_16x16x64_bf16 v[232:235], v[240:243], v[200:207], v255\n\t" \
    "ds_read_b128 v[200:203], v253 offset:32768\n\t" \
    "ds_read_b128 v[204:207], v253 offset:33792\n\t" \
    "v_smfmac_f32_16x16x64_bf16 v[236:239], v[240:243], v[208:215], v255\n\t" \
    "ds_read_b128 v[208:211], v253 offset:38912\n\t" \
    "ds_read_b128 v[212:215], v253 offset:39936\n\t" \
    "s_waitcnt lgkmcnt(5)\n\t" \
    "v_smfmac_f32_16x16x64_bf16 v[232:235], v[244:247], v[216:223], v255\n\t" \
    "ds_read_b128 v[216:219], v254 offset:13312\n\t" \
    "ds_read_b128 v[220:223], v254 offset:13824\n\t" \
    "v_smfmac_f32_16x16x64_bf16 v[236:239], v[244:247], v[224:231], v255\n\t" \
    "ds_read_b128 v[224:227], v254 offset:14336\n\t" \
    "ds_read_b128 v[228:231], v254 offset:14848\n\t" \
    "s_waitcnt lgkmcnt(6)\n\t" \
    "v_smfmac_f32_16x16x64_bf16 v[232:235], v[248:251], v[200:207], v255\n\t" \
    "s_waitcnt lgkmcnt(4)\n\t" \
    "v_smfmac_f32_16x16x64_bf16 v[236:239], v[248:251], v[208:215], v255\n\t" \
    "ds_read_b128 v[208:211], v254 offset:15360\n\t" \
    "ds_read_b128 v[212:215], v254 offset:15872\n\t" \
    "v_mov_b64 v[204:205], 0\n\t" \
    "v_mov_b64 v[206:207], 0\n\t" \
    "s_nop 1\n\t" \
    "s_waitcnt lgkmcnt(4)\n\t" \
    "v_smfmac_f32_16x16x64_bf16 v[204:207], v[240:243], v[216:223], v255\n\t" \
    "s_waitcnt lgkmcnt(2)\n\t" \
    "v_smfmac_f32_16x16x64_bf16 v[204:207], v[244:247], v[224:231], v255\n\t" \
    "v_bfe_u32 v253, v252, 5, 1\n\t" \
    "v_bfe_i32 v202, v252, 4, 1\n\t" \
    "v_lshlrev_b32 v200, 1, v252\n\t" \
    "v_and_b32 v200, 0x180, v200\n\t" \
    "v_permlane32_swap_b32 v232, v234\n\t" \
    "v_permlane32_swap_b32 v233, v235\n\t" \
    "v_permlane32_swap_b32 v236, v238\n\t" \
    "v_permlane32_swap_b32 v237, v239\n\t" \
    "v_lshrrev_b32 v201, 2, v252\n\t" \
    "v_lshrrev_b32 v203, 4, v252\n\t" \
    "v_and_b32 v201, 0x40, v201\n\t" \
    "v_and_or_b32 v201, v203, 32, v201\n\t" \
    "v_and_or_b32 v201, v252, 16, v201\n\t" \
    "v_pk_add_f32 v[232:233], v[232:233], v[234:235]\n\t" \
    "v_pk_add_f32 v[234:235], v[236:237], v[238:239]\n\t" \
    "v_or_b32 v200, v200, v201\n\t" \
    "v_mad_u32_u24 v200, v253, %[cs], v200\n\t" \
    "v_mad_u32_u24 v201, v253, %[cs], v201\n\t" \
    "v_lshrrev_b32 v203, 3, v252\n\t" \
    "v_bfe_u32 v253, v252, 7, 1\n\t" \
    "s_waitcnt lgkmcnt(0)\n\t" \
    "v_smfmac_f32_16x16x64_bf16 v[204:207], v[248:251], v[208:215], v255\n\t" \
    "v_and_or_b32 v201, v203, 8, v201\n\t" \
    "v_and_b32 v202, 0x06060606, v202\n\t" \
    "v_mad_u32_u24 v201, v253, %[cp], v201\n\t" \
    "v_xor_b32 v202, 0x01000504, v202\n\t" \
    "v_max_f32 v236, v232, v234\n\t" \
    "v_max_f32 v237, v233, v235\n\t" \
    "s_nop 1\n\t" \
    "v_permlane32_swap_b32 v204, v206\n\t" \
    "v_permlane32_swap_b32 v205, v207\n\t" \
    "v_add_f32 v204, v204, v206\n\t" \
    "v_add_f32 v205, v205, v207\n\t" \
    "v_max_f32 v236, v236, v204\n\t" \
    "v_max_f32 v237, v237, v205\n\t" \
    "s_nop 0\n\t" \
    "v_max_f32_dpp v236, v236, v236 quad_perm:[1,0,3,2] row_mask:0xf bank_mask:0xf\n\t" \
    "v_max_f32_dpp v237, v237, v237 quad_perm:[1,0,3,2] row_mask:0xf bank_mask:0xf\n\t" \
    "s_nop 0\n\t" \
    "v_max_f32_dpp v236, v236, v236 quad_perm:[2,3,0,1] row_mask:0xf bank_mask:0xf\n\t" \
    "v_max_f32_dpp v237, v237, v237 quad_perm:[2,3,0,1] row_mask:0xf bank_mask:0xf\n\t" \
    "s_nop 0\n\t" \
    "v_max_f32_dpp v236, v236, v236 row_half_mirror row_mask:0xf bank_mask:0xf\n\t" \
    "v_max_f32_dpp v237, v237, v237 row_half_mirror row_mask:0xf bank_mask:0xf\n\t" \
    "s_nop 0\n\t" \
    "v_max_f32_dpp v236, v236, v236 row_mirror row_mask:0xf bank_mask:0xf\n\t" \
    "v_max_f32_dpp v237, v237, v237 row_mirror row_mask:0xf bank_mask:0xf\n\t" \
    "v_mul_f32 v236, %[scl], v236\n\t" \
    "v_mul_f32 v237, %[scl], v237\n\t" \
    "v_fma_f32 v232, v232, %[scl], -v236\n\t" \
    "v_fma_f32 v233, v233, %[scl], -v237\n\t" \
    "v_fma_f32 v234, v234, %[scl], -v236\n\t" \
    "v_fma_f32 v235, v235, %[scl], -v237\n\t" \
    "v_exp_f32 v232, v232\n\t" \
    "v_exp_f32 v233, v233\n\t" \
    "v_fma_f32 v204, v204, %[scl], -v236\n\t" \
    "v_fma_f32 v205, v205, %[scl], -v237\n\t" \
    "v_exp_f32 v234, v234\n\t" \
    "v_exp_f32 v235, v235\n\t" \
    "v_exp_f32 v204, v204\n\t" \
    "v_exp_f32 v205, v205\n\t" \
    "v_add_f32 v238, v232, v234\n\t" \
    "v_add_f32 v239, v233, v235\n\t" \
    "v_fma_f32 v238, v204, 0.5, v238\n\t" \
    "v_fma_f32 v239, v205, 0.5, v239\n\t" \
    "s_nop 0\n\t" \
    "v_add_f32_dpp v238, v238, v238 quad_perm:[1,0,3,2] row_mask:0xf bank_mask:0xf\n\t" \
    "v_add_f32_dpp v239, v239, v239 quad_perm:[1,0,3,2] row_mask:0xf bank_mask:0xf\n\t" \
    "s_nop 0\n\t" \
    "v_add_f32_dpp v238, v238, v238 quad_perm:[2,3,0,1] row_mask:0xf bank_mask:0xf\n\t" \
    "v_add_f32_dpp v239, v239, v239 quad_perm:[2,3,0,1] row_mask:0xf bank_mask:0xf\n\t" \
    "s_nop 0\n\t" \
    "v_add_f32_dpp v238, v238, v238 row_half_mirror row_mask:0xf bank_mask:0xf\n\t" \
    "v_add_f32_dpp v239, v239, v239 row_half_mirror row_mask:0xf bank_mask:0xf\n\t" \
    "s_nop 0\n\t" \
    "v_add_f32_dpp v238, v238, v238 row_mirror row_mask:0xf bank_mask:0xf\n\t" \
    "v_add_f32_dpp v239, v239, v239 row_mirror row_mask:0xf bank_mask:0xf\n\t" \
    "v_rcp_f32 v238, v238\n\t" \
    "v_rcp_f32 v239, v239\n\t" \
    "s_nop 0\n\t" \
    "v_pk_mul_f32 v[232:233], v[232:233], v[238:239]\n\t" \
    "v_pk_mul_f32 v[234:235], v[234:235], v[238:239]\n\t" \
    "v_pk_mul_f32 v[204:205], v[204:205], v[238:239]\n\t" \
    "v_cvt_pk_bf16_f32 v232, |v232|, |v233|\n\t" \
    "v_cvt_pk_bf16_f32 v234, |v234|, |v235|\n\t" \
    "v_cvt_pk_bf16_f32 v204, |v204|, |v205|\n\t" \
    "v_mov_b32_dpp v233, v232 quad_perm:[1,0,3,2] row_mask:0xf bank_mask:0xf\n\t" \
    "v_mov_b32_dpp v235, v234 quad_perm:[1,0,3,2] row_mask:0xf bank_mask:0xf\n\t" \
    "v_mov_b32_dpp v205, v204 quad_perm:[1,0,3,2] row_mask:0xf bank_mask:0xf\n\t" \
    "v_perm_b32 v232, v232, v233, v202\n\t" \
    "v_perm_b32 v234, v234, v235, v202\n\t" \
    "v_perm_b32 v204, v204, v205, v202\n\t" \
    "v_mov_b32_dpp v240, v232 quad_perm:[0,1,0,1] row_mask:0xf bank_mask:0xf\n\t" \
    "v_mov_b32_dpp v241, v234 quad_perm:[0,1,0,1] row_mask:0xf bank_mask:0xf\n\t" \
    "v_mov_b32_dpp v242, v232 quad_perm:[2,3,2,3] row_mask:0xf bank_mask:0xf\n\t" \
    "v_mov_b32_dpp v243, v234 quad_perm:[2,3,2,3] row_mask:0xf bank_mask:0xf\n\t" \
    "buffer_store_dwordx4 v[240:243], v200, %[rr], %[sx] offen sc1\n\t" \
    "v_mov_b32_dpp v244, v204 quad_perm:[0,1,0,1] row_mask:0xf bank_mask:0xf\n\t" \
    "v_mov_b32_dpp v245, v204 quad_perm:[2,3,2,3] row_mask:0xf bank_mask:0xf\n\t" \
    "buffer_store_dwordx4 v[240:243], v200, %[rr], %[sy] offen sc1\n\t" \
    "buffer_store_dwordx2 v[244:245], v201, %[rr], %[sm] offen sc1\n\t" \
    "s_wakeup\n\t" \
    PRISM
#define ASM_P4_0 \
    "ds_read_b128 v[200:203], %[qa] offset:0\n\t" \
    "ds_read_b128 v[204:207], %[qa] offset:1024\n\t" \
    "ds_read_b128 v[208:211], %[qa] offset:2048\n\t" \
    "ds_read_b128 v[212:215], %[qa] offset:3072\n\t" \
    "ds_read_b128 v[216:219], %[qa] offset:4096\n\t" \
    "ds_read_b128 v[220:223], %[qa] offset:5120\n\t" \
    "ds_read_b128 v[224:227], %[qa] offset:12288\n\t" \
    "ds_read_b128 v[228:231], %[qa] offset:13312\n\t" \
    "ds_read_b128 v[232:235], %[qa] offset:14336\n\t" \
    "ds_read_b128 v[236:239], %[qa] offset:15360\n\t" \
    "s_waitcnt lgkmcnt(0)\n\t" \
    "s_barrier\n\t" ASM_ST \
    "v_mfma_f32_16x16x32_bf16 v[240:243], v[224:227], v[200:203], v[240:243]\n\t" \
    "ds_read_b128 v[224:227], %[qa] offset:16384\n\t" \
    "v_mfma_f32_16x16x32_bf16 v[240:243], v[228:231], v[204:207], v[240:243]\n\t" \
    "ds_read_b128 v[228:231], %[qa] offset:17408\n\t" \
    "v_mfma_f32_16x16x32_bf16 v[240:243], v[232:235], v[208:211], v[240:243]\n\t" \
    "ds_read_b128 v[232:235], %[km] offset:0\n\t" \
    "v_mfma_f32_16x16x32_bf16 v[240:243], v[236:239], v[212:215], v[240:243]\n\t" \
    "ds_read_b128 v[236:239], %[km] offset:512\n\t" \
    "s_waitcnt lgkmcnt(3)\n\t" \
    "v_mfma_f32_16x16x32_bf16 v[240:243], v[224:227], v[216:219], v[240:243]\n\t" \
    "ds_read_b128 v[224:227], %[km] offset:1024\n\t" \
    "s_waitcnt lgkmcnt(3)\n\t" \
    "v_mfma_f32_16x16x32_bf16 v[240:243], v[228:231], v[220:223], v[240:243]\n\t" \
    "ds_read_b128 v[228:231], %[km] offset:1536\n\t" \
    "s_waitcnt lgkmcnt(3)\n\t" \
    "v_mfma_f32_16x16x32_bf16 v[248:251], v[232:235], v[200:203], 0\n\t" \
    "ds_read_b128 v[232:235], %[km] offset:2048\n\t" \
    "s_waitcnt lgkmcnt(3)\n\t" \
    "v_mfma_f32_16x16x32_bf16 v[248:251], v[236:239], v[204:207], v[248:251]\n\t" \
    "ds_read_b128 v[236:239], %[km] offset:2560\n\t" \
    "s_waitcnt lgkmcnt(3)\n\t" \
    "v_mfma_f32_16x16x32_bf16 v[248:251], v[224:227], v[208:211], v[248:251]\n\t" \
    "s_waitcnt lgkmcnt(2)\n\t" \
    "v_mfma_f32_16x16x32_bf16 v[248:251], v[228:231], v[212:215], v[248:251]\n\t" \
    "s_waitcnt lgkmcnt(1)\n\t" \
    "v_mfma_f32_16x16x32_bf16 v[248:251], v[232:235], v[216:219], v[248:251]\n\t" \
    "s_waitcnt lgkmcnt(0)\n\t" \
    "v_mfma_f32_16x16x32_bf16 v[248:251], v[236:239], v[220:223], v[248:251]\n\t" \
    "v_max3_f32 v224, v240, v241, v242\n\t" \
    "v_mov_b32 v225, v243\n\t" \
    "v_lshlrev_b32 v201, 23, %[qa]\n\t" \
    "v_and_b32 v201, 0x80000000, v201\n\t" \
    "v_and_b32 v200, 0x3ff, %[qa]\n\t" \
    "v_or_b32 v200, v201, v200\n\t" \
    "s_nop 1\n\t" \
    "v_max3_f32 v224, v224, v248, v249\n\t" \
    "v_max3_f32 v225, v225, v250, v251\n\t" \
    "v_max_f32 v224, v224, v225\n\t" \
    "v_mov_b32 v225, v224\n\t" \
    "s_nop 1\n\t" \
    "v_permlane16_swap_b32 v224, v225\n\t" \
    "v_max_f32 v224, v224, v225\n\t" \
    "v_mov_b32 v225, v224\n\t" \
    "s_nop 1\n\t" \
    "v_permlane32_swap_b32 v224, v225\n\t" \
    "v_max_f32 v224, v224, v225\n\t" \
    "v_mul_f32 v224, %[scl], v224\n\t" \
    "v_fma_f32 v240, v240, %[scl], -v224\n\t" \
    "v_fma_f32 v241, v241, %[scl], -v224\n\t" \
    "v_exp_f32 v240, v240\n\t" \
    "v_fma_f32 v242, v242, %[scl], -v224\n\t" \
    "v_exp_f32 v241, v241\n\t" \
    "v_fma_f32 v243, v243, %[scl], -v224\n\t" \
    "v_exp_f32 v242, v242\n\t" \
    "v_fma_f32 v248, v248, %[scl], -v224\n\t" \
    "v_exp_f32 v243, v243\n\t" \
    "v_fma_f32 v249, v249, %[scl], -v224\n\t" \
    "v_exp_f32 v248, v248\n\t" \
    "v_fma_f32 v250, v250, %[scl], -v224\n\t" \
    "v_exp_f32 v249, v249\n\t" \
    "v_fma_f32 v251, v251, %[scl], -v224\n\t" \
    "v_exp_f32 v250, v250\n\t" \
    "v_exp_f32 v251, v251\n\t" \
    "v_add_f32 v224, v240, v241\n\t" \
    "v_add_f32 v225, v242, v243\n\t" \
    "v_add_f32 v232, v248, v249\n\t" \
    "v_add_f32 v233, v250, v251\n\t" \
    "v_add_f32 v224, v224, v225\n\t" \
    "v_add_f32 v232, v232, v233\n\t" \
    "v_fma_f32 v224, v232, 0.5, v224\n\t" \
    "v_mov_b32 v225, v224\n\t" \
    "s_nop 1\n\t" \
    "v_permlane16_swap_b32 v224, v225\n\t" \
    "v_add_f32 v224, v224, v225\n\t" \
    "v_mov_b32 v225, v224\n\t" \
    "s_nop 1\n\t" \
    "v_permlane32_swap_b32 v224, v225\n\t" \
    "v_add_f32 v224, v224, v225\n\t" \
    "v_rcp_f32 v224, v224\n\t" \
    "s_nop 0\n\t" \
    "v_mul_f32 v240, v240, v224\n\t" \
    "v_mul_f32 v241, v241, v224\n\t" \
    "v_mul_f32 v242, v242, v224\n\t" \
    "v_mul_f32 v243, v243, v224\n\t" \
    "v_mul_f32 v248, v248, v224\n\t" \
    "v_mul_f32 v249, v249, v224\n\t" \
    "v_mul_f32 v250, v250, v224\n\t" \
    "v_mul_f32 v251, v251, v224\n\t" \
    "v_cvt_pk_bf16_f32 v228, |v240|, |v241|\n\t" \
    "v_cvt_pk_bf16_f32 v229, |v242|, |v243|\n\t" \
    "v_cvt_pk_bf16_f32 v232, |v248|, |v249|\n\t" \
    "v_cvt_pk_bf16_f32 v233, |v250|, |v251|\n\t" \
    "v_permlane16_swap_b32 v228, v230\n\t" \
    "v_permlane16_swap_b32 v229, v231\n\t" \
    "buffer_store_dwordx4 v[228:231], v200, %[rr], %[so] offen sc1\n\t" \
    "buffer_store_dwordx2 v[232:233], %[pm], %[rr], %[so] offen sc1\n\t" \
    "s_nop 1\n\t"
#define ASM_P4_1 \
    "ds_read_b128 v[200:203], %[qa] offset:0\n\t" \
    "ds_read_b128 v[204:207], %[qa] offset:1024\n\t" \
    "ds_read_b128 v[208:211], %[qa] offset:2048\n\t" \
    "ds_read_b128 v[212:215], %[qa] offset:3072\n\t" \
    "ds_read_b128 v[216:219], %[qa] offset:4096\n\t" \
    "ds_read_b128 v[220:223], %[qa] offset:5120\n\t" \
    "ds_read_b128 v[224:227], %[qa] offset:12288\n\t" \
    "ds_read_b128 v[228:231], %[qa] offset:13312\n\t" \
    "ds_read_b128 v[232:235], %[qa] offset:14336\n\t" \
    "ds_read_b128 v[236:239], %[qa] offset:15360\n\t" \
    "s_waitcnt lgkmcnt(0)\n\t" \
    "s_barrier\n\t" ASM_ST \
    "v_mfma_f32_16x16x32_bf16 v[240:243], v[224:227], v[200:203], v[240:243]\n\t" \
    "ds_read_b128 v[224:227], %[qa] offset:16384\n\t" \
    "v_mfma_f32_16x16x32_bf16 v[240:243], v[228:231], v[204:207], v[240:243]\n\t" \
    "ds_read_b128 v[228:231], %[qa] offset:17408\n\t" \
    "v_mfma_f32_16x16x32_bf16 v[240:243], v[232:235], v[208:211], v[240:243]\n\t" \
    "ds_read_b128 v[232:235], %[qa] offset:6144\n\t" \
    "v_mfma_f32_16x16x32_bf16 v[240:243], v[236:239], v[212:215], v[240:243]\n\t" \
    "ds_read_b128 v[236:239], %[qa] offset:7168\n\t" \
    "s_waitcnt lgkmcnt(3)\n\t" \
    "v_mfma_f32_16x16x32_bf16 v[240:243], v[224:227], v[216:219], v[240:243]\n\t" \
    "ds_read_b128 v[224:227], %[qa] offset:8192\n\t" \
    "s_waitcnt lgkmcnt(3)\n\t" \
    "v_mfma_f32_16x16x32_bf16 v[240:243], v[228:231], v[220:223], v[240:243]\n\t" \
    "ds_read_b128 v[228:231], %[qa] offset:9216\n\t" \
    "s_waitcnt lgkmcnt(3)\n\t" \
    "v_mfma_f32_16x16x32_bf16 v[244:247], v[232:235], v[200:203], 0\n\t" \
    "ds_read_b128 v[232:235], %[qa] offset:10240\n\t" \
    "s_waitcnt lgkmcnt(3)\n\t" \
    "v_mfma_f32_16x16x32_bf16 v[244:247], v[236:239], v[204:207], v[244:247]\n\t" \
    "ds_read_b128 v[236:239], %[qa] offset:11264\n\t" \
    "s_waitcnt lgkmcnt(3)\n\t" \
    "v_mfma_f32_16x16x32_bf16 v[244:247], v[224:227], v[208:211], v[244:247]\n\t" \
    "ds_read_b128 v[224:227], %[km] offset:0\n\t" \
    "s_waitcnt lgkmcnt(3)\n\t" \
    "v_mfma_f32_16x16x32_bf16 v[244:247], v[228:231], v[212:215], v[244:247]\n\t" \
    "ds_read_b128 v[228:231], %[km] offset:512\n\t" \
    "s_waitcnt lgkmcnt(3)\n\t" \
    "v_mfma_f32_16x16x32_bf16 v[244:247], v[232:235], v[216:219], v[244:247]\n\t" \
    "ds_read_b128 v[232:235], %[km] offset:1024\n\t" \
    "s_waitcnt lgkmcnt(3)\n\t" \
    "v_mfma_f32_16x16x32_bf16 v[244:247], v[236:239], v[220:223], v[244:247]\n\t" \
    "ds_read_b128 v[236:239], %[km] offset:1536\n\t" \
    "s_waitcnt lgkmcnt(3)\n\t" \
    "v_mfma_f32_16x16x32_bf16 v[248:251], v[224:227], v[200:203], 0\n\t" \
    "ds_read_b128 v[224:227], %[km] offset:2048\n\t" \
    "s_waitcnt lgkmcnt(3)\n\t" \
    "v_mfma_f32_16x16x32_bf16 v[248:251], v[228:231], v[204:207], v[248:251]\n\t" \
    "ds_read_b128 v[228:231], %[km] offset:2560\n\t" \
    "s_waitcnt lgkmcnt(3)\n\t" \
    "v_mfma_f32_16x16x32_bf16 v[248:251], v[232:235], v[208:211], v[248:251]\n\t" \
    "s_waitcnt lgkmcnt(2)\n\t" \
    "v_mfma_f32_16x16x32_bf16 v[248:251], v[236:239], v[212:215], v[248:251]\n\t" \
    "s_waitcnt lgkmcnt(1)\n\t" \
    "v_mfma_f32_16x16x32_bf16 v[248:251], v[224:227], v[216:219], v[248:251]\n\t" \
    "s_waitcnt lgkmcnt(0)\n\t" \
    "v_mfma_f32_16x16x32_bf16 v[248:251], v[228:231], v[220:223], v[248:251]\n\t" \
    "v_max3_f32 v224, v240, v241, v242\n\t" \
    "v_max3_f32 v225, v244, v245, v246\n\t" \
    "v_max3_f32 v224, v224, v243, v247\n\t" \
    "v_and_b32 v200, 0x3ff, %[qa]\n\t" \
    "s_nop 3\n\t" \
    "v_max3_f32 v224, v224, v248, v249\n\t" \
    "v_max3_f32 v225, v225, v250, v251\n\t" \
    "v_max_f32 v224, v224, v225\n\t" \
    "v_mov_b32 v225, v224\n\t" \
    "s_nop 1\n\t" \
    "v_permlane16_swap_b32 v224, v225\n\t" \
    "v_max_f32 v224, v224, v225\n\t" \
    "v_mov_b32 v225, v224\n\t" \
    "s_nop 1\n\t" \
    "v_permlane32_swap_b32 v224, v225\n\t" \
    "v_max_f32 v224, v224, v225\n\t" \
    "v_mul_f32 v224, %[scl], v224\n\t" \
    "v_fma_f32 v240, v240, %[scl], -v224\n\t" \
    "v_fma_f32 v241, v241, %[scl], -v224\n\t" \
    "v_exp_f32 v240, v240\n\t" \
    "v_fma_f32 v242, v242, %[scl], -v224\n\t" \
    "v_exp_f32 v241, v241\n\t" \
    "v_fma_f32 v243, v243, %[scl], -v224\n\t" \
    "v_exp_f32 v242, v242\n\t" \
    "v_fma_f32 v244, v244, %[scl], -v224\n\t" \
    "v_exp_f32 v243, v243\n\t" \
    "v_fma_f32 v245, v245, %[scl], -v224\n\t" \
    "v_exp_f32 v244, v244\n\t" \
    "v_fma_f32 v246, v246, %[scl], -v224\n\t" \
    "v_exp_f32 v245, v245\n\t" \
    "v_fma_f32 v247, v247, %[scl], -v224\n\t" \
    "v_exp_f32 v246, v246\n\t" \
    "v_fma_f32 v248, v248, %[scl], -v224\n\t" \
    "v_exp_f32 v247, v247\n\t" \
    "v_fma_f32 v249, v249, %[scl], -v224\n\t" \
    "v_exp_f32 v248, v248\n\t" \
    "v_fma_f32 v250, v250, %[scl], -v224\n\t" \
    "v_exp_f32 v249, v249\n\t" \
    "v_fma_f32 v251, v251, %[scl], -v224\n\t" \
    "v_exp_f32 v250, v250\n\t" \
    "v_exp_f32 v251, v251\n\t" \
    "v_add_f32 v224, v240, v241\n\t" \
    "v_add_f32 v225, v242, v243\n\t" \
    "v_add_f32 v226, v244, v245\n\t" \
    "v_add_f32 v227, v246, v247\n\t" \
    "v_add_f32 v232, v248, v249\n\t" \
    "v_add_f32 v233, v250, v251\n\t" \
    "v_add_f32 v224, v224, v225\n\t" \
    "v_add_f32 v226, v226, v227\n\t" \
    "v_add_f32 v232, v232, v233\n\t" \
    "v_add_f32 v224, v224, v226\n\t" \
    "v_fma_f32 v224, v232, 0.5, v224\n\t" \
    "v_mov_b32 v225, v224\n\t" \
    "s_nop 1\n\t" \
    "v_permlane16_swap_b32 v224, v225\n\t" \
    "v_add_f32 v224, v224, v225\n\t" \
    "v_mov_b32 v225, v224\n\t" \
    "s_nop 1\n\t" \
    "v_permlane32_swap_b32 v224, v225\n\t" \
    "v_add_f32 v224, v224, v225\n\t" \
    "v_rcp_f32 v224, v224\n\t" \
    "s_nop 0\n\t" \
    "v_mul_f32 v240, v240, v224\n\t" \
    "v_mul_f32 v241, v241, v224\n\t" \
    "v_mul_f32 v242, v242, v224\n\t" \
    "v_mul_f32 v243, v243, v224\n\t" \
    "v_mul_f32 v244, v244, v224\n\t" \
    "v_mul_f32 v245, v245, v224\n\t" \
    "v_mul_f32 v246, v246, v224\n\t" \
    "v_mul_f32 v247, v247, v224\n\t" \
    "v_mul_f32 v248, v248, v224\n\t" \
    "v_mul_f32 v249, v249, v224\n\t" \
    "v_mul_f32 v250, v250, v224\n\t" \
    "v_mul_f32 v251, v251, v224\n\t" \
    "v_cvt_pk_bf16_f32 v230, |v240|, |v241|\n\t" \
    "v_cvt_pk_bf16_f32 v231, |v242|, |v243|\n\t" \
    "v_cvt_pk_bf16_f32 v228, |v244|, |v245|\n\t" \
    "v_cvt_pk_bf16_f32 v229, |v246|, |v247|\n\t" \
    "v_cvt_pk_bf16_f32 v232, |v248|, |v249|\n\t" \
    "v_cvt_pk_bf16_f32 v233, |v250|, |v251|\n\t" \
    "buffer_store_dwordx4 v[228:231], v200, %[rr], %[so] offen sc1\n\t" \
    "buffer_store_dwordx2 v[232:233], %[pm], %[rr], %[so] offen sc1\n\t" \
    "s_nop 1\n\t"
#define ASM_RCRIT \
    "v_mov_b64 v[176:177], -1\n\t" \
    "v_mov_b64 v[178:179], -1\n\t" \
    "v_mov_b64 v[180:181], -1\n\t" \
    "v_mov_b64 v[182:183], -1\n\t" \
    "v_mov_b64 v[184:185], -1\n\t" \
    "v_mov_b64 v[186:187], -1\n\t" \
    "v_mov_b64 v[188:189], -1\n\t" \
    "v_mov_b64 v[190:191], -1\n\t" \
    "v_mov_b64 v[192:193], -1\n\t" \
    "v_mov_b64 v[194:195], -1\n\t" \
    ".Lfa%=:\n\t" \
    "s_mov_b32 %[a], 0\n\t" \
    "v_max_u32 v196, v176, v177\n\t" \
    "v_max3_u32 v196, v178, v179, v196\n\t" \
    "v_cmp_eq_u32 vcc, -1, v196\n\t" \
    "s_cbranch_vccz .Lga0_%=\n\t" \
    "buffer_load_dwordx4 v[176:179], %[ov], %[rs], %[sv] offen sc1\n\t" \
    "s_mov_b32 %[a], 1\n\t" \
    ".Lga0_%=:\n\t" \
    "v_max_u32 v196, v180, v181\n\t" \
    "v_max3_u32 v196, v182, v183, v196\n\t" \
    "v_cmp_eq_u32 vcc, -1, v196\n\t" \
    "s_cbranch_vccz .Lga1_%=\n\t" \
    "buffer_load_dwordx4 v[180:183], %[ov], %[rs], %[sv] offen offset:1024 sc1\n\t" \
    "s_mov_b32 %[a], 1\n\t" \
    ".Lga1_%=:\n\t" \
    "s_cmp_eq_u32 %[a], 0\n\t" \
    "s_cbranch_scc1 .Lha%=\n\t" \
    "s_waitcnt vmcnt(12)\n\t" \
    ".Lia%=:\n\t" \
    "v_max_u32 v196, v176, v177\n\t" \
    "v_max3_u32 v196, v178, v179, v196\n\t" \
    "v_max3_u32 v196, v180, v181, v196\n\t" \
    "v_max3_u32 v196, v182, v183, v196\n\t" \
    "v_cmp_eq_u32 vcc, -1, v196\n\t" \
    "s_cbranch_vccz .Lha%=\n\t" \
    "s_nop 7\n\t" \
    "v_max_u32 v196, v176, v177\n\t" \
    "v_max3_u32 v196, v178, v179, v196\n\t" \
    "v_max3_u32 v196, v180, v181, v196\n\t" \
    "v_max3_u32 v196, v182, v183, v196\n\t" \
    "v_cmp_eq_u32 vcc, -1, v196\n\t" \
    "s_cbranch_vccz .Lha%=\n\t" \
    "s_nop 7\n\t" \
    "v_max_u32 v196, v176, v177\n\t" \
    "v_max3_u32 v196, v178, v179, v196\n\t" \
    "v_max3_u32 v196, v180, v181, v196\n\t" \
    "v_max3_u32 v196, v182, v183, v196\n\t" \
    "v_cmp_eq_u32 vcc, -1, v196\n\t" \
    "s_cbranch_vccz .Lha%=\n\t" \
    "s_nop 7\n\t" \
    "v_max_u32 v196, v176, v177\n\t" \
    "v_max3_u32 v196, v178, v179, v196\n\t" \
    "v_max3_u32 v196, v180, v181, v196\n\t" \
    "v_max3_u32 v196, v182, v183, v196\n\t" \
    "v_cmp_eq_u32 vcc, -1, v196\n\t" \
    "s_cbranch_vccz .Lha%=\n\t" \
    "s_nop 7\n\t" \
    "s_sub_u32 %[n], %[n], 1\n\t" \
    "s_cmp_gt_i32 %[n], 0\n\t" \
    "s_cbranch_scc1 .Lfa%=\n\t" \
    ".Lha%=:\n\t" \
    ASM_ST3 \
    "buffer_load_dwordx4 v[184:187], %[o0], %[rs], %[sp] offen sc1\n\t" \
    "buffer_load_dwordx4 v[188:191], %[o0], %[rs], %[sp] offen offset:1024 sc1\n\t" \
    "buffer_load_dwordx4 v[192:195], %[o2], %[rs], %[sp] offen sc1\n\t" \
    "s_waitcnt vmcnt(0)\n\t" \
    "v_max_u32 v196, v184, v185\n\t" \
    "v_max3_u32 v196, v186, v187, v196\n\t" \
    "v_max3_u32 v196, v188, v189, v196\n\t" \
    "v_max3_u32 v196, v190, v191, v196\n\t" \
    "v_max3_u32 v196, v192, v193, v196\n\t" \
    "v_max3_u32 v196, v194, v195, v196\n\t" \
    "v_cmp_eq_u32 vcc, -1, v196\n\t" \
    "s_cbranch_vccz .Lhb%=\n\t" \
    ".Lfb%=:\n\t" \
    "s_mov_b32 %[a], 0\n\t" \
    "v_max_u32 v196, v184, v185\n\t" \
    "v_max3_u32 v196, v186, v187, v196\n\t" \
    "v_cmp_eq_u32 vcc, -1, v196\n\t" \
    "s_cbranch_vccz .Lgb0_%=\n\t" \
    "buffer_load_dwordx4 v[184:187], %[o0], %[rs], %[sp] offen sc1\n\t" \
    "s_mov_b32 %[a], 1\n\t" \
    ".Lgb0_%=:\n\t" \
    "v_max_u32 v196, v188, v189\n\t" \
    "v_max3_u32 v196, v190, v191, v196\n\t" \
    "v_cmp_eq_u32 vcc, -1, v196\n\t" \
    "s_cbranch_vccz .Lgb1_%=\n\t" \
    "buffer_load_dwordx4 v[188:191], %[o0], %[rs], %[sp] offen offset:1024 sc1\n\t" \
    "s_mov_b32 %[a], 1\n\t" \
    ".Lgb1_%=:\n\t" \
    "v_max_u32 v196, v192, v193\n\t" \
    "v_max3_u32 v196, v194, v195, v196\n\t" \
    "v_cmp_eq_u32 vcc, -1, v196\n\t" \
    "s_cbranch_vccz .Lgb2_%=\n\t" \
    "buffer_load_dwordx4 v[192:195], %[o2], %[rs], %[sp] offen sc1\n\t" \
    "s_mov_b32 %[a], 1\n\t" \
    ".Lgb2_%=:\n\t" \
    "s_cmp_eq_u32 %[a], 0\n\t" \
    "s_cbranch_scc1 .Lhb%=\n\t" \
    "s_waitcnt vmcnt(12)\n\t" \
    ".Lib%=:\n\t" \
    "v_max_u32 v196, v184, v185\n\t" \
    "v_max3_u32 v196, v186, v187, v196\n\t" \
    "v_max3_u32 v196, v188, v189, v196\n\t" \
    "v_max3_u32 v196, v190, v191, v196\n\t" \
    "v_max3_u32 v196, v192, v193, v196\n\t" \
    "v_max3_u32 v196, v194, v195, v196\n\t" \
    "v_cmp_eq_u32 vcc, -1, v196\n\t" \
    "s_cbranch_vccz .Lhb%=\n\t" \
    "s_nop 7\n\t" \
    "v_max_u32 v196, v184, v185\n\t" \
    "v_max3_u32 v196, v186, v187, v196\n\t" \
    "v_max3_u32 v196, v188, v189, v196\n\t" \
    "v_max3_u32 v196, v190, v191, v196\n\t" \
    "v_max3_u32 v196, v192, v193, v196\n\t" \
    "v_max3_u32 v196, v194, v195, v196\n\t" \
    "v_cmp_eq_u32 vcc, -1, v196\n\t" \
    "s_cbranch_vccz .Lhb%=\n\t" \
    "s_nop 7\n\t" \
    "v_max_u32 v196, v184, v185\n\t" \
    "v_max3_u32 v196, v186, v187, v196\n\t" \
    "v_max3_u32 v196, v188, v189, v196\n\t" \
    "v_max3_u32 v196, v190, v191, v196\n\t" \
    "v_max3_u32 v196, v192, v193, v196\n\t" \
    "v_max3_u32 v196, v194, v195, v196\n\t" \
    "v_cmp_eq_u32 vcc, -1, v196\n\t" \
    "s_cbranch_vccz .Lhb%=\n\t" \
    "s_nop 7\n\t" \
    "v_max_u32 v196, v184, v185\n\t" \
    "v_max3_u32 v196, v186, v187, v196\n\t" \
    "v_max3_u32 v196, v188, v189, v196\n\t" \
    "v_max3_u32 v196, v190, v191, v196\n\t" \
    "v_max3_u32 v196, v192, v193, v196\n\t" \
    "v_max3_u32 v196, v194, v195, v196\n\t" \
    "v_cmp_eq_u32 vcc, -1, v196\n\t" \
    "s_cbranch_vccz .Lhb%=\n\t" \
    "s_nop 7\n\t" \
    "s_sub_u32 %[n], %[n], 1\n\t" \
    "s_cmp_gt_i32 %[n], 0\n\t" \
    "s_cbranch_scc1 .Lfb%=\n\t" \
    ".Lhb%=:\n\t" \
    ASM_ST2 \
    "v_mfma_f32_16x16x32_bf16 v[160:163], v[192:195], v[176:179], 0\n\t" \
    "v_mfma_f32_16x16x32_bf16 v[164:167], v[192:195], v[180:183], 0\n\t" \
    "v_smfmac_f32_16x16x64_bf16 v[160:163], v[184:187], v[128:135], %[sidx]\n\t" \
    "v_smfmac_f32_16x16x64_bf16 v[164:167], v[184:187], v[144:151], %[sidx]\n\t" \
    "v_smfmac_f32_16x16x64_bf16 v[160:163], v[188:191], v[136:143], %[sidx]\n\t" \
    "v_smfmac_f32_16x16x64_bf16 v[164:167], v[188:191], v[152:159], %[sidx]\n\t" \
    "s_cmp_eq_u32 %[pub], 0\n\t" \
    "s_nop 7\n\t" \
    "v_permlane32_swap_b32 v160, v164\n\t" \
    "v_permlane32_swap_b32 v161, v165\n\t" \
    "v_permlane32_swap_b32 v162, v166\n\t" \
    "v_permlane32_swap_b32 v163, v167\n\t" \
    "v_add_f32 v160, v160, v164\n\t" \
    "v_add_f32 v161, v161, v165\n\t" \
    "ds_write2_b32 %[rw], v160, v161 offset1:80\n\t" \
    "v_add_f32 v162, v162, v166\n\t" \
    "v_add_f32 v163, v163, v167\n\t" \
    "ds_write2_b32 %[rw], v162, v163 offset0:160 offset1:240\n\t" \
    "s_waitcnt lgkmcnt(0)\n\t" \
    "s_barrier\n\t" ASM_ST \
    "s_cbranch_scc1 .Lre%=\n\t" \
    "ds_read_b128 v[200:203], %[rd] offset:0\n\t" \
    "ds_read_b128 v[204:207], %[rd] offset:16\n\t" \
    "s_waitcnt lgkmcnt(1)\n\t" \
    "v_pk_add_f32 v[200:201], v[200:201], v[202:203]\n\t" \
    "s_waitcnt lgkmcnt(0)\n\t" \
    "v_pk_add_f32 v[204:205], v[204:205], v[206:207]\n\t" \
    "v_pk_add_f32 v[200:201], v[200:201], v[204:205]\n\t" \
    "s_nop 1\n\t" \
    "v_permlane32_swap_b32 v200, v201\n\t" \
    "v_add_f32 v202, v200, v201\n\t" \
    "v_add_f32 v203, v200, v201\n\t" \
    "s_nop 1\n\t" \
    "v_permlane32_swap_b32 v202, v203\n\t" \
    "v_cvt_pk_bf16_f32 v200, v202, v203\n\t" \
    "v_min_u32 v200, -2, v200\n\t" \
    "buffer_store_dword v200, %[oo], %[rs], %[so] offen sc1\n\t" \
    ".Lre%=:\n\t"
#define ASM_RX \
    "s_cmp_eq_u32 %[fin], 0\n\t" \
    "s_cbranch_scc1 .Lx0%=\n\t" \
    "buffer_load_dwordx2 v[250:251], %[xo], %[rx], %[sx] offen sc1\n\t" \
    ".Lx0%=:\n\t" \
    "buffer_load_dword v248, %[fo], %[rs], %[fs] offen sc1\n\t" \
    "buffer_load_dwordx4 v[200:203], %[l16], %[rs], %[s0] offen offset:0 sc1\n\t" \
    "buffer_load_dwordx4 v[204:207], %[l16], %[rs], %[s0] offen offset:1024 sc1\n\t" \
    "buffer_load_dwordx4 v[208:211], %[l16], %[rs], %[s0] offen offset:2048 sc1\n\t" \
    "buffer_load_dwordx4 v[212:215], %[l16], %[rs], %[s0] offen offset:3072 sc1\n\t" \
    "buffer_load_dwordx4 v[216:219], %[l16], %[rs], %[s1] offen offset:0 sc1\n\t" \
    "buffer_load_dwordx4 v[220:223], %[l16], %[rs], %[s1] offen offset:1024 sc1\n\t" \
    "buffer_load_dwordx4 v[224:227], %[l16], %[rs], %[s1] offen offset:2048 sc1\n\t" \
    "buffer_load_dwordx4 v[228:231], %[l16], %[rs], %[s1] offen offset:3072 sc1\n\t" \
    "v_mov_b64 v[96:97], v[128:129]\n\t" \
    "v_mov_b64 v[98:99], v[132:133]\n\t" \
    "v_mov_b64 v[100:101], v[136:137]\n\t" \
    "v_mov_b64 v[102:103], v[140:141]\n\t" \
    "v_mov_b64 v[104:105], v[144:145]\n\t" \
    "v_mov_b64 v[106:107], v[148:149]\n\t" \
    "v_mov_b64 v[108:109], v[152:153]\n\t" \
    "v_mov_b64 v[110:111], v[156:157]\n\t" \
    "s_movk_i32 %[n], 0x200\n\t" \
    "s_nop 1\n\t" \
    "v_permlane16_swap_b32 v96, v98\n\t" \
    "v_permlane16_swap_b32 v97, v99\n\t" \
    "v_permlane16_swap_b32 v100, v102\n\t" \
    "v_permlane16_swap_b32 v101, v103\n\t" \
    "v_permlane16_swap_b32 v104, v106\n\t" \
    "v_permlane16_swap_b32 v105, v107\n\t" \
    "v_permlane16_swap_b32 v108, v110\n\t" \
    "v_permlane16_swap_b32 v109, v111\n\t" \
    "s_waitcnt vmcnt(7)\n\t" \
    "v_max_u32 v196, v200, v201\n\t" \
    "v_max3_u32 v196, v202, v203, v196\n\t" \
    "s_waitcnt vmcnt(6)\n\t" \
    "v_max3_u32 v196, v204, v205, v196\n\t" \
    "v_max3_u32 v196, v206, v207, v196\n\t" \
    "s_waitcnt vmcnt(5)\n\t" \
    "v_max3_u32 v196, v208, v209, v196\n\t" \
    "v_max3_u32 v196, v210, v211, v196\n\t" \
    "s_waitcnt vmcnt(4)\n\t" \
    "v_max3_u32 v196, v212, v213, v196\n\t" \
    "v_max3_u32 v196, v214, v215, v196\n\t" \
    "s_waitcnt vmcnt(3)\n\t" \
    "v_max3_u32 v196, v216, v217, v196\n\t" \
    "v_max3_u32 v196, v218, v219, v196\n\t" \
    "s_waitcnt vmcnt(2)\n\t" \
    "v_max3_u32 v196, v220, v221, v196\n\t" \
    "v_max3_u32 v196, v222, v223, v196\n\t" \
    "s_waitcnt vmcnt(1)\n\t" \
    "v_max3_u32 v196, v224, v225, v196\n\t" \
    "v_max3_u32 v196, v226, v227, v196\n\t" \
    "s_waitcnt vmcnt(0)\n\t" \
    "v_max3_u32 v196, v228, v229, v196\n\t" \
    "v_max3_u32 v196, v230, v231, v196\n\t" \
    "v_cmp_eq_u32 vcc, -1, v196\n\t" \
    "s_cbranch_vccz .Lxd%=\n\t" \
    ".Lxl%=:\n\t" \
    "s_mov_b32 %[a], 0\n\t" \
    "v_max_u32 v196, v200, v201\n\t" \
    "v_max3_u32 v196, v202, v203, v196\n\t" \
    "v_cmp_eq_u32 vcc, -1, v196\n\t" \
    "s_cbranch_vccz .Lxs0_%=\n\t" \
    "buffer_load_dwordx4 v[200:203], %[l16], %[rs], %[s0] offen offset:0 sc1\n\t" \
    "s_mov_b32 %[a], 1\n\t" \
    ".Lxs0_%=:\n\t" \
    "v_max_u32 v196, v204, v205\n\t" \
    "v_max3_u32 v196, v206, v207, v196\n\t" \
    "v_cmp_eq_u32 vcc, -1, v196\n\t" \
    "s_cbranch_vccz .Lxs1_%=\n\t" \
    "buffer_load_dwordx4 v[204:207], %[l16], %[rs], %[s0] offen offset:1024 sc1\n\t" \
    "s_mov_b32 %[a], 1\n\t" \
    ".Lxs1_%=:\n\t" \
    "v_max_u32 v196, v208, v209\n\t" \
    "v_max3_u32 v196, v210, v211, v196\n\t" \
    "v_cmp_eq_u32 vcc, -1, v196\n\t" \
    "s_cbranch_vccz .Lxs2_%=\n\t" \
    "buffer_load_dwordx4 v[208:211], %[l16], %[rs], %[s0] offen offset:2048 sc1\n\t" \
    "s_mov_b32 %[a], 1\n\t" \
    ".Lxs2_%=:\n\t" \
    "v_max_u32 v196, v212, v213\n\t" \
    "v_max3_u32 v196, v214, v215, v196\n\t" \
    "v_cmp_eq_u32 vcc, -1, v196\n\t" \
    "s_cbranch_vccz .Lxs3_%=\n\t" \
    "buffer_load_dwordx4 v[212:215], %[l16], %[rs], %[s0] offen offset:3072 sc1\n\t" \
    "s_mov_b32 %[a], 1\n\t" \
    ".Lxs3_%=:\n\t" \
    "v_max_u32 v196, v216, v217\n\t" \
    "v_max3_u32 v196, v218, v219, v196\n\t" \
    "v_cmp_eq_u32 vcc, -1, v196\n\t" \
    "s_cbranch_vccz .Lxs4_%=\n\t" \
    "buffer_load_dwordx4 v[216:219], %[l16], %[rs], %[s1] offen offset:0 sc1\n\t" \
    "s_mov_b32 %[a], 1\n\t" \
    ".Lxs4_%=:\n\t" \
    "v_max_u32 v196, v220, v221\n\t" \
    "v_max3_u32 v196, v222, v223, v196\n\t" \
    "v_cmp_eq_u32 vcc, -1, v196\n\t" \
    "s_cbranch_vccz .Lxs5_%=\n\t" \
    "buffer_load_dwordx4 v[220:223], %[l16], %[rs], %[s1] offen offset:1024 sc1\n\t" \
    "s_mov_b32 %[a], 1\n\t" \
    ".Lxs5_%=:\n\t" \
    "v_max_u32 v196, v224, v225\n\t" \
    "v_max3_u32 v196, v226, v227, v196\n\t" \
    "v_cmp_eq_u32 vcc, -1, v196\n\t" \
    "s_cbranch_vccz .Lxs6_%=\n\t" \
    "buffer_load_dwordx4 v[224:227], %[l16], %[rs], %[s1] offen offset:2048 sc1\n\t" \
    "s_mov_b32 %[a], 1\n\t" \
    ".Lxs6_%=:\n\t" \
    "v_max_u32 v196, v228, v229\n\t" \
    "v_max3_u32 v196, v230, v231, v196\n\t" \
    "v_cmp_eq_u32 vcc, -1, v196\n\t" \
    "s_cbranch_vccz .Lxs7_%=\n\t" \
    "buffer_load_dwordx4 v[228:231], %[l16], %[rs], %[s1] offen offset:3072 sc1\n\t" \
    "s_mov_b32 %[a], 1\n\t" \
    ".Lxs7_%=:\n\t" \
    "s_cmp_eq_u32 %[a], 0\n\t" \
    "s_cbranch_scc1 .Lxd%=\n\t" \
    "s_waitcnt vmcnt(0)\n\t" \
    "s_sub_u32 %[n], %[n], 1\n\t" \
    "s_cmp_lg_u32 %[n], 0\n\t" \
    "s_cbranch_scc1 .Lxl%=\n\t" \
    "s_branch .Lxe%=\n\t" \
    ".Lxd%=:\n\t" \
    ASM_ST2 \
    "v_mfma_f32_16x16x32_bf16 v[120:123], v[128:131], v[200:203], 0\n\t" \
    "v_mfma_f32_16x16x32_bf16 v[124:127], v[144:147], v[200:203], 0\n\t" \
    "v_mfma_f32_16x16x32_bf16 v[120:123], v[132:135], v[204:207], v[120:123]\n\t" \
    "v_mfma_f32_16x16x32_bf16 v[124:127], v[148:151], v[204:207], v[124:127]\n\t" \
    "v_mfma_f32_16x16x32_bf16 v[120:123], v[136:139], v[208:211], v[120:123]\n\t" \
    "v_mfma_f32_16x16x32_bf16 v[124:127], v[152:155], v[208:211], v[124:127]\n\t" \
    "v_mfma_f32_16x16x32_bf16 v[120:123], v[140:143], v[212:215], v[120:123]\n\t" \
    "v_mfma_f32_16x16x32_bf16 v[124:127], v[156:159], v[212:215], v[124:127]\n\t" \
    "v_mfma_f32_16x16x32_bf16 v[112:115], v[96:99], v[220:223], 0\n\t" \
    "v_mfma_f32_16x16x32_bf16 v[116:119], v[104:107], v[220:223], 0\n\t" \
    "v_mfma_f32_16x16x32_bf16 v[120:123], v[176:179], v[216:219], v[120:123]\n\t" \
    "v_mfma_f32_16x16x32_bf16 v[124:127], v[180:183], v[216:219], v[124:127]\n\t" \
    "v_mfma_f32_16x16x32_bf16 v[112:115], v[100:103], v[224:227], v[112:115]\n\t" \
    "v_mfma_f32_16x16x32_bf16 v[116:119], v[108:111], v[224:227], v[116:119]\n\t" \
    "v_mfma_f32_16x16x32_bf16 v[112:115], v[176:179], v[228:231], v[112:115]\n\t" \
    "v_mfma_f32_16x16x32_bf16 v[116:119], v[180:183], v[228:231], v[116:119]\n\t" \
    "s_nop 1\n\t" \
    "ds_write_b128 %[pw], v[120:123] offset:16384\n\t" \
    "ds_write_b128 %[pw], v[124:127] offset:24576\n\t" \
    "s_nop 3\n\t" \
    "ds_write_b128 %[pw], v[112:115] offset:0\n\t" \
    "ds_write_b128 %[pw], v[116:119] offset:8192\n\t" \
    "v_cmp_ge_u32 vcc, %[tn], v248\n\t" \
    "s_mov_b32 %[a], 0\n\t" \
    "s_cbranch_vccnz .Lxv%=\n\t" \
    "buffer_load_dwordx4 v[128:131], %[l16], %[rp], %[sv0] offen offset:0 sc1\n\t" \
    "buffer_load_dwordx4 v[132:135], %[l16], %[rp], %[sv0] offen offset:1024 sc1\n\t" \
    "buffer_load_dwordx4 v[136:139], %[l16], %[rp], %[sv0] offen offset:2048 sc1\n\t" \
    "buffer_load_dwordx4 v[140:143], %[l16], %[rp], %[sv0] offen offset:3072 sc1\n\t" \
    "buffer_load_dwordx4 v[144:147], %[l16], %[rp], %[sv1] offen offset:0 sc1\n\t" \
    "buffer_load_dwordx4 v[148:151], %[l16], %[rp], %[sv1] offen offset:1024 sc1\n\t" \
    "buffer_load_dwordx4 v[152:155], %[l16], %[rp], %[sv1] offen offset:2048 sc1\n\t" \
    "buffer_load_dwordx4 v[156:159], %[l16], %[rp], %[sv1] offen offset:3072 sc1\n\t" \
    "s_mov_b32 %[a], 1\n\t" \
    ".Lxv%=:\n\t" \
    "s_cmp_eq_u32 %[fin], 0\n\t" \
    "s_waitcnt lgkmcnt(0)\n\t" \
    "s_barrier\n\t" ASM_ST \
    "s_cbranch_scc1 .Lxe%=\n\t" \
    "ds_read_b128 v[200:203], %[pr] offset:0\n\t" \
    "ds_read_b128 v[204:207], %[pr] offset:1024\n\t" \
    "ds_read_b128 v[208:211], %[pr] offset:2048\n\t" \
    "ds_read_b128 v[212:215], %[pr] offset:3072\n\t" \
    "ds_read_b128 v[216:219], %[pr] offset:4096\n\t" \
    "ds_read_b128 v[220:223], %[pr] offset:5120\n\t" \
    "ds_read_b128 v[224:227], %[pr] offset:6144\n\t" \
    "ds_read_b128 v[228:231], %[pr] offset:7168\n\t" \
    "v_lshlrev_b32 v240, 16, v250\n\t" \
    "v_and_b32 v241, 0xffff0000, v250\n\t" \
    "v_lshlrev_b32 v242, 16, v251\n\t" \
    "v_and_b32 v243, 0xffff0000, v251\n\t" \
    "s_waitcnt lgkmcnt(6)\n\t" \
    "v_pk_add_f32 v[200:201], v[200:201], v[204:205]\n\t" \
    "v_pk_add_f32 v[202:203], v[202:203], v[206:207]\n\t" \
    "s_waitcnt lgkmcnt(4)\n\t" \
    "v_pk_add_f32 v[208:209], v[208:209], v[212:213]\n\t" \
    "v_pk_add_f32 v[210:211], v[210:211], v[214:215]\n\t" \
    "v_pk_add_f32 v[200:201], v[200:201], v[208:209]\n\t" \
    "v_pk_add_f32 v[202:203], v[202:203], v[210:211]\n\t" \
    "s_waitcnt lgkmcnt(2)\n\t" \
    "v_pk_add_f32 v[216:217], v[216:217], v[220:221]\n\t" \
    "v_pk_add_f32 v[218:219], v[218:219], v[222:223]\n\t" \
    "s_waitcnt lgkmcnt(0)\n\t" \
    "v_pk_add_f32 v[224:225], v[224:225], v[228:229]\n\t" \
    "v_pk_add_f32 v[226:227], v[226:227], v[230:231]\n\t" \
    "v_pk_add_f32 v[216:217], v[216:217], v[224:225]\n\t" \
    "v_pk_add_f32 v[218:219], v[218:219], v[226:227]\n\t" \
    "v_pk_add_f32 v[200:201], v[200:201], v[216:217]\n\t" \
    "v_pk_add_f32 v[202:203], v[202:203], v[218:219]\n\t" \
    "v_pk_add_f32 v[200:201], v[200:201], v[240:241]\n\t" \
    "v_pk_add_f32 v[202:203], v[202:203], v[242:243]\n\t" \
    "v_cvt_pk_bf16_f32 v200, v200, v201\n\t" \
    "v_cvt_pk_bf16_f32 v201, v202, v203\n\t" \
    "v_min_u32 v200, -2, v200\n\t" \
    "v_min_u32 v201, -2, v201\n\t" \
    "buffer_store_dwordx2 v[200:201], %[xo], %[rd], %[sa] offen sc1\n\t" \
    ".Lxe%=:\n\t"
#define ASM_W4_M \
    ".Lc7_%=:\n\t" \
    "s_waitcnt vmcnt(0)\n\t" \
    "v_max_u32 v212, v188, v189\n\t" \
    "v_max3_u32 v212, v190, v191, v212\n\t" \
    "v_cmp_eq_u32 vcc, -1, v212\n\t" \
    "s_cbranch_vccnz .Ls7_%=\n\t" \
    ".Lk7_%=:\n\t" \
    ASM_ST2 \
    "buffer_load_dwordx4 v[184:187], %[l16], %[rs], %[s1] offen offset:2048 sc1\n\t" \
    "buffer_load_dwordx4 v[180:183], %[l16], %[rs], %[s1] offen offset:1024 sc1\n\t" \
    "buffer_load_dwordx4 v[176:179], %[l16], %[rs], %[s1] offen offset:0 sc1\n\t" \
    "buffer_load_dwordx4 v[172:175], %[l16], %[rs], %[s0] offen offset:3072 sc1\n\t" \
    "buffer_load_dwordx4 v[168:171], %[l16], %[rs], %[s0] offen offset:2048 sc1\n\t" \
    "buffer_load_dwordx4 v[164:167], %[l16], %[rs], %[s0] offen offset:1024 sc1\n\t" \
    "buffer_load_dwordx4 v[160:163], %[l16], %[rs], %[s0] offen offset:0 sc1\n\t" \
    "v_mfma_f32_16x16x32_bf16 v[192:195], v[28:31], v[188:191], 0\n\t" \
    "v_mfma_f32_16x16x32_bf16 v[196:199], v[60:63], v[188:191], 0\n\t" \
    "v_mfma_f32_16x16x32_bf16 v[200:203], v[92:95], v[188:191], 0\n\t" \
    "v_mfma_f32_16x16x32_bf16 v[204:207], v[124:127], v[188:191], 0\n\t" \
    "v_mfma_f32_16x16x32_bf16 v[208:211], v[156:159], v[188:191], 0\n\t" \
    ".Lc6_%=:\n\t" \
    "s_waitcnt vmcnt(6)\n\t" \
    "v_max_u32 v212, v184, v185\n\t" \
    "v_max3_u32 v212, v186, v187, v212\n\t" \
    "v_cmp_eq_u32 vcc, -1, v212\n\t" \
    "s_cbranch_vccnz .Ls6_%=\n\t" \
    ".Lk6_%=:\n\t" \
    "v_mfma_f32_16x16x32_bf16 v[192:195], v[24:27], v[184:187], v[192:195]\n\t" \
    "v_mfma_f32_16x16x32_bf16 v[196:199], v[56:59], v[184:187], v[196:199]\n\t" \
    "v_mfma_f32_16x16x32_bf16 v[200:203], v[88:91], v[184:187], v[200:203]\n\t" \
    "v_mfma_f32_16x16x32_bf16 v[204:207], v[120:123], v[184:187], v[204:207]\n\t" \
    "v_mfma_f32_16x16x32_bf16 v[208:211], v[152:155], v[184:187], v[208:211]\n\t" \
    ".Lc5_%=:\n\t" \
    "s_waitcnt vmcnt(5)\n\t" \
    "v_max_u32 v212, v180, v181\n\t" \
    "v_max3_u32 v212, v182, v183, v212\n\t" \
    "v_cmp_eq_u32 vcc, -1, v212\n\t" \
    "s_cbranch_vccnz .Ls5_%=\n\t" \
    ".Lk5_%=:\n\t" \
    "v_mfma_f32_16x16x32_bf16 v[192:195], v[20:23], v[180:183], v[192:195]\n\t" \
    "v_mfma_f32_16x16x32_bf16 v[196:199], v[52:55], v[180:183], v[196:199]\n\t" \
    "v_mfma_f32_16x16x32_bf16 v[200:203], v[84:87], v[180:183], v[200:203]\n\t" \
    "v_mfma_f32_16x16x32_bf16 v[204:207], v[116:119], v[180:183], v[204:207]\n\t" \
    "v_mfma_f32_16x16x32_bf16 v[208:211], v[148:151], v[180:183], v[208:211]\n\t" \
    ".Lc4_%=:\n\t" \
    "s_waitcnt vmcnt(4)\n\t" \
    "v_max_u32 v212, v176, v177\n\t" \
    "v_max3_u32 v212, v178, v179, v212\n\t" \
    "v_cmp_eq_u32 vcc, -1, v212\n\t" \
    "s_cbranch_vccnz .Ls4_%=\n\t" \
    ".Lk4_%=:\n\t" \
    "v_mfma_f32_16x16x32_bf16 v[192:195], v[16:19], v[176:179], v[192:195]\n\t" \
    "v_mfma_f32_16x16x32_bf16 v[196:199], v[48:51], v[176:179], v[196:199]\n\t" \
    "v_mfma_f32_16x16x32_bf16 v[200:203], v[80:83], v[176:179], v[200:203]\n\t" \
    "v_mfma_f32_16x16x32_bf16 v[204:207], v[112:115], v[176:179], v[204:207]\n\t" \
    "v_mfma_f32_16x16x32_bf16 v[208:211], v[144:147], v[176:179], v[208:211]\n\t" \
    ".Lc3_%=:\n\t" \
    "s_waitcnt vmcnt(3)\n\t" \
    "v_max_u32 v212, v172, v173\n\t" \
    "v_max3_u32 v212, v174, v175, v212\n\t" \
    "v_cmp_eq_u32 vcc, -1, v212\n\t" \
    "s_cbranch_vccnz .Ls3_%=\n\t" \
    ".Lk3_%=:\n\t" \
    "v_mfma_f32_16x16x32_bf16 v[192:195], v[12:15], v[172:175], v[192:195]\n\t" \
    "v_mfma_f32_16x16x32_bf16 v[196:199], v[44:47], v[172:175], v[196:199]\n\t" \
    "v_mfma_f32_16x16x32_bf16 v[200:203], v[76:79], v[172:175], v[200:203]\n\t" \
    "v_mfma_f32_16x16x32_bf16 v[204:207], v[108:111], v[172:175], v[204:207]\n\t" \
    "v_mfma_f32_16x16x32_bf16 v[208:211], v[140:143], v[172:175], v[208:211]\n\t" \
    ".Lc2_%=:\n\t" \
    "s_waitcnt vmcnt(2)\n\t" \
    "v_max_u32 v212, v168, v169\n\t" \
    "v_max3_u32 v212, v170, v171, v212\n\t" \
    "v_cmp_eq_u32 vcc, -1, v212\n\t" \
    "s_cbranch_vccnz .Ls2_%=\n\t" \
    ".Lk2_%=:\n\t" \
    "buffer_load_dwordx4 v[188:191], %[l16], %[rs], %[n1] offen offset:3072 sc1\n\t" \
    "v_mfma_f32_16x16x32_bf16 v[192:195], v[8:11], v[168:171], v[192:195]\n\t" \
    "v_mfma_f32_16x16x32_bf16 v[196:199], v[40:43], v[168:171], v[196:199]\n\t" \
    "v_mfma_f32_16x16x32_bf16 v[200:203], v[72:75], v[168:171], v[200:203]\n\t" \
    "v_mfma_f32_16x16x32_bf16 v[204:207], v[104:107], v[168:171], v[204:207]\n\t" \
    "v_mfma_f32_16x16x32_bf16 v[208:211], v[136:139], v[168:171], v[208:211]\n\t" \
    ".Lc1_%=:\n\t" \
    "s_waitcnt vmcnt(2)\n\t" \
    "v_max_u32 v212, v164, v165\n\t" \
    "v_max3_u32 v212, v166, v167, v212\n\t" \
    "v_cmp_eq_u32 vcc, -1, v212\n\t" \
    "s_cbranch_vccnz .Ls1_%=\n\t" \
    ".Lk1_%=:\n\t" \
    "v_mfma_f32_16x16x32_bf16 v[192:195], v[4:7], v[164:167], v[192:195]\n\t" \
    "v_mfma_f32_16x16x32_bf16 v[196:199], v[36:39], v[164:167], v[196:199]\n\t" \
    "v_mfma_f32_16x16x32_bf16 v[200:203], v[68:71], v[164:167], v[200:203]\n\t" \
    "v_mfma_f32_16x16x32_bf16 v[204:207], v[100:103], v[164:167], v[204:207]\n\t" \
    "v_mfma_f32_16x16x32_bf16 v[208:211], v[132:135], v[164:167], v[208:211]\n\t" \
    ".Lc0_%=:\n\t" \
    "s_waitcnt vmcnt(1)\n\t" \
    "v_max_u32 v212, v160, v161\n\t" \
    "v_max3_u32 v212, v162, v163, v212\n\t" \
    "v_cmp_eq_u32 vcc, -1, v212\n\t" \
    "s_cbranch_vccnz .Ls0_%=\n\t" \
    ".Lk0_%=:\n\t" \
    "v_mfma_f32_16x16x32_bf16 v[192:195], v[0:3], v[160:163], v[192:195]\n\t" \
    "v_mfma_f32_16x16x32_bf16 v[196:199], v[32:35], v[160:163], v[196:199]\n\t" \
    "v_mfma_f32_16x16x32_bf16 v[200:203], v[64:67], v[160:163], v[200:203]\n\t" \
    "v_mfma_f32_16x16x32_bf16 v[204:207], v[96:99], v[160:163], v[204:207]\n\t" \
    "v_mfma_f32_16x16x32_bf16 v[208:211], v[128:131], v[160:163], v[208:211]\n\t" \
    "v_mov_b32 v212, %[un]\n\t" \
    "ds_write_b128 %[ld], v[192:195] offset:0\n\t" \
    "ds_write_b128 %[ld], v[196:199] offset:6144\n\t" \
    "ds_write_b128 %[ld], v[200:203] offset:12288\n\t" \
    "s_nop 0\n\t" \
    "ds_write_b128 %[ld], v[204:207] offset:18432\n\t" \
    "s_nop 1\n\t" \
    "ds_write_b128 %[ld], v[208:211] offset:24576\n\t" \
    "ds_write_b32 %[fl], v212\n\t" \
    "s_branch .Lme%=\n\t" \
    ".Ls7_%=:\n\t" \
    "s_cmp_eq_u32 %[n], 0\n\t" \
    "s_cbranch_scc1 .Lk7_%=\n\t" \
    "s_sub_u32 %[n], %[n], 1\n\t" \
    "s_sleep 1\n\t" \
    "buffer_load_dwordx4 v[188:191], %[l16], %[rs], %[s1] offen offset:3072 sc1\n\t" \
    "s_branch .Lc7_%=\n\t" \
    ".Ls6_%=:\n\t" \
    "s_cmp_eq_u32 %[n], 0\n\t" \
    "s_cbranch_scc1 .Lk6_%=\n\t" \
    "s_sub_u32 %[n], %[n], 1\n\t" \
    "buffer_load_dwordx4 v[184:187], %[l16], %[rs], %[s1] offen offset:2048 sc1\n\t" \
    "s_waitcnt vmcnt(0)\n\t" \
    "s_branch .Lc6_%=\n\t" \
    ".Ls5_%=:\n\t" \
    "s_cmp_eq_u32 %[n], 0\n\t" \
    "s_cbranch_scc1 .Lk5_%=\n\t" \
    "s_sub_u32 %[n], %[n], 1\n\t" \
    "buffer_load_dwordx4 v[180:183], %[l16], %[rs], %[s1] offen offset:1024 sc1\n\t" \
    "s_waitcnt vmcnt(0)\n\t" \
    "s_branch .Lc5_%=\n\t" \
    ".Ls4_%=:\n\t" \
    "s_cmp_eq_u32 %[n], 0\n\t" \
    "s_cbranch_scc1 .Lk4_%=\n\t" \
    "s_sub_u32 %[n], %[n], 1\n\t" \
    "buffer_load_dwordx4 v[176:179], %[l16], %[rs], %[s1] offen offset:0 sc1\n\t" \
    "s_waitcnt vmcnt(0)\n\t" \
    "s_branch .Lc4_%=\n\t" \
    ".Ls3_%=:\n\t" \
    "s_cmp_eq_u32 %[n], 0\n\t" \
    "s_cbranch_scc1 .Lk3_%=\n\t" \
    "s_sub_u32 %[n], %[n], 1\n\t" \
    "buffer_load_dwordx4 v[172:175], %[l16], %[rs], %[s0] offen offset:3072 sc1\n\t" \
    "s_waitcnt vmcnt(0)\n\t" \
    "s_branch .Lc3_%=\n\t" \
    ".Ls2_%=:\n\t" \
    "s_cmp_eq_u32 %[n], 0\n\t" \
    "s_cbranch_scc1 .Lk2_%=\n\t" \
    "s_sub_u32 %[n], %[n], 1\n\t" \
    "buffer_load_dwordx4 v[168:171], %[l16], %[rs], %[s0] offen offset:2048 sc1\n\t" \
    "s_waitcnt vmcnt(0)\n\t" \
    "s_branch .Lc2_%=\n\t" \
    ".Ls1_%=:\n\t" \
    "s_cmp_eq_u32 %[n], 0\n\t" \
    "s_cbranch_scc1 .Lk1_%=\n\t" \
    "s_sub_u32 %[n], %[n], 1\n\t" \
    "buffer_load_dwordx4 v[164:167], %[l16], %[rs], %[s0] offen offset:1024 sc1\n\t" \
    "s_waitcnt vmcnt(0)\n\t" \
    "s_branch .Lc1_%=\n\t" \
    ".Ls0_%=:\n\t" \
    "s_cmp_eq_u32 %[n], 0\n\t" \
    "s_cbranch_scc1 .Lk0_%=\n\t" \
    "s_sub_u32 %[n], %[n], 1\n\t" \
    "buffer_load_dwordx4 v[160:163], %[l16], %[rs], %[s0] offen offset:0 sc1\n\t" \
    "s_waitcnt vmcnt(0)\n\t" \
    "s_branch .Lc0_%=\n\t" \
    ".Lme%=:\n\t"
#define ASM_W4F_0 \
    "s_branch .Lf0%=\n\t" \
    ".Lfr%=:\n\t" \
    "s_cmp_eq_u32 %[n], 0\n\t" \
    "s_cbranch_scc1 .Lfb%=\n\t" \
    "s_sub_u32 %[n], %[n], 1\n\t" \
    "s_sleep 6\n\t" \
    "buffer_load_dwordx2 v[164:165], %[o0], %[rd], %[c1] offen sc1\n\t" \
    "buffer_load_dwordx2 v[166:167], %[o1], %[rd], %[c1] offen sc1\n\t" \
    "buffer_load_dwordx2 v[168:169], %[o2], %[rd], %[c1] offen sc1\n\t" \
    "buffer_load_dwordx2 v[170:171], %[o3], %[rd], %[c1] offen sc1\n\t" \
    "buffer_load_dwordx2 v[172:173], %[o4], %[rd], %[c1] offen sc1\n\t" \
    "buffer_load_dwordx2 v[174:175], %[o0], %[rd], %[ca] offen sc1\n\t" \
    "buffer_load_dwordx2 v[176:177], %[o1], %[rd], %[ca] offen sc1\n\t" \
    "buffer_load_dwordx2 v[178:179], %[o2], %[rd], %[ca] offen sc1\n\t" \
    "buffer_load_dwordx2 v[180:181], %[o3], %[rd], %[ca] offen sc1\n\t" \
    "buffer_load_dwordx2 v[182:183], %[o4], %[rd], %[ca] offen sc1\n\t" \
    "s_waitcnt vmcnt(0)\n\t" \
    "s_branch .Lfa%=\n\t" \
    ".Lfs0_%=:\n\t" \
    "s_cmp_eq_u32 %[n], 0\n\t" \
    "s_cbranch_scc1 .Lfu0_%=\n\t" \
    "s_sub_u32 %[n], %[n], 1\n\t" \
    "s_sleep 1\n\t" \
    "s_branch .Lft0_%=\n\t" \
    ".Lfs1_%=:\n\t" \
    "s_cmp_eq_u32 %[n], 0\n\t" \
    "s_cbranch_scc1 .Lfu1_%=\n\t" \
    "s_sub_u32 %[n], %[n], 1\n\t" \
    "s_sleep 1\n\t" \
    "s_branch .Lft1_%=\n\t" \
    ".Lfx%=:\n\t" \
    "s_cmp_eq_u32 %[n], 0\n\t" \
    "s_cbranch_scc1 .Lfz%=\n\t" \
    "s_sub_u32 %[n], %[n], 1\n\t" \
    "s_branch .Lfy%=\n\t" \
    ".Lf0%=:\n\t" \
    "s_waitcnt vmcnt(1)\n\t" \
    ".Lfa%=:\n\t" \
    "v_max_u32 v194, v164, v165\n\t" \
    "v_max3_u32 v194, v166, v167, v194\n\t" \
    "v_max3_u32 v194, v168, v169, v194\n\t" \
    "v_max3_u32 v194, v170, v171, v194\n\t" \
    "v_max3_u32 v194, v172, v173, v194\n\t" \
    "v_max3_u32 v194, v174, v175, v194\n\t" \
    "v_max3_u32 v194, v176, v177, v194\n\t" \
    "v_max3_u32 v194, v178, v179, v194\n\t" \
    "v_max3_u32 v194, v180, v181, v194\n\t" \
    "v_max3_u32 v194, v182, v183, v194\n\t" \
    "v_cmp_eq_u32 vcc, -1, v194\n\t" \
    "s_cbranch_vccnz .Lfr%=\n\t" \
    ".Lfb%=:\n\t" \
    "v_lshlrev_b32 v64, 16, v164\n\t" \
    "v_and_b32 v65, 0xffff0000, v164\n\t" \
    "v_lshlrev_b32 v84, 16, v174\n\t" \
    "v_and_b32 v85, 0xffff0000, v174\n\t" \
    "v_lshlrev_b32 v66, 16, v165\n\t" \
    "v_and_b32 v67, 0xffff0000, v165\n\t" \
    "v_lshlrev_b32 v86, 16, v175\n\t" \
    "v_and_b32 v87, 0xffff0000, v175\n\t" \
    "v_lshlrev_b32 v68, 16, v166\n\t" \
    "v_and_b32 v69, 0xffff0000, v166\n\t" \
    "v_lshlrev_b32 v88, 16, v176\n\t" \
    "v_and_b32 v89, 0xffff0000, v176\n\t" \
    "v_lshlrev_b32 v70, 16, v167\n\t" \
    "v_and_b32 v71, 0xffff0000, v167\n\t" \
    "v_lshlrev_b32 v90, 16, v177\n\t" \
    "v_and_b32 v91, 0xffff0000, v177\n\t" \
    "v_lshlrev_b32 v72, 16, v168\n\t" \
    "v_and_b32 v73, 0xffff0000, v168\n\t" \
    "v_lshlrev_b32 v92, 16, v178\n\t" \
    "v_and_b32 v93, 0xffff0000, v178\n\t" \
    "v_lshlrev_b32 v74, 16, v169\n\t" \
    "v_and_b32 v75, 0xffff0000, v169\n\t" \
    "v_lshlrev_b32 v94, 16, v179\n\t" \
    "v_and_b32 v95, 0xffff0000, v179\n\t" \
    "v_lshlrev_b32 v76, 16, v170\n\t" \
    "v_and_b32 v77, 0xffff0000, v170\n\t" \
    "v_lshlrev_b32 v96, 16, v180\n\t" \
    "v_and_b32 v97, 0xffff0000, v180\n\t" \
    "v_lshlrev_b32 v78, 16, v171\n\t" \
    "v_and_b32 v79, 0xffff0000, v171\n\t" \
    "v_lshlrev_b32 v98, 16, v181\n\t" \
    "v_and_b32 v99, 0xffff0000, v181\n\t" \
    "v_lshlrev_b32 v80, 16, v172\n\t" \
    "v_and_b32 v81, 0xffff0000, v172\n\t" \
    "v_lshlrev_b32 v100, 16, v182\n\t" \
    "v_and_b32 v101, 0xffff0000, v182\n\t" \
    "v_lshlrev_b32 v82, 16, v173\n\t" \
    "v_and_b32 v83, 0xffff0000, v173\n\t" \
    "v_lshlrev_b32 v102, 16, v183\n\t" \
    "v_and_b32 v103, 0xffff0000, v183\n\t" \
    "v_pk_add_f32 v[64:65], v[64:65], v[84:85]\n\t" \
    "v_pk_add_f32 v[66:67], v[66:67], v[86:87]\n\t" \
    "v_pk_add_f32 v[68:69], v[68:69], v[88:89]\n\t" \
    "v_pk_add_f32 v[70:71], v[70:71], v[90:91]\n\t" \
    "v_pk_add_f32 v[72:73], v[72:73], v[92:93]\n\t" \
    "v_pk_add_f32 v[74:75], v[74:75], v[94:95]\n\t" \
    "v_pk_add_f32 v[76:77], v[76:77], v[96:97]\n\t" \
    "v_pk_add_f32 v[78:79], v[78:79], v[98:99]\n\t" \
    "v_pk_add_f32 v[80:81], v[80:81], v[100:101]\n\t" \
    "v_pk_add_f32 v[82:83], v[82:83], v[102:103]\n\t" \
    ".Lft0_%=:\n\t" \
    "ds_read_b32 v194, %[f0]\n\t" \
    "s_waitcnt lgkmcnt(0)\n\t" \
    "v_cmp_ge_u32 vcc, %[u], v194\n\t" \
    "s_cbranch_vccnz .Lfs0_%=\n\t" \
    ".Lfu0_%=:\n\t" \
    "ds_read_b128 v[84:87], %[la] offset:0\n\t" \
    "ds_read_b128 v[88:91], %[la] offset:6144\n\t" \
    "ds_read_b128 v[92:95], %[la] offset:12288\n\t" \
    "ds_read_b128 v[96:99], %[la] offset:18432\n\t" \
    "ds_read_b128 v[100:103], %[la] offset:24576\n\t" \
    "ds_read_b128 v[104:107], %[la] offset:1024\n\t" \
    "ds_read_b128 v[108:111], %[la] offset:7168\n\t" \
    "ds_read_b128 v[112:115], %[la] offset:13312\n\t" \
    "ds_read_b128 v[116:119], %[la] offset:19456\n\t" \
    "ds_read_b128 v[120:123], %[la] offset:25600\n\t" \
    "ds_read_b128 v[124:127], %[la] offset:2048\n\t" \
    "s_waitcnt lgkmcnt(10)\n\t" \
    "v_pk_add_f32 v[64:65], v[64:65], v[84:85]\n\t" \
    "v_pk_add_f32 v[66:67], v[66:67], v[86:87]\n\t" \
    "ds_read_b128 v[128:131], %[la] offset:8192\n\t" \
    "s_waitcnt lgkmcnt(10)\n\t" \
    "v_pk_add_f32 v[68:69], v[68:69], v[88:89]\n\t" \
    "v_pk_add_f32 v[70:71], v[70:71], v[90:91]\n\t" \
    "ds_read_b128 v[132:135], %[la] offset:14336\n\t" \
    "s_waitcnt lgkmcnt(10)\n\t" \
    "v_pk_add_f32 v[72:73], v[72:73], v[92:93]\n\t" \
    "v_pk_add_f32 v[74:75], v[74:75], v[94:95]\n\t" \
    "ds_read_b128 v[136:139], %[la] offset:20480\n\t" \
    "s_waitcnt lgkmcnt(10)\n\t" \
    "v_pk_add_f32 v[76:77], v[76:77], v[96:97]\n\t" \
    "v_pk_add_f32 v[78:79], v[78:79], v[98:99]\n\t" \
    "ds_read_b128 v[140:143], %[la] offset:26624\n\t" \
    "s_waitcnt lgkmcnt(10)\n\t" \
    "v_pk_add_f32 v[80:81], v[80:81], v[100:101]\n\t" \
    "v_pk_add_f32 v[82:83], v[82:83], v[102:103]\n\t" \
    "ds_read_b128 v[144:147], %[la] offset:3072\n\t" \
    "s_waitcnt lgkmcnt(10)\n\t" \
    "v_pk_add_f32 v[64:65], v[64:65], v[104:105]\n\t" \
    "v_pk_add_f32 v[66:67], v[66:67], v[106:107]\n\t" \
    "ds_read_b128 v[148:151], %[la] offset:9216\n\t" \
    "s_waitcnt lgkmcnt(10)\n\t" \
    "v_pk_add_f32 v[68:69], v[68:69], v[108:109]\n\t" \
    "v_pk_add_f32 v[70:71], v[70:71], v[110:111]\n\t" \
    "ds_read_b128 v[152:155], %[la] offset:15360\n\t" \
    "s_waitcnt lgkmcnt(10)\n\t" \
    "v_pk_add_f32 v[72:73], v[72:73], v[112:113]\n\t" \
    "v_pk_add_f32 v[74:75], v[74:75], v[114:115]\n\t" \
    "ds_read_b128 v[156:159], %[la] offset:21504\n\t" \
    "s_waitcnt lgkmcnt(10)\n\t" \
    "v_pk_add_f32 v[76:77], v[76:77], v[116:117]\n\t" \
    "v_pk_add_f32 v[78:79], v[78:79], v[118:119]\n\t" \
    "ds_read_b128 v[160:163], %[la] offset:27648\n\t" \
    "s_waitcnt lgkmcnt(10)\n\t" \
    "v_pk_add_f32 v[80:81], v[80:81], v[120:121]\n\t" \
    "v_pk_add_f32 v[82:83], v[82:83], v[122:123]\n\t" \
    "s_waitcnt lgkmcnt(9)\n\t" \
    "v_pk_add_f32 v[64:65], v[64:65], v[124:125]\n\t" \
    "v_pk_add_f32 v[66:67], v[66:67], v[126:127]\n\t" \
    "s_waitcnt lgkmcnt(8)\n\t" \
    "v_pk_add_f32 v[68:69], v[68:69], v[128:129]\n\t" \
    "v_pk_add_f32 v[70:71], v[70:71], v[130:131]\n\t" \
    "s_waitcnt lgkmcnt(7)\n\t" \
    "v_pk_add_f32 v[72:73], v[72:73], v[132:133]\n\t" \
    "v_pk_add_f32 v[74:75], v[74:75], v[134:135]\n\t" \
    "s_waitcnt lgkmcnt(6)\n\t" \
    "v_pk_add_f32 v[76:77], v[76:77], v[136:137]\n\t" \
    "v_pk_add_f32 v[78:79], v[78:79], v[138:139]\n\t" \
    "s_waitcnt lgkmcnt(5)\n\t" \
    "v_pk_add_f32 v[80:81], v[80:81], v[140:141]\n\t" \
    "v_pk_add_f32 v[82:83], v[82:83], v[142:143]\n\t" \
    "s_waitcnt lgkmcnt(4)\n\t" \
    "v_pk_add_f32 v[64:65], v[64:65], v[144:145]\n\t" \
    "v_pk_add_f32 v[66:67], v[66:67], v[146:147]\n\t" \
    "s_waitcnt lgkmcnt(3)\n\t" \
    "v_pk_add_f32 v[68:69], v[68:69], v[148:149]\n\t" \
    "v_pk_add_f32 v[70:71], v[70:71], v[150:151]\n\t" \
    "s_waitcnt lgkmcnt(2)\n\t" \
    "v_pk_add_f32 v[72:73], v[72:73], v[152:153]\n\t" \
    "v_pk_add_f32 v[74:75], v[74:75], v[154:155]\n\t" \
    "s_waitcnt lgkmcnt(1)\n\t" \
    "v_pk_add_f32 v[76:77], v[76:77], v[156:157]\n\t" \
    "v_pk_add_f32 v[78:79], v[78:79], v[158:159]\n\t" \
    "s_waitcnt lgkmcnt(0)\n\t" \
    "v_pk_add_f32 v[80:81], v[80:81], v[160:161]\n\t" \
    "v_pk_add_f32 v[82:83], v[82:83], v[162:163]\n\t" \
    ".Lft1_%=:\n\t" \
    "ds_read_b32 v194, %[f1]\n\t" \
    "s_waitcnt lgkmcnt(0)\n\t" \
    "v_cmp_ge_u32 vcc, %[u], v194\n\t" \
    "s_cbranch_vccnz .Lfs1_%=\n\t" \
    ".Lfu1_%=:\n\t" \
    ASM_ST2 \
    "ds_read_b128 v[84:87], %[la] offset:4096\n\t" \
    "ds_read_b128 v[88:91], %[la] offset:10240\n\t" \
    "ds_read_b128 v[92:95], %[la] offset:16384\n\t" \
    "ds_read_b128 v[96:99], %[la] offset:22528\n\t" \
    "ds_read_b128 v[100:103], %[la] offset:28672\n\t" \
    "ds_read_b128 v[104:107], %[la] offset:5120\n\t" \
    "ds_read_b128 v[108:111], %[la] offset:11264\n\t" \
    "ds_read_b128 v[112:115], %[la] offset:17408\n\t" \
    "ds_read_b128 v[116:119], %[la] offset:23552\n\t" \
    "ds_read_b128 v[120:123], %[la] offset:29696\n\t" \
    "v_mov_b32 v194, %[un]\n\t" \
    "ds_write_b32 %[fd], v194\n\t" \
    "s_waitcnt lgkmcnt(10)\n\t" \
    "v_pk_add_f32 v[64:65], v[64:65], v[84:85]\n\t" \
    "v_pk_add_f32 v[66:67], v[66:67], v[86:87]\n\t" \
    "s_waitcnt lgkmcnt(9)\n\t" \
    "v_pk_add_f32 v[68:69], v[68:69], v[88:89]\n\t" \
    "v_pk_add_f32 v[70:71], v[70:71], v[90:91]\n\t" \
    "s_waitcnt lgkmcnt(8)\n\t" \
    "v_pk_add_f32 v[72:73], v[72:73], v[92:93]\n\t" \
    "v_pk_add_f32 v[74:75], v[74:75], v[94:95]\n\t" \
    "s_waitcnt lgkmcnt(7)\n\t" \
    "v_pk_add_f32 v[76:77], v[76:77], v[96:97]\n\t" \
    "v_pk_add_f32 v[78:79], v[78:79], v[98:99]\n\t" \
    "s_waitcnt lgkmcnt(6)\n\t" \
    "v_pk_add_f32 v[80:81], v[80:81], v[100:101]\n\t" \
    "v_pk_add_f32 v[82:83], v[82:83], v[102:103]\n\t" \
    "s_waitcnt lgkmcnt(5)\n\t" \
    "v_pk_add_f32 v[64:65], v[64:65], v[104:105]\n\t" \
    "v_pk_add_f32 v[66:67], v[66:67], v[106:107]\n\t" \
    "s_waitcnt lgkmcnt(4)\n\t" \
    "v_pk_add_f32 v[68:69], v[68:69], v[108:109]\n\t" \
    "v_pk_add_f32 v[70:71], v[70:71], v[110:111]\n\t" \
    "s_waitcnt lgkmcnt(3)\n\t" \
    "v_pk_add_f32 v[72:73], v[72:73], v[112:113]\n\t" \
    "v_pk_add_f32 v[74:75], v[74:75], v[114:115]\n\t" \
    "s_waitcnt lgkmcnt(2)\n\t" \
    "v_pk_add_f32 v[76:77], v[76:77], v[116:117]\n\t" \
    "v_pk_add_f32 v[78:79], v[78:79], v[118:119]\n\t" \
    "s_waitcnt lgkmcnt(1)\n\t" \
    "v_pk_add_f32 v[80:81], v[80:81], v[120:121]\n\t" \
    "v_pk_add_f32 v[82:83], v[82:83], v[122:123]\n\t" \
    "v_pk_add_f32 v[84:85], v[64:65], v[66:67]\n\t" \
    "v_pk_mul_f32 v[88:89], v[64:65], v[64:65]\n\t" \
    "v_pk_add_f32 v[86:87], v[68:69], v[70:71]\n\t" \
    "v_pk_mul_f32 v[90:91], v[66:67], v[66:67]\n\t" \
    "v_pk_fma_f32 v[88:89], v[68:69], v[68:69], v[88:89]\n\t" \
    "v_pk_fma_f32 v[90:91], v[70:71], v[70:71], v[90:91]\n\t" \
    "v_pk_add_f32 v[84:85], v[84:85], v[72:73]\n\t" \
    "v_pk_fma_f32 v[88:89], v[72:73], v[72:73], v[88:89]\n\t" \
    "v_pk_add_f32 v[86:87], v[86:87], v[74:75]\n\t" \
    "v_pk_fma_f32 v[90:91], v[74:75], v[74:75], v[90:91]\n\t" \
    "v_pk_add_f32 v[84:85], v[84:85], v[76:77]\n\t" \
    "v_pk_fma_f32 v[88:89], v[76:77], v[76:77], v[88:89]\n\t" \
    "v_pk_add_f32 v[86:87], v[86:87], v[78:79]\n\t" \
    "v_pk_fma_f32 v[90:91], v[78:79], v[78:79], v[90:91]\n\t" \
    "v_pk_add_f32 v[84:85], v[84:85], v[80:81]\n\t" \
    "v_pk_fma_f32 v[88:89], v[80:81], v[80:81], v[88:89]\n\t" \
    "v_pk_add_f32 v[86:87], v[86:87], v[82:83]\n\t" \
    "v_pk_fma_f32 v[90:91], v[82:83], v[82:83], v[90:91]\n\t" \
    "v_pk_add_f32 v[84:85], v[84:85], v[86:87]\n\t" \
    "v_pk_add_f32 v[88:89], v[88:89], v[90:91]\n\t" \
    "v_add_f32 v194, v84, v85\n\t" \
    "v_add_f32 v195, v88, v89\n\t" \
    "s_nop 1\n\t" \
    "v_permlane16_swap_b32 v194, v195\n\t" \
    "v_add_f32 v196, v194, v195\n\t" \
    "v_add_f32 v197, v194, v195\n\t" \
    "s_nop 1\n\t" \
    "v_permlane32_swap_b32 v196, v197\n\t" \
    "v_add_f32 v194, v196, v197\n\t" \
    "v_add_f32 v195, v196, v197\n\t" \
    "s_nop 1\n\t" \
    "v_permlane16_swap_b32 v194, v195\n\t" \
    "v_min_u32 v194, -2, v194\n\t" \
    "v_min_u32 v195, -2, v195\n\t" \
    "buffer_store_dwordx2 v[194:195], %[so], %[rr], %[sp] offen sc1\n\t" \
    ASM_ST3 \
    "v_cvt_pk_bf16_f32 v184, v64, v65\n\t" \
    "v_cvt_pk_bf16_f32 v185, v66, v67\n\t" \
    "v_cvt_pk_bf16_f32 v186, v68, v69\n\t" \
    "v_cvt_pk_bf16_f32 v187, v70, v71\n\t" \
    "v_cvt_pk_bf16_f32 v188, v72, v73\n\t" \
    "v_cvt_pk_bf16_f32 v189, v74, v75\n\t" \
    "v_cvt_pk_bf16_f32 v190, v76, v77\n\t" \
    "v_cvt_pk_bf16_f32 v191, v78, v79\n\t" \
    "v_cvt_pk_bf16_f32 v192, v80, v81\n\t" \
    "v_cvt_pk_bf16_f32 v193, v82, v83\n\t" \
    "v_permlane16_swap_b32 v184, v186\n\t" \
    "v_permlane16_swap_b32 v185, v187\n\t" \
    "v_permlane16_swap_b32 v188, v190\n\t" \
    "v_permlane16_swap_b32 v189, v191\n\t" \
    W4SLEEP \
    ".Lfy%=:\n\t" \
    "buffer_load_dwordx2 v[196:197], %[si], %[rr], %[sq] offen sc1\n\t" \
    "buffer_load_dwordx2 v[198:199], %[si], %[rr], %[sq] offen offset:512 sc1\n\t" \
    "s_waitcnt vmcnt(0)\n\t" \
    "v_max_u32 v200, v196, v197\n\t" \
    "v_max3_u32 v200, v198, v199, v200\n\t" \
    "v_cmp_eq_u32 vcc, -1, v200\n\t" \
    "s_cbranch_vccnz .Lfx%=\n\t" \
    ".Lfz%=:\n\t" \
    ASM_ST \
    "v_add_f32 v194, v196, v198\n\t" \
    "v_add_f32 v195, v197, v199\n\t" \
    "buffer_store_dwordx4 v[184:187], %[pa], %[rd], %[xr] offen sc1\n\t" \
    "buffer_store_dwordx4 v[188:191], %[pb], %[rd], %[xr] offen sc1\n\t" \
    "buffer_store_dwordx2 v[192:193], %[o4], %[rd], %[xr] offen sc1\n\t" \
    "s_nop 1\n\t" \
    "v_permlane16_swap_b32 v194, v195\n\t" \
    "v_add_f32 v196, v194, v195\n\t" \
    "v_add_f32 v197, v194, v195\n\t" \
    "s_nop 1\n\t" \
    "v_permlane32_swap_b32 v196, v197\n\t" \
    "v_add_f32 v194, v196, v197\n\t" \
    "v_add_f32 v195, v196, v197\n\t" \
    "s_nop 1\n\t" \
    "v_permlane16_swap_b32 v194, v195\n\t" \
    "v_mul_f32 v194, %[kd], v194\n\t" \
    "v_mul_f32 v195, %[kd], v195\n\t" \
    "v_fma_f32 v195, -v194, v194, v195\n\t" \
    "v_max_f32 v195, 0, v195\n\t" \
    "v_cvt_pk_bf16_f32 v195, v195, v195\n\t" \
    "v_lshlrev_b32 v195, 16, v195\n\t" \
    "v_add_f32 v195, %[ke], v195\n\t" \
    "v_cvt_pk_bf16_f32 v195, v195, v195\n\t" \
    "v_lshlrev_b32 v195, 16, v195\n\t" \
    "v_sqrt_f32 v195, v195\n\t" \
    "v_cvt_pk_bf16_f32 v194, v194, v194\n\t" \
    "v_lshlrev_b32 v194, 16, v194\n\t" \
    "v_cvt_pk_bf16_f32 v195, v195, v195\n\t" \
    "v_lshlrev_b32 v195, 16, v195\n\t" \
    "v_rcp_f32 v196, v195\n\t" \
    "v_rcp_f32 v197, v195\n\t" \
    "s_nop 0\n\t" \
    "v_mul_f32 v198, -v194, v196\n\t" \
    "v_mul_f32 v199, -v194, v196\n\t" \
    "v_pk_fma_f32 v[64:65], v[64:65], v[196:197], v[198:199]\n\t" \
    "v_pk_fma_f32 v[66:67], v[66:67], v[196:197], v[198:199]\n\t" \
    "v_pk_fma_f32 v[68:69], v[68:69], v[196:197], v[198:199]\n\t" \
    "v_pk_fma_f32 v[70:71], v[70:71], v[196:197], v[198:199]\n\t" \
    "v_pk_fma_f32 v[72:73], v[72:73], v[196:197], v[198:199]\n\t" \
    "v_pk_fma_f32 v[74:75], v[74:75], v[196:197], v[198:199]\n\t" \
    "v_pk_fma_f32 v[76:77], v[76:77], v[196:197], v[198:199]\n\t" \
    "v_pk_fma_f32 v[78:79], v[78:79], v[196:197], v[198:199]\n\t" \
    "v_pk_fma_f32 v[80:81], v[80:81], v[196:197], v[198:199]\n\t" \
    "v_pk_fma_f32 v[82:83], v[82:83], v[196:197], v[198:199]\n\t" \
    "v_cvt_pk_bf16_f32 v184, v64, v65\n\t" \
    "v_cvt_pk_bf16_f32 v185, v66, v67\n\t" \
    "v_cvt_pk_bf16_f32 v186, v68, v69\n\t" \
    "v_cvt_pk_bf16_f32 v187, v70, v71\n\t" \
    "v_cvt_pk_bf16_f32 v188, v72, v73\n\t" \
    "v_cvt_pk_bf16_f32 v189, v74, v75\n\t" \
    "v_cvt_pk_bf16_f32 v190, v76, v77\n\t" \
    "v_cvt_pk_bf16_f32 v191, v78, v79\n\t" \
    "v_cvt_pk_bf16_f32 v192, v80, v81\n\t" \
    "v_cvt_pk_bf16_f32 v193, v82, v83\n\t" \
    "v_permlane16_swap_b32 v184, v186\n\t" \
    "v_permlane16_swap_b32 v185, v187\n\t" \
    "v_permlane16_swap_b32 v188, v190\n\t" \
    "v_permlane16_swap_b32 v189, v191\n\t" \
    "buffer_store_dwordx4 v[184:187], %[pa], %[rd], %[xn] offen sc1\n\t" \
    "buffer_store_dwordx4 v[188:191], %[pb], %[rd], %[xn] offen sc1\n\t" \
    "buffer_store_dwordx2 v[192:193], %[o4], %[rd], %[xn] offen sc1\n\t" \
    "buffer_load_dwordx2 v[164:165], %[o0], %[rd], %[n1] offen sc1\n\t" \
    "buffer_load_dwordx2 v[166:167], %[o1], %[rd], %[n1] offen sc1\n\t" \
    "buffer_load_dwordx2 v[168:169], %[o2], %[rd], %[n1] offen sc1\n\t" \
    "buffer_load_dwordx2 v[170:171], %[o3], %[rd], %[n1] offen sc1\n\t" \
    "buffer_load_dwordx2 v[172:173], %[o4], %[rd], %[n1] offen sc1\n\t" \
    "buffer_load_dwordx2 v[174:175], %[o0], %[rd], %[na] offen sc1\n\t" \
    "buffer_load_dwordx2 v[176:177], %[o1], %[rd], %[na] offen sc1\n\t" \
    "buffer_load_dwordx2 v[178:179], %[o2], %[rd], %[na] offen sc1\n\t" \
    "buffer_load_dwordx2 v[180:181], %[o3], %[rd], %[na] offen sc1\n\t" \
    "buffer_load_dwordx2 v[182:183], %[o4], %[rd], %[na] offen sc1\n\t" \
    "v_mov_b32 v196, %[tn]\n\t" \
    "v_mov_b32 v197, %[tn]\n\t" \
    "v_mov_b32 v198, %[tn]\n\t" \
    "v_mov_b32 v199, %[tn]\n\t" \
    "s_waitcnt vmcnt(10)\n\t" \
    "buffer_store_dwordx4 v[196:199], %[fo], %[rn], %[fs] offen sc1\n\t" \
    "s_nop 1\n\t"
#define ASM_W4F_1 \
    "s_branch .Lf0%=\n\t" \
    ".Lfr%=:\n\t" \
    "s_cmp_eq_u32 %[n], 0\n\t" \
    "s_cbranch_scc1 .Lfb%=\n\t" \
    "s_sub_u32 %[n], %[n], 1\n\t" \
    "s_sleep 6\n\t" \
    "buffer_load_dwordx2 v[164:165], %[o0], %[rd], %[c1] offen sc1\n\t" \
    "buffer_load_dwordx2 v[166:167], %[o1], %[rd], %[c1] offen sc1\n\t" \
    "buffer_load_dwordx2 v[168:169], %[o2], %[rd], %[c1] offen sc1\n\t" \
    "buffer_load_dwordx2 v[170:171], %[o3], %[rd], %[c1] offen sc1\n\t" \
    "buffer_load_dwordx2 v[172:173], %[o4], %[rd], %[c1] offen sc1\n\t" \
    "buffer_load_dwordx2 v[174:175], %[o0], %[rd], %[ca] offen sc1\n\t" \
    "buffer_load_dwordx2 v[176:177], %[o1], %[rd], %[ca] offen sc1\n\t" \
    "buffer_load_dwordx2 v[178:179], %[o2], %[rd], %[ca] offen sc1\n\t" \
    "buffer_load_dwordx2 v[180:181], %[o3], %[rd], %[ca] offen sc1\n\t" \
    "buffer_load_dwordx2 v[182:183], %[o4], %[rd], %[ca] offen sc1\n\t" \
    "s_waitcnt vmcnt(0)\n\t" \
    "s_branch .Lfa%=\n\t" \
    ".Lfs0_%=:\n\t" \
    "s_cmp_eq_u32 %[n], 0\n\t" \
    "s_cbranch_scc1 .Lfu0_%=\n\t" \
    "s_sub_u32 %[n], %[n], 1\n\t" \
    "s_sleep 1\n\t" \
    "s_branch .Lft0_%=\n\t" \
    ".Lfs1_%=:\n\t" \
    "s_cmp_eq_u32 %[n], 0\n\t" \
    "s_cbranch_scc1 .Lfu1_%=\n\t" \
    "s_sub_u32 %[n], %[n], 1\n\t" \
    "s_sleep 1\n\t" \
    "s_branch .Lft1_%=\n\t" \
    ".Lf0%=:\n\t" \
    "s_waitcnt vmcnt(0)\n\t" \
    ".Lfa%=:\n\t" \
    "v_max_u32 v194, v164, v165\n\t" \
    "v_max3_u32 v194, v166, v167, v194\n\t" \
    "v_max3_u32 v194, v168, v169, v194\n\t" \
    "v_max3_u32 v194, v170, v171, v194\n\t" \
    "v_max3_u32 v194, v172, v173, v194\n\t" \
    "v_max3_u32 v194, v174, v175, v194\n\t" \
    "v_max3_u32 v194, v176, v177, v194\n\t" \
    "v_max3_u32 v194, v178, v179, v194\n\t" \
    "v_max3_u32 v194, v180, v181, v194\n\t" \
    "v_max3_u32 v194, v182, v183, v194\n\t" \
    "v_cmp_eq_u32 vcc, -1, v194\n\t" \
    "s_cbranch_vccnz .Lfr%=\n\t" \
    ".Lfb%=:\n\t" \
    "v_lshlrev_b32 v64, 16, v164\n\t" \
    "v_and_b32 v65, 0xffff0000, v164\n\t" \
    "v_lshlrev_b32 v84, 16, v174\n\t" \
    "v_and_b32 v85, 0xffff0000, v174\n\t" \
    "v_lshlrev_b32 v66, 16, v165\n\t" \
    "v_and_b32 v67, 0xffff0000, v165\n\t" \
    "v_lshlrev_b32 v86, 16, v175\n\t" \
    "v_and_b32 v87, 0xffff0000, v175\n\t" \
    "v_lshlrev_b32 v68, 16, v166\n\t" \
    "v_and_b32 v69, 0xffff0000, v166\n\t" \
    "v_lshlrev_b32 v88, 16, v176\n\t" \
    "v_and_b32 v89, 0xffff0000, v176\n\t" \
    "v_lshlrev_b32 v70, 16, v167\n\t" \
    "v_and_b32 v71, 0xffff0000, v167\n\t" \
    "v_lshlrev_b32 v90, 16, v177\n\t" \
    "v_and_b32 v91, 0xffff0000, v177\n\t" \
    "v_lshlrev_b32 v72, 16, v168\n\t" \
    "v_and_b32 v73, 0xffff0000, v168\n\t" \
    "v_lshlrev_b32 v92, 16, v178\n\t" \
    "v_and_b32 v93, 0xffff0000, v178\n\t" \
    "v_lshlrev_b32 v74, 16, v169\n\t" \
    "v_and_b32 v75, 0xffff0000, v169\n\t" \
    "v_lshlrev_b32 v94, 16, v179\n\t" \
    "v_and_b32 v95, 0xffff0000, v179\n\t" \
    "v_lshlrev_b32 v76, 16, v170\n\t" \
    "v_and_b32 v77, 0xffff0000, v170\n\t" \
    "v_lshlrev_b32 v96, 16, v180\n\t" \
    "v_and_b32 v97, 0xffff0000, v180\n\t" \
    "v_lshlrev_b32 v78, 16, v171\n\t" \
    "v_and_b32 v79, 0xffff0000, v171\n\t" \
    "v_lshlrev_b32 v98, 16, v181\n\t" \
    "v_and_b32 v99, 0xffff0000, v181\n\t" \
    "v_lshlrev_b32 v80, 16, v172\n\t" \
    "v_and_b32 v81, 0xffff0000, v172\n\t" \
    "v_lshlrev_b32 v100, 16, v182\n\t" \
    "v_and_b32 v101, 0xffff0000, v182\n\t" \
    "v_lshlrev_b32 v82, 16, v173\n\t" \
    "v_and_b32 v83, 0xffff0000, v173\n\t" \
    "v_lshlrev_b32 v102, 16, v183\n\t" \
    "v_and_b32 v103, 0xffff0000, v183\n\t" \
    "v_pk_add_f32 v[64:65], v[64:65], v[84:85]\n\t" \
    "v_pk_add_f32 v[66:67], v[66:67], v[86:87]\n\t" \
    "v_pk_add_f32 v[68:69], v[68:69], v[88:89]\n\t" \
    "v_pk_add_f32 v[70:71], v[70:71], v[90:91]\n\t" \
    "v_pk_add_f32 v[72:73], v[72:73], v[92:93]\n\t" \
    "v_pk_add_f32 v[74:75], v[74:75], v[94:95]\n\t" \
    "v_pk_add_f32 v[76:77], v[76:77], v[96:97]\n\t" \
    "v_pk_add_f32 v[78:79], v[78:79], v[98:99]\n\t" \
    "v_pk_add_f32 v[80:81], v[80:81], v[100:101]\n\t" \
    "v_pk_add_f32 v[82:83], v[82:83], v[102:103]\n\t" \
    ".Lft0_%=:\n\t" \
    "ds_read_b32 v194, %[f0]\n\t" \
    "s_waitcnt lgkmcnt(0)\n\t" \
    "v_cmp_ge_u32 vcc, %[u], v194\n\t" \
    "s_cbranch_vccnz .Lfs0_%=\n\t" \
    ".Lfu0_%=:\n\t" \
    "ds_read_b128 v[84:87], %[la] offset:0\n\t" \
    "ds_read_b128 v[88:91], %[la] offset:6144\n\t" \
    "ds_read_b128 v[92:95], %[la] offset:12288\n\t" \
    "ds_read_b128 v[96:99], %[la] offset:18432\n\t" \
    "ds_read_b128 v[100:103], %[la] offset:24576\n\t" \
    "ds_read_b128 v[104:107], %[la] offset:1024\n\t" \
    "ds_read_b128 v[108:111], %[la] offset:7168\n\t" \
    "ds_read_b128 v[112:115], %[la] offset:13312\n\t" \
    "ds_read_b128 v[116:119], %[la] offset:19456\n\t" \
    "ds_read_b128 v[120:123], %[la] offset:25600\n\t" \
    "ds_read_b128 v[124:127], %[la] offset:2048\n\t" \
    "s_waitcnt lgkmcnt(10)\n\t" \
    "v_pk_add_f32 v[64:65], v[64:65], v[84:85]\n\t" \
    "v_pk_add_f32 v[66:67], v[66:67], v[86:87]\n\t" \
    "ds_read_b128 v[128:131], %[la] offset:8192\n\t" \
    "s_waitcnt lgkmcnt(10)\n\t" \
    "v_pk_add_f32 v[68:69], v[68:69], v[88:89]\n\t" \
    "v_pk_add_f32 v[70:71], v[70:71], v[90:91]\n\t" \
    "ds_read_b128 v[132:135], %[la] offset:14336\n\t" \
    "s_waitcnt lgkmcnt(10)\n\t" \
    "v_pk_add_f32 v[72:73], v[72:73], v[92:93]\n\t" \
    "v_pk_add_f32 v[74:75], v[74:75], v[94:95]\n\t" \
    "ds_read_b128 v[136:139], %[la] offset:20480\n\t" \
    "s_waitcnt lgkmcnt(10)\n\t" \
    "v_pk_add_f32 v[76:77], v[76:77], v[96:97]\n\t" \
    "v_pk_add_f32 v[78:79], v[78:79], v[98:99]\n\t" \
    "ds_read_b128 v[140:143], %[la] offset:26624\n\t" \
    "s_waitcnt lgkmcnt(10)\n\t" \
    "v_pk_add_f32 v[80:81], v[80:81], v[100:101]\n\t" \
    "v_pk_add_f32 v[82:83], v[82:83], v[102:103]\n\t" \
    "ds_read_b128 v[144:147], %[la] offset:3072\n\t" \
    "s_waitcnt lgkmcnt(10)\n\t" \
    "v_pk_add_f32 v[64:65], v[64:65], v[104:105]\n\t" \
    "v_pk_add_f32 v[66:67], v[66:67], v[106:107]\n\t" \
    "ds_read_b128 v[148:151], %[la] offset:9216\n\t" \
    "s_waitcnt lgkmcnt(10)\n\t" \
    "v_pk_add_f32 v[68:69], v[68:69], v[108:109]\n\t" \
    "v_pk_add_f32 v[70:71], v[70:71], v[110:111]\n\t" \
    "ds_read_b128 v[152:155], %[la] offset:15360\n\t" \
    "s_waitcnt lgkmcnt(10)\n\t" \
    "v_pk_add_f32 v[72:73], v[72:73], v[112:113]\n\t" \
    "v_pk_add_f32 v[74:75], v[74:75], v[114:115]\n\t" \
    "ds_read_b128 v[156:159], %[la] offset:21504\n\t" \
    "s_waitcnt lgkmcnt(10)\n\t" \
    "v_pk_add_f32 v[76:77], v[76:77], v[116:117]\n\t" \
    "v_pk_add_f32 v[78:79], v[78:79], v[118:119]\n\t" \
    "ds_read_b128 v[160:163], %[la] offset:27648\n\t" \
    "s_waitcnt lgkmcnt(10)\n\t" \
    "v_pk_add_f32 v[80:81], v[80:81], v[120:121]\n\t" \
    "v_pk_add_f32 v[82:83], v[82:83], v[122:123]\n\t" \
    "s_waitcnt lgkmcnt(9)\n\t" \
    "v_pk_add_f32 v[64:65], v[64:65], v[124:125]\n\t" \
    "v_pk_add_f32 v[66:67], v[66:67], v[126:127]\n\t" \
    "s_waitcnt lgkmcnt(8)\n\t" \
    "v_pk_add_f32 v[68:69], v[68:69], v[128:129]\n\t" \
    "v_pk_add_f32 v[70:71], v[70:71], v[130:131]\n\t" \
    "s_waitcnt lgkmcnt(7)\n\t" \
    "v_pk_add_f32 v[72:73], v[72:73], v[132:133]\n\t" \
    "v_pk_add_f32 v[74:75], v[74:75], v[134:135]\n\t" \
    "s_waitcnt lgkmcnt(6)\n\t" \
    "v_pk_add_f32 v[76:77], v[76:77], v[136:137]\n\t" \
    "v_pk_add_f32 v[78:79], v[78:79], v[138:139]\n\t" \
    "s_waitcnt lgkmcnt(5)\n\t" \
    "v_pk_add_f32 v[80:81], v[80:81], v[140:141]\n\t" \
    "v_pk_add_f32 v[82:83], v[82:83], v[142:143]\n\t" \
    "s_waitcnt lgkmcnt(4)\n\t" \
    "v_pk_add_f32 v[64:65], v[64:65], v[144:145]\n\t" \
    "v_pk_add_f32 v[66:67], v[66:67], v[146:147]\n\t" \
    "s_waitcnt lgkmcnt(3)\n\t" \
    "v_pk_add_f32 v[68:69], v[68:69], v[148:149]\n\t" \
    "v_pk_add_f32 v[70:71], v[70:71], v[150:151]\n\t" \
    "s_waitcnt lgkmcnt(2)\n\t" \
    "v_pk_add_f32 v[72:73], v[72:73], v[152:153]\n\t" \
    "v_pk_add_f32 v[74:75], v[74:75], v[154:155]\n\t" \
    "s_waitcnt lgkmcnt(1)\n\t" \
    "v_pk_add_f32 v[76:77], v[76:77], v[156:157]\n\t" \
    "v_pk_add_f32 v[78:79], v[78:79], v[158:159]\n\t" \
    "s_waitcnt lgkmcnt(0)\n\t" \
    "v_pk_add_f32 v[80:81], v[80:81], v[160:161]\n\t" \
    "v_pk_add_f32 v[82:83], v[82:83], v[162:163]\n\t" \
    ".Lft1_%=:\n\t" \
    "ds_read_b32 v194, %[f1]\n\t" \
    "s_waitcnt lgkmcnt(0)\n\t" \
    "v_cmp_ge_u32 vcc, %[u], v194\n\t" \
    "s_cbranch_vccnz .Lfs1_%=\n\t" \
    ".Lfu1_%=:\n\t" \
    ASM_ST2 \
    "ds_read_b128 v[84:87], %[la] offset:4096\n\t" \
    "ds_read_b128 v[88:91], %[la] offset:10240\n\t" \
    "ds_read_b128 v[92:95], %[la] offset:16384\n\t" \
    "ds_read_b128 v[96:99], %[la] offset:22528\n\t" \
    "ds_read_b128 v[100:103], %[la] offset:28672\n\t" \
    "ds_read_b128 v[104:107], %[la] offset:5120\n\t" \
    "ds_read_b128 v[108:111], %[la] offset:11264\n\t" \
    "ds_read_b128 v[112:115], %[la] offset:17408\n\t" \
    "ds_read_b128 v[116:119], %[la] offset:23552\n\t" \
    "ds_read_b128 v[120:123], %[la] offset:29696\n\t" \
    "v_mov_b32 v194, %[un]\n\t" \
    "ds_write_b32 %[fd], v194\n\t" \
    "s_waitcnt lgkmcnt(10)\n\t" \
    "v_pk_add_f32 v[64:65], v[64:65], v[84:85]\n\t" \
    "v_pk_add_f32 v[66:67], v[66:67], v[86:87]\n\t" \
    "s_waitcnt lgkmcnt(9)\n\t" \
    "v_pk_add_f32 v[68:69], v[68:69], v[88:89]\n\t" \
    "v_pk_add_f32 v[70:71], v[70:71], v[90:91]\n\t" \
    "s_waitcnt lgkmcnt(8)\n\t" \
    "v_pk_add_f32 v[72:73], v[72:73], v[92:93]\n\t" \
    "v_pk_add_f32 v[74:75], v[74:75], v[94:95]\n\t" \
    "s_waitcnt lgkmcnt(7)\n\t" \
    "v_pk_add_f32 v[76:77], v[76:77], v[96:97]\n\t" \
    "v_pk_add_f32 v[78:79], v[78:79], v[98:99]\n\t" \
    "s_waitcnt lgkmcnt(6)\n\t" \
    "v_pk_add_f32 v[80:81], v[80:81], v[100:101]\n\t" \
    "v_pk_add_f32 v[82:83], v[82:83], v[102:103]\n\t" \
    "s_waitcnt lgkmcnt(5)\n\t" \
    "v_pk_add_f32 v[64:65], v[64:65], v[104:105]\n\t" \
    "v_pk_add_f32 v[66:67], v[66:67], v[106:107]\n\t" \
    "s_waitcnt lgkmcnt(4)\n\t" \
    "v_pk_add_f32 v[68:69], v[68:69], v[108:109]\n\t" \
    "v_pk_add_f32 v[70:71], v[70:71], v[110:111]\n\t" \
    "s_waitcnt lgkmcnt(3)\n\t" \
    "v_pk_add_f32 v[72:73], v[72:73], v[112:113]\n\t" \
    "v_pk_add_f32 v[74:75], v[74:75], v[114:115]\n\t" \
    "s_waitcnt lgkmcnt(2)\n\t" \
    "v_pk_add_f32 v[76:77], v[76:77], v[116:117]\n\t" \
    "v_pk_add_f32 v[78:79], v[78:79], v[118:119]\n\t" \
    "s_waitcnt lgkmcnt(1)\n\t" \
    "v_pk_add_f32 v[80:81], v[80:81], v[120:121]\n\t" \
    "v_pk_add_f32 v[82:83], v[82:83], v[122:123]\n\t" \
    "v_cvt_pk_bf16_f32 v184, v64, v65\n\t" \
    "v_cvt_pk_bf16_f32 v185, v66, v67\n\t" \
    "v_cvt_pk_bf16_f32 v186, v68, v69\n\t" \
    "v_cvt_pk_bf16_f32 v187, v70, v71\n\t" \
    "v_cvt_pk_bf16_f32 v188, v72, v73\n\t" \
    "v_cvt_pk_bf16_f32 v189, v74, v75\n\t" \
    "v_cvt_pk_bf16_f32 v190, v76, v77\n\t" \
    "v_cvt_pk_bf16_f32 v191, v78, v79\n\t" \
    "v_cvt_pk_bf16_f32 v192, v80, v81\n\t" \
    "v_cvt_pk_bf16_f32 v193, v82, v83\n\t" \
    "v_permlane16_swap_b32 v184, v186\n\t" \
    "v_permlane16_swap_b32 v185, v187\n\t" \
    "v_permlane16_swap_b32 v188, v190\n\t" \
    "v_permlane16_swap_b32 v189, v191\n\t" \
    "buffer_store_dwordx4 v[184:187], %[pa], %[ro], %[so] offen nt\n\t" \
    "buffer_store_dwordx4 v[188:191], %[pa], %[ro], %[so] offen offset:64 nt\n\t" \
    "buffer_store_dwordx2 v[192:193], %[pb], %[ro], %[so] offen offset:128 nt\n\t" \
    "buffer_load_dwordx2 v[164:165], %[o0], %[rd], %[n1] offen sc1\n\t" \
    "buffer_load_dwordx2 v[166:167], %[o1], %[rd], %[n1] offen sc1\n\t" \
    "buffer_load_dwordx2 v[168:169], %[o2], %[rd], %[n1] offen sc1\n\t" \
    "buffer_load_dwordx2 v[170:171], %[o3], %[rd], %[n1] offen sc1\n\t" \
    "buffer_load_dwordx2 v[172:173], %[o4], %[rd], %[n1] offen sc1\n\t" \
    "buffer_load_dwordx2 v[174:175], %[o0], %[rd], %[na] offen sc1\n\t" \
    "buffer_load_dwordx2 v[176:177], %[o1], %[rd], %[na] offen sc1\n\t" \
    "buffer_load_dwordx2 v[178:179], %[o2], %[rd], %[na] offen sc1\n\t" \
    "buffer_load_dwordx2 v[180:181], %[o3], %[rd], %[na] offen sc1\n\t" \
    "buffer_load_dwordx2 v[182:183], %[o4], %[rd], %[na] offen sc1\n\t"
#define ASM_WCS \
    "v_mfma_f32_16x16x32_bf16 v[196:199], v[0:3], v[216:219], 0\n\t" \
    "v_mfma_f32_16x16x32_bf16 v[200:203], v[80:83], v[216:219], 0\n\t" \
    "v_mfma_f32_16x16x32_bf16 v[196:199], v[4:7], v[216:219], v[196:199]\n\t" \
    "v_mfma_f32_16x16x32_bf16 v[200:203], v[84:87], v[216:219], v[200:203]\n\t" \
    "v_mfma_f32_16x16x32_bf16 v[196:199], v[8:11], v[216:219], v[196:199]\n\t" \
    "v_mfma_f32_16x16x32_bf16 v[200:203], v[88:91], v[216:219], v[200:203]\n\t" \
    "v_mfma_f32_16x16x32_bf16 v[196:199], v[12:15], v[216:219], v[196:199]\n\t" \
    "v_mfma_f32_16x16x32_bf16 v[200:203], v[92:95], v[216:219], v[200:203]\n\t" \
    "v_mfma_f32_16x16x32_bf16 v[196:199], v[16:19], v[216:219], v[196:199]\n\t" \
    "v_mfma_f32_16x16x32_bf16 v[200:203], v[96:99], v[216:219], v[200:203]\n\t" \
    "v_mfma_f32_16x16x32_bf16 v[196:199], v[20:23], v[216:219], v[196:199]\n\t" \
    "v_mfma_f32_16x16x32_bf16 v[200:203], v[100:103], v[216:219], v[200:203]\n\t" \
    "v_mfma_f32_16x16x32_bf16 v[196:199], v[24:27], v[216:219], v[196:199]\n\t" \
    "v_mfma_f32_16x16x32_bf16 v[200:203], v[104:107], v[216:219], v[200:203]\n\t" \
    "v_mfma_f32_16x16x32_bf16 v[196:199], v[28:31], v[216:219], v[196:199]\n\t" \
    "v_mfma_f32_16x16x32_bf16 v[200:203], v[108:111], v[216:219], v[200:203]\n\t" \
    "v_mfma_f32_16x16x32_bf16 v[196:199], v[32:35], v[216:219], v[196:199]\n\t" \
    "v_mfma_f32_16x16x32_bf16 v[200:203], v[112:115], v[216:219], v[200:203]\n\t" \
    "v_mfma_f32_16x16x32_bf16 v[196:199], v[36:39], v[216:219], v[196:199]\n\t" \
    "v_mfma_f32_16x16x32_bf16 v[200:203], v[116:119], v[216:219], v[200:203]\n\t" \
    "v_mfma_f32_16x16x32_bf16 v[196:199], v[40:43], v[216:219], v[196:199]\n\t" \
    "v_mfma_f32_16x16x32_bf16 v[200:203], v[120:123], v[216:219], v[200:203]\n\t" \
    "v_mfma_f32_16x16x32_bf16 v[196:199], v[44:47], v[216:219], v[196:199]\n\t" \
    "v_mfma_f32_16x16x32_bf16 v[200:203], v[124:127], v[216:219], v[200:203]\n\t" \
    "v_mfma_f32_16x16x32_bf16 v[196:199], v[48:51], v[216:219], v[196:199]\n\t" \
    "v_mfma_f32_16x16x32_bf16 v[200:203], v[128:131], v[216:219], v[200:203]\n\t" \
    "v_mfma_f32_16x16x32_bf16 v[196:199], v[52:55], v[216:219], v[196:199]\n\t" \
    "v_mfma_f32_16x16x32_bf16 v[200:203], v[132:135], v[216:219], v[200:203]\n\t" \
    "v_mfma_f32_16x16x32_bf16 v[196:199], v[56:59], v[216:219], v[196:199]\n\t" \
    "v_mfma_f32_16x16x32_bf16 v[200:203], v[136:139], v[216:219], v[200:203]\n\t" \
    "v_mfma_f32_16x16x32_bf16 v[196:199], v[60:63], v[216:219], v[196:199]\n\t" \
    "v_mfma_f32_16x16x32_bf16 v[200:203], v[140:143], v[216:219], v[200:203]\n\t" \
    "v_mfma_f32_16x16x32_bf16 v[196:199], v[64:67], v[216:219], v[196:199]\n\t" \
    "v_mfma_f32_16x16x32_bf16 v[200:203], v[144:147], v[216:219], v[200:203]\n\t" \
    "v_mfma_f32_16x16x32_bf16 v[196:199], v[68:71], v[216:219], v[196:199]\n\t" \
    "v_mfma_f32_16x16x32_bf16 v[200:203], v[148:151], v[216:219], v[200:203]\n\t" \
    "v_mfma_f32_16x16x32_bf16 v[196:199], v[72:75], v[216:219], v[196:199]\n\t" \
    "v_mfma_f32_16x16x32_bf16 v[200:203], v[152:155], v[216:219], v[200:203]\n\t" \
    "v_mfma_f32_16x16x32_bf16 v[196:199], v[76:79], v[216:219], v[196:199]\n\t" \
    "v_mfma_f32_16x16x32_bf16 v[200:203], v[156:159], v[216:219], v[200:203]\n\t" \
    "s_nop 7\n\t"
#define ASM_WT1_LO \
    "s_branch .Lt0%=\n\t" \
    ".Ltr%=:\n\t" \
    "s_cmp_gt_i32 %[n], 0\n\t" \
    "s_cbranch_scc0 .Ltg%=\n\t" \
    ".Lfw%=:\n\t" \
    "s_mov_b32 %[a], 0\n\t" \
    "v_max_u32 v252, v232, v233\n\t" \
    "v_max3_u32 v252, v234, v235, v252\n\t" \
    "v_cmp_eq_u32 vcc, -1, v252\n\t" \
    "s_cbranch_vccz .Lgw0_%=\n\t" \
    "buffer_load_dwordx4 v[232:235], %[l16], %[rs], %[i0] offen offset:0 sc1\n\t" \
    "s_mov_b32 %[a], 1\n\t" \
    ".Lgw0_%=:\n\t" \
    "v_max_u32 v252, v236, v237\n\t" \
    "v_max3_u32 v252, v238, v239, v252\n\t" \
    "v_cmp_eq_u32 vcc, -1, v252\n\t" \
    "s_cbranch_vccz .Lgw1_%=\n\t" \
    "buffer_load_dwordx4 v[236:239], %[l16], %[rs], %[i0] offen offset:1024 sc1\n\t" \
    "s_mov_b32 %[a], 1\n\t" \
    ".Lgw1_%=:\n\t" \
    "v_max_u32 v252, v240, v241\n\t" \
    "v_max3_u32 v252, v242, v243, v252\n\t" \
    "v_cmp_eq_u32 vcc, -1, v252\n\t" \
    "s_cbranch_vccz .Lgw2_%=\n\t" \
    "buffer_load_dwordx4 v[240:243], %[l16], %[rs], %[i0] offen offset:2048 sc1\n\t" \
    "s_mov_b32 %[a], 1\n\t" \
    ".Lgw2_%=:\n\t" \
    "v_max_u32 v252, v244, v245\n\t" \
    "v_max3_u32 v252, v246, v247, v252\n\t" \
    "v_cmp_eq_u32 vcc, -1, v252\n\t" \
    "s_cbranch_vccz .Lgw3_%=\n\t" \
    "buffer_load_dwordx4 v[244:247], %[l16], %[rs], %[i0] offen offset:3072 sc1\n\t" \
    "s_mov_b32 %[a], 1\n\t" \
    ".Lgw3_%=:\n\t" \
    "v_max_u32 v252, v248, v249\n\t" \
    "v_max3_u32 v252, v250, v251, v252\n\t" \
    "v_cmp_eq_u32 vcc, -1, v252\n\t" \
    "s_cbranch_vccz .Lgw4_%=\n\t" \
    "buffer_load_dwordx4 v[248:251], %[l16], %[rs], %[i1] offen offset:0 sc1\n\t" \
    "s_mov_b32 %[a], 1\n\t" \
    ".Lgw4_%=:\n\t" \
    "s_cmp_eq_u32 %[a], 0\n\t" \
    "s_cbranch_scc1 .Lhw%=\n\t" \
    "s_waitcnt vmcnt(20)\n\t" \
    ".Liw%=:\n\t" \
    "v_max_u32 v252, v232, v233\n\t" \
    "v_max3_u32 v252, v234, v235, v252\n\t" \
    "v_max3_u32 v252, v236, v237, v252\n\t" \
    "v_max3_u32 v252, v238, v239, v252\n\t" \
    "v_max3_u32 v252, v240, v241, v252\n\t" \
    "v_max3_u32 v252, v242, v243, v252\n\t" \
    "v_max3_u32 v252, v244, v245, v252\n\t" \
    "v_max3_u32 v252, v246, v247, v252\n\t" \
    "v_max3_u32 v252, v248, v249, v252\n\t" \
    "v_max3_u32 v252, v250, v251, v252\n\t" \
    "v_cmp_eq_u32 vcc, -1, v252\n\t" \
    "s_cbranch_vccz .Lhw%=\n\t" \
    "s_sleep 1\n\t" \
    "v_max_u32 v252, v232, v233\n\t" \
    "v_max3_u32 v252, v234, v235, v252\n\t" \
    "v_max3_u32 v252, v236, v237, v252\n\t" \
    "v_max3_u32 v252, v238, v239, v252\n\t" \
    "v_max3_u32 v252, v240, v241, v252\n\t" \
    "v_max3_u32 v252, v242, v243, v252\n\t" \
    "v_max3_u32 v252, v244, v245, v252\n\t" \
    "v_max3_u32 v252, v246, v247, v252\n\t" \
    "v_max3_u32 v252, v248, v249, v252\n\t" \
    "v_max3_u32 v252, v250, v251, v252\n\t" \
    "v_cmp_eq_u32 vcc, -1, v252\n\t" \
    "s_cbranch_vccz .Lhw%=\n\t" \
    "s_sleep 1\n\t" \
    "v_max_u32 v252, v232, v233\n\t" \
    "v_max3_u32 v252, v234, v235, v252\n\t" \
    "v_max3_u32 v252, v236, v237, v252\n\t" \
    "v_max3_u32 v252, v238, v239, v252\n\t" \
    "v_max3_u32 v252, v240, v241, v252\n\t" \
    "v_max3_u32 v252, v242, v243, v252\n\t" \
    "v_max3_u32 v252, v244, v245, v252\n\t" \
    "v_max3_u32 v252, v246, v247, v252\n\t" \
    "v_max3_u32 v252, v248, v249, v252\n\t" \
    "v_max3_u32 v252, v250, v251, v252\n\t" \
    "v_cmp_eq_u32 vcc, -1, v252\n\t" \
    "s_cbranch_vccz .Lhw%=\n\t" \
    "s_sleep 1\n\t" \
    "v_max_u32 v252, v232, v233\n\t" \
    "v_max3_u32 v252, v234, v235, v252\n\t" \
    "v_max3_u32 v252, v236, v237, v252\n\t" \
    "v_max3_u32 v252, v238, v239, v252\n\t" \
    "v_max3_u32 v252, v240, v241, v252\n\t" \
    "v_max3_u32 v252, v242, v243, v252\n\t" \
    "v_max3_u32 v252, v244, v245, v252\n\t" \
    "v_max3_u32 v252, v246, v247, v252\n\t" \
    "v_max3_u32 v252, v248, v249, v252\n\t" \
    "v_max3_u32 v252, v250, v251, v252\n\t" \
    "v_cmp_eq_u32 vcc, -1, v252\n\t" \
    "s_cbranch_vccz .Lhw%=\n\t" \
    "s_sleep 1\n\t" \
    "v_max_u32 v252, v232, v233\n\t" \
    "v_max3_u32 v252, v234, v235, v252\n\t" \
    "v_max3_u32 v252, v236, v237, v252\n\t" \
    "v_max3_u32 v252, v238, v239, v252\n\t" \
    "v_max3_u32 v252, v240, v241, v252\n\t" \
    "v_max3_u32 v252, v242, v243, v252\n\t" \
    "v_max3_u32 v252, v244, v245, v252\n\t" \
    "v_max3_u32 v252, v246, v247, v252\n\t" \
    "v_max3_u32 v252, v248, v249, v252\n\t" \
    "v_max3_u32 v252, v250, v251, v252\n\t" \
    "v_cmp_eq_u32 vcc, -1, v252\n\t" \
    "s_cbranch_vccz .Lhw%=\n\t" \
    "s_sleep 1\n\t" \
    "v_max_u32 v252, v232, v233\n\t" \
    "v_max3_u32 v252, v234, v235, v252\n\t" \
    "v_max3_u32 v252, v236, v237, v252\n\t" \
    "v_max3_u32 v252, v238, v239, v252\n\t" \
    "v_max3_u32 v252, v240, v241, v252\n\t" \
    "v_max3_u32 v252, v242, v243, v252\n\t" \
    "v_max3_u32 v252, v244, v245, v252\n\t" \
    "v_max3_u32 v252, v246, v247, v252\n\t" \
    "v_max3_u32 v252, v248, v249, v252\n\t" \
    "v_max3_u32 v252, v250, v251, v252\n\t" \
    "v_cmp_eq_u32 vcc, -1, v252\n\t" \
    "s_cbranch_vccz .Lhw%=\n\t" \
    "s_sleep 1\n\t" \
    "s_sub_u32 %[n], %[n], 1\n\t" \
    "s_cmp_gt_i32 %[n], 0\n\t" \
    "s_cbranch_scc1 .Lfw%=\n\t" \
    ".Lhw%=:\n\t" \
    "s_branch .Lta%=\n\t" \
    ".Lt0%=:\n\t" \
    "s_waitcnt vmcnt(1)\n\t" \
    ".Lta%=:\n\t" \
    "ds_write_b128 %[lw], v[232:235] offset:0\n\t" \
    "v_max_u32 v252, v232, v233\n\t" \
    "v_max3_u32 v252, v234, v235, v252\n\t" \
    "v_mfma_f32_16x16x32_bf16 v[188:191], v[232:235], v[232:235], 0\n\t" \
    "v_mfma_f32_16x16x32_bf16 v[192:195], v[216:219], v[232:235], 0\n\t" \
    "ds_write_b128 %[lw], v[236:239] offset:1024\n\t" \
    "v_max3_u32 v252, v236, v237, v252\n\t" \
    "v_max3_u32 v252, v238, v239, v252\n\t" \
    "v_mfma_f32_16x16x32_bf16 v[188:191], v[236:239], v[236:239], v[188:191]\n\t" \
    "v_mfma_f32_16x16x32_bf16 v[192:195], v[216:219], v[236:239], v[192:195]\n\t" \
    "ds_write_b128 %[lw], v[240:243] offset:2048\n\t" \
    "v_max3_u32 v252, v240, v241, v252\n\t" \
    "v_max3_u32 v252, v242, v243, v252\n\t" \
    "v_mfma_f32_16x16x32_bf16 v[188:191], v[240:243], v[240:243], v[188:191]\n\t" \
    "v_mfma_f32_16x16x32_bf16 v[192:195], v[216:219], v[240:243], v[192:195]\n\t" \
    "ds_write_b128 %[lw], v[244:247] offset:3072\n\t" \
    "v_max3_u32 v252, v244, v245, v252\n\t" \
    "v_max3_u32 v252, v246, v247, v252\n\t" \
    "v_mfma_f32_16x16x32_bf16 v[188:191], v[244:247], v[244:247], v[188:191]\n\t" \
    "v_mfma_f32_16x16x32_bf16 v[192:195], v[216:219], v[244:247], v[192:195]\n\t" \
    "ds_write_b128 %[lw], v[248:251] offset:4096\n\t" \
    "v_max3_u32 v252, v248, v249, v252\n\t" \
    "v_max3_u32 v252, v250, v251, v252\n\t" \
    "v_mfma_f32_16x16x32_bf16 v[188:191], v[248:251], v[248:251], v[188:191]\n\t" \
    "v_mfma_f32_16x16x32_bf16 v[192:195], v[216:219], v[248:251], v[192:195]\n\t" \
    "v_cmp_eq_u32 vcc, -1, v252\n\t" \
    "s_cbranch_vccnz .Ltr%=\n\t" \
    ".Ltg%=:\n\t" \
    ASM_ST2 \
    "s_nop 5\n\t" \
    "ds_write_b128 %[lw], v[188:191] offset:5120\n\t" \
    "ds_write_b128 %[lw], v[192:195] offset:6144\n\t" \
    "s_waitcnt lgkmcnt(0)\n\t" \
    "s_setprio 2\n\t" \
    "s_barrier\n\t" ASM_ST \
    "ds_read_b128 v[160:163], %[la] offset:0\n\t" \
    "ds_read_b128 v[164:167], %[la] offset:1024\n\t" \
    "ds_read_b128 v[168:171], %[la] offset:2048\n\t" \
    "ds_read2st64_b32 v[204:205], %[sr] offset0:20 offset1:24\n\t" \
    "ds_read_b128 v[172:175], %[la] offset:3072\n\t" \
    "ds_read_b128 v[176:179], %[la] offset:8192\n\t" \
    "ds_read_b128 v[180:183], %[la] offset:9216\n\t" \
    "s_waitcnt lgkmcnt(6)\n\t" \
    "v_mfma_f32_16x16x32_bf16 v[188:191], v[0:3], v[160:163], 0\n\t" \
    "ds_read_b128 v[184:187], %[la] offset:10240\n\t" \
    "v_mfma_f32_16x16x32_bf16 v[192:195], v[80:83], v[160:163], 0\n\t" \
    "s_waitcnt lgkmcnt(6)\n\t" \
    "v_mfma_f32_16x16x32_bf16 v[188:191], v[4:7], v[164:167], v[188:191]\n\t" \
    "ds_read_b128 v[160:163], %[la] offset:11264\n\t" \
    "v_mfma_f32_16x16x32_bf16 v[192:195], v[84:87], v[164:167], v[192:195]\n\t" \
    "s_waitcnt lgkmcnt(6)\n\t" \
    "v_mfma_f32_16x16x32_bf16 v[188:191], v[8:11], v[168:171], v[188:191]\n\t" \
    "ds_read_b128 v[164:167], %[la] offset:16384\n\t" \
    "s_waitcnt lgkmcnt(6)\n\t" \
    "v_mfma_f32_16x16x32_bf16 v[192:195], v[88:91], v[168:171], v[192:195]\n\t" \
    "v_permlane16_swap_b32 v204, v205\n\t" \
    "s_waitcnt lgkmcnt(5)\n\t" \
    "v_mfma_f32_16x16x32_bf16 v[188:191], v[12:15], v[172:175], v[188:191]\n\t" \
    "ds_read_b128 v[168:171], %[la] offset:17408\n\t" \
    "v_mfma_f32_16x16x32_bf16 v[192:195], v[92:95], v[172:175], v[192:195]\n\t" \
    "v_add_f32 v206, v204, v205\n\t" \
    "s_waitcnt lgkmcnt(5)\n\t" \
    "v_mfma_f32_16x16x32_bf16 v[188:191], v[16:19], v[176:179], v[188:191]\n\t" \
    "ds_read_b128 v[172:175], %[la] offset:18432\n\t" \
    "v_add_f32 v207, v204, v205\n\t" \
    "v_mfma_f32_16x16x32_bf16 v[192:195], v[96:99], v[176:179], v[192:195]\n\t" \
    "s_waitcnt lgkmcnt(5)\n\t" \
    "v_mfma_f32_16x16x32_bf16 v[188:191], v[20:23], v[180:183], v[188:191]\n\t" \
    "ds_read_b128 v[176:179], %[la] offset:19456\n\t" \
    "v_permlane32_swap_b32 v206, v207\n\t" \
    "v_mfma_f32_16x16x32_bf16 v[192:195], v[100:103], v[180:183], v[192:195]\n\t" \
    "s_waitcnt lgkmcnt(5)\n\t" \
    "v_mfma_f32_16x16x32_bf16 v[188:191], v[24:27], v[184:187], v[188:191]\n\t" \
    "ds_read_b128 v[180:183], %[la] offset:24576\n\t" \
    "v_add_f32 v204, v206, v207\n\t" \
    "v_mfma_f32_16x16x32_bf16 v[192:195], v[104:107], v[184:187], v[192:195]\n\t" \
    "v_add_f32 v205, v206, v207\n\t" \
    "s_waitcnt lgkmcnt(5)\n\t" \
    "v_mfma_f32_16x16x32_bf16 v[188:191], v[28:31], v[160:163], v[188:191]\n\t" \
    "ds_read_b128 v[184:187], %[la] offset:25600\n\t" \
    "v_mfma_f32_16x16x32_bf16 v[192:195], v[108:111], v[160:163], v[192:195]\n\t" \
    "v_permlane16_swap_b32 v204, v205\n\t" \
    "s_waitcnt lgkmcnt(5)\n\t" \
    "v_mfma_f32_16x16x32_bf16 v[188:191], v[32:35], v[164:167], v[188:191]\n\t" \
    "ds_read_b128 v[160:163], %[la] offset:26624\n\t" \
    "v_mfma_f32_16x16x32_bf16 v[192:195], v[112:115], v[164:167], v[192:195]\n\t" \
    "v_mul_f32 v205, %[kd], v205\n\t" \
    "s_waitcnt lgkmcnt(5)\n\t" \
    "v_mfma_f32_16x16x32_bf16 v[188:191], v[36:39], v[168:171], v[188:191]\n\t" \
    "ds_read_b128 v[164:167], %[la] offset:27648\n\t" \
    "v_mul_f32 v204, %[kd], v204\n\t" \
    "v_mfma_f32_16x16x32_bf16 v[192:195], v[116:119], v[168:171], v[192:195]\n\t" \
    "v_fma_f32 v204, -v205, v205, v204\n\t" \
    "s_waitcnt lgkmcnt(5)\n\t" \
    "v_mfma_f32_16x16x32_bf16 v[188:191], v[40:43], v[172:175], v[188:191]\n\t" \
    "ds_read_b128 v[168:171], %[la] offset:4096\n\t" \
    "v_max_f32 v204, 0, v204\n\t" \
    "v_mfma_f32_16x16x32_bf16 v[192:195], v[120:123], v[172:175], v[192:195]\n\t" \
    "v_add_f32 v206, %[ke], v204\n\t" \
    "s_waitcnt lgkmcnt(5)\n\t" \
    "v_mfma_f32_16x16x32_bf16 v[188:191], v[44:47], v[176:179], v[188:191]\n\t" \
    "ds_read_b128 v[172:175], %[la] offset:12288\n\t" \
    "v_rsq_f32 v206, v206\n\t" \
    "v_mfma_f32_16x16x32_bf16 v[192:195], v[124:127], v[176:179], v[192:195]\n\t" \
    "s_waitcnt lgkmcnt(5)\n\t" \
    "v_mfma_f32_16x16x32_bf16 v[188:191], v[48:51], v[180:183], v[188:191]\n\t" \
    "ds_read_b128 v[176:179], %[la] offset:20480\n\t" \
    "v_mul_f32 v207, v204, v206\n\t" \
    "v_mfma_f32_16x16x32_bf16 v[192:195], v[128:131], v[180:183], v[192:195]\n\t" \
    "v_fma_f32 v207, v207, v206, %[ke]\n\t" \
    "s_waitcnt lgkmcnt(5)\n\t" \
    "v_mfma_f32_16x16x32_bf16 v[188:191], v[52:55], v[184:187], v[188:191]\n\t" \
    "ds_read_b128 v[180:183], %[la] offset:28672\n\t" \
    "v_rsq_f32 v207, v207\n\t" \
    "v_mfma_f32_16x16x32_bf16 v[192:195], v[132:135], v[184:187], v[192:195]\n\t" \
    "v_mul_f32 v206, v206, v207\n\t" \
    "v_mul_f32 v207, -v205, v206\n\t" \
    "s_waitcnt lgkmcnt(5)\n\t" \
    "v_mfma_f32_16x16x32_bf16 v[188:191], v[56:59], v[160:163], v[188:191]\n\t" \
    "v_mul_f32 v208, v196, v207\n\t" \
    "v_mul_f32 v209, v197, v207\n\t" \
    "v_mfma_f32_16x16x32_bf16 v[192:195], v[136:139], v[160:163], v[192:195]\n\t" \
    "v_mul_f32 v210, v198, v207\n\t" \
    "v_mul_f32 v211, v199, v207\n\t" \
    "s_waitcnt lgkmcnt(4)\n\t" \
    "v_mfma_f32_16x16x32_bf16 v[188:191], v[60:63], v[164:167], v[188:191]\n\t" \
    "v_mul_f32 v212, v200, v207\n\t" \
    "v_mul_f32 v213, v201, v207\n\t" \
    "v_mfma_f32_16x16x32_bf16 v[192:195], v[140:143], v[164:167], v[192:195]\n\t" \
    "v_mul_f32 v214, v202, v207\n\t" \
    "v_mul_f32 v215, v203, v207\n\t" \
    "s_waitcnt lgkmcnt(3)\n\t" \
    "v_mfma_f32_16x16x32_bf16 v[188:191], v[64:67], v[168:171], v[188:191]\n\t" \
    "s_cmp_lg_u32 %[sw], 0\n\t" \
    "s_cbranch_scc1 .Lts%=\n\t" \
    "v_cvt_pk_bf16_f32 v217, v204, v204\n\t" \
    "v_lshlrev_b32 v217, 16, v217\n\t" \
    "v_add_f32 v217, %[ke], v217\n\t" \
    "v_cvt_pk_bf16_f32 v217, v217, v217\n\t" \
    "v_lshlrev_b32 v217, 16, v217\n\t" \
    "v_sqrt_f32 v217, v217\n\t" \
    "v_cvt_pk_bf16_f32 v216, v205, v205\n\t" \
    "v_lshlrev_b32 v216, 16, v216\n\t" \
    "v_cvt_pk_bf16_f32 v217, v217, v217\n\t" \
    "v_lshlrev_b32 v217, 16, v217\n\t" \
    "v_rcp_f32 v217, v217\n\t" \
    "v_lshrrev_b32 v218, 1, %[l16]\n\t" \
    "v_and_b32 v218, 0x78, v218\n\t" \
    "buffer_store_dwordx2 v[216:217], v218, %[rs], %[ss] offen sc1\n\t" \
    ".Lts%=:\n\t" \
    "v_mov_b64 v[216:217], -1\n\t" \
    "v_mfma_f32_16x16x32_bf16 v[192:195], v[144:147], v[168:171], v[192:195]\n\t" \
    "v_mov_b64 v[218:219], -1\n\t" \
    "buffer_store_dwordx4 v[216:219], %[ob], %[rs], %[sa] offen sc1\n\t" \
    "s_waitcnt lgkmcnt(2)\n\t" \
    "v_mfma_f32_16x16x32_bf16 v[188:191], v[68:71], v[172:175], v[188:191]\n\t" \
    "v_mfma_f32_16x16x32_bf16 v[192:195], v[148:151], v[172:175], v[192:195]\n\t" \
    "s_waitcnt lgkmcnt(1)\n\t" \
    "v_mfma_f32_16x16x32_bf16 v[188:191], v[72:75], v[176:179], v[188:191]\n\t" \
    "v_mfma_f32_16x16x32_bf16 v[192:195], v[152:155], v[176:179], v[192:195]\n\t" \
    "s_waitcnt lgkmcnt(0)\n\t" \
    "v_mfma_f32_16x16x32_bf16 v[188:191], v[76:79], v[180:183], v[188:191]\n\t" \
    "v_mfma_f32_16x16x32_bf16 v[192:195], v[156:159], v[180:183], v[192:195]\n\t" \
    "s_setprio 2\n\t" \
    "buffer_load_dwordx4 v[232:235], %[l16], %[rs], %[n0] offen offset:0 sc1\n\t" \
    "buffer_load_dwordx4 v[236:239], %[l16], %[rs], %[n0] offen offset:1024 sc1\n\t" \
    "buffer_load_dwordx4 v[240:243], %[l16], %[rs], %[n0] offen offset:2048 sc1\n\t" \
    "buffer_load_dwordx4 v[244:247], %[l16], %[rs], %[n0] offen offset:3072 sc1\n\t" \
    "buffer_load_dwordx4 v[248:251], %[l16], %[rs], %[n1] offen offset:0 sc1\n\t" \
    ASM_ST3 \
    "v_fma_f32 v188, v188, v206, v208\n\t" \
    "v_fma_f32 v189, v189, v206, v209\n\t" \
    "v_fma_f32 v190, v190, v206, v210\n\t" \
    "v_fma_f32 v191, v191, v206, v211\n\t" \
    "v_mul_f32 v160, v188, v188\n\t" \
    "v_mul_f32 v161, v189, v189\n\t" \
    "v_mul_f32 v162, v190, v190\n\t" \
    "v_mul_f32 v163, v191, v191\n\t" \
    "v_fma_f32 v160, v160, %[k1], v220\n\t" \
    "v_fma_f32 v161, v161, %[k1], v220\n\t" \
    "v_fma_f32 v162, v162, %[k1], v220\n\t" \
    "v_fma_f32 v163, v163, %[k1], v220\n\t" \
    "v_mul_f32 v160, v188, v160\n\t" \
    "v_mul_f32 v161, v189, v161\n\t" \
    "v_mul_f32 v162, v190, v162\n\t" \
    "v_mul_f32 v163, v191, v163\n\t" \
    "v_fma_f32 v192, v192, v206, v212\n\t" \
    "v_fma_f32 v193, v193, v206, v213\n\t" \
    "v_fma_f32 v194, v194, v206, v214\n\t" \
    "v_fma_f32 v195, v195, v206, v215\n\t" \
    "v_mul_f32 v164, v192, v192\n\t" \
    "v_mul_f32 v165, v193, v193\n\t" \
    "v_mul_f32 v166, v194, v194\n\t" \
    "v_mul_f32 v167, v195, v195\n\t" \
    "v_fma_f32 v164, v164, %[k1], v220\n\t" \
    "v_fma_f32 v165, v165, %[k1], v220\n\t" \
    "v_fma_f32 v166, v166, %[k1], v220\n\t" \
    "v_fma_f32 v167, v167, %[k1], v220\n\t" \
    "v_mul_f32 v164, v192, v164\n\t" \
    "v_mul_f32 v165, v193, v165\n\t" \
    "v_mul_f32 v166, v194, v166\n\t" \
    "v_mul_f32 v167, v195, v167\n\t" \
    "v_exp_f32 v160, v160\n\t" \
    "v_exp_f32 v161, v161\n\t" \
    "v_exp_f32 v162, v162\n\t" \
    "v_exp_f32 v163, v163\n\t" \
    "v_exp_f32 v164, v164\n\t" \
    "v_exp_f32 v165, v165\n\t" \
    "v_exp_f32 v166, v166\n\t" \
    "v_exp_f32 v167, v167\n\t" \
    "v_add_f32 v160, 1.0, v160\n\t" \
    "v_add_f32 v161, 1.0, v161\n\t" \
    "v_add_f32 v162, 1.0, v162\n\t" \
    "v_add_f32 v163, 1.0, v163\n\t" \
    "v_add_f32 v164, 1.0, v164\n\t" \
    "v_add_f32 v165, 1.0, v165\n\t" \
    "v_add_f32 v166, 1.0, v166\n\t" \
    "v_add_f32 v167, 1.0, v167\n\t" \
    "v_rcp_f32 v160, v160\n\t" \
    "v_rcp_f32 v161, v161\n\t" \
    "v_rcp_f32 v162, v162\n\t" \
    "v_rcp_f32 v163, v163\n\t" \
    "v_rcp_f32 v164, v164\n\t" \
    "v_rcp_f32 v165, v165\n\t" \
    "v_rcp_f32 v166, v166\n\t" \
    "v_rcp_f32 v167, v167\n\t" \
    "v_mul_f32 v188, v188, v160\n\t" \
    "v_mul_f32 v189, v189, v161\n\t" \
    "v_mul_f32 v190, v190, v162\n\t" \
    "v_mul_f32 v191, v191, v163\n\t" \
    "v_mul_f32 v192, v192, v164\n\t" \
    "v_mul_f32 v193, v193, v165\n\t" \
    "v_mul_f32 v194, v194, v166\n\t" \
    "v_mul_f32 v195, v195, v167\n\t" \
    "v_cvt_pk_bf16_f32 v188, v188, v189\n\t" \
    "v_cvt_pk_bf16_f32 v189, v190, v191\n\t" \
    "v_cvt_pk_bf16_f32 v190, v192, v193\n\t" \
    "v_cvt_pk_bf16_f32 v191, v194, v195\n\t" \
    "v_min_u32 v188, -2, v188\n\t" \
    "v_min_u32 v189, -2, v189\n\t" \
    "v_min_u32 v190, -2, v190\n\t" \
    "v_min_u32 v191, -2, v191\n\t" \
    "v_mov_b32 v216, 0x3f803f80\n\t" \
    "v_mov_b32 v217, 0x3f803f80\n\t" \
    "v_mov_b32 v218, 0x3f803f80\n\t" \
    "v_mov_b32 v219, 0x3f803f80\n\t" \
    "v_permlane16_swap_b32 v188, v190\n\t" \
    "v_permlane16_swap_b32 v189, v191\n\t" \
    "s_waitcnt vmcnt(6)\n\t" \
    "buffer_store_dwordx4 v[188:191], %[oa], %[rs], %[so] offen sc1\n\t" \
    "s_nop 1\n\t" \
    "s_setprio 0\n\t"
#define ASM_WT1_HI \
    "s_setprio 1\n\t" \
    "s_barrier\n\t" ASM_ST \
    "ds_read_b128 v[160:163], %[la] offset:0\n\t" \
    "ds_read_b128 v[164:167], %[la] offset:1024\n\t" \
    "ds_read_b128 v[168:171], %[la] offset:2048\n\t" \
    "ds_read2st64_b32 v[204:205], %[sr] offset0:20 offset1:24\n\t" \
    "ds_read_b128 v[172:175], %[la] offset:3072\n\t" \
    "ds_read_b128 v[176:179], %[la] offset:8192\n\t" \
    "ds_read_b128 v[180:183], %[la] offset:9216\n\t" \
    "s_waitcnt lgkmcnt(6)\n\t" \
    "v_mfma_f32_16x16x32_bf16 v[188:191], v[0:3], v[160:163], 0\n\t" \
    "ds_read_b128 v[184:187], %[la] offset:10240\n\t" \
    "v_mfma_f32_16x16x32_bf16 v[192:195], v[80:83], v[160:163], 0\n\t" \
    "s_waitcnt lgkmcnt(6)\n\t" \
    "v_mfma_f32_16x16x32_bf16 v[188:191], v[4:7], v[164:167], v[188:191]\n\t" \
    "ds_read_b128 v[160:163], %[la] offset:11264\n\t" \
    "v_mfma_f32_16x16x32_bf16 v[192:195], v[84:87], v[164:167], v[192:195]\n\t" \
    "s_waitcnt lgkmcnt(6)\n\t" \
    "v_mfma_f32_16x16x32_bf16 v[188:191], v[8:11], v[168:171], v[188:191]\n\t" \
    "ds_read_b128 v[164:167], %[la] offset:16384\n\t" \
    "s_waitcnt lgkmcnt(6)\n\t" \
    "v_mfma_f32_16x16x32_bf16 v[192:195], v[88:91], v[168:171], v[192:195]\n\t" \
    "v_permlane16_swap_b32 v204, v205\n\t" \
    "s_waitcnt lgkmcnt(5)\n\t" \
    "v_mfma_f32_16x16x32_bf16 v[188:191], v[12:15], v[172:175], v[188:191]\n\t" \
    "ds_read_b128 v[168:171], %[la] offset:17408\n\t" \
    "v_mfma_f32_16x16x32_bf16 v[192:195], v[92:95], v[172:175], v[192:195]\n\t" \
    "v_add_f32 v206, v204, v205\n\t" \
    "s_waitcnt lgkmcnt(5)\n\t" \
    "v_mfma_f32_16x16x32_bf16 v[188:191], v[16:19], v[176:179], v[188:191]\n\t" \
    "ds_read_b128 v[172:175], %[la] offset:18432\n\t" \
    "v_add_f32 v207, v204, v205\n\t" \
    "v_mfma_f32_16x16x32_bf16 v[192:195], v[96:99], v[176:179], v[192:195]\n\t" \
    "s_waitcnt lgkmcnt(5)\n\t" \
    "v_mfma_f32_16x16x32_bf16 v[188:191], v[20:23], v[180:183], v[188:191]\n\t" \
    "ds_read_b128 v[176:179], %[la] offset:19456\n\t" \
    "v_permlane32_swap_b32 v206, v207\n\t" \
    "v_mfma_f32_16x16x32_bf16 v[192:195], v[100:103], v[180:183], v[192:195]\n\t" \
    "s_waitcnt lgkmcnt(5)\n\t" \
    "v_mfma_f32_16x16x32_bf16 v[188:191], v[24:27], v[184:187], v[188:191]\n\t" \
    "ds_read_b128 v[180:183], %[la] offset:24576\n\t" \
    "v_add_f32 v204, v206, v207\n\t" \
    "v_mfma_f32_16x16x32_bf16 v[192:195], v[104:107], v[184:187], v[192:195]\n\t" \
    "v_add_f32 v205, v206, v207\n\t" \
    "s_waitcnt lgkmcnt(5)\n\t" \
    "v_mfma_f32_16x16x32_bf16 v[188:191], v[28:31], v[160:163], v[188:191]\n\t" \
    "ds_read_b128 v[184:187], %[la] offset:25600\n\t" \
    "v_mfma_f32_16x16x32_bf16 v[192:195], v[108:111], v[160:163], v[192:195]\n\t" \
    "v_permlane16_swap_b32 v204, v205\n\t" \
    "s_waitcnt lgkmcnt(5)\n\t" \
    "v_mfma_f32_16x16x32_bf16 v[188:191], v[32:35], v[164:167], v[188:191]\n\t" \
    "ds_read_b128 v[160:163], %[la] offset:26624\n\t" \
    "v_mfma_f32_16x16x32_bf16 v[192:195], v[112:115], v[164:167], v[192:195]\n\t" \
    "v_mul_f32 v205, %[kd], v205\n\t" \
    "s_waitcnt lgkmcnt(5)\n\t" \
    "v_mfma_f32_16x16x32_bf16 v[188:191], v[36:39], v[168:171], v[188:191]\n\t" \
    "ds_read_b128 v[164:167], %[la] offset:27648\n\t" \
    "v_mul_f32 v204, %[kd], v204\n\t" \
    "v_mfma_f32_16x16x32_bf16 v[192:195], v[116:119], v[168:171], v[192:195]\n\t" \
    "v_fma_f32 v204, -v205, v205, v204\n\t" \
    "s_waitcnt lgkmcnt(5)\n\t" \
    "v_mfma_f32_16x16x32_bf16 v[188:191], v[40:43], v[172:175], v[188:191]\n\t" \
    "ds_read_b128 v[168:171], %[la] offset:4096\n\t" \
    "v_max_f32 v204, 0, v204\n\t" \
    "v_mfma_f32_16x16x32_bf16 v[192:195], v[120:123], v[172:175], v[192:195]\n\t" \
    "v_add_f32 v206, %[ke], v204\n\t" \
    "s_waitcnt lgkmcnt(5)\n\t" \
    "v_mfma_f32_16x16x32_bf16 v[188:191], v[44:47], v[176:179], v[188:191]\n\t" \
    "ds_read_b128 v[172:175], %[la] offset:12288\n\t" \
    "v_rsq_f32 v206, v206\n\t" \
    "v_mfma_f32_16x16x32_bf16 v[192:195], v[124:127], v[176:179], v[192:195]\n\t" \
    "s_waitcnt lgkmcnt(5)\n\t" \
    "v_mfma_f32_16x16x32_bf16 v[188:191], v[48:51], v[180:183], v[188:191]\n\t" \
    "ds_read_b128 v[176:179], %[la] offset:20480\n\t" \
    "v_mul_f32 v207, v204, v206\n\t" \
    "v_mfma_f32_16x16x32_bf16 v[192:195], v[128:131], v[180:183], v[192:195]\n\t" \
    "v_fma_f32 v207, v207, v206, %[ke]\n\t" \
    "s_waitcnt lgkmcnt(5)\n\t" \
    "v_mfma_f32_16x16x32_bf16 v[188:191], v[52:55], v[184:187], v[188:191]\n\t" \
    "ds_read_b128 v[180:183], %[la] offset:28672\n\t" \
    "v_rsq_f32 v207, v207\n\t" \
    "v_mfma_f32_16x16x32_bf16 v[192:195], v[132:135], v[184:187], v[192:195]\n\t" \
    "v_mul_f32 v206, v206, v207\n\t" \
    "v_mul_f32 v207, -v205, v206\n\t" \
    "s_waitcnt lgkmcnt(5)\n\t" \
    "v_mfma_f32_16x16x32_bf16 v[188:191], v[56:59], v[160:163], v[188:191]\n\t" \
    "v_mul_f32 v208, v196, v207\n\t" \
    "v_mul_f32 v209, v197, v207\n\t" \
    "v_mfma_f32_16x16x32_bf16 v[192:195], v[136:139], v[160:163], v[192:195]\n\t" \
    "v_mul_f32 v210, v198, v207\n\t" \
    "v_mul_f32 v211, v199, v207\n\t" \
    "s_waitcnt lgkmcnt(4)\n\t" \
    "v_mfma_f32_16x16x32_bf16 v[188:191], v[60:63], v[164:167], v[188:191]\n\t" \
    "v_mul_f32 v212, v200, v207\n\t" \
    "v_mul_f32 v213, v201, v207\n\t" \
    "v_mfma_f32_16x16x32_bf16 v[192:195], v[140:143], v[164:167], v[192:195]\n\t" \
    "v_mul_f32 v214, v202, v207\n\t" \
    "v_mul_f32 v215, v203, v207\n\t" \
    "s_waitcnt lgkmcnt(3)\n\t" \
    "v_mfma_f32_16x16x32_bf16 v[188:191], v[64:67], v[168:171], v[188:191]\n\t" \
    "v_mov_b64 v[216:217], -1\n\t" \
    "v_mov_b64 v[218:219], -1\n\t" \
    "v_mfma_f32_16x16x32_bf16 v[192:195], v[144:147], v[168:171], v[192:195]\n\t" \
    "buffer_store_dwordx4 v[216:219], %[ob], %[rs], %[sa] offen sc1\n\t" \
    "s_waitcnt lgkmcnt(2)\n\t" \
    "v_mfma_f32_16x16x32_bf16 v[188:191], v[68:71], v[172:175], v[188:191]\n\t" \
    "v_mfma_f32_16x16x32_bf16 v[192:195], v[148:151], v[172:175], v[192:195]\n\t" \
    "s_waitcnt lgkmcnt(1)\n\t" \
    "v_mfma_f32_16x16x32_bf16 v[188:191], v[72:75], v[176:179], v[188:191]\n\t" \
    "v_mfma_f32_16x16x32_bf16 v[192:195], v[152:155], v[176:179], v[192:195]\n\t" \
    "s_waitcnt lgkmcnt(0)\n\t" \
    "v_mfma_f32_16x16x32_bf16 v[188:191], v[76:79], v[180:183], v[188:191]\n\t" \
    "v_mfma_f32_16x16x32_bf16 v[192:195], v[156:159], v[180:183], v[192:195]\n\t" \
    ASM_ST3 \
    "s_nop 4\n\t" \
    "v_fma_f32 v188, v188, v206, v208\n\t" \
    "v_fma_f32 v189, v189, v206, v209\n\t" \
    "v_fma_f32 v190, v190, v206, v210\n\t" \
    "v_fma_f32 v191, v191, v206, v211\n\t" \
    "v_mul_f32 v160, v188, v188\n\t" \
    "v_mul_f32 v161, v189, v189\n\t" \
    "v_mul_f32 v162, v190, v190\n\t" \
    "v_mul_f32 v163, v191, v191\n\t" \
    "v_fma_f32 v160, v160, %[k1], v220\n\t" \
    "v_fma_f32 v161, v161, %[k1], v220\n\t" \
    "v_fma_f32 v162, v162, %[k1], v220\n\t" \
    "v_fma_f32 v163, v163, %[k1], v220\n\t" \
    "v_mul_f32 v160, v188, v160\n\t" \
    "v_mul_f32 v161, v189, v161\n\t" \
    "v_mul_f32 v162, v190, v162\n\t" \
    "v_mul_f32 v163, v191, v163\n\t" \
    "v_fma_f32 v192, v192, v206, v212\n\t" \
    "v_fma_f32 v193, v193, v206, v213\n\t" \
    "v_fma_f32 v194, v194, v206, v214\n\t" \
    "v_fma_f32 v195, v195, v206, v215\n\t" \
    "v_mul_f32 v164, v192, v192\n\t" \
    "v_mul_f32 v165, v193, v193\n\t" \
    "v_mul_f32 v166, v194, v194\n\t" \
    "v_mul_f32 v167, v195, v195\n\t" \
    "v_fma_f32 v164, v164, %[k1], v220\n\t" \
    "v_fma_f32 v165, v165, %[k1], v220\n\t" \
    "v_fma_f32 v166, v166, %[k1], v220\n\t" \
    "v_fma_f32 v167, v167, %[k1], v220\n\t" \
    "v_mul_f32 v164, v192, v164\n\t" \
    "v_mul_f32 v165, v193, v165\n\t" \
    "v_mul_f32 v166, v194, v166\n\t" \
    "v_mul_f32 v167, v195, v167\n\t" \
    "v_exp_f32 v160, v160\n\t" \
    "v_exp_f32 v161, v161\n\t" \
    "v_exp_f32 v162, v162\n\t" \
    "v_exp_f32 v163, v163\n\t" \
    "v_exp_f32 v164, v164\n\t" \
    "v_exp_f32 v165, v165\n\t" \
    "v_exp_f32 v166, v166\n\t" \
    "v_exp_f32 v167, v167\n\t" \
    "v_add_f32 v160, 1.0, v160\n\t" \
    "v_add_f32 v161, 1.0, v161\n\t" \
    "v_add_f32 v162, 1.0, v162\n\t" \
    "v_add_f32 v163, 1.0, v163\n\t" \
    "v_add_f32 v164, 1.0, v164\n\t" \
    "v_add_f32 v165, 1.0, v165\n\t" \
    "v_add_f32 v166, 1.0, v166\n\t" \
    "v_add_f32 v167, 1.0, v167\n\t" \
    "v_rcp_f32 v160, v160\n\t" \
    "v_rcp_f32 v161, v161\n\t" \
    "v_rcp_f32 v162, v162\n\t" \
    "v_rcp_f32 v163, v163\n\t" \
    "v_rcp_f32 v164, v164\n\t" \
    "v_rcp_f32 v165, v165\n\t" \
    "v_rcp_f32 v166, v166\n\t" \
    "v_rcp_f32 v167, v167\n\t" \
    "v_mul_f32 v188, v188, v160\n\t" \
    "v_mul_f32 v189, v189, v161\n\t" \
    "v_mul_f32 v190, v190, v162\n\t" \
    "v_mul_f32 v191, v191, v163\n\t" \
    "v_mul_f32 v192, v192, v164\n\t" \
    "v_mul_f32 v193, v193, v165\n\t" \
    "v_mul_f32 v194, v194, v166\n\t" \
    "v_mul_f32 v195, v195, v167\n\t" \
    "v_cvt_pk_bf16_f32 v188, v188, v189\n\t" \
    "v_cvt_pk_bf16_f32 v189, v190, v191\n\t" \
    "v_cvt_pk_bf16_f32 v190, v192, v193\n\t" \
    "v_cvt_pk_bf16_f32 v191, v194, v195\n\t" \
    "v_min_u32 v188, -2, v188\n\t" \
    "v_min_u32 v189, -2, v189\n\t" \
    "v_min_u32 v190, -2, v190\n\t" \
    "v_min_u32 v191, -2, v191\n\t" \
    "s_nop 1\n\t" \
    "v_permlane16_swap_b32 v188, v190\n\t" \
    "v_permlane16_swap_b32 v189, v191\n\t" \
    "s_waitcnt vmcnt(1)\n\t" \
    "buffer_store_dwordx4 v[188:191], %[oa], %[rs], %[so] offen sc1\n\t" \
    "s_nop 1\n\t"
#define ASM_WT2_LO \
    "s_branch .Lt0%=\n\t" \
    ".Ltr%=:\n\t" \
    "s_cmp_gt_i32 %[n], 0\n\t" \
    "s_cbranch_scc0 .Ltg%=\n\t" \
    ".Lfw%=:\n\t" \
    "s_mov_b32 %[a], 0\n\t" \
    "v_max_u32 v252, v232, v233\n\t" \
    "v_max3_u32 v252, v234, v235, v252\n\t" \
    "v_cmp_eq_u32 vcc, -1, v252\n\t" \
    "s_cbranch_vccz .Lgw0_%=\n\t" \
    "buffer_load_dwordx4 v[232:235], %[l16], %[rs], %[i0] offen offset:0 sc1\n\t" \
    "s_mov_b32 %[a], 1\n\t" \
    ".Lgw0_%=:\n\t" \
    "v_max_u32 v252, v236, v237\n\t" \
    "v_max3_u32 v252, v238, v239, v252\n\t" \
    "v_cmp_eq_u32 vcc, -1, v252\n\t" \
    "s_cbranch_vccz .Lgw1_%=\n\t" \
    "buffer_load_dwordx4 v[236:239], %[l16], %[rs], %[i0] offen offset:1024 sc1\n\t" \
    "s_mov_b32 %[a], 1\n\t" \
    ".Lgw1_%=:\n\t" \
    "v_max_u32 v252, v240, v241\n\t" \
    "v_max3_u32 v252, v242, v243, v252\n\t" \
    "v_cmp_eq_u32 vcc, -1, v252\n\t" \
    "s_cbranch_vccz .Lgw2_%=\n\t" \
    "buffer_load_dwordx4 v[240:243], %[l16], %[rs], %[i0] offen offset:2048 sc1\n\t" \
    "s_mov_b32 %[a], 1\n\t" \
    ".Lgw2_%=:\n\t" \
    "v_max_u32 v252, v244, v245\n\t" \
    "v_max3_u32 v252, v246, v247, v252\n\t" \
    "v_cmp_eq_u32 vcc, -1, v252\n\t" \
    "s_cbranch_vccz .Lgw3_%=\n\t" \
    "buffer_load_dwordx4 v[244:247], %[l16], %[rs], %[i0] offen offset:3072 sc1\n\t" \
    "s_mov_b32 %[a], 1\n\t" \
    ".Lgw3_%=:\n\t" \
    "v_max_u32 v252, v248, v249\n\t" \
    "v_max3_u32 v252, v250, v251, v252\n\t" \
    "v_cmp_eq_u32 vcc, -1, v252\n\t" \
    "s_cbranch_vccz .Lgw4_%=\n\t" \
    "buffer_load_dwordx4 v[248:251], %[l16], %[rs], %[i1] offen offset:0 sc1\n\t" \
    "s_mov_b32 %[a], 1\n\t" \
    ".Lgw4_%=:\n\t" \
    "s_cmp_eq_u32 %[a], 0\n\t" \
    "s_cbranch_scc1 .Lhw%=\n\t" \
    "s_waitcnt vmcnt(20)\n\t" \
    ".Liw%=:\n\t" \
    "v_max_u32 v252, v232, v233\n\t" \
    "v_max3_u32 v252, v234, v235, v252\n\t" \
    "v_max3_u32 v252, v236, v237, v252\n\t" \
    "v_max3_u32 v252, v238, v239, v252\n\t" \
    "v_max3_u32 v252, v240, v241, v252\n\t" \
    "v_max3_u32 v252, v242, v243, v252\n\t" \
    "v_max3_u32 v252, v244, v245, v252\n\t" \
    "v_max3_u32 v252, v246, v247, v252\n\t" \
    "v_max3_u32 v252, v248, v249, v252\n\t" \
    "v_max3_u32 v252, v250, v251, v252\n\t" \
    "v_cmp_eq_u32 vcc, -1, v252\n\t" \
    "s_cbranch_vccz .Lhw%=\n\t" \
    "s_sleep 1\n\t" \
    "v_max_u32 v252, v232, v233\n\t" \
    "v_max3_u32 v252, v234, v235, v252\n\t" \
    "v_max3_u32 v252, v236, v237, v252\n\t" \
    "v_max3_u32 v252, v238, v239, v252\n\t" \
    "v_max3_u32 v252, v240, v241, v252\n\t" \
    "v_max3_u32 v252, v242, v243, v252\n\t" \
    "v_max3_u32 v252, v244, v245, v252\n\t" \
    "v_max3_u32 v252, v246, v247, v252\n\t" \
    "v_max3_u32 v252, v248, v249, v252\n\t" \
    "v_max3_u32 v252, v250, v251, v252\n\t" \
    "v_cmp_eq_u32 vcc, -1, v252\n\t" \
    "s_cbranch_vccz .Lhw%=\n\t" \
    "s_sleep 1\n\t" \
    "v_max_u32 v252, v232, v233\n\t" \
    "v_max3_u32 v252, v234, v235, v252\n\t" \
    "v_max3_u32 v252, v236, v237, v252\n\t" \
    "v_max3_u32 v252, v238, v239, v252\n\t" \
    "v_max3_u32 v252, v240, v241, v252\n\t" \
    "v_max3_u32 v252, v242, v243, v252\n\t" \
    "v_max3_u32 v252, v244, v245, v252\n\t" \
    "v_max3_u32 v252, v246, v247, v252\n\t" \
    "v_max3_u32 v252, v248, v249, v252\n\t" \
    "v_max3_u32 v252, v250, v251, v252\n\t" \
    "v_cmp_eq_u32 vcc, -1, v252\n\t" \
    "s_cbranch_vccz .Lhw%=\n\t" \
    "s_sleep 1\n\t" \
    "v_max_u32 v252, v232, v233\n\t" \
    "v_max3_u32 v252, v234, v235, v252\n\t" \
    "v_max3_u32 v252, v236, v237, v252\n\t" \
    "v_max3_u32 v252, v238, v239, v252\n\t" \
    "v_max3_u32 v252, v240, v241, v252\n\t" \
    "v_max3_u32 v252, v242, v243, v252\n\t" \
    "v_max3_u32 v252, v244, v245, v252\n\t" \
    "v_max3_u32 v252, v246, v247, v252\n\t" \
    "v_max3_u32 v252, v248, v249, v252\n\t" \
    "v_max3_u32 v252, v250, v251, v252\n\t" \
    "v_cmp_eq_u32 vcc, -1, v252\n\t" \
    "s_cbranch_vccz .Lhw%=\n\t" \
    "s_sleep 1\n\t" \
    "v_max_u32 v252, v232, v233\n\t" \
    "v_max3_u32 v252, v234, v235, v252\n\t" \
    "v_max3_u32 v252, v236, v237, v252\n\t" \
    "v_max3_u32 v252, v238, v239, v252\n\t" \
    "v_max3_u32 v252, v240, v241, v252\n\t" \
    "v_max3_u32 v252, v242, v243, v252\n\t" \
    "v_max3_u32 v252, v244, v245, v252\n\t" \
    "v_max3_u32 v252, v246, v247, v252\n\t" \
    "v_max3_u32 v252, v248, v249, v252\n\t" \
    "v_max3_u32 v252, v250, v251, v252\n\t" \
    "v_cmp_eq_u32 vcc, -1, v252\n\t" \
    "s_cbranch_vccz .Lhw%=\n\t" \
    "s_sleep 1\n\t" \
    "v_max_u32 v252, v232, v233\n\t" \
    "v_max3_u32 v252, v234, v235, v252\n\t" \
    "v_max3_u32 v252, v236, v237, v252\n\t" \
    "v_max3_u32 v252, v238, v239, v252\n\t" \
    "v_max3_u32 v252, v240, v241, v252\n\t" \
    "v_max3_u32 v252, v242, v243, v252\n\t" \
    "v_max3_u32 v252, v244, v245, v252\n\t" \
    "v_max3_u32 v252, v246, v247, v252\n\t" \
    "v_max3_u32 v252, v248, v249, v252\n\t" \
    "v_max3_u32 v252, v250, v251, v252\n\t" \
    "v_cmp_eq_u32 vcc, -1, v252\n\t" \
    "s_cbranch_vccz .Lhw%=\n\t" \
    "s_sleep 1\n\t" \
    "s_sub_u32 %[n], %[n], 1\n\t" \
    "s_cmp_gt_i32 %[n], 0\n\t" \
    "s_cbranch_scc1 .Lfw%=\n\t" \
    ".Lhw%=:\n\t" \
    "s_branch .Lta%=\n\t" \
    ".Lt0%=:\n\t" \
    "s_waitcnt vmcnt(1)\n\t" \
    ".Lta%=:\n\t" \
    "ds_write_b128 %[lw], v[232:235] offset:0\n\t" \
    "v_max_u32 v252, v232, v233\n\t" \
    "v_max3_u32 v252, v234, v235, v252\n\t" \
    "ds_write_b128 %[lw], v[236:239] offset:1024\n\t" \
    "v_max3_u32 v252, v236, v237, v252\n\t" \
    "v_max3_u32 v252, v238, v239, v252\n\t" \
    "ds_write_b128 %[lw], v[240:243] offset:2048\n\t" \
    "v_max3_u32 v252, v240, v241, v252\n\t" \
    "v_max3_u32 v252, v242, v243, v252\n\t" \
    "ds_write_b128 %[lw], v[244:247] offset:3072\n\t" \
    "v_max3_u32 v252, v244, v245, v252\n\t" \
    "v_max3_u32 v252, v246, v247, v252\n\t" \
    "ds_write_b128 %[lw], v[248:251] offset:4096\n\t" \
    "v_max3_u32 v252, v248, v249, v252\n\t" \
    "v_max3_u32 v252, v250, v251, v252\n\t" \
    "v_cmp_eq_u32 vcc, -1, v252\n\t" \
    "s_cbranch_vccnz .Ltr%=\n\t" \
    ".Ltg%=:\n\t" \
    ASM_ST2 \
    "s_waitcnt lgkmcnt(0)\n\t" \
    "s_setprio 2\n\t" \
    "s_barrier\n\t" ASM_ST \
    "ds_read_b128 v[160:163], %[la] offset:0\n\t" \
    "ds_read_b128 v[164:167], %[la] offset:1024\n\t" \
    "ds_read_b128 v[168:171], %[la] offset:2048\n\t" \
    "ds_read_b128 v[172:175], %[la] offset:3072\n\t" \
    "ds_read_b128 v[176:179], %[la] offset:8192\n\t" \
    "ds_read_b128 v[180:183], %[la] offset:9216\n\t" \
    "s_waitcnt lgkmcnt(5)\n\t" \
    "v_mfma_f32_16x16x32_bf16 v[188:191], v[0:3], v[160:163], 0\n\t" \
    "ds_read_b128 v[184:187], %[la] offset:10240\n\t" \
    "buffer_load_dwordx2 v[196:197], %[ro], %[rs], %[sx] offen sc1\n\t" \
    "v_mfma_f32_16x16x32_bf16 v[192:195], v[80:83], v[160:163], 0\n\t" \
    "buffer_load_dwordx2 v[198:199], %[ro], %[rs], %[sx] offen offset:512 sc1\n\t" \
    "s_waitcnt lgkmcnt(5)\n\t" \
    "v_mfma_f32_16x16x32_bf16 v[188:191], v[4:7], v[164:167], v[188:191]\n\t" \
    "ds_read_b128 v[160:163], %[la] offset:11264\n\t" \
    "v_lshrrev_b32 v204, 1, %[l16]\n\t" \
    "v_mfma_f32_16x16x32_bf16 v[192:195], v[84:87], v[164:167], v[192:195]\n\t" \
    "v_and_b32 v204, 0x78, v204\n\t" \
    "s_waitcnt lgkmcnt(5)\n\t" \
    "v_mfma_f32_16x16x32_bf16 v[188:191], v[8:11], v[168:171], v[188:191]\n\t" \
    "ds_read_b128 v[164:167], %[la] offset:16384\n\t" \
    "buffer_load_dwordx2 v[200:201], v204, %[rs], %[ss] offen sc1\n\t" \
    "v_mfma_f32_16x16x32_bf16 v[192:195], v[88:91], v[168:171], v[192:195]\n\t" \
    "v_mov_b64 v[216:217], -1\n\t" \
    "s_waitcnt lgkmcnt(5)\n\t" \
    "v_mfma_f32_16x16x32_bf16 v[188:191], v[12:15], v[172:175], v[188:191]\n\t" \
    "ds_read_b128 v[168:171], %[la] offset:17408\n\t" \
    "v_mov_b64 v[218:219], -1\n\t" \
    "v_mfma_f32_16x16x32_bf16 v[192:195], v[92:95], v[172:175], v[192:195]\n\t" \
    "buffer_store_dwordx4 v[216:219], %[l16], %[rs], %[sa] offen sc1\n\t" \
    "s_waitcnt lgkmcnt(5)\n\t" \
    "v_mfma_f32_16x16x32_bf16 v[188:191], v[16:19], v[176:179], v[188:191]\n\t" \
    "ds_read_b128 v[172:175], %[la] offset:18432\n\t" \
    "v_mfma_f32_16x16x32_bf16 v[192:195], v[96:99], v[176:179], v[192:195]\n\t" \
    "s_waitcnt lgkmcnt(5)\n\t" \
    "v_mfma_f32_16x16x32_bf16 v[188:191], v[20:23], v[180:183], v[188:191]\n\t" \
    "ds_read_b128 v[176:179], %[la] offset:19456\n\t" \
    "v_mfma_f32_16x16x32_bf16 v[192:195], v[100:103], v[180:183], v[192:195]\n\t" \
    "s_waitcnt lgkmcnt(5)\n\t" \
    "v_mfma_f32_16x16x32_bf16 v[188:191], v[24:27], v[184:187], v[188:191]\n\t" \
    "ds_read_b128 v[180:183], %[la] offset:24576\n\t" \
    "v_mfma_f32_16x16x32_bf16 v[192:195], v[104:107], v[184:187], v[192:195]\n\t" \
    "s_waitcnt lgkmcnt(5)\n\t" \
    "v_mfma_f32_16x16x32_bf16 v[188:191], v[28:31], v[160:163], v[188:191]\n\t" \
    "ds_read_b128 v[184:187], %[la] offset:25600\n\t" \
    "v_mfma_f32_16x16x32_bf16 v[192:195], v[108:111], v[160:163], v[192:195]\n\t" \
    "s_waitcnt lgkmcnt(5)\n\t" \
    "v_mfma_f32_16x16x32_bf16 v[188:191], v[32:35], v[164:167], v[188:191]\n\t" \
    "ds_read_b128 v[160:163], %[la] offset:26624\n\t" \
    "v_mfma_f32_16x16x32_bf16 v[192:195], v[112:115], v[164:167], v[192:195]\n\t" \
    "s_waitcnt lgkmcnt(5)\n\t" \
    "v_mfma_f32_16x16x32_bf16 v[188:191], v[36:39], v[168:171], v[188:191]\n\t" \
    "ds_read_b128 v[164:167], %[la] offset:27648\n\t" \
    "v_mfma_f32_16x16x32_bf16 v[192:195], v[116:119], v[168:171], v[192:195]\n\t" \
    "s_waitcnt lgkmcnt(5)\n\t" \
    "v_mfma_f32_16x16x32_bf16 v[188:191], v[40:43], v[172:175], v[188:191]\n\t" \
    "ds_read_b128 v[168:171], %[la] offset:4096\n\t" \
    "v_mfma_f32_16x16x32_bf16 v[192:195], v[120:123], v[172:175], v[192:195]\n\t" \
    "s_waitcnt lgkmcnt(5)\n\t" \
    "v_mfma_f32_16x16x32_bf16 v[188:191], v[44:47], v[176:179], v[188:191]\n\t" \
    "ds_read_b128 v[172:175], %[la] offset:12288\n\t" \
    "v_mfma_f32_16x16x32_bf16 v[192:195], v[124:127], v[176:179], v[192:195]\n\t" \
    "s_waitcnt lgkmcnt(5)\n\t" \
    "v_mfma_f32_16x16x32_bf16 v[188:191], v[48:51], v[180:183], v[188:191]\n\t" \
    "ds_read_b128 v[176:179], %[la] offset:20480\n\t" \
    "v_mfma_f32_16x16x32_bf16 v[192:195], v[128:131], v[180:183], v[192:195]\n\t" \
    "s_waitcnt lgkmcnt(5)\n\t" \
    "v_mfma_f32_16x16x32_bf16 v[188:191], v[52:55], v[184:187], v[188:191]\n\t" \
    "ds_read_b128 v[180:183], %[la] offset:28672\n\t" \
    "v_mfma_f32_16x16x32_bf16 v[192:195], v[132:135], v[184:187], v[192:195]\n\t" \
    "s_waitcnt lgkmcnt(5)\n\t" \
    "v_mfma_f32_16x16x32_bf16 v[188:191], v[56:59], v[160:163], v[188:191]\n\t" \
    "v_mfma_f32_16x16x32_bf16 v[192:195], v[136:139], v[160:163], v[192:195]\n\t" \
    "s_waitcnt lgkmcnt(4)\n\t" \
    "v_mfma_f32_16x16x32_bf16 v[188:191], v[60:63], v[164:167], v[188:191]\n\t" \
    "v_mfma_f32_16x16x32_bf16 v[192:195], v[140:143], v[164:167], v[192:195]\n\t" \
    "s_waitcnt lgkmcnt(3)\n\t" \
    "v_mfma_f32_16x16x32_bf16 v[188:191], v[64:67], v[168:171], v[188:191]\n\t" \
    "v_mfma_f32_16x16x32_bf16 v[192:195], v[144:147], v[168:171], v[192:195]\n\t" \
    "s_waitcnt lgkmcnt(2)\n\t" \
    "v_mfma_f32_16x16x32_bf16 v[188:191], v[68:71], v[172:175], v[188:191]\n\t" \
    "v_mfma_f32_16x16x32_bf16 v[192:195], v[148:151], v[172:175], v[192:195]\n\t" \
    "s_waitcnt lgkmcnt(1)\n\t" \
    "v_mfma_f32_16x16x32_bf16 v[188:191], v[72:75], v[176:179], v[188:191]\n\t" \
    "v_mfma_f32_16x16x32_bf16 v[192:195], v[152:155], v[176:179], v[192:195]\n\t" \
    "s_waitcnt lgkmcnt(0)\n\t" \
    "v_mfma_f32_16x16x32_bf16 v[188:191], v[76:79], v[180:183], v[188:191]\n\t" \
    "v_mfma_f32_16x16x32_bf16 v[192:195], v[156:159], v[180:183], v[192:195]\n\t" \
    "s_setprio 2\n\t" \
    "buffer_load_dwordx4 v[232:235], %[l16], %[rs], %[n0] offen offset:0 sc1\n\t" \
    "buffer_load_dwordx4 v[236:239], %[l16], %[rs], %[n0] offen offset:1024 sc1\n\t" \
    "buffer_load_dwordx4 v[240:243], %[l16], %[rs], %[n0] offen offset:2048 sc1\n\t" \
    "buffer_load_dwordx4 v[244:247], %[l16], %[rs], %[n0] offen offset:3072 sc1\n\t" \
    "buffer_load_dwordx4 v[248:251], %[l16], %[rs], %[n1] offen offset:0 sc1\n\t" \
    ASM_ST3 \
    "s_waitcnt vmcnt(6)\n\t" \
    "v_lshlrev_b32 v160, 16, v196\n\t" \
    "v_and_b32 v161, 0xffff0000, v196\n\t" \
    "v_lshlrev_b32 v162, 16, v197\n\t" \
    "v_and_b32 v163, 0xffff0000, v197\n\t" \
    "v_lshlrev_b32 v164, 16, v198\n\t" \
    "v_and_b32 v165, 0xffff0000, v198\n\t" \
    "v_lshlrev_b32 v166, 16, v199\n\t" \
    "v_and_b32 v167, 0xffff0000, v199\n\t" \
    "v_sub_f32 v160, v160, v200\n\t" \
    "v_sub_f32 v161, v161, v200\n\t" \
    "v_sub_f32 v162, v162, v200\n\t" \
    "v_sub_f32 v163, v163, v200\n\t" \
    "v_sub_f32 v164, v164, v200\n\t" \
    "v_sub_f32 v165, v165, v200\n\t" \
    "v_sub_f32 v166, v166, v200\n\t" \
    "v_sub_f32 v167, v167, v200\n\t" \
    "v_mul_f32 v160, v160, v201\n\t" \
    "v_mul_f32 v161, v161, v201\n\t" \
    "v_mul_f32 v162, v162, v201\n\t" \
    "v_mul_f32 v163, v163, v201\n\t" \
    "v_mul_f32 v164, v164, v201\n\t" \
    "v_mul_f32 v165, v165, v201\n\t" \
    "v_mul_f32 v166, v166, v201\n\t" \
    "v_mul_f32 v167, v167, v201\n\t" \
    "v_cvt_pk_bf16_f32 v160, v160, v161\n\t" \
    "v_cvt_pk_bf16_f32 v162, v162, v163\n\t" \
    "v_cvt_pk_bf16_f32 v164, v164, v165\n\t" \
    "v_cvt_pk_bf16_f32 v166, v166, v167\n\t" \
    "v_and_b32 v161, 0xffff0000, v160\n\t" \
    "v_lshlrev_b32 v160, 16, v160\n\t" \
    "v_and_b32 v163, 0xffff0000, v162\n\t" \
    "v_lshlrev_b32 v162, 16, v162\n\t" \
    "v_and_b32 v165, 0xffff0000, v164\n\t" \
    "v_lshlrev_b32 v164, 16, v164\n\t" \
    "v_and_b32 v167, 0xffff0000, v166\n\t" \
    "v_lshlrev_b32 v166, 16, v166\n\t" \
    "v_add_f32 v188, v188, v160\n\t" \
    "v_add_f32 v189, v189, v161\n\t" \
    "v_add_f32 v190, v190, v162\n\t" \
    "v_add_f32 v191, v191, v163\n\t" \
    "v_add_f32 v192, v192, v164\n\t" \
    "v_add_f32 v193, v193, v165\n\t" \
    "v_add_f32 v194, v194, v166\n\t" \
    "v_add_f32 v195, v195, v167\n\t" \
    "v_cvt_pk_bf16_f32 v188, v188, v189\n\t" \
    "v_cvt_pk_bf16_f32 v189, v190, v191\n\t" \
    "v_cvt_pk_bf16_f32 v190, v192, v193\n\t" \
    "v_cvt_pk_bf16_f32 v191, v194, v195\n\t" \
    "v_min_u32 v188, -2, v188\n\t" \
    "v_min_u32 v189, -2, v189\n\t" \
    "v_min_u32 v190, -2, v190\n\t" \
    "v_min_u32 v191, -2, v191\n\t" \
    "s_nop 1\n\t" \
    "v_permlane16_swap_b32 v188, v190\n\t" \
    "v_permlane16_swap_b32 v189, v191\n\t" \
    "s_waitcnt vmcnt(6)\n\t" \
    "buffer_store_dwordx4 v[188:191], %[oa], %[rs], %[so] offen sc1\n\t" \
    "s_nop 1\n\t" \
    "s_setprio 0\n\t"
#define ASM_WT2_HI \
    "s_setprio 1\n\t" \
    "s_barrier\n\t" ASM_ST \
    "ds_read_b128 v[160:163], %[la] offset:0\n\t" \
    "ds_read_b128 v[164:167], %[la] offset:1024\n\t" \
    "ds_read_b128 v[168:171], %[la] offset:2048\n\t" \
    "ds_read_b128 v[172:175], %[la] offset:3072\n\t" \
    "ds_read_b128 v[176:179], %[la] offset:8192\n\t" \
    "ds_read_b128 v[180:183], %[la] offset:9216\n\t" \
    "s_waitcnt lgkmcnt(5)\n\t" \
    "v_mfma_f32_16x16x32_bf16 v[188:191], v[0:3], v[160:163], 0\n\t" \
    "ds_read_b128 v[184:187], %[la] offset:10240\n\t" \
    "buffer_load_dwordx2 v[196:197], %[ro], %[rs], %[sx] offen sc1\n\t" \
    "v_mfma_f32_16x16x32_bf16 v[192:195], v[80:83], v[160:163], 0\n\t" \
    "buffer_load_dwordx2 v[198:199], %[ro], %[rs], %[sx] offen offset:512 sc1\n\t" \
    "s_waitcnt lgkmcnt(5)\n\t" \
    "v_mfma_f32_16x16x32_bf16 v[188:191], v[4:7], v[164:167], v[188:191]\n\t" \
    "ds_read_b128 v[160:163], %[la] offset:11264\n\t" \
    "v_lshrrev_b32 v204, 1, %[l16]\n\t" \
    "v_mfma_f32_16x16x32_bf16 v[192:195], v[84:87], v[164:167], v[192:195]\n\t" \
    "v_and_b32 v204, 0x78, v204\n\t" \
    "s_waitcnt lgkmcnt(5)\n\t" \
    "v_mfma_f32_16x16x32_bf16 v[188:191], v[8:11], v[168:171], v[188:191]\n\t" \
    "ds_read_b128 v[164:167], %[la] offset:16384\n\t" \
    "buffer_load_dwordx2 v[200:201], v204, %[rs], %[ss] offen sc1\n\t" \
    "v_mfma_f32_16x16x32_bf16 v[192:195], v[88:91], v[168:171], v[192:195]\n\t" \
    "v_mov_b64 v[216:217], -1\n\t" \
    "s_waitcnt lgkmcnt(5)\n\t" \
    "v_mfma_f32_16x16x32_bf16 v[188:191], v[12:15], v[172:175], v[188:191]\n\t" \
    "ds_read_b128 v[168:171], %[la] offset:17408\n\t" \
    "v_mov_b64 v[218:219], -1\n\t" \
    "v_mfma_f32_16x16x32_bf16 v[192:195], v[92:95], v[172:175], v[192:195]\n\t" \
    "buffer_store_dwordx4 v[216:219], %[l16], %[rs], %[sa] offen sc1\n\t" \
    "s_waitcnt lgkmcnt(5)\n\t" \
    "v_mfma_f32_16x16x32_bf16 v[188:191], v[16:19], v[176:179], v[188:191]\n\t" \
    "ds_read_b128 v[172:175], %[la] offset:18432\n\t" \
    "v_mfma_f32_16x16x32_bf16 v[192:195], v[96:99], v[176:179], v[192:195]\n\t" \
    "s_waitcnt lgkmcnt(5)\n\t" \
    "v_mfma_f32_16x16x32_bf16 v[188:191], v[20:23], v[180:183], v[188:191]\n\t" \
    "ds_read_b128 v[176:179], %[la] offset:19456\n\t" \
    "v_mfma_f32_16x16x32_bf16 v[192:195], v[100:103], v[180:183], v[192:195]\n\t" \
    "s_waitcnt lgkmcnt(5)\n\t" \
    "v_mfma_f32_16x16x32_bf16 v[188:191], v[24:27], v[184:187], v[188:191]\n\t" \
    "ds_read_b128 v[180:183], %[la] offset:24576\n\t" \
    "v_mfma_f32_16x16x32_bf16 v[192:195], v[104:107], v[184:187], v[192:195]\n\t" \
    "s_waitcnt lgkmcnt(5)\n\t" \
    "v_mfma_f32_16x16x32_bf16 v[188:191], v[28:31], v[160:163], v[188:191]\n\t" \
    "ds_read_b128 v[184:187], %[la] offset:25600\n\t" \
    "v_mfma_f32_16x16x32_bf16 v[192:195], v[108:111], v[160:163], v[192:195]\n\t" \
    "s_waitcnt lgkmcnt(5)\n\t" \
    "v_mfma_f32_16x16x32_bf16 v[188:191], v[32:35], v[164:167], v[188:191]\n\t" \
    "ds_read_b128 v[160:163], %[la] offset:26624\n\t" \
    "v_mfma_f32_16x16x32_bf16 v[192:195], v[112:115], v[164:167], v[192:195]\n\t" \
    "s_waitcnt lgkmcnt(5)\n\t" \
    "v_mfma_f32_16x16x32_bf16 v[188:191], v[36:39], v[168:171], v[188:191]\n\t" \
    "ds_read_b128 v[164:167], %[la] offset:27648\n\t" \
    "v_mfma_f32_16x16x32_bf16 v[192:195], v[116:119], v[168:171], v[192:195]\n\t" \
    "s_waitcnt lgkmcnt(5)\n\t" \
    "v_mfma_f32_16x16x32_bf16 v[188:191], v[40:43], v[172:175], v[188:191]\n\t" \
    "ds_read_b128 v[168:171], %[la] offset:4096\n\t" \
    "v_mfma_f32_16x16x32_bf16 v[192:195], v[120:123], v[172:175], v[192:195]\n\t" \
    "s_waitcnt lgkmcnt(5)\n\t" \
    "v_mfma_f32_16x16x32_bf16 v[188:191], v[44:47], v[176:179], v[188:191]\n\t" \
    "ds_read_b128 v[172:175], %[la] offset:12288\n\t" \
    "v_mfma_f32_16x16x32_bf16 v[192:195], v[124:127], v[176:179], v[192:195]\n\t" \
    "s_waitcnt lgkmcnt(5)\n\t" \
    "v_mfma_f32_16x16x32_bf16 v[188:191], v[48:51], v[180:183], v[188:191]\n\t" \
    "ds_read_b128 v[176:179], %[la] offset:20480\n\t" \
    "v_mfma_f32_16x16x32_bf16 v[192:195], v[128:131], v[180:183], v[192:195]\n\t" \
    "s_waitcnt lgkmcnt(5)\n\t" \
    "v_mfma_f32_16x16x32_bf16 v[188:191], v[52:55], v[184:187], v[188:191]\n\t" \
    "ds_read_b128 v[180:183], %[la] offset:28672\n\t" \
    "v_mfma_f32_16x16x32_bf16 v[192:195], v[132:135], v[184:187], v[192:195]\n\t" \
    "s_waitcnt lgkmcnt(5)\n\t" \
    "v_mfma_f32_16x16x32_bf16 v[188:191], v[56:59], v[160:163], v[188:191]\n\t" \
    "v_mfma_f32_16x16x32_bf16 v[192:195], v[136:139], v[160:163], v[192:195]\n\t" \
    "s_waitcnt lgkmcnt(4)\n\t" \
    "v_mfma_f32_16x16x32_bf16 v[188:191], v[60:63], v[164:167], v[188:191]\n\t" \
    "v_mfma_f32_16x16x32_bf16 v[192:195], v[140:143], v[164:167], v[192:195]\n\t" \
    "s_waitcnt lgkmcnt(3)\n\t" \
    "v_mfma_f32_16x16x32_bf16 v[188:191], v[64:67], v[168:171], v[188:191]\n\t" \
    "v_mfma_f32_16x16x32_bf16 v[192:195], v[144:147], v[168:171], v[192:195]\n\t" \
    "s_waitcnt lgkmcnt(2)\n\t" \
    "v_mfma_f32_16x16x32_bf16 v[188:191], v[68:71], v[172:175], v[188:191]\n\t" \
    "v_mfma_f32_16x16x32_bf16 v[192:195], v[148:151], v[172:175], v[192:195]\n\t" \
    "s_waitcnt lgkmcnt(1)\n\t" \
    "v_mfma_f32_16x16x32_bf16 v[188:191], v[72:75], v[176:179], v[188:191]\n\t" \
    "v_mfma_f32_16x16x32_bf16 v[192:195], v[152:155], v[176:179], v[192:195]\n\t" \
    "s_waitcnt lgkmcnt(0)\n\t" \
    "v_mfma_f32_16x16x32_bf16 v[188:191], v[76:79], v[180:183], v[188:191]\n\t" \
    "v_mfma_f32_16x16x32_bf16 v[192:195], v[156:159], v[180:183], v[192:195]\n\t" \
    ASM_ST3 \
    "s_waitcnt vmcnt(1)\n\t" \
    "v_lshlrev_b32 v160, 16, v196\n\t" \
    "v_and_b32 v161, 0xffff0000, v196\n\t" \
    "v_lshlrev_b32 v162, 16, v197\n\t" \
    "v_and_b32 v163, 0xffff0000, v197\n\t" \
    "v_lshlrev_b32 v164, 16, v198\n\t" \
    "v_and_b32 v165, 0xffff0000, v198\n\t" \
    "v_lshlrev_b32 v166, 16, v199\n\t" \
    "v_and_b32 v167, 0xffff0000, v199\n\t" \
    "v_sub_f32 v160, v160, v200\n\t" \
    "v_sub_f32 v161, v161, v200\n\t" \
    "v_sub_f32 v162, v162, v200\n\t" \
    "v_sub_f32 v163, v163, v200\n\t" \
    "v_sub_f32 v164, v164, v200\n\t" \
    "v_sub_f32 v165, v165, v200\n\t" \
    "v_sub_f32 v166, v166, v200\n\t" \
    "v_sub_f32 v167, v167, v200\n\t" \
    "v_mul_f32 v160, v160, v201\n\t" \
    "v_mul_f32 v161, v161, v201\n\t" \
    "v_mul_f32 v162, v162, v201\n\t" \
    "v_mul_f32 v163, v163, v201\n\t" \
    "v_mul_f32 v164, v164, v201\n\t" \
    "v_mul_f32 v165, v165, v201\n\t" \
    "v_mul_f32 v166, v166, v201\n\t" \
    "v_mul_f32 v167, v167, v201\n\t" \
    "v_cvt_pk_bf16_f32 v160, v160, v161\n\t" \
    "v_cvt_pk_bf16_f32 v162, v162, v163\n\t" \
    "v_cvt_pk_bf16_f32 v164, v164, v165\n\t" \
    "v_cvt_pk_bf16_f32 v166, v166, v167\n\t" \
    "v_and_b32 v161, 0xffff0000, v160\n\t" \
    "v_lshlrev_b32 v160, 16, v160\n\t" \
    "v_and_b32 v163, 0xffff0000, v162\n\t" \
    "v_lshlrev_b32 v162, 16, v162\n\t" \
    "v_and_b32 v165, 0xffff0000, v164\n\t" \
    "v_lshlrev_b32 v164, 16, v164\n\t" \
    "v_and_b32 v167, 0xffff0000, v166\n\t" \
    "v_lshlrev_b32 v166, 16, v166\n\t" \
    "v_add_f32 v188, v188, v160\n\t" \
    "v_add_f32 v189, v189, v161\n\t" \
    "v_add_f32 v190, v190, v162\n\t" \
    "v_add_f32 v191, v191, v163\n\t" \
    "v_add_f32 v192, v192, v164\n\t" \
    "v_add_f32 v193, v193, v165\n\t" \
    "v_add_f32 v194, v194, v166\n\t" \
    "v_add_f32 v195, v195, v167\n\t" \
    "v_cvt_pk_bf16_f32 v188, v188, v189\n\t" \
    "v_cvt_pk_bf16_f32 v189, v190, v191\n\t" \
    "v_cvt_pk_bf16_f32 v190, v192, v193\n\t" \
    "v_cvt_pk_bf16_f32 v191, v194, v195\n\t" \
    "v_min_u32 v188, -2, v188\n\t" \
    "v_min_u32 v189, -2, v189\n\t" \
    "v_min_u32 v190, -2, v190\n\t" \
    "v_min_u32 v191, -2, v191\n\t" \
    "s_nop 1\n\t" \
    "v_permlane16_swap_b32 v188, v190\n\t" \
    "v_permlane16_swap_b32 v189, v191\n\t" \
    "s_waitcnt vmcnt(1)\n\t" \
    "buffer_store_dwordx4 v[188:191], %[oa], %[rs], %[so] offen sc1\n\t" \
    "s_nop 1\n\t"
#define ASM_WT3_LO \
    "s_branch .Lt0%=\n\t" \
    ".Ltr%=:\n\t" \
    "s_cmp_gt_i32 %[n], 0\n\t" \
    "s_cbranch_scc0 .Ltg%=\n\t" \
    ".Lfw%=:\n\t" \
    "s_mov_b32 %[a], 0\n\t" \
    "v_max_u32 v252, v232, v233\n\t" \
    "v_max3_u32 v252, v234, v235, v252\n\t" \
    "v_cmp_eq_u32 vcc, -1, v252\n\t" \
    "s_cbranch_vccz .Lgw0_%=\n\t" \
    "buffer_load_dwordx4 v[232:235], %[l16], %[rs], %[i0] offen offset:0 sc1\n\t" \
    "s_mov_b32 %[a], 1\n\t" \
    ".Lgw0_%=:\n\t" \
    "v_max_u32 v252, v236, v237\n\t" \
    "v_max3_u32 v252, v238, v239, v252\n\t" \
    "v_cmp_eq_u32 vcc, -1, v252\n\t" \
    "s_cbranch_vccz .Lgw1_%=\n\t" \
    "buffer_load_dwordx4 v[236:239], %[l16], %[rs], %[i0] offen offset:1024 sc1\n\t" \
    "s_mov_b32 %[a], 1\n\t" \
    ".Lgw1_%=:\n\t" \
    "v_max_u32 v252, v240, v241\n\t" \
    "v_max3_u32 v252, v242, v243, v252\n\t" \
    "v_cmp_eq_u32 vcc, -1, v252\n\t" \
    "s_cbranch_vccz .Lgw2_%=\n\t" \
    "buffer_load_dwordx4 v[240:243], %[l16], %[rs], %[i0] offen offset:2048 sc1\n\t" \
    "s_mov_b32 %[a], 1\n\t" \
    ".Lgw2_%=:\n\t" \
    "v_max_u32 v252, v244, v245\n\t" \
    "v_max3_u32 v252, v246, v247, v252\n\t" \
    "v_cmp_eq_u32 vcc, -1, v252\n\t" \
    "s_cbranch_vccz .Lgw3_%=\n\t" \
    "buffer_load_dwordx4 v[244:247], %[l16], %[rs], %[i0] offen offset:3072 sc1\n\t" \
    "s_mov_b32 %[a], 1\n\t" \
    ".Lgw3_%=:\n\t" \
    "v_max_u32 v252, v248, v249\n\t" \
    "v_max3_u32 v252, v250, v251, v252\n\t" \
    "v_cmp_eq_u32 vcc, -1, v252\n\t" \
    "s_cbranch_vccz .Lgw4_%=\n\t" \
    "buffer_load_dwordx4 v[248:251], %[l16], %[rs], %[i1] offen offset:0 sc1\n\t" \
    "s_mov_b32 %[a], 1\n\t" \
    ".Lgw4_%=:\n\t" \
    "s_cmp_eq_u32 %[a], 0\n\t" \
    "s_cbranch_scc1 .Lhw%=\n\t" \
    "s_waitcnt vmcnt(20)\n\t" \
    ".Liw%=:\n\t" \
    "v_max_u32 v252, v232, v233\n\t" \
    "v_max3_u32 v252, v234, v235, v252\n\t" \
    "v_max3_u32 v252, v236, v237, v252\n\t" \
    "v_max3_u32 v252, v238, v239, v252\n\t" \
    "v_max3_u32 v252, v240, v241, v252\n\t" \
    "v_max3_u32 v252, v242, v243, v252\n\t" \
    "v_max3_u32 v252, v244, v245, v252\n\t" \
    "v_max3_u32 v252, v246, v247, v252\n\t" \
    "v_max3_u32 v252, v248, v249, v252\n\t" \
    "v_max3_u32 v252, v250, v251, v252\n\t" \
    "v_cmp_eq_u32 vcc, -1, v252\n\t" \
    "s_cbranch_vccz .Lhw%=\n\t" \
    "s_sleep 1\n\t" \
    "v_max_u32 v252, v232, v233\n\t" \
    "v_max3_u32 v252, v234, v235, v252\n\t" \
    "v_max3_u32 v252, v236, v237, v252\n\t" \
    "v_max3_u32 v252, v238, v239, v252\n\t" \
    "v_max3_u32 v252, v240, v241, v252\n\t" \
    "v_max3_u32 v252, v242, v243, v252\n\t" \
    "v_max3_u32 v252, v244, v245, v252\n\t" \
    "v_max3_u32 v252, v246, v247, v252\n\t" \
    "v_max3_u32 v252, v248, v249, v252\n\t" \
    "v_max3_u32 v252, v250, v251, v252\n\t" \
    "v_cmp_eq_u32 vcc, -1, v252\n\t" \
    "s_cbranch_vccz .Lhw%=\n\t" \
    "s_sleep 1\n\t" \
    "v_max_u32 v252, v232, v233\n\t" \
    "v_max3_u32 v252, v234, v235, v252\n\t" \
    "v_max3_u32 v252, v236, v237, v252\n\t" \
    "v_max3_u32 v252, v238, v239, v252\n\t" \
    "v_max3_u32 v252, v240, v241, v252\n\t" \
    "v_max3_u32 v252, v242, v243, v252\n\t" \
    "v_max3_u32 v252, v244, v245, v252\n\t" \
    "v_max3_u32 v252, v246, v247, v252\n\t" \
    "v_max3_u32 v252, v248, v249, v252\n\t" \
    "v_max3_u32 v252, v250, v251, v252\n\t" \
    "v_cmp_eq_u32 vcc, -1, v252\n\t" \
    "s_cbranch_vccz .Lhw%=\n\t" \
    "s_sleep 1\n\t" \
    "v_max_u32 v252, v232, v233\n\t" \
    "v_max3_u32 v252, v234, v235, v252\n\t" \
    "v_max3_u32 v252, v236, v237, v252\n\t" \
    "v_max3_u32 v252, v238, v239, v252\n\t" \
    "v_max3_u32 v252, v240, v241, v252\n\t" \
    "v_max3_u32 v252, v242, v243, v252\n\t" \
    "v_max3_u32 v252, v244, v245, v252\n\t" \
    "v_max3_u32 v252, v246, v247, v252\n\t" \
    "v_max3_u32 v252, v248, v249, v252\n\t" \
    "v_max3_u32 v252, v250, v251, v252\n\t" \
    "v_cmp_eq_u32 vcc, -1, v252\n\t" \
    "s_cbranch_vccz .Lhw%=\n\t" \
    "s_sleep 1\n\t" \
    "v_max_u32 v252, v232, v233\n\t" \
    "v_max3_u32 v252, v234, v235, v252\n\t" \
    "v_max3_u32 v252, v236, v237, v252\n\t" \
    "v_max3_u32 v252, v238, v239, v252\n\t" \
    "v_max3_u32 v252, v240, v241, v252\n\t" \
    "v_max3_u32 v252, v242, v243, v252\n\t" \
    "v_max3_u32 v252, v244, v245, v252\n\t" \
    "v_max3_u32 v252, v246, v247, v252\n\t" \
    "v_max3_u32 v252, v248, v249, v252\n\t" \
    "v_max3_u32 v252, v250, v251, v252\n\t" \
    "v_cmp_eq_u32 vcc, -1, v252\n\t" \
    "s_cbranch_vccz .Lhw%=\n\t" \
    "s_sleep 1\n\t" \
    "v_max_u32 v252, v232, v233\n\t" \
    "v_max3_u32 v252, v234, v235, v252\n\t" \
    "v_max3_u32 v252, v236, v237, v252\n\t" \
    "v_max3_u32 v252, v238, v239, v252\n\t" \
    "v_max3_u32 v252, v240, v241, v252\n\t" \
    "v_max3_u32 v252, v242, v243, v252\n\t" \
    "v_max3_u32 v252, v244, v245, v252\n\t" \
    "v_max3_u32 v252, v246, v247, v252\n\t" \
    "v_max3_u32 v252, v248, v249, v252\n\t" \
    "v_max3_u32 v252, v250, v251, v252\n\t" \
    "v_cmp_eq_u32 vcc, -1, v252\n\t" \
    "s_cbranch_vccz .Lhw%=\n\t" \
    "s_sleep 1\n\t" \
    "s_sub_u32 %[n], %[n], 1\n\t" \
    "s_cmp_gt_i32 %[n], 0\n\t" \
    "s_cbranch_scc1 .Lfw%=\n\t" \
    ".Lhw%=:\n\t" \
    "s_branch .Lta%=\n\t" \
    ".Lt0%=:\n\t" \
    "s_waitcnt vmcnt(1)\n\t" \
    ".Lta%=:\n\t" \
    "ds_write_b128 %[lw], v[232:235] offset:0\n\t" \
    "v_max_u32 v252, v232, v233\n\t" \
    "v_max3_u32 v252, v234, v235, v252\n\t" \
    "v_mfma_f32_16x16x32_bf16 v[188:191], v[232:235], v[232:235], 0\n\t" \
    "v_mfma_f32_16x16x32_bf16 v[192:195], v[216:219], v[232:235], 0\n\t" \
    "ds_write_b128 %[lw], v[236:239] offset:1024\n\t" \
    "v_max3_u32 v252, v236, v237, v252\n\t" \
    "v_max3_u32 v252, v238, v239, v252\n\t" \
    "v_mfma_f32_16x16x32_bf16 v[188:191], v[236:239], v[236:239], v[188:191]\n\t" \
    "v_mfma_f32_16x16x32_bf16 v[192:195], v[216:219], v[236:239], v[192:195]\n\t" \
    "ds_write_b128 %[lw], v[240:243] offset:2048\n\t" \
    "v_max3_u32 v252, v240, v241, v252\n\t" \
    "v_max3_u32 v252, v242, v243, v252\n\t" \
    "v_mfma_f32_16x16x32_bf16 v[188:191], v[240:243], v[240:243], v[188:191]\n\t" \
    "v_mfma_f32_16x16x32_bf16 v[192:195], v[216:219], v[240:243], v[192:195]\n\t" \
    "ds_write_b128 %[lw], v[244:247] offset:3072\n\t" \
    "v_max3_u32 v252, v244, v245, v252\n\t" \
    "v_max3_u32 v252, v246, v247, v252\n\t" \
    "v_mfma_f32_16x16x32_bf16 v[188:191], v[244:247], v[244:247], v[188:191]\n\t" \
    "v_mfma_f32_16x16x32_bf16 v[192:195], v[216:219], v[244:247], v[192:195]\n\t" \
    "ds_write_b128 %[lw], v[248:251] offset:4096\n\t" \
    "v_max3_u32 v252, v248, v249, v252\n\t" \
    "v_max3_u32 v252, v250, v251, v252\n\t" \
    "v_mfma_f32_16x16x32_bf16 v[188:191], v[248:251], v[248:251], v[188:191]\n\t" \
    "v_mfma_f32_16x16x32_bf16 v[192:195], v[216:219], v[248:251], v[192:195]\n\t" \
    "v_cmp_eq_u32 vcc, -1, v252\n\t" \
    "s_cbranch_vccnz .Ltr%=\n\t" \
    ".Ltg%=:\n\t" \
    ASM_ST2 \
    "s_nop 5\n\t" \
    "ds_write_b128 %[lw], v[188:191] offset:5120\n\t" \
    "ds_write_b128 %[lw], v[192:195] offset:6144\n\t" \
    "s_waitcnt lgkmcnt(0)\n\t" \
    "s_setprio 2\n\t" \
    "s_barrier\n\t" ASM_ST \
    "ds_read_b128 v[160:163], %[la] offset:0\n\t" \
    "ds_read_b128 v[164:167], %[la] offset:1024\n\t" \
    "ds_read_b128 v[168:171], %[la] offset:2048\n\t" \
    "ds_read2st64_b32 v[204:205], %[sr] offset0:20 offset1:24\n\t" \
    "ds_read_b128 v[172:175], %[la] offset:3072\n\t" \
    "ds_read_b128 v[176:179], %[la] offset:8192\n\t" \
    "ds_read_b128 v[180:183], %[la] offset:9216\n\t" \
    "s_waitcnt lgkmcnt(6)\n\t" \
    "v_mfma_f32_16x16x32_bf16 v[188:191], v[0:3], v[160:163], 0\n\t" \
    "ds_read_b128 v[184:187], %[la] offset:10240\n\t" \
    "v_mfma_f32_16x16x32_bf16 v[192:195], v[80:83], v[160:163], 0\n\t" \
    "s_waitcnt lgkmcnt(6)\n\t" \
    "v_mfma_f32_16x16x32_bf16 v[188:191], v[4:7], v[164:167], v[188:191]\n\t" \
    "ds_read_b128 v[160:163], %[la] offset:11264\n\t" \
    "v_mfma_f32_16x16x32_bf16 v[192:195], v[84:87], v[164:167], v[192:195]\n\t" \
    "s_waitcnt lgkmcnt(6)\n\t" \
    "v_mfma_f32_16x16x32_bf16 v[188:191], v[8:11], v[168:171], v[188:191]\n\t" \
    "ds_read_b128 v[164:167], %[la] offset:16384\n\t" \
    "s_waitcnt lgkmcnt(6)\n\t" \
    "v_mfma_f32_16x16x32_bf16 v[192:195], v[88:91], v[168:171], v[192:195]\n\t" \
    "v_permlane16_swap_b32 v204, v205\n\t" \
    "s_waitcnt lgkmcnt(5)\n\t" \
    "v_mfma_f32_16x16x32_bf16 v[188:191], v[12:15], v[172:175], v[188:191]\n\t" \
    "ds_read_b128 v[168:171], %[la] offset:17408\n\t" \
    "v_mfma_f32_16x16x32_bf16 v[192:195], v[92:95], v[172:175], v[192:195]\n\t" \
    "v_add_f32 v206, v204, v205\n\t" \
    "s_waitcnt lgkmcnt(5)\n\t" \
    "v_mfma_f32_16x16x32_bf16 v[188:191], v[16:19], v[176:179], v[188:191]\n\t" \
    "ds_read_b128 v[172:175], %[la] offset:18432\n\t" \
    "v_add_f32 v207, v204, v205\n\t" \
    "v_mfma_f32_16x16x32_bf16 v[192:195], v[96:99], v[176:179], v[192:195]\n\t" \
    "s_waitcnt lgkmcnt(5)\n\t" \
    "v_mfma_f32_16x16x32_bf16 v[188:191], v[20:23], v[180:183], v[188:191]\n\t" \
    "ds_read_b128 v[176:179], %[la] offset:19456\n\t" \
    "v_permlane32_swap_b32 v206, v207\n\t" \
    "v_mfma_f32_16x16x32_bf16 v[192:195], v[100:103], v[180:183], v[192:195]\n\t" \
    "s_waitcnt lgkmcnt(5)\n\t" \
    "v_mfma_f32_16x16x32_bf16 v[188:191], v[24:27], v[184:187], v[188:191]\n\t" \
    "ds_read_b128 v[180:183], %[la] offset:24576\n\t" \
    "v_add_f32 v204, v206, v207\n\t" \
    "v_mfma_f32_16x16x32_bf16 v[192:195], v[104:107], v[184:187], v[192:195]\n\t" \
    "v_add_f32 v205, v206, v207\n\t" \
    "s_waitcnt lgkmcnt(5)\n\t" \
    "v_mfma_f32_16x16x32_bf16 v[188:191], v[28:31], v[160:163], v[188:191]\n\t" \
    "ds_read_b128 v[184:187], %[la] offset:25600\n\t" \
    "v_mfma_f32_16x16x32_bf16 v[192:195], v[108:111], v[160:163], v[192:195]\n\t" \
    "v_permlane16_swap_b32 v204, v205\n\t" \
    "s_waitcnt lgkmcnt(5)\n\t" \
    "v_mfma_f32_16x16x32_bf16 v[188:191], v[32:35], v[164:167], v[188:191]\n\t" \
    "ds_read_b128 v[160:163], %[la] offset:26624\n\t" \
    "v_mfma_f32_16x16x32_bf16 v[192:195], v[112:115], v[164:167], v[192:195]\n\t" \
    "v_mul_f32 v205, %[kd], v205\n\t" \
    "s_waitcnt lgkmcnt(5)\n\t" \
    "v_mfma_f32_16x16x32_bf16 v[188:191], v[36:39], v[168:171], v[188:191]\n\t" \
    "ds_read_b128 v[164:167], %[la] offset:27648\n\t" \
    "v_mul_f32 v204, %[kd], v204\n\t" \
    "v_mfma_f32_16x16x32_bf16 v[192:195], v[116:119], v[168:171], v[192:195]\n\t" \
    "v_fma_f32 v204, -v205, v205, v204\n\t" \
    "s_waitcnt lgkmcnt(5)\n\t" \
    "v_mfma_f32_16x16x32_bf16 v[188:191], v[40:43], v[172:175], v[188:191]\n\t" \
    "ds_read_b128 v[168:171], %[la] offset:4096\n\t" \
    "v_max_f32 v204, 0, v204\n\t" \
    "v_mfma_f32_16x16x32_bf16 v[192:195], v[120:123], v[172:175], v[192:195]\n\t" \
    "v_add_f32 v206, %[ke], v204\n\t" \
    "s_waitcnt lgkmcnt(5)\n\t" \
    "v_mfma_f32_16x16x32_bf16 v[188:191], v[44:47], v[176:179], v[188:191]\n\t" \
    "ds_read_b128 v[172:175], %[la] offset:12288\n\t" \
    "v_rsq_f32 v206, v206\n\t" \
    "v_mfma_f32_16x16x32_bf16 v[192:195], v[124:127], v[176:179], v[192:195]\n\t" \
    "s_waitcnt lgkmcnt(5)\n\t" \
    "v_mfma_f32_16x16x32_bf16 v[188:191], v[48:51], v[180:183], v[188:191]\n\t" \
    "ds_read_b128 v[176:179], %[la] offset:20480\n\t" \
    "v_mul_f32 v207, -v205, v206\n\t" \
    "v_mfma_f32_16x16x32_bf16 v[192:195], v[128:131], v[180:183], v[192:195]\n\t" \
    "v_mul_f32 v208, v196, v207\n\t" \
    "s_waitcnt lgkmcnt(5)\n\t" \
    "v_mfma_f32_16x16x32_bf16 v[188:191], v[52:55], v[184:187], v[188:191]\n\t" \
    "ds_read_b128 v[180:183], %[la] offset:28672\n\t" \
    "v_mul_f32 v209, v197, v207\n\t" \
    "v_mul_f32 v210, v198, v207\n\t" \
    "v_mfma_f32_16x16x32_bf16 v[192:195], v[132:135], v[184:187], v[192:195]\n\t" \
    "v_mul_f32 v211, v199, v207\n\t" \
    "v_mul_f32 v212, v200, v207\n\t" \
    "s_waitcnt lgkmcnt(5)\n\t" \
    "v_mfma_f32_16x16x32_bf16 v[188:191], v[56:59], v[160:163], v[188:191]\n\t" \
    "v_mul_f32 v213, v201, v207\n\t" \
    "v_mul_f32 v214, v202, v207\n\t" \
    "v_mfma_f32_16x16x32_bf16 v[192:195], v[136:139], v[160:163], v[192:195]\n\t" \
    "v_mul_f32 v215, v203, v207\n\t" \
    "v_mov_b64 v[216:217], -1\n\t" \
    "s_waitcnt lgkmcnt(4)\n\t" \
    "v_mfma_f32_16x16x32_bf16 v[188:191], v[60:63], v[164:167], v[188:191]\n\t" \
    "v_mov_b64 v[218:219], -1\n\t" \
    "buffer_store_dwordx4 v[216:219], %[l16], %[rs], %[sa] offen sc1\n\t" \
    "v_mfma_f32_16x16x32_bf16 v[192:195], v[140:143], v[164:167], v[192:195]\n\t" \
    "s_waitcnt lgkmcnt(3)\n\t" \
    "v_mfma_f32_16x16x32_bf16 v[188:191], v[64:67], v[168:171], v[188:191]\n\t" \
    "v_mfma_f32_16x16x32_bf16 v[192:195], v[144:147], v[168:171], v[192:195]\n\t" \
    "s_waitcnt lgkmcnt(2)\n\t" \
    "v_mfma_f32_16x16x32_bf16 v[188:191], v[68:71], v[172:175], v[188:191]\n\t" \
    "v_mfma_f32_16x16x32_bf16 v[192:195], v[148:151], v[172:175], v[192:195]\n\t" \
    "s_waitcnt lgkmcnt(1)\n\t" \
    "v_mfma_f32_16x16x32_bf16 v[188:191], v[72:75], v[176:179], v[188:191]\n\t" \
    "v_mfma_f32_16x16x32_bf16 v[192:195], v[152:155], v[176:179], v[192:195]\n\t" \
    "s_waitcnt lgkmcnt(0)\n\t" \
    "v_mfma_f32_16x16x32_bf16 v[188:191], v[76:79], v[180:183], v[188:191]\n\t" \
    "v_mfma_f32_16x16x32_bf16 v[192:195], v[156:159], v[180:183], v[192:195]\n\t" \
    "s_setprio 2\n\t" \
    "buffer_load_dwordx4 v[232:235], %[l16], %[rs], %[n0] offen offset:0 sc1\n\t" \
    "buffer_load_dwordx4 v[236:239], %[l16], %[rs], %[n0] offen offset:1024 sc1\n\t" \
    "buffer_load_dwordx4 v[240:243], %[l16], %[rs], %[n0] offen offset:2048 sc1\n\t" \
    "buffer_load_dwordx4 v[244:247], %[l16], %[rs], %[n0] offen offset:3072 sc1\n\t" \
    "buffer_load_dwordx4 v[248:251], %[l16], %[rs], %[n1] offen offset:0 sc1\n\t" \
    ASM_ST3 \
    "v_fma_f32 v188, v188, v206, v208\n\t" \
    "v_fma_f32 v189, v189, v206, v209\n\t" \
    "v_fma_f32 v190, v190, v206, v210\n\t" \
    "v_fma_f32 v191, v191, v206, v211\n\t" \
    "v_mul_f32 v160, v188, v188\n\t" \
    "v_mul_f32 v161, v189, v189\n\t" \
    "v_mul_f32 v162, v190, v190\n\t" \
    "v_mul_f32 v163, v191, v191\n\t" \
    "v_fma_f32 v160, v160, %[k1], v220\n\t" \
    "v_fma_f32 v161, v161, %[k1], v220\n\t" \
    "v_fma_f32 v162, v162, %[k1], v220\n\t" \
    "v_fma_f32 v163, v163, %[k1], v220\n\t" \
    "v_mul_f32 v160, v188, v160\n\t" \
    "v_mul_f32 v161, v189, v161\n\t" \
    "v_mul_f32 v162, v190, v162\n\t" \
    "v_mul_f32 v163, v191, v163\n\t" \
    "v_fma_f32 v192, v192, v206, v212\n\t" \
    "v_fma_f32 v193, v193, v206, v213\n\t" \
    "v_fma_f32 v194, v194, v206, v214\n\t" \
    "v_fma_f32 v195, v195, v206, v215\n\t" \
    "v_mul_f32 v164, v192, v192\n\t" \
    "v_mul_f32 v165, v193, v193\n\t" \
    "v_mul_f32 v166, v194, v194\n\t" \
    "v_mul_f32 v167, v195, v195\n\t" \
    "v_fma_f32 v164, v164, %[k1], v220\n\t" \
    "v_fma_f32 v165, v165, %[k1], v220\n\t" \
    "v_fma_f32 v166, v166, %[k1], v220\n\t" \
    "v_fma_f32 v167, v167, %[k1], v220\n\t" \
    "v_mul_f32 v164, v192, v164\n\t" \
    "v_mul_f32 v165, v193, v165\n\t" \
    "v_mul_f32 v166, v194, v166\n\t" \
    "v_mul_f32 v167, v195, v167\n\t" \
    "v_exp_f32 v160, v160\n\t" \
    "v_exp_f32 v161, v161\n\t" \
    "v_exp_f32 v162, v162\n\t" \
    "v_exp_f32 v163, v163\n\t" \
    "v_exp_f32 v164, v164\n\t" \
    "v_exp_f32 v165, v165\n\t" \
    "v_exp_f32 v166, v166\n\t" \
    "v_exp_f32 v167, v167\n\t" \
    "v_add_f32 v160, 1.0, v160\n\t" \
    "v_add_f32 v161, 1.0, v161\n\t" \
    "v_add_f32 v162, 1.0, v162\n\t" \
    "v_add_f32 v163, 1.0, v163\n\t" \
    "v_add_f32 v164, 1.0, v164\n\t" \
    "v_add_f32 v165, 1.0, v165\n\t" \
    "v_add_f32 v166, 1.0, v166\n\t" \
    "v_add_f32 v167, 1.0, v167\n\t" \
    "v_rcp_f32 v160, v160\n\t" \
    "v_rcp_f32 v161, v161\n\t" \
    "v_rcp_f32 v162, v162\n\t" \
    "v_rcp_f32 v163, v163\n\t" \
    "v_rcp_f32 v164, v164\n\t" \
    "v_rcp_f32 v165, v165\n\t" \
    "v_rcp_f32 v166, v166\n\t" \
    "v_rcp_f32 v167, v167\n\t" \
    "v_mul_f32 v188, v188, v160\n\t" \
    "v_mul_f32 v189, v189, v161\n\t" \
    "v_mul_f32 v190, v190, v162\n\t" \
    "v_mul_f32 v191, v191, v163\n\t" \
    "v_mul_f32 v192, v192, v164\n\t" \
    "v_mul_f32 v193, v193, v165\n\t" \
    "v_mul_f32 v194, v194, v166\n\t" \
    "v_mul_f32 v195, v195, v167\n\t" \
    "v_cvt_pk_bf16_f32 v188, v188, v189\n\t" \
    "v_cvt_pk_bf16_f32 v189, v190, v191\n\t" \
    "v_cvt_pk_bf16_f32 v190, v192, v193\n\t" \
    "v_cvt_pk_bf16_f32 v191, v194, v195\n\t" \
    "v_min_u32 v188, -2, v188\n\t" \
    "v_min_u32 v189, -2, v189\n\t" \
    "v_min_u32 v190, -2, v190\n\t" \
    "v_min_u32 v191, -2, v191\n\t" \
    "v_mov_b32 v216, 0x3f803f80\n\t" \
    "v_mov_b32 v217, 0x3f803f80\n\t" \
    "v_mov_b32 v218, 0x3f803f80\n\t" \
    "v_mov_b32 v219, 0x3f803f80\n\t" \
    "v_permlane16_swap_b32 v188, v190\n\t" \
    "v_permlane16_swap_b32 v189, v191\n\t" \
    "s_waitcnt vmcnt(6)\n\t" \
    "buffer_store_dwordx4 v[188:191], %[oa], %[rs], %[so] offen sc1\n\t" \
    "s_nop 1\n\t" \
    "s_setprio 0\n\t"
#define ASM_WT3_HI \
    "s_setprio 1\n\t" \
    "s_barrier\n\t" ASM_ST \
    "ds_read_b128 v[160:163], %[la] offset:0\n\t" \
    "ds_read_b128 v[164:167], %[la] offset:1024\n\t" \
    "ds_read_b128 v[168:171], %[la] offset:2048\n\t" \
    "ds_read2st64_b32 v[204:205], %[sr] offset0:20 offset1:24\n\t" \
    "ds_read_b128 v[172:175], %[la] offset:3072\n\t" \
    "ds_read_b128 v[176:179], %[la] offset:8192\n\t" \
    "ds_read_b128 v[180:183], %[la] offset:9216\n\t" \
    "s_waitcnt lgkmcnt(6)\n\t" \
    "v_mfma_f32_16x16x32_bf16 v[188:191], v[0:3], v[160:163], 0\n\t" \
    "ds_read_b128 v[184:187], %[la] offset:10240\n\t" \
    "v_mfma_f32_16x16x32_bf16 v[192:195], v[80:83], v[160:163], 0\n\t" \
    "s_waitcnt lgkmcnt(6)\n\t" \
    "v_mfma_f32_16x16x32_bf16 v[188:191], v[4:7], v[164:167], v[188:191]\n\t" \
    "ds_read_b128 v[160:163], %[la] offset:11264\n\t" \
    "v_mfma_f32_16x16x32_bf16 v[192:195], v[84:87], v[164:167], v[192:195]\n\t" \
    "s_waitcnt lgkmcnt(6)\n\t" \
    "v_mfma_f32_16x16x32_bf16 v[188:191], v[8:11], v[168:171], v[188:191]\n\t" \
    "ds_read_b128 v[164:167], %[la] offset:16384\n\t" \
    "s_waitcnt lgkmcnt(6)\n\t" \
    "v_mfma_f32_16x16x32_bf16 v[192:195], v[88:91], v[168:171], v[192:195]\n\t" \
    "v_permlane16_swap_b32 v204, v205\n\t" \
    "s_waitcnt lgkmcnt(5)\n\t" \
    "v_mfma_f32_16x16x32_bf16 v[188:191], v[12:15], v[172:175], v[188:191]\n\t" \
    "ds_read_b128 v[168:171], %[la] offset:17408\n\t" \
    "v_mfma_f32_16x16x32_bf16 v[192:195], v[92:95], v[172:175], v[192:195]\n\t" \
    "v_add_f32 v206, v204, v205\n\t" \
    "s_waitcnt lgkmcnt(5)\n\t" \
    "v_mfma_f32_16x16x32_bf16 v[188:191], v[16:19], v[176:179], v[188:191]\n\t" \
    "ds_read_b128 v[172:175], %[la] offset:18432\n\t" \
    "v_add_f32 v207, v204, v205\n\t" \
    "v_mfma_f32_16x16x32_bf16 v[192:195], v[96:99], v[176:179], v[192:195]\n\t" \
    "s_waitcnt lgkmcnt(5)\n\t" \
    "v_mfma_f32_16x16x32_bf16 v[188:191], v[20:23], v[180:183], v[188:191]\n\t" \
    "ds_read_b128 v[176:179], %[la] offset:19456\n\t" \
    "v_permlane32_swap_b32 v206, v207\n\t" \
    "v_mfma_f32_16x16x32_bf16 v[192:195], v[100:103], v[180:183], v[192:195]\n\t" \
    "s_waitcnt lgkmcnt(5)\n\t" \
    "v_mfma_f32_16x16x32_bf16 v[188:191], v[24:27], v[184:187], v[188:191]\n\t" \
    "ds_read_b128 v[180:183], %[la] offset:24576\n\t" \
    "v_add_f32 v204, v206, v207\n\t" \
    "v_mfma_f32_16x16x32_bf16 v[192:195], v[104:107], v[184:187], v[192:195]\n\t" \
    "v_add_f32 v205, v206, v207\n\t" \
    "s_waitcnt lgkmcnt(5)\n\t" \
    "v_mfma_f32_16x16x32_bf16 v[188:191], v[28:31], v[160:163], v[188:191]\n\t" \
    "ds_read_b128 v[184:187], %[la] offset:25600\n\t" \
    "v_mfma_f32_16x16x32_bf16 v[192:195], v[108:111], v[160:163], v[192:195]\n\t" \
    "v_permlane16_swap_b32 v204, v205\n\t" \
    "s_waitcnt lgkmcnt(5)\n\t" \
    "v_mfma_f32_16x16x32_bf16 v[188:191], v[32:35], v[164:167], v[188:191]\n\t" \
    "ds_read_b128 v[160:163], %[la] offset:26624\n\t" \
    "v_mfma_f32_16x16x32_bf16 v[192:195], v[112:115], v[164:167], v[192:195]\n\t" \
    "v_mul_f32 v205, %[kd], v205\n\t" \
    "s_waitcnt lgkmcnt(5)\n\t" \
    "v_mfma_f32_16x16x32_bf16 v[188:191], v[36:39], v[168:171], v[188:191]\n\t" \
    "ds_read_b128 v[164:167], %[la] offset:27648\n\t" \
    "v_mul_f32 v204, %[kd], v204\n\t" \
    "v_mfma_f32_16x16x32_bf16 v[192:195], v[116:119], v[168:171], v[192:195]\n\t" \
    "v_fma_f32 v204, -v205, v205, v204\n\t" \
    "s_waitcnt lgkmcnt(5)\n\t" \
    "v_mfma_f32_16x16x32_bf16 v[188:191], v[40:43], v[172:175], v[188:191]\n\t" \
    "ds_read_b128 v[168:171], %[la] offset:4096\n\t" \
    "v_max_f32 v204, 0, v204\n\t" \
    "v_mfma_f32_16x16x32_bf16 v[192:195], v[120:123], v[172:175], v[192:195]\n\t" \
    "v_add_f32 v206, %[ke], v204\n\t" \
    "s_waitcnt lgkmcnt(5)\n\t" \
    "v_mfma_f32_16x16x32_bf16 v[188:191], v[44:47], v[176:179], v[188:191]\n\t" \
    "ds_read_b128 v[172:175], %[la] offset:12288\n\t" \
    "v_rsq_f32 v206, v206\n\t" \
    "v_mfma_f32_16x16x32_bf16 v[192:195], v[124:127], v[176:179], v[192:195]\n\t" \
    "s_waitcnt lgkmcnt(5)\n\t" \
    "v_mfma_f32_16x16x32_bf16 v[188:191], v[48:51], v[180:183], v[188:191]\n\t" \
    "ds_read_b128 v[176:179], %[la] offset:20480\n\t" \
    "v_mul_f32 v207, -v205, v206\n\t" \
    "v_mfma_f32_16x16x32_bf16 v[192:195], v[128:131], v[180:183], v[192:195]\n\t" \
    "v_mul_f32 v208, v196, v207\n\t" \
    "s_waitcnt lgkmcnt(5)\n\t" \
    "v_mfma_f32_16x16x32_bf16 v[188:191], v[52:55], v[184:187], v[188:191]\n\t" \
    "ds_read_b128 v[180:183], %[la] offset:28672\n\t" \
    "v_mul_f32 v209, v197, v207\n\t" \
    "v_mul_f32 v210, v198, v207\n\t" \
    "v_mfma_f32_16x16x32_bf16 v[192:195], v[132:135], v[184:187], v[192:195]\n\t" \
    "v_mul_f32 v211, v199, v207\n\t" \
    "v_mul_f32 v212, v200, v207\n\t" \
    "s_waitcnt lgkmcnt(5)\n\t" \
    "v_mfma_f32_16x16x32_bf16 v[188:191], v[56:59], v[160:163], v[188:191]\n\t" \
    "v_mul_f32 v213, v201, v207\n\t" \
    "v_mul_f32 v214, v202, v207\n\t" \
    "v_mfma_f32_16x16x32_bf16 v[192:195], v[136:139], v[160:163], v[192:195]\n\t" \
    "v_mul_f32 v215, v203, v207\n\t" \
    "v_mov_b64 v[216:217], -1\n\t" \
    "s_waitcnt lgkmcnt(4)\n\t" \
    "v_mfma_f32_16x16x32_bf16 v[188:191], v[60:63], v[164:167], v[188:191]\n\t" \
    "v_mov_b64 v[218:219], -1\n\t" \
    "buffer_store_dwordx4 v[216:219], %[l16], %[rs], %[sa] offen sc1\n\t" \
    "v_mfma_f32_16x16x32_bf16 v[192:195], v[140:143], v[164:167], v[192:195]\n\t" \
    "s_waitcnt lgkmcnt(3)\n\t" \
    "v_mfma_f32_16x16x32_bf16 v[188:191], v[64:67], v[168:171], v[188:191]\n\t" \
    "v_mfma_f32_16x16x32_bf16 v[192:195], v[144:147], v[168:171], v[192:195]\n\t" \
    "s_waitcnt lgkmcnt(2)\n\t" \
    "v_mfma_f32_16x16x32_bf16 v[188:191], v[68:71], v[172:175], v[188:191]\n\t" \
    "v_mfma_f32_16x16x32_bf16 v[192:195], v[148:151], v[172:175], v[192:195]\n\t" \
    "s_waitcnt lgkmcnt(1)\n\t" \
    "v_mfma_f32_16x16x32_bf16 v[188:191], v[72:75], v[176:179], v[188:191]\n\t" \
    "v_mfma_f32_16x16x32_bf16 v[192:195], v[152:155], v[176:179], v[192:195]\n\t" \
    "s_waitcnt lgkmcnt(0)\n\t" \
    "v_mfma_f32_16x16x32_bf16 v[188:191], v[76:79], v[180:183], v[188:191]\n\t" \
    "v_mfma_f32_16x16x32_bf16 v[192:195], v[156:159], v[180:183], v[192:195]\n\t" \
    ASM_ST3 \
    "s_nop 4\n\t" \
    "v_fma_f32 v188, v188, v206, v208\n\t" \
    "v_fma_f32 v189, v189, v206, v209\n\t" \
    "v_fma_f32 v190, v190, v206, v210\n\t" \
    "v_fma_f32 v191, v191, v206, v211\n\t" \
    "v_mul_f32 v160, v188, v188\n\t" \
    "v_mul_f32 v161, v189, v189\n\t" \
    "v_mul_f32 v162, v190, v190\n\t" \
    "v_mul_f32 v163, v191, v191\n\t" \
    "v_fma_f32 v160, v160, %[k1], v220\n\t" \
    "v_fma_f32 v161, v161, %[k1], v220\n\t" \
    "v_fma_f32 v162, v162, %[k1], v220\n\t" \
    "v_fma_f32 v163, v163, %[k1], v220\n\t" \
    "v_mul_f32 v160, v188, v160\n\t" \
    "v_mul_f32 v161, v189, v161\n\t" \
    "v_mul_f32 v162, v190, v162\n\t" \
    "v_mul_f32 v163, v191, v163\n\t" \
    "v_fma_f32 v192, v192, v206, v212\n\t" \
    "v_fma_f32 v193, v193, v206, v213\n\t" \
    "v_fma_f32 v194, v194, v206, v214\n\t" \
    "v_fma_f32 v195, v195, v206, v215\n\t" \
    "v_mul_f32 v164, v192, v192\n\t" \
    "v_mul_f32 v165, v193, v193\n\t" \
    "v_mul_f32 v166, v194, v194\n\t" \
    "v_mul_f32 v167, v195, v195\n\t" \
    "v_fma_f32 v164, v164, %[k1], v220\n\t" \
    "v_fma_f32 v165, v165, %[k1], v220\n\t" \
    "v_fma_f32 v166, v166, %[k1], v220\n\t" \
    "v_fma_f32 v167, v167, %[k1], v220\n\t" \
    "v_mul_f32 v164, v192, v164\n\t" \
    "v_mul_f32 v165, v193, v165\n\t" \
    "v_mul_f32 v166, v194, v166\n\t" \
    "v_mul_f32 v167, v195, v167\n\t" \
    "v_exp_f32 v160, v160\n\t" \
    "v_exp_f32 v161, v161\n\t" \
    "v_exp_f32 v162, v162\n\t" \
    "v_exp_f32 v163, v163\n\t" \
    "v_exp_f32 v164, v164\n\t" \
    "v_exp_f32 v165, v165\n\t" \
    "v_exp_f32 v166, v166\n\t" \
    "v_exp_f32 v167, v167\n\t" \
    "v_add_f32 v160, 1.0, v160\n\t" \
    "v_add_f32 v161, 1.0, v161\n\t" \
    "v_add_f32 v162, 1.0, v162\n\t" \
    "v_add_f32 v163, 1.0, v163\n\t" \
    "v_add_f32 v164, 1.0, v164\n\t" \
    "v_add_f32 v165, 1.0, v165\n\t" \
    "v_add_f32 v166, 1.0, v166\n\t" \
    "v_add_f32 v167, 1.0, v167\n\t" \
    "v_rcp_f32 v160, v160\n\t" \
    "v_rcp_f32 v161, v161\n\t" \
    "v_rcp_f32 v162, v162\n\t" \
    "v_rcp_f32 v163, v163\n\t" \
    "v_rcp_f32 v164, v164\n\t" \
    "v_rcp_f32 v165, v165\n\t" \
    "v_rcp_f32 v166, v166\n\t" \
    "v_rcp_f32 v167, v167\n\t" \
    "v_mul_f32 v188, v188, v160\n\t" \
    "v_mul_f32 v189, v189, v161\n\t" \
    "v_mul_f32 v190, v190, v162\n\t" \
    "v_mul_f32 v191, v191, v163\n\t" \
    "v_mul_f32 v192, v192, v164\n\t" \
    "v_mul_f32 v193, v193, v165\n\t" \
    "v_mul_f32 v194, v194, v166\n\t" \
    "v_mul_f32 v195, v195, v167\n\t" \
    "v_cvt_pk_bf16_f32 v188, v188, v189\n\t" \
    "v_cvt_pk_bf16_f32 v189, v190, v191\n\t" \
    "v_cvt_pk_bf16_f32 v190, v192, v193\n\t" \
    "v_cvt_pk_bf16_f32 v191, v194, v195\n\t" \
    "v_min_u32 v188, -2, v188\n\t" \
    "v_min_u32 v189, -2, v189\n\t" \
    "v_min_u32 v190, -2, v190\n\t" \
    "v_min_u32 v191, -2, v191\n\t" \
    "s_nop 1\n\t" \
    "v_permlane16_swap_b32 v188, v190\n\t" \
    "v_permlane16_swap_b32 v189, v191\n\t" \
    "s_waitcnt vmcnt(1)\n\t" \
    "buffer_store_dwordx4 v[188:191], %[oa], %[rs], %[so] offen sc1\n\t" \
    "s_nop 1\n\t"
#define ASM_POLL5_I \
    "buffer_load_dwordx4 v[232:235], %[l16], %[rs], %[s0] offen offset:0 sc1\n\t" \
    "buffer_load_dwordx4 v[236:239], %[l16], %[rs], %[s0] offen offset:1024 sc1\n\t" \
    "buffer_load_dwordx4 v[240:243], %[l16], %[rs], %[s0] offen offset:2048 sc1\n\t" \
    "buffer_load_dwordx4 v[244:247], %[l16], %[rs], %[s0] offen offset:3072 sc1\n\t" \
    "buffer_load_dwordx4 v[248:251], %[l16], %[rs], %[s1] offen offset:0 sc1\n\t"
using u32x8 = __attribute__((ext_vector_type(8))) unsigned;
using u32x16 = __attribute__((ext_vector_type(16))) unsigned;
using u32x32 = __attribute__((ext_vector_type(32))) unsigned;
struct WRegs { u32x32 w[6]; u32x8 t; };
#define WREGS(W) "{v[0:31]}"(W.w[0]), "{v[32:63]}"(W.w[1]), "{v[64:95]}"(W.w[2]), "{v[96:127]}"(W.w[3]), "{v[128:159]}"(W.w[4]), "{v[160:191]}"(W.w[5]), "{v[192:199]}"(W.t)
__device__ __forceinline__ u32x32 cat32(u32x8 a, u32x8 b, u32x8 c, u32x8 d) {
    const u32x16 lo = __builtin_shufflevector(a, b, 0, 1, 2, 3, 4, 5, 6, 7, 8, 9, 10, 11, 12, 13, 14, 15);
    const u32x16 hi = __builtin_shufflevector(c, d, 0, 1, 2, 3, 4, 5, 6, 7, 8, 9, 10, 11, 12, 13, 14, 15);
    return __builtin_shufflevector(lo, hi, 0, 1, 2, 3, 4, 5, 6, 7, 8, 9, 10, 11, 12, 13, 14, 15, 16, 17, 18, 19, 20, 21, 22, 23, 24, 25, 26, 27, 28, 29, 30, 31);
}
#define WOUT(W) "=&{v[0:31]}"(W.w[0]), "=&{v[32:63]}"(W.w[1]), "=&{v[64:95]}"(W.w[2]), "=&{v[96:127]}"(W.w[3]), "=&{v[128:159]}"(W.w[4]), "=&{v[160:191]}"(W.w[5]), "=&{v[192:199]}"(W.t)
#define WTMP "memory", "scc", "v200", "v201", "v202", "v203", "v204", "v205", "v206", "v207", "v208", "v209", "v210", "v211", "v212", "v213", "v214", "v215", "v216", "v217", "v218", "v219", "v220", "v221", "v222", "v223", "v224", "v225", "v226", "v227", "v228", "v229", "v230", "v231", "v232", "v233", "v234", "v235", "v236", "v237", "v238", "v239"
__device__ __forceinline__ void load_w3(WRegs& W, const bf16_t* m, int wave, unsigned c0, bool hq) {
    const int ln = lane_id();
    unsigned o[3], s[3], t;
#pragma unroll
    for (int k = 0; k < 3; ++k) {
        const int row = (k * 64 + ln) / 24, sg = (k * 64 + ln) % 24;
        o[k] = row * (HD * 2) + c0 + (hq ? ((sg >> 1) & 1) * 384 + ((0x140 >> ((sg >> 3) * 4)) & 15) * 32 + (sg & 5) * 16 : sg * 16); s[k] = wave * 13824 + row * 416 + sg * 16;
    }
    const unsigned tr = (ln >> 4) * 3456 + ((ln >> 2) & 3) * 416 + (wave & 3) * 32 + (ln & 3) * 8;
    asm volatile(ASM_WLOAD3 : WOUT(W), [t] "=&s"(t) : [rs] "s"(mkrsrc(m, DM * HD * 2)), [wx] "s"(wave), [o0] "v"(o[0]), [o1] "v"(o[1]), [o2] "v"(o[2]), [s0] "v"(s[0]), [s1] "v"(s[1]), [s2] "v"(s[2]),
                 [t0] "v"(tr + wave * 13824), [t1] "v"(tr + (wave ^ 1) * 13824), [t2] "v"(tr + (wave ^ 2) * 13824), [t3] "v"(tr + (wave ^ 3) * 13824), [t4] "v"(tr + (wave & 4) * 13824),
                 [wl] "v"(L_W + wave * 10240 + ln * 16) : WTMP);
}
__device__ __forceinline__ void load_wo(WRegs& W, const bf16_t* m, int wave, int h) {
    const int ln = lane_id();
    unsigned o[5], s[5], t;
#pragma unroll
    for (int k = 0; k < 5; ++k) {
        const int row = (k * 64 + ln) / 20, sg = (k * 64 + ln) % 20;
        o[k] = (h * DH + row) * (DM * 2) + sg * 16 + (wave & 3) * 320; s[k] = wave * 13824 + row * 352 + sg * 16;
    }
    asm volatile(ASM_WLOADO : WOUT(W), [t] "=&s"(t) : [rs] "s"(mkrsrc(m, DM * HD * 2)), [o0] "v"(o[0]), [o1] "v"(o[1]), [o2] "v"(o[2]), [o3] "v"(o[3]), [o4] "v"(o[4]),
                 [s0] "v"(s[0]), [s1] "v"(s[1]), [s2] "v"(s[2]), [s3] "v"(s[3]), [s4] "v"(s[4]), [tr] "v"(wave * 13824 + (ln >> 2) * 352 + (ln & 3) * 8), [wl] "v"(L_W + wave * 10240 + ln * 16) : WTMP);
}
using s16x4 = __attribute__((ext_vector_type(4))) short;
template <int NT, int KS, int N>
__device__ __forceinline__ void load_wn(u32x4 (&f)[NT * KS], const bf16_t* m, int rows, int wave, int c0, int k0) {
    const int ln = lane_id();
    const rsrc_t rs = mkrsrc(m, rows * N * 2);
#pragma unroll
    for (int k = 0; k < KS; ++k)
#pragma unroll
        for (int n = 0; n < NT; ++n) f[n * KS + k] = __builtin_amdgcn_raw_buffer_load_b128(rs, ((n * 64 + ln) / (2 * NT)) * (N * 2) + ((n * 64 + ln) % (2 * NT)) * 16, (k0 + k) * (N * 64) + c0 * 32, 0);
    const unsigned st = wave * 10240 + ln * 16, tr = wave * 10240 + ((ln >> 4) * 8 + ((ln >> 2) & 3)) * (NT * 32) + (ln & 3) * 8;
#pragma unroll
    for (int k = 0; k < KS; ++k) {
#pragma unroll
        for (int n = 0; n < NT; ++n) lds_st<u32x4>(st + n * 1024, f[n * KS + k]);
        asm volatile("" ::: "memory");
#pragma unroll
        for (int n = 0; n < NT; ++n) {
            const u32x2 a = __builtin_bit_cast(u32x2, __builtin_amdgcn_ds_read_tr16_b64_v4i16((LAS s16x4*)(tr + n * 32)));
            const u32x2 b = __builtin_bit_cast(u32x2, __builtin_amdgcn_ds_read_tr16_b64_v4i16((LAS s16x4*)(tr + n * 32 + NT * 128)));
            f[n * KS + k] = (u32x4){a[0], a[1], b[0], b[1]};
        }
        asm volatile("" ::: "memory");
    }
}
struct C1Regs { u32x8 lw0, lw1, z1; u32x4 z0; };
#define XG_OUT "=&{v[200:207]}"(C.lw0), "=&{v[224:227]}"(C.z0), "=&{v[228:235]}"(C.lw1), "=&{v[244:251]}"(C.z1)
#define XG_IN WREGS(W), [xa] "v"(xa), [wl] "v"(wl), [qs] "s"(qs)
#define XG_CLOB "memory", "scc", "v208", "v209", "v210", "v211", "v212", "v213", "v214", "v215", "v216", "v217", "v218", "v219", "v220", "v221", "v222", "v223", "v236", "v237", "v238", "v239", "v240", "v241", "v242", "v243", "v252"
constexpr int XS0 = 8;
__device__ __forceinline__ void init_wait(const Params& P, int c, int ph) {
    for (int spins = 0;; ++spins) {
        unsigned o;
        asm volatile("v_mov_b32 %0, 0" : "=v"(o));
        if (__builtin_amdgcn_readfirstlane(__builtin_amdgcn_raw_buffer_load_b32(mkrsrc(P.ws + FL0, FL_BYTES), o, FL_IN + (ph * 8 + (c & 7)) * 128, 16)) >= 128u) break;
        __builtin_amdgcn_s_sleep(8);
        if (spin_fail(spins, P)) break;
    }
    asm volatile("" ::: "memory");
}
__device__ __forceinline__ void xn_wait(const Params& P, rsrc_t rr, int seg, int cpy) {
    for (int spins = 0;; ++spins) {
        const unsigned f = __builtin_amdgcn_raw_buffer_load_b32(rr, (lane_id() & 15) * 128, RG_FX + (cpy & 3) * 2048, 16);
        if (__all(f > (unsigned)seg)) break;
        __builtin_amdgcn_s_sleep(4);
        if (spin_fail(spins, P)) break;
    }
    asm volatile("" ::: "memory");
}
__device__ __forceinline__ unsigned c1_dst(int wave, bool ktype) {
    const int ln = lane_id(), l15 = ln & 15, hf = (wave >> 1) & 1;
    const int rb16 = ((ln >> 2) & 4) * 16 + (ln >> 5) * 32;
    return (ktype ? L_KM + (l15 >> 2) * 128 + hf * 8 + (l15 & 3) * 2
                  : L_Q + (l15 >> 3) * 256 + ((l15 >> 1) & 1) * 128 + ((l15 >> 2) & 1) * 8 + hf * 4 + (l15 & 1) * 2) + (wave & 1) * 1536 + rb16;
}
template <int NP>
__device__ __forceinline__ void crit1(const WRegs& W, C1Regs& C, rsrc_t rr, unsigned sbase, int wave, unsigned wl, unsigned dsto, unsigned& budget, unsigned& fv, unsigned fs
) {
    unsigned abase;
    asm volatile("v_and_b32 %0, 0x3ff, %1\n\tv_or_b32 %0, %2, %0" : "=&v"(abase) : "v"(wl), "s"(wave * 1024));
    ASM_ST_DECL;
#define C1_OUT "+{v[224:227]}"(C.z0), "+{v[244:251]}"(C.z1), "+{v[200:207]}"(C.lw0), "+{v[228:235]}"(C.lw1)
#define C1_IN WREGS(W), [ab] "v"(abase), [wl] "v"(wl), [dst] "v"(dsto)
#define C1_CLOB "memory", "vcc", "scc", "v208", "v209", "v210", "v211", "v212", "v213", "v214", "v215", "v216", "v217", "v218", "v219", "v220", "v221", "v222", "v223", "v236", "v237", "v238", "v239", "v240", "v241", "v242", "v243"
#define C1_POLL [rs] "s"(rr), [s0] "s"(sbase), [s1] "s"(sbase + 4096), [s2] "s"(sbase + 2 * BLK)
    ST(1);
    if constexpr (NP == 13) asm volatile(ASM_CRIT1_V3 "s_waitcnt vmcnt(0)" : C1_OUT, [n] "+s"(budget), [fv] "+v"(fv) ASM_ST_OUT ASM_ST2_OUT : C1_IN, C1_POLL, [fs] "s"(fs) : C1_CLOB);
    else if constexpr (NP == 12) asm volatile(ASM_CRIT1_V2 "s_waitcnt vmcnt(0)" : C1_OUT, [n] "+s"(budget), [fv] "+v"(fv) ASM_ST_OUT ASM_ST2_OUT : C1_IN, C1_POLL, [fs] "s"(fs) : C1_CLOB);
    else if constexpr (NP == 3) asm volatile(ASM_CRIT1_3 : C1_OUT, [n] "+s"(budget) ASM_ST_OUT ASM_ST2_OUT : C1_IN, C1_POLL : C1_CLOB);
    else if constexpr (NP == 2) asm volatile(ASM_CRIT1_2 : C1_OUT, [n] "+s"(budget) ASM_ST_OUT ASM_ST2_OUT : C1_IN, C1_POLL : C1_CLOB);
    else asm volatile(ASM_CRIT1_N : C1_OUT ASM_ST_OUT : C1_IN : C1_CLOB);
    SB();
    ASM_ST_SAVE(3);
    if constexpr (NP != 0) { ASM_ST2_SAVE(2); }
}
#define STARG

#define SRG 5
#undef STROLE
#define STROLE 1
__device__ __forceinline__ void s_role(const Params& P, int wave, int L, int par, int h, int cpy) {
    const rsrc_t rr = mkrsrc(P.ws + par * 8192, RG_L);
    const char* xn = P.ws + PG0 + PG_XN + L * PG_LX;
    auto xdma = [&](int seg, int hb) {
        const unsigned l16 = lane_id() * 16;
        const int mt = (wave >> 1) & 1;
        const char* g = xn + pgb(seg, par);
        if (hb == 0) {
            dma4(g + blin(mt * 20480 + (wave & 1) * 4096), l16, L_XA + mt * 16384 + (wave & 1) * 4096);
            dmaq<0>(g + blin(mt * 20480 + 16384) + (wave & 1) * 1024, l16, L_XB + mt * 4096 + (wave & 1) * 1024);
        } else {
            dma4(g + blin(mt * 20480 + 8192), l16, L_XA + mt * 16384 + 8192); dma4(g + blin(mt * 20480 + 12288), l16, L_XA + mt * 16384 + 12288);
            dmaq<2>(g + blin(mt * 20480 + 16384), l16, L_XB + mt * 4096); dmaq<3>(g + blin(mt * 20480 + 16384), l16, L_XB + mt * 4096);
        }
    };
    const float SCL = 0.07216878364870322f * 1.4426950408889634f;
    auto run = [&](auto Cc) {
    constexpr int CLS = decltype(Cc)::value;
    WRegs wf;
    MARK(!L && h == 0 && wave == 0, 0);
    load_w3(wf, P.w[CLS == 3], wave, h * 384, false);
    MARKX(!L && wave == 0, 1);
    init_wait(P, wave, L);
    if (CLS == 3) {
        if (L) xn_wait(P, rr, 0, cpy);
        xdma(0, 0);
        if (wave == 4 || wave == 7) xdma(0, 1);
        wait_vm<0>();
    }
    bar();
    if (CLS == 3 && wave == 7 && L) xn_wait(P, rr, 1, cpy);
    const unsigned qs = (CLS == 3 ? L_KX : L_QX) + (wave & 1) * 3072 + ((wave >> 1) & 1) * 8;
    unsigned xa, wl, dsto;
    if (CLS == 3) { xa = L_XA + wave * 2048 + lane_id() * 16; wl = L_W + wave * 10240 + lane_id() * 16; dsto = c1_dst(wave, true); }
    __builtin_amdgcn_s_setprio(CLS == 3 ? 1 : 2);
    unsigned budget = 1u << 18;
    STDECL;
    LDECL;
#pragma unroll 1
    for (int t = 0; t < NSEG; ++t) {
        const unsigned slot = t & (NSLOT - 1);
        C1Regs C;
        {
            const WRegs& W = wf;
            ASM_ST_DECL;
            if constexpr (CLS == 3) {
                const int mt = (wave >> 1) & 1;
                const char* g = xn + pgb(t + 1 < NSEG ? t + 1 : t, par);
                asm volatile(ASM_XG_S_HI : XG_OUT ASM_ST_OUT : XG_IN, [slp] "s"(SLP4), [g0] "s"(g + blin(mt * 20480 + (wave & 1) * 4096)), [d0] "s"(L_XA + mt * 16384 + (wave & 1) * 4096),
                             [g1] "s"(g + blin(mt * 20480 + 16384) + (wave & 1) * 1024), [d1] "s"(L_XB + mt * 4096 + (wave & 1) * 1024) : XG_CLOB);
                wait_vm<0>();
                LB(t);
            } else {
                const unsigned l16 = lane_id() * 16;
                xa = L_XA + wave * 2048 + l16; wl = L_W + wave * 10240 + l16;
#define XG_Q(A) asm volatile("s_cmp_lt_u32 %[t], 2\n\ts_cbranch_scc1 .Lq3%=\n\t" A "s_branch .Lqe%=\n\t.Lq3%=:\n\t" ASM_XG_S_LO ".Lqe%=:" : XG_OUT ASM_ST_OUT : XG_IN, [t] "s"(t) : XG_CLOB)
                if constexpr (CLS == 0 || CLS == 4) XG_Q(ASM_XG_S_LO1); else XG_Q(ASM_XG_S_LO2);
                __builtin_amdgcn_s_setprio(0);
                if constexpr (CLS == 0) {
                    const int ln = lane_id();
                    const u32x4 s4 = sent4();
                    const unsigned poff = (ln * 16 + ((ln < 8) ? h * 128 : (1 + (h >> 4)) * 2 * BLK - 128 + (h & 15) * 512)) | ((ln >= 40 || t < 2) ? 0x40000000u : 0u);
#pragma unroll
                    for (int c = 0; c < (int)NCP; ++c) st128(s4, rr, poff, RG_XP + ((t - 2) & (NSLOT - 1)) * XP_SLOT + (c >> 1) * XP_PAIR + (c & 1) * BLK);
                    const unsigned pbase = RG_PX + ((t - 3) & (NSLOT - 1)) * PX_SLOT + (h >> 2) * BLK;
                    const unsigned o2 = ((ln & 16) ? 5120 + ((h >> 1) & 1) * 1024 + (h & 1) * 256 + (ln >> 5) * 512 : 4096 + (h & 3) * 256 + (ln >> 5) * 3072) + (ln & 15) * 16;
                    if (t >= 3) { st128(s4, rr, ln * 16, pbase + (h & 3) * 1024); st128(s4, rr, o2, pbase); }
                }
                if (CLS == 4 && t >= HT0) {
                    const int ln = lane_id(), u = h >> 2;
                    for (int spins = 0;; ++spins) {
                        const unsigned fl = __builtin_amdgcn_raw_buffer_load_b32(rr, (ln & 3) * 128, RG_FH + BLK + u * 512, 16);
                        if (__all(fl > (unsigned)t)) break;
                        __builtin_amdgcn_s_sleep(2);
                        if (spin_fail(spins, P)) break;
                    }
                    asm volatile("" ::: "memory");
                    const char* g = P.ws + PG0 + pgb(t, par) + PG_HV + (16 + u) * 3 * BLK;
                    unsigned t0, t1, vo;
                    static_assert(L_QX == 16384, "");
                    asm volatile("s_add_u32 %0, %4, 0\n\ts_and_b32 %1, %0, 0xe000\n\ts_add_u32 %0, %0, %1\n\tv_add_u32 %2, %0, %5\n\ts_mov_b32 m0, 16384\n\ts_nop 0\n\tglobal_load_lds_dwordx4 %2, %3 sc1\n\t"
                                 "s_add_u32 %0, %4, 1024\n\ts_and_b32 %1, %0, 0xe000\n\ts_add_u32 %0, %0, %1\n\tv_add_u32 %2, %0, %5\n\ts_mov_b32 m0, 17408\n\ts_nop 0\n\tglobal_load_lds_dwordx4 %2, %3 sc1\n\t"
                                 "s_add_u32 %0, %4, 2048\n\ts_and_b32 %1, %0, 0xe000\n\ts_add_u32 %0, %0, %1\n\tv_add_u32 %2, %0, %5\n\ts_mov_b32 m0, 19456\n\ts_nop 0\n\tglobal_load_lds_dwordx4 %2, %3 sc1\n\t"
                                 "s_add_u32 %0, %4, 6144\n\ts_and_b32 %1, %0, 0xe000\n\ts_add_u32 %0, %0, %1\n\tv_add_u32 %2, %0, %5\n\ts_mov_b32 m0, 22528\n\ts_nop 0\n\tglobal_load_lds_dwordx4 %2, %3 sc1\n\t"
                                 "s_add_u32 %0, %4, 7168\n\ts_and_b32 %1, %0, 0xe000\n\ts_add_u32 %0, %0, %1\n\tv_add_u32 %2, %0, %5\n\ts_mov_b32 m0, 23552\n\ts_nop 0\n\tglobal_load_lds_dwordx4 %2, %3 sc1\n\t"
                                 "s_add_u32 %0, %4, 8192\n\ts_and_b32 %1, %0, 0xe000\n\ts_add_u32 %0, %0, %1\n\tv_add_u32 %2, %0, %5\n\ts_mov_b32 m0, 25600\n\ts_nop 0\n\tglobal_load_lds_dwordx4 %2, %3 sc1\n\t"
                                 : "=&s"(t0), "=&s"(t1), "=&v"(vo) : "s"(g), "s"((((h >> 1) & 1) * 12 + (h & 1) * 3) * 1024), "v"(ln * 16) : "memory", "scc");
                }
                dsto = c1_dst(wave, false);
            }
            ASM_ST_SAVE(7);
            ST(6);
        }
        ST(0);
        unsigned fv = 0;
        if (CLS == 2) fv = (lane_id() & 15) * 128;
        crit1<CLS == 3 ? 0 : CLS < 2 ? 3 : CLS == 2 ? 12 : 2>(wf, C, rr, RG_XM + slot * XM_SLOT + (cpy >> 1) * XM_PAIR + (cpy & 1) * BLK, wave, wl, dsto, budget, fv, RG_FX + (cpy & 3) * 2048 STARG);
        if constexpr (CLS == 0) {
            const unsigned sp = RG_XP + slot * XP_SLOT, sx = sp + (1 + (h >> 4)) * 2 * BLK + (h & 15) * 512;
            ASM_ST_DECL;
            asm volatile(ASM_SM : [c0] "+s"(budget) ASM_ST_OUT : WREGS(wf), [rr] "s"(rr), [sx] "s"(sx), [sy] "s"(sx + XP_PAIR), [sm] "s"(sp + h * 128), [cs] "s"(BLK), [cp] "s"(XP_PAIR), [c70] "s"(0x70), [scl] "s"(SCL)
                         : "memory", "v200", "v201", "v202", "v203", "v204", "v205", "v206", "v207", "v208", "v209", "v210", "v211", "v212", "v213", "v214", "v215", "v216", "v217", "v218", "v219", "v220", "v221", "v222", "v223", "v224", "v225", "v226", "v227", "v228", "v229", "v230", "v231", "v232", "v233", "v234", "v235", "v236", "v237", "v238", "v239", "v240", "v241", "v242", "v243", "v244", "v245", "v246", "v247", "v248", "v249", "v250", "v251", "v252", "v253", "v254", "v255");
            ASM_ST_SAVE(4);
            ST(5);
        } else if constexpr (CLS == 1 || CLS == 4) {
            const int ln = lane_id(), l15 = ln & 15, l4 = ln >> 4;
            constexpr int mt = (CLS == 4);
            const unsigned qa = L_QX + mt * 6144 + ln * 16, kmo = L_KM + l4 * 128 + (ln & 7) * 16;
            const unsigned xo = mt ? (h & 3) * 1024 : 5120 + ((h >> 1) & 1) * 1024 + (h & 1) * 256;
            const unsigned pm = l4 < 2 ? (mt ? 4096 : 7168) + (h & 3) * 256 - xo + l15 * 16 + l4 * 8 : OOB;
            const unsigned so = RG_PX + slot * PX_SLOT + (h >> 2) * BLK + xo;
            f32x4 sd;
#pragma unroll
            for (int r = 0; r < 4; ++r) sd[r] = (l4 * 4 + r > l15) ? -3.0e38f : 0.f;
            ASM_ST_DECL;
#define P4_ARGS : "+{v[240:243]}"(sd) ASM_ST_OUT : WREGS(wf), [qa] "v"(qa), [km] "v"(kmo), [pm] "v"(pm), [rr] "s"(rr), [so] "s"(so), [scl] "s"(SCL) : "memory", "v200", "v201", "v202", "v203", "v204", "v205", "v206", "v207", "v208", "v209", "v210", "v211", "v212", "v213", "v214", "v215", "v216", "v217", "v218", "v219", "v220", "v221", "v222", "v223", "v224", "v225", "v226", "v227", "v228", "v229", "v230", "v231", "v232", "v233", "v234", "v235", "v236", "v237", "v238", "v239", "v244", "v245", "v246", "v247", "v248", "v249", "v250", "v251"
            if constexpr (mt) asm volatile(ASM_P4_1 P4_ARGS); else asm volatile(ASM_P4_0 P4_ARGS);
            ASM_ST_SAVE(4);
            ST(5);
        } else {
            bar();
            ST(4);
            if (CLS == 2 && L && t + 2 < NSEG && !__all(fv > (unsigned)t + 2)) xn_wait(P, rr, t + 2, cpy);
            if (CLS == 2) { int tt = t; asm volatile("" : "+s"(tt)); if (tt + 2 == XS0) init_wait(P, h, 1); }
            if (CLS == 3) LA();
            {
                if (CLS == 3 && (wave == 4 || wave == 7)) {
                    if (t + 1 < NSEG) xdma(t + 1, 1);
                }
                ST(5);
            }
        }
        STFLUSH(t);
    }
    LF();
    };
    if (wave == 0) run(std::integral_constant<int, 0>{});
    else if (wave == 1) run(std::integral_constant<int, 1>{});
    else if (wave == 2) run(std::integral_constant<int, 4>{});
    else if (wave == 3) run(std::integral_constant<int, 2>{});
    else run(std::integral_constant<int, 3>{});
}

#undef STROLE
#define STROLE 8
__device__ __forceinline__ void h_role(const Params& P, int wave, int L, int par, int i, int cpy) {
    const rsrc_t rr = mkrsrc(P.ws + par * 8192, RG_L), rp = mkrsrc(P.ws + PG0, PG_L);
    const char* xn = P.ws + PG0 + PG_XN + L * PG_LX;
    auto run = [&](auto Cc) {
    constexpr bool HI = decltype(Cc)::value;
    const int mt = (wave >> 1) & 1;
    auto xdma = [&](int seg, int hb) {
        const unsigned l16 = lane_id() * 16;
        const char* g = xn + pgb(seg, par);
        dma4(g + blin(mt * 20480 + hb * 8192 + (wave & 1) * 4096), l16, L_XA + mt * 16384 + hb * 8192 + (wave & 1) * 4096);
        dmaq<0>(g + blin(mt * 20480 + 16384) + hb * 2048 + (wave & 1) * 1024, l16, L_XB + mt * 4096 + hb * 2048 + (wave & 1) * 1024);
    };
    WRegs wf;
    load_w3(wf, i < 16 ? P.w[2] : P.w[0], wave, (i < 16 ? i * 2 + HI : (i - 16) * 4 + HI * 2) * 384, i >= 16);
    init_wait(P, wave, L);
    const unsigned qs = (HI ? L_KX : L_QX) + (wave & 1) * 3072 + mt * 8;
    const unsigned xa = L_XA + wave * 2048 + lane_id() * 16, wl = L_W + wave * 10240 + lane_id() * 16;
    const unsigned fp = RG_FH + (i >> 4) * BLK + ((i & 15) * 4 + (wave & 3)) * 128;
    const unsigned fb = L ? RG_FX + (cpy & 3) * 2048 : RG_FV + (i & 3) * 2048, fa = L ? 0 : HAH;
    unsigned go = 0, all = 0;
    STDECL;
    LDECL;
    for (int t = HT0; t <= NSEG; ++t) {
        ST(0);
        if constexpr (HI) {
            const unsigned l16 = lane_id() * 16;
            if (t > HT0) {
                u32x4 v[6];
                const int w4 = wave - 4;
#pragma unroll
                for (int k = 0; k < 6; ++k) v[k] = lds_ld<u32x4>(L_QX + ((k >> 1) * 8 + w4 * 2 + (k & 1)) * 1024 + l16);
                const unsigned so = pgb(t - 1, par) + PG_HV + i * 3 * BLK + w4 * 2048;
#pragma unroll
                for (int k = 0; k < 6; ++k) st128(v[k], rp, l16 + (k & 1) * 1024, so + (k >> 1) * BLK);
                if (!all) {
                    wait_vm<0>();
                    if (l16 < 512) __builtin_amdgcn_raw_buffer_store_b32(t, rr, l16 >> 2, fp, 16);
                }
            }
            ST(1);
            if (t == NSEG) break;
            if (!all) bar();
            xdma(t, 1);
        } else {
            if (t == NSEG) break;
            if (!go) {
                for (int spins = 0;; ++spins) {
                    if (__all(__builtin_amdgcn_raw_buffer_load_b32(rr, (lane_id() & 15) * 128, fb, 16) + fa > (unsigned)t)) break;
                    __builtin_amdgcn_s_sleep(HSL);
                    if (spin_fail(spins, P)) break;
                }
                asm volatile("" ::: "memory");
                xdma(t, 0); wait_vm<0>();
            }
            if (!all) bar();
        }
        ST(2);
        C1Regs C;
        const WRegs& W = wf;
        ASM_ST_DECL;
        if constexpr (HI) {
            const unsigned l4 = lane_id() * 4;
            asm volatile(ASM_XG_H_HI : XG_OUT ASM_ST_OUT : XG_IN, [rr] "s"(rr), [fp] "s"(fp), [tv] "s"(t), [fv] "v"((all && l4 < 128) ? l4 : OOB) : XG_CLOB);
        } else {
            asm volatile(ASM_XG_S_LO : XG_OUT ASM_ST_OUT : XG_IN : XG_CLOB);
            if (t + 1 == XS0) init_wait(P, wave, 1);
            const unsigned fv = __builtin_amdgcn_raw_buffer_load_b32(rr, (lane_id() & 15) * 128, fb, 16) + fa;
            go = __builtin_amdgcn_readfirstlane((t + 1 < NSEG && __all(fv > (unsigned)t + 1)) ? 1u : 0u);
            if (go) xdma(t + 1, 0);
            lds_st<unsigned>(L_MEM + wave * 4, go);
            wait_vm<0>();
        }
        ASM_ST_SAVE(3);
        ST(4);
        if (!HI) { LB(t); LA(); }
        PMARK(L && wave == 7, 6, t);
        bar();
        const u32x4 gv = lds_ld<u32x4>(L_MEM);
        all = __builtin_amdgcn_readfirstlane(gv[0] & gv[1] & gv[2] & gv[3]);
        ST(5);
        STFLUSH(t);
    }
    LF();
    };
    if (wave < 4) run(std::false_type{}); else run(std::true_type{});
}

#undef STROLE
#define STROLE 2
__device__ __forceinline__ void v_role(const Params& P, int wave, int L, int par, int h, int cpy) {
    const rsrc_t rr = mkrsrc(P.ws + par * 8192, RG_L);
    WRegs wf;
    STDECL;
    auto runv = [&](auto Cc) {
        load_w3(wf, P.w[2], wave, h * 384, false);
        MARKX(!L && wave == 0, 6);
        init_wait(P, wave, L);
        unsigned budget = 1u << 18;
        const unsigned wl = L_W + wave * 10240 + lane_id() * 16, dsto = c1_dst(wave, false);
        auto vfetch = [&](int seg, int tries) {
            const int ln = lane_id();
            for (int spins = 0; tries != -2; ++spins) {
                if (spins == tries) return false;
                const unsigned f = __builtin_amdgcn_raw_buffer_load_b32(rr, (ln & 3) * 128, RG_FH + (h >> 1) * 512, 16);
                if (__all(f > (unsigned)seg)) break;
                __builtin_amdgcn_s_sleep(4);
                if (spin_fail(spins, P)) break;
            }
            asm volatile("" ::: "memory");
            const char* g = P.ws + PG0 + pgb(seg, par) + PG_HV + (h >> 1) * 3 * BLK;
#pragma unroll
            for (int k = 0; k < 3; ++k) {
                const int f = (h & 1) * 12 + wave * 3 + k;
                dmaq<0>(g + (f >> 3) * BLK + (f & 7) * 1024, ln * 16, L_QX + (seg & 1) * 12288 + (wave * 3 + k) * 1024);
            }
            return true;
        };
#pragma unroll 1
        for (int t = 0; t < HT0; ++t) {
            if (L) xn_wait(P, rr, t, cpy);
            const unsigned l16 = lane_id() * 16;
            const char* g = P.ws + PG0 + PG_XN + L * PG_LX + pgb(t, par);
            dma4(g + blin(wave * 4096), l16, L_XA + wave * 4096); dma4(g + blin(20480 + wave * 4096), l16, L_XA + 16384 + wave * 4096);
            dmaq<0>(g + blin(16384) + wave * 1024, l16, L_XB + wave * 1024); dmaq<0>(g + blin(20480 + 16384) + wave * 1024, l16, L_XB + 4096 + wave * 1024);
            wait_vm<0>();
            bar();
            C1Regs C;
            const WRegs& W = wf;
            const unsigned xa = L_XA + wave * 2048 + l16, qs = L_QX + (wave & 1) * 3072 + ((wave >> 1) & 1) * 8 + t * 12288;
            asm volatile(ASM_XG_V : XG_OUT : XG_IN : XG_CLOB);
            if (t == 0) bar();
        }
        bool seen = false;
        unsigned tb2 = 0;
        for (int t = 0; t < NSEG; ++t) {
            if (t + 1 - V1A >= HT0 && t + 1 - V1A < NSEG) { vfetch(t + 1 - V1A, seen ? -2 : -1); wait_vm<0>(); }
            ST(7);
            bar();
            C1Regs C;
            asm volatile("ds_read_b128 v[200:203], %4\n\tds_read_b128 v[204:207], %4 offset:1024\n\tds_read_b128 v[228:231], %4 offset:2048\n\tds_read_b128 v[232:235], %4 offset:3072\n\t"
                         "v_mov_b64 v[224:225], 0\n\tv_mov_b64 v[226:227], 0\n\tv_mov_b64 v[244:245], 0\n\tv_mov_b64 v[246:247], 0\n\tv_mov_b64 v[248:249], 0\n\tv_mov_b64 v[250:251], 0\n\ts_waitcnt lgkmcnt(0)"
                         : XG_OUT : "v"(wl) : "memory");
            ST(6);
            while ((unsigned)__builtin_amdgcn_s_memtime() - tb2 < VTD) __builtin_amdgcn_s_sleep(1);
            unsigned fv = (lane_id() & 3) * 128;
            crit1<10 + decltype(Cc)::value>(wf, C, rr, RG_XM + (t & (NSLOT - 1)) * XM_SLOT + (cpy >> 1) * XM_PAIR + (cpy & 1) * BLK, wave, wl, dsto, budget, fv, RG_FH + (h >> 1) * 512 STARG);
            __builtin_amdgcn_s_setprio(0);
            bar();
            seen = __all(fv > (unsigned)t + 2 - V1A);
            tb2 = (unsigned)__builtin_amdgcn_s_memtime();
            ST(4);
            STC(5);
            STFLUSH(t);
        }
    };
    if (wave < 2) runv(std::integral_constant<int, 3>{});
    else if (wave < 4) runv(std::integral_constant<int, 2>{});
    else {
        const int w4 = wave - 4;
        const rsrc_t rd = mkrsrc(P.ws + PG0, PG_L);
        load_wo(wf, P.w[3], wave, h);
        bar();
        bar();
        for (int t = -1; t < NSEG; ++t) {
            const unsigned slot = t & (NSLOT - 1);
            bar();
            ST(7);
#pragma unroll 1
            for (int n = V1A ? t + (t <= 0) : t + (t != 1) + (t == 0); n <= (V1A && t >= 0 ? t : t + 1) && n < NSEG; ++n) {
                const unsigned l16 = lane_id() * 16;
                const unsigned wl = L_W + wave * 10240 + l16, va = L_QX + (n & 1) * 12288 + l16;
                unsigned so = ((n & 7) << 22) + pgb(64 + (w4 >> 1), par) + ((w4 & 1) * 40 + (h >> 3)) * BLK + (h & 7) * 1024;
                asm volatile(ASM_VOX : [so] "+s"(so) : WREGS(wf), [l16] "v"(l16), [wl] "v"(wl), [va] "v"(va), [rd] "s"(rd) : "memory", "scc", "v200", "v201", "v202", "v203", "v204", "v205", "v206", "v207", "v208", "v209", "v210", "v211", "v212", "v213", "v214", "v215", "v216", "v217", "v218", "v219", "v220", "v221", "v222", "v223", "v224", "v225", "v226", "v227", "v228", "v229", "v230", "v231", "v232", "v233", "v234", "v235", "v236", "v237", "v238", "v239", "v240", "v241", "v242", "v243", "v244", "v245", "v246", "v247", "v248", "v249", "v250", "v251");
                if (V1A || n < 2) {
                    wait_vm<0>();
                    const unsigned l4 = lane_id() * 4;
                    if (l4 < 128) __builtin_amdgcn_raw_buffer_store_b32(n + 1, rr, l4, RG_FV + (w4 >> 1) * BLK + ((w4 & 1) * 32 + h) * 128, 16);
                }
            }
            if (t < 0) continue;
            ST(0);
            unsigned wl = L_W + wave * 10240 + lane_id() * 16;
            bf16x16 lw = lds_frag(wl, 1024);
            u32x32 cz;
            asm volatile("v_mov_b64 v[220:221], 0\n\tv_mov_b64 v[222:223], 0\n\tv_mov_b64 v[224:225], 0\n\tv_mov_b64 v[226:227], 0\n\tv_mov_b64 v[228:229], 0\n\tv_mov_b64 v[230:231], 0\n\tv_mov_b64 v[232:233], 0\n\tv_mov_b64 v[234:235], 0\n\tv_mov_b64 v[236:237], 0\n\tv_mov_b64 v[238:239], 0\n\tv_mov_b64 v[240:241], 0\n\tv_mov_b64 v[242:243], 0\n\tv_mov_b64 v[244:245], 0\n\tv_mov_b64 v[246:247], 0\n\tv_mov_b64 v[248:249], 0\n\tv_mov_b64 v[250:251], 0" : "={v[220:251]}"(cz));
            unsigned vo = ((wl >> 8) & 1) * (1 + (w4 & 1) * 2) * BLK + ((wl >> 9) & 1) * 1024 + (wl & 255);
            asm volatile("" : "+{v[212:219]}"(lw), "+{v[220:251]}"(cz), "+v"(wl), "+v"(vo));
            unsigned l16 = wl & 1023u;
            bar();
            wait_vm<0>();
            if (!V1A && l16 < 512 && t && t + 1 < NSEG) __builtin_amdgcn_raw_buffer_store_b32(t + 2, rr, l16 >> 2, RG_FV + (w4 >> 1) * BLK + ((w4 & 1) * 32 + h) * 128, 16);
            ST(1);
            const int sidx = (wl & 128u) ? 0xEEEE : 0x4444;
            unsigned so = RG_VM + slot * VM_SLOT + (unsigned)((w4 * 10 - (w4 & 1) + (h >> 4) * 2) * BLK + ((h >> 2) & 3) * 2048 + (h & 3) * 256);
            ST(3);
            ASM_ST_DECL;
            asm volatile(ASM_CRIT2 : [so] "+s"(so), "+{v[212:219]}"(lw), "+{v200}"(l16), "+{v[220:251]}"(cz) ASM_ST_OUT
                         : WREGS(wf), [wl] "v"(wl), [vo] "v"(vo), [sidx] "v"(sidx), [rr] "s"(rr)
                         : "memory", "scc", "v201", "v202", "v203", "v204", "v205", "v206", "v207", "v208", "v209", "v210", "v211");
            ASM_ST_SAVE(4);
            ST(5);
            STFLUSH(t);
        }
    }
}

#undef STROLE
#define STROLE 4
constexpr unsigned R_RED = 0;
constexpr unsigned R_PART = 16384;
__device__ __forceinline__ void r_role(const Params& P, int wave, int L, int par, int jj, int cpy) {
    const int lane = lane_id(), l15 = lane & 15, l4 = lane >> 4;
    const unsigned l16 = lane * 16;
    const rsrc_t rr = mkrsrc(P.ws + par * 8192, RG_L), rd = mkrsrc(P.ws + PG0, PG_L);
    const int sidx = (l15 < 8) ? 0x4444 : 0xEEEE;
    unsigned offp[3];
    offp[0] = RG_XP + (cpy >> 1) * XP_PAIR + (cpy & 1) * BLK + (1 + (wave >> 2)) * 2 * BLK + (wave & 3) * 2048 + (PLAY ? (l4 >> 1) * 512 + (((l15 >> 2) & 1) + (l15 & 2)) * 128 + (l4 & 1) * 64 + (l15 >> 3) * 32 + (l15 & 1) * 16 : l16);
    offp[2] = (l15 < 8) ? RG_XP + (cpy >> 1) * XP_PAIR + (cpy & 1) * BLK + wave * 512 + l4 * 128 + l15 * 16 : OOB;
    const unsigned offv = RG_VM + ((jj >> 1) * 4 + (wave >> 2) * 2 + (jj & 1)) * BLK + (wave & 3) * 2048 + l16;
    const int j = jj * 2 + ((wave >> 1) & 1), fl = (wave & 1) * 32 + (lane & 31);
    const unsigned offo = RG_XM + (wave >> 2) * XM_PAIR + (lane >> 5) * BLK + (j & 31) * 256 + (j >> 5) * 2 * BLK + fl * 4;
    init_wait(P, wave, L);
    MARK(!L && jj == 0 && wave == 0, 5);
    {
        const int row = (lane >> 2) & 7, col = 16 * j + 4 * (lane & 3) + 2 * (wave & 1);
        __builtin_amdgcn_raw_buffer_store_b32(nosent(*(const unsigned*)(P.mem0 + row * DM + col)), rr, offo, 0, 16);
    }
    auto red_addr = [](int fl) { return R_RED + fl * 80 + (fl >> 4) * 16; };
    const unsigned rw0 = (lane >> 5) * 5248 + red_addr((((l15 >> 1) & 1) * 8 + 4 * (l4 & 1)) * 4 + (l15 >> 2)) + (l15 & 1) * 4 + (wave ^ (l15 >> 3)) * 8;
    const unsigned rrd = ((wave >> 1) & 1) * 5248 + red_addr(fl) + (lane >> 5) * 32;
    u32x32 vx;
    const unsigned fo = (lane & 3) * 128, fs = RG_FV + (jj / 10) * BLK + (((jj / 5) & 1) * 32 + wave * 4) * 128, vxo = pgb(64 + jj / 10, par) + (unsigned)((jj % 10) * 8 + (wave >> 1)) * BLK + (wave & 1) * 4096;
    const rsrc_t rp = mkrsrc(P.ws + PG0, PG_L);
    const unsigned xo = (l4 >> 1) * 256 + l15 * 16 + (l4 & 1) * 8, pw = R_PART + wave * 1024 + l16, pr = R_PART + (wave & 3) * 8192 + l16;
    const int fin = wave >> 2;
    asm volatile("" : "={v[128:159]}"(vx));
    auto vx_fetch = [&](int t) {
        for (int spins = 0;; ++spins) {
            const unsigned f = __builtin_amdgcn_raw_buffer_load_b32(rr, fo, fs, 16);
            if (__all(f > (unsigned)t)) break;
            __builtin_amdgcn_s_sleep(2);
            if (spin_fail(spins, P)) break;
        }
        const unsigned sv = ((t & 7) << 22) + vxo;
        asm volatile("buffer_load_dwordx4 v[128:131], %1, %2, %3 offen sc1\n\tbuffer_load_dwordx4 v[132:135], %1, %2, %3 offen offset:1024 sc1\n\t"
                     "buffer_load_dwordx4 v[136:139], %1, %2, %3 offen offset:2048 sc1\n\tbuffer_load_dwordx4 v[140:143], %1, %2, %3 offen offset:3072 sc1\n\t"
                     "buffer_load_dwordx4 v[144:147], %1, %2, %4 offen sc1\n\tbuffer_load_dwordx4 v[148:151], %1, %2, %4 offen offset:1024 sc1\n\t"
                     "buffer_load_dwordx4 v[152:155], %1, %2, %4 offen offset:2048 sc1\n\tbuffer_load_dwordx4 v[156:159], %1, %2, %4 offen offset:3072 sc1"
                     : "+{v[128:159]}"(vx) : "v"(l16), "s"(rp), "s"(sv), "s"(sv + 4 * BLK) : "memory");
    };
    vx_fetch(0);
    unsigned budget = 1u << 18, tpub = 0;
    STDECL;
    LDECL;
    for (int t = 0; t < NSEG; ++t) {
        const unsigned slot = t & (NSLOT - 1);
        ST(0);
        while ((unsigned)__builtin_amdgcn_s_memtime() - tpub < RTD) __builtin_amdgcn_s_sleep(1);
        u32x8 vm, pv;
        u32x4 pm;
        {
            unsigned a;
            ASM_ST_DECL;
            asm volatile(ASM_RCRIT : "=&{v[176:183]}"(vm), "=&{v[184:191]}"(pv), "=&{v[192:195]}"(pm), [a] "=&s"(a), [n] "+s"(budget) ASM_ST_OUT ASM_ST2_OUT ASM_ST3_OUT
                         : [rs] "s"(rr), [ov] "v"(offv), [sv] "s"(slot * VM_SLOT), [o0] "v"(offp[0]), [o2] "v"(offp[2]), [sp] "s"(slot * XP_SLOT), [fo] "v"(fo), [sg] "s"(RG_SG + (cpy >> 1) * BLK + (cpy & 1) * 4096 + wave * 512), [tt] "s"(t),
                           "{v[128:159]}"(vx), [sidx] "v"(sidx), [rw] "v"(rw0), [rd] "v"(rrd), [oo] "v"(offo),
                           [so] "s"(((t + 1) & (NSLOT - 1)) * XM_SLOT), [pub] "s"(NSEG - 1 - t)
                         : "memory", "vcc", "scc", "v160", "v161", "v162", "v163", "v164", "v165", "v166", "v167", "v196", "v197", "v200", "v201", "v202", "v203", "v204", "v205", "v206", "v207");
            ASM_ST_SAVE(2); ASM_ST2_SAVE(1); ASM_ST3_SAVE(6);
        }
        ST(3);
        LA();
        MARK(!L && jj == 0 && wave == 0 && t < 4, 10 + t);
        PMARK(wave == 0, L * 7, t - 1 + L);
        tpub = (unsigned)__builtin_amdgcn_s_memtime();
        __builtin_amdgcn_s_sleep(RXDEL);
        unsigned early;
#pragma unroll 1
        for (int spins = 1023;; spins += 1024) {
            unsigned n;
            const unsigned sx = pgb(t, par) + blin(((wave >> 1) & 1) * 20480 + jj * 1024) + (wave & 1) * 512;
            const unsigned pxb = RG_PX + slot * PX_SLOT + wave * BLK, sv = (((t + 1) & 7) << 22) + vxo;
            ASM_ST_DECL;
            asm volatile(ASM_RX : "+{v[128:159]}"(vx), [n] "=&s"(n), [a] "=&s"(early) ASM_ST_OUT ASM_ST2_OUT
                         : "{v[176:183]}"(vm), "{v[184:191]}"(pv), "{v[192:195]}"(pm), [l16] "v"(l16), [rs] "s"(rr), [s0] "s"(pxb), [s1] "s"(pxb + 4096), [rd] "s"(rd), [rx] "s"(rd), [xo] "v"(xo),
                           [fo] "v"(fo), [fs] "s"(fs), [tn] "s"(t + 1), [sv0] "s"(sv), [sv1] "s"(sv + 4 * BLK), [rp] "s"(rp), [sx] "s"(PG_XR + L * PG_LX + sx), [sa] "s"(PG_XA + sx),
                           [pw] "v"(pw), [pr] "v"(pr), [fin] "s"(fin)
                         : "memory", "vcc", "scc", "v96", "v97", "v98", "v99", "v100", "v101", "v102", "v103", "v104", "v105", "v106", "v107", "v108", "v109", "v110", "v111", "v112", "v113", "v114", "v115", "v116", "v117", "v118", "v119", "v120", "v121", "v122", "v123", "v124", "v125", "v126", "v127", "v196", "v200", "v201", "v202", "v203", "v204", "v205", "v206", "v207", "v208", "v209", "v210", "v211", "v212", "v213", "v214", "v215", "v216", "v217", "v218", "v219", "v220", "v221", "v222", "v223", "v224", "v225", "v226", "v227", "v228", "v229", "v230", "v231", "v240", "v241", "v242", "v243", "v248", "v250", "v251");
            ASM_ST_SAVE(4); ASM_ST2_SAVE(5);
            if (__builtin_amdgcn_readfirstlane(n)) break;
            early = 0;
            if (spin_fail(spins, P)) { bar(); break; }
        }
        if (t + 1 < NSEG && !__builtin_amdgcn_readfirstlane(early)) vx_fetch(t + 1);
        PMARK(!L && wave == 7 && PMK == 1, 1, t - 1);
        st128(sent4(), rr, offv, slot * VM_SLOT); st128(sent4(), rr, offv + 1024, slot * VM_SLOT);
        __builtin_amdgcn_raw_buffer_store_b32(SENT, rr, offo, slot * XM_SLOT, 16);
        if (wave < 2 && t + AH < NSEG) st128(sent4(), rd, l16, PG_XA + pgb(t + AH, par) + blin(wave * 20480 + jj * 1024));
        ST(7);
        LB(t);
        STFLUSH(t);
    }
    LF();
}

#undef STROLE
#define STROLE 16
template <int WHICH>
__device__ __forceinline__ void w_role(const Params& P, int wave, int L, int par, int cu) {
    constexpr int KS = 20, N = (WHICH == 3) ? FF : DM, NU = 2 * NSEG;
    constexpr unsigned D_IN = WHICH == 1 ? PG_XA : WHICH == 2 ? PG_HS : PG_X1;
    constexpr unsigned D_OUT = WHICH == 1 ? PG_HS : WHICH == 2 ? PG_X1 : PG_H2;
    if (WHICH != 3 && cu * 8 + wave >= 20) {
        for (int u = 0; u <= NU; ++u) bar();
        return;
    }
    auto run = [&](auto Cc) {
    constexpr bool LO = decltype(Cc)::value;
    const int ln = lane_id(), l15 = ln & 15, l4 = ln >> 4;
    const unsigned l16 = ln * 16;
    const rsrc_t rd = mkrsrc(P.ws + PG0, PG_L);
    const int nt0 = (cu * 8 + wave) * 2;
    u32x32 w[5];
    {
        u32x4 f[2 * KS];
        load_wn<2, KS, N>(f, P.w[3 + WHICH], DM, wave, nt0, 0);
#pragma unroll
        for (int n = 0; n < 5; ++n) {
            u32x8 q[4];
#pragma unroll
            for (int i = 0; i < 4; ++i) q[i] = __builtin_shufflevector(f[n * 8 + 2 * i], f[n * 8 + 2 * i + 1], 0, 1, 2, 3, 4, 5, 6, 7);
            w[n] = cat32(q[0], q[1], q[2], q[3]);
        }
        asm volatile("" : "+{v[0:31]}"(w[0]), "+{v[32:63]}"(w[1]), "+{v[64:95]}"(w[2]), "+{v[96:127]}"(w[3]), "+{v[128:159]}"(w[4]));
    }
    init_wait(P, wave, L);
    bar();
    const int ocol = nt0 * 16 + (l4 & 1) * 16 + (l4 >> 1) * 8;
    const unsigned o3 = (unsigned)((l4 & 1) * 512 + (l4 >> 1) * 256 + l15 * 16), ro = (l4 >> 1) * 256 + l15 * 16 + (l4 & 1) * 8;
    const int grp = nt0 / 10, sh = ((grp & 1) ? -1 : 1) * ((grp >> 1) + 1);
    if (WHICH == 1) {
        unsigned zv;
        asm volatile("v_mov_b32 %0, 0" : "=v"(zv));
        st128(((u32x4){zv, zv, zv, zv}), rd, l15 <= (grp >> 1) ? toff((grp & 1) ? S_ - 1 - l15 : l15, ocol, par) : OOB, PG_HS);
    }
    auto in0 = [&](int u) { return D_IN + pgb(u >> 1, par) + blin((u & 1) * 20480 + wave * 4096); };
    auto in1 = [&](int u) { return D_IN + pgb(u >> 1, par) + blin((u & 1) * 20480 + 16384) + wave * 1024; };
    u32x16 va;
    u32x4 vb;
    u32x8 cs, k8;
#define WT_W "{v[0:31]}"(w[0]), "{v[32:63]}"(w[1]), "{v[64:95]}"(w[2]), "{v[96:127]}"(w[3]), "{v[128:159]}"(w[4])
    asm volatile("v_mov_b32 v216, 0x3f803f80\n\tv_mov_b32 v217, v216\n\tv_mov_b32 v218, v216\n\tv_mov_b32 v219, v216\n\tv_mov_b32 v220, %[c0]\n\tv_mov_b32 v232, -1\n\tv_mov_b32 v236, -1\n\t"
                 "v_mov_b32 v240, -1\n\tv_mov_b32 v244, -1\n\tv_mov_b32 v248, -1\n\t" ASM_WCS
                 : "=&{v[196:203]}"(cs), "=&{v[216:223]}"(k8), "=&{v[232:247]}"(va), "=&{v[248:251]}"(vb) : WT_W, [c0] "s"(-2.0f * 0.7978845608028654f * 1.4426950408889634f));
    unsigned la = l16, lw = wave * 8192 + l16, sr = l4 * 8192 + ((l15 >> 2) * 16 + l15) * 16 + (l15 & 3) * 4, budget = 1u << 18;
    wait_vm<0>();
    STDECL;
    LDECL;
#pragma unroll 1
    for (int u = 0; u < NU; ++u) {
        const int mt = u & 1;
        const unsigned pg = pgb(u >> 1, par), c3 = D_OUT + blin(mt * (N * 32) + (nt0 >> 1) * 1024);
        unsigned oa = o3, ob = 0, so = pg + c3, sa = u + 2 * AH < NU ? pgb((u >> 1) + AH, par) + c3 : pg + PG_SS + 4096, a;
        if (WHICH == 1) {
            const int orow = u * 16 + l15 + sh;
            ob = toff(orow + AH * SEG, ocol, par);
            oa = (unsigned)orow < (unsigned)S_ ? (ob ^ 8192u) - (AH << 21) : OOB;
            if (orow + AH * SEG >= S_ + (sh < 0 ? sh : 0)) ob = OOB;
            so = sa = D_OUT;
        }
        if (mt) { ST(0); LB(u >> 1); } else { LA(); }
        ASM_ST_DECL;
#define WT_A WT_W, [la] "v"(la), [l16] "v"(l16), [oa] "v"(oa), [rs] "s"(rd), [so] "s"(so), [sa] "s"(sa)
#define WT_G [sr] "v"(sr), [kd] "s"(1.0f / DM), [ke] "s"(1e-5f), [k1] "s"(-2.0f * 0.7978845608028654f * 1.4426950408889634f * 0.044715f)
#define WT_1 WT_G, [ob] "v"(ob), [ss] "s"(pg + PG_SS + mt * 128), [sw] "s"(nt0)
#define WT_2 [ro] "v"(ro), [sx] "s"(pg + PG_XA + blin(mt * 20480 + nt0 * 512)), [ss] "s"(pg + PG_SS + mt * 128)
#define WT_C "memory", "vcc", "scc", "v160", "v161", "v162", "v163", "v164", "v165", "v166", "v167", "v168", "v169", "v170", "v171", "v172", "v173", "v174", "v175", "v176", "v177", "v178", "v179", "v180", "v181", "v182", "v183", "v184", "v185", "v186", "v187", "v188", "v189", "v190", "v191", "v192", "v193", "v194", "v195", "v204", "v205", "v206", "v207", "v208", "v209", "v210", "v211", "v212", "v213", "v214", "v215", "v252"
#define WT_LO(A, ...) asm volatile(A : "+{v[196:203]}"(cs), "+{v[216:223]}"(k8), "+{v[232:247]}"(va), "+{v[248:251]}"(vb), [n] "+s"(budget), [a] "=&s"(a) ASM_ST_OUT ASM_ST2_OUT ASM_ST3_OUT \
                              : WT_A, __VA_ARGS__, [lw] "v"(lw), [i0] "s"(in0(u)), [i1] "s"(in1(u)), [n0] "s"(in0(u + 1)), [n1] "s"(in1(u + 1)) : WT_C)
#define WT_HI(A, ...) asm volatile(A : "+{v[196:203]}"(cs), "+{v[216:223]}"(k8) ASM_ST_OUT ASM_ST3_OUT : WT_A, __VA_ARGS__ : WT_C)
        if constexpr (LO) { if constexpr (WHICH == 1) WT_LO(ASM_WT1_LO, WT_1); else if constexpr (WHICH == 2) WT_LO(ASM_WT2_LO, WT_2); else WT_LO(ASM_WT3_LO, WT_G); }
        else { if constexpr (WHICH == 1) WT_HI(ASM_WT1_HI, WT_1); else if constexpr (WHICH == 2) WT_HI(ASM_WT2_HI, WT_2); else WT_HI(ASM_WT3_HI, WT_G); }
        la ^= 32768u; lw ^= 32768u; sr ^= 32768u;
        PMARK(!L && mt == (WHICH != 1) && (PMK == 1 || WHICH == 3), PMK == 1 ? 1 + WHICH : 1, (u >> 1) - (WHICH == 1));
    }
    wait_vm<0>();
    if constexpr (LO) asm volatile("" :: "{v[232:247]}"(va), "{v[248:251]}"(vb));
    LF();
    };
    if (wave < 4) run(std::true_type{}); else run(std::false_type{});
}

__device__ __forceinline__ void w4_role(const Params& P, int wave, int L, int par, int cu) {
    constexpr unsigned W_FL = 122880;
    const int ln = lane_id(), l15 = ln & 15, l4 = ln >> 4;
    const unsigned l16 = ln * 16;
    const rsrc_t rd = mkrsrc(P.ws + PG0, PG_L);
    lds_st<unsigned>(W_FL + wave * 4, 0u);
    STDECL;
    if (wave < 6) {
        u32x32 w[5];
        {
            u32x4 g[40];
            load_wn<5, 8, DM>(g, P.w[7], FF, wave, cu * 5, wave * 8);
#pragma unroll
            for (int n = 0; n < 5; ++n) {
                u32x8 f[4];
#pragma unroll
                for (int i = 0; i < 4; ++i) f[i] = __builtin_shufflevector(g[n * 8 + 2 * i], g[n * 8 + 2 * i + 1], 0, 1, 2, 3, 4, 5, 6, 7);
                w[n] = cat32(f[0], f[1], f[2], f[3]);
            }
            asm volatile("" : "+{v[0:31]}"(w[0]), "+{v[32:63]}"(w[1]), "+{v[64:95]}"(w[2]), "+{v[96:127]}"(w[3]), "+{v[128:159]}"(w[4]));
        }
        init_wait(P, wave, L);
        bar();
        unsigned budget = 1u << 18;
        u32x32 av;
        asm volatile("v_mov_b32 v160, -1\n\tv_mov_b32 v164, -1\n\tv_mov_b32 v168, -1\n\tv_mov_b32 v172, -1\n\tv_mov_b32 v176, -1\n\tv_mov_b32 v180, -1\n\tv_mov_b32 v184, -1\n\tv_mov_b32 v188, -1" : "={v[160:191]}"(av));
        for (int u = 0; u < 2 * NSEG; ++u) {
            if (u & 1) ST(0);
            for (int spins = 0;; ++spins) {
                asm volatile("" ::: "memory");
                if ((int)__builtin_amdgcn_readfirstlane(lds_ld<unsigned>(W_FL + 24 + (u & 1) * 4)) + 3 >= u) break;
                __builtin_amdgcn_s_sleep(2);
                if (spin_fail(spins, P)) break;
            }
            const unsigned gb = PG_H2 + pgb(u >> 1, par) + ((u & 1) * 6 + wave) * BLK, gn = PG_H2 + pgb((u + 1) >> 1, par) + ((~u & 1) * 6 + wave) * BLK;
            ASM_ST_DECL;
            asm volatile(ASM_W4_M : [n] "+s"(budget), "+{v[160:191]}"(av) ASM_ST2_OUT
                         : "{v[0:31]}"(w[0]), "{v[32:63]}"(w[1]), "{v[64:95]}"(w[2]), "{v[96:127]}"(w[3]), "{v[128:159]}"(w[4]),
                           [ld] "v"(l16 + wave * 1024 + (u & 3) * 30720), [l16] "v"(l16), [rs] "s"(rd), [s0] "s"(gb), [s1] "s"(gb + 4096), [n0] "s"(gn), [n1] "s"(gn + 4096), [un] "s"(u + 1), [fl] "v"(W_FL + wave * 4)
                         : "memory", "vcc", "scc", "v192", "v193", "v194", "v195", "v196", "v197", "v198", "v199", "v200", "v201", "v202", "v203", "v204", "v205", "v206", "v207", "v208", "v209", "v210", "v211", "v212");
            PMARK(!L && (u & 1) && PMK == 2, 2, u >> 1);
        }
        asm volatile("s_waitcnt vmcnt(0)" :: "{v[160:191]}"(av) : "memory");
    } else {
        const int mt = wave - 6;
        bar();
        const unsigned c0 = mt * 20480 + cu * 2560, o8 = (l4 >> 1) * 256 + l15 * 16 + (l4 & 1) * 8;
        unsigned budget = 1u << 18;
        u32x16 ra;
        u32x4 rb;
        asm volatile("v_mov_b32 v164, -1" : "={v[164:179]}"(ra), "={v[180:183]}"(rb));
        __builtin_amdgcn_s_setprio(W4P);
#define W4F_OUT "+{v[164:179]}"(ra), "+{v[180:183]}"(rb), [n] "+s"(budget)
#define W4F_IN [o0] "v"(o8 + blin(c0)), [o1] "v"(o8 + blin(c0 + 512)), [o2] "v"(o8 + blin(c0 + 1024)), [o3] "v"(o8 + blin(c0 + 1536)), [o4] "v"(o8 + blin(c0 + 2048)), [rd] "s"(rd), \
               [c1] "s"(PG_X1 + pg), [ca] "s"(PG_XA + pg), [n1] "s"(PG_X1 + pn), [na] "s"(PG_XA + pn), [f0] "v"(W_FL + (ln & 3) * 4), [f1] "v"(W_FL + 16 + (ln & 1) * 4), [fd] "v"(W_FL + wave * 4), \
               [la] "v"((u & 3) * 30720 + l16), [u] "s"(u), [un] "s"(u + 1)
#define W4F_CLOB "memory", "vcc", "scc", "v64", "v65", "v66", "v67", "v68", "v69", "v70", "v71", "v72", "v73", "v74", "v75", "v76", "v77", "v78", "v79", "v80", "v81", "v82", "v83", "v84", "v85", "v86", "v87", "v88", "v89", "v90", "v91", "v92", "v93", "v94", "v95", "v96", "v97", "v98", "v99", "v100", "v101", "v102", "v103", "v104", "v105", "v106", "v107", "v108", "v109", "v110", "v111", "v112", "v113", "v114", "v115", "v116", "v117", "v118", "v119", "v120", "v121", "v122", "v123", "v124", "v125", "v126", "v127", "v128", "v129", "v130", "v131", "v132", "v133", "v134", "v135", "v136", "v137", "v138", "v139", "v140", "v141", "v142", "v143", "v144", "v145", "v146", "v147", "v148", "v149", "v150", "v151", "v152", "v153", "v154", "v155", "v156", "v157", "v158", "v159", "v160", "v161", "v162", "v163", "v184", "v185", "v186", "v187", "v188", "v189", "v190", "v191", "v192", "v193", "v194", "v195", "v196", "v197", "v198", "v199", "v200"
        if (L) {
            const rsrc_t ro = mkrsrc(P.out, ACT);
#pragma unroll 1
            for (int t = 0; t < NSEG; ++t) {
                const unsigned u = t * 2 + mt, pg = pgb(t, par), pn = pgb(t + 1, par);
                ASM_ST_DECL;
                asm volatile(ASM_W4F_1 : W4F_OUT ASM_ST2_OUT : W4F_IN, [pa] "v"(l15 * (DM * 2) + (l4 & 1) * 32 + (l4 >> 1) * 16), [pb] "v"(l15 * (DM * 2) + l4 * 8), [ro] "s"(ro), [so] "s"(u * 20480 + cu * 160) : W4F_CLOB);
            }
        } else {
            const rsrc_t rr = mkrsrc(P.ws + par * 8192, RG_L), rn = mkrsrc(P.ws + (par ^ 1) * 8192, RG_L);
#pragma unroll 1
            for (int t = 0; t < NSEG; ++t) {
                const unsigned u = t * 2 + mt, pg = pgb(t, par), pn = pgb(t + 1, par), sn = (pg ^ 8192) + PG_LX, st = RG_ST + (u >> 3) * BLK + (u & 7) * 1024;
                ASM_ST_DECL;
                asm volatile(ASM_W4F_0 : W4F_OUT ASM_ST_OUT ASM_ST2_OUT ASM_ST3_OUT
                             : W4F_IN, [pa] "v"(blin(c0 + (l4 & 1) * 512) + (l4 >> 1) * 256 + l15 * 16), [pb] "v"(blin(c0 + 1024 + (l4 & 1) * 512) + (l4 >> 1) * 256 + l15 * 16),
                               [so] "v"(ln < 16 ? ln * 8 : OOB), [si] "v"(ln * 8), [rr] "s"(rr), [sp] "s"(st + cu * 128), [sq] "s"(st), [xr] "s"(PG_XR + sn), [xn] "s"(PG_XN + sn),
                               [kd] "s"(1.0f / DM), [ke] "s"(1e-5f), [tn] "s"(t + 1), [fo] "v"(ln < 32 ? (ln >> 3) * 2048 + (ln & 7) * 16 : OOB), [rn] "s"(rn), [fs] "s"(RG_FX + (cu * 2 + mt) * 128) : W4F_CLOB);
            }
        }
        asm volatile("s_waitcnt vmcnt(0)" :: "{v[164:179]}"(ra), "{v[180:183]}"(rb) : "memory");
    }
}

__device__ __forceinline__ void setup(const Params& P, int wave, int mt, int par0, int ph) {
    const bool xw = wave < 4 && (mt >= 2 * XS0) == (ph != 0);
    const int tid = threadIdx.x & 255, r15 = tid & 15, slot = tid >> 4;
    const rsrc_t rw = mkrsrc(P.ws, WS_END);
    u32x4 v[5];
    float s = 0.f, q = 0.f;
    if (xw) {
#pragma unroll
        for (int c = 0; c < 5; ++c) v[c] = *(const u32x4*)(P.x + (size_t)(mt * 16 + r15) * DM + (slot * 5 + c) * 8);
#pragma unroll
        for (int c = 0; c < 5; ++c)
#pragma unroll
            for (int e = 0; e < 4; ++e) s += lo2f(v[c][e]) + hi2f(v[c][e]);
        lds_st<float>((slot * 17 + r15) * 4, s);
    } else if (wave >= 4) {
        constexpr unsigned N0 = RG_ST / BLK, N1 = 4 * N0 + 16, N2 = PG_SS / BLK;
        const u32x4 s4 = sent4();
        for (unsigned k = mt; k < 2 * (N1 + 4 * N2); k += 128) {
            const unsigned i = k >> 1;
            const unsigned o = i < N1 ? (i < 4 * N0 ? (i / N0) * RG_SLOT + (i % N0) * BLK : (i - 3 * N0) * BLK) + (par0 ^ ph) * 8192 : PG0 + pgb((i - N1) / N2, par0 ^ ph) + ((i - N1) % N2) * BLK;
            st128(s4, rw, tid * 16, o + (k & 1) * 4096);
        }
    }
    bar();
    float mean = 0.f;
    if (xw) {
        s = 0.f;
#pragma unroll
        for (int k = 0; k < 16; ++k) s += lds_ld<float>((k * 17 + r15) * 4);
        mean = s * (1.0f / DM);
#pragma unroll
        for (int c = 0; c < 5; ++c)
#pragma unroll
            for (int e = 0; e < 4; ++e) { const float a = lo2f(v[c][e]) - mean, b = hi2f(v[c][e]) - mean; q += a * a + b * b; }
        lds_st<float>(2048 + (slot * 17 + r15) * 4, q);
    }
    bar();
    if (xw) {
        q = 0.f;
#pragma unroll
        for (int k = 0; k < 16; ++k) q += lds_ld<float>(2048 + (k * 17 + r15) * 4);
        const float m1 = rbf(mean), r = 1.0f / ln_den(q * (1.0f / DM));
#pragma unroll
        for (int c = 0; c < 5; ++c) {
            const unsigned o = toff(mt * 16 + r15, (slot * 5 + c) * 8, par0);
            st128(v[c], rw, o, PG0 + PG_XR);
            u32x4 n;
#pragma unroll
            for (int e = 0; e < 4; ++e) n[e] = pack2(rbf(lo2f(v[c][e]) - m1) * r, rbf(hi2f(v[c][e]) - m1) * r);
            st128(n, rw, o, PG0 + PG_XN);
        }
    }
    wait_vm<0>();
    bar();
    if (threadIdx.x < 8) __hip_atomic_fetch_add((unsigned*)(P.ws + FL0 + FL_IN + (ph * 8 + threadIdx.x) * 128), 1u, RLX_AGENT);
}

__global__ __launch_bounds__(512, 2) void mega_kernel(Params P) {
    const int wave = __builtin_amdgcn_readfirstlane(threadIdx.x >> 6);
    u32x4 cpf;
    {
        unsigned long long pc;
        unsigned len;
        asm volatile("s_getpc_b64 %0\n\ts_mov_b32 %1, _Z12prep1_kernel5Prep1-_Z11mega_kernel6Params" : "=s"(pc), "=s"(len));
        cpf = __builtin_amdgcn_raw_buffer_load_b128(mkrsrc((const void*)(pc & ~255ull), len), (blockIdx.x >> 3) * 8192 + threadIdx.x * 16, 0, 0);
    }
    if (threadIdx.x == 0) {
        const unsigned c0 = *(const unsigned*)(P.ws + FL0 + FL_BYTES);
        unsigned g = (unsigned)__builtin_amdgcn_s_getreg((3 << 11) | 20) & 7u, k;
        while ((k = __hip_atomic_fetch_add((unsigned*)(P.ws + FL0 + FL_TK + g * 128), 1u, RLX_AGENT)) >= 32u) g = (g + 1) & 7u;
        lds_st<unsigned>(0, (c0 & 3u) << 8 | g << 5 | k);
    }
    bar();
    const unsigned tk = __builtin_amdgcn_readfirstlane(lds_ld<unsigned>(0));
    asm volatile("" :: "v"(cpf));
    bar();
    const int g4 = (tk >> 5) & 3, k = tk & 31;
#define MAP 0
    const int L = (tk >> 7) & 1;
    const int c14 = tk >> 9;
    const bool wk = (g4 >> 1) == WEAK;
    const bool sw = wk ? (k <= 26 && k > 26 - BAL) : (k <= 20 && k > 20 - BAL);
    const int x = g4 ^ (sw ? 2 : 0), r = sw ? (wk ? k - 6 : k + 6) : k, cpy = g4 ^ c14;
    const int par = ((tk >> 7) ^ (tk >> 8)) & 1;
    if (L) {
#pragma unroll 1
        for (int ph = 0; ph < 2; ++ph) {
            setup(P, wave, (tk & 31) * 4 + g4, par ^ 1, ph);
            for (int k = ph * DLY_L; k < DLY_L; ++k) __builtin_amdgcn_s_sleep(32);
        }
    }
    {
        const int i = (r - 26) * 4 + x;
        const int d = (r < 8 ? DLY_S : r < 21 ? 0 : i < 4 ? DLY_H : DLY_W);
        for (int k = 0; k < d; ++k) __builtin_amdgcn_s_sleep(32);
    }
    if (r < 8) s_role(P, wave, L, par, x * 8 + r, cpy);
    else if (r < 16) v_role(P, wave ^ 4, L, par, x * 8 + r - 8, cpy);
    else if (r < 21) r_role(P, wave, L, par, ((r - 16) * 4 + (x ^ c14)) ^ ((r - 16) * 4 + x >= 6 ? 1 : 0), cpy);
    else {
        const int i = (r - 26) * 4 + x;
        if (i < 4) h_role(P, wave, L, par, r < 25 ? x * 4 + r - 21 : 16 + x * 2 + r - 25, cpy);
        else if (i < 7) w_role<1>(P, wave, L, par, i - 4);
        else if (i < 10) w_role<2>(P, wave, L, par, i - 7);
        else if (i < 16) w_role<3>(P, wave, L, par, i - 10);
        else w4_role(P, wave, L, par, i - 16);
    }
}

struct Prep1 { char* ws; };
__global__ __launch_bounds__(256) void prep1_kernel(Prep1 A) {
    int b = blockIdx.x;
    if (b == 0) {
        if (threadIdx.x >= 64) return;
        const rsrc_t r = mkrsrc(A.ws, RG_SLOT);
        auto probe = [&](unsigned base, unsigned bit, int n, unsigned& m0, unsigned& m1) {
            m0 = m1 = ~0u;
            for (int i = 0; i < n; ++i) {
                const unsigned long long t0 = __builtin_amdgcn_s_memtime();
                __builtin_amdgcn_raw_buffer_store_b32(0u, r, base + (i & 1) * bit + (i >> 2) * (4096 + 768), 0, 16);
                asm volatile("s_waitcnt vmcnt(0)" ::: "memory");
                const unsigned d = (unsigned)(__builtin_amdgcn_s_memtime() - t0);
                if (i & 1) m1 = d < m1 ? d : m1; else m0 = d < m0 ? d : m0;
            }
        };
        unsigned a0, a1, b0, b1;
        probe(96 * BLK, 8192, 4, a0, a1);
        const unsigned xc = (unsigned)__builtin_amdgcn_s_getreg((3 << 11) | 20), h = a0 > a1 ? 1u : 0u;
        probe(96 * BLK + h * 8192, BLK, 8, b0, b1);
        if (threadIdx.x == 0) *(u32x4*)(A.ws + FL0 + FL_BYTES) = (u32x4){(h ^ ((xc >> 2) & 1u)) | ((b0 > b1 ? 0u : 2u) ^ ((xc & 1u) << 1)), a0 | a1 << 16, b0 | b1 << 16, xc};
        return;
    }
    b -= 1;
    ((u32x4*)(A.ws + RG_FV))[b * 256 + threadIdx.x] = (u32x4){0u, 0u, 0u, 0u};
    if (b == 0 && threadIdx.x < FL_BYTES / 16) ((u32x4*)(A.ws + FL0))[threadIdx.x] = (u32x4){0u, 0u, 0u, 0u};
}

extern "C" void kernel_launch(void* const* d_in, const int* in_sizes, int n_in, void* d_out, int out_size, void* d_ws,
                              size_t ws_size, hipStream_t stream) {
    char* ws = (char*)d_ws;
    Params P;
    P.ws = ws; P.x = (const bf16_t*)d_in[0]; P.mem0 = (const bf16_t*)d_in[1]; P.out = (bf16_t*)d_out;
    for (int i = 0; i < 8; ++i) P.w[i] = (const bf16_t*)d_in[i < 4 ? 2 + i : 4 + i];
    (void)hipFuncSetAttribute((const void*)mega_kernel, hipFuncAttributeMaxDynamicSharedMemorySize, MEGA_LDS);

    Prep1 A1;
    A1.ws = ws;
    prep1_kernel<<<1 + 20, 256, 0, stream>>>(A1);
    mega_kernel<<<256, 512, MEGA_LDS, stream>>>(P);
}
```

```cpp
#include <hip/hip_runtime.h>
#include <stdint.h>
#include <type_traits>

typedef unsigned short bf16_t;
using bf16x2 = __attribute__((ext_vector_type(2))) __bf16;
using bf16x8 = __attribute__((ext_vector_type(8))) __bf16;
using bf16x16 = __attribute__((ext_vector_type(16))) __bf16;
using f32x2 = __attribute__((ext_vector_type(2))) float;
using f32x4 = __attribute__((ext_vector_type(4))) float;
using u32x2 = __attribute__((ext_vector_type(2))) unsigned;
using u32x4 = __attribute__((ext_vector_type(4))) unsigned;
typedef __amdgpu_buffer_rsrc_t rsrc_t;

__device__ __forceinline__ unsigned pack2(float a, float b) {
    bf16x2 r = __builtin_convertvector((f32x2){a, b}, bf16x2);
    return __builtin_bit_cast(unsigned, r);
}
__device__ __forceinline__ float rbf(float f) { return __uint_as_float(pack2(f, 0.f) << 16); }
__device__ __forceinline__ float lo2f(unsigned u) { return __uint_as_float(u << 16); }
__device__ __forceinline__ float hi2f(unsigned u) { return __uint_as_float(u & 0xFFFF0000u); }
__device__ __forceinline__ rsrc_t mkrsrc(const void* p, unsigned bytes) {
    return __builtin_amdgcn_make_buffer_rsrc((void*)p, 0, bytes, 0x00020000);
}

constexpr int S_ = 2048, DM = 640, NH = 32, DH = 192, HD = NH * DH  , SEG = 32, NSEG = 64;
constexpr int FF = 1536;

#define XMAP
#define NCPY 4
constexpr unsigned NSLOT = 4, NCP = NCPY;
#define FL_TK_DEF
#define DLY_L 9
#define DLY_S 1
#define DLY_W 10
#define DLY_H 2
#define EWAKE "s_nop 0"
#define LWAKE "s_wakeup\n\t"
#define SLP4 (wave == 4 ? t : 0)
#define WPF 12
#define WPRI 1
#define WPRI2 0
#define RTD 3500
#define VTD 3900
#define HAH 100
#define WEAK 0
#define BAL 0
#define HSL 8
#define OAUX 2
#define W4P 3
#define STD 3800
#define CAL1 4
#define CAL2 12
#define CALN 12
#define RSL 9
#define VTRY 0
#define VSL 1
#define NRR 5
constexpr unsigned BLK = 16384, RG_SLOT = 4u << 20;
constexpr unsigned XP_PAIR = 6 * BLK, XP_SLOT = RG_SLOT;
constexpr unsigned XM_PAIR = 4 * BLK, XM_SLOT = RG_SLOT;
constexpr unsigned VM_SLOT = RG_SLOT;
constexpr unsigned PX_SLOT = RG_SLOT;
constexpr unsigned RG_XP = 0, RG_XM = 12 * BLK, RG_VM = 20 * BLK, RG_PX = 60 * BLK;
constexpr unsigned RG_ST = 68 * BLK, RG_FV = 84 * BLK, RG_FH = 86 * BLK, RG_FX = 88 * BLK, RG_L = NSLOT * RG_SLOT;
constexpr unsigned FL0 = 89 * BLK, FL_TK = 512, FL_IN = FL_TK + 8 * 128, FL_BYTES = FL_IN + 16 * 128;
constexpr unsigned ACT = S_ * DM * 2;
#define PXROT 5
#define W4SL 5
#define RXDEL 1
#define VXD 32
#define VXAUX 16
constexpr unsigned PG0 = RG_L, PG_L = 80u << 21;
constexpr unsigned PG_XA = 0, PG_HS = 5 * BLK, PG_X1 = 10 * BLK, PG_H2 = 15 * BLK, PG_SS = 27 * BLK, PG_XR = 28 * BLK, PG_XN = 33 * BLK, PG_LX = 10 * BLK, PG_HV = 48 * BLK;
__device__ __forceinline__ unsigned pgb(unsigned p, int par) { return p << 21 | ((p ^ par) & 1) << 13; }
__device__ __forceinline__ constexpr unsigned blin(unsigned o) { return o + (o & ~8191u); }
constexpr int HT0 = 2;
constexpr int AH = 3;
constexpr unsigned WS_END = PG0 + PG_L;

#define SB() __builtin_amdgcn_sched_barrier(0)
#define LAS __attribute__((address_space(3)))
template <class T> __device__ __forceinline__ T lds_ld(unsigned a) { return *(const LAS T*)(a); }
template <class T> __device__ __forceinline__ void lds_st(unsigned a, T v) { *(LAS T*)(a) = v; }
constexpr unsigned OOB = 0x7FFFFF00u;
template <int N> __device__ __forceinline__ void wait_vm() { asm volatile("s_waitcnt vmcnt(%0)" :: "i"(N) : "memory"); }
__device__ __forceinline__ void bar() {
    asm volatile("s_waitcnt lgkmcnt(0)" ::: "memory");
    __builtin_amdgcn_s_barrier();
    asm volatile("" ::: "memory");
}
__device__ __forceinline__ int lane_id() {
    int ln;
    asm volatile("v_mbcnt_lo_u32_b32 %0, -1, 0\n\tv_mbcnt_hi_u32_b32 %0, -1, %0" : "=v"(ln));
    return ln;
}
__device__ __forceinline__ void swap16(unsigned& a, unsigned& b) { asm volatile("s_nop 3\n\tv_permlane16_swap_b32 %0, %1\n\ts_nop 1" : "+v"(a), "+v"(b)); }
__device__ __forceinline__ void swap32(unsigned& a, unsigned& b) { asm volatile("s_nop 3\n\tv_permlane32_swap_b32 %0, %1\n\ts_nop 1" : "+v"(a), "+v"(b)); }
__device__ __forceinline__ float sum_x16(float x) { unsigned a = __float_as_uint(x), b = a; swap16(a, b); return __uint_as_float(a) + __uint_as_float(b); }
__device__ __forceinline__ float sum_x32(float x) { unsigned a = __float_as_uint(x), b = a; swap32(a, b); return __uint_as_float(a) + __uint_as_float(b); }
__device__ __forceinline__ float max_x16(float x) { unsigned a = __float_as_uint(x), b = a; swap16(a, b); return __builtin_fmaxf(__uint_as_float(a), __uint_as_float(b)); }
__device__ __forceinline__ float max_x32(float x) { unsigned a = __float_as_uint(x), b = a; swap32(a, b); return __builtin_fmaxf(__uint_as_float(a), __uint_as_float(b)); }
__device__ __forceinline__ void sum2_rows(float& s, float& q) {
    unsigned a = __float_as_uint(s), b = a, c = __float_as_uint(q), d = c;
    asm volatile("s_nop 2\n\tv_permlane16_swap_b32 %0, %1\n\tv_permlane16_swap_b32 %2, %3\n\tv_add_f32 %0, %0, %1\n\tv_add_f32 %2, %2, %3\n\tv_mov_b32 %1, %0\n\tv_mov_b32 %3, %2\n\t"
                 "s_nop 1\n\tv_permlane32_swap_b32 %0, %1\n\tv_permlane32_swap_b32 %2, %3\n\tv_add_f32 %0, %0, %1\n\tv_add_f32 %2, %2, %3" : "+v"(a), "+v"(b), "+v"(c), "+v"(d));
    s = __uint_as_float(a); q = __uint_as_float(c);
}
__device__ __forceinline__ u32x4 merge16(u32x2 a, u32x2 b) {
    unsigned a0 = a[0], a1 = a[1], b0 = b[0], b1 = b[1];
    asm volatile("s_nop 3\n\tv_permlane16_swap_b32 %0, %2\n\tv_permlane16_swap_b32 %1, %3\n\ts_nop 1" : "+v"(a0), "+v"(a1), "+v"(b0), "+v"(b1));
    return (u32x4){a0, a1, b0, b1};
}
template <int AUXV>
__device__ __forceinline__ void st128_(u32x4 v, rsrc_t r, unsigned voff, unsigned soff) {
    __builtin_amdgcn_raw_buffer_store_b128(v, r, voff, soff, AUXV);
    asm volatile("s_nop 1" :: "v"(v) : "memory");
}
#define st128(V, R, VO_, SO_) st128_<16>(V, R, VO_, SO_)
#define LD128(R, VO_, SO_) __builtin_amdgcn_raw_buffer_load_b128(R, VO_, SO_, 16)
#define LD64(R, VO_, SO_) __builtin_amdgcn_raw_buffer_load_b64(R, VO_, SO_, 16)
#define BF8(X) __builtin_bit_cast(bf16x8, X)
__device__ __forceinline__ float gelu_tanh(float x) {
    const float c0 = -2.0f * 0.7978845608028654f * 1.4426950408889634f, c1 = c0 * 0.044715f;
    const float e = __builtin_amdgcn_exp2f(x * __builtin_fmaf(x * x, c1, c0));
    return x * __builtin_amdgcn_rcpf(1.0f + e);
}
#define MFMA(A_, B_, C_) C_ = __builtin_amdgcn_mfma_f32_16x16x32_bf16(A_, B_, C_, 0, 0, 0)
#define SMF(A_, B_, C_) C_ = __builtin_amdgcn_smfmac_f32_16x16x64_bf16(A_, B_, C_, sidx, 0, 0)
__device__ __forceinline__ float ln_den(float var) { return rbf(__builtin_amdgcn_sqrtf(rbf(rbf(var) + 1e-5f))); }
__device__ __forceinline__ float rcp(float x) { return __builtin_amdgcn_rcpf(x); }

struct Params {
    char* ws;
    const bf16_t* x;
    const bf16_t* mem0;
    const bf16_t* w[8];
    bf16_t* out;
};

#define RLX_AGENT __ATOMIC_RELAXED, __HIP_MEMORY_SCOPE_AGENT
#define SENT 0xFFFFFFFFu
constexpr int SPIN_CAP = 400000;
__device__ __forceinline__ bool spin_fail(int spins, const Params& P) {
    if ((spins & 1023) == 1023) {
        const rsrc_t rt = mkrsrc(P.ws + FL0, FL_BYTES);
        unsigned o;
        asm volatile("v_mov_b32 %0, 0x100" : "=v"(o));
        if (__builtin_amdgcn_readfirstlane(__builtin_amdgcn_raw_buffer_load_b32(rt, o, 0, 16)) != 0u) return true;
        if (spins > SPIN_CAP) { __builtin_amdgcn_raw_buffer_store_b32(o, rt, o, 0, 16); return true; }
    }
    return false;
}
__device__ __forceinline__ bool ok4(u32x4 v) { return v[0] != SENT && v[1] != SENT && v[2] != SENT && v[3] != SENT; }
__device__ __forceinline__ unsigned nosent(unsigned d) { return d < 0xFFFFFFFEu ? d : 0xFFFFFFFEu; }
__device__ __forceinline__ u32x4 nosent4(u32x4 v) { return (u32x4){nosent(v[0]), nosent(v[1]), nosent(v[2]), nosent(v[3])}; }
__device__ __forceinline__ unsigned umax3(unsigned a, unsigned b, unsigned c) { const unsigned m = a > b ? a : b; return m > c ? m : c; }
__device__ __forceinline__ unsigned umax4(unsigned m, u32x4 v) { return umax3(umax3(m, v[0], v[1]), v[2], v[3]); }
__device__ __forceinline__ u32x4 sent4() { unsigned sv; asm volatile("v_mov_b32 %0, -1" : "=v"(sv)); return (u32x4){sv, sv, sv, sv}; }
template <int N>
__device__ __forceinline__ void poll(rsrc_t rs, unsigned base, const unsigned (&off)[N], u32x4 (&v)[N], const Params& P) {
    for (int spins = 0;; ++spins) {
        unsigned m = 0;
#pragma unroll
        for (int k = 0; k < N; ++k) v[k] = LD128(rs, off[k], base);
#pragma unroll
        for (int k = 0; k < N; ++k) m = umax4(m, v[k]);
        if (__all(m != SENT)) return;
        asm volatile("" ::: "memory");
        if (spin_fail(spins, P)) return;
    }
}
__device__ __forceinline__ bf16x16 cat16(bf16x8 a, bf16x8 b) {
    return __builtin_shufflevector(a, b, 0, 1, 2, 3, 4, 5, 6, 7, 8, 9, 10, 11, 12, 13, 14, 15);
}
__device__ __forceinline__ bf16x8 half8(bf16x16 v, int h) {
    return h ? __builtin_shufflevector(v, v, 8, 9, 10, 11, 12, 13, 14, 15) : __builtin_shufflevector(v, v, 0, 1, 2, 3, 4, 5, 6, 7);
}
__device__ __forceinline__ bf16x16 lds_frag(unsigned a, unsigned step) { return cat16(lds_ld<bf16x8>(a), lds_ld<bf16x8>(a + step)); }
template <int I>
__device__ __forceinline__ void dmaq(const char* g, unsigned l16, unsigned dst) {
    asm volatile("s_mov_b32 m0, %2\n\ts_nop 0\n\tglobal_load_lds_dwordx4 %0, %1 offset:%3 sc1" :: "v"(l16), "s"(g), "s"(dst), "i"(I * 1024) : "memory");
}
__device__ __forceinline__ void dma4(const char* g, unsigned l16, unsigned dst) {
    asm volatile("s_mov_b32 m0, %2\n\ts_nop 0\n\tglobal_load_lds_dwordx4 %0, %1 sc1\n\tglobal_load_lds_dwordx4 %0, %1 offset:1024 sc1\n\t"
                 "global_load_lds_dwordx4 %0, %1 offset:2048 sc1\n\tglobal_load_lds_dwordx4 %0, %1 offset:3072 sc1" :: "v"(l16), "s"(g), "s"(dst) : "memory");
}
__device__ __forceinline__ unsigned toff(int row, int col, int par) {
    return pgb(row >> 5, par) + blin((unsigned)((row >> 4) & 1) * 20480u + (unsigned)(col >> 3) * 256u) + (unsigned)(row & 15) * 16u;
}

#define ASM_ST
#define ASM_ST_DECL
#define ASM_ST_OUT
#define ASM_ST_SAVE(k)
#define ASM_ST2
#define ASM_ST3
#define ASM_ST3_OUT
#define ASM_ST3_SAVE(k)
#define ASM_ST2_OUT
#define ASM_ST2_SAVE(k)
#define STDECL
#define ST(k)
#define STC(k)
#define STFLUSH(t)

#define MARK(c, k)
#define MARKX(c, k)
#define PMARK(c, st, seg)
#define LDECL
#define LA()
#define LB(t)
#define LF()
#define STROLE 3
constexpr unsigned L_MEM = 0;
constexpr unsigned L_Q = 10240;
constexpr unsigned L_KM = 13312;
constexpr unsigned L_QX = 16384;
constexpr unsigned L_KX = 28672;
constexpr unsigned L_XB = 40960;
constexpr unsigned L_XA = 49152;
constexpr unsigned L_W = 81920;
constexpr int MEGA_LDS = 163840;
constexpr int wi(int i, int j) { return (i >> 1) * 5 + ((i & 1) ? 2 + j : j - 1); }

#define RPUB2
#define WNT ""
#define PRIUP "s_setprio 2\n\t"
#define PRISM PRIUP
#define STR2(x) #x
#define STRX(x) STR2(x)
#define W4SLEEP "s_sleep " STRX(W4SL) "\n\t"
#define PLAY 0
#define V1A 0
#define ASM_WLOAD3 \
    "s_xor_b32 %[t], %[wx], 0\n\t" \
    "s_mul_i32 %[t], %[t], 0xc0000\n\t" \
    "buffer_load_dwordx4 v[200:203], %[o0], %[rs], %[t] offen" WNT "\n\t" \
    "buffer_load_dwordx4 v[0:3], %[o1], %[rs], %[t] offen" WNT "\n\t" \
    "buffer_load_dwordx4 v[8:11], %[o2], %[rs], %[t] offen" WNT "\n\t" \
    "s_add_u32 %[t], %[t], 0x18000\n\t" \
    "buffer_load_dwordx4 v[16:19], %[o0], %[rs], %[t] offen" WNT "\n\t" \
    "buffer_load_dwordx4 v[24:27], %[o1], %[rs], %[t] offen" WNT "\n\t" \
    "buffer_load_dwordx4 v[32:35], %[o2], %[rs], %[t] offen" WNT "\n\t" \
    "s_add_u32 %[t], %[t], 0x18000\n\t" \
    "buffer_load_dwordx4 v[208:211], %[o0], %[rs], %[t] offen" WNT "\n\t" \
    "buffer_load_dwordx4 v[40:43], %[o1], %[rs], %[t] offen" WNT "\n\t" \
    "buffer_load_dwordx4 v[48:51], %[o2], %[rs], %[t] offen" WNT "\n\t" \
    "s_add_u32 %[t], %[t], 0x18000\n\t" \
    "buffer_load_dwordx4 v[56:59], %[o0], %[rs], %[t] offen" WNT "\n\t" \
    "buffer_load_dwordx4 v[64:67], %[o1], %[rs], %[t] offen" WNT "\n\t" \
    "buffer_load_dwordx4 v[72:75], %[o2], %[rs], %[t] offen" WNT "\n\t" \
    "s_add_u32 %[t], %[t], 0x18000\n\t" \
    "buffer_load_dwordx4 v[204:207], %[o0], %[rs], %[t] offen" WNT "\n\t" \
    "buffer_load_dwordx4 v[4:7], %[o1], %[rs], %[t] offen" WNT "\n\t" \
    "buffer_load_dwordx4 v[12:15], %[o2], %[rs], %[t] offen" WNT "\n\t" \
    "s_add_u32 %[t], %[t], 0x18000\n\t" \
    "buffer_load_dwordx4 v[20:23], %[o0], %[rs], %[t] offen" WNT "\n\t" \
    "buffer_load_dwordx4 v[28:31], %[o1], %[rs], %[t] offen" WNT "\n\t" \
    "buffer_load_dwordx4 v[36:39], %[o2], %[rs], %[t] offen" WNT "\n\t" \
    "s_add_u32 %[t], %[t], 0x18000\n\t" \
    "buffer_load_dwordx4 v[212:215], %[o0], %[rs], %[t] offen" WNT "\n\t" \
    "buffer_load_dwordx4 v[44:47], %[o1], %[rs], %[t] offen" WNT "\n\t" \
    "buffer_load_dwordx4 v[52:55], %[o2], %[rs], %[t] offen" WNT "\n\t" \
    "s_add_u32 %[t], %[t], 0x18000\n\t" \
    "buffer_load_dwordx4 v[60:63], %[o0], %[rs], %[t] offen" WNT "\n\t" \
    "buffer_load_dwordx4 v[68:71], %[o1], %[rs], %[t] offen" WNT "\n\t" \
    "buffer_load_dwordx4 v[76:79], %[o2], %[rs], %[t] offen" WNT "\n\t" \
    "s_add_u32 %[t], %[t], 0x18000\n\t" \
    "s_waitcnt vmcnt(21)\n\t" \
    "ds_write_b128 %[s0], v[200:203] offset:0\n\t" \
    "ds_write_b128 %[s1], v[0:3] offset:0\n\t" \
    "ds_write_b128 %[s2], v[8:11] offset:0\n\t" \
    "s_waitcnt vmcnt(18)\n\t" \
    "ds_write_b128 %[s0], v[16:19] offset:3456\n\t" \
    "ds_write_b128 %[s1], v[24:27] offset:3456\n\t" \
    "ds_write_b128 %[s2], v[32:35] offset:3456\n\t" \
    "s_waitcnt vmcnt(15)\n\t" \
    "ds_write_b128 %[s0], v[208:211] offset:6912\n\t" \
    "ds_write_b128 %[s1], v[40:43] offset:6912\n\t" \
    "ds_write_b128 %[s2], v[48:51] offset:6912\n\t" \
    "s_waitcnt vmcnt(12)\n\t" \
    "ds_write_b128 %[s0], v[56:59] offset:10368\n\t" \
    "ds_write_b128 %[s1], v[64:67] offset:10368\n\t" \
    "ds_write_b128 %[s2], v[72:75] offset:10368\n\t" \
    "s_waitcnt lgkmcnt(0)\n\t" \
    "s_barrier \n\t" \
    "ds_read_b64_tr_b16 v[200:201], %[t0] offset:0\n\t" \
    "ds_read_b64_tr_b16 v[202:203], %[t0] offset:1664\n\t" \
    "ds_read_b64_tr_b16 v[0:1], %[t0] offset:128\n\t" \
    "ds_read_b64_tr_b16 v[2:3], %[t0] offset:1792\n\t" \
    "ds_read_b64_tr_b16 v[8:9], %[t0] offset:256\n\t" \
    "ds_read_b64_tr_b16 v[10:11], %[t0] offset:1920\n\t" \
    "ds_read_b64_tr_b16 v[16:17], %[t1] offset:0\n\t" \
    "ds_read_b64_tr_b16 v[18:19], %[t1] offset:1664\n\t" \
    "ds_read_b64_tr_b16 v[24:25], %[t1] offset:128\n\t" \
    "ds_read_b64_tr_b16 v[26:27], %[t1] offset:1792\n\t" \
    "ds_read_b64_tr_b16 v[32:33], %[t1] offset:256\n\t" \
    "ds_read_b64_tr_b16 v[34:35], %[t1] offset:1920\n\t" \
    "ds_read_b64_tr_b16 v[208:209], %[t2] offset:0\n\t" \
    "ds_read_b64_tr_b16 v[210:211], %[t2] offset:1664\n\t" \
    "ds_read_b64_tr_b16 v[40:41], %[t2] offset:128\n\t" \
    "ds_read_b64_tr_b16 v[42:43], %[t2] offset:1792\n\t" \
    "ds_read_b64_tr_b16 v[48:49], %[t2] offset:256\n\t" \
    "ds_read_b64_tr_b16 v[50:51], %[t2] offset:1920\n\t" \
    "ds_read_b64_tr_b16 v[56:57], %[t3] offset:0\n\t" \
    "ds_read_b64_tr_b16 v[58:59], %[t3] offset:1664\n\t" \
    "ds_read_b64_tr_b16 v[64:65], %[t3] offset:128\n\t" \
    "ds_read_b64_tr_b16 v[66:67], %[t3] offset:1792\n\t" \
    "ds_read_b64_tr_b16 v[72:73], %[t3] offset:256\n\t" \
    "ds_read_b64_tr_b16 v[74:75], %[t3] offset:1920\n\t" \
    "s_xor_b32 %[t], %[wx], 4\n\t" \
    "s_mul_i32 %[t], %[t], 0xc0000\n\t" \
    "buffer_load_dwordx4 v[216:219], %[o0], %[rs], %[t] offen" WNT "\n\t" \
    "buffer_load_dwordx4 v[80:83], %[o1], %[rs], %[t] offen" WNT "\n\t" \
    "buffer_load_dwordx4 v[88:91], %[o2], %[rs], %[t] offen" WNT "\n\t" \
    "s_add_u32 %[t], %[t], 0x18000\n\t" \
    "buffer_load_dwordx4 v[96:99], %[o0], %[rs], %[t] offen" WNT "\n\t" \
    "buffer_load_dwordx4 v[104:107], %[o1], %[rs], %[t] offen" WNT "\n\t" \
    "buffer_load_dwordx4 v[112:115], %[o2], %[rs], %[t] offen" WNT "\n\t" \
    "s_add_u32 %[t], %[t], 0x18000\n\t" \
    "buffer_load_dwordx4 v[224:227], %[o0], %[rs], %[t] offen" WNT "\n\t" \
    "buffer_load_dwordx4 v[120:123], %[o1], %[rs], %[t] offen" WNT "\n\t" \
    "buffer_load_dwordx4 v[128:131], %[o2], %[rs], %[t] offen" WNT "\n\t" \
    "s_add_u32 %[t], %[t], 0x18000\n\t" \
    "buffer_load_dwordx4 v[136:139], %[o0], %[rs], %[t] offen" WNT "\n\t" \
    "buffer_load_dwordx4 v[144:147], %[o1], %[rs], %[t] offen" WNT "\n\t" \
    "buffer_load_dwordx4 v[152:155], %[o2], %[rs], %[t] offen" WNT "\n\t" \
    "s_add_u32 %[t], %[t], 0x18000\n\t" \
    "s_waitcnt lgkmcnt(0)\n\t" \
    "s_barrier \n\t" \
    "s_waitcnt vmcnt(21)\n\t" \
    "ds_write_b128 %[s0], v[204:207] offset:0\n\t" \
    "ds_write_b128 %[s1], v[4:7] offset:0\n\t" \
    "ds_write_b128 %[s2], v[12:15] offset:0\n\t" \
    "s_waitcnt vmcnt(18)\n\t" \
    "ds_write_b128 %[s0], v[20:23] offset:3456\n\t" \
    "ds_write_b128 %[s1], v[28:31] offset:3456\n\t" \
    "ds_write_b128 %[s2], v[36:39] offset:3456\n\t" \
    "s_waitcnt vmcnt(15)\n\t" \
    "ds_write_b128 %[s0], v[212:215] offset:6912\n\t" \
    "ds_write_b128 %[s1], v[44:47] offset:6912\n\t" \
    "ds_write_b128 %[s2], v[52:55] offset:6912\n\t" \
    "s_waitcnt vmcnt(12)\n\t" \
    "ds_write_b128 %[s0], v[60:63] offset:10368\n\t" \
    "ds_write_b128 %[s1], v[68:71] offset:10368\n\t" \
    "ds_write_b128 %[s2], v[76:79] offset:10368\n\t" \
    "s_waitcnt lgkmcnt(0)\n\t" \
    "s_barrier \n\t" \
    "ds_read_b64_tr_b16 v[204:205], %[t0] offset:0\n\t" \
    "ds_read_b64_tr_b16 v[206:207], %[t0] offset:1664\n\t" \
    "ds_read_b64_tr_b16 v[4:5], %[t0] offset:128\n\t" \
    "ds_read_b64_tr_b16 v[6:7], %[t0] offset:1792\n\t" \
    "ds_read_b64_tr_b16 v[12:13], %[t0] offset:256\n\t" \
    "ds_read_b64_tr_b16 v[14:15], %[t0] offset:1920\n\t" \
    "ds_read_b64_tr_b16 v[20:21], %[t1] offset:0\n\t" \
    "ds_read_b64_tr_b16 v[22:23], %[t1] offset:1664\n\t" \
    "ds_read_b64_tr_b16 v[28:29], %[t1] offset:128\n\t" \
    "ds_read_b64_tr_b16 v[30:31], %[t1] offset:1792\n\t" \
    "ds_read_b64_tr_b16 v[36:37], %[t1] offset:256\n\t" \
    "ds_read_b64_tr_b16 v[38:39], %[t1] offset:1920\n\t" \
    "ds_read_b64_tr_b16 v[212:213], %[t2] offset:0\n\t" \
    "ds_read_b64_tr_b16 v[214:215], %[t2] offset:1664\n\t" \
    "ds_read_b64_tr_b16 v[44:45], %[t2] offset:128\n\t" \
    "ds_read_b64_tr_b16 v[46:47], %[t2] offset:1792\n\t" \
    "ds_read_b64_tr_b16 v[52:53], %[t2] offset:256\n\t" \
    "ds_read_b64_tr_b16 v[54:55], %[t2] offset:1920\n\t" \
    "ds_read_b64_tr_b16 v[60:61], %[t3] offset:0\n\t" \
    "ds_read_b64_tr_b16 v[62:63], %[t3] offset:1664\n\t" \
    "ds_read_b64_tr_b16 v[68:69], %[t3] offset:128\n\t" \
    "ds_read_b64_tr_b16 v[70:71], %[t3] offset:1792\n\t" \
    "ds_read_b64_tr_b16 v[76:77], %[t3] offset:256\n\t" \
    "ds_read_b64_tr_b16 v[78:79], %[t3] offset:1920\n\t" \
    "buffer_load_dwordx4 v[220:223], %[o0], %[rs], %[t] offen" WNT "\n\t" \
    "buffer_load_dwordx4 v[84:87], %[o1], %[rs], %[t] offen" WNT "\n\t" \
    "buffer_load_dwordx4 v[92:95], %[o2], %[rs], %[t] offen" WNT "\n\t" \
    "s_add_u32 %[t], %[t], 0x18000\n\t" \
    "buffer_load_dwordx4 v[100:103], %[o0], %[rs], %[t] offen" WNT "\n\t" \
    "buffer_load_dwordx4 v[108:111], %[o1], %[rs], %[t] offen" WNT "\n\t" \
    "buffer_load_dwordx4 v[116:119], %[o2], %[rs], %[t] offen" WNT "\n\t" \
    "s_add_u32 %[t], %[t], 0x18000\n\t" \
    "buffer_load_dwordx4 v[228:231], %[o0], %[rs], %[t] offen" WNT "\n\t" \
    "buffer_load_dwordx4 v[124:127], %[o1], %[rs], %[t] offen" WNT "\n\t" \
    "buffer_load_dwordx4 v[132:135], %[o2], %[rs], %[t] offen" WNT "\n\t" \
    "s_add_u32 %[t], %[t], 0x18000\n\t" \
    "buffer_load_dwordx4 v[140:143], %[o0], %[rs], %[t] offen" WNT "\n\t" \
    "buffer_load_dwordx4 v[148:151], %[o1], %[rs], %[t] offen" WNT "\n\t" \
    "buffer_load_dwordx4 v[156:159], %[o2], %[rs], %[t] offen" WNT "\n\t" \
    "s_add_u32 %[t], %[t], 0x18000\n\t" \
    "s_waitcnt lgkmcnt(0)\n\t" \
    "s_barrier \n\t" \
    "s_waitcnt vmcnt(21)\n\t" \
    "ds_write_b128 %[s0], v[216:219] offset:0\n\t" \
    "ds_write_b128 %[s1], v[80:83] offset:0\n\t" \
    "ds_write_b128 %[s2], v[88:91] offset:0\n\t" \
    "s_waitcnt vmcnt(18)\n\t" \
    "ds_write_b128 %[s0], v[96:99] offset:3456\n\t" \
    "ds_write_b128 %[s1], v[104:107] offset:3456\n\t" \
    "ds_write_b128 %[s2], v[112:115] offset:3456\n\t" \
    "s_waitcnt vmcnt(15)\n\t" \
    "ds_write_b128 %[s0], v[224:227] offset:6912\n\t" \
    "ds_write_b128 %[s1], v[120:123] offset:6912\n\t" \
    "ds_write_b128 %[s2], v[128:131] offset:6912\n\t" \
    "s_waitcnt vmcnt(12)\n\t" \
    "ds_write_b128 %[s0], v[136:139] offset:10368\n\t" \
    "ds_write_b128 %[s1], v[144:147] offset:10368\n\t" \
    "ds_write_b128 %[s2], v[152:155] offset:10368\n\t" \
    "s_waitcnt lgkmcnt(0)\n\t" \
    "s_barrier \n\t" \
    "ds_read_b64_tr_b16 v[216:217], %[t0] offset:0\n\t" \
    "ds_read_b64_tr_b16 v[218:219], %[t0] offset:1664\n\t" \
    "ds_read_b64_tr_b16 v[80:81], %[t0] offset:128\n\t" \
    "ds_read_b64_tr_b16 v[82:83], %[t0] offset:1792\n\t" \
    "ds_read_b64_tr_b16 v[88:89], %[t0] offset:256\n\t" \
    "ds_read_b64_tr_b16 v[90:91], %[t0] offset:1920\n\t" \
    "ds_read_b64_tr_b16 v[96:97], %[t1] offset:0\n\t" \
    "ds_read_b64_tr_b16 v[98:99], %[t1] offset:1664\n\t" \
    "ds_read_b64_tr_b16 v[104:105], %[t1] offset:128\n\t" \
    "ds_read_b64_tr_b16 v[106:107], %[t1] offset:1792\n\t" \
    "ds_read_b64_tr_b16 v[112:113], %[t1] offset:256\n\t" \
    "ds_read_b64_tr_b16 v[114:115], %[t1] offset:1920\n\t" \
    "ds_read_b64_tr_b16 v[224:225], %[t2] offset:0\n\t" \
    "ds_read_b64_tr_b16 v[226:227], %[t2] offset:1664\n\t" \
    "ds_read_b64_tr_b16 v[120:121], %[t2] offset:128\n\t" \
    "ds_read_b64_tr_b16 v[122:123], %[t2] offset:1792\n\t" \
    "ds_read_b64_tr_b16 v[128:129], %[t2] offset:256\n\t" \
    "ds_read_b64_tr_b16 v[130:131], %[t2] offset:1920\n\t" \
    "ds_read_b64_tr_b16 v[136:137], %[t3] offset:0\n\t" \
    "ds_read_b64_tr_b16 v[138:139], %[t3] offset:1664\n\t" \
    "ds_read_b64_tr_b16 v[144:145], %[t3] offset:128\n\t" \
    "ds_read_b64_tr_b16 v[146:147], %[t3] offset:1792\n\t" \
    "ds_read_b64_tr_b16 v[152:153], %[t3] offset:256\n\t" \
    "ds_read_b64_tr_b16 v[154:155], %[t3] offset:1920\n\t" \
    "s_and_b32 %[t], %[wx], 3\n\t" \
    "s_mul_i32 %[t], %[t], 0x60000\n\t" \
    "s_add_u32 %[t], %[t], 0x600000\n\t" \
    "buffer_load_dwordx4 v[232:235], %[o0], %[rs], %[t] offen" WNT "\n\t" \
    "buffer_load_dwordx4 v[160:163], %[o1], %[rs], %[t] offen" WNT "\n\t" \
    "buffer_load_dwordx4 v[168:171], %[o2], %[rs], %[t] offen" WNT "\n\t" \
    "s_add_u32 %[t], %[t], 0x18000\n\t" \
    "buffer_load_dwordx4 v[236:239], %[o0], %[rs], %[t] offen" WNT "\n\t" \
    "buffer_load_dwordx4 v[164:167], %[o1], %[rs], %[t] offen" WNT "\n\t" \
    "buffer_load_dwordx4 v[172:175], %[o2], %[rs], %[t] offen" WNT "\n\t" \
    "s_add_u32 %[t], %[t], 0x18000\n\t" \
    "buffer_load_dwordx4 v[176:179], %[o0], %[rs], %[t] offen" WNT "\n\t" \
    "buffer_load_dwordx4 v[184:187], %[o1], %[rs], %[t] offen" WNT "\n\t" \
    "buffer_load_dwordx4 v[192:195], %[o2], %[rs], %[t] offen" WNT "\n\t" \
    "s_add_u32 %[t], %[t], 0x18000\n\t" \
    "buffer_load_dwordx4 v[180:183], %[o0], %[rs], %[t] offen" WNT "\n\t" \
    "buffer_load_dwordx4 v[188:191], %[o1], %[rs], %[t] offen" WNT "\n\t" \
    "buffer_load_dwordx4 v[196:199], %[o2], %[rs], %[t] offen" WNT "\n\t" \
    "s_add_u32 %[t], %[t], 0x18000\n\t" \
    "s_waitcnt lgkmcnt(0)\n\t" \
    "s_barrier \n\t" \
    "s_waitcnt vmcnt(21)\n\t" \
    "ds_write_b128 %[s0], v[220:223] offset:0\n\t" \
    "ds_write_b128 %[s1], v[84:87] offset:0\n\t" \
    "ds_write_b128 %[s2], v[92:95] offset:0\n\t" \
    "s_waitcnt vmcnt(18)\n\t" \
    "ds_write_b128 %[s0], v[100:103] offset:3456\n\t" \
    "ds_write_b128 %[s1], v[108:111] offset:3456\n\t" \
    "ds_write_b128 %[s2], v[116:119] offset:3456\n\t" \
    "s_waitcnt vmcnt(15)\n\t" \
    "ds_write_b128 %[s0], v[228:231] offset:6912\n\t" \
    "ds_write_b128 %[s1], v[124:127] offset:6912\n\t" \
    "ds_write_b128 %[s2], v[132:135] offset:6912\n\t" \
    "s_waitcnt vmcnt(12)\n\t" \
    "ds_write_b128 %[s0], v[140:143] offset:10368\n\t" \
    "ds_write_b128 %[s1], v[148:151] offset:10368\n\t" \
    "ds_write_b128 %[s2], v[156:159] offset:10368\n\t" \
    "s_waitcnt lgkmcnt(0)\n\t" \
    "s_barrier \n\t" \
    "ds_read_b64_tr_b16 v[220:221], %[t0] offset:0\n\t" \
    "ds_read_b64_tr_b16 v[222:223], %[t0] offset:1664\n\t" \
    "ds_read_b64_tr_b16 v[84:85], %[t0] offset:128\n\t" \
    "ds_read_b64_tr_b16 v[86:87], %[t0] offset:1792\n\t" \
    "ds_read_b64_tr_b16 v[92:93], %[t0] offset:256\n\t" \
    "ds_read_b64_tr_b16 v[94:95], %[t0] offset:1920\n\t" \
    "ds_read_b64_tr_b16 v[100:101], %[t1] offset:0\n\t" \
    "ds_read_b64_tr_b16 v[102:103], %[t1] offset:1664\n\t" \
    "ds_read_b64_tr_b16 v[108:109], %[t1] offset:128\n\t" \
    "ds_read_b64_tr_b16 v[110:111], %[t1] offset:1792\n\t" \
    "ds_read_b64_tr_b16 v[116:117], %[t1] offset:256\n\t" \
    "ds_read_b64_tr_b16 v[118:119], %[t1] offset:1920\n\t" \
    "ds_read_b64_tr_b16 v[228:229], %[t2] offset:0\n\t" \
    "ds_read_b64_tr_b16 v[230:231], %[t2] offset:1664\n\t" \
    "ds_read_b64_tr_b16 v[124:125], %[t2] offset:128\n\t" \
    "ds_read_b64_tr_b16 v[126:127], %[t2] offset:1792\n\t" \
    "ds_read_b64_tr_b16 v[132:133], %[t2] offset:256\n\t" \
    "ds_read_b64_tr_b16 v[134:135], %[t2] offset:1920\n\t" \
    "ds_read_b64_tr_b16 v[140:141], %[t3] offset:0\n\t" \
    "ds_read_b64_tr_b16 v[142:143], %[t3] offset:1664\n\t" \
    "ds_read_b64_tr_b16 v[148:149], %[t3] offset:128\n\t" \
    "ds_read_b64_tr_b16 v[150:151], %[t3] offset:1792\n\t" \
    "ds_read_b64_tr_b16 v[156:157], %[t3] offset:256\n\t" \
    "ds_read_b64_tr_b16 v[158:159], %[t3] offset:1920\n\t" \
    "s_waitcnt lgkmcnt(0)\n\t" \
    "s_barrier \n\t" \
    "s_waitcnt vmcnt(9)\n\t" \
    "ds_write_b128 %[s0], v[232:235] offset:0\n\t" \
    "ds_write_b128 %[s1], v[160:163] offset:0\n\t" \
    "ds_write_b128 %[s2], v[168:171] offset:0\n\t" \
    "s_waitcnt vmcnt(6)\n\t" \
    "ds_write_b128 %[s0], v[236:239] offset:3456\n\t" \
    "ds_write_b128 %[s1], v[164:167] offset:3456\n\t" \
    "ds_write_b128 %[s2], v[172:175] offset:3456\n\t" \
    "s_waitcnt vmcnt(3)\n\t" \
    "ds_write_b128 %[s0], v[176:179] offset:6912\n\t" \
    "ds_write_b128 %[s1], v[184:187] offset:6912\n\t" \
    "ds_write_b128 %[s2], v[192:195] offset:6912\n\t" \
    "s_waitcnt vmcnt(0)\n\t" \
    "ds_write_b128 %[s0], v[180:183] offset:10368\n\t" \
    "ds_write_b128 %[s1], v[188:191] offset:10368\n\t" \
    "ds_write_b128 %[s2], v[196:199] offset:10368\n\t" \
    "s_waitcnt lgkmcnt(0)\n\t" \
    "s_barrier \n\t" \
    "ds_read_b64_tr_b16 v[232:233], %[t4] offset:0\n\t" \
    "ds_read_b64_tr_b16 v[234:235], %[t4] offset:1664\n\t" \
    "ds_read_b64_tr_b16 v[160:161], %[t4] offset:128\n\t" \
    "ds_read_b64_tr_b16 v[162:163], %[t4] offset:1792\n\t" \
    "ds_read_b64_tr_b16 v[168:169], %[t4] offset:256\n\t" \
    "ds_read_b64_tr_b16 v[170:171], %[t4] offset:1920\n\t" \
    "ds_read_b64_tr_b16 v[236:237], %[t4] offset:13824\n\t" \
    "ds_read_b64_tr_b16 v[238:239], %[t4] offset:15488\n\t" \
    "ds_read_b64_tr_b16 v[164:165], %[t4] offset:13952\n\t" \
    "ds_read_b64_tr_b16 v[166:167], %[t4] offset:15616\n\t" \
    "ds_read_b64_tr_b16 v[172:173], %[t4] offset:14080\n\t" \
    "ds_read_b64_tr_b16 v[174:175], %[t4] offset:15744\n\t" \
    "ds_read_b64_tr_b16 v[176:177], %[t4] offset:27648\n\t" \
    "ds_read_b64_tr_b16 v[178:179], %[t4] offset:29312\n\t" \
    "ds_read_b64_tr_b16 v[184:185], %[t4] offset:27776\n\t" \
    "ds_read_b64_tr_b16 v[186:187], %[t4] offset:29440\n\t" \
    "ds_read_b64_tr_b16 v[192:193], %[t4] offset:27904\n\t" \
    "ds_read_b64_tr_b16 v[194:195], %[t4] offset:29568\n\t" \
    "ds_read_b64_tr_b16 v[180:181], %[t4] offset:41472\n\t" \
    "ds_read_b64_tr_b16 v[182:183], %[t4] offset:43136\n\t" \
    "ds_read_b64_tr_b16 v[188:189], %[t4] offset:41600\n\t" \
    "ds_read_b64_tr_b16 v[190:191], %[t4] offset:43264\n\t" \
    "ds_read_b64_tr_b16 v[196:197], %[t4] offset:41728\n\t" \
    "ds_read_b64_tr_b16 v[198:199], %[t4] offset:43392\n\t" \
    "s_waitcnt lgkmcnt(0)\n\t" \
    "s_barrier \n\t" \
    "ds_write_b128 %[wl], v[200:203] offset:0\n\t" \
    "ds_write_b128 %[wl], v[204:207] offset:1024\n\t" \
    "ds_write_b128 %[wl], v[208:211] offset:2048\n\t" \
    "ds_write_b128 %[wl], v[212:215] offset:3072\n\t" \
    "ds_write_b128 %[wl], v[216:219] offset:4096\n\t" \
    "ds_write_b128 %[wl], v[220:223] offset:5120\n\t" \
    "ds_write_b128 %[wl], v[224:227] offset:6144\n\t" \
    "ds_write_b128 %[wl], v[228:231] offset:7168\n\t" \
    "ds_write_b128 %[wl], v[232:235] offset:8192\n\t" \
    "ds_write_b128 %[wl], v[236:239] offset:9216\n\t" \
    "s_waitcnt lgkmcnt(0)\n\t"
#define ASM_WLOADO \
    "s_mov_b32 %[t], 0x0\n\t" \
    "buffer_load_dwordx4 v[200:203], %[o0], %[rs], %[t] offen" WNT "\n\t" \
    "buffer_load_dwordx4 v[16:19], %[o1], %[rs], %[t] offen" WNT "\n\t" \
    "buffer_load_dwordx4 v[208:211], %[o2], %[rs], %[t] offen" WNT "\n\t" \
    "buffer_load_dwordx4 v[56:59], %[o3], %[rs], %[t] offen" WNT "\n\t" \
    "buffer_load_dwordx4 v[216:219], %[o4], %[rs], %[t] offen" WNT "\n\t" \
    "s_mov_b32 %[t], 0xa000\n\t" \
    "buffer_load_dwordx4 v[96:99], %[o0], %[rs], %[t] offen" WNT "\n\t" \
    "buffer_load_dwordx4 v[224:227], %[o1], %[rs], %[t] offen" WNT "\n\t" \
    "buffer_load_dwordx4 v[136:139], %[o2], %[rs], %[t] offen" WNT "\n\t" \
    "buffer_load_dwordx4 v[232:235], %[o3], %[rs], %[t] offen" WNT "\n\t" \
    "buffer_load_dwordx4 v[176:179], %[o4], %[rs], %[t] offen" WNT "\n\t" \
    "s_mov_b32 %[t], 0x14000\n\t" \
    "buffer_load_dwordx4 v[204:207], %[o0], %[rs], %[t] offen" WNT "\n\t" \
    "buffer_load_dwordx4 v[20:23], %[o1], %[rs], %[t] offen" WNT "\n\t" \
    "buffer_load_dwordx4 v[212:215], %[o2], %[rs], %[t] offen" WNT "\n\t" \
    "buffer_load_dwordx4 v[60:63], %[o3], %[rs], %[t] offen" WNT "\n\t" \
    "buffer_load_dwordx4 v[220:223], %[o4], %[rs], %[t] offen" WNT "\n\t" \
    "s_mov_b32 %[t], 0x1e000\n\t" \
    "buffer_load_dwordx4 v[100:103], %[o0], %[rs], %[t] offen" WNT "\n\t" \
    "buffer_load_dwordx4 v[228:231], %[o1], %[rs], %[t] offen" WNT "\n\t" \
    "buffer_load_dwordx4 v[140:143], %[o2], %[rs], %[t] offen" WNT "\n\t" \
    "buffer_load_dwordx4 v[236:239], %[o3], %[rs], %[t] offen" WNT "\n\t" \
    "buffer_load_dwordx4 v[180:183], %[o4], %[rs], %[t] offen" WNT "\n\t" \
    "s_waitcnt vmcnt(15)\n\t" \
    "ds_write_b128 %[s0], v[200:203] offset:0\n\t" \
    "ds_write_b128 %[s1], v[16:19] offset:0\n\t" \
    "ds_write_b128 %[s2], v[208:211] offset:0\n\t" \
    "ds_write_b128 %[s3], v[56:59] offset:0\n\t" \
    "ds_write_b128 %[s4], v[216:219] offset:0\n\t" \
    "s_waitcnt vmcnt(10)\n\t" \
    "ds_write_b128 %[s0], v[96:99] offset:5632\n\t" \
    "ds_write_b128 %[s1], v[224:227] offset:5632\n\t" \
    "ds_write_b128 %[s2], v[136:139] offset:5632\n\t" \
    "ds_write_b128 %[s3], v[232:235] offset:5632\n\t" \
    "ds_write_b128 %[s4], v[176:179] offset:5632\n\t" \
    "ds_read_b64_tr_b16 v[200:201], %[tr] offset:0\n\t" \
    "ds_read_b64_tr_b16 v[202:203], %[tr] offset:5632\n\t" \
    "ds_read_b64_tr_b16 v[16:17], %[tr] offset:32\n\t" \
    "ds_read_b64_tr_b16 v[18:19], %[tr] offset:5664\n\t" \
    "ds_read_b64_tr_b16 v[208:209], %[tr] offset:64\n\t" \
    "ds_read_b64_tr_b16 v[210:211], %[tr] offset:5696\n\t" \
    "ds_read_b64_tr_b16 v[56:57], %[tr] offset:96\n\t" \
    "ds_read_b64_tr_b16 v[58:59], %[tr] offset:5728\n\t" \
    "ds_read_b64_tr_b16 v[216:217], %[tr] offset:128\n\t" \
    "ds_read_b64_tr_b16 v[218:219], %[tr] offset:5760\n\t" \
    "ds_read_b64_tr_b16 v[96:97], %[tr] offset:160\n\t" \
    "ds_read_b64_tr_b16 v[98:99], %[tr] offset:5792\n\t" \
    "ds_read_b64_tr_b16 v[224:225], %[tr] offset:192\n\t" \
    "ds_read_b64_tr_b16 v[226:227], %[tr] offset:5824\n\t" \
    "ds_read_b64_tr_b16 v[136:137], %[tr] offset:224\n\t" \
    "ds_read_b64_tr_b16 v[138:139], %[tr] offset:5856\n\t" \
    "ds_read_b64_tr_b16 v[232:233], %[tr] offset:256\n\t" \
    "ds_read_b64_tr_b16 v[234:235], %[tr] offset:5888\n\t" \
    "ds_read_b64_tr_b16 v[176:177], %[tr] offset:288\n\t" \
    "ds_read_b64_tr_b16 v[178:179], %[tr] offset:5920\n\t" \
    "s_barrier \n\t" \
    "s_mov_b32 %[t], 0x28000\n\t" \
    "buffer_load_dwordx4 v[0:3], %[o0], %[rs], %[t] offen" WNT "\n\t" \
    "buffer_load_dwordx4 v[24:27], %[o1], %[rs], %[t] offen" WNT "\n\t" \
    "buffer_load_dwordx4 v[40:43], %[o2], %[rs], %[t] offen" WNT "\n\t" \
    "buffer_load_dwordx4 v[64:67], %[o3], %[rs], %[t] offen" WNT "\n\t" \
    "buffer_load_dwordx4 v[80:83], %[o4], %[rs], %[t] offen" WNT "\n\t" \
    "s_mov_b32 %[t], 0x32000\n\t" \
    "buffer_load_dwordx4 v[104:107], %[o0], %[rs], %[t] offen" WNT "\n\t" \
    "buffer_load_dwordx4 v[120:123], %[o1], %[rs], %[t] offen" WNT "\n\t" \
    "buffer_load_dwordx4 v[144:147], %[o2], %[rs], %[t] offen" WNT "\n\t" \
    "buffer_load_dwordx4 v[160:163], %[o3], %[rs], %[t] offen" WNT "\n\t" \
    "buffer_load_dwordx4 v[184:187], %[o4], %[rs], %[t] offen" WNT "\n\t" \
    "s_waitcnt lgkmcnt(0)\n\t" \
    "s_barrier \n\t" \
    "s_waitcnt vmcnt(15)\n\t" \
    "ds_write_b128 %[s0], v[204:207] offset:0\n\t" \
    "ds_write_b128 %[s1], v[20:23] offset:0\n\t" \
    "ds_write_b128 %[s2], v[212:215] offset:0\n\t" \
    "ds_write_b128 %[s3], v[60:63] offset:0\n\t" \
    "ds_write_b128 %[s4], v[220:223] offset:0\n\t" \
    "s_waitcnt vmcnt(10)\n\t" \
    "ds_write_b128 %[s0], v[100:103] offset:5632\n\t" \
    "ds_write_b128 %[s1], v[228:231] offset:5632\n\t" \
    "ds_write_b128 %[s2], v[140:143] offset:5632\n\t" \
    "ds_write_b128 %[s3], v[236:239] offset:5632\n\t" \
    "ds_write_b128 %[s4], v[180:183] offset:5632\n\t" \
    "ds_read_b64_tr_b16 v[204:205], %[tr] offset:0\n\t" \
    "ds_read_b64_tr_b16 v[206:207], %[tr] offset:5632\n\t" \
    "ds_read_b64_tr_b16 v[20:21], %[tr] offset:32\n\t" \
    "ds_read_b64_tr_b16 v[22:23], %[tr] offset:5664\n\t" \
    "ds_read_b64_tr_b16 v[212:213], %[tr] offset:64\n\t" \
    "ds_read_b64_tr_b16 v[214:215], %[tr] offset:5696\n\t" \
    "ds_read_b64_tr_b16 v[60:61], %[tr] offset:96\n\t" \
    "ds_read_b64_tr_b16 v[62:63], %[tr] offset:5728\n\t" \
    "ds_read_b64_tr_b16 v[220:221], %[tr] offset:128\n\t" \
    "ds_read_b64_tr_b16 v[222:223], %[tr] offset:5760\n\t" \
    "ds_read_b64_tr_b16 v[100:101], %[tr] offset:160\n\t" \
    "ds_read_b64_tr_b16 v[102:103], %[tr] offset:5792\n\t" \
    "ds_read_b64_tr_b16 v[228:229], %[tr] offset:192\n\t" \
    "ds_read_b64_tr_b16 v[230:231], %[tr] offset:5824\n\t" \
    "ds_read_b64_tr_b16 v[140:141], %[tr] offset:224\n\t" \
    "ds_read_b64_tr_b16 v[142:143], %[tr] offset:5856\n\t" \
    "ds_read_b64_tr_b16 v[236:237], %[tr] offset:256\n\t" \
    "ds_read_b64_tr_b16 v[238:239], %[tr] offset:5888\n\t" \
    "ds_read_b64_tr_b16 v[180:181], %[tr] offset:288\n\t" \
    "ds_read_b64_tr_b16 v[182:183], %[tr] offset:5920\n\t" \
    "s_barrier \n\t" \
    "s_mov_b32 %[t], 0x5000\n\t" \
    "buffer_load_dwordx4 v[4:7], %[o0], %[rs], %[t] offen" WNT "\n\t" \
    "buffer_load_dwordx4 v[28:31], %[o1], %[rs], %[t] offen" WNT "\n\t" \
    "buffer_load_dwordx4 v[44:47], %[o2], %[rs], %[t] offen" WNT "\n\t" \
    "buffer_load_dwordx4 v[68:71], %[o3], %[rs], %[t] offen" WNT "\n\t" \
    "buffer_load_dwordx4 v[84:87], %[o4], %[rs], %[t] offen" WNT "\n\t" \
    "s_mov_b32 %[t], 0xf000\n\t" \
    "buffer_load_dwordx4 v[108:111], %[o0], %[rs], %[t] offen" WNT "\n\t" \
    "buffer_load_dwordx4 v[124:127], %[o1], %[rs], %[t] offen" WNT "\n\t" \
    "buffer_load_dwordx4 v[148:151], %[o2], %[rs], %[t] offen" WNT "\n\t" \
    "buffer_load_dwordx4 v[164:167], %[o3], %[rs], %[t] offen" WNT "\n\t" \
    "buffer_load_dwordx4 v[188:191], %[o4], %[rs], %[t] offen" WNT "\n\t" \
    "s_waitcnt lgkmcnt(0)\n\t" \
    "s_barrier \n\t" \
    "s_waitcnt vmcnt(15)\n\t" \
    "ds_write_b128 %[s0], v[0:3] offset:0\n\t" \
    "ds_write_b128 %[s1], v[24:27] offset:0\n\t" \
    "ds_write_b128 %[s2], v[40:43] offset:0\n\t" \
    "ds_write_b128 %[s3], v[64:67] offset:0\n\t" \
    "ds_write_b128 %[s4], v[80:83] offset:0\n\t" \
    "s_waitcnt vmcnt(10)\n\t" \
    "ds_write_b128 %[s0], v[104:107] offset:5632\n\t" \
    "ds_write_b128 %[s1], v[120:123] offset:5632\n\t" \
    "ds_write_b128 %[s2], v[144:147] offset:5632\n\t" \
    "ds_write_b128 %[s3], v[160:163] offset:5632\n\t" \
    "ds_write_b128 %[s4], v[184:187] offset:5632\n\t" \
    "ds_read_b64_tr_b16 v[0:1], %[tr] offset:0\n\t" \
    "ds_read_b64_tr_b16 v[2:3], %[tr] offset:5632\n\t" \
    "ds_read_b64_tr_b16 v[24:25], %[tr] offset:32\n\t" \
    "ds_read_b64_tr_b16 v[26:27], %[tr] offset:5664\n\t" \
    "ds_read_b64_tr_b16 v[40:41], %[tr] offset:64\n\t" \
    "ds_read_b64_tr_b16 v[42:43], %[tr] offset:5696\n\t" \
    "ds_read_b64_tr_b16 v[64:65], %[tr] offset:96\n\t" \
    "ds_read_b64_tr_b16 v[66:67], %[tr] offset:5728\n\t" \
    "ds_read_b64_tr_b16 v[80:81], %[tr] offset:128\n\t" \
    "ds_read_b64_tr_b16 v[82:83], %[tr] offset:5760\n\t" \
    "ds_read_b64_tr_b16 v[104:105], %[tr] offset:160\n\t" \
    "ds_read_b64_tr_b16 v[106:107], %[tr] offset:5792\n\t" \
    "ds_read_b64_tr_b16 v[120:121], %[tr] offset:192\n\t" \
    "ds_read_b64_tr_b16 v[122:123], %[tr] offset:5824\n\t" \
    "ds_read_b64_tr_b16 v[144:145], %[tr] offset:224\n\t" \
    "ds_read_b64_tr_b16 v[146:147], %[tr] offset:5856\n\t" \
    "ds_read_b64_tr_b16 v[160:161], %[tr] offset:256\n\t" \
    "ds_read_b64_tr_b16 v[162:163], %[tr] offset:5888\n\t" \
    "ds_read_b64_tr_b16 v[184:185], %[tr] offset:288\n\t" \
    "ds_read_b64_tr_b16 v[186:187], %[tr] offset:5920\n\t" \
    "s_barrier \n\t" \
    "s_mov_b32 %[t], 0x19000\n\t" \
    "buffer_load_dwordx4 v[8:11], %[o0], %[rs], %[t] offen" WNT "\n\t" \
    "buffer_load_dwordx4 v[32:35], %[o1], %[rs], %[t] offen" WNT "\n\t" \
    "buffer_load_dwordx4 v[48:51], %[o2], %[rs], %[t] offen" WNT "\n\t" \
    "buffer_load_dwordx4 v[72:75], %[o3], %[rs], %[t] offen" WNT "\n\t" \
    "buffer_load_dwordx4 v[88:91], %[o4], %[rs], %[t] offen" WNT "\n\t" \
    "s_mov_b32 %[t], 0x23000\n\t" \
    "buffer_load_dwordx4 v[112:115], %[o0], %[rs], %[t] offen" WNT "\n\t" \
    "buffer_load_dwordx4 v[128:131], %[o1], %[rs], %[t] offen" WNT "\n\t" \
    "buffer_load_dwordx4 v[152:155], %[o2], %[rs], %[t] offen" WNT "\n\t" \
    "buffer_load_dwordx4 v[168:171], %[o3], %[rs], %[t] offen" WNT "\n\t" \
    "buffer_load_dwordx4 v[192:195], %[o4], %[rs], %[t] offen" WNT "\n\t" \
    "s_waitcnt lgkmcnt(0)\n\t" \
    "s_barrier \n\t" \
    "s_waitcnt vmcnt(15)\n\t" \
    "ds_write_b128 %[s0], v[4:7] offset:0\n\t" \
    "ds_write_b128 %[s1], v[28:31] offset:0\n\t" \
    "ds_write_b128 %[s2], v[44:47] offset:0\n\t" \
    "ds_write_b128 %[s3], v[68:71] offset:0\n\t" \
    "ds_write_b128 %[s4], v[84:87] offset:0\n\t" \
    "s_waitcnt vmcnt(10)\n\t" \
    "ds_write_b128 %[s0], v[108:111] offset:5632\n\t" \
    "ds_write_b128 %[s1], v[124:127] offset:5632\n\t" \
    "ds_write_b128 %[s2], v[148:151] offset:5632\n\t" \
    "ds_write_b128 %[s3], v[164:167] offset:5632\n\t" \
    "ds_write_b128 %[s4], v[188:191] offset:5632\n\t" \
    "ds_read_b64_tr_b16 v[4:5], %[tr] offset:0\n\t" \
    "ds_read_b64_tr_b16 v[6:7], %[tr] offset:5632\n\t" \
    "ds_read_b64_tr_b16 v[28:29], %[tr] offset:32\n\t" \
    "ds_read_b64_tr_b16 v[30:31], %[tr] offset:5664\n\t" \
    "ds_read_b64_tr_b16 v[44:45], %[tr] offset:64\n\t" \
    "ds_read_b64_tr_b16 v[46:47], %[tr] offset:5696\n\t" \
    "ds_read_b64_tr_b16 v[68:69], %[tr] offset:96\n\t" \
    "ds_read_b64_tr_b16 v[70:71], %[tr] offset:5728\n\t" \
    "ds_read_b64_tr_b16 v[84:85], %[tr] offset:128\n\t" \
    "ds_read_b64_tr_b16 v[86:87], %[tr] offset:5760\n\t" \
    "ds_read_b64_tr_b16 v[108:109], %[tr] offset:160\n\t" \
    "ds_read_b64_tr_b16 v[110:111], %[tr] offset:5792\n\t" \
    "ds_read_b64_tr_b16 v[124:125], %[tr] offset:192\n\t" \
    "ds_read_b64_tr_b16 v[126:127], %[tr] offset:5824\n\t" \
    "ds_read_b64_tr_b16 v[148:149], %[tr] offset:224\n\t" \
    "ds_read_b64_tr_b16 v[150:151], %[tr] offset:5856\n\t" \
    "ds_read_b64_tr_b16 v[164:165], %[tr] offset:256\n\t" \
    "ds_read_b64_tr_b16 v[166:167], %[tr] offset:5888\n\t" \
    "ds_read_b64_tr_b16 v[188:189], %[tr] offset:288\n\t" \
    "ds_read_b64_tr_b16 v[190:191], %[tr] offset:5920\n\t" \
    "s_barrier \n\t" \
    "s_mov_b32 %[t], 0x2d000\n\t" \
    "buffer_load_dwordx4 v[12:15], %[o0], %[rs], %[t] offen" WNT "\n\t" \
    "buffer_load_dwordx4 v[36:39], %[o1], %[rs], %[t] offen" WNT "\n\t" \
    "buffer_load_dwordx4 v[52:55], %[o2], %[rs], %[t] offen" WNT "\n\t" \
    "buffer_load_dwordx4 v[76:79], %[o3], %[rs], %[t] offen" WNT "\n\t" \
    "buffer_load_dwordx4 v[92:95], %[o4], %[rs], %[t] offen" WNT "\n\t" \
    "s_mov_b32 %[t], 0x37000\n\t" \
    "buffer_load_dwordx4 v[116:119], %[o0], %[rs], %[t] offen" WNT "\n\t" \
    "buffer_load_dwordx4 v[132:135], %[o1], %[rs], %[t] offen" WNT "\n\t" \
    "buffer_load_dwordx4 v[156:159], %[o2], %[rs], %[t] offen" WNT "\n\t" \
    "buffer_load_dwordx4 v[172:175], %[o3], %[rs], %[t] offen" WNT "\n\t" \
    "buffer_load_dwordx4 v[196:199], %[o4], %[rs], %[t] offen" WNT "\n\t" \
    "s_waitcnt lgkmcnt(0)\n\t" \
    "s_barrier \n\t" \
    "s_waitcnt vmcnt(15)\n\t" \
    "ds_write_b128 %[s0], v[8:11] offset:0\n\t" \
    "ds_write_b128 %[s1], v[32:35] offset:0\n\t" \
    "ds_write_b128 %[s2], v[48:51] offset:0\n\t" \
    "ds_write_b128 %[s3], v[72:75] offset:0\n\t" \
    "ds_write_b128 %[s4], v[88:91] offset:0\n\t" \
    "s_waitcnt vmcnt(10)\n\t" \
    "ds_write_b128 %[s0], v[112:115] offset:5632\n\t" \
    "ds_write_b128 %[s1], v[128:131] offset:5632\n\t" \
    "ds_write_b128 %[s2], v[152:155] offset:5632\n\t" \
    "ds_write_b128 %[s3], v[168:171] offset:5632\n\t" \
    "ds_write_b128 %[s4], v[192:195] offset:5632\n\t" \
    "ds_read_b64_tr_b16 v[8:9], %[tr] offset:0\n\t" \
    "ds_read_b64_tr_b16 v[10:11], %[tr] offset:5632\n\t" \
    "ds_read_b64_tr_b16 v[32:33], %[tr] offset:32\n\t" \
    "ds_read_b64_tr_b16 v[34:35], %[tr] offset:5664\n\t" \
    "ds_read_b64_tr_b16 v[48:49], %[tr] offset:64\n\t" \
    "ds_read_b64_tr_b16 v[50:51], %[tr] offset:5696\n\t" \
    "ds_read_b64_tr_b16 v[72:73], %[tr] offset:96\n\t" \
    "ds_read_b64_tr_b16 v[74:75], %[tr] offset:5728\n\t" \
    "ds_read_b64_tr_b16 v[88:89], %[tr] offset:128\n\t" \
    "ds_read_b64_tr_b16 v[90:91], %[tr] offset:5760\n\t" \
    "ds_read_b64_tr_b16 v[112:113], %[tr] offset:160\n\t" \
    "ds_read_b64_tr_b16 v[114:115], %[tr] offset:5792\n\t" \
    "ds_read_b64_tr_b16 v[128:129], %[tr] offset:192\n\t" \
    "ds_read_b64_tr_b16 v[130:131], %[tr] offset:5824\n\t" \
    "ds_read_b64_tr_b16 v[152:153], %[tr] offset:224\n\t" \
    "ds_read_b64_tr_b16 v[154:155], %[tr] offset:5856\n\t" \
    "ds_read_b64_tr_b16 v[168:169], %[tr] offset:256\n\t" \
    "ds_read_b64_tr_b16 v[170:171], %[tr] offset:5888\n\t" \
    "ds_read_b64_tr_b16 v[192:193], %[tr] offset:288\n\t" \
    "ds_read_b64_tr_b16 v[194:195], %[tr] offset:5920\n\t" \
    "s_waitcnt lgkmcnt(0)\n\t" \
    "s_barrier \n\t" \
    "s_waitcnt vmcnt(5)\n\t" \
    "ds_write_b128 %[s0], v[12:15] offset:0\n\t" \
    "ds_write_b128 %[s1], v[36:39] offset:0\n\t" \
    "ds_write_b128 %[s2], v[52:55] offset:0\n\t" \
    "ds_write_b128 %[s3], v[76:79] offset:0\n\t" \
    "ds_write_b128 %[s4], v[92:95] offset:0\n\t" \
    "s_waitcnt vmcnt(0)\n\t" \
    "ds_write_b128 %[s0], v[116:119] offset:5632\n\t" \
    "ds_write_b128 %[s1], v[132:135] offset:5632\n\t" \
    "ds_write_b128 %[s2], v[156:159] offset:5632\n\t" \
    "ds_write_b128 %[s3], v[172:175] offset:5632\n\t" \
    "ds_write_b128 %[s4], v[196:199] offset:5632\n\t" \
    "ds_read_b64_tr_b16 v[12:13], %[tr] offset:0\n\t" \
    "ds_read_b64_tr_b16 v[14:15], %[tr] offset:5632\n\t" \
    "ds_read_b64_tr_b16 v[36:37], %[tr] offset:32\n\t" \
    "ds_read_b64_tr_b16 v[38:39], %[tr] offset:5664\n\t" \
    "ds_read_b64_tr_b16 v[52:53], %[tr] offset:64\n\t" \
    "ds_read_b64_tr_b16 v[54:55], %[tr] offset:5696\n\t" \
    "ds_read_b64_tr_b16 v[76:77], %[tr] offset:96\n\t" \
    "ds_read_b64_tr_b16 v[78:79], %[tr] offset:5728\n\t" \
    "ds_read_b64_tr_b16 v[92:93], %[tr] offset:128\n\t" \
    "ds_read_b64_tr_b16 v[94:95], %[tr] offset:5760\n\t" \
    "ds_read_b64_tr_b16 v[116:117], %[tr] offset:160\n\t" \
    "ds_read_b64_tr_b16 v[118:119], %[tr] offset:5792\n\t" \
    "ds_read_b64_tr_b16 v[132:133], %[tr] offset:192\n\t" \
    "ds_read_b64_tr_b16 v[134:135], %[tr] offset:5824\n\t" \
    "ds_read_b64_tr_b16 v[156:157], %[tr] offset:224\n\t" \
    "ds_read_b64_tr_b16 v[158:159], %[tr] offset:5856\n\t" \
    "ds_read_b64_tr_b16 v[172:173], %[tr] offset:256\n\t" \
    "ds_read_b64_tr_b16 v[174:175], %[tr] offset:5888\n\t" \
    "ds_read_b64_tr_b16 v[196:197], %[tr] offset:288\n\t" \
    "ds_read_b64_tr_b16 v[198:199], %[tr] offset:5920\n\t" \
    "s_waitcnt lgkmcnt(0)\n\t" \
    "s_barrier \n\t" \
    "ds_write_b128 %[wl], v[200:203] offset:0\n\t" \
    "ds_write_b128 %[wl], v[204:207] offset:1024\n\t" \
    "ds_write_b128 %[wl], v[208:211] offset:2048\n\t" \
    "ds_write_b128 %[wl], v[212:215] offset:3072\n\t" \
    "ds_write_b128 %[wl], v[216:219] offset:4096\n\t" \
    "ds_write_b128 %[wl], v[220:223] offset:5120\n\t" \
    "ds_write_b128 %[wl], v[224:227] offset:6144\n\t" \
    "ds_write_b128 %[wl], v[228:231] offset:7168\n\t" \
    "ds_write_b128 %[wl], v[232:235] offset:8192\n\t" \
    "ds_write_b128 %[wl], v[236:239] offset:9216\n\t" \
    "s_waitcnt lgkmcnt(0)\n\t"
#define ASM_XG_S_LO \
    "s_waitcnt lgkmcnt(0)\n\t" \
    "ds_read_b128 v[224:227], %[xa] offset:0\n\t" \
    "ds_read_b128 v[244:247], %[wl] offset:0\n\t" \
    "ds_read_b128 v[228:231], %[xa] offset:16384\n\t" \
    "ds_read_b128 v[232:235], %[xa] offset:1024\n\t" \
    "ds_read_b128 v[236:239], %[xa] offset:17408\n\t" \
    "ds_read_b128 v[248:251], %[wl] offset:1024\n\t" \
    "s_waitcnt lgkmcnt(5)\n\t" \
    "v_mfma_f32_16x16x32_bf16 v[204:207], v[0:3], v[224:227], 0\n\t" \
    "v_xor_b32 v252, 0x800, %[xa]\n\t" \
    "ds_read_b128 v[240:243], v252 offset:0\n\t" \
    "v_mfma_f32_16x16x32_bf16 v[208:211], v[8:11], v[224:227], 0\n\t" \
    "s_waitcnt lgkmcnt(5)\n\t" \
    "v_mfma_f32_16x16x32_bf16 v[200:203], v[244:247], v[224:227], 0\n\t" \
    "s_waitcnt lgkmcnt(4)\n\t" \
    "v_mfma_f32_16x16x32_bf16 v[216:219], v[0:3], v[228:231], 0\n\t" \
    "ds_read_b128 v[224:227], v252 offset:16384\n\t" \
    "v_mfma_f32_16x16x32_bf16 v[220:223], v[8:11], v[228:231], 0\n\t" \
    "v_mfma_f32_16x16x32_bf16 v[212:215], v[244:247], v[228:231], 0\n\t" \
    "s_waitcnt lgkmcnt(4)\n\t" \
    "v_mfma_f32_16x16x32_bf16 v[204:207], v[4:7], v[232:235], v[204:207]\n\t" \
    "ds_read_b128 v[228:231], v252 offset:1024\n\t" \
    "v_mfma_f32_16x16x32_bf16 v[208:211], v[12:15], v[232:235], v[208:211]\n\t" \
    "ds_read_b128 v[244:247], %[wl] offset:2048\n\t" \
    "s_waitcnt lgkmcnt(4)\n\t" \
    "v_mfma_f32_16x16x32_bf16 v[200:203], v[248:251], v[232:235], v[200:203]\n\t" \
    "v_mfma_f32_16x16x32_bf16 v[216:219], v[4:7], v[236:239], v[216:219]\n\t" \
    "ds_read_b128 v[232:235], v252 offset:17408\n\t" \
    "v_mfma_f32_16x16x32_bf16 v[220:223], v[12:15], v[236:239], v[220:223]\n\t" \
    "v_mfma_f32_16x16x32_bf16 v[212:215], v[248:251], v[236:239], v[212:215]\n\t" \
    "s_waitcnt lgkmcnt(4)\n\t" \
    "v_mfma_f32_16x16x32_bf16 v[204:207], v[24:27], v[240:243], v[204:207]\n\t" \
    "v_xor_b32 v252, 0x1000, %[xa]\n\t" \
    "ds_read_b128 v[236:239], v252 offset:0\n\t" \
    "v_mfma_f32_16x16x32_bf16 v[208:211], v[32:35], v[240:243], v[208:211]\n\t" \
    "ds_read_b128 v[248:251], %[wl] offset:3072\n\t" \
    "v_mfma_f32_16x16x32_bf16 v[200:203], v[16:19], v[240:243], v[200:203]\n\t" \
    "s_waitcnt lgkmcnt(5)\n\t" \
    "v_mfma_f32_16x16x32_bf16 v[216:219], v[24:27], v[224:227], v[216:219]\n\t" \
    "ds_read_b128 v[240:243], v252 offset:16384\n\t" \
    "v_mfma_f32_16x16x32_bf16 v[220:223], v[32:35], v[224:227], v[220:223]\n\t" \
    "v_mfma_f32_16x16x32_bf16 v[212:215], v[16:19], v[224:227], v[212:215]\n\t" \
    "s_waitcnt lgkmcnt(5)\n\t" \
    "v_mfma_f32_16x16x32_bf16 v[204:207], v[28:31], v[228:231], v[204:207]\n\t" \
    "ds_read_b128 v[224:227], v252 offset:1024\n\t" \
    "v_mfma_f32_16x16x32_bf16 v[208:211], v[36:39], v[228:231], v[208:211]\n\t" \
    "v_mfma_f32_16x16x32_bf16 v[200:203], v[20:23], v[228:231], v[200:203]\n\t" \
    "s_waitcnt lgkmcnt(4)\n\t" \
    "v_mfma_f32_16x16x32_bf16 v[216:219], v[28:31], v[232:235], v[216:219]\n\t" \
    "ds_read_b128 v[228:231], v252 offset:17408\n\t" \
    "v_mfma_f32_16x16x32_bf16 v[220:223], v[36:39], v[232:235], v[220:223]\n\t" \
    "v_mfma_f32_16x16x32_bf16 v[212:215], v[20:23], v[232:235], v[212:215]\n\t" \
    "s_waitcnt lgkmcnt(4)\n\t" \
    "v_mfma_f32_16x16x32_bf16 v[204:207], v[40:43], v[236:239], v[204:207]\n\t" \
    "v_xor_b32 v252, 0x1800, %[xa]\n\t" \
    "ds_read_b128 v[232:235], v252 offset:0\n\t" \
    "v_mfma_f32_16x16x32_bf16 v[208:211], v[48:51], v[236:239], v[208:211]\n\t" \
    "v_mfma_f32_16x16x32_bf16 v[200:203], v[244:247], v[236:239], v[200:203]\n\t" \
    "s_waitcnt lgkmcnt(3)\n\t" \
    "v_mfma_f32_16x16x32_bf16 v[216:219], v[40:43], v[240:243], v[216:219]\n\t" \
    "ds_read_b128 v[236:239], v252 offset:16384\n\t" \
    "v_mfma_f32_16x16x32_bf16 v[220:223], v[48:51], v[240:243], v[220:223]\n\t" \
    "v_mfma_f32_16x16x32_bf16 v[212:215], v[244:247], v[240:243], v[212:215]\n\t" \
    "s_waitcnt lgkmcnt(3)\n\t" \
    "v_mfma_f32_16x16x32_bf16 v[204:207], v[44:47], v[224:227], v[204:207]\n\t" \
    "ds_read_b128 v[240:243], v252 offset:1024\n\t" \
    "v_mfma_f32_16x16x32_bf16 v[208:211], v[52:55], v[224:227], v[208:211]\n\t" \
    "ds_read_b128 v[244:247], %[wl] offset:8192\n\t" \
    "v_mfma_f32_16x16x32_bf16 v[200:203], v[248:251], v[224:227], v[200:203]\n\t" \
    "s_waitcnt lgkmcnt(4)\n\t" \
    "v_mfma_f32_16x16x32_bf16 v[216:219], v[44:47], v[228:231], v[216:219]\n\t" \
    "ds_read_b128 v[224:227], v252 offset:17408\n\t" \
    "v_mfma_f32_16x16x32_bf16 v[220:223], v[52:55], v[228:231], v[220:223]\n\t" \
    "v_mfma_f32_16x16x32_bf16 v[212:215], v[248:251], v[228:231], v[212:215]\n\t" \
    "s_waitcnt lgkmcnt(4)\n\t" \
    "v_mfma_f32_16x16x32_bf16 v[204:207], v[64:67], v[232:235], v[204:207]\n\t" \
    "v_and_b32 v252, 0x3ff, %[xa]\n\t" \
    "ds_read_b128 v[228:231], v252 offset:40960\n\t" \
    "v_mfma_f32_16x16x32_bf16 v[208:211], v[72:75], v[232:235], v[208:211]\n\t" \
    "ds_read_b128 v[248:251], %[wl] offset:9216\n\t" \
    "v_mfma_f32_16x16x32_bf16 v[200:203], v[56:59], v[232:235], v[200:203]\n\t" \
    "s_waitcnt lgkmcnt(5)\n\t" \
    "v_mfma_f32_16x16x32_bf16 v[216:219], v[64:67], v[236:239], v[216:219]\n\t" \
    "ds_read_b128 v[232:235], v252 offset:45056\n\t" \
    "v_mfma_f32_16x16x32_bf16 v[220:223], v[72:75], v[236:239], v[220:223]\n\t" \
    "v_mfma_f32_16x16x32_bf16 v[212:215], v[56:59], v[236:239], v[212:215]\n\t" \
    "s_waitcnt lgkmcnt(5)\n\t" \
    "v_mfma_f32_16x16x32_bf16 v[204:207], v[68:71], v[240:243], v[204:207]\n\t" \
    "ds_read_b128 v[236:239], v252 offset:41984\n\t" \
    "v_mfma_f32_16x16x32_bf16 v[208:211], v[76:79], v[240:243], v[208:211]\n\t" \
    "v_mfma_f32_16x16x32_bf16 v[200:203], v[60:63], v[240:243], v[200:203]\n\t" \
    "s_waitcnt lgkmcnt(4)\n\t" \
    "v_mfma_f32_16x16x32_bf16 v[216:219], v[68:71], v[224:227], v[216:219]\n\t" \
    "ds_read_b128 v[240:243], v252 offset:46080\n\t" \
    "v_mfma_f32_16x16x32_bf16 v[220:223], v[76:79], v[224:227], v[220:223]\n\t" \
    "v_mfma_f32_16x16x32_bf16 v[212:215], v[60:63], v[224:227], v[212:215]\n\t" \
    "s_waitcnt lgkmcnt(0)\n\t" \
    "s_barrier\n\t" ASM_ST \
    "v_mfma_f32_16x16x32_bf16 v[204:207], v[160:163], v[228:231], v[204:207]\n\t" \
    "v_xor_b32 v252, 0x2000, %[xa]\n\t" \
    "ds_read_b128 v[224:227], v252 offset:0\n\t" \
    "v_mfma_f32_16x16x32_bf16 v[208:211], v[168:171], v[228:231], v[208:211]\n\t" \
    "v_mfma_f32_16x16x32_bf16 v[200:203], v[244:247], v[228:231], v[200:203]\n\t" \
    "v_mfma_f32_16x16x32_bf16 v[216:219], v[160:163], v[232:235], v[216:219]\n\t" \
    "ds_read_b128 v[228:231], v252 offset:16384\n\t" \
    "v_mfma_f32_16x16x32_bf16 v[220:223], v[168:171], v[232:235], v[220:223]\n\t" \
    "v_mfma_f32_16x16x32_bf16 v[212:215], v[244:247], v[232:235], v[212:215]\n\t" \
    "v_mfma_f32_16x16x32_bf16 v[204:207], v[164:167], v[236:239], v[204:207]\n\t" \
    "ds_read_b128 v[232:235], v252 offset:1024\n\t" \
    "v_mfma_f32_16x16x32_bf16 v[208:211], v[172:175], v[236:239], v[208:211]\n\t" \
    "ds_read_b128 v[244:247], %[wl] offset:4096\n\t" \
    "v_mfma_f32_16x16x32_bf16 v[200:203], v[248:251], v[236:239], v[200:203]\n\t" \
    "v_mfma_f32_16x16x32_bf16 v[216:219], v[164:167], v[240:243], v[216:219]\n\t" \
    "ds_read_b128 v[236:239], v252 offset:17408\n\t" \
    "v_mfma_f32_16x16x32_bf16 v[220:223], v[172:175], v[240:243], v[220:223]\n\t" \
    "v_mfma_f32_16x16x32_bf16 v[212:215], v[248:251], v[240:243], v[212:215]\n\t" \
    "s_waitcnt lgkmcnt(4)\n\t" \
    "v_mfma_f32_16x16x32_bf16 v[204:207], v[80:83], v[224:227], v[204:207]\n\t" \
    "v_xor_b32 v252, 0x2800, %[xa]\n\t" \
    "ds_read_b128 v[240:243], v252 offset:0\n\t" \
    "v_mfma_f32_16x16x32_bf16 v[208:211], v[88:91], v[224:227], v[208:211]\n\t" \
    "ds_read_b128 v[248:251], %[wl] offset:5120\n\t" \
    "s_waitcnt lgkmcnt(3)\n\t" \
    "v_mfma_f32_16x16x32_bf16 v[200:203], v[244:247], v[224:227], v[200:203]\n\t" \
    "v_mfma_f32_16x16x32_bf16 v[216:219], v[80:83], v[228:231], v[216:219]\n\t" \
    "ds_read_b128 v[224:227], v252 offset:16384\n\t" \
    "v_mfma_f32_16x16x32_bf16 v[220:223], v[88:91], v[228:231], v[220:223]\n\t" \
    "v_mfma_f32_16x16x32_bf16 v[212:215], v[244:247], v[228:231], v[212:215]\n\t" \
    "v_mfma_f32_16x16x32_bf16 v[204:207], v[84:87], v[232:235], v[204:207]\n\t" \
    "ds_read_b128 v[228:231], v252 offset:1024\n\t" \
    "v_mfma_f32_16x16x32_bf16 v[208:211], v[92:95], v[232:235], v[208:211]\n\t" \
    "ds_read_b128 v[244:247], %[wl] offset:6144\n\t" \
    "s_waitcnt lgkmcnt(3)\n\t" \
    "v_mfma_f32_16x16x32_bf16 v[200:203], v[248:251], v[232:235], v[200:203]\n\t" \
    "v_mfma_f32_16x16x32_bf16 v[216:219], v[84:87], v[236:239], v[216:219]\n\t" \
    "ds_read_b128 v[232:235], v252 offset:17408\n\t" \
    "v_mfma_f32_16x16x32_bf16 v[220:223], v[92:95], v[236:239], v[220:223]\n\t" \
    "v_mfma_f32_16x16x32_bf16 v[212:215], v[248:251], v[236:239], v[212:215]\n\t" \
    "v_mfma_f32_16x16x32_bf16 v[204:207], v[104:107], v[240:243], v[204:207]\n\t" \
    "v_xor_b32 v252, 0x3000, %[xa]\n\t" \
    "ds_read_b128 v[236:239], v252 offset:0\n\t" \
    "v_mfma_f32_16x16x32_bf16 v[208:211], v[112:115], v[240:243], v[208:211]\n\t" \
    "ds_read_b128 v[248:251], %[wl] offset:7168\n\t" \
    "v_mfma_f32_16x16x32_bf16 v[200:203], v[96:99], v[240:243], v[200:203]\n\t" \
    "s_waitcnt lgkmcnt(5)\n\t" \
    "v_mfma_f32_16x16x32_bf16 v[216:219], v[104:107], v[224:227], v[216:219]\n\t" \
    "ds_read_b128 v[240:243], v252 offset:16384\n\t" \
    "v_mfma_f32_16x16x32_bf16 v[220:223], v[112:115], v[224:227], v[220:223]\n\t" \
    "v_mfma_f32_16x16x32_bf16 v[212:215], v[96:99], v[224:227], v[212:215]\n\t" \
    "s_waitcnt lgkmcnt(5)\n\t" \
    "v_mfma_f32_16x16x32_bf16 v[204:207], v[108:111], v[228:231], v[204:207]\n\t" \
    "ds_read_b128 v[224:227], v252 offset:1024\n\t" \
    "v_mfma_f32_16x16x32_bf16 v[208:211], v[116:119], v[228:231], v[208:211]\n\t" \
    "v_mfma_f32_16x16x32_bf16 v[200:203], v[100:103], v[228:231], v[200:203]\n\t" \
    "s_waitcnt lgkmcnt(4)\n\t" \
    "v_mfma_f32_16x16x32_bf16 v[216:219], v[108:111], v[232:235], v[216:219]\n\t" \
    "ds_read_b128 v[228:231], v252 offset:17408\n\t" \
    "v_mfma_f32_16x16x32_bf16 v[220:223], v[116:119], v[232:235], v[220:223]\n\t" \
    "v_mfma_f32_16x16x32_bf16 v[212:215], v[100:103], v[232:235], v[212:215]\n\t" \
    "s_waitcnt lgkmcnt(4)\n\t" \
    "v_mfma_f32_16x16x32_bf16 v[204:207], v[120:123], v[236:239], v[204:207]\n\t" \
    "v_xor_b32 v252, 0x3800, %[xa]\n\t" \
    "ds_read_b128 v[232:235], v252 offset:0\n\t" \
    "v_mfma_f32_16x16x32_bf16 v[208:211], v[128:131], v[236:239], v[208:211]\n\t" \
    "v_mfma_f32_16x16x32_bf16 v[200:203], v[244:247], v[236:239], v[200:203]\n\t" \
    "s_waitcnt lgkmcnt(3)\n\t" \
    "v_mfma_f32_16x16x32_bf16 v[216:219], v[120:123], v[240:243], v[216:219]\n\t" \
    "ds_read_b128 v[236:239], v252 offset:16384\n\t" \
    "v_mfma_f32_16x16x32_bf16 v[220:223], v[128:131], v[240:243], v[220:223]\n\t" \
    "v_mfma_f32_16x16x32_bf16 v[212:215], v[244:247], v[240:243], v[212:215]\n\t" \
    "s_waitcnt lgkmcnt(3)\n\t" \
    "v_mfma_f32_16x16x32_bf16 v[204:207], v[124:127], v[224:227], v[204:207]\n\t" \
    "ds_read_b128 v[240:243], v252 offset:1024\n\t" \
    "v_mfma_f32_16x16x32_bf16 v[208:211], v[132:135], v[224:227], v[208:211]\n\t" \
    "v_mfma_f32_16x16x32_bf16 v[200:203], v[248:251], v[224:227], v[200:203]\n\t" \
    "s_waitcnt lgkmcnt(3)\n\t" \
    "v_mfma_f32_16x16x32_bf16 v[216:219], v[124:127], v[228:231], v[216:219]\n\t" \
    "ds_read_b128 v[224:227], v252 offset:17408\n\t" \
    "v_mfma_f32_16x16x32_bf16 v[220:223], v[132:135], v[228:231], v[220:223]\n\t" \
    "v_mfma_f32_16x16x32_bf16 v[212:215], v[248:251], v[228:231], v[212:215]\n\t" \
    "s_waitcnt lgkmcnt(3)\n\t" \
    "v_mfma_f32_16x16x32_bf16 v[204:207], v[144:147], v[232:235], v[204:207]\n\t" \
    "v_and_b32 v252, 0x3ff, %[xa]\n\t" \
    "ds_read_b128 v[228:231], v252 offset:43008\n\t" \
    "v_mov_b64 v[244:245], 0\n\t" \
    "v_mfma_f32_16x16x32_bf16 v[208:211], v[152:155], v[232:235], v[208:211]\n\t" \
    "v_mov_b64 v[246:247], 0\n\t" \
    "v_mfma_f32_16x16x32_bf16 v[200:203], v[136:139], v[232:235], v[200:203]\n\t" \
    "v_mov_b64 v[248:249], 0\n\t" \
    "s_waitcnt lgkmcnt(3)\n\t" \
    "v_mfma_f32_16x16x32_bf16 v[216:219], v[144:147], v[236:239], v[216:219]\n\t" \
    "ds_read_b128 v[232:235], v252 offset:44032\n\t" \
    "v_mov_b64 v[250:251], 0\n\t" \
    "v_mfma_f32_16x16x32_bf16 v[220:223], v[152:155], v[236:239], v[220:223]\n\t" \
    "v_mfma_f32_16x16x32_bf16 v[212:215], v[136:139], v[236:239], v[212:215]\n\t" \
    "s_waitcnt lgkmcnt(3)\n\t" \
    "v_mfma_f32_16x16x32_bf16 v[204:207], v[148:151], v[240:243], v[204:207]\n\t" \
    "ds_read_b128 v[236:239], v252 offset:47104\n\t" \
    "v_mfma_f32_16x16x32_bf16 v[208:211], v[156:159], v[240:243], v[208:211]\n\t" \
    "v_mfma_f32_16x16x32_bf16 v[200:203], v[140:143], v[240:243], v[200:203]\n\t" \
    "s_waitcnt lgkmcnt(3)\n\t" \
    "v_mfma_f32_16x16x32_bf16 v[216:219], v[148:151], v[224:227], v[216:219]\n\t" \
    "ds_read_b128 v[240:243], v252 offset:48128\n\t" \
    "v_mfma_f32_16x16x32_bf16 v[220:223], v[156:159], v[224:227], v[220:223]\n\t" \
    "v_mfma_f32_16x16x32_bf16 v[212:215], v[140:143], v[224:227], v[212:215]\n\t" \
    "s_waitcnt lgkmcnt(3)\n\t" \
    "v_mfma_f32_16x16x32_bf16 v[204:207], v[184:187], v[228:231], v[204:207]\n\t" \
    "v_add_u32 v252, %[qs], v252\n\t" \
    "v_mfma_f32_16x16x32_bf16 v[208:211], v[192:195], v[228:231], v[208:211]\n\t" \
    "v_mov_b64 v[224:225], 0\n\t" \
    "v_mfma_f32_16x16x32_bf16 v[200:203], v[176:179], v[228:231], v[200:203]\n\t" \
    "v_mov_b64 v[226:227], 0\n\t" \
    "s_waitcnt lgkmcnt(2)\n\t" \
    "v_mfma_f32_16x16x32_bf16 v[204:207], v[188:191], v[232:235], v[204:207]\n\t" \
    "v_mfma_f32_16x16x32_bf16 v[208:211], v[196:199], v[232:235], v[208:211]\n\t" \
    "v_mfma_f32_16x16x32_bf16 v[200:203], v[180:183], v[232:235], v[200:203]\n\t" \
    "s_waitcnt lgkmcnt(1)\n\t" \
    "v_mfma_f32_16x16x32_bf16 v[216:219], v[184:187], v[236:239], v[216:219]\n\t" \
    "ds_read_b128 v[228:231], %[wl] offset:2048\n\t" \
    "ds_read_b128 v[232:235], %[wl] offset:3072\n\t" \
    "v_mfma_f32_16x16x32_bf16 v[220:223], v[192:195], v[236:239], v[220:223]\n\t" \
    "v_cvt_pk_bf16_f32 v204, v204, v205\n\t" \
    "v_cvt_pk_bf16_f32 v205, v206, v207\n\t" \
    "v_mfma_f32_16x16x32_bf16 v[212:215], v[176:179], v[236:239], v[212:215]\n\t" \
    "ds_write_b64 v252, v[204:205] offset:1024\n\t" \
    "v_cvt_pk_bf16_f32 v208, v208, v209\n\t" \
    "s_waitcnt lgkmcnt(3)\n\t" \
    "v_mfma_f32_16x16x32_bf16 v[216:219], v[188:191], v[240:243], v[216:219]\n\t" \
    "v_cvt_pk_bf16_f32 v209, v210, v211\n\t" \
    "ds_write_b64 v252, v[208:209] offset:2048\n\t" \
    "v_mfma_f32_16x16x32_bf16 v[220:223], v[196:199], v[240:243], v[220:223]\n\t" \
    "v_cvt_pk_bf16_f32 v200, v200, v201\n\t" \
    "v_cvt_pk_bf16_f32 v201, v202, v203\n\t" \
    "v_mfma_f32_16x16x32_bf16 v[212:215], v[180:183], v[240:243], v[212:215]\n\t" \
    "ds_write_b64 v252, v[200:201] offset:0\n\t" \
    "ds_read_b128 v[200:203], %[wl] offset:0\n\t" \
    "ds_read_b128 v[204:207], %[wl] offset:1024\n\t" \
    "v_cvt_pk_bf16_f32 v216, v216, v217\n\t" \
    "v_cvt_pk_bf16_f32 v217, v218, v219\n\t" \
    "ds_write_b64 v252, v[216:217] offset:7168\n\t" \
    "v_cvt_pk_bf16_f32 v220, v220, v221\n\t" \
    "v_cvt_pk_bf16_f32 v221, v222, v223\n\t" \
    "ds_write_b64 v252, v[220:221] offset:8192\n\t" \
    "v_cvt_pk_bf16_f32 v212, v212, v213\n\t" \
    "v_cvt_pk_bf16_f32 v213, v214, v215\n\t" \
    "ds_write_b64 v252, v[212:213] offset:6144\n\t" \
    "s_waitcnt lgkmcnt(0)\n\t"
#define ASM_XG_S_HI \
    "s_waitcnt lgkmcnt(0)\n\t" \
    "v_xor_b32 v252, 0x2000, %[xa]\n\t" \
    "ds_read_b128 v[224:227], v252 offset:0\n\t" \
    "ds_read_b128 v[244:247], %[wl] offset:4096\n\t" \
    "ds_read_b128 v[228:231], v252 offset:16384\n\t" \
    "ds_read_b128 v[232:235], v252 offset:1024\n\t" \
    "ds_read_b128 v[236:239], v252 offset:17408\n\t" \
    "ds_read_b128 v[248:251], %[wl] offset:5120\n\t" \
    "s_cmp_eq_u32 %[slp], 0\n\t" \
    "s_cbranch_scc1 .Lxs%=\n\t" \
    "s_sleep 10\n\t" \
    ".Lxs%=:\n\t" \
    "s_waitcnt lgkmcnt(5)\n\t" \
    "v_mfma_f32_16x16x32_bf16 v[204:207], v[80:83], v[224:227], 0\n\t" \
    "v_xor_b32 v252, 0x2800, %[xa]\n\t" \
    "ds_read_b128 v[240:243], v252 offset:0\n\t" \
    "v_mfma_f32_16x16x32_bf16 v[208:211], v[88:91], v[224:227], 0\n\t" \
    "s_waitcnt lgkmcnt(5)\n\t" \
    "v_mfma_f32_16x16x32_bf16 v[200:203], v[244:247], v[224:227], 0\n\t" \
    "s_waitcnt lgkmcnt(4)\n\t" \
    "v_mfma_f32_16x16x32_bf16 v[216:219], v[80:83], v[228:231], 0\n\t" \
    "ds_read_b128 v[224:227], v252 offset:16384\n\t" \
    "v_mfma_f32_16x16x32_bf16 v[220:223], v[88:91], v[228:231], 0\n\t" \
    "v_mfma_f32_16x16x32_bf16 v[212:215], v[244:247], v[228:231], 0\n\t" \
    "s_waitcnt lgkmcnt(4)\n\t" \
    "v_mfma_f32_16x16x32_bf16 v[204:207], v[84:87], v[232:235], v[204:207]\n\t" \
    "ds_read_b128 v[228:231], v252 offset:1024\n\t" \
    "v_mfma_f32_16x16x32_bf16 v[208:211], v[92:95], v[232:235], v[208:211]\n\t" \
    "ds_read_b128 v[244:247], %[wl] offset:6144\n\t" \
    "s_waitcnt lgkmcnt(4)\n\t" \
    "v_mfma_f32_16x16x32_bf16 v[200:203], v[248:251], v[232:235], v[200:203]\n\t" \
    "v_mfma_f32_16x16x32_bf16 v[216:219], v[84:87], v[236:239], v[216:219]\n\t" \
    "ds_read_b128 v[232:235], v252 offset:17408\n\t" \
    "v_mfma_f32_16x16x32_bf16 v[220:223], v[92:95], v[236:239], v[220:223]\n\t" \
    "v_mfma_f32_16x16x32_bf16 v[212:215], v[248:251], v[236:239], v[212:215]\n\t" \
    "s_waitcnt lgkmcnt(4)\n\t" \
    "v_mfma_f32_16x16x32_bf16 v[204:207], v[104:107], v[240:243], v[204:207]\n\t" \
    "v_xor_b32 v252, 0x3000, %[xa]\n\t" \
    "ds_read_b128 v[236:239], v252 offset:0\n\t" \
    "v_mfma_f32_16x16x32_bf16 v[208:211], v[112:115], v[240:243], v[208:211]\n\t" \
    "ds_read_b128 v[248:251], %[wl] offset:7168\n\t" \
    "v_mfma_f32_16x16x32_bf16 v[200:203], v[96:99], v[240:243], v[200:203]\n\t" \
    "s_waitcnt lgkmcnt(5)\n\t" \
    "v_mfma_f32_16x16x32_bf16 v[216:219], v[104:107], v[224:227], v[216:219]\n\t" \
    "ds_read_b128 v[240:243], v252 offset:16384\n\t" \
    "v_mfma_f32_16x16x32_bf16 v[220:223], v[112:115], v[224:227], v[220:223]\n\t" \
    "v_mfma_f32_16x16x32_bf16 v[212:215], v[96:99], v[224:227], v[212:215]\n\t" \
    "s_waitcnt lgkmcnt(5)\n\t" \
    "v_mfma_f32_16x16x32_bf16 v[204:207], v[108:111], v[228:231], v[204:207]\n\t" \
    "ds_read_b128 v[224:227], v252 offset:1024\n\t" \
    "v_mfma_f32_16x16x32_bf16 v[208:211], v[116:119], v[228:231], v[208:211]\n\t" \
    "v_mfma_f32_16x16x32_bf16 v[200:203], v[100:103], v[228:231], v[200:203]\n\t" \
    "s_waitcnt lgkmcnt(4)\n\t" \
    "v_mfma_f32_16x16x32_bf16 v[216:219], v[108:111], v[232:235], v[216:219]\n\t" \
    "ds_read_b128 v[228:231], v252 offset:17408\n\t" \
    "v_mfma_f32_16x16x32_bf16 v[220:223], v[116:119], v[232:235], v[220:223]\n\t" \
    "v_mfma_f32_16x16x32_bf16 v[212:215], v[100:103], v[232:235], v[212:215]\n\t" \
    "s_waitcnt lgkmcnt(4)\n\t" \
    "v_mfma_f32_16x16x32_bf16 v[204:207], v[120:123], v[236:239], v[204:207]\n\t" \
    "v_xor_b32 v252, 0x3800, %[xa]\n\t" \
    "ds_read_b128 v[232:235], v252 offset:0\n\t" \
    "v_mfma_f32_16x16x32_bf16 v[208:211], v[128:131], v[236:239], v[208:211]\n\t" \
    "v_mfma_f32_16x16x32_bf16 v[200:203], v[244:247], v[236:239], v[200:203]\n\t" \
    "s_waitcnt lgkmcnt(3)\n\t" \
    "v_mfma_f32_16x16x32_bf16 v[216:219], v[120:123], v[240:243], v[216:219]\n\t" \
    "ds_read_b128 v[236:239], v252 offset:16384\n\t" \
    "v_mfma_f32_16x16x32_bf16 v[220:223], v[128:131], v[240:243], v[220:223]\n\t" \
    "v_mfma_f32_16x16x32_bf16 v[212:215], v[244:247], v[240:243], v[212:215]\n\t" \
    "s_waitcnt lgkmcnt(3)\n\t" \
    "v_mfma_f32_16x16x32_bf16 v[204:207], v[124:127], v[224:227], v[204:207]\n\t" \
    "ds_read_b128 v[240:243], v252 offset:1024\n\t" \
    "v_mfma_f32_16x16x32_bf16 v[208:211], v[132:135], v[224:227], v[208:211]\n\t" \
    "ds_read_b128 v[244:247], %[wl] offset:8192\n\t" \
    "v_mfma_f32_16x16x32_bf16 v[200:203], v[248:251], v[224:227], v[200:203]\n\t" \
    "s_waitcnt lgkmcnt(4)\n\t" \
    "v_mfma_f32_16x16x32_bf16 v[216:219], v[124:127], v[228:231], v[216:219]\n\t" \
    "ds_read_b128 v[224:227], v252 offset:17408\n\t" \
    "v_mfma_f32_16x16x32_bf16 v[220:223], v[132:135], v[228:231], v[220:223]\n\t" \
    "v_mfma_f32_16x16x32_bf16 v[212:215], v[248:251], v[228:231], v[212:215]\n\t" \
    "s_waitcnt lgkmcnt(4)\n\t" \
    "v_mfma_f32_16x16x32_bf16 v[204:207], v[144:147], v[232:235], v[204:207]\n\t" \
    "v_and_b32 v252, 0x3ff, %[xa]\n\t" \
    "ds_read_b128 v[228:231], v252 offset:40960\n\t" \
    "v_mfma_f32_16x16x32_bf16 v[208:211], v[152:155], v[232:235], v[208:211]\n\t" \
    "ds_read_b128 v[248:251], %[wl] offset:9216\n\t" \
    "v_mfma_f32_16x16x32_bf16 v[200:203], v[136:139], v[232:235], v[200:203]\n\t" \
    "s_waitcnt lgkmcnt(5)\n\t" \
    "v_mfma_f32_16x16x32_bf16 v[216:219], v[144:147], v[236:239], v[216:219]\n\t" \
    "ds_read_b128 v[232:235], v252 offset:45056\n\t" \
    "v_mfma_f32_16x16x32_bf16 v[220:223], v[152:155], v[236:239], v[220:223]\n\t" \
    "v_mfma_f32_16x16x32_bf16 v[212:215], v[136:139], v[236:239], v[212:215]\n\t" \
    "s_waitcnt lgkmcnt(5)\n\t" \
    "v_mfma_f32_16x16x32_bf16 v[204:207], v[148:151], v[240:243], v[204:207]\n\t" \
    "ds_read_b128 v[236:239], v252 offset:41984\n\t" \
    "v_mfma_f32_16x16x32_bf16 v[208:211], v[156:159], v[240:243], v[208:211]\n\t" \
    "v_mfma_f32_16x16x32_bf16 v[200:203], v[140:143], v[240:243], v[200:203]\n\t" \
    "s_waitcnt lgkmcnt(4)\n\t" \
    "v_mfma_f32_16x16x32_bf16 v[216:219], v[148:151], v[224:227], v[216:219]\n\t" \
    "ds_read_b128 v[240:243], v252 offset:46080\n\t" \
    "v_mfma_f32_16x16x32_bf16 v[220:223], v[156:159], v[224:227], v[220:223]\n\t" \
    "v_mfma_f32_16x16x32_bf16 v[212:215], v[140:143], v[224:227], v[212:215]\n\t" \
    "s_waitcnt vmcnt(0) lgkmcnt(0)\n\t" \
    "s_barrier\n\t" ASM_ST \
    "s_mov_b32 m0, %[d0]\n\t" \
    "s_nop 0\n\t" \
    "global_load_lds_dwordx4 v252, %[g0] offset:0 sc1\n\t" \
    "global_load_lds_dwordx4 v252, %[g0] offset:1024 sc1\n\t" \
    "global_load_lds_dwordx4 v252, %[g0] offset:2048 sc1\n\t" \
    "global_load_lds_dwordx4 v252, %[g0] offset:3072 sc1\n\t" \
    "s_mov_b32 m0, %[d1]\n\t" \
    "s_nop 0\n\t" \
    "global_load_lds_dwordx4 v252, %[g1] sc1\n\t" \
    "v_mfma_f32_16x16x32_bf16 v[204:207], v[160:163], v[228:231], v[204:207]\n\t" \
    "ds_read_b128 v[224:227], %[xa] offset:0\n\t" \
    "v_mfma_f32_16x16x32_bf16 v[208:211], v[168:171], v[228:231], v[208:211]\n\t" \
    "v_mfma_f32_16x16x32_bf16 v[200:203], v[244:247], v[228:231], v[200:203]\n\t" \
    "v_mfma_f32_16x16x32_bf16 v[216:219], v[160:163], v[232:235], v[216:219]\n\t" \
    "ds_read_b128 v[228:231], %[xa] offset:16384\n\t" \
    "v_mfma_f32_16x16x32_bf16 v[220:223], v[168:171], v[232:235], v[220:223]\n\t" \
    "v_mfma_f32_16x16x32_bf16 v[212:215], v[244:247], v[232:235], v[212:215]\n\t" \
    "v_mfma_f32_16x16x32_bf16 v[204:207], v[164:167], v[236:239], v[204:207]\n\t" \
    "ds_read_b128 v[232:235], %[xa] offset:1024\n\t" \
    "v_mfma_f32_16x16x32_bf16 v[208:211], v[172:175], v[236:239], v[208:211]\n\t" \
    "ds_read_b128 v[244:247], %[wl] offset:0\n\t" \
    "v_mfma_f32_16x16x32_bf16 v[200:203], v[248:251], v[236:239], v[200:203]\n\t" \
    "v_mfma_f32_16x16x32_bf16 v[216:219], v[164:167], v[240:243], v[216:219]\n\t" \
    "ds_read_b128 v[236:239], %[xa] offset:17408\n\t" \
    "v_mfma_f32_16x16x32_bf16 v[220:223], v[172:175], v[240:243], v[220:223]\n\t" \
    "v_mfma_f32_16x16x32_bf16 v[212:215], v[248:251], v[240:243], v[212:215]\n\t" \
    "s_waitcnt lgkmcnt(4)\n\t" \
    "v_mfma_f32_16x16x32_bf16 v[204:207], v[0:3], v[224:227], v[204:207]\n\t" \
    "v_xor_b32 v252, 0x800, %[xa]\n\t" \
    "ds_read_b128 v[240:243], v252 offset:0\n\t" \
    "v_mfma_f32_16x16x32_bf16 v[208:211], v[8:11], v[224:227], v[208:211]\n\t" \
    "ds_read_b128 v[248:251], %[wl] offset:1024\n\t" \
    "s_waitcnt lgkmcnt(3)\n\t" \
    "v_mfma_f32_16x16x32_bf16 v[200:203], v[244:247], v[224:227], v[200:203]\n\t" \
    "v_mfma_f32_16x16x32_bf16 v[216:219], v[0:3], v[228:231], v[216:219]\n\t" \
    "ds_read_b128 v[224:227], v252 offset:16384\n\t" \
    "v_mfma_f32_16x16x32_bf16 v[220:223], v[8:11], v[228:231], v[220:223]\n\t" \
    "v_mfma_f32_16x16x32_bf16 v[212:215], v[244:247], v[228:231], v[212:215]\n\t" \
    "v_mfma_f32_16x16x32_bf16 v[204:207], v[4:7], v[232:235], v[204:207]\n\t" \
    "ds_read_b128 v[228:231], v252 offset:1024\n\t" \
    "v_mfma_f32_16x16x32_bf16 v[208:211], v[12:15], v[232:235], v[208:211]\n\t" \
    "ds_read_b128 v[244:247], %[wl] offset:2048\n\t" \
    "s_waitcnt lgkmcnt(3)\n\t" \
    "v_mfma_f32_16x16x32_bf16 v[200:203], v[248:251], v[232:235], v[200:203]\n\t" \
    "v_mfma_f32_16x16x32_bf16 v[216:219], v[4:7], v[236:239], v[216:219]\n\t" \
    "ds_read_b128 v[232:235], v252 offset:17408\n\t" \
    "v_mfma_f32_16x16x32_bf16 v[220:223], v[12:15], v[236:239], v[220:223]\n\t" \
    "v_mfma_f32_16x16x32_bf16 v[212:215], v[248:251], v[236:239], v[212:215]\n\t" \
    "v_mfma_f32_16x16x32_bf16 v[204:207], v[24:27], v[240:243], v[204:207]\n\t" \
    "v_xor_b32 v252, 0x1000, %[xa]\n\t" \
    "ds_read_b128 v[236:239], v252 offset:0\n\t" \
    "v_mfma_f32_16x16x32_bf16 v[208:211], v[32:35], v[240:243], v[208:211]\n\t" \
    "ds_read_b128 v[248:251], %[wl] offset:3072\n\t" \
    "v_mfma_f32_16x16x32_bf16 v[200:203], v[16:19], v[240:243], v[200:203]\n\t" \
    "s_waitcnt lgkmcnt(5)\n\t" \
    "v_mfma_f32_16x16x32_bf16 v[216:219], v[24:27], v[224:227], v[216:219]\n\t" \
    "ds_read_b128 v[240:243], v252 offset:16384\n\t" \
    "v_mfma_f32_16x16x32_bf16 v[220:223], v[32:35], v[224:227], v[220:223]\n\t" \
    "v_mfma_f32_16x16x32_bf16 v[212:215], v[16:19], v[224:227], v[212:215]\n\t" \
    "s_waitcnt lgkmcnt(5)\n\t" \
    "v_mfma_f32_16x16x32_bf16 v[204:207], v[28:31], v[228:231], v[204:207]\n\t" \
    "ds_read_b128 v[224:227], v252 offset:1024\n\t" \
    "v_mfma_f32_16x16x32_bf16 v[208:211], v[36:39], v[228:231], v[208:211]\n\t" \
    "v_mfma_f32_16x16x32_bf16 v[200:203], v[20:23], v[228:231], v[200:203]\n\t" \
    "s_waitcnt lgkmcnt(4)\n\t" \
    "v_mfma_f32_16x16x32_bf16 v[216:219], v[28:31], v[232:235], v[216:219]\n\t" \
    "ds_read_b128 v[228:231], v252 offset:17408\n\t" \
    "v_mfma_f32_16x16x32_bf16 v[220:223], v[36:39], v[232:235], v[220:223]\n\t" \
    "v_mfma_f32_16x16x32_bf16 v[212:215], v[20:23], v[232:235], v[212:215]\n\t" \
    "s_waitcnt lgkmcnt(4)\n\t" \
    "v_mfma_f32_16x16x32_bf16 v[204:207], v[40:43], v[236:239], v[204:207]\n\t" \
    "v_xor_b32 v252, 0x1800, %[xa]\n\t" \
    "ds_read_b128 v[232:235], v252 offset:0\n\t" \
    "v_mfma_f32_16x16x32_bf16 v[208:211], v[48:51], v[236:239], v[208:211]\n\t" \
    "v_mfma_f32_16x16x32_bf16 v[200:203], v[244:247], v[236:239], v[200:203]\n\t" \
    "s_waitcnt lgkmcnt(3)\n\t" \
    "v_mfma_f32_16x16x32_bf16 v[216:219], v[40:43], v[240:243], v[216:219]\n\t" \
    "ds_read_b128 v[236:239], v252 offset:16384\n\t" \
    "v_mfma_f32_16x16x32_bf16 v[220:223], v[48:51], v[240:243], v[220:223]\n\t" \
    "v_mfma_f32_16x16x32_bf16 v[212:215], v[244:247], v[240:243], v[212:215]\n\t" \
    "s_waitcnt lgkmcnt(3)\n\t" \
    "v_mfma_f32_16x16x32_bf16 v[204:207], v[44:47], v[224:227], v[204:207]\n\t" \
    "ds_read_b128 v[240:243], v252 offset:1024\n\t" \
    "v_mfma_f32_16x16x32_bf16 v[208:211], v[52:55], v[224:227], v[208:211]\n\t" \
    "v_mfma_f32_16x16x32_bf16 v[200:203], v[248:251], v[224:227], v[200:203]\n\t" \
    "s_waitcnt lgkmcnt(3)\n\t" \
    "v_mfma_f32_16x16x32_bf16 v[216:219], v[44:47], v[228:231], v[216:219]\n\t" \
    "ds_read_b128 v[224:227], v252 offset:17408\n\t" \
    "v_mfma_f32_16x16x32_bf16 v[220:223], v[52:55], v[228:231], v[220:223]\n\t" \
    "v_mfma_f32_16x16x32_bf16 v[212:215], v[248:251], v[228:231], v[212:215]\n\t" \
    "s_waitcnt lgkmcnt(3)\n\t" \
    "v_mfma_f32_16x16x32_bf16 v[204:207], v[64:67], v[232:235], v[204:207]\n\t" \
    "v_and_b32 v252, 0x3ff, %[xa]\n\t" \
    "ds_read_b128 v[228:231], v252 offset:43008\n\t" \
    "v_mov_b64 v[244:245], 0\n\t" \
    "v_mfma_f32_16x16x32_bf16 v[208:211], v[72:75], v[232:235], v[208:211]\n\t" \
    "v_mov_b64 v[246:247], 0\n\t" \
    "v_mfma_f32_16x16x32_bf16 v[200:203], v[56:59], v[232:235], v[200:203]\n\t" \
    "v_mov_b64 v[248:249], 0\n\t" \
    "s_waitcnt lgkmcnt(3)\n\t" \
    "v_mfma_f32_16x16x32_bf16 v[216:219], v[64:67], v[236:239], v[216:219]\n\t" \
    "ds_read_b128 v[232:235], v252 offset:44032\n\t" \
    "v_mov_b64 v[250:251], 0\n\t" \
    "v_mfma_f32_16x16x32_bf16 v[220:223], v[72:75], v[236:239], v[220:223]\n\t" \
    "v_mfma_f32_16x16x32_bf16 v[212:215], v[56:59], v[236:239], v[212:215]\n\t" \
    "s_waitcnt lgkmcnt(3)\n\t" \
    "v_mfma_f32_16x16x32_bf16 v[204:207], v[68:71], v[240:243], v[204:207]\n\t" \
    "ds_read_b128 v[236:239], v252 offset:47104\n\t" \
    "v_mfma_f32_16x16x32_bf16 v[208:211], v[76:79], v[240:243], v[208:211]\n\t" \
    "v_mfma_f32_16x16x32_bf16 v[200:203], v[60:63], v[240:243], v[200:203]\n\t" \
    "s_waitcnt lgkmcnt(3)\n\t" \
    "v_mfma_f32_16x16x32_bf16 v[216:219], v[68:71], v[224:227], v[216:219]\n\t" \
    "ds_read_b128 v[240:243], v252 offset:48128\n\t" \
    "v_mfma_f32_16x16x32_bf16 v[220:223], v[76:79], v[224:227], v[220:223]\n\t" \
    "v_mfma_f32_16x16x32_bf16 v[212:215], v[60:63], v[224:227], v[212:215]\n\t" \
    "s_waitcnt lgkmcnt(3)\n\t" \
    "v_mfma_f32_16x16x32_bf16 v[204:207], v[184:187], v[228:231], v[204:207]\n\t" \
    "v_add_u32 v252, %[qs], v252\n\t" \
    "v_mfma_f32_16x16x32_bf16 v[208:211], v[192:195], v[228:231], v[208:211]\n\t" \
    "v_mov_b64 v[224:225], 0\n\t" \
    "v_mfma_f32_16x16x32_bf16 v[200:203], v[176:179], v[228:231], v[200:203]\n\t" \
    "v_mov_b64 v[226:227], 0\n\t" \
    "s_waitcnt lgkmcnt(2)\n\t" \
    "v_mfma_f32_16x16x32_bf16 v[204:207], v[188:191], v[232:235], v[204:207]\n\t" \
    "v_mfma_f32_16x16x32_bf16 v[208:211], v[196:199], v[232:235], v[208:211]\n\t" \
    "v_mfma_f32_16x16x32_bf16 v[200:203], v[180:183], v[232:235], v[200:203]\n\t" \
    "s_waitcnt lgkmcnt(1)\n\t" \
    "v_mfma_f32_16x16x32_bf16 v[216:219], v[184:187], v[236:239], v[216:219]\n\t" \
    "ds_read_b128 v[228:231], %[wl] offset:2048\n\t" \
    "ds_read_b128 v[232:235], %[wl] offset:3072\n\t" \
    "v_mfma_f32_16x16x32_bf16 v[220:223], v[192:195], v[236:239], v[220:223]\n\t" \
    "v_cvt_pk_bf16_f32 v204, v204, v205\n\t" \
    "v_cvt_pk_bf16_f32 v205, v206, v207\n\t" \
    "v_mfma_f32_16x16x32_bf16 v[212:215], v[176:179], v[236:239], v[212:215]\n\t" \
    "ds_write_b64 v252, v[204:205] offset:1024\n\t" \
    "v_cvt_pk_bf16_f32 v208, v208, v209\n\t" \
    "s_waitcnt lgkmcnt(3)\n\t" \
    "v_mfma_f32_16x16x32_bf16 v[216:219], v[188:191], v[240:243], v[216:219]\n\t" \
    "v_cvt_pk_bf16_f32 v209, v210, v211\n\t" \
    "ds_write_b64 v252, v[208:209] offset:2048\n\t" \
    "v_mfma_f32_16x16x32_bf16 v[220:223], v[196:199], v[240:243], v[220:223]\n\t" \
    "v_cvt_pk_bf16_f32 v200, v200, v201\n\t" \
    "v_cvt_pk_bf16_f32 v201, v202, v203\n\t" \
    "v_mfma_f32_16x16x32_bf16 v[212:215], v[180:183], v[240:243], v[212:215]\n\t" \
    "ds_write_b64 v252, v[200:201] offset:0\n\t" \
    "ds_read_b128 v[200:203], %[wl] offset:0\n\t" \
    "ds_read_b128 v[204:207], %[wl] offset:1024\n\t" \
    "v_cvt_pk_bf16_f32 v216, v216, v217\n\t" \
    "v_cvt_pk_bf16_f32 v217, v218, v219\n\t" \
    "ds_write_b64 v252, v[216:217] offset:7168\n\t" \
    "v_cvt_pk_bf16_f32 v220, v220, v221\n\t" \
    "v_cvt_pk_bf16_f32 v221, v222, v223\n\t" \
    "ds_write_b64 v252, v[220:221] offset:8192\n\t" \
    "v_cvt_pk_bf16_f32 v212, v212, v213\n\t" \
    "v_cvt_pk_bf16_f32 v213, v214, v215\n\t" \
    "ds_write_b64 v252, v[212:213] offset:6144\n\t" \
    "s_waitcnt lgkmcnt(0)\n\t"
#define ASM_XG_S_LO2 \
    "s_waitcnt lgkmcnt(0)\n\t" \
    "ds_read_b128 v[224:227], %[xa] offset:0\n\t" \
    "ds_read_b128 v[228:231], %[xa] offset:16384\n\t" \
    "ds_read_b128 v[232:235], %[xa] offset:1024\n\t" \
    "ds_read_b128 v[236:239], %[xa] offset:17408\n\t" \
    "s_waitcnt lgkmcnt(3)\n\t" \
    "v_mfma_f32_16x16x32_bf16 v[204:207], v[0:3], v[224:227], 0\n\t" \
    "v_xor_b32 v252, 0x800, %[xa]\n\t" \
    "ds_read_b128 v[240:243], v252 offset:0\n\t" \
    "v_mfma_f32_16x16x32_bf16 v[208:211], v[8:11], v[224:227], 0\n\t" \
    "s_waitcnt lgkmcnt(3)\n\t" \
    "v_mfma_f32_16x16x32_bf16 v[216:219], v[0:3], v[228:231], 0\n\t" \
    "ds_read_b128 v[224:227], v252 offset:16384\n\t" \
    "v_mfma_f32_16x16x32_bf16 v[220:223], v[8:11], v[228:231], 0\n\t" \
    "s_waitcnt lgkmcnt(3)\n\t" \
    "v_mfma_f32_16x16x32_bf16 v[204:207], v[4:7], v[232:235], v[204:207]\n\t" \
    "ds_read_b128 v[228:231], v252 offset:1024\n\t" \
    "v_mfma_f32_16x16x32_bf16 v[208:211], v[12:15], v[232:235], v[208:211]\n\t" \
    "s_waitcnt lgkmcnt(3)\n\t" \
    "v_mfma_f32_16x16x32_bf16 v[216:219], v[4:7], v[236:239], v[216:219]\n\t" \
    "ds_read_b128 v[232:235], v252 offset:17408\n\t" \
    "v_mfma_f32_16x16x32_bf16 v[220:223], v[12:15], v[236:239], v[220:223]\n\t" \
    "s_waitcnt lgkmcnt(3)\n\t" \
    "v_mfma_f32_16x16x32_bf16 v[204:207], v[24:27], v[240:243], v[204:207]\n\t" \
    "v_xor_b32 v252, 0x1000, %[xa]\n\t" \
    "ds_read_b128 v[236:239], v252 offset:0\n\t" \
    "v_mfma_f32_16x16x32_bf16 v[208:211], v[32:35], v[240:243], v[208:211]\n\t" \
    "s_waitcnt lgkmcnt(3)\n\t" \
    "v_mfma_f32_16x16x32_bf16 v[216:219], v[24:27], v[224:227], v[216:219]\n\t" \
    "ds_read_b128 v[240:243], v252 offset:16384\n\t" \
    "v_mfma_f32_16x16x32_bf16 v[220:223], v[32:35], v[224:227], v[220:223]\n\t" \
    "s_waitcnt lgkmcnt(3)\n\t" \
    "v_mfma_f32_16x16x32_bf16 v[204:207], v[28:31], v[228:231], v[204:207]\n\t" \
    "ds_read_b128 v[224:227], v252 offset:1024\n\t" \
    "v_mfma_f32_16x16x32_bf16 v[208:211], v[36:39], v[228:231], v[208:211]\n\t" \
    "s_waitcnt lgkmcnt(3)\n\t" \
    "v_mfma_f32_16x16x32_bf16 v[216:219], v[28:31], v[232:235], v[216:219]\n\t" \
    "ds_read_b128 v[228:231], v252 offset:17408\n\t" \
    "v_mfma_f32_16x16x32_bf16 v[220:223], v[36:39], v[232:235], v[220:223]\n\t" \
    "s_waitcnt lgkmcnt(3)\n\t" \
    "v_mfma_f32_16x16x32_bf16 v[204:207], v[40:43], v[236:239], v[204:207]\n\t" \
    "v_xor_b32 v252, 0x1800, %[xa]\n\t" \
    "ds_read_b128 v[232:235], v252 offset:0\n\t" \
    "v_mfma_f32_16x16x32_bf16 v[208:211], v[48:51], v[236:239], v[208:211]\n\t" \
    "s_waitcnt lgkmcnt(3)\n\t" \
    "v_mfma_f32_16x16x32_bf16 v[216:219], v[40:43], v[240:243], v[216:219]\n\t" \
    "ds_read_b128 v[236:239], v252 offset:16384\n\t" \
    "v_mfma_f32_16x16x32_bf16 v[220:223], v[48:51], v[240:243], v[220:223]\n\t" \
    "s_waitcnt lgkmcnt(3)\n\t" \
    "v_mfma_f32_16x16x32_bf16 v[204:207], v[44:47], v[224:227], v[204:207]\n\t" \
    "ds_read_b128 v[240:243], v252 offset:1024\n\t" \
    "v_mfma_f32_16x16x32_bf16 v[208:211], v[52:55], v[224:227], v[208:211]\n\t" \
    "s_waitcnt lgkmcnt(3)\n\t" \
    "v_mfma_f32_16x16x32_bf16 v[216:219], v[44:47], v[228:231], v[216:219]\n\t" \
    "ds_read_b128 v[224:227], v252 offset:17408\n\t" \
    "v_mfma_f32_16x16x32_bf16 v[220:223], v[52:55], v[228:231], v[220:223]\n\t" \
    "s_waitcnt lgkmcnt(3)\n\t" \
    "v_mfma_f32_16x16x32_bf16 v[204:207], v[64:67], v[232:235], v[204:207]\n\t" \
    "v_and_b32 v252, 0x3ff, %[xa]\n\t" \
    "ds_read_b128 v[228:231], v252 offset:40960\n\t" \
    "v_mfma_f32_16x16x32_bf16 v[208:211], v[72:75], v[232:235], v[208:211]\n\t" \
    "s_waitcnt lgkmcnt(3)\n\t" \
    "v_mfma_f32_16x16x32_bf16 v[216:219], v[64:67], v[236:239], v[216:219]\n\t" \
    "ds_read_b128 v[232:235], v252 offset:45056\n\t" \
    "v_mfma_f32_16x16x32_bf16 v[220:223], v[72:75], v[236:239], v[220:223]\n\t" \
    "s_waitcnt lgkmcnt(3)\n\t" \
    "v_mfma_f32_16x16x32_bf16 v[204:207], v[68:71], v[240:243], v[204:207]\n\t" \
    "ds_read_b128 v[236:239], v252 offset:41984\n\t" \
    "v_mfma_f32_16x16x32_bf16 v[208:211], v[76:79], v[240:243], v[208:211]\n\t" \
    "s_waitcnt lgkmcnt(3)\n\t" \
    "v_mfma_f32_16x16x32_bf16 v[216:219], v[68:71], v[224:227], v[216:219]\n\t" \
    "ds_read_b128 v[240:243], v252 offset:46080\n\t" \
    "v_mfma_f32_16x16x32_bf16 v[220:223], v[76:79], v[224:227], v[220:223]\n\t" \
    "s_waitcnt lgkmcnt(0)\n\t" \
    "s_barrier\n\t" ASM_ST \
    "v_mfma_f32_16x16x32_bf16 v[204:207], v[160:163], v[228:231], v[204:207]\n\t" \
    "v_xor_b32 v252, 0x2000, %[xa]\n\t" \
    "ds_read_b128 v[224:227], v252 offset:0\n\t" \
    "v_mfma_f32_16x16x32_bf16 v[208:211], v[168:171], v[228:231], v[208:211]\n\t" \
    "v_mfma_f32_16x16x32_bf16 v[216:219], v[160:163], v[232:235], v[216:219]\n\t" \
    "ds_read_b128 v[228:231], v252 offset:16384\n\t" \
    "v_mfma_f32_16x16x32_bf16 v[220:223], v[168:171], v[232:235], v[220:223]\n\t" \
    "v_mfma_f32_16x16x32_bf16 v[204:207], v[164:167], v[236:239], v[204:207]\n\t" \
    "ds_read_b128 v[232:235], v252 offset:1024\n\t" \
    "v_mfma_f32_16x16x32_bf16 v[208:211], v[172:175], v[236:239], v[208:211]\n\t" \
    "v_mfma_f32_16x16x32_bf16 v[216:219], v[164:167], v[240:243], v[216:219]\n\t" \
    "ds_read_b128 v[236:239], v252 offset:17408\n\t" \
    "v_mfma_f32_16x16x32_bf16 v[220:223], v[172:175], v[240:243], v[220:223]\n\t" \
    "s_waitcnt lgkmcnt(3)\n\t" \
    "v_mfma_f32_16x16x32_bf16 v[204:207], v[80:83], v[224:227], v[204:207]\n\t" \
    "v_xor_b32 v252, 0x2800, %[xa]\n\t" \
    "ds_read_b128 v[240:243], v252 offset:0\n\t" \
    "v_mfma_f32_16x16x32_bf16 v[208:211], v[88:91], v[224:227], v[208:211]\n\t" \
    "s_waitcnt lgkmcnt(3)\n\t" \
    "v_mfma_f32_16x16x32_bf16 v[216:219], v[80:83], v[228:231], v[216:219]\n\t" \
    "ds_read_b128 v[224:227], v252 offset:16384\n\t" \
    "v_mfma_f32_16x16x32_bf16 v[220:223], v[88:91], v[228:231], v[220:223]\n\t" \
    "s_waitcnt lgkmcnt(3)\n\t" \
    "v_mfma_f32_16x16x32_bf16 v[204:207], v[84:87], v[232:235], v[204:207]\n\t" \
    "ds_read_b128 v[228:231], v252 offset:1024\n\t" \
    "v_mfma_f32_16x16x32_bf16 v[208:211], v[92:95], v[232:235], v[208:211]\n\t" \
    "s_waitcnt lgkmcnt(3)\n\t" \
    "v_mfma_f32_16x16x32_bf16 v[216:219], v[84:87], v[236:239], v[216:219]\n\t" \
    "ds_read_b128 v[232:235], v252 offset:17408\n\t" \
    "v_mfma_f32_16x16x32_bf16 v[220:223], v[92:95], v[236:239], v[220:223]\n\t" \
    "s_waitcnt lgkmcnt(3)\n\t" \
    "v_mfma_f32_16x16x32_bf16 v[204:207], v[104:107], v[240:243], v[204:207]\n\t" \
    "v_xor_b32 v252, 0x3000, %[xa]\n\t" \
    "ds_read_b128 v[236:239], v252 offset:0\n\t" \
    "v_mfma_f32_16x16x32_bf16 v[208:211], v[112:115], v[240:243], v[208:211]\n\t" \
    "s_waitcnt lgkmcnt(3)\n\t" \
    "v_mfma_f32_16x16x32_bf16 v[216:219], v[104:107], v[224:227], v[216:219]\n\t" \
    "ds_read_b128 v[240:243], v252 offset:16384\n\t" \
    "v_mfma_f32_16x16x32_bf16 v[220:223], v[112:115], v[224:227], v[220:223]\n\t" \
    "s_waitcnt lgkmcnt(3)\n\t" \
    "v_mfma_f32_16x16x32_bf16 v[204:207], v[108:111], v[228:231], v[204:207]\n\t" \
    "ds_read_b128 v[224:227], v252 offset:1024\n\t" \
    "v_mfma_f32_16x16x32_bf16 v[208:211], v[116:119], v[228:231], v[208:211]\n\t" \
    "s_waitcnt lgkmcnt(3)\n\t" \
    "v_mfma_f32_16x16x32_bf16 v[216:219], v[108:111], v[232:235], v[216:219]\n\t" \
    "ds_read_b128 v[228:231], v252 offset:17408\n\t" \
    "v_mfma_f32_16x16x32_bf16 v[220:223], v[116:119], v[232:235], v[220:223]\n\t" \
    "s_waitcnt lgkmcnt(3)\n\t" \
    "v_mfma_f32_16x16x32_bf16 v[204:207], v[120:123], v[236:239], v[204:207]\n\t" \
    "v_xor_b32 v252, 0x3800, %[xa]\n\t" \
    "ds_read_b128 v[232:235], v252 offset:0\n\t" \
    "v_mfma_f32_16x16x32_bf16 v[208:211], v[128:131], v[236:239], v[208:211]\n\t" \
    "s_waitcnt lgkmcnt(3)\n\t" \
    "v_mfma_f32_16x16x32_bf16 v[216:219], v[120:123], v[240:243], v[216:219]\n\t" \
    "ds_read_b128 v[236:239], v252 offset:16384\n\t" \
    "v_mfma_f32_16x16x32_bf16 v[220:223], v[128:131], v[240:243], v[220:223]\n\t" \
    "s_waitcnt lgkmcnt(3)\n\t" \
    "v_mfma_f32_16x16x32_bf16 v[204:207], v[124:127], v[224:227], v[204:207]\n\t" \
    "ds_read_b128 v[240:243], v252 offset:1024\n\t" \
    "v_mfma_f32_16x16x32_bf16 v[208:211], v[132:135], v[224:227], v[208:211]\n\t" \
    "s_waitcnt lgkmcnt(3)\n\t" \
    "v_mfma_f32_16x16x32_bf16 v[216:219], v[124:127], v[228:231], v[216:219]\n\t" \
    "ds_read_b128 v[224:227], v252 offset:17408\n\t" \
    "v_mfma_f32_16x16x32_bf16 v[220:223], v[132:135], v[228:231], v[220:223]\n\t" \
    "s_waitcnt lgkmcnt(3)\n\t" \
    "v_mfma_f32_16x16x32_bf16 v[204:207], v[144:147], v[232:235], v[204:207]\n\t" \
    "v_and_b32 v252, 0x3ff, %[xa]\n\t" \
    "ds_read_b128 v[228:231], v252 offset:43008\n\t" \
    "v_mov_b64 v[244:245], 0\n\t" \
    "v_mfma_f32_16x16x32_bf16 v[208:211], v[152:155], v[232:235], v[208:211]\n\t" \
    "v_mov_b64 v[246:247], 0\n\t" \
    "s_waitcnt lgkmcnt(3)\n\t" \
    "v_mfma_f32_16x16x32_bf16 v[216:219], v[144:147], v[236:239], v[216:219]\n\t" \
    "ds_read_b128 v[232:235], v252 offset:44032\n\t" \
    "v_mov_b64 v[248:249], 0\n\t" \
    "v_mfma_f32_16x16x32_bf16 v[220:223], v[152:155], v[236:239], v[220:223]\n\t" \
    "v_mov_b64 v[250:251], 0\n\t" \
    "s_waitcnt lgkmcnt(3)\n\t" \
    "v_mfma_f32_16x16x32_bf16 v[204:207], v[148:151], v[240:243], v[204:207]\n\t" \
    "ds_read_b128 v[236:239], v252 offset:47104\n\t" \
    "v_mfma_f32_16x16x32_bf16 v[208:211], v[156:159], v[240:243], v[208:211]\n\t" \
    "s_waitcnt lgkmcnt(3)\n\t" \
    "v_mfma_f32_16x16x32_bf16 v[216:219], v[148:151], v[224:227], v[216:219]\n\t" \
    "ds_read_b128 v[240:243], v252 offset:48128\n\t" \
    "v_mfma_f32_16x16x32_bf16 v[220:223], v[156:159], v[224:227], v[220:223]\n\t" \
    "s_waitcnt lgkmcnt(3)\n\t" \
    "v_mfma_f32_16x16x32_bf16 v[204:207], v[184:187], v[228:231], v[204:207]\n\t" \
    "v_add_u32 v252, %[qs], v252\n\t" \
    "v_mfma_f32_16x16x32_bf16 v[208:211], v[192:195], v[228:231], v[208:211]\n\t" \
    "v_mov_b64 v[224:225], 0\n\t" \
    "s_waitcnt lgkmcnt(2)\n\t" \
    "v_mfma_f32_16x16x32_bf16 v[204:207], v[188:191], v[232:235], v[204:207]\n\t" \
    "v_mov_b64 v[226:227], 0\n\t" \
    "v_mfma_f32_16x16x32_bf16 v[208:211], v[196:199], v[232:235], v[208:211]\n\t" \
    "s_waitcnt lgkmcnt(1)\n\t" \
    "v_mfma_f32_16x16x32_bf16 v[216:219], v[184:187], v[236:239], v[216:219]\n\t" \
    "ds_read_b128 v[228:231], %[wl] offset:2048\n\t" \
    "ds_read_b128 v[232:235], %[wl] offset:3072\n\t" \
    "v_mfma_f32_16x16x32_bf16 v[220:223], v[192:195], v[236:239], v[220:223]\n\t" \
    "v_cvt_pk_bf16_f32 v204, v204, v205\n\t" \
    "v_cvt_pk_bf16_f32 v205, v206, v207\n\t" \
    "s_waitcnt lgkmcnt(2)\n\t" \
    "v_mfma_f32_16x16x32_bf16 v[216:219], v[188:191], v[240:243], v[216:219]\n\t" \
    "ds_write_b64 v252, v[204:205] offset:1024\n\t" \
    "v_cvt_pk_bf16_f32 v208, v208, v209\n\t" \
    "v_mfma_f32_16x16x32_bf16 v[220:223], v[196:199], v[240:243], v[220:223]\n\t" \
    "v_cvt_pk_bf16_f32 v209, v210, v211\n\t" \
    "ds_write_b64 v252, v[208:209] offset:2048\n\t" \
    "ds_read_b128 v[200:203], %[wl] offset:0\n\t" \
    "ds_read_b128 v[204:207], %[wl] offset:1024\n\t" \
    "v_cvt_pk_bf16_f32 v216, v216, v217\n\t" \
    "v_cvt_pk_bf16_f32 v217, v218, v219\n\t" \
    "ds_write_b64 v252, v[216:217] offset:7168\n\t" \
    "s_nop 0\n\t" \
    "v_cvt_pk_bf16_f32 v220, v220, v221\n\t" \
    "v_cvt_pk_bf16_f32 v221, v222, v223\n\t" \
    "ds_write_b64 v252, v[220:221] offset:8192\n\t" \
    "s_waitcnt lgkmcnt(0)\n\t"
#define ASM_XG_S_LO1 \
    "s_waitcnt lgkmcnt(0)\n\t" \
    "ds_read_b128 v[224:227], %[xa] offset:0\n\t" \
    "ds_read_b128 v[228:231], %[xa] offset:16384\n\t" \
    "ds_read_b128 v[232:235], %[xa] offset:1024\n\t" \
    "ds_read_b128 v[236:239], %[xa] offset:17408\n\t" \
    "s_waitcnt lgkmcnt(3)\n\t" \
    "v_mfma_f32_16x16x32_bf16 v[208:211], v[8:11], v[224:227], 0\n\t" \
    "v_xor_b32 v252, 0x800, %[xa]\n\t" \
    "ds_read_b128 v[240:243], v252 offset:0\n\t" \
    "s_waitcnt lgkmcnt(3)\n\t" \
    "v_mfma_f32_16x16x32_bf16 v[220:223], v[8:11], v[228:231], 0\n\t" \
    "ds_read_b128 v[224:227], v252 offset:16384\n\t" \
    "s_waitcnt lgkmcnt(3)\n\t" \
    "v_mfma_f32_16x16x32_bf16 v[208:211], v[12:15], v[232:235], v[208:211]\n\t" \
    "ds_read_b128 v[228:231], v252 offset:1024\n\t" \
    "s_waitcnt lgkmcnt(3)\n\t" \
    "v_mfma_f32_16x16x32_bf16 v[220:223], v[12:15], v[236:239], v[220:223]\n\t" \
    "ds_read_b128 v[232:235], v252 offset:17408\n\t" \
    "s_waitcnt lgkmcnt(3)\n\t" \
    "v_mfma_f32_16x16x32_bf16 v[208:211], v[32:35], v[240:243], v[208:211]\n\t" \
    "v_xor_b32 v252, 0x1000, %[xa]\n\t" \
    "ds_read_b128 v[236:239], v252 offset:0\n\t" \
    "s_waitcnt lgkmcnt(3)\n\t" \
    "v_mfma_f32_16x16x32_bf16 v[220:223], v[32:35], v[224:227], v[220:223]\n\t" \
    "ds_read_b128 v[240:243], v252 offset:16384\n\t" \
    "s_waitcnt lgkmcnt(3)\n\t" \
    "v_mfma_f32_16x16x32_bf16 v[208:211], v[36:39], v[228:231], v[208:211]\n\t" \
    "ds_read_b128 v[224:227], v252 offset:1024\n\t" \
    "s_waitcnt lgkmcnt(3)\n\t" \
    "v_mfma_f32_16x16x32_bf16 v[220:223], v[36:39], v[232:235], v[220:223]\n\t" \
    "ds_read_b128 v[228:231], v252 offset:17408\n\t" \
    "s_waitcnt lgkmcnt(3)\n\t" \
    "v_mfma_f32_16x16x32_bf16 v[208:211], v[48:51], v[236:239], v[208:211]\n\t" \
    "v_xor_b32 v252, 0x1800, %[xa]\n\t" \
    "ds_read_b128 v[232:235], v252 offset:0\n\t" \
    "s_waitcnt lgkmcnt(3)\n\t" \
    "v_mfma_f32_16x16x32_bf16 v[220:223], v[48:51], v[240:243], v[220:223]\n\t" \
    "ds_read_b128 v[236:239], v252 offset:16384\n\t" \
    "s_waitcnt lgkmcnt(3)\n\t" \
    "v_mfma_f32_16x16x32_bf16 v[208:211], v[52:55], v[224:227], v[208:211]\n\t" \
    "ds_read_b128 v[240:243], v252 offset:1024\n\t" \
    "s_waitcnt lgkmcnt(3)\n\t" \
    "v_mfma_f32_16x16x32_bf16 v[220:223], v[52:55], v[228:231], v[220:223]\n\t" \
    "ds_read_b128 v[224:227], v252 offset:17408\n\t" \
    "s_waitcnt lgkmcnt(3)\n\t" \
    "v_mfma_f32_16x16x32_bf16 v[208:211], v[72:75], v[232:235], v[208:211]\n\t" \
    "v_and_b32 v252, 0x3ff, %[xa]\n\t" \
    "ds_read_b128 v[228:231], v252 offset:40960\n\t" \
    "s_waitcnt lgkmcnt(3)\n\t" \
    "v_mfma_f32_16x16x32_bf16 v[220:223], v[72:75], v[236:239], v[220:223]\n\t" \
    "ds_read_b128 v[232:235], v252 offset:45056\n\t" \
    "s_waitcnt lgkmcnt(3)\n\t" \
    "v_mfma_f32_16x16x32_bf16 v[208:211], v[76:79], v[240:243], v[208:211]\n\t" \
    "ds_read_b128 v[236:239], v252 offset:41984\n\t" \
    "s_waitcnt lgkmcnt(3)\n\t" \
    "v_mfma_f32_16x16x32_bf16 v[220:223], v[76:79], v[224:227], v[220:223]\n\t" \
    "ds_read_b128 v[240:243], v252 offset:46080\n\t" \
    "s_waitcnt lgkmcnt(0)\n\t" \
    "s_barrier\n\t" ASM_ST \
    "v_mfma_f32_16x16x32_bf16 v[208:211], v[168:171], v[228:231], v[208:211]\n\t" \
    "v_xor_b32 v252, 0x2000, %[xa]\n\t" \
    "ds_read_b128 v[224:227], v252 offset:0\n\t" \
    "v_mfma_f32_16x16x32_bf16 v[220:223], v[168:171], v[232:235], v[220:223]\n\t" \
    "ds_read_b128 v[228:231], v252 offset:16384\n\t" \
    "v_mfma_f32_16x16x32_bf16 v[208:211], v[172:175], v[236:239], v[208:211]\n\t" \
    "ds_read_b128 v[232:235], v252 offset:1024\n\t" \
    "v_mfma_f32_16x16x32_bf16 v[220:223], v[172:175], v[240:243], v[220:223]\n\t" \
    "ds_read_b128 v[236:239], v252 offset:17408\n\t" \
    "s_waitcnt lgkmcnt(3)\n\t" \
    "v_mfma_f32_16x16x32_bf16 v[208:211], v[88:91], v[224:227], v[208:211]\n\t" \
    "v_xor_b32 v252, 0x2800, %[xa]\n\t" \
    "ds_read_b128 v[240:243], v252 offset:0\n\t" \
    "s_waitcnt lgkmcnt(3)\n\t" \
    "v_mfma_f32_16x16x32_bf16 v[220:223], v[88:91], v[228:231], v[220:223]\n\t" \
    "ds_read_b128 v[224:227], v252 offset:16384\n\t" \
    "s_waitcnt lgkmcnt(3)\n\t" \
    "v_mfma_f32_16x16x32_bf16 v[208:211], v[92:95], v[232:235], v[208:211]\n\t" \
    "ds_read_b128 v[228:231], v252 offset:1024\n\t" \
    "s_waitcnt lgkmcnt(3)\n\t" \
    "v_mfma_f32_16x16x32_bf16 v[220:223], v[92:95], v[236:239], v[220:223]\n\t" \
    "ds_read_b128 v[232:235], v252 offset:17408\n\t" \
    "s_waitcnt lgkmcnt(3)\n\t" \
    "v_mfma_f32_16x16x32_bf16 v[208:211], v[112:115], v[240:243], v[208:211]\n\t" \
    "v_xor_b32 v252, 0x3000, %[xa]\n\t" \
    "ds_read_b128 v[236:239], v252 offset:0\n\t" \
    "s_waitcnt lgkmcnt(3)\n\t" \
    "v_mfma_f32_16x16x32_bf16 v[220:223], v[112:115], v[224:227], v[220:223]\n\t" \
    "ds_read_b128 v[240:243], v252 offset:16384\n\t" \
    "s_waitcnt lgkmcnt(3)\n\t" \
    "v_mfma_f32_16x16x32_bf16 v[208:211], v[116:119], v[228:231], v[208:211]\n\t" \
    "ds_read_b128 v[224:227], v252 offset:1024\n\t" \
    "s_waitcnt lgkmcnt(3)\n\t" \
    "v_mfma_f32_16x16x32_bf16 v[220:223], v[116:119], v[232:235], v[220:223]\n\t" \
    "ds_read_b128 v[228:231], v252 offset:17408\n\t" \
    "s_waitcnt lgkmcnt(3)\n\t" \
    "v_mfma_f32_16x16x32_bf16 v[208:211], v[128:131], v[236:239], v[208:211]\n\t" \
    "v_xor_b32 v252, 0x3800, %[xa]\n\t" \
    "ds_read_b128 v[232:235], v252 offset:0\n\t" \
    "s_waitcnt lgkmcnt(3)\n\t" \
    "v_mfma_f32_16x16x32_bf16 v[220:223], v[128:131], v[240:243], v[220:223]\n\t" \
    "ds_read_b128 v[236:239], v252 offset:16384\n\t" \
    "s_waitcnt lgkmcnt(3)\n\t" \
    "v_mfma_f32_16x16x32_bf16 v[208:211], v[132:135], v[224:227], v[208:211]\n\t" \
    "ds_read_b128 v[240:243], v252 offset:1024\n\t" \
    "s_waitcnt lgkmcnt(3)\n\t" \
    "v_mfma_f32_16x16x32_bf16 v[220:223], v[132:135], v[228:231], v[220:223]\n\t" \
    "ds_read_b128 v[224:227], v252 offset:17408\n\t" \
    "s_waitcnt lgkmcnt(3)\n\t" \
    "v_mfma_f32_16x16x32_bf16 v[208:211], v[152:155], v[232:235], v[208:211]\n\t" \
    "v_and_b32 v252, 0x3ff, %[xa]\n\t" \
    "ds_read_b128 v[228:231], v252 offset:43008\n\t" \
    "v_mov_b64 v[244:245], 0\n\t" \
    "s_waitcnt lgkmcnt(3)\n\t" \
    "v_mfma_f32_16x16x32_bf16 v[220:223], v[152:155], v[236:239], v[220:223]\n\t" \
    "ds_read_b128 v[232:235], v252 offset:44032\n\t" \
    "v_mov_b64 v[246:247], 0\n\t" \
    "s_waitcnt lgkmcnt(3)\n\t" \
    "v_mfma_f32_16x16x32_bf16 v[208:211], v[156:159], v[240:243], v[208:211]\n\t" \
    "ds_read_b128 v[236:239], v252 offset:47104\n\t" \
    "v_mov_b64 v[248:249], 0\n\t" \
    "s_waitcnt lgkmcnt(3)\n\t" \
    "v_mfma_f32_16x16x32_bf16 v[220:223], v[156:159], v[224:227], v[220:223]\n\t" \
    "ds_read_b128 v[240:243], v252 offset:48128\n\t" \
    "v_mov_b64 v[250:251], 0\n\t" \
    "s_waitcnt lgkmcnt(3)\n\t" \
    "v_mfma_f32_16x16x32_bf16 v[208:211], v[192:195], v[228:231], v[208:211]\n\t" \
    "v_add_u32 v252, %[qs], v252\n\t" \
    "s_waitcnt lgkmcnt(2)\n\t" \
    "v_mfma_f32_16x16x32_bf16 v[208:211], v[196:199], v[232:235], v[208:211]\n\t" \
    "v_mov_b64 v[224:225], 0\n\t" \
    "s_waitcnt lgkmcnt(1)\n\t" \
    "v_mfma_f32_16x16x32_bf16 v[220:223], v[192:195], v[236:239], v[220:223]\n\t" \
    "v_mov_b64 v[226:227], 0\n\t" \
    "ds_read_b128 v[228:231], %[wl] offset:2048\n\t" \
    "s_waitcnt lgkmcnt(1)\n\t" \
    "v_mfma_f32_16x16x32_bf16 v[220:223], v[196:199], v[240:243], v[220:223]\n\t" \
    "ds_read_b128 v[232:235], %[wl] offset:3072\n\t" \
    "v_cvt_pk_bf16_f32 v208, v208, v209\n\t" \
    "v_cvt_pk_bf16_f32 v209, v210, v211\n\t" \
    "ds_write_b64 v252, v[208:209] offset:2048\n\t" \
    "ds_read_b128 v[200:203], %[wl] offset:0\n\t" \
    "ds_read_b128 v[204:207], %[wl] offset:1024\n\t" \
    "s_nop 1\n\t" \
    "v_cvt_pk_bf16_f32 v220, v220, v221\n\t" \
    "v_cvt_pk_bf16_f32 v221, v222, v223\n\t" \
    "ds_write_b64 v252, v[220:221] offset:8192\n\t" \
    "s_waitcnt lgkmcnt(0)\n\t"
#define ASM_XG_V \
    "s_waitcnt lgkmcnt(0)\n\t" \
    "ds_read_b128 v[224:227], %[xa] offset:0\n\t" \
    "ds_read_b128 v[244:247], %[wl] offset:0\n\t" \
    "ds_read_b128 v[228:231], %[xa] offset:16384\n\t" \
    "ds_read_b128 v[232:235], %[xa] offset:1024\n\t" \
    "ds_read_b128 v[236:239], %[xa] offset:17408\n\t" \
    "ds_read_b128 v[248:251], %[wl] offset:1024\n\t" \
    "s_waitcnt lgkmcnt(5)\n\t" \
    "v_mfma_f32_16x16x32_bf16 v[204:207], v[0:3], v[224:227], 0\n\t" \
    "v_xor_b32 v252, 0x800, %[xa]\n\t" \
    "ds_read_b128 v[240:243], v252 offset:0\n\t" \
    "v_mfma_f32_16x16x32_bf16 v[208:211], v[8:11], v[224:227], 0\n\t" \
    "s_waitcnt lgkmcnt(5)\n\t" \
    "v_mfma_f32_16x16x32_bf16 v[200:203], v[244:247], v[224:227], 0\n\t" \
    "s_waitcnt lgkmcnt(4)\n\t" \
    "v_mfma_f32_16x16x32_bf16 v[216:219], v[0:3], v[228:231], 0\n\t" \
    "ds_read_b128 v[224:227], v252 offset:16384\n\t" \
    "v_mfma_f32_16x16x32_bf16 v[220:223], v[8:11], v[228:231], 0\n\t" \
    "v_mfma_f32_16x16x32_bf16 v[212:215], v[244:247], v[228:231], 0\n\t" \
    "s_waitcnt lgkmcnt(4)\n\t" \
    "v_mfma_f32_16x16x32_bf16 v[204:207], v[4:7], v[232:235], v[204:207]\n\t" \
    "ds_read_b128 v[228:231], v252 offset:1024\n\t" \
    "v_mfma_f32_16x16x32_bf16 v[208:211], v[12:15], v[232:235], v[208:211]\n\t" \
    "ds_read_b128 v[244:247], %[wl] offset:2048\n\t" \
    "s_waitcnt lgkmcnt(4)\n\t" \
    "v_mfma_f32_16x16x32_bf16 v[200:203], v[248:251], v[232:235], v[200:203]\n\t" \
    "v_mfma_f32_16x16x32_bf16 v[216:219], v[4:7], v[236:239], v[216:219]\n\t" \
    "ds_read_b128 v[232:235], v252 offset:17408\n\t" \
    "v_mfma_f32_16x16x32_bf16 v[220:223], v[12:15], v[236:239], v[220:223]\n\t" \
    "v_mfma_f32_16x16x32_bf16 v[212:215], v[248:251], v[236:239], v[212:215]\n\t" \
    "s_waitcnt lgkmcnt(4)\n\t" \
    "v_mfma_f32_16x16x32_bf16 v[204:207], v[24:27], v[240:243], v[204:207]\n\t" \
    "v_xor_b32 v252, 0x1000, %[xa]\n\t" \
    "ds_read_b128 v[236:239], v252 offset:0\n\t" \
    "v_mfma_f32_16x16x32_bf16 v[208:211], v[32:35], v[240:243], v[208:211]\n\t" \
    "ds_read_b128 v[248:251], %[wl] offset:3072\n\t" \
    "v_mfma_f32_16x16x32_bf16 v[200:203], v[16:19], v[240:243], v[200:203]\n\t" \
    "s_waitcnt lgkmcnt(5)\n\t" \
    "v_mfma_f32_16x16x32_bf16 v[216:219], v[24:27], v[224:227], v[216:219]\n\t" \
    "ds_read_b128 v[240:243], v252 offset:16384\n\t" \
    "v_mfma_f32_16x16x32_bf16 v[220:223], v[32:35], v[224:227], v[220:223]\n\t" \
    "v_mfma_f32_16x16x32_bf16 v[212:215], v[16:19], v[224:227], v[212:215]\n\t" \
    "s_waitcnt lgkmcnt(5)\n\t" \
    "v_mfma_f32_16x16x32_bf16 v[204:207], v[28:31], v[228:231], v[204:207]\n\t" \
    "ds_read_b128 v[224:227], v252 offset:1024\n\t" \
    "v_mfma_f32_16x16x32_bf16 v[208:211], v[36:39], v[228:231], v[208:211]\n\t" \
    "v_mfma_f32_16x16x32_bf16 v[200:203], v[20:23], v[228:231], v[200:203]\n\t" \
    "s_waitcnt lgkmcnt(4)\n\t" \
    "v_mfma_f32_16x16x32_bf16 v[216:219], v[28:31], v[232:235], v[216:219]\n\t" \
    "ds_read_b128 v[228:231], v252 offset:17408\n\t" \
    "v_mfma_f32_16x16x32_bf16 v[220:223], v[36:39], v[232:235], v[220:223]\n\t" \
    "v_mfma_f32_16x16x32_bf16 v[212:215], v[20:23], v[232:235], v[212:215]\n\t" \
    "s_waitcnt lgkmcnt(4)\n\t" \
    "v_mfma_f32_16x16x32_bf16 v[204:207], v[40:43], v[236:239], v[204:207]\n\t" \
    "v_xor_b32 v252, 0x1800, %[xa]\n\t" \
    "ds_read_b128 v[232:235], v252 offset:0\n\t" \
    "v_mfma_f32_16x16x32_bf16 v[208:211], v[48:51], v[236:239], v[208:211]\n\t" \
    "v_mfma_f32_16x16x32_bf16 v[200:203], v[244:247], v[236:239], v[200:203]\n\t" \
    "s_waitcnt lgkmcnt(3)\n\t" \
    "v_mfma_f32_16x16x32_bf16 v[216:219], v[40:43], v[240:243], v[216:219]\n\t" \
    "ds_read_b128 v[236:239], v252 offset:16384\n\t" \
    "v_mfma_f32_16x16x32_bf16 v[220:223], v[48:51], v[240:243], v[220:223]\n\t" \
    "v_mfma_f32_16x16x32_bf16 v[212:215], v[244:247], v[240:243], v[212:215]\n\t" \
    "s_waitcnt lgkmcnt(3)\n\t" \
    "v_mfma_f32_16x16x32_bf16 v[204:207], v[44:47], v[224:227], v[204:207]\n\t" \
    "ds_read_b128 v[240:243], v252 offset:1024\n\t" \
    "v_mfma_f32_16x16x32_bf16 v[208:211], v[52:55], v[224:227], v[208:211]\n\t" \
    "ds_read_b128 v[244:247], %[wl] offset:8192\n\t" \
    "v_mfma_f32_16x16x32_bf16 v[200:203], v[248:251], v[224:227], v[200:203]\n\t" \
    "s_waitcnt lgkmcnt(4)\n\t" \
    "v_mfma_f32_16x16x32_bf16 v[216:219], v[44:47], v[228:231], v[216:219]\n\t" \
    "ds_read_b128 v[224:227], v252 offset:17408\n\t" \
    "v_mfma_f32_16x16x32_bf16 v[220:223], v[52:55], v[228:231], v[220:223]\n\t" \
    "v_mfma_f32_16x16x32_bf16 v[212:215], v[248:251], v[228:231], v[212:215]\n\t" \
    "s_waitcnt lgkmcnt(4)\n\t" \
    "v_mfma_f32_16x16x32_bf16 v[204:207], v[64:67], v[232:235], v[204:207]\n\t" \
    "v_and_b32 v252, 0x3ff, %[xa]\n\t" \
    "ds_read_b128 v[228:231], v252 offset:40960\n\t" \
    "v_mfma_f32_16x16x32_bf16 v[208:211], v[72:75], v[232:235], v[208:211]\n\t" \
    "ds_read_b128 v[248:251], %[wl] offset:9216\n\t" \
    "v_mfma_f32_16x16x32_bf16 v[200:203], v[56:59], v[232:235], v[200:203]\n\t" \
    "s_waitcnt lgkmcnt(5)\n\t" \
    "v_mfma_f32_16x16x32_bf16 v[216:219], v[64:67], v[236:239], v[216:219]\n\t" \
    "ds_read_b128 v[232:235], v252 offset:45056\n\t" \
    "v_mfma_f32_16x16x32_bf16 v[220:223], v[72:75], v[236:239], v[220:223]\n\t" \
    "v_mfma_f32_16x16x32_bf16 v[212:215], v[56:59], v[236:239], v[212:215]\n\t" \
    "s_waitcnt lgkmcnt(5)\n\t" \
    "v_mfma_f32_16x16x32_bf16 v[204:207], v[68:71], v[240:243], v[204:207]\n\t" \
    "ds_read_b128 v[236:239], v252 offset:41984\n\t" \
    "v_mfma_f32_16x16x32_bf16 v[208:211], v[76:79], v[240:243], v[208:211]\n\t" \
    "v_mfma_f32_16x16x32_bf16 v[200:203], v[60:63], v[240:243], v[200:203]\n\t" \
    "s_waitcnt lgkmcnt(4)\n\t" \
    "v_mfma_f32_16x16x32_bf16 v[216:219], v[68:71], v[224:227], v[216:219]\n\t" \
    "ds_read_b128 v[240:243], v252 offset:46080\n\t" \
    "v_mfma_f32_16x16x32_bf16 v[220:223], v[76:79], v[224:227], v[220:223]\n\t" \
    "v_mfma_f32_16x16x32_bf16 v[212:215], v[60:63], v[224:227], v[212:215]\n\t" \
    "s_waitcnt lgkmcnt(4)\n\t" \
    "v_mfma_f32_16x16x32_bf16 v[204:207], v[160:163], v[228:231], v[204:207]\n\t" \
    "v_xor_b32 v252, 0x2000, %[xa]\n\t" \
    "ds_read_b128 v[224:227], v252 offset:0\n\t" \
    "v_mfma_f32_16x16x32_bf16 v[208:211], v[168:171], v[228:231], v[208:211]\n\t" \
    "v_mfma_f32_16x16x32_bf16 v[200:203], v[244:247], v[228:231], v[200:203]\n\t" \
    "s_waitcnt lgkmcnt(3)\n\t" \
    "v_mfma_f32_16x16x32_bf16 v[216:219], v[160:163], v[232:235], v[216:219]\n\t" \
    "ds_read_b128 v[228:231], v252 offset:16384\n\t" \
    "v_mfma_f32_16x16x32_bf16 v[220:223], v[168:171], v[232:235], v[220:223]\n\t" \
    "v_mfma_f32_16x16x32_bf16 v[212:215], v[244:247], v[232:235], v[212:215]\n\t" \
    "s_waitcnt lgkmcnt(3)\n\t" \
    "v_mfma_f32_16x16x32_bf16 v[204:207], v[164:167], v[236:239], v[204:207]\n\t" \
    "ds_read_b128 v[232:235], v252 offset:1024\n\t" \
    "v_mfma_f32_16x16x32_bf16 v[208:211], v[172:175], v[236:239], v[208:211]\n\t" \
    "ds_read_b128 v[244:247], %[wl] offset:4096\n\t" \
    "v_mfma_f32_16x16x32_bf16 v[200:203], v[248:251], v[236:239], v[200:203]\n\t" \
    "s_waitcnt lgkmcnt(4)\n\t" \
    "v_mfma_f32_16x16x32_bf16 v[216:219], v[164:167], v[240:243], v[216:219]\n\t" \
    "ds_read_b128 v[236:239], v252 offset:17408\n\t" \
    "v_mfma_f32_16x16x32_bf16 v[220:223], v[172:175], v[240:243], v[220:223]\n\t" \
    "v_mfma_f32_16x16x32_bf16 v[212:215], v[248:251], v[240:243], v[212:215]\n\t" \
    "s_waitcnt lgkmcnt(4)\n\t" \
    "v_mfma_f32_16x16x32_bf16 v[204:207], v[80:83], v[224:227], v[204:207]\n\t" \
    "v_xor_b32 v252, 0x2800, %[xa]\n\t" \
    "ds_read_b128 v[240:243], v252 offset:0\n\t" \
    "v_mfma_f32_16x16x32_bf16 v[208:211], v[88:91], v[224:227], v[208:211]\n\t" \
    "ds_read_b128 v[248:251], %[wl] offset:5120\n\t" \
    "s_waitcnt lgkmcnt(3)\n\t" \
    "v_mfma_f32_16x16x32_bf16 v[200:203], v[244:247], v[224:227], v[200:203]\n\t" \
    "v_mfma_f32_16x16x32_bf16 v[216:219], v[80:83], v[228:231], v[216:219]\n\t" \
    "ds_read_b128 v[224:227], v252 offset:16384\n\t" \
    "v_mfma_f32_16x16x32_bf16 v[220:223], v[88:91], v[228:231], v[220:223]\n\t" \
    "v_mfma_f32_16x16x32_bf16 v[212:215], v[244:247], v[228:231], v[212:215]\n\t" \
    "v_mfma_f32_16x16x32_bf16 v[204:207], v[84:87], v[232:235], v[204:207]\n\t" \
    "ds_read_b128 v[228:231], v252 offset:1024\n\t" \
    "v_mfma_f32_16x16x32_bf16 v[208:211], v[92:95], v[232:235], v[208:211]\n\t" \
    "ds_read_b128 v[244:247], %[wl] offset:6144\n\t" \
    "s_waitcnt lgkmcnt(3)\n\t" \
    "v_mfma_f32_16x16x32_bf16 v[200:203], v[248:251], v[232:235], v[200:203]\n\t" \
    "v_mfma_f32_16x16x32_bf16 v[216:219], v[84:87], v[236:239], v[216:219]\n\t" \
    "ds_read_b128 v[232:235], v252 offset:17408\n\t" \
    "v_mfma_f32_16x16x32_bf16 v[220:223], v[92:95], v[236:239], v[220:223]\n\t" \
    "v_mfma_f32_16x16x32_bf16 v[212:215], v[248:251], v[236:239], v[212:215]\n\t" \
    "v_mfma_f32_16x16x32_bf16 v[204:207], v[104:107], v[240:243], v[204:207]\n\t" \
    "v_xor_b32 v252, 0x3000, %[xa]\n\t" \
    "ds_read_b128 v[236:239], v252 offset:0\n\t" \
    "v_mfma_f32_16x16x32_bf16 v[208:211], v[112:115], v[240:243], v[208:211]\n\t" \
    "ds_read_b128 v[248:251], %[wl] offset:7168\n\t" \
    "v_mfma_f32_16x16x32_bf16 v[200:203], v[96:99], v[240:243], v[200:203]\n\t" \
    "s_waitcnt lgkmcnt(5)\n\t" \
    "v_mfma_f32_16x16x32_bf16 v[216:219], v[104:107], v[224:227], v[216:219]\n\t" \
    "ds_read_b128 v[240:243], v252 offset:16384\n\t" \
    "v_mfma_f32_16x16x32_bf16 v[220:223], v[112:115], v[224:227], v[220:223]\n\t" \
    "v_mfma_f32_16x16x32_bf16 v[212:215], v[96:99], v[224:227], v[212:215]\n\t" \
    "s_waitcnt lgkmcnt(5)\n\t" \
    "v_mfma_f32_16x16x32_bf16 v[204:207], v[108:111], v[228:231], v[204:207]\n\t" \
    "ds_read_b128 v[224:227], v252 offset:1024\n\t" \
    "v_mfma_f32_16x16x32_bf16 v[208:211], v[116:119], v[228:231], v[208:211]\n\t" \
    "v_mfma_f32_16x16x32_bf16 v[200:203], v[100:103], v[228:231], v[200:203]\n\t" \
    "s_waitcnt lgkmcnt(4)\n\t" \
    "v_mfma_f32_16x16x32_bf16 v[216:219], v[108:111], v[232:235], v[216:219]\n\t" \
    "ds_read_b128 v[228:231], v252 offset:17408\n\t" \
    "v_mfma_f32_16x16x32_bf16 v[220:223], v[116:119], v[232:235], v[220:223]\n\t" \
    "v_mfma_f32_16x16x32_bf16 v[212:215], v[100:103], v[232:235], v[212:215]\n\t" \
    "s_waitcnt lgkmcnt(4)\n\t" \
    "v_mfma_f32_16x16x32_bf16 v[204:207], v[120:123], v[236:239], v[204:207]\n\t" \
    "v_xor_b32 v252, 0x3800, %[xa]\n\t" \
    "ds_read_b128 v[232:235], v252 offset:0\n\t" \
    "v_mfma_f32_16x16x32_bf16 v[208:211], v[128:131], v[236:239], v[208:211]\n\t" \
    "v_mfma_f32_16x16x32_bf16 v[200:203], v[244:247], v[236:239], v[200:203]\n\t" \
    "s_waitcnt lgkmcnt(3)\n\t" \
    "v_mfma_f32_16x16x32_bf16 v[216:219], v[120:123], v[240:243], v[216:219]\n\t" \
    "ds_read_b128 v[236:239], v252 offset:16384\n\t" \
    "v_mfma_f32_16x16x32_bf16 v[220:223], v[128:131], v[240:243], v[220:223]\n\t" \
    "v_mfma_f32_16x16x32_bf16 v[212:215], v[244:247], v[240:243], v[212:215]\n\t" \
    "s_waitcnt lgkmcnt(3)\n\t" \
    "v_mfma_f32_16x16x32_bf16 v[204:207], v[124:127], v[224:227], v[204:207]\n\t" \
    "ds_read_b128 v[240:243], v252 offset:1024\n\t" \
    "v_mfma_f32_16x16x32_bf16 v[208:211], v[132:135], v[224:227], v[208:211]\n\t" \
    "v_mfma_f32_16x16x32_bf16 v[200:203], v[248:251], v[224:227], v[200:203]\n\t" \
    "s_waitcnt lgkmcnt(3)\n\t" \
    "v_mfma_f32_16x16x32_bf16 v[216:219], v[124:127], v[228:231], v[216:219]\n\t" \
    "ds_read_b128 v[224:227], v252 offset:17408\n\t" \
    "v_mfma_f32_16x16x32_bf16 v[220:223], v[132:135], v[228:231], v[220:223]\n\t" \
    "v_mfma_f32_16x16x32_bf16 v[212:215], v[248:251], v[228:231], v[212:215]\n\t" \
    "s_waitcnt lgkmcnt(3)\n\t" \
    "v_mfma_f32_16x16x32_bf16 v[204:207], v[144:147], v[232:235], v[204:207]\n\t" \
    "v_and_b32 v252, 0x3ff, %[xa]\n\t" \
    "ds_read_b128 v[228:231], v252 offset:43008\n\t" \
    "v_mov_b64 v[244:245], 0\n\t" \
    "v_mfma_f32_16x16x32_bf16 v[208:211], v[152:155], v[232:235], v[208:211]\n\t" \
    "v_mov_b64 v[246:247], 0\n\t" \
    "v_mfma_f32_16x16x32_bf16 v[200:203], v[136:139], v[232:235], v[200:203]\n\t" \
    "v_mov_b64 v[248:249], 0\n\t" \
    "s_waitcnt lgkmcnt(3)\n\t" \
    "v_mfma_f32_16x16x32_bf16 v[216:219], v[144:147], v[236:239], v[216:219]\n\t" \
    "ds_read_b128 v[232:235], v252 offset:44032\n\t" \
    "v_mov_b64 v[250:251], 0\n\t" \
    "v_mfma_f32_16x16x32_bf16 v[220:223], v[152:155], v[236:239], v[220:223]\n\t" \
    "v_mfma_f32_16x16x32_bf16 v[212:215], v[136:139], v[236:239], v[212:215]\n\t" \
    "s_waitcnt lgkmcnt(3)\n\t" \
    "v_mfma_f32_16x16x32_bf16 v[204:207], v[148:151], v[240:243], v[204:207]\n\t" \
    "ds_read_b128 v[236:239], v252 offset:47104\n\t" \
    "v_mfma_f32_16x16x32_bf16 v[208:211], v[156:159], v[240:243], v[208:211]\n\t" \
    "v_mfma_f32_16x16x32_bf16 v[200:203], v[140:143], v[240:243], v[200:203]\n\t" \
    "s_waitcnt lgkmcnt(3)\n\t" \
    "v_mfma_f32_16x16x32_bf16 v[216:219], v[148:151], v[224:227], v[216:219]\n\t" \
    "ds_read_b128 v[240:243], v252 offset:48128\n\t" \
    "v_mfma_f32_16x16x32_bf16 v[220:223], v[156:159], v[224:227], v[220:223]\n\t" \
    "v_mfma_f32_16x16x32_bf16 v[212:215], v[140:143], v[224:227], v[212:215]\n\t" \
    "s_waitcnt lgkmcnt(3)\n\t" \
    "v_mfma_f32_16x16x32_bf16 v[204:207], v[184:187], v[228:231], v[204:207]\n\t" \
    "v_add_u32 v252, %[qs], v252\n\t" \
    "v_mfma_f32_16x16x32_bf16 v[208:211], v[192:195], v[228:231], v[208:211]\n\t" \
    "v_mov_b64 v[224:225], 0\n\t" \
    "v_mfma_f32_16x16x32_bf16 v[200:203], v[176:179], v[228:231], v[200:203]\n\t" \
    "v_mov_b64 v[226:227], 0\n\t" \
    "s_waitcnt lgkmcnt(2)\n\t" \
    "v_mfma_f32_16x16x32_bf16 v[204:207], v[188:191], v[232:235], v[204:207]\n\t" \
    "v_mfma_f32_16x16x32_bf16 v[208:211], v[196:199], v[232:235], v[208:211]\n\t" \
    "v_mfma_f32_16x16x32_bf16 v[200:203], v[180:183], v[232:235], v[200:203]\n\t" \
    "s_waitcnt lgkmcnt(1)\n\t" \
    "v_mfma_f32_16x16x32_bf16 v[216:219], v[184:187], v[236:239], v[216:219]\n\t" \
    "ds_read_b128 v[228:231], %[wl] offset:2048\n\t" \
    "ds_read_b128 v[232:235], %[wl] offset:3072\n\t" \
    "v_mfma_f32_16x16x32_bf16 v[220:223], v[192:195], v[236:239], v[220:223]\n\t" \
    "v_cvt_pk_bf16_f32 v204, v204, v205\n\t" \
    "v_cvt_pk_bf16_f32 v205, v206, v207\n\t" \
    "v_mfma_f32_16x16x32_bf16 v[212:215], v[176:179], v[236:239], v[212:215]\n\t" \
    "ds_write_b64 v252, v[204:205] offset:1024\n\t" \
    "v_cvt_pk_bf16_f32 v208, v208, v209\n\t" \
    "s_waitcnt lgkmcnt(3)\n\t" \
    "v_mfma_f32_16x16x32_bf16 v[216:219], v[188:191], v[240:243], v[216:219]\n\t" \
    "v_cvt_pk_bf16_f32 v209, v210, v211\n\t" \
    "ds_write_b64 v252, v[208:209] offset:2048\n\t" \
    "v_mfma_f32_16x16x32_bf16 v[220:223], v[196:199], v[240:243], v[220:223]\n\t" \
    "v_cvt_pk_bf16_f32 v200, v200, v201\n\t" \
    "v_cvt_pk_bf16_f32 v201, v202, v203\n\t" \
    "v_mfma_f32_16x16x32_bf16 v[212:215], v[180:183], v[240:243], v[212:215]\n\t" \
    "ds_write_b64 v252, v[200:201] offset:0\n\t" \
    "ds_read_b128 v[200:203], %[wl] offset:0\n\t" \
    "ds_read_b128 v[204:207], %[wl] offset:1024\n\t" \
    "v_cvt_pk_bf16_f32 v216, v216, v217\n\t" \
    "v_cvt_pk_bf16_f32 v217, v218, v219\n\t" \
    "ds_write_b64 v252, v[216:217] offset:7168\n\t" \
    "v_cvt_pk_bf16_f32 v220, v220, v221\n\t" \
    "v_cvt_pk_bf16_f32 v221, v222, v223\n\t" \
    "ds_write_b64 v252, v[220:221] offset:8192\n\t" \
    "v_cvt_pk_bf16_f32 v212, v212, v213\n\t" \
    "v_cvt_pk_bf16_f32 v213, v214, v215\n\t" \
    "ds_write_b64 v252, v[212:213] offset:6144\n\t" \
    "s_waitcnt lgkmcnt(0)\n\t"
#define ASM_XG_H_HI \
    "s_waitcnt lgkmcnt(0)\n\t" \
    "v_xor_b32 v252, 0x2000, %[xa]\n\t" \
    "ds_read_b128 v[224:227], v252 offset:0\n\t" \
    "ds_read_b128 v[244:247], %[wl] offset:4096\n\t" \
    "ds_read_b128 v[228:231], v252 offset:16384\n\t" \
    "ds_read_b128 v[232:235], v252 offset:1024\n\t" \
    "ds_read_b128 v[236:239], v252 offset:17408\n\t" \
    "ds_read_b128 v[248:251], %[wl] offset:5120\n\t" \
    "s_waitcnt lgkmcnt(5)\n\t" \
    "v_mfma_f32_16x16x32_bf16 v[204:207], v[80:83], v[224:227], 0\n\t" \
    "v_xor_b32 v252, 0x2800, %[xa]\n\t" \
    "ds_read_b128 v[240:243], v252 offset:0\n\t" \
    "v_mfma_f32_16x16x32_bf16 v[208:211], v[88:91], v[224:227], 0\n\t" \
    "s_waitcnt lgkmcnt(5)\n\t" \
    "v_mfma_f32_16x16x32_bf16 v[200:203], v[244:247], v[224:227], 0\n\t" \
    "s_waitcnt lgkmcnt(4)\n\t" \
    "v_mfma_f32_16x16x32_bf16 v[216:219], v[80:83], v[228:231], 0\n\t" \
    "ds_read_b128 v[224:227], v252 offset:16384\n\t" \
    "v_mfma_f32_16x16x32_bf16 v[220:223], v[88:91], v[228:231], 0\n\t" \
    "v_mfma_f32_16x16x32_bf16 v[212:215], v[244:247], v[228:231], 0\n\t" \
    "s_waitcnt lgkmcnt(4)\n\t" \
    "v_mfma_f32_16x16x32_bf16 v[204:207], v[84:87], v[232:235], v[204:207]\n\t" \
    "ds_read_b128 v[228:231], v252 offset:1024\n\t" \
    "v_mfma_f32_16x16x32_bf16 v[208:211], v[92:95], v[232:235], v[208:211]\n\t" \
    "ds_read_b128 v[244:247], %[wl] offset:6144\n\t" \
    "s_waitcnt lgkmcnt(4)\n\t" \
    "v_mfma_f32_16x16x32_bf16 v[200:203], v[248:251], v[232:235], v[200:203]\n\t" \
    "v_mfma_f32_16x16x32_bf16 v[216:219], v[84:87], v[236:239], v[216:219]\n\t" \
    "ds_read_b128 v[232:235], v252 offset:17408\n\t" \
    "v_mfma_f32_16x16x32_bf16 v[220:223], v[92:95], v[236:239], v[220:223]\n\t" \
    "v_mfma_f32_16x16x32_bf16 v[212:215], v[248:251], v[236:239], v[212:215]\n\t" \
    "s_waitcnt lgkmcnt(4)\n\t" \
    "v_mfma_f32_16x16x32_bf16 v[204:207], v[104:107], v[240:243], v[204:207]\n\t" \
    "v_xor_b32 v252, 0x3000, %[xa]\n\t" \
    "ds_read_b128 v[236:239], v252 offset:0\n\t" \
    "v_mfma_f32_16x16x32_bf16 v[208:211], v[112:115], v[240:243], v[208:211]\n\t" \
    "ds_read_b128 v[248:251], %[wl] offset:7168\n\t" \
    "v_mfma_f32_16x16x32_bf16 v[200:203], v[96:99], v[240:243], v[200:203]\n\t" \
    "s_waitcnt lgkmcnt(5)\n\t" \
    "v_mfma_f32_16x16x32_bf16 v[216:219], v[104:107], v[224:227], v[216:219]\n\t" \
    "ds_read_b128 v[240:243], v252 offset:16384\n\t" \
    "v_mfma_f32_16x16x32_bf16 v[220:223], v[112:115], v[224:227], v[220:223]\n\t" \
    "v_mfma_f32_16x16x32_bf16 v[212:215], v[96:99], v[224:227], v[212:215]\n\t" \
    "s_nop 3\n\t" \
    "v_mov_b32 v224, %[tv]\n\t" \
    "s_waitcnt vmcnt(5)\n\t" \
    "buffer_store_dword v224, %[fv], %[rr], %[fp] offen sc1\n\t" \
    "s_waitcnt lgkmcnt(5)\n\t" \
    "v_mfma_f32_16x16x32_bf16 v[204:207], v[108:111], v[228:231], v[204:207]\n\t" \
    "ds_read_b128 v[224:227], v252 offset:1024\n\t" \
    "v_mfma_f32_16x16x32_bf16 v[208:211], v[116:119], v[228:231], v[208:211]\n\t" \
    "v_mfma_f32_16x16x32_bf16 v[200:203], v[100:103], v[228:231], v[200:203]\n\t" \
    "s_waitcnt lgkmcnt(4)\n\t" \
    "v_mfma_f32_16x16x32_bf16 v[216:219], v[108:111], v[232:235], v[216:219]\n\t" \
    "ds_read_b128 v[228:231], v252 offset:17408\n\t" \
    "v_mfma_f32_16x16x32_bf16 v[220:223], v[116:119], v[232:235], v[220:223]\n\t" \
    "v_mfma_f32_16x16x32_bf16 v[212:215], v[100:103], v[232:235], v[212:215]\n\t" \
    "s_waitcnt lgkmcnt(4)\n\t" \
    "v_mfma_f32_16x16x32_bf16 v[204:207], v[120:123], v[236:239], v[204:207]\n\t" \
    "v_xor_b32 v252, 0x3800, %[xa]\n\t" \
    "ds_read_b128 v[232:235], v252 offset:0\n\t" \
    "v_mfma_f32_16x16x32_bf16 v[208:211], v[128:131], v[236:239], v[208:211]\n\t" \
    "v_mfma_f32_16x16x32_bf16 v[200:203], v[244:247], v[236:239], v[200:203]\n\t" \
    "s_waitcnt lgkmcnt(3)\n\t" \
    "v_mfma_f32_16x16x32_bf16 v[216:219], v[120:123], v[240:243], v[216:219]\n\t" \
    "ds_read_b128 v[236:239], v252 offset:16384\n\t" \
    "v_mfma_f32_16x16x32_bf16 v[220:223], v[128:131], v[240:243], v[220:223]\n\t" \
    "v_mfma_f32_16x16x32_bf16 v[212:215], v[244:247], v[240:243], v[212:215]\n\t" \
    "s_waitcnt lgkmcnt(3)\n\t" \
    "v_mfma_f32_16x16x32_bf16 v[204:207], v[124:127], v[224:227], v[204:207]\n\t" \
    "ds_read_b128 v[240:243], v252 offset:1024\n\t" \
    "v_mfma_f32_16x16x32_bf16 v[208:211], v[132:135], v[224:227], v[208:211]\n\t" \
    "ds_read_b128 v[244:247], %[wl] offset:8192\n\t" \
    "v_mfma_f32_16x16x32_bf16 v[200:203], v[248:251], v[224:227], v[200:203]\n\t" \
    "s_waitcnt lgkmcnt(4)\n\t" \
    "v_mfma_f32_16x16x32_bf16 v[216:219], v[124:127], v[228:231], v[216:219]\n\t" \
    "ds_read_b128 v[224:227], v252 offset:17408\n\t" \
    "v_mfma_f32_16x16x32_bf16 v[220:223], v[132:135], v[228:231], v[220:223]\n\t" \
    "v_mfma_f32_16x16x32_bf16 v[212:215], v[248:251], v[228:231], v[212:215]\n\t" \
    "s_waitcnt lgkmcnt(4)\n\t" \
    "v_mfma_f32_16x16x32_bf16 v[204:207], v[144:147], v[232:235], v[204:207]\n\t" \
    "v_and_b32 v252, 0x3ff, %[xa]\n\t" \
    "ds_read_b128 v[228:231], v252 offset:40960\n\t" \
    "v_mfma_f32_16x16x32_bf16 v[208:211], v[152:155], v[232:235], v[208:211]\n\t" \
    "ds_read_b128 v[248:251], %[wl] offset:9216\n\t" \
    "v_mfma_f32_16x16x32_bf16 v[200:203], v[136:139], v[232:235], v[200:203]\n\t" \
    "s_waitcnt lgkmcnt(5)\n\t" \
    "v_mfma_f32_16x16x32_bf16 v[216:219], v[144:147], v[236:239], v[216:219]\n\t" \
    "ds_read_b128 v[232:235], v252 offset:45056\n\t" \
    "v_mfma_f32_16x16x32_bf16 v[220:223], v[152:155], v[236:239], v[220:223]\n\t" \
    "v_mfma_f32_16x16x32_bf16 v[212:215], v[136:139], v[236:239], v[212:215]\n\t" \
    "s_waitcnt lgkmcnt(5)\n\t" \
    "v_mfma_f32_16x16x32_bf16 v[204:207], v[148:151], v[240:243], v[204:207]\n\t" \
    "ds_read_b128 v[236:239], v252 offset:41984\n\t" \
    "v_mfma_f32_16x16x32_bf16 v[208:211], v[156:159], v[240:243], v[208:211]\n\t" \
    "v_mfma_f32_16x16x32_bf16 v[200:203], v[140:143], v[240:243], v[200:203]\n\t" \
    "s_waitcnt lgkmcnt(4)\n\t" \
    "v_mfma_f32_16x16x32_bf16 v[216:219], v[148:151], v[224:227], v[216:219]\n\t" \
    "ds_read_b128 v[240:243], v252 offset:46080\n\t" \
    "v_mfma_f32_16x16x32_bf16 v[220:223], v[156:159], v[224:227], v[220:223]\n\t" \
    "v_mfma_f32_16x16x32_bf16 v[212:215], v[140:143], v[224:227], v[212:215]\n\t" \
    "s_waitcnt vmcnt(1) lgkmcnt(0)\n\t" \
    "s_barrier\n\t" ASM_ST \
    "v_mfma_f32_16x16x32_bf16 v[204:207], v[160:163], v[228:231], v[204:207]\n\t" \
    "ds_read_b128 v[224:227], %[xa] offset:0\n\t" \
    "v_mfma_f32_16x16x32_bf16 v[208:211], v[168:171], v[228:231], v[208:211]\n\t" \
    "v_mfma_f32_16x16x32_bf16 v[200:203], v[244:247], v[228:231], v[200:203]\n\t" \
    "v_mfma_f32_16x16x32_bf16 v[216:219], v[160:163], v[232:235], v[216:219]\n\t" \
    "ds_read_b128 v[228:231], %[xa] offset:16384\n\t" \
    "v_mfma_f32_16x16x32_bf16 v[220:223], v[168:171], v[232:235], v[220:223]\n\t" \
    "v_mfma_f32_16x16x32_bf16 v[212:215], v[244:247], v[232:235], v[212:215]\n\t" \
    "v_mfma_f32_16x16x32_bf16 v[204:207], v[164:167], v[236:239], v[204:207]\n\t" \
    "ds_read_b128 v[232:235], %[xa] offset:1024\n\t" \
    "v_mfma_f32_16x16x32_bf16 v[208:211], v[172:175], v[236:239], v[208:211]\n\t" \
    "ds_read_b128 v[244:247], %[wl] offset:0\n\t" \
    "v_mfma_f32_16x16x32_bf16 v[200:203], v[248:251], v[236:239], v[200:203]\n\t" \
    "v_mfma_f32_16x16x32_bf16 v[216:219], v[164:167], v[240:243], v[216:219]\n\t" \
    "ds_read_b128 v[236:239], %[xa] offset:17408\n\t" \
    "v_mfma_f32_16x16x32_bf16 v[220:223], v[172:175], v[240:243], v[220:223]\n\t" \
    "v_mfma_f32_16x16x32_bf16 v[212:215], v[248:251], v[240:243], v[212:215]\n\t" \
    "s_waitcnt lgkmcnt(4)\n\t" \
    "v_mfma_f32_16x16x32_bf16 v[204:207], v[0:3], v[224:227], v[204:207]\n\t" \
    "v_xor_b32 v252, 0x800, %[xa]\n\t" \
    "ds_read_b128 v[240:243], v252 offset:0\n\t" \
    "v_mfma_f32_16x16x32_bf16 v[208:211], v[8:11], v[224:227], v[208:211]\n\t" \
    "ds_read_b128 v[248:251], %[wl] offset:1024\n\t" \
    "s_waitcnt lgkmcnt(3)\n\t" \
    "v_mfma_f32_16x16x32_bf16 v[200:203], v[244:247], v[224:227], v[200:203]\n\t" \
    "v_mfma_f32_16x16x32_bf16 v[216:219], v[0:3], v[228:231], v[216:219]\n\t" \
    "ds_read_b128 v[224:227], v252 offset:16384\n\t" \
    "v_mfma_f32_16x16x32_bf16 v[220:223], v[8:11], v[228:231], v[220:223]\n\t" \
    "v_mfma_f32_16x16x32_bf16 v[212:215], v[244:247], v[228:231], v[212:215]\n\t" \
    "v_mfma_f32_16x16x32_bf16 v[204:207], v[4:7], v[232:235], v[204:207]\n\t" \
    "ds_read_b128 v[228:231], v252 offset:1024\n\t" \
    "v_mfma_f32_16x16x32_bf16 v[208:211], v[12:15], v[232:235], v[208:211]\n\t" \
    "ds_read_b128 v[244:247], %[wl] offset:2048\n\t" \
    "s_waitcnt lgkmcnt(3)\n\t" \
    "v_mfma_f32_16x16x32_bf16 v[200:203], v[248:251], v[232:235], v[200:203]\n\t" \
    "v_mfma_f32_16x16x32_bf16 v[216:219], v[4:7], v[236:239], v[216:219]\n\t" \
    "ds_read_b128 v[232:235], v252 offset:17408\n\t" \
    "v_mfma_f32_16x16x32_bf16 v[220:223], v[12:15], v[236:239], v[220:223]\n\t" \
    "v_mfma_f32_16x16x32_bf16 v[212:215], v[248:251], v[236:239], v[212:215]\n\t" \
    "v_mfma_f32_16x16x32_bf16 v[204:207], v[24:27], v[240:243], v[204:207]\n\t" \
    "v_xor_b32 v252, 0x1000, %[xa]\n\t" \
    "ds_read_b128 v[236:239], v252 offset:0\n\t" \
    "v_mfma_f32_16x16x32_bf16 v[208:211], v[32:35], v[240:243], v[208:211]\n\t" \
    "ds_read_b128 v[248:251], %[wl] offset:3072\n\t" \
    "v_mfma_f32_16x16x32_bf16 v[200:203], v[16:19], v[240:243], v[200:203]\n\t" \
    "s_waitcnt lgkmcnt(5)\n\t" \
    "v_mfma_f32_16x16x32_bf16 v[216:219], v[24:27], v[224:227], v[216:219]\n\t" \
    "ds_read_b128 v[240:243], v252 offset:16384\n\t" \
    "v_mfma_f32_16x16x32_bf16 v[220:223], v[32:35], v[224:227], v[220:223]\n\t" \
    "v_mfma_f32_16x16x32_bf16 v[212:215], v[16:19], v[224:227], v[212:215]\n\t" \
    "s_waitcnt lgkmcnt(5)\n\t" \
    "v_mfma_f32_16x16x32_bf16 v[204:207], v[28:31], v[228:231], v[204:207]\n\t" \
    "ds_read_b128 v[224:227], v252 offset:1024\n\t" \
    "v_mfma_f32_16x16x32_bf16 v[208:211], v[36:39], v[228:231], v[208:211]\n\t" \
    "v_mfma_f32_16x16x32_bf16 v[200:203], v[20:23], v[228:231], v[200:203]\n\t" \
    "s_waitcnt lgkmcnt(4)\n\t" \
    "v_mfma_f32_16x16x32_bf16 v[216:219], v[28:31], v[232:235], v[216:219]\n\t" \
    "ds_read_b128 v[228:231], v252 offset:17408\n\t" \
    "v_mfma_f32_16x16x32_bf16 v[220:223], v[36:39], v[232:235], v[220:223]\n\t" \
    "v_mfma_f32_16x16x32_bf16 v[212:215], v[20:23], v[232:235], v[212:215]\n\t" \
    "s_waitcnt lgkmcnt(4)\n\t" \
    "v_mfma_f32_16x16x32_bf16 v[204:207], v[40:43], v[236:239], v[204:207]\n\t" \
    "v_xor_b32 v252, 0x1800, %[xa]\n\t" \
    "ds_read_b128 v[232:235], v252 offset:0\n\t" \
    "v_mfma_f32_16x16x32_bf16 v[208:211], v[48:51], v[236:239], v[208:211]\n\t" \
    "v_mfma_f32_16x16x32_bf16 v[200:203], v[244:247], v[236:239], v[200:203]\n\t" \
    "s_waitcnt lgkmcnt(3)\n\t" \
    "v_mfma_f32_16x16x32_bf16 v[216:219], v[40:43], v[240:243], v[216:219]\n\t" \
    "ds_read_b128 v[236:239], v252 offset:16384\n\t" \
    "v_mfma_f32_16x16x32_bf16 v[220:223], v[48:51], v[240:243], v[220:223]\n\t" \
    "v_mfma_f32_16x16x32_bf16 v[212:215], v[244:247], v[240:243], v[212:215]\n\t" \
    "s_waitcnt lgkmcnt(3)\n\t" \
    "v_mfma_f32_16x16x32_bf16 v[204:207], v[44:47], v[224:227], v[204:207]\n\t" \
    "ds_read_b128 v[240:243], v252 offset:1024\n\t" \
    "v_mfma_f32_16x16x32_bf16 v[208:211], v[52:55], v[224:227], v[208:211]\n\t" \
    "v_mfma_f32_16x16x32_bf16 v[200:203], v[248:251], v[224:227], v[200:203]\n\t" \
    "s_waitcnt lgkmcnt(3)\n\t" \
    "v_mfma_f32_16x16x32_bf16 v[216:219], v[44:47], v[228:231], v[216:219]\n\t" \
    "ds_read_b128 v[224:227], v252 offset:17408\n\t" \
    "v_mfma_f32_16x16x32_bf16 v[220:223], v[52:55], v[228:231], v[220:223]\n\t" \
    "v_mfma_f32_16x16x32_bf16 v[212:215], v[248:251], v[228:231], v[212:215]\n\t" \
    "s_waitcnt lgkmcnt(3)\n\t" \
    "v_mfma_f32_16x16x32_bf16 v[204:207], v[64:67], v[232:235], v[204:207]\n\t" \
    "v_and_b32 v252, 0x3ff, %[xa]\n\t" \
    "ds_read_b128 v[228:231], v252 offset:43008\n\t" \
    "v_mov_b64 v[244:245], 0\n\t" \
    "v_mfma_f32_16x16x32_bf16 v[208:211], v[72:75], v[232:235], v[208:211]\n\t" \
    "v_mov_b64 v[246:247], 0\n\t" \
    "v_mfma_f32_16x16x32_bf16 v[200:203], v[56:59], v[232:235], v[200:203]\n\t" \
    "v_mov_b64 v[248:249], 0\n\t" \
    "s_waitcnt lgkmcnt(3)\n\t" \
    "v_mfma_f32_16x16x32_bf16 v[216:219], v[64:67], v[236:239], v[216:219]\n\t" \
    "ds_read_b128 v[232:235], v252 offset:44032\n\t" \
    "v_mov_b64 v[250:251], 0\n\t" \
    "v_mfma_f32_16x16x32_bf16 v[220:223], v[72:75], v[236:239], v[220:223]\n\t" \
    "v_mfma_f32_16x16x32_bf16 v[212:215], v[56:59], v[236:239], v[212:215]\n\t" \
    "s_waitcnt lgkmcnt(3)\n\t" \
    "v_mfma_f32_16x16x32_bf16 v[204:207], v[68:71], v[240:243], v[204:207]\n\t" \
    "ds_read_b128 v[236:239], v252 offset:47104\n\t" \
    "v_mfma_f32_16x16x32_bf16 v[208:211], v[76:79], v[240:243], v[208:211]\n\t" \
    "v_mfma_f32_16x16x32_bf16 v[200:203], v[60:63], v[240:243], v[200:203]\n\t" \
    "s_waitcnt lgkmcnt(3)\n\t" \
    "v_mfma_f32_16x16x32_bf16 v[216:219], v[68:71], v[224:227], v[216:219]\n\t" \
    "ds_read_b128 v[240:243], v252 offset:48128\n\t" \
    "v_mfma_f32_16x16x32_bf16 v[220:223], v[76:79], v[224:227], v[220:223]\n\t" \
    "v_mfma_f32_16x16x32_bf16 v[212:215], v[60:63], v[224:227], v[212:215]\n\t" \
    "s_waitcnt lgkmcnt(3)\n\t" \
    "v_mfma_f32_16x16x32_bf16 v[204:207], v[184:187], v[228:231], v[204:207]\n\t" \
    "v_add_u32 v252, %[qs], v252\n\t" \
    "v_mfma_f32_16x16x32_bf16 v[208:211], v[192:195], v[228:231], v[208:211]\n\t" \
    "v_mov_b64 v[224:225], 0\n\t" \
    "v_mfma_f32_16x16x32_bf16 v[200:203], v[176:179], v[228:231], v[200:203]\n\t" \
    "v_mov_b64 v[226:227], 0\n\t" \
    "s_waitcnt lgkmcnt(2)\n\t" \
    "v_mfma_f32_16x16x32_bf16 v[204:207], v[188:191], v[232:235], v[204:207]\n\t" \
    "v_mfma_f32_16x16x32_bf16 v[208:211], v[196:199], v[232:235], v[208:211]\n\t" \
    "v_mfma_f32_16x16x32_bf16 v[200:203], v[180:183], v[232:235], v[200:203]\n\t" \
    "s_waitcnt lgkmcnt(1)\n\t" \
    "v_mfma_f32_16x16x32_bf16 v[216:219], v[184:187], v[236:239], v[216:219]\n\t" \
    "ds_read_b128 v[228:231], %[wl] offset:2048\n\t" \
    "ds_read_b128 v[232:235], %[wl] offset:3072\n\t" \
    "v_mfma_f32_16x16x32_bf16 v[220:223], v[192:195], v[236:239], v[220:223]\n\t" \
    "v_cvt_pk_bf16_f32 v204, v204, v205\n\t" \
    "v_cvt_pk_bf16_f32 v205, v206, v207\n\t" \
    "v_mfma_f32_16x16x32_bf16 v[212:215], v[176:179], v[236:239], v[212:215]\n\t" \
    "ds_write_b64 v252, v[204:205] offset:1024\n\t" \
    "v_cvt_pk_bf16_f32 v208, v208, v209\n\t" \
    "s_waitcnt lgkmcnt(3)\n\t" \
    "v_mfma_f32_16x16x32_bf16 v[216:219], v[188:191], v[240:243], v[216:219]\n\t" \
    "v_cvt_pk_bf16_f32 v209, v210, v211\n\t" \
    "ds_write_b64 v252, v[208:209] offset:2048\n\t" \
    "v_mfma_f32_16x16x32_bf16 v[220:223], v[196:199], v[240:243], v[220:223]\n\t" \
    "v_cvt_pk_bf16_f32 v200, v200, v201\n\t" \
    "v_cvt_pk_bf16_f32 v201, v202, v203\n\t" \
    "v_mfma_f32_16x16x32_bf16 v[212:215], v[180:183], v[240:243], v[212:215]\n\t" \
    "ds_write_b64 v252, v[200:201] offset:0\n\t" \
    "ds_read_b128 v[200:203], %[wl] offset:0\n\t" \
    "ds_read_b128 v[204:207], %[wl] offset:1024\n\t" \
    "v_cvt_pk_bf16_f32 v216, v216, v217\n\t" \
    "v_cvt_pk_bf16_f32 v217, v218, v219\n\t" \
    "ds_write_b64 v252, v[216:217] offset:7168\n\t" \
    "v_cvt_pk_bf16_f32 v220, v220, v221\n\t" \
    "v_cvt_pk_bf16_f32 v221, v222, v223\n\t" \
    "ds_write_b64 v252, v[220:221] offset:8192\n\t" \
    "v_cvt_pk_bf16_f32 v212, v212, v213\n\t" \
    "v_cvt_pk_bf16_f32 v213, v214, v215\n\t" \
    "ds_write_b64 v252, v[212:213] offset:6144\n\t" \
    "s_waitcnt lgkmcnt(0)\n\t"
#define ASM_CRIT1_3 \
    "s_branch .Lc0%=\n\t" \
    ".Lcr%=:\n\t" \
    "s_cmp_eq_u32 %[n], 0\n\t" \
    "s_cbranch_scc1 .Lcb%=\n\t" \
    "s_sub_u32 %[n], %[n], 1\n\t" \
    "v_max_u32 v243, v208, v209\n\t" \
    "v_max3_u32 v243, v210, v211, v243\n\t" \
    "v_cmp_eq_u32 vcc, -1, v243\n\t" \
    "s_cbranch_vccz .Lcs0_%=\n\t" \
    "buffer_load_dwordx4 v[208:211], %[ab], %[rs], %[s0] offen sc1\n\t" \
    ".Lcs0_%=:\n\t" \
    "v_max_u32 v243, v236, v237\n\t" \
    "v_max3_u32 v243, v238, v239, v243\n\t" \
    "v_cmp_eq_u32 vcc, -1, v243\n\t" \
    "s_cbranch_vccz .Lcs1_%=\n\t" \
    "buffer_load_dwordx4 v[236:239], %[ab], %[rs], %[s1] offen sc1\n\t" \
    ".Lcs1_%=:\n\t" \
    "v_max_u32 v243, v220, v221\n\t" \
    "v_max3_u32 v243, v222, v223, v243\n\t" \
    "v_cmp_eq_u32 vcc, -1, v243\n\t" \
    "s_cbranch_vccz .Lca%=\n\t" \
    "buffer_load_dwordx4 v[220:223], %[ab], %[rs], %[s2] offen sc1\n\t" \
    "s_branch .Lca%=\n\t" \
    ".Lc0%=:\n\t" \
    "buffer_load_dwordx4 v[208:211], %[ab], %[rs], %[s0] offen sc1\n\t" \
    "buffer_load_dwordx4 v[236:239], %[ab], %[rs], %[s1] offen sc1\n\t" \
    "buffer_load_dwordx4 v[220:223], %[ab], %[rs], %[s2] offen sc1\n\t" \
    "v_bfe_i32 v241, %[ab], 7, 1\n\t" \
    "v_and_b32 v241, 0xaaaa, v241\n\t" \
    "v_or_b32 v241, 0x4444, v241\n\t" \
    "v_xor_b32 v212, 0x400, %[ab]\n\t" \
    "v_xor_b32 v216, 0x800, %[ab]\n\t" \
    "v_xor_b32 v242, 0xc00, %[ab]\n\t" \
    "v_xor_b32 v240, 0x1400, %[ab]\n\t" \
    ".Lca%=:\n\t" \
    "s_waitcnt vmcnt(0)\n\t" \
    "ds_write_b128 %[ab], v[208:211] offset:0\n\t" \
    "ds_write_b128 %[ab], v[236:239] offset:4096\n\t" \
    "ds_write_b128 %[ab], v[220:223] offset:8192\n\t" \
    "v_max_u32 v243, v208, v209\n\t" \
    "v_max3_u32 v243, v210, v211, v243\n\t" \
    "v_max3_u32 v243, v236, v237, v243\n\t" \
    "v_max3_u32 v243, v238, v239, v243\n\t" \
    "v_max3_u32 v243, v220, v221, v243\n\t" \
    "v_max3_u32 v243, v222, v223, v243\n\t" \
    "v_cmp_eq_u32 vcc, -1, v243\n\t" \
    "s_cbranch_vccnz .Lcr%=\n\t" \
    ".Lcb%=:\n\t" \
    PRIUP \
    ASM_ST2 \
    "v_smfmac_f32_16x16x64_bf16 v[224:227], v[208:211], v[200:207], v241\n\t" \
    "ds_read_b128 v[200:203], %[wl] offset:4096\n\t" \
    "ds_read_b128 v[204:207], %[wl] offset:5120\n\t" \
    "v_smfmac_f32_16x16x64_bf16 v[244:247], v[208:211], v[0:7], v241\n\t" \
    "v_smfmac_f32_16x16x64_bf16 v[248:251], v[208:211], v[8:15], v241\n\t" \
    "s_waitcnt lgkmcnt(2)\n\t" \
    "s_barrier\n\t" ASM_ST \
    "ds_read_b128 v[212:215], v212 offset:0\n\t" \
    "ds_read_b128 v[216:219], v216 offset:0\n\t" \
    "ds_read_b128 v[220:223], v242 offset:0\n\t" \
    "ds_read_b128 v[208:211], v240 offset:0\n\t" \
    "v_smfmac_f32_16x16x64_bf16 v[244:247], v[236:239], v[80:87], v241\n\t" \
    "v_xor_b32 v240, 0x1800, %[ab]\n\t" \
    "v_smfmac_f32_16x16x64_bf16 v[248:251], v[236:239], v[88:95], v241\n\t" \
    "s_waitcnt lgkmcnt(4)\n\t" \
    "v_smfmac_f32_16x16x64_bf16 v[224:227], v[236:239], v[200:207], v241\n\t" \
    "ds_read_b128 v[200:203], %[wl] offset:6144\n\t" \
    "ds_read_b128 v[204:207], %[wl] offset:7168\n\t" \
    "ds_read_b128 v[236:239], v240 offset:0\n\t" \
    "s_waitcnt lgkmcnt(6)\n\t" \
    "v_smfmac_f32_16x16x64_bf16 v[224:227], v[212:215], v[16:23], v241\n\t" \
    "v_smfmac_f32_16x16x64_bf16 v[244:247], v[212:215], v[24:31], v241\n\t" \
    "v_xor_b32 v240, 0x1c00, %[ab]\n\t" \
    "v_smfmac_f32_16x16x64_bf16 v[248:251], v[212:215], v[32:39], v241\n\t" \
    "ds_read_b128 v[212:215], v240 offset:0\n\t" \
    "s_waitcnt lgkmcnt(6)\n\t" \
    "v_smfmac_f32_16x16x64_bf16 v[224:227], v[216:219], v[228:235], v241\n\t" \
    "ds_read_b128 v[228:231], %[wl] offset:8192\n\t" \
    "ds_read_b128 v[232:235], %[wl] offset:9216\n\t" \
    "v_smfmac_f32_16x16x64_bf16 v[244:247], v[216:219], v[40:47], v241\n\t" \
    "v_and_b32 v240, 0x3ff, %[ab]\n\t" \
    "v_smfmac_f32_16x16x64_bf16 v[248:251], v[216:219], v[48:55], v241\n\t" \
    "ds_read_b128 v[216:219], v240 offset:8192\n\t" \
    "s_waitcnt lgkmcnt(8)\n\t" \
    "v_smfmac_f32_16x16x64_bf16 v[224:227], v[220:223], v[56:63], v241\n\t" \
    "v_smfmac_f32_16x16x64_bf16 v[244:247], v[220:223], v[64:71], v241\n\t" \
    "v_smfmac_f32_16x16x64_bf16 v[248:251], v[220:223], v[72:79], v241\n\t" \
    "ds_read_b128 v[220:223], v240 offset:9216\n\t" \
    "s_waitcnt lgkmcnt(8)\n\t" \
    "v_smfmac_f32_16x16x64_bf16 v[244:247], v[208:211], v[104:111], v241\n\t" \
    "s_waitcnt lgkmcnt(5)\n\t" \
    "v_smfmac_f32_16x16x64_bf16 v[244:247], v[236:239], v[120:127], v241\n\t" \
    "s_waitcnt lgkmcnt(4)\n\t" \
    "v_smfmac_f32_16x16x64_bf16 v[244:247], v[212:215], v[144:151], v241\n\t" \
    "s_waitcnt lgkmcnt(1)\n\t" \
    "v_smfmac_f32_16x16x64_bf16 v[244:247], v[216:219], v[160:167], v241\n\t" \
    "s_waitcnt lgkmcnt(0)\n\t" \
    "v_smfmac_f32_16x16x64_bf16 v[244:247], v[220:223], v[184:191], v241\n\t" \
    "v_smfmac_f32_16x16x64_bf16 v[248:251], v[208:211], v[112:119], v241\n\t" \
    "v_smfmac_f32_16x16x64_bf16 v[248:251], v[236:239], v[128:135], v241\n\t" \
    "v_smfmac_f32_16x16x64_bf16 v[248:251], v[212:215], v[152:159], v241\n\t" \
    "v_permlane32_swap_b32 v244, v246\n\t" \
    "v_permlane32_swap_b32 v245, v247\n\t" \
    "v_smfmac_f32_16x16x64_bf16 v[248:251], v[216:219], v[168:175], v241\n\t" \
    "v_add_f32 v244, v244, v246\n\t" \
    "v_add_f32 v245, v245, v247\n\t" \
    "v_smfmac_f32_16x16x64_bf16 v[248:251], v[220:223], v[192:199], v241\n\t" \
    "v_cvt_pk_bf16_f32 v244, v244, v245\n\t" \
    "ds_write_b16 %[dst], v244 offset:512\n\t" \
    "ds_write_b16_d16_hi %[dst], v244 offset:528\n\t" \
    "v_smfmac_f32_16x16x64_bf16 v[224:227], v[208:211], v[96:103], v241\n\t" \
    "v_smfmac_f32_16x16x64_bf16 v[224:227], v[236:239], v[200:207], v241\n\t" \
    "v_smfmac_f32_16x16x64_bf16 v[224:227], v[212:215], v[136:143], v241\n\t" \
    "v_permlane32_swap_b32 v248, v250\n\t" \
    "v_permlane32_swap_b32 v249, v251\n\t" \
    "v_smfmac_f32_16x16x64_bf16 v[224:227], v[216:219], v[228:235], v241\n\t" \
    "v_add_f32 v248, v248, v250\n\t" \
    "v_add_f32 v249, v249, v251\n\t" \
    "v_smfmac_f32_16x16x64_bf16 v[224:227], v[220:223], v[176:183], v241\n\t" \
    "v_cvt_pk_bf16_f32 v248, v248, v249\n\t" \
    "ds_write_b16 %[dst], v248 offset:1024\n\t" \
    "ds_write_b16_d16_hi %[dst], v248 offset:1040\n\t" \
    "s_nop 4\n\t" \
    "v_permlane32_swap_b32 v224, v226\n\t" \
    "v_permlane32_swap_b32 v225, v227\n\t" \
    "v_add_f32 v224, v224, v226\n\t" \
    "v_add_f32 v225, v225, v227\n\t" \
    "v_cvt_pk_bf16_f32 v224, v224, v225\n\t" \
    "ds_write_b16 %[dst], v224 offset:0\n\t" \
    "ds_write_b16_d16_hi %[dst], v224 offset:16\n\t"
#define ASM_CRIT1_2 \
    "s_branch .Lc0%=\n\t" \
    ".Lcr%=:\n\t" \
    "s_cmp_eq_u32 %[n], 0\n\t" \
    "s_cbranch_scc1 .Lcb%=\n\t" \
    "s_sub_u32 %[n], %[n], 1\n\t" \
    "v_max_u32 v243, v208, v209\n\t" \
    "v_max3_u32 v243, v210, v211, v243\n\t" \
    "v_cmp_eq_u32 vcc, -1, v243\n\t" \
    "s_cbranch_vccz .Lcs0_%=\n\t" \
    "buffer_load_dwordx4 v[208:211], %[ab], %[rs], %[s0] offen sc1\n\t" \
    ".Lcs0_%=:\n\t" \
    "v_max_u32 v243, v236, v237\n\t" \
    "v_max3_u32 v243, v238, v239, v243\n\t" \
    "v_cmp_eq_u32 vcc, -1, v243\n\t" \
    "s_cbranch_vccz .Lca%=\n\t" \
    "buffer_load_dwordx4 v[236:239], %[ab], %[rs], %[s1] offen sc1\n\t" \
    "s_branch .Lca%=\n\t" \
    ".Lc0%=:\n\t" \
    "buffer_load_dwordx4 v[208:211], %[ab], %[rs], %[s0] offen sc1\n\t" \
    "buffer_load_dwordx4 v[236:239], %[ab], %[rs], %[s1] offen sc1\n\t" \
    "v_bfe_i32 v241, %[ab], 7, 1\n\t" \
    "v_and_b32 v241, 0xaaaa, v241\n\t" \
    "v_or_b32 v241, 0x4444, v241\n\t" \
    "v_xor_b32 v212, 0x400, %[ab]\n\t" \
    "v_xor_b32 v216, 0x800, %[ab]\n\t" \
    "v_xor_b32 v242, 0xc00, %[ab]\n\t" \
    "v_xor_b32 v240, 0x1400, %[ab]\n\t" \
    ".Lca%=:\n\t" \
    "s_waitcnt vmcnt(0)\n\t" \
    "ds_write_b128 %[ab], v[208:211] offset:0\n\t" \
    "ds_write_b128 %[ab], v[236:239] offset:4096\n\t" \
    "v_max_u32 v243, v208, v209\n\t" \
    "v_max3_u32 v243, v210, v211, v243\n\t" \
    "v_max3_u32 v243, v236, v237, v243\n\t" \
    "v_max3_u32 v243, v238, v239, v243\n\t" \
    "v_cmp_eq_u32 vcc, -1, v243\n\t" \
    "s_cbranch_vccnz .Lcr%=\n\t" \
    ".Lcb%=:\n\t" \
    PRIUP \
    ASM_ST2 \
    "v_smfmac_f32_16x16x64_bf16 v[224:227], v[208:211], v[200:207], v241\n\t" \
    "ds_read_b128 v[200:203], %[wl] offset:4096\n\t" \
    "ds_read_b128 v[204:207], %[wl] offset:5120\n\t" \
    "v_smfmac_f32_16x16x64_bf16 v[244:247], v[208:211], v[0:7], v241\n\t" \
    "v_smfmac_f32_16x16x64_bf16 v[248:251], v[208:211], v[8:15], v241\n\t" \
    "s_waitcnt lgkmcnt(2)\n\t" \
    "s_barrier\n\t" ASM_ST \
    "ds_read_b128 v[212:215], v212 offset:0\n\t" \
    "ds_read_b128 v[216:219], v216 offset:0\n\t" \
    "ds_read_b128 v[220:223], v242 offset:0\n\t" \
    "ds_read_b128 v[208:211], v240 offset:0\n\t" \
    "v_smfmac_f32_16x16x64_bf16 v[244:247], v[236:239], v[80:87], v241\n\t" \
    "v_xor_b32 v240, 0x1800, %[ab]\n\t" \
    "v_smfmac_f32_16x16x64_bf16 v[248:251], v[236:239], v[88:95], v241\n\t" \
    "s_waitcnt lgkmcnt(4)\n\t" \
    "v_smfmac_f32_16x16x64_bf16 v[224:227], v[236:239], v[200:207], v241\n\t" \
    "ds_read_b128 v[200:203], %[wl] offset:6144\n\t" \
    "ds_read_b128 v[204:207], %[wl] offset:7168\n\t" \
    "ds_read_b128 v[236:239], v240 offset:0\n\t" \
    "s_waitcnt lgkmcnt(6)\n\t" \
    "v_smfmac_f32_16x16x64_bf16 v[224:227], v[212:215], v[16:23], v241\n\t" \
    "v_smfmac_f32_16x16x64_bf16 v[244:247], v[212:215], v[24:31], v241\n\t" \
    "v_xor_b32 v240, 0x1c00, %[ab]\n\t" \
    "v_smfmac_f32_16x16x64_bf16 v[248:251], v[212:215], v[32:39], v241\n\t" \
    "ds_read_b128 v[212:215], v240 offset:0\n\t" \
    "s_waitcnt lgkmcnt(6)\n\t" \
    "v_smfmac_f32_16x16x64_bf16 v[224:227], v[216:219], v[228:235], v241\n\t" \
    "ds_read_b128 v[228:231], %[wl] offset:8192\n\t" \
    "ds_read_b128 v[232:235], %[wl] offset:9216\n\t" \
    "v_smfmac_f32_16x16x64_bf16 v[244:247], v[216:219], v[40:47], v241\n\t" \
    "v_and_b32 v240, 0x3ff, %[ab]\n\t" \
    "v_smfmac_f32_16x16x64_bf16 v[248:251], v[216:219], v[48:55], v241\n\t" \
    "ds_read_b128 v[216:219], v240 offset:8192\n\t" \
    "s_waitcnt lgkmcnt(8)\n\t" \
    "v_smfmac_f32_16x16x64_bf16 v[224:227], v[220:223], v[56:63], v241\n\t" \
    "v_smfmac_f32_16x16x64_bf16 v[244:247], v[220:223], v[64:71], v241\n\t" \
    "v_smfmac_f32_16x16x64_bf16 v[248:251], v[220:223], v[72:79], v241\n\t" \
    "ds_read_b128 v[220:223], v240 offset:9216\n\t" \
    "s_waitcnt lgkmcnt(8)\n\t" \
    "v_smfmac_f32_16x16x64_bf16 v[244:247], v[208:211], v[104:111], v241\n\t" \
    "s_waitcnt lgkmcnt(5)\n\t" \
    "v_smfmac_f32_16x16x64_bf16 v[244:247], v[236:239], v[120:127], v241\n\t" \
    "s_waitcnt lgkmcnt(4)\n\t" \
    "v_smfmac_f32_16x16x64_bf16 v[244:247], v[212:215], v[144:151], v241\n\t" \
    "s_waitcnt lgkmcnt(1)\n\t" \
    "v_smfmac_f32_16x16x64_bf16 v[244:247], v[216:219], v[160:167], v241\n\t" \
    "s_waitcnt lgkmcnt(0)\n\t" \
    "v_smfmac_f32_16x16x64_bf16 v[244:247], v[220:223], v[184:191], v241\n\t" \
    "v_smfmac_f32_16x16x64_bf16 v[248:251], v[208:211], v[112:119], v241\n\t" \
    "v_smfmac_f32_16x16x64_bf16 v[248:251], v[236:239], v[128:135], v241\n\t" \
    "v_smfmac_f32_16x16x64_bf16 v[248:251], v[212:215], v[152:159], v241\n\t" \
    "v_permlane32_swap_b32 v244, v246\n\t" \
    "v_permlane32_swap_b32 v245, v247\n\t" \
    "v_smfmac_f32_16x16x64_bf16 v[248:251], v[216:219], v[168:175], v241\n\t" \
    "v_add_f32 v244, v244, v246\n\t" \
    "v_add_f32 v245, v245, v247\n\t" \
    "v_smfmac_f32_16x16x64_bf16 v[248:251], v[220:223], v[192:199], v241\n\t" \
    "v_cvt_pk_bf16_f32 v244, v244, v245\n\t" \
    "ds_write_b16 %[dst], v244 offset:512\n\t" \
    "ds_write_b16_d16_hi %[dst], v244 offset:528\n\t" \
    "v_smfmac_f32_16x16x64_bf16 v[224:227], v[208:211], v[96:103], v241\n\t" \
    "v_smfmac_f32_16x16x64_bf16 v[224:227], v[236:239], v[200:207], v241\n\t" \
    "v_smfmac_f32_16x16x64_bf16 v[224:227], v[212:215], v[136:143], v241\n\t" \
    "v_permlane32_swap_b32 v248, v250\n\t" \
    "v_permlane32_swap_b32 v249, v251\n\t" \
    "v_smfmac_f32_16x16x64_bf16 v[224:227], v[216:219], v[228:235], v241\n\t" \
    "v_add_f32 v248, v248, v250\n\t" \
    "v_add_f32 v249, v249, v251\n\t" \
    "v_smfmac_f32_16x16x64_bf16 v[224:227], v[220:223], v[176:183], v241\n\t" \
    "v_cvt_pk_bf16_f32 v248, v248, v249\n\t" \
    "ds_write_b16 %[dst], v248 offset:1024\n\t" \
    "ds_write_b16_d16_hi %[dst], v248 offset:1040\n\t" \
    "s_nop 4\n\t" \
    "v_permlane32_swap_b32 v224, v226\n\t" \
    "v_permlane32_swap_b32 v225, v227\n\t" \
    "v_add_f32 v224, v224, v226\n\t" \
    "v_add_f32 v225, v225, v227\n\t" \
    "v_cvt_pk_bf16_f32 v224, v224, v225\n\t" \
    "ds_write_b16 %[dst], v224 offset:0\n\t" \
    "ds_write_b16_d16_hi %[dst], v224 offset:16\n\t"
#define ASM_CRIT1_N \
    "v_bfe_i32 v241, %[ab], 7, 1\n\t" \
    "v_and_b32 v241, 0xaaaa, v241\n\t" \
    "v_or_b32 v241, 0x4444, v241\n\t" \
    "v_xor_b32 v212, 0x400, %[ab]\n\t" \
    "v_xor_b32 v216, 0x800, %[ab]\n\t" \
    "v_xor_b32 v242, 0xc00, %[ab]\n\t" \
    "v_xor_b32 v240, 0x1400, %[ab]\n\t" \
    "v_xor_b32 v236, 0x1000, %[ab]\n\t" \
    "s_waitcnt lgkmcnt(0)\n\t" \
    "s_barrier\n\t" ASM_ST \
    "ds_read_b128 v[208:211], %[ab] offset:0\n\t" \
    "ds_read_b128 v[212:215], v212 offset:0\n\t" \
    "ds_read_b128 v[216:219], v216 offset:0\n\t" \
    "ds_read_b128 v[220:223], v242 offset:0\n\t" \
    "ds_read_b128 v[236:239], v236 offset:0\n\t" \
    "s_waitcnt lgkmcnt(4)\n\t" \
    "v_smfmac_f32_16x16x64_bf16 v[224:227], v[208:211], v[200:207], v241\n\t" \
    "ds_read_b128 v[200:203], %[wl] offset:4096\n\t" \
    "ds_read_b128 v[204:207], %[wl] offset:5120\n\t" \
    "v_smfmac_f32_16x16x64_bf16 v[244:247], v[208:211], v[0:7], v241\n\t" \
    "v_smfmac_f32_16x16x64_bf16 v[248:251], v[208:211], v[8:15], v241\n\t" \
    "ds_read_b128 v[208:211], v240 offset:0\n\t" \
    "s_waitcnt lgkmcnt(6)\n\t" \
    "v_smfmac_f32_16x16x64_bf16 v[224:227], v[212:215], v[16:23], v241\n\t" \
    "v_smfmac_f32_16x16x64_bf16 v[244:247], v[212:215], v[24:31], v241\n\t" \
    "v_xor_b32 v240, 0x1800, %[ab]\n\t" \
    "v_smfmac_f32_16x16x64_bf16 v[248:251], v[212:215], v[32:39], v241\n\t" \
    "ds_read_b128 v[212:215], v240 offset:0\n\t" \
    "s_waitcnt lgkmcnt(6)\n\t" \
    "v_smfmac_f32_16x16x64_bf16 v[224:227], v[216:219], v[228:235], v241\n\t" \
    "ds_read_b128 v[228:231], %[wl] offset:6144\n\t" \
    "ds_read_b128 v[232:235], %[wl] offset:7168\n\t" \
    "v_smfmac_f32_16x16x64_bf16 v[244:247], v[216:219], v[40:47], v241\n\t" \
    "v_xor_b32 v240, 0x1c00, %[ab]\n\t" \
    "v_smfmac_f32_16x16x64_bf16 v[248:251], v[216:219], v[48:55], v241\n\t" \
    "ds_read_b128 v[216:219], v240 offset:0\n\t" \
    "s_waitcnt lgkmcnt(8)\n\t" \
    "v_smfmac_f32_16x16x64_bf16 v[224:227], v[220:223], v[56:63], v241\n\t" \
    "v_smfmac_f32_16x16x64_bf16 v[244:247], v[220:223], v[64:71], v241\n\t" \
    "v_and_b32 v240, 0x3ff, %[ab]\n\t" \
    "v_smfmac_f32_16x16x64_bf16 v[248:251], v[220:223], v[72:79], v241\n\t" \
    "ds_read_b128 v[220:223], v240 offset:8192\n\t" \
    "s_waitcnt lgkmcnt(6)\n\t" \
    "v_smfmac_f32_16x16x64_bf16 v[224:227], v[236:239], v[200:207], v241\n\t" \
    "ds_read_b128 v[200:203], %[wl] offset:8192\n\t" \
    "ds_read_b128 v[204:207], %[wl] offset:9216\n\t" \
    "v_smfmac_f32_16x16x64_bf16 v[244:247], v[236:239], v[80:87], v241\n\t" \
    "v_smfmac_f32_16x16x64_bf16 v[248:251], v[236:239], v[88:95], v241\n\t" \
    "ds_read_b128 v[236:239], v240 offset:9216\n\t" \
    "s_waitcnt lgkmcnt(8)\n\t" \
    "v_smfmac_f32_16x16x64_bf16 v[244:247], v[208:211], v[104:111], v241\n\t" \
    "s_waitcnt lgkmcnt(7)\n\t" \
    "v_smfmac_f32_16x16x64_bf16 v[244:247], v[212:215], v[120:127], v241\n\t" \
    "s_waitcnt lgkmcnt(4)\n\t" \
    "v_smfmac_f32_16x16x64_bf16 v[244:247], v[216:219], v[144:151], v241\n\t" \
    "s_waitcnt lgkmcnt(3)\n\t" \
    "v_smfmac_f32_16x16x64_bf16 v[244:247], v[220:223], v[160:167], v241\n\t" \
    "s_waitcnt lgkmcnt(0)\n\t" \
    "v_smfmac_f32_16x16x64_bf16 v[244:247], v[236:239], v[184:191], v241\n\t" \
    "v_smfmac_f32_16x16x64_bf16 v[248:251], v[208:211], v[112:119], v241\n\t" \
    "v_smfmac_f32_16x16x64_bf16 v[248:251], v[212:215], v[128:135], v241\n\t" \
    "v_smfmac_f32_16x16x64_bf16 v[248:251], v[216:219], v[152:159], v241\n\t" \
    "v_permlane32_swap_b32 v244, v246\n\t" \
    "v_permlane32_swap_b32 v245, v247\n\t" \
    "v_smfmac_f32_16x16x64_bf16 v[248:251], v[220:223], v[168:175], v241\n\t" \
    "v_add_f32 v244, v244, v246\n\t" \
    "v_add_f32 v245, v245, v247\n\t" \
    "v_smfmac_f32_16x16x64_bf16 v[248:251], v[236:239], v[192:199], v241\n\t" \
    "v_cvt_pk_bf16_f32 v244, v244, v245\n\t" \
    "ds_write_b16 %[dst], v244 offset:512\n\t" \
    "ds_write_b16_d16_hi %[dst], v244 offset:528\n\t" \
    "v_smfmac_f32_16x16x64_bf16 v[224:227], v[208:211], v[96:103], v241\n\t" \
    "v_smfmac_f32_16x16x64_bf16 v[224:227], v[212:215], v[228:235], v241\n\t" \
    "v_smfmac_f32_16x16x64_bf16 v[224:227], v[216:219], v[136:143], v241\n\t" \
    "v_permlane32_swap_b32 v248, v250\n\t" \
    "v_permlane32_swap_b32 v249, v251\n\t" \
    "v_smfmac_f32_16x16x64_bf16 v[224:227], v[220:223], v[200:207], v241\n\t" \
    "v_add_f32 v248, v248, v250\n\t" \
    "v_add_f32 v249, v249, v251\n\t" \
    "v_smfmac_f32_16x16x64_bf16 v[224:227], v[236:239], v[176:183], v241\n\t" \
    "v_cvt_pk_bf16_f32 v248, v248, v249\n\t" \
    "ds_write_b16 %[dst], v248 offset:1024\n\t" \
    "ds_write_b16_d16_hi %[dst], v248 offset:1040\n\t" \
    "s_nop 4\n\t" \
    "v_permlane32_swap_b32 v224, v226\n\t" \
    "v_permlane32_swap_b32 v225, v227\n\t" \
    "v_add_f32 v224, v224, v226\n\t" \
    "v_add_f32 v225, v225, v227\n\t" \
    "v_cvt_pk_bf16_f32 v224, v224, v225\n\t" \
    "ds_write_b16 %[dst], v224 offset:0\n\t" \
    "ds_write_b16_d16_hi %[dst], v224 offset:16\n\t"
#define ASM_CRIT1_V3 \
    "s_branch .Lc0%=\n\t" \
    ".Lcr%=:\n\t" \
    "s_cmp_eq_u32 %[n], 0\n\t" \
    "s_cbranch_scc1 .Lcb%=\n\t" \
    "s_sub_u32 %[n], %[n], 1\n\t" \
    "v_max_u32 v243, v208, v209\n\t" \
    "v_max3_u32 v243, v210, v211, v243\n\t" \
    "v_cmp_eq_u32 vcc, -1, v243\n\t" \
    "s_cbranch_vccz .Lcs0_%=\n\t" \
    "buffer_load_dwordx4 v[208:211], %[ab], %[rs], %[s0] offen sc1\n\t" \
    ".Lcs0_%=:\n\t" \
    "v_max_u32 v243, v236, v237\n\t" \
    "v_max3_u32 v243, v238, v239, v243\n\t" \
    "v_cmp_eq_u32 vcc, -1, v243\n\t" \
    "s_cbranch_vccz .Lcs1_%=\n\t" \
    "buffer_load_dwordx4 v[236:239], %[ab], %[rs], %[s1] offen sc1\n\t" \
    ".Lcs1_%=:\n\t" \
    "v_max_u32 v243, v220, v221\n\t" \
    "v_max3_u32 v243, v222, v223, v243\n\t" \
    "v_cmp_eq_u32 vcc, -1, v243\n\t" \
    "s_cbranch_vccz .Lca%=\n\t" \
    "buffer_load_dwordx4 v[220:223], %[ab], %[rs], %[s2] offen sc1\n\t" \
    "s_branch .Lca%=\n\t" \
    ".Lc0%=:\n\t" \
    "buffer_load_dwordx4 v[208:211], %[ab], %[rs], %[s0] offen sc1\n\t" \
    "buffer_load_dwordx4 v[236:239], %[ab], %[rs], %[s1] offen sc1\n\t" \
    "buffer_load_dwordx4 v[220:223], %[ab], %[rs], %[s2] offen sc1\n\t" \
    "v_bfe_i32 v241, %[ab], 7, 1\n\t" \
    "v_and_b32 v241, 0xaaaa, v241\n\t" \
    "v_or_b32 v241, 0x4444, v241\n\t" \
    "v_xor_b32 v212, 0x400, %[ab]\n\t" \
    "v_xor_b32 v216, 0x800, %[ab]\n\t" \
    "v_xor_b32 v242, 0xc00, %[ab]\n\t" \
    "v_xor_b32 v240, 0x1400, %[ab]\n\t" \
    ".Lca%=:\n\t" \
    "s_waitcnt vmcnt(0)\n\t" \
    "ds_write_b128 %[ab], v[208:211] offset:0\n\t" \
    "ds_write_b128 %[ab], v[236:239] offset:4096\n\t" \
    "ds_write_b128 %[ab], v[220:223] offset:8192\n\t" \
    "v_max_u32 v243, v208, v209\n\t" \
    "v_max3_u32 v243, v210, v211, v243\n\t" \
    "v_max3_u32 v243, v236, v237, v243\n\t" \
    "v_max3_u32 v243, v238, v239, v243\n\t" \
    "v_max3_u32 v243, v220, v221, v243\n\t" \
    "v_max3_u32 v243, v222, v223, v243\n\t" \
    "v_cmp_eq_u32 vcc, -1, v243\n\t" \
    "s_cbranch_vccnz .Lcr%=\n\t" \
    ".Lcb%=:\n\t" \
    PRIUP \
    ASM_ST2 \
    "v_smfmac_f32_16x16x64_bf16 v[224:227], v[208:211], v[200:207], v241\n\t" \
    "ds_read_b128 v[200:203], %[wl] offset:4096\n\t" \
    "ds_read_b128 v[204:207], %[wl] offset:5120\n\t" \
    "v_smfmac_f32_16x16x64_bf16 v[244:247], v[208:211], v[0:7], v241\n\t" \
    "v_smfmac_f32_16x16x64_bf16 v[248:251], v[208:211], v[8:15], v241\n\t" \
    "s_waitcnt lgkmcnt(2)\n\t" \
    "buffer_load_dword %[fv], %[fv], %[rs], %[fs] offen sc1\n\t" \
    "s_barrier\n\t" ASM_ST \
    "ds_read_b128 v[212:215], v212 offset:0\n\t" \
    "ds_read_b128 v[216:219], v216 offset:0\n\t" \
    "ds_read_b128 v[220:223], v242 offset:0\n\t" \
    "ds_read_b128 v[208:211], v240 offset:0\n\t" \
    "v_smfmac_f32_16x16x64_bf16 v[244:247], v[236:239], v[80:87], v241\n\t" \
    "v_xor_b32 v240, 0x1800, %[ab]\n\t" \
    "v_smfmac_f32_16x16x64_bf16 v[248:251], v[236:239], v[88:95], v241\n\t" \
    "s_waitcnt lgkmcnt(4)\n\t" \
    "v_smfmac_f32_16x16x64_bf16 v[224:227], v[236:239], v[200:207], v241\n\t" \
    "ds_read_b128 v[200:203], %[wl] offset:6144\n\t" \
    "ds_read_b128 v[204:207], %[wl] offset:7168\n\t" \
    "ds_read_b128 v[236:239], v240 offset:0\n\t" \
    "s_waitcnt lgkmcnt(6)\n\t" \
    "v_smfmac_f32_16x16x64_bf16 v[224:227], v[212:215], v[16:23], v241\n\t" \
    "v_smfmac_f32_16x16x64_bf16 v[244:247], v[212:215], v[24:31], v241\n\t" \
    "v_xor_b32 v240, 0x1c00, %[ab]\n\t" \
    "v_smfmac_f32_16x16x64_bf16 v[248:251], v[212:215], v[32:39], v241\n\t" \
    "ds_read_b128 v[212:215], v240 offset:0\n\t" \
    "s_waitcnt lgkmcnt(6)\n\t" \
    "v_smfmac_f32_16x16x64_bf16 v[224:227], v[216:219], v[228:235], v241\n\t" \
    "ds_read_b128 v[228:231], %[wl] offset:8192\n\t" \
    "ds_read_b128 v[232:235], %[wl] offset:9216\n\t" \
    "v_smfmac_f32_16x16x64_bf16 v[244:247], v[216:219], v[40:47], v241\n\t" \
    "v_and_b32 v240, 0x3ff, %[ab]\n\t" \
    "v_smfmac_f32_16x16x64_bf16 v[248:251], v[216:219], v[48:55], v241\n\t" \
    "ds_read_b128 v[216:219], v240 offset:8192\n\t" \
    "s_waitcnt lgkmcnt(8)\n\t" \
    "v_smfmac_f32_16x16x64_bf16 v[224:227], v[220:223], v[56:63], v241\n\t" \
    "v_smfmac_f32_16x16x64_bf16 v[244:247], v[220:223], v[64:71], v241\n\t" \
    "v_smfmac_f32_16x16x64_bf16 v[248:251], v[220:223], v[72:79], v241\n\t" \
    "ds_read_b128 v[220:223], v240 offset:9216\n\t" \
    "s_waitcnt lgkmcnt(8)\n\t" \
    "v_smfmac_f32_16x16x64_bf16 v[244:247], v[208:211], v[104:111], v241\n\t" \
    "s_waitcnt lgkmcnt(5)\n\t" \
    "v_smfmac_f32_16x16x64_bf16 v[244:247], v[236:239], v[120:127], v241\n\t" \
    "s_waitcnt lgkmcnt(4)\n\t" \
    "v_smfmac_f32_16x16x64_bf16 v[244:247], v[212:215], v[144:151], v241\n\t" \
    "s_waitcnt lgkmcnt(1)\n\t" \
    "v_smfmac_f32_16x16x64_bf16 v[244:247], v[216:219], v[160:167], v241\n\t" \
    "s_waitcnt lgkmcnt(0)\n\t" \
    "v_smfmac_f32_16x16x64_bf16 v[244:247], v[220:223], v[184:191], v241\n\t" \
    "v_smfmac_f32_16x16x64_bf16 v[248:251], v[208:211], v[112:119], v241\n\t" \
    "v_smfmac_f32_16x16x64_bf16 v[248:251], v[236:239], v[128:135], v241\n\t" \
    "v_smfmac_f32_16x16x64_bf16 v[248:251], v[212:215], v[152:159], v241\n\t" \
    "v_permlane32_swap_b32 v244, v246\n\t" \
    "v_permlane32_swap_b32 v245, v247\n\t" \
    "v_smfmac_f32_16x16x64_bf16 v[248:251], v[216:219], v[168:175], v241\n\t" \
    "v_add_f32 v244, v244, v246\n\t" \
    "v_add_f32 v245, v245, v247\n\t" \
    "v_smfmac_f32_16x16x64_bf16 v[248:251], v[220:223], v[192:199], v241\n\t" \
    "v_cvt_pk_bf16_f32 v244, v244, v245\n\t" \
    "ds_write_b16 %[dst], v244 offset:512\n\t" \
    "ds_write_b16_d16_hi %[dst], v244 offset:528\n\t" \
    "v_smfmac_f32_16x16x64_bf16 v[224:227], v[208:211], v[96:103], v241\n\t" \
    "v_smfmac_f32_16x16x64_bf16 v[224:227], v[236:239], v[200:207], v241\n\t" \
    "v_smfmac_f32_16x16x64_bf16 v[224:227], v[212:215], v[136:143], v241\n\t" \
    "v_permlane32_swap_b32 v248, v250\n\t" \
    "v_permlane32_swap_b32 v249, v251\n\t" \
    "v_smfmac_f32_16x16x64_bf16 v[224:227], v[216:219], v[228:235], v241\n\t" \
    "v_add_f32 v248, v248, v250\n\t" \
    "v_add_f32 v249, v249, v251\n\t" \
    "v_smfmac_f32_16x16x64_bf16 v[224:227], v[220:223], v[176:183], v241\n\t" \
    "v_cvt_pk_bf16_f32 v248, v248, v249\n\t" \
    "ds_write_b16 %[dst], v248 offset:1024\n\t" \
    "ds_write_b16_d16_hi %[dst], v248 offset:1040\n\t" \
    "s_nop 4\n\t" \
    "v_permlane32_swap_b32 v224, v226\n\t" \
    "v_permlane32_swap_b32 v225, v227\n\t" \
    "v_add_f32 v224, v224, v226\n\t" \
    "v_add_f32 v225, v225, v227\n\t" \
    "v_cvt_pk_bf16_f32 v224, v224, v225\n\t" \
    "ds_write_b16 %[dst], v224 offset:0\n\t" \
    "ds_write_b16_d16_hi %[dst], v224 offset:16\n\t"
#define ASM_CRIT1_V2 \
    "s_branch .Lc0%=\n\t" \
    ".Lcr%=:\n\t" \
    "s_cmp_eq_u32 %[n], 0\n\t" \
    "s_cbranch_scc1 .Lcb%=\n\t" \
    "s_sub_u32 %[n], %[n], 1\n\t" \
    "v_max_u32 v243, v208, v209\n\t" \
    "v_max3_u32 v243, v210, v211, v243\n\t" \
    "v_cmp_eq_u32 vcc, -1, v243\n\t" \
    "s_cbranch_vccz .Lcs0_%=\n\t" \
    "buffer_load_dwordx4 v[208:211], %[ab], %[rs], %[s0] offen sc1\n\t" \
    ".Lcs0_%=:\n\t" \
    "v_max_u32 v243, v236, v237\n\t" \
    "v_max3_u32 v243, v238, v239, v243\n\t" \
    "v_cmp_eq_u32 vcc, -1, v243\n\t" \
    "s_cbranch_vccz .Lca%=\n\t" \
    "buffer_load_dwordx4 v[236:239], %[ab], %[rs], %[s1] offen sc1\n\t" \
    "s_branch .Lca%=\n\t" \
    ".Lc0%=:\n\t" \
    "buffer_load_dwordx4 v[208:211], %[ab], %[rs], %[s0] offen sc1\n\t" \
    "buffer_load_dwordx4 v[236:239], %[ab], %[rs], %[s1] offen sc1\n\t" \
    "v_bfe_i32 v241, %[ab], 7, 1\n\t" \
    "v_and_b32 v241, 0xaaaa, v241\n\t" \
    "v_or_b32 v241, 0x4444, v241\n\t" \
    "v_xor_b32 v212, 0x400, %[ab]\n\t" \
    "v_xor_b32 v216, 0x800, %[ab]\n\t" \
    "v_xor_b32 v242, 0xc00, %[ab]\n\t" \
    "v_xor_b32 v240, 0x1400, %[ab]\n\t" \
    ".Lca%=:\n\t" \
    "s_waitcnt vmcnt(0)\n\t" \
    "ds_write_b128 %[ab], v[208:211] offset:0\n\t" \
    "ds_write_b128 %[ab], v[236:239] offset:4096\n\t" \
    "v_max_u32 v243, v208, v209\n\t" \
    "v_max3_u32 v243, v210, v211, v243\n\t" \
    "v_max3_u32 v243, v236, v237, v243\n\t" \
    "v_max3_u32 v243, v238, v239, v243\n\t" \
    "v_cmp_eq_u32 vcc, -1, v243\n\t" \
    "s_cbranch_vccnz .Lcr%=\n\t" \
    ".Lcb%=:\n\t" \
    PRIUP \
    ASM_ST2 \
    "v_smfmac_f32_16x16x64_bf16 v[224:227], v[208:211], v[200:207], v241\n\t" \
    "ds_read_b128 v[200:203], %[wl] offset:4096\n\t" \
    "ds_read_b128 v[204:207], %[wl] offset:5120\n\t" \
    "v_smfmac_f32_16x16x64_bf16 v[244:247], v[208:211], v[0:7], v241\n\t" \
    "v_smfmac_f32_16x16x64_bf16 v[248:251], v[208:211], v[8:15], v241\n\t" \
    "s_waitcnt lgkmcnt(2)\n\t" \
    "buffer_load_dword %[fv], %[fv], %[rs], %[fs] offen sc1\n\t" \
    "s_barrier\n\t" ASM_ST \
    "ds_read_b128 v[212:215], v212 offset:0\n\t" \
    "ds_read_b128 v[216:219], v216 offset:0\n\t" \
    "ds_read_b128 v[220:223], v242 offset:0\n\t" \
    "ds_read_b128 v[208:211], v240 offset:0\n\t" \
    "v_smfmac_f32_16x16x64_bf16 v[244:247], v[236:239], v[80:87], v241\n\t" \
    "v_xor_b32 v240, 0x1800, %[ab]\n\t" \
    "v_smfmac_f32_16x16x64_bf16 v[248:251], v[236:239], v[88:95], v241\n\t" \
    "s_waitcnt lgkmcnt(4)\n\t" \
    "v_smfmac_f32_16x16x64_bf16 v[224:227], v[236:239], v[200:207], v241\n\t" \
    "ds_read_b128 v[200:203], %[wl] offset:6144\n\t" \
    "ds_read_b128 v[204:207], %[wl] offset:7168\n\t" \
    "ds_read_b128 v[236:239], v240 offset:0\n\t" \
    "s_waitcnt lgkmcnt(6)\n\t" \
    "v_smfmac_f32_16x16x64_bf16 v[224:227], v[212:215], v[16:23], v241\n\t" \
    "v_smfmac_f32_16x16x64_bf16 v[244:247], v[212:215], v[24:31], v241\n\t" \
    "v_xor_b32 v240, 0x1c00, %[ab]\n\t" \
    "v_smfmac_f32_16x16x64_bf16 v[248:251], v[212:215], v[32:39], v241\n\t" \
    "ds_read_b128 v[212:215], v240 offset:0\n\t" \
    "s_waitcnt lgkmcnt(6)\n\t" \
    "v_smfmac_f32_16x16x64_bf16 v[224:227], v[216:219], v[228:235], v241\n\t" \
    "ds_read_b128 v[228:231], %[wl] offset:8192\n\t" \
    "ds_read_b128 v[232:235], %[wl] offset:9216\n\t" \
    "v_smfmac_f32_16x16x64_bf16 v[244:247], v[216:219], v[40:47], v241\n\t" \
    "v_and_b32 v240, 0x3ff, %[ab]\n\t" \
    "v_smfmac_f32_16x16x64_bf16 v[248:251], v[216:219], v[48:55], v241\n\t" \
    "ds_read_b128 v[216:219], v240 offset:8192\n\t" \
    "s_waitcnt lgkmcnt(8)\n\t" \
    "v_smfmac_f32_16x16x64_bf16 v[224:227], v[220:223], v[56:63], v241\n\t" \
    "v_smfmac_f32_16x16x64_bf16 v[244:247], v[220:223], v[64:71], v241\n\t" \
    "v_smfmac_f32_16x16x64_bf16 v[248:251], v[220:223], v[72:79], v241\n\t" \
    "ds_read_b128 v[220:223], v240 offset:9216\n\t" \
    "s_waitcnt lgkmcnt(8)\n\t" \
    "v_smfmac_f32_16x16x64_bf16 v[244:247], v[208:211], v[104:111], v241\n\t" \
    "s_waitcnt lgkmcnt(5)\n\t" \
    "v_smfmac_f32_16x16x64_bf16 v[244:247], v[236:239], v[120:127], v241\n\t" \
    "s_waitcnt lgkmcnt(4)\n\t" \
    "v_smfmac_f32_16x16x64_bf16 v[244:247], v[212:215], v[144:151], v241\n\t" \
    "s_waitcnt lgkmcnt(1)\n\t" \
    "v_smfmac_f32_16x16x64_bf16 v[244:247], v[216:219], v[160:167], v241\n\t" \
    "s_waitcnt lgkmcnt(0)\n\t" \
    "v_smfmac_f32_16x16x64_bf16 v[244:247], v[220:223], v[184:191], v241\n\t" \
    "v_smfmac_f32_16x16x64_bf16 v[248:251], v[208:211], v[112:119], v241\n\t" \
    "v_smfmac_f32_16x16x64_bf16 v[248:251], v[236:239], v[128:135], v241\n\t" \
    "v_smfmac_f32_16x16x64_bf16 v[248:251], v[212:215], v[152:159], v241\n\t" \
    "v_permlane32_swap_b32 v244, v246\n\t" \
    "v_permlane32_swap_b32 v245, v247\n\t" \
    "v_smfmac_f32_16x16x64_bf16 v[248:251], v[216:219], v[168:175], v241\n\t" \
    "v_add_f32 v244, v244, v246\n\t" \
    "v_add_f32 v245, v245, v247\n\t" \
    "v_smfmac_f32_16x16x64_bf16 v[248:251], v[220:223], v[192:199], v241\n\t" \
    "v_cvt_pk_bf16_f32 v244, v244, v245\n\t" \
    "ds_write_b16 %[dst], v244 offset:512\n\t" \
    "ds_write_b16_d16_hi %[dst], v244 offset:528\n\t" \
    "v_smfmac_f32_16x16x64_bf16 v[224:227], v[208:211], v[96:103], v241\n\t" \
    "v_smfmac_f32_16x16x64_bf16 v[224:227], v[236:239], v[200:207], v241\n\t" \
    "v_smfmac_f32_16x16x64_bf16 v[224:227], v[212:215], v[136:143], v241\n\t" \
    "v_permlane32_swap_b32 v248, v250\n\t" \
    "v_permlane32_swap_b32 v249, v251\n\t" \
    "v_smfmac_f32_16x16x64_bf16 v[224:227], v[216:219], v[228:235], v241\n\t" \
    "v_add_f32 v248, v248, v250\n\t" \
    "v_add_f32 v249, v249, v251\n\t" \
    "v_smfmac_f32_16x16x64_bf16 v[224:227], v[220:223], v[176:183], v241\n\t" \
    "v_cvt_pk_bf16_f32 v248, v248, v249\n\t" \
    "ds_write_b16 %[dst], v248 offset:1024\n\t" \
    "ds_write_b16_d16_hi %[dst], v248 offset:1040\n\t" \
    "s_nop 4\n\t" \
    "v_permlane32_swap_b32 v224, v226\n\t" \
    "v_permlane32_swap_b32 v225, v227\n\t" \
    "v_add_f32 v224, v224, v226\n\t" \
    "v_add_f32 v225, v225, v227\n\t" \
    "v_cvt_pk_bf16_f32 v224, v224, v225\n\t" \
    "ds_write_b16 %[dst], v224 offset:0\n\t" \
    "ds_write_b16_d16_hi %[dst], v224 offset:16\n\t"
#define ASM_VOX \
    "s_waitcnt lgkmcnt(0)\n\t" \
    "ds_read_b128 v[208:211], %[va] offset:2048\n\t" \
    "ds_read_b128 v[212:215], %[va] offset:3072\n\t" \
    "ds_read_b128 v[216:219], %[va] offset:4096\n\t" \
    "ds_read_b128 v[220:223], %[va] offset:5120\n\t" \
    "ds_read_b128 v[200:203], %[va] offset:0\n\t" \
    "ds_read_b128 v[204:207], %[va] offset:1024\n\t" \
    "ds_read_b128 v[236:239], %[wl] offset:0\n\t" \
    "ds_read_b128 v[240:243], %[wl] offset:1024\n\t" \
    "s_waitcnt lgkmcnt(7)\n\t" \
    "v_mfma_f32_16x16x32_bf16 v[244:247], v[208:211], v[0:3], 0\n\t" \
    "s_waitcnt lgkmcnt(6)\n\t" \
    "v_mfma_f32_16x16x32_bf16 v[244:247], v[212:215], v[4:7], v[244:247]\n\t" \
    "s_waitcnt lgkmcnt(5)\n\t" \
    "v_mfma_f32_16x16x32_bf16 v[244:247], v[216:219], v[8:11], v[244:247]\n\t" \
    "s_waitcnt lgkmcnt(4)\n\t" \
    "v_mfma_f32_16x16x32_bf16 v[244:247], v[220:223], v[12:15], v[244:247]\n\t" \
    "s_waitcnt lgkmcnt(1)\n\t" \
    "v_mfma_f32_16x16x32_bf16 v[244:247], v[200:203], v[236:239], v[244:247]\n\t" \
    "ds_read_b128 v[236:239], %[wl] offset:2048\n\t" \
    "s_waitcnt lgkmcnt(1)\n\t" \
    "v_mfma_f32_16x16x32_bf16 v[244:247], v[204:207], v[240:243], v[244:247]\n\t" \
    "ds_read_b128 v[240:243], %[wl] offset:3072\n\t" \
    "v_mfma_f32_16x16x32_bf16 v[248:251], v[208:211], v[40:43], 0\n\t" \
    "v_mfma_f32_16x16x32_bf16 v[248:251], v[212:215], v[44:47], v[248:251]\n\t" \
    "v_mfma_f32_16x16x32_bf16 v[248:251], v[216:219], v[48:51], v[248:251]\n\t" \
    "v_cvt_pk_bf16_f32 v224, v244, v245\n\t" \
    "v_mfma_f32_16x16x32_bf16 v[248:251], v[220:223], v[52:55], v[248:251]\n\t" \
    "v_cvt_pk_bf16_f32 v225, v246, v247\n\t" \
    "s_waitcnt lgkmcnt(1)\n\t" \
    "v_mfma_f32_16x16x32_bf16 v[248:251], v[200:203], v[236:239], v[248:251]\n\t" \
    "ds_read_b128 v[236:239], %[wl] offset:4096\n\t" \
    "s_waitcnt lgkmcnt(1)\n\t" \
    "v_mfma_f32_16x16x32_bf16 v[248:251], v[204:207], v[240:243], v[248:251]\n\t" \
    "ds_read_b128 v[240:243], %[wl] offset:5120\n\t" \
    "v_mfma_f32_16x16x32_bf16 v[244:247], v[208:211], v[80:83], 0\n\t" \
    "v_mfma_f32_16x16x32_bf16 v[244:247], v[212:215], v[84:87], v[244:247]\n\t" \
    "v_mfma_f32_16x16x32_bf16 v[244:247], v[216:219], v[88:91], v[244:247]\n\t" \
    "v_cvt_pk_bf16_f32 v226, v248, v249\n\t" \
    "v_mfma_f32_16x16x32_bf16 v[244:247], v[220:223], v[92:95], v[244:247]\n\t" \
    "v_cvt_pk_bf16_f32 v227, v250, v251\n\t" \
    "s_waitcnt lgkmcnt(1)\n\t" \
    "v_mfma_f32_16x16x32_bf16 v[244:247], v[200:203], v[236:239], v[244:247]\n\t" \
    "ds_read_b128 v[236:239], %[wl] offset:6144\n\t" \
    "s_waitcnt lgkmcnt(1)\n\t" \
    "v_mfma_f32_16x16x32_bf16 v[244:247], v[204:207], v[240:243], v[244:247]\n\t" \
    "ds_read_b128 v[240:243], %[wl] offset:7168\n\t" \
    "v_mfma_f32_16x16x32_bf16 v[248:251], v[208:211], v[120:123], 0\n\t" \
    "v_mfma_f32_16x16x32_bf16 v[248:251], v[212:215], v[124:127], v[248:251]\n\t" \
    "v_mfma_f32_16x16x32_bf16 v[248:251], v[216:219], v[128:131], v[248:251]\n\t" \
    "v_cvt_pk_bf16_f32 v228, v244, v245\n\t" \
    "v_mfma_f32_16x16x32_bf16 v[248:251], v[220:223], v[132:135], v[248:251]\n\t" \
    "v_cvt_pk_bf16_f32 v229, v246, v247\n\t" \
    "s_waitcnt lgkmcnt(1)\n\t" \
    "v_mfma_f32_16x16x32_bf16 v[248:251], v[200:203], v[236:239], v[248:251]\n\t" \
    "ds_read_b128 v[236:239], %[wl] offset:8192\n\t" \
    "s_waitcnt lgkmcnt(1)\n\t" \
    "v_mfma_f32_16x16x32_bf16 v[248:251], v[204:207], v[240:243], v[248:251]\n\t" \
    "ds_read_b128 v[240:243], %[wl] offset:9216\n\t" \
    "v_mfma_f32_16x16x32_bf16 v[244:247], v[208:211], v[160:163], 0\n\t" \
    "v_mfma_f32_16x16x32_bf16 v[244:247], v[212:215], v[164:167], v[244:247]\n\t" \
    "v_mfma_f32_16x16x32_bf16 v[244:247], v[216:219], v[168:171], v[244:247]\n\t" \
    "v_cvt_pk_bf16_f32 v230, v248, v249\n\t" \
    "v_mfma_f32_16x16x32_bf16 v[244:247], v[220:223], v[172:175], v[244:247]\n\t" \
    "v_cvt_pk_bf16_f32 v231, v250, v251\n\t" \
    "s_waitcnt lgkmcnt(1)\n\t" \
    "v_mfma_f32_16x16x32_bf16 v[244:247], v[200:203], v[236:239], v[244:247]\n\t" \
    "s_waitcnt lgkmcnt(0)\n\t" \
    "v_mfma_f32_16x16x32_bf16 v[244:247], v[204:207], v[240:243], v[244:247]\n\t" \
    "v_mfma_f32_16x16x32_bf16 v[248:251], v[208:211], v[24:27], 0\n\t" \
    "v_mfma_f32_16x16x32_bf16 v[248:251], v[212:215], v[28:31], v[248:251]\n\t" \
    "v_mfma_f32_16x16x32_bf16 v[248:251], v[216:219], v[32:35], v[248:251]\n\t" \
    "v_cvt_pk_bf16_f32 v232, v244, v245\n\t" \
    "v_mfma_f32_16x16x32_bf16 v[248:251], v[220:223], v[36:39], v[248:251]\n\t" \
    "v_cvt_pk_bf16_f32 v233, v246, v247\n\t" \
    "v_mfma_f32_16x16x32_bf16 v[248:251], v[200:203], v[16:19], v[248:251]\n\t" \
    "v_mfma_f32_16x16x32_bf16 v[248:251], v[204:207], v[20:23], v[248:251]\n\t" \
    "v_mfma_f32_16x16x32_bf16 v[244:247], v[208:211], v[64:67], 0\n\t" \
    "v_mfma_f32_16x16x32_bf16 v[244:247], v[212:215], v[68:71], v[244:247]\n\t" \
    "v_mfma_f32_16x16x32_bf16 v[244:247], v[216:219], v[72:75], v[244:247]\n\t" \
    "v_cvt_pk_bf16_f32 v236, v248, v249\n\t" \
    "v_mfma_f32_16x16x32_bf16 v[244:247], v[220:223], v[76:79], v[244:247]\n\t" \
    "v_cvt_pk_bf16_f32 v237, v250, v251\n\t" \
    "v_mfma_f32_16x16x32_bf16 v[244:247], v[200:203], v[56:59], v[244:247]\n\t" \
    "v_mfma_f32_16x16x32_bf16 v[244:247], v[204:207], v[60:63], v[244:247]\n\t" \
    "v_mfma_f32_16x16x32_bf16 v[248:251], v[208:211], v[104:107], 0\n\t" \
    "v_mfma_f32_16x16x32_bf16 v[248:251], v[212:215], v[108:111], v[248:251]\n\t" \
    "v_mfma_f32_16x16x32_bf16 v[248:251], v[216:219], v[112:115], v[248:251]\n\t" \
    "v_cvt_pk_bf16_f32 v238, v244, v245\n\t" \
    "v_mfma_f32_16x16x32_bf16 v[248:251], v[220:223], v[116:119], v[248:251]\n\t" \
    "v_cvt_pk_bf16_f32 v239, v246, v247\n\t" \
    "v_mfma_f32_16x16x32_bf16 v[248:251], v[200:203], v[96:99], v[248:251]\n\t" \
    "v_mfma_f32_16x16x32_bf16 v[248:251], v[204:207], v[100:103], v[248:251]\n\t" \
    "v_mfma_f32_16x16x32_bf16 v[244:247], v[208:211], v[144:147], 0\n\t" \
    "v_mfma_f32_16x16x32_bf16 v[244:247], v[212:215], v[148:151], v[244:247]\n\t" \
    "v_mfma_f32_16x16x32_bf16 v[244:247], v[216:219], v[152:155], v[244:247]\n\t" \
    "v_cvt_pk_bf16_f32 v240, v248, v249\n\t" \
    "v_mfma_f32_16x16x32_bf16 v[244:247], v[220:223], v[156:159], v[244:247]\n\t" \
    "v_cvt_pk_bf16_f32 v241, v250, v251\n\t" \
    "v_mfma_f32_16x16x32_bf16 v[244:247], v[200:203], v[136:139], v[244:247]\n\t" \
    "v_mfma_f32_16x16x32_bf16 v[244:247], v[204:207], v[140:143], v[244:247]\n\t" \
    "v_mfma_f32_16x16x32_bf16 v[248:251], v[208:211], v[184:187], 0\n\t" \
    "ds_read_b128 v[208:211], %[va] offset:8192\n\t" \
    "v_mfma_f32_16x16x32_bf16 v[248:251], v[212:215], v[188:191], v[248:251]\n\t" \
    "ds_read_b128 v[212:215], %[va] offset:9216\n\t" \
    "v_mfma_f32_16x16x32_bf16 v[248:251], v[216:219], v[192:195], v[248:251]\n\t" \
    "ds_read_b128 v[216:219], %[va] offset:10240\n\t" \
    "v_cvt_pk_bf16_f32 v242, v244, v245\n\t" \
    "v_mfma_f32_16x16x32_bf16 v[248:251], v[220:223], v[196:199], v[248:251]\n\t" \
    "ds_read_b128 v[220:223], %[va] offset:11264\n\t" \
    "v_cvt_pk_bf16_f32 v243, v246, v247\n\t" \
    "v_mfma_f32_16x16x32_bf16 v[248:251], v[200:203], v[176:179], v[248:251]\n\t" \
    "ds_read_b128 v[200:203], %[va] offset:6144\n\t" \
    "v_mfma_f32_16x16x32_bf16 v[248:251], v[204:207], v[180:183], v[248:251]\n\t" \
    "ds_read_b128 v[204:207], %[va] offset:7168\n\t" \
    "s_waitcnt lgkmcnt(5)\n\t" \
    "v_mfma_f32_16x16x32_bf16 v[244:247], v[208:211], v[24:27], 0\n\t" \
    "s_waitcnt lgkmcnt(4)\n\t" \
    "v_mfma_f32_16x16x32_bf16 v[244:247], v[212:215], v[28:31], v[244:247]\n\t" \
    "s_waitcnt lgkmcnt(3)\n\t" \
    "v_mfma_f32_16x16x32_bf16 v[244:247], v[216:219], v[32:35], v[244:247]\n\t" \
    "v_cvt_pk_bf16_f32 v234, v248, v249\n\t" \
    "s_waitcnt lgkmcnt(2)\n\t" \
    "v_mfma_f32_16x16x32_bf16 v[244:247], v[220:223], v[36:39], v[244:247]\n\t" \
    "v_cvt_pk_bf16_f32 v235, v250, v251\n\t" \
    "s_waitcnt lgkmcnt(1)\n\t" \
    "v_mfma_f32_16x16x32_bf16 v[244:247], v[200:203], v[16:19], v[244:247]\n\t" \
    "s_waitcnt lgkmcnt(0)\n\t" \
    "v_mfma_f32_16x16x32_bf16 v[244:247], v[204:207], v[20:23], v[244:247]\n\t" \
    "v_mfma_f32_16x16x32_bf16 v[248:251], v[208:211], v[64:67], 0\n\t" \
    "s_add_i32 %[so], %[so], 65536\n\t" \
    "v_mfma_f32_16x16x32_bf16 v[248:251], v[212:215], v[68:71], v[248:251]\n\t" \
    "s_nop 1\n\t" \
    "v_cvt_pk_bf16_f32 v247, v246, v247\n\t" \
    "v_mfma_f32_16x16x32_bf16 v[248:251], v[216:219], v[72:75], v[248:251]\n\t" \
    "v_cvt_pk_bf16_f32 v246, v244, v245\n\t" \
    "v_mfma_f32_16x16x32_bf16 v[248:251], v[220:223], v[76:79], v[248:251]\n\t" \
    "v_mov_b64 v[244:245], v[236:237]\n\t" \
    "v_mfma_f32_16x16x32_bf16 v[248:251], v[200:203], v[56:59], v[248:251]\n\t" \
    "buffer_store_dwordx4 v[244:247], %[l16], %[rd], %[so] offen sc1\n\t" \
    "v_mfma_f32_16x16x32_bf16 v[248:251], v[204:207], v[60:63], v[248:251]\n\t" \
    "v_mfma_f32_16x16x32_bf16 v[244:247], v[208:211], v[104:107], 0\n\t" \
    "s_add_i32 %[so], %[so], 131072\n\t" \
    "v_mfma_f32_16x16x32_bf16 v[244:247], v[212:215], v[108:111], v[244:247]\n\t" \
    "s_nop 1\n\t" \
    "v_cvt_pk_bf16_f32 v251, v250, v251\n\t" \
    "v_mfma_f32_16x16x32_bf16 v[244:247], v[216:219], v[112:115], v[244:247]\n\t" \
    "v_cvt_pk_bf16_f32 v250, v248, v249\n\t" \
    "v_mfma_f32_16x16x32_bf16 v[244:247], v[220:223], v[116:119], v[244:247]\n\t" \
    "v_mov_b64 v[248:249], v[238:239]\n\t" \
    "v_mfma_f32_16x16x32_bf16 v[244:247], v[200:203], v[96:99], v[244:247]\n\t" \
    "buffer_store_dwordx4 v[248:251], %[l16], %[rd], %[so] offen sc1\n\t" \
    "v_mfma_f32_16x16x32_bf16 v[244:247], v[204:207], v[100:103], v[244:247]\n\t" \
    "v_mfma_f32_16x16x32_bf16 v[248:251], v[208:211], v[144:147], 0\n\t" \
    "s_add_i32 %[so], %[so], 131072\n\t" \
    "v_mfma_f32_16x16x32_bf16 v[248:251], v[212:215], v[148:151], v[248:251]\n\t" \
    "s_nop 1\n\t" \
    "v_cvt_pk_bf16_f32 v247, v246, v247\n\t" \
    "v_mfma_f32_16x16x32_bf16 v[248:251], v[216:219], v[152:155], v[248:251]\n\t" \
    "v_cvt_pk_bf16_f32 v246, v244, v245\n\t" \
    "v_mfma_f32_16x16x32_bf16 v[248:251], v[220:223], v[156:159], v[248:251]\n\t" \
    "v_mov_b64 v[244:245], v[240:241]\n\t" \
    "v_mfma_f32_16x16x32_bf16 v[248:251], v[200:203], v[136:139], v[248:251]\n\t" \
    "buffer_store_dwordx4 v[244:247], %[l16], %[rd], %[so] offen sc1\n\t" \
    "v_mfma_f32_16x16x32_bf16 v[248:251], v[204:207], v[140:143], v[248:251]\n\t" \
    "v_mfma_f32_16x16x32_bf16 v[244:247], v[208:211], v[184:187], 0\n\t" \
    "s_add_i32 %[so], %[so], 131072\n\t" \
    "v_mfma_f32_16x16x32_bf16 v[244:247], v[212:215], v[188:191], v[244:247]\n\t" \
    "s_nop 1\n\t" \
    "v_cvt_pk_bf16_f32 v251, v250, v251\n\t" \
    "v_mfma_f32_16x16x32_bf16 v[244:247], v[216:219], v[192:195], v[244:247]\n\t" \
    "v_cvt_pk_bf16_f32 v250, v248, v249\n\t" \
    "v_mfma_f32_16x16x32_bf16 v[244:247], v[220:223], v[196:199], v[244:247]\n\t" \
    "v_mov_b64 v[248:249], v[242:243]\n\t" \
    "v_mfma_f32_16x16x32_bf16 v[244:247], v[200:203], v[176:179], v[244:247]\n\t" \
    "buffer_store_dwordx4 v[248:251], %[l16], %[rd], %[so] offen sc1\n\t" \
    "v_mfma_f32_16x16x32_bf16 v[244:247], v[204:207], v[180:183], v[244:247]\n\t" \
    "ds_read_b128 v[236:239], %[wl] offset:0\n\t" \
    "ds_read_b128 v[240:243], %[wl] offset:1024\n\t" \
    "v_mfma_f32_16x16x32_bf16 v[248:251], v[208:211], v[0:3], 0\n\t" \
    "s_add_i32 %[so], %[so], 131072\n\t" \
    "v_mfma_f32_16x16x32_bf16 v[248:251], v[212:215], v[4:7], v[248:251]\n\t" \
    "v_cvt_pk_bf16_f32 v247, v246, v247\n\t" \
    "v_mfma_f32_16x16x32_bf16 v[248:251], v[216:219], v[8:11], v[248:251]\n\t" \
    "v_cvt_pk_bf16_f32 v246, v244, v245\n\t" \
    "v_mfma_f32_16x16x32_bf16 v[248:251], v[220:223], v[12:15], v[248:251]\n\t" \
    "v_mov_b64 v[244:245], v[234:235]\n\t" \
    "s_waitcnt lgkmcnt(1)\n\t" \
    "v_mfma_f32_16x16x32_bf16 v[248:251], v[200:203], v[236:239], v[248:251]\n\t" \
    "ds_read_b128 v[236:239], %[wl] offset:2048\n\t" \
    "buffer_store_dwordx4 v[244:247], %[l16], %[rd], %[so] offen sc1\n\t" \
    "s_waitcnt lgkmcnt(1)\n\t" \
    "v_mfma_f32_16x16x32_bf16 v[248:251], v[204:207], v[240:243], v[248:251]\n\t" \
    "ds_read_b128 v[240:243], %[wl] offset:3072\n\t" \
    "v_mfma_f32_16x16x32_bf16 v[244:247], v[208:211], v[40:43], 0\n\t" \
    "s_add_i32 %[so], %[so], -589824\n\t" \
    "v_mfma_f32_16x16x32_bf16 v[244:247], v[212:215], v[44:47], v[244:247]\n\t" \
    "s_nop 0\n\t" \
    "v_cvt_pk_bf16_f32 v251, v250, v251\n\t" \
    "v_mfma_f32_16x16x32_bf16 v[244:247], v[216:219], v[48:51], v[244:247]\n\t" \
    "v_cvt_pk_bf16_f32 v250, v248, v249\n\t" \
    "v_mfma_f32_16x16x32_bf16 v[244:247], v[220:223], v[52:55], v[244:247]\n\t" \
    "v_mov_b64 v[248:249], v[224:225]\n\t" \
    "s_waitcnt lgkmcnt(1)\n\t" \
    "v_mfma_f32_16x16x32_bf16 v[244:247], v[200:203], v[236:239], v[244:247]\n\t" \
    "ds_read_b128 v[236:239], %[wl] offset:4096\n\t" \
    "buffer_store_dwordx4 v[248:251], %[l16], %[rd], %[so] offen sc1\n\t" \
    "s_waitcnt lgkmcnt(1)\n\t" \
    "v_mfma_f32_16x16x32_bf16 v[244:247], v[204:207], v[240:243], v[244:247]\n\t" \
    "ds_read_b128 v[240:243], %[wl] offset:5120\n\t" \
    "v_mfma_f32_16x16x32_bf16 v[248:251], v[208:211], v[80:83], 0\n\t" \
    "s_add_i32 %[so], %[so], 131072\n\t" \
    "v_mfma_f32_16x16x32_bf16 v[248:251], v[212:215], v[84:87], v[248:251]\n\t" \
    "s_nop 0\n\t" \
    "v_cvt_pk_bf16_f32 v247, v246, v247\n\t" \
    "v_mfma_f32_16x16x32_bf16 v[248:251], v[216:219], v[88:91], v[248:251]\n\t" \
    "v_cvt_pk_bf16_f32 v246, v244, v245\n\t" \
    "v_mfma_f32_16x16x32_bf16 v[248:251], v[220:223], v[92:95], v[248:251]\n\t" \
    "v_mov_b64 v[244:245], v[226:227]\n\t" \
    "s_waitcnt lgkmcnt(1)\n\t" \
    "v_mfma_f32_16x16x32_bf16 v[248:251], v[200:203], v[236:239], v[248:251]\n\t" \
    "ds_read_b128 v[236:239], %[wl] offset:6144\n\t" \
    "buffer_store_dwordx4 v[244:247], %[l16], %[rd], %[so] offen sc1\n\t" \
    "s_waitcnt lgkmcnt(1)\n\t" \
    "v_mfma_f32_16x16x32_bf16 v[248:251], v[204:207], v[240:243], v[248:251]\n\t" \
    "ds_read_b128 v[240:243], %[wl] offset:7168\n\t" \
    "v_mfma_f32_16x16x32_bf16 v[244:247], v[208:211], v[120:123], 0\n\t" \
    "s_add_i32 %[so], %[so], 131072\n\t" \
    "v_mfma_f32_16x16x32_bf16 v[244:247], v[212:215], v[124:127], v[244:247]\n\t" \
    "s_nop 0\n\t" \
    "v_cvt_pk_bf16_f32 v251, v250, v251\n\t" \
    "v_mfma_f32_16x16x32_bf16 v[244:247], v[216:219], v[128:131], v[244:247]\n\t" \
    "v_cvt_pk_bf16_f32 v250, v248, v249\n\t" \
    "v_mfma_f32_16x16x32_bf16 v[244:247], v[220:223], v[132:135], v[244:247]\n\t" \
    "v_mov_b64 v[248:249], v[228:229]\n\t" \
    "s_waitcnt lgkmcnt(1)\n\t" \
    "v_mfma_f32_16x16x32_bf16 v[244:247], v[200:203], v[236:239], v[244:247]\n\t" \
    "ds_read_b128 v[236:239], %[wl] offset:8192\n\t" \
    "buffer_store_dwordx4 v[248:251], %[l16], %[rd], %[so] offen sc1\n\t" \
    "s_waitcnt lgkmcnt(1)\n\t" \
    "v_mfma_f32_16x16x32_bf16 v[244:247], v[204:207], v[240:243], v[244:247]\n\t" \
    "ds_read_b128 v[240:243], %[wl] offset:9216\n\t" \
    "v_mfma_f32_16x16x32_bf16 v[248:251], v[208:211], v[160:163], 0\n\t" \
    "s_add_i32 %[so], %[so], 131072\n\t" \
    "v_mfma_f32_16x16x32_bf16 v[248:251], v[212:215], v[164:167], v[248:251]\n\t" \
    "s_nop 0\n\t" \
    "v_cvt_pk_bf16_f32 v247, v246, v247\n\t" \
    "v_mfma_f32_16x16x32_bf16 v[248:251], v[216:219], v[168:171], v[248:251]\n\t" \
    "v_cvt_pk_bf16_f32 v246, v244, v245\n\t" \
    "v_mfma_f32_16x16x32_bf16 v[248:251], v[220:223], v[172:175], v[248:251]\n\t" \
    "v_mov_b64 v[244:245], v[230:231]\n\t" \
    "s_waitcnt lgkmcnt(1)\n\t" \
    "v_mfma_f32_16x16x32_bf16 v[248:251], v[200:203], v[236:239], v[248:251]\n\t" \
    "buffer_store_dwordx4 v[244:247], %[l16], %[rd], %[so] offen sc1\n\t" \
    "s_waitcnt lgkmcnt(0)\n\t" \
    "v_mfma_f32_16x16x32_bf16 v[248:251], v[204:207], v[240:243], v[248:251]\n\t" \
    "s_add_i32 %[so], %[so], 131072\n\t" \
    "s_nop 7\n\t" \
    "v_cvt_pk_bf16_f32 v251, v250, v251\n\t" \
    "v_cvt_pk_bf16_f32 v250, v248, v249\n\t" \
    "v_mov_b64 v[248:249], v[232:233]\n\t" \
    "buffer_store_dwordx4 v[248:251], %[l16], %[rd], %[so] offen sc1\n\t" \
    "s_nop 1\n\t"
#define ASM_CRIT2 \
    "s_waitcnt lgkmcnt(0)\n\t" \
    "s_barrier\n\t" ASM_ST \
    "ds_read_b128 v[204:207], v200 offset:11264\n\t" \
    "ds_read_b128 v[208:211], v200 offset:12288\n\t" \
    "ds_read_b128 v[200:203], v200 offset:10240\n\t" \
    "s_waitcnt lgkmcnt(2)\n\t" \
    "v_smfmac_f32_16x16x64_bf16 v[224:227], v[204:207], v[24:31], %[sidx]\n\t" \
    "v_smfmac_f32_16x16x64_bf16 v[220:223], v[204:207], v[0:7], %[sidx]\n\t" \
    "s_waitcnt lgkmcnt(1)\n\t" \
    "v_smfmac_f32_16x16x64_bf16 v[224:227], v[208:211], v[32:39], %[sidx]\n\t" \
    "v_smfmac_f32_16x16x64_bf16 v[220:223], v[208:211], v[8:15], %[sidx]\n\t" \
    "s_waitcnt lgkmcnt(0)\n\t" \
    "v_smfmac_f32_16x16x64_bf16 v[224:227], v[200:203], v[16:23], %[sidx]\n\t" \
    "v_smfmac_f32_16x16x64_bf16 v[220:223], v[200:203], v[212:219], %[sidx]\n\t" \
    "ds_read_b128 v[212:215], %[wl] offset:2048\n\t" \
    "ds_read_b128 v[216:219], %[wl] offset:3072\n\t" \
    "v_smfmac_f32_16x16x64_bf16 v[232:235], v[204:207], v[64:71], %[sidx]\n\t" \
    "v_smfmac_f32_16x16x64_bf16 v[228:231], v[204:207], v[40:47], %[sidx]\n\t" \
    "v_smfmac_f32_16x16x64_bf16 v[232:235], v[208:211], v[72:79], %[sidx]\n\t" \
    "v_permlane32_swap_b32 v220, v224\n\t" \
    "v_permlane32_swap_b32 v221, v225\n\t" \
    "v_permlane32_swap_b32 v222, v226\n\t" \
    "v_smfmac_f32_16x16x64_bf16 v[228:231], v[208:211], v[48:55], %[sidx]\n\t" \
    "v_permlane32_swap_b32 v223, v227\n\t" \
    "v_add_f32 v220, v220, v224\n\t" \
    "v_add_f32 v221, v221, v225\n\t" \
    "v_smfmac_f32_16x16x64_bf16 v[232:235], v[200:203], v[56:63], %[sidx]\n\t" \
    "v_add_f32 v222, v222, v226\n\t" \
    "v_add_f32 v223, v223, v227\n\t" \
    "v_cvt_pk_bf16_f32 v226, v220, v221\n\t" \
    "s_waitcnt lgkmcnt(0)\n\t" \
    "v_smfmac_f32_16x16x64_bf16 v[228:231], v[200:203], v[212:219], %[sidx]\n\t" \
    "ds_read_b128 v[212:215], %[wl] offset:4096\n\t" \
    "ds_read_b128 v[216:219], %[wl] offset:5120\n\t" \
    "v_cvt_pk_bf16_f32 v227, v222, v223\n\t" \
    "v_min_u32 v226, -2, v226\n\t" \
    "v_min_u32 v227, -2, v227\n\t" \
    "v_smfmac_f32_16x16x64_bf16 v[240:243], v[204:207], v[104:111], %[sidx]\n\t" \
    "v_smfmac_f32_16x16x64_bf16 v[236:239], v[204:207], v[80:87], %[sidx]\n\t" \
    "v_smfmac_f32_16x16x64_bf16 v[240:243], v[208:211], v[112:119], %[sidx]\n\t" \
    "v_permlane32_swap_b32 v228, v232\n\t" \
    "v_permlane32_swap_b32 v229, v233\n\t" \
    "v_permlane32_swap_b32 v230, v234\n\t" \
    "v_smfmac_f32_16x16x64_bf16 v[236:239], v[208:211], v[88:95], %[sidx]\n\t" \
    "v_permlane32_swap_b32 v231, v235\n\t" \
    "v_add_f32 v228, v228, v232\n\t" \
    "v_add_f32 v229, v229, v233\n\t" \
    "v_smfmac_f32_16x16x64_bf16 v[240:243], v[200:203], v[96:103], %[sidx]\n\t" \
    "v_add_f32 v230, v230, v234\n\t" \
    "v_add_f32 v231, v231, v235\n\t" \
    "v_cvt_pk_bf16_f32 v228, v228, v229\n\t" \
    "s_waitcnt lgkmcnt(0)\n\t" \
    "v_smfmac_f32_16x16x64_bf16 v[236:239], v[200:203], v[212:219], %[sidx]\n\t" \
    "ds_read_b128 v[212:215], %[wl] offset:6144\n\t" \
    "ds_read_b128 v[216:219], %[wl] offset:7168\n\t" \
    "v_cvt_pk_bf16_f32 v229, v230, v231\n\t" \
    "v_min_u32 v228, -2, v228\n\t" \
    "v_min_u32 v229, -2, v229\n\t" \
    "s_nop 1\n\t" \
    "v_permlane16_swap_b32 v226, v228\n\t" \
    "v_permlane16_swap_b32 v227, v229\n\t" \
    "buffer_store_dwordx4 v[226:229], %[vo], %[rr], %[so] offen sc1\n\t" \
    "s_add_u32 %[so], %[so], 0x10000\n\t" \
    "v_smfmac_f32_16x16x64_bf16 v[248:251], v[204:207], v[144:151], %[sidx]\n\t" \
    "v_mov_b64 v[220:221], 0\n\t" \
    "v_mov_b64 v[222:223], 0\n\t" \
    "v_smfmac_f32_16x16x64_bf16 v[244:247], v[204:207], v[120:127], %[sidx]\n\t" \
    "v_mov_b64 v[224:225], 0\n\t" \
    "v_mov_b64 v[226:227], 0\n\t" \
    "v_smfmac_f32_16x16x64_bf16 v[248:251], v[208:211], v[152:159], %[sidx]\n\t" \
    "v_permlane32_swap_b32 v236, v240\n\t" \
    "v_permlane32_swap_b32 v237, v241\n\t" \
    "v_permlane32_swap_b32 v238, v242\n\t" \
    "v_smfmac_f32_16x16x64_bf16 v[244:247], v[208:211], v[128:135], %[sidx]\n\t" \
    "v_permlane32_swap_b32 v239, v243\n\t" \
    "v_add_f32 v236, v236, v240\n\t" \
    "v_add_f32 v237, v237, v241\n\t" \
    "v_smfmac_f32_16x16x64_bf16 v[248:251], v[200:203], v[136:143], %[sidx]\n\t" \
    "v_add_f32 v238, v238, v242\n\t" \
    "v_add_f32 v239, v239, v243\n\t" \
    "v_cvt_pk_bf16_f32 v242, v236, v237\n\t" \
    "s_waitcnt lgkmcnt(0)\n\t" \
    "v_smfmac_f32_16x16x64_bf16 v[244:247], v[200:203], v[212:219], %[sidx]\n\t" \
    "ds_read_b128 v[212:215], %[wl] offset:8192\n\t" \
    "ds_read_b128 v[216:219], %[wl] offset:9216\n\t" \
    "v_cvt_pk_bf16_f32 v243, v238, v239\n\t" \
    "v_min_u32 v242, -2, v242\n\t" \
    "v_min_u32 v243, -2, v243\n\t" \
    "v_smfmac_f32_16x16x64_bf16 v[224:227], v[204:207], v[184:191], %[sidx]\n\t" \
    "v_and_b32 v232, 0x3fff, %[vo]\n\t" \
    "v_bfe_u32 v233, %[vo], 14, 1\n\t" \
    "v_smfmac_f32_16x16x64_bf16 v[220:223], v[204:207], v[160:167], %[sidx]\n\t" \
    "v_lshl_add_u32 v232, v233, 3, v232\n\t" \
    "v_permlane32_swap_b32 v244, v248\n\t" \
    "v_smfmac_f32_16x16x64_bf16 v[224:227], v[208:211], v[192:199], %[sidx]\n\t" \
    "v_permlane32_swap_b32 v245, v249\n\t" \
    "v_permlane32_swap_b32 v246, v250\n\t" \
    "v_permlane32_swap_b32 v247, v251\n\t" \
    "v_smfmac_f32_16x16x64_bf16 v[220:223], v[208:211], v[168:175], %[sidx]\n\t" \
    "v_add_f32 v244, v244, v248\n\t" \
    "v_add_f32 v245, v245, v249\n\t" \
    "v_add_f32 v246, v246, v250\n\t" \
    "v_smfmac_f32_16x16x64_bf16 v[224:227], v[200:203], v[176:183], %[sidx]\n\t" \
    "v_add_f32 v247, v247, v251\n\t" \
    "v_cvt_pk_bf16_f32 v244, v244, v245\n\t" \
    "v_cvt_pk_bf16_f32 v245, v246, v247\n\t" \
    "s_waitcnt lgkmcnt(0)\n\t" \
    "v_smfmac_f32_16x16x64_bf16 v[220:223], v[200:203], v[212:219], %[sidx]\n\t" \
    "v_min_u32 v244, -2, v244\n\t" \
    "v_min_u32 v245, -2, v245\n\t" \
    "s_nop 1\n\t" \
    "v_permlane16_swap_b32 v242, v244\n\t" \
    "v_permlane16_swap_b32 v243, v245\n\t" \
    "buffer_store_dwordx4 v[242:245], %[vo], %[rr], %[so] offen sc1\n\t" \
    "s_add_u32 %[so], %[so], 0x10000\n\t" \
    "s_nop 0\n\t" \
    "v_permlane32_swap_b32 v220, v224\n\t" \
    "v_permlane32_swap_b32 v221, v225\n\t" \
    "v_permlane32_swap_b32 v222, v226\n\t" \
    "v_permlane32_swap_b32 v223, v227\n\t" \
    "v_add_f32 v220, v220, v224\n\t" \
    "v_add_f32 v221, v221, v225\n\t" \
    "v_add_f32 v222, v222, v226\n\t" \
    "v_add_f32 v223, v223, v227\n\t" \
    "v_cvt_pk_bf16_f32 v220, v220, v221\n\t" \
    "v_cvt_pk_bf16_f32 v221, v222, v223\n\t" \
    "v_min_u32 v220, -2, v220\n\t" \
    "v_min_u32 v221, -2, v221\n\t" \
    "buffer_store_dwordx2 v[220:221], v232, %[rr], %[so] offen sc1\n\t" \
    "s_nop 1\n\t"
#define ASM_SM \
    "v_mbcnt_lo_u32_b32 v252, -1, 0\n\t" \
    "v_mbcnt_hi_u32_b32 v252, -1, v252\n\t" \
    "v_lshlrev_b32 v252, 4, v252\n\t" \
    "v_lshrrev_b32 v253, 1, v252\n\t" \
    "v_xor_b32 v253, v253, v252\n\t" \
    "v_and_b32 v253, 0x20, v253\n\t" \
    "v_mul_u32_u24 v253, 3, v253\n\t" \
    "v_xor_b32 v253, v253, v252\n\t" \
    "ds_read_b128 v[200:203], v253 offset:28672\n\t" \
    "ds_read_b128 v[204:207], v253 offset:29696\n\t" \
    "ds_read_b128 v[208:211], v253 offset:34816\n\t" \
    "ds_read_b128 v[212:215], v253 offset:35840\n\t" \
    "ds_read_b128 v[216:219], v253 offset:30720\n\t" \
    "ds_read_b128 v[220:223], v253 offset:31744\n\t" \
    "ds_read_b128 v[224:227], v253 offset:36864\n\t" \
    "ds_read_b128 v[228:231], v253 offset:37888\n\t" \
    "v_lshrrev_b32 v254, 1, v252\n\t" \
    "v_and_b32 v254, 0x180, v254\n\t" \
    "v_and_or_b32 v254, v252, %[c70], v254\n\t" \
    "v_bfe_i32 v255, v252, 7, 1\n\t" \
    "v_and_b32 v255, 0xaaaa, v255\n\t" \
    "v_or_b32 v255, 0x4444, v255\n\t" \
    "v_mov_b64 v[232:233], 0\n\t" \
    "v_mov_b64 v[234:235], 0\n\t" \
    "v_mov_b64 v[236:237], 0\n\t" \
    "v_mov_b64 v[238:239], 0\n\t" \
    "s_waitcnt lgkmcnt(0)\n\t" \
    "s_barrier\n\t" ASM_ST \
    "s_setprio 3\n\t" \
    "ds_read_b128 v[240:243], v252 offset:10240\n\t" \
    "ds_read_b128 v[244:247], v252 offset:11264\n\t" \
    "ds_read_b128 v[248:251], v252 offset:12288\n\t" \
    "s_waitcnt lgkmcnt(2)\n\t" \
    "v_smfmac_f32_16x16x64_bf16 v[232:235], v[240:243], v[200:207], v255\n\t" \
    "ds_read_b128 v[200:203], v253 offset:32768\n\t" \
    "ds_read_b128 v[204:207], v253 offset:33792\n\t" \
    "v_smfmac_f32_16x16x64_bf16 v[236:239], v[240:243], v[208:215], v255\n\t" \
    "ds_read_b128 v[208:211], v253 offset:38912\n\t" \
    "ds_read_b128 v[212:215], v253 offset:39936\n\t" \
    "s_waitcnt lgkmcnt(5)\n\t" \
    "v_smfmac_f32_16x16x64_bf16 v[232:235], v[244:247], v[216:223], v255\n\t" \
    "ds_read_b128 v[216:219], v254 offset:13312\n\t" \
    "ds_read_b128 v[220:223], v254 offset:13824\n\t" \
    "v_smfmac_f32_16x16x64_bf16 v[236:239], v[244:247], v[224:231], v255\n\t" \
    "ds_read_b128 v[224:227], v254 offset:14336\n\t" \
    "ds_read_b128 v[228:231], v254 offset:14848\n\t" \
    "s_waitcnt lgkmcnt(6)\n\t" \
    "v_smfmac_f32_16x16x64_bf16 v[232:235], v[248:251], v[200:207], v255\n\t" \
    "s_waitcnt lgkmcnt(4)\n\t" \
    "v_smfmac_f32_16x16x64_bf16 v[236:239], v[248:251], v[208:215], v255\n\t" \
    "ds_read_b128 v[208:211], v254 offset:15360\n\t" \
    "ds_read_b128 v[212:215], v254 offset:15872\n\t" \
    "v_mov_b64 v[204:205], 0\n\t" \
    "v_mov_b64 v[206:207], 0\n\t" \
    "s_nop 1\n\t" \
    "s_waitcnt lgkmcnt(4)\n\t" \
    "v_smfmac_f32_16x16x64_bf16 v[204:207], v[240:243], v[216:223], v255\n\t" \
    "s_waitcnt lgkmcnt(2)\n\t" \
    "v_smfmac_f32_16x16x64_bf16 v[204:207], v[244:247], v[224:231], v255\n\t" \
    "v_bfe_u32 v253, v252, 5, 1\n\t" \
    "v_bfe_i32 v202, v252, 4, 1\n\t" \
    "v_lshlrev_b32 v200, 1, v252\n\t" \
    "v_and_b32 v200, 0x180, v200\n\t" \
    "v_permlane32_swap_b32 v232, v234\n\t" \
    "v_permlane32_swap_b32 v233, v235\n\t" \
    "v_permlane32_swap_b32 v236, v238\n\t" \
    "v_permlane32_swap_b32 v237, v239\n\t" \
    "v_lshrrev_b32 v201, 2, v252\n\t" \
    "v_lshrrev_b32 v203, 4, v252\n\t" \
    "v_and_b32 v201, 0x40, v201\n\t" \
    "v_and_or_b32 v201, v203, 32, v201\n\t" \
    "v_and_or_b32 v201, v252, 16, v201\n\t" \
    "v_pk_add_f32 v[232:233], v[232:233], v[234:235]\n\t" \
    "v_pk_add_f32 v[234:235], v[236:237], v[238:239]\n\t" \
    "v_or_b32 v200, v200, v201\n\t" \
    "v_mad_u32_u24 v200, v253, %[cs], v200\n\t" \
    "v_mad_u32_u24 v201, v253, %[cs], v201\n\t" \
    "v_lshrrev_b32 v203, 3, v252\n\t" \
    "v_bfe_u32 v253, v252, 7, 1\n\t" \
    "s_waitcnt lgkmcnt(0)\n\t" \
    "v_smfmac_f32_16x16x64_bf16 v[204:207], v[248:251], v[208:215], v255\n\t" \
    "v_and_or_b32 v201, v203, 8, v201\n\t" \
    "v_and_b32 v202, 0x06060606, v202\n\t" \
    "v_mad_u32_u24 v201, v253, %[cp], v201\n\t" \
    "v_xor_b32 v202, 0x01000504, v202\n\t" \
    "v_max_f32 v236, v232, v234\n\t" \
    "v_max_f32 v237, v233, v235\n\t" \
    "s_nop 1\n\t" \
    "v_permlane32_swap_b32 v204, v206\n\t" \
    "v_permlane32_swap_b32 v205, v207\n\t" \
    "v_add_f32 v204, v204, v206\n\t" \
    "v_add_f32 v205, v205, v207\n\t" \
    "v_max_f32 v236, v236, v204\n\t" \
    "v_max_f32 v237, v237, v205\n\t" \
    "s_nop 0\n\t" \
    "v_max_f32_dpp v236, v236, v236 quad_perm:[1,0,3,2] row_mask:0xf bank_mask:0xf\n\t" \
    "v_max_f32_dpp v237, v237, v237 quad_perm:[1,0,3,2] row_mask:0xf bank_mask:0xf\n\t" \
    "s_nop 0\n\t" \
    "v_max_f32_dpp v236, v236, v236 quad_perm:[2,3,0,1] row_mask:0xf bank_mask:0xf\n\t" \
    "v_max_f32_dpp v237, v237, v237 quad_perm:[2,3,0,1] row_mask:0xf bank_mask:0xf\n\t" \
    "s_nop 0\n\t" \
    "v_max_f32_dpp v236, v236, v236 row_half_mirror row_mask:0xf bank_mask:0xf\n\t" \
    "v_max_f32_dpp v237, v237, v237 row_half_mirror row_mask:0xf bank_mask:0xf\n\t" \
    "s_nop 0\n\t" \
    "v_max_f32_dpp v236, v236, v236 row_mirror row_mask:0xf bank_mask:0xf\n\t" \
    "v_max_f32_dpp v237, v237, v237 row_mirror row_mask:0xf bank_mask:0xf\n\t" \
    "v_mul_f32 v236, %[scl], v236\n\t" \
    "v_mul_f32 v237, %[scl], v237\n\t" \
    "v_fma_f32 v232, v232, %[scl], -v236\n\t" \
    "v_fma_f32 v233, v233, %[scl], -v237\n\t" \
    "v_fma_f32 v234, v234, %[scl], -v236\n\t" \
    "v_fma_f32 v235, v235, %[scl], -v237\n\t" \
    "v_exp_f32 v232, v232\n\t" \
    "v_exp_f32 v233, v233\n\t" \
    "v_fma_f32 v204, v204, %[scl], -v236\n\t" \
    "v_fma_f32 v205, v205, %[scl], -v237\n\t" \
    "v_exp_f32 v234, v234\n\t" \
    "v_exp_f32 v235, v235\n\t" \
    "v_exp_f32 v204, v204\n\t" \
    "v_exp_f32 v205, v205\n\t" \
    "v_add_f32 v238, v232, v234\n\t" \
    "v_add_f32 v239, v233, v235\n\t" \
    "v_fma_f32 v238, v204, 0.5, v238\n\t" \
    "v_fma_f32 v239, v205, 0.5, v239\n\t" \
    "s_nop 0\n\t" \
    "v_add_f32_dpp v238, v238, v238 quad_perm:[1,0,3,2] row_mask:0xf bank_mask:0xf\n\t" \
    "v_add_f32_dpp v239, v239, v239 quad_perm:[1,0,3,2] row_mask:0xf bank_mask:0xf\n\t" \
    "s_nop 0\n\t" \
    "v_add_f32_dpp v238, v238, v238 quad_perm:[2,3,0,1] row_mask:0xf bank_mask:0xf\n\t" \
    "v_add_f32_dpp v239, v239, v239 quad_perm:[2,3,0,1] row_mask:0xf bank_mask:0xf\n\t" \
    "s_nop 0\n\t" \
    "v_add_f32_dpp v238, v238, v238 row_half_mirror row_mask:0xf bank_mask:0xf\n\t" \
    "v_add_f32_dpp v239, v239, v239 row_half_mirror row_mask:0xf bank_mask:0xf\n\t" \
    "s_nop 0\n\t" \
    "v_add_f32_dpp v238, v238, v238 row_mirror row_mask:0xf bank_mask:0xf\n\t" \
    "v_add_f32_dpp v239, v239, v239 row_mirror row_mask:0xf bank_mask:0xf\n\t" \
    "v_rcp_f32 v238, v238\n\t" \
    "v_rcp_f32 v239, v239\n\t" \
    "s_nop 0\n\t" \
    "v_pk_mul_f32 v[232:233], v[232:233], v[238:239]\n\t" \
    "v_pk_mul_f32 v[234:235], v[234:235], v[238:239]\n\t" \
    "v_pk_mul_f32 v[204:205], v[204:205], v[238:239]\n\t" \
    "v_cvt_pk_bf16_f32 v232, |v232|, |v233|\n\t" \
    "v_cvt_pk_bf16_f32 v234, |v234|, |v235|\n\t" \
    "v_cvt_pk_bf16_f32 v204, |v204|, |v205|\n\t" \
    "v_mov_b32_dpp v233, v232 quad_perm:[1,0,3,2] row_mask:0xf bank_mask:0xf\n\t" \
    "v_mov_b32_dpp v235, v234 quad_perm:[1,0,3,2] row_mask:0xf bank_mask:0xf\n\t" \
    "v_mov_b32_dpp v205, v204 quad_perm:[1,0,3,2] row_mask:0xf bank_mask:0xf\n\t" \
    "v_perm_b32 v232, v232, v233, v202\n\t" \
    "v_perm_b32 v234, v234, v235, v202\n\t" \
    "v_perm_b32 v204, v204, v205, v202\n\t" \
    "v_mov_b32_dpp v240, v232 quad_perm:[0,1,0,1] row_mask:0xf bank_mask:0xf\n\t" \
    "v_mov_b32_dpp v241, v234 quad_perm:[0,1,0,1] row_mask:0xf bank_mask:0xf\n\t" \
    "v_mov_b32_dpp v242, v232 quad_perm:[2,3,2,3] row_mask:0xf bank_mask:0xf\n\t" \
    "v_mov_b32_dpp v243, v234 quad_perm:[2,3,2,3] row_mask:0xf bank_mask:0xf\n\t" \
    "buffer_store_dwordx4 v[240:243], v200, %[rr], %[sx] offen sc1\n\t" \
    "v_mov_b32_dpp v244, v204 quad_perm:[0,1,0,1] row_mask:0xf bank_mask:0xf\n\t" \
    "v_mov_b32_dpp v245, v204 quad_perm:[2,3,2,3] row_mask:0xf bank_mask:0xf\n\t" \
    "buffer_store_dwordx4 v[240:243], v200, %[rr], %[sy] offen sc1\n\t" \
    "buffer_store_dwordx2 v[244:245], v201, %[rr], %[sm] offen sc1\n\t" \
    "s_wakeup\n\t" \
    PRISM
#define ASM_P4_0 \
    "ds_read_b128 v[200:203], %[qa] offset:0\n\t" \
    "ds_read_b128 v[204:207], %[qa] offset:1024\n\t" \
    "ds_read_b128 v[208:211], %[qa] offset:2048\n\t" \
    "ds_read_b128 v[212:215], %[qa] offset:3072\n\t" \
    "ds_read_b128 v[216:219], %[qa] offset:4096\n\t" \
    "ds_read_b128 v[220:223], %[qa] offset:5120\n\t" \
    "ds_read_b128 v[224:227], %[qa] offset:12288\n\t" \
    "ds_read_b128 v[228:231], %[qa] offset:13312\n\t" \
    "ds_read_b128 v[232:235], %[qa] offset:14336\n\t" \
    "ds_read_b128 v[236:239], %[qa] offset:15360\n\t" \
    "s_waitcnt lgkmcnt(0)\n\t" \
    "s_barrier\n\t" ASM_ST \
    "v_mfma_f32_16x16x32_bf16 v[240:243], v[224:227], v[200:203], v[240:243]\n\t" \
    "ds_read_b128 v[224:227], %[qa] offset:16384\n\t" \
    "v_mfma_f32_16x16x32_bf16 v[240:243], v[228:231], v[204:207], v[240:243]\n\t" \
    "ds_read_b128 v[228:231], %[qa] offset:17408\n\t" \
    "v_mfma_f32_16x16x32_bf16 v[240:243], v[232:235], v[208:211], v[240:243]\n\t" \
    "ds_read_b128 v[232:235], %[km] offset:0\n\t" \
    "v_mfma_f32_16x16x32_bf16 v[240:243], v[236:239], v[212:215], v[240:243]\n\t" \
    "ds_read_b128 v[236:239], %[km] offset:512\n\t" \
    "s_waitcnt lgkmcnt(3)\n\t" \
    "v_mfma_f32_16x16x32_bf16 v[240:243], v[224:227], v[216:219], v[240:243]\n\t" \
    "ds_read_b128 v[224:227], %[km] offset:1024\n\t" \
    "s_waitcnt lgkmcnt(3)\n\t" \
    "v_mfma_f32_16x16x32_bf16 v[240:243], v[228:231], v[220:223], v[240:243]\n\t" \
    "ds_read_b128 v[228:231], %[km] offset:1536\n\t" \
    "s_waitcnt lgkmcnt(3)\n\t" \
    "v_mfma_f32_16x16x32_bf16 v[248:251], v[232:235], v[200:203], 0\n\t" \
    "ds_read_b128 v[232:235], %[km] offset:2048\n\t" \
    "s_waitcnt lgkmcnt(3)\n\t" \
    "v_mfma_f32_16x16x32_bf16 v[248:251], v[236:239], v[204:207], v[248:251]\n\t" \
    "ds_read_b128 v[236:239], %[km] offset:2560\n\t" \
    "s_waitcnt lgkmcnt(3)\n\t" \
    "v_mfma_f32_16x16x32_bf16 v[248:251], v[224:227], v[208:211], v[248:251]\n\t" \
    "s_waitcnt lgkmcnt(2)\n\t" \
    "v_mfma_f32_16x16x32_bf16 v[248:251], v[228:231], v[212:215], v[248:251]\n\t" \
    "s_waitcnt lgkmcnt(1)\n\t" \
    "v_mfma_f32_16x16x32_bf16 v[248:251], v[232:235], v[216:219], v[248:251]\n\t" \
    "s_waitcnt lgkmcnt(0)\n\t" \
    "v_mfma_f32_16x16x32_bf16 v[248:251], v[236:239], v[220:223], v[248:251]\n\t" \
    "v_max3_f32 v224, v240, v241, v242\n\t" \
    "v_mov_b32 v225, v243\n\t" \
    "v_lshlrev_b32 v201, 23, %[qa]\n\t" \
    "v_and_b32 v201, 0x80000000, v201\n\t" \
    "v_and_b32 v200, 0x3ff, %[qa]\n\t" \
    "v_or_b32 v200, v201, v200\n\t" \
    "s_nop 1\n\t" \
    "v_max3_f32 v224, v224, v248, v249\n\t" \
    "v_max3_f32 v225, v225, v250, v251\n\t" \
    "v_max_f32 v224, v224, v225\n\t" \
    "v_mov_b32 v225, v224\n\t" \
    "s_nop 1\n\t" \
    "v_permlane16_swap_b32 v224, v225\n\t" \
    "v_max_f32 v224, v224, v225\n\t" \
    "v_mov_b32 v225, v224\n\t" \
    "s_nop 1\n\t" \
    "v_permlane32_swap_b32 v224, v225\n\t" \
    "v_max_f32 v224, v224, v225\n\t" \
    "v_mul_f32 v224, %[scl], v224\n\t" \
    "v_fma_f32 v240, v240, %[scl], -v224\n\t" \
    "v_fma_f32 v241, v241, %[scl], -v224\n\t" \
    "v_exp_f32 v240, v240\n\t" \
    "v_fma_f32 v242, v242, %[scl], -v224\n\t" \
    "v_exp_f32 v241, v241\n\t" \
    "v_fma_f32 v243, v243, %[scl], -v224\n\t" \
    "v_exp_f32 v242, v242\n\t" \
    "v_fma_f32 v248, v248, %[scl], -v224\n\t" \
    "v_exp_f32 v243, v243\n\t" \
    "v_fma_f32 v249, v249, %[scl], -v224\n\t" \
    "v_exp_f32 v248, v248\n\t" \
    "v_fma_f32 v250, v250, %[scl], -v224\n\t" \
    "v_exp_f32 v249, v249\n\t" \
    "v_fma_f32 v251, v251, %[scl], -v224\n\t" \
    "v_exp_f32 v250, v250\n\t" \
    "v_exp_f32 v251, v251\n\t" \
    "v_add_f32 v224, v240, v241\n\t" \
    "v_add_f32 v225, v242, v243\n\t" \
    "v_add_f32 v232, v248, v249\n\t" \
    "v_add_f32 v233, v250, v251\n\t" \
    "v_add_f32 v224, v224, v225\n\t" \
    "v_add_f32 v232, v232, v233\n\t" \
    "v_fma_f32 v224, v232, 0.5, v224\n\t" \
    "v_mov_b32 v225, v224\n\t" \
    "s_nop 1\n\t" \
    "v_permlane16_swap_b32 v224, v225\n\t" \
    "v_add_f32 v224, v224, v225\n\t" \
    "v_mov_b32 v225, v224\n\t" \
    "s_nop 1\n\t" \
    "v_permlane32_swap_b32 v224, v225\n\t" \
    "v_add_f32 v224, v224, v225\n\t" \
    "v_rcp_f32 v224, v224\n\t" \
    "s_nop 0\n\t" \
    "v_mul_f32 v240, v240, v224\n\t" \
    "v_mul_f32 v241, v241, v224\n\t" \
    "v_mul_f32 v242, v242, v224\n\t" \
    "v_mul_f32 v243, v243, v224\n\t" \
    "v_mul_f32 v248, v248, v224\n\t" \
    "v_mul_f32 v249, v249, v224\n\t" \
    "v_mul_f32 v250, v250, v224\n\t" \
    "v_mul_f32 v251, v251, v224\n\t" \
    "v_cvt_pk_bf16_f32 v228, |v240|, |v241|\n\t" \
    "v_cvt_pk_bf16_f32 v229, |v242|, |v243|\n\t" \
    "v_cvt_pk_bf16_f32 v232, |v248|, |v249|\n\t" \
    "v_cvt_pk_bf16_f32 v233, |v250|, |v251|\n\t" \
    "v_permlane16_swap_b32 v228, v230\n\t" \
    "v_permlane16_swap_b32 v229, v231\n\t" \
    "buffer_store_dwordx4 v[228:231], v200, %[rr], %[so] offen sc1\n\t" \
    "buffer_store_dwordx2 v[232:233], %[pm], %[rr], %[so] offen sc1\n\t" \
    "s_nop 1\n\t"
#define ASM_P4_1 \
    "ds_read_b128 v[200:203], %[qa] offset:0\n\t" \
    "ds_read_b128 v[204:207], %[qa] offset:1024\n\t" \
    "ds_read_b128 v[208:211], %[qa] offset:2048\n\t" \
    "ds_read_b128 v[212:215], %[qa] offset:3072\n\t" \
    "ds_read_b128 v[216:219], %[qa] offset:4096\n\t" \
    "ds_read_b128 v[220:223], %[qa] offset:5120\n\t" \
    "ds_read_b128 v[224:227], %[qa] offset:12288\n\t" \
    "ds_read_b128 v[228:231], %[qa] offset:13312\n\t" \
    "ds_read_b128 v[232:235], %[qa] offset:14336\n\t" \
    "ds_read_b128 v[236:239], %[qa] offset:15360\n\t" \
    "s_waitcnt lgkmcnt(0)\n\t" \
    "s_barrier\n\t" ASM_ST \
    "v_mfma_f32_16x16x32_bf16 v[240:243], v[224:227], v[200:203], v[240:243]\n\t" \
    "ds_read_b128 v[224:227], %[qa] offset:16384\n\t" \
    "v_mfma_f32_16x16x32_bf16 v[240:243], v[228:231], v[204:207], v[240:243]\n\t" \
    "ds_read_b128 v[228:231], %[qa] offset:17408\n\t" \
    "v_mfma_f32_16x16x32_bf16 v[240:243], v[232:235], v[208:211], v[240:243]\n\t" \
    "ds_read_b128 v[232:235], %[qa] offset:6144\n\t" \
    "v_mfma_f32_16x16x32_bf16 v[240:243], v[236:239], v[212:215], v[240:243]\n\t" \
    "ds_read_b128 v[236:239], %[qa] offset:7168\n\t" \
    "s_waitcnt lgkmcnt(3)\n\t" \
    "v_mfma_f32_16x16x32_bf16 v[240:243], v[224:227], v[216:219], v[240:243]\n\t" \
    "ds_read_b128 v[224:227], %[qa] offset:8192\n\t" \
    "s_waitcnt lgkmcnt(3)\n\t" \
    "v_mfma_f32_16x16x32_bf16 v[240:243], v[228:231], v[220:223], v[240:243]\n\t" \
    "ds_read_b128 v[228:231], %[qa] offset:9216\n\t" \
    "s_waitcnt lgkmcnt(3)\n\t" \
    "v_mfma_f32_16x16x32_bf16 v[244:247], v[232:235], v[200:203], 0\n\t" \
    "ds_read_b128 v[232:235], %[qa] offset:10240\n\t" \
    "s_waitcnt lgkmcnt(3)\n\t" \
    "v_mfma_f32_16x16x32_bf16 v[244:247], v[236:239], v[204:207], v[244:247]\n\t" \
    "ds_read_b128 v[236:239], %[qa] offset:11264\n\t" \
    "s_waitcnt lgkmcnt(3)\n\t" \
    "v_mfma_f32_16x16x32_bf16 v[244:247], v[224:227], v[208:211], v[244:247]\n\t" \
    "ds_read_b128 v[224:227], %[km] offset:0\n\t" \
    "s_waitcnt lgkmcnt(3)\n\t" \
    "v_mfma_f32_16x16x32_bf16 v[244:247], v[228:231], v[212:215], v[244:247]\n\t" \
    "ds_read_b128 v[228:231], %[km] offset:512\n\t" \
    "s_waitcnt lgkmcnt(3)\n\t" \
    "v_mfma_f32_16x16x32_bf16 v[244:247], v[232:235], v[216:219], v[244:247]\n\t" \
    "ds_read_b128 v[232:235], %[km] offset:1024\n\t" \
    "s_waitcnt lgkmcnt(3)\n\t" \
    "v_mfma_f32_16x16x32_bf16 v[244:247], v[236:239], v[220:223], v[244:247]\n\t" \
    "ds_read_b128 v[236:239], %[km] offset:1536\n\t" \
    "s_waitcnt lgkmcnt(3)\n\t" \
    "v_mfma_f32_16x16x32_bf16 v[248:251], v[224:227], v[200:203], 0\n\t" \
    "ds_read_b128 v[224:227], %[km] offset:2048\n\t" \
    "s_waitcnt lgkmcnt(3)\n\t" \
    "v_mfma_f32_16x16x32_bf16 v[248:251], v[228:231], v[204:207], v[248:251]\n\t" \
    "ds_read_b128 v[228:231], %[km] offset:2560\n\t" \
    "s_waitcnt lgkmcnt(3)\n\t" \
    "v_mfma_f32_16x16x32_bf16 v[248:251], v[232:235], v[208:211], v[248:251]\n\t" \
    "s_waitcnt lgkmcnt(2)\n\t" \
    "v_mfma_f32_16x16x32_bf16 v[248:251], v[236:239], v[212:215], v[248:251]\n\t" \
    "s_waitcnt lgkmcnt(1)\n\t" \
    "v_mfma_f32_16x16x32_bf16 v[248:251], v[224:227], v[216:219], v[248:251]\n\t" \
    "s_waitcnt lgkmcnt(0)\n\t" \
    "v_mfma_f32_16x16x32_bf16 v[248:251], v[228:231], v[220:223], v[248:251]\n\t" \
    "v_max3_f32 v224, v240, v241, v242\n\t" \
    "v_max3_f32 v225, v244, v245, v246\n\t" \
    "v_max3_f32 v224, v224, v243, v247\n\t" \
    "v_and_b32 v200, 0x3ff, %[qa]\n\t" \
    "s_nop 3\n\t" \
    "v_max3_f32 v224, v224, v248, v249\n\t" \
    "v_max3_f32 v225, v225, v250, v251\n\t" \
    "v_max_f32 v224, v224, v225\n\t" \
    "v_mov_b32 v225, v224\n\t" \
    "s_nop 1\n\t" \
    "v_permlane16_swap_b32 v224, v225\n\t" \
    "v_max_f32 v224, v224, v225\n\t" \
    "v_mov_b32 v225, v224\n\t" \
    "s_nop 1\n\t" \
    "v_permlane32_swap_b32 v224, v225\n\t" \
    "v_max_f32 v224, v224, v225\n\t" \
    "v_mul_f32 v224, %[scl], v224\n\t" \
    "v_fma_f32 v240, v240, %[scl], -v224\n\t" \
    "v_fma_f32 v241, v241, %[scl], -v224\n\t" \
    "v_exp_f32 v240, v240\n\t" \
    "v_fma_f32 v242, v242, %[scl], -v224\n\t" \
    "v_exp_f32 v241, v241\n\t" \
    "v_fma_f32 v243, v243, %[scl], -v224\n\t" \
    "v_exp_f32 v242, v242\n\t" \
    "v_fma_f32 v244, v244, %[scl], -v224\n\t" \
    "v_exp_f32 v243, v243\n\t" \
    "v_fma_f32 v245, v245, %[scl], -v224\n\t" \
    "v_exp_f32 v244, v244\n\t" \
    "v_fma_f32 v246, v246, %[scl], -v224\n\t" \
    "v_exp_f32 v245, v245\n\t" \
    "v_fma_f32 v247, v247, %[scl], -v224\n\t" \
    "v_exp_f32 v246, v246\n\t" \
    "v_fma_f32 v248, v248, %[scl], -v224\n\t" \
    "v_exp_f32 v247, v247\n\t" \
    "v_fma_f32 v249, v249, %[scl], -v224\n\t" \
    "v_exp_f32 v248, v248\n\t" \
    "v_fma_f32 v250, v250, %[scl], -v224\n\t" \
    "v_exp_f32 v249, v249\n\t" \
    "v_fma_f32 v251, v251, %[scl], -v224\n\t" \
    "v_exp_f32 v250, v250\n\t" \
    "v_exp_f32 v251, v251\n\t" \
    "v_add_f32 v224, v240, v241\n\t" \
    "v_add_f32 v225, v242, v243\n\t" \
    "v_add_f32 v226, v244, v245\n\t" \
    "v_add_f32 v227, v246, v247\n\t" \
    "v_add_f32 v232, v248, v249\n\t" \
    "v_add_f32 v233, v250, v251\n\t" \
    "v_add_f32 v224, v224, v225\n\t" \
    "v_add_f32 v226, v226, v227\n\t" \
    "v_add_f32 v232, v232, v233\n\t" \
    "v_add_f32 v224, v224, v226\n\t" \
    "v_fma_f32 v224, v232, 0.5, v224\n\t" \
    "v_mov_b32 v225, v224\n\t" \
    "s_nop 1\n\t" \
    "v_permlane16_swap_b32 v224, v225\n\t" \
    "v_add_f32 v224, v224, v225\n\t" \
    "v_mov_b32 v225, v224\n\t" \
    "s_nop 1\n\t" \
    "v_permlane32_swap_b32 v224, v225\n\t" \
    "v_add_f32 v224, v224, v225\n\t" \
    "v_rcp_f32 v224, v224\n\t" \
    "s_nop 0\n\t" \
    "v_mul_f32 v240, v240, v224\n\t" \
    "v_mul_f32 v241, v241, v224\n\t" \
    "v_mul_f32 v242, v242, v224\n\t" \
    "v_mul_f32 v243, v243, v224\n\t" \
    "v_mul_f32 v244, v244, v224\n\t" \
    "v_mul_f32 v245, v245, v224\n\t" \
    "v_mul_f32 v246, v246, v224\n\t" \
    "v_mul_f32 v247, v247, v224\n\t" \
    "v_mul_f32 v248, v248, v224\n\t" \
    "v_mul_f32 v249, v249, v224\n\t" \
    "v_mul_f32 v250, v250, v224\n\t" \
    "v_mul_f32 v251, v251, v224\n\t" \
    "v_cvt_pk_bf16_f32 v230, |v240|, |v241|\n\t" \
    "v_cvt_pk_bf16_f32 v231, |v242|, |v243|\n\t" \
    "v_cvt_pk_bf16_f32 v228, |v244|, |v245|\n\t" \
    "v_cvt_pk_bf16_f32 v229, |v246|, |v247|\n\t" \
    "v_cvt_pk_bf16_f32 v232, |v248|, |v249|\n\t" \
    "v_cvt_pk_bf16_f32 v233, |v250|, |v251|\n\t" \
    "buffer_store_dwordx4 v[228:231], v200, %[rr], %[so] offen sc1\n\t" \
    "buffer_store_dwordx2 v[232:233], %[pm], %[rr], %[so] offen sc1\n\t" \
    "s_nop 1\n\t"
#define ASM_RCRIT \
    "v_mov_b64 v[176:177], -1\n\t" \
    "v_mov_b64 v[178:179], -1\n\t" \
    "v_mov_b64 v[180:181], -1\n\t" \
    "v_mov_b64 v[182:183], -1\n\t" \
    "v_mov_b64 v[184:185], -1\n\t" \
    "v_mov_b64 v[186:187], -1\n\t" \
    "v_mov_b64 v[188:189], -1\n\t" \
    "v_mov_b64 v[190:191], -1\n\t" \
    "v_mov_b64 v[192:193], -1\n\t" \
    "v_mov_b64 v[194:195], -1\n\t" \
    ".Lfa%=:\n\t" \
    "s_mov_b32 %[a], 0\n\t" \
    "v_max_u32 v196, v176, v177\n\t" \
    "v_max3_u32 v196, v178, v179, v196\n\t" \
    "v_cmp_eq_u32 vcc, -1, v196\n\t" \
    "s_cbranch_vccz .Lga0_%=\n\t" \
    "buffer_load_dwordx4 v[176:179], %[ov], %[rs], %[sv] offen sc1\n\t" \
    "s_mov_b32 %[a], 1\n\t" \
    ".Lga0_%=:\n\t" \
    "v_max_u32 v196, v180, v181\n\t" \
    "v_max3_u32 v196, v182, v183, v196\n\t" \
    "v_cmp_eq_u32 vcc, -1, v196\n\t" \
    "s_cbranch_vccz .Lga1_%=\n\t" \
    "buffer_load_dwordx4 v[180:183], %[ov], %[rs], %[sv] offen offset:1024 sc1\n\t" \
    "s_mov_b32 %[a], 1\n\t" \
    ".Lga1_%=:\n\t" \
    "s_cmp_eq_u32 %[a], 0\n\t" \
    "s_cbranch_scc1 .Lha%=\n\t" \
    "s_waitcnt vmcnt(12)\n\t" \
    ".Lia%=:\n\t" \
    "v_max_u32 v196, v176, v177\n\t" \
    "v_max3_u32 v196, v178, v179, v196\n\t" \
    "v_max3_u32 v196, v180, v181, v196\n\t" \
    "v_max3_u32 v196, v182, v183, v196\n\t" \
    "v_cmp_eq_u32 vcc, -1, v196\n\t" \
    "s_cbranch_vccz .Lha%=\n\t" \
    "s_nop 7\n\t" \
    "v_max_u32 v196, v176, v177\n\t" \
    "v_max3_u32 v196, v178, v179, v196\n\t" \
    "v_max3_u32 v196, v180, v181, v196\n\t" \
    "v_max3_u32 v196, v182, v183, v196\n\t" \
    "v_cmp_eq_u32 vcc, -1, v196\n\t" \
    "s_cbranch_vccz .Lha%=\n\t" \
    "s_nop 7\n\t" \
    "v_max_u32 v196, v176, v177\n\t" \
    "v_max3_u32 v196, v178, v179, v196\n\t" \
    "v_max3_u32 v196, v180, v181, v196\n\t" \
    "v_max3_u32 v196, v182, v183, v196\n\t" \
    "v_cmp_eq_u32 vcc, -1, v196\n\t" \
    "s_cbranch_vccz .Lha%=\n\t" \
    "s_nop 7\n\t" \
    "v_max_u32 v196, v176, v177\n\t" \
    "v_max3_u32 v196, v178, v179, v196\n\t" \
    "v_max3_u32 v196, v180, v181, v196\n\t" \
    "v_max3_u32 v196, v182, v183, v196\n\t" \
    "v_cmp_eq_u32 vcc, -1, v196\n\t" \
    "s_cbranch_vccz .Lha%=\n\t" \
    "s_nop 7\n\t" \
    "s_sub_u32 %[n], %[n], 1\n\t" \
    "s_cmp_gt_i32 %[n], 0\n\t" \
    "s_cbranch_scc1 .Lfa%=\n\t" \
    ".Lha%=:\n\t" \
    ASM_ST3 \
    "buffer_load_dwordx4 v[184:187], %[o0], %[rs], %[sp] offen sc1\n\t" \
    "buffer_load_dwordx4 v[188:191], %[o0], %[rs], %[sp] offen offset:1024 sc1\n\t" \
    "buffer_load_dwordx4 v[192:195], %[o2], %[rs], %[sp] offen sc1\n\t" \
    "s_waitcnt vmcnt(0)\n\t" \
    "v_max_u32 v196, v184, v185\n\t" \
    "v_max3_u32 v196, v186, v187, v196\n\t" \
    "v_max3_u32 v196, v188, v189, v196\n\t" \
    "v_max3_u32 v196, v190, v191, v196\n\t" \
    "v_max3_u32 v196, v192, v193, v196\n\t" \
    "v_max3_u32 v196, v194, v195, v196\n\t" \
    "v_cmp_eq_u32 vcc, -1, v196\n\t" \
    "s_cbranch_vccz .Lhb%=\n\t" \
    ".Lfb%=:\n\t" \
    "s_mov_b32 %[a], 0\n\t" \
    "v_max_u32 v196, v184, v185\n\t" \
    "v_max3_u32 v196, v186, v187, v196\n\t" \
    "v_cmp_eq_u32 vcc, -1, v196\n\t" \
    "s_cbranch_vccz .Lgb0_%=\n\t" \
    "buffer_load_dwordx4 v[184:187], %[o0], %[rs], %[sp] offen sc1\n\t" \
    "s_mov_b32 %[a], 1\n\t" \
    ".Lgb0_%=:\n\t" \
    "v_max_u32 v196, v188, v189\n\t" \
    "v_max3_u32 v196, v190, v191, v196\n\t" \
    "v_cmp_eq_u32 vcc, -1, v196\n\t" \
    "s_cbranch_vccz .Lgb1_%=\n\t" \
    "buffer_load_dwordx4 v[188:191], %[o0], %[rs], %[sp] offen offset:1024 sc1\n\t" \
    "s_mov_b32 %[a], 1\n\t" \
    ".Lgb1_%=:\n\t" \
    "v_max_u32 v196, v192, v193\n\t" \
    "v_max3_u32 v196, v194, v195, v196\n\t" \
    "v_cmp_eq_u32 vcc, -1, v196\n\t" \
    "s_cbranch_vccz .Lgb2_%=\n\t" \
    "buffer_load_dwordx4 v[192:195], %[o2], %[rs], %[sp] offen sc1\n\t" \
    "s_mov_b32 %[a], 1\n\t" \
    ".Lgb2_%=:\n\t" \
    "s_cmp_eq_u32 %[a], 0\n\t" \
    "s_cbranch_scc1 .Lhb%=\n\t" \
    "s_waitcnt vmcnt(12)\n\t" \
    ".Lib%=:\n\t" \
    "v_max_u32 v196, v184, v185\n\t" \
    "v_max3_u32 v196, v186, v187, v196\n\t" \
    "v_max3_u32 v196, v188, v189, v196\n\t" \
    "v_max3_u32 v196, v190, v191, v196\n\t" \
    "v_max3_u32 v196, v192, v193, v196\n\t" \
    "v_max3_u32 v196, v194, v195, v196\n\t" \
    "v_cmp_eq_u32 vcc, -1, v196\n\t" \
    "s_cbranch_vccz .Lhb%=\n\t" \
    "s_nop 7\n\t" \
    "v_max_u32 v196, v184, v185\n\t" \
    "v_max3_u32 v196, v186, v187, v196\n\t" \
    "v_max3_u32 v196, v188, v189, v196\n\t" \
    "v_max3_u32 v196, v190, v191, v196\n\t" \
    "v_max3_u32 v196, v192, v193, v196\n\t" \
    "v_max3_u32 v196, v194, v195, v196\n\t" \
    "v_cmp_eq_u32 vcc, -1, v196\n\t" \
    "s_cbranch_vccz .Lhb%=\n\t" \
    "s_nop 7\n\t" \
    "v_max_u32 v196, v184, v185\n\t" \
    "v_max3_u32 v196, v186, v187, v196\n\t" \
    "v_max3_u32 v196, v188, v189, v196\n\t" \
    "v_max3_u32 v196, v190, v191, v196\n\t" \
    "v_max3_u32 v196, v192, v193, v196\n\t" \
    "v_max3_u32 v196, v194, v195, v196\n\t" \
    "v_cmp_eq_u32 vcc, -1, v196\n\t" \
    "s_cbranch_vccz .Lhb%=\n\t" \
    "s_nop 7\n\t" \
    "v_max_u32 v196, v184, v185\n\t" \
    "v_max3_u32 v196, v186, v187, v196\n\t" \
    "v_max3_u32 v196, v188, v189, v196\n\t" \
    "v_max3_u32 v196, v190, v191, v196\n\t" \
    "v_max3_u32 v196, v192, v193, v196\n\t" \
    "v_max3_u32 v196, v194, v195, v196\n\t" \
    "v_cmp_eq_u32 vcc, -1, v196\n\t" \
    "s_cbranch_vccz .Lhb%=\n\t" \
    "s_nop 7\n\t" \
    "s_sub_u32 %[n], %[n], 1\n\t" \
    "s_cmp_gt_i32 %[n], 0\n\t" \
    "s_cbranch_scc1 .Lfb%=\n\t" \
    ".Lhb%=:\n\t" \
    ASM_ST2 \
    "v_mfma_f32_16x16x32_bf16 v[160:163], v[192:195], v[176:179], 0\n\t" \
    "v_mfma_f32_16x16x32_bf16 v[164:167], v[192:195], v[180:183], 0\n\t" \
    "v_smfmac_f32_16x16x64_bf16 v[160:163], v[184:187], v[128:135], %[sidx]\n\t" \
    "v_smfmac_f32_16x16x64_bf16 v[164:167], v[184:187], v[144:151], %[sidx]\n\t" \
    "v_smfmac_f32_16x16x64_bf16 v[160:163], v[188:191], v[136:143], %[sidx]\n\t" \
    "v_smfmac_f32_16x16x64_bf16 v[164:167], v[188:191], v[152:159], %[sidx]\n\t" \
    "s_cmp_eq_u32 %[pub], 0\n\t" \
    "s_nop 7\n\t" \
    "v_permlane32_swap_b32 v160, v164\n\t" \
    "v_permlane32_swap_b32 v161, v165\n\t" \
    "v_permlane32_swap_b32 v162, v166\n\t" \
    "v_permlane32_swap_b32 v163, v167\n\t" \
    "v_add_f32 v160, v160, v164\n\t" \
    "v_add_f32 v161, v161, v165\n\t" \
    "ds_write2_b32 %[rw], v160, v161 offset1:80\n\t" \
    "v_add_f32 v162, v162, v166\n\t" \
    "v_add_f32 v163, v163, v167\n\t" \
    "ds_write2_b32 %[rw], v162, v163 offset0:160 offset1:240\n\t" \
    "s_waitcnt lgkmcnt(0)\n\t" \
    "s_barrier\n\t" ASM_ST \
    "s_cbranch_scc1 .Lre%=\n\t" \
    "ds_read_b128 v[200:203], %[rd] offset:0\n\t" \
    "ds_read_b128 v[204:207], %[rd] offset:16\n\t" \
    "s_waitcnt lgkmcnt(1)\n\t" \
    "v_pk_add_f32 v[200:201], v[200:201], v[202:203]\n\t" \
    "s_waitcnt lgkmcnt(0)\n\t" \
    "v_pk_add_f32 v[204:205], v[204:205], v[206:207]\n\t" \
    "v_pk_add_f32 v[200:201], v[200:201], v[204:205]\n\t" \
    "s_nop 1\n\t" \
    "v_permlane32_swap_b32 v200, v201\n\t" \
    "v_add_f32 v202, v200, v201\n\t" \
    "v_add_f32 v203, v200, v201\n\t" \
    "s_nop 1\n\t" \
    "v_permlane32_swap_b32 v202, v203\n\t" \
    "v_cvt_pk_bf16_f32 v200, v202, v203\n\t" \
    "v_min_u32 v200, -2, v200\n\t" \
    "buffer_store_dword v200, %[oo], %[rs], %[so] offen sc1\n\t" \
    ".Lre%=:\n\t"
#define ASM_RX \
    "s_cmp_eq_u32 %[fin], 0\n\t" \
    "s_cbranch_scc1 .Lx0%=\n\t" \
    "buffer_load_dwordx2 v[250:251], %[xo], %[rx], %[sx] offen sc1\n\t" \
    ".Lx0%=:\n\t" \
    "buffer_load_dword v248, %[fo], %[rs], %[fs] offen sc1\n\t" \
    "buffer_load_dwordx4 v[200:203], %[l16], %[rs], %[s0] offen offset:0 sc1\n\t" \
    "buffer_load_dwordx4 v[204:207], %[l16], %[rs], %[s0] offen offset:1024 sc1\n\t" \
    "buffer_load_dwordx4 v[208:211], %[l16], %[rs], %[s0] offen offset:2048 sc1\n\t" \
    "buffer_load_dwordx4 v[212:215], %[l16], %[rs], %[s0] offen offset:3072 sc1\n\t" \
    "buffer_load_dwordx4 v[216:219], %[l16], %[rs], %[s1] offen offset:0 sc1\n\t" \
    "buffer_load_dwordx4 v[220:223], %[l16], %[rs], %[s1] offen offset:1024 sc1\n\t" \
    "buffer_load_dwordx4 v[224:227], %[l16], %[rs], %[s1] offen offset:2048 sc1\n\t" \
    "buffer_load_dwordx4 v[228:231], %[l16], %[rs], %[s1] offen offset:3072 sc1\n\t" \
    "v_mov_b64 v[96:97], v[128:129]\n\t" \
    "v_mov_b64 v[98:99], v[132:133]\n\t" \
    "v_mov_b64 v[100:101], v[136:137]\n\t" \
    "v_mov_b64 v[102:103], v[140:141]\n\t" \
    "v_mov_b64 v[104:105], v[144:145]\n\t" \
    "v_mov_b64 v[106:107], v[148:149]\n\t" \
    "v_mov_b64 v[108:109], v[152:153]\n\t" \
    "v_mov_b64 v[110:111], v[156:157]\n\t" \
    "s_movk_i32 %[n], 0x200\n\t" \
    "s_nop 1\n\t" \
    "v_permlane16_swap_b32 v96, v98\n\t" \
    "v_permlane16_swap_b32 v97, v99\n\t" \
    "v_permlane16_swap_b32 v100, v102\n\t" \
    "v_permlane16_swap_b32 v101, v103\n\t" \
    "v_permlane16_swap_b32 v104, v106\n\t" \
    "v_permlane16_swap_b32 v105, v107\n\t" \
    "v_permlane16_swap_b32 v108, v110\n\t" \
    "v_permlane16_swap_b32 v109, v111\n\t" \
    "s_waitcnt vmcnt(7)\n\t" \
    "v_max_u32 v196, v200, v201\n\t" \
    "v_max3_u32 v196, v202, v203, v196\n\t" \
    "s_waitcnt vmcnt(6)\n\t" \
    "v_max3_u32 v196, v204, v205, v196\n\t" \
    "v_max3_u32 v196, v206, v207, v196\n\t" \
    "s_waitcnt vmcnt(5)\n\t" \
    "v_max3_u32 v196, v208, v209, v196\n\t" \
    "v_max3_u32 v196, v210, v211, v196\n\t" \
    "s_waitcnt vmcnt(4)\n\t" \
    "v_max3_u32 v196, v212, v213, v196\n\t" \
    "v_max3_u32 v196, v214, v215, v196\n\t" \
    "s_waitcnt vmcnt(3)\n\t" \
    "v_max3_u32 v196, v216, v217, v196\n\t" \
    "v_max3_u32 v196, v218, v219, v196\n\t" \
    "s_waitcnt vmcnt(2)\n\t" \
    "v_max3_u32 v196, v220, v221, v196\n\t" \
    "v_max3_u32 v196, v222, v223, v196\n\t" \
    "s_waitcnt vmcnt(1)\n\t" \
    "v_max3_u32 v196, v224, v225, v196\n\t" \
    "v_max3_u32 v196, v226, v227, v196\n\t" \
    "s_waitcnt vmcnt(0)\n\t" \
    "v_max3_u32 v196, v228, v229, v196\n\t" \
    "v_max3_u32 v196, v230, v231, v196\n\t" \
    "v_cmp_eq_u32 vcc, -1, v196\n\t" \
    "s_cbranch_vccz .Lxd%=\n\t" \
    ".Lxl%=:\n\t" \
    "s_mov_b32 %[a], 0\n\t" \
    "v_max_u32 v196, v200, v201\n\t" \
    "v_max3_u32 v196, v202, v203, v196\n\t" \
    "v_cmp_eq_u32 vcc, -1, v196\n\t" \
    "s_cbranch_vccz .Lxs0_%=\n\t" \
    "buffer_load_dwordx4 v[200:203], %[l16], %[rs], %[s0] offen offset:0 sc1\n\t" \
    "s_mov_b32 %[a], 1\n\t" \
    ".Lxs0_%=:\n\t" \
    "v_max_u32 v196, v204, v205\n\t" \
    "v_max3_u32 v196, v206, v207, v196\n\t" \
    "v_cmp_eq_u32 vcc, -1, v196\n\t" \
    "s_cbranch_vccz .Lxs1_%=\n\t" \
    "buffer_load_dwordx4 v[204:207], %[l16], %[rs], %[s0] offen offset:1024 sc1\n\t" \
    "s_mov_b32 %[a], 1\n\t" \
    ".Lxs1_%=:\n\t" \
    "v_max_u32 v196, v208, v209\n\t" \
    "v_max3_u32 v196, v210, v211, v196\n\t" \
    "v_cmp_eq_u32 vcc, -1, v196\n\t" \
    "s_cbranch_vccz .Lxs2_%=\n\t" \
    "buffer_load_dwordx4 v[208:211], %[l16], %[rs], %[s0] offen offset:2048 sc1\n\t" \
    "s_mov_b32 %[a], 1\n\t" \
    ".Lxs2_%=:\n\t" \
    "v_max_u32 v196, v212, v213\n\t" \
    "v_max3_u32 v196, v214, v215, v196\n\t" \
    "v_cmp_eq_u32 vcc, -1, v196\n\t" \
    "s_cbranch_vccz .Lxs3_%=\n\t" \
    "buffer_load_dwordx4 v[212:215], %[l16], %[rs], %[s0] offen offset:3072 sc1\n\t" \
    "s_mov_b32 %[a], 1\n\t" \
    ".Lxs3_%=:\n\t" \
    "v_max_u32 v196, v216, v217\n\t" \
    "v_max3_u32 v196, v218, v219, v196\n\t" \
    "v_cmp_eq_u32 vcc, -1, v196\n\t" \
    "s_cbranch_vccz .Lxs4_%=\n\t" \
    "buffer_load_dwordx4 v[216:219], %[l16], %[rs], %[s1] offen offset:0 sc1\n\t" \
    "s_mov_b32 %[a], 1\n\t" \
    ".Lxs4_%=:\n\t" \
    "v_max_u32 v196, v220, v221\n\t" \
    "v_max3_u32 v196, v222, v223, v196\n\t" \
    "v_cmp_eq_u32 vcc, -1, v196\n\t" \
    "s_cbranch_vccz .Lxs5_%=\n\t" \
    "buffer_load_dwordx4 v[220:223], %[l16], %[rs], %[s1] offen offset:1024 sc1\n\t" \
    "s_mov_b32 %[a], 1\n\t" \
    ".Lxs5_%=:\n\t" \
    "v_max_u32 v196, v224, v225\n\t" \
    "v_max3_u32 v196, v226, v227, v196\n\t" \
    "v_cmp_eq_u32 vcc, -1, v196\n\t" \
    "s_cbranch_vccz .Lxs6_%=\n\t" \
    "buffer_load_dwordx4 v[224:227], %[l16], %[rs], %[s1] offen offset:2048 sc1\n\t" \
    "s_mov_b32 %[a], 1\n\t" \
    ".Lxs6_%=:\n\t" \
    "v_max_u32 v196, v228, v229\n\t" \
    "v_max3_u32 v196, v230, v231, v196\n\t" \
    "v_cmp_eq_u32 vcc, -1, v196\n\t" \
    "s_cbranch_vccz .Lxs7_%=\n\t" \
    "buffer_load_dwordx4 v[228:231], %[l16], %[rs], %[s1] offen offset:3072 sc1\n\t" \
    "s_mov_b32 %[a], 1\n\t" \
    ".Lxs7_%=:\n\t" \
    "s_cmp_eq_u32 %[a], 0\n\t" \
    "s_cbranch_scc1 .Lxd%=\n\t" \
    "s_waitcnt vmcnt(0)\n\t" \
    "s_sub_u32 %[n], %[n], 1\n\t" \
    "s_cmp_lg_u32 %[n], 0\n\t" \
    "s_cbranch_scc1 .Lxl%=\n\t" \
    "s_branch .Lxe%=\n\t" \
    ".Lxd%=:\n\t" \
    ASM_ST2 \
    "v_mfma_f32_16x16x32_bf16 v[120:123], v[128:131], v[200:203], 0\n\t" \
    "v_mfma_f32_16x16x32_bf16 v[124:127], v[144:147], v[200:203], 0\n\t" \
    "v_mfma_f32_16x16x32_bf16 v[120:123], v[132:135], v[204:207], v[120:123]\n\t" \
    "v_mfma_f32_16x16x32_bf16 v[124:127], v[148:151], v[204:207], v[124:127]\n\t" \
    "v_mfma_f32_16x16x32_bf16 v[120:123], v[136:139], v[208:211], v[120:123]\n\t" \
    "v_mfma_f32_16x16x32_bf16 v[124:127], v[152:155], v[208:211], v[124:127]\n\t" \
    "v_mfma_f32_16x16x32_bf16 v[120:123], v[140:143], v[212:215], v[120:123]\n\t" \
    "v_mfma_f32_16x16x32_bf16 v[124:127], v[156:159], v[212:215], v[124:127]\n\t" \
    "v_mfma_f32_16x16x32_bf16 v[112:115], v[96:99], v[220:223], 0\n\t" \
    "v_mfma_f32_16x16x32_bf16 v[116:119], v[104:107], v[220:223], 0\n\t" \
    "v_mfma_f32_16x16x32_bf16 v[120:123], v[176:179], v[216:219], v[120:123]\n\t" \
    "v_mfma_f32_16x16x32_bf16 v[124:127], v[180:183], v[216:219], v[124:127]\n\t" \
    "v_mfma_f32_16x16x32_bf16 v[112:115], v[100:103], v[224:227], v[112:115]\n\t" \
    "v_mfma_f32_16x16x32_bf16 v[116:119], v[108:111], v[224:227], v[116:119]\n\t" \
    "v_mfma_f32_16x16x32_bf16 v[112:115], v[176:179], v[228:231], v[112:115]\n\t" \
    "v_mfma_f32_16x16x32_bf16 v[116:119], v[180:183], v[228:231], v[116:119]\n\t" \
    "s_nop 1\n\t" \
    "ds_write_b128 %[pw], v[120:123] offset:16384\n\t" \
    "ds_write_b128 %[pw], v[124:127] offset:24576\n\t" \
    "s_nop 3\n\t" \
    "ds_write_b128 %[pw], v[112:115] offset:0\n\t" \
    "ds_write_b128 %[pw], v[116:119] offset:8192\n\t" \
    "v_cmp_ge_u32 vcc, %[tn], v248\n\t" \
    "s_mov_b32 %[a], 0\n\t" \
    "s_cbranch_vccnz .Lxv%=\n\t" \
    "buffer_load_dwordx4 v[128:131], %[l16], %[rp], %[sv0] offen offset:0 sc1\n\t" \
    "buffer_load_dwordx4 v[132:135], %[l16], %[rp], %[sv0] offen offset:1024 sc1\n\t" \
    "buffer_load_dwordx4 v[136:139], %[l16], %[rp], %[sv0] offen offset:2048 sc1\n\t" \
    "buffer_load_dwordx4 v[140:143], %[l16], %[rp], %[sv0] offen offset:3072 sc1\n\t" \
    "buffer_load_dwordx4 v[144:147], %[l16], %[rp], %[sv1] offen offset:0 sc1\n\t" \
    "buffer_load_dwordx4 v[148:151], %[l16], %[rp], %[sv1] offen offset:1024 sc1\n\t" \
    "buffer_load_dwordx4 v[152:155], %[l16], %[rp], %[sv1] offen offset:2048 sc1\n\t" \
    "buffer_load_dwordx4 v[156:159], %[l16], %[rp], %[sv1] offen offset:3072 sc1\n\t" \
    "s_mov_b32 %[a], 1\n\t" \
    ".Lxv%=:\n\t" \
    "s_cmp_eq_u32 %[fin], 0\n\t" \
    "s_waitcnt lgkmcnt(0)\n\t" \
    "s_barrier\n\t" ASM_ST \
    "s_cbranch_scc1 .Lxe%=\n\t" \
    "ds_read_b128 v[200:203], %[pr] offset:0\n\t" \
    "ds_read_b128 v[204:207], %[pr] offset:1024\n\t" \
    "ds_read_b128 v[208:211], %[pr] offset:2048\n\t" \
    "ds_read_b128 v[212:215], %[pr] offset:3072\n\t" \
    "ds_read_b128 v[216:219], %[pr] offset:4096\n\t" \
    "ds_read_b128 v[220:223], %[pr] offset:5120\n\t" \
    "ds_read_b128 v[224:227], %[pr] offset:6144\n\t" \
    "ds_read_b128 v[228:231], %[pr] offset:7168\n\t" \
    "v_lshlrev_b32 v240, 16, v250\n\t" \
    "v_and_b32 v241, 0xffff0000, v250\n\t" \
    "v_lshlrev_b32 v242, 16, v251\n\t" \
    "v_and_b32 v243, 0xffff0000, v251\n\t" \
    "s_waitcnt lgkmcnt(6)\n\t" \
    "v_pk_add_f32 v[200:201], v[200:201], v[204:205]\n\t" \
    "v_pk_add_f32 v[202:203], v[202:203], v[206:207]\n\t" \
    "s_waitcnt lgkmcnt(4)\n\t" \
    "v_pk_add_f32 v[208:209], v[208:209], v[212:213]\n\t" \
    "v_pk_add_f32 v[210:211], v[210:211], v[214:215]\n\t" \
    "v_pk_add_f32 v[200:201], v[200:201], v[208:209]\n\t" \
    "v_pk_add_f32 v[202:203], v[202:203], v[210:211]\n\t" \
    "s_waitcnt lgkmcnt(2)\n\t" \
    "v_pk_add_f32 v[216:217], v[216:217], v[220:221]\n\t" \
    "v_pk_add_f32 v[218:219], v[218:219], v[222:223]\n\t" \
    "s_waitcnt lgkmcnt(0)\n\t" \
    "v_pk_add_f32 v[224:225], v[224:225], v[228:229]\n\t" \
    "v_pk_add_f32 v[226:227], v[226:227], v[230:231]\n\t" \
    "v_pk_add_f32 v[216:217], v[216:217], v[224:225]\n\t" \
    "v_pk_add_f32 v[218:219], v[218:219], v[226:227]\n\t" \
    "v_pk_add_f32 v[200:201], v[200:201], v[216:217]\n\t" \
    "v_pk_add_f32 v[202:203], v[202:203], v[218:219]\n\t" \
    "v_pk_add_f32 v[200:201], v[200:201], v[240:241]\n\t" \
    "v_pk_add_f32 v[202:203], v[202:203], v[242:243]\n\t" \
    "v_cvt_pk_bf16_f32 v200, v200, v201\n\t" \
    "v_cvt_pk_bf16_f32 v201, v202, v203\n\t" \
    "v_min_u32 v200, -2, v200\n\t" \
    "v_min_u32 v201, -2, v201\n\t" \
    "buffer_store_dwordx2 v[200:201], %[xo], %[rd], %[sa] offen sc1\n\t" \
    ".Lxe%=:\n\t"
#define ASM_W4_M \
    ".Lc7_%=:\n\t" \
    "s_waitcnt vmcnt(0)\n\t" \
    "v_max_u32 v212, v188, v189\n\t" \
    "v_max3_u32 v212, v190, v191, v212\n\t" \
    "v_cmp_eq_u32 vcc, -1, v212\n\t" \
    "s_cbranch_vccnz .Ls7_%=\n\t" \
    ".Lk7_%=:\n\t" \
    ASM_ST2 \
    "buffer_load_dwordx4 v[184:187], %[l16], %[rs], %[s1] offen offset:2048 sc1\n\t" \
    "buffer_load_dwordx4 v[180:183], %[l16], %[rs], %[s1] offen offset:1024 sc1\n\t" \
    "buffer_load_dwordx4 v[176:179], %[l16], %[rs], %[s1] offen offset:0 sc1\n\t" \
    "buffer_load_dwordx4 v[172:175], %[l16], %[rs], %[s0] offen offset:3072 sc1\n\t" \
    "buffer_load_dwordx4 v[168:171], %[l16], %[rs], %[s0] offen offset:2048 sc1\n\t" \
    "buffer_load_dwordx4 v[164:167], %[l16], %[rs], %[s0] offen offset:1024 sc1\n\t" \
    "buffer_load_dwordx4 v[160:163], %[l16], %[rs], %[s0] offen offset:0 sc1\n\t" \
    "v_mfma_f32_16x16x32_bf16 v[192:195], v[28:31], v[188:191], 0\n\t" \
    "v_mfma_f32_16x16x32_bf16 v[196:199], v[60:63], v[188:191], 0\n\t" \
    "v_mfma_f32_16x16x32_bf16 v[200:203], v[92:95], v[188:191], 0\n\t" \
    "v_mfma_f32_16x16x32_bf16 v[204:207], v[124:127], v[188:191], 0\n\t" \
    "v_mfma_f32_16x16x32_bf16 v[208:211], v[156:159], v[188:191], 0\n\t" \
    ".Lc6_%=:\n\t" \
    "s_waitcnt vmcnt(6)\n\t" \
    "v_max_u32 v212, v184, v185\n\t" \
    "v_max3_u32 v212, v186, v187, v212\n\t" \
    "v_cmp_eq_u32 vcc, -1, v212\n\t" \
    "s_cbranch_vccnz .Ls6_%=\n\t" \
    ".Lk6_%=:\n\t" \
    "v_mfma_f32_16x16x32_bf16 v[192:195], v[24:27], v[184:187], v[192:195]\n\t" \
    "v_mfma_f32_16x16x32_bf16 v[196:199], v[56:59], v[184:187], v[196:199]\n\t" \
    "v_mfma_f32_16x16x32_bf16 v[200:203], v[88:91], v[184:187], v[200:203]\n\t" \
    "v_mfma_f32_16x16x32_bf16 v[204:207], v[120:123], v[184:187], v[204:207]\n\t" \
    "v_mfma_f32_16x16x32_bf16 v[208:211], v[152:155], v[184:187], v[208:211]\n\t" \
    ".Lc5_%=:\n\t" \
    "s_waitcnt vmcnt(5)\n\t" \
    "v_max_u32 v212, v180, v181\n\t" \
    "v_max3_u32 v212, v182, v183, v212\n\t" \
    "v_cmp_eq_u32 vcc, -1, v212\n\t" \
    "s_cbranch_vccnz .Ls5_%=\n\t" \
    ".Lk5_%=:\n\t" \
    "v_mfma_f32_16x16x32_bf16 v[192:195], v[20:23], v[180:183], v[192:195]\n\t" \
    "v_mfma_f32_16x16x32_bf16 v[196:199], v[52:55], v[180:183], v[196:199]\n\t" \
    "v_mfma_f32_16x16x32_bf16 v[200:203], v[84:87], v[180:183], v[200:203]\n\t" \
    "v_mfma_f32_16x16x32_bf16 v[204:207], v[116:119], v[180:183], v[204:207]\n\t" \
    "v_mfma_f32_16x16x32_bf16 v[208:211], v[148:151], v[180:183], v[208:211]\n\t" \
    ".Lc4_%=:\n\t" \
    "s_waitcnt vmcnt(4)\n\t" \
    "v_max_u32 v212, v176, v177\n\t" \
    "v_max3_u32 v212, v178, v179, v212\n\t" \
    "v_cmp_eq_u32 vcc, -1, v212\n\t" \
    "s_cbranch_vccnz .Ls4_%=\n\t" \
    ".Lk4_%=:\n\t" \
    "v_mfma_f32_16x16x32_bf16 v[192:195], v[16:19], v[176:179], v[192:195]\n\t" \
    "v_mfma_f32_16x16x32_bf16 v[196:199], v[48:51], v[176:179], v[196:199]\n\t" \
    "v_mfma_f32_16x16x32_bf16 v[200:203], v[80:83], v[176:179], v[200:203]\n\t" \
    "v_mfma_f32_16x16x32_bf16 v[204:207], v[112:115], v[176:179], v[204:207]\n\t" \
    "v_mfma_f32_16x16x32_bf16 v[208:211], v[144:147], v[176:179], v[208:211]\n\t" \
    ".Lc3_%=:\n\t" \
    "s_waitcnt vmcnt(3)\n\t" \
    "v_max_u32 v212, v172, v173\n\t" \
    "v_max3_u32 v212, v174, v175, v212\n\t" \
    "v_cmp_eq_u32 vcc, -1, v212\n\t" \
    "s_cbranch_vccnz .Ls3_%=\n\t" \
    ".Lk3_%=:\n\t" \
    "v_mfma_f32_16x16x32_bf16 v[192:195], v[12:15], v[172:175], v[192:195]\n\t" \
    "v_mfma_f32_16x16x32_bf16 v[196:199], v[44:47], v[172:175], v[196:199]\n\t" \
    "v_mfma_f32_16x16x32_bf16 v[200:203], v[76:79], v[172:175], v[200:203]\n\t" \
    "v_mfma_f32_16x16x32_bf16 v[204:207], v[108:111], v[172:175], v[204:207]\n\t" \
    "v_mfma_f32_16x16x32_bf16 v[208:211], v[140:143], v[172:175], v[208:211]\n\t" \
    ".Lc2_%=:\n\t" \
    "s_waitcnt vmcnt(2)\n\t" \
    "v_max_u32 v212, v168, v169\n\t" \
    "v_max3_u32 v212, v170, v171, v212\n\t" \
    "v_cmp_eq_u32 vcc, -1, v212\n\t" \
    "s_cbranch_vccnz .Ls2_%=\n\t" \
    ".Lk2_%=:\n\t" \
    "buffer_load_dwordx4 v[188:191], %[l16], %[rs], %[n1] offen offset:3072 sc1\n\t" \
    "v_mfma_f32_16x16x32_bf16 v[192:195], v[8:11], v[168:171], v[192:195]\n\t" \
    "v_mfma_f32_16x16x32_bf16 v[196:199], v[40:43], v[168:171], v[196:199]\n\t" \
    "v_mfma_f32_16x16x32_bf16 v[200:203], v[72:75], v[168:171], v[200:203]\n\t" \
    "v_mfma_f32_16x16x32_bf16 v[204:207], v[104:107], v[168:171], v[204:207]\n\t" \
    "v_mfma_f32_16x16x32_bf16 v[208:211], v[136:139], v[168:171], v[208:211]\n\t" \
    ".Lc1_%=:\n\t" \
    "s_waitcnt vmcnt(2)\n\t" \
    "v_max_u32 v212, v164, v165\n\t" \
    "v_max3_u32 v212, v166, v167, v212\n\t" \
    "v_cmp_eq_u32 vcc, -1, v212\n\t" \
    "s_cbranch_vccnz .Ls1_%=\n\t" \
    ".Lk1_%=:\n\t" \
    "v_mfma_f32_16x16x32_bf16 v[192:195], v[4:7], v[164:167], v[192:195]\n\t" \
    "v_mfma_f32_16x16x32_bf16 v[196:199], v[36:39], v[164:167], v[196:199]\n\t" \
    "v_mfma_f32_16x16x32_bf16 v[200:203], v[68:71], v[164:167], v[200:203]\n\t" \
    "v_mfma_f32_16x16x32_bf16 v[204:207], v[100:103], v[164:167], v[204:207]\n\t" \
    "v_mfma_f32_16x16x32_bf16 v[208:211], v[132:135], v[164:167], v[208:211]\n\t" \
    ".Lc0_%=:\n\t" \
    "s_waitcnt vmcnt(1)\n\t" \
    "v_max_u32 v212, v160, v161\n\t" \
    "v_max3_u32 v212, v162, v163, v212\n\t" \
    "v_cmp_eq_u32 vcc, -1, v212\n\t" \
    "s_cbranch_vccnz .Ls0_%=\n\t" \
    ".Lk0_%=:\n\t" \
    "v_mfma_f32_16x16x32_bf16 v[192:195], v[0:3], v[160:163], v[192:195]\n\t" \
    "v_mfma_f32_16x16x32_bf16 v[196:199], v[32:35], v[160:163], v[196:199]\n\t" \
    "v_mfma_f32_16x16x32_bf16 v[200:203], v[64:67], v[160:163], v[200:203]\n\t" \
    "v_mfma_f32_16x16x32_bf16 v[204:207], v[96:99], v[160:163], v[204:207]\n\t" \
    "v_mfma_f32_16x16x32_bf16 v[208:211], v[128:131], v[160:163], v[208:211]\n\t" \
    "v_mov_b32 v212, %[un]\n\t" \
    "ds_write_b128 %[ld], v[192:195] offset:0\n\t" \
    "ds_write_b128 %[ld], v[196:199] offset:6144\n\t" \
    "ds_write_b128 %[ld], v[200:203] offset:12288\n\t" \
    "s_nop 0\n\t" \
    "ds_write_b128 %[ld], v[204:207] offset:18432\n\t" \
    "s_nop 1\n\t" \
    "ds_write_b128 %[ld], v[208:211] offset:24576\n\t" \
    "ds_write_b32 %[fl], v212\n\t" \
    "s_branch .Lme%=\n\t" \
    ".Ls7_%=:\n\t" \
    "s_cmp_eq_u32 %[n], 0\n\t" \
    "s_cbranch_scc1 .Lk7_%=\n\t" \
    "s_sub_u32 %[n], %[n], 1\n\t" \
    "s_sleep 1\n\t" \
    "buffer_load_dwordx4 v[188:191], %[l16], %[rs], %[s1] offen offset:3072 sc1\n\t" \
    "s_branch .Lc7_%=\n\t" \
    ".Ls6_%=:\n\t" \
    "s_cmp_eq_u32 %[n], 0\n\t" \
    "s_cbranch_scc1 .Lk6_%=\n\t" \
    "s_sub_u32 %[n], %[n], 1\n\t" \
    "buffer_load_dwordx4 v[184:187], %[l16], %[rs], %[s1] offen offset:2048 sc1\n\t" \
    "s_waitcnt vmcnt(0)\n\t" \
    "s_branch .Lc6_%=\n\t" \
    ".Ls5_%=:\n\t" \
    "s_cmp_eq_u32 %[n], 0\n\t" \
    "s_cbranch_scc1 .Lk5_%=\n\t" \
    "s_sub_u32 %[n], %[n], 1\n\t" \
    "buffer_load_dwordx4 v[180:183], %[l16], %[rs], %[s1] offen offset:1024 sc1\n\t" \
    "s_waitcnt vmcnt(0)\n\t" \
    "s_branch .Lc5_%=\n\t" \
    ".Ls4_%=:\n\t" \
    "s_cmp_eq_u32 %[n], 0\n\t" \
    "s_cbranch_scc1 .Lk4_%=\n\t" \
    "s_sub_u32 %[n], %[n], 1\n\t" \
    "buffer_load_dwordx4 v[176:179], %[l16], %[rs], %[s1] offen offset:0 sc1\n\t" \
    "s_waitcnt vmcnt(0)\n\t" \
    "s_branch .Lc4_%=\n\t" \
    ".Ls3_%=:\n\t" \
    "s_cmp_eq_u32 %[n], 0\n\t" \
    "s_cbranch_scc1 .Lk3_%=\n\t" \
    "s_sub_u32 %[n], %[n], 1\n\t" \
    "buffer_load_dwordx4 v[172:175], %[l16], %[rs], %[s0] offen offset:3072 sc1\n\t" \
    "s_waitcnt vmcnt(0)\n\t" \
    "s_branch .Lc3_%=\n\t" \
    ".Ls2_%=:\n\t" \
    "s_cmp_eq_u32 %[n], 0\n\t" \
    "s_cbranch_scc1 .Lk2_%=\n\t" \
    "s_sub_u32 %[n], %[n], 1\n\t" \
    "buffer_load_dwordx4 v[168:171], %[l16], %[rs], %[s0] offen offset:2048 sc1\n\t" \
    "s_waitcnt vmcnt(0)\n\t" \
    "s_branch .Lc2_%=\n\t" \
    ".Ls1_%=:\n\t" \
    "s_cmp_eq_u32 %[n], 0\n\t" \
    "s_cbranch_scc1 .Lk1_%=\n\t" \
    "s_sub_u32 %[n], %[n], 1\n\t" \
    "buffer_load_dwordx4 v[164:167], %[l16], %[rs], %[s0] offen offset:1024 sc1\n\t" \
    "s_waitcnt vmcnt(0)\n\t" \
    "s_branch .Lc1_%=\n\t" \
    ".Ls0_%=:\n\t" \
    "s_cmp_eq_u32 %[n], 0\n\t" \
    "s_cbranch_scc1 .Lk0_%=\n\t" \
    "s_sub_u32 %[n], %[n], 1\n\t" \
    "buffer_load_dwordx4 v[160:163], %[l16], %[rs], %[s0] offen offset:0 sc1\n\t" \
    "s_waitcnt vmcnt(0)\n\t" \
    "s_branch .Lc0_%=\n\t" \
    ".Lme%=:\n\t"
#define ASM_W4F_0 \
    "s_branch .Lf0%=\n\t" \
    ".Lfr%=:\n\t" \
    "s_cmp_eq_u32 %[n], 0\n\t" \
    "s_cbranch_scc1 .Lfb%=\n\t" \
    "s_sub_u32 %[n], %[n], 1\n\t" \
    "s_sleep 6\n\t" \
    "buffer_load_dwordx2 v[164:165], %[o0], %[rd], %[c1] offen sc1\n\t" \
    "buffer_load_dwordx2 v[166:167], %[o1], %[rd], %[c1] offen sc1\n\t" \
    "buffer_load_dwordx2 v[168:169], %[o2], %[rd], %[c1] offen sc1\n\t" \
    "buffer_load_dwordx2 v[170:171], %[o3], %[rd], %[c1] offen sc1\n\t" \
    "buffer_load_dwordx2 v[172:173], %[o4], %[rd], %[c1] offen sc1\n\t" \
    "buffer_load_dwordx2 v[174:175], %[o0], %[rd], %[ca] offen sc1\n\t" \
    "buffer_load_dwordx2 v[176:177], %[o1], %[rd], %[ca] offen sc1\n\t" \
    "buffer_load_dwordx2 v[178:179], %[o2], %[rd], %[ca] offen sc1\n\t" \
    "buffer_load_dwordx2 v[180:181], %[o3], %[rd], %[ca] offen sc1\n\t" \
    "buffer_load_dwordx2 v[182:183], %[o4], %[rd], %[ca] offen sc1\n\t" \
    "s_waitcnt vmcnt(0)\n\t" \
    "s_branch .Lfa%=\n\t" \
    ".Lfs0_%=:\n\t" \
    "s_cmp_eq_u32 %[n], 0\n\t" \
    "s_cbranch_scc1 .Lfu0_%=\n\t" \
    "s_sub_u32 %[n], %[n], 1\n\t" \
    "s_sleep 1\n\t" \
    "s_branch .Lft0_%=\n\t" \
    ".Lfs1_%=:\n\t" \
    "s_cmp_eq_u32 %[n], 0\n\t" \
    "s_cbranch_scc1 .Lfu1_%=\n\t" \
    "s_sub_u32 %[n], %[n], 1\n\t" \
    "s_sleep 1\n\t" \
    "s_branch .Lft1_%=\n\t" \
    ".Lfx%=:\n\t" \
    "s_cmp_eq_u32 %[n], 0\n\t" \
    "s_cbranch_scc1 .Lfz%=\n\t" \
    "s_sub_u32 %[n], %[n], 1\n\t" \
    "s_branch .Lfy%=\n\t" \
    ".Lf0%=:\n\t" \
    "s_waitcnt vmcnt(1)\n\t" \
    ".Lfa%=:\n\t" \
    "v_max_u32 v194, v164, v165\n\t" \
    "v_max3_u32 v194, v166, v167, v194\n\t" \
    "v_max3_u32 v194, v168, v169, v194\n\t" \
    "v_max3_u32 v194, v170, v171, v194\n\t" \
    "v_max3_u32 v194, v172, v173, v194\n\t" \
    "v_max3_u32 v194, v174, v175, v194\n\t" \
    "v_max3_u32 v194, v176, v177, v194\n\t" \
    "v_max3_u32 v194, v178, v179, v194\n\t" \
    "v_max3_u32 v194, v180, v181, v194\n\t" \
    "v_max3_u32 v194, v182, v183, v194\n\t" \
    "v_cmp_eq_u32 vcc, -1, v194\n\t" \
    "s_cbranch_vccnz .Lfr%=\n\t" \
    ".Lfb%=:\n\t" \
    "v_lshlrev_b32 v64, 16, v164\n\t" \
    "v_and_b32 v65, 0xffff0000, v164\n\t" \
    "v_lshlrev_b32 v84, 16, v174\n\t" \
    "v_and_b32 v85, 0xffff0000, v174\n\t" \
    "v_lshlrev_b32 v66, 16, v165\n\t" \
    "v_and_b32 v67, 0xffff0000, v165\n\t" \
    "v_lshlrev_b32 v86, 16, v175\n\t" \
    "v_and_b32 v87, 0xffff0000, v175\n\t" \
    "v_lshlrev_b32 v68, 16, v166\n\t" \
    "v_and_b32 v69, 0xffff0000, v166\n\t" \
    "v_lshlrev_b32 v88, 16, v176\n\t" \
    "v_and_b32 v89, 0xffff0000, v176\n\t" \
    "v_lshlrev_b32 v70, 16, v167\n\t" \
    "v_and_b32 v71, 0xffff0000, v167\n\t" \
    "v_lshlrev_b32 v90, 16, v177\n\t" \
    "v_and_b32 v91, 0xffff0000, v177\n\t" \
    "v_lshlrev_b32 v72, 16, v168\n\t" \
    "v_and_b32 v73, 0xffff0000, v168\n\t" \
    "v_lshlrev_b32 v92, 16, v178\n\t" \
    "v_and_b32 v93, 0xffff0000, v178\n\t" \
    "v_lshlrev_b32 v74, 16, v169\n\t" \
    "v_and_b32 v75, 0xffff0000, v169\n\t" \
    "v_lshlrev_b32 v94, 16, v179\n\t" \
    "v_and_b32 v95, 0xffff0000, v179\n\t" \
    "v_lshlrev_b32 v76, 16, v170\n\t" \
    "v_and_b32 v77, 0xffff0000, v170\n\t" \
    "v_lshlrev_b32 v96, 16, v180\n\t" \
    "v_and_b32 v97, 0xffff0000, v180\n\t" \
    "v_lshlrev_b32 v78, 16, v171\n\t" \
    "v_and_b32 v79, 0xffff0000, v171\n\t" \
    "v_lshlrev_b32 v98, 16, v181\n\t" \
    "v_and_b32 v99, 0xffff0000, v181\n\t" \
    "v_lshlrev_b32 v80, 16, v172\n\t" \
    "v_and_b32 v81, 0xffff0000, v172\n\t" \
    "v_lshlrev_b32 v100, 16, v182\n\t" \
    "v_and_b32 v101, 0xffff0000, v182\n\t" \
    "v_lshlrev_b32 v82, 16, v173\n\t" \
    "v_and_b32 v83, 0xffff0000, v173\n\t" \
    "v_lshlrev_b32 v102, 16, v183\n\t" \
    "v_and_b32 v103, 0xffff0000, v183\n\t" \
    "v_pk_add_f32 v[64:65], v[64:65], v[84:85]\n\t" \
    "v_pk_add_f32 v[66:67], v[66:67], v[86:87]\n\t" \
    "v_pk_add_f32 v[68:69], v[68:69], v[88:89]\n\t" \
    "v_pk_add_f32 v[70:71], v[70:71], v[90:91]\n\t" \
    "v_pk_add_f32 v[72:73], v[72:73], v[92:93]\n\t" \
    "v_pk_add_f32 v[74:75], v[74:75], v[94:95]\n\t" \
    "v_pk_add_f32 v[76:77], v[76:77], v[96:97]\n\t" \
    "v_pk_add_f32 v[78:79], v[78:79], v[98:99]\n\t" \
    "v_pk_add_f32 v[80:81], v[80:81], v[100:101]\n\t" \
    "v_pk_add_f32 v[82:83], v[82:83], v[102:103]\n\t" \
    ".Lft0_%=:\n\t" \
    "ds_read_b32 v194, %[f0]\n\t" \
    "s_waitcnt lgkmcnt(0)\n\t" \
    "v_cmp_ge_u32 vcc, %[u], v194\n\t" \
    "s_cbranch_vccnz .Lfs0_%=\n\t" \
    ".Lfu0_%=:\n\t" \
    "ds_read_b128 v[84:87], %[la] offset:0\n\t" \
    "ds_read_b128 v[88:91], %[la] offset:6144\n\t" \
    "ds_read_b128 v[92:95], %[la] offset:12288\n\t" \
    "ds_read_b128 v[96:99], %[la] offset:18432\n\t" \
    "ds_read_b128 v[100:103], %[la] offset:24576\n\t" \
    "ds_read_b128 v[104:107], %[la] offset:1024\n\t" \
    "ds_read_b128 v[108:111], %[la] offset:7168\n\t" \
    "ds_read_b128 v[112:115], %[la] offset:13312\n\t" \
    "ds_read_b128 v[116:119], %[la] offset:19456\n\t" \
    "ds_read_b128 v[120:123], %[la] offset:25600\n\t" \
    "ds_read_b128 v[124:127], %[la] offset:2048\n\t" \
    "s_waitcnt lgkmcnt(10)\n\t" \
    "v_pk_add_f32 v[64:65], v[64:65], v[84:85]\n\t" \
    "v_pk_add_f32 v[66:67], v[66:67], v[86:87]\n\t" \
    "ds_read_b128 v[128:131], %[la] offset:8192\n\t" \
    "s_waitcnt lgkmcnt(10)\n\t" \
    "v_pk_add_f32 v[68:69], v[68:69], v[88:89]\n\t" \
    "v_pk_add_f32 v[70:71], v[70:71], v[90:91]\n\t" \
    "ds_read_b128 v[132:135], %[la] offset:14336\n\t" \
    "s_waitcnt lgkmcnt(10)\n\t" \
    "v_pk_add_f32 v[72:73], v[72:73], v[92:93]\n\t" \
    "v_pk_add_f32 v[74:75], v[74:75], v[94:95]\n\t" \
    "ds_read_b128 v[136:139], %[la] offset:20480\n\t" \
    "s_waitcnt lgkmcnt(10)\n\t" \
    "v_pk_add_f32 v[76:77], v[76:77], v[96:97]\n\t" \
    "v_pk_add_f32 v[78:79], v[78:79], v[98:99]\n\t" \
    "ds_read_b128 v[140:143], %[la] offset:26624\n\t" \
    "s_waitcnt lgkmcnt(10)\n\t" \
    "v_pk_add_f32 v[80:81], v[80:81], v[100:101]\n\t" \
    "v_pk_add_f32 v[82:83], v[82:83], v[102:103]\n\t" \
    "ds_read_b128 v[144:147], %[la] offset:3072\n\t" \
    "s_waitcnt lgkmcnt(10)\n\t" \
    "v_pk_add_f32 v[64:65], v[64:65], v[104:105]\n\t" \
    "v_pk_add_f32 v[66:67], v[66:67], v[106:107]\n\t" \
    "ds_read_b128 v[148:151], %[la] offset:9216\n\t" \
    "s_waitcnt lgkmcnt(10)\n\t" \
    "v_pk_add_f32 v[68:69], v[68:69], v[108:109]\n\t" \
    "v_pk_add_f32 v[70:71], v[70:71], v[110:111]\n\t" \
    "ds_read_b128 v[152:155], %[la] offset:15360\n\t" \
    "s_waitcnt lgkmcnt(10)\n\t" \
    "v_pk_add_f32 v[72:73], v[72:73], v[112:113]\n\t" \
    "v_pk_add_f32 v[74:75], v[74:75], v[114:115]\n\t" \
    "ds_read_b128 v[156:159], %[la] offset:21504\n\t" \
    "s_waitcnt lgkmcnt(10)\n\t" \
    "v_pk_add_f32 v[76:77], v[76:77], v[116:117]\n\t" \
    "v_pk_add_f32 v[78:79], v[78:79], v[118:119]\n\t" \
    "ds_read_b128 v[160:163], %[la] offset:27648\n\t" \
    "s_waitcnt lgkmcnt(10)\n\t" \
    "v_pk_add_f32 v[80:81], v[80:81], v[120:121]\n\t" \
    "v_pk_add_f32 v[82:83], v[82:83], v[122:123]\n\t" \
    "s_waitcnt lgkmcnt(9)\n\t" \
    "v_pk_add_f32 v[64:65], v[64:65], v[124:125]\n\t" \
    "v_pk_add_f32 v[66:67], v[66:67], v[126:127]\n\t" \
    "s_waitcnt lgkmcnt(8)\n\t" \
    "v_pk_add_f32 v[68:69], v[68:69], v[128:129]\n\t" \
    "v_pk_add_f32 v[70:71], v[70:71], v[130:131]\n\t" \
    "s_waitcnt lgkmcnt(7)\n\t" \
    "v_pk_add_f32 v[72:73], v[72:73], v[132:133]\n\t" \
    "v_pk_add_f32 v[74:75], v[74:75], v[134:135]\n\t" \
    "s_waitcnt lgkmcnt(6)\n\t" \
    "v_pk_add_f32 v[76:77], v[76:77], v[136:137]\n\t" \
    "v_pk_add_f32 v[78:79], v[78:79], v[138:139]\n\t" \
    "s_waitcnt lgkmcnt(5)\n\t" \
    "v_pk_add_f32 v[80:81], v[80:81], v[140:141]\n\t" \
    "v_pk_add_f32 v[82:83], v[82:83], v[142:143]\n\t" \
    "s_waitcnt lgkmcnt(4)\n\t" \
    "v_pk_add_f32 v[64:65], v[64:65], v[144:145]\n\t" \
    "v_pk_add_f32 v[66:67], v[66:67], v[146:147]\n\t" \
    "s_waitcnt lgkmcnt(3)\n\t" \
    "v_pk_add_f32 v[68:69], v[68:69], v[148:149]\n\t" \
    "v_pk_add_f32 v[70:71], v[70:71], v[150:151]\n\t" \
    "s_waitcnt lgkmcnt(2)\n\t" \
    "v_pk_add_f32 v[72:73], v[72:73], v[152:153]\n\t" \
    "v_pk_add_f32 v[74:75], v[74:75], v[154:155]\n\t" \
    "s_waitcnt lgkmcnt(1)\n\t" \
    "v_pk_add_f32 v[76:77], v[76:77], v[156:157]\n\t" \
    "v_pk_add_f32 v[78:79], v[78:79], v[158:159]\n\t" \
    "s_waitcnt lgkmcnt(0)\n\t" \
    "v_pk_add_f32 v[80:81], v[80:81], v[160:161]\n\t" \
    "v_pk_add_f32 v[82:83], v[82:83], v[162:163]\n\t" \
    ".Lft1_%=:\n\t" \
    "ds_read_b32 v194, %[f1]\n\t" \
    "s_waitcnt lgkmcnt(0)\n\t" \
    "v_cmp_ge_u32 vcc, %[u], v194\n\t" \
    "s_cbranch_vccnz .Lfs1_%=\n\t" \
    ".Lfu1_%=:\n\t" \
    ASM_ST2 \
    "ds_read_b128 v[84:87], %[la] offset:4096\n\t" \
    "ds_read_b128 v[88:91], %[la] offset:10240\n\t" \
    "ds_read_b128 v[92:95], %[la] offset:16384\n\t" \
    "ds_read_b128 v[96:99], %[la] offset:22528\n\t" \
    "ds_read_b128 v[100:103], %[la] offset:28672\n\t" \
    "ds_read_b128 v[104:107], %[la] offset:5120\n\t" \
    "ds_read_b128 v[108:111], %[la] offset:11264\n\t" \
    "ds_read_b128 v[112:115], %[la] offset:17408\n\t" \
    "ds_read_b128 v[116:119], %[la] offset:23552\n\t" \
    "ds_read_b128 v[120:123], %[la] offset:29696\n\t" \
    "v_mov_b32 v194, %[un]\n\t" \
    "ds_write_b32 %[fd], v194\n\t" \
    "s_waitcnt lgkmcnt(10)\n\t" \
    "v_pk_add_f32 v[64:65], v[64:65], v[84:85]\n\t" \
    "v_pk_add_f32 v[66:67], v[66:67], v[86:87]\n\t" \
    "s_waitcnt lgkmcnt(9)\n\t" \
    "v_pk_add_f32 v[68:69], v[68:69], v[88:89]\n\t" \
    "v_pk_add_f32 v[70:71], v[70:71], v[90:91]\n\t" \
    "s_waitcnt lgkmcnt(8)\n\t" \
    "v_pk_add_f32 v[72:73], v[72:73], v[92:93]\n\t" \
    "v_pk_add_f32 v[74:75], v[74:75], v[94:95]\n\t" \
    "s_waitcnt lgkmcnt(7)\n\t" \
    "v_pk_add_f32 v[76:77], v[76:77], v[96:97]\n\t" \
    "v_pk_add_f32 v[78:79], v[78:79], v[98:99]\n\t" \
    "s_waitcnt lgkmcnt(6)\n\t" \
    "v_pk_add_f32 v[80:81], v[80:81], v[100:101]\n\t" \
    "v_pk_add_f32 v[82:83], v[82:83], v[102:103]\n\t" \
    "s_waitcnt lgkmcnt(5)\n\t" \
    "v_pk_add_f32 v[64:65], v[64:65], v[104:105]\n\t" \
    "v_pk_add_f32 v[66:67], v[66:67], v[106:107]\n\t" \
    "s_waitcnt lgkmcnt(4)\n\t" \
    "v_pk_add_f32 v[68:69], v[68:69], v[108:109]\n\t" \
    "v_pk_add_f32 v[70:71], v[70:71], v[110:111]\n\t" \
    "s_waitcnt lgkmcnt(3)\n\t" \
    "v_pk_add_f32 v[72:73], v[72:73], v[112:113]\n\t" \
    "v_pk_add_f32 v[74:75], v[74:75], v[114:115]\n\t" \
    "s_waitcnt lgkmcnt(2)\n\t" \
    "v_pk_add_f32 v[76:77], v[76:77], v[116:117]\n\t" \
    "v_pk_add_f32 v[78:79], v[78:79], v[118:119]\n\t" \
    "s_waitcnt lgkmcnt(1)\n\t" \
    "v_pk_add_f32 v[80:81], v[80:81], v[120:121]\n\t" \
    "v_pk_add_f32 v[82:83], v[82:83], v[122:123]\n\t" \
    "v_pk_add_f32 v[84:85], v[64:65], v[66:67]\n\t" \
    "v_pk_mul_f32 v[88:89], v[64:65], v[64:65]\n\t" \
    "v_pk_add_f32 v[86:87], v[68:69], v[70:71]\n\t" \
    "v_pk_mul_f32 v[90:91], v[66:67], v[66:67]\n\t" \
    "v_pk_fma_f32 v[88:89], v[68:69], v[68:69], v[88:89]\n\t" \
    "v_pk_fma_f32 v[90:91], v[70:71], v[70:71], v[90:91]\n\t" \
    "v_pk_add_f32 v[84:85], v[84:85], v[72:73]\n\t" \
    "v_pk_fma_f32 v[88:89], v[72:73], v[72:73], v[88:89]\n\t" \
    "v_pk_add_f32 v[86:87], v[86:87], v[74:75]\n\t" \
    "v_pk_fma_f32 v[90:91], v[74:75], v[74:75], v[90:91]\n\t" \
    "v_pk_add_f32 v[84:85], v[84:85], v[76:77]\n\t" \
    "v_pk_fma_f32 v[88:89], v[76:77], v[76:77], v[88:89]\n\t" \
    "v_pk_add_f32 v[86:87], v[86:87], v[78:79]\n\t" \
    "v_pk_fma_f32 v[90:91], v[78:79], v[78:79], v[90:91]\n\t" \
    "v_pk_add_f32 v[84:85], v[84:85], v[80:81]\n\t" \
    "v_pk_fma_f32 v[88:89], v[80:81], v[80:81], v[88:89]\n\t" \
    "v_pk_add_f32 v[86:87], v[86:87], v[82:83]\n\t" \
    "v_pk_fma_f32 v[90:91], v[82:83], v[82:83], v[90:91]\n\t" \
    "v_pk_add_f32 v[84:85], v[84:85], v[86:87]\n\t" \
    "v_pk_add_f32 v[88:89], v[88:89], v[90:91]\n\t" \
    "v_add_f32 v194, v84, v85\n\t" \
    "v_add_f32 v195, v88, v89\n\t" \
    "s_nop 1\n\t" \
    "v_permlane16_swap_b32 v194, v195\n\t" \
    "v_add_f32 v196, v194, v195\n\t" \
    "v_add_f32 v197, v194, v195\n\t" \
    "s_nop 1\n\t" \
    "v_permlane32_swap_b32 v196, v197\n\t" \
    "v_add_f32 v194, v196, v197\n\t" \
    "v_add_f32 v195, v196, v197\n\t" \
    "s_nop 1\n\t" \
    "v_permlane16_swap_b32 v194, v195\n\t" \
    "v_min_u32 v194, -2, v194\n\t" \
    "v_min_u32 v195, -2, v195\n\t" \
    "buffer_store_dwordx2 v[194:195], %[so], %[rr], %[sp] offen sc1\n\t" \
    ASM_ST3 \
    "v_cvt_pk_bf16_f32 v184, v64, v65\n\t" \
    "v_cvt_pk_bf16_f32 v185, v66, v67\n\t" \
    "v_cvt_pk_bf16_f32 v186, v68, v69\n\t" \
    "v_cvt_pk_bf16_f32 v187, v70, v71\n\t" \
    "v_cvt_pk_bf16_f32 v188, v72, v73\n\t" \
    "v_cvt_pk_bf16_f32 v189, v74, v75\n\t" \
    "v_cvt_pk_bf16_f32 v190, v76, v77\n\t" \
    "v_cvt_pk_bf16_f32 v191, v78, v79\n\t" \
    "v_cvt_pk_bf16_f32 v192, v80, v81\n\t" \
    "v_cvt_pk_bf16_f32 v193, v82, v83\n\t" \
    "v_permlane16_swap_b32 v184, v186\n\t" \
    "v_permlane16_swap_b32 v185, v187\n\t" \
    "v_permlane16_swap_b32 v188, v190\n\t" \
    "v_permlane16_swap_b32 v189, v191\n\t" \
    W4SLEEP \
    ".Lfy%=:\n\t" \
    "buffer_load_dwordx2 v[196:197], %[si], %[rr], %[sq] offen sc1\n\t" \
    "buffer_load_dwordx2 v[198:199], %[si], %[rr], %[sq] offen offset:512 sc1\n\t" \
    "s_waitcnt vmcnt(0)\n\t" \
    "v_max_u32 v200, v196, v197\n\t" \
    "v_max3_u32 v200, v198, v199, v200\n\t" \
    "v_cmp_eq_u32 vcc, -1, v200\n\t" \
    "s_cbranch_vccnz .Lfx%=\n\t" \
    ".Lfz%=:\n\t" \
    ASM_ST \
    "v_add_f32 v194, v196, v198\n\t" \
    "v_add_f32 v195, v197, v199\n\t" \
    "buffer_store_dwordx4 v[184:187], %[pa], %[rd], %[xr] offen sc1\n\t" \
    "buffer_store_dwordx4 v[188:191], %[pb], %[rd], %[xr] offen sc1\n\t" \
    "buffer_store_dwordx2 v[192:193], %[o4], %[rd], %[xr] offen sc1\n\t" \
    "s_nop 1\n\t" \
    "v_permlane16_swap_b32 v194, v195\n\t" \
    "v_add_f32 v196, v194, v195\n\t" \
    "v_add_f32 v197, v194, v195\n\t" \
    "s_nop 1\n\t" \
    "v_permlane32_swap_b32 v196, v197\n\t" \
    "v_add_f32 v194, v196, v197\n\t" \
    "v_add_f32 v195, v196, v197\n\t" \
    "s_nop 1\n\t" \
    "v_permlane16_swap_b32 v194, v195\n\t" \
    "v_mul_f32 v194, %[kd], v194\n\t" \
    "v_mul_f32 v195, %[kd], v195\n\t" \
    "v_fma_f32 v195, -v194, v194, v195\n\t" \
    "v_max_f32 v195, 0, v195\n\t" \
    "v_cvt_pk_bf16_f32 v195, v195, v195\n\t" \
    "v_lshlrev_b32 v195, 16, v195\n\t" \
    "v_add_f32 v195, %[ke], v195\n\t" \
    "v_cvt_pk_bf16_f32 v195, v195, v195\n\t" \
    "v_lshlrev_b32 v195, 16, v195\n\t" \
    "v_sqrt_f32 v195, v195\n\t" \
    "v_cvt_pk_bf16_f32 v194, v194, v194\n\t" \
    "v_lshlrev_b32 v194, 16, v194\n\t" \
    "v_cvt_pk_bf16_f32 v195, v195, v195\n\t" \
    "v_lshlrev_b32 v195, 16, v195\n\t" \
    "v_rcp_f32 v196, v195\n\t" \
    "v_rcp_f32 v197, v195\n\t" \
    "s_nop 0\n\t" \
    "v_mul_f32 v198, -v194, v196\n\t" \
    "v_mul_f32 v199, -v194, v196\n\t" \
    "v_pk_fma_f32 v[64:65], v[64:65], v[196:197], v[198:199]\n\t" \
    "v_pk_fma_f32 v[66:67], v[66:67], v[196:197], v[198:199]\n\t" \
    "v_pk_fma_f32 v[68:69], v[68:69], v[196:197], v[198:199]\n\t" \
    "v_pk_fma_f32 v[70:71], v[70:71], v[196:197], v[198:199]\n\t" \
    "v_pk_fma_f32 v[72:73], v[72:73], v[196:197], v[198:199]\n\t" \
    "v_pk_fma_f32 v[74:75], v[74:75], v[196:197], v[198:199]\n\t" \
    "v_pk_fma_f32 v[76:77], v[76:77], v[196:197], v[198:199]\n\t" \
    "v_pk_fma_f32 v[78:79], v[78:79], v[196:197], v[198:199]\n\t" \
    "v_pk_fma_f32 v[80:81], v[80:81], v[196:197], v[198:199]\n\t" \
    "v_pk_fma_f32 v[82:83], v[82:83], v[196:197], v[198:199]\n\t" \
    "v_cvt_pk_bf16_f32 v184, v64, v65\n\t" \
    "v_cvt_pk_bf16_f32 v185, v66, v67\n\t" \
    "v_cvt_pk_bf16_f32 v186, v68, v69\n\t" \
    "v_cvt_pk_bf16_f32 v187, v70, v71\n\t" \
    "v_cvt_pk_bf16_f32 v188, v72, v73\n\t" \
    "v_cvt_pk_bf16_f32 v189, v74, v75\n\t" \
    "v_cvt_pk_bf16_f32 v190, v76, v77\n\t" \
    "v_cvt_pk_bf16_f32 v191, v78, v79\n\t" \
    "v_cvt_pk_bf16_f32 v192, v80, v81\n\t" \
    "v_cvt_pk_bf16_f32 v193, v82, v83\n\t" \
    "v_permlane16_swap_b32 v184, v186\n\t" \
    "v_permlane16_swap_b32 v185, v187\n\t" \
    "v_permlane16_swap_b32 v188, v190\n\t" \
    "v_permlane16_swap_b32 v189, v191\n\t" \
    "buffer_store_dwordx4 v[184:187], %[pa], %[rd], %[xn] offen sc1\n\t" \
    "buffer_store_dwordx4 v[188:191], %[pb], %[rd], %[xn] offen sc1\n\t" \
    "buffer_store_dwordx2 v[192:193], %[o4], %[rd], %[xn] offen sc1\n\t" \
    "buffer_load_dwordx2 v[164:165], %[o0], %[rd], %[n1] offen sc1\n\t" \
    "buffer_load_dwordx2 v[166:167], %[o1], %[rd], %[n1] offen sc1\n\t" \
    "buffer_load_dwordx2 v[168:169], %[o2], %[rd], %[n1] offen sc1\n\t" \
    "buffer_load_dwordx2 v[170:171], %[o3], %[rd], %[n1] offen sc1\n\t" \
    "buffer_load_dwordx2 v[172:173], %[o4], %[rd], %[n1] offen sc1\n\t" \
    "buffer_load_dwordx2 v[174:175], %[o0], %[rd], %[na] offen sc1\n\t" \
    "buffer_load_dwordx2 v[176:177], %[o1], %[rd], %[na] offen sc1\n\t" \
    "buffer_load_dwordx2 v[178:179], %[o2], %[rd], %[na] offen sc1\n\t" \
    "buffer_load_dwordx2 v[180:181], %[o3], %[rd], %[na] offen sc1\n\t" \
    "buffer_load_dwordx2 v[182:183], %[o4], %[rd], %[na] offen sc1\n\t" \
    "v_mov_b32 v196, %[tn]\n\t" \
    "v_mov_b32 v197, %[tn]\n\t" \
    "v_mov_b32 v198, %[tn]\n\t" \
    "v_mov_b32 v199, %[tn]\n\t" \
    "s_waitcnt vmcnt(10)\n\t" \
    "buffer_store_dwordx4 v[196:199], %[fo], %[rn], %[fs] offen sc1\n\t" \
    "s_nop 1\n\t"
#define ASM_W4F_1 \
    "s_branch .Lf0%=\n\t" \
    ".Lfr%=:\n\t" \
    "s_cmp_eq_u32 %[n], 0\n\t" \
    "s_cbranch_scc1 .Lfb%=\n\t" \
    "s_sub_u32 %[n], %[n], 1\n\t" \
    "s_sleep 6\n\t" \
    "buffer_load_dwordx2 v[164:165], %[o0], %[rd], %[c1] offen sc1\n\t" \
    "buffer_load_dwordx2 v[166:167], %[o1], %[rd], %[c1] offen sc1\n\t" \
    "buffer_load_dwordx2 v[168:169], %[o2], %[rd], %[c1] offen sc1\n\t" \
    "buffer_load_dwordx2 v[170:171], %[o3], %[rd], %[c1] offen sc1\n\t" \
    "buffer_load_dwordx2 v[172:173], %[o4], %[rd], %[c1] offen sc1\n\t" \
    "buffer_load_dwordx2 v[174:175], %[o0], %[rd], %[ca] offen sc1\n\t" \
    "buffer_load_dwordx2 v[176:177], %[o1], %[rd], %[ca] offen sc1\n\t" \
    "buffer_load_dwordx2 v[178:179], %[o2], %[rd], %[ca] offen sc1\n\t" \
    "buffer_load_dwordx2 v[180:181], %[o3], %[rd], %[ca] offen sc1\n\t" \
    "buffer_load_dwordx2 v[182:183], %[o4], %[rd], %[ca] offen sc1\n\t" \
    "s_waitcnt vmcnt(0)\n\t" \
    "s_branch .Lfa%=\n\t" \
    ".Lfs0_%=:\n\t" \
    "s_cmp_eq_u32 %[n], 0\n\t" \
    "s_cbranch_scc1 .Lfu0_%=\n\t" \
    "s_sub_u32 %[n], %[n], 1\n\t" \
    "s_sleep 1\n\t" \
    "s_branch .Lft0_%=\n\t" \
    ".Lfs1_%=:\n\t" \
    "s_cmp_eq_u32 %[n], 0\n\t" \
    "s_cbranch_scc1 .Lfu1_%=\n\t" \
    "s_sub_u32 %[n], %[n], 1\n\t" \
    "s_sleep 1\n\t" \
    "s_branch .Lft1_%=\n\t" \
    ".Lf0%=:\n\t" \
    "s_waitcnt vmcnt(0)\n\t" \
    ".Lfa%=:\n\t" \
    "v_max_u32 v194, v164, v165\n\t" \
    "v_max3_u32 v194, v166, v167, v194\n\t" \
    "v_max3_u32 v194, v168, v169, v194\n\t" \
    "v_max3_u32 v194, v170, v171, v194\n\t" \
    "v_max3_u32 v194, v172, v173, v194\n\t" \
    "v_max3_u32 v194, v174, v175, v194\n\t" \
    "v_max3_u32 v194, v176, v177, v194\n\t" \
    "v_max3_u32 v194, v178, v179, v194\n\t" \
    "v_max3_u32 v194, v180, v181, v194\n\t" \
    "v_max3_u32 v194, v182, v183, v194\n\t" \
    "v_cmp_eq_u32 vcc, -1, v194\n\t" \
    "s_cbranch_vccnz .Lfr%=\n\t" \
    ".Lfb%=:\n\t" \
    "v_lshlrev_b32 v64, 16, v164\n\t" \
    "v_and_b32 v65, 0xffff0000, v164\n\t" \
    "v_lshlrev_b32 v84, 16, v174\n\t" \
    "v_and_b32 v85, 0xffff0000, v174\n\t" \
    "v_lshlrev_b32 v66, 16, v165\n\t" \
    "v_and_b32 v67, 0xffff0000, v165\n\t" \
    "v_lshlrev_b32 v86, 16, v175\n\t" \
    "v_and_b32 v87, 0xffff0000, v175\n\t" \
    "v_lshlrev_b32 v68, 16, v166\n\t" \
    "v_and_b32 v69, 0xffff0000, v166\n\t" \
    "v_lshlrev_b32 v88, 16, v176\n\t" \
    "v_and_b32 v89, 0xffff0000, v176\n\t" \
    "v_lshlrev_b32 v70, 16, v167\n\t" \
    "v_and_b32 v71, 0xffff0000, v167\n\t" \
    "v_lshlrev_b32 v90, 16, v177\n\t" \
    "v_and_b32 v91, 0xffff0000, v177\n\t" \
    "v_lshlrev_b32 v72, 16, v168\n\t" \
    "v_and_b32 v73, 0xffff0000, v168\n\t" \
    "v_lshlrev_b32 v92, 16, v178\n\t" \
    "v_and_b32 v93, 0xffff0000, v178\n\t" \
    "v_lshlrev_b32 v74, 16, v169\n\t" \
    "v_and_b32 v75, 0xffff0000, v169\n\t" \
    "v_lshlrev_b32 v94, 16, v179\n\t" \
    "v_and_b32 v95, 0xffff0000, v179\n\t" \
    "v_lshlrev_b32 v76, 16, v170\n\t" \
    "v_and_b32 v77, 0xffff0000, v170\n\t" \
    "v_lshlrev_b32 v96, 16, v180\n\t" \
    "v_and_b32 v97, 0xffff0000, v180\n\t" \
    "v_lshlrev_b32 v78, 16, v171\n\t" \
    "v_and_b32 v79, 0xffff0000, v171\n\t" \
    "v_lshlrev_b32 v98, 16, v181\n\t" \
    "v_and_b32 v99, 0xffff0000, v181\n\t" \
    "v_lshlrev_b32 v80, 16, v172\n\t" \
    "v_and_b32 v81, 0xffff0000, v172\n\t" \
    "v_lshlrev_b32 v100, 16, v182\n\t" \
    "v_and_b32 v101, 0xffff0000, v182\n\t" \
    "v_lshlrev_b32 v82, 16, v173\n\t" \
    "v_and_b32 v83, 0xffff0000, v173\n\t" \
    "v_lshlrev_b32 v102, 16, v183\n\t" \
    "v_and_b32 v103, 0xffff0000, v183\n\t" \
    "v_pk_add_f32 v[64:65], v[64:65], v[84:85]\n\t" \
    "v_pk_add_f32 v[66:67], v[66:67], v[86:87]\n\t" \
    "v_pk_add_f32 v[68:69], v[68:69], v[88:89]\n\t" \
    "v_pk_add_f32 v[70:71], v[70:71], v[90:91]\n\t" \
    "v_pk_add_f32 v[72:73], v[72:73], v[92:93]\n\t" \
    "v_pk_add_f32 v[74:75], v[74:75], v[94:95]\n\t" \
    "v_pk_add_f32 v[76:77], v[76:77], v[96:97]\n\t" \
    "v_pk_add_f32 v[78:79], v[78:79], v[98:99]\n\t" \
    "v_pk_add_f32 v[80:81], v[80:81], v[100:101]\n\t" \
    "v_pk_add_f32 v[82:83], v[82:83], v[102:103]\n\t" \
    ".Lft0_%=:\n\t" \
    "ds_read_b32 v194, %[f0]\n\t" \
    "s_waitcnt lgkmcnt(0)\n\t" \
    "v_cmp_ge_u32 vcc, %[u], v194\n\t" \
    "s_cbranch_vccnz .Lfs0_%=\n\t" \
    ".Lfu0_%=:\n\t" \
    "ds_read_b128 v[84:87], %[la] offset:0\n\t" \
    "ds_read_b128 v[88:91], %[la] offset:6144\n\t" \
    "ds_read_b128 v[92:95], %[la] offset:12288\n\t" \
    "ds_read_b128 v[96:99], %[la] offset:18432\n\t" \
    "ds_read_b128 v[100:103], %[la] offset:24576\n\t" \
    "ds_read_b128 v[104:107], %[la] offset:1024\n\t" \
    "ds_read_b128 v[108:111], %[la] offset:7168\n\t" \
    "ds_read_b128 v[112:115], %[la] offset:13312\n\t" \
    "ds_read_b128 v[116:119], %[la] offset:19456\n\t" \
    "ds_read_b128 v[120:123], %[la] offset:25600\n\t" \
    "ds_read_b128 v[124:127], %[la] offset:2048\n\t" \
    "s_waitcnt lgkmcnt(10)\n\t" \
    "v_pk_add_f32 v[64:65], v[64:65], v[84:85]\n\t" \
    "v_pk_add_f32 v[66:67], v[66:67], v[86:87]\n\t" \
    "ds_read_b128 v[128:131], %[la] offset:8192\n\t" \
    "s_waitcnt lgkmcnt(10)\n\t" \
    "v_pk_add_f32 v[68:69], v[68:69], v[88:89]\n\t" \
    "v_pk_add_f32 v[70:71], v[70:71], v[90:91]\n\t" \
    "ds_read_b128 v[132:135], %[la] offset:14336\n\t" \
    "s_waitcnt lgkmcnt(10)\n\t" \
    "v_pk_add_f32 v[72:73], v[72:73], v[92:93]\n\t" \
    "v_pk_add_f32 v[74:75], v[74:75], v[94:95]\n\t" \
    "ds_read_b128 v[136:139], %[la] offset:20480\n\t" \
    "s_waitcnt lgkmcnt(10)\n\t" \
    "v_pk_add_f32 v[76:77], v[76:77], v[96:97]\n\t" \
    "v_pk_add_f32 v[78:79], v[78:79], v[98:99]\n\t" \
    "ds_read_b128 v[140:143], %[la] offset:26624\n\t" \
    "s_waitcnt lgkmcnt(10)\n\t" \
    "v_pk_add_f32 v[80:81], v[80:81], v[100:101]\n\t" \
    "v_pk_add_f32 v[82:83], v[82:83], v[102:103]\n\t" \
    "ds_read_b128 v[144:147], %[la] offset:3072\n\t" \
    "s_waitcnt lgkmcnt(10)\n\t" \
    "v_pk_add_f32 v[64:65], v[64:65], v[104:105]\n\t" \
    "v_pk_add_f32 v[66:67], v[66:67], v[106:107]\n\t" \
    "ds_read_b128 v[148:151], %[la] offset:9216\n\t" \
    "s_waitcnt lgkmcnt(10)\n\t" \
    "v_pk_add_f32 v[68:69], v[68:69], v[108:109]\n\t" \
    "v_pk_add_f32 v[70:71], v[70:71], v[110:111]\n\t" \
    "ds_read_b128 v[152:155], %[la] offset:15360\n\t" \
    "s_waitcnt lgkmcnt(10)\n\t" \
    "v_pk_add_f32 v[72:73], v[72:73], v[112:113]\n\t" \
    "v_pk_add_f32 v[74:75], v[74:75], v[114:115]\n\t" \
    "ds_read_b128 v[156:159], %[la] offset:21504\n\t" \
    "s_waitcnt lgkmcnt(10)\n\t" \
    "v_pk_add_f32 v[76:77], v[76:77], v[116:117]\n\t" \
    "v_pk_add_f32 v[78:79], v[78:79], v[118:119]\n\t" \
    "ds_read_b128 v[160:163], %[la] offset:27648\n\t" \
    "s_waitcnt lgkmcnt(10)\n\t" \
    "v_pk_add_f32 v[80:81], v[80:81], v[120:121]\n\t" \
    "v_pk_add_f32 v[82:83], v[82:83], v[122:123]\n\t" \
    "s_waitcnt lgkmcnt(9)\n\t" \
    "v_pk_add_f32 v[64:65], v[64:65], v[124:125]\n\t" \
    "v_pk_add_f32 v[66:67], v[66:67], v[126:127]\n\t" \
    "s_waitcnt lgkmcnt(8)\n\t" \
    "v_pk_add_f32 v[68:69], v[68:69], v[128:129]\n\t" \
    "v_pk_add_f32 v[70:71], v[70:71], v[130:131]\n\t" \
    "s_waitcnt lgkmcnt(7)\n\t" \
    "v_pk_add_f32 v[72:73], v[72:73], v[132:133]\n\t" \
    "v_pk_add_f32 v[74:75], v[74:75], v[134:135]\n\t" \
    "s_waitcnt lgkmcnt(6)\n\t" \
    "v_pk_add_f32 v[76:77], v[76:77], v[136:137]\n\t" \
    "v_pk_add_f32 v[78:79], v[78:79], v[138:139]\n\t" \
    "s_waitcnt lgkmcnt(5)\n\t" \
    "v_pk_add_f32 v[80:81], v[80:81], v[140:141]\n\t" \
    "v_pk_add_f32 v[82:83], v[82:83], v[142:143]\n\t" \
    "s_waitcnt lgkmcnt(4)\n\t" \
    "v_pk_add_f32 v[64:65], v[64:65], v[144:145]\n\t" \
    "v_pk_add_f32 v[66:67], v[66:67], v[146:147]\n\t" \
    "s_waitcnt lgkmcnt(3)\n\t" \
    "v_pk_add_f32 v[68:69], v[68:69], v[148:149]\n\t" \
    "v_pk_add_f32 v[70:71], v[70:71], v[150:151]\n\t" \
    "s_waitcnt lgkmcnt(2)\n\t" \
    "v_pk_add_f32 v[72:73], v[72:73], v[152:153]\n\t" \
    "v_pk_add_f32 v[74:75], v[74:75], v[154:155]\n\t" \
    "s_waitcnt lgkmcnt(1)\n\t" \
    "v_pk_add_f32 v[76:77], v[76:77], v[156:157]\n\t" \
    "v_pk_add_f32 v[78:79], v[78:79], v[158:159]\n\t" \
    "s_waitcnt lgkmcnt(0)\n\t" \
    "v_pk_add_f32 v[80:81], v[80:81], v[160:161]\n\t" \
    "v_pk_add_f32 v[82:83], v[82:83], v[162:163]\n\t" \
    ".Lft1_%=:\n\t" \
    "ds_read_b32 v194, %[f1]\n\t" \
    "s_waitcnt lgkmcnt(0)\n\t" \
    "v_cmp_ge_u32 vcc, %[u], v194\n\t" \
    "s_cbranch_vccnz .Lfs1_%=\n\t" \
    ".Lfu1_%=:\n\t" \
    ASM_ST2 \
    "ds_read_b128 v[84:87], %[la] offset:4096\n\t" \
    "ds_read_b128 v[88:91], %[la] offset:10240\n\t" \
    "ds_read_b128 v[92:95], %[la] offset:16384\n\t" \
    "ds_read_b128 v[96:99], %[la] offset:22528\n\t" \
    "ds_read_b128 v[100:103], %[la] offset:28672\n\t" \
    "ds_read_b128 v[104:107], %[la] offset:5120\n\t" \
    "ds_read_b128 v[108:111], %[la] offset:11264\n\t" \
    "ds_read_b128 v[112:115], %[la] offset:17408\n\t" \
    "ds_read_b128 v[116:119], %[la] offset:23552\n\t" \
    "ds_read_b128 v[120:123], %[la] offset:29696\n\t" \
    "v_mov_b32 v194, %[un]\n\t" \
    "ds_write_b32 %[fd], v194\n\t" \
    "s_waitcnt lgkmcnt(10)\n\t" \
    "v_pk_add_f32 v[64:65], v[64:65], v[84:85]\n\t" \
    "v_pk_add_f32 v[66:67], v[66:67], v[86:87]\n\t" \
    "s_waitcnt lgkmcnt(9)\n\t" \
    "v_pk_add_f32 v[68:69], v[68:69], v[88:89]\n\t" \
    "v_pk_add_f32 v[70:71], v[70:71], v[90:91]\n\t" \
    "s_waitcnt lgkmcnt(8)\n\t" \
    "v_pk_add_f32 v[72:73], v[72:73], v[92:93]\n\t" \
    "v_pk_add_f32 v[74:75], v[74:75], v[94:95]\n\t" \
    "s_waitcnt lgkmcnt(7)\n\t" \
    "v_pk_add_f32 v[76:77], v[76:77], v[96:97]\n\t" \
    "v_pk_add_f32 v[78:79], v[78:79], v[98:99]\n\t" \
    "s_waitcnt lgkmcnt(6)\n\t" \
    "v_pk_add_f32 v[80:81], v[80:81], v[100:101]\n\t" \
    "v_pk_add_f32 v[82:83], v[82:83], v[102:103]\n\t" \
    "s_waitcnt lgkmcnt(5)\n\t" \
    "v_pk_add_f32 v[64:65], v[64:65], v[104:105]\n\t" \
    "v_pk_add_f32 v[66:67], v[66:67], v[106:107]\n\t" \
    "s_waitcnt lgkmcnt(4)\n\t" \
    "v_pk_add_f32 v[68:69], v[68:69], v[108:109]\n\t" \
    "v_pk_add_f32 v[70:71], v[70:71], v[110:111]\n\t" \
    "s_waitcnt lgkmcnt(3)\n\t" \
    "v_pk_add_f32 v[72:73], v[72:73], v[112:113]\n\t" \
    "v_pk_add_f32 v[74:75], v[74:75], v[114:115]\n\t" \
    "s_waitcnt lgkmcnt(2)\n\t" \
    "v_pk_add_f32 v[76:77], v[76:77], v[116:117]\n\t" \
    "v_pk_add_f32 v[78:79], v[78:79], v[118:119]\n\t" \
    "s_waitcnt lgkmcnt(1)\n\t" \
    "v_pk_add_f32 v[80:81], v[80:81], v[120:121]\n\t" \
    "v_pk_add_f32 v[82:83], v[82:83], v[122:123]\n\t" \
    "v_cvt_pk_bf16_f32 v184, v64, v65\n\t" \
    "v_cvt_pk_bf16_f32 v185, v66, v67\n\t" \
    "v_cvt_pk_bf16_f32 v186, v68, v69\n\t" \
    "v_cvt_pk_bf16_f32 v187, v70, v71\n\t" \
    "v_cvt_pk_bf16_f32 v188, v72, v73\n\t" \
    "v_cvt_pk_bf16_f32 v189, v74, v75\n\t" \
    "v_cvt_pk_bf16_f32 v190, v76, v77\n\t" \
    "v_cvt_pk_bf16_f32 v191, v78, v79\n\t" \
    "v_cvt_pk_bf16_f32 v192, v80, v81\n\t" \
    "v_cvt_pk_bf16_f32 v193, v82, v83\n\t" \
    "v_permlane16_swap_b32 v184, v186\n\t" \
    "v_permlane16_swap_b32 v185, v187\n\t" \
    "v_permlane16_swap_b32 v188, v190\n\t" \
    "v_permlane16_swap_b32 v189, v191\n\t" \
    "buffer_store_dwordx4 v[184:187], %[pa], %[ro], %[so] offen nt\n\t" \
    "buffer_store_dwordx4 v[188:191], %[pa], %[ro], %[so] offen offset:64 nt\n\t" \
    "buffer_store_dwordx2 v[192:193], %[pb], %[ro], %[so] offen offset:128 nt\n\t" \
    "buffer_load_dwordx2 v[164:165], %[o0], %[rd], %[n1] offen sc1\n\t" \
    "buffer_load_dwordx2 v[166:167], %[o1], %[rd], %[n1] offen sc1\n\t" \
    "buffer_load_dwordx2 v[168:169], %[o2], %[rd], %[n1] offen sc1\n\t" \
    "buffer_load_dwordx2 v[170:171], %[o3], %[rd], %[n1] offen sc1\n\t" \
    "buffer_load_dwordx2 v[172:173], %[o4], %[rd], %[n1] offen sc1\n\t" \
    "buffer_load_dwordx2 v[174:175], %[o0], %[rd], %[na] offen sc1\n\t" \
    "buffer_load_dwordx2 v[176:177], %[o1], %[rd], %[na] offen sc1\n\t" \
    "buffer_load_dwordx2 v[178:179], %[o2], %[rd], %[na] offen sc1\n\t" \
    "buffer_load_dwordx2 v[180:181], %[o3], %[rd], %[na] offen sc1\n\t" \
    "buffer_load_dwordx2 v[182:183], %[o4], %[rd], %[na] offen sc1\n\t"
#define ASM_WCS \
    "v_mfma_f32_16x16x32_bf16 v[196:199], v[0:3], v[216:219], 0\n\t" \
    "v_mfma_f32_16x16x32_bf16 v[200:203], v[80:83], v[216:219], 0\n\t" \
    "v_mfma_f32_16x16x32_bf16 v[196:199], v[4:7], v[216:219], v[196:199]\n\t" \
    "v_mfma_f32_16x16x32_bf16 v[200:203], v[84:87], v[216:219], v[200:203]\n\t" \
    "v_mfma_f32_16x16x32_bf16 v[196:199], v[8:11], v[216:219], v[196:199]\n\t" \
    "v_mfma_f32_16x16x32_bf16 v[200:203], v[88:91], v[216:219], v[200:203]\n\t" \
    "v_mfma_f32_16x16x32_bf16 v[196:199], v[12:15], v[216:219], v[196:199]\n\t" \
    "v_mfma_f32_16x16x32_bf16 v[200:203], v[92:95], v[216:219], v[200:203]\n\t" \
    "v_mfma_f32_16x16x32_bf16 v[196:199], v[16:19], v[216:219], v[196:199]\n\t" \
    "v_mfma_f32_16x16x32_bf16 v[200:203], v[96:99], v[216:219], v[200:203]\n\t" \
    "v_mfma_f32_16x16x32_bf16 v[196:199], v[20:23], v[216:219], v[196:199]\n\t" \
    "v_mfma_f32_16x16x32_bf16 v[200:203], v[100:103], v[216:219], v[200:203]\n\t" \
    "v_mfma_f32_16x16x32_bf16 v[196:199], v[24:27], v[216:219], v[196:199]\n\t" \
    "v_mfma_f32_16x16x32_bf16 v[200:203], v[104:107], v[216:219], v[200:203]\n\t" \
    "v_mfma_f32_16x16x32_bf16 v[196:199], v[28:31], v[216:219], v[196:199]\n\t" \
    "v_mfma_f32_16x16x32_bf16 v[200:203], v[108:111], v[216:219], v[200:203]\n\t" \
    "v_mfma_f32_16x16x32_bf16 v[196:199], v[32:35], v[216:219], v[196:199]\n\t" \
    "v_mfma_f32_16x16x32_bf16 v[200:203], v[112:115], v[216:219], v[200:203]\n\t" \
    "v_mfma_f32_16x16x32_bf16 v[196:199], v[36:39], v[216:219], v[196:199]\n\t" \
    "v_mfma_f32_16x16x32_bf16 v[200:203], v[116:119], v[216:219], v[200:203]\n\t" \
    "v_mfma_f32_16x16x32_bf16 v[196:199], v[40:43], v[216:219], v[196:199]\n\t" \
    "v_mfma_f32_16x16x32_bf16 v[200:203], v[120:123], v[216:219], v[200:203]\n\t" \
    "v_mfma_f32_16x16x32_bf16 v[196:199], v[44:47], v[216:219], v[196:199]\n\t" \
    "v_mfma_f32_16x16x32_bf16 v[200:203], v[124:127], v[216:219], v[200:203]\n\t" \
    "v_mfma_f32_16x16x32_bf16 v[196:199], v[48:51], v[216:219], v[196:199]\n\t" \
    "v_mfma_f32_16x16x32_bf16 v[200:203], v[128:131], v[216:219], v[200:203]\n\t" \
    "v_mfma_f32_16x16x32_bf16 v[196:199], v[52:55], v[216:219], v[196:199]\n\t" \
    "v_mfma_f32_16x16x32_bf16 v[200:203], v[132:135], v[216:219], v[200:203]\n\t" \
    "v_mfma_f32_16x16x32_bf16 v[196:199], v[56:59], v[216:219], v[196:199]\n\t" \
    "v_mfma_f32_16x16x32_bf16 v[200:203], v[136:139], v[216:219], v[200:203]\n\t" \
    "v_mfma_f32_16x16x32_bf16 v[196:199], v[60:63], v[216:219], v[196:199]\n\t" \
    "v_mfma_f32_16x16x32_bf16 v[200:203], v[140:143], v[216:219], v[200:203]\n\t" \
    "v_mfma_f32_16x16x32_bf16 v[196:199], v[64:67], v[216:219], v[196:199]\n\t" \
    "v_mfma_f32_16x16x32_bf16 v[200:203], v[144:147], v[216:219], v[200:203]\n\t" \
    "v_mfma_f32_16x16x32_bf16 v[196:199], v[68:71], v[216:219], v[196:199]\n\t" \
    "v_mfma_f32_16x16x32_bf16 v[200:203], v[148:151], v[216:219], v[200:203]\n\t" \
    "v_mfma_f32_16x16x32_bf16 v[196:199], v[72:75], v[216:219], v[196:199]\n\t" \
    "v_mfma_f32_16x16x32_bf16 v[200:203], v[152:155], v[216:219], v[200:203]\n\t" \
    "v_mfma_f32_16x16x32_bf16 v[196:199], v[76:79], v[216:219], v[196:199]\n\t" \
    "v_mfma_f32_16x16x32_bf16 v[200:203], v[156:159], v[216:219], v[200:203]\n\t" \
    "s_nop 7\n\t"
#define ASM_WT1_LO \
    "s_branch .Lt0%=\n\t" \
    ".Ltr%=:\n\t" \
    "s_cmp_gt_i32 %[n], 0\n\t" \
    "s_cbranch_scc0 .Ltg%=\n\t" \
    ".Lfw%=:\n\t" \
    "s_mov_b32 %[a], 0\n\t" \
    "v_max_u32 v252, v232, v233\n\t" \
    "v_max3_u32 v252, v234, v235, v252\n\t" \
    "v_cmp_eq_u32 vcc, -1, v252\n\t" \
    "s_cbranch_vccz .Lgw0_%=\n\t" \
    "buffer_load_dwordx4 v[232:235], %[l16], %[rs], %[i0] offen offset:0 sc1\n\t" \
    "s_mov_b32 %[a], 1\n\t" \
    ".Lgw0_%=:\n\t" \
    "v_max_u32 v252, v236, v237\n\t" \
    "v_max3_u32 v252, v238, v239, v252\n\t" \
    "v_cmp_eq_u32 vcc, -1, v252\n\t" \
    "s_cbranch_vccz .Lgw1_%=\n\t" \
    "buffer_load_dwordx4 v[236:239], %[l16], %[rs], %[i0] offen offset:1024 sc1\n\t" \
    "s_mov_b32 %[a], 1\n\t" \
    ".Lgw1_%=:\n\t" \
    "v_max_u32 v252, v240, v241\n\t" \
    "v_max3_u32 v252, v242, v243, v252\n\t" \
    "v_cmp_eq_u32 vcc, -1, v252\n\t" \
    "s_cbranch_vccz .Lgw2_%=\n\t" \
    "buffer_load_dwordx4 v[240:243], %[l16], %[rs], %[i0] offen offset:2048 sc1\n\t" \
    "s_mov_b32 %[a], 1\n\t" \
    ".Lgw2_%=:\n\t" \
    "v_max_u32 v252, v244, v245\n\t" \
    "v_max3_u32 v252, v246, v247, v252\n\t" \
    "v_cmp_eq_u32 vcc, -1, v252\n\t" \
    "s_cbranch_vccz .Lgw3_%=\n\t" \
    "buffer_load_dwordx4 v[244:247], %[l16], %[rs], %[i0] offen offset:3072 sc1\n\t" \
    "s_mov_b32 %[a], 1\n\t" \
    ".Lgw3_%=:\n\t" \
    "v_max_u32 v252, v248, v249\n\t" \
    "v_max3_u32 v252, v250, v251, v252\n\t" \
    "v_cmp_eq_u32 vcc, -1, v252\n\t" \
    "s_cbranch_vccz .Lgw4_%=\n\t" \
    "buffer_load_dwordx4 v[248:251], %[l16], %[rs], %[i1] offen offset:0 sc1\n\t" \
    "s_mov_b32 %[a], 1\n\t" \
    ".Lgw4_%=:\n\t" \
    "s_cmp_eq_u32 %[a], 0\n\t" \
    "s_cbranch_scc1 .Lhw%=\n\t" \
    "s_waitcnt vmcnt(20)\n\t" \
    ".Liw%=:\n\t" \
    "v_max_u32 v252, v232, v233\n\t" \
    "v_max3_u32 v252, v234, v235, v252\n\t" \
    "v_max3_u32 v252, v236, v237, v252\n\t" \
    "v_max3_u32 v252, v238, v239, v252\n\t" \
    "v_max3_u32 v252, v240, v241, v252\n\t" \
    "v_max3_u32 v252, v242, v243, v252\n\t" \
    "v_max3_u32 v252, v244, v245, v252\n\t" \
    "v_max3_u32 v252, v246, v247, v252\n\t" \
    "v_max3_u32 v252, v248, v249, v252\n\t" \
    "v_max3_u32 v252, v250, v251, v252\n\t" \
    "v_cmp_eq_u32 vcc, -1, v252\n\t" \
    "s_cbranch_vccz .Lhw%=\n\t" \
    "s_sleep 1\n\t" \
    "v_max_u32 v252, v232, v233\n\t" \
    "v_max3_u32 v252, v234, v235, v252\n\t" \
    "v_max3_u32 v252, v236, v237, v252\n\t" \
    "v_max3_u32 v252, v238, v239, v252\n\t" \
    "v_max3_u32 v252, v240, v241, v252\n\t" \
    "v_max3_u32 v252, v242, v243, v252\n\t" \
    "v_max3_u32 v252, v244, v245, v252\n\t" \
    "v_max3_u32 v252, v246, v247, v252\n\t" \
    "v_max3_u32 v252, v248, v249, v252\n\t" \
    "v_max3_u32 v252, v250, v251, v252\n\t" \
    "v_cmp_eq_u32 vcc, -1, v252\n\t" \
    "s_cbranch_vccz .Lhw%=\n\t" \
    "s_sleep 1\n\t" \
    "v_max_u32 v252, v232, v233\n\t" \
    "v_max3_u32 v252, v234, v235, v252\n\t" \
    "v_max3_u32 v252, v236, v237, v252\n\t" \
    "v_max3_u32 v252, v238, v239, v252\n\t" \
    "v_max3_u32 v252, v240, v241, v252\n\t" \
    "v_max3_u32 v252, v242, v243, v252\n\t" \
    "v_max3_u32 v252, v244, v245, v252\n\t" \
    "v_max3_u32 v252, v246, v247, v252\n\t" \
    "v_max3_u32 v252, v248, v249, v252\n\t" \
    "v_max3_u32 v252, v250, v251, v252\n\t" \
    "v_cmp_eq_u32 vcc, -1, v252\n\t" \
    "s_cbranch_vccz .Lhw%=\n\t" \
    "s_sleep 1\n\t" \
    "v_max_u32 v252, v232, v233\n\t" \
    "v_max3_u32 v252, v234, v235, v252\n\t" \
    "v_max3_u32 v252, v236, v237, v252\n\t" \
    "v_max3_u32 v252, v238, v239, v252\n\t" \
    "v_max3_u32 v252, v240, v241, v252\n\t" \
    "v_max3_u32 v252, v242, v243, v252\n\t" \
    "v_max3_u32 v252, v244, v245, v252\n\t" \
    "v_max3_u32 v252, v246, v247, v252\n\t" \
    "v_max3_u32 v252, v248, v249, v252\n\t" \
    "v_max3_u32 v252, v250, v251, v252\n\t" \
    "v_cmp_eq_u32 vcc, -1, v252\n\t" \
    "s_cbranch_vccz .Lhw%=\n\t" \
    "s_sleep 1\n\t" \
    "v_max_u32 v252, v232, v233\n\t" \
    "v_max3_u32 v252, v234, v235, v252\n\t" \
    "v_max3_u32 v252, v236, v237, v252\n\t" \
    "v_max3_u32 v252, v238, v239, v252\n\t" \
    "v_max3_u32 v252, v240, v241, v252\n\t" \
    "v_max3_u32 v252, v242, v243, v252\n\t" \
    "v_max3_u32 v252, v244, v245, v252\n\t" \
    "v_max3_u32 v252, v246, v247, v252\n\t" \
    "v_max3_u32 v252, v248, v249, v252\n\t" \
    "v_max3_u32 v252, v250, v251, v252\n\t" \
    "v_cmp_eq_u32 vcc, -1, v252\n\t" \
    "s_cbranch_vccz .Lhw%=\n\t" \
    "s_sleep 1\n\t" \
    "v_max_u32 v252, v232, v233\n\t" \
    "v_max3_u32 v252, v234, v235, v252\n\t" \
    "v_max3_u32 v252, v236, v237, v252\n\t" \
    "v_max3_u32 v252, v238, v239, v252\n\t" \
    "v_max3_u32 v252, v240, v241, v252\n\t" \
    "v_max3_u32 v252, v242, v243, v252\n\t" \
    "v_max3_u32 v252, v244, v245, v252\n\t" \
    "v_max3_u32 v252, v246, v247, v252\n\t" \
    "v_max3_u32 v252, v248, v249, v252\n\t" \
    "v_max3_u32 v252, v250, v251, v252\n\t" \
    "v_cmp_eq_u32 vcc, -1, v252\n\t" \
    "s_cbranch_vccz .Lhw%=\n\t" \
    "s_sleep 1\n\t" \
    "s_sub_u32 %[n], %[n], 1\n\t" \
    "s_cmp_gt_i32 %[n], 0\n\t" \
    "s_cbranch_scc1 .Lfw%=\n\t" \
    ".Lhw%=:\n\t" \
    "s_branch .Lta%=\n\t" \
    ".Lt0%=:\n\t" \
    "s_waitcnt vmcnt(1)\n\t" \
    ".Lta%=:\n\t" \
    "ds_write_b128 %[lw], v[232:235] offset:0\n\t" \
    "v_max_u32 v252, v232, v233\n\t" \
    "v_max3_u32 v252, v234, v235, v252\n\t" \
    "v_mfma_f32_16x16x32_bf16 v[188:191], v[232:235], v[232:235], 0\n\t" \
    "v_mfma_f32_16x16x32_bf16 v[192:195], v[216:219], v[232:235], 0\n\t" \
    "ds_write_b128 %[lw], v[236:239] offset:1024\n\t" \
    "v_max3_u32 v252, v236, v237, v252\n\t" \
    "v_max3_u32 v252, v238, v239, v252\n\t" \
    "v_mfma_f32_16x16x32_bf16 v[188:191], v[236:239], v[236:239], v[188:191]\n\t" \
    "v_mfma_f32_16x16x32_bf16 v[192:195], v[216:219], v[236:239], v[192:195]\n\t" \
    "ds_write_b128 %[lw], v[240:243] offset:2048\n\t" \
    "v_max3_u32 v252, v240, v241, v252\n\t" \
    "v_max3_u32 v252, v242, v243, v252\n\t" \
    "v_mfma_f32_16x16x32_bf16 v[188:191], v[240:243], v[240:243], v[188:191]\n\t" \
    "v_mfma_f32_16x16x32_bf16 v[192:195], v[216:219], v[240:243], v[192:195]\n\t" \
    "ds_write_b128 %[lw], v[244:247] offset:3072\n\t" \
    "v_max3_u32 v252, v244, v245, v252\n\t" \
    "v_max3_u32 v252, v246, v247, v252\n\t" \
    "v_mfma_f32_16x16x32_bf16 v[188:191], v[244:247], v[244:247], v[188:191]\n\t" \
    "v_mfma_f32_16x16x32_bf16 v[192:195], v[216:219], v[244:247], v[192:195]\n\t" \
    "ds_write_b128 %[lw], v[248:251] offset:4096\n\t" \
    "v_max3_u32 v252, v248, v249, v252\n\t" \
    "v_max3_u32 v252, v250, v251, v252\n\t" \
    "v_mfma_f32_16x16x32_bf16 v[188:191], v[248:251], v[248:251], v[188:191]\n\t" \
    "v_mfma_f32_16x16x32_bf16 v[192:195], v[216:219], v[248:251], v[192:195]\n\t" \
    "v_cmp_eq_u32 vcc, -1, v252\n\t" \
    "s_cbranch_vccnz .Ltr%=\n\t" \
    ".Ltg%=:\n\t" \
    ASM_ST2 \
    "s_nop 5\n\t" \
    "ds_write_b128 %[lw], v[188:191] offset:5120\n\t" \
    "ds_write_b128 %[lw], v[192:195] offset:6144\n\t" \
    "s_waitcnt lgkmcnt(0)\n\t" \
    "s_setprio 2\n\t" \
    "s_barrier\n\t" ASM_ST \
    "ds_read_b128 v[160:163], %[la] offset:0\n\t" \
    "ds_read_b128 v[164:167], %[la] offset:1024\n\t" \
    "ds_read_b128 v[168:171], %[la] offset:2048\n\t" \
    "ds_read2st64_b32 v[204:205], %[sr] offset0:20 offset1:24\n\t" \
    "ds_read_b128 v[172:175], %[la] offset:3072\n\t" \
    "ds_read_b128 v[176:179], %[la] offset:8192\n\t" \
    "ds_read_b128 v[180:183], %[la] offset:9216\n\t" \
    "s_waitcnt lgkmcnt(6)\n\t" \
    "v_mfma_f32_16x16x32_bf16 v[188:191], v[0:3], v[160:163], 0\n\t" \
    "ds_read_b128 v[184:187], %[la] offset:10240\n\t" \
    "v_mfma_f32_16x16x32_bf16 v[192:195], v[80:83], v[160:163], 0\n\t" \
    "s_waitcnt lgkmcnt(6)\n\t" \
    "v_mfma_f32_16x16x32_bf16 v[188:191], v[4:7], v[164:167], v[188:191]\n\t" \
    "ds_read_b128 v[160:163], %[la] offset:11264\n\t" \
    "v_mfma_f32_16x16x32_bf16 v[192:195], v[84:87], v[164:167], v[192:195]\n\t" \
    "s_waitcnt lgkmcnt(6)\n\t" \
    "v_mfma_f32_16x16x32_bf16 v[188:191], v[8:11], v[168:171], v[188:191]\n\t" \
    "ds_read_b128 v[164:167], %[la] offset:16384\n\t" \
    "s_waitcnt lgkmcnt(6)\n\t" \
    "v_mfma_f32_16x16x32_bf16 v[192:195], v[88:91], v[168:171], v[192:195]\n\t" \
    "v_permlane16_swap_b32 v204, v205\n\t" \
    "s_waitcnt lgkmcnt(5)\n\t" \
    "v_mfma_f32_16x16x32_bf16 v[188:191], v[12:15], v[172:175], v[188:191]\n\t" \
    "ds_read_b128 v[168:171], %[la] offset:17408\n\t" \
    "v_mfma_f32_16x16x32_bf16 v[192:195], v[92:95], v[172:175], v[192:195]\n\t" \
    "v_add_f32 v206, v204, v205\n\t" \
    "s_waitcnt lgkmcnt(5)\n\t" \
    "v_mfma_f32_16x16x32_bf16 v[188:191], v[16:19], v[176:179], v[188:191]\n\t" \
    "ds_read_b128 v[172:175], %[la] offset:18432\n\t" \
    "v_add_f32 v207, v204, v205\n\t" \
    "v_mfma_f32_16x16x32_bf16 v[192:195], v[96:99], v[176:179], v[192:195]\n\t" \
    "s_waitcnt lgkmcnt(5)\n\t" \
    "v_mfma_f32_16x16x32_bf16 v[188:191], v[20:23], v[180:183], v[188:191]\n\t" \
    "ds_read_b128 v[176:179], %[la] offset:19456\n\t" \
    "v_permlane32_swap_b32 v206, v207\n\t" \
    "v_mfma_f32_16x16x32_bf16 v[192:195], v[100:103], v[180:183], v[192:195]\n\t" \
    "s_waitcnt lgkmcnt(5)\n\t" \
    "v_mfma_f32_16x16x32_bf16 v[188:191], v[24:27], v[184:187], v[188:191]\n\t" \
    "ds_read_b128 v[180:183], %[la] offset:24576\n\t" \
    "v_add_f32 v204, v206, v207\n\t" \
    "v_mfma_f32_16x16x32_bf16 v[192:195], v[104:107], v[184:187], v[192:195]\n\t" \
    "v_add_f32 v205, v206, v207\n\t" \
    "s_waitcnt lgkmcnt(5)\n\t" \
    "v_mfma_f32_16x16x32_bf16 v[188:191], v[28:31], v[160:163], v[188:191]\n\t" \
    "ds_read_b128 v[184:187], %[la] offset:25600\n\t" \
    "v_mfma_f32_16x16x32_bf16 v[192:195], v[108:111], v[160:163], v[192:195]\n\t" \
    "v_permlane16_swap_b32 v204, v205\n\t" \
    "s_waitcnt lgkmcnt(5)\n\t" \
    "v_mfma_f32_16x16x32_bf16 v[188:191], v[32:35], v[164:167], v[188:191]\n\t" \
    "ds_read_b128 v[160:163], %[la] offset:26624\n\t" \
    "v_mfma_f32_16x16x32_bf16 v[192:195], v[112:115], v[164:167], v[192:195]\n\t" \
    "v_mul_f32 v205, %[kd], v205\n\t" \
    "s_waitcnt lgkmcnt(5)\n\t" \
    "v_mfma_f32_16x16x32_bf16 v[188:191], v[36:39], v[168:171], v[188:191]\n\t" \
    "ds_read_b128 v[164:167], %[la] offset:27648\n\t" \
    "v_mul_f32 v204, %[kd], v204\n\t" \
    "v_mfma_f32_16x16x32_bf16 v[192:195], v[116:119], v[168:171], v[192:195]\n\t" \
    "v_fma_f32 v204, -v205, v205, v204\n\t" \
    "s_waitcnt lgkmcnt(5)\n\t" \
    "v_mfma_f32_16x16x32_bf16 v[188:191], v[40:43], v[172:175], v[188:191]\n\t" \
    "ds_read_b128 v[168:171], %[la] offset:4096\n\t" \
    "v_max_f32 v204, 0, v204\n\t" \
    "v_mfma_f32_16x16x32_bf16 v[192:195], v[120:123], v[172:175], v[192:195]\n\t" \
    "v_add_f32 v206, %[ke], v204\n\t" \
    "s_waitcnt lgkmcnt(5)\n\t" \
    "v_mfma_f32_16x16x32_bf16 v[188:191], v[44:47], v[176:179], v[188:191]\n\t" \
    "ds_read_b128 v[172:175], %[la] offset:12288\n\t" \
    "v_rsq_f32 v206, v206\n\t" \
    "v_mfma_f32_16x16x32_bf16 v[192:195], v[124:127], v[176:179], v[192:195]\n\t" \
    "s_waitcnt lgkmcnt(5)\n\t" \
    "v_mfma_f32_16x16x32_bf16 v[188:191], v[48:51], v[180:183], v[188:191]\n\t" \
    "ds_read_b128 v[176:179], %[la] offset:20480\n\t" \
    "v_mul_f32 v207, v204, v206\n\t" \
    "v_mfma_f32_16x16x32_bf16 v[192:195], v[128:131], v[180:183], v[192:195]\n\t" \
    "v_fma_f32 v207, v207, v206, %[ke]\n\t" \
    "s_waitcnt lgkmcnt(5)\n\t" \
    "v_mfma_f32_16x16x32_bf16 v[188:191], v[52:55], v[184:187], v[188:191]\n\t" \
    "ds_read_b128 v[180:183], %[la] offset:28672\n\t" \
    "v_rsq_f32 v207, v207\n\t" \
    "v_mfma_f32_16x16x32_bf16 v[192:195], v[132:135], v[184:187], v[192:195]\n\t" \
    "v_mul_f32 v206, v206, v207\n\t" \
    "v_mul_f32 v207, -v205, v206\n\t" \
    "s_waitcnt lgkmcnt(5)\n\t" \
    "v_mfma_f32_16x16x32_bf16 v[188:191], v[56:59], v[160:163], v[188:191]\n\t" \
    "v_mul_f32 v208, v196, v207\n\t" \
    "v_mul_f32 v209, v197, v207\n\t" \
    "v_mfma_f32_16x16x32_bf16 v[192:195], v[136:139], v[160:163], v[192:195]\n\t" \
    "v_mul_f32 v210, v198, v207\n\t" \
    "v_mul_f32 v211, v199, v207\n\t" \
    "s_waitcnt lgkmcnt(4)\n\t" \
    "v_mfma_f32_16x16x32_bf16 v[188:191], v[60:63], v[164:167], v[188:191]\n\t" \
    "v_mul_f32 v212, v200, v207\n\t" \
    "v_mul_f32 v213, v201, v207\n\t" \
    "v_mfma_f32_16x16x32_bf16 v[192:195], v[140:143], v[164:167], v[192:195]\n\t" \
    "v_mul_f32 v214, v202, v207\n\t" \
    "v_mul_f32 v215, v203, v207\n\t" \
    "s_waitcnt lgkmcnt(3)\n\t" \
    "v_mfma_f32_16x16x32_bf16 v[188:191], v[64:67], v[168:171], v[188:191]\n\t" \
    "s_cmp_lg_u32 %[sw], 0\n\t" \
    "s_cbranch_scc1 .Lts%=\n\t" \
    "v_cvt_pk_bf16_f32 v217, v204, v204\n\t" \
    "v_lshlrev_b32 v217, 16, v217\n\t" \
    "v_add_f32 v217, %[ke], v217\n\t" \
    "v_cvt_pk_bf16_f32 v217, v217, v217\n\t" \
    "v_lshlrev_b32 v217, 16, v217\n\t" \
    "v_sqrt_f32 v217, v217\n\t" \
    "v_cvt_pk_bf16_f32 v216, v205, v205\n\t" \
    "v_lshlrev_b32 v216, 16, v216\n\t" \
    "v_cvt_pk_bf16_f32 v217, v217, v217\n\t" \
    "v_lshlrev_b32 v217, 16, v217\n\t" \
    "v_rcp_f32 v217, v217\n\t" \
    "v_lshrrev_b32 v218, 1, %[l16]\n\t" \
    "v_and_b32 v218, 0x78, v218\n\t" \
    "buffer_store_dwordx2 v[216:217], v218, %[rs], %[ss] offen sc1\n\t" \
    ".Lts%=:\n\t" \
    "v_mov_b64 v[216:217], -1\n\t" \
    "v_mfma_f32_16x16x32_bf16 v[192:195], v[144:147], v[168:171], v[192:195]\n\t" \
    "v_mov_b64 v[218:219], -1\n\t" \
    "buffer_store_dwordx4 v[216:219], %[ob], %[rs], %[sa] offen sc1\n\t" \
    "s_waitcnt lgkmcnt(2)\n\t" \
    "v_mfma_f32_16x16x32_bf16 v[188:191], v[68:71], v[172:175], v[188:191]\n\t" \
    "v_mfma_f32_16x16x32_bf16 v[192:195], v[148:151], v[172:175], v[192:195]\n\t" \
    "s_waitcnt lgkmcnt(1)\n\t" \
    "v_mfma_f32_16x16x32_bf16 v[188:191], v[72:75], v[176:179], v[188:191]\n\t" \
    "v_mfma_f32_16x16x32_bf16 v[192:195], v[152:155], v[176:179], v[192:195]\n\t" \
    "s_waitcnt lgkmcnt(0)\n\t" \
    "v_mfma_f32_16x16x32_bf16 v[188:191], v[76:79], v[180:183], v[188:191]\n\t" \
    "v_mfma_f32_16x16x32_bf16 v[192:195], v[156:159], v[180:183], v[192:195]\n\t" \
    "s_setprio 2\n\t" \
    "buffer_load_dwordx4 v[232:235], %[l16], %[rs], %[n0] offen offset:0 sc1\n\t" \
    "buffer_load_dwordx4 v[236:239], %[l16], %[rs], %[n0] offen offset:1024 sc1\n\t" \
    "buffer_load_dwordx4 v[240:243], %[l16], %[rs], %[n0] offen offset:2048 sc1\n\t" \
    "buffer_load_dwordx4 v[244:247], %[l16], %[rs], %[n0] offen offset:3072 sc1\n\t" \
    "buffer_load_dwordx4 v[248:251], %[l16], %[rs], %[n1] offen offset:0 sc1\n\t" \
    ASM_ST3 \
    "v_fma_f32 v188, v188, v206, v208\n\t" \
    "v_fma_f32 v189, v189, v206, v209\n\t" \
    "v_fma_f32 v190, v190, v206, v210\n\t" \
    "v_fma_f32 v191, v191, v206, v211\n\t" \
    "v_mul_f32 v160, v188, v188\n\t" \
    "v_mul_f32 v161, v189, v189\n\t" \
    "v_mul_f32 v162, v190, v190\n\t" \
    "v_mul_f32 v163, v191, v191\n\t" \
    "v_fma_f32 v160, v160, %[k1], v220\n\t" \
    "v_fma_f32 v161, v161, %[k1], v220\n\t" \
    "v_fma_f32 v162, v162, %[k1], v220\n\t" \
    "v_fma_f32 v163, v163, %[k1], v220\n\t" \
    "v_mul_f32 v160, v188, v160\n\t" \
    "v_mul_f32 v161, v189, v161\n\t" \
    "v_mul_f32 v162, v190, v162\n\t" \
    "v_mul_f32 v163, v191, v163\n\t" \
    "v_fma_f32 v192, v192, v206, v212\n\t" \
    "v_fma_f32 v193, v193, v206, v213\n\t" \
    "v_fma_f32 v194, v194, v206, v214\n\t" \
    "v_fma_f32 v195, v195, v206, v215\n\t" \
    "v_mul_f32 v164, v192, v192\n\t" \
    "v_mul_f32 v165, v193, v193\n\t" \
    "v_mul_f32 v166, v194, v194\n\t" \
    "v_mul_f32 v167, v195, v195\n\t" \
    "v_fma_f32 v164, v164, %[k1], v220\n\t" \
    "v_fma_f32 v165, v165, %[k1], v220\n\t" \
    "v_fma_f32 v166, v166, %[k1], v220\n\t" \
    "v_fma_f32 v167, v167, %[k1], v220\n\t" \
    "v_mul_f32 v164, v192, v164\n\t" \
    "v_mul_f32 v165, v193, v165\n\t" \
    "v_mul_f32 v166, v194, v166\n\t" \
    "v_mul_f32 v167, v195, v167\n\t" \
    "v_exp_f32 v160, v160\n\t" \
    "v_exp_f32 v161, v161\n\t" \
    "v_exp_f32 v162, v162\n\t" \
    "v_exp_f32 v163, v163\n\t" \
    "v_exp_f32 v164, v164\n\t" \
    "v_exp_f32 v165, v165\n\t" \
    "v_exp_f32 v166, v166\n\t" \
    "v_exp_f32 v167, v167\n\t" \
    "v_add_f32 v160, 1.0, v160\n\t" \
    "v_add_f32 v161, 1.0, v161\n\t" \
    "v_add_f32 v162, 1.0, v162\n\t" \
    "v_add_f32 v163, 1.0, v163\n\t" \
    "v_add_f32 v164, 1.0, v164\n\t" \
    "v_add_f32 v165, 1.0, v165\n\t" \
    "v_add_f32 v166, 1.0, v166\n\t" \
    "v_add_f32 v167, 1.0, v167\n\t" \
    "v_rcp_f32 v160, v160\n\t" \
    "v_rcp_f32 v161, v161\n\t" \
    "v_rcp_f32 v162, v162\n\t" \
    "v_rcp_f32 v163, v163\n\t" \
    "v_rcp_f32 v164, v164\n\t" \
    "v_rcp_f32 v165, v165\n\t" \
    "v_rcp_f32 v166, v166\n\t" \
    "v_rcp_f32 v167, v167\n\t" \
    "v_mul_f32 v188, v188, v160\n\t" \
    "v_mul_f32 v189, v189, v161\n\t" \
    "v_mul_f32 v190, v190, v162\n\t" \
    "v_mul_f32 v191, v191, v163\n\t" \
    "v_mul_f32 v192, v192, v164\n\t" \
    "v_mul_f32 v193, v193, v165\n\t" \
    "v_mul_f32 v194, v194, v166\n\t" \
    "v_mul_f32 v195, v195, v167\n\t" \
    "v_cvt_pk_bf16_f32 v188, v188, v189\n\t" \
    "v_cvt_pk_bf16_f32 v189, v190, v191\n\t" \
    "v_cvt_pk_bf16_f32 v190, v192, v193\n\t" \
    "v_cvt_pk_bf16_f32 v191, v194, v195\n\t" \
    "v_min_u32 v188, -2, v188\n\t" \
    "v_min_u32 v189, -2, v189\n\t" \
    "v_min_u32 v190, -2, v190\n\t" \
    "v_min_u32 v191, -2, v191\n\t" \
    "v_mov_b32 v216, 0x3f803f80\n\t" \
    "v_mov_b32 v217, 0x3f803f80\n\t" \
    "v_mov_b32 v218, 0x3f803f80\n\t" \
    "v_mov_b32 v219, 0x3f803f80\n\t" \
    "v_permlane16_swap_b32 v188, v190\n\t" \
    "v_permlane16_swap_b32 v189, v191\n\t" \
    "s_waitcnt vmcnt(6)\n\t" \
    "buffer_store_dwordx4 v[188:191], %[oa], %[rs], %[so] offen sc1\n\t" \
    "s_nop 1\n\t" \
    "s_setprio 0\n\t"
#define ASM_WT1_HI \
    "s_setprio 1\n\t" \
    "s_barrier\n\t" ASM_ST \
    "ds_read_b128 v[160:163], %[la] offset:0\n\t" \
    "ds_read_b128 v[164:167], %[la] offset:1024\n\t" \
    "ds_read_b128 v[168:171], %[la] offset:2048\n\t" \
    "ds_read2st64_b32 v[204:205], %[sr] offset0:20 offset1:24\n\t" \
    "ds_read_b128 v[172:175], %[la] offset:3072\n\t" \
    "ds_read_b128 v[176:179], %[la] offset:8192\n\t" \
    "ds_read_b128 v[180:183], %[la] offset:9216\n\t" \
    "s_waitcnt lgkmcnt(6)\n\t" \
    "v_mfma_f32_16x16x32_bf16 v[188:191], v[0:3], v[160:163], 0\n\t" \
    "ds_read_b128 v[184:187], %[la] offset:10240\n\t" \
    "v_mfma_f32_16x16x32_bf16 v[192:195], v[80:83], v[160:163], 0\n\t" \
    "s_waitcnt lgkmcnt(6)\n\t" \
    "v_mfma_f32_16x16x32_bf16 v[188:191], v[4:7], v[164:167], v[188:191]\n\t" \
    "ds_read_b128 v[160:163], %[la] offset:11264\n\t" \
    "v_mfma_f32_16x16x32_bf16 v[192:195], v[84:87], v[164:167], v[192:195]\n\t" \
    "s_waitcnt lgkmcnt(6)\n\t" \
    "v_mfma_f32_16x16x32_bf16 v[188:191], v[8:11], v[168:171], v[188:191]\n\t" \
    "ds_read_b128 v[164:167], %[la] offset:16384\n\t" \
    "s_waitcnt lgkmcnt(6)\n\t" \
    "v_mfma_f32_16x16x32_bf16 v[192:195], v[88:91], v[168:171], v[192:195]\n\t" \
    "v_permlane16_swap_b32 v204, v205\n\t" \
    "s_waitcnt lgkmcnt(5)\n\t" \
    "v_mfma_f32_16x16x32_bf16 v[188:191], v[12:15], v[172:175], v[188:191]\n\t" \
    "ds_read_b128 v[168:171], %[la] offset:17408\n\t" \
    "v_mfma_f32_16x16x32_bf16 v[192:195], v[92:95], v[172:175], v[192:195]\n\t" \
    "v_add_f32 v206, v204, v205\n\t" \
    "s_waitcnt lgkmcnt(5)\n\t" \
    "v_mfma_f32_16x16x32_bf16 v[188:191], v[16:19], v[176:179], v[188:191]\n\t" \
    "ds_read_b128 v[172:175], %[la] offset:18432\n\t" \
    "v_add_f32 v207, v204, v205\n\t" \
    "v_mfma_f32_16x16x32_bf16 v[192:195], v[96:99], v[176:179], v[192:195]\n\t" \
    "s_waitcnt lgkmcnt(5)\n\t" \
    "v_mfma_f32_16x16x32_bf16 v[188:191], v[20:23], v[180:183], v[188:191]\n\t" \
    "ds_read_b128 v[176:179], %[la] offset:19456\n\t" \
    "v_permlane32_swap_b32 v206, v207\n\t" \
    "v_mfma_f32_16x16x32_bf16 v[192:195], v[100:103], v[180:183], v[192:195]\n\t" \
    "s_waitcnt lgkmcnt(5)\n\t" \
    "v_mfma_f32_16x16x32_bf16 v[188:191], v[24:27], v[184:187], v[188:191]\n\t" \
    "ds_read_b128 v[180:183], %[la] offset:24576\n\t" \
    "v_add_f32 v204, v206, v207\n\t" \
    "v_mfma_f32_16x16x32_bf16 v[192:195], v[104:107], v[184:187], v[192:195]\n\t" \
    "v_add_f32 v205, v206, v207\n\t" \
    "s_waitcnt lgkmcnt(5)\n\t" \
    "v_mfma_f32_16x16x32_bf16 v[188:191], v[28:31], v[160:163], v[188:191]\n\t" \
    "ds_read_b128 v[184:187], %[la] offset:25600\n\t" \
    "v_mfma_f32_16x16x32_bf16 v[192:195], v[108:111], v[160:163], v[192:195]\n\t" \
    "v_permlane16_swap_b32 v204, v205\n\t" \
    "s_waitcnt lgkmcnt(5)\n\t" \
    "v_mfma_f32_16x16x32_bf16 v[188:191], v[32:35], v[164:167], v[188:191]\n\t" \
    "ds_read_b128 v[160:163], %[la] offset:26624\n\t" \
    "v_mfma_f32_16x16x32_bf16 v[192:195], v[112:115], v[164:167], v[192:195]\n\t" \
    "v_mul_f32 v205, %[kd], v205\n\t" \
    "s_waitcnt lgkmcnt(5)\n\t" \
    "v_mfma_f32_16x16x32_bf16 v[188:191], v[36:39], v[168:171], v[188:191]\n\t" \
    "ds_read_b128 v[164:167], %[la] offset:27648\n\t" \
    "v_mul_f32 v204, %[kd], v204\n\t" \
    "v_mfma_f32_16x16x32_bf16 v[192:195], v[116:119], v[168:171], v[192:195]\n\t" \
    "v_fma_f32 v204, -v205, v205, v204\n\t" \
    "s_waitcnt lgkmcnt(5)\n\t" \
    "v_mfma_f32_16x16x32_bf16 v[188:191], v[40:43], v[172:175], v[188:191]\n\t" \
    "ds_read_b128 v[168:171], %[la] offset:4096\n\t" \
    "v_max_f32 v204, 0, v204\n\t" \
    "v_mfma_f32_16x16x32_bf16 v[192:195], v[120:123], v[172:175], v[192:195]\n\t" \
    "v_add_f32 v206, %[ke], v204\n\t" \
    "s_waitcnt lgkmcnt(5)\n\t" \
    "v_mfma_f32_16x16x32_bf16 v[188:191], v[44:47], v[176:179], v[188:191]\n\t" \
    "ds_read_b128 v[172:175], %[la] offset:12288\n\t" \
    "v_rsq_f32 v206, v206\n\t" \
    "v_mfma_f32_16x16x32_bf16 v[192:195], v[124:127], v[176:179], v[192:195]\n\t" \
    "s_waitcnt lgkmcnt(5)\n\t" \
    "v_mfma_f32_16x16x32_bf16 v[188:191], v[48:51], v[180:183], v[188:191]\n\t" \
    "ds_read_b128 v[176:179], %[la] offset:20480\n\t" \
    "v_mul_f32 v207, v204, v206\n\t" \
    "v_mfma_f32_16x16x32_bf16 v[192:195], v[128:131], v[180:183], v[192:195]\n\t" \
    "v_fma_f32 v207, v207, v206, %[ke]\n\t" \
    "s_waitcnt lgkmcnt(5)\n\t" \
    "v_mfma_f32_16x16x32_bf16 v[188:191], v[52:55], v[184:187], v[188:191]\n\t" \
    "ds_read_b128 v[180:183], %[la] offset:28672\n\t" \
    "v_rsq_f32 v207, v207\n\t" \
    "v_mfma_f32_16x16x32_bf16 v[192:195], v[132:135], v[184:187], v[192:195]\n\t" \
    "v_mul_f32 v206, v206, v207\n\t" \
    "v_mul_f32 v207, -v205, v206\n\t" \
    "s_waitcnt lgkmcnt(5)\n\t" \
    "v_mfma_f32_16x16x32_bf16 v[188:191], v[56:59], v[160:163], v[188:191]\n\t" \
    "v_mul_f32 v208, v196, v207\n\t" \
    "v_mul_f32 v209, v197, v207\n\t" \
    "v_mfma_f32_16x16x32_bf16 v[192:195], v[136:139], v[160:163], v[192:195]\n\t" \
    "v_mul_f32 v210, v198, v207\n\t" \
    "v_mul_f32 v211, v199, v207\n\t" \
    "s_waitcnt lgkmcnt(4)\n\t" \
    "v_mfma_f32_16x16x32_bf16 v[188:191], v[60:63], v[164:167], v[188:191]\n\t" \
    "v_mul_f32 v212, v200, v207\n\t" \
    "v_mul_f32 v213, v201, v207\n\t" \
    "v_mfma_f32_16x16x32_bf16 v[192:195], v[140:143], v[164:167], v[192:195]\n\t" \
    "v_mul_f32 v214, v202, v207\n\t" \
    "v_mul_f32 v215, v203, v207\n\t" \
    "s_waitcnt lgkmcnt(3)\n\t" \
    "v_mfma_f32_16x16x32_bf16 v[188:191], v[64:67], v[168:171], v[188:191]\n\t" \
    "v_mov_b64 v[216:217], -1\n\t" \
    "v_mov_b64 v[218:219], -1\n\t" \
    "v_mfma_f32_16x16x32_bf16 v[192:195], v[144:147], v[168:171], v[192:195]\n\t" \
    "buffer_store_dwordx4 v[216:219], %[ob], %[rs], %[sa] offen sc1\n\t" \
    "s_waitcnt lgkmcnt(2)\n\t" \
    "v_mfma_f32_16x16x32_bf16 v[188:191], v[68:71], v[172:175], v[188:191]\n\t" \
    "v_mfma_f32_16x16x32_bf16 v[192:195], v[148:151], v[172:175], v[192:195]\n\t" \
    "s_waitcnt lgkmcnt(1)\n\t" \
    "v_mfma_f32_16x16x32_bf16 v[188:191], v[72:75], v[176:179], v[188:191]\n\t" \
    "v_mfma_f32_16x16x32_bf16 v[192:195], v[152:155], v[176:179], v[192:195]\n\t" \
    "s_waitcnt lgkmcnt(0)\n\t" \
    "v_mfma_f32_16x16x32_bf16 v[188:191], v[76:79], v[180:183], v[188:191]\n\t" \
    "v_mfma_f32_16x16x32_bf16 v[192:195], v[156:159], v[180:183], v[192:195]\n\t" \
    ASM_ST3 \
    "s_nop 4\n\t" \
    "v_fma_f32 v188, v188, v206, v208\n\t" \
    "v_fma_f32 v189, v189, v206, v209\n\t" \
    "v_fma_f32 v190, v190, v206, v210\n\t" \
    "v_fma_f32 v191, v191, v206, v211\n\t" \
    "v_mul_f32 v160, v188, v188\n\t" \
    "v_mul_f32 v161, v189, v189\n\t" \
    "v_mul_f32 v162, v190, v190\n\t" \
    "v_mul_f32 v163, v191, v191\n\t" \
    "v_fma_f32 v160, v160, %[k1], v220\n\t" \
    "v_fma_f32 v161, v161, %[k1], v220\n\t" \
    "v_fma_f32 v162, v162, %[k1], v220\n\t" \
    "v_fma_f32 v163, v163, %[k1], v220\n\t" \
    "v_mul_f32 v160, v188, v160\n\t" \
    "v_mul_f32 v161, v189, v161\n\t" \
    "v_mul_f32 v162, v190, v162\n\t" \
    "v_mul_f32 v163, v191, v163\n\t" \
    "v_fma_f32 v192, v192, v206, v212\n\t" \
    "v_fma_f32 v193, v193, v206, v213\n\t" \
    "v_fma_f32 v194, v194, v206, v214\n\t" \
    "v_fma_f32 v195, v195, v206, v215\n\t" \
    "v_mul_f32 v164, v192, v192\n\t" \
    "v_mul_f32 v165, v193, v193\n\t" \
    "v_mul_f32 v166, v194, v194\n\t" \
    "v_mul_f32 v167, v195, v195\n\t" \
    "v_fma_f32 v164, v164, %[k1], v220\n\t" \
    "v_fma_f32 v165, v165, %[k1], v220\n\t" \
    "v_fma_f32 v166, v166, %[k1], v220\n\t" \
    "v_fma_f32 v167, v167, %[k1], v220\n\t" \
    "v_mul_f32 v164, v192, v164\n\t" \
    "v_mul_f32 v165, v193, v165\n\t" \
    "v_mul_f32 v166, v194, v166\n\t" \
    "v_mul_f32 v167, v195, v167\n\t" \
    "v_exp_f32 v160, v160\n\t" \
    "v_exp_f32 v161, v161\n\t" \
    "v_exp_f32 v162, v162\n\t" \
    "v_exp_f32 v163, v163\n\t" \
    "v_exp_f32 v164, v164\n\t" \
    "v_exp_f32 v165, v165\n\t" \
    "v_exp_f32 v166, v166\n\t" \
    "v_exp_f32 v167, v167\n\t" \
    "v_add_f32 v160, 1.0, v160\n\t" \
    "v_add_f32 v161, 1.0, v161\n\t" \
    "v_add_f32 v162, 1.0, v162\n\t" \
    "v_add_f32 v163, 1.0, v163\n\t" \
    "v_add_f32 v164, 1.0, v164\n\t" \
    "v_add_f32 v165, 1.0, v165\n\t" \
    "v_add_f32 v166, 1.0, v166\n\t" \
    "v_add_f32 v167, 1.0, v167\n\t" \
    "v_rcp_f32 v160, v160\n\t" \
    "v_rcp_f32 v161, v161\n\t" \
    "v_rcp_f32 v162, v162\n\t" \
    "v_rcp_f32 v163, v163\n\t" \
    "v_rcp_f32 v164, v164\n\t" \
    "v_rcp_f32 v165, v165\n\t" \
    "v_rcp_f32 v166, v166\n\t" \
    "v_rcp_f32 v167, v167\n\t" \
    "v_mul_f32 v188, v188, v160\n\t" \
    "v_mul_f32 v189, v189, v161\n\t" \
    "v_mul_f32 v190, v190, v162\n\t" \
    "v_mul_f32 v191, v191, v163\n\t" \
    "v_mul_f32 v192, v192, v164\n\t" \
    "v_mul_f32 v193, v193, v165\n\t" \
    "v_mul_f32 v194, v194, v166\n\t" \
    "v_mul_f32 v195, v195, v167\n\t" \
    "v_cvt_pk_bf16_f32 v188, v188, v189\n\t" \
    "v_cvt_pk_bf16_f32 v189, v190, v191\n\t" \
    "v_cvt_pk_bf16_f32 v190, v192, v193\n\t" \
    "v_cvt_pk_bf16_f32 v191, v194, v195\n\t" \
    "v_min_u32 v188, -2, v188\n\t" \
    "v_min_u32 v189, -2, v189\n\t" \
    "v_min_u32 v190, -2, v190\n\t" \
    "v_min_u32 v191, -2, v191\n\t" \
    "s_nop 1\n\t" \
    "v_permlane16_swap_b32 v188, v190\n\t" \
    "v_permlane16_swap_b32 v189, v191\n\t" \
    "s_waitcnt vmcnt(1)\n\t" \
    "buffer_store_dwordx4 v[188:191], %[oa], %[rs], %[so] offen sc1\n\t" \
    "s_nop 1\n\t"
#define ASM_WT2_LO \
    "s_branch .Lt0%=\n\t" \
    ".Ltr%=:\n\t" \
    "s_cmp_gt_i32 %[n], 0\n\t" \
    "s_cbranch_scc0 .Ltg%=\n\t" \
    ".Lfw%=:\n\t" \
    "s_mov_b32 %[a], 0\n\t" \
    "v_max_u32 v252, v232, v233\n\t" \
    "v_max3_u32 v252, v234, v235, v252\n\t" \
    "v_cmp_eq_u32 vcc, -1, v252\n\t" \
    "s_cbranch_vccz .Lgw0_%=\n\t" \
    "buffer_load_dwordx4 v[232:235], %[l16], %[rs], %[i0] offen offset:0 sc1\n\t" \
    "s_mov_b32 %[a], 1\n\t" \
    ".Lgw0_%=:\n\t" \
    "v_max_u32 v252, v236, v237\n\t" \
    "v_max3_u32 v252, v238, v239, v252\n\t" \
    "v_cmp_eq_u32 vcc, -1, v252\n\t" \
    "s_cbranch_vccz .Lgw1_%=\n\t" \
    "buffer_load_dwordx4 v[236:239], %[l16], %[rs], %[i0] offen offset:1024 sc1\n\t" \
    "s_mov_b32 %[a], 1\n\t" \
    ".Lgw1_%=:\n\t" \
    "v_max_u32 v252, v240, v241\n\t" \
    "v_max3_u32 v252, v242, v243, v252\n\t" \
    "v_cmp_eq_u32 vcc, -1, v252\n\t" \
    "s_cbranch_vccz .Lgw2_%=\n\t" \
    "buffer_load_dwordx4 v[240:243], %[l16], %[rs], %[i0] offen offset:2048 sc1\n\t" \
    "s_mov_b32 %[a], 1\n\t" \
    ".Lgw2_%=:\n\t" \
    "v_max_u32 v252, v244, v245\n\t" \
    "v_max3_u32 v252, v246, v247, v252\n\t" \
    "v_cmp_eq_u32 vcc, -1, v252\n\t" \
    "s_cbranch_vccz .Lgw3_%=\n\t" \
    "buffer_load_dwordx4 v[244:247], %[l16], %[rs], %[i0] offen offset:3072 sc1\n\t" \
    "s_mov_b32 %[a], 1\n\t" \
    ".Lgw3_%=:\n\t" \
    "v_max_u32 v252, v248, v249\n\t" \
    "v_max3_u32 v252, v250, v251, v252\n\t" \
    "v_cmp_eq_u32 vcc, -1, v252\n\t" \
    "s_cbranch_vccz .Lgw4_%=\n\t" \
    "buffer_load_dwordx4 v[248:251], %[l16], %[rs], %[i1] offen offset:0 sc1\n\t" \
    "s_mov_b32 %[a], 1\n\t" \
    ".Lgw4_%=:\n\t" \
    "s_cmp_eq_u32 %[a], 0\n\t" \
    "s_cbranch_scc1 .Lhw%=\n\t" \
    "s_waitcnt vmcnt(20)\n\t" \
    ".Liw%=:\n\t" \
    "v_max_u32 v252, v232, v233\n\t" \
    "v_max3_u32 v252, v234, v235, v252\n\t" \
    "v_max3_u32 v252, v236, v237, v252\n\t" \
    "v_max3_u32 v252, v238, v239, v252\n\t" \
    "v_max3_u32 v252, v240, v241, v252\n\t" \
    "v_max3_u32 v252, v242, v243, v252\n\t" \
    "v_max3_u32 v252, v244, v245, v252\n\t" \
    "v_max3_u32 v252, v246, v247, v252\n\t" \
    "v_max3_u32 v252, v248, v249, v252\n\t" \
    "v_max3_u32 v252, v250, v251, v252\n\t" \
    "v_cmp_eq_u32 vcc, -1, v252\n\t" \
    "s_cbranch_vccz .Lhw%=\n\t" \
    "s_sleep 1\n\t" \
    "v_max_u32 v252, v232, v233\n\t" \
    "v_max3_u32 v252, v234, v235, v252\n\t" \
    "v_max3_u32 v252, v236, v237, v252\n\t" \
    "v_max3_u32 v252, v238, v239, v252\n\t" \
    "v_max3_u32 v252, v240, v241, v252\n\t" \
    "v_max3_u32 v252, v242, v243, v252\n\t" \
    "v_max3_u32 v252, v244, v245, v252\n\t" \
    "v_max3_u32 v252, v246, v247, v252\n\t" \
    "v_max3_u32 v252, v248, v249, v252\n\t" \
    "v_max3_u32 v252, v250, v251, v252\n\t" \
    "v_cmp_eq_u32 vcc, -1, v252\n\t" \
    "s_cbranch_vccz .Lhw%=\n\t" \
    "s_sleep 1\n\t" \
    "v_max_u32 v252, v232, v233\n\t" \
    "v_max3_u32 v252, v234, v235, v252\n\t" \
    "v_max3_u32 v252, v236, v237, v252\n\t" \
    "v_max3_u32 v252, v238, v239, v252\n\t" \
    "v_max3_u32 v252, v240, v241, v252\n\t" \
    "v_max3_u32 v252, v242, v243, v252\n\t" \
    "v_max3_u32 v252, v244, v245, v252\n\t" \
    "v_max3_u32 v252, v246, v247, v252\n\t" \
    "v_max3_u32 v252, v248, v249, v252\n\t" \
    "v_max3_u32 v252, v250, v251, v252\n\t" \
    "v_cmp_eq_u32 vcc, -1, v252\n\t" \
    "s_cbranch_vccz .Lhw%=\n\t" \
    "s_sleep 1\n\t" \
    "v_max_u32 v252, v232, v233\n\t" \
    "v_max3_u32 v252, v234, v235, v252\n\t" \
    "v_max3_u32 v252, v236, v237, v252\n\t" \
    "v_max3_u32 v252, v238, v239, v252\n\t" \
    "v_max3_u32 v252, v240, v241, v252\n\t" \
    "v_max3_u32 v252, v242, v243, v252\n\t" \
    "v_max3_u32 v252, v244, v245, v252\n\t" \
    "v_max3_u32 v252, v246, v247, v252\n\t" \
    "v_max3_u32 v252, v248, v249, v252\n\t" \
    "v_max3_u32 v252, v250, v251, v252\n\t" \
    "v_cmp_eq_u32 vcc, -1, v252\n\t" \
    "s_cbranch_vccz .Lhw%=\n\t" \
    "s_sleep 1\n\t" \
    "v_max_u32 v252, v232, v233\n\t" \
    "v_max3_u32 v252, v234, v235, v252\n\t" \
    "v_max3_u32 v252, v236, v237, v252\n\t" \
    "v_max3_u32 v252, v238, v239, v252\n\t" \
    "v_max3_u32 v252, v240, v241, v252\n\t" \
    "v_max3_u32 v252, v242, v243, v252\n\t" \
    "v_max3_u32 v252, v244, v245, v252\n\t" \
    "v_max3_u32 v252, v246, v247, v252\n\t" \
    "v_max3_u32 v252, v248, v249, v252\n\t" \
    "v_max3_u32 v252, v250, v251, v252\n\t" \
    "v_cmp_eq_u32 vcc, -1, v252\n\t" \
    "s_cbranch_vccz .Lhw%=\n\t" \
    "s_sleep 1\n\t" \
    "v_max_u32 v252, v232, v233\n\t" \
    "v_max3_u32 v252, v234, v235, v252\n\t" \
    "v_max3_u32 v252, v236, v237, v252\n\t" \
    "v_max3_u32 v252, v238, v239, v252\n\t" \
    "v_max3_u32 v252, v240, v241, v252\n\t" \
    "v_max3_u32 v252, v242, v243, v252\n\t" \
    "v_max3_u32 v252, v244, v245, v252\n\t" \
    "v_max3_u32 v252, v246, v247, v252\n\t" \
    "v_max3_u32 v252, v248, v249, v252\n\t" \
    "v_max3_u32 v252, v250, v251, v252\n\t" \
    "v_cmp_eq_u32 vcc, -1, v252\n\t" \
    "s_cbranch_vccz .Lhw%=\n\t" \
    "s_sleep 1\n\t" \
    "s_sub_u32 %[n], %[n], 1\n\t" \
    "s_cmp_gt_i32 %[n], 0\n\t" \
    "s_cbranch_scc1 .Lfw%=\n\t" \
    ".Lhw%=:\n\t" \
    "s_branch .Lta%=\n\t" \
    ".Lt0%=:\n\t" \
    "s_waitcnt vmcnt(1)\n\t" \
    ".Lta%=:\n\t" \
    "ds_write_b128 %[lw], v[232:235] offset:0\n\t" \
    "v_max_u32 v252, v232, v233\n\t" \
    "v_max3_u32 v252, v234, v235, v252\n\t" \
    "ds_write_b128 %[lw], v[236:239] offset:1024\n\t" \
    "v_max3_u32 v252, v236, v237, v252\n\t" \
    "v_max3_u32 v252, v238, v239, v252\n\t" \
    "ds_write_b128 %[lw], v[240:243] offset:2048\n\t" \
    "v_max3_u32 v252, v240, v241, v252\n\t" \
    "v_max3_u32 v252, v242, v243, v252\n\t" \
    "ds_write_b128 %[lw], v[244:247] offset:3072\n\t" \
    "v_max3_u32 v252, v244, v245, v252\n\t" \
    "v_max3_u32 v252, v246, v247, v252\n\t" \
    "ds_write_b128 %[lw], v[248:251] offset:4096\n\t" \
    "v_max3_u32 v252, v248, v249, v252\n\t" \
    "v_max3_u32 v252, v250, v251, v252\n\t" \
    "v_cmp_eq_u32 vcc, -1, v252\n\t" \
    "s_cbranch_vccnz .Ltr%=\n\t" \
    ".Ltg%=:\n\t" \
    ASM_ST2 \
    "s_waitcnt lgkmcnt(0)\n\t" \
    "s_setprio 2\n\t" \
    "s_barrier\n\t" ASM_ST \
    "ds_read_b128 v[160:163], %[la] offset:0\n\t" \
    "ds_read_b128 v[164:167], %[la] offset:1024\n\t" \
    "ds_read_b128 v[168:171], %[la] offset:2048\n\t" \
    "ds_read_b128 v[172:175], %[la] offset:3072\n\t" \
    "ds_read_b128 v[176:179], %[la] offset:8192\n\t" \
    "ds_read_b128 v[180:183], %[la] offset:9216\n\t" \
    "s_waitcnt lgkmcnt(5)\n\t" \
    "v_mfma_f32_16x16x32_bf16 v[188:191], v[0:3], v[160:163], 0\n\t" \
    "ds_read_b128 v[184:187], %[la] offset:10240\n\t" \
    "buffer_load_dwordx2 v[196:197], %[ro], %[rs], %[sx] offen sc1\n\t" \
    "v_mfma_f32_16x16x32_bf16 v[192:195], v[80:83], v[160:163], 0\n\t" \
    "buffer_load_dwordx2 v[198:199], %[ro], %[rs], %[sx] offen offset:512 sc1\n\t" \
    "s_waitcnt lgkmcnt(5)\n\t" \
    "v_mfma_f32_16x16x32_bf16 v[188:191], v[4:7], v[164:167], v[188:191]\n\t" \
    "ds_read_b128 v[160:163], %[la] offset:11264\n\t" \
    "v_lshrrev_b32 v204, 1, %[l16]\n\t" \
    "v_mfma_f32_16x16x32_bf16 v[192:195], v[84:87], v[164:167], v[192:195]\n\t" \
    "v_and_b32 v204, 0x78, v204\n\t" \
    "s_waitcnt lgkmcnt(5)\n\t" \
    "v_mfma_f32_16x16x32_bf16 v[188:191], v[8:11], v[168:171], v[188:191]\n\t" \
    "ds_read_b128 v[164:167], %[la] offset:16384\n\t" \
    "buffer_load_dwordx2 v[200:201], v204, %[rs], %[ss] offen sc1\n\t" \
    "v_mfma_f32_16x16x32_bf16 v[192:195], v[88:91], v[168:171], v[192:195]\n\t" \
    "v_mov_b64 v[216:217], -1\n\t" \
    "s_waitcnt lgkmcnt(5)\n\t" \
    "v_mfma_f32_16x16x32_bf16 v[188:191], v[12:15], v[172:175], v[188:191]\n\t" \
    "ds_read_b128 v[168:171], %[la] offset:17408\n\t" \
    "v_mov_b64 v[218:219], -1\n\t" \
    "v_mfma_f32_16x16x32_bf16 v[192:195], v[92:95], v[172:175], v[192:195]\n\t" \
    "buffer_store_dwordx4 v[216:219], %[l16], %[rs], %[sa] offen sc1\n\t" \
    "s_waitcnt lgkmcnt(5)\n\t" \
    "v_mfma_f32_16x16x32_bf16 v[188:191], v[16:19], v[176:179], v[188:191]\n\t" \
    "ds_read_b128 v[172:175], %[la] offset:18432\n\t" \
    "v_mfma_f32_16x16x32_bf16 v[192:195], v[96:99], v[176:179], v[192:195]\n\t" \
    "s_waitcnt lgkmcnt(5)\n\t" \
    "v_mfma_f32_16x16x32_bf16 v[188:191], v[20:23], v[180:183], v[188:191]\n\t" \
    "ds_read_b128 v[176:179], %[la] offset:19456\n\t" \
    "v_mfma_f32_16x16x32_bf16 v[192:195], v[100:103], v[180:183], v[192:195]\n\t" \
    "s_waitcnt lgkmcnt(5)\n\t" \
    "v_mfma_f32_16x16x32_bf16 v[188:191], v[24:27], v[184:187], v[188:191]\n\t" \
    "ds_read_b128 v[180:183], %[la] offset:24576\n\t" \
    "v_mfma_f32_16x16x32_bf16 v[192:195], v[104:107], v[184:187], v[192:195]\n\t" \
    "s_waitcnt lgkmcnt(5)\n\t" \
    "v_mfma_f32_16x16x32_bf16 v[188:191], v[28:31], v[160:163], v[188:191]\n\t" \
    "ds_read_b128 v[184:187], %[la] offset:25600\n\t" \
    "v_mfma_f32_16x16x32_bf16 v[192:195], v[108:111], v[160:163], v[192:195]\n\t" \
    "s_waitcnt lgkmcnt(5)\n\t" \
    "v_mfma_f32_16x16x32_bf16 v[188:191], v[32:35], v[164:167], v[188:191]\n\t" \
    "ds_read_b128 v[160:163], %[la] offset:26624\n\t" \
    "v_mfma_f32_16x16x32_bf16 v[192:195], v[112:115], v[164:167], v[192:195]\n\t" \
    "s_waitcnt lgkmcnt(5)\n\t" \
    "v_mfma_f32_16x16x32_bf16 v[188:191], v[36:39], v[168:171], v[188:191]\n\t" \
    "ds_read_b128 v[164:167], %[la] offset:27648\n\t" \
    "v_mfma_f32_16x16x32_bf16 v[192:195], v[116:119], v[168:171], v[192:195]\n\t" \
    "s_waitcnt lgkmcnt(5)\n\t" \
    "v_mfma_f32_16x16x32_bf16 v[188:191], v[40:43], v[172:175], v[188:191]\n\t" \
    "ds_read_b128 v[168:171], %[la] offset:4096\n\t" \
    "v_mfma_f32_16x16x32_bf16 v[192:195], v[120:123], v[172:175], v[192:195]\n\t" \
    "s_waitcnt lgkmcnt(5)\n\t" \
    "v_mfma_f32_16x16x32_bf16 v[188:191], v[44:47], v[176:179], v[188:191]\n\t" \
    "ds_read_b128 v[172:175], %[la] offset:12288\n\t" \
    "v_mfma_f32_16x16x32_bf16 v[192:195], v[124:127], v[176:179], v[192:195]\n\t" \
    "s_waitcnt lgkmcnt(5)\n\t" \
    "v_mfma_f32_16x16x32_bf16 v[188:191], v[48:51], v[180:183], v[188:191]\n\t" \
    "ds_read_b128 v[176:179], %[la] offset:20480\n\t" \
    "v_mfma_f32_16x16x32_bf16 v[192:195], v[128:131], v[180:183], v[192:195]\n\t" \
    "s_waitcnt lgkmcnt(5)\n\t" \
    "v_mfma_f32_16x16x32_bf16 v[188:191], v[52:55], v[184:187], v[188:191]\n\t" \
    "ds_read_b128 v[180:183], %[la] offset:28672\n\t" \
    "v_mfma_f32_16x16x32_bf16 v[192:195], v[132:135], v[184:187], v[192:195]\n\t" \
    "s_waitcnt lgkmcnt(5)\n\t" \
    "v_mfma_f32_16x16x32_bf16 v[188:191], v[56:59], v[160:163], v[188:191]\n\t" \
    "v_mfma_f32_16x16x32_bf16 v[192:195], v[136:139], v[160:163], v[192:195]\n\t" \
    "s_waitcnt lgkmcnt(4)\n\t" \
    "v_mfma_f32_16x16x32_bf16 v[188:191], v[60:63], v[164:167], v[188:191]\n\t" \
    "v_mfma_f32_16x16x32_bf16 v[192:195], v[140:143], v[164:167], v[192:195]\n\t" \
    "s_waitcnt lgkmcnt(3)\n\t" \
    "v_mfma_f32_16x16x32_bf16 v[188:191], v[64:67], v[168:171], v[188:191]\n\t" \
    "v_mfma_f32_16x16x32_bf16 v[192:195], v[144:147], v[168:171], v[192:195]\n\t" \
    "s_waitcnt lgkmcnt(2)\n\t" \
    "v_mfma_f32_16x16x32_bf16 v[188:191], v[68:71], v[172:175], v[188:191]\n\t" \
    "v_mfma_f32_16x16x32_bf16 v[192:195], v[148:151], v[172:175], v[192:195]\n\t" \
    "s_waitcnt lgkmcnt(1)\n\t" \
    "v_mfma_f32_16x16x32_bf16 v[188:191], v[72:75], v[176:179], v[188:191]\n\t" \
    "v_mfma_f32_16x16x32_bf16 v[192:195], v[152:155], v[176:179], v[192:195]\n\t" \
    "s_waitcnt lgkmcnt(0)\n\t" \
    "v_mfma_f32_16x16x32_bf16 v[188:191], v[76:79], v[180:183], v[188:191]\n\t" \
    "v_mfma_f32_16x16x32_bf16 v[192:195], v[156:159], v[180:183], v[192:195]\n\t" \
    "s_setprio 2\n\t" \
    "buffer_load_dwordx4 v[232:235], %[l16], %[rs], %[n0] offen offset:0 sc1\n\t" \
    "buffer_load_dwordx4 v[236:239], %[l16], %[rs], %[n0] offen offset:1024 sc1\n\t" \
    "buffer_load_dwordx4 v[240:243], %[l16], %[rs], %[n0] offen offset:2048 sc1\n\t" \
    "buffer_load_dwordx4 v[244:247], %[l16], %[rs], %[n0] offen offset:3072 sc1\n\t" \
    "buffer_load_dwordx4 v[248:251], %[l16], %[rs], %[n1] offen offset:0 sc1\n\t" \
    ASM_ST3 \
    "s_waitcnt vmcnt(6)\n\t" \
    "v_lshlrev_b32 v160, 16, v196\n\t" \
    "v_and_b32 v161, 0xffff0000, v196\n\t" \
    "v_lshlrev_b32 v162, 16, v197\n\t" \
    "v_and_b32 v163, 0xffff0000, v197\n\t" \
    "v_lshlrev_b32 v164, 16, v198\n\t" \
    "v_and_b32 v165, 0xffff0000, v198\n\t" \
    "v_lshlrev_b32 v166, 16, v199\n\t" \
    "v_and_b32 v167, 0xffff0000, v199\n\t" \
    "v_sub_f32 v160, v160, v200\n\t" \
    "v_sub_f32 v161, v161, v200\n\t" \
    "v_sub_f32 v162, v162, v200\n\t" \
    "v_sub_f32 v163, v163, v200\n\t" \
    "v_sub_f32 v164, v164, v200\n\t" \
    "v_sub_f32 v165, v165, v200\n\t" \
    "v_sub_f32 v166, v166, v200\n\t" \
    "v_sub_f32 v167, v167, v200\n\t" \
    "v_mul_f32 v160, v160, v201\n\t" \
    "v_mul_f32 v161, v161, v201\n\t" \
    "v_mul_f32 v162, v162, v201\n\t" \
    "v_mul_f32 v163, v163, v201\n\t" \
    "v_mul_f32 v164, v164, v201\n\t" \
    "v_mul_f32 v165, v165, v201\n\t" \
    "v_mul_f32 v166, v166, v201\n\t" \
    "v_mul_f32 v167, v167, v201\n\t" \
    "v_cvt_pk_bf16_f32 v160, v160, v161\n\t" \
    "v_cvt_pk_bf16_f32 v162, v162, v163\n\t" \
    "v_cvt_pk_bf16_f32 v164, v164, v165\n\t" \
    "v_cvt_pk_bf16_f32 v166, v166, v167\n\t" \
    "v_and_b32 v161, 0xffff0000, v160\n\t" \
    "v_lshlrev_b32 v160, 16, v160\n\t" \
    "v_and_b32 v163, 0xffff0000, v162\n\t" \
    "v_lshlrev_b32 v162, 16, v162\n\t" \
    "v_and_b32 v165, 0xffff0000, v164\n\t" \
    "v_lshlrev_b32 v164, 16, v164\n\t" \
    "v_and_b32 v167, 0xffff0000, v166\n\t" \
    "v_lshlrev_b32 v166, 16, v166\n\t" \
    "v_add_f32 v188, v188, v160\n\t" \
    "v_add_f32 v189, v189, v161\n\t" \
    "v_add_f32 v190, v190, v162\n\t" \
    "v_add_f32 v191, v191, v163\n\t" \
    "v_add_f32 v192, v192, v164\n\t" \
    "v_add_f32 v193, v193, v165\n\t" \
    "v_add_f32 v194, v194, v166\n\t" \
    "v_add_f32 v195, v195, v167\n\t" \
    "v_cvt_pk_bf16_f32 v188, v188, v189\n\t" \
    "v_cvt_pk_bf16_f32 v189, v190, v191\n\t" \
    "v_cvt_pk_bf16_f32 v190, v192, v193\n\t" \
    "v_cvt_pk_bf16_f32 v191, v194, v195\n\t" \
    "v_min_u32 v188, -2, v188\n\t" \
    "v_min_u32 v189, -2, v189\n\t" \
    "v_min_u32 v190, -2, v190\n\t" \
    "v_min_u32 v191, -2, v191\n\t" \
    "s_nop 1\n\t" \
    "v_permlane16_swap_b32 v188, v190\n\t" \
    "v_permlane16_swap_b32 v189, v191\n\t" \
    "s_waitcnt vmcnt(6)\n\t" \
    "buffer_store_dwordx4 v[188:191], %[oa], %[rs], %[so] offen sc1\n\t" \
    "s_nop 1\n\t" \
    "s_setprio 0\n\t"
#define ASM_WT2_HI \
    "s_setprio 1\n\t" \
    "s_barrier\n\t" ASM_ST \
    "ds_read_b128 v[160:163], %[la] offset:0\n\t" \
    "ds_read_b128 v[164:167], %[la] offset:1024\n\t" \
    "ds_read_b128 v[168:171], %[la] offset:2048\n\t" \
    "ds_read_b128 v[172:175], %[la] offset:3072\n\t" \
    "ds_read_b128 v[176:179], %[la] offset:8192\n\t" \
    "ds_read_b128 v[180:183], %[la] offset:9216\n\t" \
    "s_waitcnt lgkmcnt(5)\n\t" \
    "v_mfma_f32_16x16x32_bf16 v[188:191], v[0:3], v[160:163], 0\n\t" \
    "ds_read_b128 v[184:187], %[la] offset:10240\n\t" \
    "buffer_load_dwordx2 v[196:197], %[ro], %[rs], %[sx] offen sc1\n\t" \
    "v_mfma_f32_16x16x32_bf16 v[192:195], v[80:83], v[160:163], 0\n\t" \
    "buffer_load_dwordx2 v[198:199], %[ro], %[rs], %[sx] offen offset:512 sc1\n\t" \
    "s_waitcnt lgkmcnt(5)\n\t" \
    "v_mfma_f32_16x16x32_bf16 v[188:191], v[4:7], v[164:167], v[188:191]\n\t" \
    "ds_read_b128 v[160:163], %[la] offset:11264\n\t" \
    "v_lshrrev_b32 v204, 1, %[l16]\n\t" \
    "v_mfma_f32_16x16x32_bf16 v[192:195], v[84:87], v[164:167], v[192:195]\n\t" \
    "v_and_b32 v204, 0x78, v204\n\t" \
    "s_waitcnt lgkmcnt(5)\n\t" \
    "v_mfma_f32_16x16x32_bf16 v[188:191], v[8:11], v[168:171], v[188:191]\n\t" \
    "ds_read_b128 v[164:167], %[la] offset:16384\n\t" \
    "buffer_load_dwordx2 v[200:201], v204, %[rs], %[ss] offen sc1\n\t" \
    "v_mfma_f32_16x16x32_bf16 v[192:195], v[88:91], v[168:171], v[192:195]\n\t" \
    "v_mov_b64 v[216:217], -1\n\t" \
    "s_waitcnt lgkmcnt(5)\n\t" \
    "v_mfma_f32_16x16x32_bf16 v[188:191], v[12:15], v[172:175], v[188:191]\n\t" \
    "ds_read_b128 v[168:171], %[la] offset:17408\n\t" \
    "v_mov_b64 v[218:219], -1\n\t" \
    "v_mfma_f32_16x16x32_bf16 v[192:195], v[92:95], v[172:175], v[192:195]\n\t" \
    "buffer_store_dwordx4 v[216:219], %[l16], %[rs], %[sa] offen sc1\n\t" \
    "s_waitcnt lgkmcnt(5)\n\t" \
    "v_mfma_f32_16x16x32_bf16 v[188:191], v[16:19], v[176:179], v[188:191]\n\t" \
    "ds_read_b128 v[172:175], %[la] offset:18432\n\t" \
    "v_mfma_f32_16x16x32_bf16 v[192:195], v[96:99], v[176:179], v[192:195]\n\t" \
    "s_waitcnt lgkmcnt(5)\n\t" \
    "v_mfma_f32_16x16x32_bf16 v[188:191], v[20:23], v[180:183], v[188:191]\n\t" \
    "ds_read_b128 v[176:179], %[la] offset:19456\n\t" \
    "v_mfma_f32_16x16x32_bf16 v[192:195], v[100:103], v[180:183], v[192:195]\n\t" \
    "s_waitcnt lgkmcnt(5)\n\t" \
    "v_mfma_f32_16x16x32_bf16 v[188:191], v[24:27], v[184:187], v[188:191]\n\t" \
    "ds_read_b128 v[180:183], %[la] offset:24576\n\t" \
    "v_mfma_f32_16x16x32_bf16 v[192:195], v[104:107], v[184:187], v[192:195]\n\t" \
    "s_waitcnt lgkmcnt(5)\n\t" \
    "v_mfma_f32_16x16x32_bf16 v[188:191], v[28:31], v[160:163], v[188:191]\n\t" \
    "ds_read_b128 v[184:187], %[la] offset:25600\n\t" \
    "v_mfma_f32_16x16x32_bf16 v[192:195], v[108:111], v[160:163], v[192:195]\n\t" \
    "s_waitcnt lgkmcnt(5)\n\t" \
    "v_mfma_f32_16x16x32_bf16 v[188:191], v[32:35], v[164:167], v[188:191]\n\t" \
    "ds_read_b128 v[160:163], %[la] offset:26624\n\t" \
    "v_mfma_f32_16x16x32_bf16 v[192:195], v[112:115], v[164:167], v[192:195]\n\t" \
    "s_waitcnt lgkmcnt(5)\n\t" \
    "v_mfma_f32_16x16x32_bf16 v[188:191], v[36:39], v[168:171], v[188:191]\n\t" \
    "ds_read_b128 v[164:167], %[la] offset:27648\n\t" \
    "v_mfma_f32_16x16x32_bf16 v[192:195], v[116:119], v[168:171], v[192:195]\n\t" \
    "s_waitcnt lgkmcnt(5)\n\t" \
    "v_mfma_f32_16x16x32_bf16 v[188:191], v[40:43], v[172:175], v[188:191]\n\t" \
    "ds_read_b128 v[168:171], %[la] offset:4096\n\t" \
    "v_mfma_f32_16x16x32_bf16 v[192:195], v[120:123], v[172:175], v[192:195]\n\t" \
    "s_waitcnt lgkmcnt(5)\n\t" \
    "v_mfma_f32_16x16x32_bf16 v[188:191], v[44:47], v[176:179], v[188:191]\n\t" \
    "ds_read_b128 v[172:175], %[la] offset:12288\n\t" \
    "v_mfma_f32_16x16x32_bf16 v[192:195], v[124:127], v[176:179], v[192:195]\n\t" \
    "s_waitcnt lgkmcnt(5)\n\t" \
    "v_mfma_f32_16x16x32_bf16 v[188:191], v[48:51], v[180:183], v[188:191]\n\t" \
    "ds_read_b128 v[176:179], %[la] offset:20480\n\t" \
    "v_mfma_f32_16x16x32_bf16 v[192:195], v[128:131], v[180:183], v[192:195]\n\t" \
    "s_waitcnt lgkmcnt(5)\n\t" \
    "v_mfma_f32_16x16x32_bf16 v[188:191], v[52:55], v[184:187], v[188:191]\n\t" \
    "ds_read_b128 v[180:183], %[la] offset:28672\n\t" \
    "v_mfma_f32_16x16x32_bf16 v[192:195], v[132:135], v[184:187], v[192:195]\n\t" \
    "s_waitcnt lgkmcnt(5)\n\t" \
    "v_mfma_f32_16x16x32_bf16 v[188:191], v[56:59], v[160:163], v[188:191]\n\t" \
    "v_mfma_f32_16x16x32_bf16 v[192:195], v[136:139], v[160:163], v[192:195]\n\t" \
    "s_waitcnt lgkmcnt(4)\n\t" \
    "v_mfma_f32_16x16x32_bf16 v[188:191], v[60:63], v[164:167], v[188:191]\n\t" \
    "v_mfma_f32_16x16x32_bf16 v[192:195], v[140:143], v[164:167], v[192:195]\n\t" \
    "s_waitcnt lgkmcnt(3)\n\t" \
    "v_mfma_f32_16x16x32_bf16 v[188:191], v[64:67], v[168:171], v[188:191]\n\t" \
    "v_mfma_f32_16x16x32_bf16 v[192:195], v[144:147], v[168:171], v[192:195]\n\t" \
    "s_waitcnt lgkmcnt(2)\n\t" \
    "v_mfma_f32_16x16x32_bf16 v[188:191], v[68:71], v[172:175], v[188:191]\n\t" \
    "v_mfma_f32_16x16x32_bf16 v[192:195], v[148:151], v[172:175], v[192:195]\n\t" \
    "s_waitcnt lgkmcnt(1)\n\t" \
    "v_mfma_f32_16x16x32_bf16 v[188:191], v[72:75], v[176:179], v[188:191]\n\t" \
    "v_mfma_f32_16x16x32_bf16 v[192:195], v[152:155], v[176:179], v[192:195]\n\t" \
    "s_waitcnt lgkmcnt(0)\n\t" \
    "v_mfma_f32_16x16x32_bf16 v[188:191], v[76:79], v[180:183], v[188:191]\n\t" \
    "v_mfma_f32_16x16x32_bf16 v[192:195], v[156:159], v[180:183], v[192:195]\n\t" \
    ASM_ST3 \
    "s_waitcnt vmcnt(1)\n\t" \
    "v_lshlrev_b32 v160, 16, v196\n\t" \
    "v_and_b32 v161, 0xffff0000, v196\n\t" \
    "v_lshlrev_b32 v162, 16, v197\n\t" \
    "v_and_b32 v163, 0xffff0000, v197\n\t" \
    "v_lshlrev_b32 v164, 16, v198\n\t" \
    "v_and_b32 v165, 0xffff0000, v198\n\t" \
    "v_lshlrev_b32 v166, 16, v199\n\t" \
    "v_and_b32 v167, 0xffff0000, v199\n\t" \
    "v_sub_f32 v160, v160, v200\n\t" \
    "v_sub_f32 v161, v161, v200\n\t" \
    "v_sub_f32 v162, v162, v200\n\t" \
    "v_sub_f32 v163, v163, v200\n\t" \
    "v_sub_f32 v164, v164, v200\n\t" \
    "v_sub_f32 v165, v165, v200\n\t" \
    "v_sub_f32 v166, v166, v200\n\t" \
    "v_sub_f32 v167, v167, v200\n\t" \
    "v_mul_f32 v160, v160, v201\n\t" \
    "v_mul_f32 v161, v161, v201\n\t" \
    "v_mul_f32 v162, v162, v201\n\t" \
    "v_mul_f32 v163, v163, v201\n\t" \
    "v_mul_f32 v164, v164, v201\n\t" \
    "v_mul_f32 v165, v165, v201\n\t" \
    "v_mul_f32 v166, v166, v201\n\t" \
    "v_mul_f32 v167, v167, v201\n\t" \
    "v_cvt_pk_bf16_f32 v160, v160, v161\n\t" \
    "v_cvt_pk_bf16_f32 v162, v162, v163\n\t" \
    "v_cvt_pk_bf16_f32 v164, v164, v165\n\t" \
    "v_cvt_pk_bf16_f32 v166, v166, v167\n\t" \
    "v_and_b32 v161, 0xffff0000, v160\n\t" \
    "v_lshlrev_b32 v160, 16, v160\n\t" \
    "v_and_b32 v163, 0xffff0000, v162\n\t" \
    "v_lshlrev_b32 v162, 16, v162\n\t" \
    "v_and_b32 v165, 0xffff0000, v164\n\t" \
    "v_lshlrev_b32 v164, 16, v164\n\t" \
    "v_and_b32 v167, 0xffff0000, v166\n\t" \
    "v_lshlrev_b32 v166, 16, v166\n\t" \
    "v_add_f32 v188, v188, v160\n\t" \
    "v_add_f32 v189, v189, v161\n\t" \
    "v_add_f32 v190, v190, v162\n\t" \
    "v_add_f32 v191, v191, v163\n\t" \
    "v_add_f32 v192, v192, v164\n\t" \
    "v_add_f32 v193, v193, v165\n\t" \
    "v_add_f32 v194, v194, v166\n\t" \
    "v_add_f32 v195, v195, v167\n\t" \
    "v_cvt_pk_bf16_f32 v188, v188, v189\n\t" \
    "v_cvt_pk_bf16_f32 v189, v190, v191\n\t" \
    "v_cvt_pk_bf16_f32 v190, v192, v193\n\t" \
    "v_cvt_pk_bf16_f32 v191, v194, v195\n\t" \
    "v_min_u32 v188, -2, v188\n\t" \
    "v_min_u32 v189, -2, v189\n\t" \
    "v_min_u32 v190, -2, v190\n\t" \
    "v_min_u32 v191, -2, v191\n\t" \
    "s_nop 1\n\t" \
    "v_permlane16_swap_b32 v188, v190\n\t" \
    "v_permlane16_swap_b32 v189, v191\n\t" \
    "s_waitcnt vmcnt(1)\n\t" \
    "buffer_store_dwordx4 v[188:191], %[oa], %[rs], %[so] offen sc1\n\t" \
    "s_nop 1\n\t"
#define ASM_WT3_LO \
    "s_branch .Lt0%=\n\t" \
    ".Ltr%=:\n\t" \
    "s_cmp_gt_i32 %[n], 0\n\t" \
    "s_cbranch_scc0 .Ltg%=\n\t" \
    ".Lfw%=:\n\t" \
    "s_mov_b32 %[a], 0\n\t" \
    "v_max_u32 v252, v232, v233\n\t" \
    "v_max3_u32 v252, v234, v235, v252\n\t" \
    "v_cmp_eq_u32 vcc, -1, v252\n\t" \
    "s_cbranch_vccz .Lgw0_%=\n\t" \
    "buffer_load_dwordx4 v[232:235], %[l16], %[rs], %[i0] offen offset:0 sc1\n\t" \
    "s_mov_b32 %[a], 1\n\t" \
    ".Lgw0_%=:\n\t" \
    "v_max_u32 v252, v236, v237\n\t" \
    "v_max3_u32 v252, v238, v239, v252\n\t" \
    "v_cmp_eq_u32 vcc, -1, v252\n\t" \
    "s_cbranch_vccz .Lgw1_%=\n\t" \
    "buffer_load_dwordx4 v[236:239], %[l16], %[rs], %[i0] offen offset:1024 sc1\n\t" \
    "s_mov_b32 %[a], 1\n\t" \
    ".Lgw1_%=:\n\t" \
    "v_max_u32 v252, v240, v241\n\t" \
    "v_max3_u32 v252, v242, v243, v252\n\t" \
    "v_cmp_eq_u32 vcc, -1, v252\n\t" \
    "s_cbranch_vccz .Lgw2_%=\n\t" \
    "buffer_load_dwordx4 v[240:243], %[l16], %[rs], %[i0] offen offset:2048 sc1\n\t" \
    "s_mov_b32 %[a], 1\n\t" \
    ".Lgw2_%=:\n\t" \
    "v_max_u32 v252, v244, v245\n\t" \
    "v_max3_u32 v252, v246, v247, v252\n\t" \
    "v_cmp_eq_u32 vcc, -1, v252\n\t" \
    "s_cbranch_vccz .Lgw3_%=\n\t" \
    "buffer_load_dwordx4 v[244:247], %[l16], %[rs], %[i0] offen offset:3072 sc1\n\t" \
    "s_mov_b32 %[a], 1\n\t" \
    ".Lgw3_%=:\n\t" \
    "v_max_u32 v252, v248, v249\n\t" \
    "v_max3_u32 v252, v250, v251, v252\n\t" \
    "v_cmp_eq_u32 vcc, -1, v252\n\t" \
    "s_cbranch_vccz .Lgw4_%=\n\t" \
    "buffer_load_dwordx4 v[248:251], %[l16], %[rs], %[i1] offen offset:0 sc1\n\t" \
    "s_mov_b32 %[a], 1\n\t" \
    ".Lgw4_%=:\n\t" \
    "s_cmp_eq_u32 %[a], 0\n\t" \
    "s_cbranch_scc1 .Lhw%=\n\t" \
    "s_waitcnt vmcnt(20)\n\t" \
    ".Liw%=:\n\t" \
    "v_max_u32 v252, v232, v233\n\t" \
    "v_max3_u32 v252, v234, v235, v252\n\t" \
    "v_max3_u32 v252, v236, v237, v252\n\t" \
    "v_max3_u32 v252, v238, v239, v252\n\t" \
    "v_max3_u32 v252, v240, v241, v252\n\t" \
    "v_max3_u32 v252, v242, v243, v252\n\t" \
    "v_max3_u32 v252, v244, v245, v252\n\t" \
    "v_max3_u32 v252, v246, v247, v252\n\t" \
    "v_max3_u32 v252, v248, v249, v252\n\t" \
    "v_max3_u32 v252, v250, v251, v252\n\t" \
    "v_cmp_eq_u32 vcc, -1, v252\n\t" \
    "s_cbranch_vccz .Lhw%=\n\t" \
    "s_sleep 1\n\t" \
    "v_max_u32 v252, v232, v233\n\t" \
    "v_max3_u32 v252, v234, v235, v252\n\t" \
    "v_max3_u32 v252, v236, v237, v252\n\t" \
    "v_max3_u32 v252, v238, v239, v252\n\t" \
    "v_max3_u32 v252, v240, v241, v252\n\t" \
    "v_max3_u32 v252, v242, v243, v252\n\t" \
    "v_max3_u32 v252, v244, v245, v252\n\t" \
    "v_max3_u32 v252, v246, v247, v252\n\t" \
    "v_max3_u32 v252, v248, v249, v252\n\t" \
    "v_max3_u32 v252, v250, v251, v252\n\t" \
    "v_cmp_eq_u32 vcc, -1, v252\n\t" \
    "s_cbranch_vccz .Lhw%=\n\t" \
    "s_sleep 1\n\t" \
    "v_max_u32 v252, v232, v233\n\t" \
    "v_max3_u32 v252, v234, v235, v252\n\t" \
    "v_max3_u32 v252, v236, v237, v252\n\t" \
    "v_max3_u32 v252, v238, v239, v252\n\t" \
    "v_max3_u32 v252, v240, v241, v252\n\t" \
    "v_max3_u32 v252, v242, v243, v252\n\t" \
    "v_max3_u32 v252, v244, v245, v252\n\t" \
    "v_max3_u32 v252, v246, v247, v252\n\t" \
    "v_max3_u32 v252, v248, v249, v252\n\t" \
    "v_max3_u32 v252, v250, v251, v252\n\t" \
    "v_cmp_eq_u32 vcc, -1, v252\n\t" \
    "s_cbranch_vccz .Lhw%=\n\t" \
    "s_sleep 1\n\t" \
    "v_max_u32 v252, v232, v233\n\t" \
    "v_max3_u32 v252, v234, v235, v252\n\t" \
    "v_max3_u32 v252, v236, v237, v252\n\t" \
    "v_max3_u32 v252, v238, v239, v252\n\t" \
    "v_max3_u32 v252, v240, v241, v252\n\t" \
    "v_max3_u32 v252, v242, v243, v252\n\t" \
    "v_max3_u32 v252, v244, v245, v252\n\t" \
    "v_max3_u32 v252, v246, v247, v252\n\t" \
    "v_max3_u32 v252, v248, v249, v252\n\t" \
    "v_max3_u32 v252, v250, v251, v252\n\t" \
    "v_cmp_eq_u32 vcc, -1, v252\n\t" \
    "s_cbranch_vccz .Lhw%=\n\t" \
    "s_sleep 1\n\t" \
    "v_max_u32 v252, v232, v233\n\t" \
    "v_max3_u32 v252, v234, v235, v252\n\t" \
    "v_max3_u32 v252, v236, v237, v252\n\t" \
    "v_max3_u32 v252, v238, v239, v252\n\t" \
    "v_max3_u32 v252, v240, v241, v252\n\t" \
    "v_max3_u32 v252, v242, v243, v252\n\t" \
    "v_max3_u32 v252, v244, v245, v252\n\t" \
    "v_max3_u32 v252, v246, v247, v252\n\t" \
    "v_max3_u32 v252, v248, v249, v252\n\t" \
    "v_max3_u32 v252, v250, v251, v252\n\t" \
    "v_cmp_eq_u32 vcc, -1, v252\n\t" \
    "s_cbranch_vccz .Lhw%=\n\t" \
    "s_sleep 1\n\t" \
    "v_max_u32 v252, v232, v233\n\t" \
    "v_max3_u32 v252, v234, v235, v252\n\t" \
    "v_max3_u32 v252, v236, v237, v252\n\t" \
    "v_max3_u32 v252, v238, v239, v252\n\t" \
    "v_max3_u32 v252, v240, v241, v252\n\t" \
    "v_max3_u32 v252, v242, v243, v252\n\t" \
    "v_max3_u32 v252, v244, v245, v252\n\t" \
    "v_max3_u32 v252, v246, v247, v252\n\t" \
    "v_max3_u32 v252, v248, v249, v252\n\t" \
    "v_max3_u32 v252, v250, v251, v252\n\t" \
    "v_cmp_eq_u32 vcc, -1, v252\n\t" \
    "s_cbranch_vccz .Lhw%=\n\t" \
    "s_sleep 1\n\t" \
    "s_sub_u32 %[n], %[n], 1\n\t" \
    "s_cmp_gt_i32 %[n], 0\n\t" \
    "s_cbranch_scc1 .Lfw%=\n\t" \
    ".Lhw%=:\n\t" \
    "s_branch .Lta%=\n\t" \
    ".Lt0%=:\n\t" \
    "s_waitcnt vmcnt(1)\n\t" \
    ".Lta%=:\n\t" \
    "ds_write_b128 %[lw], v[232:235] offset:0\n\t" \
    "v_max_u32 v252, v232, v233\n\t" \
    "v_max3_u32 v252, v234, v235, v252\n\t" \
    "v_mfma_f32_16x16x32_bf16 v[188:191], v[232:235], v[232:235], 0\n\t" \
    "v_mfma_f32_16x16x32_bf16 v[192:195], v[216:219], v[232:235], 0\n\t" \
    "ds_write_b128 %[lw], v[236:239] offset:1024\n\t" \
    "v_max3_u32 v252, v236, v237, v252\n\t" \
    "v_max3_u32 v252, v238, v239, v252\n\t" \
    "v_mfma_f32_16x16x32_bf16 v[188:191], v[236:239], v[236:239], v[188:191]\n\t" \
    "v_mfma_f32_16x16x32_bf16 v[192:195], v[216:219], v[236:239], v[192:195]\n\t" \
    "ds_write_b128 %[lw], v[240:243] offset:2048\n\t" \
    "v_max3_u32 v252, v240, v241, v252\n\t" \
    "v_max3_u32 v252, v242, v243, v252\n\t" \
    "v_mfma_f32_16x16x32_bf16 v[188:191], v[240:243], v[240:243], v[188:191]\n\t" \
    "v_mfma_f32_16x16x32_bf16 v[192:195], v[216:219], v[240:243], v[192:195]\n\t" \
    "ds_write_b128 %[lw], v[244:247] offset:3072\n\t" \
    "v_max3_u32 v252, v244, v245, v252\n\t" \
    "v_max3_u32 v252, v246, v247, v252\n\t" \
    "v_mfma_f32_16x16x32_bf16 v[188:191], v[244:247], v[244:247], v[188:191]\n\t" \
    "v_mfma_f32_16x16x32_bf16 v[192:195], v[216:219], v[244:247], v[192:195]\n\t" \
    "ds_write_b128 %[lw], v[248:251] offset:4096\n\t" \
    "v_max3_u32 v252, v248, v249, v252\n\t" \
    "v_max3_u32 v252, v250, v251, v252\n\t" \
    "v_mfma_f32_16x16x32_bf16 v[188:191], v[248:251], v[248:251], v[188:191]\n\t" \
    "v_mfma_f32_16x16x32_bf16 v[192:195], v[216:219], v[248:251], v[192:195]\n\t" \
    "v_cmp_eq_u32 vcc, -1, v252\n\t" \
    "s_cbranch_vccnz .Ltr%=\n\t" \
    ".Ltg%=:\n\t" \
    ASM_ST2 \
    "s_nop 5\n\t" \
    "ds_write_b128 %[lw], v[188:191] offset:5120\n\t" \
    "ds_write_b128 %[lw], v[192:195] offset:6144\n\t" \
    "s_waitcnt lgkmcnt(0)\n\t" \
    "s_setprio 2\n\t" \
    "s_barrier\n\t" ASM_ST \
    "ds_read_b128 v[160:163], %[la] offset:0\n\t" \
    "ds_read_b128 v[164:167], %[la] offset:1024\n\t" \
    "ds_read_b128 v[168:171], %[la] offset:2048\n\t" \
    "ds_read2st64_b32 v[204:205], %[sr] offset0:20 offset1:24\n\t" \
    "ds_read_b128 v[172:175], %[la] offset:3072\n\t" \
    "ds_read_b128 v[176:179], %[la] offset:8192\n\t" \
    "ds_read_b128 v[180:183], %[la] offset:9216\n\t" \
    "s_waitcnt lgkmcnt(6)\n\t" \
    "v_mfma_f32_16x16x32_bf16 v[188:191], v[0:3], v[160:163], 0\n\t" \
    "ds_read_b128 v[184:187], %[la] offset:10240\n\t" \
    "v_mfma_f32_16x16x32_bf16 v[192:195], v[80:83], v[160:163], 0\n\t" \
    "s_waitcnt lgkmcnt(6)\n\t" \
    "v_mfma_f32_16x16x32_bf16 v[188:191], v[4:7], v[164:167], v[188:191]\n\t" \
    "ds_read_b128 v[160:163], %[la] offset:11264\n\t" \
    "v_mfma_f32_16x16x32_bf16 v[192:195], v[84:87], v[164:167], v[192:195]\n\t" \
    "s_waitcnt lgkmcnt(6)\n\t" \
    "v_mfma_f32_16x16x32_bf16 v[188:191], v[8:11], v[168:171], v[188:191]\n\t" \
    "ds_read_b128 v[164:167], %[la] offset:16384\n\t" \
    "s_waitcnt lgkmcnt(6)\n\t" \
    "v_mfma_f32_16x16x32_bf16 v[192:195], v[88:91], v[168:171], v[192:195]\n\t" \
    "v_permlane16_swap_b32 v204, v205\n\t" \
    "s_waitcnt lgkmcnt(5)\n\t" \
    "v_mfma_f32_16x16x32_bf16 v[188:191], v[12:15], v[172:175], v[188:191]\n\t" \
    "ds_read_b128 v[168:171], %[la] offset:17408\n\t" \
    "v_mfma_f32_16x16x32_bf16 v[192:195], v[92:95], v[172:175], v[192:195]\n\t" \
    "v_add_f32 v206, v204, v205\n\t" \
    "s_waitcnt lgkmcnt(5)\n\t" \
    "v_mfma_f32_16x16x32_bf16 v[188:191], v[16:19], v[176:179], v[188:191]\n\t" \
    "ds_read_b128 v[172:175], %[la] offset:18432\n\t" \
    "v_add_f32 v207, v204, v205\n\t" \
    "v_mfma_f32_16x16x32_bf16 v[192:195], v[96:99], v[176:179], v[192:195]\n\t" \
    "s_waitcnt lgkmcnt(5)\n\t" \
    "v_mfma_f32_16x16x32_bf16 v[188:191], v[20:23], v[180:183], v[188:191]\n\t" \
    "ds_read_b128 v[176:179], %[la] offset:19456\n\t" \
    "v_permlane32_swap_b32 v206, v207\n\t" \
    "v_mfma_f32_16x16x32_bf16 v[192:195], v[100:103], v[180:183], v[192:195]\n\t" \
    "s_waitcnt lgkmcnt(5)\n\t" \
    "v_mfma_f32_16x16x32_bf16 v[188:191], v[24:27], v[184:187], v[188:191]\n\t" \
    "ds_read_b128 v[180:183], %[la] offset:24576\n\t" \
    "v_add_f32 v204, v206, v207\n\t" \
    "v_mfma_f32_16x16x32_bf16 v[192:195], v[104:107], v[184:187], v[192:195]\n\t" \
    "v_add_f32 v205, v206, v207\n\t" \
    "s_waitcnt lgkmcnt(5)\n\t" \
    "v_mfma_f32_16x16x32_bf16 v[188:191], v[28:31], v[160:163], v[188:191]\n\t" \
    "ds_read_b128 v[184:187], %[la] offset:25600\n\t" \
    "v_mfma_f32_16x16x32_bf16 v[192:195], v[108:111], v[160:163], v[192:195]\n\t" \
    "v_permlane16_swap_b32 v204, v205\n\t" \
    "s_waitcnt lgkmcnt(5)\n\t" \
    "v_mfma_f32_16x16x32_bf16 v[188:191], v[32:35], v[164:167], v[188:191]\n\t" \
    "ds_read_b128 v[160:163], %[la] offset:26624\n\t" \
    "v_mfma_f32_16x16x32_bf16 v[192:195], v[112:115], v[164:167], v[192:195]\n\t" \
    "v_mul_f32 v205, %[kd], v205\n\t" \
    "s_waitcnt lgkmcnt(5)\n\t" \
    "v_mfma_f32_16x16x32_bf16 v[188:191], v[36:39], v[168:171], v[188:191]\n\t" \
    "ds_read_b128 v[164:167], %[la] offset:27648\n\t" \
    "v_mul_f32 v204, %[kd], v204\n\t" \
    "v_mfma_f32_16x16x32_bf16 v[192:195], v[116:119], v[168:171], v[192:195]\n\t" \
    "v_fma_f32 v204, -v205, v205, v204\n\t" \
    "s_waitcnt lgkmcnt(5)\n\t" \
    "v_mfma_f32_16x16x32_bf16 v[188:191], v[40:43], v[172:175], v[188:191]\n\t" \
    "ds_read_b128 v[168:171], %[la] offset:4096\n\t" \
    "v_max_f32 v204, 0, v204\n\t" \
    "v_mfma_f32_16x16x32_bf16 v[192:195], v[120:123], v[172:175], v[192:195]\n\t" \
    "v_add_f32 v206, %[ke], v204\n\t" \
    "s_waitcnt lgkmcnt(5)\n\t" \
    "v_mfma_f32_16x16x32_bf16 v[188:191], v[44:47], v[176:179], v[188:191]\n\t" \
    "ds_read_b128 v[172:175], %[la] offset:12288\n\t" \
    "v_rsq_f32 v206, v206\n\t" \
    "v_mfma_f32_16x16x32_bf16 v[192:195], v[124:127], v[176:179], v[192:195]\n\t" \
    "s_waitcnt lgkmcnt(5)\n\t" \
    "v_mfma_f32_16x16x32_bf16 v[188:191], v[48:51], v[180:183], v[188:191]\n\t" \
    "ds_read_b128 v[176:179], %[la] offset:20480\n\t" \
    "v_mul_f32 v207, -v205, v206\n\t" \
    "v_mfma_f32_16x16x32_bf16 v[192:195], v[128:131], v[180:183], v[192:195]\n\t" \
    "v_mul_f32 v208, v196, v207\n\t" \
    "s_waitcnt lgkmcnt(5)\n\t" \
    "v_mfma_f32_16x16x32_bf16 v[188:191], v[52:55], v[184:187], v[188:191]\n\t" \
    "ds_read_b128 v[180:183], %[la] offset:28672\n\t" \
    "v_mul_f32 v209, v197, v207\n\t" \
    "v_mul_f32 v210, v198, v207\n\t" \
    "v_mfma_f32_16x16x32_bf16 v[192:195], v[132:135], v[184:187], v[192:195]\n\t" \
    "v_mul_f32 v211, v199, v207\n\t" \
    "v_mul_f32 v212, v200, v207\n\t" \
    "s_waitcnt lgkmcnt(5)\n\t" \
    "v_mfma_f32_16x16x32_bf16 v[188:191], v[56:59], v[160:163], v[188:191]\n\t" \
    "v_mul_f32 v213, v201, v207\n\t" \
    "v_mul_f32 v214, v202, v207\n\t" \
    "v_mfma_f32_16x16x32_bf16 v[192:195], v[136:139], v[160:163], v[192:195]\n\t" \
    "v_mul_f32 v215, v203, v207\n\t" \
    "v_mov_b64 v[216:217], -1\n\t" \
    "s_waitcnt lgkmcnt(4)\n\t" \
    "v_mfma_f32_16x16x32_bf16 v[188:191], v[60:63], v[164:167], v[188:191]\n\t" \
    "v_mov_b64 v[218:219], -1\n\t" \
    "buffer_store_dwordx4 v[216:219], %[l16], %[rs], %[sa] offen sc1\n\t" \
    "v_mfma_f32_16x16x32_bf16 v[192:195], v[140:143], v[164:167], v[192:195]\n\t" \
    "s_waitcnt lgkmcnt(3)\n\t" \
    "v_mfma_f32_16x16x32_bf16 v[188:191], v[64:67], v[168:171], v[188:191]\n\t" \
    "v_mfma_f32_16x16x32_bf16 v[192:195], v[144:147], v[168:171], v[192:195]\n\t" \
    "s_waitcnt lgkmcnt(2)\n\t" \
    "v_mfma_f32_16x16x32_bf16 v[188:191], v[68:71], v[172:175], v[188:191]\n\t" \
    "v_mfma_f32_16x16x32_bf16 v[192:195], v[148:151], v[172:175], v[192:195]\n\t" \
    "s_waitcnt lgkmcnt(1)\n\t" \
    "v_mfma_f32_16x16x32_bf16 v[188:191], v[72:75], v[176:179], v[188:191]\n\t" \
    "v_mfma_f32_16x16x32_bf16 v[192:195], v[152:155], v[176:179], v[192:195]\n\t" \
    "s_waitcnt lgkmcnt(0)\n\t" \
    "v_mfma_f32_16x16x32_bf16 v[188:191], v[76:79], v[180:183], v[188:191]\n\t" \
    "v_mfma_f32_16x16x32_bf16 v[192:195], v[156:159], v[180:183], v[192:195]\n\t" \
    "s_setprio 2\n\t" \
    "buffer_load_dwordx4 v[232:235], %[l16], %[rs], %[n0] offen offset:0 sc1\n\t" \
    "buffer_load_dwordx4 v[236:239], %[l16], %[rs], %[n0] offen offset:1024 sc1\n\t" \
    "buffer_load_dwordx4 v[240:243], %[l16], %[rs], %[n0] offen offset:2048 sc1\n\t" \
    "buffer_load_dwordx4 v[244:247], %[l16], %[rs], %[n0] offen offset:3072 sc1\n\t" \
    "buffer_load_dwordx4 v[248:251], %[l16], %[rs], %[n1] offen offset:0 sc1\n\t" \
    ASM_ST3 \
    "v_fma_f32 v188, v188, v206, v208\n\t" \
    "v_fma_f32 v189, v189, v206, v209\n\t" \
    "v_fma_f32 v190, v190, v206, v210\n\t" \
    "v_fma_f32 v191, v191, v206, v211\n\t" \
    "v_mul_f32 v160, v188, v188\n\t" \
    "v_mul_f32 v161, v189, v189\n\t" \
    "v_mul_f32 v162, v190, v190\n\t" \
    "v_mul_f32 v163, v191, v191\n\t" \
    "v_fma_f32 v160, v160, %[k1], v220\n\t" \
    "v_fma_f32 v161, v161, %[k1], v220\n\t" \
    "v_fma_f32 v162, v162, %[k1], v220\n\t" \
    "v_fma_f32 v163, v163, %[k1], v220\n\t" \
    "v_mul_f32 v160, v188, v160\n\t" \
    "v_mul_f32 v161, v189, v161\n\t" \
    "v_mul_f32 v162, v190, v162\n\t" \
    "v_mul_f32 v163, v191, v163\n\t" \
    "v_fma_f32 v192, v192, v206, v212\n\t" \
    "v_fma_f32 v193, v193, v206, v213\n\t" \
    "v_fma_f32 v194, v194, v206, v214\n\t" \
    "v_fma_f32 v195, v195, v206, v215\n\t" \
    "v_mul_f32 v164, v192, v192\n\t" \
    "v_mul_f32 v165, v193, v193\n\t" \
    "v_mul_f32 v166, v194, v194\n\t" \
    "v_mul_f32 v167, v195, v195\n\t" \
    "v_fma_f32 v164, v164, %[k1], v220\n\t" \
    "v_fma_f32 v165, v165, %[k1], v220\n\t" \
    "v_fma_f32 v166, v166, %[k1], v220\n\t" \
    "v_fma_f32 v167, v167, %[k1], v220\n\t" \
    "v_mul_f32 v164, v192, v164\n\t" \
    "v_mul_f32 v165, v193, v165\n\t" \
    "v_mul_f32 v166, v194, v166\n\t" \
    "v_mul_f32 v167, v195, v167\n\t" \
    "v_exp_f32 v160, v160\n\t" \
    "v_exp_f32 v161, v161\n\t" \
    "v_exp_f32 v162, v162\n\t" \
    "v_exp_f32 v163, v163\n\t" \
    "v_exp_f32 v164, v164\n\t" \
    "v_exp_f32 v165, v165\n\t" \
    "v_exp_f32 v166, v166\n\t" \
    "v_exp_f32 v167, v167\n\t" \
    "v_add_f32 v160, 1.0, v160\n\t" \
    "v_add_f32 v161, 1.0, v161\n\t" \
    "v_add_f32 v162, 1.0, v162\n\t" \
    "v_add_f32 v163, 1.0, v163\n\t" \
    "v_add_f32 v164, 1.0, v164\n\t" \
    "v_add_f32 v165, 1.0, v165\n\t" \
    "v_add_f32 v166, 1.0, v166\n\t" \
    "v_add_f32 v167, 1.0, v167\n\t" \
    "v_rcp_f32 v160, v160\n\t" \
    "v_rcp_f32 v161, v161\n\t" \
    "v_rcp_f32 v162, v162\n\t" \
    "v_rcp_f32 v163, v163\n\t" \
    "v_rcp_f32 v164, v164\n\t" \
    "v_rcp_f32 v165, v165\n\t" \
    "v_rcp_f32 v166, v166\n\t" \
    "v_rcp_f32 v167, v167\n\t" \
    "v_mul_f32 v188, v188, v160\n\t" \
    "v_mul_f32 v189, v189, v161\n\t" \
    "v_mul_f32 v190, v190, v162\n\t" \
    "v_mul_f32 v191, v191, v163\n\t" \
    "v_mul_f32 v192, v192, v164\n\t" \
    "v_mul_f32 v193, v193, v165\n\t" \
    "v_mul_f32 v194, v194, v166\n\t" \
    "v_mul_f32 v195, v195, v167\n\t" \
    "v_cvt_pk_bf16_f32 v188, v188, v189\n\t" \
    "v_cvt_pk_bf16_f32 v189, v190, v191\n\t" \
    "v_cvt_pk_bf16_f32 v190, v192, v193\n\t" \
    "v_cvt_pk_bf16_f32 v191, v194, v195\n\t" \
    "v_min_u32 v188, -2, v188\n\t" \
    "v_min_u32 v189, -2, v189\n\t" \
    "v_min_u32 v190, -2, v190\n\t" \
    "v_min_u32 v191, -2, v191\n\t" \
    "v_mov_b32 v216, 0x3f803f80\n\t" \
    "v_mov_b32 v217, 0x3f803f80\n\t" \
    "v_mov_b32 v218, 0x3f803f80\n\t" \
    "v_mov_b32 v219, 0x3f803f80\n\t" \
    "v_permlane16_swap_b32 v188, v190\n\t" \
    "v_permlane16_swap_b32 v189, v191\n\t" \
    "s_waitcnt vmcnt(6)\n\t" \
    "buffer_store_dwordx4 v[188:191], %[oa], %[rs], %[so] offen sc1\n\t" \
    "s_nop 1\n\t" \
    "s_setprio 0\n\t"
#define ASM_WT3_HI \
    "s_setprio 1\n\t" \
    "s_barrier\n\t" ASM_ST \
    "ds_read_b128 v[160:163], %[la] offset:0\n\t" \
    "ds_read_b128 v[164:167], %[la] offset:1024\n\t" \
    "ds_read_b128 v[168:171], %[la] offset:2048\n\t" \
    "ds_read2st64_b32 v[204:205], %[sr] offset0:20 offset1:24\n\t" \
    "ds_read_b128 v[172:175], %[la] offset:3072\n\t" \
    "ds_read_b128 v[176:179], %[la] offset:8192\n\t" \
    "ds_read_b128 v[180:183], %[la] offset:9216\n\t" \
    "s_waitcnt lgkmcnt(6)\n\t" \
    "v_mfma_f32_16x16x32_bf16 v[188:191], v[0:3], v[160:163], 0\n\t" \
    "ds_read_b128 v[184:187], %[la] offset:10240\n\t" \
    "v_mfma_f32_16x16x32_bf16 v[192:195], v[80:83], v[160:163], 0\n\t" \
    "s_waitcnt lgkmcnt(6)\n\t" \
    "v_mfma_f32_16x16x32_bf16 v[188:191], v[4:7], v[164:167], v[188:191]\n\t" \
    "ds_read_b128 v[160:163], %[la] offset:11264\n\t" \
    "v_mfma_f32_16x16x32_bf16 v[192:195], v[84:87], v[164:167], v[192:195]\n\t" \
    "s_waitcnt lgkmcnt(6)\n\t" \
    "v_mfma_f32_16x16x32_bf16 v[188:191], v[8:11], v[168:171], v[188:191]\n\t" \
    "ds_read_b128 v[164:167], %[la] offset:16384\n\t" \
    "s_waitcnt lgkmcnt(6)\n\t" \
    "v_mfma_f32_16x16x32_bf16 v[192:195], v[88:91], v[168:171], v[192:195]\n\t" \
    "v_permlane16_swap_b32 v204, v205\n\t" \
    "s_waitcnt lgkmcnt(5)\n\t" \
    "v_mfma_f32_16x16x32_bf16 v[188:191], v[12:15], v[172:175], v[188:191]\n\t" \
    "ds_read_b128 v[168:171], %[la] offset:17408\n\t" \
    "v_mfma_f32_16x16x32_bf16 v[192:195], v[92:95], v[172:175], v[192:195]\n\t" \
    "v_add_f32 v206, v204, v205\n\t" \
    "s_waitcnt lgkmcnt(5)\n\t" \
    "v_mfma_f32_16x16x32_bf16 v[188:191], v[16:19], v[176:179], v[188:191]\n\t" \
    "ds_read_b128 v[172:175], %[la] offset:18432\n\t" \
    "v_add_f32 v207, v204, v205\n\t" \
    "v_mfma_f32_16x16x32_bf16 v[192:195], v[96:99], v[176:179], v[192:195]\n\t" \
    "s_waitcnt lgkmcnt(5)\n\t" \
    "v_mfma_f32_16x16x32_bf16 v[188:191], v[20:23], v[180:183], v[188:191]\n\t" \
    "ds_read_b128 v[176:179], %[la] offset:19456\n\t" \
    "v_permlane32_swap_b32 v206, v207\n\t" \
    "v_mfma_f32_16x16x32_bf16 v[192:195], v[100:103], v[180:183], v[192:195]\n\t" \
    "s_waitcnt lgkmcnt(5)\n\t" \
    "v_mfma_f32_16x16x32_bf16 v[188:191], v[24:27], v[184:187], v[188:191]\n\t" \
    "ds_read_b128 v[180:183], %[la] offset:24576\n\t" \
    "v_add_f32 v204, v206, v207\n\t" \
    "v_mfma_f32_16x16x32_bf16 v[192:195], v[104:107], v[184:187], v[192:195]\n\t" \
    "v_add_f32 v205, v206, v207\n\t" \
    "s_waitcnt lgkmcnt(5)\n\t" \
    "v_mfma_f32_16x16x32_bf16 v[188:191], v[28:31], v[160:163], v[188:191]\n\t" \
    "ds_read_b128 v[184:187], %[la] offset:25600\n\t" \
    "v_mfma_f32_16x16x32_bf16 v[192:195], v[108:111], v[160:163], v[192:195]\n\t" \
    "v_permlane16_swap_b32 v204, v205\n\t" \
    "s_waitcnt lgkmcnt(5)\n\t" \
    "v_mfma_f32_16x16x32_bf16 v[188:191], v[32:35], v[164:167], v[188:191]\n\t" \
    "ds_read_b128 v[160:163], %[la] offset:26624\n\t" \
    "v_mfma_f32_16x16x32_bf16 v[192:195], v[112:115], v[164:167], v[192:195]\n\t" \
    "v_mul_f32 v205, %[kd], v205\n\t" \
    "s_waitcnt lgkmcnt(5)\n\t" \
    "v_mfma_f32_16x16x32_bf16 v[188:191], v[36:39], v[168:171], v[188:191]\n\t" \
    "ds_read_b128 v[164:167], %[la] offset:27648\n\t" \
    "v_mul_f32 v204, %[kd], v204\n\t" \
    "v_mfma_f32_16x16x32_bf16 v[192:195], v[116:119], v[168:171], v[192:195]\n\t" \
    "v_fma_f32 v204, -v205, v205, v204\n\t" \
    "s_waitcnt lgkmcnt(5)\n\t" \
    "v_mfma_f32_16x16x32_bf16 v[188:191], v[40:43], v[172:175], v[188:191]\n\t" \
    "ds_read_b128 v[168:171], %[la] offset:4096\n\t" \
    "v_max_f32 v204, 0, v204\n\t" \
    "v_mfma_f32_16x16x32_bf16 v[192:195], v[120:123], v[172:175], v[192:195]\n\t" \
    "v_add_f32 v206, %[ke], v204\n\t" \
    "s_waitcnt lgkmcnt(5)\n\t" \
    "v_mfma_f32_16x16x32_bf16 v[188:191], v[44:47], v[176:179], v[188:191]\n\t" \
    "ds_read_b128 v[172:175], %[la] offset:12288\n\t" \
    "v_rsq_f32 v206, v206\n\t" \
    "v_mfma_f32_16x16x32_bf16 v[192:195], v[124:127], v[176:179], v[192:195]\n\t" \
    "s_waitcnt lgkmcnt(5)\n\t" \
    "v_mfma_f32_16x16x32_bf16 v[188:191], v[48:51], v[180:183], v[188:191]\n\t" \
    "ds_read_b128 v[176:179], %[la] offset:20480\n\t" \
    "v_mul_f32 v207, -v205, v206\n\t" \
    "v_mfma_f32_16x16x32_bf16 v[192:195], v[128:131], v[180:183], v[192:195]\n\t" \
    "v_mul_f32 v208, v196, v207\n\t" \
    "s_waitcnt lgkmcnt(5)\n\t" \
    "v_mfma_f32_16x16x32_bf16 v[188:191], v[52:55], v[184:187], v[188:191]\n\t" \
    "ds_read_b128 v[180:183], %[la] offset:28672\n\t" \
    "v_mul_f32 v209, v197, v207\n\t" \
    "v_mul_f32 v210, v198, v207\n\t" \
    "v_mfma_f32_16x16x32_bf16 v[192:195], v[132:135], v[184:187], v[192:195]\n\t" \
    "v_mul_f32 v211, v199, v207\n\t" \
    "v_mul_f32 v212, v200, v207\n\t" \
    "s_waitcnt lgkmcnt(5)\n\t" \
    "v_mfma_f32_16x16x32_bf16 v[188:191], v[56:59], v[160:163], v[188:191]\n\t" \
    "v_mul_f32 v213, v201, v207\n\t" \
    "v_mul_f32 v214, v202, v207\n\t" \
    "v_mfma_f32_16x16x32_bf16 v[192:195], v[136:139], v[160:163], v[192:195]\n\t" \
    "v_mul_f32 v215, v203, v207\n\t" \
    "v_mov_b64 v[216:217], -1\n\t" \
    "s_waitcnt lgkmcnt(4)\n\t" \
    "v_mfma_f32_16x16x32_bf16 v[188:191], v[60:63], v[164:167], v[188:191]\n\t" \
    "v_mov_b64 v[218:219], -1\n\t" \
    "buffer_store_dwordx4 v[216:219], %[l16], %[rs], %[sa] offen sc1\n\t" \
    "v_mfma_f32_16x16x32_bf16 v[192:195], v[140:143], v[164:167], v[192:195]\n\t" \
    "s_waitcnt lgkmcnt(3)\n\t" \
    "v_mfma_f32_16x16x32_bf16 v[188:191], v[64:67], v[168:171], v[188:191]\n\t" \
    "v_mfma_f32_16x16x32_bf16 v[192:195], v[144:147], v[168:171], v[192:195]\n\t" \
    "s_waitcnt lgkmcnt(2)\n\t" \
    "v_mfma_f32_16x16x32_bf16 v[188:191], v[68:71], v[172:175], v[188:191]\n\t" \
    "v_mfma_f32_16x16x32_bf16 v[192:195], v[148:151], v[172:175], v[192:195]\n\t" \
    "s_waitcnt lgkmcnt(1)\n\t" \
    "v_mfma_f32_16x16x32_bf16 v[188:191], v[72:75], v[176:179], v[188:191]\n\t" \
    "v_mfma_f32_16x16x32_bf16 v[192:195], v[152:155], v[176:179], v[192:195]\n\t" \
    "s_waitcnt lgkmcnt(0)\n\t" \
    "v_mfma_f32_16x16x32_bf16 v[188:191], v[76:79], v[180:183], v[188:191]\n\t" \
    "v_mfma_f32_16x16x32_bf16 v[192:195], v[156:159], v[180:183], v[192:195]\n\t" \
    ASM_ST3 \
    "s_nop 4\n\t" \
    "v_fma_f32 v188, v188, v206, v208\n\t" \
    "v_fma_f32 v189, v189, v206, v209\n\t" \
    "v_fma_f32 v190, v190, v206, v210\n\t" \
    "v_fma_f32 v191, v191, v206, v211\n\t" \
    "v_mul_f32 v160, v188, v188\n\t" \
    "v_mul_f32 v161, v189, v189\n\t" \
    "v_mul_f32 v162, v190, v190\n\t" \
    "v_mul_f32 v163, v191, v191\n\t" \
    "v_fma_f32 v160, v160, %[k1], v220\n\t" \
    "v_fma_f32 v161, v161, %[k1], v220\n\t" \
    "v_fma_f32 v162, v162, %[k1], v220\n\t" \
    "v_fma_f32 v163, v163, %[k1], v220\n\t" \
    "v_mul_f32 v160, v188, v160\n\t" \
    "v_mul_f32 v161, v189, v161\n\t" \
    "v_mul_f32 v162, v190, v162\n\t" \
    "v_mul_f32 v163, v191, v163\n\t" \
    "v_fma_f32 v192, v192, v206, v212\n\t" \
    "v_fma_f32 v193, v193, v206, v213\n\t" \
    "v_fma_f32 v194, v194, v206, v214\n\t" \
    "v_fma_f32 v195, v195, v206, v215\n\t" \
    "v_mul_f32 v164, v192, v192\n\t" \
    "v_mul_f32 v165, v193, v193\n\t" \
    "v_mul_f32 v166, v194, v194\n\t" \
    "v_mul_f32 v167, v195, v195\n\t" \
    "v_fma_f32 v164, v164, %[k1], v220\n\t" \
    "v_fma_f32 v165, v165, %[k1], v220\n\t" \
    "v_fma_f32 v166, v166, %[k1], v220\n\t" \
    "v_fma_f32 v167, v167, %[k1], v220\n\t" \
    "v_mul_f32 v164, v192, v164\n\t" \
    "v_mul_f32 v165, v193, v165\n\t" \
    "v_mul_f32 v166, v194, v166\n\t" \
    "v_mul_f32 v167, v195, v167\n\t" \
    "v_exp_f32 v160, v160\n\t" \
    "v_exp_f32 v161, v161\n\t" \
    "v_exp_f32 v162, v162\n\t" \
    "v_exp_f32 v163, v163\n\t" \
    "v_exp_f32 v164, v164\n\t" \
    "v_exp_f32 v165, v165\n\t" \
    "v_exp_f32 v166, v166\n\t" \
    "v_exp_f32 v167, v167\n\t" \
    "v_add_f32 v160, 1.0, v160\n\t" \
    "v_add_f32 v161, 1.0, v161\n\t" \
    "v_add_f32 v162, 1.0, v162\n\t" \
    "v_add_f32 v163, 1.0, v163\n\t" \
    "v_add_f32 v164, 1.0, v164\n\t" \
    "v_add_f32 v165, 1.0, v165\n\t" \
    "v_add_f32 v166, 1.0, v166\n\t" \
    "v_add_f32 v167, 1.0, v167\n\t" \
    "v_rcp_f32 v160, v160\n\t" \
    "v_rcp_f32 v161, v161\n\t" \
    "v_rcp_f32 v162, v162\n\t" \
    "v_rcp_f32 v163, v163\n\t" \
    "v_rcp_f32 v164, v164\n\t" \
    "v_rcp_f32 v165, v165\n\t" \
    "v_rcp_f32 v166, v166\n\t" \
    "v_rcp_f32 v167, v167\n\t" \
    "v_mul_f32 v188, v188, v160\n\t" \
    "v_mul_f32 v189, v189, v161\n\t" \
    "v_mul_f32 v190, v190, v162\n\t" \
    "v_mul_f32 v191, v191, v163\n\t" \
    "v_mul_f32 v192, v192, v164\n\t" \
    "v_mul_f32 v193, v193, v165\n\t" \
    "v_mul_f32 v194, v194, v166\n\t" \
    "v_mul_f32 v195, v195, v167\n\t" \
    "v_cvt_pk_bf16_f32 v188, v188, v189\n\t" \
    "v_cvt_pk_bf16_f32 v189, v190, v191\n\t" \
    "v_cvt_pk_bf16_f32 v190, v192, v193\n\t" \
    "v_cvt_pk_bf16_f32 v191, v194, v195\n\t" \
    "v_min_u32 v188, -2, v188\n\t" \
    "v_min_u32 v189, -2, v189\n\t" \
    "v_min_u32 v190, -2, v190\n\t" \
    "v_min_u32 v191, -2, v191\n\t" \
    "s_nop 1\n\t" \
    "v_permlane16_swap_b32 v188, v190\n\t" \
    "v_permlane16_swap_b32 v189, v191\n\t" \
    "s_waitcnt vmcnt(1)\n\t" \
    "buffer_store_dwordx4 v[188:191], %[oa], %[rs], %[so] offen sc1\n\t" \
    "s_nop 1\n\t"
#define ASM_POLL5_I \
    "buffer_load_dwordx4 v[232:235], %[l16], %[rs], %[s0] offen offset:0 sc1\n\t" \
    "buffer_load_dwordx4 v[236:239], %[l16], %[rs], %[s0] offen offset:1024 sc1\n\t" \
    "buffer_load_dwordx4 v[240:243], %[l16], %[rs], %[s0] offen offset:2048 sc1\n\t" \
    "buffer_load_dwordx4 v[244:247], %[l16], %[rs], %[s0] offen offset:3072 sc1\n\t" \
    "buffer_load_dwordx4 v[248:251], %[l16], %[rs], %[s1] offen offset:0 sc1\n\t"
using u32x8 = __attribute__((ext_vector_type(8))) unsigned;
using u32x16 = __attribute__((ext_vector_type(16))) unsigned;
using u32x32 = __attribute__((ext_vector_type(32))) unsigned;
struct WRegs { u32x32 w[6]; u32x8 t; };
#define WREGS(W) "{v[0:31]}"(W.w[0]), "{v[32:63]}"(W.w[1]), "{v[64:95]}"(W.w[2]), "{v[96:127]}"(W.w[3]), "{v[128:159]}"(W.w[4]), "{v[160:191]}"(W.w[5]), "{v[192:199]}"(W.t)
__device__ __forceinline__ u32x32 cat32(u32x8 a, u32x8 b, u32x8 c, u32x8 d) {
    const u32x16 lo = __builtin_shufflevector(a, b, 0, 1, 2, 3, 4, 5, 6, 7, 8, 9, 10, 11, 12, 13, 14, 15);
    const u32x16 hi = __builtin_shufflevector(c, d, 0, 1, 2, 3, 4, 5, 6, 7, 8, 9, 10, 11, 12, 13, 14, 15);
    return __builtin_shufflevector(lo, hi, 0, 1, 2, 3, 4, 5, 6, 7, 8, 9, 10, 11, 12, 13, 14, 15, 16, 17, 18, 19, 20, 21, 22, 23, 24, 25, 26, 27, 28, 29, 30, 31);
}
#define WOUT(W) "=&{v[0:31]}"(W.w[0]), "=&{v[32:63]}"(W.w[1]), "=&{v[64:95]}"(W.w[2]), "=&{v[96:127]}"(W.w[3]), "=&{v[128:159]}"(W.w[4]), "=&{v[160:191]}"(W.w[5]), "=&{v[192:199]}"(W.t)
#define WTMP "memory", "scc", "v200", "v201", "v202", "v203", "v204", "v205", "v206", "v207", "v208", "v209", "v210", "v211", "v212", "v213", "v214", "v215", "v216", "v217", "v218", "v219", "v220", "v221", "v222", "v223", "v224", "v225", "v226", "v227", "v228", "v229", "v230", "v231", "v232", "v233", "v234", "v235", "v236", "v237", "v238", "v239"
__device__ __forceinline__ void load_w3(WRegs& W, const bf16_t* m, int wave, unsigned c0, bool hq) {
    const int ln = lane_id();
    unsigned o[3], s[3], t;
#pragma unroll
    for (int k = 0; k < 3; ++k) {
        const int row = (k * 64 + ln) / 24, sg = (k * 64 + ln) % 24;
        o[k] = row * (HD * 2) + c0 + (hq ? ((sg >> 1) & 1) * 384 + ((0x140 >> ((sg >> 3) * 4)) & 15) * 32 + (sg & 5) * 16 : sg * 16); s[k] = wave * 13824 + row * 416 + sg * 16;
    }
    const unsigned tr = (ln >> 4) * 3456 + ((ln >> 2) & 3) * 416 + (wave & 3) * 32 + (ln & 3) * 8;
    asm volatile(ASM_WLOAD3 : WOUT(W), [t] "=&s"(t) : [rs] "s"(mkrsrc(m, DM * HD * 2)), [wx] "s"(wave), [o0] "v"(o[0]), [o1] "v"(o[1]), [o2] "v"(o[2]), [s0] "v"(s[0]), [s1] "v"(s[1]), [s2] "v"(s[2]),
                 [t0] "v"(tr + wave * 13824), [t1] "v"(tr + (wave ^ 1) * 13824), [t2] "v"(tr + (wave ^ 2) * 13824), [t3] "v"(tr + (wave ^ 3) * 13824), [t4] "v"(tr + (wave & 4) * 13824),
                 [wl] "v"(L_W + wave * 10240 + ln * 16) : WTMP);
}
__device__ __forceinline__ void load_wo(WRegs& W, const bf16_t* m, int wave, int h) {
    const int ln = lane_id();
    unsigned o[5], s[5], t;
#pragma unroll
    for (int k = 0; k < 5; ++k) {
        const int row = (k * 64 + ln) / 20, sg = (k * 64 + ln) % 20;
        o[k] = (h * DH + row) * (DM * 2) + sg * 16 + (wave & 3) * 320; s[k] = wave * 13824 + row * 352 + sg * 16;
    }
    asm volatile(ASM_WLOADO : WOUT(W), [t] "=&s"(t) : [rs] "s"(mkrsrc(m, DM * HD * 2)), [o0] "v"(o[0]), [o1] "v"(o[1]), [o2] "v"(o[2]), [o3] "v"(o[3]), [o4] "v"(o[4]),
                 [s0] "v"(s[0]), [s1] "v"(s[1]), [s2] "v"(s[2]), [s3] "v"(s[3]), [s4] "v"(s[4]), [tr] "v"(wave * 13824 + (ln >> 2) * 352 + (ln & 3) * 8), [wl] "v"(L_W + wave * 10240 + ln * 16) : WTMP);
}
using s16x4 = __attribute__((ext_vector_type(4))) short;
template <int NT, int KS, int N>
__device__ __forceinline__ void load_wn(u32x4 (&f)[NT * KS], const bf16_t* m, int rows, int wave, int c0, int k0) {
    const int ln = lane_id();
    const rsrc_t rs = mkrsrc(m, rows * N * 2);
#pragma unroll
    for (int k = 0; k < KS; ++k)
#pragma unroll
        for (int n = 0; n < NT; ++n) f[n * KS + k] = __builtin_amdgcn_raw_buffer_load_b128(rs, ((n * 64 + ln) / (2 * NT)) * (N * 2) + ((n * 64 + ln) % (2 * NT)) * 16, (k0 + k) * (N * 64) + c0 * 32, 0);
    const unsigned st = wave * 10240 + ln * 16, tr = wave * 10240 + ((ln >> 4) * 8 + ((ln >> 2) & 3)) * (NT * 32) + (ln & 3) * 8;
#pragma unroll
    for (int k = 0; k < KS; ++k) {
#pragma unroll
        for (int n = 0; n < NT; ++n) lds_st<u32x4>(st + n * 1024, f[n * KS + k]);
        asm volatile("" ::: "memory");
#pragma unroll
        for (int n = 0; n < NT; ++n) {
            const u32x2 a = __builtin_bit_cast(u32x2, __builtin_amdgcn_ds_read_tr16_b64_v4i16((LAS s16x4*)(tr + n * 32)));
            const u32x2 b = __builtin_bit_cast(u32x2, __builtin_amdgcn_ds_read_tr16_b64_v4i16((LAS s16x4*)(tr + n * 32 + NT * 128)));
            f[n * KS + k] = (u32x4){a[0], a[1], b[0], b[1]};
        }
        asm volatile("" ::: "memory");
    }
}
struct C1Regs { u32x8 lw0, lw1, z1; u32x4 z0; };
#define XG_OUT "=&{v[200:207]}"(C.lw0), "=&{v[224:227]}"(C.z0), "=&{v[228:235]}"(C.lw1), "=&{v[244:251]}"(C.z1)
#define XG_IN WREGS(W), [xa] "v"(xa), [wl] "v"(wl), [qs] "s"(qs)
#define XG_CLOB "memory", "scc", "v208", "v209", "v210", "v211", "v212", "v213", "v214", "v215", "v216", "v217", "v218", "v219", "v220", "v221", "v222", "v223", "v236", "v237", "v238", "v239", "v240", "v241", "v242", "v243", "v252"
constexpr int XS0 = 8;
__device__ __forceinline__ unsigned init_wait(const Params& P, int c, int ph, unsigned need = 0x220080u) {
    unsigned w;
    for (int spins = 0;; ++spins) {
        unsigned o;
        asm volatile("v_mov_b32 %0, 0" : "=v"(o));
        w = __builtin_amdgcn_readfirstlane(__builtin_amdgcn_raw_buffer_load_b32(mkrsrc(P.ws + FL0, FL_BYTES), o, FL_IN + (ph * 8 + (c & 7)) * 128, 16));
        if ((w & need) == need) break;
        __builtin_amdgcn_s_sleep(8);
        if (spin_fail(spins, P)) break;
    }
    asm volatile("" ::: "memory");
    return w;
}
#define PLACE() const unsigned pw_ = init_wait(P, wave, L); const int par = (L ^ (pw_ >> 16)) & 1, cpy = x ^ ((pw_ >> 20) & 1); const rsrc_t rr = mkrsrc(P.ws + par * 8192, RG_L); (void)cpy; (void)rr
__device__ __forceinline__ void xn_wait(const Params& P, rsrc_t rr, int seg, int cpy) {
    for (int spins = 0;; ++spins) {
        const unsigned f = __builtin_amdgcn_raw_buffer_load_b32(rr, (lane_id() & 15) * 128, RG_FX + (cpy & 3) * 2048, 16);
        if (__all(f > (unsigned)seg)) break;
        __builtin_amdgcn_s_sleep(4);
        if (spin_fail(spins, P)) break;
    }
    asm volatile("" ::: "memory");
}
__device__ __forceinline__ unsigned c1_dst(int wave, bool ktype) {
    const int ln = lane_id(), l15 = ln & 15, hf = (wave >> 1) & 1;
    const int rb16 = ((ln >> 2) & 4) * 16 + (ln >> 5) * 32;
    return (ktype ? L_KM + (l15 >> 2) * 128 + hf * 8 + (l15 & 3) * 2
                  : L_Q + (l15 >> 3) * 256 + ((l15 >> 1) & 1) * 128 + ((l15 >> 2) & 1) * 8 + hf * 4 + (l15 & 1) * 2) + (wave & 1) * 1536 + rb16;
}
template <int NP>
__device__ __forceinline__ void crit1(const WRegs& W, C1Regs& C, rsrc_t rr, unsigned sbase, int wave, unsigned wl, unsigned dsto, unsigned& budget, unsigned& fv, unsigned fs
) {
    unsigned abase;
    asm volatile("v_and_b32 %0, 0x3ff, %1\n\tv_or_b32 %0, %2, %0" : "=&v"(abase) : "v"(wl), "s"(wave * 1024));
    ASM_ST_DECL;
#define C1_OUT "+{v[224:227]}"(C.z0), "+{v[244:251]}"(C.z1), "+{v[200:207]}"(C.lw0), "+{v[228:235]}"(C.lw1)
#define C1_IN WREGS(W), [ab] "v"(abase), [wl] "v"(wl), [dst] "v"(dsto)
#define C1_CLOB "memory", "vcc", "scc", "v208", "v209", "v210", "v211", "v212", "v213", "v214", "v215", "v216", "v217", "v218", "v219", "v220", "v221", "v222", "v223", "v236", "v237", "v238", "v239", "v240", "v241", "v242", "v243"
#define C1_POLL [rs] "s"(rr), [s0] "s"(sbase), [s1] "s"(sbase + 4096), [s2] "s"(sbase + 2 * BLK)
    ST(1);
    if constexpr (NP == 13) asm volatile(ASM_CRIT1_V3 "s_waitcnt vmcnt(0)" : C1_OUT, [n] "+s"(budget), [fv] "+v"(fv) ASM_ST_OUT ASM_ST2_OUT : C1_IN, C1_POLL, [fs] "s"(fs) : C1_CLOB);
    else if constexpr (NP == 12) asm volatile(ASM_CRIT1_V2 "s_waitcnt vmcnt(0)" : C1_OUT, [n] "+s"(budget), [fv] "+v"(fv) ASM_ST_OUT ASM_ST2_OUT : C1_IN, C1_POLL, [fs] "s"(fs) : C1_CLOB);
    else if constexpr (NP == 3) asm volatile(ASM_CRIT1_3 : C1_OUT, [n] "+s"(budget) ASM_ST_OUT ASM_ST2_OUT : C1_IN, C1_POLL : C1_CLOB);
    else if constexpr (NP == 2) asm volatile(ASM_CRIT1_2 : C1_OUT, [n] "+s"(budget) ASM_ST_OUT ASM_ST2_OUT : C1_IN, C1_POLL : C1_CLOB);
    else asm volatile(ASM_CRIT1_N : C1_OUT ASM_ST_OUT : C1_IN : C1_CLOB);
    SB();
    ASM_ST_SAVE(3);
    if constexpr (NP != 0) { ASM_ST2_SAVE(2); }
}
#define STARG

#define SRG 5
#undef STROLE
#define STROLE 1
__device__ __forceinline__ void s_role(const Params& P, int wave, int L, int h, int x) {
    const char* xn = P.ws + PG0 + PG_XN + L * PG_LX;
    auto xdma = [&](int seg, int hb, int par) {
        const unsigned l16 = lane_id() * 16;
        const int mt = (wave >> 1) & 1;
        const char* g = xn + pgb(seg, par);
        if (hb == 0) {
            dma4(g + blin(mt * 20480 + (wave & 1) * 4096), l16, L_XA + mt * 16384 + (wave & 1) * 4096);
            dmaq<0>(g + blin(mt * 20480 + 16384) + (wave & 1) * 1024, l16, L_XB + mt * 4096 + (wave & 1) * 1024);
        } else {
            dma4(g + blin(mt * 20480 + 8192), l16, L_XA + mt * 16384 + 8192); dma4(g + blin(mt * 20480 + 12288), l16, L_XA + mt * 16384 + 12288);
            dmaq<2>(g + blin(mt * 20480 + 16384), l16, L_XB + mt * 4096); dmaq<3>(g + blin(mt * 20480 + 16384), l16, L_XB + mt * 4096);
        }
    };
    const float SCL = 0.07216878364870322f * 1.4426950408889634f;
    auto run = [&](auto Cc) {
    constexpr int CLS = decltype(Cc)::value;
    WRegs wf;
    MARK(!L && h == 0 && wave == 0, 0);
    load_w3(wf, P.w[CLS == 3], wave, h * 384, false);
    MARKX(!L && wave == 0, 1);
    PLACE();
    if (CLS == 3) {
        if (L) xn_wait(P, rr, 0, cpy);
        xdma(0, 0, par);
        if (wave == 4 || wave == 7) xdma(0, 1, par);
        wait_vm<0>();
    }
    bar();
    if (CLS == 3 && wave == 7 && L) xn_wait(P, rr, 1, cpy);
    const unsigned qs = (CLS == 3 ? L_KX : L_QX) + (wave & 1) * 3072 + ((wave >> 1) & 1) * 8;
    unsigned xa, wl, dsto;
    if (CLS == 3) { xa = L_XA + wave * 2048 + lane_id() * 16; wl = L_W + wave * 10240 + lane_id() * 16; dsto = c1_dst(wave, true); }
    __builtin_amdgcn_s_setprio(CLS == 3 ? 1 : 2);
    unsigned budget = 1u << 18, tb2 = 0;
    STDECL;
    LDECL;
#pragma unroll 1
    for (int t = 0; t < NSEG; ++t) {
        const unsigned slot = t & (NSLOT - 1);
        C1Regs C;
        {
            const WRegs& W = wf;
            ASM_ST_DECL;
            if constexpr (CLS == 3) {
                const int mt = (wave >> 1) & 1;
                const char* g = xn + pgb(t + 1 < NSEG ? t + 1 : t, par);
                asm volatile(ASM_XG_S_HI : XG_OUT ASM_ST_OUT : XG_IN, [slp] "s"(SLP4), [g0] "s"(g + blin(mt * 20480 + (wave & 1) * 4096)), [d0] "s"(L_XA + mt * 16384 + (wave & 1) * 4096),
                             [g1] "s"(g + blin(mt * 20480 + 16384) + (wave & 1) * 1024), [d1] "s"(L_XB + mt * 4096 + (wave & 1) * 1024) : XG_CLOB);
                wait_vm<0>();
                LB(t);
            } else {
                const unsigned l16 = lane_id() * 16;
                xa = L_XA + wave * 2048 + l16; wl = L_W + wave * 10240 + l16;
#define XG_Q(A) asm volatile("s_cmp_lt_u32 %[t], 2\n\ts_cbranch_scc1 .Lq3%=\n\t" A "s_branch .Lqe%=\n\t.Lq3%=:\n\t" ASM_XG_S_LO ".Lqe%=:" : XG_OUT ASM_ST_OUT : XG_IN, [t] "s"(t) : XG_CLOB)
                if constexpr (CLS == 0 || CLS == 4) XG_Q(ASM_XG_S_LO1); else XG_Q(ASM_XG_S_LO2);
                __builtin_amdgcn_s_setprio(0);
                if constexpr (CLS == 0) {
                    const int ln = lane_id();
                    const u32x4 s4 = sent4();
                    const unsigned poff = (ln * 16 + ((ln < 8) ? h * 128 : (1 + (h >> 4)) * 2 * BLK - 128 + (h & 15) * 512)) | ((ln >= 40 || t < 2) ? 0x40000000u : 0u);
#pragma unroll
                    for (int c = 0; c < (int)NCP; ++c) st128(s4, rr, poff, RG_XP + ((t - 2) & (NSLOT - 1)) * XP_SLOT + (c >> 1) * XP_PAIR + (c & 1) * BLK);
                    const unsigned pbase = RG_PX + ((t - 3) & (NSLOT - 1)) * PX_SLOT + (h >> 2) * BLK;
                    const unsigned o2 = ((ln & 16) ? 5120 + ((h >> 1) & 1) * 1024 + (h & 1) * 256 + (ln >> 5) * 512 : 4096 + (h & 3) * 256 + (ln >> 5) * 3072) + (ln & 15) * 16;
                    if (t >= 3) { st128(s4, rr, ln * 16, pbase + (h & 3) * 1024); st128(s4, rr, o2, pbase); }
                }
                if (CLS == 4 && t >= HT0) {
                    const int ln = lane_id(), u = h >> 2;
                    for (int spins = 0;; ++spins) {
                        const unsigned fl = __builtin_amdgcn_raw_buffer_load_b32(rr, (ln & 3) * 128, RG_FH + BLK + u * 512, 16);
                        if (__all(fl > (unsigned)t)) break;
                        __builtin_amdgcn_s_sleep(2);
                        if (spin_fail(spins, P)) break;
                    }
                    asm volatile("" ::: "memory");
                    const char* g = P.ws + PG0 + pgb(t, par) + PG_HV + (16 + u) * 3 * BLK;
                    unsigned t0, t1, vo;
                    static_assert(L_QX == 16384, "");
                    asm volatile("s_add_u32 %0, %4, 0\n\ts_and_b32 %1, %0, 0xe000\n\ts_add_u32 %0, %0, %1\n\tv_add_u32 %2, %0, %5\n\ts_mov_b32 m0, 16384\n\ts_nop 0\n\tglobal_load_lds_dwordx4 %2, %3 sc1\n\t"
                                 "s_add_u32 %0, %4, 1024\n\ts_and_b32 %1, %0, 0xe000\n\ts_add_u32 %0, %0, %1\n\tv_add_u32 %2, %0, %5\n\ts_mov_b32 m0, 17408\n\ts_nop 0\n\tglobal_load_lds_dwordx4 %2, %3 sc1\n\t"
                                 "s_add_u32 %0, %4, 2048\n\ts_and_b32 %1, %0, 0xe000\n\ts_add_u32 %0, %0, %1\n\tv_add_u32 %2, %0, %5\n\ts_mov_b32 m0, 19456\n\ts_nop 0\n\tglobal_load_lds_dwordx4 %2, %3 sc1\n\t"
                                 "s_add_u32 %0, %4, 6144\n\ts_and_b32 %1, %0, 0xe000\n\ts_add_u32 %0, %0, %1\n\tv_add_u32 %2, %0, %5\n\ts_mov_b32 m0, 22528\n\ts_nop 0\n\tglobal_load_lds_dwordx4 %2, %3 sc1\n\t"
                                 "s_add_u32 %0, %4, 7168\n\ts_and_b32 %1, %0, 0xe000\n\ts_add_u32 %0, %0, %1\n\tv_add_u32 %2, %0, %5\n\ts_mov_b32 m0, 23552\n\ts_nop 0\n\tglobal_load_lds_dwordx4 %2, %3 sc1\n\t"
                                 "s_add_u32 %0, %4, 8192\n\ts_and_b32 %1, %0, 0xe000\n\ts_add_u32 %0, %0, %1\n\tv_add_u32 %2, %0, %5\n\ts_mov_b32 m0, 25600\n\ts_nop 0\n\tglobal_load_lds_dwordx4 %2, %3 sc1\n\t"
                                 : "=&s"(t0), "=&s"(t1), "=&v"(vo) : "s"(g), "s"((((h >> 1) & 1) * 12 + (h & 1) * 3) * 1024), "v"(ln * 16) : "memory", "scc");
                }
                dsto = c1_dst(wave, false);
                while ((unsigned)__builtin_amdgcn_s_memtime() - tb2 < STD) __builtin_amdgcn_s_sleep(2);
            }
            ASM_ST_SAVE(7);
            ST(6);
        }
        ST(0);
        unsigned fv = 0;
        if (CLS == 2) fv = (lane_id() & 15) * 128;
        crit1<CLS == 3 ? 0 : CLS < 2 ? 3 : CLS == 2 ? 12 : 2>(wf, C, rr, RG_XM + slot * XM_SLOT + (cpy >> 1) * XM_PAIR + (cpy & 1) * BLK, wave, wl, dsto, budget, fv, RG_FX + (cpy & 3) * 2048 STARG);
        if (CLS != 3) tb2 = (unsigned)__builtin_amdgcn_s_memtime();
        if constexpr (CLS == 0) {
            const unsigned sp = RG_XP + slot * XP_SLOT, sx = sp + (1 + (h >> 4)) * 2 * BLK + (h & 15) * 512;
            ASM_ST_DECL;
            asm volatile(ASM_SM : [c0] "+s"(budget) ASM_ST_OUT : WREGS(wf), [rr] "s"(rr), [sx] "s"(sx), [sy] "s"(sx + XP_PAIR), [sm] "s"(sp + h * 128), [cs] "s"(BLK), [cp] "s"(XP_PAIR), [c70] "s"(0x70), [scl] "s"(SCL)
                         : "memory", "v200", "v201", "v202", "v203", "v204", "v205", "v206", "v207", "v208", "v209", "v210", "v211", "v212", "v213", "v214", "v215", "v216", "v217", "v218", "v219", "v220", "v221", "v222", "v223", "v224", "v225", "v226", "v227", "v228", "v229", "v230", "v231", "v232", "v233", "v234", "v235", "v236", "v237", "v238", "v239", "v240", "v241", "v242", "v243", "v244", "v245", "v246", "v247", "v248", "v249", "v250", "v251", "v252", "v253", "v254", "v255");
            ASM_ST_SAVE(4);
            ST(5);
        } else if constexpr (CLS == 1 || CLS == 4) {
            const int ln = lane_id(), l15 = ln & 15, l4 = ln >> 4;
            constexpr int mt = (CLS == 4);
            const unsigned qa = L_QX + mt * 6144 + ln * 16, kmo = L_KM + l4 * 128 + (ln & 7) * 16;
            const unsigned xo = mt ? (h & 3) * 1024 : 5120 + ((h >> 1) & 1) * 1024 + (h & 1) * 256;
            const unsigned pm = l4 < 2 ? (mt ? 4096 : 7168) + (h & 3) * 256 - xo + l15 * 16 + l4 * 8 : OOB;
            const unsigned so = RG_PX + slot * PX_SLOT + (h >> 2) * BLK + xo;
            f32x4 sd;
#pragma unroll
            for (int r = 0; r < 4; ++r) sd[r] = (l4 * 4 + r > l15) ? -3.0e38f : 0.f;
            ASM_ST_DECL;
#define P4_ARGS : "+{v[240:243]}"(sd) ASM_ST_OUT : WREGS(wf), [qa] "v"(qa), [km] "v"(kmo), [pm] "v"(pm), [rr] "s"(rr), [so] "s"(so), [scl] "s"(SCL) : "memory", "v200", "v201", "v202", "v203", "v204", "v205", "v206", "v207", "v208", "v209", "v210", "v211", "v212", "v213", "v214", "v215", "v216", "v217", "v218", "v219", "v220", "v221", "v222", "v223", "v224", "v225", "v226", "v227", "v228", "v229", "v230", "v231", "v232", "v233", "v234", "v235", "v236", "v237", "v238", "v239", "v244", "v245", "v246", "v247", "v248", "v249", "v250", "v251"
            if constexpr (mt) asm volatile(ASM_P4_1 P4_ARGS); else asm volatile(ASM_P4_0 P4_ARGS);
            ASM_ST_SAVE(4);
            ST(5);
        } else {
            bar();
            ST(4);
            if (CLS == 2 && L && t + 2 < NSEG && !__all(fv > (unsigned)t + 2)) xn_wait(P, rr, t + 2, cpy);
            if (CLS == 2) { int tt = t; asm volatile("" : "+s"(tt)); if (tt + 2 == XS0) init_wait(P, h, 1); }
            if (CLS == 3) LA();
            {
                if (CLS == 3 && (wave == 4 || wave == 7)) {
                    if (t + 1 < NSEG) xdma(t + 1, 1, par);
                }
                ST(5);
            }
        }
        STFLUSH(t);
    }
    LF();
    };
    if (wave == 0) run(std::integral_constant<int, 0>{});
    else if (wave == 1) run(std::integral_constant<int, 1>{});
    else if (wave == 2) run(std::integral_constant<int, 4>{});
    else if (wave == 3) run(std::integral_constant<int, 2>{});
    else run(std::integral_constant<int, 3>{});
}

#undef STROLE
#define STROLE 8
__device__ __forceinline__ void h_role(const Params& P, int wave, int L, int i, int x) {
    const rsrc_t rp = mkrsrc(P.ws + PG0, PG_L);
    const char* xn = P.ws + PG0 + PG_XN + L * PG_LX;
    auto run = [&](auto Cc) {
    constexpr bool HI = decltype(Cc)::value;
    const int mt = (wave >> 1) & 1;
    WRegs wf;
    load_w3(wf, i < 16 ? P.w[2] : P.w[0], wave, (i < 16 ? i * 2 + HI : (i - 16) * 4 + HI * 2) * 384, i >= 16);
    PLACE();
    auto xdma = [&](int seg, int hb) {
        const unsigned l16 = lane_id() * 16;
        const char* g = xn + pgb(seg, par);
        dma4(g + blin(mt * 20480 + hb * 8192 + (wave & 1) * 4096), l16, L_XA + mt * 16384 + hb * 8192 + (wave & 1) * 4096);
        dmaq<0>(g + blin(mt * 20480 + 16384) + hb * 2048 + (wave & 1) * 1024, l16, L_XB + mt * 4096 + hb * 2048 + (wave & 1) * 1024);
    };
    const unsigned qs = (HI ? L_KX : L_QX) + (wave & 1) * 3072 + mt * 8;
    const unsigned xa = L_XA + wave * 2048 + lane_id() * 16, wl = L_W + wave * 10240 + lane_id() * 16;
    const unsigned fp = RG_FH + (i >> 4) * BLK + ((i & 15) * 4 + (wave & 3)) * 128;
    const unsigned fb = L ? RG_FX + (cpy & 3) * 2048 : RG_FV + (i & 3) * 2048, fa = L ? 0 : HAH;
    unsigned go = 0, all = 0;
    STDECL;
    LDECL;
    for (int t = HT0; t <= NSEG; ++t) {
        ST(0);
        if constexpr (HI) {
            const unsigned l16 = lane_id() * 16;
            if (t > HT0) {
                u32x4 v[6];
                const int w4 = wave - 4;
#pragma unroll
                for (int k = 0; k < 6; ++k) v[k] = lds_ld<u32x4>(L_QX + ((k >> 1) * 8 + w4 * 2 + (k & 1)) * 1024 + l16);
                const unsigned so = pgb(t - 1, par) + PG_HV + i * 3 * BLK + w4 * 2048;
#pragma unroll
                for (int k = 0; k < 6; ++k) st128(v[k], rp, l16 + (k & 1) * 1024, so + (k >> 1) * BLK);
                if (!all) {
                    wait_vm<0>();
                    if (l16 < 512) __builtin_amdgcn_raw_buffer_store_b32(t, rr, l16 >> 2, fp, 16);
                }
            }
            ST(1);
            if (t == NSEG) break;
            if (!all) bar();
            xdma(t, 1);
        } else {
            if (t == NSEG) break;
            if (!go) {
                for (int spins = 0;; ++spins) {
                    if (__all(__builtin_amdgcn_raw_buffer_load_b32(rr, (lane_id() & 15) * 128, fb, 16) + fa > (unsigned)t)) break;
                    __builtin_amdgcn_s_sleep(HSL);
                    if (spin_fail(spins, P)) break;
                }
                asm volatile("" ::: "memory");
                xdma(t, 0); wait_vm<0>();
            }
            if (!all) bar();
        }
        ST(2);
        C1Regs C;
        const WRegs& W = wf;
        ASM_ST_DECL;
        if constexpr (HI) {
            const unsigned l4 = lane_id() * 4;
            asm volatile(ASM_XG_H_HI : XG_OUT ASM_ST_OUT : XG_IN, [rr] "s"(rr), [fp] "s"(fp), [tv] "s"(t), [fv] "v"((all && l4 < 128) ? l4 : OOB) : XG_CLOB);
        } else {
            asm volatile(ASM_XG_S_LO : XG_OUT ASM_ST_OUT : XG_IN : XG_CLOB);
            if (t + 1 == XS0) init_wait(P, wave, 1);
            const unsigned fv = __builtin_amdgcn_raw_buffer_load_b32(rr, (lane_id() & 15) * 128, fb, 16) + fa;
            go = __builtin_amdgcn_readfirstlane((t + 1 < NSEG && __all(fv > (unsigned)t + 1)) ? 1u : 0u);
            if (go) xdma(t + 1, 0);
            lds_st<unsigned>(L_MEM + wave * 4, go);
            wait_vm<0>();
        }
        ASM_ST_SAVE(3);
        ST(4);
        if (!HI) { LB(t); LA(); }
        PMARK(L && wave == 7, 6, t);
        bar();
        const u32x4 gv = lds_ld<u32x4>(L_MEM);
        all = __builtin_amdgcn_readfirstlane(gv[0] & gv[1] & gv[2] & gv[3]);
        ST(5);
        STFLUSH(t);
    }
    LF();
    };
    if (wave < 4) run(std::false_type{}); else run(std::true_type{});
}

#undef STROLE
#define STROLE 2
__device__ __forceinline__ void v_role(const Params& P, int wave, int L, int h, int x) {
    WRegs wf;
    STDECL;
    auto runv = [&](auto Cc) {
        load_w3(wf, P.w[2], wave, h * 384, false);
        MARKX(!L && wave == 0, 6);
        PLACE();
        unsigned budget = 1u << 18;
        const unsigned wl = L_W + wave * 10240 + lane_id() * 16, dsto = c1_dst(wave, false);
        auto vfetch = [&](int seg, int tries) {
            const int ln = lane_id();
            for (int spins = 0; tries != -2; ++spins) {
                if (spins == tries) return false;
                const unsigned f = __builtin_amdgcn_raw_buffer_load_b32(rr, (ln & 3) * 128, RG_FH + (h >> 1) * 512, 16);
                if (__all(f > (unsigned)seg)) break;
                __builtin_amdgcn_s_sleep(4);
                if (spin_fail(spins, P)) break;
            }
            asm volatile("" ::: "memory");
            const char* g = P.ws + PG0 + pgb(seg, par) + PG_HV + (h >> 1) * 3 * BLK;
#pragma unroll
            for (int k = 0; k < 3; ++k) {
                const int f = (h & 1) * 12 + wave * 3 + k;
                dmaq<0>(g + (f >> 3) * BLK + (f & 7) * 1024, ln * 16, L_QX + (seg & 1) * 12288 + (wave * 3 + k) * 1024);
            }
            return true;
        };
#pragma unroll 1
        for (int t = 0; t < HT0; ++t) {
            if (L) xn_wait(P, rr, t, cpy);
            const unsigned l16 = lane_id() * 16;
            const char* g = P.ws + PG0 + PG_XN + L * PG_LX + pgb(t, par);
            dma4(g + blin(wave * 4096), l16, L_XA + wave * 4096); dma4(g + blin(20480 + wave * 4096), l16, L_XA + 16384 + wave * 4096);
            dmaq<0>(g + blin(16384) + wave * 1024, l16, L_XB + wave * 1024); dmaq<0>(g + blin(20480 + 16384) + wave * 1024, l16, L_XB + 4096 + wave * 1024);
            wait_vm<0>();
            bar();
            C1Regs C;
            const WRegs& W = wf;
            const unsigned xa = L_XA + wave * 2048 + l16, qs = L_QX + (wave & 1) * 3072 + ((wave >> 1) & 1) * 8 + t * 12288;
            asm volatile(ASM_XG_V : XG_OUT : XG_IN : XG_CLOB);
            if (t == 0) bar();
        }
        bool seen = false;
        unsigned tb2 = 0;
        for (int t = 0; t < NSEG; ++t) {
            if (t + 1 - V1A >= HT0 && t + 1 - V1A < NSEG) { vfetch(t + 1 - V1A, seen ? -2 : -1); wait_vm<0>(); }
            ST(7);
            bar();
            C1Regs C;
            asm volatile("ds_read_b128 v[200:203], %4\n\tds_read_b128 v[204:207], %4 offset:1024\n\tds_read_b128 v[228:231], %4 offset:2048\n\tds_read_b128 v[232:235], %4 offset:3072\n\t"
                         "v_mov_b64 v[224:225], 0\n\tv_mov_b64 v[226:227], 0\n\tv_mov_b64 v[244:245], 0\n\tv_mov_b64 v[246:247], 0\n\tv_mov_b64 v[248:249], 0\n\tv_mov_b64 v[250:251], 0\n\ts_waitcnt lgkmcnt(0)"
                         : XG_OUT : "v"(wl) : "memory");
            ST(6);
            while ((unsigned)__builtin_amdgcn_s_memtime() - tb2 < VTD) __builtin_amdgcn_s_sleep(1);
            unsigned fv = (lane_id() & 3) * 128;
            crit1<10 + decltype(Cc)::value>(wf, C, rr, RG_XM + (t & (NSLOT - 1)) * XM_SLOT + (cpy >> 1) * XM_PAIR + (cpy & 1) * BLK, wave, wl, dsto, budget, fv, RG_FH + (h >> 1) * 512 STARG);
            __builtin_amdgcn_s_setprio(0);
            bar();
            seen = __all(fv > (unsigned)t + 2 - V1A);
            tb2 = (unsigned)__builtin_amdgcn_s_memtime();
            ST(4);
            STC(5);
            STFLUSH(t);
        }
    };
    if (wave < 2) runv(std::integral_constant<int, 3>{});
    else if (wave < 4) runv(std::integral_constant<int, 2>{});
    else {
        const int w4 = wave - 4;
        const rsrc_t rd = mkrsrc(P.ws + PG0, PG_L);
        load_wo(wf, P.w[3], wave, h);
        PLACE();
        bar();
        bar();
        for (int t = -1; t < NSEG; ++t) {
            const unsigned slot = t & (NSLOT - 1);
            bar();
            ST(7);
#pragma unroll 1
            for (int n = V1A ? t + (t <= 0) : t + (t != 1) + (t == 0); n <= (V1A && t >= 0 ? t : t + 1) && n < NSEG; ++n) {
                const unsigned l16 = lane_id() * 16;
                const unsigned wl = L_W + wave * 10240 + l16, va = L_QX + (n & 1) * 12288 + l16;
                unsigned so = ((n & 7) << 22) + pgb(64 + (w4 >> 1), par) + ((w4 & 1) * 40 + (h >> 3)) * BLK + (h & 7) * 1024;
                asm volatile(ASM_VOX : [so] "+s"(so) : WREGS(wf), [l16] "v"(l16), [wl] "v"(wl), [va] "v"(va), [rd] "s"(rd) : "memory", "scc", "v200", "v201", "v202", "v203", "v204", "v205", "v206", "v207", "v208", "v209", "v210", "v211", "v212", "v213", "v214", "v215", "v216", "v217", "v218", "v219", "v220", "v221", "v222", "v223", "v224", "v225", "v226", "v227", "v228", "v229", "v230", "v231", "v232", "v233", "v234", "v235", "v236", "v237", "v238", "v239", "v240", "v241", "v242", "v243", "v244", "v245", "v246", "v247", "v248", "v249", "v250", "v251");
                if (V1A || n < 2) {
                    wait_vm<0>();
                    const unsigned l4 = lane_id() * 4;
                    if (l4 < 128) __builtin_amdgcn_raw_buffer_store_b32(n + 1, rr, l4, RG_FV + (w4 >> 1) * BLK + ((w4 & 1) * 32 + h) * 128, 16);
                }
            }
            if (t < 0) continue;
            ST(0);
            unsigned wl = L_W + wave * 10240 + lane_id() * 16;
            bf16x16 lw = lds_frag(wl, 1024);
            u32x32 cz;
            asm volatile("v_mov_b64 v[220:221], 0\n\tv_mov_b64 v[222:223], 0\n\tv_mov_b64 v[224:225], 0\n\tv_mov_b64 v[226:227], 0\n\tv_mov_b64 v[228:229], 0\n\tv_mov_b64 v[230:231], 0\n\tv_mov_b64 v[232:233], 0\n\tv_mov_b64 v[234:235], 0\n\tv_mov_b64 v[236:237], 0\n\tv_mov_b64 v[238:239], 0\n\tv_mov_b64 v[240:241], 0\n\tv_mov_b64 v[242:243], 0\n\tv_mov_b64 v[244:245], 0\n\tv_mov_b64 v[246:247], 0\n\tv_mov_b64 v[248:249], 0\n\tv_mov_b64 v[250:251], 0" : "={v[220:251]}"(cz));
            unsigned vo = ((wl >> 8) & 1) * (1 + (w4 & 1) * 2) * BLK + ((wl >> 9) & 1) * 1024 + (wl & 255);
            asm volatile("" : "+{v[212:219]}"(lw), "+{v[220:251]}"(cz), "+v"(wl), "+v"(vo));
            unsigned l16 = wl & 1023u;
            bar();
            wait_vm<0>();
            if (!V1A && l16 < 512 && t && t + 1 < NSEG) __builtin_amdgcn_raw_buffer_store_b32(t + 2, rr, l16 >> 2, RG_FV + (w4 >> 1) * BLK + ((w4 & 1) * 32 + h) * 128, 16);
            ST(1);
            const int sidx = (wl & 128u) ? 0xEEEE : 0x4444;
            unsigned so = RG_VM + slot * VM_SLOT + (unsigned)((w4 * 10 - (w4 & 1) + (h >> 4) * 2) * BLK + ((h >> 2) & 3) * 2048 + (h & 3) * 256);
            ST(3);
            ASM_ST_DECL;
            asm volatile(ASM_CRIT2 : [so] "+s"(so), "+{v[212:219]}"(lw), "+{v200}"(l16), "+{v[220:251]}"(cz) ASM_ST_OUT
                         : WREGS(wf), [wl] "v"(wl), [vo] "v"(vo), [sidx] "v"(sidx), [rr] "s"(rr)
                         : "memory", "scc", "v201", "v202", "v203", "v204", "v205", "v206", "v207", "v208", "v209", "v210", "v211");
            ASM_ST_SAVE(4);
            ST(5);
            STFLUSH(t);
        }
    }
}

#undef STROLE
#define STROLE 4
constexpr unsigned R_RED = 0;
constexpr unsigned R_PART = 16384;
__device__ __forceinline__ void r_role(const Params& P, int wave, int L, int r5, int x) {
    PLACE();
    const int jj = (r5 * 4 + cpy) ^ (r5 * 4 + x >= 6 ? 1 : 0);
    const int lane = lane_id(), l15 = lane & 15, l4 = lane >> 4;
    const unsigned l16 = lane * 16;
    const rsrc_t rd = mkrsrc(P.ws + PG0, PG_L);
    const int sidx = (l15 < 8) ? 0x4444 : 0xEEEE;
    unsigned offp[3];
    offp[0] = RG_XP + (cpy >> 1) * XP_PAIR + (cpy & 1) * BLK + (1 + (wave >> 2)) * 2 * BLK + (wave & 3) * 2048 + (PLAY ? (l4 >> 1) * 512 + (((l15 >> 2) & 1) + (l15 & 2)) * 128 + (l4 & 1) * 64 + (l15 >> 3) * 32 + (l15 & 1) * 16 : l16);
    offp[2] = (l15 < 8) ? RG_XP + (cpy >> 1) * XP_PAIR + (cpy & 1) * BLK + wave * 512 + l4 * 128 + l15 * 16 : OOB;
    const unsigned offv = RG_VM + ((jj >> 1) * 4 + (wave >> 2) * 2 + (jj & 1)) * BLK + (wave & 3) * 2048 + l16;
    const int j = jj * 2 + ((wave >> 1) & 1), fl = (wave & 1) * 32 + (lane & 31);
    const unsigned offo = RG_XM + (wave >> 2) * XM_PAIR + (lane >> 5) * BLK + (j & 31) * 256 + (j >> 5) * 2 * BLK + fl * 4;
    MARK(!L && jj == 0 && wave == 0, 5);
    {
        const int row = (lane >> 2) & 7, col = 16 * j + 4 * (lane & 3) + 2 * (wave & 1);
        __builtin_amdgcn_raw_buffer_store_b32(nosent(*(const unsigned*)(P.mem0 + row * DM + col)), rr, offo, 0, 16);
    }
    auto red_addr = [](int fl) { return R_RED + fl * 80 + (fl >> 4) * 16; };
    const unsigned rw0 = (lane >> 5) * 5248 + red_addr((((l15 >> 1) & 1) * 8 + 4 * (l4 & 1)) * 4 + (l15 >> 2)) + (l15 & 1) * 4 + (wave ^ (l15 >> 3)) * 8;
    const unsigned rrd = ((wave >> 1) & 1) * 5248 + red_addr(fl) + (lane >> 5) * 32;
    u32x32 vx;
    const unsigned fo = (lane & 3) * 128, fs = RG_FV + (jj / 10) * BLK + (((jj / 5) & 1) * 32 + wave * 4) * 128, vxo = pgb(64 + jj / 10, par) + (unsigned)((jj % 10) * 8 + (wave >> 1)) * BLK + (wave & 1) * 4096;
    const rsrc_t rp = mkrsrc(P.ws + PG0, PG_L);
    const unsigned xo = (l4 >> 1) * 256 + l15 * 16 + (l4 & 1) * 8, pw = R_PART + wave * 1024 + l16, pr = R_PART + (wave & 3) * 8192 + l16;
    const int fin = wave >> 2;
    asm volatile("" : "={v[128:159]}"(vx));
    auto vx_fetch = [&](int t) {
        for (int spins = 0;; ++spins) {
            const unsigned f = __builtin_amdgcn_raw_buffer_load_b32(rr, fo, fs, 16);
            if (__all(f > (unsigned)t)) break;
            __builtin_amdgcn_s_sleep(2);
            if (spin_fail(spins, P)) break;
        }
        const unsigned sv = ((t & 7) << 22) + vxo;
        asm volatile("buffer_load_dwordx4 v[128:131], %1, %2, %3 offen sc1\n\tbuffer_load_dwordx4 v[132:135], %1, %2, %3 offen offset:1024 sc1\n\t"
                     "buffer_load_dwordx4 v[136:139], %1, %2, %3 offen offset:2048 sc1\n\tbuffer_load_dwordx4 v[140:143], %1, %2, %3 offen offset:3072 sc1\n\t"
                     "buffer_load_dwordx4 v[144:147], %1, %2, %4 offen sc1\n\tbuffer_load_dwordx4 v[148:151], %1, %2, %4 offen offset:1024 sc1\n\t"
                     "buffer_load_dwordx4 v[152:155], %1, %2, %4 offen offset:2048 sc1\n\tbuffer_load_dwordx4 v[156:159], %1, %2, %4 offen offset:3072 sc1"
                     : "+{v[128:159]}"(vx) : "v"(l16), "s"(rp), "s"(sv), "s"(sv + 4 * BLK) : "memory");
    };
    vx_fetch(0);
    unsigned budget = 1u << 18, tpub = 0;
    STDECL;
    LDECL;
    for (int t = 0; t < NSEG; ++t) {
        const unsigned slot = t & (NSLOT - 1);
        ST(0);
        while ((unsigned)__builtin_amdgcn_s_memtime() - tpub < RTD) __builtin_amdgcn_s_sleep(1);
        u32x8 vm, pv;
        u32x4 pm;
        {
            unsigned a;
            ASM_ST_DECL;
            asm volatile(ASM_RCRIT : "=&{v[176:183]}"(vm), "=&{v[184:191]}"(pv), "=&{v[192:195]}"(pm), [a] "=&s"(a), [n] "+s"(budget) ASM_ST_OUT ASM_ST2_OUT ASM_ST3_OUT
                         : [rs] "s"(rr), [ov] "v"(offv), [sv] "s"(slot * VM_SLOT), [o0] "v"(offp[0]), [o2] "v"(offp[2]), [sp] "s"(slot * XP_SLOT),
                           "{v[128:159]}"(vx), [sidx] "v"(sidx), [rw] "v"(rw0), [rd] "v"(rrd), [oo] "v"(offo),
                           [so] "s"(((t + 1) & (NSLOT - 1)) * XM_SLOT), [pub] "s"(NSEG - 1 - t)
                         : "memory", "vcc", "scc", "v160", "v161", "v162", "v163", "v164", "v165", "v166", "v167", "v196", "v197", "v200", "v201", "v202", "v203", "v204", "v205", "v206", "v207");
            ASM_ST_SAVE(2); ASM_ST2_SAVE(1); ASM_ST3_SAVE(6);
        }
        ST(3);
        LA();
        MARK(!L && jj == 0 && wave == 0 && t < 4, 10 + t);
        PMARK(wave == 0, L * 7, t - 1 + L);
        tpub = (unsigned)__builtin_amdgcn_s_memtime();
        __builtin_amdgcn_s_sleep(RXDEL);
        unsigned early;
#pragma unroll 1
        for (int spins = 1023;; spins += 1024) {
            unsigned n;
            const unsigned sx = pgb(t, par) + blin(((wave >> 1) & 1) * 20480 + jj * 1024) + (wave & 1) * 512;
            const unsigned pxb = RG_PX + slot * PX_SLOT + wave * BLK, sv = (((t + 1) & 7) << 22) + vxo;
            ASM_ST_DECL;
            asm volatile(ASM_RX : "+{v[128:159]}"(vx), [n] "=&s"(n), [a] "=&s"(early) ASM_ST_OUT ASM_ST2_OUT
                         : "{v[176:183]}"(vm), "{v[184:191]}"(pv), "{v[192:195]}"(pm), [l16] "v"(l16), [rs] "s"(rr), [s0] "s"(pxb), [s1] "s"(pxb + 4096), [rd] "s"(rd), [rx] "s"(rd), [xo] "v"(xo),
                           [fo] "v"(fo), [fs] "s"(fs), [tn] "s"(t + 1), [sv0] "s"(sv), [sv1] "s"(sv + 4 * BLK), [rp] "s"(rp), [sx] "s"(PG_XR + L * PG_LX + sx), [sa] "s"(PG_XA + sx),
                           [pw] "v"(pw), [pr] "v"(pr), [fin] "s"(fin)
                         : "memory", "vcc", "scc", "v96", "v97", "v98", "v99", "v100", "v101", "v102", "v103", "v104", "v105", "v106", "v107", "v108", "v109", "v110", "v111", "v112", "v113", "v114", "v115", "v116", "v117", "v118", "v119", "v120", "v121", "v122", "v123", "v124", "v125", "v126", "v127", "v196", "v200", "v201", "v202", "v203", "v204", "v205", "v206", "v207", "v208", "v209", "v210", "v211", "v212", "v213", "v214", "v215", "v216", "v217", "v218", "v219", "v220", "v221", "v222", "v223", "v224", "v225", "v226", "v227", "v228", "v229", "v230", "v231", "v240", "v241", "v242", "v243", "v248", "v250", "v251");
            ASM_ST_SAVE(4); ASM_ST2_SAVE(5);
            if (__builtin_amdgcn_readfirstlane(n)) break;
            early = 0;
            if (spin_fail(spins, P)) { bar(); break; }
        }
        if (t + 1 < NSEG && !__builtin_amdgcn_readfirstlane(early)) vx_fetch(t + 1);
        PMARK(!L && wave == 7 && PMK == 1, 1, t - 1);
        st128(sent4(), rr, offv, slot * VM_SLOT); st128(sent4(), rr, offv + 1024, slot * VM_SLOT);
        __builtin_amdgcn_raw_buffer_store_b32(SENT, rr, offo, slot * XM_SLOT, 16);
        if (wave < 2 && t + AH < NSEG) st128(sent4(), rd, l16, PG_XA + pgb(t + AH, par) + blin(wave * 20480 + jj * 1024));
        ST(7);
        LB(t);
        STFLUSH(t);
    }
    LF();
}

#undef STROLE
#define STROLE 16
template <int WHICH>
__device__ __forceinline__ void w_role(const Params& P, int wave, int L, int cu, int x) {
    constexpr int KS = 20, N = (WHICH == 3) ? FF : DM, NU = 2 * NSEG;
    constexpr unsigned D_IN = WHICH == 1 ? PG_XA : WHICH == 2 ? PG_HS : PG_X1;
    constexpr unsigned D_OUT = WHICH == 1 ? PG_HS : WHICH == 2 ? PG_X1 : PG_H2;
    if (WHICH != 3 && cu * 8 + wave >= 20) {
        for (int u = 0; u <= NU; ++u) bar();
        return;
    }
    auto run = [&](auto Cc) {
    constexpr bool LO = decltype(Cc)::value;
    const int ln = lane_id(), l15 = ln & 15, l4 = ln >> 4;
    const unsigned l16 = ln * 16;
    const rsrc_t rd = mkrsrc(P.ws + PG0, PG_L);
    const int nt0 = (cu * 8 + wave) * 2;
    u32x32 w[5];
    {
        u32x4 f[2 * KS];
        load_wn<2, KS, N>(f, P.w[3 + WHICH], DM, wave, nt0, 0);
#pragma unroll
        for (int n = 0; n < 5; ++n) {
            u32x8 q[4];
#pragma unroll
            for (int i = 0; i < 4; ++i) q[i] = __builtin_shufflevector(f[n * 8 + 2 * i], f[n * 8 + 2 * i + 1], 0, 1, 2, 3, 4, 5, 6, 7);
            w[n] = cat32(q[0], q[1], q[2], q[3]);
        }
        asm volatile("" : "+{v[0:31]}"(w[0]), "+{v[32:63]}"(w[1]), "+{v[64:95]}"(w[2]), "+{v[96:127]}"(w[3]), "+{v[128:159]}"(w[4]));
    }
    PLACE();
    bar();
    const int ocol = nt0 * 16 + (l4 & 1) * 16 + (l4 >> 1) * 8;
    const unsigned o3 = (unsigned)((l4 & 1) * 512 + (l4 >> 1) * 256 + l15 * 16), ro = (l4 >> 1) * 256 + l15 * 16 + (l4 & 1) * 8;
    const int grp = nt0 / 10, sh = ((grp & 1) ? -1 : 1) * ((grp >> 1) + 1);
    if (WHICH == 1) {
        unsigned zv;
        asm volatile("v_mov_b32 %0, 0" : "=v"(zv));
        st128(((u32x4){zv, zv, zv, zv}), rd, l15 <= (grp >> 1) ? toff((grp & 1) ? S_ - 1 - l15 : l15, ocol, par) : OOB, PG_HS);
    }
    auto in0 = [&](int u) { return D_IN + pgb(u >> 1, par) + blin((u & 1) * 20480 + wave * 4096); };
    auto in1 = [&](int u) { return D_IN + pgb(u >> 1, par) + blin((u & 1) * 20480 + 16384) + wave * 1024; };
    u32x16 va;
    u32x4 vb;
    u32x8 cs, k8;
#define WT_W "{v[0:31]}"(w[0]), "{v[32:63]}"(w[1]), "{v[64:95]}"(w[2]), "{v[96:127]}"(w[3]), "{v[128:159]}"(w[4])
    asm volatile("v_mov_b32 v216, 0x3f803f80\n\tv_mov_b32 v217, v216\n\tv_mov_b32 v218, v216\n\tv_mov_b32 v219, v216\n\tv_mov_b32 v220, %[c0]\n\tv_mov_b32 v232, -1\n\tv_mov_b32 v236, -1\n\t"
                 "v_mov_b32 v240, -1\n\tv_mov_b32 v244, -1\n\tv_mov_b32 v248, -1\n\t" ASM_WCS
                 : "=&{v[196:203]}"(cs), "=&{v[216:223]}"(k8), "=&{v[232:247]}"(va), "=&{v[248:251]}"(vb) : WT_W, [c0] "s"(-2.0f * 0.7978845608028654f * 1.4426950408889634f));
    unsigned la = l16, lw = wave * 8192 + l16, sr = l4 * 8192 + ((l15 >> 2) * 16 + l15) * 16 + (l15 & 3) * 4, budget = 1u << 18;
    wait_vm<0>();
    STDECL;
    LDECL;
#pragma unroll 1
    for (int u = 0; u < NU; ++u) {
        const int mt = u & 1;
        const unsigned pg = pgb(u >> 1, par), c3 = D_OUT + blin(mt * (N * 32) + (nt0 >> 1) * 1024);
        unsigned oa = o3, ob = 0, so = pg + c3, sa = u + 2 * AH < NU ? pgb((u >> 1) + AH, par) + c3 : pg + PG_SS + 4096, a;
        if (WHICH == 1) {
            const int orow = u * 16 + l15 + sh;
            ob = toff(orow + AH * SEG, ocol, par);
            oa = (unsigned)orow < (unsigned)S_ ? (ob ^ 8192u) - (AH << 21) : OOB;
            if (orow + AH * SEG >= S_ + (sh < 0 ? sh : 0)) ob = OOB;
            so = sa = D_OUT;
        }
        if (mt) { ST(0); LB(u >> 1); } else { LA(); }
        ASM_ST_DECL;
#define WT_A WT_W, [la] "v"(la), [l16] "v"(l16), [oa] "v"(oa), [rs] "s"(rd), [so] "s"(so), [sa] "s"(sa)
#define WT_G [sr] "v"(sr), [kd] "s"(1.0f / DM), [ke] "s"(1e-5f), [k1] "s"(-2.0f * 0.7978845608028654f * 1.4426950408889634f * 0.044715f)
#define WT_1 WT_G, [ob] "v"(ob), [ss] "s"(pg + PG_SS + mt * 128), [sw] "s"(nt0)
#define WT_2 [ro] "v"(ro), [sx] "s"(pg + PG_XA + blin(mt * 20480 + nt0 * 512)), [ss] "s"(pg + PG_SS + mt * 128)
#define WT_C "memory", "vcc", "scc", "v160", "v161", "v162", "v163", "v164", "v165", "v166", "v167", "v168", "v169", "v170", "v171", "v172", "v173", "v174", "v175", "v176", "v177", "v178", "v179", "v180", "v181", "v182", "v183", "v184", "v185", "v186", "v187", "v188", "v189", "v190", "v191", "v192", "v193", "v194", "v195", "v204", "v205", "v206", "v207", "v208", "v209", "v210", "v211", "v212", "v213", "v214", "v215", "v252"
#define WT_LO(A, ...) asm volatile(A : "+{v[196:203]}"(cs), "+{v[216:223]}"(k8), "+{v[232:247]}"(va), "+{v[248:251]}"(vb), [n] "+s"(budget), [a] "=&s"(a) ASM_ST_OUT ASM_ST2_OUT ASM_ST3_OUT \
                              : WT_A, __VA_ARGS__, [lw] "v"(lw), [i0] "s"(in0(u)), [i1] "s"(in1(u)), [n0] "s"(in0(u + 1)), [n1] "s"(in1(u + 1)) : WT_C)
#define WT_HI(A, ...) asm volatile(A : "+{v[196:203]}"(cs), "+{v[216:223]}"(k8) ASM_ST_OUT ASM_ST3_OUT : WT_A, __VA_ARGS__ : WT_C)
        if constexpr (LO) { if constexpr (WHICH == 1) WT_LO(ASM_WT1_LO, WT_1); else if constexpr (WHICH == 2) WT_LO(ASM_WT2_LO, WT_2); else WT_LO(ASM_WT3_LO, WT_G); }
        else { if constexpr (WHICH == 1) WT_HI(ASM_WT1_HI, WT_1); else if constexpr (WHICH == 2) WT_HI(ASM_WT2_HI, WT_2); else WT_HI(ASM_WT3_HI, WT_G); }
        la ^= 32768u; lw ^= 32768u; sr ^= 32768u;
        PMARK(!L && mt == (WHICH != 1) && (PMK == 1 || WHICH == 3), PMK == 1 ? 1 + WHICH : 1, (u >> 1) - (WHICH == 1));
    }
    wait_vm<0>();
    if constexpr (LO) asm volatile("" :: "{v[232:247]}"(va), "{v[248:251]}"(vb));
    LF();
    };
    if (wave < 4) run(std::true_type{}); else run(std::false_type{});
}

__device__ __forceinline__ void w4_role(const Params& P, int wave, int L, int cu, int x) {
    constexpr unsigned W_FL = 122880;
    const int ln = lane_id(), l15 = ln & 15, l4 = ln >> 4;
    const unsigned l16 = ln * 16;
    const rsrc_t rd = mkrsrc(P.ws + PG0, PG_L);
    lds_st<unsigned>(W_FL + wave * 4, 0u);
    STDECL;
    if (wave < 6) {
        u32x32 w[5];
        {
            u32x4 g[40];
            load_wn<5, 8, DM>(g, P.w[7], FF, wave, cu * 5, wave * 8);
#pragma unroll
            for (int n = 0; n < 5; ++n) {
                u32x8 f[4];
#pragma unroll
                for (int i = 0; i < 4; ++i) f[i] = __builtin_shufflevector(g[n * 8 + 2 * i], g[n * 8 + 2 * i + 1], 0, 1, 2, 3, 4, 5, 6, 7);
                w[n] = cat32(f[0], f[1], f[2], f[3]);
            }
            asm volatile("" : "+{v[0:31]}"(w[0]), "+{v[32:63]}"(w[1]), "+{v[64:95]}"(w[2]), "+{v[96:127]}"(w[3]), "+{v[128:159]}"(w[4]));
        }
        PLACE();
        bar();
        unsigned budget = 1u << 18;
        u32x32 av;
        asm volatile("v_mov_b32 v160, -1\n\tv_mov_b32 v164, -1\n\tv_mov_b32 v168, -1\n\tv_mov_b32 v172, -1\n\tv_mov_b32 v176, -1\n\tv_mov_b32 v180, -1\n\tv_mov_b32 v184, -1\n\tv_mov_b32 v188, -1" : "={v[160:191]}"(av));
        for (int u = 0; u < 2 * NSEG; ++u) {
            if (u & 1) ST(0);
            for (int spins = 0;; ++spins) {
                asm volatile("" ::: "memory");
                if ((int)__builtin_amdgcn_readfirstlane(lds_ld<unsigned>(W_FL + 24 + (u & 1) * 4)) + 3 >= u) break;
                __builtin_amdgcn_s_sleep(2);
                if (spin_fail(spins, P)) break;
            }
            const unsigned gb = PG_H2 + pgb(u >> 1, par) + ((u & 1) * 6 + wave) * BLK, gn = PG_H2 + pgb((u + 1) >> 1, par) + ((~u & 1) * 6 + wave) * BLK;
            ASM_ST_DECL;
            asm volatile(ASM_W4_M : [n] "+s"(budget), "+{v[160:191]}"(av) ASM_ST2_OUT
                         : "{v[0:31]}"(w[0]), "{v[32:63]}"(w[1]), "{v[64:95]}"(w[2]), "{v[96:127]}"(w[3]), "{v[128:159]}"(w[4]),
                           [ld] "v"(l16 + wave * 1024 + (u & 3) * 30720), [l16] "v"(l16), [rs] "s"(rd), [s0] "s"(gb), [s1] "s"(gb + 4096), [n0] "s"(gn), [n1] "s"(gn + 4096), [un] "s"(u + 1), [fl] "v"(W_FL + wave * 4)
                         : "memory", "vcc", "scc", "v192", "v193", "v194", "v195", "v196", "v197", "v198", "v199", "v200", "v201", "v202", "v203", "v204", "v205", "v206", "v207", "v208", "v209", "v210", "v211", "v212");
            PMARK(!L && (u & 1) && PMK == 2, 2, u >> 1);
        }
        asm volatile("s_waitcnt vmcnt(0)" :: "{v[160:191]}"(av) : "memory");
    } else {
        const int mt = wave - 6;
        PLACE();
        bar();
        const unsigned c0 = mt * 20480 + cu * 2560, o8 = (l4 >> 1) * 256 + l15 * 16 + (l4 & 1) * 8;
        unsigned budget = 1u << 18;
        u32x16 ra;
        u32x4 rb;
        asm volatile("v_mov_b32 v164, -1" : "={v[164:179]}"(ra), "={v[180:183]}"(rb));
        __builtin_amdgcn_s_setprio(W4P);
#define W4F_OUT "+{v[164:179]}"(ra), "+{v[180:183]}"(rb), [n] "+s"(budget)
#define W4F_IN [o0] "v"(o8 + blin(c0)), [o1] "v"(o8 + blin(c0 + 512)), [o2] "v"(o8 + blin(c0 + 1024)), [o3] "v"(o8 + blin(c0 + 1536)), [o4] "v"(o8 + blin(c0 + 2048)), [rd] "s"(rd), \
               [c1] "s"(PG_X1 + pg), [ca] "s"(PG_XA + pg), [n1] "s"(PG_X1 + pn), [na] "s"(PG_XA + pn), [f0] "v"(W_FL + (ln & 3) * 4), [f1] "v"(W_FL + 16 + (ln & 1) * 4), [fd] "v"(W_FL + wave * 4), \
               [la] "v"((u & 3) * 30720 + l16), [u] "s"(u), [un] "s"(u + 1)
#define W4F_CLOB "memory", "vcc", "scc", "v64", "v65", "v66", "v67", "v68", "v69", "v70", "v71", "v72", "v73", "v74", "v75", "v76", "v77", "v78", "v79", "v80", "v81", "v82", "v83", "v84", "v85", "v86", "v87", "v88", "v89", "v90", "v91", "v92", "v93", "v94", "v95", "v96", "v97", "v98", "v99", "v100", "v101", "v102", "v103", "v104", "v105", "v106", "v107", "v108", "v109", "v110", "v111", "v112", "v113", "v114", "v115", "v116", "v117", "v118", "v119", "v120", "v121", "v122", "v123", "v124", "v125", "v126", "v127", "v128", "v129", "v130", "v131", "v132", "v133", "v134", "v135", "v136", "v137", "v138", "v139", "v140", "v141", "v142", "v143", "v144", "v145", "v146", "v147", "v148", "v149", "v150", "v151", "v152", "v153", "v154", "v155", "v156", "v157", "v158", "v159", "v160", "v161", "v162", "v163", "v184", "v185", "v186", "v187", "v188", "v189", "v190", "v191", "v192", "v193", "v194", "v195", "v196", "v197", "v198", "v199", "v200"
        if (L) {
            const rsrc_t ro = mkrsrc(P.out, ACT);
#pragma unroll 1
            for (int t = 0; t < NSEG; ++t) {
                const unsigned u = t * 2 + mt, pg = pgb(t, par), pn = pgb(t + 1, par);
                ASM_ST_DECL;
                asm volatile(ASM_W4F_1 : W4F_OUT ASM_ST2_OUT : W4F_IN, [pa] "v"(l15 * (DM * 2) + (l4 & 1) * 32 + (l4 >> 1) * 16), [pb] "v"(l15 * (DM * 2) + l4 * 8), [ro] "s"(ro), [so] "s"(u * 20480 + cu * 160) : W4F_CLOB);
            }
        } else {
            const rsrc_t rn = mkrsrc(P.ws + (par ^ 1) * 8192, RG_L);
#pragma unroll 1
            for (int t = 0; t < NSEG; ++t) {
                const unsigned u = t * 2 + mt, pg = pgb(t, par), pn = pgb(t + 1, par), sn = (pg ^ 8192) + PG_LX, st = RG_ST + (u >> 3) * BLK + (u & 7) * 1024;
                ASM_ST_DECL;
                asm volatile(ASM_W4F_0 : W4F_OUT ASM_ST_OUT ASM_ST2_OUT ASM_ST3_OUT
                             : W4F_IN, [pa] "v"(blin(c0 + (l4 & 1) * 512) + (l4 >> 1) * 256 + l15 * 16), [pb] "v"(blin(c0 + 1024 + (l4 & 1) * 512) + (l4 >> 1) * 256 + l15 * 16),
                               [so] "v"(ln < 16 ? ln * 8 : OOB), [si] "v"(ln * 8), [rr] "s"(rr), [sp] "s"(st + cu * 128), [sq] "s"(st), [xr] "s"(PG_XR + sn), [xn] "s"(PG_XN + sn),
                               [kd] "s"(1.0f / DM), [ke] "s"(1e-5f), [tn] "s"(t + 1), [fo] "v"(ln < 32 ? (ln >> 3) * 2048 + (ln & 7) * 16 : OOB), [rn] "s"(rn), [fs] "s"(RG_FX + (cu * 2 + mt) * 128) : W4F_CLOB);
            }
        }
        asm volatile("s_waitcnt vmcnt(0)" :: "{v[164:179]}"(ra), "{v[180:183]}"(rb) : "memory");
    }
}

__device__ __forceinline__ void setup(const Params& P, int wave, int mt, int par0, int ph) {
    const bool xw = wave < 4 && (mt >= 2 * XS0) == (ph != 0);
    const int tid = threadIdx.x & 255, r15 = tid & 15, slot = tid >> 4;
    const rsrc_t rw = mkrsrc(P.ws, WS_END);
    u32x4 v[5];
    float s = 0.f, q = 0.f;
    if (xw) {
#pragma unroll
        for (int c = 0; c < 5; ++c) v[c] = *(const u32x4*)(P.x + (size_t)(mt * 16 + r15) * DM + (slot * 5 + c) * 8);
#pragma unroll
        for (int c = 0; c < 5; ++c)
#pragma unroll
            for (int e = 0; e < 4; ++e) s += lo2f(v[c][e]) + hi2f(v[c][e]);
        lds_st<float>((slot * 17 + r15) * 4, s);
    } else if (wave >= 4) {
        constexpr unsigned N0 = RG_ST / BLK, N1 = 4 * N0 + 16, N2 = PG_SS / BLK;
        const u32x4 s4 = sent4();
        for (unsigned k = mt; k < 2 * (N1 + 4 * N2); k += 128) {
            const unsigned i = k >> 1;
            const unsigned o = i < N1 ? (i < 4 * N0 ? (i / N0) * RG_SLOT + (i % N0) * BLK : (i - 3 * N0) * BLK) + (par0 ^ ph) * 8192 : PG0 + pgb((i - N1) / N2, par0 ^ ph) + ((i - N1) % N2) * BLK;
            st128(s4, rw, tid * 16, o + (k & 1) * 4096);
        }
    }
    bar();
    float mean = 0.f;
    if (xw) {
        s = 0.f;
#pragma unroll
        for (int k = 0; k < 16; ++k) s += lds_ld<float>((k * 17 + r15) * 4);
        mean = s * (1.0f / DM);
#pragma unroll
        for (int c = 0; c < 5; ++c)
#pragma unroll
            for (int e = 0; e < 4; ++e) { const float a = lo2f(v[c][e]) - mean, b = hi2f(v[c][e]) - mean; q += a * a + b * b; }
        lds_st<float>(2048 + (slot * 17 + r15) * 4, q);
    }
    bar();
    if (xw) {
        q = 0.f;
#pragma unroll
        for (int k = 0; k < 16; ++k) q += lds_ld<float>(2048 + (k * 17 + r15) * 4);
        const float m1 = rbf(mean), r = 1.0f / ln_den(q * (1.0f / DM));
#pragma unroll
        for (int c = 0; c < 5; ++c) {
            const unsigned o = toff(mt * 16 + r15, (slot * 5 + c) * 8, par0);
            st128(v[c], rw, o, PG0 + PG_XR);
            u32x4 n;
#pragma unroll
            for (int e = 0; e < 4; ++e) n[e] = pack2(rbf(lo2f(v[c][e]) - m1) * r, rbf(hi2f(v[c][e]) - m1) * r);
            st128(n, rw, o, PG0 + PG_XN);
        }
    }
    wait_vm<0>();
    bar();
    if (threadIdx.x < 8) __hip_atomic_fetch_add((unsigned*)(P.ws + FL0 + FL_IN + (ph * 8 + threadIdx.x) * 128), 1u, RLX_AGENT);
}

__global__ __launch_bounds__(512, 2) void mega_kernel(Params P) {
    const int wave = __builtin_amdgcn_readfirstlane(threadIdx.x >> 6);
    u32x4 cpf;
    {
        unsigned long long pc;
        unsigned len;
        asm volatile("s_getpc_b64 %0\n\ts_mov_b32 %1, _Z12prep1_kernel5Prep1-_Z11mega_kernel6Params" : "=s"(pc), "=s"(len));
        cpf = __builtin_amdgcn_raw_buffer_load_b128(mkrsrc((const void*)(pc & ~255ull), len), (blockIdx.x >> 3) * 8192 + threadIdx.x * 16, 0, 0);
    }
    if (threadIdx.x == 0) {
        unsigned g = (unsigned)__builtin_amdgcn_s_getreg((3 << 11) | 20) & 7u, k;
        while ((k = __hip_atomic_fetch_add((unsigned*)(P.ws + FL0 + FL_TK + g * 128), 1u, RLX_AGENT)) >= 32u) g = (g + 1) & 7u;
        lds_st<unsigned>(0, g << 5 | k);
    }
    bar();
    const unsigned tk = __builtin_amdgcn_readfirstlane(lds_ld<unsigned>(0));
    asm volatile("" :: "v"(cpf));
    bar();
    const int g4 = (tk >> 5) & 3, k = tk & 31;
    const int L = tk >> 7, x = g4, r = k;
    if (L) {
        if (tk == 255 && wave == 0) {
        const rsrc_t r = mkrsrc(P.ws, RG_SLOT);
        auto probe = [&](unsigned base, unsigned bit, int n, unsigned& m0, unsigned& m1) {
            m0 = m1 = ~0u;
            for (int i = 0; i < n; ++i) {
                const unsigned long long t0 = __builtin_amdgcn_s_memtime();
                __builtin_amdgcn_raw_buffer_store_b32(0u, r, base + (i & 1) * bit + ((i >> 2) & 1) * 4096 + (i >> 2) * 768, 0, 16);
                asm volatile("s_waitcnt vmcnt(0)" ::: "memory");
                const unsigned d = (unsigned)(__builtin_amdgcn_s_memtime() - t0);
                if (i & 1) m1 = d < m1 ? d : m1; else m0 = d < m0 ? d : m0;
            }
        };
            unsigned a0, a1;
            unsigned* c = (unsigned*)(P.ws + FL0 + FL_IN + (threadIdx.x & 15) * 128);
            probe(96 * BLK, 8192, CAL1, a0, a1);
            const unsigned xc = (unsigned)__builtin_amdgcn_s_getreg((3 << 11) | 20), h = a0 > a1 ? 1u : 0u;
            if (threadIdx.x < 16) __hip_atomic_fetch_add(c, (2u + (h ^ ((xc >> 2) & 1u))) << 16, RLX_AGENT);
            wait_vm<0>();
            probe(96 * BLK + h * 8192, BLK, CAL2, a0, a1);
            if (threadIdx.x < 16) __hip_atomic_fetch_add(c, (2u + ((a0 > a1 ? 0u : 1u) ^ (xc & 1u))) << 20, RLX_AGENT);
        }
        const int par0 = (init_wait(P, wave, 1, 0x20000u) >> 16) & 1;
#pragma unroll 1
        for (int ph = 0; ph < 2; ++ph) {
            setup(P, wave, (tk & 31) * 4 + g4, par0, ph);
            for (int k = ph * DLY_L; k < DLY_L; ++k) __builtin_amdgcn_s_sleep(32);
        }
    }
    {
        const int i = (r - 26) * 4 + x;
        const int d = (r < 8 ? DLY_S : r < 21 ? 0 : i < 4 ? DLY_H : DLY_W);
        for (int k = 0; k < d; ++k) __builtin_amdgcn_s_sleep(32);
    }
    if (r < 8) s_role(P, wave, L, x * 8 + r, x);
    else if (r < 16) v_role(P, wave ^ 4, L, x * 8 + r - 8, x);
    else if (r < 21) r_role(P, wave, L, r - 16, x);
    else {
        const int i = (r - 26) * 4 + x;
        if (i < 4) h_role(P, wave, L, r < 25 ? x * 4 + r - 21 : 16 + x * 2 + r - 25, x);
        else if (i < 7) w_role<1>(P, wave, L, i - 4, x);
        else if (i < 10) w_role<2>(P, wave, L, i - 7, x);
        else if (i < 16) w_role<3>(P, wave, L, i - 10, x);
        else w4_role(P, wave, L, i - 16, x);
    }
}

struct Prep1 { char* ws; };
__global__ __launch_bounds__(256) void prep1_kernel(Prep1 A) {
    int b = blockIdx.x;
    ((u32x4*)(A.ws + RG_FV))[b * 256 + threadIdx.x] = (u32x4){0u, 0u, 0u, 0u};
    if (b == 0 && threadIdx.x < FL_BYTES / 16) ((u32x4*)(A.ws + FL0))[threadIdx.x] = (u32x4){0u, 0u, 0u, 0u};
}

extern "C" void kernel_launch(void* const* d_in, const int* in_sizes, int n_in, void* d_out, int out_size, void* d_ws,
                              size_t ws_size, hipStream_t stream) {
    char* ws = (char*)d_ws;
    Params P;
    P.ws = ws; P.x = (const bf16_t*)d_in[0]; P.mem0 = (const bf16_t*)d_in[1]; P.out = (bf16_t*)d_out;
    for (int i = 0; i < 8; ++i) P.w[i] = (const bf16_t*)d_in[i < 4 ? 2 + i : 4 + i];
    (void)hipFuncSetAttribute((const void*)mega_kernel, hipFuncAttributeMaxDynamicSharedMemorySize, MEGA_LDS);

    Prep1 A1;
    A1.ws = ws;
    prep1_kernel<<<20, 256, 0, stream>>>(A1);
    mega_kernel<<<256, 512, MEGA_LDS, stream>>>(P);
}
```

```cpp
#include <hip/hip_runtime.h>
#include <stdint.h>
#include <type_traits>

typedef unsigned short bf16_t;
using bf16x2 = __attribute__((ext_vector_type(2))) __bf16;
using bf16x8 = __attribute__((ext_vector_type(8))) __bf16;
using bf16x16 = __attribute__((ext_vector_type(16))) __bf16;
using f32x2 = __attribute__((ext_vector_type(2))) float;
using f32x4 = __attribute__((ext_vector_type(4))) float;
using u32x2 = __attribute__((ext_vector_type(2))) unsigned;
using u32x4 = __attribute__((ext_vector_type(4))) unsigned;
typedef __amdgpu_buffer_rsrc_t rsrc_t;

__device__ __forceinline__ unsigned pack2(float a, float b) {
    bf16x2 r = __builtin_convertvector((f32x2){a, b}, bf16x2);
    return __builtin_bit_cast(unsigned, r);
}
__device__ __forceinline__ float rbf(float f) { return __uint_as_float(pack2(f, 0.f) << 16); }
__device__ __forceinline__ float lo2f(unsigned u) { return __uint_as_float(u << 16); }
__device__ __forceinline__ float hi2f(unsigned u) { return __uint_as_float(u & 0xFFFF0000u); }
__device__ __forceinline__ rsrc_t mkrsrc(const void* p, unsigned bytes) {
    return __builtin_amdgcn_make_buffer_rsrc((void*)p, 0, bytes, 0x00020000);
}

constexpr int S_ = 2048, DM = 640, NH = 32, DH = 192, HD = NH * DH  , SEG = 32, NSEG = 64;
constexpr int FF = 1536;

#define XMAP
#define NCPY 4
constexpr unsigned NSLOT = 4, NCP = NCPY;
#define FL_TK_DEF
#define DLY_L 12
#define DLY_S 1
#define DLY_W 10
#define DLY_H 2
#define EWAKE "s_nop 0"
#define LWAKE "s_wakeup\n\t"
#define SLP4 (wave == 4 ? t : 0)
#define WPF 12
#define WPRI 1
#define WPRI2 0
#define RTD 3500
#define VTD 3900
#define HAH 100
#define WEAK 0
#define BAL 0
#define HSL 8
#define OAUX 2
#define W4P 3
#define STD 3800
#define CAL1 4
#define CAL2 12
#define CALN 12
#define RSL 9
#define VTRY 0
#define VSL 1
#define NRR 5
constexpr unsigned BLK = 16384, RG_SLOT = 4u << 20;
constexpr unsigned XP_PAIR = 6 * BLK, XP_SLOT = RG_SLOT;
constexpr unsigned XM_PAIR = 4 * BLK, XM_SLOT = RG_SLOT;
constexpr unsigned VM_SLOT = RG_SLOT;
constexpr unsigned PX_SLOT = RG_SLOT;
constexpr unsigned RG_XP = 0, RG_XM = 12 * BLK, RG_VM = 20 * BLK, RG_PX = 60 * BLK;
constexpr unsigned RG_ST = 68 * BLK, RG_FV = 84 * BLK, RG_FH = 86 * BLK, RG_FX = 88 * BLK, RG_L = NSLOT * RG_SLOT;
constexpr unsigned FL0 = 89 * BLK, FL_TK = 512, FL_IN = FL_TK + 8 * 128, FL_BYTES = FL_IN + 16 * 128;
constexpr unsigned ACT = S_ * DM * 2;
#define PXROT 5
#define W4SL 5
#define RXDEL 1
#define VXD 32
#define VXAUX 16
constexpr unsigned PG0 = RG_L, PG_L = 80u << 21;
constexpr unsigned PG_XA = 0, PG_HS = 5 * BLK, PG_X1 = 10 * BLK, PG_H2 = 15 * BLK, PG_SS = 27 * BLK, PG_XR = 28 * BLK, PG_XN = 33 * BLK, PG_LX = 10 * BLK, PG_HV = 48 * BLK;
__device__ __forceinline__ unsigned pgb(unsigned p, int par) { return p << 21 | ((p ^ par) & 1) << 13; }
__device__ __forceinline__ constexpr unsigned blin(unsigned o) { return o + (o & ~8191u); }
constexpr int HT0 = 2;
constexpr int AH = 3;
constexpr unsigned WS_END = PG0 + PG_L;

#define SB() __builtin_amdgcn_sched_barrier(0)
#define LAS __attribute__((address_space(3)))
template <class T> __device__ __forceinline__ T lds_ld(unsigned a) { return *(const LAS T*)(a); }
template <class T> __device__ __forceinline__ void lds_st(unsigned a, T v) { *(LAS T*)(a) = v; }
constexpr unsigned OOB = 0x7FFFFF00u;
template <int N> __device__ __forceinline__ void wait_vm() { asm volatile("s_waitcnt vmcnt(%0)" :: "i"(N) : "memory"); }
__device__ __forceinline__ void bar() {
    asm volatile("s_waitcnt lgkmcnt(0)" ::: "memory");
    __builtin_amdgcn_s_barrier();
    asm volatile("" ::: "memory");
}
__device__ __forceinline__ int lane_id() {
    int ln;
    asm volatile("v_mbcnt_lo_u32_b32 %0, -1, 0\n\tv_mbcnt_hi_u32_b32 %0, -1, %0" : "=v"(ln));
    return ln;
}
__device__ __forceinline__ void swap16(unsigned& a, unsigned& b) { asm volatile("s_nop 3\n\tv_permlane16_swap_b32 %0, %1\n\ts_nop 1" : "+v"(a), "+v"(b)); }
__device__ __forceinline__ void swap32(unsigned& a, unsigned& b) { asm volatile("s_nop 3\n\tv_permlane32_swap_b32 %0, %1\n\ts_nop 1" : "+v"(a), "+v"(b)); }
__device__ __forceinline__ float sum_x16(float x) { unsigned a = __float_as_uint(x), b = a; swap16(a, b); return __uint_as_float(a) + __uint_as_float(b); }
__device__ __forceinline__ float sum_x32(float x) { unsigned a = __float_as_uint(x), b = a; swap32(a, b); return __uint_as_float(a) + __uint_as_float(b); }
__device__ __forceinline__ float max_x16(float x) { unsigned a = __float_as_uint(x), b = a; swap16(a, b); return __builtin_fmaxf(__uint_as_float(a), __uint_as_float(b)); }
__device__ __forceinline__ float max_x32(float x) { unsigned a = __float_as_uint(x), b = a; swap32(a, b); return __builtin_fmaxf(__uint_as_float(a), __uint_as_float(b)); }
__device__ __forceinline__ void sum2_rows(float& s, float& q) {
    unsigned a = __float_as_uint(s), b = a, c = __float_as_uint(q), d = c;
    asm volatile("s_nop 2\n\tv_permlane16_swap_b32 %0, %1\n\tv_permlane16_swap_b32 %2, %3\n\tv_add_f32 %0, %0, %1\n\tv_add_f32 %2, %2, %3\n\tv_mov_b32 %1, %0\n\tv_mov_b32 %3, %2\n\t"
                 "s_nop 1\n\tv_permlane32_swap_b32 %0, %1\n\tv_permlane32_swap_b32 %2, %3\n\tv_add_f32 %0, %0, %1\n\tv_add_f32 %2, %2, %3" : "+v"(a), "+v"(b), "+v"(c), "+v"(d));
    s = __uint_as_float(a); q = __uint_as_float(c);
}
__device__ __forceinline__ u32x4 merge16(u32x2 a, u32x2 b) {
    unsigned a0 = a[0], a1 = a[1], b0 = b[0], b1 = b[1];
    asm volatile("s_nop 3\n\tv_permlane16_swap_b32 %0, %2\n\tv_permlane16_swap_b32 %1, %3\n\ts_nop 1" : "+v"(a0), "+v"(a1), "+v"(b0), "+v"(b1));
    return (u32x4){a0, a1, b0, b1};
}
template <int AUXV>
__device__ __forceinline__ void st128_(u32x4 v, rsrc_t r, unsigned voff, unsigned soff) {
    __builtin_amdgcn_raw_buffer_store_b128(v, r, voff, soff, AUXV);
    asm volatile("s_nop 1" :: "v"(v) : "memory");
}
#define st128(V, R, VO_, SO_) st128_<16>(V, R, VO_, SO_)
#define LD128(R, VO_, SO_) __builtin_amdgcn_raw_buffer_load_b128(R, VO_, SO_, 16)
#define LD64(R, VO_, SO_) __builtin_amdgcn_raw_buffer_load_b64(R, VO_, SO_, 16)
#define BF8(X) __builtin_bit_cast(bf16x8, X)
__device__ __forceinline__ float gelu_tanh(float x) {
    const float c0 = -2.0f * 0.7978845608028654f * 1.4426950408889634f, c1 = c0 * 0.044715f;
    const float e = __builtin_amdgcn_exp2f(x * __builtin_fmaf(x * x, c1, c0));
    return x * __builtin_amdgcn_rcpf(1.0f + e);
}
#define MFMA(A_, B_, C_) C_ = __builtin_amdgcn_mfma_f32_16x16x32_bf16(A_, B_, C_, 0, 0, 0)
#define SMF(A_, B_, C_) C_ = __builtin_amdgcn_smfmac_f32_16x16x64_bf16(A_, B_, C_, sidx, 0, 0)
__device__ __forceinline__ float ln_den(float var) { return rbf(__builtin_amdgcn_sqrtf(rbf(rbf(var) + 1e-5f))); }
__device__ __forceinline__ float rcp(float x) { return __builtin_amdgcn_rcpf(x); }

struct Params {
    char* ws;
    const bf16_t* x;
    const bf16_t* mem0;
    const bf16_t* w[8];
    bf16_t* out;
};

#define RLX_AGENT __ATOMIC_RELAXED, __HIP_MEMORY_SCOPE_AGENT
#define SENT 0xFFFFFFFFu
constexpr int SPIN_CAP = 400000;
__device__ __forceinline__ bool spin_fail(int spins, const Params& P) {
    if ((spins & 1023) == 1023) {
        const rsrc_t rt = mkrsrc(P.ws + FL0, FL_BYTES);
        unsigned o;
        asm volatile("v_mov_b32 %0, 0x100" : "=v"(o));
        if (__builtin_amdgcn_readfirstlane(__builtin_amdgcn_raw_buffer_load_b32(rt, o, 0, 16)) != 0u) return true;
        if (spins > SPIN_CAP) { __builtin_amdgcn_raw_buffer_store_b32(o, rt, o, 0, 16); return true; }
    }
    return false;
}
__device__ __forceinline__ bool ok4(u32x4 v) { return v[0] != SENT && v[1] != SENT && v[2] != SENT && v[3] != SENT; }
__device__ __forceinline__ unsigned nosent(unsigned d) { return d < 0xFFFFFFFEu ? d : 0xFFFFFFFEu; }
__device__ __forceinline__ u32x4 nosent4(u32x4 v) { return (u32x4){nosent(v[0]), nosent(v[1]), nosent(v[2]), nosent(v[3])}; }
__device__ __forceinline__ unsigned umax3(unsigned a, unsigned b, unsigned c) { const unsigned m = a > b ? a : b; return m > c ? m : c; }
__device__ __forceinline__ unsigned umax4(unsigned m, u32x4 v) { return umax3(umax3(m, v[0], v[1]), v[2], v[3]); }
__device__ __forceinline__ u32x4 sent4() { unsigned sv; asm volatile("v_mov_b32 %0, -1" : "=v"(sv)); return (u32x4){sv, sv, sv, sv}; }
template <int N>
__device__ __forceinline__ void poll(rsrc_t rs, unsigned base, const unsigned (&off)[N], u32x4 (&v)[N], const Params& P) {
    for (int spins = 0;; ++spins) {
        unsigned m = 0;
#pragma unroll
        for (int k = 0; k < N; ++k) v[k] = LD128(rs, off[k], base);
#pragma unroll
        for (int k = 0; k < N; ++k) m = umax4(m, v[k]);
        if (__all(m != SENT)) return;
        asm volatile("" ::: "memory");
        if (spin_fail(spins, P)) return;
    }
}
__device__ __forceinline__ bf16x16 cat16(bf16x8 a, bf16x8 b) {
    return __builtin_shufflevector(a, b, 0, 1, 2, 3, 4, 5, 6, 7, 8, 9, 10, 11, 12, 13, 14, 15);
}
__device__ __forceinline__ bf16x8 half8(bf16x16 v, int h) {
    return h ? __builtin_shufflevector(v, v, 8, 9, 10, 11, 12, 13, 14, 15) : __builtin_shufflevector(v, v, 0, 1, 2, 3, 4, 5, 6, 7);
}
__device__ __forceinline__ bf16x16 lds_frag(unsigned a, unsigned step) { return cat16(lds_ld<bf16x8>(a), lds_ld<bf16x8>(a + step)); }
template <int I>
__device__ __forceinline__ void dmaq(const char* g, unsigned l16, unsigned dst) {
    asm volatile("s_mov_b32 m0, %2\n\ts_nop 0\n\tglobal_load_lds_dwordx4 %0, %1 offset:%3 sc1" :: "v"(l16), "s"(g), "s"(dst), "i"(I * 1024) : "memory");
}
__device__ __forceinline__ void dma4(const char* g, unsigned l16, unsigned dst) {
    asm volatile("s_mov_b32 m0, %2\n\ts_nop 0\n\tglobal_load_lds_dwordx4 %0, %1 sc1\n\tglobal_load_lds_dwordx4 %0, %1 offset:1024 sc1\n\t"
                 "global_load_lds_dwordx4 %0, %1 offset:2048 sc1\n\tglobal_load_lds_dwordx4 %0, %1 offset:3072 sc1" :: "v"(l16), "s"(g), "s"(dst) : "memory");
}
__device__ __forceinline__ unsigned toff(int row, int col, int par) {
    return pgb(row >> 5, par) + blin((unsigned)((row >> 4) & 1) * 20480u + (unsigned)(col >> 3) * 256u) + (unsigned)(row & 15) * 16u;
}

#define ASM_ST
#define ASM_ST_DECL
#define ASM_ST_OUT
#define ASM_ST_SAVE(k)
#define ASM_ST2
#define ASM_ST3
#define ASM_ST3_OUT
#define ASM_ST3_SAVE(k)
#define ASM_ST2_OUT
#define ASM_ST2_SAVE(k)
#define STDECL
#define ST(k)
#define STC(k)
#define STFLUSH(t)

#define MARK(c, k)
#define MARKX(c, k)
#define PMARK(c, st, seg)
#define LDECL
#define LA()
#define LB(t)
#define LF()
#define STROLE 3
constexpr unsigned L_MEM = 0;
constexpr unsigned L_Q = 10240;
constexpr unsigned L_KM = 13312;
constexpr unsigned L_QX = 16384;
constexpr unsigned L_KX = 28672;
constexpr unsigned L_XB = 40960;
constexpr unsigned L_XA = 49152;
constexpr unsigned L_W = 81920;
constexpr int MEGA_LDS = 163840;
constexpr int wi(int i, int j) { return (i >> 1) * 5 + ((i & 1) ? 2 + j : j - 1); }

#define RPUB2
#define WNT ""
#define PRIUP "s_setprio 2\n\t"
#define PRISM PRIUP
#define STR2(x) #x
#define STRX(x) STR2(x)
#define W4SLEEP "s_sleep " STRX(W4SL) "\n\t"
#define PLAY 0
#define V1A 0
#define ASM_WLOAD3 \
    "s_xor_b32 %[t], %[wx], 0\n\t" \
    "s_mul_i32 %[t], %[t], 0xc0000\n\t" \
    "buffer_load_dwordx4 v[200:203], %[o0], %[rs], %[t] offen" WNT "\n\t" \
    "buffer_load_dwordx4 v[0:3], %[o1], %[rs], %[t] offen" WNT "\n\t" \
    "buffer_load_dwordx4 v[8:11], %[o2], %[rs], %[t] offen" WNT "\n\t" \
    "s_add_u32 %[t], %[t], 0x18000\n\t" \
    "buffer_load_dwordx4 v[16:19], %[o0], %[rs], %[t] offen" WNT "\n\t" \
    "buffer_load_dwordx4 v[24:27], %[o1], %[rs], %[t] offen" WNT "\n\t" \
    "buffer_load_dwordx4 v[32:35], %[o2], %[rs], %[t] offen" WNT "\n\t" \
    "s_add_u32 %[t], %[t], 0x18000\n\t" \
    "buffer_load_dwordx4 v[208:211], %[o0], %[rs], %[t] offen" WNT "\n\t" \
    "buffer_load_dwordx4 v[40:43], %[o1], %[rs], %[t] offen" WNT "\n\t" \
    "buffer_load_dwordx4 v[48:51], %[o2], %[rs], %[t] offen" WNT "\n\t" \
    "s_add_u32 %[t], %[t], 0x18000\n\t" \
    "buffer_load_dwordx4 v[56:59], %[o0], %[rs], %[t] offen" WNT "\n\t" \
    "buffer_load_dwordx4 v[64:67], %[o1], %[rs], %[t] offen" WNT "\n\t" \
    "buffer_load_dwordx4 v[72:75], %[o2], %[rs], %[t] offen" WNT "\n\t" \
    "s_add_u32 %[t], %[t], 0x18000\n\t" \
    "buffer_load_dwordx4 v[204:207], %[o0], %[rs], %[t] offen" WNT "\n\t" \
    "buffer_load_dwordx4 v[4:7], %[o1], %[rs], %[t] offen" WNT "\n\t" \
    "buffer_load_dwordx4 v[12:15], %[o2], %[rs], %[t] offen" WNT "\n\t" \
    "s_add_u32 %[t], %[t], 0x18000\n\t" \
    "buffer_load_dwordx4 v[20:23], %[o0], %[rs], %[t] offen" WNT "\n\t" \
    "buffer_load_dwordx4 v[28:31], %[o1], %[rs], %[t] offen" WNT "\n\t" \
    "buffer_load_dwordx4 v[36:39], %[o2], %[rs], %[t] offen" WNT "\n\t" \
    "s_add_u32 %[t], %[t], 0x18000\n\t" \
    "buffer_load_dwordx4 v[212:215], %[o0], %[rs], %[t] offen" WNT "\n\t" \
    "buffer_load_dwordx4 v[44:47], %[o1], %[rs], %[t] offen" WNT "\n\t" \
    "buffer_load_dwordx4 v[52:55], %[o2], %[rs], %[t] offen" WNT "\n\t" \
    "s_add_u32 %[t], %[t], 0x18000\n\t" \
    "buffer_load_dwordx4 v[60:63], %[o0], %[rs], %[t] offen" WNT "\n\t" \
    "buffer_load_dwordx4 v[68:71], %[o1], %[rs], %[t] offen" WNT "\n\t" \
    "buffer_load_dwordx4 v[76:79], %[o2], %[rs], %[t] offen" WNT "\n\t" \
    "s_add_u32 %[t], %[t], 0x18000\n\t" \
    "s_waitcnt vmcnt(21)\n\t" \
    "ds_write_b128 %[s0], v[200:203] offset:0\n\t" \
    "ds_write_b128 %[s1], v[0:3] offset:0\n\t" \
    "ds_write_b128 %[s2], v[8:11] offset:0\n\t" \
    "s_waitcnt vmcnt(18)\n\t" \
    "ds_write_b128 %[s0], v[16:19] offset:3456\n\t" \
    "ds_write_b128 %[s1], v[24:27] offset:3456\n\t" \
    "ds_write_b128 %[s2], v[32:35] offset:3456\n\t" \
    "s_waitcnt vmcnt(15)\n\t" \
    "ds_write_b128 %[s0], v[208:211] offset:6912\n\t" \
    "ds_write_b128 %[s1], v[40:43] offset:6912\n\t" \
    "ds_write_b128 %[s2], v[48:51] offset:6912\n\t" \
    "s_waitcnt vmcnt(12)\n\t" \
    "ds_write_b128 %[s0], v[56:59] offset:10368\n\t" \
    "ds_write_b128 %[s1], v[64:67] offset:10368\n\t" \
    "ds_write_b128 %[s2], v[72:75] offset:10368\n\t" \
    "s_waitcnt lgkmcnt(0)\n\t" \
    "s_barrier \n\t" \
    "ds_read_b64_tr_b16 v[200:201], %[t0] offset:0\n\t" \
    "ds_read_b64_tr_b16 v[202:203], %[t0] offset:1664\n\t" \
    "ds_read_b64_tr_b16 v[0:1], %[t0] offset:128\n\t" \
    "ds_read_b64_tr_b16 v[2:3], %[t0] offset:1792\n\t" \
    "ds_read_b64_tr_b16 v[8:9], %[t0] offset:256\n\t" \
    "ds_read_b64_tr_b16 v[10:11], %[t0] offset:1920\n\t" \
    "ds_read_b64_tr_b16 v[16:17], %[t1] offset:0\n\t" \
    "ds_read_b64_tr_b16 v[18:19], %[t1] offset:1664\n\t" \
    "ds_read_b64_tr_b16 v[24:25], %[t1] offset:128\n\t" \
    "ds_read_b64_tr_b16 v[26:27], %[t1] offset:1792\n\t" \
    "ds_read_b64_tr_b16 v[32:33], %[t1] offset:256\n\t" \
    "ds_read_b64_tr_b16 v[34:35], %[t1] offset:1920\n\t" \
    "ds_read_b64_tr_b16 v[208:209], %[t2] offset:0\n\t" \
    "ds_read_b64_tr_b16 v[210:211], %[t2] offset:1664\n\t" \
    "ds_read_b64_tr_b16 v[40:41], %[t2] offset:128\n\t" \
    "ds_read_b64_tr_b16 v[42:43], %[t2] offset:1792\n\t" \
    "ds_read_b64_tr_b16 v[48:49], %[t2] offset:256\n\t" \
    "ds_read_b64_tr_b16 v[50:51], %[t2] offset:1920\n\t" \
    "ds_read_b64_tr_b16 v[56:57], %[t3] offset:0\n\t" \
    "ds_read_b64_tr_b16 v[58:59], %[t3] offset:1664\n\t" \
    "ds_read_b64_tr_b16 v[64:65], %[t3] offset:128\n\t" \
    "ds_read_b64_tr_b16 v[66:67], %[t3] offset:1792\n\t" \
    "ds_read_b64_tr_b16 v[72:73], %[t3] offset:256\n\t" \
    "ds_read_b64_tr_b16 v[74:75], %[t3] offset:1920\n\t" \
    "s_xor_b32 %[t], %[wx], 4\n\t" \
    "s_mul_i32 %[t], %[t], 0xc0000\n\t" \
    "buffer_load_dwordx4 v[216:219], %[o0], %[rs], %[t] offen" WNT "\n\t" \
    "buffer_load_dwordx4 v[80:83], %[o1], %[rs], %[t] offen" WNT "\n\t" \
    "buffer_load_dwordx4 v[88:91], %[o2], %[rs], %[t] offen" WNT "\n\t" \
    "s_add_u32 %[t], %[t], 0x18000\n\t" \
    "buffer_load_dwordx4 v[96:99], %[o0], %[rs], %[t] offen" WNT "\n\t" \
    "buffer_load_dwordx4 v[104:107], %[o1], %[rs], %[t] offen" WNT "\n\t" \
    "buffer_load_dwordx4 v[112:115], %[o2], %[rs], %[t] offen" WNT "\n\t" \
    "s_add_u32 %[t], %[t], 0x18000\n\t" \
    "buffer_load_dwordx4 v[224:227], %[o0], %[rs], %[t] offen" WNT "\n\t" \
    "buffer_load_dwordx4 v[120:123], %[o1], %[rs], %[t] offen" WNT "\n\t" \
    "buffer_load_dwordx4 v[128:131], %[o2], %[rs], %[t] offen" WNT "\n\t" \
    "s_add_u32 %[t], %[t], 0x18000\n\t" \
    "buffer_load_dwordx4 v[136:139], %[o0], %[rs], %[t] offen" WNT "\n\t" \
    "buffer_load_dwordx4 v[144:147], %[o1], %[rs], %[t] offen" WNT "\n\t" \
    "buffer_load_dwordx4 v[152:155], %[o2], %[rs], %[t] offen" WNT "\n\t" \
    "s_add_u32 %[t], %[t], 0x18000\n\t" \
    "s_waitcnt lgkmcnt(0)\n\t" \
    "s_barrier \n\t" \
    "s_waitcnt vmcnt(21)\n\t" \
    "ds_write_b128 %[s0], v[204:207] offset:0\n\t" \
    "ds_write_b128 %[s1], v[4:7] offset:0\n\t" \
    "ds_write_b128 %[s2], v[12:15] offset:0\n\t" \
    "s_waitcnt vmcnt(18)\n\t" \
    "ds_write_b128 %[s0], v[20:23] offset:3456\n\t" \
    "ds_write_b128 %[s1], v[28:31] offset:3456\n\t" \
    "ds_write_b128 %[s2], v[36:39] offset:3456\n\t" \
    "s_waitcnt vmcnt(15)\n\t" \
    "ds_write_b128 %[s0], v[212:215] offset:6912\n\t" \
    "ds_write_b128 %[s1], v[44:47] offset:6912\n\t" \
    "ds_write_b128 %[s2], v[52:55] offset:6912\n\t" \
    "s_waitcnt vmcnt(12)\n\t" \
    "ds_write_b128 %[s0], v[60:63] offset:10368\n\t" \
    "ds_write_b128 %[s1], v[68:71] offset:10368\n\t" \
    "ds_write_b128 %[s2], v[76:79] offset:10368\n\t" \
    "s_waitcnt lgkmcnt(0)\n\t" \
    "s_barrier \n\t" \
    "ds_read_b64_tr_b16 v[204:205], %[t0] offset:0\n\t" \
    "ds_read_b64_tr_b16 v[206:207], %[t0] offset:1664\n\t" \
    "ds_read_b64_tr_b16 v[4:5], %[t0] offset:128\n\t" \
    "ds_read_b64_tr_b16 v[6:7], %[t0] offset:1792\n\t" \
    "ds_read_b64_tr_b16 v[12:13], %[t0] offset:256\n\t" \
    "ds_read_b64_tr_b16 v[14:15], %[t0] offset:1920\n\t" \
    "ds_read_b64_tr_b16 v[20:21], %[t1] offset:0\n\t" \
    "ds_read_b64_tr_b16 v[22:23], %[t1] offset:1664\n\t" \
    "ds_read_b64_tr_b16 v[28:29], %[t1] offset:128\n\t" \
    "ds_read_b64_tr_b16 v[30:31], %[t1] offset:1792\n\t" \
    "ds_read_b64_tr_b16 v[36:37], %[t1] offset:256\n\t" \
    "ds_read_b64_tr_b16 v[38:39], %[t1] offset:1920\n\t" \
    "ds_read_b64_tr_b16 v[212:213], %[t2] offset:0\n\t" \
    "ds_read_b64_tr_b16 v[214:215], %[t2] offset:1664\n\t" \
    "ds_read_b64_tr_b16 v[44:45], %[t2] offset:128\n\t" \
    "ds_read_b64_tr_b16 v[46:47], %[t2] offset:1792\n\t" \
    "ds_read_b64_tr_b16 v[52:53], %[t2] offset:256\n\t" \
    "ds_read_b64_tr_b16 v[54:55], %[t2] offset:1920\n\t" \
    "ds_read_b64_tr_b16 v[60:61], %[t3] offset:0\n\t" \
    "ds_read_b64_tr_b16 v[62:63], %[t3] offset:1664\n\t" \
    "ds_read_b64_tr_b16 v[68:69], %[t3] offset:128\n\t" \
    "ds_read_b64_tr_b16 v[70:71], %[t3] offset:1792\n\t" \
    "ds_read_b64_tr_b16 v[76:77], %[t3] offset:256\n\t" \
    "ds_read_b64_tr_b16 v[78:79], %[t3] offset:1920\n\t" \
    "buffer_load_dwordx4 v[220:223], %[o0], %[rs], %[t] offen" WNT "\n\t" \
    "buffer_load_dwordx4 v[84:87], %[o1], %[rs], %[t] offen" WNT "\n\t" \
    "buffer_load_dwordx4 v[92:95], %[o2], %[rs], %[t] offen" WNT "\n\t" \
    "s_add_u32 %[t], %[t], 0x18000\n\t" \
    "buffer_load_dwordx4 v[100:103], %[o0], %[rs], %[t] offen" WNT "\n\t" \
    "buffer_load_dwordx4 v[108:111], %[o1], %[rs], %[t] offen" WNT "\n\t" \
    "buffer_load_dwordx4 v[116:119], %[o2], %[rs], %[t] offen" WNT "\n\t" \
    "s_add_u32 %[t], %[t], 0x18000\n\t" \
    "buffer_load_dwordx4 v[228:231], %[o0], %[rs], %[t] offen" WNT "\n\t" \
    "buffer_load_dwordx4 v[124:127], %[o1], %[rs], %[t] offen" WNT "\n\t" \
    "buffer_load_dwordx4 v[132:135], %[o2], %[rs], %[t] offen" WNT "\n\t" \
    "s_add_u32 %[t], %[t], 0x18000\n\t" \
    "buffer_load_dwordx4 v[140:143], %[o0], %[rs], %[t] offen" WNT "\n\t" \
    "buffer_load_dwordx4 v[148:151], %[o1], %[rs], %[t] offen" WNT "\n\t" \
    "buffer_load_dwordx4 v[156:159], %[o2], %[rs], %[t] offen" WNT "\n\t" \
    "s_add_u32 %[t], %[t], 0x18000\n\t" \
    "s_waitcnt lgkmcnt(0)\n\t" \
    "s_barrier \n\t" \
    "s_waitcnt vmcnt(21)\n\t" \
    "ds_write_b128 %[s0], v[216:219] offset:0\n\t" \
    "ds_write_b128 %[s1], v[80:83] offset:0\n\t" \
    "ds_write_b128 %[s2], v[88:91] offset:0\n\t" \
    "s_waitcnt vmcnt(18)\n\t" \
    "ds_write_b128 %[s0], v[96:99] offset:3456\n\t" \
    "ds_write_b128 %[s1], v[104:107] offset:3456\n\t" \
    "ds_write_b128 %[s2], v[112:115] offset:3456\n\t" \
    "s_waitcnt vmcnt(15)\n\t" \
    "ds_write_b128 %[s0], v[224:227] offset:6912\n\t" \
    "ds_write_b128 %[s1], v[120:123] offset:6912\n\t" \
    "ds_write_b128 %[s2], v[128:131] offset:6912\n\t" \
    "s_waitcnt vmcnt(12)\n\t" \
    "ds_write_b128 %[s0], v[136:139] offset:10368\n\t" \
    "ds_write_b128 %[s1], v[144:147] offset:10368\n\t" \
    "ds_write_b128 %[s2], v[152:155] offset:10368\n\t" \
    "s_waitcnt lgkmcnt(0)\n\t" \
    "s_barrier \n\t" \
    "ds_read_b64_tr_b16 v[216:217], %[t0] offset:0\n\t" \
    "ds_read_b64_tr_b16 v[218:219], %[t0] offset:1664\n\t" \
    "ds_read_b64_tr_b16 v[80:81], %[t0] offset:128\n\t" \
    "ds_read_b64_tr_b16 v[82:83], %[t0] offset:1792\n\t" \
    "ds_read_b64_tr_b16 v[88:89], %[t0] offset:256\n\t" \
    "ds_read_b64_tr_b16 v[90:91], %[t0] offset:1920\n\t" \
    "ds_read_b64_tr_b16 v[96:97], %[t1] offset:0\n\t" \
    "ds_read_b64_tr_b16 v[98:99], %[t1] offset:1664\n\t" \
    "ds_read_b64_tr_b16 v[104:105], %[t1] offset:128\n\t" \
    "ds_read_b64_tr_b16 v[106:107], %[t1] offset:1792\n\t" \
    "ds_read_b64_tr_b16 v[112:113], %[t1] offset:256\n\t" \
    "ds_read_b64_tr_b16 v[114:115], %[t1] offset:1920\n\t" \
    "ds_read_b64_tr_b16 v[224:225], %[t2] offset:0\n\t" \
    "ds_read_b64_tr_b16 v[226:227], %[t2] offset:1664\n\t" \
    "ds_read_b64_tr_b16 v[120:121], %[t2] offset:128\n\t" \
    "ds_read_b64_tr_b16 v[122:123], %[t2] offset:1792\n\t" \
    "ds_read_b64_tr_b16 v[128:129], %[t2] offset:256\n\t" \
    "ds_read_b64_tr_b16 v[130:131], %[t2] offset:1920\n\t" \
    "ds_read_b64_tr_b16 v[136:137], %[t3] offset:0\n\t" \
    "ds_read_b64_tr_b16 v[138:139], %[t3] offset:1664\n\t" \
    "ds_read_b64_tr_b16 v[144:145], %[t3] offset:128\n\t" \
    "ds_read_b64_tr_b16 v[146:147], %[t3] offset:1792\n\t" \
    "ds_read_b64_tr_b16 v[152:153], %[t3] offset:256\n\t" \
    "ds_read_b64_tr_b16 v[154:155], %[t3] offset:1920\n\t" \
    "s_and_b32 %[t], %[wx], 3\n\t" \
    "s_mul_i32 %[t], %[t], 0x60000\n\t" \
    "s_add_u32 %[t], %[t], 0x600000\n\t" \
    "buffer_load_dwordx4 v[232:235], %[o0], %[rs], %[t] offen" WNT "\n\t" \
    "buffer_load_dwordx4 v[160:163], %[o1], %[rs], %[t] offen" WNT "\n\t" \
    "buffer_load_dwordx4 v[168:171], %[o2], %[rs], %[t] offen" WNT "\n\t" \
    "s_add_u32 %[t], %[t], 0x18000\n\t" \
    "buffer_load_dwordx4 v[236:239], %[o0], %[rs], %[t] offen" WNT "\n\t" \
    "buffer_load_dwordx4 v[164:167], %[o1], %[rs], %[t] offen" WNT "\n\t" \
    "buffer_load_dwordx4 v[172:175], %[o2], %[rs], %[t] offen" WNT "\n\t" \
    "s_add_u32 %[t], %[t], 0x18000\n\t" \
    "buffer_load_dwordx4 v[176:179], %[o0], %[rs], %[t] offen" WNT "\n\t" \
    "buffer_load_dwordx4 v[184:187], %[o1], %[rs], %[t] offen" WNT "\n\t" \
    "buffer_load_dwordx4 v[192:195], %[o2], %[rs], %[t] offen" WNT "\n\t" \
    "s_add_u32 %[t], %[t], 0x18000\n\t" \
    "buffer_load_dwordx4 v[180:183], %[o0], %[rs], %[t] offen" WNT "\n\t" \
    "buffer_load_dwordx4 v[188:191], %[o1], %[rs], %[t] offen" WNT "\n\t" \
    "buffer_load_dwordx4 v[196:199], %[o2], %[rs], %[t] offen" WNT "\n\t" \
    "s_add_u32 %[t], %[t], 0x18000\n\t" \
    "s_waitcnt lgkmcnt(0)\n\t" \
    "s_barrier \n\t" \
    "s_waitcnt vmcnt(21)\n\t" \
    "ds_write_b128 %[s0], v[220:223] offset:0\n\t" \
    "ds_write_b128 %[s1], v[84:87] offset:0\n\t" \
    "ds_write_b128 %[s2], v[92:95] offset:0\n\t" \
    "s_waitcnt vmcnt(18)\n\t" \
    "ds_write_b128 %[s0], v[100:103] offset:3456\n\t" \
    "ds_write_b128 %[s1], v[108:111] offset:3456\n\t" \
    "ds_write_b128 %[s2], v[116:119] offset:3456\n\t" \
    "s_waitcnt vmcnt(15)\n\t" \
    "ds_write_b128 %[s0], v[228:231] offset:6912\n\t" \
    "ds_write_b128 %[s1], v[124:127] offset:6912\n\t" \
    "ds_write_b128 %[s2], v[132:135] offset:6912\n\t" \
    "s_waitcnt vmcnt(12)\n\t" \
    "ds_write_b128 %[s0], v[140:143] offset:10368\n\t" \
    "ds_write_b128 %[s1], v[148:151] offset:10368\n\t" \
    "ds_write_b128 %[s2], v[156:159] offset:10368\n\t" \
    "s_waitcnt lgkmcnt(0)\n\t" \
    "s_barrier \n\t" \
    "ds_read_b64_tr_b16 v[220:221], %[t0] offset:0\n\t" \
    "ds_read_b64_tr_b16 v[222:223], %[t0] offset:1664\n\t" \
    "ds_read_b64_tr_b16 v[84:85], %[t0] offset:128\n\t" \
    "ds_read_b64_tr_b16 v[86:87], %[t0] offset:1792\n\t" \
    "ds_read_b64_tr_b16 v[92:93], %[t0] offset:256\n\t" \
    "ds_read_b64_tr_b16 v[94:95], %[t0] offset:1920\n\t" \
    "ds_read_b64_tr_b16 v[100:101], %[t1] offset:0\n\t" \
    "ds_read_b64_tr_b16 v[102:103], %[t1] offset:1664\n\t" \
    "ds_read_b64_tr_b16 v[108:109], %[t1] offset:128\n\t" \
    "ds_read_b64_tr_b16 v[110:111], %[t1] offset:1792\n\t" \
    "ds_read_b64_tr_b16 v[116:117], %[t1] offset:256\n\t" \
    "ds_read_b64_tr_b16 v[118:119], %[t1] offset:1920\n\t" \
    "ds_read_b64_tr_b16 v[228:229], %[t2] offset:0\n\t" \
    "ds_read_b64_tr_b16 v[230:231], %[t2] offset:1664\n\t" \
    "ds_read_b64_tr_b16 v[124:125], %[t2] offset:128\n\t" \
    "ds_read_b64_tr_b16 v[126:127], %[t2] offset:1792\n\t" \
    "ds_read_b64_tr_b16 v[132:133], %[t2] offset:256\n\t" \
    "ds_read_b64_tr_b16 v[134:135], %[t2] offset:1920\n\t" \
    "ds_read_b64_tr_b16 v[140:141], %[t3] offset:0\n\t" \
    "ds_read_b64_tr_b16 v[142:143], %[t3] offset:1664\n\t" \
    "ds_read_b64_tr_b16 v[148:149], %[t3] offset:128\n\t" \
    "ds_read_b64_tr_b16 v[150:151], %[t3] offset:1792\n\t" \
    "ds_read_b64_tr_b16 v[156:157], %[t3] offset:256\n\t" \
    "ds_read_b64_tr_b16 v[158:159], %[t3] offset:1920\n\t" \
    "s_waitcnt lgkmcnt(0)\n\t" \
    "s_barrier \n\t" \
    "s_waitcnt vmcnt(9)\n\t" \
    "ds_write_b128 %[s0], v[232:235] offset:0\n\t" \
    "ds_write_b128 %[s1], v[160:163] offset:0\n\t" \
    "ds_write_b128 %[s2], v[168:171] offset:0\n\t" \
    "s_waitcnt vmcnt(6)\n\t" \
    "ds_write_b128 %[s0], v[236:239] offset:3456\n\t" \
    "ds_write_b128 %[s1], v[164:167] offset:3456\n\t" \
    "ds_write_b128 %[s2], v[172:175] offset:3456\n\t" \
    "s_waitcnt vmcnt(3)\n\t" \
    "ds_write_b128 %[s0], v[176:179] offset:6912\n\t" \
    "ds_write_b128 %[s1], v[184:187] offset:6912\n\t" \
    "ds_write_b128 %[s2], v[192:195] offset:6912\n\t" \
    "s_waitcnt vmcnt(0)\n\t" \
    "ds_write_b128 %[s0], v[180:183] offset:10368\n\t" \
    "ds_write_b128 %[s1], v[188:191] offset:10368\n\t" \
    "ds_write_b128 %[s2], v[196:199] offset:10368\n\t" \
    "s_waitcnt lgkmcnt(0)\n\t" \
    "s_barrier \n\t" \
    "ds_read_b64_tr_b16 v[232:233], %[t4] offset:0\n\t" \
    "ds_read_b64_tr_b16 v[234:235], %[t4] offset:1664\n\t" \
    "ds_read_b64_tr_b16 v[160:161], %[t4] offset:128\n\t" \
    "ds_read_b64_tr_b16 v[162:163], %[t4] offset:1792\n\t" \
    "ds_read_b64_tr_b16 v[168:169], %[t4] offset:256\n\t" \
    "ds_read_b64_tr_b16 v[170:171], %[t4] offset:1920\n\t" \
    "ds_read_b64_tr_b16 v[236:237], %[t4] offset:13824\n\t" \
    "ds_read_b64_tr_b16 v[238:239], %[t4] offset:15488\n\t" \
    "ds_read_b64_tr_b16 v[164:165], %[t4] offset:13952\n\t" \
    "ds_read_b64_tr_b16 v[166:167], %[t4] offset:15616\n\t" \
    "ds_read_b64_tr_b16 v[172:173], %[t4] offset:14080\n\t" \
    "ds_read_b64_tr_b16 v[174:175], %[t4] offset:15744\n\t" \
    "ds_read_b64_tr_b16 v[176:177], %[t4] offset:27648\n\t" \
    "ds_read_b64_tr_b16 v[178:179], %[t4] offset:29312\n\t" \
    "ds_read_b64_tr_b16 v[184:185], %[t4] offset:27776\n\t" \
    "ds_read_b64_tr_b16 v[186:187], %[t4] offset:29440\n\t" \
    "ds_read_b64_tr_b16 v[192:193], %[t4] offset:27904\n\t" \
    "ds_read_b64_tr_b16 v[194:195], %[t4] offset:29568\n\t" \
    "ds_read_b64_tr_b16 v[180:181], %[t4] offset:41472\n\t" \
    "ds_read_b64_tr_b16 v[182:183], %[t4] offset:43136\n\t" \
    "ds_read_b64_tr_b16 v[188:189], %[t4] offset:41600\n\t" \
    "ds_read_b64_tr_b16 v[190:191], %[t4] offset:43264\n\t" \
    "ds_read_b64_tr_b16 v[196:197], %[t4] offset:41728\n\t" \
    "ds_read_b64_tr_b16 v[198:199], %[t4] offset:43392\n\t" \
    "s_waitcnt lgkmcnt(0)\n\t" \
    "s_barrier \n\t" \
    "ds_write_b128 %[wl], v[200:203] offset:0\n\t" \
    "ds_write_b128 %[wl], v[204:207] offset:1024\n\t" \
    "ds_write_b128 %[wl], v[208:211] offset:2048\n\t" \
    "ds_write_b128 %[wl], v[212:215] offset:3072\n\t" \
    "ds_write_b128 %[wl], v[216:219] offset:4096\n\t" \
    "ds_write_b128 %[wl], v[220:223] offset:5120\n\t" \
    "ds_write_b128 %[wl], v[224:227] offset:6144\n\t" \
    "ds_write_b128 %[wl], v[228:231] offset:7168\n\t" \
    "ds_write_b128 %[wl], v[232:235] offset:8192\n\t" \
    "ds_write_b128 %[wl], v[236:239] offset:9216\n\t" \
    "s_waitcnt lgkmcnt(0)\n\t"
#define ASM_WLOADO \
    "s_mov_b32 %[t], 0x0\n\t" \
    "buffer_load_dwordx4 v[200:203], %[o0], %[rs], %[t] offen" WNT "\n\t" \
    "buffer_load_dwordx4 v[16:19], %[o1], %[rs], %[t] offen" WNT "\n\t" \
    "buffer_load_dwordx4 v[208:211], %[o2], %[rs], %[t] offen" WNT "\n\t" \
    "buffer_load_dwordx4 v[56:59], %[o3], %[rs], %[t] offen" WNT "\n\t" \
    "buffer_load_dwordx4 v[216:219], %[o4], %[rs], %[t] offen" WNT "\n\t" \
    "s_mov_b32 %[t], 0xa000\n\t" \
    "buffer_load_dwordx4 v[96:99], %[o0], %[rs], %[t] offen" WNT "\n\t" \
    "buffer_load_dwordx4 v[224:227], %[o1], %[rs], %[t] offen" WNT "\n\t" \
    "buffer_load_dwordx4 v[136:139], %[o2], %[rs], %[t] offen" WNT "\n\t" \
    "buffer_load_dwordx4 v[232:235], %[o3], %[rs], %[t] offen" WNT "\n\t" \
    "buffer_load_dwordx4 v[176:179], %[o4], %[rs], %[t] offen" WNT "\n\t" \
    "s_mov_b32 %[t], 0x14000\n\t" \
    "buffer_load_dwordx4 v[204:207], %[o0], %[rs], %[t] offen" WNT "\n\t" \
    "buffer_load_dwordx4 v[20:23], %[o1], %[rs], %[t] offen" WNT "\n\t" \
    "buffer_load_dwordx4 v[212:215], %[o2], %[rs], %[t] offen" WNT "\n\t" \
    "buffer_load_dwordx4 v[60:63], %[o3], %[rs], %[t] offen" WNT "\n\t" \
    "buffer_load_dwordx4 v[220:223], %[o4], %[rs], %[t] offen" WNT "\n\t" \
    "s_mov_b32 %[t], 0x1e000\n\t" \
    "buffer_load_dwordx4 v[100:103], %[o0], %[rs], %[t] offen" WNT "\n\t" \
    "buffer_load_dwordx4 v[228:231], %[o1], %[rs], %[t] offen" WNT "\n\t" \
    "buffer_load_dwordx4 v[140:143], %[o2], %[rs], %[t] offen" WNT "\n\t" \
    "buffer_load_dwordx4 v[236:239], %[o3], %[rs], %[t] offen" WNT "\n\t" \
    "buffer_load_dwordx4 v[180:183], %[o4], %[rs], %[t] offen" WNT "\n\t" \
    "s_waitcnt vmcnt(15)\n\t" \
    "ds_write_b128 %[s0], v[200:203] offset:0\n\t" \
    "ds_write_b128 %[s1], v[16:19] offset:0\n\t" \
    "ds_write_b128 %[s2], v[208:211] offset:0\n\t" \
    "ds_write_b128 %[s3], v[56:59] offset:0\n\t" \
    "ds_write_b128 %[s4], v[216:219] offset:0\n\t" \
    "s_waitcnt vmcnt(10)\n\t" \
    "ds_write_b128 %[s0], v[96:99] offset:5632\n\t" \
    "ds_write_b128 %[s1], v[224:227] offset:5632\n\t" \
    "ds_write_b128 %[s2], v[136:139] offset:5632\n\t" \
    "ds_write_b128 %[s3], v[232:235] offset:5632\n\t" \
    "ds_write_b128 %[s4], v[176:179] offset:5632\n\t" \
    "ds_read_b64_tr_b16 v[200:201], %[tr] offset:0\n\t" \
    "ds_read_b64_tr_b16 v[202:203], %[tr] offset:5632\n\t" \
    "ds_read_b64_tr_b16 v[16:17], %[tr] offset:32\n\t" \
    "ds_read_b64_tr_b16 v[18:19], %[tr] offset:5664\n\t" \
    "ds_read_b64_tr_b16 v[208:209], %[tr] offset:64\n\t" \
    "ds_read_b64_tr_b16 v[210:211], %[tr] offset:5696\n\t" \
    "ds_read_b64_tr_b16 v[56:57], %[tr] offset:96\n\t" \
    "ds_read_b64_tr_b16 v[58:59], %[tr] offset:5728\n\t" \
    "ds_read_b64_tr_b16 v[216:217], %[tr] offset:128\n\t" \
    "ds_read_b64_tr_b16 v[218:219], %[tr] offset:5760\n\t" \
    "ds_read_b64_tr_b16 v[96:97], %[tr] offset:160\n\t" \
    "ds_read_b64_tr_b16 v[98:99], %[tr] offset:5792\n\t" \
    "ds_read_b64_tr_b16 v[224:225], %[tr] offset:192\n\t" \
    "ds_read_b64_tr_b16 v[226:227], %[tr] offset:5824\n\t" \
    "ds_read_b64_tr_b16 v[136:137], %[tr] offset:224\n\t" \
    "ds_read_b64_tr_b16 v[138:139], %[tr] offset:5856\n\t" \
    "ds_read_b64_tr_b16 v[232:233], %[tr] offset:256\n\t" \
    "ds_read_b64_tr_b16 v[234:235], %[tr] offset:5888\n\t" \
    "ds_read_b64_tr_b16 v[176:177], %[tr] offset:288\n\t" \
    "ds_read_b64_tr_b16 v[178:179], %[tr] offset:5920\n\t" \
    "s_barrier \n\t" \
    "s_mov_b32 %[t], 0x28000\n\t" \
    "buffer_load_dwordx4 v[0:3], %[o0], %[rs], %[t] offen" WNT "\n\t" \
    "buffer_load_dwordx4 v[24:27], %[o1], %[rs], %[t] offen" WNT "\n\t" \
    "buffer_load_dwordx4 v[40:43], %[o2], %[rs], %[t] offen" WNT "\n\t" \
    "buffer_load_dwordx4 v[64:67], %[o3], %[rs], %[t] offen" WNT "\n\t" \
    "buffer_load_dwordx4 v[80:83], %[o4], %[rs], %[t] offen" WNT "\n\t" \
    "s_mov_b32 %[t], 0x32000\n\t" \
    "buffer_load_dwordx4 v[104:107], %[o0], %[rs], %[t] offen" WNT "\n\t" \
    "buffer_load_dwordx4 v[120:123], %[o1], %[rs], %[t] offen" WNT "\n\t" \
    "buffer_load_dwordx4 v[144:147], %[o2], %[rs], %[t] offen" WNT "\n\t" \
    "buffer_load_dwordx4 v[160:163], %[o3], %[rs], %[t] offen" WNT "\n\t" \
    "buffer_load_dwordx4 v[184:187], %[o4], %[rs], %[t] offen" WNT "\n\t" \
    "s_waitcnt lgkmcnt(0)\n\t" \
    "s_barrier \n\t" \
    "s_waitcnt vmcnt(15)\n\t" \
    "ds_write_b128 %[s0], v[204:207] offset:0\n\t" \
    "ds_write_b128 %[s1], v[20:23] offset:0\n\t" \
    "ds_write_b128 %[s2], v[212:215] offset:0\n\t" \
    "ds_write_b128 %[s3], v[60:63] offset:0\n\t" \
    "ds_write_b128 %[s4], v[220:223] offset:0\n\t" \
    "s_waitcnt vmcnt(10)\n\t" \
    "ds_write_b128 %[s0], v[100:103] offset:5632\n\t" \
    "ds_write_b128 %[s1], v[228:231] offset:5632\n\t" \
    "ds_write_b128 %[s2], v[140:143] offset:5632\n\t" \
    "ds_write_b128 %[s3], v[236:239] offset:5632\n\t" \
    "ds_write_b128 %[s4], v[180:183] offset:5632\n\t" \
    "ds_read_b64_tr_b16 v[204:205], %[tr] offset:0\n\t" \
    "ds_read_b64_tr_b16 v[206:207], %[tr] offset:5632\n\t" \
    "ds_read_b64_tr_b16 v[20:21], %[tr] offset:32\n\t" \
    "ds_read_b64_tr_b16 v[22:23], %[tr] offset:5664\n\t" \
    "ds_read_b64_tr_b16 v[212:213], %[tr] offset:64\n\t" \
    "ds_read_b64_tr_b16 v[214:215], %[tr] offset:5696\n\t" \
    "ds_read_b64_tr_b16 v[60:61], %[tr] offset:96\n\t" \
    "ds_read_b64_tr_b16 v[62:63], %[tr] offset:5728\n\t" \
    "ds_read_b64_tr_b16 v[220:221], %[tr] offset:128\n\t" \
    "ds_read_b64_tr_b16 v[222:223], %[tr] offset:5760\n\t" \
    "ds_read_b64_tr_b16 v[100:101], %[tr] offset:160\n\t" \
    "ds_read_b64_tr_b16 v[102:103], %[tr] offset:5792\n\t" \
    "ds_read_b64_tr_b16 v[228:229], %[tr] offset:192\n\t" \
    "ds_read_b64_tr_b16 v[230:231], %[tr] offset:5824\n\t" \
    "ds_read_b64_tr_b16 v[140:141], %[tr] offset:224\n\t" \
    "ds_read_b64_tr_b16 v[142:143], %[tr] offset:5856\n\t" \
    "ds_read_b64_tr_b16 v[236:237], %[tr] offset:256\n\t" \
    "ds_read_b64_tr_b16 v[238:239], %[tr] offset:5888\n\t" \
    "ds_read_b64_tr_b16 v[180:181], %[tr] offset:288\n\t" \
    "ds_read_b64_tr_b16 v[182:183], %[tr] offset:5920\n\t" \
    "s_barrier \n\t" \
    "s_mov_b32 %[t], 0x5000\n\t" \
    "buffer_load_dwordx4 v[4:7], %[o0], %[rs], %[t] offen" WNT "\n\t" \
    "buffer_load_dwordx4 v[28:31], %[o1], %[rs], %[t] offen" WNT "\n\t" \
    "buffer_load_dwordx4 v[44:47], %[o2], %[rs], %[t] offen" WNT "\n\t" \
    "buffer_load_dwordx4 v[68:71], %[o3], %[rs], %[t] offen" WNT "\n\t" \
    "buffer_load_dwordx4 v[84:87], %[o4], %[rs], %[t] offen" WNT "\n\t" \
    "s_mov_b32 %[t], 0xf000\n\t" \
    "buffer_load_dwordx4 v[108:111], %[o0], %[rs], %[t] offen" WNT "\n\t" \
    "buffer_load_dwordx4 v[124:127], %[o1], %[rs], %[t] offen" WNT "\n\t" \
    "buffer_load_dwordx4 v[148:151], %[o2], %[rs], %[t] offen" WNT "\n\t" \
    "buffer_load_dwordx4 v[164:167], %[o3], %[rs], %[t] offen" WNT "\n\t" \
    "buffer_load_dwordx4 v[188:191], %[o4], %[rs], %[t] offen" WNT "\n\t" \
    "s_waitcnt lgkmcnt(0)\n\t" \
    "s_barrier \n\t" \
    "s_waitcnt vmcnt(15)\n\t" \
    "ds_write_b128 %[s0], v[0:3] offset:0\n\t" \
    "ds_write_b128 %[s1], v[24:27] offset:0\n\t" \
    "ds_write_b128 %[s2], v[40:43] offset:0\n\t" \
    "ds_write_b128 %[s3], v[64:67] offset:0\n\t" \
    "ds_write_b128 %[s4], v[80:83] offset:0\n\t" \
    "s_waitcnt vmcnt(10)\n\t" \
    "ds_write_b128 %[s0], v[104:107] offset:5632\n\t" \
    "ds_write_b128 %[s1], v[120:123] offset:5632\n\t" \
    "ds_write_b128 %[s2], v[144:147] offset:5632\n\t" \
    "ds_write_b128 %[s3], v[160:163] offset:5632\n\t" \
    "ds_write_b128 %[s4], v[184:187] offset:5632\n\t" \
    "ds_read_b64_tr_b16 v[0:1], %[tr] offset:0\n\t" \
    "ds_read_b64_tr_b16 v[2:3], %[tr] offset:5632\n\t" \
    "ds_read_b64_tr_b16 v[24:25], %[tr] offset:32\n\t" \
    "ds_read_b64_tr_b16 v[26:27], %[tr] offset:5664\n\t" \
    "ds_read_b64_tr_b16 v[40:41], %[tr] offset:64\n\t" \
    "ds_read_b64_tr_b16 v[42:43], %[tr] offset:5696\n\t" \
    "ds_read_b64_tr_b16 v[64:65], %[tr] offset:96\n\t" \
    "ds_read_b64_tr_b16 v[66:67], %[tr] offset:5728\n\t" \
    "ds_read_b64_tr_b16 v[80:81], %[tr] offset:128\n\t" \
    "ds_read_b64_tr_b16 v[82:83], %[tr] offset:5760\n\t" \
    "ds_read_b64_tr_b16 v[104:105], %[tr] offset:160\n\t" \
    "ds_read_b64_tr_b16 v[106:107], %[tr] offset:5792\n\t" \
    "ds_read_b64_tr_b16 v[120:121], %[tr] offset:192\n\t" \
    "ds_read_b64_tr_b16 v[122:123], %[tr] offset:5824\n\t" \
    "ds_read_b64_tr_b16 v[144:145], %[tr] offset:224\n\t" \
    "ds_read_b64_tr_b16 v[146:147], %[tr] offset:5856\n\t" \
    "ds_read_b64_tr_b16 v[160:161], %[tr] offset:256\n\t" \
    "ds_read_b64_tr_b16 v[162:163], %[tr] offset:5888\n\t" \
    "ds_read_b64_tr_b16 v[184:185], %[tr] offset:288\n\t" \
    "ds_read_b64_tr_b16 v[186:187], %[tr] offset:5920\n\t" \
    "s_barrier \n\t" \
    "s_mov_b32 %[t], 0x19000\n\t" \
    "buffer_load_dwordx4 v[8:11], %[o0], %[rs], %[t] offen" WNT "\n\t" \
    "buffer_load_dwordx4 v[32:35], %[o1], %[rs], %[t] offen" WNT "\n\t" \
    "buffer_load_dwordx4 v[48:51], %[o2], %[rs], %[t] offen" WNT "\n\t" \
    "buffer_load_dwordx4 v[72:75], %[o3], %[rs], %[t] offen" WNT "\n\t" \
    "buffer_load_dwordx4 v[88:91], %[o4], %[rs], %[t] offen" WNT "\n\t" \
    "s_mov_b32 %[t], 0x23000\n\t" \
    "buffer_load_dwordx4 v[112:115], %[o0], %[rs], %[t] offen" WNT "\n\t" \
    "buffer_load_dwordx4 v[128:131], %[o1], %[rs], %[t] offen" WNT "\n\t" \
    "buffer_load_dwordx4 v[152:155], %[o2], %[rs], %[t] offen" WNT "\n\t" \
    "buffer_load_dwordx4 v[168:171], %[o3], %[rs], %[t] offen" WNT "\n\t" \
    "buffer_load_dwordx4 v[192:195], %[o4], %[rs], %[t] offen" WNT "\n\t" \
    "s_waitcnt lgkmcnt(0)\n\t" \
    "s_barrier \n\t" \
    "s_waitcnt vmcnt(15)\n\t" \
    "ds_write_b128 %[s0], v[4:7] offset:0\n\t" \
    "ds_write_b128 %[s1], v[28:31] offset:0\n\t" \
    "ds_write_b128 %[s2], v[44:47] offset:0\n\t" \
    "ds_write_b128 %[s3], v[68:71] offset:0\n\t" \
    "ds_write_b128 %[s4], v[84:87] offset:0\n\t" \
    "s_waitcnt vmcnt(10)\n\t" \
    "ds_write_b128 %[s0], v[108:111] offset:5632\n\t" \
    "ds_write_b128 %[s1], v[124:127] offset:5632\n\t" \
    "ds_write_b128 %[s2], v[148:151] offset:5632\n\t" \
    "ds_write_b128 %[s3], v[164:167] offset:5632\n\t" \
    "ds_write_b128 %[s4], v[188:191] offset:5632\n\t" \
    "ds_read_b64_tr_b16 v[4:5], %[tr] offset:0\n\t" \
    "ds_read_b64_tr_b16 v[6:7], %[tr] offset:5632\n\t" \
    "ds_read_b64_tr_b16 v[28:29], %[tr] offset:32\n\t" \
    "ds_read_b64_tr_b16 v[30:31], %[tr] offset:5664\n\t" \
    "ds_read_b64_tr_b16 v[44:45], %[tr] offset:64\n\t" \
    "ds_read_b64_tr_b16 v[46:47], %[tr] offset:5696\n\t" \
    "ds_read_b64_tr_b16 v[68:69], %[tr] offset:96\n\t" \
    "ds_read_b64_tr_b16 v[70:71], %[tr] offset:5728\n\t" \
    "ds_read_b64_tr_b16 v[84:85], %[tr] offset:128\n\t" \
    "ds_read_b64_tr_b16 v[86:87], %[tr] offset:5760\n\t" \
    "ds_read_b64_tr_b16 v[108:109], %[tr] offset:160\n\t" \
    "ds_read_b64_tr_b16 v[110:111], %[tr] offset:5792\n\t" \
    "ds_read_b64_tr_b16 v[124:125], %[tr] offset:192\n\t" \
    "ds_read_b64_tr_b16 v[126:127], %[tr] offset:5824\n\t" \
    "ds_read_b64_tr_b16 v[148:149], %[tr] offset:224\n\t" \
    "ds_read_b64_tr_b16 v[150:151], %[tr] offset:5856\n\t" \
    "ds_read_b64_tr_b16 v[164:165], %[tr] offset:256\n\t" \
    "ds_read_b64_tr_b16 v[166:167], %[tr] offset:5888\n\t" \
    "ds_read_b64_tr_b16 v[188:189], %[tr] offset:288\n\t" \
    "ds_read_b64_tr_b16 v[190:191], %[tr] offset:5920\n\t" \
    "s_barrier \n\t" \
    "s_mov_b32 %[t], 0x2d000\n\t" \
    "buffer_load_dwordx4 v[12:15], %[o0], %[rs], %[t] offen" WNT "\n\t" \
    "buffer_load_dwordx4 v[36:39], %[o1], %[rs], %[t] offen" WNT "\n\t" \
    "buffer_load_dwordx4 v[52:55], %[o2], %[rs], %[t] offen" WNT "\n\t" \
    "buffer_load_dwordx4 v[76:79], %[o3], %[rs], %[t] offen" WNT "\n\t" \
    "buffer_load_dwordx4 v[92:95], %[o4], %[rs], %[t] offen" WNT "\n\t" \
    "s_mov_b32 %[t], 0x37000\n\t" \
    "buffer_load_dwordx4 v[116:119], %[o0], %[rs], %[t] offen" WNT "\n\t" \
    "buffer_load_dwordx4 v[132:135], %[o1], %[rs], %[t] offen" WNT "\n\t" \
    "buffer_load_dwordx4 v[156:159], %[o2], %[rs], %[t] offen" WNT "\n\t" \
    "buffer_load_dwordx4 v[172:175], %[o3], %[rs], %[t] offen" WNT "\n\t" \
    "buffer_load_dwordx4 v[196:199], %[o4], %[rs], %[t] offen" WNT "\n\t" \
    "s_waitcnt lgkmcnt(0)\n\t" \
    "s_barrier \n\t" \
    "s_waitcnt vmcnt(15)\n\t" \
    "ds_write_b128 %[s0], v[8:11] offset:0\n\t" \
    "ds_write_b128 %[s1], v[32:35] offset:0\n\t" \
    "ds_write_b128 %[s2], v[48:51] offset:0\n\t" \
    "ds_write_b128 %[s3], v[72:75] offset:0\n\t" \
    "ds_write_b128 %[s4], v[88:91] offset:0\n\t" \
    "s_waitcnt vmcnt(10)\n\t" \
    "ds_write_b128 %[s0], v[112:115] offset:5632\n\t" \
    "ds_write_b128 %[s1], v[128:131] offset:5632\n\t" \
    "ds_write_b128 %[s2], v[152:155] offset:5632\n\t" \
    "ds_write_b128 %[s3], v[168:171] offset:5632\n\t" \
    "ds_write_b128 %[s4], v[192:195] offset:5632\n\t" \
    "ds_read_b64_tr_b16 v[8:9], %[tr] offset:0\n\t" \
    "ds_read_b64_tr_b16 v[10:11], %[tr] offset:5632\n\t" \
    "ds_read_b64_tr_b16 v[32:33], %[tr] offset:32\n\t" \
    "ds_read_b64_tr_b16 v[34:35], %[tr] offset:5664\n\t" \
    "ds_read_b64_tr_b16 v[48:49], %[tr] offset:64\n\t" \
    "ds_read_b64_tr_b16 v[50:51], %[tr] offset:5696\n\t" \
    "ds_read_b64_tr_b16 v[72:73], %[tr] offset:96\n\t" \
    "ds_read_b64_tr_b16 v[74:75], %[tr] offset:5728\n\t" \
    "ds_read_b64_tr_b16 v[88:89], %[tr] offset:128\n\t" \
    "ds_read_b64_tr_b16 v[90:91], %[tr] offset:5760\n\t" \
    "ds_read_b64_tr_b16 v[112:113], %[tr] offset:160\n\t" \
    "ds_read_b64_tr_b16 v[114:115], %[tr] offset:5792\n\t" \
    "ds_read_b64_tr_b16 v[128:129], %[tr] offset:192\n\t" \
    "ds_read_b64_tr_b16 v[130:131], %[tr] offset:5824\n\t" \
    "ds_read_b64_tr_b16 v[152:153], %[tr] offset:224\n\t" \
    "ds_read_b64_tr_b16 v[154:155], %[tr] offset:5856\n\t" \
    "ds_read_b64_tr_b16 v[168:169], %[tr] offset:256\n\t" \
    "ds_read_b64_tr_b16 v[170:171], %[tr] offset:5888\n\t" \
    "ds_read_b64_tr_b16 v[192:193], %[tr] offset:288\n\t" \
    "ds_read_b64_tr_b16 v[194:195], %[tr] offset:5920\n\t" \
    "s_waitcnt lgkmcnt(0)\n\t" \
    "s_barrier \n\t" \
    "s_waitcnt vmcnt(5)\n\t" \
    "ds_write_b128 %[s0], v[12:15] offset:0\n\t" \
    "ds_write_b128 %[s1], v[36:39] offset:0\n\t" \
    "ds_write_b128 %[s2], v[52:55] offset:0\n\t" \
    "ds_write_b128 %[s3], v[76:79] offset:0\n\t" \
    "ds_write_b128 %[s4], v[92:95] offset:0\n\t" \
    "s_waitcnt vmcnt(0)\n\t" \
    "ds_write_b128 %[s0], v[116:119] offset:5632\n\t" \
    "ds_write_b128 %[s1], v[132:135] offset:5632\n\t" \
    "ds_write_b128 %[s2], v[156:159] offset:5632\n\t" \
    "ds_write_b128 %[s3], v[172:175] offset:5632\n\t" \
    "ds_write_b128 %[s4], v[196:199] offset:5632\n\t" \
    "ds_read_b64_tr_b16 v[12:13], %[tr] offset:0\n\t" \
    "ds_read_b64_tr_b16 v[14:15], %[tr] offset:5632\n\t" \
    "ds_read_b64_tr_b16 v[36:37], %[tr] offset:32\n\t" \
    "ds_read_b64_tr_b16 v[38:39], %[tr] offset:5664\n\t" \
    "ds_read_b64_tr_b16 v[52:53], %[tr] offset:64\n\t" \
    "ds_read_b64_tr_b16 v[54:55], %[tr] offset:5696\n\t" \
    "ds_read_b64_tr_b16 v[76:77], %[tr] offset:96\n\t" \
    "ds_read_b64_tr_b16 v[78:79], %[tr] offset:5728\n\t" \
    "ds_read_b64_tr_b16 v[92:93], %[tr] offset:128\n\t" \
    "ds_read_b64_tr_b16 v[94:95], %[tr] offset:5760\n\t" \
    "ds_read_b64_tr_b16 v[116:117], %[tr] offset:160\n\t" \
    "ds_read_b64_tr_b16 v[118:119], %[tr] offset:5792\n\t" \
    "ds_read_b64_tr_b16 v[132:133], %[tr] offset:192\n\t" \
    "ds_read_b64_tr_b16 v[134:135], %[tr] offset:5824\n\t" \
    "ds_read_b64_tr_b16 v[156:157], %[tr] offset:224\n\t" \
    "ds_read_b64_tr_b16 v[158:159], %[tr] offset:5856\n\t" \
    "ds_read_b64_tr_b16 v[172:173], %[tr] offset:256\n\t" \
    "ds_read_b64_tr_b16 v[174:175], %[tr] offset:5888\n\t" \
    "ds_read_b64_tr_b16 v[196:197], %[tr] offset:288\n\t" \
    "ds_read_b64_tr_b16 v[198:199], %[tr] offset:5920\n\t" \
    "s_waitcnt lgkmcnt(0)\n\t" \
    "s_barrier \n\t" \
    "ds_write_b128 %[wl], v[200:203] offset:0\n\t" \
    "ds_write_b128 %[wl], v[204:207] offset:1024\n\t" \
    "ds_write_b128 %[wl], v[208:211] offset:2048\n\t" \
    "ds_write_b128 %[wl], v[212:215] offset:3072\n\t" \
    "ds_write_b128 %[wl], v[216:219] offset:4096\n\t" \
    "ds_write_b128 %[wl], v[220:223] offset:5120\n\t" \
    "ds_write_b128 %[wl], v[224:227] offset:6144\n\t" \
    "ds_write_b128 %[wl], v[228:231] offset:7168\n\t" \
    "ds_write_b128 %[wl], v[232:235] offset:8192\n\t" \
    "ds_write_b128 %[wl], v[236:239] offset:9216\n\t" \
    "s_waitcnt lgkmcnt(0)\n\t"
#define ASM_XG_S_LO \
    "s_waitcnt lgkmcnt(0)\n\t" \
    "ds_read_b128 v[224:227], %[xa] offset:0\n\t" \
    "ds_read_b128 v[244:247], %[wl] offset:0\n\t" \
    "ds_read_b128 v[228:231], %[xa] offset:16384\n\t" \
    "ds_read_b128 v[232:235], %[xa] offset:1024\n\t" \
    "ds_read_b128 v[236:239], %[xa] offset:17408\n\t" \
    "ds_read_b128 v[248:251], %[wl] offset:1024\n\t" \
    "s_waitcnt lgkmcnt(5)\n\t" \
    "v_mfma_f32_16x16x32_bf16 v[204:207], v[0:3], v[224:227], 0\n\t" \
    "v_xor_b32 v252, 0x800, %[xa]\n\t" \
    "ds_read_b128 v[240:243], v252 offset:0\n\t" \
    "v_mfma_f32_16x16x32_bf16 v[208:211], v[8:11], v[224:227], 0\n\t" \
    "s_waitcnt lgkmcnt(5)\n\t" \
    "v_mfma_f32_16x16x32_bf16 v[200:203], v[244:247], v[224:227], 0\n\t" \
    "s_waitcnt lgkmcnt(4)\n\t" \
    "v_mfma_f32_16x16x32_bf16 v[216:219], v[0:3], v[228:231], 0\n\t" \
    "ds_read_b128 v[224:227], v252 offset:16384\n\t" \
    "v_mfma_f32_16x16x32_bf16 v[220:223], v[8:11], v[228:231], 0\n\t" \
    "v_mfma_f32_16x16x32_bf16 v[212:215], v[244:247], v[228:231], 0\n\t" \
    "s_waitcnt lgkmcnt(4)\n\t" \
    "v_mfma_f32_16x16x32_bf16 v[204:207], v[4:7], v[232:235], v[204:207]\n\t" \
    "ds_read_b128 v[228:231], v252 offset:1024\n\t" \
    "v_mfma_f32_16x16x32_bf16 v[208:211], v[12:15], v[232:235], v[208:211]\n\t" \
    "ds_read_b128 v[244:247], %[wl] offset:2048\n\t" \
    "s_waitcnt lgkmcnt(4)\n\t" \
    "v_mfma_f32_16x16x32_bf16 v[200:203], v[248:251], v[232:235], v[200:203]\n\t" \
    "v_mfma_f32_16x16x32_bf16 v[216:219], v[4:7], v[236:239], v[216:219]\n\t" \
    "ds_read_b128 v[232:235], v252 offset:17408\n\t" \
    "v_mfma_f32_16x16x32_bf16 v[220:223], v[12:15], v[236:239], v[220:223]\n\t" \
    "v_mfma_f32_16x16x32_bf16 v[212:215], v[248:251], v[236:239], v[212:215]\n\t" \
    "s_waitcnt lgkmcnt(4)\n\t" \
    "v_mfma_f32_16x16x32_bf16 v[204:207], v[24:27], v[240:243], v[204:207]\n\t" \
    "v_xor_b32 v252, 0x1000, %[xa]\n\t" \
    "ds_read_b128 v[236:239], v252 offset:0\n\t" \
    "v_mfma_f32_16x16x32_bf16 v[208:211], v[32:35], v[240:243], v[208:211]\n\t" \
    "ds_read_b128 v[248:251], %[wl] offset:3072\n\t" \
    "v_mfma_f32_16x16x32_bf16 v[200:203], v[16:19], v[240:243], v[200:203]\n\t" \
    "s_waitcnt lgkmcnt(5)\n\t" \
    "v_mfma_f32_16x16x32_bf16 v[216:219], v[24:27], v[224:227], v[216:219]\n\t" \
    "ds_read_b128 v[240:243], v252 offset:16384\n\t" \
    "v_mfma_f32_16x16x32_bf16 v[220:223], v[32:35], v[224:227], v[220:223]\n\t" \
    "v_mfma_f32_16x16x32_bf16 v[212:215], v[16:19], v[224:227], v[212:215]\n\t" \
    "s_waitcnt lgkmcnt(5)\n\t" \
    "v_mfma_f32_16x16x32_bf16 v[204:207], v[28:31], v[228:231], v[204:207]\n\t" \
    "ds_read_b128 v[224:227], v252 offset:1024\n\t" \
    "v_mfma_f32_16x16x32_bf16 v[208:211], v[36:39], v[228:231], v[208:211]\n\t" \
    "v_mfma_f32_16x16x32_bf16 v[200:203], v[20:23], v[228:231], v[200:203]\n\t" \
    "s_waitcnt lgkmcnt(4)\n\t" \
    "v_mfma_f32_16x16x32_bf16 v[216:219], v[28:31], v[232:235], v[216:219]\n\t" \
    "ds_read_b128 v[228:231], v252 offset:17408\n\t" \
    "v_mfma_f32_16x16x32_bf16 v[220:223], v[36:39], v[232:235], v[220:223]\n\t" \
    "v_mfma_f32_16x16x32_bf16 v[212:215], v[20:23], v[232:235], v[212:215]\n\t" \
    "s_waitcnt lgkmcnt(4)\n\t" \
    "v_mfma_f32_16x16x32_bf16 v[204:207], v[40:43], v[236:239], v[204:207]\n\t" \
    "v_xor_b32 v252, 0x1800, %[xa]\n\t" \
    "ds_read_b128 v[232:235], v252 offset:0\n\t" \
    "v_mfma_f32_16x16x32_bf16 v[208:211], v[48:51], v[236:239], v[208:211]\n\t" \
    "v_mfma_f32_16x16x32_bf16 v[200:203], v[244:247], v[236:239], v[200:203]\n\t" \
    "s_waitcnt lgkmcnt(3)\n\t" \
    "v_mfma_f32_16x16x32_bf16 v[216:219], v[40:43], v[240:243], v[216:219]\n\t" \
    "ds_read_b128 v[236:239], v252 offset:16384\n\t" \
    "v_mfma_f32_16x16x32_bf16 v[220:223], v[48:51], v[240:243], v[220:223]\n\t" \
    "v_mfma_f32_16x16x32_bf16 v[212:215], v[244:247], v[240:243], v[212:215]\n\t" \
    "s_waitcnt lgkmcnt(3)\n\t" \
    "v_mfma_f32_16x16x32_bf16 v[204:207], v[44:47], v[224:227], v[204:207]\n\t" \
    "ds_read_b128 v[240:243], v252 offset:1024\n\t" \
    "v_mfma_f32_16x16x32_bf16 v[208:211], v[52:55], v[224:227], v[208:211]\n\t" \
    "ds_read_b128 v[244:247], %[wl] offset:8192\n\t" \
    "v_mfma_f32_16x16x32_bf16 v[200:203], v[248:251], v[224:227], v[200:203]\n\t" \
    "s_waitcnt lgkmcnt(4)\n\t" \
    "v_mfma_f32_16x16x32_bf16 v[216:219], v[44:47], v[228:231], v[216:219]\n\t" \
    "ds_read_b128 v[224:227], v252 offset:17408\n\t" \
    "v_mfma_f32_16x16x32_bf16 v[220:223], v[52:55], v[228:231], v[220:223]\n\t" \
    "v_mfma_f32_16x16x32_bf16 v[212:215], v[248:251], v[228:231], v[212:215]\n\t" \
    "s_waitcnt lgkmcnt(4)\n\t" \
    "v_mfma_f32_16x16x32_bf16 v[204:207], v[64:67], v[232:235], v[204:207]\n\t" \
    "v_and_b32 v252, 0x3ff, %[xa]\n\t" \
    "ds_read_b128 v[228:231], v252 offset:40960\n\t" \
    "v_mfma_f32_16x16x32_bf16 v[208:211], v[72:75], v[232:235], v[208:211]\n\t" \
    "ds_read_b128 v[248:251], %[wl] offset:9216\n\t" \
    "v_mfma_f32_16x16x32_bf16 v[200:203], v[56:59], v[232:235], v[200:203]\n\t" \
    "s_waitcnt lgkmcnt(5)\n\t" \
    "v_mfma_f32_16x16x32_bf16 v[216:219], v[64:67], v[236:239], v[216:219]\n\t" \
    "ds_read_b128 v[232:235], v252 offset:45056\n\t" \
    "v_mfma_f32_16x16x32_bf16 v[220:223], v[72:75], v[236:239], v[220:223]\n\t" \
    "v_mfma_f32_16x16x32_bf16 v[212:215], v[56:59], v[236:239], v[212:215]\n\t" \
    "s_waitcnt lgkmcnt(5)\n\t" \
    "v_mfma_f32_16x16x32_bf16 v[204:207], v[68:71], v[240:243], v[204:207]\n\t" \
    "ds_read_b128 v[236:239], v252 offset:41984\n\t" \
    "v_mfma_f32_16x16x32_bf16 v[208:211], v[76:79], v[240:243], v[208:211]\n\t" \
    "v_mfma_f32_16x16x32_bf16 v[200:203], v[60:63], v[240:243], v[200:203]\n\t" \
    "s_waitcnt lgkmcnt(4)\n\t" \
    "v_mfma_f32_16x16x32_bf16 v[216:219], v[68:71], v[224:227], v[216:219]\n\t" \
    "ds_read_b128 v[240:243], v252 offset:46080\n\t" \
    "v_mfma_f32_16x16x32_bf16 v[220:223], v[76:79], v[224:227], v[220:223]\n\t" \
    "v_mfma_f32_16x16x32_bf16 v[212:215], v[60:63], v[224:227], v[212:215]\n\t" \
    "s_waitcnt lgkmcnt(0)\n\t" \
    "s_barrier\n\t" ASM_ST \
    "v_mfma_f32_16x16x32_bf16 v[204:207], v[160:163], v[228:231], v[204:207]\n\t" \
    "v_xor_b32 v252, 0x2000, %[xa]\n\t" \
    "ds_read_b128 v[224:227], v252 offset:0\n\t" \
    "v_mfma_f32_16x16x32_bf16 v[208:211], v[168:171], v[228:231], v[208:211]\n\t" \
    "v_mfma_f32_16x16x32_bf16 v[200:203], v[244:247], v[228:231], v[200:203]\n\t" \
    "v_mfma_f32_16x16x32_bf16 v[216:219], v[160:163], v[232:235], v[216:219]\n\t" \
    "ds_read_b128 v[228:231], v252 offset:16384\n\t" \
    "v_mfma_f32_16x16x32_bf16 v[220:223], v[168:171], v[232:235], v[220:223]\n\t" \
    "v_mfma_f32_16x16x32_bf16 v[212:215], v[244:247], v[232:235], v[212:215]\n\t" \
    "v_mfma_f32_16x16x32_bf16 v[204:207], v[164:167], v[236:239], v[204:207]\n\t" \
    "ds_read_b128 v[232:235], v252 offset:1024\n\t" \
    "v_mfma_f32_16x16x32_bf16 v[208:211], v[172:175], v[236:239], v[208:211]\n\t" \
    "ds_read_b128 v[244:247], %[wl] offset:4096\n\t" \
    "v_mfma_f32_16x16x32_bf16 v[200:203], v[248:251], v[236:239], v[200:203]\n\t" \
    "v_mfma_f32_16x16x32_bf16 v[216:219], v[164:167], v[240:243], v[216:219]\n\t" \
    "ds_read_b128 v[236:239], v252 offset:17408\n\t" \
    "v_mfma_f32_16x16x32_bf16 v[220:223], v[172:175], v[240:243], v[220:223]\n\t" \
    "v_mfma_f32_16x16x32_bf16 v[212:215], v[248:251], v[240:243], v[212:215]\n\t" \
    "s_waitcnt lgkmcnt(4)\n\t" \
    "v_mfma_f32_16x16x32_bf16 v[204:207], v[80:83], v[224:227], v[204:207]\n\t" \
    "v_xor_b32 v252, 0x2800, %[xa]\n\t" \
    "ds_read_b128 v[240:243], v252 offset:0\n\t" \
    "v_mfma_f32_16x16x32_bf16 v[208:211], v[88:91], v[224:227], v[208:211]\n\t" \
    "ds_read_b128 v[248:251], %[wl] offset:5120\n\t" \
    "s_waitcnt lgkmcnt(3)\n\t" \
    "v_mfma_f32_16x16x32_bf16 v[200:203], v[244:247], v[224:227], v[200:203]\n\t" \
    "v_mfma_f32_16x16x32_bf16 v[216:219], v[80:83], v[228:231], v[216:219]\n\t" \
    "ds_read_b128 v[224:227], v252 offset:16384\n\t" \
    "v_mfma_f32_16x16x32_bf16 v[220:223], v[88:91], v[228:231], v[220:223]\n\t" \
    "v_mfma_f32_16x16x32_bf16 v[212:215], v[244:247], v[228:231], v[212:215]\n\t" \
    "v_mfma_f32_16x16x32_bf16 v[204:207], v[84:87], v[232:235], v[204:207]\n\t" \
    "ds_read_b128 v[228:231], v252 offset:1024\n\t" \
    "v_mfma_f32_16x16x32_bf16 v[208:211], v[92:95], v[232:235], v[208:211]\n\t" \
    "ds_read_b128 v[244:247], %[wl] offset:6144\n\t" \
    "s_waitcnt lgkmcnt(3)\n\t" \
    "v_mfma_f32_16x16x32_bf16 v[200:203], v[248:251], v[232:235], v[200:203]\n\t" \
    "v_mfma_f32_16x16x32_bf16 v[216:219], v[84:87], v[236:239], v[216:219]\n\t" \
    "ds_read_b128 v[232:235], v252 offset:17408\n\t" \
    "v_mfma_f32_16x16x32_bf16 v[220:223], v[92:95], v[236:239], v[220:223]\n\t" \
    "v_mfma_f32_16x16x32_bf16 v[212:215], v[248:251], v[236:239], v[212:215]\n\t" \
    "v_mfma_f32_16x16x32_bf16 v[204:207], v[104:107], v[240:243], v[204:207]\n\t" \
    "v_xor_b32 v252, 0x3000, %[xa]\n\t" \
    "ds_read_b128 v[236:239], v252 offset:0\n\t" \
    "v_mfma_f32_16x16x32_bf16 v[208:211], v[112:115], v[240:243], v[208:211]\n\t" \
    "ds_read_b128 v[248:251], %[wl] offset:7168\n\t" \
    "v_mfma_f32_16x16x32_bf16 v[200:203], v[96:99], v[240:243], v[200:203]\n\t" \
    "s_waitcnt lgkmcnt(5)\n\t" \
    "v_mfma_f32_16x16x32_bf16 v[216:219], v[104:107], v[224:227], v[216:219]\n\t" \
    "ds_read_b128 v[240:243], v252 offset:16384\n\t" \
    "v_mfma_f32_16x16x32_bf16 v[220:223], v[112:115], v[224:227], v[220:223]\n\t" \
    "v_mfma_f32_16x16x32_bf16 v[212:215], v[96:99], v[224:227], v[212:215]\n\t" \
    "s_waitcnt lgkmcnt(5)\n\t" \
    "v_mfma_f32_16x16x32_bf16 v[204:207], v[108:111], v[228:231], v[204:207]\n\t" \
    "ds_read_b128 v[224:227], v252 offset:1024\n\t" \
    "v_mfma_f32_16x16x32_bf16 v[208:211], v[116:119], v[228:231], v[208:211]\n\t" \
    "v_mfma_f32_16x16x32_bf16 v[200:203], v[100:103], v[228:231], v[200:203]\n\t" \
    "s_waitcnt lgkmcnt(4)\n\t" \
    "v_mfma_f32_16x16x32_bf16 v[216:219], v[108:111], v[232:235], v[216:219]\n\t" \
    "ds_read_b128 v[228:231], v252 offset:17408\n\t" \
    "v_mfma_f32_16x16x32_bf16 v[220:223], v[116:119], v[232:235], v[220:223]\n\t" \
    "v_mfma_f32_16x16x32_bf16 v[212:215], v[100:103], v[232:235], v[212:215]\n\t" \
    "s_waitcnt lgkmcnt(4)\n\t" \
    "v_mfma_f32_16x16x32_bf16 v[204:207], v[120:123], v[236:239], v[204:207]\n\t" \
    "v_xor_b32 v252, 0x3800, %[xa]\n\t" \
    "ds_read_b128 v[232:235], v252 offset:0\n\t" \
    "v_mfma_f32_16x16x32_bf16 v[208:211], v[128:131], v[236:239], v[208:211]\n\t" \
    "v_mfma_f32_16x16x32_bf16 v[200:203], v[244:247], v[236:239], v[200:203]\n\t" \
    "s_waitcnt lgkmcnt(3)\n\t" \
    "v_mfma_f32_16x16x32_bf16 v[216:219], v[120:123], v[240:243], v[216:219]\n\t" \
    "ds_read_b128 v[236:239], v252 offset:16384\n\t" \
    "v_mfma_f32_16x16x32_bf16 v[220:223], v[128:131], v[240:243], v[220:223]\n\t" \
    "v_mfma_f32_16x16x32_bf16 v[212:215], v[244:247], v[240:243], v[212:215]\n\t" \
    "s_waitcnt lgkmcnt(3)\n\t" \
    "v_mfma_f32_16x16x32_bf16 v[204:207], v[124:127], v[224:227], v[204:207]\n\t" \
    "ds_read_b128 v[240:243], v252 offset:1024\n\t" \
    "v_mfma_f32_16x16x32_bf16 v[208:211], v[132:135], v[224:227], v[208:211]\n\t" \
    "v_mfma_f32_16x16x32_bf16 v[200:203], v[248:251], v[224:227], v[200:203]\n\t" \
    "s_waitcnt lgkmcnt(3)\n\t" \
    "v_mfma_f32_16x16x32_bf16 v[216:219], v[124:127], v[228:231], v[216:219]\n\t" \
    "ds_read_b128 v[224:227], v252 offset:17408\n\t" \
    "v_mfma_f32_16x16x32_bf16 v[220:223], v[132:135], v[228:231], v[220:223]\n\t" \
    "v_mfma_f32_16x16x32_bf16 v[212:215], v[248:251], v[228:231], v[212:215]\n\t" \
    "s_waitcnt lgkmcnt(3)\n\t" \
    "v_mfma_f32_16x16x32_bf16 v[204:207], v[144:147], v[232:235], v[204:207]\n\t" \
    "v_and_b32 v252, 0x3ff, %[xa]\n\t" \
    "ds_read_b128 v[228:231], v252 offset:43008\n\t" \
    "v_mov_b64 v[244:245], 0\n\t" \
    "v_mfma_f32_16x16x32_bf16 v[208:211], v[152:155], v[232:235], v[208:211]\n\t" \
    "v_mov_b64 v[246:247], 0\n\t" \
    "v_mfma_f32_16x16x32_bf16 v[200:203], v[136:139], v[232:235], v[200:203]\n\t" \
    "v_mov_b64 v[248:249], 0\n\t" \
    "s_waitcnt lgkmcnt(3)\n\t" \
    "v_mfma_f32_16x16x32_bf16 v[216:219], v[144:147], v[236:239], v[216:219]\n\t" \
    "ds_read_b128 v[232:235], v252 offset:44032\n\t" \
    "v_mov_b64 v[250:251], 0\n\t" \
    "v_mfma_f32_16x16x32_bf16 v[220:223], v[152:155], v[236:239], v[220:223]\n\t" \
    "v_mfma_f32_16x16x32_bf16 v[212:215], v[136:139], v[236:239], v[212:215]\n\t" \
    "s_waitcnt lgkmcnt(3)\n\t" \
    "v_mfma_f32_16x16x32_bf16 v[204:207], v[148:151], v[240:243], v[204:207]\n\t" \
    "ds_read_b128 v[236:239], v252 offset:47104\n\t" \
    "v_mfma_f32_16x16x32_bf16 v[208:211], v[156:159], v[240:243], v[208:211]\n\t" \
    "v_mfma_f32_16x16x32_bf16 v[200:203], v[140:143], v[240:243], v[200:203]\n\t" \
    "s_waitcnt lgkmcnt(3)\n\t" \
    "v_mfma_f32_16x16x32_bf16 v[216:219], v[148:151], v[224:227], v[216:219]\n\t" \
    "ds_read_b128 v[240:243], v252 offset:48128\n\t" \
    "v_mfma_f32_16x16x32_bf16 v[220:223], v[156:159], v[224:227], v[220:223]\n\t" \
    "v_mfma_f32_16x16x32_bf16 v[212:215], v[140:143], v[224:227], v[212:215]\n\t" \
    "s_waitcnt lgkmcnt(3)\n\t" \
    "v_mfma_f32_16x16x32_bf16 v[204:207], v[184:187], v[228:231], v[204:207]\n\t" \
    "v_add_u32 v252, %[qs], v252\n\t" \
    "v_mfma_f32_16x16x32_bf16 v[208:211], v[192:195], v[228:231], v[208:211]\n\t" \
    "v_mov_b64 v[224:225], 0\n\t" \
    "v_mfma_f32_16x16x32_bf16 v[200:203], v[176:179], v[228:231], v[200:203]\n\t" \
    "v_mov_b64 v[226:227], 0\n\t" \
    "s_waitcnt lgkmcnt(2)\n\t" \
    "v_mfma_f32_16x16x32_bf16 v[204:207], v[188:191], v[232:235], v[204:207]\n\t" \
    "v_mfma_f32_16x16x32_bf16 v[208:211], v[196:199], v[232:235], v[208:211]\n\t" \
    "v_mfma_f32_16x16x32_bf16 v[200:203], v[180:183], v[232:235], v[200:203]\n\t" \
    "s_waitcnt lgkmcnt(1)\n\t" \
    "v_mfma_f32_16x16x32_bf16 v[216:219], v[184:187], v[236:239], v[216:219]\n\t" \
    "ds_read_b128 v[228:231], %[wl] offset:2048\n\t" \
    "ds_read_b128 v[232:235], %[wl] offset:3072\n\t" \
    "v_mfma_f32_16x16x32_bf16 v[220:223], v[192:195], v[236:239], v[220:223]\n\t" \
    "v_cvt_pk_bf16_f32 v204, v204, v205\n\t" \
    "v_cvt_pk_bf16_f32 v205, v206, v207\n\t" \
    "v_mfma_f32_16x16x32_bf16 v[212:215], v[176:179], v[236:239], v[212:215]\n\t" \
    "ds_write_b64 v252, v[204:205] offset:1024\n\t" \
    "v_cvt_pk_bf16_f32 v208, v208, v209\n\t" \
    "s_waitcnt lgkmcnt(3)\n\t" \
    "v_mfma_f32_16x16x32_bf16 v[216:219], v[188:191], v[240:243], v[216:219]\n\t" \
    "v_cvt_pk_bf16_f32 v209, v210, v211\n\t" \
    "ds_write_b64 v252, v[208:209] offset:2048\n\t" \
    "v_mfma_f32_16x16x32_bf16 v[220:223], v[196:199], v[240:243], v[220:223]\n\t" \
    "v_cvt_pk_bf16_f32 v200, v200, v201\n\t" \
    "v_cvt_pk_bf16_f32 v201, v202, v203\n\t" \
    "v_mfma_f32_16x16x32_bf16 v[212:215], v[180:183], v[240:243], v[212:215]\n\t" \
    "ds_write_b64 v252, v[200:201] offset:0\n\t" \
    "ds_read_b128 v[200:203], %[wl] offset:0\n\t" \
    "ds_read_b128 v[204:207], %[wl] offset:1024\n\t" \
    "v_cvt_pk_bf16_f32 v216, v216, v217\n\t" \
    "v_cvt_pk_bf16_f32 v217, v218, v219\n\t" \
    "ds_write_b64 v252, v[216:217] offset:7168\n\t" \
    "v_cvt_pk_bf16_f32 v220, v220, v221\n\t" \
    "v_cvt_pk_bf16_f32 v221, v222, v223\n\t" \
    "ds_write_b64 v252, v[220:221] offset:8192\n\t" \
    "v_cvt_pk_bf16_f32 v212, v212, v213\n\t" \
    "v_cvt_pk_bf16_f32 v213, v214, v215\n\t" \
    "ds_write_b64 v252, v[212:213] offset:6144\n\t" \
    "s_waitcnt lgkmcnt(0)\n\t"
#define ASM_XG_S_HI \
    "s_waitcnt lgkmcnt(0)\n\t" \
    "v_xor_b32 v252, 0x2000, %[xa]\n\t" \
    "ds_read_b128 v[224:227], v252 offset:0\n\t" \
    "ds_read_b128 v[244:247], %[wl] offset:4096\n\t" \
    "ds_read_b128 v[228:231], v252 offset:16384\n\t" \
    "ds_read_b128 v[232:235], v252 offset:1024\n\t" \
    "ds_read_b128 v[236:239], v252 offset:17408\n\t" \
    "ds_read_b128 v[248:251], %[wl] offset:5120\n\t" \
    "s_cmp_eq_u32 %[slp], 0\n\t" \
    "s_cbranch_scc1 .Lxs%=\n\t" \
    "s_sleep 10\n\t" \
    ".Lxs%=:\n\t" \
    "s_waitcnt lgkmcnt(5)\n\t" \
    "v_mfma_f32_16x16x32_bf16 v[204:207], v[80:83], v[224:227], 0\n\t" \
    "v_xor_b32 v252, 0x2800, %[xa]\n\t" \
    "ds_read_b128 v[240:243], v252 offset:0\n\t" \
    "v_mfma_f32_16x16x32_bf16 v[208:211], v[88:91], v[224:227], 0\n\t" \
    "s_waitcnt lgkmcnt(5)\n\t" \
    "v_mfma_f32_16x16x32_bf16 v[200:203], v[244:247], v[224:227], 0\n\t" \
    "s_waitcnt lgkmcnt(4)\n\t" \
    "v_mfma_f32_16x16x32_bf16 v[216:219], v[80:83], v[228:231], 0\n\t" \
    "ds_read_b128 v[224:227], v252 offset:16384\n\t" \
    "v_mfma_f32_16x16x32_bf16 v[220:223], v[88:91], v[228:231], 0\n\t" \
    "v_mfma_f32_16x16x32_bf16 v[212:215], v[244:247], v[228:231], 0\n\t" \
    "s_waitcnt lgkmcnt(4)\n\t" \
    "v_mfma_f32_16x16x32_bf16 v[204:207], v[84:87], v[232:235], v[204:207]\n\t" \
    "ds_read_b128 v[228:231], v252 offset:1024\n\t" \
    "v_mfma_f32_16x16x32_bf16 v[208:211], v[92:95], v[232:235], v[208:211]\n\t" \
    "ds_read_b128 v[244:247], %[wl] offset:6144\n\t" \
    "s_waitcnt lgkmcnt(4)\n\t" \
    "v_mfma_f32_16x16x32_bf16 v[200:203], v[248:251], v[232:235], v[200:203]\n\t" \
    "v_mfma_f32_16x16x32_bf16 v[216:219], v[84:87], v[236:239], v[216:219]\n\t" \
    "ds_read_b128 v[232:235], v252 offset:17408\n\t" \
    "v_mfma_f32_16x16x32_bf16 v[220:223], v[92:95], v[236:239], v[220:223]\n\t" \
    "v_mfma_f32_16x16x32_bf16 v[212:215], v[248:251], v[236:239], v[212:215]\n\t" \
    "s_waitcnt lgkmcnt(4)\n\t" \
    "v_mfma_f32_16x16x32_bf16 v[204:207], v[104:107], v[240:243], v[204:207]\n\t" \
    "v_xor_b32 v252, 0x3000, %[xa]\n\t" \
    "ds_read_b128 v[236:239], v252 offset:0\n\t" \
    "v_mfma_f32_16x16x32_bf16 v[208:211], v[112:115], v[240:243], v[208:211]\n\t" \
    "ds_read_b128 v[248:251], %[wl] offset:7168\n\t" \
    "v_mfma_f32_16x16x32_bf16 v[200:203], v[96:99], v[240:243], v[200:203]\n\t" \
    "s_waitcnt lgkmcnt(5)\n\t" \
    "v_mfma_f32_16x16x32_bf16 v[216:219], v[104:107], v[224:227], v[216:219]\n\t" \
    "ds_read_b128 v[240:243], v252 offset:16384\n\t" \
    "v_mfma_f32_16x16x32_bf16 v[220:223], v[112:115], v[224:227], v[220:223]\n\t" \
    "v_mfma_f32_16x16x32_bf16 v[212:215], v[96:99], v[224:227], v[212:215]\n\t" \
    "s_waitcnt lgkmcnt(5)\n\t" \
    "v_mfma_f32_16x16x32_bf16 v[204:207], v[108:111], v[228:231], v[204:207]\n\t" \
    "ds_read_b128 v[224:227], v252 offset:1024\n\t" \
    "v_mfma_f32_16x16x32_bf16 v[208:211], v[116:119], v[228:231], v[208:211]\n\t" \
    "v_mfma_f32_16x16x32_bf16 v[200:203], v[100:103], v[228:231], v[200:203]\n\t" \
    "s_waitcnt lgkmcnt(4)\n\t" \
    "v_mfma_f32_16x16x32_bf16 v[216:219], v[108:111], v[232:235], v[216:219]\n\t" \
    "ds_read_b128 v[228:231], v252 offset:17408\n\t" \
    "v_mfma_f32_16x16x32_bf16 v[220:223], v[116:119], v[232:235], v[220:223]\n\t" \
    "v_mfma_f32_16x16x32_bf16 v[212:215], v[100:103], v[232:235], v[212:215]\n\t" \
    "s_waitcnt lgkmcnt(4)\n\t" \
    "v_mfma_f32_16x16x32_bf16 v[204:207], v[120:123], v[236:239], v[204:207]\n\t" \
    "v_xor_b32 v252, 0x3800, %[xa]\n\t" \
    "ds_read_b128 v[232:235], v252 offset:0\n\t" \
    "v_mfma_f32_16x16x32_bf16 v[208:211], v[128:131], v[236:239], v[208:211]\n\t" \
    "v_mfma_f32_16x16x32_bf16 v[200:203], v[244:247], v[236:239], v[200:203]\n\t" \
    "s_waitcnt lgkmcnt(3)\n\t" \
    "v_mfma_f32_16x16x32_bf16 v[216:219], v[120:123], v[240:243], v[216:219]\n\t" \
    "ds_read_b128 v[236:239], v252 offset:16384\n\t" \
    "v_mfma_f32_16x16x32_bf16 v[220:223], v[128:131], v[240:243], v[220:223]\n\t" \
    "v_mfma_f32_16x16x32_bf16 v[212:215], v[244:247], v[240:243], v[212:215]\n\t" \
    "s_waitcnt lgkmcnt(3)\n\t" \
    "v_mfma_f32_16x16x32_bf16 v[204:207], v[124:127], v[224:227], v[204:207]\n\t" \
    "ds_read_b128 v[240:243], v252 offset:1024\n\t" \
    "v_mfma_f32_16x16x32_bf16 v[208:211], v[132:135], v[224:227], v[208:211]\n\t" \
    "ds_read_b128 v[244:247], %[wl] offset:8192\n\t" \
    "v_mfma_f32_16x16x32_bf16 v[200:203], v[248:251], v[224:227], v[200:203]\n\t" \
    "s_waitcnt lgkmcnt(4)\n\t" \
    "v_mfma_f32_16x16x32_bf16 v[216:219], v[124:127], v[228:231], v[216:219]\n\t" \
    "ds_read_b128 v[224:227], v252 offset:17408\n\t" \
    "v_mfma_f32_16x16x32_bf16 v[220:223], v[132:135], v[228:231], v[220:223]\n\t" \
    "v_mfma_f32_16x16x32_bf16 v[212:215], v[248:251], v[228:231], v[212:215]\n\t" \
    "s_waitcnt lgkmcnt(4)\n\t" \
    "v_mfma_f32_16x16x32_bf16 v[204:207], v[144:147], v[232:235], v[204:207]\n\t" \
    "v_and_b32 v252, 0x3ff, %[xa]\n\t" \
    "ds_read_b128 v[228:231], v252 offset:40960\n\t" \
    "v_mfma_f32_16x16x32_bf16 v[208:211], v[152:155], v[232:235], v[208:211]\n\t" \
    "ds_read_b128 v[248:251], %[wl] offset:9216\n\t" \
    "v_mfma_f32_16x16x32_bf16 v[200:203], v[136:139], v[232:235], v[200:203]\n\t" \
    "s_waitcnt lgkmcnt(5)\n\t" \
    "v_mfma_f32_16x16x32_bf16 v[216:219], v[144:147], v[236:239], v[216:219]\n\t" \
    "ds_read_b128 v[232:235], v252 offset:45056\n\t" \
    "v_mfma_f32_16x16x32_bf16 v[220:223], v[152:155], v[236:239], v[220:223]\n\t" \
    "v_mfma_f32_16x16x32_bf16 v[212:215], v[136:139], v[236:239], v[212:215]\n\t" \
    "s_waitcnt lgkmcnt(5)\n\t" \
    "v_mfma_f32_16x16x32_bf16 v[204:207], v[148:151], v[240:243], v[204:207]\n\t" \
    "ds_read_b128 v[236:239], v252 offset:41984\n\t" \
    "v_mfma_f32_16x16x32_bf16 v[208:211], v[156:159], v[240:243], v[208:211]\n\t" \
    "v_mfma_f32_16x16x32_bf16 v[200:203], v[140:143], v[240:243], v[200:203]\n\t" \
    "s_waitcnt lgkmcnt(4)\n\t" \
    "v_mfma_f32_16x16x32_bf16 v[216:219], v[148:151], v[224:227], v[216:219]\n\t" \
    "ds_read_b128 v[240:243], v252 offset:46080\n\t" \
    "v_mfma_f32_16x16x32_bf16 v[220:223], v[156:159], v[224:227], v[220:223]\n\t" \
    "v_mfma_f32_16x16x32_bf16 v[212:215], v[140:143], v[224:227], v[212:215]\n\t" \
    "s_waitcnt vmcnt(0) lgkmcnt(0)\n\t" \
    "s_barrier\n\t" ASM_ST \
    "s_mov_b32 m0, %[d0]\n\t" \
    "s_nop 0\n\t" \
    "global_load_lds_dwordx4 v252, %[g0] offset:0 sc1\n\t" \
    "global_load_lds_dwordx4 v252, %[g0] offset:1024 sc1\n\t" \
    "global_load_lds_dwordx4 v252, %[g0] offset:2048 sc1\n\t" \
    "global_load_lds_dwordx4 v252, %[g0] offset:3072 sc1\n\t" \
    "s_mov_b32 m0, %[d1]\n\t" \
    "s_nop 0\n\t" \
    "global_load_lds_dwordx4 v252, %[g1] sc1\n\t" \
    "v_mfma_f32_16x16x32_bf16 v[204:207], v[160:163], v[228:231], v[204:207]\n\t" \
    "ds_read_b128 v[224:227], %[xa] offset:0\n\t" \
    "v_mfma_f32_16x16x32_bf16 v[208:211], v[168:171], v[228:231], v[208:211]\n\t" \
    "v_mfma_f32_16x16x32_bf16 v[200:203], v[244:247], v[228:231], v[200:203]\n\t" \
    "v_mfma_f32_16x16x32_bf16 v[216:219], v[160:163], v[232:235], v[216:219]\n\t" \
    "ds_read_b128 v[228:231], %[xa] offset:16384\n\t" \
    "v_mfma_f32_16x16x32_bf16 v[220:223], v[168:171], v[232:235], v[220:223]\n\t" \
    "v_mfma_f32_16x16x32_bf16 v[212:215], v[244:247], v[232:235], v[212:215]\n\t" \
    "v_mfma_f32_16x16x32_bf16 v[204:207], v[164:167], v[236:239], v[204:207]\n\t" \
    "ds_read_b128 v[232:235], %[xa] offset:1024\n\t" \
    "v_mfma_f32_16x16x32_bf16 v[208:211], v[172:175], v[236:239], v[208:211]\n\t" \
    "ds_read_b128 v[244:247], %[wl] offset:0\n\t" \
    "v_mfma_f32_16x16x32_bf16 v[200:203], v[248:251], v[236:239], v[200:203]\n\t" \
    "v_mfma_f32_16x16x32_bf16 v[216:219], v[164:167], v[240:243], v[216:219]\n\t" \
    "ds_read_b128 v[236:239], %[xa] offset:17408\n\t" \
    "v_mfma_f32_16x16x32_bf16 v[220:223], v[172:175], v[240:243], v[220:223]\n\t" \
    "v_mfma_f32_16x16x32_bf16 v[212:215], v[248:251], v[240:243], v[212:215]\n\t" \
    "s_waitcnt lgkmcnt(4)\n\t" \
    "v_mfma_f32_16x16x32_bf16 v[204:207], v[0:3], v[224:227], v[204:207]\n\t" \
    "v_xor_b32 v252, 0x800, %[xa]\n\t" \
    "ds_read_b128 v[240:243], v252 offset:0\n\t" \
    "v_mfma_f32_16x16x32_bf16 v[208:211], v[8:11], v[224:227], v[208:211]\n\t" \
    "ds_read_b128 v[248:251], %[wl] offset:1024\n\t" \
    "s_waitcnt lgkmcnt(3)\n\t" \
    "v_mfma_f32_16x16x32_bf16 v[200:203], v[244:247], v[224:227], v[200:203]\n\t" \
    "v_mfma_f32_16x16x32_bf16 v[216:219], v[0:3], v[228:231], v[216:219]\n\t" \
    "ds_read_b128 v[224:227], v252 offset:16384\n\t" \
    "v_mfma_f32_16x16x32_bf16 v[220:223], v[8:11], v[228:231], v[220:223]\n\t" \
    "v_mfma_f32_16x16x32_bf16 v[212:215], v[244:247], v[228:231], v[212:215]\n\t" \
    "v_mfma_f32_16x16x32_bf16 v[204:207], v[4:7], v[232:235], v[204:207]\n\t" \
    "ds_read_b128 v[228:231], v252 offset:1024\n\t" \
    "v_mfma_f32_16x16x32_bf16 v[208:211], v[12:15], v[232:235], v[208:211]\n\t" \
    "ds_read_b128 v[244:247], %[wl] offset:2048\n\t" \
    "s_waitcnt lgkmcnt(3)\n\t" \
    "v_mfma_f32_16x16x32_bf16 v[200:203], v[248:251], v[232:235], v[200:203]\n\t" \
    "v_mfma_f32_16x16x32_bf16 v[216:219], v[4:7], v[236:239], v[216:219]\n\t" \
    "ds_read_b128 v[232:235], v252 offset:17408\n\t" \
    "v_mfma_f32_16x16x32_bf16 v[220:223], v[12:15], v[236:239], v[220:223]\n\t" \
    "v_mfma_f32_16x16x32_bf16 v[212:215], v[248:251], v[236:239], v[212:215]\n\t" \
    "v_mfma_f32_16x16x32_bf16 v[204:207], v[24:27], v[240:243], v[204:207]\n\t" \
    "v_xor_b32 v252, 0x1000, %[xa]\n\t" \
    "ds_read_b128 v[236:239], v252 offset:0\n\t" \
    "v_mfma_f32_16x16x32_bf16 v[208:211], v[32:35], v[240:243], v[208:211]\n\t" \
    "ds_read_b128 v[248:251], %[wl] offset:3072\n\t" \
    "v_mfma_f32_16x16x32_bf16 v[200:203], v[16:19], v[240:243], v[200:203]\n\t" \
    "s_waitcnt lgkmcnt(5)\n\t" \
    "v_mfma_f32_16x16x32_bf16 v[216:219], v[24:27], v[224:227], v[216:219]\n\t" \
    "ds_read_b128 v[240:243], v252 offset:16384\n\t" \
    "v_mfma_f32_16x16x32_bf16 v[220:223], v[32:35], v[224:227], v[220:223]\n\t" \
    "v_mfma_f32_16x16x32_bf16 v[212:215], v[16:19], v[224:227], v[212:215]\n\t" \
    "s_waitcnt lgkmcnt(5)\n\t" \
    "v_mfma_f32_16x16x32_bf16 v[204:207], v[28:31], v[228:231], v[204:207]\n\t" \
    "ds_read_b128 v[224:227], v252 offset:1024\n\t" \
    "v_mfma_f32_16x16x32_bf16 v[208:211], v[36:39], v[228:231], v[208:211]\n\t" \
    "v_mfma_f32_16x16x32_bf16 v[200:203], v[20:23], v[228:231], v[200:203]\n\t" \
    "s_waitcnt lgkmcnt(4)\n\t" \
    "v_mfma_f32_16x16x32_bf16 v[216:219], v[28:31], v[232:235], v[216:219]\n\t" \
    "ds_read_b128 v[228:231], v252 offset:17408\n\t" \
    "v_mfma_f32_16x16x32_bf16 v[220:223], v[36:39], v[232:235], v[220:223]\n\t" \
    "v_mfma_f32_16x16x32_bf16 v[212:215], v[20:23], v[232:235], v[212:215]\n\t" \
    "s_waitcnt lgkmcnt(4)\n\t" \
    "v_mfma_f32_16x16x32_bf16 v[204:207], v[40:43], v[236:239], v[204:207]\n\t" \
    "v_xor_b32 v252, 0x1800, %[xa]\n\t" \
    "ds_read_b128 v[232:235], v252 offset:0\n\t" \
    "v_mfma_f32_16x16x32_bf16 v[208:211], v[48:51], v[236:239], v[208:211]\n\t" \
    "v_mfma_f32_16x16x32_bf16 v[200:203], v[244:247], v[236:239], v[200:203]\n\t" \
    "s_waitcnt lgkmcnt(3)\n\t" \
    "v_mfma_f32_16x16x32_bf16 v[216:219], v[40:43], v[240:243], v[216:219]\n\t" \
    "ds_read_b128 v[236:239], v252 offset:16384\n\t" \
    "v_mfma_f32_16x16x32_bf16 v[220:223], v[48:51], v[240:243], v[220:223]\n\t" \
    "v_mfma_f32_16x16x32_bf16 v[212:215], v[244:247], v[240:243], v[212:215]\n\t" \
    "s_waitcnt lgkmcnt(3)\n\t" \
    "v_mfma_f32_16x16x32_bf16 v[204:207], v[44:47], v[224:227], v[204:207]\n\t" \
    "ds_read_b128 v[240:243], v252 offset:1024\n\t" \
    "v_mfma_f32_16x16x32_bf16 v[208:211], v[52:55], v[224:227], v[208:211]\n\t" \
    "v_mfma_f32_16x16x32_bf16 v[200:203], v[248:251], v[224:227], v[200:203]\n\t" \
    "s_waitcnt lgkmcnt(3)\n\t" \
    "v_mfma_f32_16x16x32_bf16 v[216:219], v[44:47], v[228:231], v[216:219]\n\t" \
    "ds_read_b128 v[224:227], v252 offset:17408\n\t" \
    "v_mfma_f32_16x16x32_bf16 v[220:223], v[52:55], v[228:231], v[220:223]\n\t" \
    "v_mfma_f32_16x16x32_bf16 v[212:215], v[248:251], v[228:231], v[212:215]\n\t" \
    "s_waitcnt lgkmcnt(3)\n\t" \
    "v_mfma_f32_16x16x32_bf16 v[204:207], v[64:67], v[232:235], v[204:207]\n\t" \
    "v_and_b32 v252, 0x3ff, %[xa]\n\t" \
    "ds_read_b128 v[228:231], v252 offset:43008\n\t" \
    "v_mov_b64 v[244:245], 0\n\t" \
    "v_mfma_f32_16x16x32_bf16 v[208:211], v[72:75], v[232:235], v[208:211]\n\t" \
    "v_mov_b64 v[246:247], 0\n\t" \
    "v_mfma_f32_16x16x32_bf16 v[200:203], v[56:59], v[232:235], v[200:203]\n\t" \
    "v_mov_b64 v[248:249], 0\n\t" \
    "s_waitcnt lgkmcnt(3)\n\t" \
    "v_mfma_f32_16x16x32_bf16 v[216:219], v[64:67], v[236:239], v[216:219]\n\t" \
    "ds_read_b128 v[232:235], v252 offset:44032\n\t" \
    "v_mov_b64 v[250:251], 0\n\t" \
    "v_mfma_f32_16x16x32_bf16 v[220:223], v[72:75], v[236:239], v[220:223]\n\t" \
    "v_mfma_f32_16x16x32_bf16 v[212:215], v[56:59], v[236:239], v[212:215]\n\t" \
    "s_waitcnt lgkmcnt(3)\n\t" \
    "v_mfma_f32_16x16x32_bf16 v[204:207], v[68:71], v[240:243], v[204:207]\n\t" \
    "ds_read_b128 v[236:239], v252 offset:47104\n\t" \
    "v_mfma_f32_16x16x32_bf16 v[208:211], v[76:79], v[240:243], v[208:211]\n\t" \
    "v_mfma_f32_16x16x32_bf16 v[200:203], v[60:63], v[240:243], v[200:203]\n\t" \
    "s_waitcnt lgkmcnt(3)\n\t" \
    "v_mfma_f32_16x16x32_bf16 v[216:219], v[68:71], v[224:227], v[216:219]\n\t" \
    "ds_read_b128 v[240:243], v252 offset:48128\n\t" \
    "v_mfma_f32_16x16x32_bf16 v[220:223], v[76:79], v[224:227], v[220:223]\n\t" \
    "v_mfma_f32_16x16x32_bf16 v[212:215], v[60:63], v[224:227], v[212:215]\n\t" \
    "s_waitcnt lgkmcnt(3)\n\t" \
    "v_mfma_f32_16x16x32_bf16 v[204:207], v[184:187], v[228:231], v[204:207]\n\t" \
    "v_add_u32 v252, %[qs], v252\n\t" \
    "v_mfma_f32_16x16x32_bf16 v[208:211], v[192:195], v[228:231], v[208:211]\n\t" \
    "v_mov_b64 v[224:225], 0\n\t" \
    "v_mfma_f32_16x16x32_bf16 v[200:203], v[176:179], v[228:231], v[200:203]\n\t" \
    "v_mov_b64 v[226:227], 0\n\t" \
    "s_waitcnt lgkmcnt(2)\n\t" \
    "v_mfma_f32_16x16x32_bf16 v[204:207], v[188:191], v[232:235], v[204:207]\n\t" \
    "v_mfma_f32_16x16x32_bf16 v[208:211], v[196:199], v[232:235], v[208:211]\n\t" \
    "v_mfma_f32_16x16x32_bf16 v[200:203], v[180:183], v[232:235], v[200:203]\n\t" \
    "s_waitcnt lgkmcnt(1)\n\t" \
    "v_mfma_f32_16x16x32_bf16 v[216:219], v[184:187], v[236:239], v[216:219]\n\t" \
    "ds_read_b128 v[228:231], %[wl] offset:2048\n\t" \
    "ds_read_b128 v[232:235], %[wl] offset:3072\n\t" \
    "v_mfma_f32_16x16x32_bf16 v[220:223], v[192:195], v[236:239], v[220:223]\n\t" \
    "v_cvt_pk_bf16_f32 v204, v204, v205\n\t" \
    "v_cvt_pk_bf16_f32 v205, v206, v207\n\t" \
    "v_mfma_f32_16x16x32_bf16 v[212:215], v[176:179], v[236:239], v[212:215]\n\t" \
    "ds_write_b64 v252, v[204:205] offset:1024\n\t" \
    "v_cvt_pk_bf16_f32 v208, v208, v209\n\t" \
    "s_waitcnt lgkmcnt(3)\n\t" \
    "v_mfma_f32_16x16x32_bf16 v[216:219], v[188:191], v[240:243], v[216:219]\n\t" \
    "v_cvt_pk_bf16_f32 v209, v210, v211\n\t" \
    "ds_write_b64 v252, v[208:209] offset:2048\n\t" \
    "v_mfma_f32_16x16x32_bf16 v[220:223], v[196:199], v[240:243], v[220:223]\n\t" \
    "v_cvt_pk_bf16_f32 v200, v200, v201\n\t" \
    "v_cvt_pk_bf16_f32 v201, v202, v203\n\t" \
    "v_mfma_f32_16x16x32_bf16 v[212:215], v[180:183], v[240:243], v[212:215]\n\t" \
    "ds_write_b64 v252, v[200:201] offset:0\n\t" \
    "ds_read_b128 v[200:203], %[wl] offset:0\n\t" \
    "ds_read_b128 v[204:207], %[wl] offset:1024\n\t" \
    "v_cvt_pk_bf16_f32 v216, v216, v217\n\t" \
    "v_cvt_pk_bf16_f32 v217, v218, v219\n\t" \
    "ds_write_b64 v252, v[216:217] offset:7168\n\t" \
    "v_cvt_pk_bf16_f32 v220, v220, v221\n\t" \
    "v_cvt_pk_bf16_f32 v221, v222, v223\n\t" \
    "ds_write_b64 v252, v[220:221] offset:8192\n\t" \
    "v_cvt_pk_bf16_f32 v212, v212, v213\n\t" \
    "v_cvt_pk_bf16_f32 v213, v214, v215\n\t" \
    "ds_write_b64 v252, v[212:213] offset:6144\n\t" \
    "s_waitcnt lgkmcnt(0)\n\t"
#define ASM_XG_S_LO2 \
    "s_waitcnt lgkmcnt(0)\n\t" \
    "ds_read_b128 v[224:227], %[xa] offset:0\n\t" \
    "ds_read_b128 v[228:231], %[xa] offset:16384\n\t" \
    "ds_read_b128 v[232:235], %[xa] offset:1024\n\t" \
    "ds_read_b128 v[236:239], %[xa] offset:17408\n\t" \
    "s_waitcnt lgkmcnt(3)\n\t" \
    "v_mfma_f32_16x16x32_bf16 v[204:207], v[0:3], v[224:227], 0\n\t" \
    "v_xor_b32 v252, 0x800, %[xa]\n\t" \
    "ds_read_b128 v[240:243], v252 offset:0\n\t" \
    "v_mfma_f32_16x16x32_bf16 v[208:211], v[8:11], v[224:227], 0\n\t" \
    "s_waitcnt lgkmcnt(3)\n\t" \
    "v_mfma_f32_16x16x32_bf16 v[216:219], v[0:3], v[228:231], 0\n\t" \
    "ds_read_b128 v[224:227], v252 offset:16384\n\t" \
    "v_mfma_f32_16x16x32_bf16 v[220:223], v[8:11], v[228:231], 0\n\t" \
    "s_waitcnt lgkmcnt(3)\n\t" \
    "v_mfma_f32_16x16x32_bf16 v[204:207], v[4:7], v[232:235], v[204:207]\n\t" \
    "ds_read_b128 v[228:231], v252 offset:1024\n\t" \
    "v_mfma_f32_16x16x32_bf16 v[208:211], v[12:15], v[232:235], v[208:211]\n\t" \
    "s_waitcnt lgkmcnt(3)\n\t" \
    "v_mfma_f32_16x16x32_bf16 v[216:219], v[4:7], v[236:239], v[216:219]\n\t" \
    "ds_read_b128 v[232:235], v252 offset:17408\n\t" \
    "v_mfma_f32_16x16x32_bf16 v[220:223], v[12:15], v[236:239], v[220:223]\n\t" \
    "s_waitcnt lgkmcnt(3)\n\t" \
    "v_mfma_f32_16x16x32_bf16 v[204:207], v[24:27], v[240:243], v[204:207]\n\t" \
    "v_xor_b32 v252, 0x1000, %[xa]\n\t" \
    "ds_read_b128 v[236:239], v252 offset:0\n\t" \
    "v_mfma_f32_16x16x32_bf16 v[208:211], v[32:35], v[240:243], v[208:211]\n\t" \
    "s_waitcnt lgkmcnt(3)\n\t" \
    "v_mfma_f32_16x16x32_bf16 v[216:219], v[24:27], v[224:227], v[216:219]\n\t" \
    "ds_read_b128 v[240:243], v252 offset:16384\n\t" \
    "v_mfma_f32_16x16x32_bf16 v[220:223], v[32:35], v[224:227], v[220:223]\n\t" \
    "s_waitcnt lgkmcnt(3)\n\t" \
    "v_mfma_f32_16x16x32_bf16 v[204:207], v[28:31], v[228:231], v[204:207]\n\t" \
    "ds_read_b128 v[224:227], v252 offset:1024\n\t" \
    "v_mfma_f32_16x16x32_bf16 v[208:211], v[36:39], v[228:231], v[208:211]\n\t" \
    "s_waitcnt lgkmcnt(3)\n\t" \
    "v_mfma_f32_16x16x32_bf16 v[216:219], v[28:31], v[232:235], v[216:219]\n\t" \
    "ds_read_b128 v[228:231], v252 offset:17408\n\t" \
    "v_mfma_f32_16x16x32_bf16 v[220:223], v[36:39], v[232:235], v[220:223]\n\t" \
    "s_waitcnt lgkmcnt(3)\n\t" \
    "v_mfma_f32_16x16x32_bf16 v[204:207], v[40:43], v[236:239], v[204:207]\n\t" \
    "v_xor_b32 v252, 0x1800, %[xa]\n\t" \
    "ds_read_b128 v[232:235], v252 offset:0\n\t" \
    "v_mfma_f32_16x16x32_bf16 v[208:211], v[48:51], v[236:239], v[208:211]\n\t" \
    "s_waitcnt lgkmcnt(3)\n\t" \
    "v_mfma_f32_16x16x32_bf16 v[216:219], v[40:43], v[240:243], v[216:219]\n\t" \
    "ds_read_b128 v[236:239], v252 offset:16384\n\t" \
    "v_mfma_f32_16x16x32_bf16 v[220:223], v[48:51], v[240:243], v[220:223]\n\t" \
    "s_waitcnt lgkmcnt(3)\n\t" \
    "v_mfma_f32_16x16x32_bf16 v[204:207], v[44:47], v[224:227], v[204:207]\n\t" \
    "ds_read_b128 v[240:243], v252 offset:1024\n\t" \
    "v_mfma_f32_16x16x32_bf16 v[208:211], v[52:55], v[224:227], v[208:211]\n\t" \
    "s_waitcnt lgkmcnt(3)\n\t" \
    "v_mfma_f32_16x16x32_bf16 v[216:219], v[44:47], v[228:231], v[216:219]\n\t" \
    "ds_read_b128 v[224:227], v252 offset:17408\n\t" \
    "v_mfma_f32_16x16x32_bf16 v[220:223], v[52:55], v[228:231], v[220:223]\n\t" \
    "s_waitcnt lgkmcnt(3)\n\t" \
    "v_mfma_f32_16x16x32_bf16 v[204:207], v[64:67], v[232:235], v[204:207]\n\t" \
    "v_and_b32 v252, 0x3ff, %[xa]\n\t" \
    "ds_read_b128 v[228:231], v252 offset:40960\n\t" \
    "v_mfma_f32_16x16x32_bf16 v[208:211], v[72:75], v[232:235], v[208:211]\n\t" \
    "s_waitcnt lgkmcnt(3)\n\t" \
    "v_mfma_f32_16x16x32_bf16 v[216:219], v[64:67], v[236:239], v[216:219]\n\t" \
    "ds_read_b128 v[232:235], v252 offset:45056\n\t" \
    "v_mfma_f32_16x16x32_bf16 v[220:223], v[72:75], v[236:239], v[220:223]\n\t" \
    "s_waitcnt lgkmcnt(3)\n\t" \
    "v_mfma_f32_16x16x32_bf16 v[204:207], v[68:71], v[240:243], v[204:207]\n\t" \
    "ds_read_b128 v[236:239], v252 offset:41984\n\t" \
    "v_mfma_f32_16x16x32_bf16 v[208:211], v[76:79], v[240:243], v[208:211]\n\t" \
    "s_waitcnt lgkmcnt(3)\n\t" \
    "v_mfma_f32_16x16x32_bf16 v[216:219], v[68:71], v[224:227], v[216:219]\n\t" \
    "ds_read_b128 v[240:243], v252 offset:46080\n\t" \
    "v_mfma_f32_16x16x32_bf16 v[220:223], v[76:79], v[224:227], v[220:223]\n\t" \
    "s_waitcnt lgkmcnt(0)\n\t" \
    "s_barrier\n\t" ASM_ST \
    "v_mfma_f32_16x16x32_bf16 v[204:207], v[160:163], v[228:231], v[204:207]\n\t" \
    "v_xor_b32 v252, 0x2000, %[xa]\n\t" \
    "ds_read_b128 v[224:227], v252 offset:0\n\t" \
    "v_mfma_f32_16x16x32_bf16 v[208:211], v[168:171], v[228:231], v[208:211]\n\t" \
    "v_mfma_f32_16x16x32_bf16 v[216:219], v[160:163], v[232:235], v[216:219]\n\t" \
    "ds_read_b128 v[228:231], v252 offset:16384\n\t" \
    "v_mfma_f32_16x16x32_bf16 v[220:223], v[168:171], v[232:235], v[220:223]\n\t" \
    "v_mfma_f32_16x16x32_bf16 v[204:207], v[164:167], v[236:239], v[204:207]\n\t" \
    "ds_read_b128 v[232:235], v252 offset:1024\n\t" \
    "v_mfma_f32_16x16x32_bf16 v[208:211], v[172:175], v[236:239], v[208:211]\n\t" \
    "v_mfma_f32_16x16x32_bf16 v[216:219], v[164:167], v[240:243], v[216:219]\n\t" \
    "ds_read_b128 v[236:239], v252 offset:17408\n\t" \
    "v_mfma_f32_16x16x32_bf16 v[220:223], v[172:175], v[240:243], v[220:223]\n\t" \
    "s_waitcnt lgkmcnt(3)\n\t" \
    "v_mfma_f32_16x16x32_bf16 v[204:207], v[80:83], v[224:227], v[204:207]\n\t" \
    "v_xor_b32 v252, 0x2800, %[xa]\n\t" \
    "ds_read_b128 v[240:243], v252 offset:0\n\t" \
    "v_mfma_f32_16x16x32_bf16 v[208:211], v[88:91], v[224:227], v[208:211]\n\t" \
    "s_waitcnt lgkmcnt(3)\n\t" \
    "v_mfma_f32_16x16x32_bf16 v[216:219], v[80:83], v[228:231], v[216:219]\n\t" \
    "ds_read_b128 v[224:227], v252 offset:16384\n\t" \
    "v_mfma_f32_16x16x32_bf16 v[220:223], v[88:91], v[228:231], v[220:223]\n\t" \
    "s_waitcnt lgkmcnt(3)\n\t" \
    "v_mfma_f32_16x16x32_bf16 v[204:207], v[84:87], v[232:235], v[204:207]\n\t" \
    "ds_read_b128 v[228:231], v252 offset:1024\n\t" \
    "v_mfma_f32_16x16x32_bf16 v[208:211], v[92:95], v[232:235], v[208:211]\n\t" \
    "s_waitcnt lgkmcnt(3)\n\t" \
    "v_mfma_f32_16x16x32_bf16 v[216:219], v[84:87], v[236:239], v[216:219]\n\t" \
    "ds_read_b128 v[232:235], v252 offset:17408\n\t" \
    "v_mfma_f32_16x16x32_bf16 v[220:223], v[92:95], v[236:239], v[220:223]\n\t" \
    "s_waitcnt lgkmcnt(3)\n\t" \
    "v_mfma_f32_16x16x32_bf16 v[204:207], v[104:107], v[240:243], v[204:207]\n\t" \
    "v_xor_b32 v252, 0x3000, %[xa]\n\t" \
    "ds_read_b128 v[236:239], v252 offset:0\n\t" \
    "v_mfma_f32_16x16x32_bf16 v[208:211], v[112:115], v[240:243], v[208:211]\n\t" \
    "s_waitcnt lgkmcnt(3)\n\t" \
    "v_mfma_f32_16x16x32_bf16 v[216:219], v[104:107], v[224:227], v[216:219]\n\t" \
    "ds_read_b128 v[240:243], v252 offset:16384\n\t" \
    "v_mfma_f32_16x16x32_bf16 v[220:223], v[112:115], v[224:227], v[220:223]\n\t" \
    "s_waitcnt lgkmcnt(3)\n\t" \
    "v_mfma_f32_16x16x32_bf16 v[204:207], v[108:111], v[228:231], v[204:207]\n\t" \
    "ds_read_b128 v[224:227], v252 offset:1024\n\t" \
    "v_mfma_f32_16x16x32_bf16 v[208:211], v[116:119], v[228:231], v[208:211]\n\t" \
    "s_waitcnt lgkmcnt(3)\n\t" \
    "v_mfma_f32_16x16x32_bf16 v[216:219], v[108:111], v[232:235], v[216:219]\n\t" \
    "ds_read_b128 v[228:231], v252 offset:17408\n\t" \
    "v_mfma_f32_16x16x32_bf16 v[220:223], v[116:119], v[232:235], v[220:223]\n\t" \
    "s_waitcnt lgkmcnt(3)\n\t" \
    "v_mfma_f32_16x16x32_bf16 v[204:207], v[120:123], v[236:239], v[204:207]\n\t" \
    "v_xor_b32 v252, 0x3800, %[xa]\n\t" \
    "ds_read_b128 v[232:235], v252 offset:0\n\t" \
    "v_mfma_f32_16x16x32_bf16 v[208:211], v[128:131], v[236:239], v[208:211]\n\t" \
    "s_waitcnt lgkmcnt(3)\n\t" \
    "v_mfma_f32_16x16x32_bf16 v[216:219], v[120:123], v[240:243], v[216:219]\n\t" \
    "ds_read_b128 v[236:239], v252 offset:16384\n\t" \
    "v_mfma_f32_16x16x32_bf16 v[220:223], v[128:131], v[240:243], v[220:223]\n\t" \
    "s_waitcnt lgkmcnt(3)\n\t" \
    "v_mfma_f32_16x16x32_bf16 v[204:207], v[124:127], v[224:227], v[204:207]\n\t" \
    "ds_read_b128 v[240:243], v252 offset:1024\n\t" \
    "v_mfma_f32_16x16x32_bf16 v[208:211], v[132:135], v[224:227], v[208:211]\n\t" \
    "s_waitcnt lgkmcnt(3)\n\t" \
    "v_mfma_f32_16x16x32_bf16 v[216:219], v[124:127], v[228:231], v[216:219]\n\t" \
    "ds_read_b128 v[224:227], v252 offset:17408\n\t" \
    "v_mfma_f32_16x16x32_bf16 v[220:223], v[132:135], v[228:231], v[220:223]\n\t" \
    "s_waitcnt lgkmcnt(3)\n\t" \
    "v_mfma_f32_16x16x32_bf16 v[204:207], v[144:147], v[232:235], v[204:207]\n\t" \
    "v_and_b32 v252, 0x3ff, %[xa]\n\t" \
    "ds_read_b128 v[228:231], v252 offset:43008\n\t" \
    "v_mov_b64 v[244:245], 0\n\t" \
    "v_mfma_f32_16x16x32_bf16 v[208:211], v[152:155], v[232:235], v[208:211]\n\t" \
    "v_mov_b64 v[246:247], 0\n\t" \
    "s_waitcnt lgkmcnt(3)\n\t" \
    "v_mfma_f32_16x16x32_bf16 v[216:219], v[144:147], v[236:239], v[216:219]\n\t" \
    "ds_read_b128 v[232:235], v252 offset:44032\n\t" \
    "v_mov_b64 v[248:249], 0\n\t" \
    "v_mfma_f32_16x16x32_bf16 v[220:223], v[152:155], v[236:239], v[220:223]\n\t" \
    "v_mov_b64 v[250:251], 0\n\t" \
    "s_waitcnt lgkmcnt(3)\n\t" \
    "v_mfma_f32_16x16x32_bf16 v[204:207], v[148:151], v[240:243], v[204:207]\n\t" \
    "ds_read_b128 v[236:239], v252 offset:47104\n\t" \
    "v_mfma_f32_16x16x32_bf16 v[208:211], v[156:159], v[240:243], v[208:211]\n\t" \
    "s_waitcnt lgkmcnt(3)\n\t" \
    "v_mfma_f32_16x16x32_bf16 v[216:219], v[148:151], v[224:227], v[216:219]\n\t" \
    "ds_read_b128 v[240:243], v252 offset:48128\n\t" \
    "v_mfma_f32_16x16x32_bf16 v[220:223], v[156:159], v[224:227], v[220:223]\n\t" \
    "s_waitcnt lgkmcnt(3)\n\t" \
    "v_mfma_f32_16x16x32_bf16 v[204:207], v[184:187], v[228:231], v[204:207]\n\t" \
    "v_add_u32 v252, %[qs], v252\n\t" \
    "v_mfma_f32_16x16x32_bf16 v[208:211], v[192:195], v[228:231], v[208:211]\n\t" \
    "v_mov_b64 v[224:225], 0\n\t" \
    "s_waitcnt lgkmcnt(2)\n\t" \
    "v_mfma_f32_16x16x32_bf16 v[204:207], v[188:191], v[232:235], v[204:207]\n\t" \
    "v_mov_b64 v[226:227], 0\n\t" \
    "v_mfma_f32_16x16x32_bf16 v[208:211], v[196:199], v[232:235], v[208:211]\n\t" \
    "s_waitcnt lgkmcnt(1)\n\t" \
    "v_mfma_f32_16x16x32_bf16 v[216:219], v[184:187], v[236:239], v[216:219]\n\t" \
    "ds_read_b128 v[228:231], %[wl] offset:2048\n\t" \
    "ds_read_b128 v[232:235], %[wl] offset:3072\n\t" \
    "v_mfma_f32_16x16x32_bf16 v[220:223], v[192:195], v[236:239], v[220:223]\n\t" \
    "v_cvt_pk_bf16_f32 v204, v204, v205\n\t" \
    "v_cvt_pk_bf16_f32 v205, v206, v207\n\t" \
    "s_waitcnt lgkmcnt(2)\n\t" \
    "v_mfma_f32_16x16x32_bf16 v[216:219], v[188:191], v[240:243], v[216:219]\n\t" \
    "ds_write_b64 v252, v[204:205] offset:1024\n\t" \
    "v_cvt_pk_bf16_f32 v208, v208, v209\n\t" \
    "v_mfma_f32_16x16x32_bf16 v[220:223], v[196:199], v[240:243], v[220:223]\n\t" \
    "v_cvt_pk_bf16_f32 v209, v210, v211\n\t" \
    "ds_write_b64 v252, v[208:209] offset:2048\n\t" \
    "ds_read_b128 v[200:203], %[wl] offset:0\n\t" \
    "ds_read_b128 v[204:207], %[wl] offset:1024\n\t" \
    "v_cvt_pk_bf16_f32 v216, v216, v217\n\t" \
    "v_cvt_pk_bf16_f32 v217, v218, v219\n\t" \
    "ds_write_b64 v252, v[216:217] offset:7168\n\t" \
    "s_nop 0\n\t" \
    "v_cvt_pk_bf16_f32 v220, v220, v221\n\t" \
    "v_cvt_pk_bf16_f32 v221, v222, v223\n\t" \
    "ds_write_b64 v252, v[220:221] offset:8192\n\t" \
    "s_waitcnt lgkmcnt(0)\n\t"
#define ASM_XG_S_LO1 \
    "s_waitcnt lgkmcnt(0)\n\t" \
    "ds_read_b128 v[224:227], %[xa] offset:0\n\t" \
    "ds_read_b128 v[228:231], %[xa] offset:16384\n\t" \
    "ds_read_b128 v[232:235], %[xa] offset:1024\n\t" \
    "ds_read_b128 v[236:239], %[xa] offset:17408\n\t" \
    "s_waitcnt lgkmcnt(3)\n\t" \
    "v_mfma_f32_16x16x32_bf16 v[208:211], v[8:11], v[224:227], 0\n\t" \
    "v_xor_b32 v252, 0x800, %[xa]\n\t" \
    "ds_read_b128 v[240:243], v252 offset:0\n\t" \
    "s_waitcnt lgkmcnt(3)\n\t" \
    "v_mfma_f32_16x16x32_bf16 v[220:223], v[8:11], v[228:231], 0\n\t" \
    "ds_read_b128 v[224:227], v252 offset:16384\n\t" \
    "s_waitcnt lgkmcnt(3)\n\t" \
    "v_mfma_f32_16x16x32_bf16 v[208:211], v[12:15], v[232:235], v[208:211]\n\t" \
    "ds_read_b128 v[228:231], v252 offset:1024\n\t" \
    "s_waitcnt lgkmcnt(3)\n\t" \
    "v_mfma_f32_16x16x32_bf16 v[220:223], v[12:15], v[236:239], v[220:223]\n\t" \
    "ds_read_b128 v[232:235], v252 offset:17408\n\t" \
    "s_waitcnt lgkmcnt(3)\n\t" \
    "v_mfma_f32_16x16x32_bf16 v[208:211], v[32:35], v[240:243], v[208:211]\n\t" \
    "v_xor_b32 v252, 0x1000, %[xa]\n\t" \
    "ds_read_b128 v[236:239], v252 offset:0\n\t" \
    "s_waitcnt lgkmcnt(3)\n\t" \
    "v_mfma_f32_16x16x32_bf16 v[220:223], v[32:35], v[224:227], v[220:223]\n\t" \
    "ds_read_b128 v[240:243], v252 offset:16384\n\t" \
    "s_waitcnt lgkmcnt(3)\n\t" \
    "v_mfma_f32_16x16x32_bf16 v[208:211], v[36:39], v[228:231], v[208:211]\n\t" \
    "ds_read_b128 v[224:227], v252 offset:1024\n\t" \
    "s_waitcnt lgkmcnt(3)\n\t" \
    "v_mfma_f32_16x16x32_bf16 v[220:223], v[36:39], v[232:235], v[220:223]\n\t" \
    "ds_read_b128 v[228:231], v252 offset:17408\n\t" \
    "s_waitcnt lgkmcnt(3)\n\t" \
    "v_mfma_f32_16x16x32_bf16 v[208:211], v[48:51], v[236:239], v[208:211]\n\t" \
    "v_xor_b32 v252, 0x1800, %[xa]\n\t" \
    "ds_read_b128 v[232:235], v252 offset:0\n\t" \
    "s_waitcnt lgkmcnt(3)\n\t" \
    "v_mfma_f32_16x16x32_bf16 v[220:223], v[48:51], v[240:243], v[220:223]\n\t" \
    "ds_read_b128 v[236:239], v252 offset:16384\n\t" \
    "s_waitcnt lgkmcnt(3)\n\t" \
    "v_mfma_f32_16x16x32_bf16 v[208:211], v[52:55], v[224:227], v[208:211]\n\t" \
    "ds_read_b128 v[240:243], v252 offset:1024\n\t" \
    "s_waitcnt lgkmcnt(3)\n\t" \
    "v_mfma_f32_16x16x32_bf16 v[220:223], v[52:55], v[228:231], v[220:223]\n\t" \
    "ds_read_b128 v[224:227], v252 offset:17408\n\t" \
    "s_waitcnt lgkmcnt(3)\n\t" \
    "v_mfma_f32_16x16x32_bf16 v[208:211], v[72:75], v[232:235], v[208:211]\n\t" \
    "v_and_b32 v252, 0x3ff, %[xa]\n\t" \
    "ds_read_b128 v[228:231], v252 offset:40960\n\t" \
    "s_waitcnt lgkmcnt(3)\n\t" \
    "v_mfma_f32_16x16x32_bf16 v[220:223], v[72:75], v[236:239], v[220:223]\n\t" \
    "ds_read_b128 v[232:235], v252 offset:45056\n\t" \
    "s_waitcnt lgkmcnt(3)\n\t" \
    "v_mfma_f32_16x16x32_bf16 v[208:211], v[76:79], v[240:243], v[208:211]\n\t" \
    "ds_read_b128 v[236:239], v252 offset:41984\n\t" \
    "s_waitcnt lgkmcnt(3)\n\t" \
    "v_mfma_f32_16x16x32_bf16 v[220:223], v[76:79], v[224:227], v[220:223]\n\t" \
    "ds_read_b128 v[240:243], v252 offset:46080\n\t" \
    "s_waitcnt lgkmcnt(0)\n\t" \
    "s_barrier\n\t" ASM_ST \
    "v_mfma_f32_16x16x32_bf16 v[208:211], v[168:171], v[228:231], v[208:211]\n\t" \
    "v_xor_b32 v252, 0x2000, %[xa]\n\t" \
    "ds_read_b128 v[224:227], v252 offset:0\n\t" \
    "v_mfma_f32_16x16x32_bf16 v[220:223], v[168:171], v[232:235], v[220:223]\n\t" \
    "ds_read_b128 v[228:231], v252 offset:16384\n\t" \
    "v_mfma_f32_16x16x32_bf16 v[208:211], v[172:175], v[236:239], v[208:211]\n\t" \
    "ds_read_b128 v[232:235], v252 offset:1024\n\t" \
    "v_mfma_f32_16x16x32_bf16 v[220:223], v[172:175], v[240:243], v[220:223]\n\t" \
    "ds_read_b128 v[236:239], v252 offset:17408\n\t" \
    "s_waitcnt lgkmcnt(3)\n\t" \
    "v_mfma_f32_16x16x32_bf16 v[208:211], v[88:91], v[224:227], v[208:211]\n\t" \
    "v_xor_b32 v252, 0x2800, %[xa]\n\t" \
    "ds_read_b128 v[240:243], v252 offset:0\n\t" \
    "s_waitcnt lgkmcnt(3)\n\t" \
    "v_mfma_f32_16x16x32_bf16 v[220:223], v[88:91], v[228:231], v[220:223]\n\t" \
    "ds_read_b128 v[224:227], v252 offset:16384\n\t" \
    "s_waitcnt lgkmcnt(3)\n\t" \
    "v_mfma_f32_16x16x32_bf16 v[208:211], v[92:95], v[232:235], v[208:211]\n\t" \
    "ds_read_b128 v[228:231], v252 offset:1024\n\t" \
    "s_waitcnt lgkmcnt(3)\n\t" \
    "v_mfma_f32_16x16x32_bf16 v[220:223], v[92:95], v[236:239], v[220:223]\n\t" \
    "ds_read_b128 v[232:235], v252 offset:17408\n\t" \
    "s_waitcnt lgkmcnt(3)\n\t" \
    "v_mfma_f32_16x16x32_bf16 v[208:211], v[112:115], v[240:243], v[208:211]\n\t" \
    "v_xor_b32 v252, 0x3000, %[xa]\n\t" \
    "ds_read_b128 v[236:239], v252 offset:0\n\t" \
    "s_waitcnt lgkmcnt(3)\n\t" \
    "v_mfma_f32_16x16x32_bf16 v[220:223], v[112:115], v[224:227], v[220:223]\n\t" \
    "ds_read_b128 v[240:243], v252 offset:16384\n\t" \
    "s_waitcnt lgkmcnt(3)\n\t" \
    "v_mfma_f32_16x16x32_bf16 v[208:211], v[116:119], v[228:231], v[208:211]\n\t" \
    "ds_read_b128 v[224:227], v252 offset:1024\n\t" \
    "s_waitcnt lgkmcnt(3)\n\t" \
    "v_mfma_f32_16x16x32_bf16 v[220:223], v[116:119], v[232:235], v[220:223]\n\t" \
    "ds_read_b128 v[228:231], v252 offset:17408\n\t" \
    "s_waitcnt lgkmcnt(3)\n\t" \
    "v_mfma_f32_16x16x32_bf16 v[208:211], v[128:131], v[236:239], v[208:211]\n\t" \
    "v_xor_b32 v252, 0x3800, %[xa]\n\t" \
    "ds_read_b128 v[232:235], v252 offset:0\n\t" \
    "s_waitcnt lgkmcnt(3)\n\t" \
    "v_mfma_f32_16x16x32_bf16 v[220:223], v[128:131], v[240:243], v[220:223]\n\t" \
    "ds_read_b128 v[236:239], v252 offset:16384\n\t" \
    "s_waitcnt lgkmcnt(3)\n\t" \
    "v_mfma_f32_16x16x32_bf16 v[208:211], v[132:135], v[224:227], v[208:211]\n\t" \
    "ds_read_b128 v[240:243], v252 offset:1024\n\t" \
    "s_waitcnt lgkmcnt(3)\n\t" \
    "v_mfma_f32_16x16x32_bf16 v[220:223], v[132:135], v[228:231], v[220:223]\n\t" \
    "ds_read_b128 v[224:227], v252 offset:17408\n\t" \
    "s_waitcnt lgkmcnt(3)\n\t" \
    "v_mfma_f32_16x16x32_bf16 v[208:211], v[152:155], v[232:235], v[208:211]\n\t" \
    "v_and_b32 v252, 0x3ff, %[xa]\n\t" \
    "ds_read_b128 v[228:231], v252 offset:43008\n\t" \
    "v_mov_b64 v[244:245], 0\n\t" \
    "s_waitcnt lgkmcnt(3)\n\t" \
    "v_mfma_f32_16x16x32_bf16 v[220:223], v[152:155], v[236:239], v[220:223]\n\t" \
    "ds_read_b128 v[232:235], v252 offset:44032\n\t" \
    "v_mov_b64 v[246:247], 0\n\t" \
    "s_waitcnt lgkmcnt(3)\n\t" \
    "v_mfma_f32_16x16x32_bf16 v[208:211], v[156:159], v[240:243], v[208:211]\n\t" \
    "ds_read_b128 v[236:239], v252 offset:47104\n\t" \
    "v_mov_b64 v[248:249], 0\n\t" \
    "s_waitcnt lgkmcnt(3)\n\t" \
    "v_mfma_f32_16x16x32_bf16 v[220:223], v[156:159], v[224:227], v[220:223]\n\t" \
    "ds_read_b128 v[240:243], v252 offset:48128\n\t" \
    "v_mov_b64 v[250:251], 0\n\t" \
    "s_waitcnt lgkmcnt(3)\n\t" \
    "v_mfma_f32_16x16x32_bf16 v[208:211], v[192:195], v[228:231], v[208:211]\n\t" \
    "v_add_u32 v252, %[qs], v252\n\t" \
    "s_waitcnt lgkmcnt(2)\n\t" \
    "v_mfma_f32_16x16x32_bf16 v[208:211], v[196:199], v[232:235], v[208:211]\n\t" \
    "v_mov_b64 v[224:225], 0\n\t" \
    "s_waitcnt lgkmcnt(1)\n\t" \
    "v_mfma_f32_16x16x32_bf16 v[220:223], v[192:195], v[236:239], v[220:223]\n\t" \
    "v_mov_b64 v[226:227], 0\n\t" \
    "ds_read_b128 v[228:231], %[wl] offset:2048\n\t" \
    "s_waitcnt lgkmcnt(1)\n\t" \
    "v_mfma_f32_16x16x32_bf16 v[220:223], v[196:199], v[240:243], v[220:223]\n\t" \
    "ds_read_b128 v[232:235], %[wl] offset:3072\n\t" \
    "v_cvt_pk_bf16_f32 v208, v208, v209\n\t" \
    "v_cvt_pk_bf16_f32 v209, v210, v211\n\t" \
    "ds_write_b64 v252, v[208:209] offset:2048\n\t" \
    "ds_read_b128 v[200:203], %[wl] offset:0\n\t" \
    "ds_read_b128 v[204:207], %[wl] offset:1024\n\t" \
    "s_nop 1\n\t" \
    "v_cvt_pk_bf16_f32 v220, v220, v221\n\t" \
    "v_cvt_pk_bf16_f32 v221, v222, v223\n\t" \
    "ds_write_b64 v252, v[220:221] offset:8192\n\t" \
    "s_waitcnt lgkmcnt(0)\n\t"
#define ASM_XG_V \
    "s_waitcnt lgkmcnt(0)\n\t" \
    "ds_read_b128 v[224:227], %[xa] offset:0\n\t" \
    "ds_read_b128 v[244:247], %[wl] offset:0\n\t" \
    "ds_read_b128 v[228:231], %[xa] offset:16384\n\t" \
    "ds_read_b128 v[232:235], %[xa] offset:1024\n\t" \
    "ds_read_b128 v[236:239], %[xa] offset:17408\n\t" \
    "ds_read_b128 v[248:251], %[wl] offset:1024\n\t" \
    "s_waitcnt lgkmcnt(5)\n\t" \
    "v_mfma_f32_16x16x32_bf16 v[204:207], v[0:3], v[224:227], 0\n\t" \
    "v_xor_b32 v252, 0x800, %[xa]\n\t" \
    "ds_read_b128 v[240:243], v252 offset:0\n\t" \
    "v_mfma_f32_16x16x32_bf16 v[208:211], v[8:11], v[224:227], 0\n\t" \
    "s_waitcnt lgkmcnt(5)\n\t" \
    "v_mfma_f32_16x16x32_bf16 v[200:203], v[244:247], v[224:227], 0\n\t" \
    "s_waitcnt lgkmcnt(4)\n\t" \
    "v_mfma_f32_16x16x32_bf16 v[216:219], v[0:3], v[228:231], 0\n\t" \
    "ds_read_b128 v[224:227], v252 offset:16384\n\t" \
    "v_mfma_f32_16x16x32_bf16 v[220:223], v[8:11], v[228:231], 0\n\t" \
    "v_mfma_f32_16x16x32_bf16 v[212:215], v[244:247], v[228:231], 0\n\t" \
    "s_waitcnt lgkmcnt(4)\n\t" \
    "v_mfma_f32_16x16x32_bf16 v[204:207], v[4:7], v[232:235], v[204:207]\n\t" \
    "ds_read_b128 v[228:231], v252 offset:1024\n\t" \
    "v_mfma_f32_16x16x32_bf16 v[208:211], v[12:15], v[232:235], v[208:211]\n\t" \
    "ds_read_b128 v[244:247], %[wl] offset:2048\n\t" \
    "s_waitcnt lgkmcnt(4)\n\t" \
    "v_mfma_f32_16x16x32_bf16 v[200:203], v[248:251], v[232:235], v[200:203]\n\t" \
    "v_mfma_f32_16x16x32_bf16 v[216:219], v[4:7], v[236:239], v[216:219]\n\t" \
    "ds_read_b128 v[232:235], v252 offset:17408\n\t" \
    "v_mfma_f32_16x16x32_bf16 v[220:223], v[12:15], v[236:239], v[220:223]\n\t" \
    "v_mfma_f32_16x16x32_bf16 v[212:215], v[248:251], v[236:239], v[212:215]\n\t" \
    "s_waitcnt lgkmcnt(4)\n\t" \
    "v_mfma_f32_16x16x32_bf16 v[204:207], v[24:27], v[240:243], v[204:207]\n\t" \
    "v_xor_b32 v252, 0x1000, %[xa]\n\t" \
    "ds_read_b128 v[236:239], v252 offset:0\n\t" \
    "v_mfma_f32_16x16x32_bf16 v[208:211], v[32:35], v[240:243], v[208:211]\n\t" \
    "ds_read_b128 v[248:251], %[wl] offset:3072\n\t" \
    "v_mfma_f32_16x16x32_bf16 v[200:203], v[16:19], v[240:243], v[200:203]\n\t" \
    "s_waitcnt lgkmcnt(5)\n\t" \
    "v_mfma_f32_16x16x32_bf16 v[216:219], v[24:27], v[224:227], v[216:219]\n\t" \
    "ds_read_b128 v[240:243], v252 offset:16384\n\t" \
    "v_mfma_f32_16x16x32_bf16 v[220:223], v[32:35], v[224:227], v[220:223]\n\t" \
    "v_mfma_f32_16x16x32_bf16 v[212:215], v[16:19], v[224:227], v[212:215]\n\t" \
    "s_waitcnt lgkmcnt(5)\n\t" \
    "v_mfma_f32_16x16x32_bf16 v[204:207], v[28:31], v[228:231], v[204:207]\n\t" \
    "ds_read_b128 v[224:227], v252 offset:1024\n\t" \
    "v_mfma_f32_16x16x32_bf16 v[208:211], v[36:39], v[228:231], v[208:211]\n\t" \
    "v_mfma_f32_16x16x32_bf16 v[200:203], v[20:23], v[228:231], v[200:203]\n\t" \
    "s_waitcnt lgkmcnt(4)\n\t" \
    "v_mfma_f32_16x16x32_bf16 v[216:219], v[28:31], v[232:235], v[216:219]\n\t" \
    "ds_read_b128 v[228:231], v252 offset:17408\n\t" \
    "v_mfma_f32_16x16x32_bf16 v[220:223], v[36:39], v[232:235], v[220:223]\n\t" \
    "v_mfma_f32_16x16x32_bf16 v[212:215], v[20:23], v[232:235], v[212:215]\n\t" \
    "s_waitcnt lgkmcnt(4)\n\t" \
    "v_mfma_f32_16x16x32_bf16 v[204:207], v[40:43], v[236:239], v[204:207]\n\t" \
    "v_xor_b32 v252, 0x1800, %[xa]\n\t" \
    "ds_read_b128 v[232:235], v252 offset:0\n\t" \
    "v_mfma_f32_16x16x32_bf16 v[208:211], v[48:51], v[236:239], v[208:211]\n\t" \
    "v_mfma_f32_16x16x32_bf16 v[200:203], v[244:247], v[236:239], v[200:203]\n\t" \
    "s_waitcnt lgkmcnt(3)\n\t" \
    "v_mfma_f32_16x16x32_bf16 v[216:219], v[40:43], v[240:243], v[216:219]\n\t" \
    "ds_read_b128 v[236:239], v252 offset:16384\n\t" \
    "v_mfma_f32_16x16x32_bf16 v[220:223], v[48:51], v[240:243], v[220:223]\n\t" \
    "v_mfma_f32_16x16x32_bf16 v[212:215], v[244:247], v[240:243], v[212:215]\n\t" \
    "s_waitcnt lgkmcnt(3)\n\t" \
    "v_mfma_f32_16x16x32_bf16 v[204:207], v[44:47], v[224:227], v[204:207]\n\t" \
    "ds_read_b128 v[240:243], v252 offset:1024\n\t" \
    "v_mfma_f32_16x16x32_bf16 v[208:211], v[52:55], v[224:227], v[208:211]\n\t" \
    "ds_read_b128 v[244:247], %[wl] offset:8192\n\t" \
    "v_mfma_f32_16x16x32_bf16 v[200:203], v[248:251], v[224:227], v[200:203]\n\t" \
    "s_waitcnt lgkmcnt(4)\n\t" \
    "v_mfma_f32_16x16x32_bf16 v[216:219], v[44:47], v[228:231], v[216:219]\n\t" \
    "ds_read_b128 v[224:227], v252 offset:17408\n\t" \
    "v_mfma_f32_16x16x32_bf16 v[220:223], v[52:55], v[228:231], v[220:223]\n\t" \
    "v_mfma_f32_16x16x32_bf16 v[212:215], v[248:251], v[228:231], v[212:215]\n\t" \
    "s_waitcnt lgkmcnt(4)\n\t" \
    "v_mfma_f32_16x16x32_bf16 v[204:207], v[64:67], v[232:235], v[204:207]\n\t" \
    "v_and_b32 v252, 0x3ff, %[xa]\n\t" \
    "ds_read_b128 v[228:231], v252 offset:40960\n\t" \
    "v_mfma_f32_16x16x32_bf16 v[208:211], v[72:75], v[232:235], v[208:211]\n\t" \
    "ds_read_b128 v[248:251], %[wl] offset:9216\n\t" \
    "v_mfma_f32_16x16x32_bf16 v[200:203], v[56:59], v[232:235], v[200:203]\n\t" \
    "s_waitcnt lgkmcnt(5)\n\t" \
    "v_mfma_f32_16x16x32_bf16 v[216:219], v[64:67], v[236:239], v[216:219]\n\t" \
    "ds_read_b128 v[232:235], v252 offset:45056\n\t" \
    "v_mfma_f32_16x16x32_bf16 v[220:223], v[72:75], v[236:239], v[220:223]\n\t" \
    "v_mfma_f32_16x16x32_bf16 v[212:215], v[56:59], v[236:239], v[212:215]\n\t" \
    "s_waitcnt lgkmcnt(5)\n\t" \
    "v_mfma_f32_16x16x32_bf16 v[204:207], v[68:71], v[240:243], v[204:207]\n\t" \
    "ds_read_b128 v[236:239], v252 offset:41984\n\t" \
    "v_mfma_f32_16x16x32_bf16 v[208:211], v[76:79], v[240:243], v[208:211]\n\t" \
    "v_mfma_f32_16x16x32_bf16 v[200:203], v[60:63], v[240:243], v[200:203]\n\t" \
    "s_waitcnt lgkmcnt(4)\n\t" \
    "v_mfma_f32_16x16x32_bf16 v[216:219], v[68:71], v[224:227], v[216:219]\n\t" \
    "ds_read_b128 v[240:243], v252 offset:46080\n\t" \
    "v_mfma_f32_16x16x32_bf16 v[220:223], v[76:79], v[224:227], v[220:223]\n\t" \
    "v_mfma_f32_16x16x32_bf16 v[212:215], v[60:63], v[224:227], v[212:215]\n\t" \
    "s_waitcnt lgkmcnt(4)\n\t" \
    "v_mfma_f32_16x16x32_bf16 v[204:207], v[160:163], v[228:231], v[204:207]\n\t" \
    "v_xor_b32 v252, 0x2000, %[xa]\n\t" \
    "ds_read_b128 v[224:227], v252 offset:0\n\t" \
    "v_mfma_f32_16x16x32_bf16 v[208:211], v[168:171], v[228:231], v[208:211]\n\t" \
    "v_mfma_f32_16x16x32_bf16 v[200:203], v[244:247], v[228:231], v[200:203]\n\t" \
    "s_waitcnt lgkmcnt(3)\n\t" \
    "v_mfma_f32_16x16x32_bf16 v[216:219], v[160:163], v[232:235], v[216:219]\n\t" \
    "ds_read_b128 v[228:231], v252 offset:16384\n\t" \
    "v_mfma_f32_16x16x32_bf16 v[220:223], v[168:171], v[232:235], v[220:223]\n\t" \
    "v_mfma_f32_16x16x32_bf16 v[212:215], v[244:247], v[232:235], v[212:215]\n\t" \
    "s_waitcnt lgkmcnt(3)\n\t" \
    "v_mfma_f32_16x16x32_bf16 v[204:207], v[164:167], v[236:239], v[204:207]\n\t" \
    "ds_read_b128 v[232:235], v252 offset:1024\n\t" \
    "v_mfma_f32_16x16x32_bf16 v[208:211], v[172:175], v[236:239], v[208:211]\n\t" \
    "ds_read_b128 v[244:247], %[wl] offset:4096\n\t" \
    "v_mfma_f32_16x16x32_bf16 v[200:203], v[248:251], v[236:239], v[200:203]\n\t" \
    "s_waitcnt lgkmcnt(4)\n\t" \
    "v_mfma_f32_16x16x32_bf16 v[216:219], v[164:167], v[240:243], v[216:219]\n\t" \
    "ds_read_b128 v[236:239], v252 offset:17408\n\t" \
    "v_mfma_f32_16x16x32_bf16 v[220:223], v[172:175], v[240:243], v[220:223]\n\t" \
    "v_mfma_f32_16x16x32_bf16 v[212:215], v[248:251], v[240:243], v[212:215]\n\t" \
    "s_waitcnt lgkmcnt(4)\n\t" \
    "v_mfma_f32_16x16x32_bf16 v[204:207], v[80:83], v[224:227], v[204:207]\n\t" \
    "v_xor_b32 v252, 0x2800, %[xa]\n\t" \
    "ds_read_b128 v[240:243], v252 offset:0\n\t" \
    "v_mfma_f32_16x16x32_bf16 v[208:211], v[88:91], v[224:227], v[208:211]\n\t" \
    "ds_read_b128 v[248:251], %[wl] offset:5120\n\t" \
    "s_waitcnt lgkmcnt(3)\n\t" \
    "v_mfma_f32_16x16x32_bf16 v[200:203], v[244:247], v[224:227], v[200:203]\n\t" \
    "v_mfma_f32_16x16x32_bf16 v[216:219], v[80:83], v[228:231], v[216:219]\n\t" \
    "ds_read_b128 v[224:227], v252 offset:16384\n\t" \
    "v_mfma_f32_16x16x32_bf16 v[220:223], v[88:91], v[228:231], v[220:223]\n\t" \
    "v_mfma_f32_16x16x32_bf16 v[212:215], v[244:247], v[228:231], v[212:215]\n\t" \
    "v_mfma_f32_16x16x32_bf16 v[204:207], v[84:87], v[232:235], v[204:207]\n\t" \
    "ds_read_b128 v[228:231], v252 offset:1024\n\t" \
    "v_mfma_f32_16x16x32_bf16 v[208:211], v[92:95], v[232:235], v[208:211]\n\t" \
    "ds_read_b128 v[244:247], %[wl] offset:6144\n\t" \
    "s_waitcnt lgkmcnt(3)\n\t" \
    "v_mfma_f32_16x16x32_bf16 v[200:203], v[248:251], v[232:235], v[200:203]\n\t" \
    "v_mfma_f32_16x16x32_bf16 v[216:219], v[84:87], v[236:239], v[216:219]\n\t" \
    "ds_read_b128 v[232:235], v252 offset:17408\n\t" \
    "v_mfma_f32_16x16x32_bf16 v[220:223], v[92:95], v[236:239], v[220:223]\n\t" \
    "v_mfma_f32_16x16x32_bf16 v[212:215], v[248:251], v[236:239], v[212:215]\n\t" \
    "v_mfma_f32_16x16x32_bf16 v[204:207], v[104:107], v[240:243], v[204:207]\n\t" \
    "v_xor_b32 v252, 0x3000, %[xa]\n\t" \
    "ds_read_b128 v[236:239], v252 offset:0\n\t" \
    "v_mfma_f32_16x16x32_bf16 v[208:211], v[112:115], v[240:243], v[208:211]\n\t" \
    "ds_read_b128 v[248:251], %[wl] offset:7168\n\t" \
    "v_mfma_f32_16x16x32_bf16 v[200:203], v[96:99], v[240:243], v[200:203]\n\t" \
    "s_waitcnt lgkmcnt(5)\n\t" \
    "v_mfma_f32_16x16x32_bf16 v[216:219], v[104:107], v[224:227], v[216:219]\n\t" \
    "ds_read_b128 v[240:243], v252 offset:16384\n\t" \
    "v_mfma_f32_16x16x32_bf16 v[220:223], v[112:115], v[224:227], v[220:223]\n\t" \
    "v_mfma_f32_16x16x32_bf16 v[212:215], v[96:99], v[224:227], v[212:215]\n\t" \
    "s_waitcnt lgkmcnt(5)\n\t" \
    "v_mfma_f32_16x16x32_bf16 v[204:207], v[108:111], v[228:231], v[204:207]\n\t" \
    "ds_read_b128 v[224:227], v252 offset:1024\n\t" \
    "v_mfma_f32_16x16x32_bf16 v[208:211], v[116:119], v[228:231], v[208:211]\n\t" \
    "v_mfma_f32_16x16x32_bf16 v[200:203], v[100:103], v[228:231], v[200:203]\n\t" \
    "s_waitcnt lgkmcnt(4)\n\t" \
    "v_mfma_f32_16x16x32_bf16 v[216:219], v[108:111], v[232:235], v[216:219]\n\t" \
    "ds_read_b128 v[228:231], v252 offset:17408\n\t" \
    "v_mfma_f32_16x16x32_bf16 v[220:223], v[116:119], v[232:235], v[220:223]\n\t" \
    "v_mfma_f32_16x16x32_bf16 v[212:215], v[100:103], v[232:235], v[212:215]\n\t" \
    "s_waitcnt lgkmcnt(4)\n\t" \
    "v_mfma_f32_16x16x32_bf16 v[204:207], v[120:123], v[236:239], v[204:207]\n\t" \
    "v_xor_b32 v252, 0x3800, %[xa]\n\t" \
    "ds_read_b128 v[232:235], v252 offset:0\n\t" \
    "v_mfma_f32_16x16x32_bf16 v[208:211], v[128:131], v[236:239], v[208:211]\n\t" \
    "v_mfma_f32_16x16x32_bf16 v[200:203], v[244:247], v[236:239], v[200:203]\n\t" \
    "s_waitcnt lgkmcnt(3)\n\t" \
    "v_mfma_f32_16x16x32_bf16 v[216:219], v[120:123], v[240:243], v[216:219]\n\t" \
    "ds_read_b128 v[236:239], v252 offset:16384\n\t" \
    "v_mfma_f32_16x16x32_bf16 v[220:223], v[128:131], v[240:243], v[220:223]\n\t" \
    "v_mfma_f32_16x16x32_bf16 v[212:215], v[244:247], v[240:243], v[212:215]\n\t" \
    "s_waitcnt lgkmcnt(3)\n\t" \
    "v_mfma_f32_16x16x32_bf16 v[204:207], v[124:127], v[224:227], v[204:207]\n\t" \
    "ds_read_b128 v[240:243], v252 offset:1024\n\t" \
    "v_mfma_f32_16x16x32_bf16 v[208:211], v[132:135], v[224:227], v[208:211]\n\t" \
    "v_mfma_f32_16x16x32_bf16 v[200:203], v[248:251], v[224:227], v[200:203]\n\t" \
    "s_waitcnt lgkmcnt(3)\n\t" \
    "v_mfma_f32_16x16x32_bf16 v[216:219], v[124:127], v[228:231], v[216:219]\n\t" \
    "ds_read_b128 v[224:227], v252 offset:17408\n\t" \
    "v_mfma_f32_16x16x32_bf16 v[220:223], v[132:135], v[228:231], v[220:223]\n\t" \
    "v_mfma_f32_16x16x32_bf16 v[212:215], v[248:251], v[228:231], v[212:215]\n\t" \
    "s_waitcnt lgkmcnt(3)\n\t" \
    "v_mfma_f32_16x16x32_bf16 v[204:207], v[144:147], v[232:235], v[204:207]\n\t" \
    "v_and_b32 v252, 0x3ff, %[xa]\n\t" \
    "ds_read_b128 v[228:231], v252 offset:43008\n\t" \
    "v_mov_b64 v[244:245], 0\n\t" \
    "v_mfma_f32_16x16x32_bf16 v[208:211], v[152:155], v[232:235], v[208:211]\n\t" \
    "v_mov_b64 v[246:247], 0\n\t" \
    "v_mfma_f32_16x16x32_bf16 v[200:203], v[136:139], v[232:235], v[200:203]\n\t" \
    "v_mov_b64 v[248:249], 0\n\t" \
    "s_waitcnt lgkmcnt(3)\n\t" \
    "v_mfma_f32_16x16x32_bf16 v[216:219], v[144:147], v[236:239], v[216:219]\n\t" \
    "ds_read_b128 v[232:235], v252 offset:44032\n\t" \
    "v_mov_b64 v[250:251], 0\n\t" \
    "v_mfma_f32_16x16x32_bf16 v[220:223], v[152:155], v[236:239], v[220:223]\n\t" \
    "v_mfma_f32_16x16x32_bf16 v[212:215], v[136:139], v[236:239], v[212:215]\n\t" \
    "s_waitcnt lgkmcnt(3)\n\t" \
    "v_mfma_f32_16x16x32_bf16 v[204:207], v[148:151], v[240:243], v[204:207]\n\t" \
    "ds_read_b128 v[236:239], v252 offset:47104\n\t" \
    "v_mfma_f32_16x16x32_bf16 v[208:211], v[156:159], v[240:243], v[208:211]\n\t" \
    "v_mfma_f32_16x16x32_bf16 v[200:203], v[140:143], v[240:243], v[200:203]\n\t" \
    "s_waitcnt lgkmcnt(3)\n\t" \
    "v_mfma_f32_16x16x32_bf16 v[216:219], v[148:151], v[224:227], v[216:219]\n\t" \
    "ds_read_b128 v[240:243], v252 offset:48128\n\t" \
    "v_mfma_f32_16x16x32_bf16 v[220:223], v[156:159], v[224:227], v[220:223]\n\t" \
    "v_mfma_f32_16x16x32_bf16 v[212:215], v[140:143], v[224:227], v[212:215]\n\t" \
    "s_waitcnt lgkmcnt(3)\n\t" \
    "v_mfma_f32_16x16x32_bf16 v[204:207], v[184:187], v[228:231], v[204:207]\n\t" \
    "v_add_u32 v252, %[qs], v252\n\t" \
    "v_mfma_f32_16x16x32_bf16 v[208:211], v[192:195], v[228:231], v[208:211]\n\t" \
    "v_mov_b64 v[224:225], 0\n\t" \
    "v_mfma_f32_16x16x32_bf16 v[200:203], v[176:179], v[228:231], v[200:203]\n\t" \
    "v_mov_b64 v[226:227], 0\n\t" \
    "s_waitcnt lgkmcnt(2)\n\t" \
    "v_mfma_f32_16x16x32_bf16 v[204:207], v[188:191], v[232:235], v[204:207]\n\t" \
    "v_mfma_f32_16x16x32_bf16 v[208:211], v[196:199], v[232:235], v[208:211]\n\t" \
    "v_mfma_f32_16x16x32_bf16 v[200:203], v[180:183], v[232:235], v[200:203]\n\t" \
    "s_waitcnt lgkmcnt(1)\n\t" \
    "v_mfma_f32_16x16x32_bf16 v[216:219], v[184:187], v[236:239], v[216:219]\n\t" \
    "ds_read_b128 v[228:231], %[wl] offset:2048\n\t" \
    "ds_read_b128 v[232:235], %[wl] offset:3072\n\t" \
    "v_mfma_f32_16x16x32_bf16 v[220:223], v[192:195], v[236:239], v[220:223]\n\t" \
    "v_cvt_pk_bf16_f32 v204, v204, v205\n\t" \
    "v_cvt_pk_bf16_f32 v205, v206, v207\n\t" \
    "v_mfma_f32_16x16x32_bf16 v[212:215], v[176:179], v[236:239], v[212:215]\n\t" \
    "ds_write_b64 v252, v[204:205] offset:1024\n\t" \
    "v_cvt_pk_bf16_f32 v208, v208, v209\n\t" \
    "s_waitcnt lgkmcnt(3)\n\t" \
    "v_mfma_f32_16x16x32_bf16 v[216:219], v[188:191], v[240:243], v[216:219]\n\t" \
    "v_cvt_pk_bf16_f32 v209, v210, v211\n\t" \
    "ds_write_b64 v252, v[208:209] offset:2048\n\t" \
    "v_mfma_f32_16x16x32_bf16 v[220:223], v[196:199], v[240:243], v[220:223]\n\t" \
    "v_cvt_pk_bf16_f32 v200, v200, v201\n\t" \
    "v_cvt_pk_bf16_f32 v201, v202, v203\n\t" \
    "v_mfma_f32_16x16x32_bf16 v[212:215], v[180:183], v[240:243], v[212:215]\n\t" \
    "ds_write_b64 v252, v[200:201] offset:0\n\t" \
    "ds_read_b128 v[200:203], %[wl] offset:0\n\t" \
    "ds_read_b128 v[204:207], %[wl] offset:1024\n\t" \
    "v_cvt_pk_bf16_f32 v216, v216, v217\n\t" \
    "v_cvt_pk_bf16_f32 v217, v218, v219\n\t" \
    "ds_write_b64 v252, v[216:217] offset:7168\n\t" \
    "v_cvt_pk_bf16_f32 v220, v220, v221\n\t" \
    "v_cvt_pk_bf16_f32 v221, v222, v223\n\t" \
    "ds_write_b64 v252, v[220:221] offset:8192\n\t" \
    "v_cvt_pk_bf16_f32 v212, v212, v213\n\t" \
    "v_cvt_pk_bf16_f32 v213, v214, v215\n\t" \
    "ds_write_b64 v252, v[212:213] offset:6144\n\t" \
    "s_waitcnt lgkmcnt(0)\n\t"
#define ASM_XG_H_HI \
    "s_waitcnt lgkmcnt(0)\n\t" \
    "v_xor_b32 v252, 0x2000, %[xa]\n\t" \
    "ds_read_b128 v[224:227], v252 offset:0\n\t" \
    "ds_read_b128 v[244:247], %[wl] offset:4096\n\t" \
    "ds_read_b128 v[228:231], v252 offset:16384\n\t" \
    "ds_read_b128 v[232:235], v252 offset:1024\n\t" \
    "ds_read_b128 v[236:239], v252 offset:17408\n\t" \
    "ds_read_b128 v[248:251], %[wl] offset:5120\n\t" \
    "s_waitcnt lgkmcnt(5)\n\t" \
    "v_mfma_f32_16x16x32_bf16 v[204:207], v[80:83], v[224:227], 0\n\t" \
    "v_xor_b32 v252, 0x2800, %[xa]\n\t" \
    "ds_read_b128 v[240:243], v252 offset:0\n\t" \
    "v_mfma_f32_16x16x32_bf16 v[208:211], v[88:91], v[224:227], 0\n\t" \
    "s_waitcnt lgkmcnt(5)\n\t" \
    "v_mfma_f32_16x16x32_bf16 v[200:203], v[244:247], v[224:227], 0\n\t" \
    "s_waitcnt lgkmcnt(4)\n\t" \
    "v_mfma_f32_16x16x32_bf16 v[216:219], v[80:83], v[228:231], 0\n\t" \
    "ds_read_b128 v[224:227], v252 offset:16384\n\t" \
    "v_mfma_f32_16x16x32_bf16 v[220:223], v[88:91], v[228:231], 0\n\t" \
    "v_mfma_f32_16x16x32_bf16 v[212:215], v[244:247], v[228:231], 0\n\t" \
    "s_waitcnt lgkmcnt(4)\n\t" \
    "v_mfma_f32_16x16x32_bf16 v[204:207], v[84:87], v[232:235], v[204:207]\n\t" \
    "ds_read_b128 v[228:231], v252 offset:1024\n\t" \
    "v_mfma_f32_16x16x32_bf16 v[208:211], v[92:95], v[232:235], v[208:211]\n\t" \
    "ds_read_b128 v[244:247], %[wl] offset:6144\n\t" \
    "s_waitcnt lgkmcnt(4)\n\t" \
    "v_mfma_f32_16x16x32_bf16 v[200:203], v[248:251], v[232:235], v[200:203]\n\t" \
    "v_mfma_f32_16x16x32_bf16 v[216:219], v[84:87], v[236:239], v[216:219]\n\t" \
    "ds_read_b128 v[232:235], v252 offset:17408\n\t" \
    "v_mfma_f32_16x16x32_bf16 v[220:223], v[92:95], v[236:239], v[220:223]\n\t" \
    "v_mfma_f32_16x16x32_bf16 v[212:215], v[248:251], v[236:239], v[212:215]\n\t" \
    "s_waitcnt lgkmcnt(4)\n\t" \
    "v_mfma_f32_16x16x32_bf16 v[204:207], v[104:107], v[240:243], v[204:207]\n\t" \
    "v_xor_b32 v252, 0x3000, %[xa]\n\t" \
    "ds_read_b128 v[236:239], v252 offset:0\n\t" \
    "v_mfma_f32_16x16x32_bf16 v[208:211], v[112:115], v[240:243], v[208:211]\n\t" \
    "ds_read_b128 v[248:251], %[wl] offset:7168\n\t" \
    "v_mfma_f32_16x16x32_bf16 v[200:203], v[96:99], v[240:243], v[200:203]\n\t" \
    "s_waitcnt lgkmcnt(5)\n\t" \
    "v_mfma_f32_16x16x32_bf16 v[216:219], v[104:107], v[224:227], v[216:219]\n\t" \
    "ds_read_b128 v[240:243], v252 offset:16384\n\t" \
    "v_mfma_f32_16x16x32_bf16 v[220:223], v[112:115], v[224:227], v[220:223]\n\t" \
    "v_mfma_f32_16x16x32_bf16 v[212:215], v[96:99], v[224:227], v[212:215]\n\t" \
    "s_nop 3\n\t" \
    "v_mov_b32 v224, %[tv]\n\t" \
    "s_waitcnt vmcnt(5)\n\t" \
    "buffer_store_dword v224, %[fv], %[rr], %[fp] offen sc1\n\t" \
    "s_waitcnt lgkmcnt(5)\n\t" \
    "v_mfma_f32_16x16x32_bf16 v[204:207], v[108:111], v[228:231], v[204:207]\n\t" \
    "ds_read_b128 v[224:227], v252 offset:1024\n\t" \
    "v_mfma_f32_16x16x32_bf16 v[208:211], v[116:119], v[228:231], v[208:211]\n\t" \
    "v_mfma_f32_16x16x32_bf16 v[200:203], v[100:103], v[228:231], v[200:203]\n\t" \
    "s_waitcnt lgkmcnt(4)\n\t" \
    "v_mfma_f32_16x16x32_bf16 v[216:219], v[108:111], v[232:235], v[216:219]\n\t" \
    "ds_read_b128 v[228:231], v252 offset:17408\n\t" \
    "v_mfma_f32_16x16x32_bf16 v[220:223], v[116:119], v[232:235], v[220:223]\n\t" \
    "v_mfma_f32_16x16x32_bf16 v[212:215], v[100:103], v[232:235], v[212:215]\n\t" \
    "s_waitcnt lgkmcnt(4)\n\t" \
    "v_mfma_f32_16x16x32_bf16 v[204:207], v[120:123], v[236:239], v[204:207]\n\t" \
    "v_xor_b32 v252, 0x3800, %[xa]\n\t" \
    "ds_read_b128 v[232:235], v252 offset:0\n\t" \
    "v_mfma_f32_16x16x32_bf16 v[208:211], v[128:131], v[236:239], v[208:211]\n\t" \
    "v_mfma_f32_16x16x32_bf16 v[200:203], v[244:247], v[236:239], v[200:203]\n\t" \
    "s_waitcnt lgkmcnt(3)\n\t" \
    "v_mfma_f32_16x16x32_bf16 v[216:219], v[120:123], v[240:243], v[216:219]\n\t" \
    "ds_read_b128 v[236:239], v252 offset:16384\n\t" \
    "v_mfma_f32_16x16x32_bf16 v[220:223], v[128:131], v[240:243], v[220:223]\n\t" \
    "v_mfma_f32_16x16x32_bf16 v[212:215], v[244:247], v[240:243], v[212:215]\n\t" \
    "s_waitcnt lgkmcnt(3)\n\t" \
    "v_mfma_f32_16x16x32_bf16 v[204:207], v[124:127], v[224:227], v[204:207]\n\t" \
    "ds_read_b128 v[240:243], v252 offset:1024\n\t" \
    "v_mfma_f32_16x16x32_bf16 v[208:211], v[132:135], v[224:227], v[208:211]\n\t" \
    "ds_read_b128 v[244:247], %[wl] offset:8192\n\t" \
    "v_mfma_f32_16x16x32_bf16 v[200:203], v[248:251], v[224:227], v[200:203]\n\t" \
    "s_waitcnt lgkmcnt(4)\n\t" \
    "v_mfma_f32_16x16x32_bf16 v[216:219], v[124:127], v[228:231], v[216:219]\n\t" \
    "ds_read_b128 v[224:227], v252 offset:17408\n\t" \
    "v_mfma_f32_16x16x32_bf16 v[220:223], v[132:135], v[228:231], v[220:223]\n\t" \
    "v_mfma_f32_16x16x32_bf16 v[212:215], v[248:251], v[228:231], v[212:215]\n\t" \
    "s_waitcnt lgkmcnt(4)\n\t" \
    "v_mfma_f32_16x16x32_bf16 v[204:207], v[144:147], v[232:235], v[204:207]\n\t" \
    "v_and_b32 v252, 0x3ff, %[xa]\n\t" \
    "ds_read_b128 v[228:231], v252 offset:40960\n\t" \
    "v_mfma_f32_16x16x32_bf16 v[208:211], v[152:155], v[232:235], v[208:211]\n\t" \
    "ds_read_b128 v[248:251], %[wl] offset:9216\n\t" \
    "v_mfma_f32_16x16x32_bf16 v[200:203], v[136:139], v[232:235], v[200:203]\n\t" \
    "s_waitcnt lgkmcnt(5)\n\t" \
    "v_mfma_f32_16x16x32_bf16 v[216:219], v[144:147], v[236:239], v[216:219]\n\t" \
    "ds_read_b128 v[232:235], v252 offset:45056\n\t" \
    "v_mfma_f32_16x16x32_bf16 v[220:223], v[152:155], v[236:239], v[220:223]\n\t" \
    "v_mfma_f32_16x16x32_bf16 v[212:215], v[136:139], v[236:239], v[212:215]\n\t" \
    "s_waitcnt lgkmcnt(5)\n\t" \
    "v_mfma_f32_16x16x32_bf16 v[204:207], v[148:151], v[240:243], v[204:207]\n\t" \
    "ds_read_b128 v[236:239], v252 offset:41984\n\t" \
    "v_mfma_f32_16x16x32_bf16 v[208:211], v[156:159], v[240:243], v[208:211]\n\t" \
    "v_mfma_f32_16x16x32_bf16 v[200:203], v[140:143], v[240:243], v[200:203]\n\t" \
    "s_waitcnt lgkmcnt(4)\n\t" \
    "v_mfma_f32_16x16x32_bf16 v[216:219], v[148:151], v[224:227], v[216:219]\n\t" \
    "ds_read_b128 v[240:243], v252 offset:46080\n\t" \
    "v_mfma_f32_16x16x32_bf16 v[220:223], v[156:159], v[224:227], v[220:223]\n\t" \
    "v_mfma_f32_16x16x32_bf16 v[212:215], v[140:143], v[224:227], v[212:215]\n\t" \
    "s_waitcnt vmcnt(1) lgkmcnt(0)\n\t" \
    "s_barrier\n\t" ASM_ST \
    "v_mfma_f32_16x16x32_bf16 v[204:207], v[160:163], v[228:231], v[204:207]\n\t" \
    "ds_read_b128 v[224:227], %[xa] offset:0\n\t" \
    "v_mfma_f32_16x16x32_bf16 v[208:211], v[168:171], v[228:231], v[208:211]\n\t" \
    "v_mfma_f32_16x16x32_bf16 v[200:203], v[244:247], v[228:231], v[200:203]\n\t" \
    "v_mfma_f32_16x16x32_bf16 v[216:219], v[160:163], v[232:235], v[216:219]\n\t" \
    "ds_read_b128 v[228:231], %[xa] offset:16384\n\t" \
    "v_mfma_f32_16x16x32_bf16 v[220:223], v[168:171], v[232:235], v[220:223]\n\t" \
    "v_mfma_f32_16x16x32_bf16 v[212:215], v[244:247], v[232:235], v[212:215]\n\t" \
    "v_mfma_f32_16x16x32_bf16 v[204:207], v[164:167], v[236:239], v[204:207]\n\t" \
    "ds_read_b128 v[232:235], %[xa] offset:1024\n\t" \
    "v_mfma_f32_16x16x32_bf16 v[208:211], v[172:175], v[236:239], v[208:211]\n\t" \
    "ds_read_b128 v[244:247], %[wl] offset:0\n\t" \
    "v_mfma_f32_16x16x32_bf16 v[200:203], v[248:251], v[236:239], v[200:203]\n\t" \
    "v_mfma_f32_16x16x32_bf16 v[216:219], v[164:167], v[240:243], v[216:219]\n\t" \
    "ds_read_b128 v[236:239], %[xa] offset:17408\n\t" \
    "v_mfma_f32_16x16x32_bf16 v[220:223], v[172:175], v[240:243], v[220:223]\n\t" \
    "v_mfma_f32_16x16x32_bf16 v[212:215], v[248:251], v[240:243], v[212:215]\n\t" \
    "s_waitcnt lgkmcnt(4)\n\t" \
    "v_mfma_f32_16x16x32_bf16 v[204:207], v[0:3], v[224:227], v[204:207]\n\t" \
    "v_xor_b32 v252, 0x800, %[xa]\n\t" \
    "ds_read_b128 v[240:243], v252 offset:0\n\t" \
    "v_mfma_f32_16x16x32_bf16 v[208:211], v[8:11], v[224:227], v[208:211]\n\t" \
    "ds_read_b128 v[248:251], %[wl] offset:1024\n\t" \
    "s_waitcnt lgkmcnt(3)\n\t" \
    "v_mfma_f32_16x16x32_bf16 v[200:203], v[244:247], v[224:227], v[200:203]\n\t" \
    "v_mfma_f32_16x16x32_bf16 v[216:219], v[0:3], v[228:231], v[216:219]\n\t" \
    "ds_read_b128 v[224:227], v252 offset:16384\n\t" \
    "v_mfma_f32_16x16x32_bf16 v[220:223], v[8:11], v[228:231], v[220:223]\n\t" \
    "v_mfma_f32_16x16x32_bf16 v[212:215], v[244:247], v[228:231], v[212:215]\n\t" \
    "v_mfma_f32_16x16x32_bf16 v[204:207], v[4:7], v[232:235], v[204:207]\n\t" \
    "ds_read_b128 v[228:231], v252 offset:1024\n\t" \
    "v_mfma_f32_16x16x32_bf16 v[208:211], v[12:15], v[232:235], v[208:211]\n\t" \
    "ds_read_b128 v[244:247], %[wl] offset:2048\n\t" \
    "s_waitcnt lgkmcnt(3)\n\t" \
    "v_mfma_f32_16x16x32_bf16 v[200:203], v[248:251], v[232:235], v[200:203]\n\t" \
    "v_mfma_f32_16x16x32_bf16 v[216:219], v[4:7], v[236:239], v[216:219]\n\t" \
    "ds_read_b128 v[232:235], v252 offset:17408\n\t" \
    "v_mfma_f32_16x16x32_bf16 v[220:223], v[12:15], v[236:239], v[220:223]\n\t" \
    "v_mfma_f32_16x16x32_bf16 v[212:215], v[248:251], v[236:239], v[212:215]\n\t" \
    "v_mfma_f32_16x16x32_bf16 v[204:207], v[24:27], v[240:243], v[204:207]\n\t" \
    "v_xor_b32 v252, 0x1000, %[xa]\n\t" \
    "ds_read_b128 v[236:239], v252 offset:0\n\t" \
    "v_mfma_f32_16x16x32_bf16 v[208:211], v[32:35], v[240:243], v[208:211]\n\t" \
    "ds_read_b128 v[248:251], %[wl] offset:3072\n\t" \
    "v_mfma_f32_16x16x32_bf16 v[200:203], v[16:19], v[240:243], v[200:203]\n\t" \
    "s_waitcnt lgkmcnt(5)\n\t" \
    "v_mfma_f32_16x16x32_bf16 v[216:219], v[24:27], v[224:227], v[216:219]\n\t" \
    "ds_read_b128 v[240:243], v252 offset:16384\n\t" \
    "v_mfma_f32_16x16x32_bf16 v[220:223], v[32:35], v[224:227], v[220:223]\n\t" \
    "v_mfma_f32_16x16x32_bf16 v[212:215], v[16:19], v[224:227], v[212:215]\n\t" \
    "s_waitcnt lgkmcnt(5)\n\t" \
    "v_mfma_f32_16x16x32_bf16 v[204:207], v[28:31], v[228:231], v[204:207]\n\t" \
    "ds_read_b128 v[224:227], v252 offset:1024\n\t" \
    "v_mfma_f32_16x16x32_bf16 v[208:211], v[36:39], v[228:231], v[208:211]\n\t" \
    "v_mfma_f32_16x16x32_bf16 v[200:203], v[20:23], v[228:231], v[200:203]\n\t" \
    "s_waitcnt lgkmcnt(4)\n\t" \
    "v_mfma_f32_16x16x32_bf16 v[216:219], v[28:31], v[232:235], v[216:219]\n\t" \
    "ds_read_b128 v[228:231], v252 offset:17408\n\t" \
    "v_mfma_f32_16x16x32_bf16 v[220:223], v[36:39], v[232:235], v[220:223]\n\t" \
    "v_mfma_f32_16x16x32_bf16 v[212:215], v[20:23], v[232:235], v[212:215]\n\t" \
    "s_waitcnt lgkmcnt(4)\n\t" \
    "v_mfma_f32_16x16x32_bf16 v[204:207], v[40:43], v[236:239], v[204:207]\n\t" \
    "v_xor_b32 v252, 0x1800, %[xa]\n\t" \
    "ds_read_b128 v[232:235], v252 offset:0\n\t" \
    "v_mfma_f32_16x16x32_bf16 v[208:211], v[48:51], v[236:239], v[208:211]\n\t" \
    "v_mfma_f32_16x16x32_bf16 v[200:203], v[244:247], v[236:239], v[200:203]\n\t" \
    "s_waitcnt lgkmcnt(3)\n\t" \
    "v_mfma_f32_16x16x32_bf16 v[216:219], v[40:43], v[240:243], v[216:219]\n\t" \
    "ds_read_b128 v[236:239], v252 offset:16384\n\t" \
    "v_mfma_f32_16x16x32_bf16 v[220:223], v[48:51], v[240:243], v[220:223]\n\t" \
    "v_mfma_f32_16x16x32_bf16 v[212:215], v[244:247], v[240:243], v[212:215]\n\t" \
    "s_waitcnt lgkmcnt(3)\n\t" \
    "v_mfma_f32_16x16x32_bf16 v[204:207], v[44:47], v[224:227], v[204:207]\n\t" \
    "ds_read_b128 v[240:243], v252 offset:1024\n\t" \
    "v_mfma_f32_16x16x32_bf16 v[208:211], v[52:55], v[224:227], v[208:211]\n\t" \
    "v_mfma_f32_16x16x32_bf16 v[200:203], v[248:251], v[224:227], v[200:203]\n\t" \
    "s_waitcnt lgkmcnt(3)\n\t" \
    "v_mfma_f32_16x16x32_bf16 v[216:219], v[44:47], v[228:231], v[216:219]\n\t" \
    "ds_read_b128 v[224:227], v252 offset:17408\n\t" \
    "v_mfma_f32_16x16x32_bf16 v[220:223], v[52:55], v[228:231], v[220:223]\n\t" \
    "v_mfma_f32_16x16x32_bf16 v[212:215], v[248:251], v[228:231], v[212:215]\n\t" \
    "s_waitcnt lgkmcnt(3)\n\t" \
    "v_mfma_f32_16x16x32_bf16 v[204:207], v[64:67], v[232:235], v[204:207]\n\t" \
    "v_and_b32 v252, 0x3ff, %[xa]\n\t" \
    "ds_read_b128 v[228:231], v252 offset:43008\n\t" \
    "v_mov_b64 v[244:245], 0\n\t" \
    "v_mfma_f32_16x16x32_bf16 v[208:211], v[72:75], v[232:235], v[208:211]\n\t" \
    "v_mov_b64 v[246:247], 0\n\t" \
    "v_mfma_f32_16x16x32_bf16 v[200:203], v[56:59], v[232:235], v[200:203]\n\t" \
    "v_mov_b64 v[248:249], 0\n\t" \
    "s_waitcnt lgkmcnt(3)\n\t" \
    "v_mfma_f32_16x16x32_bf16 v[216:219], v[64:67], v[236:239], v[216:219]\n\t" \
    "ds_read_b128 v[232:235], v252 offset:44032\n\t" \
    "v_mov_b64 v[250:251], 0\n\t" \
    "v_mfma_f32_16x16x32_bf16 v[220:223], v[72:75], v[236:239], v[220:223]\n\t" \
    "v_mfma_f32_16x16x32_bf16 v[212:215], v[56:59], v[236:239], v[212:215]\n\t" \
    "s_waitcnt lgkmcnt(3)\n\t" \
    "v_mfma_f32_16x16x32_bf16 v[204:207], v[68:71], v[240:243], v[204:207]\n\t" \
    "ds_read_b128 v[236:239], v252 offset:47104\n\t" \
    "v_mfma_f32_16x16x32_bf16 v[208:211], v[76:79], v[240:243], v[208:211]\n\t" \
    "v_mfma_f32_16x16x32_bf16 v[200:203], v[60:63], v[240:243], v[200:203]\n\t" \
    "s_waitcnt lgkmcnt(3)\n\t" \
    "v_mfma_f32_16x16x32_bf16 v[216:219], v[68:71], v[224:227], v[216:219]\n\t" \
    "ds_read_b128 v[240:243], v252 offset:48128\n\t" \
    "v_mfma_f32_16x16x32_bf16 v[220:223], v[76:79], v[224:227], v[220:223]\n\t" \
    "v_mfma_f32_16x16x32_bf16 v[212:215], v[60:63], v[224:227], v[212:215]\n\t" \
    "s_waitcnt lgkmcnt(3)\n\t" \
    "v_mfma_f32_16x16x32_bf16 v[204:207], v[184:187], v[228:231], v[204:207]\n\t" \
    "v_add_u32 v252, %[qs], v252\n\t" \
    "v_mfma_f32_16x16x32_bf16 v[208:211], v[192:195], v[228:231], v[208:211]\n\t" \
    "v_mov_b64 v[224:225], 0\n\t" \
    "v_mfma_f32_16x16x32_bf16 v[200:203], v[176:179], v[228:231], v[200:203]\n\t" \
    "v_mov_b64 v[226:227], 0\n\t" \
    "s_waitcnt lgkmcnt(2)\n\t" \
    "v_mfma_f32_16x16x32_bf16 v[204:207], v[188:191], v[232:235], v[204:207]\n\t" \
    "v_mfma_f32_16x16x32_bf16 v[208:211], v[196:199], v[232:235], v[208:211]\n\t" \
    "v_mfma_f32_16x16x32_bf16 v[200:203], v[180:183], v[232:235], v[200:203]\n\t" \
    "s_waitcnt lgkmcnt(1)\n\t" \
    "v_mfma_f32_16x16x32_bf16 v[216:219], v[184:187], v[236:239], v[216:219]\n\t" \
    "ds_read_b128 v[228:231], %[wl] offset:2048\n\t" \
    "ds_read_b128 v[232:235], %[wl] offset:3072\n\t" \
    "v_mfma_f32_16x16x32_bf16 v[220:223], v[192:195], v[236:239], v[220:223]\n\t" \
    "v_cvt_pk_bf16_f32 v204, v204, v205\n\t" \
    "v_cvt_pk_bf16_f32 v205, v206, v207\n\t" \
    "v_mfma_f32_16x16x32_bf16 v[212:215], v[176:179], v[236:239], v[212:215]\n\t" \
    "ds_write_b64 v252, v[204:205] offset:1024\n\t" \
    "v_cvt_pk_bf16_f32 v208, v208, v209\n\t" \
    "s_waitcnt lgkmcnt(3)\n\t" \
    "v_mfma_f32_16x16x32_bf16 v[216:219], v[188:191], v[240:243], v[216:219]\n\t" \
    "v_cvt_pk_bf16_f32 v209, v210, v211\n\t" \
    "ds_write_b64 v252, v[208:209] offset:2048\n\t" \
    "v_mfma_f32_16x16x32_bf16 v[220:223], v[196:199], v[240:243], v[220:223]\n\t" \
    "v_cvt_pk_bf16_f32 v200, v200, v201\n\t" \
    "v_cvt_pk_bf16_f32 v201, v202, v203\n\t" \
    "v_mfma_f32_16x16x32_bf16 v[212:215], v[180:183], v[240:243], v[212:215]\n\t" \
    "ds_write_b64 v252, v[200:201] offset:0\n\t" \
    "ds_read_b128 v[200:203], %[wl] offset:0\n\t" \
    "ds_read_b128 v[204:207], %[wl] offset:1024\n\t" \
    "v_cvt_pk_bf16_f32 v216, v216, v217\n\t" \
    "v_cvt_pk_bf16_f32 v217, v218, v219\n\t" \
    "ds_write_b64 v252, v[216:217] offset:7168\n\t" \
    "v_cvt_pk_bf16_f32 v220, v220, v221\n\t" \
    "v_cvt_pk_bf16_f32 v221, v222, v223\n\t" \
    "ds_write_b64 v252, v[220:221] offset:8192\n\t" \
    "v_cvt_pk_bf16_f32 v212, v212, v213\n\t" \
    "v_cvt_pk_bf16_f32 v213, v214, v215\n\t" \
    "ds_write_b64 v252, v[212:213] offset:6144\n\t" \
    "s_waitcnt lgkmcnt(0)\n\t"
#define ASM_CRIT1_3 \
    "s_branch .Lc0%=\n\t" \
    ".Lcr%=:\n\t" \
    "s_cmp_eq_u32 %[n], 0\n\t" \
    "s_cbranch_scc1 .Lcb%=\n\t" \
    "s_sub_u32 %[n], %[n], 1\n\t" \
    "v_max_u32 v243, v208, v209\n\t" \
    "v_max3_u32 v243, v210, v211, v243\n\t" \
    "v_cmp_eq_u32 vcc, -1, v243\n\t" \
    "s_cbranch_vccz .Lcs0_%=\n\t" \
    "buffer_load_dwordx4 v[208:211], %[ab], %[rs], %[s0] offen sc1\n\t" \
    ".Lcs0_%=:\n\t" \
    "v_max_u32 v243, v236, v237\n\t" \
    "v_max3_u32 v243, v238, v239, v243\n\t" \
    "v_cmp_eq_u32 vcc, -1, v243\n\t" \
    "s_cbranch_vccz .Lcs1_%=\n\t" \
    "buffer_load_dwordx4 v[236:239], %[ab], %[rs], %[s1] offen sc1\n\t" \
    ".Lcs1_%=:\n\t" \
    "v_max_u32 v243, v220, v221\n\t" \
    "v_max3_u32 v243, v222, v223, v243\n\t" \
    "v_cmp_eq_u32 vcc, -1, v243\n\t" \
    "s_cbranch_vccz .Lca%=\n\t" \
    "buffer_load_dwordx4 v[220:223], %[ab], %[rs], %[s2] offen sc1\n\t" \
    "s_branch .Lca%=\n\t" \
    ".Lc0%=:\n\t" \
    "buffer_load_dwordx4 v[208:211], %[ab], %[rs], %[s0] offen sc1\n\t" \
    "buffer_load_dwordx4 v[236:239], %[ab], %[rs], %[s1] offen sc1\n\t" \
    "buffer_load_dwordx4 v[220:223], %[ab], %[rs], %[s2] offen sc1\n\t" \
    "v_bfe_i32 v241, %[ab], 7, 1\n\t" \
    "v_and_b32 v241, 0xaaaa, v241\n\t" \
    "v_or_b32 v241, 0x4444, v241\n\t" \
    "v_xor_b32 v212, 0x400, %[ab]\n\t" \
    "v_xor_b32 v216, 0x800, %[ab]\n\t" \
    "v_xor_b32 v242, 0xc00, %[ab]\n\t" \
    "v_xor_b32 v240, 0x1400, %[ab]\n\t" \
    ".Lca%=:\n\t" \
    "s_waitcnt vmcnt(0)\n\t" \
    "ds_write_b128 %[ab], v[208:211] offset:0\n\t" \
    "ds_write_b128 %[ab], v[236:239] offset:4096\n\t" \
    "ds_write_b128 %[ab], v[220:223] offset:8192\n\t" \
    "v_max_u32 v243, v208, v209\n\t" \
    "v_max3_u32 v243, v210, v211, v243\n\t" \
    "v_max3_u32 v243, v236, v237, v243\n\t" \
    "v_max3_u32 v243, v238, v239, v243\n\t" \
    "v_max3_u32 v243, v220, v221, v243\n\t" \
    "v_max3_u32 v243, v222, v223, v243\n\t" \
    "v_cmp_eq_u32 vcc, -1, v243\n\t" \
    "s_cbranch_vccnz .Lcr%=\n\t" \
    ".Lcb%=:\n\t" \
    PRIUP \
    ASM_ST2 \
    "v_smfmac_f32_16x16x64_bf16 v[224:227], v[208:211], v[200:207], v241\n\t" \
    "ds_read_b128 v[200:203], %[wl] offset:4096\n\t" \
    "ds_read_b128 v[204:207], %[wl] offset:5120\n\t" \
    "v_smfmac_f32_16x16x64_bf16 v[244:247], v[208:211], v[0:7], v241\n\t" \
    "v_smfmac_f32_16x16x64_bf16 v[248:251], v[208:211], v[8:15], v241\n\t" \
    "s_waitcnt lgkmcnt(2)\n\t" \
    "s_barrier\n\t" ASM_ST \
    "ds_read_b128 v[212:215], v212 offset:0\n\t" \
    "ds_read_b128 v[216:219], v216 offset:0\n\t" \
    "ds_read_b128 v[220:223], v242 offset:0\n\t" \
    "ds_read_b128 v[208:211], v240 offset:0\n\t" \
    "v_smfmac_f32_16x16x64_bf16 v[244:247], v[236:239], v[80:87], v241\n\t" \
    "v_xor_b32 v240, 0x1800, %[ab]\n\t" \
    "v_smfmac_f32_16x16x64_bf16 v[248:251], v[236:239], v[88:95], v241\n\t" \
    "s_waitcnt lgkmcnt(4)\n\t" \
    "v_smfmac_f32_16x16x64_bf16 v[224:227], v[236:239], v[200:207], v241\n\t" \
    "ds_read_b128 v[200:203], %[wl] offset:6144\n\t" \
    "ds_read_b128 v[204:207], %[wl] offset:7168\n\t" \
    "ds_read_b128 v[236:239], v240 offset:0\n\t" \
    "s_waitcnt lgkmcnt(6)\n\t" \
    "v_smfmac_f32_16x16x64_bf16 v[224:227], v[212:215], v[16:23], v241\n\t" \
    "v_smfmac_f32_16x16x64_bf16 v[244:247], v[212:215], v[24:31], v241\n\t" \
    "v_xor_b32 v240, 0x1c00, %[ab]\n\t" \
    "v_smfmac_f32_16x16x64_bf16 v[248:251], v[212:215], v[32:39], v241\n\t" \
    "ds_read_b128 v[212:215], v240 offset:0\n\t" \
    "s_waitcnt lgkmcnt(6)\n\t" \
    "v_smfmac_f32_16x16x64_bf16 v[224:227], v[216:219], v[228:235], v241\n\t" \
    "ds_read_b128 v[228:231], %[wl] offset:8192\n\t" \
    "ds_read_b128 v[232:235], %[wl] offset:9216\n\t" \
    "v_smfmac_f32_16x16x64_bf16 v[244:247], v[216:219], v[40:47], v241\n\t" \
    "v_and_b32 v240, 0x3ff, %[ab]\n\t" \
    "v_smfmac_f32_16x16x64_bf16 v[248:251], v[216:219], v[48:55], v241\n\t" \
    "ds_read_b128 v[216:219], v240 offset:8192\n\t" \
    "s_waitcnt lgkmcnt(8)\n\t" \
    "v_smfmac_f32_16x16x64_bf16 v[224:227], v[220:223], v[56:63], v241\n\t" \
    "v_smfmac_f32_16x16x64_bf16 v[244:247], v[220:223], v[64:71], v241\n\t" \
    "v_smfmac_f32_16x16x64_bf16 v[248:251], v[220:223], v[72:79], v241\n\t" \
    "ds_read_b128 v[220:223], v240 offset:9216\n\t" \
    "s_waitcnt lgkmcnt(8)\n\t" \
    "v_smfmac_f32_16x16x64_bf16 v[244:247], v[208:211], v[104:111], v241\n\t" \
    "s_waitcnt lgkmcnt(5)\n\t" \
    "v_smfmac_f32_16x16x64_bf16 v[244:247], v[236:239], v[120:127], v241\n\t" \
    "s_waitcnt lgkmcnt(4)\n\t" \
    "v_smfmac_f32_16x16x64_bf16 v[244:247], v[212:215], v[144:151], v241\n\t" \
    "s_waitcnt lgkmcnt(1)\n\t" \
    "v_smfmac_f32_16x16x64_bf16 v[244:247], v[216:219], v[160:167], v241\n\t" \
    "s_waitcnt lgkmcnt(0)\n\t" \
    "v_smfmac_f32_16x16x64_bf16 v[244:247], v[220:223], v[184:191], v241\n\t" \
    "v_smfmac_f32_16x16x64_bf16 v[248:251], v[208:211], v[112:119], v241\n\t" \
    "v_smfmac_f32_16x16x64_bf16 v[248:251], v[236:239], v[128:135], v241\n\t" \
    "v_smfmac_f32_16x16x64_bf16 v[248:251], v[212:215], v[152:159], v241\n\t" \
    "v_permlane32_swap_b32 v244, v246\n\t" \
    "v_permlane32_swap_b32 v245, v247\n\t" \
    "v_smfmac_f32_16x16x64_bf16 v[248:251], v[216:219], v[168:175], v241\n\t" \
    "v_add_f32 v244, v244, v246\n\t" \
    "v_add_f32 v245, v245, v247\n\t" \
    "v_smfmac_f32_16x16x64_bf16 v[248:251], v[220:223], v[192:199], v241\n\t" \
    "v_cvt_pk_bf16_f32 v244, v244, v245\n\t" \
    "ds_write_b16 %[dst], v244 offset:512\n\t" \
    "ds_write_b16_d16_hi %[dst], v244 offset:528\n\t" \
    "v_smfmac_f32_16x16x64_bf16 v[224:227], v[208:211], v[96:103], v241\n\t" \
    "v_smfmac_f32_16x16x64_bf16 v[224:227], v[236:239], v[200:207], v241\n\t" \
    "v_smfmac_f32_16x16x64_bf16 v[224:227], v[212:215], v[136:143], v241\n\t" \
    "v_permlane32_swap_b32 v248, v250\n\t" \
    "v_permlane32_swap_b32 v249, v251\n\t" \
    "v_smfmac_f32_16x16x64_bf16 v[224:227], v[216:219], v[228:235], v241\n\t" \
    "v_add_f32 v248, v248, v250\n\t" \
    "v_add_f32 v249, v249, v251\n\t" \
    "v_smfmac_f32_16x16x64_bf16 v[224:227], v[220:223], v[176:183], v241\n\t" \
    "v_cvt_pk_bf16_f32 v248, v248, v249\n\t" \
    "ds_write_b16 %[dst], v248 offset:1024\n\t" \
    "ds_write_b16_d16_hi %[dst], v248 offset:1040\n\t" \
    "s_nop 4\n\t" \
    "v_permlane32_swap_b32 v224, v226\n\t" \
    "v_permlane32_swap_b32 v225, v227\n\t" \
    "v_add_f32 v224, v224, v226\n\t" \
    "v_add_f32 v225, v225, v227\n\t" \
    "v_cvt_pk_bf16_f32 v224, v224, v225\n\t" \
    "ds_write_b16 %[dst], v224 offset:0\n\t" \
    "ds_write_b16_d16_hi %[dst], v224 offset:16\n\t"
#define ASM_CRIT1_2 \
    "s_branch .Lc0%=\n\t" \
    ".Lcr%=:\n\t" \
    "s_cmp_eq_u32 %[n], 0\n\t" \
    "s_cbranch_scc1 .Lcb%=\n\t" \
    "s_sub_u32 %[n], %[n], 1\n\t" \
    "v_max_u32 v243, v208, v209\n\t" \
    "v_max3_u32 v243, v210, v211, v243\n\t" \
    "v_cmp_eq_u32 vcc, -1, v243\n\t" \
    "s_cbranch_vccz .Lcs0_%=\n\t" \
    "buffer_load_dwordx4 v[208:211], %[ab], %[rs], %[s0] offen sc1\n\t" \
    ".Lcs0_%=:\n\t" \
    "v_max_u32 v243, v236, v237\n\t" \
    "v_max3_u32 v243, v238, v239, v243\n\t" \
    "v_cmp_eq_u32 vcc, -1, v243\n\t" \
    "s_cbranch_vccz .Lca%=\n\t" \
    "buffer_load_dwordx4 v[236:239], %[ab], %[rs], %[s1] offen sc1\n\t" \
    "s_branch .Lca%=\n\t" \
    ".Lc0%=:\n\t" \
    "buffer_load_dwordx4 v[208:211], %[ab], %[rs], %[s0] offen sc1\n\t" \
    "buffer_load_dwordx4 v[236:239], %[ab], %[rs], %[s1] offen sc1\n\t" \
    "v_bfe_i32 v241, %[ab], 7, 1\n\t" \
    "v_and_b32 v241, 0xaaaa, v241\n\t" \
    "v_or_b32 v241, 0x4444, v241\n\t" \
    "v_xor_b32 v212, 0x400, %[ab]\n\t" \
    "v_xor_b32 v216, 0x800, %[ab]\n\t" \
    "v_xor_b32 v242, 0xc00, %[ab]\n\t" \
    "v_xor_b32 v240, 0x1400, %[ab]\n\t" \
    ".Lca%=:\n\t" \
    "s_waitcnt vmcnt(0)\n\t" \
    "ds_write_b128 %[ab], v[208:211] offset:0\n\t" \
    "ds_write_b128 %[ab], v[236:239] offset:4096\n\t" \
    "v_max_u32 v243, v208, v209\n\t" \
    "v_max3_u32 v243, v210, v211, v243\n\t" \
    "v_max3_u32 v243, v236, v237, v243\n\t" \
    "v_max3_u32 v243, v238, v239, v243\n\t" \
    "v_cmp_eq_u32 vcc, -1, v243\n\t" \
    "s_cbranch_vccnz .Lcr%=\n\t" \
    ".Lcb%=:\n\t" \
    PRIUP \
    ASM_ST2 \
    "v_smfmac_f32_16x16x64_bf16 v[224:227], v[208:211], v[200:207], v241\n\t" \
    "ds_read_b128 v[200:203], %[wl] offset:4096\n\t" \
    "ds_read_b128 v[204:207], %[wl] offset:5120\n\t" \
    "v_smfmac_f32_16x16x64_bf16 v[244:247], v[208:211], v[0:7], v241\n\t" \
    "v_smfmac_f32_16x16x64_bf16 v[248:251], v[208:211], v[8:15], v241\n\t" \
    "s_waitcnt lgkmcnt(2)\n\t" \
    "s_barrier\n\t" ASM_ST \
    "ds_read_b128 v[212:215], v212 offset:0\n\t" \
    "ds_read_b128 v[216:219], v216 offset:0\n\t" \
    "ds_read_b128 v[220:223], v242 offset:0\n\t" \
    "ds_read_b128 v[208:211], v240 offset:0\n\t" \
    "v_smfmac_f32_16x16x64_bf16 v[244:247], v[236:239], v[80:87], v241\n\t" \
    "v_xor_b32 v240, 0x1800, %[ab]\n\t" \
    "v_smfmac_f32_16x16x64_bf16 v[248:251], v[236:239], v[88:95], v241\n\t" \
    "s_waitcnt lgkmcnt(4)\n\t" \
    "v_smfmac_f32_16x16x64_bf16 v[224:227], v[236:239], v[200:207], v241\n\t" \
    "ds_read_b128 v[200:203], %[wl] offset:6144\n\t" \
    "ds_read_b128 v[204:207], %[wl] offset:7168\n\t" \
    "ds_read_b128 v[236:239], v240 offset:0\n\t" \
    "s_waitcnt lgkmcnt(6)\n\t" \
    "v_smfmac_f32_16x16x64_bf16 v[224:227], v[212:215], v[16:23], v241\n\t" \
    "v_smfmac_f32_16x16x64_bf16 v[244:247], v[212:215], v[24:31], v241\n\t" \
    "v_xor_b32 v240, 0x1c00, %[ab]\n\t" \
    "v_smfmac_f32_16x16x64_bf16 v[248:251], v[212:215], v[32:39], v241\n\t" \
    "ds_read_b128 v[212:215], v240 offset:0\n\t" \
    "s_waitcnt lgkmcnt(6)\n\t" \
    "v_smfmac_f32_16x16x64_bf16 v[224:227], v[216:219], v[228:235], v241\n\t" \
    "ds_read_b128 v[228:231], %[wl] offset:8192\n\t" \
    "ds_read_b128 v[232:235], %[wl] offset:9216\n\t" \
    "v_smfmac_f32_16x16x64_bf16 v[244:247], v[216:219], v[40:47], v241\n\t" \
    "v_and_b32 v240, 0x3ff, %[ab]\n\t" \
    "v_smfmac_f32_16x16x64_bf16 v[248:251], v[216:219], v[48:55], v241\n\t" \
    "ds_read_b128 v[216:219], v240 offset:8192\n\t" \
    "s_waitcnt lgkmcnt(8)\n\t" \
    "v_smfmac_f32_16x16x64_bf16 v[224:227], v[220:223], v[56:63], v241\n\t" \
    "v_smfmac_f32_16x16x64_bf16 v[244:247], v[220:223], v[64:71], v241\n\t" \
    "v_smfmac_f32_16x16x64_bf16 v[248:251], v[220:223], v[72:79], v241\n\t" \
    "ds_read_b128 v[220:223], v240 offset:9216\n\t" \
    "s_waitcnt lgkmcnt(8)\n\t" \
    "v_smfmac_f32_16x16x64_bf16 v[244:247], v[208:211], v[104:111], v241\n\t" \
    "s_waitcnt lgkmcnt(5)\n\t" \
    "v_smfmac_f32_16x16x64_bf16 v[244:247], v[236:239], v[120:127], v241\n\t" \
    "s_waitcnt lgkmcnt(4)\n\t" \
    "v_smfmac_f32_16x16x64_bf16 v[244:247], v[212:215], v[144:151], v241\n\t" \
    "s_waitcnt lgkmcnt(1)\n\t" \
    "v_smfmac_f32_16x16x64_bf16 v[244:247], v[216:219], v[160:167], v241\n\t" \
    "s_waitcnt lgkmcnt(0)\n\t" \
    "v_smfmac_f32_16x16x64_bf16 v[244:247], v[220:223], v[184:191], v241\n\t" \
    "v_smfmac_f32_16x16x64_bf16 v[248:251], v[208:211], v[112:119], v241\n\t" \
    "v_smfmac_f32_16x16x64_bf16 v[248:251], v[236:239], v[128:135], v241\n\t" \
    "v_smfmac_f32_16x16x64_bf16 v[248:251], v[212:215], v[152:159], v241\n\t" \
    "v_permlane32_swap_b32 v244, v246\n\t" \
    "v_permlane32_swap_b32 v245, v247\n\t" \
    "v_smfmac_f32_16x16x64_bf16 v[248:251], v[216:219], v[168:175], v241\n\t" \
    "v_add_f32 v244, v244, v246\n\t" \
    "v_add_f32 v245, v245, v247\n\t" \
    "v_smfmac_f32_16x16x64_bf16 v[248:251], v[220:223], v[192:199], v241\n\t" \
    "v_cvt_pk_bf16_f32 v244, v244, v245\n\t" \
    "ds_write_b16 %[dst], v244 offset:512\n\t" \
    "ds_write_b16_d16_hi %[dst], v244 offset:528\n\t" \
    "v_smfmac_f32_16x16x64_bf16 v[224:227], v[208:211], v[96:103], v241\n\t" \
    "v_smfmac_f32_16x16x64_bf16 v[224:227], v[236:239], v[200:207], v241\n\t" \
    "v_smfmac_f32_16x16x64_bf16 v[224:227], v[212:215], v[136:143], v241\n\t" \
    "v_permlane32_swap_b32 v248, v250\n\t" \
    "v_permlane32_swap_b32 v249, v251\n\t" \
    "v_smfmac_f32_16x16x64_bf16 v[224:227], v[216:219], v[228:235], v241\n\t" \
    "v_add_f32 v248, v248, v250\n\t" \
    "v_add_f32 v249, v249, v251\n\t" \
    "v_smfmac_f32_16x16x64_bf16 v[224:227], v[220:223], v[176:183], v241\n\t" \
    "v_cvt_pk_bf16_f32 v248, v248, v249\n\t" \
    "ds_write_b16 %[dst], v248 offset:1024\n\t" \
    "ds_write_b16_d16_hi %[dst], v248 offset:1040\n\t" \
    "s_nop 4\n\t" \
    "v_permlane32_swap_b32 v224, v226\n\t" \
    "v_permlane32_swap_b32 v225, v227\n\t" \
    "v_add_f32 v224, v224, v226\n\t" \
    "v_add_f32 v225, v225, v227\n\t" \
    "v_cvt_pk_bf16_f32 v224, v224, v225\n\t" \
    "ds_write_b16 %[dst], v224 offset:0\n\t" \
    "ds_write_b16_d16_hi %[dst], v224 offset:16\n\t"
#define ASM_CRIT1_N \
    "v_bfe_i32 v241, %[ab], 7, 1\n\t" \
    "v_and_b32 v241, 0xaaaa, v241\n\t" \
    "v_or_b32 v241, 0x4444, v241\n\t" \
    "v_xor_b32 v212, 0x400, %[ab]\n\t" \
    "v_xor_b32 v216, 0x800, %[ab]\n\t" \
    "v_xor_b32 v242, 0xc00, %[ab]\n\t" \
    "v_xor_b32 v240, 0x1400, %[ab]\n\t" \
    "v_xor_b32 v236, 0x1000, %[ab]\n\t" \
    "s_waitcnt lgkmcnt(0)\n\t" \
    "s_barrier\n\t" ASM_ST \
    "ds_read_b128 v[208:211], %[ab] offset:0\n\t" \
    "ds_read_b128 v[212:215], v212 offset:0\n\t" \
    "ds_read_b128 v[216:219], v216 offset:0\n\t" \
    "ds_read_b128 v[220:223], v242 offset:0\n\t" \
    "ds_read_b128 v[236:239], v236 offset:0\n\t" \
    "s_waitcnt lgkmcnt(4)\n\t" \
    "v_smfmac_f32_16x16x64_bf16 v[224:227], v[208:211], v[200:207], v241\n\t" \
    "ds_read_b128 v[200:203], %[wl] offset:4096\n\t" \
    "ds_read_b128 v[204:207], %[wl] offset:5120\n\t" \
    "v_smfmac_f32_16x16x64_bf16 v[244:247], v[208:211], v[0:7], v241\n\t" \
    "v_smfmac_f32_16x16x64_bf16 v[248:251], v[208:211], v[8:15], v241\n\t" \
    "ds_read_b128 v[208:211], v240 offset:0\n\t" \
    "s_waitcnt lgkmcnt(6)\n\t" \
    "v_smfmac_f32_16x16x64_bf16 v[224:227], v[212:215], v[16:23], v241\n\t" \
    "v_smfmac_f32_16x16x64_bf16 v[244:247], v[212:215], v[24:31], v241\n\t" \
    "v_xor_b32 v240, 0x1800, %[ab]\n\t" \
    "v_smfmac_f32_16x16x64_bf16 v[248:251], v[212:215], v[32:39], v241\n\t" \
    "ds_read_b128 v[212:215], v240 offset:0\n\t" \
    "s_waitcnt lgkmcnt(6)\n\t" \
    "v_smfmac_f32_16x16x64_bf16 v[224:227], v[216:219], v[228:235], v241\n\t" \
    "ds_read_b128 v[228:231], %[wl] offset:6144\n\t" \
    "ds_read_b128 v[232:235], %[wl] offset:7168\n\t" \
    "v_smfmac_f32_16x16x64_bf16 v[244:247], v[216:219], v[40:47], v241\n\t" \
    "v_xor_b32 v240, 0x1c00, %[ab]\n\t" \
    "v_smfmac_f32_16x16x64_bf16 v[248:251], v[216:219], v[48:55], v241\n\t" \
    "ds_read_b128 v[216:219], v240 offset:0\n\t" \
    "s_waitcnt lgkmcnt(8)\n\t" \
    "v_smfmac_f32_16x16x64_bf16 v[224:227], v[220:223], v[56:63], v241\n\t" \
    "v_smfmac_f32_16x16x64_bf16 v[244:247], v[220:223], v[64:71], v241\n\t" \
    "v_and_b32 v240, 0x3ff, %[ab]\n\t" \
    "v_smfmac_f32_16x16x64_bf16 v[248:251], v[220:223], v[72:79], v241\n\t" \
    "ds_read_b128 v[220:223], v240 offset:8192\n\t" \
    "s_waitcnt lgkmcnt(6)\n\t" \
    "v_smfmac_f32_16x16x64_bf16 v[224:227], v[236:239], v[200:207], v241\n\t" \
    "ds_read_b128 v[200:203], %[wl] offset:8192\n\t" \
    "ds_read_b128 v[204:207], %[wl] offset:9216\n\t" \
    "v_smfmac_f32_16x16x64_bf16 v[244:247], v[236:239], v[80:87], v241\n\t" \
    "v_smfmac_f32_16x16x64_bf16 v[248:251], v[236:239], v[88:95], v241\n\t" \
    "ds_read_b128 v[236:239], v240 offset:9216\n\t" \
    "s_waitcnt lgkmcnt(8)\n\t" \
    "v_smfmac_f32_16x16x64_bf16 v[244:247], v[208:211], v[104:111], v241\n\t" \
    "s_waitcnt lgkmcnt(7)\n\t" \
    "v_smfmac_f32_16x16x64_bf16 v[244:247], v[212:215], v[120:127], v241\n\t" \
    "s_waitcnt lgkmcnt(4)\n\t" \
    "v_smfmac_f32_16x16x64_bf16 v[244:247], v[216:219], v[144:151], v241\n\t" \
    "s_waitcnt lgkmcnt(3)\n\t" \
    "v_smfmac_f32_16x16x64_bf16 v[244:247], v[220:223], v[160:167], v241\n\t" \
    "s_waitcnt lgkmcnt(0)\n\t" \
    "v_smfmac_f32_16x16x64_bf16 v[244:247], v[236:239], v[184:191], v241\n\t" \
    "v_smfmac_f32_16x16x64_bf16 v[248:251], v[208:211], v[112:119], v241\n\t" \
    "v_smfmac_f32_16x16x64_bf16 v[248:251], v[212:215], v[128:135], v241\n\t" \
    "v_smfmac_f32_16x16x64_bf16 v[248:251], v[216:219], v[152:159], v241\n\t" \
    "v_permlane32_swap_b32 v244, v246\n\t" \
    "v_permlane32_swap_b32 v245, v247\n\t" \
    "v_smfmac_f32_16x16x64_bf16 v[248:251], v[220:223], v[168:175], v241\n\t" \
    "v_add_f32 v244, v244, v246\n\t" \
    "v_add_f32 v245, v245, v247\n\t" \
    "v_smfmac_f32_16x16x64_bf16 v[248:251], v[236:239], v[192:199], v241\n\t" \
    "v_cvt_pk_bf16_f32 v244, v244, v245\n\t" \
    "ds_write_b16 %[dst], v244 offset:512\n\t" \
    "ds_write_b16_d16_hi %[dst], v244 offset:528\n\t" \
    "v_smfmac_f32_16x16x64_bf16 v[224:227], v[208:211], v[96:103], v241\n\t" \
    "v_smfmac_f32_16x16x64_bf16 v[224:227], v[212:215], v[228:235], v241\n\t" \
    "v_smfmac_f32_16x16x64_bf16 v[224:227], v[216:219], v[136:143], v241\n\t" \
    "v_permlane32_swap_b32 v248, v250\n\t" \
    "v_permlane32_swap_b32 v249, v251\n\t" \
    "v_smfmac_f32_16x16x64_bf16 v[224:227], v[220:223], v[200:207], v241\n\t" \
    "v_add_f32 v248, v248, v250\n\t" \
    "v_add_f32 v249, v249, v251\n\t" \
    "v_smfmac_f32_16x16x64_bf16 v[224:227], v[236:239], v[176:183], v241\n\t" \
    "v_cvt_pk_bf16_f32 v248, v248, v249\n\t" \
    "ds_write_b16 %[dst], v248 offset:1024\n\t" \
    "ds_write_b16_d16_hi %[dst], v248 offset:1040\n\t" \
    "s_nop 4\n\t" \
    "v_permlane32_swap_b32 v224, v226\n\t" \
    "v_permlane32_swap_b32 v225, v227\n\t" \
    "v_add_f32 v224, v224, v226\n\t" \
    "v_add_f32 v225, v225, v227\n\t" \
    "v_cvt_pk_bf16_f32 v224, v224, v225\n\t" \
    "ds_write_b16 %[dst], v224 offset:0\n\t" \
    "ds_write_b16_d16_hi %[dst], v224 offset:16\n\t"
#define ASM_CRIT1_V3 \
    "s_branch .Lc0%=\n\t" \
    ".Lcr%=:\n\t" \
    "s_cmp_eq_u32 %[n], 0\n\t" \
    "s_cbranch_scc1 .Lcb%=\n\t" \
    "s_sub_u32 %[n], %[n], 1\n\t" \
    "v_max_u32 v243, v208, v209\n\t" \
    "v_max3_u32 v243, v210, v211, v243\n\t" \
    "v_cmp_eq_u32 vcc, -1, v243\n\t" \
    "s_cbranch_vccz .Lcs0_%=\n\t" \
    "buffer_load_dwordx4 v[208:211], %[ab], %[rs], %[s0] offen sc1\n\t" \
    ".Lcs0_%=:\n\t" \
    "v_max_u32 v243, v236, v237\n\t" \
    "v_max3_u32 v243, v238, v239, v243\n\t" \
    "v_cmp_eq_u32 vcc, -1, v243\n\t" \
    "s_cbranch_vccz .Lcs1_%=\n\t" \
    "buffer_load_dwordx4 v[236:239], %[ab], %[rs], %[s1] offen sc1\n\t" \
    ".Lcs1_%=:\n\t" \
    "v_max_u32 v243, v220, v221\n\t" \
    "v_max3_u32 v243, v222, v223, v243\n\t" \
    "v_cmp_eq_u32 vcc, -1, v243\n\t" \
    "s_cbranch_vccz .Lca%=\n\t" \
    "buffer_load_dwordx4 v[220:223], %[ab], %[rs], %[s2] offen sc1\n\t" \
    "s_branch .Lca%=\n\t" \
    ".Lc0%=:\n\t" \
    "buffer_load_dwordx4 v[208:211], %[ab], %[rs], %[s0] offen sc1\n\t" \
    "buffer_load_dwordx4 v[236:239], %[ab], %[rs], %[s1] offen sc1\n\t" \
    "buffer_load_dwordx4 v[220:223], %[ab], %[rs], %[s2] offen sc1\n\t" \
    "v_bfe_i32 v241, %[ab], 7, 1\n\t" \
    "v_and_b32 v241, 0xaaaa, v241\n\t" \
    "v_or_b32 v241, 0x4444, v241\n\t" \
    "v_xor_b32 v212, 0x400, %[ab]\n\t" \
    "v_xor_b32 v216, 0x800, %[ab]\n\t" \
    "v_xor_b32 v242, 0xc00, %[ab]\n\t" \
    "v_xor_b32 v240, 0x1400, %[ab]\n\t" \
    ".Lca%=:\n\t" \
    "s_waitcnt vmcnt(0)\n\t" \
    "ds_write_b128 %[ab], v[208:211] offset:0\n\t" \
    "ds_write_b128 %[ab], v[236:239] offset:4096\n\t" \
    "ds_write_b128 %[ab], v[220:223] offset:8192\n\t" \
    "v_max_u32 v243, v208, v209\n\t" \
    "v_max3_u32 v243, v210, v211, v243\n\t" \
    "v_max3_u32 v243, v236, v237, v243\n\t" \
    "v_max3_u32 v243, v238, v239, v243\n\t" \
    "v_max3_u32 v243, v220, v221, v243\n\t" \
    "v_max3_u32 v243, v222, v223, v243\n\t" \
    "v_cmp_eq_u32 vcc, -1, v243\n\t" \
    "s_cbranch_vccnz .Lcr%=\n\t" \
    ".Lcb%=:\n\t" \
    PRIUP \
    ASM_ST2 \
    "v_smfmac_f32_16x16x64_bf16 v[224:227], v[208:211], v[200:207], v241\n\t" \
    "ds_read_b128 v[200:203], %[wl] offset:4096\n\t" \
    "ds_read_b128 v[204:207], %[wl] offset:5120\n\t" \
    "v_smfmac_f32_16x16x64_bf16 v[244:247], v[208:211], v[0:7], v241\n\t" \
    "v_smfmac_f32_16x16x64_bf16 v[248:251], v[208:211], v[8:15], v241\n\t" \
    "s_waitcnt lgkmcnt(2)\n\t" \
    "buffer_load_dword %[fv], %[fv], %[rs], %[fs] offen sc1\n\t" \
    "s_barrier\n\t" ASM_ST \
    "ds_read_b128 v[212:215], v212 offset:0\n\t" \
    "ds_read_b128 v[216:219], v216 offset:0\n\t" \
    "ds_read_b128 v[220:223], v242 offset:0\n\t" \
    "ds_read_b128 v[208:211], v240 offset:0\n\t" \
    "v_smfmac_f32_16x16x64_bf16 v[244:247], v[236:239], v[80:87], v241\n\t" \
    "v_xor_b32 v240, 0x1800, %[ab]\n\t" \
    "v_smfmac_f32_16x16x64_bf16 v[248:251], v[236:239], v[88:95], v241\n\t" \
    "s_waitcnt lgkmcnt(4)\n\t" \
    "v_smfmac_f32_16x16x64_bf16 v[224:227], v[236:239], v[200:207], v241\n\t" \
    "ds_read_b128 v[200:203], %[wl] offset:6144\n\t" \
    "ds_read_b128 v[204:207], %[wl] offset:7168\n\t" \
    "ds_read_b128 v[236:239], v240 offset:0\n\t" \
    "s_waitcnt lgkmcnt(6)\n\t" \
    "v_smfmac_f32_16x16x64_bf16 v[224:227], v[212:215], v[16:23], v241\n\t" \
    "v_smfmac_f32_16x16x64_bf16 v[244:247], v[212:215], v[24:31], v241\n\t" \
    "v_xor_b32 v240, 0x1c00, %[ab]\n\t" \
    "v_smfmac_f32_16x16x64_bf16 v[248:251], v[212:215], v[32:39], v241\n\t" \
    "ds_read_b128 v[212:215], v240 offset:0\n\t" \
    "s_waitcnt lgkmcnt(6)\n\t" \
    "v_smfmac_f32_16x16x64_bf16 v[224:227], v[216:219], v[228:235], v241\n\t" \
    "ds_read_b128 v[228:231], %[wl] offset:8192\n\t" \
    "ds_read_b128 v[232:235], %[wl] offset:9216\n\t" \
    "v_smfmac_f32_16x16x64_bf16 v[244:247], v[216:219], v[40:47], v241\n\t" \
    "v_and_b32 v240, 0x3ff, %[ab]\n\t" \
    "v_smfmac_f32_16x16x64_bf16 v[248:251], v[216:219], v[48:55], v241\n\t" \
    "ds_read_b128 v[216:219], v240 offset:8192\n\t" \
    "s_waitcnt lgkmcnt(8)\n\t" \
    "v_smfmac_f32_16x16x64_bf16 v[224:227], v[220:223], v[56:63], v241\n\t" \
    "v_smfmac_f32_16x16x64_bf16 v[244:247], v[220:223], v[64:71], v241\n\t" \
    "v_smfmac_f32_16x16x64_bf16 v[248:251], v[220:223], v[72:79], v241\n\t" \
    "ds_read_b128 v[220:223], v240 offset:9216\n\t" \
    "s_waitcnt lgkmcnt(8)\n\t" \
    "v_smfmac_f32_16x16x64_bf16 v[244:247], v[208:211], v[104:111], v241\n\t" \
    "s_waitcnt lgkmcnt(5)\n\t" \
    "v_smfmac_f32_16x16x64_bf16 v[244:247], v[236:239], v[120:127], v241\n\t" \
    "s_waitcnt lgkmcnt(4)\n\t" \
    "v_smfmac_f32_16x16x64_bf16 v[244:247], v[212:215], v[144:151], v241\n\t" \
    "s_waitcnt lgkmcnt(1)\n\t" \
    "v_smfmac_f32_16x16x64_bf16 v[244:247], v[216:219], v[160:167], v241\n\t" \
    "s_waitcnt lgkmcnt(0)\n\t" \
    "v_smfmac_f32_16x16x64_bf16 v[244:247], v[220:223], v[184:191], v241\n\t" \
    "v_smfmac_f32_16x16x64_bf16 v[248:251], v[208:211], v[112:119], v241\n\t" \
    "v_smfmac_f32_16x16x64_bf16 v[248:251], v[236:239], v[128:135], v241\n\t" \
    "v_smfmac_f32_16x16x64_bf16 v[248:251], v[212:215], v[152:159], v241\n\t" \
    "v_permlane32_swap_b32 v244, v246\n\t" \
    "v_permlane32_swap_b32 v245, v247\n\t" \
    "v_smfmac_f32_16x16x64_bf16 v[248:251], v[216:219], v[168:175], v241\n\t" \
    "v_add_f32 v244, v244, v246\n\t" \
    "v_add_f32 v245, v245, v247\n\t" \
    "v_smfmac_f32_16x16x64_bf16 v[248:251], v[220:223], v[192:199], v241\n\t" \
    "v_cvt_pk_bf16_f32 v244, v244, v245\n\t" \
    "ds_write_b16 %[dst], v244 offset:512\n\t" \
    "ds_write_b16_d16_hi %[dst], v244 offset:528\n\t" \
    "v_smfmac_f32_16x16x64_bf16 v[224:227], v[208:211], v[96:103], v241\n\t" \
    "v_smfmac_f32_16x16x64_bf16 v[224:227], v[236:239], v[200:207], v241\n\t" \
    "v_smfmac_f32_16x16x64_bf16 v[224:227], v[212:215], v[136:143], v241\n\t" \
    "v_permlane32_swap_b32 v248, v250\n\t" \
    "v_permlane32_swap_b32 v249, v251\n\t" \
    "v_smfmac_f32_16x16x64_bf16 v[224:227], v[216:219], v[228:235], v241\n\t" \
    "v_add_f32 v248, v248, v250\n\t" \
    "v_add_f32 v249, v249, v251\n\t" \
    "v_smfmac_f32_16x16x64_bf16 v[224:227], v[220:223], v[176:183], v241\n\t" \
    "v_cvt_pk_bf16_f32 v248, v248, v249\n\t" \
    "ds_write_b16 %[dst], v248 offset:1024\n\t" \
    "ds_write_b16_d16_hi %[dst], v248 offset:1040\n\t" \
    "s_nop 4\n\t" \
    "v_permlane32_swap_b32 v224, v226\n\t" \
    "v_permlane32_swap_b32 v225, v227\n\t" \
    "v_add_f32 v224, v224, v226\n\t" \
    "v_add_f32 v225, v225, v227\n\t" \
    "v_cvt_pk_bf16_f32 v224, v224, v225\n\t" \
    "ds_write_b16 %[dst], v224 offset:0\n\t" \
    "ds_write_b16_d16_hi %[dst], v224 offset:16\n\t"
#define ASM_CRIT1_V2 \
    "s_branch .Lc0%=\n\t" \
    ".Lcr%=:\n\t" \
    "s_cmp_eq_u32 %[n], 0\n\t" \
    "s_cbranch_scc1 .Lcb%=\n\t" \
    "s_sub_u32 %[n], %[n], 1\n\t" \
    "v_max_u32 v243, v208, v209\n\t" \
    "v_max3_u32 v243, v210, v211, v243\n\t" \
    "v_cmp_eq_u32 vcc, -1, v243\n\t" \
    "s_cbranch_vccz .Lcs0_%=\n\t" \
    "buffer_load_dwordx4 v[208:211], %[ab], %[rs], %[s0] offen sc1\n\t" \
    ".Lcs0_%=:\n\t" \
    "v_max_u32 v243, v236, v237\n\t" \
    "v_max3_u32 v243, v238, v239, v243\n\t" \
    "v_cmp_eq_u32 vcc, -1, v243\n\t" \
    "s_cbranch_vccz .Lca%=\n\t" \
    "buffer_load_dwordx4 v[236:239], %[ab], %[rs], %[s1] offen sc1\n\t" \
    "s_branch .Lca%=\n\t" \
    ".Lc0%=:\n\t" \
    "buffer_load_dwordx4 v[208:211], %[ab], %[rs], %[s0] offen sc1\n\t" \
    "buffer_load_dwordx4 v[236:239], %[ab], %[rs], %[s1] offen sc1\n\t" \
    "v_bfe_i32 v241, %[ab], 7, 1\n\t" \
    "v_and_b32 v241, 0xaaaa, v241\n\t" \
    "v_or_b32 v241, 0x4444, v241\n\t" \
    "v_xor_b32 v212, 0x400, %[ab]\n\t" \
    "v_xor_b32 v216, 0x800, %[ab]\n\t" \
    "v_xor_b32 v242, 0xc00, %[ab]\n\t" \
    "v_xor_b32 v240, 0x1400, %[ab]\n\t" \
    ".Lca%=:\n\t" \
    "s_waitcnt vmcnt(0)\n\t" \
    "ds_write_b128 %[ab], v[208:211] offset:0\n\t" \
    "ds_write_b128 %[ab], v[236:239] offset:4096\n\t" \
    "v_max_u32 v243, v208, v209\n\t" \
    "v_max3_u32 v243, v210, v211, v243\n\t" \
    "v_max3_u32 v243, v236, v237, v243\n\t" \
    "v_max3_u32 v243, v238, v239, v243\n\t" \
    "v_cmp_eq_u32 vcc, -1, v243\n\t" \
    "s_cbranch_vccnz .Lcr%=\n\t" \
    ".Lcb%=:\n\t" \
    PRIUP \
    ASM_ST2 \
    "v_smfmac_f32_16x16x64_bf16 v[224:227], v[208:211], v[200:207], v241\n\t" \
    "ds_read_b128 v[200:203], %[wl] offset:4096\n\t" \
    "ds_read_b128 v[204:207], %[wl] offset:5120\n\t" \
    "v_smfmac_f32_16x16x64_bf16 v[244:247], v[208:211], v[0:7], v241\n\t" \
    "v_smfmac_f32_16x16x64_bf16 v[248:251], v[208:211], v[8:15], v241\n\t" \
    "s_waitcnt lgkmcnt(2)\n\t" \
    "buffer_load_dword %[fv], %[fv], %[rs], %[fs] offen sc1\n\t" \
    "s_barrier\n\t" ASM_ST \
    "ds_read_b128 v[212:215], v212 offset:0\n\t" \
    "ds_read_b128 v[216:219], v216 offset:0\n\t" \
    "ds_read_b128 v[220:223], v242 offset:0\n\t" \
    "ds_read_b128 v[208:211], v240 offset:0\n\t" \
    "v_smfmac_f32_16x16x64_bf16 v[244:247], v[236:239], v[80:87], v241\n\t" \
    "v_xor_b32 v240, 0x1800, %[ab]\n\t" \
    "v_smfmac_f32_16x16x64_bf16 v[248:251], v[236:239], v[88:95], v241\n\t" \
    "s_waitcnt lgkmcnt(4)\n\t" \
    "v_smfmac_f32_16x16x64_bf16 v[224:227], v[236:239], v[200:207], v241\n\t" \
    "ds_read_b128 v[200:203], %[wl] offset:6144\n\t" \
    "ds_read_b128 v[204:207], %[wl] offset:7168\n\t" \
    "ds_read_b128 v[236:239], v240 offset:0\n\t" \
    "s_waitcnt lgkmcnt(6)\n\t" \
    "v_smfmac_f32_16x16x64_bf16 v[224:227], v[212:215], v[16:23], v241\n\t" \
    "v_smfmac_f32_16x16x64_bf16 v[244:247], v[212:215], v[24:31], v241\n\t" \
    "v_xor_b32 v240, 0x1c00, %[ab]\n\t" \
    "v_smfmac_f32_16x16x64_bf16 v[248:251], v[212:215], v[32:39], v241\n\t" \
    "ds_read_b128 v[212:215], v240 offset:0\n\t" \
    "s_waitcnt lgkmcnt(6)\n\t" \
    "v_smfmac_f32_16x16x64_bf16 v[224:227], v[216:219], v[228:235], v241\n\t" \
    "ds_read_b128 v[228:231], %[wl] offset:8192\n\t" \
    "ds_read_b128 v[232:235], %[wl] offset:9216\n\t" \
    "v_smfmac_f32_16x16x64_bf16 v[244:247], v[216:219], v[40:47], v241\n\t" \
    "v_and_b32 v240, 0x3ff, %[ab]\n\t" \
    "v_smfmac_f32_16x16x64_bf16 v[248:251], v[216:219], v[48:55], v241\n\t" \
    "ds_read_b128 v[216:219], v240 offset:8192\n\t" \
    "s_waitcnt lgkmcnt(8)\n\t" \
    "v_smfmac_f32_16x16x64_bf16 v[224:227], v[220:223], v[56:63], v241\n\t" \
    "v_smfmac_f32_16x16x64_bf16 v[244:247], v[220:223], v[64:71], v241\n\t" \
    "v_smfmac_f32_16x16x64_bf16 v[248:251], v[220:223], v[72:79], v241\n\t" \
    "ds_read_b128 v[220:223], v240 offset:9216\n\t" \
    "s_waitcnt lgkmcnt(8)\n\t" \
    "v_smfmac_f32_16x16x64_bf16 v[244:247], v[208:211], v[104:111], v241\n\t" \
    "s_waitcnt lgkmcnt(5)\n\t" \
    "v_smfmac_f32_16x16x64_bf16 v[244:247], v[236:239], v[120:127], v241\n\t" \
    "s_waitcnt lgkmcnt(4)\n\t" \
    "v_smfmac_f32_16x16x64_bf16 v[244:247], v[212:215], v[144:151], v241\n\t" \
    "s_waitcnt lgkmcnt(1)\n\t" \
    "v_smfmac_f32_16x16x64_bf16 v[244:247], v[216:219], v[160:167], v241\n\t" \
    "s_waitcnt lgkmcnt(0)\n\t" \
    "v_smfmac_f32_16x16x64_bf16 v[244:247], v[220:223], v[184:191], v241\n\t" \
    "v_smfmac_f32_16x16x64_bf16 v[248:251], v[208:211], v[112:119], v241\n\t" \
    "v_smfmac_f32_16x16x64_bf16 v[248:251], v[236:239], v[128:135], v241\n\t" \
    "v_smfmac_f32_16x16x64_bf16 v[248:251], v[212:215], v[152:159], v241\n\t" \
    "v_permlane32_swap_b32 v244, v246\n\t" \
    "v_permlane32_swap_b32 v245, v247\n\t" \
    "v_smfmac_f32_16x16x64_bf16 v[248:251], v[216:219], v[168:175], v241\n\t" \
    "v_add_f32 v244, v244, v246\n\t" \
    "v_add_f32 v245, v245, v247\n\t" \
    "v_smfmac_f32_16x16x64_bf16 v[248:251], v[220:223], v[192:199], v241\n\t" \
    "v_cvt_pk_bf16_f32 v244, v244, v245\n\t" \
    "ds_write_b16 %[dst], v244 offset:512\n\t" \
    "ds_write_b16_d16_hi %[dst], v244 offset:528\n\t" \
    "v_smfmac_f32_16x16x64_bf16 v[224:227], v[208:211], v[96:103], v241\n\t" \
    "v_smfmac_f32_16x16x64_bf16 v[224:227], v[236:239], v[200:207], v241\n\t" \
    "v_smfmac_f32_16x16x64_bf16 v[224:227], v[212:215], v[136:143], v241\n\t" \
    "v_permlane32_swap_b32 v248, v250\n\t" \
    "v_permlane32_swap_b32 v249, v251\n\t" \
    "v_smfmac_f32_16x16x64_bf16 v[224:227], v[216:219], v[228:235], v241\n\t" \
    "v_add_f32 v248, v248, v250\n\t" \
    "v_add_f32 v249, v249, v251\n\t" \
    "v_smfmac_f32_16x16x64_bf16 v[224:227], v[220:223], v[176:183], v241\n\t" \
    "v_cvt_pk_bf16_f32 v248, v248, v249\n\t" \
    "ds_write_b16 %[dst], v248 offset:1024\n\t" \
    "ds_write_b16_d16_hi %[dst], v248 offset:1040\n\t" \
    "s_nop 4\n\t" \
    "v_permlane32_swap_b32 v224, v226\n\t" \
    "v_permlane32_swap_b32 v225, v227\n\t" \
    "v_add_f32 v224, v224, v226\n\t" \
    "v_add_f32 v225, v225, v227\n\t" \
    "v_cvt_pk_bf16_f32 v224, v224, v225\n\t" \
    "ds_write_b16 %[dst], v224 offset:0\n\t" \
    "ds_write_b16_d16_hi %[dst], v224 offset:16\n\t"
#define ASM_VOX \
    "s_waitcnt lgkmcnt(0)\n\t" \
    "ds_read_b128 v[208:211], %[va] offset:2048\n\t" \
    "ds_read_b128 v[212:215], %[va] offset:3072\n\t" \
    "ds_read_b128 v[216:219], %[va] offset:4096\n\t" \
    "ds_read_b128 v[220:223], %[va] offset:5120\n\t" \
    "ds_read_b128 v[200:203], %[va] offset:0\n\t" \
    "ds_read_b128 v[204:207], %[va] offset:1024\n\t" \
    "ds_read_b128 v[236:239], %[wl] offset:0\n\t" \
    "ds_read_b128 v[240:243], %[wl] offset:1024\n\t" \
    "s_waitcnt lgkmcnt(7)\n\t" \
    "v_mfma_f32_16x16x32_bf16 v[244:247], v[208:211], v[0:3], 0\n\t" \
    "s_waitcnt lgkmcnt(6)\n\t" \
    "v_mfma_f32_16x16x32_bf16 v[244:247], v[212:215], v[4:7], v[244:247]\n\t" \
    "s_waitcnt lgkmcnt(5)\n\t" \
    "v_mfma_f32_16x16x32_bf16 v[244:247], v[216:219], v[8:11], v[244:247]\n\t" \
    "s_waitcnt lgkmcnt(4)\n\t" \
    "v_mfma_f32_16x16x32_bf16 v[244:247], v[220:223], v[12:15], v[244:247]\n\t" \
    "s_waitcnt lgkmcnt(1)\n\t" \
    "v_mfma_f32_16x16x32_bf16 v[244:247], v[200:203], v[236:239], v[244:247]\n\t" \
    "ds_read_b128 v[236:239], %[wl] offset:2048\n\t" \
    "s_waitcnt lgkmcnt(1)\n\t" \
    "v_mfma_f32_16x16x32_bf16 v[244:247], v[204:207], v[240:243], v[244:247]\n\t" \
    "ds_read_b128 v[240:243], %[wl] offset:3072\n\t" \
    "v_mfma_f32_16x16x32_bf16 v[248:251], v[208:211], v[40:43], 0\n\t" \
    "v_mfma_f32_16x16x32_bf16 v[248:251], v[212:215], v[44:47], v[248:251]\n\t" \
    "v_mfma_f32_16x16x32_bf16 v[248:251], v[216:219], v[48:51], v[248:251]\n\t" \
    "v_cvt_pk_bf16_f32 v224, v244, v245\n\t" \
    "v_mfma_f32_16x16x32_bf16 v[248:251], v[220:223], v[52:55], v[248:251]\n\t" \
    "v_cvt_pk_bf16_f32 v225, v246, v247\n\t" \
    "s_waitcnt lgkmcnt(1)\n\t" \
    "v_mfma_f32_16x16x32_bf16 v[248:251], v[200:203], v[236:239], v[248:251]\n\t" \
    "ds_read_b128 v[236:239], %[wl] offset:4096\n\t" \
    "s_waitcnt lgkmcnt(1)\n\t" \
    "v_mfma_f32_16x16x32_bf16 v[248:251], v[204:207], v[240:243], v[248:251]\n\t" \
    "ds_read_b128 v[240:243], %[wl] offset:5120\n\t" \
    "v_mfma_f32_16x16x32_bf16 v[244:247], v[208:211], v[80:83], 0\n\t" \
    "v_mfma_f32_16x16x32_bf16 v[244:247], v[212:215], v[84:87], v[244:247]\n\t" \
    "v_mfma_f32_16x16x32_bf16 v[244:247], v[216:219], v[88:91], v[244:247]\n\t" \
    "v_cvt_pk_bf16_f32 v226, v248, v249\n\t" \
    "v_mfma_f32_16x16x32_bf16 v[244:247], v[220:223], v[92:95], v[244:247]\n\t" \
    "v_cvt_pk_bf16_f32 v227, v250, v251\n\t" \
    "s_waitcnt lgkmcnt(1)\n\t" \
    "v_mfma_f32_16x16x32_bf16 v[244:247], v[200:203], v[236:239], v[244:247]\n\t" \
    "ds_read_b128 v[236:239], %[wl] offset:6144\n\t" \
    "s_waitcnt lgkmcnt(1)\n\t" \
    "v_mfma_f32_16x16x32_bf16 v[244:247], v[204:207], v[240:243], v[244:247]\n\t" \
    "ds_read_b128 v[240:243], %[wl] offset:7168\n\t" \
    "v_mfma_f32_16x16x32_bf16 v[248:251], v[208:211], v[120:123], 0\n\t" \
    "v_mfma_f32_16x16x32_bf16 v[248:251], v[212:215], v[124:127], v[248:251]\n\t" \
    "v_mfma_f32_16x16x32_bf16 v[248:251], v[216:219], v[128:131], v[248:251]\n\t" \
    "v_cvt_pk_bf16_f32 v228, v244, v245\n\t" \
    "v_mfma_f32_16x16x32_bf16 v[248:251], v[220:223], v[132:135], v[248:251]\n\t" \
    "v_cvt_pk_bf16_f32 v229, v246, v247\n\t" \
    "s_waitcnt lgkmcnt(1)\n\t" \
    "v_mfma_f32_16x16x32_bf16 v[248:251], v[200:203], v[236:239], v[248:251]\n\t" \
    "ds_read_b128 v[236:239], %[wl] offset:8192\n\t" \
    "s_waitcnt lgkmcnt(1)\n\t" \
    "v_mfma_f32_16x16x32_bf16 v[248:251], v[204:207], v[240:243], v[248:251]\n\t" \
    "ds_read_b128 v[240:243], %[wl] offset:9216\n\t" \
    "v_mfma_f32_16x16x32_bf16 v[244:247], v[208:211], v[160:163], 0\n\t" \
    "v_mfma_f32_16x16x32_bf16 v[244:247], v[212:215], v[164:167], v[244:247]\n\t" \
    "v_mfma_f32_16x16x32_bf16 v[244:247], v[216:219], v[168:171], v[244:247]\n\t" \
    "v_cvt_pk_bf16_f32 v230, v248, v249\n\t" \
    "v_mfma_f32_16x16x32_bf16 v[244:247], v[220:223], v[172:175], v[244:247]\n\t" \
    "v_cvt_pk_bf16_f32 v231, v250, v251\n\t" \
    "s_waitcnt lgkmcnt(1)\n\t" \
    "v_mfma_f32_16x16x32_bf16 v[244:247], v[200:203], v[236:239], v[244:247]\n\t" \
    "s_waitcnt lgkmcnt(0)\n\t" \
    "v_mfma_f32_16x16x32_bf16 v[244:247], v[204:207], v[240:243], v[244:247]\n\t" \
    "v_mfma_f32_16x16x32_bf16 v[248:251], v[208:211], v[24:27], 0\n\t" \
    "v_mfma_f32_16x16x32_bf16 v[248:251], v[212:215], v[28:31], v[248:251]\n\t" \
    "v_mfma_f32_16x16x32_bf16 v[248:251], v[216:219], v[32:35], v[248:251]\n\t" \
    "v_cvt_pk_bf16_f32 v232, v244, v245\n\t" \
    "v_mfma_f32_16x16x32_bf16 v[248:251], v[220:223], v[36:39], v[248:251]\n\t" \
    "v_cvt_pk_bf16_f32 v233, v246, v247\n\t" \
    "v_mfma_f32_16x16x32_bf16 v[248:251], v[200:203], v[16:19], v[248:251]\n\t" \
    "v_mfma_f32_16x16x32_bf16 v[248:251], v[204:207], v[20:23], v[248:251]\n\t" \
    "v_mfma_f32_16x16x32_bf16 v[244:247], v[208:211], v[64:67], 0\n\t" \
    "v_mfma_f32_16x16x32_bf16 v[244:247], v[212:215], v[68:71], v[244:247]\n\t" \
    "v_mfma_f32_16x16x32_bf16 v[244:247], v[216:219], v[72:75], v[244:247]\n\t" \
    "v_cvt_pk_bf16_f32 v236, v248, v249\n\t" \
    "v_mfma_f32_16x16x32_bf16 v[244:247], v[220:223], v[76:79], v[244:247]\n\t" \
    "v_cvt_pk_bf16_f32 v237, v250, v251\n\t" \
    "v_mfma_f32_16x16x32_bf16 v[244:247], v[200:203], v[56:59], v[244:247]\n\t" \
    "v_mfma_f32_16x16x32_bf16 v[244:247], v[204:207], v[60:63], v[244:247]\n\t" \
    "v_mfma_f32_16x16x32_bf16 v[248:251], v[208:211], v[104:107], 0\n\t" \
    "v_mfma_f32_16x16x32_bf16 v[248:251], v[212:215], v[108:111], v[248:251]\n\t" \
    "v_mfma_f32_16x16x32_bf16 v[248:251], v[216:219], v[112:115], v[248:251]\n\t" \
    "v_cvt_pk_bf16_f32 v238, v244, v245\n\t" \
    "v_mfma_f32_16x16x32_bf16 v[248:251], v[220:223], v[116:119], v[248:251]\n\t" \
    "v_cvt_pk_bf16_f32 v239, v246, v247\n\t" \
    "v_mfma_f32_16x16x32_bf16 v[248:251], v[200:203], v[96:99], v[248:251]\n\t" \
    "v_mfma_f32_16x16x32_bf16 v[248:251], v[204:207], v[100:103], v[248:251]\n\t" \
    "v_mfma_f32_16x16x32_bf16 v[244:247], v[208:211], v[144:147], 0\n\t" \
    "v_mfma_f32_16x16x32_bf16 v[244:247], v[212:215], v[148:151], v[244:247]\n\t" \
    "v_mfma_f32_16x16x32_bf16 v[244:247], v[216:219], v[152:155], v[244:247]\n\t" \
    "v_cvt_pk_bf16_f32 v240, v248, v249\n\t" \
    "v_mfma_f32_16x16x32_bf16 v[244:247], v[220:223], v[156:159], v[244:247]\n\t" \
    "v_cvt_pk_bf16_f32 v241, v250, v251\n\t" \
    "v_mfma_f32_16x16x32_bf16 v[244:247], v[200:203], v[136:139], v[244:247]\n\t" \
    "v_mfma_f32_16x16x32_bf16 v[244:247], v[204:207], v[140:143], v[244:247]\n\t" \
    "v_mfma_f32_16x16x32_bf16 v[248:251], v[208:211], v[184:187], 0\n\t" \
    "ds_read_b128 v[208:211], %[va] offset:8192\n\t" \
    "v_mfma_f32_16x16x32_bf16 v[248:251], v[212:215], v[188:191], v[248:251]\n\t" \
    "ds_read_b128 v[212:215], %[va] offset:9216\n\t" \
    "v_mfma_f32_16x16x32_bf16 v[248:251], v[216:219], v[192:195], v[248:251]\n\t" \
    "ds_read_b128 v[216:219], %[va] offset:10240\n\t" \
    "v_cvt_pk_bf16_f32 v242, v244, v245\n\t" \
    "v_mfma_f32_16x16x32_bf16 v[248:251], v[220:223], v[196:199], v[248:251]\n\t" \
    "ds_read_b128 v[220:223], %[va] offset:11264\n\t" \
    "v_cvt_pk_bf16_f32 v243, v246, v247\n\t" \
    "v_mfma_f32_16x16x32_bf16 v[248:251], v[200:203], v[176:179], v[248:251]\n\t" \
    "ds_read_b128 v[200:203], %[va] offset:6144\n\t" \
    "v_mfma_f32_16x16x32_bf16 v[248:251], v[204:207], v[180:183], v[248:251]\n\t" \
    "ds_read_b128 v[204:207], %[va] offset:7168\n\t" \
    "s_waitcnt lgkmcnt(5)\n\t" \
    "v_mfma_f32_16x16x32_bf16 v[244:247], v[208:211], v[24:27], 0\n\t" \
    "s_waitcnt lgkmcnt(4)\n\t" \
    "v_mfma_f32_16x16x32_bf16 v[244:247], v[212:215], v[28:31], v[244:247]\n\t" \
    "s_waitcnt lgkmcnt(3)\n\t" \
    "v_mfma_f32_16x16x32_bf16 v[244:247], v[216:219], v[32:35], v[244:247]\n\t" \
    "v_cvt_pk_bf16_f32 v234, v248, v249\n\t" \
    "s_waitcnt lgkmcnt(2)\n\t" \
    "v_mfma_f32_16x16x32_bf16 v[244:247], v[220:223], v[36:39], v[244:247]\n\t" \
    "v_cvt_pk_bf16_f32 v235, v250, v251\n\t" \
    "s_waitcnt lgkmcnt(1)\n\t" \
    "v_mfma_f32_16x16x32_bf16 v[244:247], v[200:203], v[16:19], v[244:247]\n\t" \
    "s_waitcnt lgkmcnt(0)\n\t" \
    "v_mfma_f32_16x16x32_bf16 v[244:247], v[204:207], v[20:23], v[244:247]\n\t" \
    "v_mfma_f32_16x16x32_bf16 v[248:251], v[208:211], v[64:67], 0\n\t" \
    "s_add_i32 %[so], %[so], 65536\n\t" \
    "v_mfma_f32_16x16x32_bf16 v[248:251], v[212:215], v[68:71], v[248:251]\n\t" \
    "s_nop 1\n\t" \
    "v_cvt_pk_bf16_f32 v247, v246, v247\n\t" \
    "v_mfma_f32_16x16x32_bf16 v[248:251], v[216:219], v[72:75], v[248:251]\n\t" \
    "v_cvt_pk_bf16_f32 v246, v244, v245\n\t" \
    "v_mfma_f32_16x16x32_bf16 v[248:251], v[220:223], v[76:79], v[248:251]\n\t" \
    "v_mov_b64 v[244:245], v[236:237]\n\t" \
    "v_mfma_f32_16x16x32_bf16 v[248:251], v[200:203], v[56:59], v[248:251]\n\t" \
    "buffer_store_dwordx4 v[244:247], %[l16], %[rd], %[so] offen sc1\n\t" \
    "v_mfma_f32_16x16x32_bf16 v[248:251], v[204:207], v[60:63], v[248:251]\n\t" \
    "v_mfma_f32_16x16x32_bf16 v[244:247], v[208:211], v[104:107], 0\n\t" \
    "s_add_i32 %[so], %[so], 131072\n\t" \
    "v_mfma_f32_16x16x32_bf16 v[244:247], v[212:215], v[108:111], v[244:247]\n\t" \
    "s_nop 1\n\t" \
    "v_cvt_pk_bf16_f32 v251, v250, v251\n\t" \
    "v_mfma_f32_16x16x32_bf16 v[244:247], v[216:219], v[112:115], v[244:247]\n\t" \
    "v_cvt_pk_bf16_f32 v250, v248, v249\n\t" \
    "v_mfma_f32_16x16x32_bf16 v[244:247], v[220:223], v[116:119], v[244:247]\n\t" \
    "v_mov_b64 v[248:249], v[238:239]\n\t" \
    "v_mfma_f32_16x16x32_bf16 v[244:247], v[200:203], v[96:99], v[244:247]\n\t" \
    "buffer_store_dwordx4 v[248:251], %[l16], %[rd], %[so] offen sc1\n\t" \
    "v_mfma_f32_16x16x32_bf16 v[244:247], v[204:207], v[100:103], v[244:247]\n\t" \
    "v_mfma_f32_16x16x32_bf16 v[248:251], v[208:211], v[144:147], 0\n\t" \
    "s_add_i32 %[so], %[so], 131072\n\t" \
    "v_mfma_f32_16x16x32_bf16 v[248:251], v[212:215], v[148:151], v[248:251]\n\t" \
    "s_nop 1\n\t" \
    "v_cvt_pk_bf16_f32 v247, v246, v247\n\t" \
    "v_mfma_f32_16x16x32_bf16 v[248:251], v[216:219], v[152:155], v[248:251]\n\t" \
    "v_cvt_pk_bf16_f32 v246, v244, v245\n\t" \
    "v_mfma_f32_16x16x32_bf16 v[248:251], v[220:223], v[156:159], v[248:251]\n\t" \
    "v_mov_b64 v[244:245], v[240:241]\n\t" \
    "v_mfma_f32_16x16x32_bf16 v[248:251], v[200:203], v[136:139], v[248:251]\n\t" \
    "buffer_store_dwordx4 v[244:247], %[l16], %[rd], %[so] offen sc1\n\t" \
    "v_mfma_f32_16x16x32_bf16 v[248:251], v[204:207], v[140:143], v[248:251]\n\t" \
    "v_mfma_f32_16x16x32_bf16 v[244:247], v[208:211], v[184:187], 0\n\t" \
    "s_add_i32 %[so], %[so], 131072\n\t" \
    "v_mfma_f32_16x16x32_bf16 v[244:247], v[212:215], v[188:191], v[244:247]\n\t" \
    "s_nop 1\n\t" \
    "v_cvt_pk_bf16_f32 v251, v250, v251\n\t" \
    "v_mfma_f32_16x16x32_bf16 v[244:247], v[216:219], v[192:195], v[244:247]\n\t" \
    "v_cvt_pk_bf16_f32 v250, v248, v249\n\t" \
    "v_mfma_f32_16x16x32_bf16 v[244:247], v[220:223], v[196:199], v[244:247]\n\t" \
    "v_mov_b64 v[248:249], v[242:243]\n\t" \
    "v_mfma_f32_16x16x32_bf16 v[244:247], v[200:203], v[176:179], v[244:247]\n\t" \
    "buffer_store_dwordx4 v[248:251], %[l16], %[rd], %[so] offen sc1\n\t" \
    "v_mfma_f32_16x16x32_bf16 v[244:247], v[204:207], v[180:183], v[244:247]\n\t" \
    "ds_read_b128 v[236:239], %[wl] offset:0\n\t" \
    "ds_read_b128 v[240:243], %[wl] offset:1024\n\t" \
    "v_mfma_f32_16x16x32_bf16 v[248:251], v[208:211], v[0:3], 0\n\t" \
    "s_add_i32 %[so], %[so], 131072\n\t" \
    "v_mfma_f32_16x16x32_bf16 v[248:251], v[212:215], v[4:7], v[248:251]\n\t" \
    "v_cvt_pk_bf16_f32 v247, v246, v247\n\t" \
    "v_mfma_f32_16x16x32_bf16 v[248:251], v[216:219], v[8:11], v[248:251]\n\t" \
    "v_cvt_pk_bf16_f32 v246, v244, v245\n\t" \
    "v_mfma_f32_16x16x32_bf16 v[248:251], v[220:223], v[12:15], v[248:251]\n\t" \
    "v_mov_b64 v[244:245], v[234:235]\n\t" \
    "s_waitcnt lgkmcnt(1)\n\t" \
    "v_mfma_f32_16x16x32_bf16 v[248:251], v[200:203], v[236:239], v[248:251]\n\t" \
    "ds_read_b128 v[236:239], %[wl] offset:2048\n\t" \
    "buffer_store_dwordx4 v[244:247], %[l16], %[rd], %[so] offen sc1\n\t" \
    "s_waitcnt lgkmcnt(1)\n\t" \
    "v_mfma_f32_16x16x32_bf16 v[248:251], v[204:207], v[240:243], v[248:251]\n\t" \
    "ds_read_b128 v[240:243], %[wl] offset:3072\n\t" \
    "v_mfma_f32_16x16x32_bf16 v[244:247], v[208:211], v[40:43], 0\n\t" \
    "s_add_i32 %[so], %[so], -589824\n\t" \
    "v_mfma_f32_16x16x32_bf16 v[244:247], v[212:215], v[44:47], v[244:247]\n\t" \
    "s_nop 0\n\t" \
    "v_cvt_pk_bf16_f32 v251, v250, v251\n\t" \
    "v_mfma_f32_16x16x32_bf16 v[244:247], v[216:219], v[48:51], v[244:247]\n\t" \
    "v_cvt_pk_bf16_f32 v250, v248, v249\n\t" \
    "v_mfma_f32_16x16x32_bf16 v[244:247], v[220:223], v[52:55], v[244:247]\n\t" \
    "v_mov_b64 v[248:249], v[224:225]\n\t" \
    "s_waitcnt lgkmcnt(1)\n\t" \
    "v_mfma_f32_16x16x32_bf16 v[244:247], v[200:203], v[236:239], v[244:247]\n\t" \
    "ds_read_b128 v[236:239], %[wl] offset:4096\n\t" \
    "buffer_store_dwordx4 v[248:251], %[l16], %[rd], %[so] offen sc1\n\t" \
    "s_waitcnt lgkmcnt(1)\n\t" \
    "v_mfma_f32_16x16x32_bf16 v[244:247], v[204:207], v[240:243], v[244:247]\n\t" \
    "ds_read_b128 v[240:243], %[wl] offset:5120\n\t" \
    "v_mfma_f32_16x16x32_bf16 v[248:251], v[208:211], v[80:83], 0\n\t" \
    "s_add_i32 %[so], %[so], 131072\n\t" \
    "v_mfma_f32_16x16x32_bf16 v[248:251], v[212:215], v[84:87], v[248:251]\n\t" \
    "s_nop 0\n\t" \
    "v_cvt_pk_bf16_f32 v247, v246, v247\n\t" \
    "v_mfma_f32_16x16x32_bf16 v[248:251], v[216:219], v[88:91], v[248:251]\n\t" \
    "v_cvt_pk_bf16_f32 v246, v244, v245\n\t" \
    "v_mfma_f32_16x16x32_bf16 v[248:251], v[220:223], v[92:95], v[248:251]\n\t" \
    "v_mov_b64 v[244:245], v[226:227]\n\t" \
    "s_waitcnt lgkmcnt(1)\n\t" \
    "v_mfma_f32_16x16x32_bf16 v[248:251], v[200:203], v[236:239], v[248:251]\n\t" \
    "ds_read_b128 v[236:239], %[wl] offset:6144\n\t" \
    "buffer_store_dwordx4 v[244:247], %[l16], %[rd], %[so] offen sc1\n\t" \
    "s_waitcnt lgkmcnt(1)\n\t" \
    "v_mfma_f32_16x16x32_bf16 v[248:251], v[204:207], v[240:243], v[248:251]\n\t" \
    "ds_read_b128 v[240:243], %[wl] offset:7168\n\t" \
    "v_mfma_f32_16x16x32_bf16 v[244:247], v[208:211], v[120:123], 0\n\t" \
    "s_add_i32 %[so], %[so], 131072\n\t" \
    "v_mfma_f32_16x16x32_bf16 v[244:247], v[212:215], v[124:127], v[244:247]\n\t" \
    "s_nop 0\n\t" \
    "v_cvt_pk_bf16_f32 v251, v250, v251\n\t" \
    "v_mfma_f32_16x16x32_bf16 v[244:247], v[216:219], v[128:131], v[244:247]\n\t" \
    "v_cvt_pk_bf16_f32 v250, v248, v249\n\t" \
    "v_mfma_f32_16x16x32_bf16 v[244:247], v[220:223], v[132:135], v[244:247]\n\t" \
    "v_mov_b64 v[248:249], v[228:229]\n\t" \
    "s_waitcnt lgkmcnt(1)\n\t" \
    "v_mfma_f32_16x16x32_bf16 v[244:247], v[200:203], v[236:239], v[244:247]\n\t" \
    "ds_read_b128 v[236:239], %[wl] offset:8192\n\t" \
    "buffer_store_dwordx4 v[248:251], %[l16], %[rd], %[so] offen sc1\n\t" \
    "s_waitcnt lgkmcnt(1)\n\t" \
    "v_mfma_f32_16x16x32_bf16 v[244:247], v[204:207], v[240:243], v[244:247]\n\t" \
    "ds_read_b128 v[240:243], %[wl] offset:9216\n\t" \
    "v_mfma_f32_16x16x32_bf16 v[248:251], v[208:211], v[160:163], 0\n\t" \
    "s_add_i32 %[so], %[so], 131072\n\t" \
    "v_mfma_f32_16x16x32_bf16 v[248:251], v[212:215], v[164:167], v[248:251]\n\t" \
    "s_nop 0\n\t" \
    "v_cvt_pk_bf16_f32 v247, v246, v247\n\t" \
    "v_mfma_f32_16x16x32_bf16 v[248:251], v[216:219], v[168:171], v[248:251]\n\t" \
    "v_cvt_pk_bf16_f32 v246, v244, v245\n\t" \
    "v_mfma_f32_16x16x32_bf16 v[248:251], v[220:223], v[172:175], v[248:251]\n\t" \
    "v_mov_b64 v[244:245], v[230:231]\n\t" \
    "s_waitcnt lgkmcnt(1)\n\t" \
    "v_mfma_f32_16x16x32_bf16 v[248:251], v[200:203], v[236:239], v[248:251]\n\t" \
    "buffer_store_dwordx4 v[244:247], %[l16], %[rd], %[so] offen sc1\n\t" \
    "s_waitcnt lgkmcnt(0)\n\t" \
    "v_mfma_f32_16x16x32_bf16 v[248:251], v[204:207], v[240:243], v[248:251]\n\t" \
    "s_add_i32 %[so], %[so], 131072\n\t" \
    "s_nop 7\n\t" \
    "v_cvt_pk_bf16_f32 v251, v250, v251\n\t" \
    "v_cvt_pk_bf16_f32 v250, v248, v249\n\t" \
    "v_mov_b64 v[248:249], v[232:233]\n\t" \
    "buffer_store_dwordx4 v[248:251], %[l16], %[rd], %[so] offen sc1\n\t" \
    "s_nop 1\n\t"
#define ASM_CRIT2 \
    "s_waitcnt lgkmcnt(0)\n\t" \
    "s_barrier\n\t" ASM_ST \
    "ds_read_b128 v[204:207], v200 offset:11264\n\t" \
    "ds_read_b128 v[208:211], v200 offset:12288\n\t" \
    "ds_read_b128 v[200:203], v200 offset:10240\n\t" \
    "s_waitcnt lgkmcnt(2)\n\t" \
    "v_smfmac_f32_16x16x64_bf16 v[224:227], v[204:207], v[24:31], %[sidx]\n\t" \
    "v_smfmac_f32_16x16x64_bf16 v[220:223], v[204:207], v[0:7], %[sidx]\n\t" \
    "s_waitcnt lgkmcnt(1)\n\t" \
    "v_smfmac_f32_16x16x64_bf16 v[224:227], v[208:211], v[32:39], %[sidx]\n\t" \
    "v_smfmac_f32_16x16x64_bf16 v[220:223], v[208:211], v[8:15], %[sidx]\n\t" \
    "s_waitcnt lgkmcnt(0)\n\t" \
    "v_smfmac_f32_16x16x64_bf16 v[224:227], v[200:203], v[16:23], %[sidx]\n\t" \
    "v_smfmac_f32_16x16x64_bf16 v[220:223], v[200:203], v[212:219], %[sidx]\n\t" \
    "ds_read_b128 v[212:215], %[wl] offset:2048\n\t" \
    "ds_read_b128 v[216:219], %[wl] offset:3072\n\t" \
    "v_smfmac_f32_16x16x64_bf16 v[232:235], v[204:207], v[64:71], %[sidx]\n\t" \
    "v_smfmac_f32_16x16x64_bf16 v[228:231], v[204:207], v[40:47], %[sidx]\n\t" \
    "v_smfmac_f32_16x16x64_bf16 v[232:235], v[208:211], v[72:79], %[sidx]\n\t" \
    "v_permlane32_swap_b32 v220, v224\n\t" \
    "v_permlane32_swap_b32 v221, v225\n\t" \
    "v_permlane32_swap_b32 v222, v226\n\t" \
    "v_smfmac_f32_16x16x64_bf16 v[228:231], v[208:211], v[48:55], %[sidx]\n\t" \
    "v_permlane32_swap_b32 v223, v227\n\t" \
    "v_add_f32 v220, v220, v224\n\t" \
    "v_add_f32 v221, v221, v225\n\t" \
    "v_smfmac_f32_16x16x64_bf16 v[232:235], v[200:203], v[56:63], %[sidx]\n\t" \
    "v_add_f32 v222, v222, v226\n\t" \
    "v_add_f32 v223, v223, v227\n\t" \
    "v_cvt_pk_bf16_f32 v226, v220, v221\n\t" \
    "s_waitcnt lgkmcnt(0)\n\t" \
    "v_smfmac_f32_16x16x64_bf16 v[228:231], v[200:203], v[212:219], %[sidx]\n\t" \
    "ds_read_b128 v[212:215], %[wl] offset:4096\n\t" \
    "ds_read_b128 v[216:219], %[wl] offset:5120\n\t" \
    "v_cvt_pk_bf16_f32 v227, v222, v223\n\t" \
    "v_min_u32 v226, -2, v226\n\t" \
    "v_min_u32 v227, -2, v227\n\t" \
    "v_smfmac_f32_16x16x64_bf16 v[240:243], v[204:207], v[104:111], %[sidx]\n\t" \
    "v_smfmac_f32_16x16x64_bf16 v[236:239], v[204:207], v[80:87], %[sidx]\n\t" \
    "v_smfmac_f32_16x16x64_bf16 v[240:243], v[208:211], v[112:119], %[sidx]\n\t" \
    "v_permlane32_swap_b32 v228, v232\n\t" \
    "v_permlane32_swap_b32 v229, v233\n\t" \
    "v_permlane32_swap_b32 v230, v234\n\t" \
    "v_smfmac_f32_16x16x64_bf16 v[236:239], v[208:211], v[88:95], %[sidx]\n\t" \
    "v_permlane32_swap_b32 v231, v235\n\t" \
    "v_add_f32 v228, v228, v232\n\t" \
    "v_add_f32 v229, v229, v233\n\t" \
    "v_smfmac_f32_16x16x64_bf16 v[240:243], v[200:203], v[96:103], %[sidx]\n\t" \
    "v_add_f32 v230, v230, v234\n\t" \
    "v_add_f32 v231, v231, v235\n\t" \
    "v_cvt_pk_bf16_f32 v228, v228, v229\n\t" \
    "s_waitcnt lgkmcnt(0)\n\t" \
    "v_smfmac_f32_16x16x64_bf16 v[236:239], v[200:203], v[212:219], %[sidx]\n\t" \
    "ds_read_b128 v[212:215], %[wl] offset:6144\n\t" \
    "ds_read_b128 v[216:219], %[wl] offset:7168\n\t" \
    "v_cvt_pk_bf16_f32 v229, v230, v231\n\t" \
    "v_min_u32 v228, -2, v228\n\t" \
    "v_min_u32 v229, -2, v229\n\t" \
    "s_nop 1\n\t" \
    "v_permlane16_swap_b32 v226, v228\n\t" \
    "v_permlane16_swap_b32 v227, v229\n\t" \
    "buffer_store_dwordx4 v[226:229], %[vo], %[rr], %[so] offen sc1\n\t" \
    "s_add_u32 %[so], %[so], 0x10000\n\t" \
    "v_smfmac_f32_16x16x64_bf16 v[248:251], v[204:207], v[144:151], %[sidx]\n\t" \
    "v_mov_b64 v[220:221], 0\n\t" \
    "v_mov_b64 v[222:223], 0\n\t" \
    "v_smfmac_f32_16x16x64_bf16 v[244:247], v[204:207], v[120:127], %[sidx]\n\t" \
    "v_mov_b64 v[224:225], 0\n\t" \
    "v_mov_b64 v[226:227], 0\n\t" \
    "v_smfmac_f32_16x16x64_bf16 v[248:251], v[208:211], v[152:159], %[sidx]\n\t" \
    "v_permlane32_swap_b32 v236, v240\n\t" \
    "v_permlane32_swap_b32 v237, v241\n\t" \
    "v_permlane32_swap_b32 v238, v242\n\t" \
    "v_smfmac_f32_16x16x64_bf16 v[244:247], v[208:211], v[128:135], %[sidx]\n\t" \
    "v_permlane32_swap_b32 v239, v243\n\t" \
    "v_add_f32 v236, v236, v240\n\t" \
    "v_add_f32 v237, v237, v241\n\t" \
    "v_smfmac_f32_16x16x64_bf16 v[248:251], v[200:203], v[136:143], %[sidx]\n\t" \
    "v_add_f32 v238, v238, v242\n\t" \
    "v_add_f32 v239, v239, v243\n\t" \
    "v_cvt_pk_bf16_f32 v242, v236, v237\n\t" \
    "s_waitcnt lgkmcnt(0)\n\t" \
    "v_smfmac_f32_16x16x64_bf16 v[244:247], v[200:203], v[212:219], %[sidx]\n\t" \
    "ds_read_b128 v[212:215], %[wl] offset:8192\n\t" \
    "ds_read_b128 v[216:219], %[wl] offset:9216\n\t" \
    "v_cvt_pk_bf16_f32 v243, v238, v239\n\t" \
    "v_min_u32 v242, -2, v242\n\t" \
    "v_min_u32 v243, -2, v243\n\t" \
    "v_smfmac_f32_16x16x64_bf16 v[224:227], v[204:207], v[184:191], %[sidx]\n\t" \
    "v_and_b32 v232, 0x3fff, %[vo]\n\t" \
    "v_bfe_u32 v233, %[vo], 14, 1\n\t" \
    "v_smfmac_f32_16x16x64_bf16 v[220:223], v[204:207], v[160:167], %[sidx]\n\t" \
    "v_lshl_add_u32 v232, v233, 3, v232\n\t" \
    "v_permlane32_swap_b32 v244, v248\n\t" \
    "v_smfmac_f32_16x16x64_bf16 v[224:227], v[208:211], v[192:199], %[sidx]\n\t" \
    "v_permlane32_swap_b32 v245, v249\n\t" \
    "v_permlane32_swap_b32 v246, v250\n\t" \
    "v_permlane32_swap_b32 v247, v251\n\t" \
    "v_smfmac_f32_16x16x64_bf16 v[220:223], v[208:211], v[168:175], %[sidx]\n\t" \
    "v_add_f32 v244, v244, v248\n\t" \
    "v_add_f32 v245, v245, v249\n\t" \
    "v_add_f32 v246, v246, v250\n\t" \
    "v_smfmac_f32_16x16x64_bf16 v[224:227], v[200:203], v[176:183], %[sidx]\n\t" \
    "v_add_f32 v247, v247, v251\n\t" \
    "v_cvt_pk_bf16_f32 v244, v244, v245\n\t" \
    "v_cvt_pk_bf16_f32 v245, v246, v247\n\t" \
    "s_waitcnt lgkmcnt(0)\n\t" \
    "v_smfmac_f32_16x16x64_bf16 v[220:223], v[200:203], v[212:219], %[sidx]\n\t" \
    "v_min_u32 v244, -2, v244\n\t" \
    "v_min_u32 v245, -2, v245\n\t" \
    "s_nop 1\n\t" \
    "v_permlane16_swap_b32 v242, v244\n\t" \
    "v_permlane16_swap_b32 v243, v245\n\t" \
    "buffer_store_dwordx4 v[242:245], %[vo], %[rr], %[so] offen sc1\n\t" \
    "s_add_u32 %[so], %[so], 0x10000\n\t" \
    "s_nop 0\n\t" \
    "v_permlane32_swap_b32 v220, v224\n\t" \
    "v_permlane32_swap_b32 v221, v225\n\t" \
    "v_permlane32_swap_b32 v222, v226\n\t" \
    "v_permlane32_swap_b32 v223, v227\n\t" \
    "v_add_f32 v220, v220, v224\n\t" \
    "v_add_f32 v221, v221, v225\n\t" \
    "v_add_f32 v222, v222, v226\n\t" \
    "v_add_f32 v223, v223, v227\n\t" \
    "v_cvt_pk_bf16_f32 v220, v220, v221\n\t" \
    "v_cvt_pk_bf16_f32 v221, v222, v223\n\t" \
    "v_min_u32 v220, -2, v220\n\t" \
    "v_min_u32 v221, -2, v221\n\t" \
    "buffer_store_dwordx2 v[220:221], v232, %[rr], %[so] offen sc1\n\t" \
    "s_nop 1\n\t"
#define ASM_SM \
    "v_mbcnt_lo_u32_b32 v252, -1, 0\n\t" \
    "v_mbcnt_hi_u32_b32 v252, -1, v252\n\t" \
    "v_lshlrev_b32 v252, 4, v252\n\t" \
    "v_lshrrev_b32 v253, 1, v252\n\t" \
    "v_xor_b32 v253, v253, v252\n\t" \
    "v_and_b32 v253, 0x20, v253\n\t" \
    "v_mul_u32_u24 v253, 3, v253\n\t" \
    "v_xor_b32 v253, v253, v252\n\t" \
    "ds_read_b128 v[200:203], v253 offset:28672\n\t" \
    "ds_read_b128 v[204:207], v253 offset:29696\n\t" \
    "ds_read_b128 v[208:211], v253 offset:34816\n\t" \
    "ds_read_b128 v[212:215], v253 offset:35840\n\t" \
    "ds_read_b128 v[216:219], v253 offset:30720\n\t" \
    "ds_read_b128 v[220:223], v253 offset:31744\n\t" \
    "ds_read_b128 v[224:227], v253 offset:36864\n\t" \
    "ds_read_b128 v[228:231], v253 offset:37888\n\t" \
    "v_lshrrev_b32 v254, 1, v252\n\t" \
    "v_and_b32 v254, 0x180, v254\n\t" \
    "v_and_or_b32 v254, v252, %[c70], v254\n\t" \
    "v_bfe_i32 v255, v252, 7, 1\n\t" \
    "v_and_b32 v255, 0xaaaa, v255\n\t" \
    "v_or_b32 v255, 0x4444, v255\n\t" \
    "v_mov_b64 v[232:233], 0\n\t" \
    "v_mov_b64 v[234:235], 0\n\t" \
    "v_mov_b64 v[236:237], 0\n\t" \
    "v_mov_b64 v[238:239], 0\n\t" \
    "s_waitcnt lgkmcnt(0)\n\t" \
    "s_barrier\n\t" ASM_ST \
    "s_setprio 3\n\t" \
    "ds_read_b128 v[240:243], v252 offset:10240\n\t" \
    "ds_read_b128 v[244:247], v252 offset:11264\n\t" \
    "ds_read_b128 v[248:251], v252 offset:12288\n\t" \
    "s_waitcnt lgkmcnt(2)\n\t" \
    "v_smfmac_f32_16x16x64_bf16 v[232:235], v[240:243], v[200:207], v255\n\t" \
    "ds_read_b128 v[200:203], v253 offset:32768\n\t" \
    "ds_read_b128 v[204:207], v253 offset:33792\n\t" \
    "v_smfmac_f32_16x16x64_bf16 v[236:239], v[240:243], v[208:215], v255\n\t" \
    "ds_read_b128 v[208:211], v253 offset:38912\n\t" \
    "ds_read_b128 v[212:215], v253 offset:39936\n\t" \
    "s_waitcnt lgkmcnt(5)\n\t" \
    "v_smfmac_f32_16x16x64_bf16 v[232:235], v[244:247], v[216:223], v255\n\t" \
    "ds_read_b128 v[216:219], v254 offset:13312\n\t" \
    "ds_read_b128 v[220:223], v254 offset:13824\n\t" \
    "v_smfmac_f32_16x16x64_bf16 v[236:239], v[244:247], v[224:231], v255\n\t" \
    "ds_read_b128 v[224:227], v254 offset:14336\n\t" \
    "ds_read_b128 v[228:231], v254 offset:14848\n\t" \
    "s_waitcnt lgkmcnt(6)\n\t" \
    "v_smfmac_f32_16x16x64_bf16 v[232:235], v[248:251], v[200:207], v255\n\t" \
    "s_waitcnt lgkmcnt(4)\n\t" \
    "v_smfmac_f32_16x16x64_bf16 v[236:239], v[248:251], v[208:215], v255\n\t" \
    "ds_read_b128 v[208:211], v254 offset:15360\n\t" \
    "ds_read_b128 v[212:215], v254 offset:15872\n\t" \
    "v_mov_b64 v[204:205], 0\n\t" \
    "v_mov_b64 v[206:207], 0\n\t" \
    "s_nop 1\n\t" \
    "s_waitcnt lgkmcnt(4)\n\t" \
    "v_smfmac_f32_16x16x64_bf16 v[204:207], v[240:243], v[216:223], v255\n\t" \
    "s_waitcnt lgkmcnt(2)\n\t" \
    "v_smfmac_f32_16x16x64_bf16 v[204:207], v[244:247], v[224:231], v255\n\t" \
    "v_bfe_u32 v253, v252, 5, 1\n\t" \
    "v_bfe_i32 v202, v252, 4, 1\n\t" \
    "v_lshlrev_b32 v200, 1, v252\n\t" \
    "v_and_b32 v200, 0x180, v200\n\t" \
    "v_permlane32_swap_b32 v232, v234\n\t" \
    "v_permlane32_swap_b32 v233, v235\n\t" \
    "v_permlane32_swap_b32 v236, v238\n\t" \
    "v_permlane32_swap_b32 v237, v239\n\t" \
    "v_lshrrev_b32 v201, 2, v252\n\t" \
    "v_lshrrev_b32 v203, 4, v252\n\t" \
    "v_and_b32 v201, 0x40, v201\n\t" \
    "v_and_or_b32 v201, v203, 32, v201\n\t" \
    "v_and_or_b32 v201, v252, 16, v201\n\t" \
    "v_pk_add_f32 v[232:233], v[232:233], v[234:235]\n\t" \
    "v_pk_add_f32 v[234:235], v[236:237], v[238:239]\n\t" \
    "v_or_b32 v200, v200, v201\n\t" \
    "v_mad_u32_u24 v200, v253, %[cs], v200\n\t" \
    "v_mad_u32_u24 v201, v253, %[cs], v201\n\t" \
    "v_lshrrev_b32 v203, 3, v252\n\t" \
    "v_bfe_u32 v253, v252, 7, 1\n\t" \
    "s_waitcnt lgkmcnt(0)\n\t" \
    "v_smfmac_f32_16x16x64_bf16 v[204:207], v[248:251], v[208:215], v255\n\t" \
    "v_and_or_b32 v201, v203, 8, v201\n\t" \
    "v_and_b32 v202, 0x06060606, v202\n\t" \
    "v_mad_u32_u24 v201, v253, %[cp], v201\n\t" \
    "v_xor_b32 v202, 0x01000504, v202\n\t" \
    "v_max_f32 v236, v232, v234\n\t" \
    "v_max_f32 v237, v233, v235\n\t" \
    "s_nop 1\n\t" \
    "v_permlane32_swap_b32 v204, v206\n\t" \
    "v_permlane32_swap_b32 v205, v207\n\t" \
    "v_add_f32 v204, v204, v206\n\t" \
    "v_add_f32 v205, v205, v207\n\t" \
    "v_max_f32 v236, v236, v204\n\t" \
    "v_max_f32 v237, v237, v205\n\t" \
    "s_nop 0\n\t" \
    "v_max_f32_dpp v236, v236, v236 quad_perm:[1,0,3,2] row_mask:0xf bank_mask:0xf\n\t" \
    "v_max_f32_dpp v237, v237, v237 quad_perm:[1,0,3,2] row_mask:0xf bank_mask:0xf\n\t" \
    "s_nop 0\n\t" \
    "v_max_f32_dpp v236, v236, v236 quad_perm:[2,3,0,1] row_mask:0xf bank_mask:0xf\n\t" \
    "v_max_f32_dpp v237, v237, v237 quad_perm:[2,3,0,1] row_mask:0xf bank_mask:0xf\n\t" \
    "s_nop 0\n\t" \
    "v_max_f32_dpp v236, v236, v236 row_half_mirror row_mask:0xf bank_mask:0xf\n\t" \
    "v_max_f32_dpp v237, v237, v237 row_half_mirror row_mask:0xf bank_mask:0xf\n\t" \
    "s_nop 0\n\t" \
    "v_max_f32_dpp v236, v236, v236 row_mirror row_mask:0xf bank_mask:0xf\n\t" \
    "v_max_f32_dpp v237, v237, v237 row_mirror row_mask:0xf bank_mask:0xf\n\t" \
    "v_mul_f32 v236, %[scl], v236\n\t" \
    "v_mul_f32 v237, %[scl], v237\n\t" \
    "v_fma_f32 v232, v232, %[scl], -v236\n\t" \
    "v_fma_f32 v233, v233, %[scl], -v237\n\t" \
    "v_fma_f32 v234, v234, %[scl], -v236\n\t" \
    "v_fma_f32 v235, v235, %[scl], -v237\n\t" \
    "v_exp_f32 v232, v232\n\t" \
    "v_exp_f32 v233, v233\n\t" \
    "v_fma_f32 v204, v204, %[scl], -v236\n\t" \
    "v_fma_f32 v205, v205, %[scl], -v237\n\t" \
    "v_exp_f32 v234, v234\n\t" \
    "v_exp_f32 v235, v235\n\t" \
    "v_exp_f32 v204, v204\n\t" \
    "v_exp_f32 v205, v205\n\t" \
    "v_add_f32 v238, v232, v234\n\t" \
    "v_add_f32 v239, v233, v235\n\t" \
    "v_fma_f32 v238, v204, 0.5, v238\n\t" \
    "v_fma_f32 v239, v205, 0.5, v239\n\t" \
    "s_nop 0\n\t" \
    "v_add_f32_dpp v238, v238, v238 quad_perm:[1,0,3,2] row_mask:0xf bank_mask:0xf\n\t" \
    "v_add_f32_dpp v239, v239, v239 quad_perm:[1,0,3,2] row_mask:0xf bank_mask:0xf\n\t" \
    "s_nop 0\n\t" \
    "v_add_f32_dpp v238, v238, v238 quad_perm:[2,3,0,1] row_mask:0xf bank_mask:0xf\n\t" \
    "v_add_f32_dpp v239, v239, v239 quad_perm:[2,3,0,1] row_mask:0xf bank_mask:0xf\n\t" \
    "s_nop 0\n\t" \
    "v_add_f32_dpp v238, v238, v238 row_half_mirror row_mask:0xf bank_mask:0xf\n\t" \
    "v_add_f32_dpp v239, v239, v239 row_half_mirror row_mask:0xf bank_mask:0xf\n\t" \
    "s_nop 0\n\t" \
    "v_add_f32_dpp v238, v238, v238 row_mirror row_mask:0xf bank_mask:0xf\n\t" \
    "v_add_f32_dpp v239, v239, v239 row_mirror row_mask:0xf bank_mask:0xf\n\t" \
    "v_rcp_f32 v238, v238\n\t" \
    "v_rcp_f32 v239, v239\n\t" \
    "s_nop 0\n\t" \
    "v_pk_mul_f32 v[232:233], v[232:233], v[238:239]\n\t" \
    "v_pk_mul_f32 v[234:235], v[234:235], v[238:239]\n\t" \
    "v_pk_mul_f32 v[204:205], v[204:205], v[238:239]\n\t" \
    "v_cvt_pk_bf16_f32 v232, |v232|, |v233|\n\t" \
    "v_cvt_pk_bf16_f32 v234, |v234|, |v235|\n\t" \
    "v_cvt_pk_bf16_f32 v204, |v204|, |v205|\n\t" \
    "v_mov_b32_dpp v233, v232 quad_perm:[1,0,3,2] row_mask:0xf bank_mask:0xf\n\t" \
    "v_mov_b32_dpp v235, v234 quad_perm:[1,0,3,2] row_mask:0xf bank_mask:0xf\n\t" \
    "v_mov_b32_dpp v205, v204 quad_perm:[1,0,3,2] row_mask:0xf bank_mask:0xf\n\t" \
    "v_perm_b32 v232, v232, v233, v202\n\t" \
    "v_perm_b32 v234, v234, v235, v202\n\t" \
    "v_perm_b32 v204, v204, v205, v202\n\t" \
    "v_mov_b32_dpp v240, v232 quad_perm:[0,1,0,1] row_mask:0xf bank_mask:0xf\n\t" \
    "v_mov_b32_dpp v241, v234 quad_perm:[0,1,0,1] row_mask:0xf bank_mask:0xf\n\t" \
    "v_mov_b32_dpp v242, v232 quad_perm:[2,3,2,3] row_mask:0xf bank_mask:0xf\n\t" \
    "v_mov_b32_dpp v243, v234 quad_perm:[2,3,2,3] row_mask:0xf bank_mask:0xf\n\t" \
    "buffer_store_dwordx4 v[240:243], v200, %[rr], %[sx] offen sc1\n\t" \
    "v_mov_b32_dpp v244, v204 quad_perm:[0,1,0,1] row_mask:0xf bank_mask:0xf\n\t" \
    "v_mov_b32_dpp v245, v204 quad_perm:[2,3,2,3] row_mask:0xf bank_mask:0xf\n\t" \
    "buffer_store_dwordx4 v[240:243], v200, %[rr], %[sy] offen sc1\n\t" \
    "buffer_store_dwordx2 v[244:245], v201, %[rr], %[sm] offen sc1\n\t" \
    "s_wakeup\n\t" \
    PRISM
#define ASM_P4_0 \
    "ds_read_b128 v[200:203], %[qa] offset:0\n\t" \
    "ds_read_b128 v[204:207], %[qa] offset:1024\n\t" \
    "ds_read_b128 v[208:211], %[qa] offset:2048\n\t" \
    "ds_read_b128 v[212:215], %[qa] offset:3072\n\t" \
    "ds_read_b128 v[216:219], %[qa] offset:4096\n\t" \
    "ds_read_b128 v[220:223], %[qa] offset:5120\n\t" \
    "ds_read_b128 v[224:227], %[qa] offset:12288\n\t" \
    "ds_read_b128 v[228:231], %[qa] offset:13312\n\t" \
    "ds_read_b128 v[232:235], %[qa] offset:14336\n\t" \
    "ds_read_b128 v[236:239], %[qa] offset:15360\n\t" \
    "s_waitcnt lgkmcnt(0)\n\t" \
    "s_barrier\n\t" ASM_ST \
    "v_mfma_f32_16x16x32_bf16 v[240:243], v[224:227], v[200:203], v[240:243]\n\t" \
    "ds_read_b128 v[224:227], %[qa] offset:16384\n\t" \
    "v_mfma_f32_16x16x32_bf16 v[240:243], v[228:231], v[204:207], v[240:243]\n\t" \
    "ds_read_b128 v[228:231], %[qa] offset:17408\n\t" \
    "v_mfma_f32_16x16x32_bf16 v[240:243], v[232:235], v[208:211], v[240:243]\n\t" \
    "ds_read_b128 v[232:235], %[km] offset:0\n\t" \
    "v_mfma_f32_16x16x32_bf16 v[240:243], v[236:239], v[212:215], v[240:243]\n\t" \
    "ds_read_b128 v[236:239], %[km] offset:512\n\t" \
    "s_waitcnt lgkmcnt(3)\n\t" \
    "v_mfma_f32_16x16x32_bf16 v[240:243], v[224:227], v[216:219], v[240:243]\n\t" \
    "ds_read_b128 v[224:227], %[km] offset:1024\n\t" \
    "s_waitcnt lgkmcnt(3)\n\t" \
    "v_mfma_f32_16x16x32_bf16 v[240:243], v[228:231], v[220:223], v[240:243]\n\t" \
    "ds_read_b128 v[228:231], %[km] offset:1536\n\t" \
    "s_waitcnt lgkmcnt(3)\n\t" \
    "v_mfma_f32_16x16x32_bf16 v[248:251], v[232:235], v[200:203], 0\n\t" \
    "ds_read_b128 v[232:235], %[km] offset:2048\n\t" \
    "s_waitcnt lgkmcnt(3)\n\t" \
    "v_mfma_f32_16x16x32_bf16 v[248:251], v[236:239], v[204:207], v[248:251]\n\t" \
    "ds_read_b128 v[236:239], %[km] offset:2560\n\t" \
    "s_waitcnt lgkmcnt(3)\n\t" \
    "v_mfma_f32_16x16x32_bf16 v[248:251], v[224:227], v[208:211], v[248:251]\n\t" \
    "s_waitcnt lgkmcnt(2)\n\t" \
    "v_mfma_f32_16x16x32_bf16 v[248:251], v[228:231], v[212:215], v[248:251]\n\t" \
    "s_waitcnt lgkmcnt(1)\n\t" \
    "v_mfma_f32_16x16x32_bf16 v[248:251], v[232:235], v[216:219], v[248:251]\n\t" \
    "s_waitcnt lgkmcnt(0)\n\t" \
    "v_mfma_f32_16x16x32_bf16 v[248:251], v[236:239], v[220:223], v[248:251]\n\t" \
    "v_max3_f32 v224, v240, v241, v242\n\t" \
    "v_mov_b32 v225, v243\n\t" \
    "v_lshlrev_b32 v201, 23, %[qa]\n\t" \
    "v_and_b32 v201, 0x80000000, v201\n\t" \
    "v_and_b32 v200, 0x3ff, %[qa]\n\t" \
    "v_or_b32 v200, v201, v200\n\t" \
    "s_nop 1\n\t" \
    "v_max3_f32 v224, v224, v248, v249\n\t" \
    "v_max3_f32 v225, v225, v250, v251\n\t" \
    "v_max_f32 v224, v224, v225\n\t" \
    "v_mov_b32 v225, v224\n\t" \
    "s_nop 1\n\t" \
    "v_permlane16_swap_b32 v224, v225\n\t" \
    "v_max_f32 v224, v224, v225\n\t" \
    "v_mov_b32 v225, v224\n\t" \
    "s_nop 1\n\t" \
    "v_permlane32_swap_b32 v224, v225\n\t" \
    "v_max_f32 v224, v224, v225\n\t" \
    "v_mul_f32 v224, %[scl], v224\n\t" \
    "v_fma_f32 v240, v240, %[scl], -v224\n\t" \
    "v_fma_f32 v241, v241, %[scl], -v224\n\t" \
    "v_exp_f32 v240, v240\n\t" \
    "v_fma_f32 v242, v242, %[scl], -v224\n\t" \
    "v_exp_f32 v241, v241\n\t" \
    "v_fma_f32 v243, v243, %[scl], -v224\n\t" \
    "v_exp_f32 v242, v242\n\t" \
    "v_fma_f32 v248, v248, %[scl], -v224\n\t" \
    "v_exp_f32 v243, v243\n\t" \
    "v_fma_f32 v249, v249, %[scl], -v224\n\t" \
    "v_exp_f32 v248, v248\n\t" \
    "v_fma_f32 v250, v250, %[scl], -v224\n\t" \
    "v_exp_f32 v249, v249\n\t" \
    "v_fma_f32 v251, v251, %[scl], -v224\n\t" \
    "v_exp_f32 v250, v250\n\t" \
    "v_exp_f32 v251, v251\n\t" \
    "v_add_f32 v224, v240, v241\n\t" \
    "v_add_f32 v225, v242, v243\n\t" \
    "v_add_f32 v232, v248, v249\n\t" \
    "v_add_f32 v233, v250, v251\n\t" \
    "v_add_f32 v224, v224, v225\n\t" \
    "v_add_f32 v232, v232, v233\n\t" \
    "v_fma_f32 v224, v232, 0.5, v224\n\t" \
    "v_mov_b32 v225, v224\n\t" \
    "s_nop 1\n\t" \
    "v_permlane16_swap_b32 v224, v225\n\t" \
    "v_add_f32 v224, v224, v225\n\t" \
    "v_mov_b32 v225, v224\n\t" \
    "s_nop 1\n\t" \
    "v_permlane32_swap_b32 v224, v225\n\t" \
    "v_add_f32 v224, v224, v225\n\t" \
    "v_rcp_f32 v224, v224\n\t" \
    "s_nop 0\n\t" \
    "v_mul_f32 v240, v240, v224\n\t" \
    "v_mul_f32 v241, v241, v224\n\t" \
    "v_mul_f32 v242, v242, v224\n\t" \
    "v_mul_f32 v243, v243, v224\n\t" \
    "v_mul_f32 v248, v248, v224\n\t" \
    "v_mul_f32 v249, v249, v224\n\t" \
    "v_mul_f32 v250, v250, v224\n\t" \
    "v_mul_f32 v251, v251, v224\n\t" \
    "v_cvt_pk_bf16_f32 v228, |v240|, |v241|\n\t" \
    "v_cvt_pk_bf16_f32 v229, |v242|, |v243|\n\t" \
    "v_cvt_pk_bf16_f32 v232, |v248|, |v249|\n\t" \
    "v_cvt_pk_bf16_f32 v233, |v250|, |v251|\n\t" \
    "v_permlane16_swap_b32 v228, v230\n\t" \
    "v_permlane16_swap_b32 v229, v231\n\t" \
    "buffer_store_dwordx4 v[228:231], v200, %[rr], %[so] offen sc1\n\t" \
    "buffer_store_dwordx2 v[232:233], %[pm], %[rr], %[so] offen sc1\n\t" \
    "s_nop 1\n\t"
#define ASM_P4_1 \
    "ds_read_b128 v[200:203], %[qa] offset:0\n\t" \
    "ds_read_b128 v[204:207], %[qa] offset:1024\n\t" \
    "ds_read_b128 v[208:211], %[qa] offset:2048\n\t" \
    "ds_read_b128 v[212:215], %[qa] offset:3072\n\t" \
    "ds_read_b128 v[216:219], %[qa] offset:4096\n\t" \
    "ds_read_b128 v[220:223], %[qa] offset:5120\n\t" \
    "ds_read_b128 v[224:227], %[qa] offset:12288\n\t" \
    "ds_read_b128 v[228:231], %[qa] offset:13312\n\t" \
    "ds_read_b128 v[232:235], %[qa] offset:14336\n\t" \
    "ds_read_b128 v[236:239], %[qa] offset:15360\n\t" \
    "s_waitcnt lgkmcnt(0)\n\t" \
    "s_barrier\n\t" ASM_ST \
    "v_mfma_f32_16x16x32_bf16 v[240:243], v[224:227], v[200:203], v[240:243]\n\t" \
    "ds_read_b128 v[224:227], %[qa] offset:16384\n\t" \
    "v_mfma_f32_16x16x32_bf16 v[240:243], v[228:231], v[204:207], v[240:243]\n\t" \
    "ds_read_b128 v[228:231], %[qa] offset:17408\n\t" \
    "v_mfma_f32_16x16x32_bf16 v[240:243], v[232:235], v[208:211], v[240:243]\n\t" \
    "ds_read_b128 v[232:235], %[qa] offset:6144\n\t" \
    "v_mfma_f32_16x16x32_bf16 v[240:243], v[236:239], v[212:215], v[240:243]\n\t" \
    "ds_read_b128 v[236:239], %[qa] offset:7168\n\t" \
    "s_waitcnt lgkmcnt(3)\n\t" \
    "v_mfma_f32_16x16x32_bf16 v[240:243], v[224:227], v[216:219], v[240:243]\n\t" \
    "ds_read_b128 v[224:227], %[qa] offset:8192\n\t" \
    "s_waitcnt lgkmcnt(3)\n\t" \
    "v_mfma_f32_16x16x32_bf16 v[240:243], v[228:231], v[220:223], v[240:243]\n\t" \
    "ds_read_b128 v[228:231], %[qa] offset:9216\n\t" \
    "s_waitcnt lgkmcnt(3)\n\t" \
    "v_mfma_f32_16x16x32_bf16 v[244:247], v[232:235], v[200:203], 0\n\t" \
    "ds_read_b128 v[232:235], %[qa] offset:10240\n\t" \
    "s_waitcnt lgkmcnt(3)\n\t" \
    "v_mfma_f32_16x16x32_bf16 v[244:247], v[236:239], v[204:207], v[244:247]\n\t" \
    "ds_read_b128 v[236:239], %[qa] offset:11264\n\t" \
    "s_waitcnt lgkmcnt(3)\n\t" \
    "v_mfma_f32_16x16x32_bf16 v[244:247], v[224:227], v[208:211], v[244:247]\n\t" \
    "ds_read_b128 v[224:227], %[km] offset:0\n\t" \
    "s_waitcnt lgkmcnt(3)\n\t" \
    "v_mfma_f32_16x16x32_bf16 v[244:247], v[228:231], v[212:215], v[244:247]\n\t" \
    "ds_read_b128 v[228:231], %[km] offset:512\n\t" \
    "s_waitcnt lgkmcnt(3)\n\t" \
    "v_mfma_f32_16x16x32_bf16 v[244:247], v[232:235], v[216:219], v[244:247]\n\t" \
    "ds_read_b128 v[232:235], %[km] offset:1024\n\t" \
    "s_waitcnt lgkmcnt(3)\n\t" \
    "v_mfma_f32_16x16x32_bf16 v[244:247], v[236:239], v[220:223], v[244:247]\n\t" \
    "ds_read_b128 v[236:239], %[km] offset:1536\n\t" \
    "s_waitcnt lgkmcnt(3)\n\t" \
    "v_mfma_f32_16x16x32_bf16 v[248:251], v[224:227], v[200:203], 0\n\t" \
    "ds_read_b128 v[224:227], %[km] offset:2048\n\t" \
    "s_waitcnt lgkmcnt(3)\n\t" \
    "v_mfma_f32_16x16x32_bf16 v[248:251], v[228:231], v[204:207], v[248:251]\n\t" \
    "ds_read_b128 v[228:231], %[km] offset:2560\n\t" \
    "s_waitcnt lgkmcnt(3)\n\t" \
    "v_mfma_f32_16x16x32_bf16 v[248:251], v[232:235], v[208:211], v[248:251]\n\t" \
    "s_waitcnt lgkmcnt(2)\n\t" \
    "v_mfma_f32_16x16x32_bf16 v[248:251], v[236:239], v[212:215], v[248:251]\n\t" \
    "s_waitcnt lgkmcnt(1)\n\t" \
    "v_mfma_f32_16x16x32_bf16 v[248:251], v[224:227], v[216:219], v[248:251]\n\t" \
    "s_waitcnt lgkmcnt(0)\n\t" \
    "v_mfma_f32_16x16x32_bf16 v[248:251], v[228:231], v[220:223], v[248:251]\n\t" \
    "v_max3_f32 v224, v240, v241, v242\n\t" \
    "v_max3_f32 v225, v244, v245, v246\n\t" \
    "v_max3_f32 v224, v224, v243, v247\n\t" \
    "v_and_b32 v200, 0x3ff, %[qa]\n\t" \
    "s_nop 3\n\t" \
    "v_max3_f32 v224, v224, v248, v249\n\t" \
    "v_max3_f32 v225, v225, v250, v251\n\t" \
    "v_max_f32 v224, v224, v225\n\t" \
    "v_mov_b32 v225, v224\n\t" \
    "s_nop 1\n\t" \
    "v_permlane16_swap_b32 v224, v225\n\t" \
    "v_max_f32 v224, v224, v225\n\t" \
    "v_mov_b32 v225, v224\n\t" \
    "s_nop 1\n\t" \
    "v_permlane32_swap_b32 v224, v225\n\t" \
    "v_max_f32 v224, v224, v225\n\t" \
    "v_mul_f32 v224, %[scl], v224\n\t" \
    "v_fma_f32 v240, v240, %[scl], -v224\n\t" \
    "v_fma_f32 v241, v241, %[scl], -v224\n\t" \
    "v_exp_f32 v240, v240\n\t" \
    "v_fma_f32 v242, v242, %[scl], -v224\n\t" \
    "v_exp_f32 v241, v241\n\t" \
    "v_fma_f32 v243, v243, %[scl], -v224\n\t" \
    "v_exp_f32 v242, v242\n\t" \
    "v_fma_f32 v244, v244, %[scl], -v224\n\t" \
    "v_exp_f32 v243, v243\n\t" \
    "v_fma_f32 v245, v245, %[scl], -v224\n\t" \
    "v_exp_f32 v244, v244\n\t" \
    "v_fma_f32 v246, v246, %[scl], -v224\n\t" \
    "v_exp_f32 v245, v245\n\t" \
    "v_fma_f32 v247, v247, %[scl], -v224\n\t" \
    "v_exp_f32 v246, v246\n\t" \
    "v_fma_f32 v248, v248, %[scl], -v224\n\t" \
    "v_exp_f32 v247, v247\n\t" \
    "v_fma_f32 v249, v249, %[scl], -v224\n\t" \
    "v_exp_f32 v248, v248\n\t" \
    "v_fma_f32 v250, v250, %[scl], -v224\n\t" \
    "v_exp_f32 v249, v249\n\t" \
    "v_fma_f32 v251, v251, %[scl], -v224\n\t" \
    "v_exp_f32 v250, v250\n\t" \
    "v_exp_f32 v251, v251\n\t" \
    "v_add_f32 v224, v240, v241\n\t" \
    "v_add_f32 v225, v242, v243\n\t" \
    "v_add_f32 v226, v244, v245\n\t" \
    "v_add_f32 v227, v246, v247\n\t" \
    "v_add_f32 v232, v248, v249\n\t" \
    "v_add_f32 v233, v250, v251\n\t" \
    "v_add_f32 v224, v224, v225\n\t" \
    "v_add_f32 v226, v226, v227\n\t" \
    "v_add_f32 v232, v232, v233\n\t" \
    "v_add_f32 v224, v224, v226\n\t" \
    "v_fma_f32 v224, v232, 0.5, v224\n\t" \
    "v_mov_b32 v225, v224\n\t" \
    "s_nop 1\n\t" \
    "v_permlane16_swap_b32 v224, v225\n\t" \
    "v_add_f32 v224, v224, v225\n\t" \
    "v_mov_b32 v225, v224\n\t" \
    "s_nop 1\n\t" \
    "v_permlane32_swap_b32 v224, v225\n\t" \
    "v_add_f32 v224, v224, v225\n\t" \
    "v_rcp_f32 v224, v224\n\t" \
    "s_nop 0\n\t" \
    "v_mul_f32 v240, v240, v224\n\t" \
    "v_mul_f32 v241, v241, v224\n\t" \
    "v_mul_f32 v242, v242, v224\n\t" \
    "v_mul_f32 v243, v243, v224\n\t" \
    "v_mul_f32 v244, v244, v224\n\t" \
    "v_mul_f32 v245, v245, v224\n\t" \
    "v_mul_f32 v246, v246, v224\n\t" \
    "v_mul_f32 v247, v247, v224\n\t" \
    "v_mul_f32 v248, v248, v224\n\t" \
    "v_mul_f32 v249, v249, v224\n\t" \
    "v_mul_f32 v250, v250, v224\n\t" \
    "v_mul_f32 v251, v251, v224\n\t" \
    "v_cvt_pk_bf16_f32 v230, |v240|, |v241|\n\t" \
    "v_cvt_pk_bf16_f32 v231, |v242|, |v243|\n\t" \
    "v_cvt_pk_bf16_f32 v228, |v244|, |v245|\n\t" \
    "v_cvt_pk_bf16_f32 v229, |v246|, |v247|\n\t" \
    "v_cvt_pk_bf16_f32 v232, |v248|, |v249|\n\t" \
    "v_cvt_pk_bf16_f32 v233, |v250|, |v251|\n\t" \
    "buffer_store_dwordx4 v[228:231], v200, %[rr], %[so] offen sc1\n\t" \
    "buffer_store_dwordx2 v[232:233], %[pm], %[rr], %[so] offen sc1\n\t" \
    "s_nop 1\n\t"
#define ASM_RCRIT \
    "v_mov_b64 v[176:177], -1\n\t" \
    "v_mov_b64 v[178:179], -1\n\t" \
    "v_mov_b64 v[180:181], -1\n\t" \
    "v_mov_b64 v[182:183], -1\n\t" \
    "v_mov_b64 v[184:185], -1\n\t" \
    "v_mov_b64 v[186:187], -1\n\t" \
    "v_mov_b64 v[188:189], -1\n\t" \
    "v_mov_b64 v[190:191], -1\n\t" \
    "v_mov_b64 v[192:193], -1\n\t" \
    "v_mov_b64 v[194:195], -1\n\t" \
    ".Lfa%=:\n\t" \
    "s_mov_b32 %[a], 0\n\t" \
    "v_max_u32 v196, v176, v177\n\t" \
    "v_max3_u32 v196, v178, v179, v196\n\t" \
    "v_cmp_eq_u32 vcc, -1, v196\n\t" \
    "s_cbranch_vccz .Lga0_%=\n\t" \
    "buffer_load_dwordx4 v[176:179], %[ov], %[rs], %[sv] offen sc1\n\t" \
    "s_mov_b32 %[a], 1\n\t" \
    ".Lga0_%=:\n\t" \
    "v_max_u32 v196, v180, v181\n\t" \
    "v_max3_u32 v196, v182, v183, v196\n\t" \
    "v_cmp_eq_u32 vcc, -1, v196\n\t" \
    "s_cbranch_vccz .Lga1_%=\n\t" \
    "buffer_load_dwordx4 v[180:183], %[ov], %[rs], %[sv] offen offset:1024 sc1\n\t" \
    "s_mov_b32 %[a], 1\n\t" \
    ".Lga1_%=:\n\t" \
    "s_cmp_eq_u32 %[a], 0\n\t" \
    "s_cbranch_scc1 .Lha%=\n\t" \
    "s_waitcnt vmcnt(12)\n\t" \
    ".Lia%=:\n\t" \
    "v_max_u32 v196, v176, v177\n\t" \
    "v_max3_u32 v196, v178, v179, v196\n\t" \
    "v_max3_u32 v196, v180, v181, v196\n\t" \
    "v_max3_u32 v196, v182, v183, v196\n\t" \
    "v_cmp_eq_u32 vcc, -1, v196\n\t" \
    "s_cbranch_vccz .Lha%=\n\t" \
    "s_nop 7\n\t" \
    "v_max_u32 v196, v176, v177\n\t" \
    "v_max3_u32 v196, v178, v179, v196\n\t" \
    "v_max3_u32 v196, v180, v181, v196\n\t" \
    "v_max3_u32 v196, v182, v183, v196\n\t" \
    "v_cmp_eq_u32 vcc, -1, v196\n\t" \
    "s_cbranch_vccz .Lha%=\n\t" \
    "s_nop 7\n\t" \
    "v_max_u32 v196, v176, v177\n\t" \
    "v_max3_u32 v196, v178, v179, v196\n\t" \
    "v_max3_u32 v196, v180, v181, v196\n\t" \
    "v_max3_u32 v196, v182, v183, v196\n\t" \
    "v_cmp_eq_u32 vcc, -1, v196\n\t" \
    "s_cbranch_vccz .Lha%=\n\t" \
    "s_nop 7\n\t" \
    "v_max_u32 v196, v176, v177\n\t" \
    "v_max3_u32 v196, v178, v179, v196\n\t" \
    "v_max3_u32 v196, v180, v181, v196\n\t" \
    "v_max3_u32 v196, v182, v183, v196\n\t" \
    "v_cmp_eq_u32 vcc, -1, v196\n\t" \
    "s_cbranch_vccz .Lha%=\n\t" \
    "s_nop 7\n\t" \
    "s_sub_u32 %[n], %[n], 1\n\t" \
    "s_cmp_gt_i32 %[n], 0\n\t" \
    "s_cbranch_scc1 .Lfa%=\n\t" \
    ".Lha%=:\n\t" \
    ASM_ST3 \
    "buffer_load_dwordx4 v[184:187], %[o0], %[rs], %[sp] offen sc1\n\t" \
    "buffer_load_dwordx4 v[188:191], %[o0], %[rs], %[sp] offen offset:1024 sc1\n\t" \
    "buffer_load_dwordx4 v[192:195], %[o2], %[rs], %[sp] offen sc1\n\t" \
    "s_waitcnt vmcnt(0)\n\t" \
    "v_max_u32 v196, v184, v185\n\t" \
    "v_max3_u32 v196, v186, v187, v196\n\t" \
    "v_max3_u32 v196, v188, v189, v196\n\t" \
    "v_max3_u32 v196, v190, v191, v196\n\t" \
    "v_max3_u32 v196, v192, v193, v196\n\t" \
    "v_max3_u32 v196, v194, v195, v196\n\t" \
    "v_cmp_eq_u32 vcc, -1, v196\n\t" \
    "s_cbranch_vccz .Lhb%=\n\t" \
    ".Lfb%=:\n\t" \
    "s_mov_b32 %[a], 0\n\t" \
    "v_max_u32 v196, v184, v185\n\t" \
    "v_max3_u32 v196, v186, v187, v196\n\t" \
    "v_cmp_eq_u32 vcc, -1, v196\n\t" \
    "s_cbranch_vccz .Lgb0_%=\n\t" \
    "buffer_load_dwordx4 v[184:187], %[o0], %[rs], %[sp] offen sc1\n\t" \
    "s_mov_b32 %[a], 1\n\t" \
    ".Lgb0_%=:\n\t" \
    "v_max_u32 v196, v188, v189\n\t" \
    "v_max3_u32 v196, v190, v191, v196\n\t" \
    "v_cmp_eq_u32 vcc, -1, v196\n\t" \
    "s_cbranch_vccz .Lgb1_%=\n\t" \
    "buffer_load_dwordx4 v[188:191], %[o0], %[rs], %[sp] offen offset:1024 sc1\n\t" \
    "s_mov_b32 %[a], 1\n\t" \
    ".Lgb1_%=:\n\t" \
    "v_max_u32 v196, v192, v193\n\t" \
    "v_max3_u32 v196, v194, v195, v196\n\t" \
    "v_cmp_eq_u32 vcc, -1, v196\n\t" \
    "s_cbranch_vccz .Lgb2_%=\n\t" \
    "buffer_load_dwordx4 v[192:195], %[o2], %[rs], %[sp] offen sc1\n\t" \
    "s_mov_b32 %[a], 1\n\t" \
    ".Lgb2_%=:\n\t" \
    "s_cmp_eq_u32 %[a], 0\n\t" \
    "s_cbranch_scc1 .Lhb%=\n\t" \
    "s_waitcnt vmcnt(12)\n\t" \
    ".Lib%=:\n\t" \
    "v_max_u32 v196, v184, v185\n\t" \
    "v_max3_u32 v196, v186, v187, v196\n\t" \
    "v_max3_u32 v196, v188, v189, v196\n\t" \
    "v_max3_u32 v196, v190, v191, v196\n\t" \
    "v_max3_u32 v196, v192, v193, v196\n\t" \
    "v_max3_u32 v196, v194, v195, v196\n\t" \
    "v_cmp_eq_u32 vcc, -1, v196\n\t" \
    "s_cbranch_vccz .Lhb%=\n\t" \
    "s_nop 7\n\t" \
    "v_max_u32 v196, v184, v185\n\t" \
    "v_max3_u32 v196, v186, v187, v196\n\t" \
    "v_max3_u32 v196, v188, v189, v196\n\t" \
    "v_max3_u32 v196, v190, v191, v196\n\t" \
    "v_max3_u32 v196, v192, v193, v196\n\t" \
    "v_max3_u32 v196, v194, v195, v196\n\t" \
    "v_cmp_eq_u32 vcc, -1, v196\n\t" \
    "s_cbranch_vccz .Lhb%=\n\t" \
    "s_nop 7\n\t" \
    "v_max_u32 v196, v184, v185\n\t" \
    "v_max3_u32 v196, v186, v187, v196\n\t" \
    "v_max3_u32 v196, v188, v189, v196\n\t" \
    "v_max3_u32 v196, v190, v191, v196\n\t" \
    "v_max3_u32 v196, v192, v193, v196\n\t" \
    "v_max3_u32 v196, v194, v195, v196\n\t" \
    "v_cmp_eq_u32 vcc, -1, v196\n\t" \
    "s_cbranch_vccz .Lhb%=\n\t" \
    "s_nop 7\n\t" \
    "v_max_u32 v196, v184, v185\n\t" \
    "v_max3_u32 v196, v186, v187, v196\n\t" \
    "v_max3_u32 v196, v188, v189, v196\n\t" \
    "v_max3_u32 v196, v190, v191, v196\n\t" \
    "v_max3_u32 v196, v192, v193, v196\n\t" \
    "v_max3_u32 v196, v194, v195, v196\n\t" \
    "v_cmp_eq_u32 vcc, -1, v196\n\t" \
    "s_cbranch_vccz .Lhb%=\n\t" \
    "s_nop 7\n\t" \
    "s_sub_u32 %[n], %[n], 1\n\t" \
    "s_cmp_gt_i32 %[n], 0\n\t" \
    "s_cbranch_scc1 .Lfb%=\n\t" \
    ".Lhb%=:\n\t" \
    ASM_ST2 \
    "v_mfma_f32_16x16x32_bf16 v[160:163], v[192:195], v[176:179], 0\n\t" \
    "v_mfma_f32_16x16x32_bf16 v[164:167], v[192:195], v[180:183], 0\n\t" \
    "v_smfmac_f32_16x16x64_bf16 v[160:163], v[184:187], v[128:135], %[sidx]\n\t" \
    "v_smfmac_f32_16x16x64_bf16 v[164:167], v[184:187], v[144:151], %[sidx]\n\t" \
    "v_smfmac_f32_16x16x64_bf16 v[160:163], v[188:191], v[136:143], %[sidx]\n\t" \
    "v_smfmac_f32_16x16x64_bf16 v[164:167], v[188:191], v[152:159], %[sidx]\n\t" \
    "s_cmp_eq_u32 %[pub], 0\n\t" \
    "s_nop 7\n\t" \
    "v_permlane32_swap_b32 v160, v164\n\t" \
    "v_permlane32_swap_b32 v161, v165\n\t" \
    "v_permlane32_swap_b32 v162, v166\n\t" \
    "v_permlane32_swap_b32 v163, v167\n\t" \
    "v_add_f32 v160, v160, v164\n\t" \
    "v_add_f32 v161, v161, v165\n\t" \
    "ds_write2_b32 %[rw], v160, v161 offset1:80\n\t" \
    "v_add_f32 v162, v162, v166\n\t" \
    "v_add_f32 v163, v163, v167\n\t" \
    "ds_write2_b32 %[rw], v162, v163 offset0:160 offset1:240\n\t" \
    "s_waitcnt lgkmcnt(0)\n\t" \
    "s_barrier\n\t" ASM_ST \
    "s_cbranch_scc1 .Lre%=\n\t" \
    "ds_read_b128 v[200:203], %[rd] offset:0\n\t" \
    "ds_read_b128 v[204:207], %[rd] offset:16\n\t" \
    "s_waitcnt lgkmcnt(1)\n\t" \
    "v_pk_add_f32 v[200:201], v[200:201], v[202:203]\n\t" \
    "s_waitcnt lgkmcnt(0)\n\t" \
    "v_pk_add_f32 v[204:205], v[204:205], v[206:207]\n\t" \
    "v_pk_add_f32 v[200:201], v[200:201], v[204:205]\n\t" \
    "s_nop 1\n\t" \
    "v_permlane32_swap_b32 v200, v201\n\t" \
    "v_add_f32 v202, v200, v201\n\t" \
    "v_add_f32 v203, v200, v201\n\t" \
    "s_nop 1\n\t" \
    "v_permlane32_swap_b32 v202, v203\n\t" \
    "v_cvt_pk_bf16_f32 v200, v202, v203\n\t" \
    "v_min_u32 v200, -2, v200\n\t" \
    "buffer_store_dword v200, %[oo], %[rs], %[so] offen sc1\n\t" \
    ".Lre%=:\n\t"
#define ASM_RX \
    "s_cmp_eq_u32 %[fin], 0\n\t" \
    "s_cbranch_scc1 .Lx0%=\n\t" \
    "buffer_load_dwordx2 v[250:251], %[xo], %[rx], %[sx] offen sc1\n\t" \
    ".Lx0%=:\n\t" \
    "buffer_load_dword v248, %[fo], %[rs], %[fs] offen sc1\n\t" \
    "buffer_load_dwordx4 v[200:203], %[l16], %[rs], %[s0] offen offset:0 sc1\n\t" \
    "buffer_load_dwordx4 v[204:207], %[l16], %[rs], %[s0] offen offset:1024 sc1\n\t" \
    "buffer_load_dwordx4 v[208:211], %[l16], %[rs], %[s0] offen offset:2048 sc1\n\t" \
    "buffer_load_dwordx4 v[212:215], %[l16], %[rs], %[s0] offen offset:3072 sc1\n\t" \
    "buffer_load_dwordx4 v[216:219], %[l16], %[rs], %[s1] offen offset:0 sc1\n\t" \
    "buffer_load_dwordx4 v[220:223], %[l16], %[rs], %[s1] offen offset:1024 sc1\n\t" \
    "buffer_load_dwordx4 v[224:227], %[l16], %[rs], %[s1] offen offset:2048 sc1\n\t" \
    "buffer_load_dwordx4 v[228:231], %[l16], %[rs], %[s1] offen offset:3072 sc1\n\t" \
    "v_mov_b64 v[96:97], v[128:129]\n\t" \
    "v_mov_b64 v[98:99], v[132:133]\n\t" \
    "v_mov_b64 v[100:101], v[136:137]\n\t" \
    "v_mov_b64 v[102:103], v[140:141]\n\t" \
    "v_mov_b64 v[104:105], v[144:145]\n\t" \
    "v_mov_b64 v[106:107], v[148:149]\n\t" \
    "v_mov_b64 v[108:109], v[152:153]\n\t" \
    "v_mov_b64 v[110:111], v[156:157]\n\t" \
    "s_movk_i32 %[n], 0x200\n\t" \
    "s_nop 1\n\t" \
    "v_permlane16_swap_b32 v96, v98\n\t" \
    "v_permlane16_swap_b32 v97, v99\n\t" \
    "v_permlane16_swap_b32 v100, v102\n\t" \
    "v_permlane16_swap_b32 v101, v103\n\t" \
    "v_permlane16_swap_b32 v104, v106\n\t" \
    "v_permlane16_swap_b32 v105, v107\n\t" \
    "v_permlane16_swap_b32 v108, v110\n\t" \
    "v_permlane16_swap_b32 v109, v111\n\t" \
    "s_waitcnt vmcnt(7)\n\t" \
    "v_max_u32 v196, v200, v201\n\t" \
    "v_max3_u32 v196, v202, v203, v196\n\t" \
    "s_waitcnt vmcnt(6)\n\t" \
    "v_max3_u32 v196, v204, v205, v196\n\t" \
    "v_max3_u32 v196, v206, v207, v196\n\t" \
    "s_waitcnt vmcnt(5)\n\t" \
    "v_max3_u32 v196, v208, v209, v196\n\t" \
    "v_max3_u32 v196, v210, v211, v196\n\t" \
    "s_waitcnt vmcnt(4)\n\t" \
    "v_max3_u32 v196, v212, v213, v196\n\t" \
    "v_max3_u32 v196, v214, v215, v196\n\t" \
    "s_waitcnt vmcnt(3)\n\t" \
    "v_max3_u32 v196, v216, v217, v196\n\t" \
    "v_max3_u32 v196, v218, v219, v196\n\t" \
    "s_waitcnt vmcnt(2)\n\t" \
    "v_max3_u32 v196, v220, v221, v196\n\t" \
    "v_max3_u32 v196, v222, v223, v196\n\t" \
    "s_waitcnt vmcnt(1)\n\t" \
    "v_max3_u32 v196, v224, v225, v196\n\t" \
    "v_max3_u32 v196, v226, v227, v196\n\t" \
    "s_waitcnt vmcnt(0)\n\t" \
    "v_max3_u32 v196, v228, v229, v196\n\t" \
    "v_max3_u32 v196, v230, v231, v196\n\t" \
    "v_cmp_eq_u32 vcc, -1, v196\n\t" \
    "s_cbranch_vccz .Lxd%=\n\t" \
    ".Lxl%=:\n\t" \
    "s_mov_b32 %[a], 0\n\t" \
    "v_max_u32 v196, v200, v201\n\t" \
    "v_max3_u32 v196, v202, v203, v196\n\t" \
    "v_cmp_eq_u32 vcc, -1, v196\n\t" \
    "s_cbranch_vccz .Lxs0_%=\n\t" \
    "buffer_load_dwordx4 v[200:203], %[l16], %[rs], %[s0] offen offset:0 sc1\n\t" \
    "s_mov_b32 %[a], 1\n\t" \
    ".Lxs0_%=:\n\t" \
    "v_max_u32 v196, v204, v205\n\t" \
    "v_max3_u32 v196, v206, v207, v196\n\t" \
    "v_cmp_eq_u32 vcc, -1, v196\n\t" \
    "s_cbranch_vccz .Lxs1_%=\n\t" \
    "buffer_load_dwordx4 v[204:207], %[l16], %[rs], %[s0] offen offset:1024 sc1\n\t" \
    "s_mov_b32 %[a], 1\n\t" \
    ".Lxs1_%=:\n\t" \
    "v_max_u32 v196, v208, v209\n\t" \
    "v_max3_u32 v196, v210, v211, v196\n\t" \
    "v_cmp_eq_u32 vcc, -1, v196\n\t" \
    "s_cbranch_vccz .Lxs2_%=\n\t" \
    "buffer_load_dwordx4 v[208:211], %[l16], %[rs], %[s0] offen offset:2048 sc1\n\t" \
    "s_mov_b32 %[a], 1\n\t" \
    ".Lxs2_%=:\n\t" \
    "v_max_u32 v196, v212, v213\n\t" \
    "v_max3_u32 v196, v214, v215, v196\n\t" \
    "v_cmp_eq_u32 vcc, -1, v196\n\t" \
    "s_cbranch_vccz .Lxs3_%=\n\t" \
    "buffer_load_dwordx4 v[212:215], %[l16], %[rs], %[s0] offen offset:3072 sc1\n\t" \
    "s_mov_b32 %[a], 1\n\t" \
    ".Lxs3_%=:\n\t" \
    "v_max_u32 v196, v216, v217\n\t" \
    "v_max3_u32 v196, v218, v219, v196\n\t" \
    "v_cmp_eq_u32 vcc, -1, v196\n\t" \
    "s_cbranch_vccz .Lxs4_%=\n\t" \
    "buffer_load_dwordx4 v[216:219], %[l16], %[rs], %[s1] offen offset:0 sc1\n\t" \
    "s_mov_b32 %[a], 1\n\t" \
    ".Lxs4_%=:\n\t" \
    "v_max_u32 v196, v220, v221\n\t" \
    "v_max3_u32 v196, v222, v223, v196\n\t" \
    "v_cmp_eq_u32 vcc, -1, v196\n\t" \
    "s_cbranch_vccz .Lxs5_%=\n\t" \
    "buffer_load_dwordx4 v[220:223], %[l16], %[rs], %[s1] offen offset:1024 sc1\n\t" \
    "s_mov_b32 %[a], 1\n\t" \
    ".Lxs5_%=:\n\t" \
    "v_max_u32 v196, v224, v225\n\t" \
    "v_max3_u32 v196, v226, v227, v196\n\t" \
    "v_cmp_eq_u32 vcc, -1, v196\n\t" \
    "s_cbranch_vccz .Lxs6_%=\n\t" \
    "buffer_load_dwordx4 v[224:227], %[l16], %[rs], %[s1] offen offset:2048 sc1\n\t" \
    "s_mov_b32 %[a], 1\n\t" \
    ".Lxs6_%=:\n\t" \
    "v_max_u32 v196, v228, v229\n\t" \
    "v_max3_u32 v196, v230, v231, v196\n\t" \
    "v_cmp_eq_u32 vcc, -1, v196\n\t" \
    "s_cbranch_vccz .Lxs7_%=\n\t" \
    "buffer_load_dwordx4 v[228:231], %[l16], %[rs], %[s1] offen offset:3072 sc1\n\t" \
    "s_mov_b32 %[a], 1\n\t" \
    ".Lxs7_%=:\n\t" \
    "s_cmp_eq_u32 %[a], 0\n\t" \
    "s_cbranch_scc1 .Lxd%=\n\t" \
    "s_waitcnt vmcnt(0)\n\t" \
    "s_sub_u32 %[n], %[n], 1\n\t" \
    "s_cmp_lg_u32 %[n], 0\n\t" \
    "s_cbranch_scc1 .Lxl%=\n\t" \
    "s_branch .Lxe%=\n\t" \
    ".Lxd%=:\n\t" \
    ASM_ST2 \
    "v_mfma_f32_16x16x32_bf16 v[120:123], v[128:131], v[200:203], 0\n\t" \
    "v_mfma_f32_16x16x32_bf16 v[124:127], v[144:147], v[200:203], 0\n\t" \
    "v_mfma_f32_16x16x32_bf16 v[120:123], v[132:135], v[204:207], v[120:123]\n\t" \
    "v_mfma_f32_16x16x32_bf16 v[124:127], v[148:151], v[204:207], v[124:127]\n\t" \
    "v_mfma_f32_16x16x32_bf16 v[120:123], v[136:139], v[208:211], v[120:123]\n\t" \
    "v_mfma_f32_16x16x32_bf16 v[124:127], v[152:155], v[208:211], v[124:127]\n\t" \
    "v_mfma_f32_16x16x32_bf16 v[120:123], v[140:143], v[212:215], v[120:123]\n\t" \
    "v_mfma_f32_16x16x32_bf16 v[124:127], v[156:159], v[212:215], v[124:127]\n\t" \
    "v_mfma_f32_16x16x32_bf16 v[112:115], v[96:99], v[220:223], 0\n\t" \
    "v_mfma_f32_16x16x32_bf16 v[116:119], v[104:107], v[220:223], 0\n\t" \
    "v_mfma_f32_16x16x32_bf16 v[120:123], v[176:179], v[216:219], v[120:123]\n\t" \
    "v_mfma_f32_16x16x32_bf16 v[124:127], v[180:183], v[216:219], v[124:127]\n\t" \
    "v_mfma_f32_16x16x32_bf16 v[112:115], v[100:103], v[224:227], v[112:115]\n\t" \
    "v_mfma_f32_16x16x32_bf16 v[116:119], v[108:111], v[224:227], v[116:119]\n\t" \
    "v_mfma_f32_16x16x32_bf16 v[112:115], v[176:179], v[228:231], v[112:115]\n\t" \
    "v_mfma_f32_16x16x32_bf16 v[116:119], v[180:183], v[228:231], v[116:119]\n\t" \
    "s_nop 1\n\t" \
    "ds_write_b128 %[pw], v[120:123] offset:16384\n\t" \
    "ds_write_b128 %[pw], v[124:127] offset:24576\n\t" \
    "s_nop 3\n\t" \
    "ds_write_b128 %[pw], v[112:115] offset:0\n\t" \
    "ds_write_b128 %[pw], v[116:119] offset:8192\n\t" \
    "v_cmp_ge_u32 vcc, %[tn], v248\n\t" \
    "s_mov_b32 %[a], 0\n\t" \
    "s_cbranch_vccnz .Lxv%=\n\t" \
    "buffer_load_dwordx4 v[128:131], %[l16], %[rp], %[sv0] offen offset:0 sc1\n\t" \
    "buffer_load_dwordx4 v[132:135], %[l16], %[rp], %[sv0] offen offset:1024 sc1\n\t" \
    "buffer_load_dwordx4 v[136:139], %[l16], %[rp], %[sv0] offen offset:2048 sc1\n\t" \
    "buffer_load_dwordx4 v[140:143], %[l16], %[rp], %[sv0] offen offset:3072 sc1\n\t" \
    "buffer_load_dwordx4 v[144:147], %[l16], %[rp], %[sv1] offen offset:0 sc1\n\t" \
    "buffer_load_dwordx4 v[148:151], %[l16], %[rp], %[sv1] offen offset:1024 sc1\n\t" \
    "buffer_load_dwordx4 v[152:155], %[l16], %[rp], %[sv1] offen offset:2048 sc1\n\t" \
    "buffer_load_dwordx4 v[156:159], %[l16], %[rp], %[sv1] offen offset:3072 sc1\n\t" \
    "s_mov_b32 %[a], 1\n\t" \
    ".Lxv%=:\n\t" \
    "s_cmp_eq_u32 %[fin], 0\n\t" \
    "s_waitcnt lgkmcnt(0)\n\t" \
    "s_barrier\n\t" ASM_ST \
    "s_cbranch_scc1 .Lxe%=\n\t" \
    "ds_read_b128 v[200:203], %[pr] offset:0\n\t" \
    "ds_read_b128 v[204:207], %[pr] offset:1024\n\t" \
    "ds_read_b128 v[208:211], %[pr] offset:2048\n\t" \
    "ds_read_b128 v[212:215], %[pr] offset:3072\n\t" \
    "ds_read_b128 v[216:219], %[pr] offset:4096\n\t" \
    "ds_read_b128 v[220:223], %[pr] offset:5120\n\t" \
    "ds_read_b128 v[224:227], %[pr] offset:6144\n\t" \
    "ds_read_b128 v[228:231], %[pr] offset:7168\n\t" \
    "v_lshlrev_b32 v240, 16, v250\n\t" \
    "v_and_b32 v241, 0xffff0000, v250\n\t" \
    "v_lshlrev_b32 v242, 16, v251\n\t" \
    "v_and_b32 v243, 0xffff0000, v251\n\t" \
    "s_waitcnt lgkmcnt(6)\n\t" \
    "v_pk_add_f32 v[200:201], v[200:201], v[204:205]\n\t" \
    "v_pk_add_f32 v[202:203], v[202:203], v[206:207]\n\t" \
    "s_waitcnt lgkmcnt(4)\n\t" \
    "v_pk_add_f32 v[208:209], v[208:209], v[212:213]\n\t" \
    "v_pk_add_f32 v[210:211], v[210:211], v[214:215]\n\t" \
    "v_pk_add_f32 v[200:201], v[200:201], v[208:209]\n\t" \
    "v_pk_add_f32 v[202:203], v[202:203], v[210:211]\n\t" \
    "s_waitcnt lgkmcnt(2)\n\t" \
    "v_pk_add_f32 v[216:217], v[216:217], v[220:221]\n\t" \
    "v_pk_add_f32 v[218:219], v[218:219], v[222:223]\n\t" \
    "s_waitcnt lgkmcnt(0)\n\t" \
    "v_pk_add_f32 v[224:225], v[224:225], v[228:229]\n\t" \
    "v_pk_add_f32 v[226:227], v[226:227], v[230:231]\n\t" \
    "v_pk_add_f32 v[216:217], v[216:217], v[224:225]\n\t" \
    "v_pk_add_f32 v[218:219], v[218:219], v[226:227]\n\t" \
    "v_pk_add_f32 v[200:201], v[200:201], v[216:217]\n\t" \
    "v_pk_add_f32 v[202:203], v[202:203], v[218:219]\n\t" \
    "v_pk_add_f32 v[200:201], v[200:201], v[240:241]\n\t" \
    "v_pk_add_f32 v[202:203], v[202:203], v[242:243]\n\t" \
    "v_cvt_pk_bf16_f32 v200, v200, v201\n\t" \
    "v_cvt_pk_bf16_f32 v201, v202, v203\n\t" \
    "v_min_u32 v200, -2, v200\n\t" \
    "v_min_u32 v201, -2, v201\n\t" \
    "buffer_store_dwordx2 v[200:201], %[xo], %[rd], %[sa] offen sc1\n\t" \
    ".Lxe%=:\n\t"
#define ASM_W4_M \
    ".Lc7_%=:\n\t" \
    "s_waitcnt vmcnt(0)\n\t" \
    "v_max_u32 v212, v188, v189\n\t" \
    "v_max3_u32 v212, v190, v191, v212\n\t" \
    "v_cmp_eq_u32 vcc, -1, v212\n\t" \
    "s_cbranch_vccnz .Ls7_%=\n\t" \
    ".Lk7_%=:\n\t" \
    ASM_ST2 \
    "buffer_load_dwordx4 v[184:187], %[l16], %[rs], %[s1] offen offset:2048 sc1\n\t" \
    "buffer_load_dwordx4 v[180:183], %[l16], %[rs], %[s1] offen offset:1024 sc1\n\t" \
    "buffer_load_dwordx4 v[176:179], %[l16], %[rs], %[s1] offen offset:0 sc1\n\t" \
    "buffer_load_dwordx4 v[172:175], %[l16], %[rs], %[s0] offen offset:3072 sc1\n\t" \
    "buffer_load_dwordx4 v[168:171], %[l16], %[rs], %[s0] offen offset:2048 sc1\n\t" \
    "buffer_load_dwordx4 v[164:167], %[l16], %[rs], %[s0] offen offset:1024 sc1\n\t" \
    "buffer_load_dwordx4 v[160:163], %[l16], %[rs], %[s0] offen offset:0 sc1\n\t" \
    "v_mfma_f32_16x16x32_bf16 v[192:195], v[28:31], v[188:191], 0\n\t" \
    "v_mfma_f32_16x16x32_bf16 v[196:199], v[60:63], v[188:191], 0\n\t" \
    "v_mfma_f32_16x16x32_bf16 v[200:203], v[92:95], v[188:191], 0\n\t" \
    "v_mfma_f32_16x16x32_bf16 v[204:207], v[124:127], v[188:191], 0\n\t" \
    "v_mfma_f32_16x16x32_bf16 v[208:211], v[156:159], v[188:191], 0\n\t" \
    ".Lc6_%=:\n\t" \
    "s_waitcnt vmcnt(6)\n\t" \
    "v_max_u32 v212, v184, v185\n\t" \
    "v_max3_u32 v212, v186, v187, v212\n\t" \
    "v_cmp_eq_u32 vcc, -1, v212\n\t" \
    "s_cbranch_vccnz .Ls6_%=\n\t" \
    ".Lk6_%=:\n\t" \
    "v_mfma_f32_16x16x32_bf16 v[192:195], v[24:27], v[184:187], v[192:195]\n\t" \
    "v_mfma_f32_16x16x32_bf16 v[196:199], v[56:59], v[184:187], v[196:199]\n\t" \
    "v_mfma_f32_16x16x32_bf16 v[200:203], v[88:91], v[184:187], v[200:203]\n\t" \
    "v_mfma_f32_16x16x32_bf16 v[204:207], v[120:123], v[184:187], v[204:207]\n\t" \
    "v_mfma_f32_16x16x32_bf16 v[208:211], v[152:155], v[184:187], v[208:211]\n\t" \
    ".Lc5_%=:\n\t" \
    "s_waitcnt vmcnt(5)\n\t" \
    "v_max_u32 v212, v180, v181\n\t" \
    "v_max3_u32 v212, v182, v183, v212\n\t" \
    "v_cmp_eq_u32 vcc, -1, v212\n\t" \
    "s_cbranch_vccnz .Ls5_%=\n\t" \
    ".Lk5_%=:\n\t" \
    "v_mfma_f32_16x16x32_bf16 v[192:195], v[20:23], v[180:183], v[192:195]\n\t" \
    "v_mfma_f32_16x16x32_bf16 v[196:199], v[52:55], v[180:183], v[196:199]\n\t" \
    "v_mfma_f32_16x16x32_bf16 v[200:203], v[84:87], v[180:183], v[200:203]\n\t" \
    "v_mfma_f32_16x16x32_bf16 v[204:207], v[116:119], v[180:183], v[204:207]\n\t" \
    "v_mfma_f32_16x16x32_bf16 v[208:211], v[148:151], v[180:183], v[208:211]\n\t" \
    ".Lc4_%=:\n\t" \
    "s_waitcnt vmcnt(4)\n\t" \
    "v_max_u32 v212, v176, v177\n\t" \
    "v_max3_u32 v212, v178, v179, v212\n\t" \
    "v_cmp_eq_u32 vcc, -1, v212\n\t" \
    "s_cbranch_vccnz .Ls4_%=\n\t" \
    ".Lk4_%=:\n\t" \
    "v_mfma_f32_16x16x32_bf16 v[192:195], v[16:19], v[176:179], v[192:195]\n\t" \
    "v_mfma_f32_16x16x32_bf16 v[196:199], v[48:51], v[176:179], v[196:199]\n\t" \
    "v_mfma_f32_16x16x32_bf16 v[200:203], v[80:83], v[176:179], v[200:203]\n\t" \
    "v_mfma_f32_16x16x32_bf16 v[204:207], v[112:115], v[176:179], v[204:207]\n\t" \
    "v_mfma_f32_16x16x32_bf16 v[208:211], v[144:147], v[176:179], v[208:211]\n\t" \
    ".Lc3_%=:\n\t" \
    "s_waitcnt vmcnt(3)\n\t" \
    "v_max_u32 v212, v172, v173\n\t" \
    "v_max3_u32 v212, v174, v175, v212\n\t" \
    "v_cmp_eq_u32 vcc, -1, v212\n\t" \
    "s_cbranch_vccnz .Ls3_%=\n\t" \
    ".Lk3_%=:\n\t" \
    "v_mfma_f32_16x16x32_bf16 v[192:195], v[12:15], v[172:175], v[192:195]\n\t" \
    "v_mfma_f32_16x16x32_bf16 v[196:199], v[44:47], v[172:175], v[196:199]\n\t" \
    "v_mfma_f32_16x16x32_bf16 v[200:203], v[76:79], v[172:175], v[200:203]\n\t" \
    "v_mfma_f32_16x16x32_bf16 v[204:207], v[108:111], v[172:175], v[204:207]\n\t" \
    "v_mfma_f32_16x16x32_bf16 v[208:211], v[140:143], v[172:175], v[208:211]\n\t" \
    ".Lc2_%=:\n\t" \
    "s_waitcnt vmcnt(2)\n\t" \
    "v_max_u32 v212, v168, v169\n\t" \
    "v_max3_u32 v212, v170, v171, v212\n\t" \
    "v_cmp_eq_u32 vcc, -1, v212\n\t" \
    "s_cbranch_vccnz .Ls2_%=\n\t" \
    ".Lk2_%=:\n\t" \
    "buffer_load_dwordx4 v[188:191], %[l16], %[rs], %[n1] offen offset:3072 sc1\n\t" \
    "v_mfma_f32_16x16x32_bf16 v[192:195], v[8:11], v[168:171], v[192:195]\n\t" \
    "v_mfma_f32_16x16x32_bf16 v[196:199], v[40:43], v[168:171], v[196:199]\n\t" \
    "v_mfma_f32_16x16x32_bf16 v[200:203], v[72:75], v[168:171], v[200:203]\n\t" \
    "v_mfma_f32_16x16x32_bf16 v[204:207], v[104:107], v[168:171], v[204:207]\n\t" \
    "v_mfma_f32_16x16x32_bf16 v[208:211], v[136:139], v[168:171], v[208:211]\n\t" \
    ".Lc1_%=:\n\t" \
    "s_waitcnt vmcnt(2)\n\t" \
    "v_max_u32 v212, v164, v165\n\t" \
    "v_max3_u32 v212, v166, v167, v212\n\t" \
    "v_cmp_eq_u32 vcc, -1, v212\n\t" \
    "s_cbranch_vccnz .Ls1_%=\n\t" \
    ".Lk1_%=:\n\t" \
    "v_mfma_f32_16x16x32_bf16 v[192:195], v[4:7], v[164:167], v[192:195]\n\t" \
    "v_mfma_f32_16x16x32_bf16 v[196:199], v[36:39], v[164:167], v[196:199]\n\t" \
    "v_mfma_f32_16x16x32_bf16 v[200:203], v[68:71], v[164:167], v[200:203]\n\t" \
    "v_mfma_f32_16x16x32_bf16 v[204:207], v[100:103], v[164:167], v[204:207]\n\t" \
    "v_mfma_f32_16x16x32_bf16 v[208:211], v[132:135], v[164:167], v[208:211]\n\t" \
    ".Lc0_%=:\n\t" \
    "s_waitcnt vmcnt(1)\n\t" \
    "v_max_u32 v212, v160, v161\n\t" \
    "v_max3_u32 v212, v162, v163, v212\n\t" \
    "v_cmp_eq_u32 vcc, -1, v212\n\t" \
    "s_cbranch_vccnz .Ls0_%=\n\t" \
    ".Lk0_%=:\n\t" \
    "v_mfma_f32_16x16x32_bf16 v[192:195], v[0:3], v[160:163], v[192:195]\n\t" \
    "v_mfma_f32_16x16x32_bf16 v[196:199], v[32:35], v[160:163], v[196:199]\n\t" \
    "v_mfma_f32_16x16x32_bf16 v[200:203], v[64:67], v[160:163], v[200:203]\n\t" \
    "v_mfma_f32_16x16x32_bf16 v[204:207], v[96:99], v[160:163], v[204:207]\n\t" \
    "v_mfma_f32_16x16x32_bf16 v[208:211], v[128:131], v[160:163], v[208:211]\n\t" \
    "v_mov_b32 v212, %[un]\n\t" \
    "ds_write_b128 %[ld], v[192:195] offset:0\n\t" \
    "ds_write_b128 %[ld], v[196:199] offset:6144\n\t" \
    "ds_write_b128 %[ld], v[200:203] offset:12288\n\t" \
    "s_nop 0\n\t" \
    "ds_write_b128 %[ld], v[204:207] offset:18432\n\t" \
    "s_nop 1\n\t" \
    "ds_write_b128 %[ld], v[208:211] offset:24576\n\t" \
    "ds_write_b32 %[fl], v212\n\t" \
    "s_branch .Lme%=\n\t" \
    ".Ls7_%=:\n\t" \
    "s_cmp_eq_u32 %[n], 0\n\t" \
    "s_cbranch_scc1 .Lk7_%=\n\t" \
    "s_sub_u32 %[n], %[n], 1\n\t" \
    "s_sleep 1\n\t" \
    "buffer_load_dwordx4 v[188:191], %[l16], %[rs], %[s1] offen offset:3072 sc1\n\t" \
    "s_branch .Lc7_%=\n\t" \
    ".Ls6_%=:\n\t" \
    "s_cmp_eq_u32 %[n], 0\n\t" \
    "s_cbranch_scc1 .Lk6_%=\n\t" \
    "s_sub_u32 %[n], %[n], 1\n\t" \
    "buffer_load_dwordx4 v[184:187], %[l16], %[rs], %[s1] offen offset:2048 sc1\n\t" \
    "s_waitcnt vmcnt(0)\n\t" \
    "s_branch .Lc6_%=\n\t" \
    ".Ls5_%=:\n\t" \
    "s_cmp_eq_u32 %[n], 0\n\t" \
    "s_cbranch_scc1 .Lk5_%=\n\t" \
    "s_sub_u32 %[n], %[n], 1\n\t" \
    "buffer_load_dwordx4 v[180:183], %[l16], %[rs], %[s1] offen offset:1024 sc1\n\t" \
    "s_waitcnt vmcnt(0)\n\t" \
    "s_branch .Lc5_%=\n\t" \
    ".Ls4_%=:\n\t" \
    "s_cmp_eq_u32 %[n], 0\n\t" \
    "s_cbranch_scc1 .Lk4_%=\n\t" \
    "s_sub_u32 %[n], %[n], 1\n\t" \
    "buffer_load_dwordx4 v[176:179], %[l16], %[rs], %[s1] offen offset:0 sc1\n\t" \
    "s_waitcnt vmcnt(0)\n\t" \
    "s_branch .Lc4_%=\n\t" \
    ".Ls3_%=:\n\t" \
    "s_cmp_eq_u32 %[n], 0\n\t" \
    "s_cbranch_scc1 .Lk3_%=\n\t" \
    "s_sub_u32 %[n], %[n], 1\n\t" \
    "buffer_load_dwordx4 v[172:175], %[l16], %[rs], %[s0] offen offset:3072 sc1\n\t" \
    "s_waitcnt vmcnt(0)\n\t" \
    "s_branch .Lc3_%=\n\t" \
    ".Ls2_%=:\n\t" \
    "s_cmp_eq_u32 %[n], 0\n\t" \
    "s_cbranch_scc1 .Lk2_%=\n\t" \
    "s_sub_u32 %[n], %[n], 1\n\t" \
    "buffer_load_dwordx4 v[168:171], %[l16], %[rs], %[s0] offen offset:2048 sc1\n\t" \
    "s_waitcnt vmcnt(0)\n\t" \
    "s_branch .Lc2_%=\n\t" \
    ".Ls1_%=:\n\t" \
    "s_cmp_eq_u32 %[n], 0\n\t" \
    "s_cbranch_scc1 .Lk1_%=\n\t" \
    "s_sub_u32 %[n], %[n], 1\n\t" \
    "buffer_load_dwordx4 v[164:167], %[l16], %[rs], %[s0] offen offset:1024 sc1\n\t" \
    "s_waitcnt vmcnt(0)\n\t" \
    "s_branch .Lc1_%=\n\t" \
    ".Ls0_%=:\n\t" \
    "s_cmp_eq_u32 %[n], 0\n\t" \
    "s_cbranch_scc1 .Lk0_%=\n\t" \
    "s_sub_u32 %[n], %[n], 1\n\t" \
    "buffer_load_dwordx4 v[160:163], %[l16], %[rs], %[s0] offen offset:0 sc1\n\t" \
    "s_waitcnt vmcnt(0)\n\t" \
    "s_branch .Lc0_%=\n\t" \
    ".Lme%=:\n\t"
#define ASM_W4F_0 \
    "s_branch .Lf0%=\n\t" \
    ".Lfr%=:\n\t" \
    "s_cmp_eq_u32 %[n], 0\n\t" \
    "s_cbranch_scc1 .Lfb%=\n\t" \
    "s_sub_u32 %[n], %[n], 1\n\t" \
    "s_sleep 6\n\t" \
    "buffer_load_dwordx2 v[164:165], %[o0], %[rd], %[c1] offen sc1\n\t" \
    "buffer_load_dwordx2 v[166:167], %[o1], %[rd], %[c1] offen sc1\n\t" \
    "buffer_load_dwordx2 v[168:169], %[o2], %[rd], %[c1] offen sc1\n\t" \
    "buffer_load_dwordx2 v[170:171], %[o3], %[rd], %[c1] offen sc1\n\t" \
    "buffer_load_dwordx2 v[172:173], %[o4], %[rd], %[c1] offen sc1\n\t" \
    "buffer_load_dwordx2 v[174:175], %[o0], %[rd], %[ca] offen sc1\n\t" \
    "buffer_load_dwordx2 v[176:177], %[o1], %[rd], %[ca] offen sc1\n\t" \
    "buffer_load_dwordx2 v[178:179], %[o2], %[rd], %[ca] offen sc1\n\t" \
    "buffer_load_dwordx2 v[180:181], %[o3], %[rd], %[ca] offen sc1\n\t" \
    "buffer_load_dwordx2 v[182:183], %[o4], %[rd], %[ca] offen sc1\n\t" \
    "s_waitcnt vmcnt(0)\n\t" \
    "s_branch .Lfa%=\n\t" \
    ".Lfs0_%=:\n\t" \
    "s_cmp_eq_u32 %[n], 0\n\t" \
    "s_cbranch_scc1 .Lfu0_%=\n\t" \
    "s_sub_u32 %[n], %[n], 1\n\t" \
    "s_sleep 1\n\t" \
    "s_branch .Lft0_%=\n\t" \
    ".Lfs1_%=:\n\t" \
    "s_cmp_eq_u32 %[n], 0\n\t" \
    "s_cbranch_scc1 .Lfu1_%=\n\t" \
    "s_sub_u32 %[n], %[n], 1\n\t" \
    "s_sleep 1\n\t" \
    "s_branch .Lft1_%=\n\t" \
    ".Lfx%=:\n\t" \
    "s_cmp_eq_u32 %[n], 0\n\t" \
    "s_cbranch_scc1 .Lfz%=\n\t" \
    "s_sub_u32 %[n], %[n], 1\n\t" \
    "s_branch .Lfy%=\n\t" \
    ".Lf0%=:\n\t" \
    "s_waitcnt vmcnt(1)\n\t" \
    ".Lfa%=:\n\t" \
    "v_max_u32 v194, v164, v165\n\t" \
    "v_max3_u32 v194, v166, v167, v194\n\t" \
    "v_max3_u32 v194, v168, v169, v194\n\t" \
    "v_max3_u32 v194, v170, v171, v194\n\t" \
    "v_max3_u32 v194, v172, v173, v194\n\t" \
    "v_max3_u32 v194, v174, v175, v194\n\t" \
    "v_max3_u32 v194, v176, v177, v194\n\t" \
    "v_max3_u32 v194, v178, v179, v194\n\t" \
    "v_max3_u32 v194, v180, v181, v194\n\t" \
    "v_max3_u32 v194, v182, v183, v194\n\t" \
    "v_cmp_eq_u32 vcc, -1, v194\n\t" \
    "s_cbranch_vccnz .Lfr%=\n\t" \
    ".Lfb%=:\n\t" \
    "v_lshlrev_b32 v64, 16, v164\n\t" \
    "v_and_b32 v65, 0xffff0000, v164\n\t" \
    "v_lshlrev_b32 v84, 16, v174\n\t" \
    "v_and_b32 v85, 0xffff0000, v174\n\t" \
    "v_lshlrev_b32 v66, 16, v165\n\t" \
    "v_and_b32 v67, 0xffff0000, v165\n\t" \
    "v_lshlrev_b32 v86, 16, v175\n\t" \
    "v_and_b32 v87, 0xffff0000, v175\n\t" \
    "v_lshlrev_b32 v68, 16, v166\n\t" \
    "v_and_b32 v69, 0xffff0000, v166\n\t" \
    "v_lshlrev_b32 v88, 16, v176\n\t" \
    "v_and_b32 v89, 0xffff0000, v176\n\t" \
    "v_lshlrev_b32 v70, 16, v167\n\t" \
    "v_and_b32 v71, 0xffff0000, v167\n\t" \
    "v_lshlrev_b32 v90, 16, v177\n\t" \
    "v_and_b32 v91, 0xffff0000, v177\n\t" \
    "v_lshlrev_b32 v72, 16, v168\n\t" \
    "v_and_b32 v73, 0xffff0000, v168\n\t" \
    "v_lshlrev_b32 v92, 16, v178\n\t" \
    "v_and_b32 v93, 0xffff0000, v178\n\t" \
    "v_lshlrev_b32 v74, 16, v169\n\t" \
    "v_and_b32 v75, 0xffff0000, v169\n\t" \
    "v_lshlrev_b32 v94, 16, v179\n\t" \
    "v_and_b32 v95, 0xffff0000, v179\n\t" \
    "v_lshlrev_b32 v76, 16, v170\n\t" \
    "v_and_b32 v77, 0xffff0000, v170\n\t" \
    "v_lshlrev_b32 v96, 16, v180\n\t" \
    "v_and_b32 v97, 0xffff0000, v180\n\t" \
    "v_lshlrev_b32 v78, 16, v171\n\t" \
    "v_and_b32 v79, 0xffff0000, v171\n\t" \
    "v_lshlrev_b32 v98, 16, v181\n\t" \
    "v_and_b32 v99, 0xffff0000, v181\n\t" \
    "v_lshlrev_b32 v80, 16, v172\n\t" \
    "v_and_b32 v81, 0xffff0000, v172\n\t" \
    "v_lshlrev_b32 v100, 16, v182\n\t" \
    "v_and_b32 v101, 0xffff0000, v182\n\t" \
    "v_lshlrev_b32 v82, 16, v173\n\t" \
    "v_and_b32 v83, 0xffff0000, v173\n\t" \
    "v_lshlrev_b32 v102, 16, v183\n\t" \
    "v_and_b32 v103, 0xffff0000, v183\n\t" \
    "v_pk_add_f32 v[64:65], v[64:65], v[84:85]\n\t" \
    "v_pk_add_f32 v[66:67], v[66:67], v[86:87]\n\t" \
    "v_pk_add_f32 v[68:69], v[68:69], v[88:89]\n\t" \
    "v_pk_add_f32 v[70:71], v[70:71], v[90:91]\n\t" \
    "v_pk_add_f32 v[72:73], v[72:73], v[92:93]\n\t" \
    "v_pk_add_f32 v[74:75], v[74:75], v[94:95]\n\t" \
    "v_pk_add_f32 v[76:77], v[76:77], v[96:97]\n\t" \
    "v_pk_add_f32 v[78:79], v[78:79], v[98:99]\n\t" \
    "v_pk_add_f32 v[80:81], v[80:81], v[100:101]\n\t" \
    "v_pk_add_f32 v[82:83], v[82:83], v[102:103]\n\t" \
    ".Lft0_%=:\n\t" \
    "ds_read_b32 v194, %[f0]\n\t" \
    "s_waitcnt lgkmcnt(0)\n\t" \
    "v_cmp_ge_u32 vcc, %[u], v194\n\t" \
    "s_cbranch_vccnz .Lfs0_%=\n\t" \
    ".Lfu0_%=:\n\t" \
    "ds_read_b128 v[84:87], %[la] offset:0\n\t" \
    "ds_read_b128 v[88:91], %[la] offset:6144\n\t" \
    "ds_read_b128 v[92:95], %[la] offset:12288\n\t" \
    "ds_read_b128 v[96:99], %[la] offset:18432\n\t" \
    "ds_read_b128 v[100:103], %[la] offset:24576\n\t" \
    "ds_read_b128 v[104:107], %[la] offset:1024\n\t" \
    "ds_read_b128 v[108:111], %[la] offset:7168\n\t" \
    "ds_read_b128 v[112:115], %[la] offset:13312\n\t" \
    "ds_read_b128 v[116:119], %[la] offset:19456\n\t" \
    "ds_read_b128 v[120:123], %[la] offset:25600\n\t" \
    "ds_read_b128 v[124:127], %[la] offset:2048\n\t" \
    "s_waitcnt lgkmcnt(10)\n\t" \
    "v_pk_add_f32 v[64:65], v[64:65], v[84:85]\n\t" \
    "v_pk_add_f32 v[66:67], v[66:67], v[86:87]\n\t" \
    "ds_read_b128 v[128:131], %[la] offset:8192\n\t" \
    "s_waitcnt lgkmcnt(10)\n\t" \
    "v_pk_add_f32 v[68:69], v[68:69], v[88:89]\n\t" \
    "v_pk_add_f32 v[70:71], v[70:71], v[90:91]\n\t" \
    "ds_read_b128 v[132:135], %[la] offset:14336\n\t" \
    "s_waitcnt lgkmcnt(10)\n\t" \
    "v_pk_add_f32 v[72:73], v[72:73], v[92:93]\n\t" \
    "v_pk_add_f32 v[74:75], v[74:75], v[94:95]\n\t" \
    "ds_read_b128 v[136:139], %[la] offset:20480\n\t" \
    "s_waitcnt lgkmcnt(10)\n\t" \
    "v_pk_add_f32 v[76:77], v[76:77], v[96:97]\n\t" \
    "v_pk_add_f32 v[78:79], v[78:79], v[98:99]\n\t" \
    "ds_read_b128 v[140:143], %[la] offset:26624\n\t" \
    "s_waitcnt lgkmcnt(10)\n\t" \
    "v_pk_add_f32 v[80:81], v[80:81], v[100:101]\n\t" \
    "v_pk_add_f32 v[82:83], v[82:83], v[102:103]\n\t" \
    "ds_read_b128 v[144:147], %[la] offset:3072\n\t" \
    "s_waitcnt lgkmcnt(10)\n\t" \
    "v_pk_add_f32 v[64:65], v[64:65], v[104:105]\n\t" \
    "v_pk_add_f32 v[66:67], v[66:67], v[106:107]\n\t" \
    "ds_read_b128 v[148:151], %[la] offset:9216\n\t" \
    "s_waitcnt lgkmcnt(10)\n\t" \
    "v_pk_add_f32 v[68:69], v[68:69], v[108:109]\n\t" \
    "v_pk_add_f32 v[70:71], v[70:71], v[110:111]\n\t" \
    "ds_read_b128 v[152:155], %[la] offset:15360\n\t" \
    "s_waitcnt lgkmcnt(10)\n\t" \
    "v_pk_add_f32 v[72:73], v[72:73], v[112:113]\n\t" \
    "v_pk_add_f32 v[74:75], v[74:75], v[114:115]\n\t" \
    "ds_read_b128 v[156:159], %[la] offset:21504\n\t" \
    "s_waitcnt lgkmcnt(10)\n\t" \
    "v_pk_add_f32 v[76:77], v[76:77], v[116:117]\n\t" \
    "v_pk_add_f32 v[78:79], v[78:79], v[118:119]\n\t" \
    "ds_read_b128 v[160:163], %[la] offset:27648\n\t" \
    "s_waitcnt lgkmcnt(10)\n\t" \
    "v_pk_add_f32 v[80:81], v[80:81], v[120:121]\n\t" \
    "v_pk_add_f32 v[82:83], v[82:83], v[122:123]\n\t" \
    "s_waitcnt lgkmcnt(9)\n\t" \
    "v_pk_add_f32 v[64:65], v[64:65], v[124:125]\n\t" \
    "v_pk_add_f32 v[66:67], v[66:67], v[126:127]\n\t" \
    "s_waitcnt lgkmcnt(8)\n\t" \
    "v_pk_add_f32 v[68:69], v[68:69], v[128:129]\n\t" \
    "v_pk_add_f32 v[70:71], v[70:71], v[130:131]\n\t" \
    "s_waitcnt lgkmcnt(7)\n\t" \
    "v_pk_add_f32 v[72:73], v[72:73], v[132:133]\n\t" \
    "v_pk_add_f32 v[74:75], v[74:75], v[134:135]\n\t" \
    "s_waitcnt lgkmcnt(6)\n\t" \
    "v_pk_add_f32 v[76:77], v[76:77], v[136:137]\n\t" \
    "v_pk_add_f32 v[78:79], v[78:79], v[138:139]\n\t" \
    "s_waitcnt lgkmcnt(5)\n\t" \
    "v_pk_add_f32 v[80:81], v[80:81], v[140:141]\n\t" \
    "v_pk_add_f32 v[82:83], v[82:83], v[142:143]\n\t" \
    "s_waitcnt lgkmcnt(4)\n\t" \
    "v_pk_add_f32 v[64:65], v[64:65], v[144:145]\n\t" \
    "v_pk_add_f32 v[66:67], v[66:67], v[146:147]\n\t" \
    "s_waitcnt lgkmcnt(3)\n\t" \
    "v_pk_add_f32 v[68:69], v[68:69], v[148:149]\n\t" \
    "v_pk_add_f32 v[70:71], v[70:71], v[150:151]\n\t" \
    "s_waitcnt lgkmcnt(2)\n\t" \
    "v_pk_add_f32 v[72:73], v[72:73], v[152:153]\n\t" \
    "v_pk_add_f32 v[74:75], v[74:75], v[154:155]\n\t" \
    "s_waitcnt lgkmcnt(1)\n\t" \
    "v_pk_add_f32 v[76:77], v[76:77], v[156:157]\n\t" \
    "v_pk_add_f32 v[78:79], v[78:79], v[158:159]\n\t" \
    "s_waitcnt lgkmcnt(0)\n\t" \
    "v_pk_add_f32 v[80:81], v[80:81], v[160:161]\n\t" \
    "v_pk_add_f32 v[82:83], v[82:83], v[162:163]\n\t" \
    ".Lft1_%=:\n\t" \
    "ds_read_b32 v194, %[f1]\n\t" \
    "s_waitcnt lgkmcnt(0)\n\t" \
    "v_cmp_ge_u32 vcc, %[u], v194\n\t" \
    "s_cbranch_vccnz .Lfs1_%=\n\t" \
    ".Lfu1_%=:\n\t" \
    ASM_ST2 \
    "ds_read_b128 v[84:87], %[la] offset:4096\n\t" \
    "ds_read_b128 v[88:91], %[la] offset:10240\n\t" \
    "ds_read_b128 v[92:95], %[la] offset:16384\n\t" \
    "ds_read_b128 v[96:99], %[la] offset:22528\n\t" \
    "ds_read_b128 v[100:103], %[la] offset:28672\n\t" \
    "ds_read_b128 v[104:107], %[la] offset:5120\n\t" \
    "ds_read_b128 v[108:111], %[la] offset:11264\n\t" \
    "ds_read_b128 v[112:115], %[la] offset:17408\n\t" \
    "ds_read_b128 v[116:119], %[la] offset:23552\n\t" \
    "ds_read_b128 v[120:123], %[la] offset:29696\n\t" \
    "v_mov_b32 v194, %[un]\n\t" \
    "ds_write_b32 %[fd], v194\n\t" \
    "s_waitcnt lgkmcnt(10)\n\t" \
    "v_pk_add_f32 v[64:65], v[64:65], v[84:85]\n\t" \
    "v_pk_add_f32 v[66:67], v[66:67], v[86:87]\n\t" \
    "s_waitcnt lgkmcnt(9)\n\t" \
    "v_pk_add_f32 v[68:69], v[68:69], v[88:89]\n\t" \
    "v_pk_add_f32 v[70:71], v[70:71], v[90:91]\n\t" \
    "s_waitcnt lgkmcnt(8)\n\t" \
    "v_pk_add_f32 v[72:73], v[72:73], v[92:93]\n\t" \
    "v_pk_add_f32 v[74:75], v[74:75], v[94:95]\n\t" \
    "s_waitcnt lgkmcnt(7)\n\t" \
    "v_pk_add_f32 v[76:77], v[76:77], v[96:97]\n\t" \
    "v_pk_add_f32 v[78:79], v[78:79], v[98:99]\n\t" \
    "s_waitcnt lgkmcnt(6)\n\t" \
    "v_pk_add_f32 v[80:81], v[80:81], v[100:101]\n\t" \
    "v_pk_add_f32 v[82:83], v[82:83], v[102:103]\n\t" \
    "s_waitcnt lgkmcnt(5)\n\t" \
    "v_pk_add_f32 v[64:65], v[64:65], v[104:105]\n\t" \
    "v_pk_add_f32 v[66:67], v[66:67], v[106:107]\n\t" \
    "s_waitcnt lgkmcnt(4)\n\t" \
    "v_pk_add_f32 v[68:69], v[68:69], v[108:109]\n\t" \
    "v_pk_add_f32 v[70:71], v[70:71], v[110:111]\n\t" \
    "s_waitcnt lgkmcnt(3)\n\t" \
    "v_pk_add_f32 v[72:73], v[72:73], v[112:113]\n\t" \
    "v_pk_add_f32 v[74:75], v[74:75], v[114:115]\n\t" \
    "s_waitcnt lgkmcnt(2)\n\t" \
    "v_pk_add_f32 v[76:77], v[76:77], v[116:117]\n\t" \
    "v_pk_add_f32 v[78:79], v[78:79], v[118:119]\n\t" \
    "s_waitcnt lgkmcnt(1)\n\t" \
    "v_pk_add_f32 v[80:81], v[80:81], v[120:121]\n\t" \
    "v_pk_add_f32 v[82:83], v[82:83], v[122:123]\n\t" \
    "v_pk_add_f32 v[84:85], v[64:65], v[66:67]\n\t" \
    "v_pk_mul_f32 v[88:89], v[64:65], v[64:65]\n\t" \
    "v_pk_add_f32 v[86:87], v[68:69], v[70:71]\n\t" \
    "v_pk_mul_f32 v[90:91], v[66:67], v[66:67]\n\t" \
    "v_pk_fma_f32 v[88:89], v[68:69], v[68:69], v[88:89]\n\t" \
    "v_pk_fma_f32 v[90:91], v[70:71], v[70:71], v[90:91]\n\t" \
    "v_pk_add_f32 v[84:85], v[84:85], v[72:73]\n\t" \
    "v_pk_fma_f32 v[88:89], v[72:73], v[72:73], v[88:89]\n\t" \
    "v_pk_add_f32 v[86:87], v[86:87], v[74:75]\n\t" \
    "v_pk_fma_f32 v[90:91], v[74:75], v[74:75], v[90:91]\n\t" \
    "v_pk_add_f32 v[84:85], v[84:85], v[76:77]\n\t" \
    "v_pk_fma_f32 v[88:89], v[76:77], v[76:77], v[88:89]\n\t" \
    "v_pk_add_f32 v[86:87], v[86:87], v[78:79]\n\t" \
    "v_pk_fma_f32 v[90:91], v[78:79], v[78:79], v[90:91]\n\t" \
    "v_pk_add_f32 v[84:85], v[84:85], v[80:81]\n\t" \
    "v_pk_fma_f32 v[88:89], v[80:81], v[80:81], v[88:89]\n\t" \
    "v_pk_add_f32 v[86:87], v[86:87], v[82:83]\n\t" \
    "v_pk_fma_f32 v[90:91], v[82:83], v[82:83], v[90:91]\n\t" \
    "v_pk_add_f32 v[84:85], v[84:85], v[86:87]\n\t" \
    "v_pk_add_f32 v[88:89], v[88:89], v[90:91]\n\t" \
    "v_add_f32 v194, v84, v85\n\t" \
    "v_add_f32 v195, v88, v89\n\t" \
    "s_nop 1\n\t" \
    "v_permlane16_swap_b32 v194, v195\n\t" \
    "v_add_f32 v196, v194, v195\n\t" \
    "v_add_f32 v197, v194, v195\n\t" \
    "s_nop 1\n\t" \
    "v_permlane32_swap_b32 v196, v197\n\t" \
    "v_add_f32 v194, v196, v197\n\t" \
    "v_add_f32 v195, v196, v197\n\t" \
    "s_nop 1\n\t" \
    "v_permlane16_swap_b32 v194, v195\n\t" \
    "v_min_u32 v194, -2, v194\n\t" \
    "v_min_u32 v195, -2, v195\n\t" \
    "buffer_store_dwordx2 v[194:195], %[so], %[rr], %[sp] offen sc1\n\t" \
    ASM_ST3 \
    "v_cvt_pk_bf16_f32 v184, v64, v65\n\t" \
    "v_cvt_pk_bf16_f32 v185, v66, v67\n\t" \
    "v_cvt_pk_bf16_f32 v186, v68, v69\n\t" \
    "v_cvt_pk_bf16_f32 v187, v70, v71\n\t" \
    "v_cvt_pk_bf16_f32 v188, v72, v73\n\t" \
    "v_cvt_pk_bf16_f32 v189, v74, v75\n\t" \
    "v_cvt_pk_bf16_f32 v190, v76, v77\n\t" \
    "v_cvt_pk_bf16_f32 v191, v78, v79\n\t" \
    "v_cvt_pk_bf16_f32 v192, v80, v81\n\t" \
    "v_cvt_pk_bf16_f32 v193, v82, v83\n\t" \
    "v_permlane16_swap_b32 v184, v186\n\t" \
    "v_permlane16_swap_b32 v185, v187\n\t" \
    "v_permlane16_swap_b32 v188, v190\n\t" \
    "v_permlane16_swap_b32 v189, v191\n\t" \
    W4SLEEP \
    ".Lfy%=:\n\t" \
    "buffer_load_dwordx2 v[196:197], %[si], %[rr], %[sq] offen sc1\n\t" \
    "buffer_load_dwordx2 v[198:199], %[si], %[rr], %[sq] offen offset:512 sc1\n\t" \
    "s_waitcnt vmcnt(0)\n\t" \
    "v_max_u32 v200, v196, v197\n\t" \
    "v_max3_u32 v200, v198, v199, v200\n\t" \
    "v_cmp_eq_u32 vcc, -1, v200\n\t" \
    "s_cbranch_vccnz .Lfx%=\n\t" \
    ".Lfz%=:\n\t" \
    ASM_ST \
    "v_add_f32 v194, v196, v198\n\t" \
    "v_add_f32 v195, v197, v199\n\t" \
    "buffer_store_dwordx4 v[184:187], %[pa], %[rd], %[xr] offen sc1\n\t" \
    "buffer_store_dwordx4 v[188:191], %[pb], %[rd], %[xr] offen sc1\n\t" \
    "buffer_store_dwordx2 v[192:193], %[o4], %[rd], %[xr] offen sc1\n\t" \
    "s_nop 1\n\t" \
    "v_permlane16_swap_b32 v194, v195\n\t" \
    "v_add_f32 v196, v194, v195\n\t" \
    "v_add_f32 v197, v194, v195\n\t" \
    "s_nop 1\n\t" \
    "v_permlane32_swap_b32 v196, v197\n\t" \
    "v_add_f32 v194, v196, v197\n\t" \
    "v_add_f32 v195, v196, v197\n\t" \
    "s_nop 1\n\t" \
    "v_permlane16_swap_b32 v194, v195\n\t" \
    "v_mul_f32 v194, %[kd], v194\n\t" \
    "v_mul_f32 v195, %[kd], v195\n\t" \
    "v_fma_f32 v195, -v194, v194, v195\n\t" \
    "v_max_f32 v195, 0, v195\n\t" \
    "v_cvt_pk_bf16_f32 v195, v195, v195\n\t" \
    "v_lshlrev_b32 v195, 16, v195\n\t" \
    "v_add_f32 v195, %[ke], v195\n\t" \
    "v_cvt_pk_bf16_f32 v195, v195, v195\n\t" \
    "v_lshlrev_b32 v195, 16, v195\n\t" \
    "v_sqrt_f32 v195, v195\n\t" \
    "v_cvt_pk_bf16_f32 v194, v194, v194\n\t" \
    "v_lshlrev_b32 v194, 16, v194\n\t" \
    "v_cvt_pk_bf16_f32 v195, v195, v195\n\t" \
    "v_lshlrev_b32 v195, 16, v195\n\t" \
    "v_rcp_f32 v196, v195\n\t" \
    "v_rcp_f32 v197, v195\n\t" \
    "s_nop 0\n\t" \
    "v_mul_f32 v198, -v194, v196\n\t" \
    "v_mul_f32 v199, -v194, v196\n\t" \
    "v_pk_fma_f32 v[64:65], v[64:65], v[196:197], v[198:199]\n\t" \
    "v_pk_fma_f32 v[66:67], v[66:67], v[196:197], v[198:199]\n\t" \
    "v_pk_fma_f32 v[68:69], v[68:69], v[196:197], v[198:199]\n\t" \
    "v_pk_fma_f32 v[70:71], v[70:71], v[196:197], v[198:199]\n\t" \
    "v_pk_fma_f32 v[72:73], v[72:73], v[196:197], v[198:199]\n\t" \
    "v_pk_fma_f32 v[74:75], v[74:75], v[196:197], v[198:199]\n\t" \
    "v_pk_fma_f32 v[76:77], v[76:77], v[196:197], v[198:199]\n\t" \
    "v_pk_fma_f32 v[78:79], v[78:79], v[196:197], v[198:199]\n\t" \
    "v_pk_fma_f32 v[80:81], v[80:81], v[196:197], v[198:199]\n\t" \
    "v_pk_fma_f32 v[82:83], v[82:83], v[196:197], v[198:199]\n\t" \
    "v_cvt_pk_bf16_f32 v184, v64, v65\n\t" \
    "v_cvt_pk_bf16_f32 v185, v66, v67\n\t" \
    "v_cvt_pk_bf16_f32 v186, v68, v69\n\t" \
    "v_cvt_pk_bf16_f32 v187, v70, v71\n\t" \
    "v_cvt_pk_bf16_f32 v188, v72, v73\n\t" \
    "v_cvt_pk_bf16_f32 v189, v74, v75\n\t" \
    "v_cvt_pk_bf16_f32 v190, v76, v77\n\t" \
    "v_cvt_pk_bf16_f32 v191, v78, v79\n\t" \
    "v_cvt_pk_bf16_f32 v192, v80, v81\n\t" \
    "v_cvt_pk_bf16_f32 v193, v82, v83\n\t" \
    "v_permlane16_swap_b32 v184, v186\n\t" \
    "v_permlane16_swap_b32 v185, v187\n\t" \
    "v_permlane16_swap_b32 v188, v190\n\t" \
    "v_permlane16_swap_b32 v189, v191\n\t" \
    "buffer_store_dwordx4 v[184:187], %[pa], %[rd], %[xn] offen sc1\n\t" \
    "buffer_store_dwordx4 v[188:191], %[pb], %[rd], %[xn] offen sc1\n\t" \
    "buffer_store_dwordx2 v[192:193], %[o4], %[rd], %[xn] offen sc1\n\t" \
    "buffer_load_dwordx2 v[164:165], %[o0], %[rd], %[n1] offen sc1\n\t" \
    "buffer_load_dwordx2 v[166:167], %[o1], %[rd], %[n1] offen sc1\n\t" \
    "buffer_load_dwordx2 v[168:169], %[o2], %[rd], %[n1] offen sc1\n\t" \
    "buffer_load_dwordx2 v[170:171], %[o3], %[rd], %[n1] offen sc1\n\t" \
    "buffer_load_dwordx2 v[172:173], %[o4], %[rd], %[n1] offen sc1\n\t" \
    "buffer_load_dwordx2 v[174:175], %[o0], %[rd], %[na] offen sc1\n\t" \
    "buffer_load_dwordx2 v[176:177], %[o1], %[rd], %[na] offen sc1\n\t" \
    "buffer_load_dwordx2 v[178:179], %[o2], %[rd], %[na] offen sc1\n\t" \
    "buffer_load_dwordx2 v[180:181], %[o3], %[rd], %[na] offen sc1\n\t" \
    "buffer_load_dwordx2 v[182:183], %[o4], %[rd], %[na] offen sc1\n\t" \
    "v_mov_b32 v196, %[tn]\n\t" \
    "v_mov_b32 v197, %[tn]\n\t" \
    "v_mov_b32 v198, %[tn]\n\t" \
    "v_mov_b32 v199, %[tn]\n\t" \
    "s_waitcnt vmcnt(10)\n\t" \
    "buffer_store_dwordx4 v[196:199], %[fo], %[rn], %[fs] offen sc1\n\t" \
    "s_nop 1\n\t"
#define ASM_W4F_1 \
    "s_branch .Lf0%=\n\t" \
    ".Lfr%=:\n\t" \
    "s_cmp_eq_u32 %[n], 0\n\t" \
    "s_cbranch_scc1 .Lfb%=\n\t" \
    "s_sub_u32 %[n], %[n], 1\n\t" \
    "s_sleep 6\n\t" \
    "buffer_load_dwordx2 v[164:165], %[o0], %[rd], %[c1] offen sc1\n\t" \
    "buffer_load_dwordx2 v[166:167], %[o1], %[rd], %[c1] offen sc1\n\t" \
    "buffer_load_dwordx2 v[168:169], %[o2], %[rd], %[c1] offen sc1\n\t" \
    "buffer_load_dwordx2 v[170:171], %[o3], %[rd], %[c1] offen sc1\n\t" \
    "buffer_load_dwordx2 v[172:173], %[o4], %[rd], %[c1] offen sc1\n\t" \
    "buffer_load_dwordx2 v[174:175], %[o0], %[rd], %[ca] offen sc1\n\t" \
    "buffer_load_dwordx2 v[176:177], %[o1], %[rd], %[ca] offen sc1\n\t" \
    "buffer_load_dwordx2 v[178:179], %[o2], %[rd], %[ca] offen sc1\n\t" \
    "buffer_load_dwordx2 v[180:181], %[o3], %[rd], %[ca] offen sc1\n\t" \
    "buffer_load_dwordx2 v[182:183], %[o4], %[rd], %[ca] offen sc1\n\t" \
    "s_waitcnt vmcnt(0)\n\t" \
    "s_branch .Lfa%=\n\t" \
    ".Lfs0_%=:\n\t" \
    "s_cmp_eq_u32 %[n], 0\n\t" \
    "s_cbranch_scc1 .Lfu0_%=\n\t" \
    "s_sub_u32 %[n], %[n], 1\n\t" \
    "s_sleep 1\n\t" \
    "s_branch .Lft0_%=\n\t" \
    ".Lfs1_%=:\n\t" \
    "s_cmp_eq_u32 %[n], 0\n\t" \
    "s_cbranch_scc1 .Lfu1_%=\n\t" \
    "s_sub_u32 %[n], %[n], 1\n\t" \
    "s_sleep 1\n\t" \
    "s_branch .Lft1_%=\n\t" \
    ".Lf0%=:\n\t" \
    "s_waitcnt vmcnt(0)\n\t" \
    ".Lfa%=:\n\t" \
    "v_max_u32 v194, v164, v165\n\t" \
    "v_max3_u32 v194, v166, v167, v194\n\t" \
    "v_max3_u32 v194, v168, v169, v194\n\t" \
    "v_max3_u32 v194, v170, v171, v194\n\t" \
    "v_max3_u32 v194, v172, v173, v194\n\t" \
    "v_max3_u32 v194, v174, v175, v194\n\t" \
    "v_max3_u32 v194, v176, v177, v194\n\t" \
    "v_max3_u32 v194, v178, v179, v194\n\t" \
    "v_max3_u32 v194, v180, v181, v194\n\t" \
    "v_max3_u32 v194, v182, v183, v194\n\t" \
    "v_cmp_eq_u32 vcc, -1, v194\n\t" \
    "s_cbranch_vccnz .Lfr%=\n\t" \
    ".Lfb%=:\n\t" \
    "v_lshlrev_b32 v64, 16, v164\n\t" \
    "v_and_b32 v65, 0xffff0000, v164\n\t" \
    "v_lshlrev_b32 v84, 16, v174\n\t" \
    "v_and_b32 v85, 0xffff0000, v174\n\t" \
    "v_lshlrev_b32 v66, 16, v165\n\t" \
    "v_and_b32 v67, 0xffff0000, v165\n\t" \
    "v_lshlrev_b32 v86, 16, v175\n\t" \
    "v_and_b32 v87, 0xffff0000, v175\n\t" \
    "v_lshlrev_b32 v68, 16, v166\n\t" \
    "v_and_b32 v69, 0xffff0000, v166\n\t" \
    "v_lshlrev_b32 v88, 16, v176\n\t" \
    "v_and_b32 v89, 0xffff0000, v176\n\t" \
    "v_lshlrev_b32 v70, 16, v167\n\t" \
    "v_and_b32 v71, 0xffff0000, v167\n\t" \
    "v_lshlrev_b32 v90, 16, v177\n\t" \
    "v_and_b32 v91, 0xffff0000, v177\n\t" \
    "v_lshlrev_b32 v72, 16, v168\n\t" \
    "v_and_b32 v73, 0xffff0000, v168\n\t" \
    "v_lshlrev_b32 v92, 16, v178\n\t" \
    "v_and_b32 v93, 0xffff0000, v178\n\t" \
    "v_lshlrev_b32 v74, 16, v169\n\t" \
    "v_and_b32 v75, 0xffff0000, v169\n\t" \
    "v_lshlrev_b32 v94, 16, v179\n\t" \
    "v_and_b32 v95, 0xffff0000, v179\n\t" \
    "v_lshlrev_b32 v76, 16, v170\n\t" \
    "v_and_b32 v77, 0xffff0000, v170\n\t" \
    "v_lshlrev_b32 v96, 16, v180\n\t" \
    "v_and_b32 v97, 0xffff0000, v180\n\t" \
    "v_lshlrev_b32 v78, 16, v171\n\t" \
    "v_and_b32 v79, 0xffff0000, v171\n\t" \
    "v_lshlrev_b32 v98, 16, v181\n\t" \
    "v_and_b32 v99, 0xffff0000, v181\n\t" \
    "v_lshlrev_b32 v80, 16, v172\n\t" \
    "v_and_b32 v81, 0xffff0000, v172\n\t" \
    "v_lshlrev_b32 v100, 16, v182\n\t" \
    "v_and_b32 v101, 0xffff0000, v182\n\t" \
    "v_lshlrev_b32 v82, 16, v173\n\t" \
    "v_and_b32 v83, 0xffff0000, v173\n\t" \
    "v_lshlrev_b32 v102, 16, v183\n\t" \
    "v_and_b32 v103, 0xffff0000, v183\n\t" \
    "v_pk_add_f32 v[64:65], v[64:65], v[84:85]\n\t" \
    "v_pk_add_f32 v[66:67], v[66:67], v[86:87]\n\t" \
    "v_pk_add_f32 v[68:69], v[68:69], v[88:89]\n\t" \
    "v_pk_add_f32 v[70:71], v[70:71], v[90:91]\n\t" \
    "v_pk_add_f32 v[72:73], v[72:73], v[92:93]\n\t" \
    "v_pk_add_f32 v[74:75], v[74:75], v[94:95]\n\t" \
    "v_pk_add_f32 v[76:77], v[76:77], v[96:97]\n\t" \
    "v_pk_add_f32 v[78:79], v[78:79], v[98:99]\n\t" \
    "v_pk_add_f32 v[80:81], v[80:81], v[100:101]\n\t" \
    "v_pk_add_f32 v[82:83], v[82:83], v[102:103]\n\t" \
    ".Lft0_%=:\n\t" \
    "ds_read_b32 v194, %[f0]\n\t" \
    "s_waitcnt lgkmcnt(0)\n\t" \
    "v_cmp_ge_u32 vcc, %[u], v194\n\t" \
    "s_cbranch_vccnz .Lfs0_%=\n\t" \
    ".Lfu0_%=:\n\t" \
    "ds_read_b128 v[84:87], %[la] offset:0\n\t" \
    "ds_read_b128 v[88:91], %[la] offset:6144\n\t" \
    "ds_read_b128 v[92:95], %[la] offset:12288\n\t" \
    "ds_read_b128 v[96:99], %[la] offset:18432\n\t" \
    "ds_read_b128 v[100:103], %[la] offset:24576\n\t" \
    "ds_read_b128 v[104:107], %[la] offset:1024\n\t" \
    "ds_read_b128 v[108:111], %[la] offset:7168\n\t" \
    "ds_read_b128 v[112:115], %[la] offset:13312\n\t" \
    "ds_read_b128 v[116:119], %[la] offset:19456\n\t" \
    "ds_read_b128 v[120:123], %[la] offset:25600\n\t" \
    "ds_read_b128 v[124:127], %[la] offset:2048\n\t" \
    "s_waitcnt lgkmcnt(10)\n\t" \
    "v_pk_add_f32 v[64:65], v[64:65], v[84:85]\n\t" \
    "v_pk_add_f32 v[66:67], v[66:67], v[86:87]\n\t" \
    "ds_read_b128 v[128:131], %[la] offset:8192\n\t" \
    "s_waitcnt lgkmcnt(10)\n\t" \
    "v_pk_add_f32 v[68:69], v[68:69], v[88:89]\n\t" \
    "v_pk_add_f32 v[70:71], v[70:71], v[90:91]\n\t" \
    "ds_read_b128 v[132:135], %[la] offset:14336\n\t" \
    "s_waitcnt lgkmcnt(10)\n\t" \
    "v_pk_add_f32 v[72:73], v[72:73], v[92:93]\n\t" \
    "v_pk_add_f32 v[74:75], v[74:75], v[94:95]\n\t" \
    "ds_read_b128 v[136:139], %[la] offset:20480\n\t" \
    "s_waitcnt lgkmcnt(10)\n\t" \
    "v_pk_add_f32 v[76:77], v[76:77], v[96:97]\n\t" \
    "v_pk_add_f32 v[78:79], v[78:79], v[98:99]\n\t" \
    "ds_read_b128 v[140:143], %[la] offset:26624\n\t" \
    "s_waitcnt lgkmcnt(10)\n\t" \
    "v_pk_add_f32 v[80:81], v[80:81], v[100:101]\n\t" \
    "v_pk_add_f32 v[82:83], v[82:83], v[102:103]\n\t" \
    "ds_read_b128 v[144:147], %[la] offset:3072\n\t" \
    "s_waitcnt lgkmcnt(10)\n\t" \
    "v_pk_add_f32 v[64:65], v[64:65], v[104:105]\n\t" \
    "v_pk_add_f32 v[66:67], v[66:67], v[106:107]\n\t" \
    "ds_read_b128 v[148:151], %[la] offset:9216\n\t" \
    "s_waitcnt lgkmcnt(10)\n\t" \
    "v_pk_add_f32 v[68:69], v[68:69], v[108:109]\n\t" \
    "v_pk_add_f32 v[70:71], v[70:71], v[110:111]\n\t" \
    "ds_read_b128 v[152:155], %[la] offset:15360\n\t" \
    "s_waitcnt lgkmcnt(10)\n\t" \
    "v_pk_add_f32 v[72:73], v[72:73], v[112:113]\n\t" \
    "v_pk_add_f32 v[74:75], v[74:75], v[114:115]\n\t" \
    "ds_read_b128 v[156:159], %[la] offset:21504\n\t" \
    "s_waitcnt lgkmcnt(10)\n\t" \
    "v_pk_add_f32 v[76:77], v[76:77], v[116:117]\n\t" \
    "v_pk_add_f32 v[78:79], v[78:79], v[118:119]\n\t" \
    "ds_read_b128 v[160:163], %[la] offset:27648\n\t" \
    "s_waitcnt lgkmcnt(10)\n\t" \
    "v_pk_add_f32 v[80:81], v[80:81], v[120:121]\n\t" \
    "v_pk_add_f32 v[82:83], v[82:83], v[122:123]\n\t" \
    "s_waitcnt lgkmcnt(9)\n\t" \
    "v_pk_add_f32 v[64:65], v[64:65], v[124:125]\n\t" \
    "v_pk_add_f32 v[66:67], v[66:67], v[126:127]\n\t" \
    "s_waitcnt lgkmcnt(8)\n\t" \
    "v_pk_add_f32 v[68:69], v[68:69], v[128:129]\n\t" \
    "v_pk_add_f32 v[70:71], v[70:71], v[130:131]\n\t" \
    "s_waitcnt lgkmcnt(7)\n\t" \
    "v_pk_add_f32 v[72:73], v[72:73], v[132:133]\n\t" \
    "v_pk_add_f32 v[74:75], v[74:75], v[134:135]\n\t" \
    "s_waitcnt lgkmcnt(6)\n\t" \
    "v_pk_add_f32 v[76:77], v[76:77], v[136:137]\n\t" \
    "v_pk_add_f32 v[78:79], v[78:79], v[138:139]\n\t" \
    "s_waitcnt lgkmcnt(5)\n\t" \
    "v_pk_add_f32 v[80:81], v[80:81], v[140:141]\n\t" \
    "v_pk_add_f32 v[82:83], v[82:83], v[142:143]\n\t" \
    "s_waitcnt lgkmcnt(4)\n\t" \
    "v_pk_add_f32 v[64:65], v[64:65], v[144:145]\n\t" \
    "v_pk_add_f32 v[66:67], v[66:67], v[146:147]\n\t" \
    "s_waitcnt lgkmcnt(3)\n\t" \
    "v_pk_add_f32 v[68:69], v[68:69], v[148:149]\n\t" \
    "v_pk_add_f32 v[70:71], v[70:71], v[150:151]\n\t" \
    "s_waitcnt lgkmcnt(2)\n\t" \
    "v_pk_add_f32 v[72:73], v[72:73], v[152:153]\n\t" \
    "v_pk_add_f32 v[74:75], v[74:75], v[154:155]\n\t" \
    "s_waitcnt lgkmcnt(1)\n\t" \
    "v_pk_add_f32 v[76:77], v[76:77], v[156:157]\n\t" \
    "v_pk_add_f32 v[78:79], v[78:79], v[158:159]\n\t" \
    "s_waitcnt lgkmcnt(0)\n\t" \
    "v_pk_add_f32 v[80:81], v[80:81], v[160:161]\n\t" \
    "v_pk_add_f32 v[82:83], v[82:83], v[162:163]\n\t" \
    ".Lft1_%=:\n\t" \
    "ds_read_b32 v194, %[f1]\n\t" \
    "s_waitcnt lgkmcnt(0)\n\t" \
    "v_cmp_ge_u32 vcc, %[u], v194\n\t" \
    "s_cbranch_vccnz .Lfs1_%=\n\t" \
    ".Lfu1_%=:\n\t" \
    ASM_ST2 \
    "ds_read_b128 v[84:87], %[la] offset:4096\n\t" \
    "ds_read_b128 v[88:91], %[la] offset:10240\n\t" \
    "ds_read_b128 v[92:95], %[la] offset:16384\n\t" \
    "ds_read_b128 v[96:99], %[la] offset:22528\n\t" \
    "ds_read_b128 v[100:103], %[la] offset:28672\n\t" \
    "ds_read_b128 v[104:107], %[la] offset:5120\n\t" \
    "ds_read_b128 v[108:111], %[la] offset:11264\n\t" \
    "ds_read_b128 v[112:115], %[la] offset:17408\n\t" \
    "ds_read_b128 v[116:119], %[la] offset:23552\n\t" \
    "ds_read_b128 v[120:123], %[la] offset:29696\n\t" \
    "v_mov_b32 v194, %[un]\n\t" \
    "ds_write_b32 %[fd], v194\n\t" \
    "s_waitcnt lgkmcnt(10)\n\t" \
    "v_pk_add_f32 v[64:65], v[64:65], v[84:85]\n\t" \
    "v_pk_add_f32 v[66:67], v[66:67], v[86:87]\n\t" \
    "s_waitcnt lgkmcnt(9)\n\t" \
    "v_pk_add_f32 v[68:69], v[68:69], v[88:89]\n\t" \
    "v_pk_add_f32 v[70:71], v[70:71], v[90:91]\n\t" \
    "s_waitcnt lgkmcnt(8)\n\t" \
    "v_pk_add_f32 v[72:73], v[72:73], v[92:93]\n\t" \
    "v_pk_add_f32 v[74:75], v[74:75], v[94:95]\n\t" \
    "s_waitcnt lgkmcnt(7)\n\t" \
    "v_pk_add_f32 v[76:77], v[76:77], v[96:97]\n\t" \
    "v_pk_add_f32 v[78:79], v[78:79], v[98:99]\n\t" \
    "s_waitcnt lgkmcnt(6)\n\t" \
    "v_pk_add_f32 v[80:81], v[80:81], v[100:101]\n\t" \
    "v_pk_add_f32 v[82:83], v[82:83], v[102:103]\n\t" \
    "s_waitcnt lgkmcnt(5)\n\t" \
    "v_pk_add_f32 v[64:65], v[64:65], v[104:105]\n\t" \
    "v_pk_add_f32 v[66:67], v[66:67], v[106:107]\n\t" \
    "s_waitcnt lgkmcnt(4)\n\t" \
    "v_pk_add_f32 v[68:69], v[68:69], v[108:109]\n\t" \
    "v_pk_add_f32 v[70:71], v[70:71], v[110:111]\n\t" \
    "s_waitcnt lgkmcnt(3)\n\t" \
    "v_pk_add_f32 v[72:73], v[72:73], v[112:113]\n\t" \
    "v_pk_add_f32 v[74:75], v[74:75], v[114:115]\n\t" \
    "s_waitcnt lgkmcnt(2)\n\t" \
    "v_pk_add_f32 v[76:77], v[76:77], v[116:117]\n\t" \
    "v_pk_add_f32 v[78:79], v[78:79], v[118:119]\n\t" \
    "s_waitcnt lgkmcnt(1)\n\t" \
    "v_pk_add_f32 v[80:81], v[80:81], v[120:121]\n\t" \
    "v_pk_add_f32 v[82:83], v[82:83], v[122:123]\n\t" \
    "v_cvt_pk_bf16_f32 v184, v64, v65\n\t" \
    "v_cvt_pk_bf16_f32 v185, v66, v67\n\t" \
    "v_cvt_pk_bf16_f32 v186, v68, v69\n\t" \
    "v_cvt_pk_bf16_f32 v187, v70, v71\n\t" \
    "v_cvt_pk_bf16_f32 v188, v72, v73\n\t" \
    "v_cvt_pk_bf16_f32 v189, v74, v75\n\t" \
    "v_cvt_pk_bf16_f32 v190, v76, v77\n\t" \
    "v_cvt_pk_bf16_f32 v191, v78, v79\n\t" \
    "v_cvt_pk_bf16_f32 v192, v80, v81\n\t" \
    "v_cvt_pk_bf16_f32 v193, v82, v83\n\t" \
    "v_permlane16_swap_b32 v184, v186\n\t" \
    "v_permlane16_swap_b32 v185, v187\n\t" \
    "v_permlane16_swap_b32 v188, v190\n\t" \
    "v_permlane16_swap_b32 v189, v191\n\t" \
    "buffer_store_dwordx4 v[184:187], %[pa], %[ro], %[so] offen nt\n\t" \
    "buffer_store_dwordx4 v[188:191], %[pa], %[ro], %[so] offen offset:64 nt\n\t" \
    "buffer_store_dwordx2 v[192:193], %[pb], %[ro], %[so] offen offset:128 nt\n\t" \
    "buffer_load_dwordx2 v[164:165], %[o0], %[rd], %[n1] offen sc1\n\t" \
    "buffer_load_dwordx2 v[166:167], %[o1], %[rd], %[n1] offen sc1\n\t" \
    "buffer_load_dwordx2 v[168:169], %[o2], %[rd], %[n1] offen sc1\n\t" \
    "buffer_load_dwordx2 v[170:171], %[o3], %[rd], %[n1] offen sc1\n\t" \
    "buffer_load_dwordx2 v[172:173], %[o4], %[rd], %[n1] offen sc1\n\t" \
    "buffer_load_dwordx2 v[174:175], %[o0], %[rd], %[na] offen sc1\n\t" \
    "buffer_load_dwordx2 v[176:177], %[o1], %[rd], %[na] offen sc1\n\t" \
    "buffer_load_dwordx2 v[178:179], %[o2], %[rd], %[na] offen sc1\n\t" \
    "buffer_load_dwordx2 v[180:181], %[o3], %[rd], %[na] offen sc1\n\t" \
    "buffer_load_dwordx2 v[182:183], %[o4], %[rd], %[na] offen sc1\n\t"
#define ASM_WCS \
    "v_mfma_f32_16x16x32_bf16 v[196:199], v[0:3], v[216:219], 0\n\t" \
    "v_mfma_f32_16x16x32_bf16 v[200:203], v[80:83], v[216:219], 0\n\t" \
    "v_mfma_f32_16x16x32_bf16 v[196:199], v[4:7], v[216:219], v[196:199]\n\t" \
    "v_mfma_f32_16x16x32_bf16 v[200:203], v[84:87], v[216:219], v[200:203]\n\t" \
    "v_mfma_f32_16x16x32_bf16 v[196:199], v[8:11], v[216:219], v[196:199]\n\t" \
    "v_mfma_f32_16x16x32_bf16 v[200:203], v[88:91], v[216:219], v[200:203]\n\t" \
    "v_mfma_f32_16x16x32_bf16 v[196:199], v[12:15], v[216:219], v[196:199]\n\t" \
    "v_mfma_f32_16x16x32_bf16 v[200:203], v[92:95], v[216:219], v[200:203]\n\t" \
    "v_mfma_f32_16x16x32_bf16 v[196:199], v[16:19], v[216:219], v[196:199]\n\t" \
    "v_mfma_f32_16x16x32_bf16 v[200:203], v[96:99], v[216:219], v[200:203]\n\t" \
    "v_mfma_f32_16x16x32_bf16 v[196:199], v[20:23], v[216:219], v[196:199]\n\t" \
    "v_mfma_f32_16x16x32_bf16 v[200:203], v[100:103], v[216:219], v[200:203]\n\t" \
    "v_mfma_f32_16x16x32_bf16 v[196:199], v[24:27], v[216:219], v[196:199]\n\t" \
    "v_mfma_f32_16x16x32_bf16 v[200:203], v[104:107], v[216:219], v[200:203]\n\t" \
    "v_mfma_f32_16x16x32_bf16 v[196:199], v[28:31], v[216:219], v[196:199]\n\t" \
    "v_mfma_f32_16x16x32_bf16 v[200:203], v[108:111], v[216:219], v[200:203]\n\t" \
    "v_mfma_f32_16x16x32_bf16 v[196:199], v[32:35], v[216:219], v[196:199]\n\t" \
    "v_mfma_f32_16x16x32_bf16 v[200:203], v[112:115], v[216:219], v[200:203]\n\t" \
    "v_mfma_f32_16x16x32_bf16 v[196:199], v[36:39], v[216:219], v[196:199]\n\t" \
    "v_mfma_f32_16x16x32_bf16 v[200:203], v[116:119], v[216:219], v[200:203]\n\t" \
    "v_mfma_f32_16x16x32_bf16 v[196:199], v[40:43], v[216:219], v[196:199]\n\t" \
    "v_mfma_f32_16x16x32_bf16 v[200:203], v[120:123], v[216:219], v[200:203]\n\t" \
    "v_mfma_f32_16x16x32_bf16 v[196:199], v[44:47], v[216:219], v[196:199]\n\t" \
    "v_mfma_f32_16x16x32_bf16 v[200:203], v[124:127], v[216:219], v[200:203]\n\t" \
    "v_mfma_f32_16x16x32_bf16 v[196:199], v[48:51], v[216:219], v[196:199]\n\t" \
    "v_mfma_f32_16x16x32_bf16 v[200:203], v[128:131], v[216:219], v[200:203]\n\t" \
    "v_mfma_f32_16x16x32_bf16 v[196:199], v[52:55], v[216:219], v[196:199]\n\t" \
    "v_mfma_f32_16x16x32_bf16 v[200:203], v[132:135], v[216:219], v[200:203]\n\t" \
    "v_mfma_f32_16x16x32_bf16 v[196:199], v[56:59], v[216:219], v[196:199]\n\t" \
    "v_mfma_f32_16x16x32_bf16 v[200:203], v[136:139], v[216:219], v[200:203]\n\t" \
    "v_mfma_f32_16x16x32_bf16 v[196:199], v[60:63], v[216:219], v[196:199]\n\t" \
    "v_mfma_f32_16x16x32_bf16 v[200:203], v[140:143], v[216:219], v[200:203]\n\t" \
    "v_mfma_f32_16x16x32_bf16 v[196:199], v[64:67], v[216:219], v[196:199]\n\t" \
    "v_mfma_f32_16x16x32_bf16 v[200:203], v[144:147], v[216:219], v[200:203]\n\t" \
    "v_mfma_f32_16x16x32_bf16 v[196:199], v[68:71], v[216:219], v[196:199]\n\t" \
    "v_mfma_f32_16x16x32_bf16 v[200:203], v[148:151], v[216:219], v[200:203]\n\t" \
    "v_mfma_f32_16x16x32_bf16 v[196:199], v[72:75], v[216:219], v[196:199]\n\t" \
    "v_mfma_f32_16x16x32_bf16 v[200:203], v[152:155], v[216:219], v[200:203]\n\t" \
    "v_mfma_f32_16x16x32_bf16 v[196:199], v[76:79], v[216:219], v[196:199]\n\t" \
    "v_mfma_f32_16x16x32_bf16 v[200:203], v[156:159], v[216:219], v[200:203]\n\t" \
    "s_nop 7\n\t"
#define ASM_WT1_LO \
    "s_branch .Lt0%=\n\t" \
    ".Ltr%=:\n\t" \
    "s_cmp_gt_i32 %[n], 0\n\t" \
    "s_cbranch_scc0 .Ltg%=\n\t" \
    ".Lfw%=:\n\t" \
    "s_mov_b32 %[a], 0\n\t" \
    "v_max_u32 v252, v232, v233\n\t" \
    "v_max3_u32 v252, v234, v235, v252\n\t" \
    "v_cmp_eq_u32 vcc, -1, v252\n\t" \
    "s_cbranch_vccz .Lgw0_%=\n\t" \
    "buffer_load_dwordx4 v[232:235], %[l16], %[rs], %[i0] offen offset:0 sc1\n\t" \
    "s_mov_b32 %[a], 1\n\t" \
    ".Lgw0_%=:\n\t" \
    "v_max_u32 v252, v236, v237\n\t" \
    "v_max3_u32 v252, v238, v239, v252\n\t" \
    "v_cmp_eq_u32 vcc, -1, v252\n\t" \
    "s_cbranch_vccz .Lgw1_%=\n\t" \
    "buffer_load_dwordx4 v[236:239], %[l16], %[rs], %[i0] offen offset:1024 sc1\n\t" \
    "s_mov_b32 %[a], 1\n\t" \
    ".Lgw1_%=:\n\t" \
    "v_max_u32 v252, v240, v241\n\t" \
    "v_max3_u32 v252, v242, v243, v252\n\t" \
    "v_cmp_eq_u32 vcc, -1, v252\n\t" \
    "s_cbranch_vccz .Lgw2_%=\n\t" \
    "buffer_load_dwordx4 v[240:243], %[l16], %[rs], %[i0] offen offset:2048 sc1\n\t" \
    "s_mov_b32 %[a], 1\n\t" \
    ".Lgw2_%=:\n\t" \
    "v_max_u32 v252, v244, v245\n\t" \
    "v_max3_u32 v252, v246, v247, v252\n\t" \
    "v_cmp_eq_u32 vcc, -1, v252\n\t" \
    "s_cbranch_vccz .Lgw3_%=\n\t" \
    "buffer_load_dwordx4 v[244:247], %[l16], %[rs], %[i0] offen offset:3072 sc1\n\t" \
    "s_mov_b32 %[a], 1\n\t" \
    ".Lgw3_%=:\n\t" \
    "v_max_u32 v252, v248, v249\n\t" \
    "v_max3_u32 v252, v250, v251, v252\n\t" \
    "v_cmp_eq_u32 vcc, -1, v252\n\t" \
    "s_cbranch_vccz .Lgw4_%=\n\t" \
    "buffer_load_dwordx4 v[248:251], %[l16], %[rs], %[i1] offen offset:0 sc1\n\t" \
    "s_mov_b32 %[a], 1\n\t" \
    ".Lgw4_%=:\n\t" \
    "s_cmp_eq_u32 %[a], 0\n\t" \
    "s_cbranch_scc1 .Lhw%=\n\t" \
    "s_waitcnt vmcnt(20)\n\t" \
    ".Liw%=:\n\t" \
    "v_max_u32 v252, v232, v233\n\t" \
    "v_max3_u32 v252, v234, v235, v252\n\t" \
    "v_max3_u32 v252, v236, v237, v252\n\t" \
    "v_max3_u32 v252, v238, v239, v252\n\t" \
    "v_max3_u32 v252, v240, v241, v252\n\t" \
    "v_max3_u32 v252, v242, v243, v252\n\t" \
    "v_max3_u32 v252, v244, v245, v252\n\t" \
    "v_max3_u32 v252, v246, v247, v252\n\t" \
    "v_max3_u32 v252, v248, v249, v252\n\t" \
    "v_max3_u32 v252, v250, v251, v252\n\t" \
    "v_cmp_eq_u32 vcc, -1, v252\n\t" \
    "s_cbranch_vccz .Lhw%=\n\t" \
    "s_sleep 1\n\t" \
    "v_max_u32 v252, v232, v233\n\t" \
    "v_max3_u32 v252, v234, v235, v252\n\t" \
    "v_max3_u32 v252, v236, v237, v252\n\t" \
    "v_max3_u32 v252, v238, v239, v252\n\t" \
    "v_max3_u32 v252, v240, v241, v252\n\t" \
    "v_max3_u32 v252, v242, v243, v252\n\t" \
    "v_max3_u32 v252, v244, v245, v252\n\t" \
    "v_max3_u32 v252, v246, v247, v252\n\t" \
    "v_max3_u32 v252, v248, v249, v252\n\t" \
    "v_max3_u32 v252, v250, v251, v252\n\t" \
    "v_cmp_eq_u32 vcc, -1, v252\n\t" \
    "s_cbranch_vccz .Lhw%=\n\t" \
    "s_sleep 1\n\t" \
    "v_max_u32 v252, v232, v233\n\t" \
    "v_max3_u32 v252, v234, v235, v252\n\t" \
    "v_max3_u32 v252, v236, v237, v252\n\t" \
    "v_max3_u32 v252, v238, v239, v252\n\t" \
    "v_max3_u32 v252, v240, v241, v252\n\t" \
    "v_max3_u32 v252, v242, v243, v252\n\t" \
    "v_max3_u32 v252, v244, v245, v252\n\t" \
    "v_max3_u32 v252, v246, v247, v252\n\t" \
    "v_max3_u32 v252, v248, v249, v252\n\t" \
    "v_max3_u32 v252, v250, v251, v252\n\t" \
    "v_cmp_eq_u32 vcc, -1, v252\n\t" \
    "s_cbranch_vccz .Lhw%=\n\t" \
    "s_sleep 1\n\t" \
    "v_max_u32 v252, v232, v233\n\t" \
    "v_max3_u32 v252, v234, v235, v252\n\t" \
    "v_max3_u32 v252, v236, v237, v252\n\t" \
    "v_max3_u32 v252, v238, v239, v252\n\t" \
    "v_max3_u32 v252, v240, v241, v252\n\t" \
    "v_max3_u32 v252, v242, v243, v252\n\t" \
    "v_max3_u32 v252, v244, v245, v252\n\t" \
    "v_max3_u32 v252, v246, v247, v252\n\t" \
    "v_max3_u32 v252, v248, v249, v252\n\t" \
    "v_max3_u32 v252, v250, v251, v252\n\t" \
    "v_cmp_eq_u32 vcc, -1, v252\n\t" \
    "s_cbranch_vccz .Lhw%=\n\t" \
    "s_sleep 1\n\t" \
    "v_max_u32 v252, v232, v233\n\t" \
    "v_max3_u32 v252, v234, v235, v252\n\t" \
    "v_max3_u32 v252, v236, v237, v252\n\t" \
    "v_max3_u32 v252, v238, v239, v252\n\t" \
    "v_max3_u32 v252, v240, v241, v252\n\t" \
    "v_max3_u32 v252, v242, v243, v252\n\t" \
    "v_max3_u32 v252, v244, v245, v252\n\t" \
    "v_max3_u32 v252, v246, v247, v252\n\t" \
    "v_max3_u32 v252, v248, v249, v252\n\t" \
    "v_max3_u32 v252, v250, v251, v252\n\t" \
    "v_cmp_eq_u32 vcc, -1, v252\n\t" \
    "s_cbranch_vccz .Lhw%=\n\t" \
    "s_sleep 1\n\t" \
    "v_max_u32 v252, v232, v233\n\t" \
    "v_max3_u32 v252, v234, v235, v252\n\t" \
    "v_max3_u32 v252, v236, v237, v252\n\t" \
    "v_max3_u32 v252, v238, v239, v252\n\t" \
    "v_max3_u32 v252, v240, v241, v252\n\t" \
    "v_max3_u32 v252, v242, v243, v252\n\t" \
    "v_max3_u32 v252, v244, v245, v252\n\t" \
    "v_max3_u32 v252, v246, v247, v252\n\t" \
    "v_max3_u32 v252, v248, v249, v252\n\t" \
    "v_max3_u32 v252, v250, v251, v252\n\t" \
    "v_cmp_eq_u32 vcc, -1, v252\n\t" \
    "s_cbranch_vccz .Lhw%=\n\t" \
    "s_sleep 1\n\t" \
    "s_sub_u32 %[n], %[n], 1\n\t" \
    "s_cmp_gt_i32 %[n], 0\n\t" \
    "s_cbranch_scc1 .Lfw%=\n\t" \
    ".Lhw%=:\n\t" \
    "s_branch .Lta%=\n\t" \
    ".Lt0%=:\n\t" \
    "s_waitcnt vmcnt(1)\n\t" \
    ".Lta%=:\n\t" \
    "ds_write_b128 %[lw], v[232:235] offset:0\n\t" \
    "v_max_u32 v252, v232, v233\n\t" \
    "v_max3_u32 v252, v234, v235, v252\n\t" \
    "v_mfma_f32_16x16x32_bf16 v[188:191], v[232:235], v[232:235], 0\n\t" \
    "v_mfma_f32_16x16x32_bf16 v[192:195], v[216:219], v[232:235], 0\n\t" \
    "ds_write_b128 %[lw], v[236:239] offset:1024\n\t" \
    "v_max3_u32 v252, v236, v237, v252\n\t" \
    "v_max3_u32 v252, v238, v239, v252\n\t" \
    "v_mfma_f32_16x16x32_bf16 v[188:191], v[236:239], v[236:239], v[188:191]\n\t" \
    "v_mfma_f32_16x16x32_bf16 v[192:195], v[216:219], v[236:239], v[192:195]\n\t" \
    "ds_write_b128 %[lw], v[240:243] offset:2048\n\t" \
    "v_max3_u32 v252, v240, v241, v252\n\t" \
    "v_max3_u32 v252, v242, v243, v252\n\t" \
    "v_mfma_f32_16x16x32_bf16 v[188:191], v[240:243], v[240:243], v[188:191]\n\t" \
    "v_mfma_f32_16x16x32_bf16 v[192:195], v[216:219], v[240:243], v[192:195]\n\t" \
    "ds_write_b128 %[lw], v[244:247] offset:3072\n\t" \
    "v_max3_u32 v252, v244, v245, v252\n\t" \
    "v_max3_u32 v252, v246, v247, v252\n\t" \
    "v_mfma_f32_16x16x32_bf16 v[188:191], v[244:247], v[244:247], v[188:191]\n\t" \
    "v_mfma_f32_16x16x32_bf16 v[192:195], v[216:219], v[244:247], v[192:195]\n\t" \
    "ds_write_b128 %[lw], v[248:251] offset:4096\n\t" \
    "v_max3_u32 v252, v248, v249, v252\n\t" \
    "v_max3_u32 v252, v250, v251, v252\n\t" \
    "v_mfma_f32_16x16x32_bf16 v[188:191], v[248:251], v[248:251], v[188:191]\n\t" \
    "v_mfma_f32_16x16x32_bf16 v[192:195], v[216:219], v[248:251], v[192:195]\n\t" \
    "v_cmp_eq_u32 vcc, -1, v252\n\t" \
    "s_cbranch_vccnz .Ltr%=\n\t" \
    ".Ltg%=:\n\t" \
    ASM_ST2 \
    "s_nop 5\n\t" \
    "ds_write_b128 %[lw], v[188:191] offset:5120\n\t" \
    "ds_write_b128 %[lw], v[192:195] offset:6144\n\t" \
    "s_waitcnt lgkmcnt(0)\n\t" \
    "s_setprio 2\n\t" \
    "s_barrier\n\t" ASM_ST \
    "ds_read_b128 v[160:163], %[la] offset:0\n\t" \
    "ds_read_b128 v[164:167], %[la] offset:1024\n\t" \
    "ds_read_b128 v[168:171], %[la] offset:2048\n\t" \
    "ds_read2st64_b32 v[204:205], %[sr] offset0:20 offset1:24\n\t" \
    "ds_read_b128 v[172:175], %[la] offset:3072\n\t" \
    "ds_read_b128 v[176:179], %[la] offset:8192\n\t" \
    "ds_read_b128 v[180:183], %[la] offset:9216\n\t" \
    "s_waitcnt lgkmcnt(6)\n\t" \
    "v_mfma_f32_16x16x32_bf16 v[188:191], v[0:3], v[160:163], 0\n\t" \
    "ds_read_b128 v[184:187], %[la] offset:10240\n\t" \
    "v_mfma_f32_16x16x32_bf16 v[192:195], v[80:83], v[160:163], 0\n\t" \
    "s_waitcnt lgkmcnt(6)\n\t" \
    "v_mfma_f32_16x16x32_bf16 v[188:191], v[4:7], v[164:167], v[188:191]\n\t" \
    "ds_read_b128 v[160:163], %[la] offset:11264\n\t" \
    "v_mfma_f32_16x16x32_bf16 v[192:195], v[84:87], v[164:167], v[192:195]\n\t" \
    "s_waitcnt lgkmcnt(6)\n\t" \
    "v_mfma_f32_16x16x32_bf16 v[188:191], v[8:11], v[168:171], v[188:191]\n\t" \
    "ds_read_b128 v[164:167], %[la] offset:16384\n\t" \
    "s_waitcnt lgkmcnt(6)\n\t" \
    "v_mfma_f32_16x16x32_bf16 v[192:195], v[88:91], v[168:171], v[192:195]\n\t" \
    "v_permlane16_swap_b32 v204, v205\n\t" \
    "s_waitcnt lgkmcnt(5)\n\t" \
    "v_mfma_f32_16x16x32_bf16 v[188:191], v[12:15], v[172:175], v[188:191]\n\t" \
    "ds_read_b128 v[168:171], %[la] offset:17408\n\t" \
    "v_mfma_f32_16x16x32_bf16 v[192:195], v[92:95], v[172:175], v[192:195]\n\t" \
    "v_add_f32 v206, v204, v205\n\t" \
    "s_waitcnt lgkmcnt(5)\n\t" \
    "v_mfma_f32_16x16x32_bf16 v[188:191], v[16:19], v[176:179], v[188:191]\n\t" \
    "ds_read_b128 v[172:175], %[la] offset:18432\n\t" \
    "v_add_f32 v207, v204, v205\n\t" \
    "v_mfma_f32_16x16x32_bf16 v[192:195], v[96:99], v[176:179], v[192:195]\n\t" \
    "s_waitcnt lgkmcnt(5)\n\t" \
    "v_mfma_f32_16x16x32_bf16 v[188:191], v[20:23], v[180:183], v[188:191]\n\t" \
    "ds_read_b128 v[176:179], %[la] offset:19456\n\t" \
    "v_permlane32_swap_b32 v206, v207\n\t" \
    "v_mfma_f32_16x16x32_bf16 v[192:195], v[100:103], v[180:183], v[192:195]\n\t" \
    "s_waitcnt lgkmcnt(5)\n\t" \
    "v_mfma_f32_16x16x32_bf16 v[188:191], v[24:27], v[184:187], v[188:191]\n\t" \
    "ds_read_b128 v[180:183], %[la] offset:24576\n\t" \
    "v_add_f32 v204, v206, v207\n\t" \
    "v_mfma_f32_16x16x32_bf16 v[192:195], v[104:107], v[184:187], v[192:195]\n\t" \
    "v_add_f32 v205, v206, v207\n\t" \
    "s_waitcnt lgkmcnt(5)\n\t" \
    "v_mfma_f32_16x16x32_bf16 v[188:191], v[28:31], v[160:163], v[188:191]\n\t" \
    "ds_read_b128 v[184:187], %[la] offset:25600\n\t" \
    "v_mfma_f32_16x16x32_bf16 v[192:195], v[108:111], v[160:163], v[192:195]\n\t" \
    "v_permlane16_swap_b32 v204, v205\n\t" \
    "s_waitcnt lgkmcnt(5)\n\t" \
    "v_mfma_f32_16x16x32_bf16 v[188:191], v[32:35], v[164:167], v[188:191]\n\t" \
    "ds_read_b128 v[160:163], %[la] offset:26624\n\t" \
    "v_mfma_f32_16x16x32_bf16 v[192:195], v[112:115], v[164:167], v[192:195]\n\t" \
    "v_mul_f32 v205, %[kd], v205\n\t" \
    "s_waitcnt lgkmcnt(5)\n\t" \
    "v_mfma_f32_16x16x32_bf16 v[188:191], v[36:39], v[168:171], v[188:191]\n\t" \
    "ds_read_b128 v[164:167], %[la] offset:27648\n\t" \
    "v_mul_f32 v204, %[kd], v204\n\t" \
    "v_mfma_f32_16x16x32_bf16 v[192:195], v[116:119], v[168:171], v[192:195]\n\t" \
    "v_fma_f32 v204, -v205, v205, v204\n\t" \
    "s_waitcnt lgkmcnt(5)\n\t" \
    "v_mfma_f32_16x16x32_bf16 v[188:191], v[40:43], v[172:175], v[188:191]\n\t" \
    "ds_read_b128 v[168:171], %[la] offset:4096\n\t" \
    "v_max_f32 v204, 0, v204\n\t" \
    "v_mfma_f32_16x16x32_bf16 v[192:195], v[120:123], v[172:175], v[192:195]\n\t" \
    "v_add_f32 v206, %[ke], v204\n\t" \
    "s_waitcnt lgkmcnt(5)\n\t" \
    "v_mfma_f32_16x16x32_bf16 v[188:191], v[44:47], v[176:179], v[188:191]\n\t" \
    "ds_read_b128 v[172:175], %[la] offset:12288\n\t" \
    "v_rsq_f32 v206, v206\n\t" \
    "v_mfma_f32_16x16x32_bf16 v[192:195], v[124:127], v[176:179], v[192:195]\n\t" \
    "s_waitcnt lgkmcnt(5)\n\t" \
    "v_mfma_f32_16x16x32_bf16 v[188:191], v[48:51], v[180:183], v[188:191]\n\t" \
    "ds_read_b128 v[176:179], %[la] offset:20480\n\t" \
    "v_mul_f32 v207, v204, v206\n\t" \
    "v_mfma_f32_16x16x32_bf16 v[192:195], v[128:131], v[180:183], v[192:195]\n\t" \
    "v_fma_f32 v207, v207, v206, %[ke]\n\t" \
    "s_waitcnt lgkmcnt(5)\n\t" \
    "v_mfma_f32_16x16x32_bf16 v[188:191], v[52:55], v[184:187], v[188:191]\n\t" \
    "ds_read_b128 v[180:183], %[la] offset:28672\n\t" \
    "v_rsq_f32 v207, v207\n\t" \
    "v_mfma_f32_16x16x32_bf16 v[192:195], v[132:135], v[184:187], v[192:195]\n\t" \
    "v_mul_f32 v206, v206, v207\n\t" \
    "v_mul_f32 v207, -v205, v206\n\t" \
    "s_waitcnt lgkmcnt(5)\n\t" \
    "v_mfma_f32_16x16x32_bf16 v[188:191], v[56:59], v[160:163], v[188:191]\n\t" \
    "v_mul_f32 v208, v196, v207\n\t" \
    "v_mul_f32 v209, v197, v207\n\t" \
    "v_mfma_f32_16x16x32_bf16 v[192:195], v[136:139], v[160:163], v[192:195]\n\t" \
    "v_mul_f32 v210, v198, v207\n\t" \
    "v_mul_f32 v211, v199, v207\n\t" \
    "s_waitcnt lgkmcnt(4)\n\t" \
    "v_mfma_f32_16x16x32_bf16 v[188:191], v[60:63], v[164:167], v[188:191]\n\t" \
    "v_mul_f32 v212, v200, v207\n\t" \
    "v_mul_f32 v213, v201, v207\n\t" \
    "v_mfma_f32_16x16x32_bf16 v[192:195], v[140:143], v[164:167], v[192:195]\n\t" \
    "v_mul_f32 v214, v202, v207\n\t" \
    "v_mul_f32 v215, v203, v207\n\t" \
    "s_waitcnt lgkmcnt(3)\n\t" \
    "v_mfma_f32_16x16x32_bf16 v[188:191], v[64:67], v[168:171], v[188:191]\n\t" \
    "s_cmp_lg_u32 %[sw], 0\n\t" \
    "s_cbranch_scc1 .Lts%=\n\t" \
    "v_cvt_pk_bf16_f32 v217, v204, v204\n\t" \
    "v_lshlrev_b32 v217, 16, v217\n\t" \
    "v_add_f32 v217, %[ke], v217\n\t" \
    "v_cvt_pk_bf16_f32 v217, v217, v217\n\t" \
    "v_lshlrev_b32 v217, 16, v217\n\t" \
    "v_sqrt_f32 v217, v217\n\t" \
    "v_cvt_pk_bf16_f32 v216, v205, v205\n\t" \
    "v_lshlrev_b32 v216, 16, v216\n\t" \
    "v_cvt_pk_bf16_f32 v217, v217, v217\n\t" \
    "v_lshlrev_b32 v217, 16, v217\n\t" \
    "v_rcp_f32 v217, v217\n\t" \
    "v_lshrrev_b32 v218, 1, %[l16]\n\t" \
    "v_and_b32 v218, 0x78, v218\n\t" \
    "buffer_store_dwordx2 v[216:217], v218, %[rs], %[ss] offen sc1\n\t" \
    ".Lts%=:\n\t" \
    "v_mov_b64 v[216:217], -1\n\t" \
    "v_mfma_f32_16x16x32_bf16 v[192:195], v[144:147], v[168:171], v[192:195]\n\t" \
    "v_mov_b64 v[218:219], -1\n\t" \
    "buffer_store_dwordx4 v[216:219], %[ob], %[rs], %[sa] offen sc1\n\t" \
    "s_waitcnt lgkmcnt(2)\n\t" \
    "v_mfma_f32_16x16x32_bf16 v[188:191], v[68:71], v[172:175], v[188:191]\n\t" \
    "v_mfma_f32_16x16x32_bf16 v[192:195], v[148:151], v[172:175], v[192:195]\n\t" \
    "s_waitcnt lgkmcnt(1)\n\t" \
    "v_mfma_f32_16x16x32_bf16 v[188:191], v[72:75], v[176:179], v[188:191]\n\t" \
    "v_mfma_f32_16x16x32_bf16 v[192:195], v[152:155], v[176:179], v[192:195]\n\t" \
    "s_waitcnt lgkmcnt(0)\n\t" \
    "v_mfma_f32_16x16x32_bf16 v[188:191], v[76:79], v[180:183], v[188:191]\n\t" \
    "v_mfma_f32_16x16x32_bf16 v[192:195], v[156:159], v[180:183], v[192:195]\n\t" \
    "s_setprio 2\n\t" \
    "buffer_load_dwordx4 v[232:235], %[l16], %[rs], %[n0] offen offset:0 sc1\n\t" \
    "buffer_load_dwordx4 v[236:239], %[l16], %[rs], %[n0] offen offset:1024 sc1\n\t" \
    "buffer_load_dwordx4 v[240:243], %[l16], %[rs], %[n0] offen offset:2048 sc1\n\t" \
    "buffer_load_dwordx4 v[244:247], %[l16], %[rs], %[n0] offen offset:3072 sc1\n\t" \
    "buffer_load_dwordx4 v[248:251], %[l16], %[rs], %[n1] offen offset:0 sc1\n\t" \
    ASM_ST3 \
    "v_fma_f32 v188, v188, v206, v208\n\t" \
    "v_fma_f32 v189, v189, v206, v209\n\t" \
    "v_fma_f32 v190, v190, v206, v210\n\t" \
    "v_fma_f32 v191, v191, v206, v211\n\t" \
    "v_mul_f32 v160, v188, v188\n\t" \
    "v_mul_f32 v161, v189, v189\n\t" \
    "v_mul_f32 v162, v190, v190\n\t" \
    "v_mul_f32 v163, v191, v191\n\t" \
    "v_fma_f32 v160, v160, %[k1], v220\n\t" \
    "v_fma_f32 v161, v161, %[k1], v220\n\t" \
    "v_fma_f32 v162, v162, %[k1], v220\n\t" \
    "v_fma_f32 v163, v163, %[k1], v220\n\t" \
    "v_mul_f32 v160, v188, v160\n\t" \
    "v_mul_f32 v161, v189, v161\n\t" \
    "v_mul_f32 v162, v190, v162\n\t" \
    "v_mul_f32 v163, v191, v163\n\t" \
    "v_fma_f32 v192, v192, v206, v212\n\t" \
    "v_fma_f32 v193, v193, v206, v213\n\t" \
    "v_fma_f32 v194, v194, v206, v214\n\t" \
    "v_fma_f32 v195, v195, v206, v215\n\t" \
    "v_mul_f32 v164, v192, v192\n\t" \
    "v_mul_f32 v165, v193, v193\n\t" \
    "v_mul_f32 v166, v194, v194\n\t" \
    "v_mul_f32 v167, v195, v195\n\t" \
    "v_fma_f32 v164, v164, %[k1], v220\n\t" \
    "v_fma_f32 v165, v165, %[k1], v220\n\t" \
    "v_fma_f32 v166, v166, %[k1], v220\n\t" \
    "v_fma_f32 v167, v167, %[k1], v220\n\t" \
    "v_mul_f32 v164, v192, v164\n\t" \
    "v_mul_f32 v165, v193, v165\n\t" \
    "v_mul_f32 v166, v194, v166\n\t" \
    "v_mul_f32 v167, v195, v167\n\t" \
    "v_exp_f32 v160, v160\n\t" \
    "v_exp_f32 v161, v161\n\t" \
    "v_exp_f32 v162, v162\n\t" \
    "v_exp_f32 v163, v163\n\t" \
    "v_exp_f32 v164, v164\n\t" \
    "v_exp_f32 v165, v165\n\t" \
    "v_exp_f32 v166, v166\n\t" \
    "v_exp_f32 v167, v167\n\t" \
    "v_add_f32 v160, 1.0, v160\n\t" \
    "v_add_f32 v161, 1.0, v161\n\t" \
    "v_add_f32 v162, 1.0, v162\n\t" \
    "v_add_f32 v163, 1.0, v163\n\t" \
    "v_add_f32 v164, 1.0, v164\n\t" \
    "v_add_f32 v165, 1.0, v165\n\t" \
    "v_add_f32 v166, 1.0, v166\n\t" \
    "v_add_f32 v167, 1.0, v167\n\t" \
    "v_rcp_f32 v160, v160\n\t" \
    "v_rcp_f32 v161, v161\n\t" \
    "v_rcp_f32 v162, v162\n\t" \
    "v_rcp_f32 v163, v163\n\t" \
    "v_rcp_f32 v164, v164\n\t" \
    "v_rcp_f32 v165, v165\n\t" \
    "v_rcp_f32 v166, v166\n\t" \
    "v_rcp_f32 v167, v167\n\t" \
    "v_mul_f32 v188, v188, v160\n\t" \
    "v_mul_f32 v189, v189, v161\n\t" \
    "v_mul_f32 v190, v190, v162\n\t" \
    "v_mul_f32 v191, v191, v163\n\t" \
    "v_mul_f32 v192, v192, v164\n\t" \
    "v_mul_f32 v193, v193, v165\n\t" \
    "v_mul_f32 v194, v194, v166\n\t" \
    "v_mul_f32 v195, v195, v167\n\t" \
    "v_cvt_pk_bf16_f32 v188, v188, v189\n\t" \
    "v_cvt_pk_bf16_f32 v189, v190, v191\n\t" \
    "v_cvt_pk_bf16_f32 v190, v192, v193\n\t" \
    "v_cvt_pk_bf16_f32 v191, v194, v195\n\t" \
    "v_min_u32 v188, -2, v188\n\t" \
    "v_min_u32 v189, -2, v189\n\t" \
    "v_min_u32 v190, -2, v190\n\t" \
    "v_min_u32 v191, -2, v191\n\t" \
    "v_mov_b32 v216, 0x3f803f80\n\t" \
    "v_mov_b32 v217, 0x3f803f80\n\t" \
    "v_mov_b32 v218, 0x3f803f80\n\t" \
    "v_mov_b32 v219, 0x3f803f80\n\t" \
    "v_permlane16_swap_b32 v188, v190\n\t" \
    "v_permlane16_swap_b32 v189, v191\n\t" \
    "s_waitcnt vmcnt(6)\n\t" \
    "buffer_store_dwordx4 v[188:191], %[oa], %[rs], %[so] offen sc1\n\t" \
    "s_nop 1\n\t" \
    "s_setprio 0\n\t"
#define ASM_WT1_HI \
    "s_setprio 1\n\t" \
    "s_barrier\n\t" ASM_ST \
    "ds_read_b128 v[160:163], %[la] offset:0\n\t" \
    "ds_read_b128 v[164:167], %[la] offset:1024\n\t" \
    "ds_read_b128 v[168:171], %[la] offset:2048\n\t" \
    "ds_read2st64_b32 v[204:205], %[sr] offset0:20 offset1:24\n\t" \
    "ds_read_b128 v[172:175], %[la] offset:3072\n\t" \
    "ds_read_b128 v[176:179], %[la] offset:8192\n\t" \
    "ds_read_b128 v[180:183], %[la] offset:9216\n\t" \
    "s_waitcnt lgkmcnt(6)\n\t" \
    "v_mfma_f32_16x16x32_bf16 v[188:191], v[0:3], v[160:163], 0\n\t" \
    "ds_read_b128 v[184:187], %[la] offset:10240\n\t" \
    "v_mfma_f32_16x16x32_bf16 v[192:195], v[80:83], v[160:163], 0\n\t" \
    "s_waitcnt lgkmcnt(6)\n\t" \
    "v_mfma_f32_16x16x32_bf16 v[188:191], v[4:7], v[164:167], v[188:191]\n\t" \
    "ds_read_b128 v[160:163], %[la] offset:11264\n\t" \
    "v_mfma_f32_16x16x32_bf16 v[192:195], v[84:87], v[164:167], v[192:195]\n\t" \
    "s_waitcnt lgkmcnt(6)\n\t" \
    "v_mfma_f32_16x16x32_bf16 v[188:191], v[8:11], v[168:171], v[188:191]\n\t" \
    "ds_read_b128 v[164:167], %[la] offset:16384\n\t" \
    "s_waitcnt lgkmcnt(6)\n\t" \
    "v_mfma_f32_16x16x32_bf16 v[192:195], v[88:91], v[168:171], v[192:195]\n\t" \
    "v_permlane16_swap_b32 v204, v205\n\t" \
    "s_waitcnt lgkmcnt(5)\n\t" \
    "v_mfma_f32_16x16x32_bf16 v[188:191], v[12:15], v[172:175], v[188:191]\n\t" \
    "ds_read_b128 v[168:171], %[la] offset:17408\n\t" \
    "v_mfma_f32_16x16x32_bf16 v[192:195], v[92:95], v[172:175], v[192:195]\n\t" \
    "v_add_f32 v206, v204, v205\n\t" \
    "s_waitcnt lgkmcnt(5)\n\t" \
    "v_mfma_f32_16x16x32_bf16 v[188:191], v[16:19], v[176:179], v[188:191]\n\t" \
    "ds_read_b128 v[172:175], %[la] offset:18432\n\t" \
    "v_add_f32 v207, v204, v205\n\t" \
    "v_mfma_f32_16x16x32_bf16 v[192:195], v[96:99], v[176:179], v[192:195]\n\t" \
    "s_waitcnt lgkmcnt(5)\n\t" \
    "v_mfma_f32_16x16x32_bf16 v[188:191], v[20:23], v[180:183], v[188:191]\n\t" \
    "ds_read_b128 v[176:179], %[la] offset:19456\n\t" \
    "v_permlane32_swap_b32 v206, v207\n\t" \
    "v_mfma_f32_16x16x32_bf16 v[192:195], v[100:103], v[180:183], v[192:195]\n\t" \
    "s_waitcnt lgkmcnt(5)\n\t" \
    "v_mfma_f32_16x16x32_bf16 v[188:191], v[24:27], v[184:187], v[188:191]\n\t" \
    "ds_read_b128 v[180:183], %[la] offset:24576\n\t" \
    "v_add_f32 v204, v206, v207\n\t" \
    "v_mfma_f32_16x16x32_bf16 v[192:195], v[104:107], v[184:187], v[192:195]\n\t" \
    "v_add_f32 v205, v206, v207\n\t" \
    "s_waitcnt lgkmcnt(5)\n\t" \
    "v_mfma_f32_16x16x32_bf16 v[188:191], v[28:31], v[160:163], v[188:191]\n\t" \
    "ds_read_b128 v[184:187], %[la] offset:25600\n\t" \
    "v_mfma_f32_16x16x32_bf16 v[192:195], v[108:111], v[160:163], v[192:195]\n\t" \
    "v_permlane16_swap_b32 v204, v205\n\t" \
    "s_waitcnt lgkmcnt(5)\n\t" \
    "v_mfma_f32_16x16x32_bf16 v[188:191], v[32:35], v[164:167], v[188:191]\n\t" \
    "ds_read_b128 v[160:163], %[la] offset:26624\n\t" \
    "v_mfma_f32_16x16x32_bf16 v[192:195], v[112:115], v[164:167], v[192:195]\n\t" \
    "v_mul_f32 v205, %[kd], v205\n\t" \
    "s_waitcnt lgkmcnt(5)\n\t" \
    "v_mfma_f32_16x16x32_bf16 v[188:191], v[36:39], v[168:171], v[188:191]\n\t" \
    "ds_read_b128 v[164:167], %[la] offset:27648\n\t" \
    "v_mul_f32 v204, %[kd], v204\n\t" \
    "v_mfma_f32_16x16x32_bf16 v[192:195], v[116:119], v[168:171], v[192:195]\n\t" \
    "v_fma_f32 v204, -v205, v205, v204\n\t" \
    "s_waitcnt lgkmcnt(5)\n\t" \
    "v_mfma_f32_16x16x32_bf16 v[188:191], v[40:43], v[172:175], v[188:191]\n\t" \
    "ds_read_b128 v[168:171], %[la] offset:4096\n\t" \
    "v_max_f32 v204, 0, v204\n\t" \
    "v_mfma_f32_16x16x32_bf16 v[192:195], v[120:123], v[172:175], v[192:195]\n\t" \
    "v_add_f32 v206, %[ke], v204\n\t" \
    "s_waitcnt lgkmcnt(5)\n\t" \
    "v_mfma_f32_16x16x32_bf16 v[188:191], v[44:47], v[176:179], v[188:191]\n\t" \
    "ds_read_b128 v[172:175], %[la] offset:12288\n\t" \
    "v_rsq_f32 v206, v206\n\t" \
    "v_mfma_f32_16x16x32_bf16 v[192:195], v[124:127], v[176:179], v[192:195]\n\t" \
    "s_waitcnt lgkmcnt(5)\n\t" \
    "v_mfma_f32_16x16x32_bf16 v[188:191], v[48:51], v[180:183], v[188:191]\n\t" \
    "ds_read_b128 v[176:179], %[la] offset:20480\n\t" \
    "v_mul_f32 v207, v204, v206\n\t" \
    "v_mfma_f32_16x16x32_bf16 v[192:195], v[128:131], v[180:183], v[192:195]\n\t" \
    "v_fma_f32 v207, v207, v206, %[ke]\n\t" \
    "s_waitcnt lgkmcnt(5)\n\t" \
    "v_mfma_f32_16x16x32_bf16 v[188:191], v[52:55], v[184:187], v[188:191]\n\t" \
    "ds_read_b128 v[180:183], %[la] offset:28672\n\t" \
    "v_rsq_f32 v207, v207\n\t" \
    "v_mfma_f32_16x16x32_bf16 v[192:195], v[132:135], v[184:187], v[192:195]\n\t" \
    "v_mul_f32 v206, v206, v207\n\t" \
    "v_mul_f32 v207, -v205, v206\n\t" \
    "s_waitcnt lgkmcnt(5)\n\t" \
    "v_mfma_f32_16x16x32_bf16 v[188:191], v[56:59], v[160:163], v[188:191]\n\t" \
    "v_mul_f32 v208, v196, v207\n\t" \
    "v_mul_f32 v209, v197, v207\n\t" \
    "v_mfma_f32_16x16x32_bf16 v[192:195], v[136:139], v[160:163], v[192:195]\n\t" \
    "v_mul_f32 v210, v198, v207\n\t" \
    "v_mul_f32 v211, v199, v207\n\t" \
    "s_waitcnt lgkmcnt(4)\n\t" \
    "v_mfma_f32_16x16x32_bf16 v[188:191], v[60:63], v[164:167], v[188:191]\n\t" \
    "v_mul_f32 v212, v200, v207\n\t" \
    "v_mul_f32 v213, v201, v207\n\t" \
    "v_mfma_f32_16x16x32_bf16 v[192:195], v[140:143], v[164:167], v[192:195]\n\t" \
    "v_mul_f32 v214, v202, v207\n\t" \
    "v_mul_f32 v215, v203, v207\n\t" \
    "s_waitcnt lgkmcnt(3)\n\t" \
    "v_mfma_f32_16x16x32_bf16 v[188:191], v[64:67], v[168:171], v[188:191]\n\t" \
    "v_mov_b64 v[216:217], -1\n\t" \
    "v_mov_b64 v[218:219], -1\n\t" \
    "v_mfma_f32_16x16x32_bf16 v[192:195], v[144:147], v[168:171], v[192:195]\n\t" \
    "buffer_store_dwordx4 v[216:219], %[ob], %[rs], %[sa] offen sc1\n\t" \
    "s_waitcnt lgkmcnt(2)\n\t" \
    "v_mfma_f32_16x16x32_bf16 v[188:191], v[68:71], v[172:175], v[188:191]\n\t" \
    "v_mfma_f32_16x16x32_bf16 v[192:195], v[148:151], v[172:175], v[192:195]\n\t" \
    "s_waitcnt lgkmcnt(1)\n\t" \
    "v_mfma_f32_16x16x32_bf16 v[188:191], v[72:75], v[176:179], v[188:191]\n\t" \
    "v_mfma_f32_16x16x32_bf16 v[192:195], v[152:155], v[176:179], v[192:195]\n\t" \
    "s_waitcnt lgkmcnt(0)\n\t" \
    "v_mfma_f32_16x16x32_bf16 v[188:191], v[76:79], v[180:183], v[188:191]\n\t" \
    "v_mfma_f32_16x16x32_bf16 v[192:195], v[156:159], v[180:183], v[192:195]\n\t" \
    ASM_ST3 \
    "s_nop 4\n\t" \
    "v_fma_f32 v188, v188, v206, v208\n\t" \
    "v_fma_f32 v189, v189, v206, v209\n\t" \
    "v_fma_f32 v190, v190, v206, v210\n\t" \
    "v_fma_f32 v191, v191, v206, v211\n\t" \
    "v_mul_f32 v160, v188, v188\n\t" \
    "v_mul_f32 v161, v189, v189\n\t" \
    "v_mul_f32 v162, v190, v190\n\t" \
    "v_mul_f32 v163, v191, v191\n\t" \
    "v_fma_f32 v160, v160, %[k1], v220\n\t" \
    "v_fma_f32 v161, v161, %[k1], v220\n\t" \
    "v_fma_f32 v162, v162, %[k1], v220\n\t" \
    "v_fma_f32 v163, v163, %[k1], v220\n\t" \
    "v_mul_f32 v160, v188, v160\n\t" \
    "v_mul_f32 v161, v189, v161\n\t" \
    "v_mul_f32 v162, v190, v162\n\t" \
    "v_mul_f32 v163, v191, v163\n\t" \
    "v_fma_f32 v192, v192, v206, v212\n\t" \
    "v_fma_f32 v193, v193, v206, v213\n\t" \
    "v_fma_f32 v194, v194, v206, v214\n\t" \
    "v_fma_f32 v195, v195, v206, v215\n\t" \
    "v_mul_f32 v164, v192, v192\n\t" \
    "v_mul_f32 v165, v193, v193\n\t" \
    "v_mul_f32 v166, v194, v194\n\t" \
    "v_mul_f32 v167, v195, v195\n\t" \
    "v_fma_f32 v164, v164, %[k1], v220\n\t" \
    "v_fma_f32 v165, v165, %[k1], v220\n\t" \
    "v_fma_f32 v166, v166, %[k1], v220\n\t" \
    "v_fma_f32 v167, v167, %[k1], v220\n\t" \
    "v_mul_f32 v164, v192, v164\n\t" \
    "v_mul_f32 v165, v193, v165\n\t" \
    "v_mul_f32 v166, v194, v166\n\t" \
    "v_mul_f32 v167, v195, v167\n\t" \
    "v_exp_f32 v160, v160\n\t" \
    "v_exp_f32 v161, v161\n\t" \
    "v_exp_f32 v162, v162\n\t" \
    "v_exp_f32 v163, v163\n\t" \
    "v_exp_f32 v164, v164\n\t" \
    "v_exp_f32 v165, v165\n\t" \
    "v_exp_f32 v166, v166\n\t" \
    "v_exp_f32 v167, v167\n\t" \
    "v_add_f32 v160, 1.0, v160\n\t" \
    "v_add_f32 v161, 1.0, v161\n\t" \
    "v_add_f32 v162, 1.0, v162\n\t" \
    "v_add_f32 v163, 1.0, v163\n\t" \
    "v_add_f32 v164, 1.0, v164\n\t" \
    "v_add_f32 v165, 1.0, v165\n\t" \
    "v_add_f32 v166, 1.0, v166\n\t" \
    "v_add_f32 v167, 1.0, v167\n\t" \
    "v_rcp_f32 v160, v160\n\t" \
    "v_rcp_f32 v161, v161\n\t" \
    "v_rcp_f32 v162, v162\n\t" \
    "v_rcp_f32 v163, v163\n\t" \
    "v_rcp_f32 v164, v164\n\t" \
    "v_rcp_f32 v165, v165\n\t" \
    "v_rcp_f32 v166, v166\n\t" \
    "v_rcp_f32 v167, v167\n\t" \
    "v_mul_f32 v188, v188, v160\n\t" \
    "v_mul_f32 v189, v189, v161\n\t" \
    "v_mul_f32 v190, v190, v162\n\t" \
    "v_mul_f32 v191, v191, v163\n\t" \
    "v_mul_f32 v192, v192, v164\n\t" \
    "v_mul_f32 v193, v193, v165\n\t" \
    "v_mul_f32 v194, v194, v166\n\t" \
    "v_mul_f32 v195, v195, v167\n\t" \
    "v_cvt_pk_bf16_f32 v188, v188, v189\n\t" \
    "v_cvt_pk_bf16_f32 v189, v190, v191\n\t" \
    "v_cvt_pk_bf16_f32 v190, v192, v193\n\t" \
    "v_cvt_pk_bf16_f32 v191, v194, v195\n\t" \
    "v_min_u32 v188, -2, v188\n\t" \
    "v_min_u32 v189, -2, v189\n\t" \
    "v_min_u32 v190, -2, v190\n\t" \
    "v_min_u32 v191, -2, v191\n\t" \
    "s_nop 1\n\t" \
    "v_permlane16_swap_b32 v188, v190\n\t" \
    "v_permlane16_swap_b32 v189, v191\n\t" \
    "s_waitcnt vmcnt(1)\n\t" \
    "buffer_store_dwordx4 v[188:191], %[oa], %[rs], %[so] offen sc1\n\t" \
    "s_nop 1\n\t"
#define ASM_WT2_LO \
    "s_branch .Lt0%=\n\t" \
    ".Ltr%=:\n\t" \
    "s_cmp_gt_i32 %[n], 0\n\t" \
    "s_cbranch_scc0 .Ltg%=\n\t" \
    ".Lfw%=:\n\t" \
    "s_mov_b32 %[a], 0\n\t" \
    "v_max_u32 v252, v232, v233\n\t" \
    "v_max3_u32 v252, v234, v235, v252\n\t" \
    "v_cmp_eq_u32 vcc, -1, v252\n\t" \
    "s_cbranch_vccz .Lgw0_%=\n\t" \
    "buffer_load_dwordx4 v[232:235], %[l16], %[rs], %[i0] offen offset:0 sc1\n\t" \
    "s_mov_b32 %[a], 1\n\t" \
    ".Lgw0_%=:\n\t" \
    "v_max_u32 v252, v236, v237\n\t" \
    "v_max3_u32 v252, v238, v239, v252\n\t" \
    "v_cmp_eq_u32 vcc, -1, v252\n\t" \
    "s_cbranch_vccz .Lgw1_%=\n\t" \
    "buffer_load_dwordx4 v[236:239], %[l16], %[rs], %[i0] offen offset:1024 sc1\n\t" \
    "s_mov_b32 %[a], 1\n\t" \
    ".Lgw1_%=:\n\t" \
    "v_max_u32 v252, v240, v241\n\t" \
    "v_max3_u32 v252, v242, v243, v252\n\t" \
    "v_cmp_eq_u32 vcc, -1, v252\n\t" \
    "s_cbranch_vccz .Lgw2_%=\n\t" \
    "buffer_load_dwordx4 v[240:243], %[l16], %[rs], %[i0] offen offset:2048 sc1\n\t" \
    "s_mov_b32 %[a], 1\n\t" \
    ".Lgw2_%=:\n\t" \
    "v_max_u32 v252, v244, v245\n\t" \
    "v_max3_u32 v252, v246, v247, v252\n\t" \
    "v_cmp_eq_u32 vcc, -1, v252\n\t" \
    "s_cbranch_vccz .Lgw3_%=\n\t" \
    "buffer_load_dwordx4 v[244:247], %[l16], %[rs], %[i0] offen offset:3072 sc1\n\t" \
    "s_mov_b32 %[a], 1\n\t" \
    ".Lgw3_%=:\n\t" \
    "v_max_u32 v252, v248, v249\n\t" \
    "v_max3_u32 v252, v250, v251, v252\n\t" \
    "v_cmp_eq_u32 vcc, -1, v252\n\t" \
    "s_cbranch_vccz .Lgw4_%=\n\t" \
    "buffer_load_dwordx4 v[248:251], %[l16], %[rs], %[i1] offen offset:0 sc1\n\t" \
    "s_mov_b32 %[a], 1\n\t" \
    ".Lgw4_%=:\n\t" \
    "s_cmp_eq_u32 %[a], 0\n\t" \
    "s_cbranch_scc1 .Lhw%=\n\t" \
    "s_waitcnt vmcnt(20)\n\t" \
    ".Liw%=:\n\t" \
    "v_max_u32 v252, v232, v233\n\t" \
    "v_max3_u32 v252, v234, v235, v252\n\t" \
    "v_max3_u32 v252, v236, v237, v252\n\t" \
    "v_max3_u32 v252, v238, v239, v252\n\t" \
    "v_max3_u32 v252, v240, v241, v252\n\t" \
    "v_max3_u32 v252, v242, v243, v252\n\t" \
    "v_max3_u32 v252, v244, v245, v252\n\t" \
    "v_max3_u32 v252, v246, v247, v252\n\t" \
    "v_max3_u32 v252, v248, v249, v252\n\t" \
    "v_max3_u32 v252, v250, v251, v252\n\t" \
    "v_cmp_eq_u32 vcc, -1, v252\n\t" \
    "s_cbranch_vccz .Lhw%=\n\t" \
    "s_sleep 1\n\t" \
    "v_max_u32 v252, v232, v233\n\t" \
    "v_max3_u32 v252, v234, v235, v252\n\t" \
    "v_max3_u32 v252, v236, v237, v252\n\t" \
    "v_max3_u32 v252, v238, v239, v252\n\t" \
    "v_max3_u32 v252, v240, v241, v252\n\t" \
    "v_max3_u32 v252, v242, v243, v252\n\t" \
    "v_max3_u32 v252, v244, v245, v252\n\t" \
    "v_max3_u32 v252, v246, v247, v252\n\t" \
    "v_max3_u32 v252, v248, v249, v252\n\t" \
    "v_max3_u32 v252, v250, v251, v252\n\t" \
    "v_cmp_eq_u32 vcc, -1, v252\n\t" \
    "s_cbranch_vccz .Lhw%=\n\t" \
    "s_sleep 1\n\t" \
    "v_max_u32 v252, v232, v233\n\t" \
    "v_max3_u32 v252, v234, v235, v252\n\t" \
    "v_max3_u32 v252, v236, v237, v252\n\t" \
    "v_max3_u32 v252, v238, v239, v252\n\t" \
    "v_max3_u32 v252, v240, v241, v252\n\t" \
    "v_max3_u32 v252, v242, v243, v252\n\t" \
    "v_max3_u32 v252, v244, v245, v252\n\t" \
    "v_max3_u32 v252, v246, v247, v252\n\t" \
    "v_max3_u32 v252, v248, v249, v252\n\t" \
    "v_max3_u32 v252, v250, v251, v252\n\t" \
    "v_cmp_eq_u32 vcc, -1, v252\n\t" \
    "s_cbranch_vccz .Lhw%=\n\t" \
    "s_sleep 1\n\t" \
    "v_max_u32 v252, v232, v233\n\t" \
    "v_max3_u32 v252, v234, v235, v252\n\t" \
    "v_max3_u32 v252, v236, v237, v252\n\t" \
    "v_max3_u32 v252, v238, v239, v252\n\t" \
    "v_max3_u32 v252, v240, v241, v252\n\t" \
    "v_max3_u32 v252, v242, v243, v252\n\t" \
    "v_max3_u32 v252, v244, v245, v252\n\t" \
    "v_max3_u32 v252, v246, v247, v252\n\t" \
    "v_max3_u32 v252, v248, v249, v252\n\t" \
    "v_max3_u32 v252, v250, v251, v252\n\t" \
    "v_cmp_eq_u32 vcc, -1, v252\n\t" \
    "s_cbranch_vccz .Lhw%=\n\t" \
    "s_sleep 1\n\t" \
    "v_max_u32 v252, v232, v233\n\t" \
    "v_max3_u32 v252, v234, v235, v252\n\t" \
    "v_max3_u32 v252, v236, v237, v252\n\t" \
    "v_max3_u32 v252, v238, v239, v252\n\t" \
    "v_max3_u32 v252, v240, v241, v252\n\t" \
    "v_max3_u32 v252, v242, v243, v252\n\t" \
    "v_max3_u32 v252, v244, v245, v252\n\t" \
    "v_max3_u32 v252, v246, v247, v252\n\t" \
    "v_max3_u32 v252, v248, v249, v252\n\t" \
    "v_max3_u32 v252, v250, v251, v252\n\t" \
    "v_cmp_eq_u32 vcc, -1, v252\n\t" \
    "s_cbranch_vccz .Lhw%=\n\t" \
    "s_sleep 1\n\t" \
    "v_max_u32 v252, v232, v233\n\t" \
    "v_max3_u32 v252, v234, v235, v252\n\t" \
    "v_max3_u32 v252, v236, v237, v252\n\t" \
    "v_max3_u32 v252, v238, v239, v252\n\t" \
    "v_max3_u32 v252, v240, v241, v252\n\t" \
    "v_max3_u32 v252, v242, v243, v252\n\t" \
    "v_max3_u32 v252, v244, v245, v252\n\t" \
    "v_max3_u32 v252, v246, v247, v252\n\t" \
    "v_max3_u32 v252, v248, v249, v252\n\t" \
    "v_max3_u32 v252, v250, v251, v252\n\t" \
    "v_cmp_eq_u32 vcc, -1, v252\n\t" \
    "s_cbranch_vccz .Lhw%=\n\t" \
    "s_sleep 1\n\t" \
    "s_sub_u32 %[n], %[n], 1\n\t" \
    "s_cmp_gt_i32 %[n], 0\n\t" \
    "s_cbranch_scc1 .Lfw%=\n\t" \
    ".Lhw%=:\n\t" \
    "s_branch .Lta%=\n\t" \
    ".Lt0%=:\n\t" \
    "s_waitcnt vmcnt(1)\n\t" \
    ".Lta%=:\n\t" \
    "ds_write_b128 %[lw], v[232:235] offset:0\n\t" \
    "v_max_u32 v252, v232, v233\n\t" \
    "v_max3_u32 v252, v234, v235, v252\n\t" \
    "ds_write_b128 %[lw], v[236:239] offset:1024\n\t" \
    "v_max3_u32 v252, v236, v237, v252\n\t" \
    "v_max3_u32 v252, v238, v239, v252\n\t" \
    "ds_write_b128 %[lw], v[240:243] offset:2048\n\t" \
    "v_max3_u32 v252, v240, v241, v252\n\t" \
    "v_max3_u32 v252, v242, v243, v252\n\t" \
    "ds_write_b128 %[lw], v[244:247] offset:3072\n\t" \
    "v_max3_u32 v252, v244, v245, v252\n\t" \
    "v_max3_u32 v252, v246, v247, v252\n\t" \
    "ds_write_b128 %[lw], v[248:251] offset:4096\n\t" \
    "v_max3_u32 v252, v248, v249, v252\n\t" \
    "v_max3_u32 v252, v250, v251, v252\n\t" \
    "v_cmp_eq_u32 vcc, -1, v252\n\t" \
    "s_cbranch_vccnz .Ltr%=\n\t" \
    ".Ltg%=:\n\t" \
    ASM_ST2 \
    "s_waitcnt lgkmcnt(0)\n\t" \
    "s_setprio 2\n\t" \
    "s_barrier\n\t" ASM_ST \
    "ds_read_b128 v[160:163], %[la] offset:0\n\t" \
    "ds_read_b128 v[164:167], %[la] offset:1024\n\t" \
    "ds_read_b128 v[168:171], %[la] offset:2048\n\t" \
    "ds_read_b128 v[172:175], %[la] offset:3072\n\t" \
    "ds_read_b128 v[176:179], %[la] offset:8192\n\t" \
    "ds_read_b128 v[180:183], %[la] offset:9216\n\t" \
    "s_waitcnt lgkmcnt(5)\n\t" \
    "v_mfma_f32_16x16x32_bf16 v[188:191], v[0:3], v[160:163], 0\n\t" \
    "ds_read_b128 v[184:187], %[la] offset:10240\n\t" \
    "buffer_load_dwordx2 v[196:197], %[ro], %[rs], %[sx] offen sc1\n\t" \
    "v_mfma_f32_16x16x32_bf16 v[192:195], v[80:83], v[160:163], 0\n\t" \
    "buffer_load_dwordx2 v[198:199], %[ro], %[rs], %[sx] offen offset:512 sc1\n\t" \
    "s_waitcnt lgkmcnt(5)\n\t" \
    "v_mfma_f32_16x16x32_bf16 v[188:191], v[4:7], v[164:167], v[188:191]\n\t" \
    "ds_read_b128 v[160:163], %[la] offset:11264\n\t" \
    "v_lshrrev_b32 v204, 1, %[l16]\n\t" \
    "v_mfma_f32_16x16x32_bf16 v[192:195], v[84:87], v[164:167], v[192:195]\n\t" \
    "v_and_b32 v204, 0x78, v204\n\t" \
    "s_waitcnt lgkmcnt(5)\n\t" \
    "v_mfma_f32_16x16x32_bf16 v[188:191], v[8:11], v[168:171], v[188:191]\n\t" \
    "ds_read_b128 v[164:167], %[la] offset:16384\n\t" \
    "buffer_load_dwordx2 v[200:201], v204, %[rs], %[ss] offen sc1\n\t" \
    "v_mfma_f32_16x16x32_bf16 v[192:195], v[88:91], v[168:171], v[192:195]\n\t" \
    "v_mov_b64 v[216:217], -1\n\t" \
    "s_waitcnt lgkmcnt(5)\n\t" \
    "v_mfma_f32_16x16x32_bf16 v[188:191], v[12:15], v[172:175], v[188:191]\n\t" \
    "ds_read_b128 v[168:171], %[la] offset:17408\n\t" \
    "v_mov_b64 v[218:219], -1\n\t" \
    "v_mfma_f32_16x16x32_bf16 v[192:195], v[92:95], v[172:175], v[192:195]\n\t" \
    "buffer_store_dwordx4 v[216:219], %[l16], %[rs], %[sa] offen sc1\n\t" \
    "s_waitcnt lgkmcnt(5)\n\t" \
    "v_mfma_f32_16x16x32_bf16 v[188:191], v[16:19], v[176:179], v[188:191]\n\t" \
    "ds_read_b128 v[172:175], %[la] offset:18432\n\t" \
    "v_mfma_f32_16x16x32_bf16 v[192:195], v[96:99], v[176:179], v[192:195]\n\t" \
    "s_waitcnt lgkmcnt(5)\n\t" \
    "v_mfma_f32_16x16x32_bf16 v[188:191], v[20:23], v[180:183], v[188:191]\n\t" \
    "ds_read_b128 v[176:179], %[la] offset:19456\n\t" \
    "v_mfma_f32_16x16x32_bf16 v[192:195], v[100:103], v[180:183], v[192:195]\n\t" \
    "s_waitcnt lgkmcnt(5)\n\t" \
    "v_mfma_f32_16x16x32_bf16 v[188:191], v[24:27], v[184:187], v[188:191]\n\t" \
    "ds_read_b128 v[180:183], %[la] offset:24576\n\t" \
    "v_mfma_f32_16x16x32_bf16 v[192:195], v[104:107], v[184:187], v[192:195]\n\t" \
    "s_waitcnt lgkmcnt(5)\n\t" \
    "v_mfma_f32_16x16x32_bf16 v[188:191], v[28:31], v[160:163], v[188:191]\n\t" \
    "ds_read_b128 v[184:187], %[la] offset:25600\n\t" \
    "v_mfma_f32_16x16x32_bf16 v[192:195], v[108:111], v[160:163], v[192:195]\n\t" \
    "s_waitcnt lgkmcnt(5)\n\t" \
    "v_mfma_f32_16x16x32_bf16 v[188:191], v[32:35], v[164:167], v[188:191]\n\t" \
    "ds_read_b128 v[160:163], %[la] offset:26624\n\t" \
    "v_mfma_f32_16x16x32_bf16 v[192:195], v[112:115], v[164:167], v[192:195]\n\t" \
    "s_waitcnt lgkmcnt(5)\n\t" \
    "v_mfma_f32_16x16x32_bf16 v[188:191], v[36:39], v[168:171], v[188:191]\n\t" \
    "ds_read_b128 v[164:167], %[la] offset:27648\n\t" \
    "v_mfma_f32_16x16x32_bf16 v[192:195], v[116:119], v[168:171], v[192:195]\n\t" \
    "s_waitcnt lgkmcnt(5)\n\t" \
    "v_mfma_f32_16x16x32_bf16 v[188:191], v[40:43], v[172:175], v[188:191]\n\t" \
    "ds_read_b128 v[168:171], %[la] offset:4096\n\t" \
    "v_mfma_f32_16x16x32_bf16 v[192:195], v[120:123], v[172:175], v[192:195]\n\t" \
    "s_waitcnt lgkmcnt(5)\n\t" \
    "v_mfma_f32_16x16x32_bf16 v[188:191], v[44:47], v[176:179], v[188:191]\n\t" \
    "ds_read_b128 v[172:175], %[la] offset:12288\n\t" \
    "v_mfma_f32_16x16x32_bf16 v[192:195], v[124:127], v[176:179], v[192:195]\n\t" \
    "s_waitcnt lgkmcnt(5)\n\t" \
    "v_mfma_f32_16x16x32_bf16 v[188:191], v[48:51], v[180:183], v[188:191]\n\t" \
    "ds_read_b128 v[176:179], %[la] offset:20480\n\t" \
    "v_mfma_f32_16x16x32_bf16 v[192:195], v[128:131], v[180:183], v[192:195]\n\t" \
    "s_waitcnt lgkmcnt(5)\n\t" \
    "v_mfma_f32_16x16x32_bf16 v[188:191], v[52:55], v[184:187], v[188:191]\n\t" \
    "ds_read_b128 v[180:183], %[la] offset:28672\n\t" \
    "v_mfma_f32_16x16x32_bf16 v[192:195], v[132:135], v[184:187], v[192:195]\n\t" \
    "s_waitcnt lgkmcnt(5)\n\t" \
    "v_mfma_f32_16x16x32_bf16 v[188:191], v[56:59], v[160:163], v[188:191]\n\t" \
    "v_mfma_f32_16x16x32_bf16 v[192:195], v[136:139], v[160:163], v[192:195]\n\t" \
    "s_waitcnt lgkmcnt(4)\n\t" \
    "v_mfma_f32_16x16x32_bf16 v[188:191], v[60:63], v[164:167], v[188:191]\n\t" \
    "v_mfma_f32_16x16x32_bf16 v[192:195], v[140:143], v[164:167], v[192:195]\n\t" \
    "s_waitcnt lgkmcnt(3)\n\t" \
    "v_mfma_f32_16x16x32_bf16 v[188:191], v[64:67], v[168:171], v[188:191]\n\t" \
    "v_mfma_f32_16x16x32_bf16 v[192:195], v[144:147], v[168:171], v[192:195]\n\t" \
    "s_waitcnt lgkmcnt(2)\n\t" \
    "v_mfma_f32_16x16x32_bf16 v[188:191], v[68:71], v[172:175], v[188:191]\n\t" \
    "v_mfma_f32_16x16x32_bf16 v[192:195], v[148:151], v[172:175], v[192:195]\n\t" \
    "s_waitcnt lgkmcnt(1)\n\t" \
    "v_mfma_f32_16x16x32_bf16 v[188:191], v[72:75], v[176:179], v[188:191]\n\t" \
    "v_mfma_f32_16x16x32_bf16 v[192:195], v[152:155], v[176:179], v[192:195]\n\t" \
    "s_waitcnt lgkmcnt(0)\n\t" \
    "v_mfma_f32_16x16x32_bf16 v[188:191], v[76:79], v[180:183], v[188:191]\n\t" \
    "v_mfma_f32_16x16x32_bf16 v[192:195], v[156:159], v[180:183], v[192:195]\n\t" \
    "s_setprio 2\n\t" \
    "buffer_load_dwordx4 v[232:235], %[l16], %[rs], %[n0] offen offset:0 sc1\n\t" \
    "buffer_load_dwordx4 v[236:239], %[l16], %[rs], %[n0] offen offset:1024 sc1\n\t" \
    "buffer_load_dwordx4 v[240:243], %[l16], %[rs], %[n0] offen offset:2048 sc1\n\t" \
    "buffer_load_dwordx4 v[244:247], %[l16], %[rs], %[n0] offen offset:3072 sc1\n\t" \
    "buffer_load_dwordx4 v[248:251], %[l16], %[rs], %[n1] offen offset:0 sc1\n\t" \
    ASM_ST3 \
    "s_waitcnt vmcnt(6)\n\t" \
    "v_lshlrev_b32 v160, 16, v196\n\t" \
    "v_and_b32 v161, 0xffff0000, v196\n\t" \
    "v_lshlrev_b32 v162, 16, v197\n\t" \
    "v_and_b32 v163, 0xffff0000, v197\n\t" \
    "v_lshlrev_b32 v164, 16, v198\n\t" \
    "v_and_b32 v165, 0xffff0000, v198\n\t" \
    "v_lshlrev_b32 v166, 16, v199\n\t" \
    "v_and_b32 v167, 0xffff0000, v199\n\t" \
    "v_sub_f32 v160, v160, v200\n\t" \
    "v_sub_f32 v161, v161, v200\n\t" \
    "v_sub_f32 v162, v162, v200\n\t" \
    "v_sub_f32 v163, v163, v200\n\t" \
    "v_sub_f32 v164, v164, v200\n\t" \
    "v_sub_f32 v165, v165, v200\n\t" \
    "v_sub_f32 v166, v166, v200\n\t" \
    "v_sub_f32 v167, v167, v200\n\t" \
    "v_mul_f32 v160, v160, v201\n\t" \
    "v_mul_f32 v161, v161, v201\n\t" \
    "v_mul_f32 v162, v162, v201\n\t" \
    "v_mul_f32 v163, v163, v201\n\t" \
    "v_mul_f32 v164, v164, v201\n\t" \
    "v_mul_f32 v165, v165, v201\n\t" \
    "v_mul_f32 v166, v166, v201\n\t" \
    "v_mul_f32 v167, v167, v201\n\t" \
    "v_cvt_pk_bf16_f32 v160, v160, v161\n\t" \
    "v_cvt_pk_bf16_f32 v162, v162, v163\n\t" \
    "v_cvt_pk_bf16_f32 v164, v164, v165\n\t" \
    "v_cvt_pk_bf16_f32 v166, v166, v167\n\t" \
    "v_and_b32 v161, 0xffff0000, v160\n\t" \
    "v_lshlrev_b32 v160, 16, v160\n\t" \
    "v_and_b32 v163, 0xffff0000, v162\n\t" \
    "v_lshlrev_b32 v162, 16, v162\n\t" \
    "v_and_b32 v165, 0xffff0000, v164\n\t" \
    "v_lshlrev_b32 v164, 16, v164\n\t" \
    "v_and_b32 v167, 0xffff0000, v166\n\t" \
    "v_lshlrev_b32 v166, 16, v166\n\t" \
    "v_add_f32 v188, v188, v160\n\t" \
    "v_add_f32 v189, v189, v161\n\t" \
    "v_add_f32 v190, v190, v162\n\t" \
    "v_add_f32 v191, v191, v163\n\t" \
    "v_add_f32 v192, v192, v164\n\t" \
    "v_add_f32 v193, v193, v165\n\t" \
    "v_add_f32 v194, v194, v166\n\t" \
    "v_add_f32 v195, v195, v167\n\t" \
    "v_cvt_pk_bf16_f32 v188, v188, v189\n\t" \
    "v_cvt_pk_bf16_f32 v189, v190, v191\n\t" \
    "v_cvt_pk_bf16_f32 v190, v192, v193\n\t" \
    "v_cvt_pk_bf16_f32 v191, v194, v195\n\t" \
    "v_min_u32 v188, -2, v188\n\t" \
    "v_min_u32 v189, -2, v189\n\t" \
    "v_min_u32 v190, -2, v190\n\t" \
    "v_min_u32 v191, -2, v191\n\t" \
    "s_nop 1\n\t" \
    "v_permlane16_swap_b32 v188, v190\n\t" \
    "v_permlane16_swap_b32 v189, v191\n\t" \
    "s_waitcnt vmcnt(6)\n\t" \
    "buffer_store_dwordx4 v[188:191], %[oa], %[rs], %[so] offen sc1\n\t" \
    "s_nop 1\n\t" \
    "s_setprio 0\n\t"
#define ASM_WT2_HI \
    "s_setprio 1\n\t" \
    "s_barrier\n\t" ASM_ST \
    "ds_read_b128 v[160:163], %[la] offset:0\n\t" \
    "ds_read_b128 v[164:167], %[la] offset:1024\n\t" \
    "ds_read_b128 v[168:171], %[la] offset:2048\n\t" \
    "ds_read_b128 v[172:175], %[la] offset:3072\n\t" \
    "ds_read_b128 v[176:179], %[la] offset:8192\n\t" \
    "ds_read_b128 v[180:183], %[la] offset:9216\n\t" \
    "s_waitcnt lgkmcnt(5)\n\t" \
    "v_mfma_f32_16x16x32_bf16 v[188:191], v[0:3], v[160:163], 0\n\t" \
    "ds_read_b128 v[184:187], %[la] offset:10240\n\t" \
    "buffer_load_dwordx2 v[196:197], %[ro], %[rs], %[sx] offen sc1\n\t" \
    "v_mfma_f32_16x16x32_bf16 v[192:195], v[80:83], v[160:163], 0\n\t" \
    "buffer_load_dwordx2 v[198:199], %[ro], %[rs], %[sx] offen offset:512 sc1\n\t" \
    "s_waitcnt lgkmcnt(5)\n\t" \
    "v_mfma_f32_16x16x32_bf16 v[188:191], v[4:7], v[164:167], v[188:191]\n\t" \
    "ds_read_b128 v[160:163], %[la] offset:11264\n\t" \
    "v_lshrrev_b32 v204, 1, %[l16]\n\t" \
    "v_mfma_f32_16x16x32_bf16 v[192:195], v[84:87], v[164:167], v[192:195]\n\t" \
    "v_and_b32 v204, 0x78, v204\n\t" \
    "s_waitcnt lgkmcnt(5)\n\t" \
    "v_mfma_f32_16x16x32_bf16 v[188:191], v[8:11], v[168:171], v[188:191]\n\t" \
    "ds_read_b128 v[164:167], %[la] offset:16384\n\t" \
    "buffer_load_dwordx2 v[200:201], v204, %[rs], %[ss] offen sc1\n\t" \
    "v_mfma_f32_16x16x32_bf16 v[192:195], v[88:91], v[168:171], v[192:195]\n\t" \
    "v_mov_b64 v[216:217], -1\n\t" \
    "s_waitcnt lgkmcnt(5)\n\t" \
    "v_mfma_f32_16x16x32_bf16 v[188:191], v[12:15], v[172:175], v[188:191]\n\t" \
    "ds_read_b128 v[168:171], %[la] offset:17408\n\t" \
    "v_mov_b64 v[218:219], -1\n\t" \
    "v_mfma_f32_16x16x32_bf16 v[192:195], v[92:95], v[172:175], v[192:195]\n\t" \
    "buffer_store_dwordx4 v[216:219], %[l16], %[rs], %[sa] offen sc1\n\t" \
    "s_waitcnt lgkmcnt(5)\n\t" \
    "v_mfma_f32_16x16x32_bf16 v[188:191], v[16:19], v[176:179], v[188:191]\n\t" \
    "ds_read_b128 v[172:175], %[la] offset:18432\n\t" \
    "v_mfma_f32_16x16x32_bf16 v[192:195], v[96:99], v[176:179], v[192:195]\n\t" \
    "s_waitcnt lgkmcnt(5)\n\t" \
    "v_mfma_f32_16x16x32_bf16 v[188:191], v[20:23], v[180:183], v[188:191]\n\t" \
    "ds_read_b128 v[176:179], %[la] offset:19456\n\t" \
    "v_mfma_f32_16x16x32_bf16 v[192:195], v[100:103], v[180:183], v[192:195]\n\t" \
    "s_waitcnt lgkmcnt(5)\n\t" \
    "v_mfma_f32_16x16x32_bf16 v[188:191], v[24:27], v[184:187], v[188:191]\n\t" \
    "ds_read_b128 v[180:183], %[la] offset:24576\n\t" \
    "v_mfma_f32_16x16x32_bf16 v[192:195], v[104:107], v[184:187], v[192:195]\n\t" \
    "s_waitcnt lgkmcnt(5)\n\t" \
    "v_mfma_f32_16x16x32_bf16 v[188:191], v[28:31], v[160:163], v[188:191]\n\t" \
    "ds_read_b128 v[184:187], %[la] offset:25600\n\t" \
    "v_mfma_f32_16x16x32_bf16 v[192:195], v[108:111], v[160:163], v[192:195]\n\t" \
    "s_waitcnt lgkmcnt(5)\n\t" \
    "v_mfma_f32_16x16x32_bf16 v[188:191], v[32:35], v[164:167], v[188:191]\n\t" \
    "ds_read_b128 v[160:163], %[la] offset:26624\n\t" \
    "v_mfma_f32_16x16x32_bf16 v[192:195], v[112:115], v[164:167], v[192:195]\n\t" \
    "s_waitcnt lgkmcnt(5)\n\t" \
    "v_mfma_f32_16x16x32_bf16 v[188:191], v[36:39], v[168:171], v[188:191]\n\t" \
    "ds_read_b128 v[164:167], %[la] offset:27648\n\t" \
    "v_mfma_f32_16x16x32_bf16 v[192:195], v[116:119], v[168:171], v[192:195]\n\t" \
    "s_waitcnt lgkmcnt(5)\n\t" \
    "v_mfma_f32_16x16x32_bf16 v[188:191], v[40:43], v[172:175], v[188:191]\n\t" \
    "ds_read_b128 v[168:171], %[la] offset:4096\n\t" \
    "v_mfma_f32_16x16x32_bf16 v[192:195], v[120:123], v[172:175], v[192:195]\n\t" \
    "s_waitcnt lgkmcnt(5)\n\t" \
    "v_mfma_f32_16x16x32_bf16 v[188:191], v[44:47], v[176:179], v[188:191]\n\t" \
    "ds_read_b128 v[172:175], %[la] offset:12288\n\t" \
    "v_mfma_f32_16x16x32_bf16 v[192:195], v[124:127], v[176:179], v[192:195]\n\t" \
    "s_waitcnt lgkmcnt(5)\n\t" \
    "v_mfma_f32_16x16x32_bf16 v[188:191], v[48:51], v[180:183], v[188:191]\n\t" \
    "ds_read_b128 v[176:179], %[la] offset:20480\n\t" \
    "v_mfma_f32_16x16x32_bf16 v[192:195], v[128:131], v[180:183], v[192:195]\n\t" \
    "s_waitcnt lgkmcnt(5)\n\t" \
    "v_mfma_f32_16x16x32_bf16 v[188:191], v[52:55], v[184:187], v[188:191]\n\t" \
    "ds_read_b128 v[180:183], %[la] offset:28672\n\t" \
    "v_mfma_f32_16x16x32_bf16 v[192:195], v[132:135], v[184:187], v[192:195]\n\t" \
    "s_waitcnt lgkmcnt(5)\n\t" \
    "v_mfma_f32_16x16x32_bf16 v[188:191], v[56:59], v[160:163], v[188:191]\n\t" \
    "v_mfma_f32_16x16x32_bf16 v[192:195], v[136:139], v[160:163], v[192:195]\n\t" \
    "s_waitcnt lgkmcnt(4)\n\t" \
    "v_mfma_f32_16x16x32_bf16 v[188:191], v[60:63], v[164:167], v[188:191]\n\t" \
    "v_mfma_f32_16x16x32_bf16 v[192:195], v[140:143], v[164:167], v[192:195]\n\t" \
    "s_waitcnt lgkmcnt(3)\n\t" \
    "v_mfma_f32_16x16x32_bf16 v[188:191], v[64:67], v[168:171], v[188:191]\n\t" \
    "v_mfma_f32_16x16x32_bf16 v[192:195], v[144:147], v[168:171], v[192:195]\n\t" \
    "s_waitcnt lgkmcnt(2)\n\t" \
    "v_mfma_f32_16x16x32_bf16 v[188:191], v[68:71], v[172:175], v[188:191]\n\t" \
    "v_mfma_f32_16x16x32_bf16 v[192:195], v[148:151], v[172:175], v[192:195]\n\t" \
    "s_waitcnt lgkmcnt(1)\n\t" \
    "v_mfma_f32_16x16x32_bf16 v[188:191], v[72:75], v[176:179], v[188:191]\n\t" \
    "v_mfma_f32_16x16x32_bf16 v[192:195], v[152:155], v[176:179], v[192:195]\n\t" \
    "s_waitcnt lgkmcnt(0)\n\t" \
    "v_mfma_f32_16x16x32_bf16 v[188:191], v[76:79], v[180:183], v[188:191]\n\t" \
    "v_mfma_f32_16x16x32_bf16 v[192:195], v[156:159], v[180:183], v[192:195]\n\t" \
    ASM_ST3 \
    "s_waitcnt vmcnt(1)\n\t" \
    "v_lshlrev_b32 v160, 16, v196\n\t" \
    "v_and_b32 v161, 0xffff0000, v196\n\t" \
    "v_lshlrev_b32 v162, 16, v197\n\t" \
    "v_and_b32 v163, 0xffff0000, v197\n\t" \
    "v_lshlrev_b32 v164, 16, v198\n\t" \
    "v_and_b32 v165, 0xffff0000, v198\n\t" \
    "v_lshlrev_b32 v166, 16, v199\n\t" \
    "v_and_b32 v167, 0xffff0000, v199\n\t" \
    "v_sub_f32 v160, v160, v200\n\t" \
    "v_sub_f32 v161, v161, v200\n\t" \
    "v_sub_f32 v162, v162, v200\n\t" \
    "v_sub_f32 v163, v163, v200\n\t" \
    "v_sub_f32 v164, v164, v200\n\t" \
    "v_sub_f32 v165, v165, v200\n\t" \
    "v_sub_f32 v166, v166, v200\n\t" \
    "v_sub_f32 v167, v167, v200\n\t" \
    "v_mul_f32 v160, v160, v201\n\t" \
    "v_mul_f32 v161, v161, v201\n\t" \
    "v_mul_f32 v162, v162, v201\n\t" \
    "v_mul_f32 v163, v163, v201\n\t" \
    "v_mul_f32 v164, v164, v201\n\t" \
    "v_mul_f32 v165, v165, v201\n\t" \
    "v_mul_f32 v166, v166, v201\n\t" \
    "v_mul_f32 v167, v167, v201\n\t" \
    "v_cvt_pk_bf16_f32 v160, v160, v161\n\t" \
    "v_cvt_pk_bf16_f32 v162, v162, v163\n\t" \
    "v_cvt_pk_bf16_f32 v164, v164, v165\n\t" \
    "v_cvt_pk_bf16_f32 v166, v166, v167\n\t" \
    "v_and_b32 v161, 0xffff0000, v160\n\t" \
    "v_lshlrev_b32 v160, 16, v160\n\t" \
    "v_and_b32 v163, 0xffff0000, v162\n\t" \
    "v_lshlrev_b32 v162, 16, v162\n\t" \
    "v_and_b32 v165, 0xffff0000, v164\n\t" \
    "v_lshlrev_b32 v164, 16, v164\n\t" \
    "v_and_b32 v167, 0xffff0000, v166\n\t" \
    "v_lshlrev_b32 v166, 16, v166\n\t" \
    "v_add_f32 v188, v188, v160\n\t" \
    "v_add_f32 v189, v189, v161\n\t" \
    "v_add_f32 v190, v190, v162\n\t" \
    "v_add_f32 v191, v191, v163\n\t" \
    "v_add_f32 v192, v192, v164\n\t" \
    "v_add_f32 v193, v193, v165\n\t" \
    "v_add_f32 v194, v194, v166\n\t" \
    "v_add_f32 v195, v195, v167\n\t" \
    "v_cvt_pk_bf16_f32 v188, v188, v189\n\t" \
    "v_cvt_pk_bf16_f32 v189, v190, v191\n\t" \
    "v_cvt_pk_bf16_f32 v190, v192, v193\n\t" \
    "v_cvt_pk_bf16_f32 v191, v194, v195\n\t" \
    "v_min_u32 v188, -2, v188\n\t" \
    "v_min_u32 v189, -2, v189\n\t" \
    "v_min_u32 v190, -2, v190\n\t" \
    "v_min_u32 v191, -2, v191\n\t" \
    "s_nop 1\n\t" \
    "v_permlane16_swap_b32 v188, v190\n\t" \
    "v_permlane16_swap_b32 v189, v191\n\t" \
    "s_waitcnt vmcnt(1)\n\t" \
    "buffer_store_dwordx4 v[188:191], %[oa], %[rs], %[so] offen sc1\n\t" \
    "s_nop 1\n\t"
#define ASM_WT3_LO \
    "s_branch .Lt0%=\n\t" \
    ".Ltr%=:\n\t" \
    "s_cmp_gt_i32 %[n], 0\n\t" \
    "s_cbranch_scc0 .Ltg%=\n\t" \
    ".Lfw%=:\n\t" \
    "s_mov_b32 %[a], 0\n\t" \
    "v_max_u32 v252, v232, v233\n\t" \
    "v_max3_u32 v252, v234, v235, v252\n\t" \
    "v_cmp_eq_u32 vcc, -1, v252\n\t" \
    "s_cbranch_vccz .Lgw0_%=\n\t" \
    "buffer_load_dwordx4 v[232:235], %[l16], %[rs], %[i0] offen offset:0 sc1\n\t" \
    "s_mov_b32 %[a], 1\n\t" \
    ".Lgw0_%=:\n\t" \
    "v_max_u32 v252, v236, v237\n\t" \
    "v_max3_u32 v252, v238, v239, v252\n\t" \
    "v_cmp_eq_u32 vcc, -1, v252\n\t" \
    "s_cbranch_vccz .Lgw1_%=\n\t" \
    "buffer_load_dwordx4 v[236:239], %[l16], %[rs], %[i0] offen offset:1024 sc1\n\t" \
    "s_mov_b32 %[a], 1\n\t" \
    ".Lgw1_%=:\n\t" \
    "v_max_u32 v252, v240, v241\n\t" \
    "v_max3_u32 v252, v242, v243, v252\n\t" \
    "v_cmp_eq_u32 vcc, -1, v252\n\t" \
    "s_cbranch_vccz .Lgw2_%=\n\t" \
    "buffer_load_dwordx4 v[240:243], %[l16], %[rs], %[i0] offen offset:2048 sc1\n\t" \
    "s_mov_b32 %[a], 1\n\t" \
    ".Lgw2_%=:\n\t" \
    "v_max_u32 v252, v244, v245\n\t" \
    "v_max3_u32 v252, v246, v247, v252\n\t" \
    "v_cmp_eq_u32 vcc, -1, v252\n\t" \
    "s_cbranch_vccz .Lgw3_%=\n\t" \
    "buffer_load_dwordx4 v[244:247], %[l16], %[rs], %[i0] offen offset:3072 sc1\n\t" \
    "s_mov_b32 %[a], 1\n\t" \
    ".Lgw3_%=:\n\t" \
    "v_max_u32 v252, v248, v249\n\t" \
    "v_max3_u32 v252, v250, v251, v252\n\t" \
    "v_cmp_eq_u32 vcc, -1, v252\n\t" \
    "s_cbranch_vccz .Lgw4_%=\n\t" \
    "buffer_load_dwordx4 v[248:251], %[l16], %[rs], %[i1] offen offset:0 sc1\n\t" \
    "s_mov_b32 %[a], 1\n\t" \
    ".Lgw4_%=:\n\t" \
    "s_cmp_eq_u32 %[a], 0\n\t" \
    "s_cbranch_scc1 .Lhw%=\n\t" \
    "s_waitcnt vmcnt(20)\n\t" \
    ".Liw%=:\n\t" \
    "v_max_u32 v252, v232, v233\n\t" \
    "v_max3_u32 v252, v234, v235, v252\n\t" \
    "v_max3_u32 v252, v236, v237, v252\n\t" \
    "v_max3_u32 v252, v238, v239, v252\n\t" \
    "v_max3_u32 v252, v240, v241, v252\n\t" \
    "v_max3_u32 v252, v242, v243, v252\n\t" \
    "v_max3_u32 v252, v244, v245, v252\n\t" \
    "v_max3_u32 v252, v246, v247, v252\n\t" \
    "v_max3_u32 v252, v248, v249, v252\n\t" \
    "v_max3_u32 v252, v250, v251, v252\n\t" \
    "v_cmp_eq_u32 vcc, -1, v252\n\t" \
    "s_cbranch_vccz .Lhw%=\n\t" \
    "s_sleep 1\n\t" \
    "v_max_u32 v252, v232, v233\n\t" \
    "v_max3_u32 v252, v234, v235, v252\n\t" \
    "v_max3_u32 v252, v236, v237, v252\n\t" \
    "v_max3_u32 v252, v238, v239, v252\n\t" \
    "v_max3_u32 v252, v240, v241, v252\n\t" \
    "v_max3_u32 v252, v242, v243, v252\n\t" \
    "v_max3_u32 v252, v244, v245, v252\n\t" \
    "v_max3_u32 v252, v246, v247, v252\n\t" \
    "v_max3_u32 v252, v248, v249, v252\n\t" \
    "v_max3_u32 v252, v250, v251, v252\n\t" \
    "v_cmp_eq_u32 vcc, -1, v252\n\t" \
    "s_cbranch_vccz .Lhw%=\n\t" \
    "s_sleep 1\n\t" \
    "v_max_u32 v252, v232, v233\n\t" \
    "v_max3_u32 v252, v234, v235, v252\n\t" \
    "v_max3_u32 v252, v236, v237, v252\n\t" \
    "v_max3_u32 v252, v238, v239, v252\n\t" \
    "v_max3_u32 v252, v240, v241, v252\n\t" \
    "v_max3_u32 v252, v242, v243, v252\n\t" \
    "v_max3_u32 v252, v244, v245, v252\n\t" \
    "v_max3_u32 v252, v246, v247, v252\n\t" \
    "v_max3_u32 v252, v248, v249, v252\n\t" \
    "v_max3_u32 v252, v250, v251, v252\n\t" \
    "v_cmp_eq_u32 vcc, -1, v252\n\t" \
    "s_cbranch_vccz .Lhw%=\n\t" \
    "s_sleep 1\n\t" \
    "v_max_u32 v252, v232, v233\n\t" \
    "v_max3_u32 v252, v234, v235, v252\n\t" \
    "v_max3_u32 v252, v236, v237, v252\n\t" \
    "v_max3_u32 v252, v238, v239, v252\n\t" \
    "v_max3_u32 v252, v240, v241, v252\n\t" \
    "v_max3_u32 v252, v242, v243, v252\n\t" \
    "v_max3_u32 v252, v244, v245, v252\n\t" \
    "v_max3_u32 v252, v246, v247, v252\n\t" \
    "v_max3_u32 v252, v248, v249, v252\n\t" \
    "v_max3_u32 v252, v250, v251, v252\n\t" \
    "v_cmp_eq_u32 vcc, -1, v252\n\t" \
    "s_cbranch_vccz .Lhw%=\n\t" \
    "s_sleep 1\n\t" \
    "v_max_u32 v252, v232, v233\n\t" \
    "v_max3_u32 v252, v234, v235, v252\n\t" \
    "v_max3_u32 v252, v236, v237, v252\n\t" \
    "v_max3_u32 v252, v238, v239, v252\n\t" \
    "v_max3_u32 v252, v240, v241, v252\n\t" \
    "v_max3_u32 v252, v242, v243, v252\n\t" \
    "v_max3_u32 v252, v244, v245, v252\n\t" \
    "v_max3_u32 v252, v246, v247, v252\n\t" \
    "v_max3_u32 v252, v248, v249, v252\n\t" \
    "v_max3_u32 v252, v250, v251, v252\n\t" \
    "v_cmp_eq_u32 vcc, -1, v252\n\t" \
    "s_cbranch_vccz .Lhw%=\n\t" \
    "s_sleep 1\n\t" \
    "v_max_u32 v252, v232, v233\n\t" \
    "v_max3_u32 v252, v234, v235, v252\n\t" \
    "v_max3_u32 v252, v236, v237, v252\n\t" \
    "v_max3_u32 v252, v238, v239, v252\n\t" \
    "v_max3_u32 v252, v240, v241, v252\n\t" \
    "v_max3_u32 v252, v242, v243, v252\n\t" \
    "v_max3_u32 v252, v244, v245, v252\n\t" \
    "v_max3_u32 v252, v246, v247, v252\n\t" \
    "v_max3_u32 v252, v248, v249, v252\n\t" \
    "v_max3_u32 v252, v250, v251, v252\n\t" \
    "v_cmp_eq_u32 vcc, -1, v252\n\t" \
    "s_cbranch_vccz .Lhw%=\n\t" \
    "s_sleep 1\n\t" \
    "s_sub_u32 %[n], %[n], 1\n\t" \
    "s_cmp_gt_i32 %[n], 0\n\t" \
    "s_cbranch_scc1 .Lfw%=\n\t" \
    ".Lhw%=:\n\t" \
    "s_branch .Lta%=\n\t" \
    ".Lt0%=:\n\t" \
    "s_waitcnt vmcnt(1)\n\t" \
    ".Lta%=:\n\t" \
    "ds_write_b128 %[lw], v[232:235] offset:0\n\t" \
    "v_max_u32 v252, v232, v233\n\t" \
    "v_max3_u32 v252, v234, v235, v252\n\t" \
    "v_mfma_f32_16x16x32_bf16 v[188:191], v[232:235], v[232:235], 0\n\t" \
    "v_mfma_f32_16x16x32_bf16 v[192:195], v[216:219], v[232:235], 0\n\t" \
    "ds_write_b128 %[lw], v[236:239] offset:1024\n\t" \
    "v_max3_u32 v252, v236, v237, v252\n\t" \
    "v_max3_u32 v252, v238, v239, v252\n\t" \
    "v_mfma_f32_16x16x32_bf16 v[188:191], v[236:239], v[236:239], v[188:191]\n\t" \
    "v_mfma_f32_16x16x32_bf16 v[192:195], v[216:219], v[236:239], v[192:195]\n\t" \
    "ds_write_b128 %[lw], v[240:243] offset:2048\n\t" \
    "v_max3_u32 v252, v240, v241, v252\n\t" \
    "v_max3_u32 v252, v242, v243, v252\n\t" \
    "v_mfma_f32_16x16x32_bf16 v[188:191], v[240:243], v[240:243], v[188:191]\n\t" \
    "v_mfma_f32_16x16x32_bf16 v[192:195], v[216:219], v[240:243], v[192:195]\n\t" \
    "ds_write_b128 %[lw], v[244:247] offset:3072\n\t" \
    "v_max3_u32 v252, v244, v245, v252\n\t" \
    "v_max3_u32 v252, v246, v247, v252\n\t" \
    "v_mfma_f32_16x16x32_bf16 v[188:191], v[244:247], v[244:247], v[188:191]\n\t" \
    "v_mfma_f32_16x16x32_bf16 v[192:195], v[216:219], v[244:247], v[192:195]\n\t" \
    "ds_write_b128 %[lw], v[248:251] offset:4096\n\t" \
    "v_max3_u32 v252, v248, v249, v252\n\t" \
    "v_max3_u32 v252, v250, v251, v252\n\t" \
    "v_mfma_f32_16x16x32_bf16 v[188:191], v[248:251], v[248:251], v[188:191]\n\t" \
    "v_mfma_f32_16x16x32_bf16 v[192:195], v[216:219], v[248:251], v[192:195]\n\t" \
    "v_cmp_eq_u32 vcc, -1, v252\n\t" \
    "s_cbranch_vccnz .Ltr%=\n\t" \
    ".Ltg%=:\n\t" \
    ASM_ST2 \
    "s_nop 5\n\t" \
    "ds_write_b128 %[lw], v[188:191] offset:5120\n\t" \
    "ds_write_b128 %[lw], v[192:195] offset:6144\n\t" \
    "s_waitcnt lgkmcnt(0)\n\t" \
    "s_setprio 2\n\t" \
    "s_barrier\n\t" ASM_ST \
    "ds_read_b128 v[160:163], %[la] offset:0\n\t" \
    "ds_read_b128 v[164:167], %[la] offset:1024\n\t" \
    "ds_read_b128 v[168:171], %[la] offset:2048\n\t" \
    "ds_read2st64_b32 v[204:205], %[sr] offset0:20 offset1:24\n\t" \
    "ds_read_b128 v[172:175], %[la] offset:3072\n\t" \
    "ds_read_b128 v[176:179], %[la] offset:8192\n\t" \
    "ds_read_b128 v[180:183], %[la] offset:9216\n\t" \
    "s_waitcnt lgkmcnt(6)\n\t" \
    "v_mfma_f32_16x16x32_bf16 v[188:191], v[0:3], v[160:163], 0\n\t" \
    "ds_read_b128 v[184:187], %[la] offset:10240\n\t" \
    "v_mfma_f32_16x16x32_bf16 v[192:195], v[80:83], v[160:163], 0\n\t" \
    "s_waitcnt lgkmcnt(6)\n\t" \
    "v_mfma_f32_16x16x32_bf16 v[188:191], v[4:7], v[164:167], v[188:191]\n\t" \
    "ds_read_b128 v[160:163], %[la] offset:11264\n\t" \
    "v_mfma_f32_16x16x32_bf16 v[192:195], v[84:87], v[164:167], v[192:195]\n\t" \
    "s_waitcnt lgkmcnt(6)\n\t" \
    "v_mfma_f32_16x16x32_bf16 v[188:191], v[8:11], v[168:171], v[188:191]\n\t" \
    "ds_read_b128 v[164:167], %[la] offset:16384\n\t" \
    "s_waitcnt lgkmcnt(6)\n\t" \
    "v_mfma_f32_16x16x32_bf16 v[192:195], v[88:91], v[168:171], v[192:195]\n\t" \
    "v_permlane16_swap_b32 v204, v205\n\t" \
    "s_waitcnt lgkmcnt(5)\n\t" \
    "v_mfma_f32_16x16x32_bf16 v[188:191], v[12:15], v[172:175], v[188:191]\n\t" \
    "ds_read_b128 v[168:171], %[la] offset:17408\n\t" \
    "v_mfma_f32_16x16x32_bf16 v[192:195], v[92:95], v[172:175], v[192:195]\n\t" \
    "v_add_f32 v206, v204, v205\n\t" \
    "s_waitcnt lgkmcnt(5)\n\t" \
    "v_mfma_f32_16x16x32_bf16 v[188:191], v[16:19], v[176:179], v[188:191]\n\t" \
    "ds_read_b128 v[172:175], %[la] offset:18432\n\t" \
    "v_add_f32 v207, v204, v205\n\t" \
    "v_mfma_f32_16x16x32_bf16 v[192:195], v[96:99], v[176:179], v[192:195]\n\t" \
    "s_waitcnt lgkmcnt(5)\n\t" \
    "v_mfma_f32_16x16x32_bf16 v[188:191], v[20:23], v[180:183], v[188:191]\n\t" \
    "ds_read_b128 v[176:179], %[la] offset:19456\n\t" \
    "v_permlane32_swap_b32 v206, v207\n\t" \
    "v_mfma_f32_16x16x32_bf16 v[192:195], v[100:103], v[180:183], v[192:195]\n\t" \
    "s_waitcnt lgkmcnt(5)\n\t" \
    "v_mfma_f32_16x16x32_bf16 v[188:191], v[24:27], v[184:187], v[188:191]\n\t" \
    "ds_read_b128 v[180:183], %[la] offset:24576\n\t" \
    "v_add_f32 v204, v206, v207\n\t" \
    "v_mfma_f32_16x16x32_bf16 v[192:195], v[104:107], v[184:187], v[192:195]\n\t" \
    "v_add_f32 v205, v206, v207\n\t" \
    "s_waitcnt lgkmcnt(5)\n\t" \
    "v_mfma_f32_16x16x32_bf16 v[188:191], v[28:31], v[160:163], v[188:191]\n\t" \
    "ds_read_b128 v[184:187], %[la] offset:25600\n\t" \
    "v_mfma_f32_16x16x32_bf16 v[192:195], v[108:111], v[160:163], v[192:195]\n\t" \
    "v_permlane16_swap_b32 v204, v205\n\t" \
    "s_waitcnt lgkmcnt(5)\n\t" \
    "v_mfma_f32_16x16x32_bf16 v[188:191], v[32:35], v[164:167], v[188:191]\n\t" \
    "ds_read_b128 v[160:163], %[la] offset:26624\n\t" \
    "v_mfma_f32_16x16x32_bf16 v[192:195], v[112:115], v[164:167], v[192:195]\n\t" \
    "v_mul_f32 v205, %[kd], v205\n\t" \
    "s_waitcnt lgkmcnt(5)\n\t" \
    "v_mfma_f32_16x16x32_bf16 v[188:191], v[36:39], v[168:171], v[188:191]\n\t" \
    "ds_read_b128 v[164:167], %[la] offset:27648\n\t" \
    "v_mul_f32 v204, %[kd], v204\n\t" \
    "v_mfma_f32_16x16x32_bf16 v[192:195], v[116:119], v[168:171], v[192:195]\n\t" \
    "v_fma_f32 v204, -v205, v205, v204\n\t" \
    "s_waitcnt lgkmcnt(5)\n\t" \
    "v_mfma_f32_16x16x32_bf16 v[188:191], v[40:43], v[172:175], v[188:191]\n\t" \
    "ds_read_b128 v[168:171], %[la] offset:4096\n\t" \
    "v_max_f32 v204, 0, v204\n\t" \
    "v_mfma_f32_16x16x32_bf16 v[192:195], v[120:123], v[172:175], v[192:195]\n\t" \
    "v_add_f32 v206, %[ke], v204\n\t" \
    "s_waitcnt lgkmcnt(5)\n\t" \
    "v_mfma_f32_16x16x32_bf16 v[188:191], v[44:47], v[176:179], v[188:191]\n\t" \
    "ds_read_b128 v[172:175], %[la] offset:12288\n\t" \
    "v_rsq_f32 v206, v206\n\t" \
    "v_mfma_f32_16x16x32_bf16 v[192:195], v[124:127], v[176:179], v[192:195]\n\t" \
    "s_waitcnt lgkmcnt(5)\n\t" \
    "v_mfma_f32_16x16x32_bf16 v[188:191], v[48:51], v[180:183], v[188:191]\n\t" \
    "ds_read_b128 v[176:179], %[la] offset:20480\n\t" \
    "v_mul_f32 v207, -v205, v206\n\t" \
    "v_mfma_f32_16x16x32_bf16 v[192:195], v[128:131], v[180:183], v[192:195]\n\t" \
    "v_mul_f32 v208, v196, v207\n\t" \
    "s_waitcnt lgkmcnt(5)\n\t" \
    "v_mfma_f32_16x16x32_bf16 v[188:191], v[52:55], v[184:187], v[188:191]\n\t" \
    "ds_read_b128 v[180:183], %[la] offset:28672\n\t" \
    "v_mul_f32 v209, v197, v207\n\t" \
    "v_mul_f32 v210, v198, v207\n\t" \
    "v_mfma_f32_16x16x32_bf16 v[192:195], v[132:135], v[184:187], v[192:195]\n\t" \
    "v_mul_f32 v211, v199, v207\n\t" \
    "v_mul_f32 v212, v200, v207\n\t" \
    "s_waitcnt lgkmcnt(5)\n\t" \
    "v_mfma_f32_16x16x32_bf16 v[188:191], v[56:59], v[160:163], v[188:191]\n\t" \
    "v_mul_f32 v213, v201, v207\n\t" \
    "v_mul_f32 v214, v202, v207\n\t" \
    "v_mfma_f32_16x16x32_bf16 v[192:195], v[136:139], v[160:163], v[192:195]\n\t" \
    "v_mul_f32 v215, v203, v207\n\t" \
    "v_mov_b64 v[216:217], -1\n\t" \
    "s_waitcnt lgkmcnt(4)\n\t" \
    "v_mfma_f32_16x16x32_bf16 v[188:191], v[60:63], v[164:167], v[188:191]\n\t" \
    "v_mov_b64 v[218:219], -1\n\t" \
    "buffer_store_dwordx4 v[216:219], %[l16], %[rs], %[sa] offen sc1\n\t" \
    "v_mfma_f32_16x16x32_bf16 v[192:195], v[140:143], v[164:167], v[192:195]\n\t" \
    "s_waitcnt lgkmcnt(3)\n\t" \
    "v_mfma_f32_16x16x32_bf16 v[188:191], v[64:67], v[168:171], v[188:191]\n\t" \
    "v_mfma_f32_16x16x32_bf16 v[192:195], v[144:147], v[168:171], v[192:195]\n\t" \
    "s_waitcnt lgkmcnt(2)\n\t" \
    "v_mfma_f32_16x16x32_bf16 v[188:191], v[68:71], v[172:175], v[188:191]\n\t" \
    "v_mfma_f32_16x16x32_bf16 v[192:195], v[148:151], v[172:175], v[192:195]\n\t" \
    "s_waitcnt lgkmcnt(1)\n\t" \
    "v_mfma_f32_16x16x32_bf16 v[188:191], v[72:75], v[176:179], v[188:191]\n\t" \
    "v_mfma_f32_16x16x32_bf16 v[192:195], v[152:155], v[176:179], v[192:195]\n\t" \
    "s_waitcnt lgkmcnt(0)\n\t" \
    "v_mfma_f32_16x16x32_bf16 v[188:191], v[76:79], v[180:183], v[188:191]\n\t" \
    "v_mfma_f32_16x16x32_bf16 v[192:195], v[156:159], v[180:183], v[192:195]\n\t" \
    "s_setprio 2\n\t" \
    "buffer_load_dwordx4 v[232:235], %[l16], %[rs], %[n0] offen offset:0 sc1\n\t" \
    "buffer_load_dwordx4 v[236:239], %[l16], %[rs], %[n0] offen offset:1024 sc1\n\t" \
    "buffer_load_dwordx4 v[240:243], %[l16], %[rs], %[n0] offen offset:2048 sc1\n\t" \
    "buffer_load_dwordx4 v[244:247], %[l16], %[rs], %[n0] offen offset:3072 sc1\n\t" \
    "buffer_load_dwordx4 v[248:251], %[l16], %[rs], %[n1] offen offset:0 sc1\n\t" \
    ASM_ST3 \
    "v_fma_f32 v188, v188, v206, v208\n\t" \
    "v_fma_f32 v189, v189, v206, v209\n\t" \
    "v_fma_f32 v190, v190, v206, v210\n\t" \
    "v_fma_f32 v191, v191, v206, v211\n\t" \
    "v_mul_f32 v160, v188, v188\n\t" \
    "v_mul_f32 v161, v189, v189\n\t" \
    "v_mul_f32 v162, v190, v190\n\t" \
    "v_mul_f32 v163, v191, v191\n\t" \
    "v_fma_f32 v160, v160, %[k1], v220\n\t" \
    "v_fma_f32 v161, v161, %[k1], v220\n\t" \
    "v_fma_f32 v162, v162, %[k1], v220\n\t" \
    "v_fma_f32 v163, v163, %[k1], v220\n\t" \
    "v_mul_f32 v160, v188, v160\n\t" \
    "v_mul_f32 v161, v189, v161\n\t" \
    "v_mul_f32 v162, v190, v162\n\t" \
    "v_mul_f32 v163, v191, v163\n\t" \
    "v_fma_f32 v192, v192, v206, v212\n\t" \
    "v_fma_f32 v193, v193, v206, v213\n\t" \
    "v_fma_f32 v194, v194, v206, v214\n\t" \
    "v_fma_f32 v195, v195, v206, v215\n\t" \
    "v_mul_f32 v164, v192, v192\n\t" \
    "v_mul_f32 v165, v193, v193\n\t" \
    "v_mul_f32 v166, v194, v194\n\t" \
    "v_mul_f32 v167, v195, v195\n\t" \
    "v_fma_f32 v164, v164, %[k1], v220\n\t" \
    "v_fma_f32 v165, v165, %[k1], v220\n\t" \
    "v_fma_f32 v166, v166, %[k1], v220\n\t" \
    "v_fma_f32 v167, v167, %[k1], v220\n\t" \
    "v_mul_f32 v164, v192, v164\n\t" \
    "v_mul_f32 v165, v193, v165\n\t" \
    "v_mul_f32 v166, v194, v166\n\t" \
    "v_mul_f32 v167, v195, v167\n\t" \
    "v_exp_f32 v160, v160\n\t" \
    "v_exp_f32 v161, v161\n\t" \
    "v_exp_f32 v162, v162\n\t" \
    "v_exp_f32 v163, v163\n\t" \
    "v_exp_f32 v164, v164\n\t" \
    "v_exp_f32 v165, v165\n\t" \
    "v_exp_f32 v166, v166\n\t" \
    "v_exp_f32 v167, v167\n\t" \
    "v_add_f32 v160, 1.0, v160\n\t" \
    "v_add_f32 v161, 1.0, v161\n\t" \
    "v_add_f32 v162, 1.0, v162\n\t" \
    "v_add_f32 v163, 1.0, v163\n\t" \
    "v_add_f32 v164, 1.0, v164\n\t" \
    "v_add_f32 v165, 1.0, v165\n\t" \
    "v_add_f32 v166, 1.0, v166\n\t" \
    "v_add_f32 v167, 1.0, v167\n\t" \
    "v_rcp_f32 v160, v160\n\t" \
    "v_rcp_f32 v161, v161\n\t" \
    "v_rcp_f32 v162, v162\n\t" \
    "v_rcp_f32 v163, v163\n\t" \
    "v_rcp_f32 v164, v164\n\t" \
    "v_rcp_f32 v165, v165\n\t" \
    "v_rcp_f32 v166, v166\n\t" \
    "v_rcp_f32 v167, v167\n\t" \
    "v_mul_f32 v188, v188, v160\n\t" \
    "v_mul_f32 v189, v189, v161\n\t" \
    "v_mul_f32 v190, v190, v162\n\t" \
    "v_mul_f32 v191, v191, v163\n\t" \
    "v_mul_f32 v192, v192, v164\n\t" \
    "v_mul_f32 v193, v193, v165\n\t" \
    "v_mul_f32 v194, v194, v166\n\t" \
    "v_mul_f32 v195, v195, v167\n\t" \
    "v_cvt_pk_bf16_f32 v188, v188, v189\n\t" \
    "v_cvt_pk_bf16_f32 v189, v190, v191\n\t" \
    "v_cvt_pk_bf16_f32 v190, v192, v193\n\t" \
    "v_cvt_pk_bf16_f32 v191, v194, v195\n\t" \
    "v_min_u32 v188, -2, v188\n\t" \
    "v_min_u32 v189, -2, v189\n\t" \
    "v_min_u32 v190, -2, v190\n\t" \
    "v_min_u32 v191, -2, v191\n\t" \
    "v_mov_b32 v216, 0x3f803f80\n\t" \
    "v_mov_b32 v217, 0x3f803f80\n\t" \
    "v_mov_b32 v218, 0x3f803f80\n\t" \
    "v_mov_b32 v219, 0x3f803f80\n\t" \
    "v_permlane16_swap_b32 v188, v190\n\t" \
    "v_permlane16_swap_b32 v189, v191\n\t" \
    "s_waitcnt vmcnt(6)\n\t" \
    "buffer_store_dwordx4 v[188:191], %[oa], %[rs], %[so] offen sc1\n\t" \
    "s_nop 1\n\t" \
    "s_setprio 0\n\t"
#define ASM_WT3_HI \
    "s_setprio 1\n\t" \
    "s_barrier\n\t" ASM_ST \
    "ds_read_b128 v[160:163], %[la] offset:0\n\t" \
    "ds_read_b128 v[164:167], %[la] offset:1024\n\t" \
    "ds_read_b128 v[168:171], %[la] offset:2048\n\t" \
    "ds_read2st64_b32 v[204:205], %[sr] offset0:20 offset1:24\n\t" \
    "ds_read_b128 v[172:175], %[la] offset:3072\n\t" \
    "ds_read_b128 v[176:179], %[la] offset:8192\n\t" \
    "ds_read_b128 v[180:183], %[la] offset:9216\n\t" \
    "s_waitcnt lgkmcnt(6)\n\t" \
    "v_mfma_f32_16x16x32_bf16 v[188:191], v[0:3], v[160:163], 0\n\t" \
    "ds_read_b128 v[184:187], %[la] offset:10240\n\t" \
    "v_mfma_f32_16x16x32_bf16 v[192:195], v[80:83], v[160:163], 0\n\t" \
    "s_waitcnt lgkmcnt(6)\n\t" \
    "v_mfma_f32_16x16x32_bf16 v[188:191], v[4:7], v[164:167], v[188:191]\n\t" \
    "ds_read_b128 v[160:163], %[la] offset:11264\n\t" \
    "v_mfma_f32_16x16x32_bf16 v[192:195], v[84:87], v[164:167], v[192:195]\n\t" \
    "s_waitcnt lgkmcnt(6)\n\t" \
    "v_mfma_f32_16x16x32_bf16 v[188:191], v[8:11], v[168:171], v[188:191]\n\t" \
    "ds_read_b128 v[164:167], %[la] offset:16384\n\t" \
    "s_waitcnt lgkmcnt(6)\n\t" \
    "v_mfma_f32_16x16x32_bf16 v[192:195], v[88:91], v[168:171], v[192:195]\n\t" \
    "v_permlane16_swap_b32 v204, v205\n\t" \
    "s_waitcnt lgkmcnt(5)\n\t" \
    "v_mfma_f32_16x16x32_bf16 v[188:191], v[12:15], v[172:175], v[188:191]\n\t" \
    "ds_read_b128 v[168:171], %[la] offset:17408\n\t" \
    "v_mfma_f32_16x16x32_bf16 v[192:195], v[92:95], v[172:175], v[192:195]\n\t" \
    "v_add_f32 v206, v204, v205\n\t" \
    "s_waitcnt lgkmcnt(5)\n\t" \
    "v_mfma_f32_16x16x32_bf16 v[188:191], v[16:19], v[176:179], v[188:191]\n\t" \
    "ds_read_b128 v[172:175], %[la] offset:18432\n\t" \
    "v_add_f32 v207, v204, v205\n\t" \
    "v_mfma_f32_16x16x32_bf16 v[192:195], v[96:99], v[176:179], v[192:195]\n\t" \
    "s_waitcnt lgkmcnt(5)\n\t" \
    "v_mfma_f32_16x16x32_bf16 v[188:191], v[20:23], v[180:183], v[188:191]\n\t" \
    "ds_read_b128 v[176:179], %[la] offset:19456\n\t" \
    "v_permlane32_swap_b32 v206, v207\n\t" \
    "v_mfma_f32_16x16x32_bf16 v[192:195], v[100:103], v[180:183], v[192:195]\n\t" \
    "s_waitcnt lgkmcnt(5)\n\t" \
    "v_mfma_f32_16x16x32_bf16 v[188:191], v[24:27], v[184:187], v[188:191]\n\t" \
    "ds_read_b128 v[180:183], %[la] offset:24576\n\t" \
    "v_add_f32 v204, v206, v207\n\t" \
    "v_mfma_f32_16x16x32_bf16 v[192:195], v[104:107], v[184:187], v[192:195]\n\t" \
    "v_add_f32 v205, v206, v207\n\t" \
    "s_waitcnt lgkmcnt(5)\n\t" \
    "v_mfma_f32_16x16x32_bf16 v[188:191], v[28:31], v[160:163], v[188:191]\n\t" \
    "ds_read_b128 v[184:187], %[la] offset:25600\n\t" \
    "v_mfma_f32_16x16x32_bf16 v[192:195], v[108:111], v[160:163], v[192:195]\n\t" \
    "v_permlane16_swap_b32 v204, v205\n\t" \
    "s_waitcnt lgkmcnt(5)\n\t" \
    "v_mfma_f32_16x16x32_bf16 v[188:191], v[32:35], v[164:167], v[188:191]\n\t" \
    "ds_read_b128 v[160:163], %[la] offset:26624\n\t" \
    "v_mfma_f32_16x16x32_bf16 v[192:195], v[112:115], v[164:167], v[192:195]\n\t" \
    "v_mul_f32 v205, %[kd], v205\n\t" \
    "s_waitcnt lgkmcnt(5)\n\t" \
    "v_mfma_f32_16x16x32_bf16 v[188:191], v[36:39], v[168:171], v[188:191]\n\t" \
    "ds_read_b128 v[164:167], %[la] offset:27648\n\t" \
    "v_mul_f32 v204, %[kd], v204\n\t" \
    "v_mfma_f32_16x16x32_bf16 v[192:195], v[116:119], v[168:171], v[192:195]\n\t" \
    "v_fma_f32 v204, -v205, v205, v204\n\t" \
    "s_waitcnt lgkmcnt(5)\n\t" \
    "v_mfma_f32_16x16x32_bf16 v[188:191], v[40:43], v[172:175], v[188:191]\n\t" \
    "ds_read_b128 v[168:171], %[la] offset:4096\n\t" \
    "v_max_f32 v204, 0, v204\n\t" \
    "v_mfma_f32_16x16x32_bf16 v[192:195], v[120:123], v[172:175], v[192:195]\n\t" \
    "v_add_f32 v206, %[ke], v204\n\t" \
    "s_waitcnt lgkmcnt(5)\n\t" \
    "v_mfma_f32_16x16x32_bf16 v[188:191], v[44:47], v[176:179], v[188:191]\n\t" \
    "ds_read_b128 v[172:175], %[la] offset:12288\n\t" \
    "v_rsq_f32 v206, v206\n\t" \
    "v_mfma_f32_16x16x32_bf16 v[192:195], v[124:127], v[176:179], v[192:195]\n\t" \
    "s_waitcnt lgkmcnt(5)\n\t" \
    "v_mfma_f32_16x16x32_bf16 v[188:191], v[48:51], v[180:183], v[188:191]\n\t" \
    "ds_read_b128 v[176:179], %[la] offset:20480\n\t" \
    "v_mul_f32 v207, -v205, v206\n\t" \
    "v_mfma_f32_16x16x32_bf16 v[192:195], v[128:131], v[180:183], v[192:195]\n\t" \
    "v_mul_f32 v208, v196, v207\n\t" \
    "s_waitcnt lgkmcnt(5)\n\t" \
    "v_mfma_f32_16x16x32_bf16 v[188:191], v[52:55], v[184:187], v[188:191]\n\t" \
    "ds_read_b128 v[180:183], %[la] offset:28672\n\t" \
    "v_mul_f32 v209, v197, v207\n\t" \
    "v_mul_f32 v210, v198, v207\n\t" \
    "v_mfma_f32_16x16x32_bf16 v[192:195], v[132:135], v[184:187], v[192:195]\n\t" \
    "v_mul_f32 v211, v199, v207\n\t" \
    "v_mul_f32 v212, v200, v207\n\t" \
    "s_waitcnt lgkmcnt(5)\n\t" \
    "v_mfma_f32_16x16x32_bf16 v[188:191], v[56:59], v[160:163], v[188:191]\n\t" \
    "v_mul_f32 v213, v201, v207\n\t" \
    "v_mul_f32 v214, v202, v207\n\t" \
    "v_mfma_f32_16x16x32_bf16 v[192:195], v[136:139], v[160:163], v[192:195]\n\t" \
    "v_mul_f32 v215, v203, v207\n\t" \
    "v_mov_b64 v[216:217], -1\n\t" \
    "s_waitcnt lgkmcnt(4)\n\t" \
    "v_mfma_f32_16x16x32_bf16 v[188:191], v[60:63], v[164:167], v[188:191]\n\t" \
    "v_mov_b64 v[218:219], -1\n\t" \
    "buffer_store_dwordx4 v[216:219], %[l16], %[rs], %[sa] offen sc1\n\t" \
    "v_mfma_f32_16x16x32_bf16 v[192:195], v[140:143], v[164:167], v[192:195]\n\t" \
    "s_waitcnt lgkmcnt(3)\n\t" \
    "v_mfma_f32_16x16x32_bf16 v[188:191], v[64:67], v[168:171], v[188:191]\n\t" \
    "v_mfma_f32_16x16x32_bf16 v[192:195], v[144:147], v[168:171], v[192:195]\n\t" \
    "s_waitcnt lgkmcnt(2)\n\t" \
    "v_mfma_f32_16x16x32_bf16 v[188:191], v[68:71], v[172:175], v[188:191]\n\t" \
    "v_mfma_f32_16x16x32_bf16 v[192:195], v[148:151], v[172:175], v[192:195]\n\t" \
    "s_waitcnt lgkmcnt(1)\n\t" \
    "v_mfma_f32_16x16x32_bf16 v[188:191], v[72:75], v[176:179], v[188:191]\n\t" \
    "v_mfma_f32_16x16x32_bf16 v[192:195], v[152:155], v[176:179], v[192:195]\n\t" \
    "s_waitcnt lgkmcnt(0)\n\t" \
    "v_mfma_f32_16x16x32_bf16 v[188:191], v[76:79], v[180:183], v[188:191]\n\t" \
    "v_mfma_f32_16x16x32_bf16 v[192:195], v[156:159], v[180:183], v[192:195]\n\t" \
    ASM_ST3 \
    "s_nop 4\n\t" \
    "v_fma_f32 v188, v188, v206, v208\n\t" \
    "v_fma_f32 v189, v189, v206, v209\n\t" \
    "v_fma_f32 v190, v190, v206, v210\n\t" \
    "v_fma_f32 v191, v191, v206, v211\n\t" \
    "v_mul_f32 v160, v188, v188\n\t" \
    "v_mul_f32 v161, v189, v189\n\t" \
    "v_mul_f32 v162, v190, v190\n\t" \
    "v_mul_f32 v163, v191, v191\n\t" \
    "v_fma_f32 v160, v160, %[k1], v220\n\t" \
    "v_fma_f32 v161, v161, %[k1], v220\n\t" \
    "v_fma_f32 v162, v162, %[k1], v220\n\t" \
    "v_fma_f32 v163, v163, %[k1], v220\n\t" \
    "v_mul_f32 v160, v188, v160\n\t" \
    "v_mul_f32 v161, v189, v161\n\t" \
    "v_mul_f32 v162, v190, v162\n\t" \
    "v_mul_f32 v163, v191, v163\n\t" \
    "v_fma_f32 v192, v192, v206, v212\n\t" \
    "v_fma_f32 v193, v193, v206, v213\n\t" \
    "v_fma_f32 v194, v194, v206, v214\n\t" \
    "v_fma_f32 v195, v195, v206, v215\n\t" \
    "v_mul_f32 v164, v192, v192\n\t" \
    "v_mul_f32 v165, v193, v193\n\t" \
    "v_mul_f32 v166, v194, v194\n\t" \
    "v_mul_f32 v167, v195, v195\n\t" \
    "v_fma_f32 v164, v164, %[k1], v220\n\t" \
    "v_fma_f32 v165, v165, %[k1], v220\n\t" \
    "v_fma_f32 v166, v166, %[k1], v220\n\t" \
    "v_fma_f32 v167, v167, %[k1], v220\n\t" \
    "v_mul_f32 v164, v192, v164\n\t" \
    "v_mul_f32 v165, v193, v165\n\t" \
    "v_mul_f32 v166, v194, v166\n\t" \
    "v_mul_f32 v167, v195, v167\n\t" \
    "v_exp_f32 v160, v160\n\t" \
    "v_exp_f32 v161, v161\n\t" \
    "v_exp_f32 v162, v162\n\t" \
    "v_exp_f32 v163, v163\n\t" \
    "v_exp_f32 v164, v164\n\t" \
    "v_exp_f32 v165, v165\n\t" \
    "v_exp_f32 v166, v166\n\t" \
    "v_exp_f32 v167, v167\n\t" \
    "v_add_f32 v160, 1.0, v160\n\t" \
    "v_add_f32 v161, 1.0, v161\n\t" \
    "v_add_f32 v162, 1.0, v162\n\t" \
    "v_add_f32 v163, 1.0, v163\n\t" \
    "v_add_f32 v164, 1.0, v164\n\t" \
    "v_add_f32 v165, 1.0, v165\n\t" \
    "v_add_f32 v166, 1.0, v166\n\t" \
    "v_add_f32 v167, 1.0, v167\n\t" \
    "v_rcp_f32 v160, v160\n\t" \
    "v_rcp_f32 v161, v161\n\t" \
    "v_rcp_f32 v162, v162\n\t" \
    "v_rcp_f32 v163, v163\n\t" \
    "v_rcp_f32 v164, v164\n\t" \
    "v_rcp_f32 v165, v165\n\t" \
    "v_rcp_f32 v166, v166\n\t" \
    "v_rcp_f32 v167, v167\n\t" \
    "v_mul_f32 v188, v188, v160\n\t" \
    "v_mul_f32 v189, v189, v161\n\t" \
    "v_mul_f32 v190, v190, v162\n\t" \
    "v_mul_f32 v191, v191, v163\n\t" \
    "v_mul_f32 v192, v192, v164\n\t" \
    "v_mul_f32 v193, v193, v165\n\t" \
    "v_mul_f32 v194, v194, v166\n\t" \
    "v_mul_f32 v195, v195, v167\n\t" \
    "v_cvt_pk_bf16_f32 v188, v188, v189\n\t" \
    "v_cvt_pk_bf16_f32 v189, v190, v191\n\t" \
    "v_cvt_pk_bf16_f32 v190, v192, v193\n\t" \
    "v_cvt_pk_bf16_f32 v191, v194, v195\n\t" \
    "v_min_u32 v188, -2, v188\n\t" \
    "v_min_u32 v189, -2, v189\n\t" \
    "v_min_u32 v190, -2, v190\n\t" \
    "v_min_u32 v191, -2, v191\n\t" \
    "s_nop 1\n\t" \
    "v_permlane16_swap_b32 v188, v190\n\t" \
    "v_permlane16_swap_b32 v189, v191\n\t" \
    "s_waitcnt vmcnt(1)\n\t" \
    "buffer_store_dwordx4 v[188:191], %[oa], %[rs], %[so] offen sc1\n\t" \
    "s_nop 1\n\t"
#define ASM_POLL5_I \
    "buffer_load_dwordx4 v[232:235], %[l16], %[rs], %[s0] offen offset:0 sc1\n\t" \
    "buffer_load_dwordx4 v[236:239], %[l16], %[rs], %[s0] offen offset:1024 sc1\n\t" \
    "buffer_load_dwordx4 v[240:243], %[l16], %[rs], %[s0] offen offset:2048 sc1\n\t" \
    "buffer_load_dwordx4 v[244:247], %[l16], %[rs], %[s0] offen offset:3072 sc1\n\t" \
    "buffer_load_dwordx4 v[248:251], %[l16], %[rs], %[s1] offen offset:0 sc1\n\t"
using u32x8 = __attribute__((ext_vector_type(8))) unsigned;
using u32x16 = __attribute__((ext_vector_type(16))) unsigned;
using u32x32 = __attribute__((ext_vector_type(32))) unsigned;
struct WRegs { u32x32 w[6]; u32x8 t; };
#define WREGS(W) "{v[0:31]}"(W.w[0]), "{v[32:63]}"(W.w[1]), "{v[64:95]}"(W.w[2]), "{v[96:127]}"(W.w[3]), "{v[128:159]}"(W.w[4]), "{v[160:191]}"(W.w[5]), "{v[192:199]}"(W.t)
__device__ __forceinline__ u32x32 cat32(u32x8 a, u32x8 b, u32x8 c, u32x8 d) {
    const u32x16 lo = __builtin_shufflevector(a, b, 0, 1, 2, 3, 4, 5, 6, 7, 8, 9, 10, 11, 12, 13, 14, 15);
    const u32x16 hi = __builtin_shufflevector(c, d, 0, 1, 2, 3, 4, 5, 6, 7, 8, 9, 10, 11, 12, 13, 14, 15);
    return __builtin_shufflevector(lo, hi, 0, 1, 2, 3, 4, 5, 6, 7, 8, 9, 10, 11, 12, 13, 14, 15, 16, 17, 18, 19, 20, 21, 22, 23, 24, 25, 26, 27, 28, 29, 30, 31);
}
#define WOUT(W) "=&{v[0:31]}"(W.w[0]), "=&{v[32:63]}"(W.w[1]), "=&{v[64:95]}"(W.w[2]), "=&{v[96:127]}"(W.w[3]), "=&{v[128:159]}"(W.w[4]), "=&{v[160:191]}"(W.w[5]), "=&{v[192:199]}"(W.t)
#define WTMP "memory", "scc", "v200", "v201", "v202", "v203", "v204", "v205", "v206", "v207", "v208", "v209", "v210", "v211", "v212", "v213", "v214", "v215", "v216", "v217", "v218", "v219", "v220", "v221", "v222", "v223", "v224", "v225", "v226", "v227", "v228", "v229", "v230", "v231", "v232", "v233", "v234", "v235", "v236", "v237", "v238", "v239"
__device__ __forceinline__ void load_w3(WRegs& W, const bf16_t* m, int wave, unsigned c0, bool hq) {
    const int ln = lane_id();
    unsigned o[3], s[3], t;
#pragma unroll
    for (int k = 0; k < 3; ++k) {
        const int row = (k * 64 + ln) / 24, sg = (k * 64 + ln) % 24;
        o[k] = row * (HD * 2) + c0 + (hq ? ((sg >> 1) & 1) * 384 + ((0x140 >> ((sg >> 3) * 4)) & 15) * 32 + (sg & 5) * 16 : sg * 16); s[k] = wave * 13824 + row * 416 + sg * 16;
    }
    const unsigned tr = (ln >> 4) * 3456 + ((ln >> 2) & 3) * 416 + (wave & 3) * 32 + (ln & 3) * 8;
    asm volatile(ASM_WLOAD3 : WOUT(W), [t] "=&s"(t) : [rs] "s"(mkrsrc(m, DM * HD * 2)), [wx] "s"(wave), [o0] "v"(o[0]), [o1] "v"(o[1]), [o2] "v"(o[2]), [s0] "v"(s[0]), [s1] "v"(s[1]), [s2] "v"(s[2]),
                 [t0] "v"(tr + wave * 13824), [t1] "v"(tr + (wave ^ 1) * 13824), [t2] "v"(tr + (wave ^ 2) * 13824), [t3] "v"(tr + (wave ^ 3) * 13824), [t4] "v"(tr + (wave & 4) * 13824),
                 [wl] "v"(L_W + wave * 10240 + ln * 16) : WTMP);
}
__device__ __forceinline__ void load_wo(WRegs& W, const bf16_t* m, int wave, int h) {
    const int ln = lane_id();
    unsigned o[5], s[5], t;
#pragma unroll
    for (int k = 0; k < 5; ++k) {
        const int row = (k * 64 + ln) / 20, sg = (k * 64 + ln) % 20;
        o[k] = (h * DH + row) * (DM * 2) + sg * 16 + (wave & 3) * 320; s[k] = wave * 13824 + row * 352 + sg * 16;
    }
    asm volatile(ASM_WLOADO : WOUT(W), [t] "=&s"(t) : [rs] "s"(mkrsrc(m, DM * HD * 2)), [o0] "v"(o[0]), [o1] "v"(o[1]), [o2] "v"(o[2]), [o3] "v"(o[3]), [o4] "v"(o[4]),
                 [s0] "v"(s[0]), [s1] "v"(s[1]), [s2] "v"(s[2]), [s3] "v"(s[3]), [s4] "v"(s[4]), [tr] "v"(wave * 13824 + (ln >> 2) * 352 + (ln & 3) * 8), [wl] "v"(L_W + wave * 10240 + ln * 16) : WTMP);
}
using s16x4 = __attribute__((ext_vector_type(4))) short;
template <int NT, int KS, int N>
__device__ __forceinline__ void load_wn(u32x4 (&f)[NT * KS], const bf16_t* m, int rows, int wave, int c0, int k0) {
    const int ln = lane_id();
    const rsrc_t rs = mkrsrc(m, rows * N * 2);
#pragma unroll
    for (int k = 0; k < KS; ++k)
#pragma unroll
        for (int n = 0; n < NT; ++n) f[n * KS + k] = __builtin_amdgcn_raw_buffer_load_b128(rs, ((n * 64 + ln) / (2 * NT)) * (N * 2) + ((n * 64 + ln) % (2 * NT)) * 16, (k0 + k) * (N * 64) + c0 * 32, 0);
    const unsigned st = wave * 10240 + ln * 16, tr = wave * 10240 + ((ln >> 4) * 8 + ((ln >> 2) & 3)) * (NT * 32) + (ln & 3) * 8;
#pragma unroll
    for (int k = 0; k < KS; ++k) {
#pragma unroll
        for (int n = 0; n < NT; ++n) lds_st<u32x4>(st + n * 1024, f[n * KS + k]);
        asm volatile("" ::: "memory");
#pragma unroll
        for (int n = 0; n < NT; ++n) {
            const u32x2 a = __builtin_bit_cast(u32x2, __builtin_amdgcn_ds_read_tr16_b64_v4i16((LAS s16x4*)(tr + n * 32)));
            const u32x2 b = __builtin_bit_cast(u32x2, __builtin_amdgcn_ds_read_tr16_b64_v4i16((LAS s16x4*)(tr + n * 32 + NT * 128)));
            f[n * KS + k] = (u32x4){a[0], a[1], b[0], b[1]};
        }
        asm volatile("" ::: "memory");
    }
}
struct C1Regs { u32x8 lw0, lw1, z1; u32x4 z0; };
#define XG_OUT "=&{v[200:207]}"(C.lw0), "=&{v[224:227]}"(C.z0), "=&{v[228:235]}"(C.lw1), "=&{v[244:251]}"(C.z1)
#define XG_IN WREGS(W), [xa] "v"(xa), [wl] "v"(wl), [qs] "s"(qs)
#define XG_CLOB "memory", "scc", "v208", "v209", "v210", "v211", "v212", "v213", "v214", "v215", "v216", "v217", "v218", "v219", "v220", "v221", "v222", "v223", "v236", "v237", "v238", "v239", "v240", "v241", "v242", "v243", "v252"
constexpr int XS0 = 8;
__device__ __forceinline__ unsigned init_wait(const Params& P, int c, int ph, unsigned need = 0x220080u) {
    unsigned w;
    for (int spins = 0;; ++spins) {
        unsigned o;
        asm volatile("v_mov_b32 %0, 0" : "=v"(o));
        w = __builtin_amdgcn_readfirstlane(__builtin_amdgcn_raw_buffer_load_b32(mkrsrc(P.ws + FL0, FL_BYTES), o, FL_IN + (ph * 8 + (c & 7)) * 128, 16));
        if ((w & need) == need) break;
        __builtin_amdgcn_s_sleep(8);
        if (spin_fail(spins, P)) break;
    }
    asm volatile("" ::: "memory");
    return w;
}
#define PLACE() const unsigned pw_ = init_wait(P, wave, L); const int par = (L ^ (pw_ >> 16)) & 1, cpy = x ^ ((pw_ >> 20) & 1); const rsrc_t rr = mkrsrc(P.ws + par * 8192, RG_L); (void)cpy; (void)rr
__device__ __forceinline__ void xn_wait(const Params& P, rsrc_t rr, int seg, int cpy) {
    for (int spins = 0;; ++spins) {
        const unsigned f = __builtin_amdgcn_raw_buffer_load_b32(rr, (lane_id() & 15) * 128, RG_FX + (cpy & 3) * 2048, 16);
        if (__all(f > (unsigned)seg)) break;
        __builtin_amdgcn_s_sleep(4);
        if (spin_fail(spins, P)) break;
    }
    asm volatile("" ::: "memory");
}
__device__ __forceinline__ unsigned c1_dst(int wave, bool ktype) {
    const int ln = lane_id(), l15 = ln & 15, hf = (wave >> 1) & 1;
    const int rb16 = ((ln >> 2) & 4) * 16 + (ln >> 5) * 32;
    return (ktype ? L_KM + (l15 >> 2) * 128 + hf * 8 + (l15 & 3) * 2
                  : L_Q + (l15 >> 3) * 256 + ((l15 >> 1) & 1) * 128 + ((l15 >> 2) & 1) * 8 + hf * 4 + (l15 & 1) * 2) + (wave & 1) * 1536 + rb16;
}
template <int NP>
__device__ __forceinline__ void crit1(const WRegs& W, C1Regs& C, rsrc_t rr, unsigned sbase, int wave, unsigned wl, unsigned dsto, unsigned& budget, unsigned& fv, unsigned fs
) {
    unsigned abase;
    asm volatile("v_and_b32 %0, 0x3ff, %1\n\tv_or_b32 %0, %2, %0" : "=&v"(abase) : "v"(wl), "s"(wave * 1024));
    ASM_ST_DECL;
#define C1_OUT "+{v[224:227]}"(C.z0), "+{v[244:251]}"(C.z1), "+{v[200:207]}"(C.lw0), "+{v[228:235]}"(C.lw1)
#define C1_IN WREGS(W), [ab] "v"(abase), [wl] "v"(wl), [dst] "v"(dsto)
#define C1_CLOB "memory", "vcc", "scc", "v208", "v209", "v210", "v211", "v212", "v213", "v214", "v215", "v216", "v217", "v218", "v219", "v220", "v221", "v222", "v223", "v236", "v237", "v238", "v239", "v240", "v241", "v242", "v243"
#define C1_POLL [rs] "s"(rr), [s0] "s"(sbase), [s1] "s"(sbase + 4096), [s2] "s"(sbase + 2 * BLK)
    ST(1);
    if constexpr (NP == 13) asm volatile(ASM_CRIT1_V3 "s_waitcnt vmcnt(0)" : C1_OUT, [n] "+s"(budget), [fv] "+v"(fv) ASM_ST_OUT ASM_ST2_OUT : C1_IN, C1_POLL, [fs] "s"(fs) : C1_CLOB);
    else if constexpr (NP == 12) asm volatile(ASM_CRIT1_V2 "s_waitcnt vmcnt(0)" : C1_OUT, [n] "+s"(budget), [fv] "+v"(fv) ASM_ST_OUT ASM_ST2_OUT : C1_IN, C1_POLL, [fs] "s"(fs) : C1_CLOB);
    else if constexpr (NP == 3) asm volatile(ASM_CRIT1_3 : C1_OUT, [n] "+s"(budget) ASM_ST_OUT ASM_ST2_OUT : C1_IN, C1_POLL : C1_CLOB);
    else if constexpr (NP == 2) asm volatile(ASM_CRIT1_2 : C1_OUT, [n] "+s"(budget) ASM_ST_OUT ASM_ST2_OUT : C1_IN, C1_POLL : C1_CLOB);
    else asm volatile(ASM_CRIT1_N : C1_OUT ASM_ST_OUT : C1_IN : C1_CLOB);
    SB();
    ASM_ST_SAVE(3);
    if constexpr (NP != 0) { ASM_ST2_SAVE(2); }
}
#define STARG

#define SRG 5
#undef STROLE
#define STROLE 1
__device__ __forceinline__ void s_role(const Params& P, int wave, int L, int h, int x) {
    const char* xn = P.ws + PG0 + PG_XN + L * PG_LX;
    auto xdma = [&](int seg, int hb, int par) {
        const unsigned l16 = lane_id() * 16;
        const int mt = (wave >> 1) & 1;
        const char* g = xn + pgb(seg, par);
        if (hb == 0) {
            dma4(g + blin(mt * 20480 + (wave & 1) * 4096), l16, L_XA + mt * 16384 + (wave & 1) * 4096);
            dmaq<0>(g + blin(mt * 20480 + 16384) + (wave & 1) * 1024, l16, L_XB + mt * 4096 + (wave & 1) * 1024);
        } else {
            dma4(g + blin(mt * 20480 + 8192), l16, L_XA + mt * 16384 + 8192); dma4(g + blin(mt * 20480 + 12288), l16, L_XA + mt * 16384 + 12288);
            dmaq<2>(g + blin(mt * 20480 + 16384), l16, L_XB + mt * 4096); dmaq<3>(g + blin(mt * 20480 + 16384), l16, L_XB + mt * 4096);
        }
    };
    const float SCL = 0.07216878364870322f * 1.4426950408889634f;
    auto run = [&](auto Cc) {
    constexpr int CLS = decltype(Cc)::value;
    WRegs wf;
    MARK(!L && h == 0 && wave == 0, 0);
    load_w3(wf, P.w[CLS == 3], wave, h * 384, false);
    MARKX(!L && wave == 0, 1);
    PLACE();
    if (CLS == 3) {
        if (L) xn_wait(P, rr, 0, cpy);
        xdma(0, 0, par);
        if (wave == 4 || wave == 7) xdma(0, 1, par);
        wait_vm<0>();
    }
    bar();
    if (CLS == 3 && wave == 7 && L) xn_wait(P, rr, 1, cpy);
    const unsigned qs = (CLS == 3 ? L_KX : L_QX) + (wave & 1) * 3072 + ((wave >> 1) & 1) * 8;
    unsigned xa, wl, dsto;
    if (CLS == 3) { xa = L_XA + wave * 2048 + lane_id() * 16; wl = L_W + wave * 10240 + lane_id() * 16; dsto = c1_dst(wave, true); }
    __builtin_amdgcn_s_setprio(CLS == 3 ? 1 : 2);
    unsigned budget = 1u << 18, tb2 = 0;
    STDECL;
    LDECL;
#pragma unroll 1
    for (int t = 0; t < NSEG; ++t) {
        const unsigned slot = t & (NSLOT - 1);
        C1Regs C;
        {
            const WRegs& W = wf;
            ASM_ST_DECL;
            if constexpr (CLS == 3) {
                const int mt = (wave >> 1) & 1;
                const char* g = xn + pgb(t + 1 < NSEG ? t + 1 : t, par);
                asm volatile(ASM_XG_S_HI : XG_OUT ASM_ST_OUT : XG_IN, [slp] "s"(SLP4), [g0] "s"(g + blin(mt * 20480 + (wave & 1) * 4096)), [d0] "s"(L_XA + mt * 16384 + (wave & 1) * 4096),
                             [g1] "s"(g + blin(mt * 20480 + 16384) + (wave & 1) * 1024), [d1] "s"(L_XB + mt * 4096 + (wave & 1) * 1024) : XG_CLOB);
                wait_vm<0>();
                LB(t);
            } else {
                const unsigned l16 = lane_id() * 16;
                xa = L_XA + wave * 2048 + l16; wl = L_W + wave * 10240 + l16;
#define XG_Q(A) asm volatile("s_cmp_lt_u32 %[t], 2\n\ts_cbranch_scc1 .Lq3%=\n\t" A "s_branch .Lqe%=\n\t.Lq3%=:\n\t" ASM_XG_S_LO ".Lqe%=:" : XG_OUT ASM_ST_OUT : XG_IN, [t] "s"(t) : XG_CLOB)
                if constexpr (CLS == 0 || CLS == 4) XG_Q(ASM_XG_S_LO1); else XG_Q(ASM_XG_S_LO2);
                __builtin_amdgcn_s_setprio(0);
                if constexpr (CLS == 0) {
                    const int ln = lane_id();
                    const u32x4 s4 = sent4();
                    const unsigned poff = (ln * 16 + ((ln < 8) ? h * 128 : (1 + (h >> 4)) * 2 * BLK - 128 + (h & 15) * 512)) | ((ln >= 40 || t < 2) ? 0x40000000u : 0u);
#pragma unroll
                    for (int c = 0; c < (int)NCP; ++c) st128(s4, rr, poff, RG_XP + ((t - 2) & (NSLOT - 1)) * XP_SLOT + (c >> 1) * XP_PAIR + (c & 1) * BLK);
                    const unsigned pbase = RG_PX + ((t - 3) & (NSLOT - 1)) * PX_SLOT + (h >> 2) * BLK;
                    const unsigned o2 = ((ln & 16) ? 5120 + ((h >> 1) & 1) * 1024 + (h & 1) * 256 + (ln >> 5) * 512 : 4096 + (h & 3) * 256 + (ln >> 5) * 3072) + (ln & 15) * 16;
                    if (t >= 3) { st128(s4, rr, ln * 16, pbase + (h & 3) * 1024); st128(s4, rr, o2, pbase); }
                }
                if (CLS == 4 && t >= HT0) {
                    const int ln = lane_id(), u = h >> 2;
                    for (int spins = 0;; ++spins) {
                        const unsigned fl = __builtin_amdgcn_raw_buffer_load_b32(rr, (ln & 3) * 128, RG_FH + BLK + u * 512, 16);
                        if (__all(fl > (unsigned)t)) break;
                        __builtin_amdgcn_s_sleep(2);
                        if (spin_fail(spins, P)) break;
                    }
                    asm volatile("" ::: "memory");
                    const char* g = P.ws + PG0 + pgb(t, par) + PG_HV + (16 + u) * 3 * BLK;
                    unsigned t0, t1, vo;
                    static_assert(L_QX == 16384, "");
                    asm volatile("s_add_u32 %0, %4, 0\n\ts_and_b32 %1, %0, 0xe000\n\ts_add_u32 %0, %0, %1\n\tv_add_u32 %2, %0, %5\n\ts_mov_b32 m0, 16384\n\ts_nop 0\n\tglobal_load_lds_dwordx4 %2, %3 sc1\n\t"
                                 "s_add_u32 %0, %4, 1024\n\ts_and_b32 %1, %0, 0xe000\n\ts_add_u32 %0, %0, %1\n\tv_add_u32 %2, %0, %5\n\ts_mov_b32 m0, 17408\n\ts_nop 0\n\tglobal_load_lds_dwordx4 %2, %3 sc1\n\t"
                                 "s_add_u32 %0, %4, 2048\n\ts_and_b32 %1, %0, 0xe000\n\ts_add_u32 %0, %0, %1\n\tv_add_u32 %2, %0, %5\n\ts_mov_b32 m0, 19456\n\ts_nop 0\n\tglobal_load_lds_dwordx4 %2, %3 sc1\n\t"
                                 "s_add_u32 %0, %4, 6144\n\ts_and_b32 %1, %0, 0xe000\n\ts_add_u32 %0, %0, %1\n\tv_add_u32 %2, %0, %5\n\ts_mov_b32 m0, 22528\n\ts_nop 0\n\tglobal_load_lds_dwordx4 %2, %3 sc1\n\t"
                                 "s_add_u32 %0, %4, 7168\n\ts_and_b32 %1, %0, 0xe000\n\ts_add_u32 %0, %0, %1\n\tv_add_u32 %2, %0, %5\n\ts_mov_b32 m0, 23552\n\ts_nop 0\n\tglobal_load_lds_dwordx4 %2, %3 sc1\n\t"
                                 "s_add_u32 %0, %4, 8192\n\ts_and_b32 %1, %0, 0xe000\n\ts_add_u32 %0, %0, %1\n\tv_add_u32 %2, %0, %5\n\ts_mov_b32 m0, 25600\n\ts_nop 0\n\tglobal_load_lds_dwordx4 %2, %3 sc1\n\t"
                                 : "=&s"(t0), "=&s"(t1), "=&v"(vo) : "s"(g), "s"((((h >> 1) & 1) * 12 + (h & 1) * 3) * 1024), "v"(ln * 16) : "memory", "scc");
                }
                dsto = c1_dst(wave, false);
                while ((unsigned)__builtin_amdgcn_s_memtime() - tb2 < STD) __builtin_amdgcn_s_sleep(2);
            }
            ASM_ST_SAVE(7);
            ST(6);
        }
        ST(0);
        unsigned fv = 0;
        if (CLS == 2) fv = (lane_id() & 15) * 128;
        crit1<CLS == 3 ? 0 : CLS < 2 ? 3 : CLS == 2 ? 12 : 2>(wf, C, rr, RG_XM + slot * XM_SLOT + (cpy >> 1) * XM_PAIR + (cpy & 1) * BLK, wave, wl, dsto, budget, fv, RG_FX + (cpy & 3) * 2048 STARG);
        if (CLS != 3) tb2 = (unsigned)__builtin_amdgcn_s_memtime();
        if constexpr (CLS == 0) {
            const unsigned sp = RG_XP + slot * XP_SLOT, sx = sp + (1 + (h >> 4)) * 2 * BLK + (h & 15) * 512;
            ASM_ST_DECL;
            asm volatile(ASM_SM : [c0] "+s"(budget) ASM_ST_OUT : WREGS(wf), [rr] "s"(rr), [sx] "s"(sx), [sy] "s"(sx + XP_PAIR), [sm] "s"(sp + h * 128), [cs] "s"(BLK), [cp] "s"(XP_PAIR), [c70] "s"(0x70), [scl] "s"(SCL)
                         : "memory", "v200", "v201", "v202", "v203", "v204", "v205", "v206", "v207", "v208", "v209", "v210", "v211", "v212", "v213", "v214", "v215", "v216", "v217", "v218", "v219", "v220", "v221", "v222", "v223", "v224", "v225", "v226", "v227", "v228", "v229", "v230", "v231", "v232", "v233", "v234", "v235", "v236", "v237", "v238", "v239", "v240", "v241", "v242", "v243", "v244", "v245", "v246", "v247", "v248", "v249", "v250", "v251", "v252", "v253", "v254", "v255");
            ASM_ST_SAVE(4);
            ST(5);
        } else if constexpr (CLS == 1 || CLS == 4) {
            const int ln = lane_id(), l15 = ln & 15, l4 = ln >> 4;
            constexpr int mt = (CLS == 4);
            const unsigned qa = L_QX + mt * 6144 + ln * 16, kmo = L_KM + l4 * 128 + (ln & 7) * 16;
            const unsigned xo = mt ? (h & 3) * 1024 : 5120 + ((h >> 1) & 1) * 1024 + (h & 1) * 256;
            const unsigned pm = l4 < 2 ? (mt ? 4096 : 7168) + (h & 3) * 256 - xo + l15 * 16 + l4 * 8 : OOB;
            const unsigned so = RG_PX + slot * PX_SLOT + (h >> 2) * BLK + xo;
            f32x4 sd;
#pragma unroll
            for (int r = 0; r < 4; ++r) sd[r] = (l4 * 4 + r > l15) ? -3.0e38f : 0.f;
            ASM_ST_DECL;
#define P4_ARGS : "+{v[240:243]}"(sd) ASM_ST_OUT : WREGS(wf), [qa] "v"(qa), [km] "v"(kmo), [pm] "v"(pm), [rr] "s"(rr), [so] "s"(so), [scl] "s"(SCL) : "memory", "v200", "v201", "v202", "v203", "v204", "v205", "v206", "v207", "v208", "v209", "v210", "v211", "v212", "v213", "v214", "v215", "v216", "v217", "v218", "v219", "v220", "v221", "v222", "v223", "v224", "v225", "v226", "v227", "v228", "v229", "v230", "v231", "v232", "v233", "v234", "v235", "v236", "v237", "v238", "v239", "v244", "v245", "v246", "v247", "v248", "v249", "v250", "v251"
            if constexpr (mt) asm volatile(ASM_P4_1 P4_ARGS); else asm volatile(ASM_P4_0 P4_ARGS);
            ASM_ST_SAVE(4);
            ST(5);
        } else {
            bar();
            ST(4);
            if (CLS == 2 && L && t + 2 < NSEG && !__all(fv > (unsigned)t + 2)) xn_wait(P, rr, t + 2, cpy);
            if (CLS == 2) { int tt = t; asm volatile("" : "+s"(tt)); if (tt + 2 == XS0) init_wait(P, h, 1); }
            if (CLS == 3) LA();
            {
                if (CLS == 3 && (wave == 4 || wave == 7)) {
                    if (t + 1 < NSEG) xdma(t + 1, 1, par);
                }
                ST(5);
            }
        }
        STFLUSH(t);
    }
    LF();
    };
    if (wave == 0) run(std::integral_constant<int, 0>{});
    else if (wave == 1) run(std::integral_constant<int, 1>{});
    else if (wave == 2) run(std::integral_constant<int, 4>{});
    else if (wave == 3) run(std::integral_constant<int, 2>{});
    else run(std::integral_constant<int, 3>{});
}

#undef STROLE
#define STROLE 8
__device__ __forceinline__ void h_role(const Params& P, int wave, int L, int i, int x) {
    const rsrc_t rp = mkrsrc(P.ws + PG0, PG_L);
    const char* xn = P.ws + PG0 + PG_XN + L * PG_LX;
    auto run = [&](auto Cc) {
    constexpr bool HI = decltype(Cc)::value;
    const int mt = (wave >> 1) & 1;
    WRegs wf;
    load_w3(wf, i < 16 ? P.w[2] : P.w[0], wave, (i < 16 ? i * 2 + HI : (i - 16) * 4 + HI * 2) * 384, i >= 16);
    PLACE();
    auto xdma = [&](int seg, int hb) {
        const unsigned l16 = lane_id() * 16;
        const char* g = xn + pgb(seg, par);
        dma4(g + blin(mt * 20480 + hb * 8192 + (wave & 1) * 4096), l16, L_XA + mt * 16384 + hb * 8192 + (wave & 1) * 4096);
        dmaq<0>(g + blin(mt * 20480 + 16384) + hb * 2048 + (wave & 1) * 1024, l16, L_XB + mt * 4096 + hb * 2048 + (wave & 1) * 1024);
    };
    const unsigned qs = (HI ? L_KX : L_QX) + (wave & 1) * 3072 + mt * 8;
    const unsigned xa = L_XA + wave * 2048 + lane_id() * 16, wl = L_W + wave * 10240 + lane_id() * 16;
    const unsigned fp = RG_FH + (i >> 4) * BLK + ((i & 15) * 4 + (wave & 3)) * 128;
    const unsigned fb = L ? RG_FX + (cpy & 3) * 2048 : RG_FV + (i & 3) * 2048, fa = L ? 0 : HAH;
    unsigned go = 0, all = 0;
    STDECL;
    LDECL;
    for (int t = HT0; t <= NSEG; ++t) {
        ST(0);
        if constexpr (HI) {
            const unsigned l16 = lane_id() * 16;
            if (t > HT0) {
                u32x4 v[6];
                const int w4 = wave - 4;
#pragma unroll
                for (int k = 0; k < 6; ++k) v[k] = lds_ld<u32x4>(L_QX + ((k >> 1) * 8 + w4 * 2 + (k & 1)) * 1024 + l16);
                const unsigned so = pgb(t - 1, par) + PG_HV + i * 3 * BLK + w4 * 2048;
#pragma unroll
                for (int k = 0; k < 6; ++k) st128(v[k], rp, l16 + (k & 1) * 1024, so + (k >> 1) * BLK);
                if (!all) {
                    wait_vm<0>();
                    if (l16 < 512) __builtin_amdgcn_raw_buffer_store_b32(t, rr, l16 >> 2, fp, 16);
                }
            }
            ST(1);
            if (t == NSEG) break;
            if (!all) bar();
            xdma(t, 1);
        } else {
            if (t == NSEG) break;
            if (!go) {
                for (int spins = 0;; ++spins) {
                    if (__all(__builtin_amdgcn_raw_buffer_load_b32(rr, (lane_id() & 15) * 128, fb, 16) + fa > (unsigned)t)) break;
                    __builtin_amdgcn_s_sleep(HSL);
                    if (spin_fail(spins, P)) break;
                }
                asm volatile("" ::: "memory");
                xdma(t, 0); wait_vm<0>();
            }
            if (!all) bar();
        }
        ST(2);
        C1Regs C;
        const WRegs& W = wf;
        ASM_ST_DECL;
        if constexpr (HI) {
            const unsigned l4 = lane_id() * 4;
            asm volatile(ASM_XG_H_HI : XG_OUT ASM_ST_OUT : XG_IN, [rr] "s"(rr), [fp] "s"(fp), [tv] "s"(t), [fv] "v"((all && l4 < 128) ? l4 : OOB) : XG_CLOB);
        } else {
            asm volatile(ASM_XG_S_LO : XG_OUT ASM_ST_OUT : XG_IN : XG_CLOB);
            if (t + 1 == XS0) init_wait(P, wave, 1);
            const unsigned fv = __builtin_amdgcn_raw_buffer_load_b32(rr, (lane_id() & 15) * 128, fb, 16) + fa;
            go = __builtin_amdgcn_readfirstlane((t + 1 < NSEG && __all(fv > (unsigned)t + 1)) ? 1u : 0u);
            if (go) xdma(t + 1, 0);
            lds_st<unsigned>(L_MEM + wave * 4, go);
            wait_vm<0>();
        }
        ASM_ST_SAVE(3);
        ST(4);
        if (!HI) { LB(t); LA(); }
        PMARK(L && wave == 7, 6, t);
        bar();
        const u32x4 gv = lds_ld<u32x4>(L_MEM);
        all = __builtin_amdgcn_readfirstlane(gv[0] & gv[1] & gv[2] & gv[3]);
        ST(5);
        STFLUSH(t);
    }
    LF();
    };
    if (wave < 4) run(std::false_type{}); else run(std::true_type{});
}

#undef STROLE
#define STROLE 2
__device__ __forceinline__ void v_role(const Params& P, int wave, int L, int h, int x) {
    WRegs wf;
    STDECL;
    auto runv = [&](auto Cc) {
        load_w3(wf, P.w[2], wave, h * 384, false);
        MARKX(!L && wave == 0, 6);
        PLACE();
        unsigned budget = 1u << 18;
        const unsigned wl = L_W + wave * 10240 + lane_id() * 16, dsto = c1_dst(wave, false);
        auto vfetch = [&](int seg, int tries) {
            const int ln = lane_id();
            for (int spins = 0; tries != -2; ++spins) {
                if (spins == tries) return false;
                const unsigned f = __builtin_amdgcn_raw_buffer_load_b32(rr, (ln & 3) * 128, RG_FH + (h >> 1) * 512, 16);
                if (__all(f > (unsigned)seg)) break;
                __builtin_amdgcn_s_sleep(4);
                if (spin_fail(spins, P)) break;
            }
            asm volatile("" ::: "memory");
            const char* g = P.ws + PG0 + pgb(seg, par) + PG_HV + (h >> 1) * 3 * BLK;
#pragma unroll
            for (int k = 0; k < 3; ++k) {
                const int f = (h & 1) * 12 + wave * 3 + k;
                dmaq<0>(g + (f >> 3) * BLK + (f & 7) * 1024, ln * 16, L_QX + (seg & 1) * 12288 + (wave * 3 + k) * 1024);
            }
            return true;
        };
#pragma unroll 1
        for (int t = 0; t < HT0; ++t) {
            if (L) xn_wait(P, rr, t, cpy);
            const unsigned l16 = lane_id() * 16;
            const char* g = P.ws + PG0 + PG_XN + L * PG_LX + pgb(t, par);
            dma4(g + blin(wave * 4096), l16, L_XA + wave * 4096); dma4(g + blin(20480 + wave * 4096), l16, L_XA + 16384 + wave * 4096);
            dmaq<0>(g + blin(16384) + wave * 1024, l16, L_XB + wave * 1024); dmaq<0>(g + blin(20480 + 16384) + wave * 1024, l16, L_XB + 4096 + wave * 1024);
            wait_vm<0>();
            bar();
            C1Regs C;
            const WRegs& W = wf;
            const unsigned xa = L_XA + wave * 2048 + l16, qs = L_QX + (wave & 1) * 3072 + ((wave >> 1) & 1) * 8 + t * 12288;
            asm volatile(ASM_XG_V : XG_OUT : XG_IN : XG_CLOB);
            if (t == 0) bar();
        }
        bool seen = false;
        unsigned tb2 = 0;
        for (int t = 0; t < NSEG; ++t) {
            if (t + 1 - V1A >= HT0 && t + 1 - V1A < NSEG) { vfetch(t + 1 - V1A, seen ? -2 : -1); wait_vm<0>(); }
            ST(7);
            bar();
            C1Regs C;
            asm volatile("ds_read_b128 v[200:203], %4\n\tds_read_b128 v[204:207], %4 offset:1024\n\tds_read_b128 v[228:231], %4 offset:2048\n\tds_read_b128 v[232:235], %4 offset:3072\n\t"
                         "v_mov_b64 v[224:225], 0\n\tv_mov_b64 v[226:227], 0\n\tv_mov_b64 v[244:245], 0\n\tv_mov_b64 v[246:247], 0\n\tv_mov_b64 v[248:249], 0\n\tv_mov_b64 v[250:251], 0\n\ts_waitcnt lgkmcnt(0)"
                         : XG_OUT : "v"(wl) : "memory");
            ST(6);
            while ((unsigned)__builtin_amdgcn_s_memtime() - tb2 < VTD) __builtin_amdgcn_s_sleep(1);
            unsigned fv = (lane_id() & 3) * 128;
            crit1<10 + decltype(Cc)::value>(wf, C, rr, RG_XM + (t & (NSLOT - 1)) * XM_SLOT + (cpy >> 1) * XM_PAIR + (cpy & 1) * BLK, wave, wl, dsto, budget, fv, RG_FH + (h >> 1) * 512 STARG);
            __builtin_amdgcn_s_setprio(0);
            bar();
            seen = __all(fv > (unsigned)t + 2 - V1A);
            tb2 = (unsigned)__builtin_amdgcn_s_memtime();
            ST(4);
            STC(5);
            STFLUSH(t);
        }
    };
    if (wave < 2) runv(std::integral_constant<int, 3>{});
    else if (wave < 4) runv(std::integral_constant<int, 2>{});
    else {
        const int w4 = wave - 4;
        const rsrc_t rd = mkrsrc(P.ws + PG0, PG_L);
        load_wo(wf, P.w[3], wave, h);
        PLACE();
        bar();
        bar();
        for (int t = -1; t < NSEG; ++t) {
            const unsigned slot = t & (NSLOT - 1);
            bar();
            ST(7);
#pragma unroll 1
            for (int n = V1A ? t + (t <= 0) : t + (t != 1) + (t == 0); n <= (V1A && t >= 0 ? t : t + 1) && n < NSEG; ++n) {
                const unsigned l16 = lane_id() * 16;
                const unsigned wl = L_W + wave * 10240 + l16, va = L_QX + (n & 1) * 12288 + l16;
                unsigned so = ((n & 7) << 22) + pgb(64 + (w4 >> 1), par) + ((w4 & 1) * 40 + (h >> 3)) * BLK + (h & 7) * 1024;
                asm volatile(ASM_VOX : [so] "+s"(so) : WREGS(wf), [l16] "v"(l16), [wl] "v"(wl), [va] "v"(va), [rd] "s"(rd) : "memory", "scc", "v200", "v201", "v202", "v203", "v204", "v205", "v206", "v207", "v208", "v209", "v210", "v211", "v212", "v213", "v214", "v215", "v216", "v217", "v218", "v219", "v220", "v221", "v222", "v223", "v224", "v225", "v226", "v227", "v228", "v229", "v230", "v231", "v232", "v233", "v234", "v235", "v236", "v237", "v238", "v239", "v240", "v241", "v242", "v243", "v244", "v245", "v246", "v247", "v248", "v249", "v250", "v251");
                if (V1A || n < 2) {
                    wait_vm<0>();
                    const unsigned l4 = lane_id() * 4;
                    if (l4 < 128) __builtin_amdgcn_raw_buffer_store_b32(n + 1, rr, l4, RG_FV + (w4 >> 1) * BLK + ((w4 & 1) * 32 + h) * 128, 16);
                }
            }
            if (t < 0) continue;
            ST(0);
            unsigned wl = L_W + wave * 10240 + lane_id() * 16;
            bf16x16 lw = lds_frag(wl, 1024);
            u32x32 cz;
            asm volatile("v_mov_b64 v[220:221], 0\n\tv_mov_b64 v[222:223], 0\n\tv_mov_b64 v[224:225], 0\n\tv_mov_b64 v[226:227], 0\n\tv_mov_b64 v[228:229], 0\n\tv_mov_b64 v[230:231], 0\n\tv_mov_b64 v[232:233], 0\n\tv_mov_b64 v[234:235], 0\n\tv_mov_b64 v[236:237], 0\n\tv_mov_b64 v[238:239], 0\n\tv_mov_b64 v[240:241], 0\n\tv_mov_b64 v[242:243], 0\n\tv_mov_b64 v[244:245], 0\n\tv_mov_b64 v[246:247], 0\n\tv_mov_b64 v[248:249], 0\n\tv_mov_b64 v[250:251], 0" : "={v[220:251]}"(cz));
            unsigned vo = ((wl >> 8) & 1) * (1 + (w4 & 1) * 2) * BLK + ((wl >> 9) & 1) * 1024 + (wl & 255);
            asm volatile("" : "+{v[212:219]}"(lw), "+{v[220:251]}"(cz), "+v"(wl), "+v"(vo));
            unsigned l16 = wl & 1023u;
            bar();
            wait_vm<0>();
            if (!V1A && l16 < 512 && t && t + 1 < NSEG) __builtin_amdgcn_raw_buffer_store_b32(t + 2, rr, l16 >> 2, RG_FV + (w4 >> 1) * BLK + ((w4 & 1) * 32 + h) * 128, 16);
            ST(1);
            const int sidx = (wl & 128u) ? 0xEEEE : 0x4444;
            unsigned so = RG_VM + slot * VM_SLOT + (unsigned)((w4 * 10 - (w4 & 1) + (h >> 4) * 2) * BLK + ((h >> 2) & 3) * 2048 + (h & 3) * 256);
            ST(3);
            ASM_ST_DECL;
            asm volatile(ASM_CRIT2 : [so] "+s"(so), "+{v[212:219]}"(lw), "+{v200}"(l16), "+{v[220:251]}"(cz) ASM_ST_OUT
                         : WREGS(wf), [wl] "v"(wl), [vo] "v"(vo), [sidx] "v"(sidx), [rr] "s"(rr)
                         : "memory", "scc", "v201", "v202", "v203", "v204", "v205", "v206", "v207", "v208", "v209", "v210", "v211");
            ASM_ST_SAVE(4);
            ST(5);
            STFLUSH(t);
        }
    }
}

#undef STROLE
#define STROLE 4
constexpr unsigned R_RED = 0;
constexpr unsigned R_PART = 16384;
__device__ __forceinline__ void r_role(const Params& P, int wave, int L, int r5, int x) {
    PLACE();
    const int jj = (r5 * 4 + cpy) ^ (r5 * 4 + x >= 6 ? 1 : 0);
    const int lane = lane_id(), l15 = lane & 15, l4 = lane >> 4;
    const unsigned l16 = lane * 16;
    const rsrc_t rd = mkrsrc(P.ws + PG0, PG_L);
    const int sidx = (l15 < 8) ? 0x4444 : 0xEEEE;
    unsigned offp[3];
    offp[0] = RG_XP + (cpy >> 1) * XP_PAIR + (cpy & 1) * BLK + (1 + (wave >> 2)) * 2 * BLK + (wave & 3) * 2048 + (PLAY ? (l4 >> 1) * 512 + (((l15 >> 2) & 1) + (l15 & 2)) * 128 + (l4 & 1) * 64 + (l15 >> 3) * 32 + (l15 & 1) * 16 : l16);
    offp[2] = (l15 < 8) ? RG_XP + (cpy >> 1) * XP_PAIR + (cpy & 1) * BLK + wave * 512 + l4 * 128 + l15 * 16 : OOB;
    const unsigned offv = RG_VM + ((jj >> 1) * 4 + (wave >> 2) * 2 + (jj & 1)) * BLK + (wave & 3) * 2048 + l16;
    const int j = jj * 2 + ((wave >> 1) & 1), fl = (wave & 1) * 32 + (lane & 31);
    const unsigned offo = RG_XM + (wave >> 2) * XM_PAIR + (lane >> 5) * BLK + (j & 31) * 256 + (j >> 5) * 2 * BLK + fl * 4;
    MARK(!L && jj == 0 && wave == 0, 5);
    {
        const int row = (lane >> 2) & 7, col = 16 * j + 4 * (lane & 3) + 2 * (wave & 1);
        __builtin_amdgcn_raw_buffer_store_b32(nosent(*(const unsigned*)(P.mem0 + row * DM + col)), rr, offo, 0, 16);
    }
    auto red_addr = [](int fl) { return R_RED + fl * 80 + (fl >> 4) * 16; };
    const unsigned rw0 = (lane >> 5) * 5248 + red_addr((((l15 >> 1) & 1) * 8 + 4 * (l4 & 1)) * 4 + (l15 >> 2)) + (l15 & 1) * 4 + (wave ^ (l15 >> 3)) * 8;
    const unsigned rrd = ((wave >> 1) & 1) * 5248 + red_addr(fl) + (lane >> 5) * 32;
    u32x32 vx;
    const unsigned fo = (lane & 3) * 128, fs = RG_FV + (jj / 10) * BLK + (((jj / 5) & 1) * 32 + wave * 4) * 128, vxo = pgb(64 + jj / 10, par) + (unsigned)((jj % 10) * 8 + (wave >> 1)) * BLK + (wave & 1) * 4096;
    const rsrc_t rp = mkrsrc(P.ws + PG0, PG_L);
    const unsigned xo = (l4 >> 1) * 256 + l15 * 16 + (l4 & 1) * 8, pw = R_PART + wave * 1024 + l16, pr = R_PART + (wave & 3) * 8192 + l16;
    const int fin = wave >> 2;
    asm volatile("" : "={v[128:159]}"(vx));
    auto vx_fetch = [&](int t) {
        for (int spins = 0;; ++spins) {
            const unsigned f = __builtin_amdgcn_raw_buffer_load_b32(rr, fo, fs, 16);
            if (__all(f > (unsigned)t)) break;
            __builtin_amdgcn_s_sleep(2);
            if (spin_fail(spins, P)) break;
        }
        const unsigned sv = ((t & 7) << 22) + vxo;
        asm volatile("buffer_load_dwordx4 v[128:131], %1, %2, %3 offen sc1\n\tbuffer_load_dwordx4 v[132:135], %1, %2, %3 offen offset:1024 sc1\n\t"
                     "buffer_load_dwordx4 v[136:139], %1, %2, %3 offen offset:2048 sc1\n\tbuffer_load_dwordx4 v[140:143], %1, %2, %3 offen offset:3072 sc1\n\t"
                     "buffer_load_dwordx4 v[144:147], %1, %2, %4 offen sc1\n\tbuffer_load_dwordx4 v[148:151], %1, %2, %4 offen offset:1024 sc1\n\t"
                     "buffer_load_dwordx4 v[152:155], %1, %2, %4 offen offset:2048 sc1\n\tbuffer_load_dwordx4 v[156:159], %1, %2, %4 offen offset:3072 sc1"
                     : "+{v[128:159]}"(vx) : "v"(l16), "s"(rp), "s"(sv), "s"(sv + 4 * BLK) : "memory");
    };
    vx_fetch(0);
    unsigned budget = 1u << 18, tpub = 0;
    STDECL;
    LDECL;
    for (int t = 0; t < NSEG; ++t) {
        const unsigned slot = t & (NSLOT - 1);
        ST(0);
        while ((unsigned)__builtin_amdgcn_s_memtime() - tpub < RTD) __builtin_amdgcn_s_sleep(1);
        u32x8 vm, pv;
        u32x4 pm;
        {
            unsigned a;
            ASM_ST_DECL;
            asm volatile(ASM_RCRIT : "=&{v[176:183]}"(vm), "=&{v[184:191]}"(pv), "=&{v[192:195]}"(pm), [a] "=&s"(a), [n] "+s"(budget) ASM_ST_OUT ASM_ST2_OUT ASM_ST3_OUT
                         : [rs] "s"(rr), [ov] "v"(offv), [sv] "s"(slot * VM_SLOT), [o0] "v"(offp[0]), [o2] "v"(offp[2]), [sp] "s"(slot * XP_SLOT),
                           "{v[128:159]}"(vx), [sidx] "v"(sidx), [rw] "v"(rw0), [rd] "v"(rrd), [oo] "v"(offo),
                           [so] "s"(((t + 1) & (NSLOT - 1)) * XM_SLOT), [pub] "s"(NSEG - 1 - t)
                         : "memory", "vcc", "scc", "v160", "v161", "v162", "v163", "v164", "v165", "v166", "v167", "v196", "v197", "v200", "v201", "v202", "v203", "v204", "v205", "v206", "v207");
            ASM_ST_SAVE(2); ASM_ST2_SAVE(1); ASM_ST3_SAVE(6);
        }
        ST(3);
        LA();
        MARK(!L && jj == 0 && wave == 0 && t < 4, 10 + t);
        PMARK(wave == 0, L * 7, t - 1 + L);
        tpub = (unsigned)__builtin_amdgcn_s_memtime();
        __builtin_amdgcn_s_sleep(RXDEL);
        unsigned early;
#pragma unroll 1
        for (int spins = 1023;; spins += 1024) {
            unsigned n;
            const unsigned sx = pgb(t, par) + blin(((wave >> 1) & 1) * 20480 + jj * 1024) + (wave & 1) * 512;
            const unsigned pxb = RG_PX + slot * PX_SLOT + wave * BLK, sv = (((t + 1) & 7) << 22) + vxo;
            ASM_ST_DECL;
            asm volatile(ASM_RX : "+{v[128:159]}"(vx), [n] "=&s"(n), [a] "=&s"(early) ASM_ST_OUT ASM_ST2_OUT
                         : "{v[176:183]}"(vm), "{v[184:191]}"(pv), "{v[192:195]}"(pm), [l16] "v"(l16), [rs] "s"(rr), [s0] "s"(pxb), [s1] "s"(pxb + 4096), [rd] "s"(rd), [rx] "s"(rd), [xo] "v"(xo),
                           [fo] "v"(fo), [fs] "s"(fs), [tn] "s"(t + 1), [sv0] "s"(sv), [sv1] "s"(sv + 4 * BLK), [rp] "s"(rp), [sx] "s"(PG_XR + L * PG_LX + sx), [sa] "s"(PG_XA + sx),
                           [pw] "v"(pw), [pr] "v"(pr), [fin] "s"(fin)
                         : "memory", "vcc", "scc", "v96", "v97", "v98", "v99", "v100", "v101", "v102", "v103", "v104", "v105", "v106", "v107", "v108", "v109", "v110", "v111", "v112", "v113", "v114", "v115", "v116", "v117", "v118", "v119", "v120", "v121", "v122", "v123", "v124", "v125", "v126", "v127", "v196", "v200", "v201", "v202", "v203", "v204", "v205", "v206", "v207", "v208", "v209", "v210", "v211", "v212", "v213", "v214", "v215", "v216", "v217", "v218", "v219", "v220", "v221", "v222", "v223", "v224", "v225", "v226", "v227", "v228", "v229", "v230", "v231", "v240", "v241", "v242", "v243", "v248", "v250", "v251");
            ASM_ST_SAVE(4); ASM_ST2_SAVE(5);
            if (__builtin_amdgcn_readfirstlane(n)) break;
            early = 0;
            if (spin_fail(spins, P)) { bar(); break; }
        }
        if (t + 1 < NSEG && !__builtin_amdgcn_readfirstlane(early)) vx_fetch(t + 1);
        PMARK(!L && wave == 7 && PMK == 1, 1, t - 1);
        st128(sent4(), rr, offv, slot * VM_SLOT); st128(sent4(), rr, offv + 1024, slot * VM_SLOT);
        __builtin_amdgcn_raw_buffer_store_b32(SENT, rr, offo, slot * XM_SLOT, 16);
        if (wave < 2 && t + AH < NSEG) st128(sent4(), rd, l16, PG_XA + pgb(t + AH, par) + blin(wave * 20480 + jj * 1024));
        ST(7);
        LB(t);
        STFLUSH(t);
    }
    LF();
}

#undef STROLE
#define STROLE 16
template <int WHICH>
__device__ __forceinline__ void w_role(const Params& P, int wave, int L, int cu, int x) {
    constexpr int KS = 20, N = (WHICH == 3) ? FF : DM, NU = 2 * NSEG;
    constexpr unsigned D_IN = WHICH == 1 ? PG_XA : WHICH == 2 ? PG_HS : PG_X1;
    constexpr unsigned D_OUT = WHICH == 1 ? PG_HS : WHICH == 2 ? PG_X1 : PG_H2;
    if (WHICH != 3 && cu * 8 + wave >= 20) {
        for (int u = 0; u <= NU; ++u) bar();
        return;
    }
    auto run = [&](auto Cc) {
    constexpr bool LO = decltype(Cc)::value;
    const int ln = lane_id(), l15 = ln & 15, l4 = ln >> 4;
    const unsigned l16 = ln * 16;
    const rsrc_t rd = mkrsrc(P.ws + PG0, PG_L);
    const int nt0 = (cu * 8 + wave) * 2;
    u32x32 w[5];
    {
        u32x4 f[2 * KS];
        load_wn<2, KS, N>(f, P.w[3 + WHICH], DM, wave, nt0, 0);
#pragma unroll
        for (int n = 0; n < 5; ++n) {
            u32x8 q[4];
#pragma unroll
            for (int i = 0; i < 4; ++i) q[i] = __builtin_shufflevector(f[n * 8 + 2 * i], f[n * 8 + 2 * i + 1], 0, 1, 2, 3, 4, 5, 6, 7);
            w[n] = cat32(q[0], q[1], q[2], q[3]);
        }
        asm volatile("" : "+{v[0:31]}"(w[0]), "+{v[32:63]}"(w[1]), "+{v[64:95]}"(w[2]), "+{v[96:127]}"(w[3]), "+{v[128:159]}"(w[4]));
    }
    PLACE();
    bar();
    const int ocol = nt0 * 16 + (l4 & 1) * 16 + (l4 >> 1) * 8;
    const unsigned o3 = (unsigned)((l4 & 1) * 512 + (l4 >> 1) * 256 + l15 * 16), ro = (l4 >> 1) * 256 + l15 * 16 + (l4 & 1) * 8;
    const int grp = nt0 / 10, sh = ((grp & 1) ? -1 : 1) * ((grp >> 1) + 1);
    if (WHICH == 1) {
        unsigned zv;
        asm volatile("v_mov_b32 %0, 0" : "=v"(zv));
        st128(((u32x4){zv, zv, zv, zv}), rd, l15 <= (grp >> 1) ? toff((grp & 1) ? S_ - 1 - l15 : l15, ocol, par) : OOB, PG_HS);
    }
    auto in0 = [&](int u) { return D_IN + pgb(u >> 1, par) + blin((u & 1) * 20480 + wave * 4096); };
    auto in1 = [&](int u) { return D_IN + pgb(u >> 1, par) + blin((u & 1) * 20480 + 16384) + wave * 1024; };
    u32x16 va;
    u32x4 vb;
    u32x8 cs, k8;
#define WT_W "{v[0:31]}"(w[0]), "{v[32:63]}"(w[1]), "{v[64:95]}"(w[2]), "{v[96:127]}"(w[3]), "{v[128:159]}"(w[4])
    asm volatile("v_mov_b32 v216, 0x3f803f80\n\tv_mov_b32 v217, v216\n\tv_mov_b32 v218, v216\n\tv_mov_b32 v219, v216\n\tv_mov_b32 v220, %[c0]\n\tv_mov_b32 v232, -1\n\tv_mov_b32 v236, -1\n\t"
                 "v_mov_b32 v240, -1\n\tv_mov_b32 v244, -1\n\tv_mov_b32 v248, -1\n\t" ASM_WCS
                 : "=&{v[196:203]}"(cs), "=&{v[216:223]}"(k8), "=&{v[232:247]}"(va), "=&{v[248:251]}"(vb) : WT_W, [c0] "s"(-2.0f * 0.7978845608028654f * 1.4426950408889634f));
    unsigned la = l16, lw = wave * 8192 + l16, sr = l4 * 8192 + ((l15 >> 2) * 16 + l15) * 16 + (l15 & 3) * 4, budget = 1u << 18;
    wait_vm<0>();
    STDECL;
    LDECL;
#pragma unroll 1
    for (int u = 0; u < NU; ++u) {
        const int mt = u & 1;
        const unsigned pg = pgb(u >> 1, par), c3 = D_OUT + blin(mt * (N * 32) + (nt0 >> 1) * 1024);
        unsigned oa = o3, ob = 0, so = pg + c3, sa = u + 2 * AH < NU ? pgb((u >> 1) + AH, par) + c3 : pg + PG_SS + 4096, a;
        if (WHICH == 1) {
            const int orow = u * 16 + l15 + sh;
            ob = toff(orow + AH * SEG, ocol, par);
            oa = (unsigned)orow < (unsigned)S_ ? (ob ^ 8192u) - (AH << 21) : OOB;
            if (orow + AH * SEG >= S_ + (sh < 0 ? sh : 0)) ob = OOB;
            so = sa = D_OUT;
        }
        if (mt) { ST(0); LB(u >> 1); } else { LA(); }
        ASM_ST_DECL;
#define WT_A WT_W, [la] "v"(la), [l16] "v"(l16), [oa] "v"(oa), [rs] "s"(rd), [so] "s"(so), [sa] "s"(sa)
#define WT_G [sr] "v"(sr), [kd] "s"(1.0f / DM), [ke] "s"(1e-5f), [k1] "s"(-2.0f * 0.7978845608028654f * 1.4426950408889634f * 0.044715f)
#define WT_1 WT_G, [ob] "v"(ob), [ss] "s"(pg + PG_SS + mt * 128), [sw] "s"(nt0)
#define WT_2 [ro] "v"(ro), [sx] "s"(pg + PG_XA + blin(mt * 20480 + nt0 * 512)), [ss] "s"(pg + PG_SS + mt * 128)
#define WT_C "memory", "vcc", "scc", "v160", "v161", "v162", "v163", "v164", "v165", "v166", "v167", "v168", "v169", "v170", "v171", "v172", "v173", "v174", "v175", "v176", "v177", "v178", "v179", "v180", "v181", "v182", "v183", "v184", "v185", "v186", "v187", "v188", "v189", "v190", "v191", "v192", "v193", "v194", "v195", "v204", "v205", "v206", "v207", "v208", "v209", "v210", "v211", "v212", "v213", "v214", "v215", "v252"
#define WT_LO(A, ...) asm volatile(A : "+{v[196:203]}"(cs), "+{v[216:223]}"(k8), "+{v[232:247]}"(va), "+{v[248:251]}"(vb), [n] "+s"(budget), [a] "=&s"(a) ASM_ST_OUT ASM_ST2_OUT ASM_ST3_OUT \
                              : WT_A, __VA_ARGS__, [lw] "v"(lw), [i0] "s"(in0(u)), [i1] "s"(in1(u)), [n0] "s"(in0(u + 1)), [n1] "s"(in1(u + 1)) : WT_C)
#define WT_HI(A, ...) asm volatile(A : "+{v[196:203]}"(cs), "+{v[216:223]}"(k8) ASM_ST_OUT ASM_ST3_OUT : WT_A, __VA_ARGS__ : WT_C)
        if constexpr (LO) { if constexpr (WHICH == 1) WT_LO(ASM_WT1_LO, WT_1); else if constexpr (WHICH == 2) WT_LO(ASM_WT2_LO, WT_2); else WT_LO(ASM_WT3_LO, WT_G); }
        else { if constexpr (WHICH == 1) WT_HI(ASM_WT1_HI, WT_1); else if constexpr (WHICH == 2) WT_HI(ASM_WT2_HI, WT_2); else WT_HI(ASM_WT3_HI, WT_G); }
        la ^= 32768u; lw ^= 32768u; sr ^= 32768u;
        PMARK(!L && mt == (WHICH != 1) && (PMK == 1 || WHICH == 3), PMK == 1 ? 1 + WHICH : 1, (u >> 1) - (WHICH == 1));
    }
    wait_vm<0>();
    if constexpr (LO) asm volatile("" :: "{v[232:247]}"(va), "{v[248:251]}"(vb));
    LF();
    };
    if (wave < 4) run(std::true_type{}); else run(std::false_type{});
}

__device__ __forceinline__ void w4_role(const Params& P, int wave, int L, int cu, int x) {
    constexpr unsigned W_FL = 122880;
    const int ln = lane_id(), l15 = ln & 15, l4 = ln >> 4;
    const unsigned l16 = ln * 16;
    const rsrc_t rd = mkrsrc(P.ws + PG0, PG_L);
    lds_st<unsigned>(W_FL + wave * 4, 0u);
    STDECL;
    if (wave < 6) {
        u32x32 w[5];
        {
            u32x4 g[40];
            load_wn<5, 8, DM>(g, P.w[7], FF, wave, cu * 5, wave * 8);
#pragma unroll
            for (int n = 0; n < 5; ++n) {
                u32x8 f[4];
#pragma unroll
                for (int i = 0; i < 4; ++i) f[i] = __builtin_shufflevector(g[n * 8 + 2 * i], g[n * 8 + 2 * i + 1], 0, 1, 2, 3, 4, 5, 6, 7);
                w[n] = cat32(f[0], f[1], f[2], f[3]);
            }
            asm volatile("" : "+{v[0:31]}"(w[0]), "+{v[32:63]}"(w[1]), "+{v[64:95]}"(w[2]), "+{v[96:127]}"(w[3]), "+{v[128:159]}"(w[4]));
        }
        PLACE();
        bar();
        unsigned budget = 1u << 18;
        u32x32 av;
        asm volatile("v_mov_b32 v160, -1\n\tv_mov_b32 v164, -1\n\tv_mov_b32 v168, -1\n\tv_mov_b32 v172, -1\n\tv_mov_b32 v176, -1\n\tv_mov_b32 v180, -1\n\tv_mov_b32 v184, -1\n\tv_mov_b32 v188, -1" : "={v[160:191]}"(av));
        for (int u = 0; u < 2 * NSEG; ++u) {
            if (u & 1) ST(0);
            for (int spins = 0;; ++spins) {
                asm volatile("" ::: "memory");
                if ((int)__builtin_amdgcn_readfirstlane(lds_ld<unsigned>(W_FL + 24 + (u & 1) * 4)) + 3 >= u) break;
                __builtin_amdgcn_s_sleep(2);
                if (spin_fail(spins, P)) break;
            }
            const unsigned gb = PG_H2 + pgb(u >> 1, par) + ((u & 1) * 6 + wave) * BLK, gn = PG_H2 + pgb((u + 1) >> 1, par) + ((~u & 1) * 6 + wave) * BLK;
            ASM_ST_DECL;
            asm volatile(ASM_W4_M : [n] "+s"(budget), "+{v[160:191]}"(av) ASM_ST2_OUT
                         : "{v[0:31]}"(w[0]), "{v[32:63]}"(w[1]), "{v[64:95]}"(w[2]), "{v[96:127]}"(w[3]), "{v[128:159]}"(w[4]),
                           [ld] "v"(l16 + wave * 1024 + (u & 3) * 30720), [l16] "v"(l16), [rs] "s"(rd), [s0] "s"(gb), [s1] "s"(gb + 4096), [n0] "s"(gn), [n1] "s"(gn + 4096), [un] "s"(u + 1), [fl] "v"(W_FL + wave * 4)
                         : "memory", "vcc", "scc", "v192", "v193", "v194", "v195", "v196", "v197", "v198", "v199", "v200", "v201", "v202", "v203", "v204", "v205", "v206", "v207", "v208", "v209", "v210", "v211", "v212");
            PMARK(!L && (u & 1) && PMK == 2, 2, u >> 1);
        }
        asm volatile("s_waitcnt vmcnt(0)" :: "{v[160:191]}"(av) : "memory");
    } else {
        const int mt = wave - 6;
        PLACE();
        bar();
        const unsigned c0 = mt * 20480 + cu * 2560, o8 = (l4 >> 1) * 256 + l15 * 16 + (l4 & 1) * 8;
        unsigned budget = 1u << 18;
        u32x16 ra;
        u32x4 rb;
        asm volatile("v_mov_b32 v164, -1" : "={v[164:179]}"(ra), "={v[180:183]}"(rb));
        __builtin_amdgcn_s_setprio(W4P);
#define W4F_OUT "+{v[164:179]}"(ra), "+{v[180:183]}"(rb), [n] "+s"(budget)
#define W4F_IN [o0] "v"(o8 + blin(c0)), [o1] "v"(o8 + blin(c0 + 512)), [o2] "v"(o8 + blin(c0 + 1024)), [o3] "v"(o8 + blin(c0 + 1536)), [o4] "v"(o8 + blin(c0 + 2048)), [rd] "s"(rd), \
               [c1] "s"(PG_X1 + pg), [ca] "s"(PG_XA + pg), [n1] "s"(PG_X1 + pn), [na] "s"(PG_XA + pn), [f0] "v"(W_FL + (ln & 3) * 4), [f1] "v"(W_FL + 16 + (ln & 1) * 4), [fd] "v"(W_FL + wave * 4), \
               [la] "v"((u & 3) * 30720 + l16), [u] "s"(u), [un] "s"(u + 1)
#define W4F_CLOB "memory", "vcc", "scc", "v64", "v65", "v66", "v67", "v68", "v69", "v70", "v71", "v72", "v73", "v74", "v75", "v76", "v77", "v78", "v79", "v80", "v81", "v82", "v83", "v84", "v85", "v86", "v87", "v88", "v89", "v90", "v91", "v92", "v93", "v94", "v95", "v96", "v97", "v98", "v99", "v100", "v101", "v102", "v103", "v104", "v105", "v106", "v107", "v108", "v109", "v110", "v111", "v112", "v113", "v114", "v115", "v116", "v117", "v118", "v119", "v120", "v121", "v122", "v123", "v124", "v125", "v126", "v127", "v128", "v129", "v130", "v131", "v132", "v133", "v134", "v135", "v136", "v137", "v138", "v139", "v140", "v141", "v142", "v143", "v144", "v145", "v146", "v147", "v148", "v149", "v150", "v151", "v152", "v153", "v154", "v155", "v156", "v157", "v158", "v159", "v160", "v161", "v162", "v163", "v184", "v185", "v186", "v187", "v188", "v189", "v190", "v191", "v192", "v193", "v194", "v195", "v196", "v197", "v198", "v199", "v200"
        if (L) {
            const rsrc_t ro = mkrsrc(P.out, ACT);
#pragma unroll 1
            for (int t = 0; t < NSEG; ++t) {
                const unsigned u = t * 2 + mt, pg = pgb(t, par), pn = pgb(t + 1, par);
                ASM_ST_DECL;
                asm volatile(ASM_W4F_1 : W4F_OUT ASM_ST2_OUT : W4F_IN, [pa] "v"(l15 * (DM * 2) + (l4 & 1) * 32 + (l4 >> 1) * 16), [pb] "v"(l15 * (DM * 2) + l4 * 8), [ro] "s"(ro), [so] "s"(u * 20480 + cu * 160) : W4F_CLOB);
            }
        } else {
            const rsrc_t rn = mkrsrc(P.ws + (par ^ 1) * 8192, RG_L);
#pragma unroll 1
            for (int t = 0; t < NSEG; ++t) {
                const unsigned u = t * 2 + mt, pg = pgb(t, par), pn = pgb(t + 1, par), sn = (pg ^ 8192) + PG_LX, st = RG_ST + (u >> 3) * BLK + (u & 7) * 1024;
                ASM_ST_DECL;
                asm volatile(ASM_W4F_0 : W4F_OUT ASM_ST_OUT ASM_ST2_OUT ASM_ST3_OUT
                             : W4F_IN, [pa] "v"(blin(c0 + (l4 & 1) * 512) + (l4 >> 1) * 256 + l15 * 16), [pb] "v"(blin(c0 + 1024 + (l4 & 1) * 512) + (l4 >> 1) * 256 + l15 * 16),
                               [so] "v"(ln < 16 ? ln * 8 : OOB), [si] "v"(ln * 8), [rr] "s"(rr), [sp] "s"(st + cu * 128), [sq] "s"(st), [xr] "s"(PG_XR + sn), [xn] "s"(PG_XN + sn),
                               [kd] "s"(1.0f / DM), [ke] "s"(1e-5f), [tn] "s"(t + 1), [fo] "v"(ln < 32 ? (ln >> 3) * 2048 + (ln & 7) * 16 : OOB), [rn] "s"(rn), [fs] "s"(RG_FX + (cu * 2 + mt) * 128) : W4F_CLOB);
            }
        }
        asm volatile("s_waitcnt vmcnt(0)" :: "{v[164:179]}"(ra), "{v[180:183]}"(rb) : "memory");
    }
}

__device__ __forceinline__ unsigned probe(char* ws, unsigned base, unsigned bit, int n) {
    const rsrc_t r = mkrsrc(ws, RG_SLOT);
    unsigned m0 = ~0u, m1 = ~0u;
    for (int i = 0; i < n; ++i) {
        const unsigned long long t0 = __builtin_amdgcn_s_memtime();
        __builtin_amdgcn_raw_buffer_store_b32(0u, r, 96 * BLK + base + (i & 1) * bit + ((i >> 2) & 1) * 4096 + (i >> 2) * 768, 0, 16);
        asm volatile("s_waitcnt vmcnt(0)" ::: "memory");
        const unsigned d = (unsigned)(__builtin_amdgcn_s_memtime() - t0);
        if (i & 1) m1 = d < m1 ? d : m1; else m0 = d < m0 ? d : m0;
    }
    return m0 > m1 ? 1u : 0u;
}
__device__ __forceinline__ void setup(const Params& P, int wave, int mt, int par0, int ph) {
    const bool xw = wave < 4 && (mt >= 2 * XS0) == (ph != 0);
    const int tid = threadIdx.x & 255, r15 = tid & 15, slot = tid >> 4;
    const rsrc_t rw = mkrsrc(P.ws, WS_END);
    u32x4 v[5];
    float s = 0.f, q = 0.f;
    if (xw) {
#pragma unroll
        for (int c = 0; c < 5; ++c) v[c] = *(const u32x4*)(P.x + (size_t)(mt * 16 + r15) * DM + (slot * 5 + c) * 8);
#pragma unroll
        for (int c = 0; c < 5; ++c)
#pragma unroll
            for (int e = 0; e < 4; ++e) s += lo2f(v[c][e]) + hi2f(v[c][e]);
        lds_st<float>((slot * 17 + r15) * 4, s);
    } else if (wave >= 4) {
        constexpr unsigned N0 = RG_ST / BLK, N1 = 4 * N0 + 16, N2 = PG_SS / BLK;
        const u32x4 s4 = sent4();
        for (unsigned k = mt; k < 2 * (N1 + 4 * N2); k += 128) {
            const unsigned i = k >> 1;
            const unsigned o = i < N1 ? (i < 4 * N0 ? (i / N0) * RG_SLOT + (i % N0) * BLK : (i - 3 * N0) * BLK) + (par0 ^ ph) * 8192 : PG0 + pgb((i - N1) / N2, par0 ^ ph) + ((i - N1) % N2) * BLK;
            st128(s4, rw, tid * 16, o + (k & 1) * 4096);
        }
    }
    bar();
    float mean = 0.f;
    if (xw) {
        s = 0.f;
#pragma unroll
        for (int k = 0; k < 16; ++k) s += lds_ld<float>((k * 17 + r15) * 4);
        mean = s * (1.0f / DM);
#pragma unroll
        for (int c = 0; c < 5; ++c)
#pragma unroll
            for (int e = 0; e < 4; ++e) { const float a = lo2f(v[c][e]) - mean, b = hi2f(v[c][e]) - mean; q += a * a + b * b; }
        lds_st<float>(2048 + (slot * 17 + r15) * 4, q);
    }
    bar();
    if (xw) {
        q = 0.f;
#pragma unroll
        for (int k = 0; k < 16; ++k) q += lds_ld<float>(2048 + (k * 17 + r15) * 4);
        const float m1 = rbf(mean), r = 1.0f / ln_den(q * (1.0f / DM));
#pragma unroll
        for (int c = 0; c < 5; ++c) {
            const unsigned o = toff(mt * 16 + r15, (slot * 5 + c) * 8, par0);
            st128(v[c], rw, o, PG0 + PG_XR);
            u32x4 n;
#pragma unroll
            for (int e = 0; e < 4; ++e) n[e] = pack2(rbf(lo2f(v[c][e]) - m1) * r, rbf(hi2f(v[c][e]) - m1) * r);
            st128(n, rw, o, PG0 + PG_XN);
        }
    }
    wait_vm<0>();
    bar();
    if (threadIdx.x < 8) __hip_atomic_fetch_add((unsigned*)(P.ws + FL0 + FL_IN + (ph * 8 + threadIdx.x) * 128), 1u, RLX_AGENT);
}

__global__ __launch_bounds__(512, 2) void mega_kernel(Params P) {
    const int wave = __builtin_amdgcn_readfirstlane(threadIdx.x >> 6);
    u32x4 cpf;
    {
        unsigned long long pc;
        unsigned len;
        asm volatile("s_getpc_b64 %0\n\ts_mov_b32 %1, _Z12prep1_kernel5Prep1-_Z11mega_kernel6Params" : "=s"(pc), "=s"(len));
        cpf = __builtin_amdgcn_raw_buffer_load_b128(mkrsrc((const void*)(pc & ~255ull), len), (blockIdx.x >> 3) * 8192 + threadIdx.x * 16, 0, 0);
    }
    if (threadIdx.x == 0) {
        unsigned g = (unsigned)__builtin_amdgcn_s_getreg((3 << 11) | 20) & 7u, k;
        while ((k = __hip_atomic_fetch_add((unsigned*)(P.ws + FL0 + FL_TK + g * 128), 1u, RLX_AGENT)) >= 32u) g = (g + 1) & 7u;
        lds_st<unsigned>(0, g << 5 | k);
    }
    bar();
    const unsigned tk = __builtin_amdgcn_readfirstlane(lds_ld<unsigned>(0));
    asm volatile("" :: "v"(cpf));
    bar();
    const int g4 = (tk >> 5) & 3, k = tk & 31;
    const int L = tk >> 7, x = g4, r = k;
    if (L) {
        const int par0 = (init_wait(P, wave, 1, 0u) >> 16) & 1;
        if (tk == 255 && wave == 0) {
            const unsigned xc = (unsigned)__builtin_amdgcn_s_getreg((3 << 11) | 20);
            const unsigned f = (probe(P.ws, ((par0 ^ (xc >> 2)) & 1) * 8192, BLK, CAL2) ^ 1u ^ xc) & 1u;
            if (threadIdx.x < 16) __hip_atomic_fetch_add((unsigned*)(P.ws + FL0 + FL_IN + threadIdx.x * 128), (2u + f) << 20, RLX_AGENT);
        }
#pragma unroll 1
        for (int ph = 0; ph < 2; ++ph) {
            setup(P, wave, (tk & 31) * 4 + g4, par0, ph);
            for (int k = ph * DLY_L; k < DLY_L; ++k) __builtin_amdgcn_s_sleep(32);
        }
    }
    {
        const int i = (r - 26) * 4 + x;
        const int d = (r < 8 ? DLY_S : r < 21 ? 0 : i < 4 ? DLY_H : DLY_W);
        for (int k = 0; k < d; ++k) __builtin_amdgcn_s_sleep(32);
    }
    if (r < 8) s_role(P, wave, L, x * 8 + r, x);
    else if (r < 16) v_role(P, wave ^ 4, L, x * 8 + r - 8, x);
    else if (r < 21) r_role(P, wave, L, r - 16, x);
    else {
        const int i = (r - 26) * 4 + x;
        if (i < 4) h_role(P, wave, L, r < 25 ? x * 4 + r - 21 : 16 + x * 2 + r - 25, x);
        else if (i < 7) w_role<1>(P, wave, L, i - 4, x);
        else if (i < 10) w_role<2>(P, wave, L, i - 7, x);
        else if (i < 16) w_role<3>(P, wave, L, i - 10, x);
        else w4_role(P, wave, L, i - 16, x);
    }
}

struct Prep1 { char* ws; };
__global__ __launch_bounds__(256) void prep1_kernel(Prep1 A) {
    int b = blockIdx.x;
    if (b == 0) {
        if (threadIdx.x >= 64) return;
        const unsigned h = (probe(A.ws, 0, 8192, CAL1) ^ ((unsigned)__builtin_amdgcn_s_getreg((3 << 11) | 20) >> 2)) & 1u;
        if (threadIdx.x < 16) *(unsigned*)(A.ws + FL0 + FL_IN + threadIdx.x * 128) = (2u + h) << 16;
        return;
    }
    b -= 1;
    ((u32x4*)(A.ws + RG_FV))[b * 256 + threadIdx.x] = (u32x4){0u, 0u, 0u, 0u};
    if (b == 0 && threadIdx.x < FL_IN / 16) ((u32x4*)(A.ws + FL0))[threadIdx.x] = (u32x4){0u, 0u, 0u, 0u};
}

extern "C" void kernel_launch(void* const* d_in, const int* in_sizes, int n_in, void* d_out, int out_size, void* d_ws,
                              size_t ws_size, hipStream_t stream) {
    char* ws = (char*)d_ws;
    Params P;
    P.ws = ws; P.x = (const bf16_t*)d_in[0]; P.mem0 = (const bf16_t*)d_in[1]; P.out = (bf16_t*)d_out;
    for (int i = 0; i < 8; ++i) P.w[i] = (const bf16_t*)d_in[i < 4 ? 2 + i : 4 + i];
    (void)hipFuncSetAttribute((const void*)mega_kernel, hipFuncAttributeMaxDynamicSharedMemorySize, MEGA_LDS);

    Prep1 A1;
    A1.ws = ws;
    prep1_kernel<<<21, 256, 0, stream>>>(A1);
    mega_kernel<<<256, 512, MEGA_LDS, stream>>>(P);
}
```

```cpp
#include <hip/hip_runtime.h>
#include <stdint.h>
#include <type_traits>

typedef unsigned short bf16_t;
using bf16x2 = __attribute__((ext_vector_type(2))) __bf16;
using bf16x8 = __attribute__((ext_vector_type(8))) __bf16;
using bf16x16 = __attribute__((ext_vector_type(16))) __bf16;
using f32x2 = __attribute__((ext_vector_type(2))) float;
using f32x4 = __attribute__((ext_vector_type(4))) float;
using u32x2 = __attribute__((ext_vector_type(2))) unsigned;
using u32x4 = __attribute__((ext_vector_type(4))) unsigned;
typedef __amdgpu_buffer_rsrc_t rsrc_t;

__device__ __forceinline__ unsigned pack2(float a, float b) {
    bf16x2 r = __builtin_convertvector((f32x2){a, b}, bf16x2);
    return __builtin_bit_cast(unsigned, r);
}
__device__ __forceinline__ float rbf(float f) { return __uint_as_float(pack2(f, 0.f) << 16); }
__device__ __forceinline__ float lo2f(unsigned u) { return __uint_as_float(u << 16); }
__device__ __forceinline__ float hi2f(unsigned u) { return __uint_as_float(u & 0xFFFF0000u); }
__device__ __forceinline__ rsrc_t mkrsrc(const void* p, unsigned bytes) {
    return __builtin_amdgcn_make_buffer_rsrc((void*)p, 0, bytes, 0x00020000);
}

constexpr int S_ = 2048, DM = 640, NH = 32, DH = 192, HD = NH * DH  , SEG = 32, NSEG = 64;
constexpr int FF = 1536;

#define XMAP
#define NCPY 4
constexpr unsigned NSLOT = 4, NCP = NCPY;
#define FL_TK_DEF
#define DLY_L 12
#define DLY_S 1
#define DLY_W 10
#define DLY_H 2
#define EWAKE "s_nop 0"
#define LWAKE "s_wakeup\n\t"
#define SLP4 (wave == 4 ? t : 0)
#define WPF 12
#define WPRI 1
#define WPRI2 0
#define RTD 3500
#define VTD 3900
#define HAH 100
#define WEAK 0
#define BAL 0
#define HSL 8
#define OAUX 2
#define W4P 3
#define STD 3800
#define CAL1 4
#define CAL2 12
#define CALN 12
#define RSL 9
#define VTRY 0
#define VSL 1
#define NRR 5
constexpr unsigned BLK = 16384, RG_SLOT = 4u << 20;
constexpr unsigned XP_PAIR = 6 * BLK, XP_SLOT = RG_SLOT;
constexpr unsigned XM_PAIR = 4 * BLK, XM_SLOT = RG_SLOT;
constexpr unsigned VM_SLOT = RG_SLOT;
constexpr unsigned PX_SLOT = RG_SLOT;
constexpr unsigned RG_XP = 0, RG_XM = 12 * BLK, RG_VM = 20 * BLK, RG_PX = 60 * BLK;
constexpr unsigned RG_ST = 68 * BLK, RG_FV = 84 * BLK, RG_FH = 86 * BLK, RG_FX = 88 * BLK, RG_L = NSLOT * RG_SLOT;
constexpr unsigned FL0 = 89 * BLK, FL_TK = 512, FL_IN = FL_TK + 8 * 128, FL_BYTES = FL_IN + 16 * 128;
constexpr unsigned ACT = S_ * DM * 2;
#define PXROT 5
#define W4SL 5
#define RXDEL 1
#define VXD 32
#define VXAUX 16
constexpr unsigned PG0 = RG_L, PG_L = 80u << 21;
constexpr unsigned PG_XA = 0, PG_HS = 5 * BLK, PG_X1 = 10 * BLK, PG_H2 = 15 * BLK, PG_SS = 27 * BLK, PG_XR = 28 * BLK, PG_XN = 33 * BLK, PG_LX = 10 * BLK, PG_HV = 48 * BLK;
__device__ __forceinline__ unsigned pgb(unsigned p, int par) { return p << 21 | ((p ^ par) & 1) << 13; }
__device__ __forceinline__ constexpr unsigned blin(unsigned o) { return o + (o & ~8191u); }
constexpr int HT0 = 2;
constexpr int AH = 3;
constexpr unsigned WS_END = PG0 + PG_L;

#define SB() __builtin_amdgcn_sched_barrier(0)
#define LAS __attribute__((address_space(3)))
template <class T> __device__ __forceinline__ T lds_ld(unsigned a) { return *(const LAS T*)(a); }
template <class T> __device__ __forceinline__ void lds_st(unsigned a, T v) { *(LAS T*)(a) = v; }
constexpr unsigned OOB = 0x7FFFFF00u;
template <int N> __device__ __forceinline__ void wait_vm() { asm volatile("s_waitcnt vmcnt(%0)" :: "i"(N) : "memory"); }
__device__ __forceinline__ void bar() {
    asm volatile("s_waitcnt lgkmcnt(0)" ::: "memory");
    __builtin_amdgcn_s_barrier();
    asm volatile("" ::: "memory");
}
__device__ __forceinline__ int lane_id() {
    int ln;
    asm volatile("v_mbcnt_lo_u32_b32 %0, -1, 0\n\tv_mbcnt_hi_u32_b32 %0, -1, %0" : "=v"(ln));
    return ln;
}
__device__ __forceinline__ void swap16(unsigned& a, unsigned& b) { asm volatile("s_nop 3\n\tv_permlane16_swap_b32 %0, %1\n\ts_nop 1" : "+v"(a), "+v"(b)); }
__device__ __forceinline__ void swap32(unsigned& a, unsigned& b) { asm volatile("s_nop 3\n\tv_permlane32_swap_b32 %0, %1\n\ts_nop 1" : "+v"(a), "+v"(b)); }
__device__ __forceinline__ float sum_x16(float x) { unsigned a = __float_as_uint(x), b = a; swap16(a, b); return __uint_as_float(a) + __uint_as_float(b); }
__device__ __forceinline__ float sum_x32(float x) { unsigned a = __float_as_uint(x), b = a; swap32(a, b); return __uint_as_float(a) + __uint_as_float(b); }
__device__ __forceinline__ float max_x16(float x) { unsigned a = __float_as_uint(x), b = a; swap16(a, b); return __builtin_fmaxf(__uint_as_float(a), __uint_as_float(b)); }
__device__ __forceinline__ float max_x32(float x) { unsigned a = __float_as_uint(x), b = a; swap32(a, b); return __builtin_fmaxf(__uint_as_float(a), __uint_as_float(b)); }
__device__ __forceinline__ void sum2_rows(float& s, float& q) {
    unsigned a = __float_as_uint(s), b = a, c = __float_as_uint(q), d = c;
    asm volatile("s_nop 2\n\tv_permlane16_swap_b32 %0, %1\n\tv_permlane16_swap_b32 %2, %3\n\tv_add_f32 %0, %0, %1\n\tv_add_f32 %2, %2, %3\n\tv_mov_b32 %1, %0\n\tv_mov_b32 %3, %2\n\t"
                 "s_nop 1\n\tv_permlane32_swap_b32 %0, %1\n\tv_permlane32_swap_b32 %2, %3\n\tv_add_f32 %0, %0, %1\n\tv_add_f32 %2, %2, %3" : "+v"(a), "+v"(b), "+v"(c), "+v"(d));
    s = __uint_as_float(a); q = __uint_as_float(c);
}
__device__ __forceinline__ u32x4 merge16(u32x2 a, u32x2 b) {
    unsigned a0 = a[0], a1 = a[1], b0 = b[0], b1 = b[1];
    asm volatile("s_nop 3\n\tv_permlane16_swap_b32 %0, %2\n\tv_permlane16_swap_b32 %1, %3\n\ts_nop 1" : "+v"(a0), "+v"(a1), "+v"(b0), "+v"(b1));
    return (u32x4){a0, a1, b0, b1};
}
template <int AUXV>
__device__ __forceinline__ void st128_(u32x4 v, rsrc_t r, unsigned voff, unsigned soff) {
    __builtin_amdgcn_raw_buffer_store_b128(v, r, voff, soff, AUXV);
    asm volatile("s_nop 1" :: "v"(v) : "memory");
}
#define st128(V, R, VO_, SO_) st128_<16>(V, R, VO_, SO_)
#define LD128(R, VO_, SO_) __builtin_amdgcn_raw_buffer_load_b128(R, VO_, SO_, 16)
#define LD64(R, VO_, SO_) __builtin_amdgcn_raw_buffer_load_b64(R, VO_, SO_, 16)
#define BF8(X) __builtin_bit_cast(bf16x8, X)
__device__ __forceinline__ float gelu_tanh(float x) {
    const float c0 = -2.0f * 0.7978845608028654f * 1.4426950408889634f, c1 = c0 * 0.044715f;
    const float e = __builtin_amdgcn_exp2f(x * __builtin_fmaf(x * x, c1, c0));
    return x * __builtin_amdgcn_rcpf(1.0f + e);
}
#define MFMA(A_, B_, C_) C_ = __builtin_amdgcn_mfma_f32_16x16x32_bf16(A_, B_, C_, 0, 0, 0)
#define SMF(A_, B_, C_) C_ = __builtin_amdgcn_smfmac_f32_16x16x64_bf16(A_, B_, C_, sidx, 0, 0)
__device__ __forceinline__ float ln_den(float var) { return rbf(__builtin_amdgcn_sqrtf(rbf(rbf(var) + 1e-5f))); }
__device__ __forceinline__ float rcp(float x) { return __builtin_amdgcn_rcpf(x); }

struct Params {
    char* ws;
    const bf16_t* x;
    const bf16_t* mem0;
    const bf16_t* w[8];
    bf16_t* out;
};

#define RLX_AGENT __ATOMIC_RELAXED, __HIP_MEMORY_SCOPE_AGENT
#define SENT 0xFFFFFFFFu
constexpr int SPIN_CAP = 400000;
__device__ __forceinline__ bool spin_fail(int spins, const Params& P) {
    if ((spins & 1023) == 1023) {
        const rsrc_t rt = mkrsrc(P.ws + FL0, FL_BYTES);
        unsigned o;
        asm volatile("v_mov_b32 %0, 0x100" : "=v"(o));
        if (__builtin_amdgcn_readfirstlane(__builtin_amdgcn_raw_buffer_load_b32(rt, o, 0, 16)) != 0u) return true;
        if (spins > SPIN_CAP) { __builtin_amdgcn_raw_buffer_store_b32(o, rt, o, 0, 16); return true; }
    }
    return false;
}
__device__ __forceinline__ bool ok4(u32x4 v) { return v[0] != SENT && v[1] != SENT && v[2] != SENT && v[3] != SENT; }
__device__ __forceinline__ unsigned nosent(unsigned d) { return d < 0xFFFFFFFEu ? d : 0xFFFFFFFEu; }
__device__ __forceinline__ u32x4 nosent4(u32x4 v) { return (u32x4){nosent(v[0]), nosent(v[1]), nosent(v[2]), nosent(v[3])}; }
__device__ __forceinline__ unsigned umax3(unsigned a, unsigned b, unsigned c) { const unsigned m = a > b ? a : b; return m > c ? m : c; }
__device__ __forceinline__ unsigned umax4(unsigned m, u32x4 v) { return umax3(umax3(m, v[0], v[1]), v[2], v[3]); }
__device__ __forceinline__ u32x4 sent4() { unsigned sv; asm volatile("v_mov_b32 %0, -1" : "=v"(sv)); return (u32x4){sv, sv, sv, sv}; }
template <int N>
__device__ __forceinline__ void poll(rsrc_t rs, unsigned base, const unsigned (&off)[N], u32x4 (&v)[N], const Params& P) {
    for (int spins = 0;; ++spins) {
        unsigned m = 0;
#pragma unroll
        for (int k = 0; k < N; ++k) v[k] = LD128(rs, off[k], base);
#pragma unroll
        for (int k = 0; k < N; ++k) m = umax4(m, v[k]);
        if (__all(m != SENT)) return;
        asm volatile("" ::: "memory");
        if (spin_fail(spins, P)) return;
    }
}
__device__ __forceinline__ bf16x16 cat16(bf16x8 a, bf16x8 b) {
    return __builtin_shufflevector(a, b, 0, 1, 2, 3, 4, 5, 6, 7, 8, 9, 10, 11, 12, 13, 14, 15);
}
__device__ __forceinline__ bf16x8 half8(bf16x16 v, int h) {
    return h ? __builtin_shufflevector(v, v, 8, 9, 10, 11, 12, 13, 14, 15) : __builtin_shufflevector(v, v, 0, 1, 2, 3, 4, 5, 6, 7);
}
__device__ __forceinline__ bf16x16 lds_frag(unsigned a, unsigned step) { return cat16(lds_ld<bf16x8>(a), lds_ld<bf16x8>(a + step)); }
template <int I>
__device__ __forceinline__ void dmaq(const char* g, unsigned l16, unsigned dst) {
    asm volatile("s_mov_b32 m0, %2\n\ts_nop 0\n\tglobal_load_lds_dwordx4 %0, %1 offset:%3 sc1" :: "v"(l16), "s"(g), "s"(dst), "i"(I * 1024) : "memory");
}
__device__ __forceinline__ void dma4(const char* g, unsigned l16, unsigned dst) {
    asm volatile("s_mov_b32 m0, %2\n\ts_nop 0\n\tglobal_load_lds_dwordx4 %0, %1 sc1\n\tglobal_load_lds_dwordx4 %0, %1 offset:1024 sc1\n\t"
                 "global_load_lds_dwordx4 %0, %1 offset:2048 sc1\n\tglobal_load_lds_dwordx4 %0, %1 offset:3072 sc1" :: "v"(l16), "s"(g), "s"(dst) : "memory");
}
__device__ __forceinline__ unsigned toff(int row, int col, int par) {
    return pgb(row >> 5, par) + blin((unsigned)((row >> 4) & 1) * 20480u + (unsigned)(col >> 3) * 256u) + (unsigned)(row & 15) * 16u;
}

#define ASM_ST
#define ASM_ST_DECL
#define ASM_ST_OUT
#define ASM_ST_SAVE(k)
#define ASM_ST2
#define ASM_ST3
#define ASM_ST3_OUT
#define ASM_ST3_SAVE(k)
#define ASM_ST2_OUT
#define ASM_ST2_SAVE(k)
#define STDECL
#define ST(k)
#define STC(k)
#define STFLUSH(t)

#define MARK(c, k)
#define MARKX(c, k)
#define PMARK(c, st, seg)
#define LDECL
#define LA()
#define LB(t)
#define LF()
#define STROLE 3
constexpr unsigned L_MEM = 0;
constexpr unsigned L_Q = 10240;
constexpr unsigned L_KM = 13312;
constexpr unsigned L_QX = 16384;
constexpr unsigned L_KX = 28672;
constexpr unsigned L_XB = 40960;
constexpr unsigned L_XA = 49152;
constexpr unsigned L_W = 81920;
constexpr int MEGA_LDS = 163840;
constexpr int wi(int i, int j) { return (i >> 1) * 5 + ((i & 1) ? 2 + j : j - 1); }

#define RPUB2
#define WNT ""
#define PRIUP "s_setprio 2\n\t"
#define PRISM PRIUP
#define STR2(x) #x
#define STRX(x) STR2(x)
#define W4SLEEP "s_sleep " STRX(W4SL) "\n\t"
#define PLAY 0
#define V1A 0
#define ASM_WLOAD3 \
    "s_xor_b32 %[t], %[wx], 0\n\t" \
    "s_mul_i32 %[t], %[t], 0xc0000\n\t" \
    "buffer_load_dwordx4 v[200:203], %[o0], %[rs], %[t] offen" WNT "\n\t" \
    "buffer_load_dwordx4 v[0:3], %[o1], %[rs], %[t] offen" WNT "\n\t" \
    "buffer_load_dwordx4 v[8:11], %[o2], %[rs], %[t] offen" WNT "\n\t" \
    "s_add_u32 %[t], %[t], 0x18000\n\t" \
    "buffer_load_dwordx4 v[16:19], %[o0], %[rs], %[t] offen" WNT "\n\t" \
    "buffer_load_dwordx4 v[24:27], %[o1], %[rs], %[t] offen" WNT "\n\t" \
    "buffer_load_dwordx4 v[32:35], %[o2], %[rs], %[t] offen" WNT "\n\t" \
    "s_add_u32 %[t], %[t], 0x18000\n\t" \
    "buffer_load_dwordx4 v[208:211], %[o0], %[rs], %[t] offen" WNT "\n\t" \
    "buffer_load_dwordx4 v[40:43], %[o1], %[rs], %[t] offen" WNT "\n\t" \
    "buffer_load_dwordx4 v[48:51], %[o2], %[rs], %[t] offen" WNT "\n\t" \
    "s_add_u32 %[t], %[t], 0x18000\n\t" \
    "buffer_load_dwordx4 v[56:59], %[o0], %[rs], %[t] offen" WNT "\n\t" \
    "buffer_load_dwordx4 v[64:67], %[o1], %[rs], %[t] offen" WNT "\n\t" \
    "buffer_load_dwordx4 v[72:75], %[o2], %[rs], %[t] offen" WNT "\n\t" \
    "s_add_u32 %[t], %[t], 0x18000\n\t" \
    "buffer_load_dwordx4 v[204:207], %[o0], %[rs], %[t] offen" WNT "\n\t" \
    "buffer_load_dwordx4 v[4:7], %[o1], %[rs], %[t] offen" WNT "\n\t" \
    "buffer_load_dwordx4 v[12:15], %[o2], %[rs], %[t] offen" WNT "\n\t" \
    "s_add_u32 %[t], %[t], 0x18000\n\t" \
    "buffer_load_dwordx4 v[20:23], %[o0], %[rs], %[t] offen" WNT "\n\t" \
    "buffer_load_dwordx4 v[28:31], %[o1], %[rs], %[t] offen" WNT "\n\t" \
    "buffer_load_dwordx4 v[36:39], %[o2], %[rs], %[t] offen" WNT "\n\t" \
    "s_add_u32 %[t], %[t], 0x18000\n\t" \
    "buffer_load_dwordx4 v[212:215], %[o0], %[rs], %[t] offen" WNT "\n\t" \
    "buffer_load_dwordx4 v[44:47], %[o1], %[rs], %[t] offen" WNT "\n\t" \
    "buffer_load_dwordx4 v[52:55], %[o2], %[rs], %[t] offen" WNT "\n\t" \
    "s_add_u32 %[t], %[t], 0x18000\n\t" \
    "buffer_load_dwordx4 v[60:63], %[o0], %[rs], %[t] offen" WNT "\n\t" \
    "buffer_load_dwordx4 v[68:71], %[o1], %[rs], %[t] offen" WNT "\n\t" \
    "buffer_load_dwordx4 v[76:79], %[o2], %[rs], %[t] offen" WNT "\n\t" \
    "s_add_u32 %[t], %[t], 0x18000\n\t" \
    "s_waitcnt vmcnt(21)\n\t" \
    "ds_write_b128 %[s0], v[200:203] offset:0\n\t" \
    "ds_write_b128 %[s1], v[0:3] offset:0\n\t" \
    "ds_write_b128 %[s2], v[8:11] offset:0\n\t" \
    "s_waitcnt vmcnt(18)\n\t" \
    "ds_write_b128 %[s0], v[16:19] offset:3456\n\t" \
    "ds_write_b128 %[s1], v[24:27] offset:3456\n\t" \
    "ds_write_b128 %[s2], v[32:35] offset:3456\n\t" \
    "s_waitcnt vmcnt(15)\n\t" \
    "ds_write_b128 %[s0], v[208:211] offset:6912\n\t" \
    "ds_write_b128 %[s1], v[40:43] offset:6912\n\t" \
    "ds_write_b128 %[s2], v[48:51] offset:6912\n\t" \
    "s_waitcnt vmcnt(12)\n\t" \
    "ds_write_b128 %[s0], v[56:59] offset:10368\n\t" \
    "ds_write_b128 %[s1], v[64:67] offset:10368\n\t" \
    "ds_write_b128 %[s2], v[72:75] offset:10368\n\t" \
    "s_waitcnt lgkmcnt(0)\n\t" \
    "s_barrier \n\t" \
    "ds_read_b64_tr_b16 v[200:201], %[t0] offset:0\n\t" \
    "ds_read_b64_tr_b16 v[202:203], %[t0] offset:1664\n\t" \
    "ds_read_b64_tr_b16 v[0:1], %[t0] offset:128\n\t" \
    "ds_read_b64_tr_b16 v[2:3], %[t0] offset:1792\n\t" \
    "ds_read_b64_tr_b16 v[8:9], %[t0] offset:256\n\t" \
    "ds_read_b64_tr_b16 v[10:11], %[t0] offset:1920\n\t" \
    "ds_read_b64_tr_b16 v[16:17], %[t1] offset:0\n\t" \
    "ds_read_b64_tr_b16 v[18:19], %[t1] offset:1664\n\t" \
    "ds_read_b64_tr_b16 v[24:25], %[t1] offset:128\n\t" \
    "ds_read_b64_tr_b16 v[26:27], %[t1] offset:1792\n\t" \
    "ds_read_b64_tr_b16 v[32:33], %[t1] offset:256\n\t" \
    "ds_read_b64_tr_b16 v[34:35], %[t1] offset:1920\n\t" \
    "ds_read_b64_tr_b16 v[208:209], %[t2] offset:0\n\t" \
    "ds_read_b64_tr_b16 v[210:211], %[t2] offset:1664\n\t" \
    "ds_read_b64_tr_b16 v[40:41], %[t2] offset:128\n\t" \
    "ds_read_b64_tr_b16 v[42:43], %[t2] offset:1792\n\t" \
    "ds_read_b64_tr_b16 v[48:49], %[t2] offset:256\n\t" \
    "ds_read_b64_tr_b16 v[50:51], %[t2] offset:1920\n\t" \
    "ds_read_b64_tr_b16 v[56:57], %[t3] offset:0\n\t" \
    "ds_read_b64_tr_b16 v[58:59], %[t3] offset:1664\n\t" \
    "ds_read_b64_tr_b16 v[64:65], %[t3] offset:128\n\t" \
    "ds_read_b64_tr_b16 v[66:67], %[t3] offset:1792\n\t" \
    "ds_read_b64_tr_b16 v[72:73], %[t3] offset:256\n\t" \
    "ds_read_b64_tr_b16 v[74:75], %[t3] offset:1920\n\t" \
    "s_xor_b32 %[t], %[wx], 4\n\t" \
    "s_mul_i32 %[t], %[t], 0xc0000\n\t" \
    "buffer_load_dwordx4 v[216:219], %[o0], %[rs], %[t] offen" WNT "\n\t" \
    "buffer_load_dwordx4 v[80:83], %[o1], %[rs], %[t] offen" WNT "\n\t" \
    "buffer_load_dwordx4 v[88:91], %[o2], %[rs], %[t] offen" WNT "\n\t" \
    "s_add_u32 %[t], %[t], 0x18000\n\t" \
    "buffer_load_dwordx4 v[96:99], %[o0], %[rs], %[t] offen" WNT "\n\t" \
    "buffer_load_dwordx4 v[104:107], %[o1], %[rs], %[t] offen" WNT "\n\t" \
    "buffer_load_dwordx4 v[112:115], %[o2], %[rs], %[t] offen" WNT "\n\t" \
    "s_add_u32 %[t], %[t], 0x18000\n\t" \
    "buffer_load_dwordx4 v[224:227], %[o0], %[rs], %[t] offen" WNT "\n\t" \
    "buffer_load_dwordx4 v[120:123], %[o1], %[rs], %[t] offen" WNT "\n\t" \
    "buffer_load_dwordx4 v[128:131], %[o2], %[rs], %[t] offen" WNT "\n\t" \
    "s_add_u32 %[t], %[t], 0x18000\n\t" \
    "buffer_load_dwordx4 v[136:139], %[o0], %[rs], %[t] offen" WNT "\n\t" \
    "buffer_load_dwordx4 v[144:147], %[o1], %[rs], %[t] offen" WNT "\n\t" \
    "buffer_load_dwordx4 v[152:155], %[o2], %[rs], %[t] offen" WNT "\n\t" \
    "s_add_u32 %[t], %[t], 0x18000\n\t" \
    "s_waitcnt lgkmcnt(0)\n\t" \
    "s_barrier \n\t" \
    "s_waitcnt vmcnt(21)\n\t" \
    "ds_write_b128 %[s0], v[204:207] offset:0\n\t" \
    "ds_write_b128 %[s1], v[4:7] offset:0\n\t" \
    "ds_write_b128 %[s2], v[12:15] offset:0\n\t" \
    "s_waitcnt vmcnt(18)\n\t" \
    "ds_write_b128 %[s0], v[20:23] offset:3456\n\t" \
    "ds_write_b128 %[s1], v[28:31] offset:3456\n\t" \
    "ds_write_b128 %[s2], v[36:39] offset:3456\n\t" \
    "s_waitcnt vmcnt(15)\n\t" \
    "ds_write_b128 %[s0], v[212:215] offset:6912\n\t" \
    "ds_write_b128 %[s1], v[44:47] offset:6912\n\t" \
    "ds_write_b128 %[s2], v[52:55] offset:6912\n\t" \
    "s_waitcnt vmcnt(12)\n\t" \
    "ds_write_b128 %[s0], v[60:63] offset:10368\n\t" \
    "ds_write_b128 %[s1], v[68:71] offset:10368\n\t" \
    "ds_write_b128 %[s2], v[76:79] offset:10368\n\t" \
    "s_waitcnt lgkmcnt(0)\n\t" \
    "s_barrier \n\t" \
    "ds_read_b64_tr_b16 v[204:205], %[t0] offset:0\n\t" \
    "ds_read_b64_tr_b16 v[206:207], %[t0] offset:1664\n\t" \
    "ds_read_b64_tr_b16 v[4:5], %[t0] offset:128\n\t" \
    "ds_read_b64_tr_b16 v[6:7], %[t0] offset:1792\n\t" \
    "ds_read_b64_tr_b16 v[12:13], %[t0] offset:256\n\t" \
    "ds_read_b64_tr_b16 v[14:15], %[t0] offset:1920\n\t" \
    "ds_read_b64_tr_b16 v[20:21], %[t1] offset:0\n\t" \
    "ds_read_b64_tr_b16 v[22:23], %[t1] offset:1664\n\t" \
    "ds_read_b64_tr_b16 v[28:29], %[t1] offset:128\n\t" \
    "ds_read_b64_tr_b16 v[30:31], %[t1] offset:1792\n\t" \
    "ds_read_b64_tr_b16 v[36:37], %[t1] offset:256\n\t" \
    "ds_read_b64_tr_b16 v[38:39], %[t1] offset:1920\n\t" \
    "ds_read_b64_tr_b16 v[212:213], %[t2] offset:0\n\t" \
    "ds_read_b64_tr_b16 v[214:215], %[t2] offset:1664\n\t" \
    "ds_read_b64_tr_b16 v[44:45], %[t2] offset:128\n\t" \
    "ds_read_b64_tr_b16 v[46:47], %[t2] offset:1792\n\t" \
    "ds_read_b64_tr_b16 v[52:53], %[t2] offset:256\n\t" \
    "ds_read_b64_tr_b16 v[54:55], %[t2] offset:1920\n\t" \
    "ds_read_b64_tr_b16 v[60:61], %[t3] offset:0\n\t" \
    "ds_read_b64_tr_b16 v[62:63], %[t3] offset:1664\n\t" \
    "ds_read_b64_tr_b16 v[68:69], %[t3] offset:128\n\t" \
    "ds_read_b64_tr_b16 v[70:71], %[t3] offset:1792\n\t" \
    "ds_read_b64_tr_b16 v[76:77], %[t3] offset:256\n\t" \
    "ds_read_b64_tr_b16 v[78:79], %[t3] offset:1920\n\t" \
    "buffer_load_dwordx4 v[220:223], %[o0], %[rs], %[t] offen" WNT "\n\t" \
    "buffer_load_dwordx4 v[84:87], %[o1], %[rs], %[t] offen" WNT "\n\t" \
    "buffer_load_dwordx4 v[92:95], %[o2], %[rs], %[t] offen" WNT "\n\t" \
    "s_add_u32 %[t], %[t], 0x18000\n\t" \
    "buffer_load_dwordx4 v[100:103], %[o0], %[rs], %[t] offen" WNT "\n\t" \
    "buffer_load_dwordx4 v[108:111], %[o1], %[rs], %[t] offen" WNT "\n\t" \
    "buffer_load_dwordx4 v[116:119], %[o2], %[rs], %[t] offen" WNT "\n\t" \
    "s_add_u32 %[t], %[t], 0x18000\n\t" \
    "buffer_load_dwordx4 v[228:231], %[o0], %[rs], %[t] offen" WNT "\n\t" \
    "buffer_load_dwordx4 v[124:127], %[o1], %[rs], %[t] offen" WNT "\n\t" \
    "buffer_load_dwordx4 v[132:135], %[o2], %[rs], %[t] offen" WNT "\n\t" \
    "s_add_u32 %[t], %[t], 0x18000\n\t" \
    "buffer_load_dwordx4 v[140:143], %[o0], %[rs], %[t] offen" WNT "\n\t" \
    "buffer_load_dwordx4 v[148:151], %[o1], %[rs], %[t] offen" WNT "\n\t" \
    "buffer_load_dwordx4 v[156:159], %[o2], %[rs], %[t] offen" WNT "\n\t" \
    "s_add_u32 %[t], %[t], 0x18000\n\t" \
    "s_waitcnt lgkmcnt(0)\n\t" \
    "s_barrier \n\t" \
    "s_waitcnt vmcnt(21)\n\t" \
    "ds_write_b128 %[s0], v[216:219] offset:0\n\t" \
    "ds_write_b128 %[s1], v[80:83] offset:0\n\t" \
    "ds_write_b128 %[s2], v[88:91] offset:0\n\t" \
    "s_waitcnt vmcnt(18)\n\t" \
    "ds_write_b128 %[s0], v[96:99] offset:3456\n\t" \
    "ds_write_b128 %[s1], v[104:107] offset:3456\n\t" \
    "ds_write_b128 %[s2], v[112:115] offset:3456\n\t" \
    "s_waitcnt vmcnt(15)\n\t" \
    "ds_write_b128 %[s0], v[224:227] offset:6912\n\t" \
    "ds_write_b128 %[s1], v[120:123] offset:6912\n\t" \
    "ds_write_b128 %[s2], v[128:131] offset:6912\n\t" \
    "s_waitcnt vmcnt(12)\n\t" \
    "ds_write_b128 %[s0], v[136:139] offset:10368\n\t" \
    "ds_write_b128 %[s1], v[144:147] offset:10368\n\t" \
    "ds_write_b128 %[s2], v[152:155] offset:10368\n\t" \
    "s_waitcnt lgkmcnt(0)\n\t" \
    "s_barrier \n\t" \
    "ds_read_b64_tr_b16 v[216:217], %[t0] offset:0\n\t" \
    "ds_read_b64_tr_b16 v[218:219], %[t0] offset:1664\n\t" \
    "ds_read_b64_tr_b16 v[80:81], %[t0] offset:128\n\t" \
    "ds_read_b64_tr_b16 v[82:83], %[t0] offset:1792\n\t" \
    "ds_read_b64_tr_b16 v[88:89], %[t0] offset:256\n\t" \
    "ds_read_b64_tr_b16 v[90:91], %[t0] offset:1920\n\t" \
    "ds_read_b64_tr_b16 v[96:97], %[t1] offset:0\n\t" \
    "ds_read_b64_tr_b16 v[98:99], %[t1] offset:1664\n\t" \
    "ds_read_b64_tr_b16 v[104:105], %[t1] offset:128\n\t" \
    "ds_read_b64_tr_b16 v[106:107], %[t1] offset:1792\n\t" \
    "ds_read_b64_tr_b16 v[112:113], %[t1] offset:256\n\t" \
    "ds_read_b64_tr_b16 v[114:115], %[t1] offset:1920\n\t" \
    "ds_read_b64_tr_b16 v[224:225], %[t2] offset:0\n\t" \
    "ds_read_b64_tr_b16 v[226:227], %[t2] offset:1664\n\t" \
    "ds_read_b64_tr_b16 v[120:121], %[t2] offset:128\n\t" \
    "ds_read_b64_tr_b16 v[122:123], %[t2] offset:1792\n\t" \
    "ds_read_b64_tr_b16 v[128:129], %[t2] offset:256\n\t" \
    "ds_read_b64_tr_b16 v[130:131], %[t2] offset:1920\n\t" \
    "ds_read_b64_tr_b16 v[136:137], %[t3] offset:0\n\t" \
    "ds_read_b64_tr_b16 v[138:139], %[t3] offset:1664\n\t" \
    "ds_read_b64_tr_b16 v[144:145], %[t3] offset:128\n\t" \
    "ds_read_b64_tr_b16 v[146:147], %[t3] offset:1792\n\t" \
    "ds_read_b64_tr_b16 v[152:153], %[t3] offset:256\n\t" \
    "ds_read_b64_tr_b16 v[154:155], %[t3] offset:1920\n\t" \
    "s_and_b32 %[t], %[wx], 3\n\t" \
    "s_mul_i32 %[t], %[t], 0x60000\n\t" \
    "s_add_u32 %[t], %[t], 0x600000\n\t" \
    "buffer_load_dwordx4 v[232:235], %[o0], %[rs], %[t] offen" WNT "\n\t" \
    "buffer_load_dwordx4 v[160:163], %[o1], %[rs], %[t] offen" WNT "\n\t" \
    "buffer_load_dwordx4 v[168:171], %[o2], %[rs], %[t] offen" WNT "\n\t" \
    "s_add_u32 %[t], %[t], 0x18000\n\t" \
    "buffer_load_dwordx4 v[236:239], %[o0], %[rs], %[t] offen" WNT "\n\t" \
    "buffer_load_dwordx4 v[164:167], %[o1], %[rs], %[t] offen" WNT "\n\t" \
    "buffer_load_dwordx4 v[172:175], %[o2], %[rs], %[t] offen" WNT "\n\t" \
    "s_add_u32 %[t], %[t], 0x18000\n\t" \
    "buffer_load_dwordx4 v[176:179], %[o0], %[rs], %[t] offen" WNT "\n\t" \
    "buffer_load_dwordx4 v[184:187], %[o1], %[rs], %[t] offen" WNT "\n\t" \
    "buffer_load_dwordx4 v[192:195], %[o2], %[rs], %[t] offen" WNT "\n\t" \
    "s_add_u32 %[t], %[t], 0x18000\n\t" \
    "buffer_load_dwordx4 v[180:183], %[o0], %[rs], %[t] offen" WNT "\n\t" \
    "buffer_load_dwordx4 v[188:191], %[o1], %[rs], %[t] offen" WNT "\n\t" \
    "buffer_load_dwordx4 v[196:199], %[o2], %[rs], %[t] offen" WNT "\n\t" \
    "s_add_u32 %[t], %[t], 0x18000\n\t" \
    "s_waitcnt lgkmcnt(0)\n\t" \
    "s_barrier \n\t" \
    "s_waitcnt vmcnt(21)\n\t" \
    "ds_write_b128 %[s0], v[220:223] offset:0\n\t" \
    "ds_write_b128 %[s1], v[84:87] offset:0\n\t" \
    "ds_write_b128 %[s2], v[92:95] offset:0\n\t" \
    "s_waitcnt vmcnt(18)\n\t" \
    "ds_write_b128 %[s0], v[100:103] offset:3456\n\t" \
    "ds_write_b128 %[s1], v[108:111] offset:3456\n\t" \
    "ds_write_b128 %[s2], v[116:119] offset:3456\n\t" \
    "s_waitcnt vmcnt(15)\n\t" \
    "ds_write_b128 %[s0], v[228:231] offset:6912\n\t" \
    "ds_write_b128 %[s1], v[124:127] offset:6912\n\t" \
    "ds_write_b128 %[s2], v[132:135] offset:6912\n\t" \
    "s_waitcnt vmcnt(12)\n\t" \
    "ds_write_b128 %[s0], v[140:143] offset:10368\n\t" \
    "ds_write_b128 %[s1], v[148:151] offset:10368\n\t" \
    "ds_write_b128 %[s2], v[156:159] offset:10368\n\t" \
    "s_waitcnt lgkmcnt(0)\n\t" \
    "s_barrier \n\t" \
    "ds_read_b64_tr_b16 v[220:221], %[t0] offset:0\n\t" \
    "ds_read_b64_tr_b16 v[222:223], %[t0] offset:1664\n\t" \
    "ds_read_b64_tr_b16 v[84:85], %[t0] offset:128\n\t" \
    "ds_read_b64_tr_b16 v[86:87], %[t0] offset:1792\n\t" \
    "ds_read_b64_tr_b16 v[92:93], %[t0] offset:256\n\t" \
    "ds_read_b64_tr_b16 v[94:95], %[t0] offset:1920\n\t" \
    "ds_read_b64_tr_b16 v[100:101], %[t1] offset:0\n\t" \
    "ds_read_b64_tr_b16 v[102:103], %[t1] offset:1664\n\t" \
    "ds_read_b64_tr_b16 v[108:109], %[t1] offset:128\n\t" \
    "ds_read_b64_tr_b16 v[110:111], %[t1] offset:1792\n\t" \
    "ds_read_b64_tr_b16 v[116:117], %[t1] offset:256\n\t" \
    "ds_read_b64_tr_b16 v[118:119], %[t1] offset:1920\n\t" \
    "ds_read_b64_tr_b16 v[228:229], %[t2] offset:0\n\t" \
    "ds_read_b64_tr_b16 v[230:231], %[t2] offset:1664\n\t" \
    "ds_read_b64_tr_b16 v[124:125], %[t2] offset:128\n\t" \
    "ds_read_b64_tr_b16 v[126:127], %[t2] offset:1792\n\t" \
    "ds_read_b64_tr_b16 v[132:133], %[t2] offset:256\n\t" \
    "ds_read_b64_tr_b16 v[134:135], %[t2] offset:1920\n\t" \
    "ds_read_b64_tr_b16 v[140:141], %[t3] offset:0\n\t" \
    "ds_read_b64_tr_b16 v[142:143], %[t3] offset:1664\n\t" \
    "ds_read_b64_tr_b16 v[148:149], %[t3] offset:128\n\t" \
    "ds_read_b64_tr_b16 v[150:151], %[t3] offset:1792\n\t" \
    "ds_read_b64_tr_b16 v[156:157], %[t3] offset:256\n\t" \
    "ds_read_b64_tr_b16 v[158:159], %[t3] offset:1920\n\t" \
    "s_waitcnt lgkmcnt(0)\n\t" \
    "s_barrier \n\t" \
    "s_waitcnt vmcnt(9)\n\t" \
    "ds_write_b128 %[s0], v[232:235] offset:0\n\t" \
    "ds_write_b128 %[s1], v[160:163] offset:0\n\t" \
    "ds_write_b128 %[s2], v[168:171] offset:0\n\t" \
    "s_waitcnt vmcnt(6)\n\t" \
    "ds_write_b128 %[s0], v[236:239] offset:3456\n\t" \
    "ds_write_b128 %[s1], v[164:167] offset:3456\n\t" \
    "ds_write_b128 %[s2], v[172:175] offset:3456\n\t" \
    "s_waitcnt vmcnt(3)\n\t" \
    "ds_write_b128 %[s0], v[176:179] offset:6912\n\t" \
    "ds_write_b128 %[s1], v[184:187] offset:6912\n\t" \
    "ds_write_b128 %[s2], v[192:195] offset:6912\n\t" \
    "s_waitcnt vmcnt(0)\n\t" \
    "ds_write_b128 %[s0], v[180:183] offset:10368\n\t" \
    "ds_write_b128 %[s1], v[188:191] offset:10368\n\t" \
    "ds_write_b128 %[s2], v[196:199] offset:10368\n\t" \
    "s_waitcnt lgkmcnt(0)\n\t" \
    "s_barrier \n\t" \
    "ds_read_b64_tr_b16 v[232:233], %[t4] offset:0\n\t" \
    "ds_read_b64_tr_b16 v[234:235], %[t4] offset:1664\n\t" \
    "ds_read_b64_tr_b16 v[160:161], %[t4] offset:128\n\t" \
    "ds_read_b64_tr_b16 v[162:163], %[t4] offset:1792\n\t" \
    "ds_read_b64_tr_b16 v[168:169], %[t4] offset:256\n\t" \
    "ds_read_b64_tr_b16 v[170:171], %[t4] offset:1920\n\t" \
    "ds_read_b64_tr_b16 v[236:237], %[t4] offset:13824\n\t" \
    "ds_read_b64_tr_b16 v[238:239], %[t4] offset:15488\n\t" \
    "ds_read_b64_tr_b16 v[164:165], %[t4] offset:13952\n\t" \
    "ds_read_b64_tr_b16 v[166:167], %[t4] offset:15616\n\t" \
    "ds_read_b64_tr_b16 v[172:173], %[t4] offset:14080\n\t" \
    "ds_read_b64_tr_b16 v[174:175], %[t4] offset:15744\n\t" \
    "ds_read_b64_tr_b16 v[176:177], %[t4] offset:27648\n\t" \
    "ds_read_b64_tr_b16 v[178:179], %[t4] offset:29312\n\t" \
    "ds_read_b64_tr_b16 v[184:185], %[t4] offset:27776\n\t" \
    "ds_read_b64_tr_b16 v[186:187], %[t4] offset:29440\n\t" \
    "ds_read_b64_tr_b16 v[192:193], %[t4] offset:27904\n\t" \
    "ds_read_b64_tr_b16 v[194:195], %[t4] offset:29568\n\t" \
    "ds_read_b64_tr_b16 v[180:181], %[t4] offset:41472\n\t" \
    "ds_read_b64_tr_b16 v[182:183], %[t4] offset:43136\n\t" \
    "ds_read_b64_tr_b16 v[188:189], %[t4] offset:41600\n\t" \
    "ds_read_b64_tr_b16 v[190:191], %[t4] offset:43264\n\t" \
    "ds_read_b64_tr_b16 v[196:197], %[t4] offset:41728\n\t" \
    "ds_read_b64_tr_b16 v[198:199], %[t4] offset:43392\n\t" \
    "s_waitcnt lgkmcnt(0)\n\t" \
    "s_barrier \n\t" \
    "ds_write_b128 %[wl], v[200:203] offset:0\n\t" \
    "ds_write_b128 %[wl], v[204:207] offset:1024\n\t" \
    "ds_write_b128 %[wl], v[208:211] offset:2048\n\t" \
    "ds_write_b128 %[wl], v[212:215] offset:3072\n\t" \
    "ds_write_b128 %[wl], v[216:219] offset:4096\n\t" \
    "ds_write_b128 %[wl], v[220:223] offset:5120\n\t" \
    "ds_write_b128 %[wl], v[224:227] offset:6144\n\t" \
    "ds_write_b128 %[wl], v[228:231] offset:7168\n\t" \
    "ds_write_b128 %[wl], v[232:235] offset:8192\n\t" \
    "ds_write_b128 %[wl], v[236:239] offset:9216\n\t" \
    "s_waitcnt lgkmcnt(0)\n\t"
#define ASM_WLOADO \
    "s_mov_b32 %[t], 0x0\n\t" \
    "buffer_load_dwordx4 v[200:203], %[o0], %[rs], %[t] offen" WNT "\n\t" \
    "buffer_load_dwordx4 v[16:19], %[o1], %[rs], %[t] offen" WNT "\n\t" \
    "buffer_load_dwordx4 v[208:211], %[o2], %[rs], %[t] offen" WNT "\n\t" \
    "buffer_load_dwordx4 v[56:59], %[o3], %[rs], %[t] offen" WNT "\n\t" \
    "buffer_load_dwordx4 v[216:219], %[o4], %[rs], %[t] offen" WNT "\n\t" \
    "s_mov_b32 %[t], 0xa000\n\t" \
    "buffer_load_dwordx4 v[96:99], %[o0], %[rs], %[t] offen" WNT "\n\t" \
    "buffer_load_dwordx4 v[224:227], %[o1], %[rs], %[t] offen" WNT "\n\t" \
    "buffer_load_dwordx4 v[136:139], %[o2], %[rs], %[t] offen" WNT "\n\t" \
    "buffer_load_dwordx4 v[232:235], %[o3], %[rs], %[t] offen" WNT "\n\t" \
    "buffer_load_dwordx4 v[176:179], %[o4], %[rs], %[t] offen" WNT "\n\t" \
    "s_mov_b32 %[t], 0x14000\n\t" \
    "buffer_load_dwordx4 v[204:207], %[o0], %[rs], %[t] offen" WNT "\n\t" \
    "buffer_load_dwordx4 v[20:23], %[o1], %[rs], %[t] offen" WNT "\n\t" \
    "buffer_load_dwordx4 v[212:215], %[o2], %[rs], %[t] offen" WNT "\n\t" \
    "buffer_load_dwordx4 v[60:63], %[o3], %[rs], %[t] offen" WNT "\n\t" \
    "buffer_load_dwordx4 v[220:223], %[o4], %[rs], %[t] offen" WNT "\n\t" \
    "s_mov_b32 %[t], 0x1e000\n\t" \
    "buffer_load_dwordx4 v[100:103], %[o0], %[rs], %[t] offen" WNT "\n\t" \
    "buffer_load_dwordx4 v[228:231], %[o1], %[rs], %[t] offen" WNT "\n\t" \
    "buffer_load_dwordx4 v[140:143], %[o2], %[rs], %[t] offen" WNT "\n\t" \
    "buffer_load_dwordx4 v[236:239], %[o3], %[rs], %[t] offen" WNT "\n\t" \
    "buffer_load_dwordx4 v[180:183], %[o4], %[rs], %[t] offen" WNT "\n\t" \
    "s_waitcnt vmcnt(15)\n\t" \
    "ds_write_b128 %[s0], v[200:203] offset:0\n\t" \
    "ds_write_b128 %[s1], v[16:19] offset:0\n\t" \
    "ds_write_b128 %[s2], v[208:211] offset:0\n\t" \
    "ds_write_b128 %[s3], v[56:59] offset:0\n\t" \
    "ds_write_b128 %[s4], v[216:219] offset:0\n\t" \
    "s_waitcnt vmcnt(10)\n\t" \
    "ds_write_b128 %[s0], v[96:99] offset:5632\n\t" \
    "ds_write_b128 %[s1], v[224:227] offset:5632\n\t" \
    "ds_write_b128 %[s2], v[136:139] offset:5632\n\t" \
    "ds_write_b128 %[s3], v[232:235] offset:5632\n\t" \
    "ds_write_b128 %[s4], v[176:179] offset:5632\n\t" \
    "ds_read_b64_tr_b16 v[200:201], %[tr] offset:0\n\t" \
    "ds_read_b64_tr_b16 v[202:203], %[tr] offset:5632\n\t" \
    "ds_read_b64_tr_b16 v[16:17], %[tr] offset:32\n\t" \
    "ds_read_b64_tr_b16 v[18:19], %[tr] offset:5664\n\t" \
    "ds_read_b64_tr_b16 v[208:209], %[tr] offset:64\n\t" \
    "ds_read_b64_tr_b16 v[210:211], %[tr] offset:5696\n\t" \
    "ds_read_b64_tr_b16 v[56:57], %[tr] offset:96\n\t" \
    "ds_read_b64_tr_b16 v[58:59], %[tr] offset:5728\n\t" \
    "ds_read_b64_tr_b16 v[216:217], %[tr] offset:128\n\t" \
    "ds_read_b64_tr_b16 v[218:219], %[tr] offset:5760\n\t" \
    "ds_read_b64_tr_b16 v[96:97], %[tr] offset:160\n\t" \
    "ds_read_b64_tr_b16 v[98:99], %[tr] offset:5792\n\t" \
    "ds_read_b64_tr_b16 v[224:225], %[tr] offset:192\n\t" \
    "ds_read_b64_tr_b16 v[226:227], %[tr] offset:5824\n\t" \
    "ds_read_b64_tr_b16 v[136:137], %[tr] offset:224\n\t" \
    "ds_read_b64_tr_b16 v[138:139], %[tr] offset:5856\n\t" \
    "ds_read_b64_tr_b16 v[232:233], %[tr] offset:256\n\t" \
    "ds_read_b64_tr_b16 v[234:235], %[tr] offset:5888\n\t" \
    "ds_read_b64_tr_b16 v[176:177], %[tr] offset:288\n\t" \
    "ds_read_b64_tr_b16 v[178:179], %[tr] offset:5920\n\t" \
    "s_barrier \n\t" \
    "s_mov_b32 %[t], 0x28000\n\t" \
    "buffer_load_dwordx4 v[0:3], %[o0], %[rs], %[t] offen" WNT "\n\t" \
    "buffer_load_dwordx4 v[24:27], %[o1], %[rs], %[t] offen" WNT "\n\t" \
    "buffer_load_dwordx4 v[40:43], %[o2], %[rs], %[t] offen" WNT "\n\t" \
    "buffer_load_dwordx4 v[64:67], %[o3], %[rs], %[t] offen" WNT "\n\t" \
    "buffer_load_dwordx4 v[80:83], %[o4], %[rs], %[t] offen" WNT "\n\t" \
    "s_mov_b32 %[t], 0x32000\n\t" \
    "buffer_load_dwordx4 v[104:107], %[o0], %[rs], %[t] offen" WNT "\n\t" \
    "buffer_load_dwordx4 v[120:123], %[o1], %[rs], %[t] offen" WNT "\n\t" \
    "buffer_load_dwordx4 v[144:147], %[o2], %[rs], %[t] offen" WNT "\n\t" \
    "buffer_load_dwordx4 v[160:163], %[o3], %[rs], %[t] offen" WNT "\n\t" \
    "buffer_load_dwordx4 v[184:187], %[o4], %[rs], %[t] offen" WNT "\n\t" \
    "s_waitcnt lgkmcnt(0)\n\t" \
    "s_barrier \n\t" \
    "s_waitcnt vmcnt(15)\n\t" \
    "ds_write_b128 %[s0], v[204:207] offset:0\n\t" \
    "ds_write_b128 %[s1], v[20:23] offset:0\n\t" \
    "ds_write_b128 %[s2], v[212:215] offset:0\n\t" \
    "ds_write_b128 %[s3], v[60:63] offset:0\n\t" \
    "ds_write_b128 %[s4], v[220:223] offset:0\n\t" \
    "s_waitcnt vmcnt(10)\n\t" \
    "ds_write_b128 %[s0], v[100:103] offset:5632\n\t" \
    "ds_write_b128 %[s1], v[228:231] offset:5632\n\t" \
    "ds_write_b128 %[s2], v[140:143] offset:5632\n\t" \
    "ds_write_b128 %[s3], v[236:239] offset:5632\n\t" \
    "ds_write_b128 %[s4], v[180:183] offset:5632\n\t" \
    "ds_read_b64_tr_b16 v[204:205], %[tr] offset:0\n\t" \
    "ds_read_b64_tr_b16 v[206:207], %[tr] offset:5632\n\t" \
    "ds_read_b64_tr_b16 v[20:21], %[tr] offset:32\n\t" \
    "ds_read_b64_tr_b16 v[22:23], %[tr] offset:5664\n\t" \
    "ds_read_b64_tr_b16 v[212:213], %[tr] offset:64\n\t" \
    "ds_read_b64_tr_b16 v[214:215], %[tr] offset:5696\n\t" \
    "ds_read_b64_tr_b16 v[60:61], %[tr] offset:96\n\t" \
    "ds_read_b64_tr_b16 v[62:63], %[tr] offset:5728\n\t" \
    "ds_read_b64_tr_b16 v[220:221], %[tr] offset:128\n\t" \
    "ds_read_b64_tr_b16 v[222:223], %[tr] offset:5760\n\t" \
    "ds_read_b64_tr_b16 v[100:101], %[tr] offset:160\n\t" \
    "ds_read_b64_tr_b16 v[102:103], %[tr] offset:5792\n\t" \
    "ds_read_b64_tr_b16 v[228:229], %[tr] offset:192\n\t" \
    "ds_read_b64_tr_b16 v[230:231], %[tr] offset:5824\n\t" \
    "ds_read_b64_tr_b16 v[140:141], %[tr] offset:224\n\t" \
    "ds_read_b64_tr_b16 v[142:143], %[tr] offset:5856\n\t" \
    "ds_read_b64_tr_b16 v[236:237], %[tr] offset:256\n\t" \
    "ds_read_b64_tr_b16 v[238:239], %[tr] offset:5888\n\t" \
    "ds_read_b64_tr_b16 v[180:181], %[tr] offset:288\n\t" \
    "ds_read_b64_tr_b16 v[182:183], %[tr] offset:5920\n\t" \
    "s_barrier \n\t" \
    "s_mov_b32 %[t], 0x5000\n\t" \
    "buffer_load_dwordx4 v[4:7], %[o0], %[rs], %[t] offen" WNT "\n\t" \
    "buffer_load_dwordx4 v[28:31], %[o1], %[rs], %[t] offen" WNT "\n\t" \
    "buffer_load_dwordx4 v[44:47], %[o2], %[rs], %[t] offen" WNT "\n\t" \
    "buffer_load_dwordx4 v[68:71], %[o3], %[rs], %[t] offen" WNT "\n\t" \
    "buffer_load_dwordx4 v[84:87], %[o4], %[rs], %[t] offen" WNT "\n\t" \
    "s_mov_b32 %[t], 0xf000\n\t" \
    "buffer_load_dwordx4 v[108:111], %[o0], %[rs], %[t] offen" WNT "\n\t" \
    "buffer_load_dwordx4 v[124:127], %[o1], %[rs], %[t] offen" WNT "\n\t" \
    "buffer_load_dwordx4 v[148:151], %[o2], %[rs], %[t] offen" WNT "\n\t" \
    "buffer_load_dwordx4 v[164:167], %[o3], %[rs], %[t] offen" WNT "\n\t" \
    "buffer_load_dwordx4 v[188:191], %[o4], %[rs], %[t] offen" WNT "\n\t" \
    "s_waitcnt lgkmcnt(0)\n\t" \
    "s_barrier \n\t" \
    "s_waitcnt vmcnt(15)\n\t" \
    "ds_write_b128 %[s0], v[0:3] offset:0\n\t" \
    "ds_write_b128 %[s1], v[24:27] offset:0\n\t" \
    "ds_write_b128 %[s2], v[40:43] offset:0\n\t" \
    "ds_write_b128 %[s3], v[64:67] offset:0\n\t" \
    "ds_write_b128 %[s4], v[80:83] offset:0\n\t" \
    "s_waitcnt vmcnt(10)\n\t" \
    "ds_write_b128 %[s0], v[104:107] offset:5632\n\t" \
    "ds_write_b128 %[s1], v[120:123] offset:5632\n\t" \
    "ds_write_b128 %[s2], v[144:147] offset:5632\n\t" \
    "ds_write_b128 %[s3], v[160:163] offset:5632\n\t" \
    "ds_write_b128 %[s4], v[184:187] offset:5632\n\t" \
    "ds_read_b64_tr_b16 v[0:1], %[tr] offset:0\n\t" \
    "ds_read_b64_tr_b16 v[2:3], %[tr] offset:5632\n\t" \
    "ds_read_b64_tr_b16 v[24:25], %[tr] offset:32\n\t" \
    "ds_read_b64_tr_b16 v[26:27], %[tr] offset:5664\n\t" \
    "ds_read_b64_tr_b16 v[40:41], %[tr] offset:64\n\t" \
    "ds_read_b64_tr_b16 v[42:43], %[tr] offset:5696\n\t" \
    "ds_read_b64_tr_b16 v[64:65], %[tr] offset:96\n\t" \
    "ds_read_b64_tr_b16 v[66:67], %[tr] offset:5728\n\t" \
    "ds_read_b64_tr_b16 v[80:81], %[tr] offset:128\n\t" \
    "ds_read_b64_tr_b16 v[82:83], %[tr] offset:5760\n\t" \
    "ds_read_b64_tr_b16 v[104:105], %[tr] offset:160\n\t" \
    "ds_read_b64_tr_b16 v[106:107], %[tr] offset:5792\n\t" \
    "ds_read_b64_tr_b16 v[120:121], %[tr] offset:192\n\t" \
    "ds_read_b64_tr_b16 v[122:123], %[tr] offset:5824\n\t" \
    "ds_read_b64_tr_b16 v[144:145], %[tr] offset:224\n\t" \
    "ds_read_b64_tr_b16 v[146:147], %[tr] offset:5856\n\t" \
    "ds_read_b64_tr_b16 v[160:161], %[tr] offset:256\n\t" \
    "ds_read_b64_tr_b16 v[162:163], %[tr] offset:5888\n\t" \
    "ds_read_b64_tr_b16 v[184:185], %[tr] offset:288\n\t" \
    "ds_read_b64_tr_b16 v[186:187], %[tr] offset:5920\n\t" \
    "s_barrier \n\t" \
    "s_mov_b32 %[t], 0x19000\n\t" \
    "buffer_load_dwordx4 v[8:11], %[o0], %[rs], %[t] offen" WNT "\n\t" \
    "buffer_load_dwordx4 v[32:35], %[o1], %[rs], %[t] offen" WNT "\n\t" \
    "buffer_load_dwordx4 v[48:51], %[o2], %[rs], %[t] offen" WNT "\n\t" \
    "buffer_load_dwordx4 v[72:75], %[o3], %[rs], %[t] offen" WNT "\n\t" \
    "buffer_load_dwordx4 v[88:91], %[o4], %[rs], %[t] offen" WNT "\n\t" \
    "s_mov_b32 %[t], 0x23000\n\t" \
    "buffer_load_dwordx4 v[112:115], %[o0], %[rs], %[t] offen" WNT "\n\t" \
    "buffer_load_dwordx4 v[128:131], %[o1], %[rs], %[t] offen" WNT "\n\t" \
    "buffer_load_dwordx4 v[152:155], %[o2], %[rs], %[t] offen" WNT "\n\t" \
    "buffer_load_dwordx4 v[168:171], %[o3], %[rs], %[t] offen" WNT "\n\t" \
    "buffer_load_dwordx4 v[192:195], %[o4], %[rs], %[t] offen" WNT "\n\t" \
    "s_waitcnt lgkmcnt(0)\n\t" \
    "s_barrier \n\t" \
    "s_waitcnt vmcnt(15)\n\t" \
    "ds_write_b128 %[s0], v[4:7] offset:0\n\t" \
    "ds_write_b128 %[s1], v[28:31] offset:0\n\t" \
    "ds_write_b128 %[s2], v[44:47] offset:0\n\t" \
    "ds_write_b128 %[s3], v[68:71] offset:0\n\t" \
    "ds_write_b128 %[s4], v[84:87] offset:0\n\t" \
    "s_waitcnt vmcnt(10)\n\t" \
    "ds_write_b128 %[s0], v[108:111] offset:5632\n\t" \
    "ds_write_b128 %[s1], v[124:127] offset:5632\n\t" \
    "ds_write_b128 %[s2], v[148:151] offset:5632\n\t" \
    "ds_write_b128 %[s3], v[164:167] offset:5632\n\t" \
    "ds_write_b128 %[s4], v[188:191] offset:5632\n\t" \
    "ds_read_b64_tr_b16 v[4:5], %[tr] offset:0\n\t" \
    "ds_read_b64_tr_b16 v[6:7], %[tr] offset:5632\n\t" \
    "ds_read_b64_tr_b16 v[28:29], %[tr] offset:32\n\t" \
    "ds_read_b64_tr_b16 v[30:31], %[tr] offset:5664\n\t" \
    "ds_read_b64_tr_b16 v[44:45], %[tr] offset:64\n\t" \
    "ds_read_b64_tr_b16 v[46:47], %[tr] offset:5696\n\t" \
    "ds_read_b64_tr_b16 v[68:69], %[tr] offset:96\n\t" \
    "ds_read_b64_tr_b16 v[70:71], %[tr] offset:5728\n\t" \
    "ds_read_b64_tr_b16 v[84:85], %[tr] offset:128\n\t" \
    "ds_read_b64_tr_b16 v[86:87], %[tr] offset:5760\n\t" \
    "ds_read_b64_tr_b16 v[108:109], %[tr] offset:160\n\t" \
    "ds_read_b64_tr_b16 v[110:111], %[tr] offset:5792\n\t" \
    "ds_read_b64_tr_b16 v[124:125], %[tr] offset:192\n\t" \
    "ds_read_b64_tr_b16 v[126:127], %[tr] offset:5824\n\t" \
    "ds_read_b64_tr_b16 v[148:149], %[tr] offset:224\n\t" \
    "ds_read_b64_tr_b16 v[150:151], %[tr] offset:5856\n\t" \
    "ds_read_b64_tr_b16 v[164:165], %[tr] offset:256\n\t" \
    "ds_read_b64_tr_b16 v[166:167], %[tr] offset:5888\n\t" \
    "ds_read_b64_tr_b16 v[188:189], %[tr] offset:288\n\t" \
    "ds_read_b64_tr_b16 v[190:191], %[tr] offset:5920\n\t" \
    "s_barrier \n\t" \
    "s_mov_b32 %[t], 0x2d000\n\t" \
    "buffer_load_dwordx4 v[12:15], %[o0], %[rs], %[t] offen" WNT "\n\t" \
    "buffer_load_dwordx4 v[36:39], %[o1], %[rs], %[t] offen" WNT "\n\t" \
    "buffer_load_dwordx4 v[52:55], %[o2], %[rs], %[t] offen" WNT "\n\t" \
    "buffer_load_dwordx4 v[76:79], %[o3], %[rs], %[t] offen" WNT "\n\t" \
    "buffer_load_dwordx4 v[92:95], %[o4], %[rs], %[t] offen" WNT "\n\t" \
    "s_mov_b32 %[t], 0x37000\n\t" \
    "buffer_load_dwordx4 v[116:119], %[o0], %[rs], %[t] offen" WNT "\n\t" \
    "buffer_load_dwordx4 v[132:135], %[o1], %[rs], %[t] offen" WNT "\n\t" \
    "buffer_load_dwordx4 v[156:159], %[o2], %[rs], %[t] offen" WNT "\n\t" \
    "buffer_load_dwordx4 v[172:175], %[o3], %[rs], %[t] offen" WNT "\n\t" \
    "buffer_load_dwordx4 v[196:199], %[o4], %[rs], %[t] offen" WNT "\n\t" \
    "s_waitcnt lgkmcnt(0)\n\t" \
    "s_barrier \n\t" \
    "s_waitcnt vmcnt(15)\n\t" \
    "ds_write_b128 %[s0], v[8:11] offset:0\n\t" \
    "ds_write_b128 %[s1], v[32:35] offset:0\n\t" \
    "ds_write_b128 %[s2], v[48:51] offset:0\n\t" \
    "ds_write_b128 %[s3], v[72:75] offset:0\n\t" \
    "ds_write_b128 %[s4], v[88:91] offset:0\n\t" \
    "s_waitcnt vmcnt(10)\n\t" \
    "ds_write_b128 %[s0], v[112:115] offset:5632\n\t" \
    "ds_write_b128 %[s1], v[128:131] offset:5632\n\t" \
    "ds_write_b128 %[s2], v[152:155] offset:5632\n\t" \
    "ds_write_b128 %[s3], v[168:171] offset:5632\n\t" \
    "ds_write_b128 %[s4], v[192:195] offset:5632\n\t" \
    "ds_read_b64_tr_b16 v[8:9], %[tr] offset:0\n\t" \
    "ds_read_b64_tr_b16 v[10:11], %[tr] offset:5632\n\t" \
    "ds_read_b64_tr_b16 v[32:33], %[tr] offset:32\n\t" \
    "ds_read_b64_tr_b16 v[34:35], %[tr] offset:5664\n\t" \
    "ds_read_b64_tr_b16 v[48:49], %[tr] offset:64\n\t" \
    "ds_read_b64_tr_b16 v[50:51], %[tr] offset:5696\n\t" \
    "ds_read_b64_tr_b16 v[72:73], %[tr] offset:96\n\t" \
    "ds_read_b64_tr_b16 v[74:75], %[tr] offset:5728\n\t" \
    "ds_read_b64_tr_b16 v[88:89], %[tr] offset:128\n\t" \
    "ds_read_b64_tr_b16 v[90:91], %[tr] offset:5760\n\t" \
    "ds_read_b64_tr_b16 v[112:113], %[tr] offset:160\n\t" \
    "ds_read_b64_tr_b16 v[114:115], %[tr] offset:5792\n\t" \
    "ds_read_b64_tr_b16 v[128:129], %[tr] offset:192\n\t" \
    "ds_read_b64_tr_b16 v[130:131], %[tr] offset:5824\n\t" \
    "ds_read_b64_tr_b16 v[152:153], %[tr] offset:224\n\t" \
    "ds_read_b64_tr_b16 v[154:155], %[tr] offset:5856\n\t" \
    "ds_read_b64_tr_b16 v[168:169], %[tr] offset:256\n\t" \
    "ds_read_b64_tr_b16 v[170:171], %[tr] offset:5888\n\t" \
    "ds_read_b64_tr_b16 v[192:193], %[tr] offset:288\n\t" \
    "ds_read_b64_tr_b16 v[194:195], %[tr] offset:5920\n\t" \
    "s_waitcnt lgkmcnt(0)\n\t" \
    "s_barrier \n\t" \
    "s_waitcnt vmcnt(5)\n\t" \
    "ds_write_b128 %[s0], v[12:15] offset:0\n\t" \
    "ds_write_b128 %[s1], v[36:39] offset:0\n\t" \
    "ds_write_b128 %[s2], v[52:55] offset:0\n\t" \
    "ds_write_b128 %[s3], v[76:79] offset:0\n\t" \
    "ds_write_b128 %[s4], v[92:95] offset:0\n\t" \
    "s_waitcnt vmcnt(0)\n\t" \
    "ds_write_b128 %[s0], v[116:119] offset:5632\n\t" \
    "ds_write_b128 %[s1], v[132:135] offset:5632\n\t" \
    "ds_write_b128 %[s2], v[156:159] offset:5632\n\t" \
    "ds_write_b128 %[s3], v[172:175] offset:5632\n\t" \
    "ds_write_b128 %[s4], v[196:199] offset:5632\n\t" \
    "ds_read_b64_tr_b16 v[12:13], %[tr] offset:0\n\t" \
    "ds_read_b64_tr_b16 v[14:15], %[tr] offset:5632\n\t" \
    "ds_read_b64_tr_b16 v[36:37], %[tr] offset:32\n\t" \
    "ds_read_b64_tr_b16 v[38:39], %[tr] offset:5664\n\t" \
    "ds_read_b64_tr_b16 v[52:53], %[tr] offset:64\n\t" \
    "ds_read_b64_tr_b16 v[54:55], %[tr] offset:5696\n\t" \
    "ds_read_b64_tr_b16 v[76:77], %[tr] offset:96\n\t" \
    "ds_read_b64_tr_b16 v[78:79], %[tr] offset:5728\n\t" \
    "ds_read_b64_tr_b16 v[92:93], %[tr] offset:128\n\t" \
    "ds_read_b64_tr_b16 v[94:95], %[tr] offset:5760\n\t" \
    "ds_read_b64_tr_b16 v[116:117], %[tr] offset:160\n\t" \
    "ds_read_b64_tr_b16 v[118:119], %[tr] offset:5792\n\t" \
    "ds_read_b64_tr_b16 v[132:133], %[tr] offset:192\n\t" \
    "ds_read_b64_tr_b16 v[134:135], %[tr] offset:5824\n\t" \
    "ds_read_b64_tr_b16 v[156:157], %[tr] offset:224\n\t" \
    "ds_read_b64_tr_b16 v[158:159], %[tr] offset:5856\n\t" \
    "ds_read_b64_tr_b16 v[172:173], %[tr] offset:256\n\t" \
    "ds_read_b64_tr_b16 v[174:175], %[tr] offset:5888\n\t" \
    "ds_read_b64_tr_b16 v[196:197], %[tr] offset:288\n\t" \
    "ds_read_b64_tr_b16 v[198:199], %[tr] offset:5920\n\t" \
    "s_waitcnt lgkmcnt(0)\n\t" \
    "s_barrier \n\t" \
    "ds_write_b128 %[wl], v[200:203] offset:0\n\t" \
    "ds_write_b128 %[wl], v[204:207] offset:1024\n\t" \
    "ds_write_b128 %[wl], v[208:211] offset:2048\n\t" \
    "ds_write_b128 %[wl], v[212:215] offset:3072\n\t" \
    "ds_write_b128 %[wl], v[216:219] offset:4096\n\t" \
    "ds_write_b128 %[wl], v[220:223] offset:5120\n\t" \
    "ds_write_b128 %[wl], v[224:227] offset:6144\n\t" \
    "ds_write_b128 %[wl], v[228:231] offset:7168\n\t" \
    "ds_write_b128 %[wl], v[232:235] offset:8192\n\t" \
    "ds_write_b128 %[wl], v[236:239] offset:9216\n\t" \
    "s_waitcnt lgkmcnt(0)\n\t"
#define ASM_XG_S_LO \
    "s_waitcnt lgkmcnt(0)\n\t" \
    "ds_read_b128 v[224:227], %[xa] offset:16384\n\t" \
    "ds_read_b128 v[228:231], %[xa] offset:17408\n\t" \
    "ds_read_b128 v[244:247], %[wl] offset:0\n\t" \
    "ds_read_b128 v[248:251], %[wl] offset:1024\n\t" \
    "ds_read_b128 v[232:235], %[xa] offset:0\n\t" \
    "ds_read_b128 v[236:239], %[xa] offset:1024\n\t" \
    "v_xor_b32 v252, 0x800, %[xa]\n\t" \
    "ds_read_b128 v[240:243], v252 offset:0\n\t" \
    "s_waitcnt lgkmcnt(6)\n\t" \
    "v_mfma_f32_16x16x32_bf16 v[220:223], v[8:11], v[224:227], 0\n\t" \
    "s_waitcnt lgkmcnt(5)\n\t" \
    "v_mfma_f32_16x16x32_bf16 v[220:223], v[12:15], v[228:231], v[220:223]\n\t" \
    "v_mfma_f32_16x16x32_bf16 v[216:219], v[0:3], v[224:227], 0\n\t" \
    "v_mfma_f32_16x16x32_bf16 v[216:219], v[4:7], v[228:231], v[216:219]\n\t" \
    "s_waitcnt lgkmcnt(4)\n\t" \
    "v_mfma_f32_16x16x32_bf16 v[212:215], v[244:247], v[224:227], 0\n\t" \
    "ds_read_b128 v[224:227], v252 offset:1024\n\t" \
    "s_waitcnt lgkmcnt(4)\n\t" \
    "v_mfma_f32_16x16x32_bf16 v[212:215], v[248:251], v[228:231], v[212:215]\n\t" \
    "ds_read_b128 v[228:231], v252 offset:16384\n\t" \
    "s_waitcnt lgkmcnt(4)\n\t" \
    "v_mfma_f32_16x16x32_bf16 v[200:203], v[244:247], v[232:235], 0\n\t" \
    "ds_read_b128 v[244:247], %[wl] offset:2048\n\t" \
    "s_waitcnt lgkmcnt(4)\n\t" \
    "v_mfma_f32_16x16x32_bf16 v[200:203], v[248:251], v[236:239], v[200:203]\n\t" \
    "ds_read_b128 v[248:251], %[wl] offset:3072\n\t" \
    "v_mfma_f32_16x16x32_bf16 v[204:207], v[0:3], v[232:235], 0\n\t" \
    "v_mfma_f32_16x16x32_bf16 v[204:207], v[4:7], v[236:239], v[204:207]\n\t" \
    "v_mfma_f32_16x16x32_bf16 v[208:211], v[8:11], v[232:235], 0\n\t" \
    "ds_read_b128 v[232:235], v252 offset:17408\n\t" \
    "v_mfma_f32_16x16x32_bf16 v[208:211], v[12:15], v[236:239], v[208:211]\n\t" \
    "v_xor_b32 v252, 0x1000, %[xa]\n\t" \
    "ds_read_b128 v[236:239], v252 offset:16384\n\t" \
    "s_waitcnt lgkmcnt(6)\n\t" \
    "v_mfma_f32_16x16x32_bf16 v[208:211], v[32:35], v[240:243], v[208:211]\n\t" \
    "s_waitcnt lgkmcnt(5)\n\t" \
    "v_mfma_f32_16x16x32_bf16 v[208:211], v[36:39], v[224:227], v[208:211]\n\t" \
    "v_mfma_f32_16x16x32_bf16 v[204:207], v[24:27], v[240:243], v[204:207]\n\t" \
    "v_mfma_f32_16x16x32_bf16 v[204:207], v[28:31], v[224:227], v[204:207]\n\t" \
    "v_mfma_f32_16x16x32_bf16 v[200:203], v[16:19], v[240:243], v[200:203]\n\t" \
    "ds_read_b128 v[240:243], v252 offset:17408\n\t" \
    "v_mfma_f32_16x16x32_bf16 v[200:203], v[20:23], v[224:227], v[200:203]\n\t" \
    "ds_read_b128 v[224:227], v252 offset:0\n\t" \
    "s_waitcnt lgkmcnt(6)\n\t" \
    "v_mfma_f32_16x16x32_bf16 v[212:215], v[16:19], v[228:231], v[212:215]\n\t" \
    "s_waitcnt lgkmcnt(3)\n\t" \
    "v_mfma_f32_16x16x32_bf16 v[212:215], v[20:23], v[232:235], v[212:215]\n\t" \
    "v_mfma_f32_16x16x32_bf16 v[216:219], v[24:27], v[228:231], v[216:219]\n\t" \
    "v_mfma_f32_16x16x32_bf16 v[216:219], v[28:31], v[232:235], v[216:219]\n\t" \
    "v_mfma_f32_16x16x32_bf16 v[220:223], v[32:35], v[228:231], v[220:223]\n\t" \
    "ds_read_b128 v[228:231], v252 offset:1024\n\t" \
    "v_mfma_f32_16x16x32_bf16 v[220:223], v[36:39], v[232:235], v[220:223]\n\t" \
    "v_xor_b32 v252, 0x1800, %[xa]\n\t" \
    "ds_read_b128 v[232:235], v252 offset:0\n\t" \
    "s_waitcnt lgkmcnt(4)\n\t" \
    "v_mfma_f32_16x16x32_bf16 v[220:223], v[48:51], v[236:239], v[220:223]\n\t" \
    "s_waitcnt lgkmcnt(3)\n\t" \
    "v_mfma_f32_16x16x32_bf16 v[220:223], v[52:55], v[240:243], v[220:223]\n\t" \
    "v_mfma_f32_16x16x32_bf16 v[216:219], v[40:43], v[236:239], v[216:219]\n\t" \
    "v_mfma_f32_16x16x32_bf16 v[216:219], v[44:47], v[240:243], v[216:219]\n\t" \
    "v_mfma_f32_16x16x32_bf16 v[212:215], v[244:247], v[236:239], v[212:215]\n\t" \
    "ds_read_b128 v[236:239], v252 offset:1024\n\t" \
    "v_mfma_f32_16x16x32_bf16 v[212:215], v[248:251], v[240:243], v[212:215]\n\t" \
    "ds_read_b128 v[240:243], v252 offset:16384\n\t" \
    "s_waitcnt lgkmcnt(4)\n\t" \
    "v_mfma_f32_16x16x32_bf16 v[200:203], v[244:247], v[224:227], v[200:203]\n\t" \
    "ds_read_b128 v[244:247], %[wl] offset:8192\n\t" \
    "s_waitcnt lgkmcnt(4)\n\t" \
    "v_mfma_f32_16x16x32_bf16 v[200:203], v[248:251], v[228:231], v[200:203]\n\t" \
    "ds_read_b128 v[248:251], %[wl] offset:9216\n\t" \
    "v_mfma_f32_16x16x32_bf16 v[204:207], v[40:43], v[224:227], v[204:207]\n\t" \
    "v_mfma_f32_16x16x32_bf16 v[204:207], v[44:47], v[228:231], v[204:207]\n\t" \
    "v_mfma_f32_16x16x32_bf16 v[208:211], v[48:51], v[224:227], v[208:211]\n\t" \
    "ds_read_b128 v[224:227], v252 offset:17408\n\t" \
    "v_mfma_f32_16x16x32_bf16 v[208:211], v[52:55], v[228:231], v[208:211]\n\t" \
    "v_and_b32 v252, 0x3ff, %[xa]\n\t" \
    "ds_read_b128 v[228:231], v252 offset:45056\n\t" \
    "s_waitcnt lgkmcnt(6)\n\t" \
    "v_mfma_f32_16x16x32_bf16 v[208:211], v[72:75], v[232:235], v[208:211]\n\t" \
    "s_waitcnt lgkmcnt(5)\n\t" \
    "v_mfma_f32_16x16x32_bf16 v[208:211], v[76:79], v[236:239], v[208:211]\n\t" \
    "v_mfma_f32_16x16x32_bf16 v[204:207], v[64:67], v[232:235], v[204:207]\n\t" \
    "v_mfma_f32_16x16x32_bf16 v[204:207], v[68:71], v[236:239], v[204:207]\n\t" \
    "v_mfma_f32_16x16x32_bf16 v[200:203], v[56:59], v[232:235], v[200:203]\n\t" \
    "ds_read_b128 v[232:235], v252 offset:46080\n\t" \
    "v_mfma_f32_16x16x32_bf16 v[200:203], v[60:63], v[236:239], v[200:203]\n\t" \
    "ds_read_b128 v[236:239], v252 offset:40960\n\t" \
    "s_waitcnt lgkmcnt(6)\n\t" \
    "v_mfma_f32_16x16x32_bf16 v[212:215], v[56:59], v[240:243], v[212:215]\n\t" \
    "s_waitcnt lgkmcnt(3)\n\t" \
    "v_mfma_f32_16x16x32_bf16 v[212:215], v[60:63], v[224:227], v[212:215]\n\t" \
    "v_mfma_f32_16x16x32_bf16 v[216:219], v[64:67], v[240:243], v[216:219]\n\t" \
    "v_mfma_f32_16x16x32_bf16 v[216:219], v[68:71], v[224:227], v[216:219]\n\t" \
    "v_mfma_f32_16x16x32_bf16 v[220:223], v[72:75], v[240:243], v[220:223]\n\t" \
    "ds_read_b128 v[240:243], v252 offset:41984\n\t" \
    "v_mfma_f32_16x16x32_bf16 v[220:223], v[76:79], v[224:227], v[220:223]\n\t" \
    "s_waitcnt lgkmcnt(0)\n\t" \
    "s_barrier\n\t" ASM_ST \
    "v_xor_b32 v252, 0x2000, %[xa]\n\t" \
    "ds_read_b128 v[224:227], v252 offset:0\n\t" \
    "v_mfma_f32_16x16x32_bf16 v[220:223], v[168:171], v[228:231], v[220:223]\n\t" \
    "v_mfma_f32_16x16x32_bf16 v[220:223], v[172:175], v[232:235], v[220:223]\n\t" \
    "v_mfma_f32_16x16x32_bf16 v[216:219], v[160:163], v[228:231], v[216:219]\n\t" \
    "v_mfma_f32_16x16x32_bf16 v[216:219], v[164:167], v[232:235], v[216:219]\n\t" \
    "v_mfma_f32_16x16x32_bf16 v[212:215], v[244:247], v[228:231], v[212:215]\n\t" \
    "ds_read_b128 v[228:231], v252 offset:1024\n\t" \
    "v_mfma_f32_16x16x32_bf16 v[212:215], v[248:251], v[232:235], v[212:215]\n\t" \
    "ds_read_b128 v[232:235], v252 offset:16384\n\t" \
    "v_mfma_f32_16x16x32_bf16 v[200:203], v[244:247], v[236:239], v[200:203]\n\t" \
    "ds_read_b128 v[244:247], %[wl] offset:4096\n\t" \
    "v_mfma_f32_16x16x32_bf16 v[200:203], v[248:251], v[240:243], v[200:203]\n\t" \
    "ds_read_b128 v[248:251], %[wl] offset:5120\n\t" \
    "v_mfma_f32_16x16x32_bf16 v[204:207], v[160:163], v[236:239], v[204:207]\n\t" \
    "v_mfma_f32_16x16x32_bf16 v[204:207], v[164:167], v[240:243], v[204:207]\n\t" \
    "v_mfma_f32_16x16x32_bf16 v[208:211], v[168:171], v[236:239], v[208:211]\n\t" \
    "ds_read_b128 v[236:239], v252 offset:17408\n\t" \
    "v_mfma_f32_16x16x32_bf16 v[208:211], v[172:175], v[240:243], v[208:211]\n\t" \
    "v_xor_b32 v252, 0x2800, %[xa]\n\t" \
    "ds_read_b128 v[240:243], v252 offset:16384\n\t" \
    "s_waitcnt lgkmcnt(6)\n\t" \
    "v_mfma_f32_16x16x32_bf16 v[208:211], v[88:91], v[224:227], v[208:211]\n\t" \
    "s_waitcnt lgkmcnt(5)\n\t" \
    "v_mfma_f32_16x16x32_bf16 v[208:211], v[92:95], v[228:231], v[208:211]\n\t" \
    "v_mfma_f32_16x16x32_bf16 v[204:207], v[80:83], v[224:227], v[204:207]\n\t" \
    "v_mfma_f32_16x16x32_bf16 v[204:207], v[84:87], v[228:231], v[204:207]\n\t" \
    "s_waitcnt lgkmcnt(3)\n\t" \
    "v_mfma_f32_16x16x32_bf16 v[200:203], v[244:247], v[224:227], v[200:203]\n\t" \
    "ds_read_b128 v[224:227], v252 offset:17408\n\t" \
    "s_waitcnt lgkmcnt(3)\n\t" \
    "v_mfma_f32_16x16x32_bf16 v[200:203], v[248:251], v[228:231], v[200:203]\n\t" \
    "ds_read_b128 v[228:231], v252 offset:0\n\t" \
    "v_mfma_f32_16x16x32_bf16 v[212:215], v[244:247], v[232:235], v[212:215]\n\t" \
    "ds_read_b128 v[244:247], %[wl] offset:6144\n\t" \
    "s_waitcnt lgkmcnt(4)\n\t" \
    "v_mfma_f32_16x16x32_bf16 v[212:215], v[248:251], v[236:239], v[212:215]\n\t" \
    "ds_read_b128 v[248:251], %[wl] offset:7168\n\t" \
    "v_mfma_f32_16x16x32_bf16 v[216:219], v[80:83], v[232:235], v[216:219]\n\t" \
    "v_mfma_f32_16x16x32_bf16 v[216:219], v[84:87], v[236:239], v[216:219]\n\t" \
    "v_mfma_f32_16x16x32_bf16 v[220:223], v[88:91], v[232:235], v[220:223]\n\t" \
    "ds_read_b128 v[232:235], v252 offset:1024\n\t" \
    "v_mfma_f32_16x16x32_bf16 v[220:223], v[92:95], v[236:239], v[220:223]\n\t" \
    "v_xor_b32 v252, 0x3000, %[xa]\n\t" \
    "ds_read_b128 v[236:239], v252 offset:0\n\t" \
    "s_waitcnt lgkmcnt(6)\n\t" \
    "v_mfma_f32_16x16x32_bf16 v[220:223], v[112:115], v[240:243], v[220:223]\n\t" \
    "s_waitcnt lgkmcnt(5)\n\t" \
    "v_mfma_f32_16x16x32_bf16 v[220:223], v[116:119], v[224:227], v[220:223]\n\t" \
    "v_mfma_f32_16x16x32_bf16 v[216:219], v[104:107], v[240:243], v[216:219]\n\t" \
    "v_mfma_f32_16x16x32_bf16 v[216:219], v[108:111], v[224:227], v[216:219]\n\t" \
    "v_mfma_f32_16x16x32_bf16 v[212:215], v[96:99], v[240:243], v[212:215]\n\t" \
    "ds_read_b128 v[240:243], v252 offset:1024\n\t" \
    "v_mfma_f32_16x16x32_bf16 v[212:215], v[100:103], v[224:227], v[212:215]\n\t" \
    "ds_read_b128 v[224:227], v252 offset:16384\n\t" \
    "s_waitcnt lgkmcnt(6)\n\t" \
    "v_mfma_f32_16x16x32_bf16 v[200:203], v[96:99], v[228:231], v[200:203]\n\t" \
    "s_waitcnt lgkmcnt(3)\n\t" \
    "v_mfma_f32_16x16x32_bf16 v[200:203], v[100:103], v[232:235], v[200:203]\n\t" \
    "v_mfma_f32_16x16x32_bf16 v[204:207], v[104:107], v[228:231], v[204:207]\n\t" \
    "v_mfma_f32_16x16x32_bf16 v[204:207], v[108:111], v[232:235], v[204:207]\n\t" \
    "v_mfma_f32_16x16x32_bf16 v[208:211], v[112:115], v[228:231], v[208:211]\n\t" \
    "ds_read_b128 v[228:231], v252 offset:17408\n\t" \
    "v_mfma_f32_16x16x32_bf16 v[208:211], v[116:119], v[232:235], v[208:211]\n\t" \
    "v_xor_b32 v252, 0x3800, %[xa]\n\t" \
    "ds_read_b128 v[232:235], v252 offset:16384\n\t" \
    "s_waitcnt lgkmcnt(4)\n\t" \
    "v_mfma_f32_16x16x32_bf16 v[208:211], v[128:131], v[236:239], v[208:211]\n\t" \
    "s_waitcnt lgkmcnt(3)\n\t" \
    "v_mfma_f32_16x16x32_bf16 v[208:211], v[132:135], v[240:243], v[208:211]\n\t" \
    "v_mfma_f32_16x16x32_bf16 v[204:207], v[120:123], v[236:239], v[204:207]\n\t" \
    "v_mfma_f32_16x16x32_bf16 v[204:207], v[124:127], v[240:243], v[204:207]\n\t" \
    "v_mfma_f32_16x16x32_bf16 v[200:203], v[244:247], v[236:239], v[200:203]\n\t" \
    "ds_read_b128 v[236:239], v252 offset:17408\n\t" \
    "v_mfma_f32_16x16x32_bf16 v[200:203], v[248:251], v[240:243], v[200:203]\n\t" \
    "ds_read_b128 v[240:243], v252 offset:0\n\t" \
    "s_waitcnt lgkmcnt(4)\n\t" \
    "v_mfma_f32_16x16x32_bf16 v[212:215], v[244:247], v[224:227], v[212:215]\n\t" \
    "s_waitcnt lgkmcnt(3)\n\t" \
    "v_mfma_f32_16x16x32_bf16 v[212:215], v[248:251], v[228:231], v[212:215]\n\t" \
    "v_mfma_f32_16x16x32_bf16 v[216:219], v[120:123], v[224:227], v[216:219]\n\t" \
    "v_mfma_f32_16x16x32_bf16 v[216:219], v[124:127], v[228:231], v[216:219]\n\t" \
    "v_mfma_f32_16x16x32_bf16 v[220:223], v[128:131], v[224:227], v[220:223]\n\t" \
    "ds_read_b128 v[224:227], v252 offset:1024\n\t" \
    "v_mfma_f32_16x16x32_bf16 v[220:223], v[132:135], v[228:231], v[220:223]\n\t" \
    "v_and_b32 v252, 0x3ff, %[xa]\n\t" \
    "ds_read_b128 v[228:231], v252 offset:43008\n\t" \
    "s_waitcnt lgkmcnt(4)\n\t" \
    "v_mfma_f32_16x16x32_bf16 v[220:223], v[152:155], v[232:235], v[220:223]\n\t" \
    "v_mov_b64 v[244:245], 0\n\t" \
    "s_waitcnt lgkmcnt(3)\n\t" \
    "v_mfma_f32_16x16x32_bf16 v[220:223], v[156:159], v[236:239], v[220:223]\n\t" \
    "v_mov_b64 v[246:247], 0\n\t" \
    "v_mfma_f32_16x16x32_bf16 v[216:219], v[144:147], v[232:235], v[216:219]\n\t" \
    "v_mov_b64 v[248:249], 0\n\t" \
    "v_mfma_f32_16x16x32_bf16 v[216:219], v[148:151], v[236:239], v[216:219]\n\t" \
    "v_mov_b64 v[250:251], 0\n\t" \
    "v_mfma_f32_16x16x32_bf16 v[212:215], v[136:139], v[232:235], v[212:215]\n\t" \
    "ds_read_b128 v[232:235], v252 offset:44032\n\t" \
    "v_mfma_f32_16x16x32_bf16 v[212:215], v[140:143], v[236:239], v[212:215]\n\t" \
    "ds_read_b128 v[236:239], v252 offset:47104\n\t" \
    "s_waitcnt lgkmcnt(4)\n\t" \
    "v_mfma_f32_16x16x32_bf16 v[200:203], v[136:139], v[240:243], v[200:203]\n\t" \
    "s_waitcnt lgkmcnt(3)\n\t" \
    "v_mfma_f32_16x16x32_bf16 v[200:203], v[140:143], v[224:227], v[200:203]\n\t" \
    "v_mfma_f32_16x16x32_bf16 v[204:207], v[144:147], v[240:243], v[204:207]\n\t" \
    "v_mfma_f32_16x16x32_bf16 v[204:207], v[148:151], v[224:227], v[204:207]\n\t" \
    "v_mfma_f32_16x16x32_bf16 v[208:211], v[152:155], v[240:243], v[208:211]\n\t" \
    "ds_read_b128 v[240:243], v252 offset:48128\n\t" \
    "v_mfma_f32_16x16x32_bf16 v[208:211], v[156:159], v[224:227], v[208:211]\n\t" \
    "s_waitcnt lgkmcnt(3)\n\t" \
    "v_mfma_f32_16x16x32_bf16 v[208:211], v[192:195], v[228:231], v[208:211]\n\t" \
    "v_add_u32 v252, %[qs], v252\n\t" \
    "v_mov_b64 v[224:225], 0\n\t" \
    "s_waitcnt lgkmcnt(2)\n\t" \
    "v_mfma_f32_16x16x32_bf16 v[208:211], v[196:199], v[232:235], v[208:211]\n\t" \
    "v_mov_b64 v[226:227], 0\n\t" \
    "v_mfma_f32_16x16x32_bf16 v[204:207], v[184:187], v[228:231], v[204:207]\n\t" \
    "v_mfma_f32_16x16x32_bf16 v[204:207], v[188:191], v[232:235], v[204:207]\n\t" \
    "v_mfma_f32_16x16x32_bf16 v[200:203], v[176:179], v[228:231], v[200:203]\n\t" \
    "v_mfma_f32_16x16x32_bf16 v[200:203], v[180:183], v[232:235], v[200:203]\n\t" \
    "s_waitcnt lgkmcnt(1)\n\t" \
    "v_mfma_f32_16x16x32_bf16 v[212:215], v[176:179], v[236:239], v[212:215]\n\t" \
    "ds_read_b128 v[228:231], %[wl] offset:2048\n\t" \
    "ds_read_b128 v[232:235], %[wl] offset:3072\n\t" \
    "s_waitcnt lgkmcnt(2)\n\t" \
    "v_mfma_f32_16x16x32_bf16 v[212:215], v[180:183], v[240:243], v[212:215]\n\t" \
    "v_cvt_pk_bf16_f32 v208, v208, v209\n\t" \
    "v_cvt_pk_bf16_f32 v209, v210, v211\n\t" \
    "v_mfma_f32_16x16x32_bf16 v[216:219], v[184:187], v[236:239], v[216:219]\n\t" \
    "ds_write_b64 v252, v[208:209] offset:2048\n\t" \
    "v_cvt_pk_bf16_f32 v204, v204, v205\n\t" \
    "v_mfma_f32_16x16x32_bf16 v[216:219], v[188:191], v[240:243], v[216:219]\n\t" \
    "v_cvt_pk_bf16_f32 v205, v206, v207\n\t" \
    "ds_write_b64 v252, v[204:205] offset:1024\n\t" \
    "v_mfma_f32_16x16x32_bf16 v[220:223], v[192:195], v[236:239], v[220:223]\n\t" \
    "v_cvt_pk_bf16_f32 v200, v200, v201\n\t" \
    "v_cvt_pk_bf16_f32 v201, v202, v203\n\t" \
    "v_mfma_f32_16x16x32_bf16 v[220:223], v[196:199], v[240:243], v[220:223]\n\t" \
    "ds_write_b64 v252, v[200:201] offset:0\n\t" \
    "ds_read_b128 v[200:203], %[wl] offset:0\n\t" \
    "ds_read_b128 v[204:207], %[wl] offset:1024\n\t" \
    "v_cvt_pk_bf16_f32 v212, v212, v213\n\t" \
    "v_cvt_pk_bf16_f32 v213, v214, v215\n\t" \
    "ds_write_b64 v252, v[212:213] offset:6144\n\t" \
    "v_cvt_pk_bf16_f32 v216, v216, v217\n\t" \
    "v_cvt_pk_bf16_f32 v217, v218, v219\n\t" \
    "ds_write_b64 v252, v[216:217] offset:7168\n\t" \
    "v_cvt_pk_bf16_f32 v220, v220, v221\n\t" \
    "v_cvt_pk_bf16_f32 v221, v222, v223\n\t" \
    "ds_write_b64 v252, v[220:221] offset:8192\n\t" \
    "s_waitcnt lgkmcnt(0)\n\t"
#define ASM_XG_S_HI \
    "s_waitcnt lgkmcnt(0)\n\t" \
    "v_xor_b32 v252, 0x2000, %[xa]\n\t" \
    "ds_read_b128 v[224:227], v252 offset:16384\n\t" \
    "ds_read_b128 v[228:231], v252 offset:17408\n\t" \
    "ds_read_b128 v[244:247], %[wl] offset:4096\n\t" \
    "ds_read_b128 v[248:251], %[wl] offset:5120\n\t" \
    "ds_read_b128 v[232:235], v252 offset:0\n\t" \
    "ds_read_b128 v[236:239], v252 offset:1024\n\t" \
    "v_xor_b32 v252, 0x2800, %[xa]\n\t" \
    "ds_read_b128 v[240:243], v252 offset:0\n\t" \
    "s_cmp_eq_u32 %[slp], 0\n\t" \
    "s_cbranch_scc1 .Lxs%=\n\t" \
    "s_sleep 10\n\t" \
    ".Lxs%=:\n\t" \
    "s_waitcnt lgkmcnt(6)\n\t" \
    "v_mfma_f32_16x16x32_bf16 v[220:223], v[88:91], v[224:227], 0\n\t" \
    "s_waitcnt lgkmcnt(5)\n\t" \
    "v_mfma_f32_16x16x32_bf16 v[220:223], v[92:95], v[228:231], v[220:223]\n\t" \
    "v_mfma_f32_16x16x32_bf16 v[216:219], v[80:83], v[224:227], 0\n\t" \
    "v_mfma_f32_16x16x32_bf16 v[216:219], v[84:87], v[228:231], v[216:219]\n\t" \
    "s_waitcnt lgkmcnt(4)\n\t" \
    "v_mfma_f32_16x16x32_bf16 v[212:215], v[244:247], v[224:227], 0\n\t" \
    "ds_read_b128 v[224:227], v252 offset:1024\n\t" \
    "s_waitcnt lgkmcnt(4)\n\t" \
    "v_mfma_f32_16x16x32_bf16 v[212:215], v[248:251], v[228:231], v[212:215]\n\t" \
    "ds_read_b128 v[228:231], v252 offset:16384\n\t" \
    "s_waitcnt lgkmcnt(4)\n\t" \
    "v_mfma_f32_16x16x32_bf16 v[200:203], v[244:247], v[232:235], 0\n\t" \
    "ds_read_b128 v[244:247], %[wl] offset:6144\n\t" \
    "s_waitcnt lgkmcnt(4)\n\t" \
    "v_mfma_f32_16x16x32_bf16 v[200:203], v[248:251], v[236:239], v[200:203]\n\t" \
    "ds_read_b128 v[248:251], %[wl] offset:7168\n\t" \
    "v_mfma_f32_16x16x32_bf16 v[204:207], v[80:83], v[232:235], 0\n\t" \
    "v_mfma_f32_16x16x32_bf16 v[204:207], v[84:87], v[236:239], v[204:207]\n\t" \
    "v_mfma_f32_16x16x32_bf16 v[208:211], v[88:91], v[232:235], 0\n\t" \
    "ds_read_b128 v[232:235], v252 offset:17408\n\t" \
    "v_mfma_f32_16x16x32_bf16 v[208:211], v[92:95], v[236:239], v[208:211]\n\t" \
    "v_xor_b32 v252, 0x3000, %[xa]\n\t" \
    "ds_read_b128 v[236:239], v252 offset:16384\n\t" \
    "s_waitcnt lgkmcnt(6)\n\t" \
    "v_mfma_f32_16x16x32_bf16 v[208:211], v[112:115], v[240:243], v[208:211]\n\t" \
    "s_waitcnt lgkmcnt(5)\n\t" \
    "v_mfma_f32_16x16x32_bf16 v[208:211], v[116:119], v[224:227], v[208:211]\n\t" \
    "v_mfma_f32_16x16x32_bf16 v[204:207], v[104:107], v[240:243], v[204:207]\n\t" \
    "v_mfma_f32_16x16x32_bf16 v[204:207], v[108:111], v[224:227], v[204:207]\n\t" \
    "v_mfma_f32_16x16x32_bf16 v[200:203], v[96:99], v[240:243], v[200:203]\n\t" \
    "ds_read_b128 v[240:243], v252 offset:17408\n\t" \
    "v_mfma_f32_16x16x32_bf16 v[200:203], v[100:103], v[224:227], v[200:203]\n\t" \
    "ds_read_b128 v[224:227], v252 offset:0\n\t" \
    "s_waitcnt lgkmcnt(6)\n\t" \
    "v_mfma_f32_16x16x32_bf16 v[212:215], v[96:99], v[228:231], v[212:215]\n\t" \
    "s_waitcnt lgkmcnt(3)\n\t" \
    "v_mfma_f32_16x16x32_bf16 v[212:215], v[100:103], v[232:235], v[212:215]\n\t" \
    "v_mfma_f32_16x16x32_bf16 v[216:219], v[104:107], v[228:231], v[216:219]\n\t" \
    "v_mfma_f32_16x16x32_bf16 v[216:219], v[108:111], v[232:235], v[216:219]\n\t" \
    "v_mfma_f32_16x16x32_bf16 v[220:223], v[112:115], v[228:231], v[220:223]\n\t" \
    "ds_read_b128 v[228:231], v252 offset:1024\n\t" \
    "v_mfma_f32_16x16x32_bf16 v[220:223], v[116:119], v[232:235], v[220:223]\n\t" \
    "v_xor_b32 v252, 0x3800, %[xa]\n\t" \
    "ds_read_b128 v[232:235], v252 offset:0\n\t" \
    "s_waitcnt lgkmcnt(4)\n\t" \
    "v_mfma_f32_16x16x32_bf16 v[220:223], v[128:131], v[236:239], v[220:223]\n\t" \
    "s_waitcnt lgkmcnt(3)\n\t" \
    "v_mfma_f32_16x16x32_bf16 v[220:223], v[132:135], v[240:243], v[220:223]\n\t" \
    "v_mfma_f32_16x16x32_bf16 v[216:219], v[120:123], v[236:239], v[216:219]\n\t" \
    "v_mfma_f32_16x16x32_bf16 v[216:219], v[124:127], v[240:243], v[216:219]\n\t" \
    "v_mfma_f32_16x16x32_bf16 v[212:215], v[244:247], v[236:239], v[212:215]\n\t" \
    "ds_read_b128 v[236:239], v252 offset:1024\n\t" \
    "v_mfma_f32_16x16x32_bf16 v[212:215], v[248:251], v[240:243], v[212:215]\n\t" \
    "ds_read_b128 v[240:243], v252 offset:16384\n\t" \
    "s_waitcnt lgkmcnt(4)\n\t" \
    "v_mfma_f32_16x16x32_bf16 v[200:203], v[244:247], v[224:227], v[200:203]\n\t" \
    "ds_read_b128 v[244:247], %[wl] offset:8192\n\t" \
    "s_waitcnt lgkmcnt(4)\n\t" \
    "v_mfma_f32_16x16x32_bf16 v[200:203], v[248:251], v[228:231], v[200:203]\n\t" \
    "ds_read_b128 v[248:251], %[wl] offset:9216\n\t" \
    "v_mfma_f32_16x16x32_bf16 v[204:207], v[120:123], v[224:227], v[204:207]\n\t" \
    "v_mfma_f32_16x16x32_bf16 v[204:207], v[124:127], v[228:231], v[204:207]\n\t" \
    "v_mfma_f32_16x16x32_bf16 v[208:211], v[128:131], v[224:227], v[208:211]\n\t" \
    "ds_read_b128 v[224:227], v252 offset:17408\n\t" \
    "v_mfma_f32_16x16x32_bf16 v[208:211], v[132:135], v[228:231], v[208:211]\n\t" \
    "v_and_b32 v252, 0x3ff, %[xa]\n\t" \
    "ds_read_b128 v[228:231], v252 offset:45056\n\t" \
    "s_waitcnt lgkmcnt(6)\n\t" \
    "v_mfma_f32_16x16x32_bf16 v[208:211], v[152:155], v[232:235], v[208:211]\n\t" \
    "s_waitcnt lgkmcnt(5)\n\t" \
    "v_mfma_f32_16x16x32_bf16 v[208:211], v[156:159], v[236:239], v[208:211]\n\t" \
    "v_mfma_f32_16x16x32_bf16 v[204:207], v[144:147], v[232:235], v[204:207]\n\t" \
    "v_mfma_f32_16x16x32_bf16 v[204:207], v[148:151], v[236:239], v[204:207]\n\t" \
    "v_mfma_f32_16x16x32_bf16 v[200:203], v[136:139], v[232:235], v[200:203]\n\t" \
    "ds_read_b128 v[232:235], v252 offset:46080\n\t" \
    "v_mfma_f32_16x16x32_bf16 v[200:203], v[140:143], v[236:239], v[200:203]\n\t" \
    "ds_read_b128 v[236:239], v252 offset:40960\n\t" \
    "s_waitcnt lgkmcnt(6)\n\t" \
    "v_mfma_f32_16x16x32_bf16 v[212:215], v[136:139], v[240:243], v[212:215]\n\t" \
    "s_waitcnt lgkmcnt(3)\n\t" \
    "v_mfma_f32_16x16x32_bf16 v[212:215], v[140:143], v[224:227], v[212:215]\n\t" \
    "v_mfma_f32_16x16x32_bf16 v[216:219], v[144:147], v[240:243], v[216:219]\n\t" \
    "v_mfma_f32_16x16x32_bf16 v[216:219], v[148:151], v[224:227], v[216:219]\n\t" \
    "v_mfma_f32_16x16x32_bf16 v[220:223], v[152:155], v[240:243], v[220:223]\n\t" \
    "ds_read_b128 v[240:243], v252 offset:41984\n\t" \
    "v_mfma_f32_16x16x32_bf16 v[220:223], v[156:159], v[224:227], v[220:223]\n\t" \
    "s_waitcnt vmcnt(0) lgkmcnt(0)\n\t" \
    "s_barrier\n\t" ASM_ST \
    "s_mov_b32 m0, %[d0]\n\t" \
    "s_nop 0\n\t" \
    "global_load_lds_dwordx4 v252, %[g0] offset:0 sc1\n\t" \
    "global_load_lds_dwordx4 v252, %[g0] offset:1024 sc1\n\t" \
    "global_load_lds_dwordx4 v252, %[g0] offset:2048 sc1\n\t" \
    "global_load_lds_dwordx4 v252, %[g0] offset:3072 sc1\n\t" \
    "s_mov_b32 m0, %[d1]\n\t" \
    "s_nop 0\n\t" \
    "global_load_lds_dwordx4 v252, %[g1] sc1\n\t" \
    "ds_read_b128 v[224:227], %[xa] offset:0\n\t" \
    "v_mfma_f32_16x16x32_bf16 v[220:223], v[168:171], v[228:231], v[220:223]\n\t" \
    "v_mfma_f32_16x16x32_bf16 v[220:223], v[172:175], v[232:235], v[220:223]\n\t" \
    "v_mfma_f32_16x16x32_bf16 v[216:219], v[160:163], v[228:231], v[216:219]\n\t" \
    "v_mfma_f32_16x16x32_bf16 v[216:219], v[164:167], v[232:235], v[216:219]\n\t" \
    "v_mfma_f32_16x16x32_bf16 v[212:215], v[244:247], v[228:231], v[212:215]\n\t" \
    "ds_read_b128 v[228:231], %[xa] offset:1024\n\t" \
    "v_mfma_f32_16x16x32_bf16 v[212:215], v[248:251], v[232:235], v[212:215]\n\t" \
    "ds_read_b128 v[232:235], %[xa] offset:16384\n\t" \
    "v_mfma_f32_16x16x32_bf16 v[200:203], v[244:247], v[236:239], v[200:203]\n\t" \
    "ds_read_b128 v[244:247], %[wl] offset:0\n\t" \
    "v_mfma_f32_16x16x32_bf16 v[200:203], v[248:251], v[240:243], v[200:203]\n\t" \
    "ds_read_b128 v[248:251], %[wl] offset:1024\n\t" \
    "v_mfma_f32_16x16x32_bf16 v[204:207], v[160:163], v[236:239], v[204:207]\n\t" \
    "v_mfma_f32_16x16x32_bf16 v[204:207], v[164:167], v[240:243], v[204:207]\n\t" \
    "v_mfma_f32_16x16x32_bf16 v[208:211], v[168:171], v[236:239], v[208:211]\n\t" \
    "ds_read_b128 v[236:239], %[xa] offset:17408\n\t" \
    "v_mfma_f32_16x16x32_bf16 v[208:211], v[172:175], v[240:243], v[208:211]\n\t" \
    "v_xor_b32 v252, 0x800, %[xa]\n\t" \
    "ds_read_b128 v[240:243], v252 offset:16384\n\t" \
    "s_waitcnt lgkmcnt(6)\n\t" \
    "v_mfma_f32_16x16x32_bf16 v[208:211], v[8:11], v[224:227], v[208:211]\n\t" \
    "s_waitcnt lgkmcnt(5)\n\t" \
    "v_mfma_f32_16x16x32_bf16 v[208:211], v[12:15], v[228:231], v[208:211]\n\t" \
    "v_mfma_f32_16x16x32_bf16 v[204:207], v[0:3], v[224:227], v[204:207]\n\t" \
    "v_mfma_f32_16x16x32_bf16 v[204:207], v[4:7], v[228:231], v[204:207]\n\t" \
    "s_waitcnt lgkmcnt(3)\n\t" \
    "v_mfma_f32_16x16x32_bf16 v[200:203], v[244:247], v[224:227], v[200:203]\n\t" \
    "ds_read_b128 v[224:227], v252 offset:17408\n\t" \
    "s_waitcnt lgkmcnt(3)\n\t" \
    "v_mfma_f32_16x16x32_bf16 v[200:203], v[248:251], v[228:231], v[200:203]\n\t" \
    "ds_read_b128 v[228:231], v252 offset:0\n\t" \
    "v_mfma_f32_16x16x32_bf16 v[212:215], v[244:247], v[232:235], v[212:215]\n\t" \
    "ds_read_b128 v[244:247], %[wl] offset:2048\n\t" \
    "s_waitcnt lgkmcnt(4)\n\t" \
    "v_mfma_f32_16x16x32_bf16 v[212:215], v[248:251], v[236:239], v[212:215]\n\t" \
    "ds_read_b128 v[248:251], %[wl] offset:3072\n\t" \
    "v_mfma_f32_16x16x32_bf16 v[216:219], v[0:3], v[232:235], v[216:219]\n\t" \
    "v_mfma_f32_16x16x32_bf16 v[216:219], v[4:7], v[236:239], v[216:219]\n\t" \
    "v_mfma_f32_16x16x32_bf16 v[220:223], v[8:11], v[232:235], v[220:223]\n\t" \
    "ds_read_b128 v[232:235], v252 offset:1024\n\t" \
    "v_mfma_f32_16x16x32_bf16 v[220:223], v[12:15], v[236:239], v[220:223]\n\t" \
    "v_xor_b32 v252, 0x1000, %[xa]\n\t" \
    "ds_read_b128 v[236:239], v252 offset:0\n\t" \
    "s_waitcnt lgkmcnt(6)\n\t" \
    "v_mfma_f32_16x16x32_bf16 v[220:223], v[32:35], v[240:243], v[220:223]\n\t" \
    "s_waitcnt lgkmcnt(5)\n\t" \
    "v_mfma_f32_16x16x32_bf16 v[220:223], v[36:39], v[224:227], v[220:223]\n\t" \
    "v_mfma_f32_16x16x32_bf16 v[216:219], v[24:27], v[240:243], v[216:219]\n\t" \
    "v_mfma_f32_16x16x32_bf16 v[216:219], v[28:31], v[224:227], v[216:219]\n\t" \
    "v_mfma_f32_16x16x32_bf16 v[212:215], v[16:19], v[240:243], v[212:215]\n\t" \
    "ds_read_b128 v[240:243], v252 offset:1024\n\t" \
    "v_mfma_f32_16x16x32_bf16 v[212:215], v[20:23], v[224:227], v[212:215]\n\t" \
    "ds_read_b128 v[224:227], v252 offset:16384\n\t" \
    "s_waitcnt lgkmcnt(6)\n\t" \
    "v_mfma_f32_16x16x32_bf16 v[200:203], v[16:19], v[228:231], v[200:203]\n\t" \
    "s_waitcnt lgkmcnt(3)\n\t" \
    "v_mfma_f32_16x16x32_bf16 v[200:203], v[20:23], v[232:235], v[200:203]\n\t" \
    "v_mfma_f32_16x16x32_bf16 v[204:207], v[24:27], v[228:231], v[204:207]\n\t" \
    "v_mfma_f32_16x16x32_bf16 v[204:207], v[28:31], v[232:235], v[204:207]\n\t" \
    "v_mfma_f32_16x16x32_bf16 v[208:211], v[32:35], v[228:231], v[208:211]\n\t" \
    "ds_read_b128 v[228:231], v252 offset:17408\n\t" \
    "v_mfma_f32_16x16x32_bf16 v[208:211], v[36:39], v[232:235], v[208:211]\n\t" \
    "v_xor_b32 v252, 0x1800, %[xa]\n\t" \
    "ds_read_b128 v[232:235], v252 offset:16384\n\t" \
    "s_waitcnt lgkmcnt(4)\n\t" \
    "v_mfma_f32_16x16x32_bf16 v[208:211], v[48:51], v[236:239], v[208:211]\n\t" \
    "s_waitcnt lgkmcnt(3)\n\t" \
    "v_mfma_f32_16x16x32_bf16 v[208:211], v[52:55], v[240:243], v[208:211]\n\t" \
    "v_mfma_f32_16x16x32_bf16 v[204:207], v[40:43], v[236:239], v[204:207]\n\t" \
    "v_mfma_f32_16x16x32_bf16 v[204:207], v[44:47], v[240:243], v[204:207]\n\t" \
    "v_mfma_f32_16x16x32_bf16 v[200:203], v[244:247], v[236:239], v[200:203]\n\t" \
    "ds_read_b128 v[236:239], v252 offset:17408\n\t" \
    "v_mfma_f32_16x16x32_bf16 v[200:203], v[248:251], v[240:243], v[200:203]\n\t" \
    "ds_read_b128 v[240:243], v252 offset:0\n\t" \
    "s_waitcnt lgkmcnt(4)\n\t" \
    "v_mfma_f32_16x16x32_bf16 v[212:215], v[244:247], v[224:227], v[212:215]\n\t" \
    "s_waitcnt lgkmcnt(3)\n\t" \
    "v_mfma_f32_16x16x32_bf16 v[212:215], v[248:251], v[228:231], v[212:215]\n\t" \
    "v_mfma_f32_16x16x32_bf16 v[216:219], v[40:43], v[224:227], v[216:219]\n\t" \
    "v_mfma_f32_16x16x32_bf16 v[216:219], v[44:47], v[228:231], v[216:219]\n\t" \
    "v_mfma_f32_16x16x32_bf16 v[220:223], v[48:51], v[224:227], v[220:223]\n\t" \
    "ds_read_b128 v[224:227], v252 offset:1024\n\t" \
    "v_mfma_f32_16x16x32_bf16 v[220:223], v[52:55], v[228:231], v[220:223]\n\t" \
    "v_and_b32 v252, 0x3ff, %[xa]\n\t" \
    "ds_read_b128 v[228:231], v252 offset:43008\n\t" \
    "s_waitcnt lgkmcnt(4)\n\t" \
    "v_mfma_f32_16x16x32_bf16 v[220:223], v[72:75], v[232:235], v[220:223]\n\t" \
    "v_mov_b64 v[244:245], 0\n\t" \
    "s_waitcnt lgkmcnt(3)\n\t" \
    "v_mfma_f32_16x16x32_bf16 v[220:223], v[76:79], v[236:239], v[220:223]\n\t" \
    "v_mov_b64 v[246:247], 0\n\t" \
    "v_mfma_f32_16x16x32_bf16 v[216:219], v[64:67], v[232:235], v[216:219]\n\t" \
    "v_mov_b64 v[248:249], 0\n\t" \
    "v_mfma_f32_16x16x32_bf16 v[216:219], v[68:71], v[236:239], v[216:219]\n\t" \
    "v_mov_b64 v[250:251], 0\n\t" \
    "v_mfma_f32_16x16x32_bf16 v[212:215], v[56:59], v[232:235], v[212:215]\n\t" \
    "ds_read_b128 v[232:235], v252 offset:44032\n\t" \
    "v_mfma_f32_16x16x32_bf16 v[212:215], v[60:63], v[236:239], v[212:215]\n\t" \
    "ds_read_b128 v[236:239], v252 offset:47104\n\t" \
    "s_waitcnt lgkmcnt(4)\n\t" \
    "v_mfma_f32_16x16x32_bf16 v[200:203], v[56:59], v[240:243], v[200:203]\n\t" \
    "s_waitcnt lgkmcnt(3)\n\t" \
    "v_mfma_f32_16x16x32_bf16 v[200:203], v[60:63], v[224:227], v[200:203]\n\t" \
    "v_mfma_f32_16x16x32_bf16 v[204:207], v[64:67], v[240:243], v[204:207]\n\t" \
    "v_mfma_f32_16x16x32_bf16 v[204:207], v[68:71], v[224:227], v[204:207]\n\t" \
    "v_mfma_f32_16x16x32_bf16 v[208:211], v[72:75], v[240:243], v[208:211]\n\t" \
    "ds_read_b128 v[240:243], v252 offset:48128\n\t" \
    "v_mfma_f32_16x16x32_bf16 v[208:211], v[76:79], v[224:227], v[208:211]\n\t" \
    "s_waitcnt lgkmcnt(3)\n\t" \
    "v_mfma_f32_16x16x32_bf16 v[208:211], v[192:195], v[228:231], v[208:211]\n\t" \
    "v_add_u32 v252, %[qs], v252\n\t" \
    "v_mov_b64 v[224:225], 0\n\t" \
    "s_waitcnt lgkmcnt(2)\n\t" \
    "v_mfma_f32_16x16x32_bf16 v[208:211], v[196:199], v[232:235], v[208:211]\n\t" \
    "v_mov_b64 v[226:227], 0\n\t" \
    "v_mfma_f32_16x16x32_bf16 v[204:207], v[184:187], v[228:231], v[204:207]\n\t" \
    "v_mfma_f32_16x16x32_bf16 v[204:207], v[188:191], v[232:235], v[204:207]\n\t" \
    "v_mfma_f32_16x16x32_bf16 v[200:203], v[176:179], v[228:231], v[200:203]\n\t" \
    "v_mfma_f32_16x16x32_bf16 v[200:203], v[180:183], v[232:235], v[200:203]\n\t" \
    "s_waitcnt lgkmcnt(1)\n\t" \
    "v_mfma_f32_16x16x32_bf16 v[212:215], v[176:179], v[236:239], v[212:215]\n\t" \
    "ds_read_b128 v[228:231], %[wl] offset:2048\n\t" \
    "ds_read_b128 v[232:235], %[wl] offset:3072\n\t" \
    "s_waitcnt lgkmcnt(2)\n\t" \
    "v_mfma_f32_16x16x32_bf16 v[212:215], v[180:183], v[240:243], v[212:215]\n\t" \
    "v_cvt_pk_bf16_f32 v208, v208, v209\n\t" \
    "v_cvt_pk_bf16_f32 v209, v210, v211\n\t" \
    "v_mfma_f32_16x16x32_bf16 v[216:219], v[184:187], v[236:239], v[216:219]\n\t" \
    "ds_write_b64 v252, v[208:209] offset:2048\n\t" \
    "v_cvt_pk_bf16_f32 v204, v204, v205\n\t" \
    "v_mfma_f32_16x16x32_bf16 v[216:219], v[188:191], v[240:243], v[216:219]\n\t" \
    "v_cvt_pk_bf16_f32 v205, v206, v207\n\t" \
    "ds_write_b64 v252, v[204:205] offset:1024\n\t" \
    "v_mfma_f32_16x16x32_bf16 v[220:223], v[192:195], v[236:239], v[220:223]\n\t" \
    "v_cvt_pk_bf16_f32 v200, v200, v201\n\t" \
    "v_cvt_pk_bf16_f32 v201, v202, v203\n\t" \
    "v_mfma_f32_16x16x32_bf16 v[220:223], v[196:199], v[240:243], v[220:223]\n\t" \
    "ds_write_b64 v252, v[200:201] offset:0\n\t" \
    "ds_read_b128 v[200:203], %[wl] offset:0\n\t" \
    "ds_read_b128 v[204:207], %[wl] offset:1024\n\t" \
    "v_cvt_pk_bf16_f32 v212, v212, v213\n\t" \
    "v_cvt_pk_bf16_f32 v213, v214, v215\n\t" \
    "ds_write_b64 v252, v[212:213] offset:6144\n\t" \
    "v_cvt_pk_bf16_f32 v216, v216, v217\n\t" \
    "v_cvt_pk_bf16_f32 v217, v218, v219\n\t" \
    "ds_write_b64 v252, v[216:217] offset:7168\n\t" \
    "v_cvt_pk_bf16_f32 v220, v220, v221\n\t" \
    "v_cvt_pk_bf16_f32 v221, v222, v223\n\t" \
    "ds_write_b64 v252, v[220:221] offset:8192\n\t" \
    "s_waitcnt lgkmcnt(0)\n\t"
#define ASM_XG_S_LO2 \
    "s_waitcnt lgkmcnt(0)\n\t" \
    "ds_read_b128 v[224:227], %[xa] offset:16384\n\t" \
    "ds_read_b128 v[228:231], %[xa] offset:17408\n\t" \
    "v_xor_b32 v252, 0x800, %[xa]\n\t" \
    "ds_read_b128 v[232:235], v252 offset:16384\n\t" \
    "ds_read_b128 v[236:239], v252 offset:17408\n\t" \
    "v_xor_b32 v252, 0x1000, %[xa]\n\t" \
    "ds_read_b128 v[240:243], v252 offset:16384\n\t" \
    "ds_read_b128 v[212:215], v252 offset:17408\n\t" \
    "v_xor_b32 v252, 0x1800, %[xa]\n\t" \
    "ds_read_b128 v[200:203], v252 offset:16384\n\t" \
    "s_waitcnt lgkmcnt(6)\n\t" \
    "v_mfma_f32_16x16x32_bf16 v[220:223], v[8:11], v[224:227], 0\n\t" \
    "s_waitcnt lgkmcnt(5)\n\t" \
    "v_mfma_f32_16x16x32_bf16 v[220:223], v[12:15], v[228:231], v[220:223]\n\t" \
    "v_mfma_f32_16x16x32_bf16 v[216:219], v[0:3], v[224:227], 0\n\t" \
    "ds_read_b128 v[224:227], v252 offset:17408\n\t" \
    "v_mfma_f32_16x16x32_bf16 v[216:219], v[4:7], v[228:231], v[216:219]\n\t" \
    "v_and_b32 v252, 0x3ff, %[xa]\n\t" \
    "ds_read_b128 v[228:231], v252 offset:45056\n\t" \
    "s_waitcnt lgkmcnt(6)\n\t" \
    "v_mfma_f32_16x16x32_bf16 v[216:219], v[24:27], v[232:235], v[216:219]\n\t" \
    "s_waitcnt lgkmcnt(5)\n\t" \
    "v_mfma_f32_16x16x32_bf16 v[216:219], v[28:31], v[236:239], v[216:219]\n\t" \
    "v_mfma_f32_16x16x32_bf16 v[220:223], v[32:35], v[232:235], v[220:223]\n\t" \
    "ds_read_b128 v[232:235], v252 offset:46080\n\t" \
    "v_mfma_f32_16x16x32_bf16 v[220:223], v[36:39], v[236:239], v[220:223]\n\t" \
    "ds_read_b128 v[236:239], %[xa] offset:0\n\t" \
    "s_waitcnt lgkmcnt(6)\n\t" \
    "v_mfma_f32_16x16x32_bf16 v[220:223], v[48:51], v[240:243], v[220:223]\n\t" \
    "s_waitcnt lgkmcnt(5)\n\t" \
    "v_mfma_f32_16x16x32_bf16 v[220:223], v[52:55], v[212:215], v[220:223]\n\t" \
    "v_mfma_f32_16x16x32_bf16 v[216:219], v[40:43], v[240:243], v[216:219]\n\t" \
    "ds_read_b128 v[240:243], %[xa] offset:1024\n\t" \
    "v_mfma_f32_16x16x32_bf16 v[216:219], v[44:47], v[212:215], v[216:219]\n\t" \
    "v_xor_b32 v252, 0x800, %[xa]\n\t" \
    "ds_read_b128 v[212:215], v252 offset:0\n\t" \
    "s_waitcnt lgkmcnt(6)\n\t" \
    "v_mfma_f32_16x16x32_bf16 v[216:219], v[64:67], v[200:203], v[216:219]\n\t" \
    "s_waitcnt lgkmcnt(5)\n\t" \
    "v_mfma_f32_16x16x32_bf16 v[216:219], v[68:71], v[224:227], v[216:219]\n\t" \
    "v_mfma_f32_16x16x32_bf16 v[220:223], v[72:75], v[200:203], v[220:223]\n\t" \
    "ds_read_b128 v[200:203], v252 offset:1024\n\t" \
    "v_mfma_f32_16x16x32_bf16 v[220:223], v[76:79], v[224:227], v[220:223]\n\t" \
    "v_xor_b32 v252, 0x1000, %[xa]\n\t" \
    "ds_read_b128 v[224:227], v252 offset:0\n\t" \
    "s_waitcnt lgkmcnt(6)\n\t" \
    "v_mfma_f32_16x16x32_bf16 v[220:223], v[168:171], v[228:231], v[220:223]\n\t" \
    "s_waitcnt lgkmcnt(5)\n\t" \
    "v_mfma_f32_16x16x32_bf16 v[220:223], v[172:175], v[232:235], v[220:223]\n\t" \
    "v_mfma_f32_16x16x32_bf16 v[216:219], v[160:163], v[228:231], v[216:219]\n\t" \
    "ds_read_b128 v[228:231], v252 offset:1024\n\t" \
    "v_mfma_f32_16x16x32_bf16 v[216:219], v[164:167], v[232:235], v[216:219]\n\t" \
    "v_xor_b32 v252, 0x1800, %[xa]\n\t" \
    "ds_read_b128 v[232:235], v252 offset:0\n\t" \
    "s_waitcnt lgkmcnt(6)\n\t" \
    "v_mfma_f32_16x16x32_bf16 v[204:207], v[0:3], v[236:239], 0\n\t" \
    "s_waitcnt lgkmcnt(5)\n\t" \
    "v_mfma_f32_16x16x32_bf16 v[204:207], v[4:7], v[240:243], v[204:207]\n\t" \
    "v_mfma_f32_16x16x32_bf16 v[208:211], v[8:11], v[236:239], 0\n\t" \
    "ds_read_b128 v[236:239], v252 offset:1024\n\t" \
    "v_mfma_f32_16x16x32_bf16 v[208:211], v[12:15], v[240:243], v[208:211]\n\t" \
    "v_and_b32 v252, 0x3ff, %[xa]\n\t" \
    "ds_read_b128 v[240:243], v252 offset:40960\n\t" \
    "s_waitcnt lgkmcnt(6)\n\t" \
    "v_mfma_f32_16x16x32_bf16 v[208:211], v[32:35], v[212:215], v[208:211]\n\t" \
    "s_waitcnt lgkmcnt(5)\n\t" \
    "v_mfma_f32_16x16x32_bf16 v[208:211], v[36:39], v[200:203], v[208:211]\n\t" \
    "v_mfma_f32_16x16x32_bf16 v[204:207], v[24:27], v[212:215], v[204:207]\n\t" \
    "ds_read_b128 v[212:215], v252 offset:41984\n\t" \
    "v_mfma_f32_16x16x32_bf16 v[204:207], v[28:31], v[200:203], v[204:207]\n\t" \
    "s_waitcnt lgkmcnt(0)\n\t" \
    "s_barrier\n\t" ASM_ST \
    "v_xor_b32 v252, 0x2000, %[xa]\n\t" \
    "ds_read_b128 v[200:203], v252 offset:0\n\t" \
    "v_mfma_f32_16x16x32_bf16 v[204:207], v[40:43], v[224:227], v[204:207]\n\t" \
    "v_mfma_f32_16x16x32_bf16 v[204:207], v[44:47], v[228:231], v[204:207]\n\t" \
    "v_mfma_f32_16x16x32_bf16 v[208:211], v[48:51], v[224:227], v[208:211]\n\t" \
    "ds_read_b128 v[224:227], v252 offset:1024\n\t" \
    "v_mfma_f32_16x16x32_bf16 v[208:211], v[52:55], v[228:231], v[208:211]\n\t" \
    "v_xor_b32 v252, 0x2800, %[xa]\n\t" \
    "ds_read_b128 v[228:231], v252 offset:0\n\t" \
    "v_mfma_f32_16x16x32_bf16 v[208:211], v[72:75], v[232:235], v[208:211]\n\t" \
    "v_mfma_f32_16x16x32_bf16 v[208:211], v[76:79], v[236:239], v[208:211]\n\t" \
    "v_mfma_f32_16x16x32_bf16 v[204:207], v[64:67], v[232:235], v[204:207]\n\t" \
    "ds_read_b128 v[232:235], v252 offset:1024\n\t" \
    "v_mfma_f32_16x16x32_bf16 v[204:207], v[68:71], v[236:239], v[204:207]\n\t" \
    "v_xor_b32 v252, 0x3000, %[xa]\n\t" \
    "ds_read_b128 v[236:239], v252 offset:0\n\t" \
    "v_mfma_f32_16x16x32_bf16 v[204:207], v[160:163], v[240:243], v[204:207]\n\t" \
    "v_mfma_f32_16x16x32_bf16 v[204:207], v[164:167], v[212:215], v[204:207]\n\t" \
    "v_mfma_f32_16x16x32_bf16 v[208:211], v[168:171], v[240:243], v[208:211]\n\t" \
    "ds_read_b128 v[240:243], v252 offset:1024\n\t" \
    "v_mfma_f32_16x16x32_bf16 v[208:211], v[172:175], v[212:215], v[208:211]\n\t" \
    "v_xor_b32 v252, 0x3800, %[xa]\n\t" \
    "ds_read_b128 v[212:215], v252 offset:0\n\t" \
    "s_waitcnt lgkmcnt(6)\n\t" \
    "v_mfma_f32_16x16x32_bf16 v[208:211], v[88:91], v[200:203], v[208:211]\n\t" \
    "s_waitcnt lgkmcnt(5)\n\t" \
    "v_mfma_f32_16x16x32_bf16 v[208:211], v[92:95], v[224:227], v[208:211]\n\t" \
    "v_mfma_f32_16x16x32_bf16 v[204:207], v[80:83], v[200:203], v[204:207]\n\t" \
    "ds_read_b128 v[200:203], v252 offset:1024\n\t" \
    "v_mfma_f32_16x16x32_bf16 v[204:207], v[84:87], v[224:227], v[204:207]\n\t" \
    "v_and_b32 v252, 0x3ff, %[xa]\n\t" \
    "ds_read_b128 v[224:227], v252 offset:43008\n\t" \
    "s_waitcnt lgkmcnt(6)\n\t" \
    "v_mfma_f32_16x16x32_bf16 v[204:207], v[104:107], v[228:231], v[204:207]\n\t" \
    "s_waitcnt lgkmcnt(5)\n\t" \
    "v_mfma_f32_16x16x32_bf16 v[204:207], v[108:111], v[232:235], v[204:207]\n\t" \
    "v_mfma_f32_16x16x32_bf16 v[208:211], v[112:115], v[228:231], v[208:211]\n\t" \
    "ds_read_b128 v[228:231], v252 offset:44032\n\t" \
    "v_mfma_f32_16x16x32_bf16 v[208:211], v[116:119], v[232:235], v[208:211]\n\t" \
    "v_xor_b32 v252, 0x2000, %[xa]\n\t" \
    "ds_read_b128 v[232:235], v252 offset:16384\n\t" \
    "s_waitcnt lgkmcnt(6)\n\t" \
    "v_mfma_f32_16x16x32_bf16 v[208:211], v[128:131], v[236:239], v[208:211]\n\t" \
    "s_waitcnt lgkmcnt(5)\n\t" \
    "v_mfma_f32_16x16x32_bf16 v[208:211], v[132:135], v[240:243], v[208:211]\n\t" \
    "v_mfma_f32_16x16x32_bf16 v[204:207], v[120:123], v[236:239], v[204:207]\n\t" \
    "ds_read_b128 v[236:239], v252 offset:17408\n\t" \
    "v_mfma_f32_16x16x32_bf16 v[204:207], v[124:127], v[240:243], v[204:207]\n\t" \
    "v_xor_b32 v252, 0x2800, %[xa]\n\t" \
    "ds_read_b128 v[240:243], v252 offset:16384\n\t" \
    "s_waitcnt lgkmcnt(6)\n\t" \
    "v_mfma_f32_16x16x32_bf16 v[204:207], v[144:147], v[212:215], v[204:207]\n\t" \
    "s_waitcnt lgkmcnt(5)\n\t" \
    "v_mfma_f32_16x16x32_bf16 v[204:207], v[148:151], v[200:203], v[204:207]\n\t" \
    "v_mfma_f32_16x16x32_bf16 v[208:211], v[152:155], v[212:215], v[208:211]\n\t" \
    "ds_read_b128 v[212:215], v252 offset:17408\n\t" \
    "v_mfma_f32_16x16x32_bf16 v[208:211], v[156:159], v[200:203], v[208:211]\n\t" \
    "v_xor_b32 v252, 0x3000, %[xa]\n\t" \
    "ds_read_b128 v[200:203], v252 offset:16384\n\t" \
    "s_waitcnt lgkmcnt(6)\n\t" \
    "v_mfma_f32_16x16x32_bf16 v[208:211], v[192:195], v[224:227], v[208:211]\n\t" \
    "s_waitcnt lgkmcnt(5)\n\t" \
    "v_mfma_f32_16x16x32_bf16 v[208:211], v[196:199], v[228:231], v[208:211]\n\t" \
    "v_mfma_f32_16x16x32_bf16 v[204:207], v[184:187], v[224:227], v[204:207]\n\t" \
    "ds_read_b128 v[224:227], v252 offset:17408\n\t" \
    "v_mfma_f32_16x16x32_bf16 v[204:207], v[188:191], v[228:231], v[204:207]\n\t" \
    "v_xor_b32 v252, 0x3800, %[xa]\n\t" \
    "ds_read_b128 v[228:231], v252 offset:16384\n\t" \
    "s_waitcnt lgkmcnt(6)\n\t" \
    "v_mfma_f32_16x16x32_bf16 v[216:219], v[80:83], v[232:235], v[216:219]\n\t" \
    "s_waitcnt lgkmcnt(5)\n\t" \
    "v_mfma_f32_16x16x32_bf16 v[216:219], v[84:87], v[236:239], v[216:219]\n\t" \
    "v_mfma_f32_16x16x32_bf16 v[220:223], v[88:91], v[232:235], v[220:223]\n\t" \
    "ds_read_b128 v[232:235], v252 offset:17408\n\t" \
    "v_mfma_f32_16x16x32_bf16 v[220:223], v[92:95], v[236:239], v[220:223]\n\t" \
    "v_and_b32 v252, 0x3ff, %[xa]\n\t" \
    "ds_read_b128 v[236:239], v252 offset:47104\n\t" \
    "s_waitcnt lgkmcnt(6)\n\t" \
    "v_mfma_f32_16x16x32_bf16 v[220:223], v[112:115], v[240:243], v[220:223]\n\t" \
    "v_mov_b64 v[244:245], 0\n\t" \
    "s_waitcnt lgkmcnt(5)\n\t" \
    "v_mfma_f32_16x16x32_bf16 v[220:223], v[116:119], v[212:215], v[220:223]\n\t" \
    "v_mov_b64 v[246:247], 0\n\t" \
    "v_mfma_f32_16x16x32_bf16 v[216:219], v[104:107], v[240:243], v[216:219]\n\t" \
    "ds_read_b128 v[240:243], v252 offset:48128\n\t" \
    "v_mov_b64 v[248:249], 0\n\t" \
    "v_mfma_f32_16x16x32_bf16 v[216:219], v[108:111], v[212:215], v[216:219]\n\t" \
    "v_mov_b64 v[250:251], 0\n\t" \
    "s_waitcnt lgkmcnt(5)\n\t" \
    "v_mfma_f32_16x16x32_bf16 v[216:219], v[120:123], v[200:203], v[216:219]\n\t" \
    "s_waitcnt lgkmcnt(4)\n\t" \
    "v_mfma_f32_16x16x32_bf16 v[216:219], v[124:127], v[224:227], v[216:219]\n\t" \
    "v_mfma_f32_16x16x32_bf16 v[220:223], v[128:131], v[200:203], v[220:223]\n\t" \
    "v_mfma_f32_16x16x32_bf16 v[220:223], v[132:135], v[224:227], v[220:223]\n\t" \
    "s_waitcnt lgkmcnt(3)\n\t" \
    "v_mfma_f32_16x16x32_bf16 v[220:223], v[152:155], v[228:231], v[220:223]\n\t" \
    "v_add_u32 v252, %[qs], v252\n\t" \
    "v_mov_b64 v[224:225], 0\n\t" \
    "s_waitcnt lgkmcnt(2)\n\t" \
    "v_mfma_f32_16x16x32_bf16 v[220:223], v[156:159], v[232:235], v[220:223]\n\t" \
    "v_mov_b64 v[226:227], 0\n\t" \
    "v_cvt_pk_bf16_f32 v208, v208, v209\n\t" \
    "v_mfma_f32_16x16x32_bf16 v[216:219], v[144:147], v[228:231], v[216:219]\n\t" \
    "v_cvt_pk_bf16_f32 v209, v210, v211\n\t" \
    "ds_write_b64 v252, v[208:209] offset:2048\n\t" \
    "v_mfma_f32_16x16x32_bf16 v[216:219], v[148:151], v[232:235], v[216:219]\n\t" \
    "v_cvt_pk_bf16_f32 v204, v204, v205\n\t" \
    "v_cvt_pk_bf16_f32 v205, v206, v207\n\t" \
    "s_waitcnt lgkmcnt(2)\n\t" \
    "v_mfma_f32_16x16x32_bf16 v[216:219], v[184:187], v[236:239], v[216:219]\n\t" \
    "ds_write_b64 v252, v[204:205] offset:1024\n\t" \
    "ds_read_b128 v[228:231], %[wl] offset:2048\n\t" \
    "s_waitcnt lgkmcnt(3)\n\t" \
    "v_mfma_f32_16x16x32_bf16 v[216:219], v[188:191], v[240:243], v[216:219]\n\t" \
    "ds_read_b128 v[232:235], %[wl] offset:3072\n\t" \
    "v_mfma_f32_16x16x32_bf16 v[220:223], v[192:195], v[236:239], v[220:223]\n\t" \
    "v_mfma_f32_16x16x32_bf16 v[220:223], v[196:199], v[240:243], v[220:223]\n\t" \
    "ds_read_b128 v[200:203], %[wl] offset:0\n\t" \
    "ds_read_b128 v[204:207], %[wl] offset:1024\n\t" \
    "v_cvt_pk_bf16_f32 v216, v216, v217\n\t" \
    "v_cvt_pk_bf16_f32 v217, v218, v219\n\t" \
    "ds_write_b64 v252, v[216:217] offset:7168\n\t" \
    "s_nop 2\n\t" \
    "v_cvt_pk_bf16_f32 v220, v220, v221\n\t" \
    "v_cvt_pk_bf16_f32 v221, v222, v223\n\t" \
    "ds_write_b64 v252, v[220:221] offset:8192\n\t" \
    "s_waitcnt lgkmcnt(0)\n\t"
#define ASM_XG_S_LO1 \
    "s_waitcnt lgkmcnt(0)\n\t" \
    "ds_read_b128 v[224:227], %[xa] offset:16384\n\t" \
    "ds_read_b128 v[228:231], %[xa] offset:17408\n\t" \
    "v_xor_b32 v252, 0x800, %[xa]\n\t" \
    "ds_read_b128 v[232:235], v252 offset:16384\n\t" \
    "ds_read_b128 v[236:239], v252 offset:17408\n\t" \
    "v_xor_b32 v252, 0x1000, %[xa]\n\t" \
    "ds_read_b128 v[240:243], v252 offset:16384\n\t" \
    "ds_read_b128 v[212:215], v252 offset:17408\n\t" \
    "v_xor_b32 v252, 0x1800, %[xa]\n\t" \
    "ds_read_b128 v[200:203], v252 offset:16384\n\t" \
    "s_waitcnt lgkmcnt(6)\n\t" \
    "v_mfma_f32_16x16x32_bf16 v[220:223], v[8:11], v[224:227], 0\n\t" \
    "ds_read_b128 v[224:227], v252 offset:17408\n\t" \
    "s_waitcnt lgkmcnt(6)\n\t" \
    "v_mfma_f32_16x16x32_bf16 v[220:223], v[12:15], v[228:231], v[220:223]\n\t" \
    "v_and_b32 v252, 0x3ff, %[xa]\n\t" \
    "ds_read_b128 v[228:231], v252 offset:45056\n\t" \
    "s_waitcnt lgkmcnt(6)\n\t" \
    "v_mfma_f32_16x16x32_bf16 v[220:223], v[32:35], v[232:235], v[220:223]\n\t" \
    "ds_read_b128 v[232:235], v252 offset:46080\n\t" \
    "s_waitcnt lgkmcnt(6)\n\t" \
    "v_mfma_f32_16x16x32_bf16 v[220:223], v[36:39], v[236:239], v[220:223]\n\t" \
    "ds_read_b128 v[236:239], %[xa] offset:0\n\t" \
    "s_waitcnt lgkmcnt(6)\n\t" \
    "v_mfma_f32_16x16x32_bf16 v[220:223], v[48:51], v[240:243], v[220:223]\n\t" \
    "ds_read_b128 v[240:243], %[xa] offset:1024\n\t" \
    "s_waitcnt lgkmcnt(6)\n\t" \
    "v_mfma_f32_16x16x32_bf16 v[220:223], v[52:55], v[212:215], v[220:223]\n\t" \
    "v_xor_b32 v252, 0x800, %[xa]\n\t" \
    "ds_read_b128 v[212:215], v252 offset:0\n\t" \
    "s_waitcnt lgkmcnt(6)\n\t" \
    "v_mfma_f32_16x16x32_bf16 v[220:223], v[72:75], v[200:203], v[220:223]\n\t" \
    "ds_read_b128 v[200:203], v252 offset:1024\n\t" \
    "s_waitcnt lgkmcnt(6)\n\t" \
    "v_mfma_f32_16x16x32_bf16 v[220:223], v[76:79], v[224:227], v[220:223]\n\t" \
    "v_xor_b32 v252, 0x1000, %[xa]\n\t" \
    "ds_read_b128 v[224:227], v252 offset:0\n\t" \
    "s_waitcnt lgkmcnt(6)\n\t" \
    "v_mfma_f32_16x16x32_bf16 v[220:223], v[168:171], v[228:231], v[220:223]\n\t" \
    "ds_read_b128 v[228:231], v252 offset:1024\n\t" \
    "s_waitcnt lgkmcnt(6)\n\t" \
    "v_mfma_f32_16x16x32_bf16 v[220:223], v[172:175], v[232:235], v[220:223]\n\t" \
    "v_xor_b32 v252, 0x1800, %[xa]\n\t" \
    "ds_read_b128 v[232:235], v252 offset:0\n\t" \
    "s_waitcnt lgkmcnt(6)\n\t" \
    "v_mfma_f32_16x16x32_bf16 v[208:211], v[8:11], v[236:239], 0\n\t" \
    "ds_read_b128 v[236:239], v252 offset:1024\n\t" \
    "s_waitcnt lgkmcnt(6)\n\t" \
    "v_mfma_f32_16x16x32_bf16 v[208:211], v[12:15], v[240:243], v[208:211]\n\t" \
    "v_and_b32 v252, 0x3ff, %[xa]\n\t" \
    "ds_read_b128 v[240:243], v252 offset:40960\n\t" \
    "s_waitcnt lgkmcnt(6)\n\t" \
    "v_mfma_f32_16x16x32_bf16 v[208:211], v[32:35], v[212:215], v[208:211]\n\t" \
    "ds_read_b128 v[212:215], v252 offset:41984\n\t" \
    "s_waitcnt lgkmcnt(6)\n\t" \
    "v_mfma_f32_16x16x32_bf16 v[208:211], v[36:39], v[200:203], v[208:211]\n\t" \
    "s_waitcnt lgkmcnt(0)\n\t" \
    "s_barrier\n\t" ASM_ST \
    "v_xor_b32 v252, 0x2000, %[xa]\n\t" \
    "ds_read_b128 v[200:203], v252 offset:0\n\t" \
    "v_mfma_f32_16x16x32_bf16 v[208:211], v[48:51], v[224:227], v[208:211]\n\t" \
    "ds_read_b128 v[224:227], v252 offset:1024\n\t" \
    "v_mfma_f32_16x16x32_bf16 v[208:211], v[52:55], v[228:231], v[208:211]\n\t" \
    "v_xor_b32 v252, 0x2800, %[xa]\n\t" \
    "ds_read_b128 v[228:231], v252 offset:0\n\t" \
    "v_mfma_f32_16x16x32_bf16 v[208:211], v[72:75], v[232:235], v[208:211]\n\t" \
    "ds_read_b128 v[232:235], v252 offset:1024\n\t" \
    "v_mfma_f32_16x16x32_bf16 v[208:211], v[76:79], v[236:239], v[208:211]\n\t" \
    "v_xor_b32 v252, 0x3000, %[xa]\n\t" \
    "ds_read_b128 v[236:239], v252 offset:0\n\t" \
    "v_mfma_f32_16x16x32_bf16 v[208:211], v[168:171], v[240:243], v[208:211]\n\t" \
    "ds_read_b128 v[240:243], v252 offset:1024\n\t" \
    "v_mfma_f32_16x16x32_bf16 v[208:211], v[172:175], v[212:215], v[208:211]\n\t" \
    "v_xor_b32 v252, 0x3800, %[xa]\n\t" \
    "ds_read_b128 v[212:215], v252 offset:0\n\t" \
    "s_waitcnt lgkmcnt(6)\n\t" \
    "v_mfma_f32_16x16x32_bf16 v[208:211], v[88:91], v[200:203], v[208:211]\n\t" \
    "ds_read_b128 v[200:203], v252 offset:1024\n\t" \
    "s_waitcnt lgkmcnt(6)\n\t" \
    "v_mfma_f32_16x16x32_bf16 v[208:211], v[92:95], v[224:227], v[208:211]\n\t" \
    "v_and_b32 v252, 0x3ff, %[xa]\n\t" \
    "ds_read_b128 v[224:227], v252 offset:43008\n\t" \
    "s_waitcnt lgkmcnt(6)\n\t" \
    "v_mfma_f32_16x16x32_bf16 v[208:211], v[112:115], v[228:231], v[208:211]\n\t" \
    "ds_read_b128 v[228:231], v252 offset:44032\n\t" \
    "s_waitcnt lgkmcnt(6)\n\t" \
    "v_mfma_f32_16x16x32_bf16 v[208:211], v[116:119], v[232:235], v[208:211]\n\t" \
    "v_xor_b32 v252, 0x2000, %[xa]\n\t" \
    "ds_read_b128 v[232:235], v252 offset:16384\n\t" \
    "s_waitcnt lgkmcnt(6)\n\t" \
    "v_mfma_f32_16x16x32_bf16 v[208:211], v[128:131], v[236:239], v[208:211]\n\t" \
    "ds_read_b128 v[236:239], v252 offset:17408\n\t" \
    "s_waitcnt lgkmcnt(6)\n\t" \
    "v_mfma_f32_16x16x32_bf16 v[208:211], v[132:135], v[240:243], v[208:211]\n\t" \
    "v_xor_b32 v252, 0x2800, %[xa]\n\t" \
    "ds_read_b128 v[240:243], v252 offset:16384\n\t" \
    "s_waitcnt lgkmcnt(6)\n\t" \
    "v_mfma_f32_16x16x32_bf16 v[208:211], v[152:155], v[212:215], v[208:211]\n\t" \
    "ds_read_b128 v[212:215], v252 offset:17408\n\t" \
    "s_waitcnt lgkmcnt(6)\n\t" \
    "v_mfma_f32_16x16x32_bf16 v[208:211], v[156:159], v[200:203], v[208:211]\n\t" \
    "v_xor_b32 v252, 0x3000, %[xa]\n\t" \
    "ds_read_b128 v[200:203], v252 offset:16384\n\t" \
    "s_waitcnt lgkmcnt(6)\n\t" \
    "v_mfma_f32_16x16x32_bf16 v[208:211], v[192:195], v[224:227], v[208:211]\n\t" \
    "ds_read_b128 v[224:227], v252 offset:17408\n\t" \
    "s_waitcnt lgkmcnt(6)\n\t" \
    "v_mfma_f32_16x16x32_bf16 v[208:211], v[196:199], v[228:231], v[208:211]\n\t" \
    "v_xor_b32 v252, 0x3800, %[xa]\n\t" \
    "ds_read_b128 v[228:231], v252 offset:16384\n\t" \
    "s_waitcnt lgkmcnt(6)\n\t" \
    "v_mfma_f32_16x16x32_bf16 v[220:223], v[88:91], v[232:235], v[220:223]\n\t" \
    "ds_read_b128 v[232:235], v252 offset:17408\n\t" \
    "s_waitcnt lgkmcnt(6)\n\t" \
    "v_mfma_f32_16x16x32_bf16 v[220:223], v[92:95], v[236:239], v[220:223]\n\t" \
    "v_and_b32 v252, 0x3ff, %[xa]\n\t" \
    "ds_read_b128 v[236:239], v252 offset:47104\n\t" \
    "s_waitcnt lgkmcnt(6)\n\t" \
    "v_mfma_f32_16x16x32_bf16 v[220:223], v[112:115], v[240:243], v[220:223]\n\t" \
    "ds_read_b128 v[240:243], v252 offset:48128\n\t" \
    "v_mov_b64 v[244:245], 0\n\t" \
    "s_waitcnt lgkmcnt(6)\n\t" \
    "v_mfma_f32_16x16x32_bf16 v[220:223], v[116:119], v[212:215], v[220:223]\n\t" \
    "v_mov_b64 v[246:247], 0\n\t" \
    "s_waitcnt lgkmcnt(5)\n\t" \
    "v_mfma_f32_16x16x32_bf16 v[220:223], v[128:131], v[200:203], v[220:223]\n\t" \
    "v_mov_b64 v[248:249], 0\n\t" \
    "s_waitcnt lgkmcnt(4)\n\t" \
    "v_mfma_f32_16x16x32_bf16 v[220:223], v[132:135], v[224:227], v[220:223]\n\t" \
    "v_mov_b64 v[250:251], 0\n\t" \
    "s_waitcnt lgkmcnt(3)\n\t" \
    "v_mfma_f32_16x16x32_bf16 v[220:223], v[152:155], v[228:231], v[220:223]\n\t" \
    "v_add_u32 v252, %[qs], v252\n\t" \
    "v_mov_b64 v[224:225], 0\n\t" \
    "s_waitcnt lgkmcnt(2)\n\t" \
    "v_mfma_f32_16x16x32_bf16 v[220:223], v[156:159], v[232:235], v[220:223]\n\t" \
    "v_mov_b64 v[226:227], 0\n\t" \
    "v_cvt_pk_bf16_f32 v208, v208, v209\n\t" \
    "s_waitcnt lgkmcnt(1)\n\t" \
    "v_mfma_f32_16x16x32_bf16 v[220:223], v[192:195], v[236:239], v[220:223]\n\t" \
    "v_cvt_pk_bf16_f32 v209, v210, v211\n\t" \
    "ds_write_b64 v252, v[208:209] offset:2048\n\t" \
    "s_waitcnt lgkmcnt(1)\n\t" \
    "v_mfma_f32_16x16x32_bf16 v[220:223], v[196:199], v[240:243], v[220:223]\n\t" \
    "ds_read_b128 v[228:231], %[wl] offset:2048\n\t" \
    "ds_read_b128 v[232:235], %[wl] offset:3072\n\t" \
    "ds_read_b128 v[200:203], %[wl] offset:0\n\t" \
    "ds_read_b128 v[204:207], %[wl] offset:1024\n\t" \
    "s_nop 3\n\t" \
    "v_cvt_pk_bf16_f32 v220, v220, v221\n\t" \
    "v_cvt_pk_bf16_f32 v221, v222, v223\n\t" \
    "ds_write_b64 v252, v[220:221] offset:8192\n\t" \
    "s_waitcnt lgkmcnt(0)\n\t"
#define ASM_XG_V \
    "s_waitcnt lgkmcnt(0)\n\t" \
    "ds_read_b128 v[224:227], %[xa] offset:16384\n\t" \
    "ds_read_b128 v[228:231], %[xa] offset:17408\n\t" \
    "ds_read_b128 v[244:247], %[wl] offset:0\n\t" \
    "ds_read_b128 v[248:251], %[wl] offset:1024\n\t" \
    "ds_read_b128 v[232:235], %[xa] offset:0\n\t" \
    "ds_read_b128 v[236:239], %[xa] offset:1024\n\t" \
    "v_xor_b32 v252, 0x800, %[xa]\n\t" \
    "ds_read_b128 v[240:243], v252 offset:0\n\t" \
    "s_waitcnt lgkmcnt(6)\n\t" \
    "v_mfma_f32_16x16x32_bf16 v[220:223], v[8:11], v[224:227], 0\n\t" \
    "s_waitcnt lgkmcnt(5)\n\t" \
    "v_mfma_f32_16x16x32_bf16 v[220:223], v[12:15], v[228:231], v[220:223]\n\t" \
    "v_mfma_f32_16x16x32_bf16 v[216:219], v[0:3], v[224:227], 0\n\t" \
    "v_mfma_f32_16x16x32_bf16 v[216:219], v[4:7], v[228:231], v[216:219]\n\t" \
    "s_waitcnt lgkmcnt(4)\n\t" \
    "v_mfma_f32_16x16x32_bf16 v[212:215], v[244:247], v[224:227], 0\n\t" \
    "ds_read_b128 v[224:227], v252 offset:1024\n\t" \
    "s_waitcnt lgkmcnt(4)\n\t" \
    "v_mfma_f32_16x16x32_bf16 v[212:215], v[248:251], v[228:231], v[212:215]\n\t" \
    "ds_read_b128 v[228:231], v252 offset:16384\n\t" \
    "s_waitcnt lgkmcnt(4)\n\t" \
    "v_mfma_f32_16x16x32_bf16 v[200:203], v[244:247], v[232:235], 0\n\t" \
    "ds_read_b128 v[244:247], %[wl] offset:2048\n\t" \
    "s_waitcnt lgkmcnt(4)\n\t" \
    "v_mfma_f32_16x16x32_bf16 v[200:203], v[248:251], v[236:239], v[200:203]\n\t" \
    "ds_read_b128 v[248:251], %[wl] offset:3072\n\t" \
    "v_mfma_f32_16x16x32_bf16 v[204:207], v[0:3], v[232:235], 0\n\t" \
    "v_mfma_f32_16x16x32_bf16 v[204:207], v[4:7], v[236:239], v[204:207]\n\t" \
    "v_mfma_f32_16x16x32_bf16 v[208:211], v[8:11], v[232:235], 0\n\t" \
    "ds_read_b128 v[232:235], v252 offset:17408\n\t" \
    "v_mfma_f32_16x16x32_bf16 v[208:211], v[12:15], v[236:239], v[208:211]\n\t" \
    "v_xor_b32 v252, 0x1000, %[xa]\n\t" \
    "ds_read_b128 v[236:239], v252 offset:16384\n\t" \
    "s_waitcnt lgkmcnt(6)\n\t" \
    "v_mfma_f32_16x16x32_bf16 v[208:211], v[32:35], v[240:243], v[208:211]\n\t" \
    "s_waitcnt lgkmcnt(5)\n\t" \
    "v_mfma_f32_16x16x32_bf16 v[208:211], v[36:39], v[224:227], v[208:211]\n\t" \
    "v_mfma_f32_16x16x32_bf16 v[204:207], v[24:27], v[240:243], v[204:207]\n\t" \
    "v_mfma_f32_16x16x32_bf16 v[204:207], v[28:31], v[224:227], v[204:207]\n\t" \
    "v_mfma_f32_16x16x32_bf16 v[200:203], v[16:19], v[240:243], v[200:203]\n\t" \
    "ds_read_b128 v[240:243], v252 offset:17408\n\t" \
    "v_mfma_f32_16x16x32_bf16 v[200:203], v[20:23], v[224:227], v[200:203]\n\t" \
    "ds_read_b128 v[224:227], v252 offset:0\n\t" \
    "s_waitcnt lgkmcnt(6)\n\t" \
    "v_mfma_f32_16x16x32_bf16 v[212:215], v[16:19], v[228:231], v[212:215]\n\t" \
    "s_waitcnt lgkmcnt(3)\n\t" \
    "v_mfma_f32_16x16x32_bf16 v[212:215], v[20:23], v[232:235], v[212:215]\n\t" \
    "v_mfma_f32_16x16x32_bf16 v[216:219], v[24:27], v[228:231], v[216:219]\n\t" \
    "v_mfma_f32_16x16x32_bf16 v[216:219], v[28:31], v[232:235], v[216:219]\n\t" \
    "v_mfma_f32_16x16x32_bf16 v[220:223], v[32:35], v[228:231], v[220:223]\n\t" \
    "ds_read_b128 v[228:231], v252 offset:1024\n\t" \
    "v_mfma_f32_16x16x32_bf16 v[220:223], v[36:39], v[232:235], v[220:223]\n\t" \
    "v_xor_b32 v252, 0x1800, %[xa]\n\t" \
    "ds_read_b128 v[232:235], v252 offset:0\n\t" \
    "s_waitcnt lgkmcnt(4)\n\t" \
    "v_mfma_f32_16x16x32_bf16 v[220:223], v[48:51], v[236:239], v[220:223]\n\t" \
    "s_waitcnt lgkmcnt(3)\n\t" \
    "v_mfma_f32_16x16x32_bf16 v[220:223], v[52:55], v[240:243], v[220:223]\n\t" \
    "v_mfma_f32_16x16x32_bf16 v[216:219], v[40:43], v[236:239], v[216:219]\n\t" \
    "v_mfma_f32_16x16x32_bf16 v[216:219], v[44:47], v[240:243], v[216:219]\n\t" \
    "v_mfma_f32_16x16x32_bf16 v[212:215], v[244:247], v[236:239], v[212:215]\n\t" \
    "ds_read_b128 v[236:239], v252 offset:1024\n\t" \
    "v_mfma_f32_16x16x32_bf16 v[212:215], v[248:251], v[240:243], v[212:215]\n\t" \
    "ds_read_b128 v[240:243], v252 offset:16384\n\t" \
    "s_waitcnt lgkmcnt(4)\n\t" \
    "v_mfma_f32_16x16x32_bf16 v[200:203], v[244:247], v[224:227], v[200:203]\n\t" \
    "ds_read_b128 v[244:247], %[wl] offset:8192\n\t" \
    "s_waitcnt lgkmcnt(4)\n\t" \
    "v_mfma_f32_16x16x32_bf16 v[200:203], v[248:251], v[228:231], v[200:203]\n\t" \
    "ds_read_b128 v[248:251], %[wl] offset:9216\n\t" \
    "v_mfma_f32_16x16x32_bf16 v[204:207], v[40:43], v[224:227], v[204:207]\n\t" \
    "v_mfma_f32_16x16x32_bf16 v[204:207], v[44:47], v[228:231], v[204:207]\n\t" \
    "v_mfma_f32_16x16x32_bf16 v[208:211], v[48:51], v[224:227], v[208:211]\n\t" \
    "ds_read_b128 v[224:227], v252 offset:17408\n\t" \
    "v_mfma_f32_16x16x32_bf16 v[208:211], v[52:55], v[228:231], v[208:211]\n\t" \
    "v_and_b32 v252, 0x3ff, %[xa]\n\t" \
    "ds_read_b128 v[228:231], v252 offset:45056\n\t" \
    "s_waitcnt lgkmcnt(6)\n\t" \
    "v_mfma_f32_16x16x32_bf16 v[208:211], v[72:75], v[232:235], v[208:211]\n\t" \
    "s_waitcnt lgkmcnt(5)\n\t" \
    "v_mfma_f32_16x16x32_bf16 v[208:211], v[76:79], v[236:239], v[208:211]\n\t" \
    "v_mfma_f32_16x16x32_bf16 v[204:207], v[64:67], v[232:235], v[204:207]\n\t" \
    "v_mfma_f32_16x16x32_bf16 v[204:207], v[68:71], v[236:239], v[204:207]\n\t" \
    "v_mfma_f32_16x16x32_bf16 v[200:203], v[56:59], v[232:235], v[200:203]\n\t" \
    "ds_read_b128 v[232:235], v252 offset:46080\n\t" \
    "v_mfma_f32_16x16x32_bf16 v[200:203], v[60:63], v[236:239], v[200:203]\n\t" \
    "ds_read_b128 v[236:239], v252 offset:40960\n\t" \
    "s_waitcnt lgkmcnt(6)\n\t" \
    "v_mfma_f32_16x16x32_bf16 v[212:215], v[56:59], v[240:243], v[212:215]\n\t" \
    "s_waitcnt lgkmcnt(3)\n\t" \
    "v_mfma_f32_16x16x32_bf16 v[212:215], v[60:63], v[224:227], v[212:215]\n\t" \
    "v_mfma_f32_16x16x32_bf16 v[216:219], v[64:67], v[240:243], v[216:219]\n\t" \
    "v_mfma_f32_16x16x32_bf16 v[216:219], v[68:71], v[224:227], v[216:219]\n\t" \
    "v_mfma_f32_16x16x32_bf16 v[220:223], v[72:75], v[240:243], v[220:223]\n\t" \
    "ds_read_b128 v[240:243], v252 offset:41984\n\t" \
    "v_mfma_f32_16x16x32_bf16 v[220:223], v[76:79], v[224:227], v[220:223]\n\t" \
    "v_xor_b32 v252, 0x2000, %[xa]\n\t" \
    "ds_read_b128 v[224:227], v252 offset:0\n\t" \
    "s_waitcnt lgkmcnt(4)\n\t" \
    "v_mfma_f32_16x16x32_bf16 v[220:223], v[168:171], v[228:231], v[220:223]\n\t" \
    "s_waitcnt lgkmcnt(3)\n\t" \
    "v_mfma_f32_16x16x32_bf16 v[220:223], v[172:175], v[232:235], v[220:223]\n\t" \
    "v_mfma_f32_16x16x32_bf16 v[216:219], v[160:163], v[228:231], v[216:219]\n\t" \
    "v_mfma_f32_16x16x32_bf16 v[216:219], v[164:167], v[232:235], v[216:219]\n\t" \
    "v_mfma_f32_16x16x32_bf16 v[212:215], v[244:247], v[228:231], v[212:215]\n\t" \
    "ds_read_b128 v[228:231], v252 offset:1024\n\t" \
    "v_mfma_f32_16x16x32_bf16 v[212:215], v[248:251], v[232:235], v[212:215]\n\t" \
    "ds_read_b128 v[232:235], v252 offset:16384\n\t" \
    "s_waitcnt lgkmcnt(4)\n\t" \
    "v_mfma_f32_16x16x32_bf16 v[200:203], v[244:247], v[236:239], v[200:203]\n\t" \
    "ds_read_b128 v[244:247], %[wl] offset:4096\n\t" \
    "s_waitcnt lgkmcnt(4)\n\t" \
    "v_mfma_f32_16x16x32_bf16 v[200:203], v[248:251], v[240:243], v[200:203]\n\t" \
    "ds_read_b128 v[248:251], %[wl] offset:5120\n\t" \
    "v_mfma_f32_16x16x32_bf16 v[204:207], v[160:163], v[236:239], v[204:207]\n\t" \
    "v_mfma_f32_16x16x32_bf16 v[204:207], v[164:167], v[240:243], v[204:207]\n\t" \
    "v_mfma_f32_16x16x32_bf16 v[208:211], v[168:171], v[236:239], v[208:211]\n\t" \
    "ds_read_b128 v[236:239], v252 offset:17408\n\t" \
    "v_mfma_f32_16x16x32_bf16 v[208:211], v[172:175], v[240:243], v[208:211]\n\t" \
    "v_xor_b32 v252, 0x2800, %[xa]\n\t" \
    "ds_read_b128 v[240:243], v252 offset:16384\n\t" \
    "s_waitcnt lgkmcnt(6)\n\t" \
    "v_mfma_f32_16x16x32_bf16 v[208:211], v[88:91], v[224:227], v[208:211]\n\t" \
    "s_waitcnt lgkmcnt(5)\n\t" \
    "v_mfma_f32_16x16x32_bf16 v[208:211], v[92:95], v[228:231], v[208:211]\n\t" \
    "v_mfma_f32_16x16x32_bf16 v[204:207], v[80:83], v[224:227], v[204:207]\n\t" \
    "v_mfma_f32_16x16x32_bf16 v[204:207], v[84:87], v[228:231], v[204:207]\n\t" \
    "s_waitcnt lgkmcnt(3)\n\t" \
    "v_mfma_f32_16x16x32_bf16 v[200:203], v[244:247], v[224:227], v[200:203]\n\t" \
    "ds_read_b128 v[224:227], v252 offset:17408\n\t" \
    "s_waitcnt lgkmcnt(3)\n\t" \
    "v_mfma_f32_16x16x32_bf16 v[200:203], v[248:251], v[228:231], v[200:203]\n\t" \
    "ds_read_b128 v[228:231], v252 offset:0\n\t" \
    "v_mfma_f32_16x16x32_bf16 v[212:215], v[244:247], v[232:235], v[212:215]\n\t" \
    "ds_read_b128 v[244:247], %[wl] offset:6144\n\t" \
    "s_waitcnt lgkmcnt(4)\n\t" \
    "v_mfma_f32_16x16x32_bf16 v[212:215], v[248:251], v[236:239], v[212:215]\n\t" \
    "ds_read_b128 v[248:251], %[wl] offset:7168\n\t" \
    "v_mfma_f32_16x16x32_bf16 v[216:219], v[80:83], v[232:235], v[216:219]\n\t" \
    "v_mfma_f32_16x16x32_bf16 v[216:219], v[84:87], v[236:239], v[216:219]\n\t" \
    "v_mfma_f32_16x16x32_bf16 v[220:223], v[88:91], v[232:235], v[220:223]\n\t" \
    "ds_read_b128 v[232:235], v252 offset:1024\n\t" \
    "v_mfma_f32_16x16x32_bf16 v[220:223], v[92:95], v[236:239], v[220:223]\n\t" \
    "v_xor_b32 v252, 0x3000, %[xa]\n\t" \
    "ds_read_b128 v[236:239], v252 offset:0\n\t" \
    "s_waitcnt lgkmcnt(6)\n\t" \
    "v_mfma_f32_16x16x32_bf16 v[220:223], v[112:115], v[240:243], v[220:223]\n\t" \
    "s_waitcnt lgkmcnt(5)\n\t" \
    "v_mfma_f32_16x16x32_bf16 v[220:223], v[116:119], v[224:227], v[220:223]\n\t" \
    "v_mfma_f32_16x16x32_bf16 v[216:219], v[104:107], v[240:243], v[216:219]\n\t" \
    "v_mfma_f32_16x16x32_bf16 v[216:219], v[108:111], v[224:227], v[216:219]\n\t" \
    "v_mfma_f32_16x16x32_bf16 v[212:215], v[96:99], v[240:243], v[212:215]\n\t" \
    "ds_read_b128 v[240:243], v252 offset:1024\n\t" \
    "v_mfma_f32_16x16x32_bf16 v[212:215], v[100:103], v[224:227], v[212:215]\n\t" \
    "ds_read_b128 v[224:227], v252 offset:16384\n\t" \
    "s_waitcnt lgkmcnt(6)\n\t" \
    "v_mfma_f32_16x16x32_bf16 v[200:203], v[96:99], v[228:231], v[200:203]\n\t" \
    "s_waitcnt lgkmcnt(3)\n\t" \
    "v_mfma_f32_16x16x32_bf16 v[200:203], v[100:103], v[232:235], v[200:203]\n\t" \
    "v_mfma_f32_16x16x32_bf16 v[204:207], v[104:107], v[228:231], v[204:207]\n\t" \
    "v_mfma_f32_16x16x32_bf16 v[204:207], v[108:111], v[232:235], v[204:207]\n\t" \
    "v_mfma_f32_16x16x32_bf16 v[208:211], v[112:115], v[228:231], v[208:211]\n\t" \
    "ds_read_b128 v[228:231], v252 offset:17408\n\t" \
    "v_mfma_f32_16x16x32_bf16 v[208:211], v[116:119], v[232:235], v[208:211]\n\t" \
    "v_xor_b32 v252, 0x3800, %[xa]\n\t" \
    "ds_read_b128 v[232:235], v252 offset:16384\n\t" \
    "s_waitcnt lgkmcnt(4)\n\t" \
    "v_mfma_f32_16x16x32_bf16 v[208:211], v[128:131], v[236:239], v[208:211]\n\t" \
    "s_waitcnt lgkmcnt(3)\n\t" \
    "v_mfma_f32_16x16x32_bf16 v[208:211], v[132:135], v[240:243], v[208:211]\n\t" \
    "v_mfma_f32_16x16x32_bf16 v[204:207], v[120:123], v[236:239], v[204:207]\n\t" \
    "v_mfma_f32_16x16x32_bf16 v[204:207], v[124:127], v[240:243], v[204:207]\n\t" \
    "v_mfma_f32_16x16x32_bf16 v[200:203], v[244:247], v[236:239], v[200:203]\n\t" \
    "ds_read_b128 v[236:239], v252 offset:17408\n\t" \
    "v_mfma_f32_16x16x32_bf16 v[200:203], v[248:251], v[240:243], v[200:203]\n\t" \
    "ds_read_b128 v[240:243], v252 offset:0\n\t" \
    "s_waitcnt lgkmcnt(4)\n\t" \
    "v_mfma_f32_16x16x32_bf16 v[212:215], v[244:247], v[224:227], v[212:215]\n\t" \
    "s_waitcnt lgkmcnt(3)\n\t" \
    "v_mfma_f32_16x16x32_bf16 v[212:215], v[248:251], v[228:231], v[212:215]\n\t" \
    "v_mfma_f32_16x16x32_bf16 v[216:219], v[120:123], v[224:227], v[216:219]\n\t" \
    "v_mfma_f32_16x16x32_bf16 v[216:219], v[124:127], v[228:231], v[216:219]\n\t" \
    "v_mfma_f32_16x16x32_bf16 v[220:223], v[128:131], v[224:227], v[220:223]\n\t" \
    "ds_read_b128 v[224:227], v252 offset:1024\n\t" \
    "v_mfma_f32_16x16x32_bf16 v[220:223], v[132:135], v[228:231], v[220:223]\n\t" \
    "v_and_b32 v252, 0x3ff, %[xa]\n\t" \
    "ds_read_b128 v[228:231], v252 offset:43008\n\t" \
    "s_waitcnt lgkmcnt(4)\n\t" \
    "v_mfma_f32_16x16x32_bf16 v[220:223], v[152:155], v[232:235], v[220:223]\n\t" \
    "s_waitcnt lgkmcnt(3)\n\t" \
    "v_mfma_f32_16x16x32_bf16 v[220:223], v[156:159], v[236:239], v[220:223]\n\t" \
    "v_mfma_f32_16x16x32_bf16 v[216:219], v[144:147], v[232:235], v[216:219]\n\t" \
    "v_mfma_f32_16x16x32_bf16 v[216:219], v[148:151], v[236:239], v[216:219]\n\t" \
    "v_mfma_f32_16x16x32_bf16 v[212:215], v[136:139], v[232:235], v[212:215]\n\t" \
    "ds_read_b128 v[232:235], v252 offset:44032\n\t" \
    "v_mfma_f32_16x16x32_bf16 v[212:215], v[140:143], v[236:239], v[212:215]\n\t" \
    "ds_read_b128 v[236:239], v252 offset:47104\n\t" \
    "s_waitcnt lgkmcnt(4)\n\t" \
    "v_mfma_f32_16x16x32_bf16 v[200:203], v[136:139], v[240:243], v[200:203]\n\t" \
    "s_waitcnt lgkmcnt(3)\n\t" \
    "v_mfma_f32_16x16x32_bf16 v[200:203], v[140:143], v[224:227], v[200:203]\n\t" \
    "v_mfma_f32_16x16x32_bf16 v[204:207], v[144:147], v[240:243], v[204:207]\n\t" \
    "v_mfma_f32_16x16x32_bf16 v[204:207], v[148:151], v[224:227], v[204:207]\n\t" \
    "v_mfma_f32_16x16x32_bf16 v[208:211], v[152:155], v[240:243], v[208:211]\n\t" \
    "ds_read_b128 v[240:243], v252 offset:48128\n\t" \
    "v_mfma_f32_16x16x32_bf16 v[208:211], v[156:159], v[224:227], v[208:211]\n\t" \
    "s_waitcnt lgkmcnt(3)\n\t" \
    "v_mfma_f32_16x16x32_bf16 v[208:211], v[192:195], v[228:231], v[208:211]\n\t" \
    "v_add_u32 v252, %[qs], v252\n\t" \
    "s_waitcnt lgkmcnt(2)\n\t" \
    "v_mfma_f32_16x16x32_bf16 v[208:211], v[196:199], v[232:235], v[208:211]\n\t" \
    "v_mfma_f32_16x16x32_bf16 v[204:207], v[184:187], v[228:231], v[204:207]\n\t" \
    "v_mfma_f32_16x16x32_bf16 v[204:207], v[188:191], v[232:235], v[204:207]\n\t" \
    "v_mfma_f32_16x16x32_bf16 v[200:203], v[176:179], v[228:231], v[200:203]\n\t" \
    "v_mfma_f32_16x16x32_bf16 v[200:203], v[180:183], v[232:235], v[200:203]\n\t" \
    "s_waitcnt lgkmcnt(1)\n\t" \
    "v_mfma_f32_16x16x32_bf16 v[212:215], v[176:179], v[236:239], v[212:215]\n\t" \
    "v_cvt_pk_bf16_f32 v208, v208, v209\n\t" \
    "v_cvt_pk_bf16_f32 v209, v210, v211\n\t" \
    "s_waitcnt lgkmcnt(0)\n\t" \
    "v_mfma_f32_16x16x32_bf16 v[212:215], v[180:183], v[240:243], v[212:215]\n\t" \
    "ds_write_b64 v252, v[208:209] offset:2048\n\t" \
    "v_cvt_pk_bf16_f32 v204, v204, v205\n\t" \
    "v_mfma_f32_16x16x32_bf16 v[216:219], v[184:187], v[236:239], v[216:219]\n\t" \
    "v_cvt_pk_bf16_f32 v205, v206, v207\n\t" \
    "ds_write_b64 v252, v[204:205] offset:1024\n\t" \
    "v_mfma_f32_16x16x32_bf16 v[216:219], v[188:191], v[240:243], v[216:219]\n\t" \
    "v_cvt_pk_bf16_f32 v200, v200, v201\n\t" \
    "v_cvt_pk_bf16_f32 v201, v202, v203\n\t" \
    "v_mfma_f32_16x16x32_bf16 v[220:223], v[192:195], v[236:239], v[220:223]\n\t" \
    "ds_write_b64 v252, v[200:201] offset:0\n\t" \
    "v_mfma_f32_16x16x32_bf16 v[220:223], v[196:199], v[240:243], v[220:223]\n\t" \
    "v_cvt_pk_bf16_f32 v212, v212, v213\n\t" \
    "v_cvt_pk_bf16_f32 v213, v214, v215\n\t" \
    "ds_write_b64 v252, v[212:213] offset:6144\n\t" \
    "v_cvt_pk_bf16_f32 v216, v216, v217\n\t" \
    "v_cvt_pk_bf16_f32 v217, v218, v219\n\t" \
    "ds_write_b64 v252, v[216:217] offset:7168\n\t" \
    "s_nop 1\n\t" \
    "v_cvt_pk_bf16_f32 v220, v220, v221\n\t" \
    "v_cvt_pk_bf16_f32 v221, v222, v223\n\t" \
    "ds_write_b64 v252, v[220:221] offset:8192\n\t" \
    "s_waitcnt lgkmcnt(0)\n\t"
#define ASM_XG_H_LO \
    "s_waitcnt lgkmcnt(0)\n\t" \
    "ds_read_b128 v[224:227], %[xa] offset:16384\n\t" \
    "ds_read_b128 v[228:231], %[xa] offset:17408\n\t" \
    "ds_read_b128 v[244:247], %[wl] offset:0\n\t" \
    "ds_read_b128 v[248:251], %[wl] offset:1024\n\t" \
    "ds_read_b128 v[232:235], %[xa] offset:0\n\t" \
    "ds_read_b128 v[236:239], %[xa] offset:1024\n\t" \
    "v_xor_b32 v252, 0x800, %[xa]\n\t" \
    "ds_read_b128 v[240:243], v252 offset:0\n\t" \
    "s_waitcnt lgkmcnt(6)\n\t" \
    "v_mfma_f32_16x16x32_bf16 v[220:223], v[8:11], v[224:227], 0\n\t" \
    "s_waitcnt lgkmcnt(5)\n\t" \
    "v_mfma_f32_16x16x32_bf16 v[220:223], v[12:15], v[228:231], v[220:223]\n\t" \
    "v_mfma_f32_16x16x32_bf16 v[216:219], v[0:3], v[224:227], 0\n\t" \
    "v_mfma_f32_16x16x32_bf16 v[216:219], v[4:7], v[228:231], v[216:219]\n\t" \
    "s_waitcnt lgkmcnt(4)\n\t" \
    "v_mfma_f32_16x16x32_bf16 v[212:215], v[244:247], v[224:227], 0\n\t" \
    "ds_read_b128 v[224:227], v252 offset:1024\n\t" \
    "s_waitcnt lgkmcnt(4)\n\t" \
    "v_mfma_f32_16x16x32_bf16 v[212:215], v[248:251], v[228:231], v[212:215]\n\t" \
    "ds_read_b128 v[228:231], v252 offset:16384\n\t" \
    "s_waitcnt lgkmcnt(4)\n\t" \
    "v_mfma_f32_16x16x32_bf16 v[200:203], v[244:247], v[232:235], 0\n\t" \
    "ds_read_b128 v[244:247], %[wl] offset:2048\n\t" \
    "s_waitcnt lgkmcnt(4)\n\t" \
    "v_mfma_f32_16x16x32_bf16 v[200:203], v[248:251], v[236:239], v[200:203]\n\t" \
    "ds_read_b128 v[248:251], %[wl] offset:3072\n\t" \
    "v_mfma_f32_16x16x32_bf16 v[204:207], v[0:3], v[232:235], 0\n\t" \
    "v_mfma_f32_16x16x32_bf16 v[204:207], v[4:7], v[236:239], v[204:207]\n\t" \
    "v_mfma_f32_16x16x32_bf16 v[208:211], v[8:11], v[232:235], 0\n\t" \
    "ds_read_b128 v[232:235], v252 offset:17408\n\t" \
    "v_mfma_f32_16x16x32_bf16 v[208:211], v[12:15], v[236:239], v[208:211]\n\t" \
    "v_xor_b32 v252, 0x1000, %[xa]\n\t" \
    "ds_read_b128 v[236:239], v252 offset:16384\n\t" \
    "s_waitcnt lgkmcnt(6)\n\t" \
    "v_mfma_f32_16x16x32_bf16 v[208:211], v[32:35], v[240:243], v[208:211]\n\t" \
    "s_waitcnt lgkmcnt(5)\n\t" \
    "v_mfma_f32_16x16x32_bf16 v[208:211], v[36:39], v[224:227], v[208:211]\n\t" \
    "v_mfma_f32_16x16x32_bf16 v[204:207], v[24:27], v[240:243], v[204:207]\n\t" \
    "v_mfma_f32_16x16x32_bf16 v[204:207], v[28:31], v[224:227], v[204:207]\n\t" \
    "v_mfma_f32_16x16x32_bf16 v[200:203], v[16:19], v[240:243], v[200:203]\n\t" \
    "ds_read_b128 v[240:243], v252 offset:17408\n\t" \
    "v_mfma_f32_16x16x32_bf16 v[200:203], v[20:23], v[224:227], v[200:203]\n\t" \
    "ds_read_b128 v[224:227], v252 offset:0\n\t" \
    "s_waitcnt lgkmcnt(6)\n\t" \
    "v_mfma_f32_16x16x32_bf16 v[212:215], v[16:19], v[228:231], v[212:215]\n\t" \
    "s_waitcnt lgkmcnt(3)\n\t" \
    "v_mfma_f32_16x16x32_bf16 v[212:215], v[20:23], v[232:235], v[212:215]\n\t" \
    "v_mfma_f32_16x16x32_bf16 v[216:219], v[24:27], v[228:231], v[216:219]\n\t" \
    "v_mfma_f32_16x16x32_bf16 v[216:219], v[28:31], v[232:235], v[216:219]\n\t" \
    "v_mfma_f32_16x16x32_bf16 v[220:223], v[32:35], v[228:231], v[220:223]\n\t" \
    "ds_read_b128 v[228:231], v252 offset:1024\n\t" \
    "v_mfma_f32_16x16x32_bf16 v[220:223], v[36:39], v[232:235], v[220:223]\n\t" \
    "v_xor_b32 v252, 0x1800, %[xa]\n\t" \
    "ds_read_b128 v[232:235], v252 offset:0\n\t" \
    "s_waitcnt lgkmcnt(4)\n\t" \
    "v_mfma_f32_16x16x32_bf16 v[220:223], v[48:51], v[236:239], v[220:223]\n\t" \
    "s_waitcnt lgkmcnt(3)\n\t" \
    "v_mfma_f32_16x16x32_bf16 v[220:223], v[52:55], v[240:243], v[220:223]\n\t" \
    "v_mfma_f32_16x16x32_bf16 v[216:219], v[40:43], v[236:239], v[216:219]\n\t" \
    "v_mfma_f32_16x16x32_bf16 v[216:219], v[44:47], v[240:243], v[216:219]\n\t" \
    "v_mfma_f32_16x16x32_bf16 v[212:215], v[244:247], v[236:239], v[212:215]\n\t" \
    "ds_read_b128 v[236:239], v252 offset:1024\n\t" \
    "v_mfma_f32_16x16x32_bf16 v[212:215], v[248:251], v[240:243], v[212:215]\n\t" \
    "ds_read_b128 v[240:243], v252 offset:16384\n\t" \
    "s_waitcnt lgkmcnt(4)\n\t" \
    "v_mfma_f32_16x16x32_bf16 v[200:203], v[244:247], v[224:227], v[200:203]\n\t" \
    "ds_read_b128 v[244:247], %[wl] offset:8192\n\t" \
    "s_waitcnt lgkmcnt(4)\n\t" \
    "v_mfma_f32_16x16x32_bf16 v[200:203], v[248:251], v[228:231], v[200:203]\n\t" \
    "ds_read_b128 v[248:251], %[wl] offset:9216\n\t" \
    "v_mfma_f32_16x16x32_bf16 v[204:207], v[40:43], v[224:227], v[204:207]\n\t" \
    "v_mfma_f32_16x16x32_bf16 v[204:207], v[44:47], v[228:231], v[204:207]\n\t" \
    "v_mfma_f32_16x16x32_bf16 v[208:211], v[48:51], v[224:227], v[208:211]\n\t" \
    "ds_read_b128 v[224:227], v252 offset:17408\n\t" \
    "v_mfma_f32_16x16x32_bf16 v[208:211], v[52:55], v[228:231], v[208:211]\n\t" \
    "v_and_b32 v252, 0x3ff, %[xa]\n\t" \
    "ds_read_b128 v[228:231], v252 offset:45056\n\t" \
    "s_waitcnt lgkmcnt(6)\n\t" \
    "v_mfma_f32_16x16x32_bf16 v[208:211], v[72:75], v[232:235], v[208:211]\n\t" \
    "s_waitcnt lgkmcnt(5)\n\t" \
    "v_mfma_f32_16x16x32_bf16 v[208:211], v[76:79], v[236:239], v[208:211]\n\t" \
    "v_mfma_f32_16x16x32_bf16 v[204:207], v[64:67], v[232:235], v[204:207]\n\t" \
    "v_mfma_f32_16x16x32_bf16 v[204:207], v[68:71], v[236:239], v[204:207]\n\t" \
    "v_mfma_f32_16x16x32_bf16 v[200:203], v[56:59], v[232:235], v[200:203]\n\t" \
    "ds_read_b128 v[232:235], v252 offset:46080\n\t" \
    "v_mfma_f32_16x16x32_bf16 v[200:203], v[60:63], v[236:239], v[200:203]\n\t" \
    "ds_read_b128 v[236:239], v252 offset:40960\n\t" \
    "s_waitcnt lgkmcnt(6)\n\t" \
    "v_mfma_f32_16x16x32_bf16 v[212:215], v[56:59], v[240:243], v[212:215]\n\t" \
    "s_waitcnt lgkmcnt(3)\n\t" \
    "v_mfma_f32_16x16x32_bf16 v[212:215], v[60:63], v[224:227], v[212:215]\n\t" \
    "v_mfma_f32_16x16x32_bf16 v[216:219], v[64:67], v[240:243], v[216:219]\n\t" \
    "v_mfma_f32_16x16x32_bf16 v[216:219], v[68:71], v[224:227], v[216:219]\n\t" \
    "v_mfma_f32_16x16x32_bf16 v[220:223], v[72:75], v[240:243], v[220:223]\n\t" \
    "ds_read_b128 v[240:243], v252 offset:41984\n\t" \
    "v_mfma_f32_16x16x32_bf16 v[220:223], v[76:79], v[224:227], v[220:223]\n\t" \
    "s_waitcnt lgkmcnt(0)\n\t" \
    "s_barrier\n\t" ASM_ST \
    "v_xor_b32 v252, 0x2000, %[xa]\n\t" \
    "ds_read_b128 v[224:227], v252 offset:0\n\t" \
    "v_mfma_f32_16x16x32_bf16 v[220:223], v[168:171], v[228:231], v[220:223]\n\t" \
    "v_mfma_f32_16x16x32_bf16 v[220:223], v[172:175], v[232:235], v[220:223]\n\t" \
    "v_mfma_f32_16x16x32_bf16 v[216:219], v[160:163], v[228:231], v[216:219]\n\t" \
    "v_mfma_f32_16x16x32_bf16 v[216:219], v[164:167], v[232:235], v[216:219]\n\t" \
    "v_mfma_f32_16x16x32_bf16 v[212:215], v[244:247], v[228:231], v[212:215]\n\t" \
    "ds_read_b128 v[228:231], v252 offset:1024\n\t" \
    "v_mfma_f32_16x16x32_bf16 v[212:215], v[248:251], v[232:235], v[212:215]\n\t" \
    "ds_read_b128 v[232:235], v252 offset:16384\n\t" \
    "v_mfma_f32_16x16x32_bf16 v[200:203], v[244:247], v[236:239], v[200:203]\n\t" \
    "ds_read_b128 v[244:247], %[wl] offset:4096\n\t" \
    "v_mfma_f32_16x16x32_bf16 v[200:203], v[248:251], v[240:243], v[200:203]\n\t" \
    "ds_read_b128 v[248:251], %[wl] offset:5120\n\t" \
    "v_mfma_f32_16x16x32_bf16 v[204:207], v[160:163], v[236:239], v[204:207]\n\t" \
    "v_mfma_f32_16x16x32_bf16 v[204:207], v[164:167], v[240:243], v[204:207]\n\t" \
    "v_mfma_f32_16x16x32_bf16 v[208:211], v[168:171], v[236:239], v[208:211]\n\t" \
    "ds_read_b128 v[236:239], v252 offset:17408\n\t" \
    "v_mfma_f32_16x16x32_bf16 v[208:211], v[172:175], v[240:243], v[208:211]\n\t" \
    "v_xor_b32 v252, 0x2800, %[xa]\n\t" \
    "ds_read_b128 v[240:243], v252 offset:16384\n\t" \
    "s_waitcnt lgkmcnt(6)\n\t" \
    "v_mfma_f32_16x16x32_bf16 v[208:211], v[88:91], v[224:227], v[208:211]\n\t" \
    "s_waitcnt lgkmcnt(5)\n\t" \
    "v_mfma_f32_16x16x32_bf16 v[208:211], v[92:95], v[228:231], v[208:211]\n\t" \
    "v_mfma_f32_16x16x32_bf16 v[204:207], v[80:83], v[224:227], v[204:207]\n\t" \
    "v_mfma_f32_16x16x32_bf16 v[204:207], v[84:87], v[228:231], v[204:207]\n\t" \
    "s_waitcnt lgkmcnt(3)\n\t" \
    "v_mfma_f32_16x16x32_bf16 v[200:203], v[244:247], v[224:227], v[200:203]\n\t" \
    "ds_read_b128 v[224:227], v252 offset:17408\n\t" \
    "s_waitcnt lgkmcnt(3)\n\t" \
    "v_mfma_f32_16x16x32_bf16 v[200:203], v[248:251], v[228:231], v[200:203]\n\t" \
    "ds_read_b128 v[228:231], v252 offset:0\n\t" \
    "v_mfma_f32_16x16x32_bf16 v[212:215], v[244:247], v[232:235], v[212:215]\n\t" \
    "ds_read_b128 v[244:247], %[wl] offset:6144\n\t" \
    "s_waitcnt lgkmcnt(4)\n\t" \
    "v_mfma_f32_16x16x32_bf16 v[212:215], v[248:251], v[236:239], v[212:215]\n\t" \
    "ds_read_b128 v[248:251], %[wl] offset:7168\n\t" \
    "v_mfma_f32_16x16x32_bf16 v[216:219], v[80:83], v[232:235], v[216:219]\n\t" \
    "v_mfma_f32_16x16x32_bf16 v[216:219], v[84:87], v[236:239], v[216:219]\n\t" \
    "v_mfma_f32_16x16x32_bf16 v[220:223], v[88:91], v[232:235], v[220:223]\n\t" \
    "ds_read_b128 v[232:235], v252 offset:1024\n\t" \
    "v_mfma_f32_16x16x32_bf16 v[220:223], v[92:95], v[236:239], v[220:223]\n\t" \
    "v_xor_b32 v252, 0x3000, %[xa]\n\t" \
    "ds_read_b128 v[236:239], v252 offset:0\n\t" \
    "s_waitcnt lgkmcnt(6)\n\t" \
    "v_mfma_f32_16x16x32_bf16 v[220:223], v[112:115], v[240:243], v[220:223]\n\t" \
    "s_waitcnt lgkmcnt(5)\n\t" \
    "v_mfma_f32_16x16x32_bf16 v[220:223], v[116:119], v[224:227], v[220:223]\n\t" \
    "v_mfma_f32_16x16x32_bf16 v[216:219], v[104:107], v[240:243], v[216:219]\n\t" \
    "v_mfma_f32_16x16x32_bf16 v[216:219], v[108:111], v[224:227], v[216:219]\n\t" \
    "v_mfma_f32_16x16x32_bf16 v[212:215], v[96:99], v[240:243], v[212:215]\n\t" \
    "ds_read_b128 v[240:243], v252 offset:1024\n\t" \
    "v_mfma_f32_16x16x32_bf16 v[212:215], v[100:103], v[224:227], v[212:215]\n\t" \
    "ds_read_b128 v[224:227], v252 offset:16384\n\t" \
    "s_waitcnt lgkmcnt(6)\n\t" \
    "v_mfma_f32_16x16x32_bf16 v[200:203], v[96:99], v[228:231], v[200:203]\n\t" \
    "s_waitcnt lgkmcnt(3)\n\t" \
    "v_mfma_f32_16x16x32_bf16 v[200:203], v[100:103], v[232:235], v[200:203]\n\t" \
    "v_mfma_f32_16x16x32_bf16 v[204:207], v[104:107], v[228:231], v[204:207]\n\t" \
    "v_mfma_f32_16x16x32_bf16 v[204:207], v[108:111], v[232:235], v[204:207]\n\t" \
    "v_mfma_f32_16x16x32_bf16 v[208:211], v[112:115], v[228:231], v[208:211]\n\t" \
    "ds_read_b128 v[228:231], v252 offset:17408\n\t" \
    "v_mfma_f32_16x16x32_bf16 v[208:211], v[116:119], v[232:235], v[208:211]\n\t" \
    "v_xor_b32 v252, 0x3800, %[xa]\n\t" \
    "ds_read_b128 v[232:235], v252 offset:16384\n\t" \
    "s_waitcnt lgkmcnt(4)\n\t" \
    "v_mfma_f32_16x16x32_bf16 v[208:211], v[128:131], v[236:239], v[208:211]\n\t" \
    "s_waitcnt lgkmcnt(3)\n\t" \
    "v_mfma_f32_16x16x32_bf16 v[208:211], v[132:135], v[240:243], v[208:211]\n\t" \
    "v_mfma_f32_16x16x32_bf16 v[204:207], v[120:123], v[236:239], v[204:207]\n\t" \
    "v_mfma_f32_16x16x32_bf16 v[204:207], v[124:127], v[240:243], v[204:207]\n\t" \
    "v_mfma_f32_16x16x32_bf16 v[200:203], v[244:247], v[236:239], v[200:203]\n\t" \
    "ds_read_b128 v[236:239], v252 offset:17408\n\t" \
    "v_mfma_f32_16x16x32_bf16 v[200:203], v[248:251], v[240:243], v[200:203]\n\t" \
    "ds_read_b128 v[240:243], v252 offset:0\n\t" \
    "s_waitcnt lgkmcnt(4)\n\t" \
    "v_mfma_f32_16x16x32_bf16 v[212:215], v[244:247], v[224:227], v[212:215]\n\t" \
    "s_waitcnt lgkmcnt(3)\n\t" \
    "v_mfma_f32_16x16x32_bf16 v[212:215], v[248:251], v[228:231], v[212:215]\n\t" \
    "v_mfma_f32_16x16x32_bf16 v[216:219], v[120:123], v[224:227], v[216:219]\n\t" \
    "v_mfma_f32_16x16x32_bf16 v[216:219], v[124:127], v[228:231], v[216:219]\n\t" \
    "v_mfma_f32_16x16x32_bf16 v[220:223], v[128:131], v[224:227], v[220:223]\n\t" \
    "ds_read_b128 v[224:227], v252 offset:1024\n\t" \
    "v_mfma_f32_16x16x32_bf16 v[220:223], v[132:135], v[228:231], v[220:223]\n\t" \
    "v_and_b32 v252, 0x3ff, %[xa]\n\t" \
    "ds_read_b128 v[228:231], v252 offset:43008\n\t" \
    "s_waitcnt lgkmcnt(4)\n\t" \
    "v_mfma_f32_16x16x32_bf16 v[220:223], v[152:155], v[232:235], v[220:223]\n\t" \
    "s_waitcnt lgkmcnt(3)\n\t" \
    "v_mfma_f32_16x16x32_bf16 v[220:223], v[156:159], v[236:239], v[220:223]\n\t" \
    "v_mfma_f32_16x16x32_bf16 v[216:219], v[144:147], v[232:235], v[216:219]\n\t" \
    "v_mfma_f32_16x16x32_bf16 v[216:219], v[148:151], v[236:239], v[216:219]\n\t" \
    "v_mfma_f32_16x16x32_bf16 v[212:215], v[136:139], v[232:235], v[212:215]\n\t" \
    "ds_read_b128 v[232:235], v252 offset:44032\n\t" \
    "v_mfma_f32_16x16x32_bf16 v[212:215], v[140:143], v[236:239], v[212:215]\n\t" \
    "ds_read_b128 v[236:239], v252 offset:47104\n\t" \
    "s_waitcnt lgkmcnt(4)\n\t" \
    "v_mfma_f32_16x16x32_bf16 v[200:203], v[136:139], v[240:243], v[200:203]\n\t" \
    "s_waitcnt lgkmcnt(3)\n\t" \
    "v_mfma_f32_16x16x32_bf16 v[200:203], v[140:143], v[224:227], v[200:203]\n\t" \
    "v_mfma_f32_16x16x32_bf16 v[204:207], v[144:147], v[240:243], v[204:207]\n\t" \
    "v_mfma_f32_16x16x32_bf16 v[204:207], v[148:151], v[224:227], v[204:207]\n\t" \
    "v_mfma_f32_16x16x32_bf16 v[208:211], v[152:155], v[240:243], v[208:211]\n\t" \
    "ds_read_b128 v[240:243], v252 offset:48128\n\t" \
    "v_mfma_f32_16x16x32_bf16 v[208:211], v[156:159], v[224:227], v[208:211]\n\t" \
    "s_waitcnt lgkmcnt(3)\n\t" \
    "v_mfma_f32_16x16x32_bf16 v[208:211], v[192:195], v[228:231], v[208:211]\n\t" \
    "v_add_u32 v252, %[qs], v252\n\t" \
    "s_waitcnt lgkmcnt(2)\n\t" \
    "v_mfma_f32_16x16x32_bf16 v[208:211], v[196:199], v[232:235], v[208:211]\n\t" \
    "v_mfma_f32_16x16x32_bf16 v[204:207], v[184:187], v[228:231], v[204:207]\n\t" \
    "v_mfma_f32_16x16x32_bf16 v[204:207], v[188:191], v[232:235], v[204:207]\n\t" \
    "v_mfma_f32_16x16x32_bf16 v[200:203], v[176:179], v[228:231], v[200:203]\n\t" \
    "v_mfma_f32_16x16x32_bf16 v[200:203], v[180:183], v[232:235], v[200:203]\n\t" \
    "s_waitcnt lgkmcnt(1)\n\t" \
    "v_mfma_f32_16x16x32_bf16 v[212:215], v[176:179], v[236:239], v[212:215]\n\t" \
    "v_cvt_pk_bf16_f32 v208, v208, v209\n\t" \
    "v_cvt_pk_bf16_f32 v209, v210, v211\n\t" \
    "s_waitcnt lgkmcnt(0)\n\t" \
    "v_mfma_f32_16x16x32_bf16 v[212:215], v[180:183], v[240:243], v[212:215]\n\t" \
    "ds_write_b64 v252, v[208:209] offset:2048\n\t" \
    "v_cvt_pk_bf16_f32 v204, v204, v205\n\t" \
    "v_mfma_f32_16x16x32_bf16 v[216:219], v[184:187], v[236:239], v[216:219]\n\t" \
    "v_cvt_pk_bf16_f32 v205, v206, v207\n\t" \
    "ds_write_b64 v252, v[204:205] offset:1024\n\t" \
    "v_mfma_f32_16x16x32_bf16 v[216:219], v[188:191], v[240:243], v[216:219]\n\t" \
    "v_cvt_pk_bf16_f32 v200, v200, v201\n\t" \
    "v_cvt_pk_bf16_f32 v201, v202, v203\n\t" \
    "v_mfma_f32_16x16x32_bf16 v[220:223], v[192:195], v[236:239], v[220:223]\n\t" \
    "ds_write_b64 v252, v[200:201] offset:0\n\t" \
    "v_mfma_f32_16x16x32_bf16 v[220:223], v[196:199], v[240:243], v[220:223]\n\t" \
    "v_cvt_pk_bf16_f32 v212, v212, v213\n\t" \
    "v_cvt_pk_bf16_f32 v213, v214, v215\n\t" \
    "ds_write_b64 v252, v[212:213] offset:6144\n\t" \
    "v_cvt_pk_bf16_f32 v216, v216, v217\n\t" \
    "v_cvt_pk_bf16_f32 v217, v218, v219\n\t" \
    "ds_write_b64 v252, v[216:217] offset:7168\n\t" \
    "s_nop 1\n\t" \
    "v_cvt_pk_bf16_f32 v220, v220, v221\n\t" \
    "v_cvt_pk_bf16_f32 v221, v222, v223\n\t" \
    "ds_write_b64 v252, v[220:221] offset:8192\n\t" \
    "s_waitcnt lgkmcnt(0)\n\t"
#define ASM_XG_H_HI \
    "s_waitcnt lgkmcnt(0)\n\t" \
    "v_xor_b32 v252, 0x2000, %[xa]\n\t" \
    "ds_read_b128 v[224:227], v252 offset:16384\n\t" \
    "ds_read_b128 v[228:231], v252 offset:17408\n\t" \
    "ds_read_b128 v[244:247], %[wl] offset:4096\n\t" \
    "ds_read_b128 v[248:251], %[wl] offset:5120\n\t" \
    "ds_read_b128 v[232:235], v252 offset:0\n\t" \
    "ds_read_b128 v[236:239], v252 offset:1024\n\t" \
    "v_xor_b32 v252, 0x2800, %[xa]\n\t" \
    "ds_read_b128 v[240:243], v252 offset:0\n\t" \
    "s_waitcnt lgkmcnt(6)\n\t" \
    "v_mfma_f32_16x16x32_bf16 v[220:223], v[88:91], v[224:227], 0\n\t" \
    "s_waitcnt lgkmcnt(5)\n\t" \
    "v_mfma_f32_16x16x32_bf16 v[220:223], v[92:95], v[228:231], v[220:223]\n\t" \
    "v_mfma_f32_16x16x32_bf16 v[216:219], v[80:83], v[224:227], 0\n\t" \
    "v_mfma_f32_16x16x32_bf16 v[216:219], v[84:87], v[228:231], v[216:219]\n\t" \
    "s_waitcnt lgkmcnt(4)\n\t" \
    "v_mfma_f32_16x16x32_bf16 v[212:215], v[244:247], v[224:227], 0\n\t" \
    "ds_read_b128 v[224:227], v252 offset:1024\n\t" \
    "s_waitcnt lgkmcnt(4)\n\t" \
    "v_mfma_f32_16x16x32_bf16 v[212:215], v[248:251], v[228:231], v[212:215]\n\t" \
    "ds_read_b128 v[228:231], v252 offset:16384\n\t" \
    "s_waitcnt lgkmcnt(4)\n\t" \
    "v_mfma_f32_16x16x32_bf16 v[200:203], v[244:247], v[232:235], 0\n\t" \
    "ds_read_b128 v[244:247], %[wl] offset:6144\n\t" \
    "s_waitcnt lgkmcnt(4)\n\t" \
    "v_mfma_f32_16x16x32_bf16 v[200:203], v[248:251], v[236:239], v[200:203]\n\t" \
    "ds_read_b128 v[248:251], %[wl] offset:7168\n\t" \
    "v_mfma_f32_16x16x32_bf16 v[204:207], v[80:83], v[232:235], 0\n\t" \
    "v_mfma_f32_16x16x32_bf16 v[204:207], v[84:87], v[236:239], v[204:207]\n\t" \
    "v_mfma_f32_16x16x32_bf16 v[208:211], v[88:91], v[232:235], 0\n\t" \
    "ds_read_b128 v[232:235], v252 offset:17408\n\t" \
    "v_mfma_f32_16x16x32_bf16 v[208:211], v[92:95], v[236:239], v[208:211]\n\t" \
    "v_xor_b32 v252, 0x3000, %[xa]\n\t" \
    "ds_read_b128 v[236:239], v252 offset:16384\n\t" \
    "s_waitcnt lgkmcnt(6)\n\t" \
    "v_mfma_f32_16x16x32_bf16 v[208:211], v[112:115], v[240:243], v[208:211]\n\t" \
    "s_waitcnt lgkmcnt(5)\n\t" \
    "v_mfma_f32_16x16x32_bf16 v[208:211], v[116:119], v[224:227], v[208:211]\n\t" \
    "v_mfma_f32_16x16x32_bf16 v[204:207], v[104:107], v[240:243], v[204:207]\n\t" \
    "v_mfma_f32_16x16x32_bf16 v[204:207], v[108:111], v[224:227], v[204:207]\n\t" \
    "v_mfma_f32_16x16x32_bf16 v[200:203], v[96:99], v[240:243], v[200:203]\n\t" \
    "ds_read_b128 v[240:243], v252 offset:17408\n\t" \
    "v_mfma_f32_16x16x32_bf16 v[200:203], v[100:103], v[224:227], v[200:203]\n\t" \
    "s_nop 3\n\t" \
    "v_mov_b32 v224, %[tv]\n\t" \
    "s_waitcnt vmcnt(5)\n\t" \
    "buffer_store_dword v224, %[fv], %[rr], %[fp] offen sc1\n\t" \
    "ds_read_b128 v[224:227], v252 offset:0\n\t" \
    "s_waitcnt lgkmcnt(6)\n\t" \
    "v_mfma_f32_16x16x32_bf16 v[212:215], v[96:99], v[228:231], v[212:215]\n\t" \
    "s_waitcnt lgkmcnt(3)\n\t" \
    "v_mfma_f32_16x16x32_bf16 v[212:215], v[100:103], v[232:235], v[212:215]\n\t" \
    "v_mfma_f32_16x16x32_bf16 v[216:219], v[104:107], v[228:231], v[216:219]\n\t" \
    "v_mfma_f32_16x16x32_bf16 v[216:219], v[108:111], v[232:235], v[216:219]\n\t" \
    "v_mfma_f32_16x16x32_bf16 v[220:223], v[112:115], v[228:231], v[220:223]\n\t" \
    "ds_read_b128 v[228:231], v252 offset:1024\n\t" \
    "v_mfma_f32_16x16x32_bf16 v[220:223], v[116:119], v[232:235], v[220:223]\n\t" \
    "v_xor_b32 v252, 0x3800, %[xa]\n\t" \
    "ds_read_b128 v[232:235], v252 offset:0\n\t" \
    "s_waitcnt lgkmcnt(4)\n\t" \
    "v_mfma_f32_16x16x32_bf16 v[220:223], v[128:131], v[236:239], v[220:223]\n\t" \
    "s_waitcnt lgkmcnt(3)\n\t" \
    "v_mfma_f32_16x16x32_bf16 v[220:223], v[132:135], v[240:243], v[220:223]\n\t" \
    "v_mfma_f32_16x16x32_bf16 v[216:219], v[120:123], v[236:239], v[216:219]\n\t" \
    "v_mfma_f32_16x16x32_bf16 v[216:219], v[124:127], v[240:243], v[216:219]\n\t" \
    "v_mfma_f32_16x16x32_bf16 v[212:215], v[244:247], v[236:239], v[212:215]\n\t" \
    "ds_read_b128 v[236:239], v252 offset:1024\n\t" \
    "v_mfma_f32_16x16x32_bf16 v[212:215], v[248:251], v[240:243], v[212:215]\n\t" \
    "ds_read_b128 v[240:243], v252 offset:16384\n\t" \
    "s_waitcnt lgkmcnt(4)\n\t" \
    "v_mfma_f32_16x16x32_bf16 v[200:203], v[244:247], v[224:227], v[200:203]\n\t" \
    "ds_read_b128 v[244:247], %[wl] offset:8192\n\t" \
    "s_waitcnt lgkmcnt(4)\n\t" \
    "v_mfma_f32_16x16x32_bf16 v[200:203], v[248:251], v[228:231], v[200:203]\n\t" \
    "ds_read_b128 v[248:251], %[wl] offset:9216\n\t" \
    "v_mfma_f32_16x16x32_bf16 v[204:207], v[120:123], v[224:227], v[204:207]\n\t" \
    "v_mfma_f32_16x16x32_bf16 v[204:207], v[124:127], v[228:231], v[204:207]\n\t" \
    "v_mfma_f32_16x16x32_bf16 v[208:211], v[128:131], v[224:227], v[208:211]\n\t" \
    "ds_read_b128 v[224:227], v252 offset:17408\n\t" \
    "v_mfma_f32_16x16x32_bf16 v[208:211], v[132:135], v[228:231], v[208:211]\n\t" \
    "v_and_b32 v252, 0x3ff, %[xa]\n\t" \
    "ds_read_b128 v[228:231], v252 offset:45056\n\t" \
    "s_waitcnt lgkmcnt(6)\n\t" \
    "v_mfma_f32_16x16x32_bf16 v[208:211], v[152:155], v[232:235], v[208:211]\n\t" \
    "s_waitcnt lgkmcnt(5)\n\t" \
    "v_mfma_f32_16x16x32_bf16 v[208:211], v[156:159], v[236:239], v[208:211]\n\t" \
    "v_mfma_f32_16x16x32_bf16 v[204:207], v[144:147], v[232:235], v[204:207]\n\t" \
    "v_mfma_f32_16x16x32_bf16 v[204:207], v[148:151], v[236:239], v[204:207]\n\t" \
    "v_mfma_f32_16x16x32_bf16 v[200:203], v[136:139], v[232:235], v[200:203]\n\t" \
    "ds_read_b128 v[232:235], v252 offset:46080\n\t" \
    "v_mfma_f32_16x16x32_bf16 v[200:203], v[140:143], v[236:239], v[200:203]\n\t" \
    "ds_read_b128 v[236:239], v252 offset:40960\n\t" \
    "s_waitcnt lgkmcnt(6)\n\t" \
    "v_mfma_f32_16x16x32_bf16 v[212:215], v[136:139], v[240:243], v[212:215]\n\t" \
    "s_waitcnt lgkmcnt(3)\n\t" \
    "v_mfma_f32_16x16x32_bf16 v[212:215], v[140:143], v[224:227], v[212:215]\n\t" \
    "v_mfma_f32_16x16x32_bf16 v[216:219], v[144:147], v[240:243], v[216:219]\n\t" \
    "v_mfma_f32_16x16x32_bf16 v[216:219], v[148:151], v[224:227], v[216:219]\n\t" \
    "v_mfma_f32_16x16x32_bf16 v[220:223], v[152:155], v[240:243], v[220:223]\n\t" \
    "ds_read_b128 v[240:243], v252 offset:41984\n\t" \
    "v_mfma_f32_16x16x32_bf16 v[220:223], v[156:159], v[224:227], v[220:223]\n\t" \
    "s_waitcnt vmcnt(1) lgkmcnt(0)\n\t" \
    "s_barrier\n\t" ASM_ST \
    "ds_read_b128 v[224:227], %[xa] offset:0\n\t" \
    "v_mfma_f32_16x16x32_bf16 v[220:223], v[168:171], v[228:231], v[220:223]\n\t" \
    "v_mfma_f32_16x16x32_bf16 v[220:223], v[172:175], v[232:235], v[220:223]\n\t" \
    "v_mfma_f32_16x16x32_bf16 v[216:219], v[160:163], v[228:231], v[216:219]\n\t" \
    "v_mfma_f32_16x16x32_bf16 v[216:219], v[164:167], v[232:235], v[216:219]\n\t" \
    "v_mfma_f32_16x16x32_bf16 v[212:215], v[244:247], v[228:231], v[212:215]\n\t" \
    "ds_read_b128 v[228:231], %[xa] offset:1024\n\t" \
    "v_mfma_f32_16x16x32_bf16 v[212:215], v[248:251], v[232:235], v[212:215]\n\t" \
    "ds_read_b128 v[232:235], %[xa] offset:16384\n\t" \
    "v_mfma_f32_16x16x32_bf16 v[200:203], v[244:247], v[236:239], v[200:203]\n\t" \
    "ds_read_b128 v[244:247], %[wl] offset:0\n\t" \
    "v_mfma_f32_16x16x32_bf16 v[200:203], v[248:251], v[240:243], v[200:203]\n\t" \
    "ds_read_b128 v[248:251], %[wl] offset:1024\n\t" \
    "v_mfma_f32_16x16x32_bf16 v[204:207], v[160:163], v[236:239], v[204:207]\n\t" \
    "v_mfma_f32_16x16x32_bf16 v[204:207], v[164:167], v[240:243], v[204:207]\n\t" \
    "v_mfma_f32_16x16x32_bf16 v[208:211], v[168:171], v[236:239], v[208:211]\n\t" \
    "ds_read_b128 v[236:239], %[xa] offset:17408\n\t" \
    "v_mfma_f32_16x16x32_bf16 v[208:211], v[172:175], v[240:243], v[208:211]\n\t" \
    "v_xor_b32 v252, 0x800, %[xa]\n\t" \
    "ds_read_b128 v[240:243], v252 offset:16384\n\t" \
    "s_waitcnt lgkmcnt(6)\n\t" \
    "v_mfma_f32_16x16x32_bf16 v[208:211], v[8:11], v[224:227], v[208:211]\n\t" \
    "s_waitcnt lgkmcnt(5)\n\t" \
    "v_mfma_f32_16x16x32_bf16 v[208:211], v[12:15], v[228:231], v[208:211]\n\t" \
    "v_mfma_f32_16x16x32_bf16 v[204:207], v[0:3], v[224:227], v[204:207]\n\t" \
    "v_mfma_f32_16x16x32_bf16 v[204:207], v[4:7], v[228:231], v[204:207]\n\t" \
    "s_waitcnt lgkmcnt(3)\n\t" \
    "v_mfma_f32_16x16x32_bf16 v[200:203], v[244:247], v[224:227], v[200:203]\n\t" \
    "ds_read_b128 v[224:227], v252 offset:17408\n\t" \
    "s_waitcnt lgkmcnt(3)\n\t" \
    "v_mfma_f32_16x16x32_bf16 v[200:203], v[248:251], v[228:231], v[200:203]\n\t" \
    "ds_read_b128 v[228:231], v252 offset:0\n\t" \
    "v_mfma_f32_16x16x32_bf16 v[212:215], v[244:247], v[232:235], v[212:215]\n\t" \
    "ds_read_b128 v[244:247], %[wl] offset:2048\n\t" \
    "s_waitcnt lgkmcnt(4)\n\t" \
    "v_mfma_f32_16x16x32_bf16 v[212:215], v[248:251], v[236:239], v[212:215]\n\t" \
    "ds_read_b128 v[248:251], %[wl] offset:3072\n\t" \
    "v_mfma_f32_16x16x32_bf16 v[216:219], v[0:3], v[232:235], v[216:219]\n\t" \
    "v_mfma_f32_16x16x32_bf16 v[216:219], v[4:7], v[236:239], v[216:219]\n\t" \
    "v_mfma_f32_16x16x32_bf16 v[220:223], v[8:11], v[232:235], v[220:223]\n\t" \
    "ds_read_b128 v[232:235], v252 offset:1024\n\t" \
    "v_mfma_f32_16x16x32_bf16 v[220:223], v[12:15], v[236:239], v[220:223]\n\t" \
    "v_xor_b32 v252, 0x1000, %[xa]\n\t" \
    "ds_read_b128 v[236:239], v252 offset:0\n\t" \
    "s_waitcnt lgkmcnt(6)\n\t" \
    "v_mfma_f32_16x16x32_bf16 v[220:223], v[32:35], v[240:243], v[220:223]\n\t" \
    "s_waitcnt lgkmcnt(5)\n\t" \
    "v_mfma_f32_16x16x32_bf16 v[220:223], v[36:39], v[224:227], v[220:223]\n\t" \
    "v_mfma_f32_16x16x32_bf16 v[216:219], v[24:27], v[240:243], v[216:219]\n\t" \
    "v_mfma_f32_16x16x32_bf16 v[216:219], v[28:31], v[224:227], v[216:219]\n\t" \
    "v_mfma_f32_16x16x32_bf16 v[212:215], v[16:19], v[240:243], v[212:215]\n\t" \
    "ds_read_b128 v[240:243], v252 offset:1024\n\t" \
    "v_mfma_f32_16x16x32_bf16 v[212:215], v[20:23], v[224:227], v[212:215]\n\t" \
    "ds_read_b128 v[224:227], v252 offset:16384\n\t" \
    "s_waitcnt lgkmcnt(6)\n\t" \
    "v_mfma_f32_16x16x32_bf16 v[200:203], v[16:19], v[228:231], v[200:203]\n\t" \
    "s_waitcnt lgkmcnt(3)\n\t" \
    "v_mfma_f32_16x16x32_bf16 v[200:203], v[20:23], v[232:235], v[200:203]\n\t" \
    "v_mfma_f32_16x16x32_bf16 v[204:207], v[24:27], v[228:231], v[204:207]\n\t" \
    "v_mfma_f32_16x16x32_bf16 v[204:207], v[28:31], v[232:235], v[204:207]\n\t" \
    "v_mfma_f32_16x16x32_bf16 v[208:211], v[32:35], v[228:231], v[208:211]\n\t" \
    "ds_read_b128 v[228:231], v252 offset:17408\n\t" \
    "v_mfma_f32_16x16x32_bf16 v[208:211], v[36:39], v[232:235], v[208:211]\n\t" \
    "v_xor_b32 v252, 0x1800, %[xa]\n\t" \
    "ds_read_b128 v[232:235], v252 offset:16384\n\t" \
    "s_waitcnt lgkmcnt(4)\n\t" \
    "v_mfma_f32_16x16x32_bf16 v[208:211], v[48:51], v[236:239], v[208:211]\n\t" \
    "s_waitcnt lgkmcnt(3)\n\t" \
    "v_mfma_f32_16x16x32_bf16 v[208:211], v[52:55], v[240:243], v[208:211]\n\t" \
    "v_mfma_f32_16x16x32_bf16 v[204:207], v[40:43], v[236:239], v[204:207]\n\t" \
    "v_mfma_f32_16x16x32_bf16 v[204:207], v[44:47], v[240:243], v[204:207]\n\t" \
    "v_mfma_f32_16x16x32_bf16 v[200:203], v[244:247], v[236:239], v[200:203]\n\t" \
    "ds_read_b128 v[236:239], v252 offset:17408\n\t" \
    "v_mfma_f32_16x16x32_bf16 v[200:203], v[248:251], v[240:243], v[200:203]\n\t" \
    "ds_read_b128 v[240:243], v252 offset:0\n\t" \
    "s_waitcnt lgkmcnt(4)\n\t" \
    "v_mfma_f32_16x16x32_bf16 v[212:215], v[244:247], v[224:227], v[212:215]\n\t" \
    "s_waitcnt lgkmcnt(3)\n\t" \
    "v_mfma_f32_16x16x32_bf16 v[212:215], v[248:251], v[228:231], v[212:215]\n\t" \
    "v_mfma_f32_16x16x32_bf16 v[216:219], v[40:43], v[224:227], v[216:219]\n\t" \
    "v_mfma_f32_16x16x32_bf16 v[216:219], v[44:47], v[228:231], v[216:219]\n\t" \
    "v_mfma_f32_16x16x32_bf16 v[220:223], v[48:51], v[224:227], v[220:223]\n\t" \
    "ds_read_b128 v[224:227], v252 offset:1024\n\t" \
    "v_mfma_f32_16x16x32_bf16 v[220:223], v[52:55], v[228:231], v[220:223]\n\t" \
    "v_and_b32 v252, 0x3ff, %[xa]\n\t" \
    "ds_read_b128 v[228:231], v252 offset:43008\n\t" \
    "s_waitcnt lgkmcnt(4)\n\t" \
    "v_mfma_f32_16x16x32_bf16 v[220:223], v[72:75], v[232:235], v[220:223]\n\t" \
    "s_waitcnt lgkmcnt(3)\n\t" \
    "v_mfma_f32_16x16x32_bf16 v[220:223], v[76:79], v[236:239], v[220:223]\n\t" \
    "v_mfma_f32_16x16x32_bf16 v[216:219], v[64:67], v[232:235], v[216:219]\n\t" \
    "v_mfma_f32_16x16x32_bf16 v[216:219], v[68:71], v[236:239], v[216:219]\n\t" \
    "v_mfma_f32_16x16x32_bf16 v[212:215], v[56:59], v[232:235], v[212:215]\n\t" \
    "ds_read_b128 v[232:235], v252 offset:44032\n\t" \
    "v_mfma_f32_16x16x32_bf16 v[212:215], v[60:63], v[236:239], v[212:215]\n\t" \
    "ds_read_b128 v[236:239], v252 offset:47104\n\t" \
    "s_waitcnt lgkmcnt(4)\n\t" \
    "v_mfma_f32_16x16x32_bf16 v[200:203], v[56:59], v[240:243], v[200:203]\n\t" \
    "s_waitcnt lgkmcnt(3)\n\t" \
    "v_mfma_f32_16x16x32_bf16 v[200:203], v[60:63], v[224:227], v[200:203]\n\t" \
    "v_mfma_f32_16x16x32_bf16 v[204:207], v[64:67], v[240:243], v[204:207]\n\t" \
    "v_mfma_f32_16x16x32_bf16 v[204:207], v[68:71], v[224:227], v[204:207]\n\t" \
    "v_mfma_f32_16x16x32_bf16 v[208:211], v[72:75], v[240:243], v[208:211]\n\t" \
    "ds_read_b128 v[240:243], v252 offset:48128\n\t" \
    "v_mfma_f32_16x16x32_bf16 v[208:211], v[76:79], v[224:227], v[208:211]\n\t" \
    "s_waitcnt lgkmcnt(3)\n\t" \
    "v_mfma_f32_16x16x32_bf16 v[208:211], v[192:195], v[228:231], v[208:211]\n\t" \
    "v_add_u32 v252, %[qs], v252\n\t" \
    "s_waitcnt lgkmcnt(2)\n\t" \
    "v_mfma_f32_16x16x32_bf16 v[208:211], v[196:199], v[232:235], v[208:211]\n\t" \
    "v_mfma_f32_16x16x32_bf16 v[204:207], v[184:187], v[228:231], v[204:207]\n\t" \
    "v_mfma_f32_16x16x32_bf16 v[204:207], v[188:191], v[232:235], v[204:207]\n\t" \
    "v_mfma_f32_16x16x32_bf16 v[200:203], v[176:179], v[228:231], v[200:203]\n\t" \
    "v_mfma_f32_16x16x32_bf16 v[200:203], v[180:183], v[232:235], v[200:203]\n\t" \
    "s_waitcnt lgkmcnt(1)\n\t" \
    "v_mfma_f32_16x16x32_bf16 v[212:215], v[176:179], v[236:239], v[212:215]\n\t" \
    "v_cvt_pk_bf16_f32 v208, v208, v209\n\t" \
    "v_cvt_pk_bf16_f32 v209, v210, v211\n\t" \
    "s_waitcnt lgkmcnt(0)\n\t" \
    "v_mfma_f32_16x16x32_bf16 v[212:215], v[180:183], v[240:243], v[212:215]\n\t" \
    "ds_write_b64 v252, v[208:209] offset:2048\n\t" \
    "v_cvt_pk_bf16_f32 v204, v204, v205\n\t" \
    "v_mfma_f32_16x16x32_bf16 v[216:219], v[184:187], v[236:239], v[216:219]\n\t" \
    "v_cvt_pk_bf16_f32 v205, v206, v207\n\t" \
    "ds_write_b64 v252, v[204:205] offset:1024\n\t" \
    "v_mfma_f32_16x16x32_bf16 v[216:219], v[188:191], v[240:243], v[216:219]\n\t" \
    "v_cvt_pk_bf16_f32 v200, v200, v201\n\t" \
    "v_cvt_pk_bf16_f32 v201, v202, v203\n\t" \
    "v_mfma_f32_16x16x32_bf16 v[220:223], v[192:195], v[236:239], v[220:223]\n\t" \
    "ds_write_b64 v252, v[200:201] offset:0\n\t" \
    "v_mfma_f32_16x16x32_bf16 v[220:223], v[196:199], v[240:243], v[220:223]\n\t" \
    "v_cvt_pk_bf16_f32 v212, v212, v213\n\t" \
    "v_cvt_pk_bf16_f32 v213, v214, v215\n\t" \
    "ds_write_b64 v252, v[212:213] offset:6144\n\t" \
    "v_cvt_pk_bf16_f32 v216, v216, v217\n\t" \
    "v_cvt_pk_bf16_f32 v217, v218, v219\n\t" \
    "ds_write_b64 v252, v[216:217] offset:7168\n\t" \
    "s_nop 1\n\t" \
    "v_cvt_pk_bf16_f32 v220, v220, v221\n\t" \
    "v_cvt_pk_bf16_f32 v221, v222, v223\n\t" \
    "ds_write_b64 v252, v[220:221] offset:8192\n\t" \
    "s_waitcnt lgkmcnt(0)\n\t"
#define ASM_CRIT1_3 \
    "s_branch .Lc0%=\n\t" \
    ".Lcr%=:\n\t" \
    "s_cmp_eq_u32 %[n], 0\n\t" \
    "s_cbranch_scc1 .Lcb%=\n\t" \
    "s_sub_u32 %[n], %[n], 1\n\t" \
    "v_max_u32 v243, v208, v209\n\t" \
    "v_max3_u32 v243, v210, v211, v243\n\t" \
    "v_cmp_eq_u32 vcc, -1, v243\n\t" \
    "s_cbranch_vccz .Lcs0_%=\n\t" \
    "buffer_load_dwordx4 v[208:211], %[ab], %[rs], %[s0] offen sc1\n\t" \
    ".Lcs0_%=:\n\t" \
    "v_max_u32 v243, v236, v237\n\t" \
    "v_max3_u32 v243, v238, v239, v243\n\t" \
    "v_cmp_eq_u32 vcc, -1, v243\n\t" \
    "s_cbranch_vccz .Lcs1_%=\n\t" \
    "buffer_load_dwordx4 v[236:239], %[ab], %[rs], %[s1] offen sc1\n\t" \
    ".Lcs1_%=:\n\t" \
    "v_max_u32 v243, v220, v221\n\t" \
    "v_max3_u32 v243, v222, v223, v243\n\t" \
    "v_cmp_eq_u32 vcc, -1, v243\n\t" \
    "s_cbranch_vccz .Lca%=\n\t" \
    "buffer_load_dwordx4 v[220:223], %[ab], %[rs], %[s2] offen sc1\n\t" \
    "s_branch .Lca%=\n\t" \
    ".Lc0%=:\n\t" \
    "buffer_load_dwordx4 v[208:211], %[ab], %[rs], %[s0] offen sc1\n\t" \
    "buffer_load_dwordx4 v[236:239], %[ab], %[rs], %[s1] offen sc1\n\t" \
    "buffer_load_dwordx4 v[220:223], %[ab], %[rs], %[s2] offen sc1\n\t" \
    "v_bfe_i32 v241, %[ab], 7, 1\n\t" \
    "v_and_b32 v241, 0xaaaa, v241\n\t" \
    "v_or_b32 v241, 0x4444, v241\n\t" \
    "v_xor_b32 v212, 0x400, %[ab]\n\t" \
    "v_xor_b32 v216, 0x800, %[ab]\n\t" \
    "v_xor_b32 v242, 0xc00, %[ab]\n\t" \
    "v_xor_b32 v240, 0x1400, %[ab]\n\t" \
    ".Lca%=:\n\t" \
    "s_waitcnt vmcnt(0)\n\t" \
    "ds_write_b128 %[ab], v[208:211] offset:0\n\t" \
    "ds_write_b128 %[ab], v[236:239] offset:4096\n\t" \
    "ds_write_b128 %[ab], v[220:223] offset:8192\n\t" \
    "v_max_u32 v243, v208, v209\n\t" \
    "v_max3_u32 v243, v210, v211, v243\n\t" \
    "v_max3_u32 v243, v236, v237, v243\n\t" \
    "v_max3_u32 v243, v238, v239, v243\n\t" \
    "v_max3_u32 v243, v220, v221, v243\n\t" \
    "v_max3_u32 v243, v222, v223, v243\n\t" \
    "v_cmp_eq_u32 vcc, -1, v243\n\t" \
    "s_cbranch_vccnz .Lcr%=\n\t" \
    ".Lcb%=:\n\t" \
    PRIUP \
    ASM_ST2 \
    "v_smfmac_f32_16x16x64_bf16 v[224:227], v[208:211], v[200:207], v241\n\t" \
    "ds_read_b128 v[200:203], %[wl] offset:4096\n\t" \
    "ds_read_b128 v[204:207], %[wl] offset:5120\n\t" \
    "v_smfmac_f32_16x16x64_bf16 v[244:247], v[208:211], v[0:7], v241\n\t" \
    "v_smfmac_f32_16x16x64_bf16 v[248:251], v[208:211], v[8:15], v241\n\t" \
    "s_waitcnt lgkmcnt(2)\n\t" \
    "s_barrier\n\t" ASM_ST \
    "ds_read_b128 v[212:215], v212 offset:0\n\t" \
    "ds_read_b128 v[216:219], v216 offset:0\n\t" \
    "ds_read_b128 v[220:223], v242 offset:0\n\t" \
    "ds_read_b128 v[208:211], v240 offset:0\n\t" \
    "v_smfmac_f32_16x16x64_bf16 v[244:247], v[236:239], v[80:87], v241\n\t" \
    "v_xor_b32 v240, 0x1800, %[ab]\n\t" \
    "v_smfmac_f32_16x16x64_bf16 v[248:251], v[236:239], v[88:95], v241\n\t" \
    "s_waitcnt lgkmcnt(4)\n\t" \
    "v_smfmac_f32_16x16x64_bf16 v[224:227], v[236:239], v[200:207], v241\n\t" \
    "ds_read_b128 v[200:203], %[wl] offset:6144\n\t" \
    "ds_read_b128 v[204:207], %[wl] offset:7168\n\t" \
    "ds_read_b128 v[236:239], v240 offset:0\n\t" \
    "s_waitcnt lgkmcnt(6)\n\t" \
    "v_smfmac_f32_16x16x64_bf16 v[224:227], v[212:215], v[16:23], v241\n\t" \
    "v_smfmac_f32_16x16x64_bf16 v[244:247], v[212:215], v[24:31], v241\n\t" \
    "v_xor_b32 v240, 0x1c00, %[ab]\n\t" \
    "v_smfmac_f32_16x16x64_bf16 v[248:251], v[212:215], v[32:39], v241\n\t" \
    "ds_read_b128 v[212:215], v240 offset:0\n\t" \
    "s_waitcnt lgkmcnt(6)\n\t" \
    "v_smfmac_f32_16x16x64_bf16 v[224:227], v[216:219], v[228:235], v241\n\t" \
    "ds_read_b128 v[228:231], %[wl] offset:8192\n\t" \
    "ds_read_b128 v[232:235], %[wl] offset:9216\n\t" \
    "v_smfmac_f32_16x16x64_bf16 v[244:247], v[216:219], v[40:47], v241\n\t" \
    "v_and_b32 v240, 0x3ff, %[ab]\n\t" \
    "v_smfmac_f32_16x16x64_bf16 v[248:251], v[216:219], v[48:55], v241\n\t" \
    "ds_read_b128 v[216:219], v240 offset:8192\n\t" \
    "s_waitcnt lgkmcnt(8)\n\t" \
    "v_smfmac_f32_16x16x64_bf16 v[224:227], v[220:223], v[56:63], v241\n\t" \
    "v_smfmac_f32_16x16x64_bf16 v[244:247], v[220:223], v[64:71], v241\n\t" \
    "v_smfmac_f32_16x16x64_bf16 v[248:251], v[220:223], v[72:79], v241\n\t" \
    "ds_read_b128 v[220:223], v240 offset:9216\n\t" \
    "s_waitcnt lgkmcnt(8)\n\t" \
    "v_smfmac_f32_16x16x64_bf16 v[244:247], v[208:211], v[104:111], v241\n\t" \
    "s_waitcnt lgkmcnt(5)\n\t" \
    "v_smfmac_f32_16x16x64_bf16 v[244:247], v[236:239], v[120:127], v241\n\t" \
    "s_waitcnt lgkmcnt(4)\n\t" \
    "v_smfmac_f32_16x16x64_bf16 v[244:247], v[212:215], v[144:151], v241\n\t" \
    "s_waitcnt lgkmcnt(1)\n\t" \
    "v_smfmac_f32_16x16x64_bf16 v[244:247], v[216:219], v[160:167], v241\n\t" \
    "s_waitcnt lgkmcnt(0)\n\t" \
    "v_smfmac_f32_16x16x64_bf16 v[244:247], v[220:223], v[184:191], v241\n\t" \
    "v_smfmac_f32_16x16x64_bf16 v[248:251], v[208:211], v[112:119], v241\n\t" \
    "v_smfmac_f32_16x16x64_bf16 v[248:251], v[236:239], v[128:135], v241\n\t" \
    "v_smfmac_f32_16x16x64_bf16 v[248:251], v[212:215], v[152:159], v241\n\t" \
    "v_permlane32_swap_b32 v244, v246\n\t" \
    "v_permlane32_swap_b32 v245, v247\n\t" \
    "v_smfmac_f32_16x16x64_bf16 v[248:251], v[216:219], v[168:175], v241\n\t" \
    "v_add_f32 v244, v244, v246\n\t" \
    "v_add_f32 v245, v245, v247\n\t" \
    "v_smfmac_f32_16x16x64_bf16 v[248:251], v[220:223], v[192:199], v241\n\t" \
    "v_cvt_pk_bf16_f32 v244, v244, v245\n\t" \
    "ds_write_b16 %[dst], v244 offset:512\n\t" \
    "ds_write_b16_d16_hi %[dst], v244 offset:528\n\t" \
    "v_smfmac_f32_16x16x64_bf16 v[224:227], v[208:211], v[96:103], v241\n\t" \
    "v_smfmac_f32_16x16x64_bf16 v[224:227], v[236:239], v[200:207], v241\n\t" \
    "v_smfmac_f32_16x16x64_bf16 v[224:227], v[212:215], v[136:143], v241\n\t" \
    "v_permlane32_swap_b32 v248, v250\n\t" \
    "v_permlane32_swap_b32 v249, v251\n\t" \
    "v_smfmac_f32_16x16x64_bf16 v[224:227], v[216:219], v[228:235], v241\n\t" \
    "v_add_f32 v248, v248, v250\n\t" \
    "v_add_f32 v249, v249, v251\n\t" \
    "v_smfmac_f32_16x16x64_bf16 v[224:227], v[220:223], v[176:183], v241\n\t" \
    "v_cvt_pk_bf16_f32 v248, v248, v249\n\t" \
    "ds_write_b16 %[dst], v248 offset:1024\n\t" \
    "ds_write_b16_d16_hi %[dst], v248 offset:1040\n\t" \
    "s_nop 4\n\t" \
    "v_permlane32_swap_b32 v224, v226\n\t" \
    "v_permlane32_swap_b32 v225, v227\n\t" \
    "v_add_f32 v224, v224, v226\n\t" \
    "v_add_f32 v225, v225, v227\n\t" \
    "v_cvt_pk_bf16_f32 v224, v224, v225\n\t" \
    "ds_write_b16 %[dst], v224 offset:0\n\t" \
    "ds_write_b16_d16_hi %[dst], v224 offset:16\n\t"
#define ASM_CRIT1_2 \
    "s_branch .Lc0%=\n\t" \
    ".Lcr%=:\n\t" \
    "s_cmp_eq_u32 %[n], 0\n\t" \
    "s_cbranch_scc1 .Lcb%=\n\t" \
    "s_sub_u32 %[n], %[n], 1\n\t" \
    "v_max_u32 v243, v208, v209\n\t" \
    "v_max3_u32 v243, v210, v211, v243\n\t" \
    "v_cmp_eq_u32 vcc, -1, v243\n\t" \
    "s_cbranch_vccz .Lcs0_%=\n\t" \
    "buffer_load_dwordx4 v[208:211], %[ab], %[rs], %[s0] offen sc1\n\t" \
    ".Lcs0_%=:\n\t" \
    "v_max_u32 v243, v236, v237\n\t" \
    "v_max3_u32 v243, v238, v239, v243\n\t" \
    "v_cmp_eq_u32 vcc, -1, v243\n\t" \
    "s_cbranch_vccz .Lca%=\n\t" \
    "buffer_load_dwordx4 v[236:239], %[ab], %[rs], %[s1] offen sc1\n\t" \
    "s_branch .Lca%=\n\t" \
    ".Lc0%=:\n\t" \
    "buffer_load_dwordx4 v[208:211], %[ab], %[rs], %[s0] offen sc1\n\t" \
    "buffer_load_dwordx4 v[236:239], %[ab], %[rs], %[s1] offen sc1\n\t" \
    "v_bfe_i32 v241, %[ab], 7, 1\n\t" \
    "v_and_b32 v241, 0xaaaa, v241\n\t" \
    "v_or_b32 v241, 0x4444, v241\n\t" \
    "v_xor_b32 v212, 0x400, %[ab]\n\t" \
    "v_xor_b32 v216, 0x800, %[ab]\n\t" \
    "v_xor_b32 v242, 0xc00, %[ab]\n\t" \
    "v_xor_b32 v240, 0x1400, %[ab]\n\t" \
    ".Lca%=:\n\t" \
    "s_waitcnt vmcnt(0)\n\t" \
    "ds_write_b128 %[ab], v[208:211] offset:0\n\t" \
    "ds_write_b128 %[ab], v[236:239] offset:4096\n\t" \
    "v_max_u32 v243, v208, v209\n\t" \
    "v_max3_u32 v243, v210, v211, v243\n\t" \
    "v_max3_u32 v243, v236, v237, v243\n\t" \
    "v_max3_u32 v243, v238, v239, v243\n\t" \
    "v_cmp_eq_u32 vcc, -1, v243\n\t" \
    "s_cbranch_vccnz .Lcr%=\n\t" \
    ".Lcb%=:\n\t" \
    PRIUP \
    ASM_ST2 \
    "v_smfmac_f32_16x16x64_bf16 v[224:227], v[208:211], v[200:207], v241\n\t" \
    "ds_read_b128 v[200:203], %[wl] offset:4096\n\t" \
    "ds_read_b128 v[204:207], %[wl] offset:5120\n\t" \
    "v_smfmac_f32_16x16x64_bf16 v[244:247], v[208:211], v[0:7], v241\n\t" \
    "v_smfmac_f32_16x16x64_bf16 v[248:251], v[208:211], v[8:15], v241\n\t" \
    "s_waitcnt lgkmcnt(2)\n\t" \
    "s_barrier\n\t" ASM_ST \
    "ds_read_b128 v[212:215], v212 offset:0\n\t" \
    "ds_read_b128 v[216:219], v216 offset:0\n\t" \
    "ds_read_b128 v[220:223], v242 offset:0\n\t" \
    "ds_read_b128 v[208:211], v240 offset:0\n\t" \
    "v_smfmac_f32_16x16x64_bf16 v[244:247], v[236:239], v[80:87], v241\n\t" \
    "v_xor_b32 v240, 0x1800, %[ab]\n\t" \
    "v_smfmac_f32_16x16x64_bf16 v[248:251], v[236:239], v[88:95], v241\n\t" \
    "s_waitcnt lgkmcnt(4)\n\t" \
    "v_smfmac_f32_16x16x64_bf16 v[224:227], v[236:239], v[200:207], v241\n\t" \
    "ds_read_b128 v[200:203], %[wl] offset:6144\n\t" \
    "ds_read_b128 v[204:207], %[wl] offset:7168\n\t" \
    "ds_read_b128 v[236:239], v240 offset:0\n\t" \
    "s_waitcnt lgkmcnt(6)\n\t" \
    "v_smfmac_f32_16x16x64_bf16 v[224:227], v[212:215], v[16:23], v241\n\t" \
    "v_smfmac_f32_16x16x64_bf16 v[244:247], v[212:215], v[24:31], v241\n\t" \
    "v_xor_b32 v240, 0x1c00, %[ab]\n\t" \
    "v_smfmac_f32_16x16x64_bf16 v[248:251], v[212:215], v[32:39], v241\n\t" \
    "ds_read_b128 v[212:215], v240 offset:0\n\t" \
    "s_waitcnt lgkmcnt(6)\n\t" \
    "v_smfmac_f32_16x16x64_bf16 v[224:227], v[216:219], v[228:235], v241\n\t" \
    "ds_read_b128 v[228:231], %[wl] offset:8192\n\t" \
    "ds_read_b128 v[232:235], %[wl] offset:9216\n\t" \
    "v_smfmac_f32_16x16x64_bf16 v[244:247], v[216:219], v[40:47], v241\n\t" \
    "v_and_b32 v240, 0x3ff, %[ab]\n\t" \
    "v_smfmac_f32_16x16x64_bf16 v[248:251], v[216:219], v[48:55], v241\n\t" \
    "ds_read_b128 v[216:219], v240 offset:8192\n\t" \
    "s_waitcnt lgkmcnt(8)\n\t" \
    "v_smfmac_f32_16x16x64_bf16 v[224:227], v[220:223], v[56:63], v241\n\t" \
    "v_smfmac_f32_16x16x64_bf16 v[244:247], v[220:223], v[64:71], v241\n\t" \
    "v_smfmac_f32_16x16x64_bf16 v[248:251], v[220:223], v[72:79], v241\n\t" \
    "ds_read_b128 v[220:223], v240 offset:9216\n\t" \
    "s_waitcnt lgkmcnt(8)\n\t" \
    "v_smfmac_f32_16x16x64_bf16 v[244:247], v[208:211], v[104:111], v241\n\t" \
    "s_waitcnt lgkmcnt(5)\n\t" \
    "v_smfmac_f32_16x16x64_bf16 v[244:247], v[236:239], v[120:127], v241\n\t" \
    "s_waitcnt lgkmcnt(4)\n\t" \
    "v_smfmac_f32_16x16x64_bf16 v[244:247], v[212:215], v[144:151], v241\n\t" \
    "s_waitcnt lgkmcnt(1)\n\t" \
    "v_smfmac_f32_16x16x64_bf16 v[244:247], v[216:219], v[160:167], v241\n\t" \
    "s_waitcnt lgkmcnt(0)\n\t" \
    "v_smfmac_f32_16x16x64_bf16 v[244:247], v[220:223], v[184:191], v241\n\t" \
    "v_smfmac_f32_16x16x64_bf16 v[248:251], v[208:211], v[112:119], v241\n\t" \
    "v_smfmac_f32_16x16x64_bf16 v[248:251], v[236:239], v[128:135], v241\n\t" \
    "v_smfmac_f32_16x16x64_bf16 v[248:251], v[212:215], v[152:159], v241\n\t" \
    "v_permlane32_swap_b32 v244, v246\n\t" \
    "v_permlane32_swap_b32 v245, v247\n\t" \
    "v_smfmac_f32_16x16x64_bf16 v[248:251], v[216:219], v[168:175], v241\n\t" \
    "v_add_f32 v244, v244, v246\n\t" \
    "v_add_f32 v245, v245, v247\n\t" \
    "v_smfmac_f32_16x16x64_bf16 v[248:251], v[220:223], v[192:199], v241\n\t" \
    "v_cvt_pk_bf16_f32 v244, v244, v245\n\t" \
    "ds_write_b16 %[dst], v244 offset:512\n\t" \
    "ds_write_b16_d16_hi %[dst], v244 offset:528\n\t" \
    "v_smfmac_f32_16x16x64_bf16 v[224:227], v[208:211], v[96:103], v241\n\t" \
    "v_smfmac_f32_16x16x64_bf16 v[224:227], v[236:239], v[200:207], v241\n\t" \
    "v_smfmac_f32_16x16x64_bf16 v[224:227], v[212:215], v[136:143], v241\n\t" \
    "v_permlane32_swap_b32 v248, v250\n\t" \
    "v_permlane32_swap_b32 v249, v251\n\t" \
    "v_smfmac_f32_16x16x64_bf16 v[224:227], v[216:219], v[228:235], v241\n\t" \
    "v_add_f32 v248, v248, v250\n\t" \
    "v_add_f32 v249, v249, v251\n\t" \
    "v_smfmac_f32_16x16x64_bf16 v[224:227], v[220:223], v[176:183], v241\n\t" \
    "v_cvt_pk_bf16_f32 v248, v248, v249\n\t" \
    "ds_write_b16 %[dst], v248 offset:1024\n\t" \
    "ds_write_b16_d16_hi %[dst], v248 offset:1040\n\t" \
    "s_nop 4\n\t" \
    "v_permlane32_swap_b32 v224, v226\n\t" \
    "v_permlane32_swap_b32 v225, v227\n\t" \
    "v_add_f32 v224, v224, v226\n\t" \
    "v_add_f32 v225, v225, v227\n\t" \
    "v_cvt_pk_bf16_f32 v224, v224, v225\n\t" \
    "ds_write_b16 %[dst], v224 offset:0\n\t" \
    "ds_write_b16_d16_hi %[dst], v224 offset:16\n\t"
#define ASM_CRIT1_N \
    "v_bfe_i32 v241, %[ab], 7, 1\n\t" \
    "v_and_b32 v241, 0xaaaa, v241\n\t" \
    "v_or_b32 v241, 0x4444, v241\n\t" \
    "v_xor_b32 v212, 0x400, %[ab]\n\t" \
    "v_xor_b32 v216, 0x800, %[ab]\n\t" \
    "v_xor_b32 v242, 0xc00, %[ab]\n\t" \
    "v_xor_b32 v240, 0x1400, %[ab]\n\t" \
    "v_xor_b32 v236, 0x1000, %[ab]\n\t" \
    "s_waitcnt lgkmcnt(0)\n\t" \
    "s_barrier\n\t" ASM_ST \
    "ds_read_b128 v[208:211], %[ab] offset:0\n\t" \
    "ds_read_b128 v[212:215], v212 offset:0\n\t" \
    "ds_read_b128 v[216:219], v216 offset:0\n\t" \
    "ds_read_b128 v[220:223], v242 offset:0\n\t" \
    "ds_read_b128 v[236:239], v236 offset:0\n\t" \
    "s_waitcnt lgkmcnt(4)\n\t" \
    "v_smfmac_f32_16x16x64_bf16 v[224:227], v[208:211], v[200:207], v241\n\t" \
    "ds_read_b128 v[200:203], %[wl] offset:4096\n\t" \
    "ds_read_b128 v[204:207], %[wl] offset:5120\n\t" \
    "v_smfmac_f32_16x16x64_bf16 v[244:247], v[208:211], v[0:7], v241\n\t" \
    "v_smfmac_f32_16x16x64_bf16 v[248:251], v[208:211], v[8:15], v241\n\t" \
    "ds_read_b128 v[208:211], v240 offset:0\n\t" \
    "s_waitcnt lgkmcnt(6)\n\t" \
    "v_smfmac_f32_16x16x64_bf16 v[224:227], v[212:215], v[16:23], v241\n\t" \
    "v_smfmac_f32_16x16x64_bf16 v[244:247], v[212:215], v[24:31], v241\n\t" \
    "v_xor_b32 v240, 0x1800, %[ab]\n\t" \
    "v_smfmac_f32_16x16x64_bf16 v[248:251], v[212:215], v[32:39], v241\n\t" \
    "ds_read_b128 v[212:215], v240 offset:0\n\t" \
    "s_waitcnt lgkmcnt(6)\n\t" \
    "v_smfmac_f32_16x16x64_bf16 v[224:227], v[216:219], v[228:235], v241\n\t" \
    "ds_read_b128 v[228:231], %[wl] offset:6144\n\t" \
    "ds_read_b128 v[232:235], %[wl] offset:7168\n\t" \
    "v_smfmac_f32_16x16x64_bf16 v[244:247], v[216:219], v[40:47], v241\n\t" \
    "v_xor_b32 v240, 0x1c00, %[ab]\n\t" \
    "v_smfmac_f32_16x16x64_bf16 v[248:251], v[216:219], v[48:55], v241\n\t" \
    "ds_read_b128 v[216:219], v240 offset:0\n\t" \
    "s_waitcnt lgkmcnt(8)\n\t" \
    "v_smfmac_f32_16x16x64_bf16 v[224:227], v[220:223], v[56:63], v241\n\t" \
    "v_smfmac_f32_16x16x64_bf16 v[244:247], v[220:223], v[64:71], v241\n\t" \
    "v_and_b32 v240, 0x3ff, %[ab]\n\t" \
    "v_smfmac_f32_16x16x64_bf16 v[248:251], v[220:223], v[72:79], v241\n\t" \
    "ds_read_b128 v[220:223], v240 offset:8192\n\t" \
    "s_waitcnt lgkmcnt(6)\n\t" \
    "v_smfmac_f32_16x16x64_bf16 v[224:227], v[236:239], v[200:207], v241\n\t" \
    "ds_read_b128 v[200:203], %[wl] offset:8192\n\t" \
    "ds_read_b128 v[204:207], %[wl] offset:9216\n\t" \
    "v_smfmac_f32_16x16x64_bf16 v[244:247], v[236:239], v[80:87], v241\n\t" \
    "v_smfmac_f32_16x16x64_bf16 v[248:251], v[236:239], v[88:95], v241\n\t" \
    "ds_read_b128 v[236:239], v240 offset:9216\n\t" \
    "s_waitcnt lgkmcnt(8)\n\t" \
    "v_smfmac_f32_16x16x64_bf16 v[244:247], v[208:211], v[104:111], v241\n\t" \
    "s_waitcnt lgkmcnt(7)\n\t" \
    "v_smfmac_f32_16x16x64_bf16 v[244:247], v[212:215], v[120:127], v241\n\t" \
    "s_waitcnt lgkmcnt(4)\n\t" \
    "v_smfmac_f32_16x16x64_bf16 v[244:247], v[216:219], v[144:151], v241\n\t" \
    "s_waitcnt lgkmcnt(3)\n\t" \
    "v_smfmac_f32_16x16x64_bf16 v[244:247], v[220:223], v[160:167], v241\n\t" \
    "s_waitcnt lgkmcnt(0)\n\t" \
    "v_smfmac_f32_16x16x64_bf16 v[244:247], v[236:239], v[184:191], v241\n\t" \
    "v_smfmac_f32_16x16x64_bf16 v[248:251], v[208:211], v[112:119], v241\n\t" \
    "v_smfmac_f32_16x16x64_bf16 v[248:251], v[212:215], v[128:135], v241\n\t" \
    "v_smfmac_f32_16x16x64_bf16 v[248:251], v[216:219], v[152:159], v241\n\t" \
    "v_permlane32_swap_b32 v244, v246\n\t" \
    "v_permlane32_swap_b32 v245, v247\n\t" \
    "v_smfmac_f32_16x16x64_bf16 v[248:251], v[220:223], v[168:175], v241\n\t" \
    "v_add_f32 v244, v244, v246\n\t" \
    "v_add_f32 v245, v245, v247\n\t" \
    "v_smfmac_f32_16x16x64_bf16 v[248:251], v[236:239], v[192:199], v241\n\t" \
    "v_cvt_pk_bf16_f32 v244, v244, v245\n\t" \
    "ds_write_b16 %[dst], v244 offset:512\n\t" \
    "ds_write_b16_d16_hi %[dst], v244 offset:528\n\t" \
    "v_smfmac_f32_16x16x64_bf16 v[224:227], v[208:211], v[96:103], v241\n\t" \
    "v_smfmac_f32_16x16x64_bf16 v[224:227], v[212:215], v[228:235], v241\n\t" \
    "v_smfmac_f32_16x16x64_bf16 v[224:227], v[216:219], v[136:143], v241\n\t" \
    "v_permlane32_swap_b32 v248, v250\n\t" \
    "v_permlane32_swap_b32 v249, v251\n\t" \
    "v_smfmac_f32_16x16x64_bf16 v[224:227], v[220:223], v[200:207], v241\n\t" \
    "v_add_f32 v248, v248, v250\n\t" \
    "v_add_f32 v249, v249, v251\n\t" \
    "v_smfmac_f32_16x16x64_bf16 v[224:227], v[236:239], v[176:183], v241\n\t" \
    "v_cvt_pk_bf16_f32 v248, v248, v249\n\t" \
    "ds_write_b16 %[dst], v248 offset:1024\n\t" \
    "ds_write_b16_d16_hi %[dst], v248 offset:1040\n\t" \
    "s_nop 4\n\t" \
    "v_permlane32_swap_b32 v224, v226\n\t" \
    "v_permlane32_swap_b32 v225, v227\n\t" \
    "v_add_f32 v224, v224, v226\n\t" \
    "v_add_f32 v225, v225, v227\n\t" \
    "v_cvt_pk_bf16_f32 v224, v224, v225\n\t" \
    "ds_write_b16 %[dst], v224 offset:0\n\t" \
    "ds_write_b16_d16_hi %[dst], v224 offset:16\n\t"
#define ASM_CRIT1_V3 \
    "s_branch .Lc0%=\n\t" \
    ".Lcr%=:\n\t" \
    "s_cmp_eq_u32 %[n], 0\n\t" \
    "s_cbranch_scc1 .Lcb%=\n\t" \
    "s_sub_u32 %[n], %[n], 1\n\t" \
    "v_max_u32 v243, v208, v209\n\t" \
    "v_max3_u32 v243, v210, v211, v243\n\t" \
    "v_cmp_eq_u32 vcc, -1, v243\n\t" \
    "s_cbranch_vccz .Lcs0_%=\n\t" \
    "buffer_load_dwordx4 v[208:211], %[ab], %[rs], %[s0] offen sc1\n\t" \
    ".Lcs0_%=:\n\t" \
    "v_max_u32 v243, v236, v237\n\t" \
    "v_max3_u32 v243, v238, v239, v243\n\t" \
    "v_cmp_eq_u32 vcc, -1, v243\n\t" \
    "s_cbranch_vccz .Lcs1_%=\n\t" \
    "buffer_load_dwordx4 v[236:239], %[ab], %[rs], %[s1] offen sc1\n\t" \
    ".Lcs1_%=:\n\t" \
    "v_max_u32 v243, v220, v221\n\t" \
    "v_max3_u32 v243, v222, v223, v243\n\t" \
    "v_cmp_eq_u32 vcc, -1, v243\n\t" \
    "s_cbranch_vccz .Lca%=\n\t" \
    "buffer_load_dwordx4 v[220:223], %[ab], %[rs], %[s2] offen sc1\n\t" \
    "s_branch .Lca%=\n\t" \
    ".Lc0%=:\n\t" \
    "buffer_load_dwordx4 v[208:211], %[ab], %[rs], %[s0] offen sc1\n\t" \
    "buffer_load_dwordx4 v[236:239], %[ab], %[rs], %[s1] offen sc1\n\t" \
    "buffer_load_dwordx4 v[220:223], %[ab], %[rs], %[s2] offen sc1\n\t" \
    "v_bfe_i32 v241, %[ab], 7, 1\n\t" \
    "v_and_b32 v241, 0xaaaa, v241\n\t" \
    "v_or_b32 v241, 0x4444, v241\n\t" \
    "v_xor_b32 v212, 0x400, %[ab]\n\t" \
    "v_xor_b32 v216, 0x800, %[ab]\n\t" \
    "v_xor_b32 v242, 0xc00, %[ab]\n\t" \
    "v_xor_b32 v240, 0x1400, %[ab]\n\t" \
    ".Lca%=:\n\t" \
    "s_waitcnt vmcnt(0)\n\t" \
    "ds_write_b128 %[ab], v[208:211] offset:0\n\t" \
    "ds_write_b128 %[ab], v[236:239] offset:4096\n\t" \
    "ds_write_b128 %[ab], v[220:223] offset:8192\n\t" \
    "v_max_u32 v243, v208, v209\n\t" \
    "v_max3_u32 v243, v210, v211, v243\n\t" \
    "v_max3_u32 v243, v236, v237, v243\n\t" \
    "v_max3_u32 v243, v238, v239, v243\n\t" \
    "v_max3_u32 v243, v220, v221, v243\n\t" \
    "v_max3_u32 v243, v222, v223, v243\n\t" \
    "v_cmp_eq_u32 vcc, -1, v243\n\t" \
    "s_cbranch_vccnz .Lcr%=\n\t" \
    ".Lcb%=:\n\t" \
    PRIUP \
    ASM_ST2 \
    "v_smfmac_f32_16x16x64_bf16 v[224:227], v[208:211], v[200:207], v241\n\t" \
    "ds_read_b128 v[200:203], %[wl] offset:4096\n\t" \
    "ds_read_b128 v[204:207], %[wl] offset:5120\n\t" \
    "v_smfmac_f32_16x16x64_bf16 v[244:247], v[208:211], v[0:7], v241\n\t" \
    "v_smfmac_f32_16x16x64_bf16 v[248:251], v[208:211], v[8:15], v241\n\t" \
    "s_waitcnt lgkmcnt(2)\n\t" \
    "buffer_load_dword %[fv], %[fv], %[rs], %[fs] offen sc1\n\t" \
    "s_barrier\n\t" ASM_ST \
    "ds_read_b128 v[212:215], v212 offset:0\n\t" \
    "ds_read_b128 v[216:219], v216 offset:0\n\t" \
    "ds_read_b128 v[220:223], v242 offset:0\n\t" \
    "ds_read_b128 v[208:211], v240 offset:0\n\t" \
    "v_smfmac_f32_16x16x64_bf16 v[244:247], v[236:239], v[80:87], v241\n\t" \
    "v_xor_b32 v240, 0x1800, %[ab]\n\t" \
    "v_smfmac_f32_16x16x64_bf16 v[248:251], v[236:239], v[88:95], v241\n\t" \
    "s_waitcnt lgkmcnt(4)\n\t" \
    "v_smfmac_f32_16x16x64_bf16 v[224:227], v[236:239], v[200:207], v241\n\t" \
    "ds_read_b128 v[200:203], %[wl] offset:6144\n\t" \
    "ds_read_b128 v[204:207], %[wl] offset:7168\n\t" \
    "ds_read_b128 v[236:239], v240 offset:0\n\t" \
    "s_waitcnt lgkmcnt(6)\n\t" \
    "v_smfmac_f32_16x16x64_bf16 v[224:227], v[212:215], v[16:23], v241\n\t" \
    "v_smfmac_f32_16x16x64_bf16 v[244:247], v[212:215], v[24:31], v241\n\t" \
    "v_xor_b32 v240, 0x1c00, %[ab]\n\t" \
    "v_smfmac_f32_16x16x64_bf16 v[248:251], v[212:215], v[32:39], v241\n\t" \
    "ds_read_b128 v[212:215], v240 offset:0\n\t" \
    "s_waitcnt lgkmcnt(6)\n\t" \
    "v_smfmac_f32_16x16x64_bf16 v[224:227], v[216:219], v[228:235], v241\n\t" \
    "ds_read_b128 v[228:231], %[wl] offset:8192\n\t" \
    "ds_read_b128 v[232:235], %[wl] offset:9216\n\t" \
    "v_smfmac_f32_16x16x64_bf16 v[244:247], v[216:219], v[40:47], v241\n\t" \
    "v_and_b32 v240, 0x3ff, %[ab]\n\t" \
    "v_smfmac_f32_16x16x64_bf16 v[248:251], v[216:219], v[48:55], v241\n\t" \
    "ds_read_b128 v[216:219], v240 offset:8192\n\t" \
    "s_waitcnt lgkmcnt(8)\n\t" \
    "v_smfmac_f32_16x16x64_bf16 v[224:227], v[220:223], v[56:63], v241\n\t" \
    "v_smfmac_f32_16x16x64_bf16 v[244:247], v[220:223], v[64:71], v241\n\t" \
    "v_smfmac_f32_16x16x64_bf16 v[248:251], v[220:223], v[72:79], v241\n\t" \
    "ds_read_b128 v[220:223], v240 offset:9216\n\t" \
    "s_waitcnt lgkmcnt(8)\n\t" \
    "v_smfmac_f32_16x16x64_bf16 v[244:247], v[208:211], v[104:111], v241\n\t" \
    "s_waitcnt lgkmcnt(5)\n\t" \
    "v_smfmac_f32_16x16x64_bf16 v[244:247], v[236:239], v[120:127], v241\n\t" \
    "s_waitcnt lgkmcnt(4)\n\t" \
    "v_smfmac_f32_16x16x64_bf16 v[244:247], v[212:215], v[144:151], v241\n\t" \
    "s_waitcnt lgkmcnt(1)\n\t" \
    "v_smfmac_f32_16x16x64_bf16 v[244:247], v[216:219], v[160:167], v241\n\t" \
    "s_waitcnt lgkmcnt(0)\n\t" \
    "v_smfmac_f32_16x16x64_bf16 v[244:247], v[220:223], v[184:191], v241\n\t" \
    "v_smfmac_f32_16x16x64_bf16 v[248:251], v[208:211], v[112:119], v241\n\t" \
    "v_smfmac_f32_16x16x64_bf16 v[248:251], v[236:239], v[128:135], v241\n\t" \
    "v_smfmac_f32_16x16x64_bf16 v[248:251], v[212:215], v[152:159], v241\n\t" \
    "v_permlane32_swap_b32 v244, v246\n\t" \
    "v_permlane32_swap_b32 v245, v247\n\t" \
    "v_smfmac_f32_16x16x64_bf16 v[248:251], v[216:219], v[168:175], v241\n\t" \
    "v_add_f32 v244, v244, v246\n\t" \
    "v_add_f32 v245, v245, v247\n\t" \
    "v_smfmac_f32_16x16x64_bf16 v[248:251], v[220:223], v[192:199], v241\n\t" \
    "v_cvt_pk_bf16_f32 v244, v244, v245\n\t" \
    "ds_write_b16 %[dst], v244 offset:512\n\t" \
    "ds_write_b16_d16_hi %[dst], v244 offset:528\n\t" \
    "v_smfmac_f32_16x16x64_bf16 v[224:227], v[208:211], v[96:103], v241\n\t" \
    "v_smfmac_f32_16x16x64_bf16 v[224:227], v[236:239], v[200:207], v241\n\t" \
    "v_smfmac_f32_16x16x64_bf16 v[224:227], v[212:215], v[136:143], v241\n\t" \
    "v_permlane32_swap_b32 v248, v250\n\t" \
    "v_permlane32_swap_b32 v249, v251\n\t" \
    "v_smfmac_f32_16x16x64_bf16 v[224:227], v[216:219], v[228:235], v241\n\t" \
    "v_add_f32 v248, v248, v250\n\t" \
    "v_add_f32 v249, v249, v251\n\t" \
    "v_smfmac_f32_16x16x64_bf16 v[224:227], v[220:223], v[176:183], v241\n\t" \
    "v_cvt_pk_bf16_f32 v248, v248, v249\n\t" \
    "ds_write_b16 %[dst], v248 offset:1024\n\t" \
    "ds_write_b16_d16_hi %[dst], v248 offset:1040\n\t" \
    "s_nop 4\n\t" \
    "v_permlane32_swap_b32 v224, v226\n\t" \
    "v_permlane32_swap_b32 v225, v227\n\t" \
    "v_add_f32 v224, v224, v226\n\t" \
    "v_add_f32 v225, v225, v227\n\t" \
    "v_cvt_pk_bf16_f32 v224, v224, v225\n\t" \
    "ds_write_b16 %[dst], v224 offset:0\n\t" \
    "ds_write_b16_d16_hi %[dst], v224 offset:16\n\t"
#define ASM_CRIT1_V2 \
    "s_branch .Lc0%=\n\t" \
    ".Lcr%=:\n\t" \
    "s_cmp_eq_u32 %[n], 0\n\t" \
    "s_cbranch_scc1 .Lcb%=\n\t" \
    "s_sub_u32 %[n], %[n], 1\n\t" \
    "v_max_u32 v243, v208, v209\n\t" \
    "v_max3_u32 v243, v210, v211, v243\n\t" \
    "v_cmp_eq_u32 vcc, -1, v243\n\t" \
    "s_cbranch_vccz .Lcs0_%=\n\t" \
    "buffer_load_dwordx4 v[208:211], %[ab], %[rs], %[s0] offen sc1\n\t" \
    ".Lcs0_%=:\n\t" \
    "v_max_u32 v243, v236, v237\n\t" \
    "v_max3_u32 v243, v238, v239, v243\n\t" \
    "v_cmp_eq_u32 vcc, -1, v243\n\t" \
    "s_cbranch_vccz .Lca%=\n\t" \
    "buffer_load_dwordx4 v[236:239], %[ab], %[rs], %[s1] offen sc1\n\t" \
    "s_branch .Lca%=\n\t" \
    ".Lc0%=:\n\t" \
    "buffer_load_dwordx4 v[208:211], %[ab], %[rs], %[s0] offen sc1\n\t" \
    "buffer_load_dwordx4 v[236:239], %[ab], %[rs], %[s1] offen sc1\n\t" \
    "v_bfe_i32 v241, %[ab], 7, 1\n\t" \
    "v_and_b32 v241, 0xaaaa, v241\n\t" \
    "v_or_b32 v241, 0x4444, v241\n\t" \
    "v_xor_b32 v212, 0x400, %[ab]\n\t" \
    "v_xor_b32 v216, 0x800, %[ab]\n\t" \
    "v_xor_b32 v242, 0xc00, %[ab]\n\t" \
    "v_xor_b32 v240, 0x1400, %[ab]\n\t" \
    ".Lca%=:\n\t" \
    "s_waitcnt vmcnt(0)\n\t" \
    "ds_write_b128 %[ab], v[208:211] offset:0\n\t" \
    "ds_write_b128 %[ab], v[236:239] offset:4096\n\t" \
    "v_max_u32 v243, v208, v209\n\t" \
    "v_max3_u32 v243, v210, v211, v243\n\t" \
    "v_max3_u32 v243, v236, v237, v243\n\t" \
    "v_max3_u32 v243, v238, v239, v243\n\t" \
    "v_cmp_eq_u32 vcc, -1, v243\n\t" \
    "s_cbranch_vccnz .Lcr%=\n\t" \
    ".Lcb%=:\n\t" \
    PRIUP \
    ASM_ST2 \
    "v_smfmac_f32_16x16x64_bf16 v[224:227], v[208:211], v[200:207], v241\n\t" \
    "ds_read_b128 v[200:203], %[wl] offset:4096\n\t" \
    "ds_read_b128 v[204:207], %[wl] offset:5120\n\t" \
    "v_smfmac_f32_16x16x64_bf16 v[244:247], v[208:211], v[0:7], v241\n\t" \
    "v_smfmac_f32_16x16x64_bf16 v[248:251], v[208:211], v[8:15], v241\n\t" \
    "s_waitcnt lgkmcnt(2)\n\t" \
    "buffer_load_dword %[fv], %[fv], %[rs], %[fs] offen sc1\n\t" \
    "s_barrier\n\t" ASM_ST \
    "ds_read_b128 v[212:215], v212 offset:0\n\t" \
    "ds_read_b128 v[216:219], v216 offset:0\n\t" \
    "ds_read_b128 v[220:223], v242 offset:0\n\t" \
    "ds_read_b128 v[208:211], v240 offset:0\n\t" \
    "v_smfmac_f32_16x16x64_bf16 v[244:247], v[236:239], v[80:87], v241\n\t" \
    "v_xor_b32 v240, 0x1800, %[ab]\n\t" \
    "v_smfmac_f32_16x16x64_bf16 v[248:251], v[236:239], v[88:95], v241\n\t" \
    "s_waitcnt lgkmcnt(4)\n\t" \
    "v_smfmac_f32_16x16x64_bf16 v[224:227], v[236:239], v[200:207], v241\n\t" \
    "ds_read_b128 v[200:203], %[wl] offset:6144\n\t" \
    "ds_read_b128 v[204:207], %[wl] offset:7168\n\t" \
    "ds_read_b128 v[236:239], v240 offset:0\n\t" \
    "s_waitcnt lgkmcnt(6)\n\t" \
    "v_smfmac_f32_16x16x64_bf16 v[224:227], v[212:215], v[16:23], v241\n\t" \
    "v_smfmac_f32_16x16x64_bf16 v[244:247], v[212:215], v[24:31], v241\n\t" \
    "v_xor_b32 v240, 0x1c00, %[ab]\n\t" \
    "v_smfmac_f32_16x16x64_bf16 v[248:251], v[212:215], v[32:39], v241\n\t" \
    "ds_read_b128 v[212:215], v240 offset:0\n\t" \
    "s_waitcnt lgkmcnt(6)\n\t" \
    "v_smfmac_f32_16x16x64_bf16 v[224:227], v[216:219], v[228:235], v241\n\t" \
    "ds_read_b128 v[228:231], %[wl] offset:8192\n\t" \
    "ds_read_b128 v[232:235], %[wl] offset:9216\n\t" \
    "v_smfmac_f32_16x16x64_bf16 v[244:247], v[216:219], v[40:47], v241\n\t" \
    "v_and_b32 v240, 0x3ff, %[ab]\n\t" \
    "v_smfmac_f32_16x16x64_bf16 v[248:251], v[216:219], v[48:55], v241\n\t" \
    "ds_read_b128 v[216:219], v240 offset:8192\n\t" \
    "s_waitcnt lgkmcnt(8)\n\t" \
    "v_smfmac_f32_16x16x64_bf16 v[224:227], v[220:223], v[56:63], v241\n\t" \
    "v_smfmac_f32_16x16x64_bf16 v[244:247], v[220:223], v[64:71], v241\n\t" \
    "v_smfmac_f32_16x16x64_bf16 v[248:251], v[220:223], v[72:79], v241\n\t" \
    "ds_read_b128 v[220:223], v240 offset:9216\n\t" \
    "s_waitcnt lgkmcnt(8)\n\t" \
    "v_smfmac_f32_16x16x64_bf16 v[244:247], v[208:211], v[104:111], v241\n\t" \
    "s_waitcnt lgkmcnt(5)\n\t" \
    "v_smfmac_f32_16x16x64_bf16 v[244:247], v[236:239], v[120:127], v241\n\t" \
    "s_waitcnt lgkmcnt(4)\n\t" \
    "v_smfmac_f32_16x16x64_bf16 v[244:247], v[212:215], v[144:151], v241\n\t" \
    "s_waitcnt lgkmcnt(1)\n\t" \
    "v_smfmac_f32_16x16x64_bf16 v[244:247], v[216:219], v[160:167], v241\n\t" \
    "s_waitcnt lgkmcnt(0)\n\t" \
    "v_smfmac_f32_16x16x64_bf16 v[244:247], v[220:223], v[184:191], v241\n\t" \
    "v_smfmac_f32_16x16x64_bf16 v[248:251], v[208:211], v[112:119], v241\n\t" \
    "v_smfmac_f32_16x16x64_bf16 v[248:251], v[236:239], v[128:135], v241\n\t" \
    "v_smfmac_f32_16x16x64_bf16 v[248:251], v[212:215], v[152:159], v241\n\t" \
    "v_permlane32_swap_b32 v244, v246\n\t" \
    "v_permlane32_swap_b32 v245, v247\n\t" \
    "v_smfmac_f32_16x16x64_bf16 v[248:251], v[216:219], v[168:175], v241\n\t" \
    "v_add_f32 v244, v244, v246\n\t" \
    "v_add_f32 v245, v245, v247\n\t" \
    "v_smfmac_f32_16x16x64_bf16 v[248:251], v[220:223], v[192:199], v241\n\t" \
    "v_cvt_pk_bf16_f32 v244, v244, v245\n\t" \
    "ds_write_b16 %[dst], v244 offset:512\n\t" \
    "ds_write_b16_d16_hi %[dst], v244 offset:528\n\t" \
    "v_smfmac_f32_16x16x64_bf16 v[224:227], v[208:211], v[96:103], v241\n\t" \
    "v_smfmac_f32_16x16x64_bf16 v[224:227], v[236:239], v[200:207], v241\n\t" \
    "v_smfmac_f32_16x16x64_bf16 v[224:227], v[212:215], v[136:143], v241\n\t" \
    "v_permlane32_swap_b32 v248, v250\n\t" \
    "v_permlane32_swap_b32 v249, v251\n\t" \
    "v_smfmac_f32_16x16x64_bf16 v[224:227], v[216:219], v[228:235], v241\n\t" \
    "v_add_f32 v248, v248, v250\n\t" \
    "v_add_f32 v249, v249, v251\n\t" \
    "v_smfmac_f32_16x16x64_bf16 v[224:227], v[220:223], v[176:183], v241\n\t" \
    "v_cvt_pk_bf16_f32 v248, v248, v249\n\t" \
    "ds_write_b16 %[dst], v248 offset:1024\n\t" \
    "ds_write_b16_d16_hi %[dst], v248 offset:1040\n\t" \
    "s_nop 4\n\t" \
    "v_permlane32_swap_b32 v224, v226\n\t" \
    "v_permlane32_swap_b32 v225, v227\n\t" \
    "v_add_f32 v224, v224, v226\n\t" \
    "v_add_f32 v225, v225, v227\n\t" \
    "v_cvt_pk_bf16_f32 v224, v224, v225\n\t" \
    "ds_write_b16 %[dst], v224 offset:0\n\t" \
    "ds_write_b16_d16_hi %[dst], v224 offset:16\n\t"
#define ASM_VOX \
    "s_waitcnt lgkmcnt(0)\n\t" \
    "ds_read_b128 v[208:211], %[va] offset:2048\n\t" \
    "ds_read_b128 v[212:215], %[va] offset:3072\n\t" \
    "ds_read_b128 v[216:219], %[va] offset:4096\n\t" \
    "ds_read_b128 v[220:223], %[va] offset:5120\n\t" \
    "ds_read_b128 v[200:203], %[va] offset:0\n\t" \
    "ds_read_b128 v[204:207], %[va] offset:1024\n\t" \
    "ds_read_b128 v[236:239], %[wl] offset:0\n\t" \
    "ds_read_b128 v[240:243], %[wl] offset:1024\n\t" \
    "s_waitcnt lgkmcnt(7)\n\t" \
    "v_mfma_f32_16x16x32_bf16 v[244:247], v[208:211], v[0:3], 0\n\t" \
    "s_waitcnt lgkmcnt(6)\n\t" \
    "v_mfma_f32_16x16x32_bf16 v[244:247], v[212:215], v[4:7], v[244:247]\n\t" \
    "s_waitcnt lgkmcnt(5)\n\t" \
    "v_mfma_f32_16x16x32_bf16 v[244:247], v[216:219], v[8:11], v[244:247]\n\t" \
    "s_waitcnt lgkmcnt(4)\n\t" \
    "v_mfma_f32_16x16x32_bf16 v[244:247], v[220:223], v[12:15], v[244:247]\n\t" \
    "s_waitcnt lgkmcnt(1)\n\t" \
    "v_mfma_f32_16x16x32_bf16 v[244:247], v[200:203], v[236:239], v[244:247]\n\t" \
    "ds_read_b128 v[236:239], %[wl] offset:2048\n\t" \
    "s_waitcnt lgkmcnt(1)\n\t" \
    "v_mfma_f32_16x16x32_bf16 v[244:247], v[204:207], v[240:243], v[244:247]\n\t" \
    "ds_read_b128 v[240:243], %[wl] offset:3072\n\t" \
    "v_mfma_f32_16x16x32_bf16 v[248:251], v[208:211], v[40:43], 0\n\t" \
    "v_mfma_f32_16x16x32_bf16 v[248:251], v[212:215], v[44:47], v[248:251]\n\t" \
    "v_mfma_f32_16x16x32_bf16 v[248:251], v[216:219], v[48:51], v[248:251]\n\t" \
    "v_cvt_pk_bf16_f32 v224, v244, v245\n\t" \
    "v_mfma_f32_16x16x32_bf16 v[248:251], v[220:223], v[52:55], v[248:251]\n\t" \
    "v_cvt_pk_bf16_f32 v225, v246, v247\n\t" \
    "s_waitcnt lgkmcnt(1)\n\t" \
    "v_mfma_f32_16x16x32_bf16 v[248:251], v[200:203], v[236:239], v[248:251]\n\t" \
    "ds_read_b128 v[236:239], %[wl] offset:4096\n\t" \
    "s_waitcnt lgkmcnt(1)\n\t" \
    "v_mfma_f32_16x16x32_bf16 v[248:251], v[204:207], v[240:243], v[248:251]\n\t" \
    "ds_read_b128 v[240:243], %[wl] offset:5120\n\t" \
    "v_mfma_f32_16x16x32_bf16 v[244:247], v[208:211], v[80:83], 0\n\t" \
    "v_mfma_f32_16x16x32_bf16 v[244:247], v[212:215], v[84:87], v[244:247]\n\t" \
    "v_mfma_f32_16x16x32_bf16 v[244:247], v[216:219], v[88:91], v[244:247]\n\t" \
    "v_cvt_pk_bf16_f32 v226, v248, v249\n\t" \
    "v_mfma_f32_16x16x32_bf16 v[244:247], v[220:223], v[92:95], v[244:247]\n\t" \
    "v_cvt_pk_bf16_f32 v227, v250, v251\n\t" \
    "s_waitcnt lgkmcnt(1)\n\t" \
    "v_mfma_f32_16x16x32_bf16 v[244:247], v[200:203], v[236:239], v[244:247]\n\t" \
    "ds_read_b128 v[236:239], %[wl] offset:6144\n\t" \
    "s_waitcnt lgkmcnt(1)\n\t" \
    "v_mfma_f32_16x16x32_bf16 v[244:247], v[204:207], v[240:243], v[244:247]\n\t" \
    "ds_read_b128 v[240:243], %[wl] offset:7168\n\t" \
    "v_mfma_f32_16x16x32_bf16 v[248:251], v[208:211], v[120:123], 0\n\t" \
    "v_mfma_f32_16x16x32_bf16 v[248:251], v[212:215], v[124:127], v[248:251]\n\t" \
    "v_mfma_f32_16x16x32_bf16 v[248:251], v[216:219], v[128:131], v[248:251]\n\t" \
    "v_cvt_pk_bf16_f32 v228, v244, v245\n\t" \
    "v_mfma_f32_16x16x32_bf16 v[248:251], v[220:223], v[132:135], v[248:251]\n\t" \
    "v_cvt_pk_bf16_f32 v229, v246, v247\n\t" \
    "s_waitcnt lgkmcnt(1)\n\t" \
    "v_mfma_f32_16x16x32_bf16 v[248:251], v[200:203], v[236:239], v[248:251]\n\t" \
    "ds_read_b128 v[236:239], %[wl] offset:8192\n\t" \
    "s_waitcnt lgkmcnt(1)\n\t" \
    "v_mfma_f32_16x16x32_bf16 v[248:251], v[204:207], v[240:243], v[248:251]\n\t" \
    "ds_read_b128 v[240:243], %[wl] offset:9216\n\t" \
    "v_mfma_f32_16x16x32_bf16 v[244:247], v[208:211], v[160:163], 0\n\t" \
    "v_mfma_f32_16x16x32_bf16 v[244:247], v[212:215], v[164:167], v[244:247]\n\t" \
    "v_mfma_f32_16x16x32_bf16 v[244:247], v[216:219], v[168:171], v[244:247]\n\t" \
    "v_cvt_pk_bf16_f32 v230, v248, v249\n\t" \
    "v_mfma_f32_16x16x32_bf16 v[244:247], v[220:223], v[172:175], v[244:247]\n\t" \
    "v_cvt_pk_bf16_f32 v231, v250, v251\n\t" \
    "s_waitcnt lgkmcnt(1)\n\t" \
    "v_mfma_f32_16x16x32_bf16 v[244:247], v[200:203], v[236:239], v[244:247]\n\t" \
    "s_waitcnt lgkmcnt(0)\n\t" \
    "v_mfma_f32_16x16x32_bf16 v[244:247], v[204:207], v[240:243], v[244:247]\n\t" \
    "v_mfma_f32_16x16x32_bf16 v[248:251], v[208:211], v[24:27], 0\n\t" \
    "v_mfma_f32_16x16x32_bf16 v[248:251], v[212:215], v[28:31], v[248:251]\n\t" \
    "v_mfma_f32_16x16x32_bf16 v[248:251], v[216:219], v[32:35], v[248:251]\n\t" \
    "v_cvt_pk_bf16_f32 v232, v244, v245\n\t" \
    "v_mfma_f32_16x16x32_bf16 v[248:251], v[220:223], v[36:39], v[248:251]\n\t" \
    "v_cvt_pk_bf16_f32 v233, v246, v247\n\t" \
    "v_mfma_f32_16x16x32_bf16 v[248:251], v[200:203], v[16:19], v[248:251]\n\t" \
    "v_mfma_f32_16x16x32_bf16 v[248:251], v[204:207], v[20:23], v[248:251]\n\t" \
    "v_mfma_f32_16x16x32_bf16 v[244:247], v[208:211], v[64:67], 0\n\t" \
    "v_mfma_f32_16x16x32_bf16 v[244:247], v[212:215], v[68:71], v[244:247]\n\t" \
    "v_mfma_f32_16x16x32_bf16 v[244:247], v[216:219], v[72:75], v[244:247]\n\t" \
    "v_cvt_pk_bf16_f32 v236, v248, v249\n\t" \
    "v_mfma_f32_16x16x32_bf16 v[244:247], v[220:223], v[76:79], v[244:247]\n\t" \
    "v_cvt_pk_bf16_f32 v237, v250, v251\n\t" \
    "v_mfma_f32_16x16x32_bf16 v[244:247], v[200:203], v[56:59], v[244:247]\n\t" \
    "v_mfma_f32_16x16x32_bf16 v[244:247], v[204:207], v[60:63], v[244:247]\n\t" \
    "v_mfma_f32_16x16x32_bf16 v[248:251], v[208:211], v[104:107], 0\n\t" \
    "v_mfma_f32_16x16x32_bf16 v[248:251], v[212:215], v[108:111], v[248:251]\n\t" \
    "v_mfma_f32_16x16x32_bf16 v[248:251], v[216:219], v[112:115], v[248:251]\n\t" \
    "v_cvt_pk_bf16_f32 v238, v244, v245\n\t" \
    "v_mfma_f32_16x16x32_bf16 v[248:251], v[220:223], v[116:119], v[248:251]\n\t" \
    "v_cvt_pk_bf16_f32 v239, v246, v247\n\t" \
    "v_mfma_f32_16x16x32_bf16 v[248:251], v[200:203], v[96:99], v[248:251]\n\t" \
    "v_mfma_f32_16x16x32_bf16 v[248:251], v[204:207], v[100:103], v[248:251]\n\t" \
    "v_mfma_f32_16x16x32_bf16 v[244:247], v[208:211], v[144:147], 0\n\t" \
    "v_mfma_f32_16x16x32_bf16 v[244:247], v[212:215], v[148:151], v[244:247]\n\t" \
    "v_mfma_f32_16x16x32_bf16 v[244:247], v[216:219], v[152:155], v[244:247]\n\t" \
    "v_cvt_pk_bf16_f32 v240, v248, v249\n\t" \
    "v_mfma_f32_16x16x32_bf16 v[244:247], v[220:223], v[156:159], v[244:247]\n\t" \
    "v_cvt_pk_bf16_f32 v241, v250, v251\n\t" \
    "v_mfma_f32_16x16x32_bf16 v[244:247], v[200:203], v[136:139], v[244:247]\n\t" \
    "v_mfma_f32_16x16x32_bf16 v[244:247], v[204:207], v[140:143], v[244:247]\n\t" \
    "v_mfma_f32_16x16x32_bf16 v[248:251], v[208:211], v[184:187], 0\n\t" \
    "ds_read_b128 v[208:211], %[va] offset:8192\n\t" \
    "v_mfma_f32_16x16x32_bf16 v[248:251], v[212:215], v[188:191], v[248:251]\n\t" \
    "ds_read_b128 v[212:215], %[va] offset:9216\n\t" \
    "v_mfma_f32_16x16x32_bf16 v[248:251], v[216:219], v[192:195], v[248:251]\n\t" \
    "ds_read_b128 v[216:219], %[va] offset:10240\n\t" \
    "v_cvt_pk_bf16_f32 v242, v244, v245\n\t" \
    "v_mfma_f32_16x16x32_bf16 v[248:251], v[220:223], v[196:199], v[248:251]\n\t" \
    "ds_read_b128 v[220:223], %[va] offset:11264\n\t" \
    "v_cvt_pk_bf16_f32 v243, v246, v247\n\t" \
    "v_mfma_f32_16x16x32_bf16 v[248:251], v[200:203], v[176:179], v[248:251]\n\t" \
    "ds_read_b128 v[200:203], %[va] offset:6144\n\t" \
    "v_mfma_f32_16x16x32_bf16 v[248:251], v[204:207], v[180:183], v[248:251]\n\t" \
    "ds_read_b128 v[204:207], %[va] offset:7168\n\t" \
    "s_waitcnt lgkmcnt(5)\n\t" \
    "v_mfma_f32_16x16x32_bf16 v[244:247], v[208:211], v[24:27], 0\n\t" \
    "s_waitcnt lgkmcnt(4)\n\t" \
    "v_mfma_f32_16x16x32_bf16 v[244:247], v[212:215], v[28:31], v[244:247]\n\t" \
    "s_waitcnt lgkmcnt(3)\n\t" \
    "v_mfma_f32_16x16x32_bf16 v[244:247], v[216:219], v[32:35], v[244:247]\n\t" \
    "v_cvt_pk_bf16_f32 v234, v248, v249\n\t" \
    "s_waitcnt lgkmcnt(2)\n\t" \
    "v_mfma_f32_16x16x32_bf16 v[244:247], v[220:223], v[36:39], v[244:247]\n\t" \
    "v_cvt_pk_bf16_f32 v235, v250, v251\n\t" \
    "s_waitcnt lgkmcnt(1)\n\t" \
    "v_mfma_f32_16x16x32_bf16 v[244:247], v[200:203], v[16:19], v[244:247]\n\t" \
    "s_waitcnt lgkmcnt(0)\n\t" \
    "v_mfma_f32_16x16x32_bf16 v[244:247], v[204:207], v[20:23], v[244:247]\n\t" \
    "v_mfma_f32_16x16x32_bf16 v[248:251], v[208:211], v[64:67], 0\n\t" \
    "s_add_i32 %[so], %[so], 65536\n\t" \
    "v_mfma_f32_16x16x32_bf16 v[248:251], v[212:215], v[68:71], v[248:251]\n\t" \
    "s_nop 1\n\t" \
    "v_cvt_pk_bf16_f32 v247, v246, v247\n\t" \
    "v_mfma_f32_16x16x32_bf16 v[248:251], v[216:219], v[72:75], v[248:251]\n\t" \
    "v_cvt_pk_bf16_f32 v246, v244, v245\n\t" \
    "v_mfma_f32_16x16x32_bf16 v[248:251], v[220:223], v[76:79], v[248:251]\n\t" \
    "v_mov_b64 v[244:245], v[236:237]\n\t" \
    "buffer_store_dwordx4 v[244:247], %[l16], %[rd], %[so] offen sc1\n\t" \
    "v_mfma_f32_16x16x32_bf16 v[248:251], v[200:203], v[56:59], v[248:251]\n\t" \
    "v_mfma_f32_16x16x32_bf16 v[248:251], v[204:207], v[60:63], v[248:251]\n\t" \
    "v_mfma_f32_16x16x32_bf16 v[244:247], v[208:211], v[104:107], 0\n\t" \
    "s_add_i32 %[so], %[so], 131072\n\t" \
    "v_mfma_f32_16x16x32_bf16 v[244:247], v[212:215], v[108:111], v[244:247]\n\t" \
    "s_nop 1\n\t" \
    "v_cvt_pk_bf16_f32 v251, v250, v251\n\t" \
    "v_mfma_f32_16x16x32_bf16 v[244:247], v[216:219], v[112:115], v[244:247]\n\t" \
    "v_cvt_pk_bf16_f32 v250, v248, v249\n\t" \
    "v_mfma_f32_16x16x32_bf16 v[244:247], v[220:223], v[116:119], v[244:247]\n\t" \
    "v_mov_b64 v[248:249], v[238:239]\n\t" \
    "buffer_store_dwordx4 v[248:251], %[l16], %[rd], %[so] offen sc1\n\t" \
    "v_mfma_f32_16x16x32_bf16 v[244:247], v[200:203], v[96:99], v[244:247]\n\t" \
    "v_mfma_f32_16x16x32_bf16 v[244:247], v[204:207], v[100:103], v[244:247]\n\t" \
    "v_mfma_f32_16x16x32_bf16 v[248:251], v[208:211], v[144:147], 0\n\t" \
    "s_add_i32 %[so], %[so], 131072\n\t" \
    "v_mfma_f32_16x16x32_bf16 v[248:251], v[212:215], v[148:151], v[248:251]\n\t" \
    "s_nop 1\n\t" \
    "v_cvt_pk_bf16_f32 v247, v246, v247\n\t" \
    "v_mfma_f32_16x16x32_bf16 v[248:251], v[216:219], v[152:155], v[248:251]\n\t" \
    "v_cvt_pk_bf16_f32 v246, v244, v245\n\t" \
    "v_mfma_f32_16x16x32_bf16 v[248:251], v[220:223], v[156:159], v[248:251]\n\t" \
    "v_mov_b64 v[244:245], v[240:241]\n\t" \
    "buffer_store_dwordx4 v[244:247], %[l16], %[rd], %[so] offen sc1\n\t" \
    "v_mfma_f32_16x16x32_bf16 v[248:251], v[200:203], v[136:139], v[248:251]\n\t" \
    "v_mfma_f32_16x16x32_bf16 v[248:251], v[204:207], v[140:143], v[248:251]\n\t" \
    "v_mfma_f32_16x16x32_bf16 v[244:247], v[208:211], v[184:187], 0\n\t" \
    "s_add_i32 %[so], %[so], 131072\n\t" \
    "v_mfma_f32_16x16x32_bf16 v[244:247], v[212:215], v[188:191], v[244:247]\n\t" \
    "s_nop 1\n\t" \
    "v_cvt_pk_bf16_f32 v251, v250, v251\n\t" \
    "v_mfma_f32_16x16x32_bf16 v[244:247], v[216:219], v[192:195], v[244:247]\n\t" \
    "v_cvt_pk_bf16_f32 v250, v248, v249\n\t" \
    "v_mfma_f32_16x16x32_bf16 v[244:247], v[220:223], v[196:199], v[244:247]\n\t" \
    "v_mov_b64 v[248:249], v[242:243]\n\t" \
    "buffer_store_dwordx4 v[248:251], %[l16], %[rd], %[so] offen sc1\n\t" \
    "v_mfma_f32_16x16x32_bf16 v[244:247], v[200:203], v[176:179], v[244:247]\n\t" \
    "v_mfma_f32_16x16x32_bf16 v[244:247], v[204:207], v[180:183], v[244:247]\n\t" \
    "ds_read_b128 v[236:239], %[wl] offset:0\n\t" \
    "ds_read_b128 v[240:243], %[wl] offset:1024\n\t" \
    "v_mfma_f32_16x16x32_bf16 v[248:251], v[208:211], v[0:3], 0\n\t" \
    "s_add_i32 %[so], %[so], 131072\n\t" \
    "v_mfma_f32_16x16x32_bf16 v[248:251], v[212:215], v[4:7], v[248:251]\n\t" \
    "v_cvt_pk_bf16_f32 v247, v246, v247\n\t" \
    "v_mfma_f32_16x16x32_bf16 v[248:251], v[216:219], v[8:11], v[248:251]\n\t" \
    "v_cvt_pk_bf16_f32 v246, v244, v245\n\t" \
    "v_mfma_f32_16x16x32_bf16 v[248:251], v[220:223], v[12:15], v[248:251]\n\t" \
    "v_mov_b64 v[244:245], v[234:235]\n\t" \
    "buffer_store_dwordx4 v[244:247], %[l16], %[rd], %[so] offen sc1\n\t" \
    "s_waitcnt lgkmcnt(1)\n\t" \
    "v_mfma_f32_16x16x32_bf16 v[248:251], v[200:203], v[236:239], v[248:251]\n\t" \
    "ds_read_b128 v[236:239], %[wl] offset:2048\n\t" \
    "s_waitcnt lgkmcnt(1)\n\t" \
    "v_mfma_f32_16x16x32_bf16 v[248:251], v[204:207], v[240:243], v[248:251]\n\t" \
    "ds_read_b128 v[240:243], %[wl] offset:3072\n\t" \
    "v_mfma_f32_16x16x32_bf16 v[244:247], v[208:211], v[40:43], 0\n\t" \
    "s_add_i32 %[so], %[so], -589824\n\t" \
    "v_mfma_f32_16x16x32_bf16 v[244:247], v[212:215], v[44:47], v[244:247]\n\t" \
    "s_nop 0\n\t" \
    "v_cvt_pk_bf16_f32 v251, v250, v251\n\t" \
    "v_mfma_f32_16x16x32_bf16 v[244:247], v[216:219], v[48:51], v[244:247]\n\t" \
    "v_cvt_pk_bf16_f32 v250, v248, v249\n\t" \
    "v_mfma_f32_16x16x32_bf16 v[244:247], v[220:223], v[52:55], v[244:247]\n\t" \
    "v_mov_b64 v[248:249], v[224:225]\n\t" \
    "buffer_store_dwordx4 v[248:251], %[l16], %[rd], %[so] offen sc1\n\t" \
    "s_waitcnt lgkmcnt(1)\n\t" \
    "v_mfma_f32_16x16x32_bf16 v[244:247], v[200:203], v[236:239], v[244:247]\n\t" \
    "ds_read_b128 v[236:239], %[wl] offset:4096\n\t" \
    "s_waitcnt lgkmcnt(1)\n\t" \
    "v_mfma_f32_16x16x32_bf16 v[244:247], v[204:207], v[240:243], v[244:247]\n\t" \
    "ds_read_b128 v[240:243], %[wl] offset:5120\n\t" \
    "v_mfma_f32_16x16x32_bf16 v[248:251], v[208:211], v[80:83], 0\n\t" \
    "s_add_i32 %[so], %[so], 131072\n\t" \
    "v_mfma_f32_16x16x32_bf16 v[248:251], v[212:215], v[84:87], v[248:251]\n\t" \
    "s_nop 0\n\t" \
    "v_cvt_pk_bf16_f32 v247, v246, v247\n\t" \
    "v_mfma_f32_16x16x32_bf16 v[248:251], v[216:219], v[88:91], v[248:251]\n\t" \
    "v_cvt_pk_bf16_f32 v246, v244, v245\n\t" \
    "v_mfma_f32_16x16x32_bf16 v[248:251], v[220:223], v[92:95], v[248:251]\n\t" \
    "v_mov_b64 v[244:245], v[226:227]\n\t" \
    "buffer_store_dwordx4 v[244:247], %[l16], %[rd], %[so] offen sc1\n\t" \
    "s_waitcnt lgkmcnt(1)\n\t" \
    "v_mfma_f32_16x16x32_bf16 v[248:251], v[200:203], v[236:239], v[248:251]\n\t" \
    "ds_read_b128 v[236:239], %[wl] offset:6144\n\t" \
    "s_waitcnt lgkmcnt(1)\n\t" \
    "v_mfma_f32_16x16x32_bf16 v[248:251], v[204:207], v[240:243], v[248:251]\n\t" \
    "ds_read_b128 v[240:243], %[wl] offset:7168\n\t" \
    "v_mfma_f32_16x16x32_bf16 v[244:247], v[208:211], v[120:123], 0\n\t" \
    "s_add_i32 %[so], %[so], 131072\n\t" \
    "v_mfma_f32_16x16x32_bf16 v[244:247], v[212:215], v[124:127], v[244:247]\n\t" \
    "s_nop 0\n\t" \
    "v_cvt_pk_bf16_f32 v251, v250, v251\n\t" \
    "v_mfma_f32_16x16x32_bf16 v[244:247], v[216:219], v[128:131], v[244:247]\n\t" \
    "v_cvt_pk_bf16_f32 v250, v248, v249\n\t" \
    "v_mfma_f32_16x16x32_bf16 v[244:247], v[220:223], v[132:135], v[244:247]\n\t" \
    "v_mov_b64 v[248:249], v[228:229]\n\t" \
    "buffer_store_dwordx4 v[248:251], %[l16], %[rd], %[so] offen sc1\n\t" \
    "s_waitcnt lgkmcnt(1)\n\t" \
    "v_mfma_f32_16x16x32_bf16 v[244:247], v[200:203], v[236:239], v[244:247]\n\t" \
    "ds_read_b128 v[236:239], %[wl] offset:8192\n\t" \
    "s_waitcnt lgkmcnt(1)\n\t" \
    "v_mfma_f32_16x16x32_bf16 v[244:247], v[204:207], v[240:243], v[244:247]\n\t" \
    "ds_read_b128 v[240:243], %[wl] offset:9216\n\t" \
    "v_mfma_f32_16x16x32_bf16 v[248:251], v[208:211], v[160:163], 0\n\t" \
    "s_add_i32 %[so], %[so], 131072\n\t" \
    "v_mfma_f32_16x16x32_bf16 v[248:251], v[212:215], v[164:167], v[248:251]\n\t" \
    "s_nop 0\n\t" \
    "v_cvt_pk_bf16_f32 v247, v246, v247\n\t" \
    "v_mfma_f32_16x16x32_bf16 v[248:251], v[216:219], v[168:171], v[248:251]\n\t" \
    "v_cvt_pk_bf16_f32 v246, v244, v245\n\t" \
    "v_mfma_f32_16x16x32_bf16 v[248:251], v[220:223], v[172:175], v[248:251]\n\t" \
    "v_mov_b64 v[244:245], v[230:231]\n\t" \
    "buffer_store_dwordx4 v[244:247], %[l16], %[rd], %[so] offen sc1\n\t" \
    "s_waitcnt lgkmcnt(1)\n\t" \
    "v_mfma_f32_16x16x32_bf16 v[248:251], v[200:203], v[236:239], v[248:251]\n\t" \
    "s_waitcnt lgkmcnt(0)\n\t" \
    "v_mfma_f32_16x16x32_bf16 v[248:251], v[204:207], v[240:243], v[248:251]\n\t" \
    "s_add_i32 %[so], %[so], 131072\n\t" \
    "s_nop 7\n\t" \
    "v_cvt_pk_bf16_f32 v251, v250, v251\n\t" \
    "v_cvt_pk_bf16_f32 v250, v248, v249\n\t" \
    "v_mov_b64 v[248:249], v[232:233]\n\t" \
    "buffer_store_dwordx4 v[248:251], %[l16], %[rd], %[so] offen sc1\n\t" \
    "s_nop 1\n\t"
#define ASM_CRIT2 \
    "s_waitcnt lgkmcnt(0)\n\t" \
    "s_barrier\n\t" ASM_ST \
    "ds_read_b128 v[204:207], v200 offset:11264\n\t" \
    "ds_read_b128 v[208:211], v200 offset:12288\n\t" \
    "ds_read_b128 v[200:203], v200 offset:10240\n\t" \
    "s_waitcnt lgkmcnt(2)\n\t" \
    "v_smfmac_f32_16x16x64_bf16 v[224:227], v[204:207], v[24:31], %[sidx]\n\t" \
    "s_waitcnt lgkmcnt(1)\n\t" \
    "v_smfmac_f32_16x16x64_bf16 v[224:227], v[208:211], v[32:39], %[sidx]\n\t" \
    "s_waitcnt lgkmcnt(0)\n\t" \
    "v_smfmac_f32_16x16x64_bf16 v[224:227], v[200:203], v[16:23], %[sidx]\n\t" \
    "v_smfmac_f32_16x16x64_bf16 v[220:223], v[204:207], v[0:7], %[sidx]\n\t" \
    "v_smfmac_f32_16x16x64_bf16 v[220:223], v[208:211], v[8:15], %[sidx]\n\t" \
    "v_smfmac_f32_16x16x64_bf16 v[220:223], v[200:203], v[212:219], %[sidx]\n\t" \
    "ds_read_b128 v[212:215], %[wl] offset:2048\n\t" \
    "ds_read_b128 v[216:219], %[wl] offset:3072\n\t" \
    "v_smfmac_f32_16x16x64_bf16 v[232:235], v[204:207], v[64:71], %[sidx]\n\t" \
    "v_smfmac_f32_16x16x64_bf16 v[232:235], v[208:211], v[72:79], %[sidx]\n\t" \
    "v_smfmac_f32_16x16x64_bf16 v[232:235], v[200:203], v[56:63], %[sidx]\n\t" \
    "v_permlane32_swap_b32 v220, v224\n\t" \
    "v_permlane32_swap_b32 v221, v225\n\t" \
    "v_permlane32_swap_b32 v222, v226\n\t" \
    "v_smfmac_f32_16x16x64_bf16 v[228:231], v[204:207], v[40:47], %[sidx]\n\t" \
    "v_permlane32_swap_b32 v223, v227\n\t" \
    "v_add_f32 v220, v220, v224\n\t" \
    "v_add_f32 v221, v221, v225\n\t" \
    "v_smfmac_f32_16x16x64_bf16 v[228:231], v[208:211], v[48:55], %[sidx]\n\t" \
    "v_add_f32 v222, v222, v226\n\t" \
    "v_add_f32 v223, v223, v227\n\t" \
    "v_cvt_pk_bf16_f32 v226, v220, v221\n\t" \
    "s_waitcnt lgkmcnt(0)\n\t" \
    "v_smfmac_f32_16x16x64_bf16 v[228:231], v[200:203], v[212:219], %[sidx]\n\t" \
    "ds_read_b128 v[212:215], %[wl] offset:4096\n\t" \
    "ds_read_b128 v[216:219], %[wl] offset:5120\n\t" \
    "v_cvt_pk_bf16_f32 v227, v222, v223\n\t" \
    "v_min_u32 v226, -2, v226\n\t" \
    "v_min_u32 v227, -2, v227\n\t" \
    "v_smfmac_f32_16x16x64_bf16 v[240:243], v[204:207], v[104:111], %[sidx]\n\t" \
    "v_smfmac_f32_16x16x64_bf16 v[240:243], v[208:211], v[112:119], %[sidx]\n\t" \
    "v_smfmac_f32_16x16x64_bf16 v[240:243], v[200:203], v[96:103], %[sidx]\n\t" \
    "v_permlane32_swap_b32 v228, v232\n\t" \
    "v_permlane32_swap_b32 v229, v233\n\t" \
    "v_permlane32_swap_b32 v230, v234\n\t" \
    "v_smfmac_f32_16x16x64_bf16 v[236:239], v[204:207], v[80:87], %[sidx]\n\t" \
    "v_permlane32_swap_b32 v231, v235\n\t" \
    "v_add_f32 v228, v228, v232\n\t" \
    "v_add_f32 v229, v229, v233\n\t" \
    "v_smfmac_f32_16x16x64_bf16 v[236:239], v[208:211], v[88:95], %[sidx]\n\t" \
    "v_add_f32 v230, v230, v234\n\t" \
    "v_add_f32 v231, v231, v235\n\t" \
    "v_cvt_pk_bf16_f32 v228, v228, v229\n\t" \
    "s_waitcnt lgkmcnt(0)\n\t" \
    "v_smfmac_f32_16x16x64_bf16 v[236:239], v[200:203], v[212:219], %[sidx]\n\t" \
    "ds_read_b128 v[212:215], %[wl] offset:6144\n\t" \
    "ds_read_b128 v[216:219], %[wl] offset:7168\n\t" \
    "v_cvt_pk_bf16_f32 v229, v230, v231\n\t" \
    "v_min_u32 v228, -2, v228\n\t" \
    "v_min_u32 v229, -2, v229\n\t" \
    "s_nop 1\n\t" \
    "v_permlane16_swap_b32 v226, v228\n\t" \
    "v_permlane16_swap_b32 v227, v229\n\t" \
    "buffer_store_dwordx4 v[226:229], %[vo], %[rr], %[so] offen sc1\n\t" \
    "s_add_u32 %[so], %[so], 0x10000\n\t" \
    "v_smfmac_f32_16x16x64_bf16 v[248:251], v[204:207], v[144:151], %[sidx]\n\t" \
    "v_mov_b64 v[220:221], 0\n\t" \
    "v_mov_b64 v[222:223], 0\n\t" \
    "v_smfmac_f32_16x16x64_bf16 v[248:251], v[208:211], v[152:159], %[sidx]\n\t" \
    "v_mov_b64 v[224:225], 0\n\t" \
    "v_mov_b64 v[226:227], 0\n\t" \
    "v_smfmac_f32_16x16x64_bf16 v[248:251], v[200:203], v[136:143], %[sidx]\n\t" \
    "v_permlane32_swap_b32 v236, v240\n\t" \
    "v_permlane32_swap_b32 v237, v241\n\t" \
    "v_permlane32_swap_b32 v238, v242\n\t" \
    "v_smfmac_f32_16x16x64_bf16 v[244:247], v[204:207], v[120:127], %[sidx]\n\t" \
    "v_permlane32_swap_b32 v239, v243\n\t" \
    "v_add_f32 v236, v236, v240\n\t" \
    "v_add_f32 v237, v237, v241\n\t" \
    "v_smfmac_f32_16x16x64_bf16 v[244:247], v[208:211], v[128:135], %[sidx]\n\t" \
    "v_add_f32 v238, v238, v242\n\t" \
    "v_add_f32 v239, v239, v243\n\t" \
    "v_cvt_pk_bf16_f32 v242, v236, v237\n\t" \
    "s_waitcnt lgkmcnt(0)\n\t" \
    "v_smfmac_f32_16x16x64_bf16 v[244:247], v[200:203], v[212:219], %[sidx]\n\t" \
    "ds_read_b128 v[212:215], %[wl] offset:8192\n\t" \
    "ds_read_b128 v[216:219], %[wl] offset:9216\n\t" \
    "v_cvt_pk_bf16_f32 v243, v238, v239\n\t" \
    "v_min_u32 v242, -2, v242\n\t" \
    "v_min_u32 v243, -2, v243\n\t" \
    "v_smfmac_f32_16x16x64_bf16 v[224:227], v[204:207], v[184:191], %[sidx]\n\t" \
    "v_and_b32 v232, 0x3fff, %[vo]\n\t" \
    "v_bfe_u32 v233, %[vo], 14, 1\n\t" \
    "v_smfmac_f32_16x16x64_bf16 v[224:227], v[208:211], v[192:199], %[sidx]\n\t" \
    "v_lshl_add_u32 v232, v233, 3, v232\n\t" \
    "v_permlane32_swap_b32 v244, v248\n\t" \
    "v_smfmac_f32_16x16x64_bf16 v[224:227], v[200:203], v[176:183], %[sidx]\n\t" \
    "v_permlane32_swap_b32 v245, v249\n\t" \
    "v_permlane32_swap_b32 v246, v250\n\t" \
    "v_permlane32_swap_b32 v247, v251\n\t" \
    "v_smfmac_f32_16x16x64_bf16 v[220:223], v[204:207], v[160:167], %[sidx]\n\t" \
    "v_add_f32 v244, v244, v248\n\t" \
    "v_add_f32 v245, v245, v249\n\t" \
    "v_add_f32 v246, v246, v250\n\t" \
    "v_smfmac_f32_16x16x64_bf16 v[220:223], v[208:211], v[168:175], %[sidx]\n\t" \
    "v_add_f32 v247, v247, v251\n\t" \
    "v_cvt_pk_bf16_f32 v244, v244, v245\n\t" \
    "v_cvt_pk_bf16_f32 v245, v246, v247\n\t" \
    "s_waitcnt lgkmcnt(0)\n\t" \
    "v_smfmac_f32_16x16x64_bf16 v[220:223], v[200:203], v[212:219], %[sidx]\n\t" \
    "v_min_u32 v244, -2, v244\n\t" \
    "v_min_u32 v245, -2, v245\n\t" \
    "s_nop 1\n\t" \
    "v_permlane16_swap_b32 v242, v244\n\t" \
    "v_permlane16_swap_b32 v243, v245\n\t" \
    "buffer_store_dwordx4 v[242:245], %[vo], %[rr], %[so] offen sc1\n\t" \
    "s_add_u32 %[so], %[so], 0x10000\n\t" \
    "s_nop 0\n\t" \
    "v_permlane32_swap_b32 v220, v224\n\t" \
    "v_permlane32_swap_b32 v221, v225\n\t" \
    "v_permlane32_swap_b32 v222, v226\n\t" \
    "v_permlane32_swap_b32 v223, v227\n\t" \
    "v_add_f32 v220, v220, v224\n\t" \
    "v_add_f32 v221, v221, v225\n\t" \
    "v_add_f32 v222, v222, v226\n\t" \
    "v_add_f32 v223, v223, v227\n\t" \
    "v_cvt_pk_bf16_f32 v220, v220, v221\n\t" \
    "v_cvt_pk_bf16_f32 v221, v222, v223\n\t" \
    "v_min_u32 v220, -2, v220\n\t" \
    "v_min_u32 v221, -2, v221\n\t" \
    "buffer_store_dwordx2 v[220:221], v232, %[rr], %[so] offen sc1\n\t" \
    "s_nop 1\n\t"
#define ASM_SM \
    "v_mbcnt_lo_u32_b32 v252, -1, 0\n\t" \
    "v_mbcnt_hi_u32_b32 v252, -1, v252\n\t" \
    "v_lshlrev_b32 v252, 4, v252\n\t" \
    "v_lshrrev_b32 v253, 1, v252\n\t" \
    "v_xor_b32 v253, v253, v252\n\t" \
    "v_and_b32 v253, 0x20, v253\n\t" \
    "v_mul_u32_u24 v253, 3, v253\n\t" \
    "v_xor_b32 v253, v253, v252\n\t" \
    "ds_read_b128 v[200:203], v253 offset:28672\n\t" \
    "ds_read_b128 v[204:207], v253 offset:29696\n\t" \
    "ds_read_b128 v[208:211], v253 offset:34816\n\t" \
    "ds_read_b128 v[212:215], v253 offset:35840\n\t" \
    "ds_read_b128 v[216:219], v253 offset:30720\n\t" \
    "ds_read_b128 v[220:223], v253 offset:31744\n\t" \
    "ds_read_b128 v[224:227], v253 offset:36864\n\t" \
    "ds_read_b128 v[228:231], v253 offset:37888\n\t" \
    "v_lshrrev_b32 v254, 1, v252\n\t" \
    "v_and_b32 v254, 0x180, v254\n\t" \
    "v_and_or_b32 v254, v252, %[c70], v254\n\t" \
    "v_bfe_i32 v255, v252, 7, 1\n\t" \
    "v_and_b32 v255, 0xaaaa, v255\n\t" \
    "v_or_b32 v255, 0x4444, v255\n\t" \
    "v_mov_b64 v[232:233], 0\n\t" \
    "v_mov_b64 v[234:235], 0\n\t" \
    "v_mov_b64 v[236:237], 0\n\t" \
    "v_mov_b64 v[238:239], 0\n\t" \
    "s_waitcnt lgkmcnt(0)\n\t" \
    "s_barrier\n\t" ASM_ST \
    "s_setprio 3\n\t" \
    "ds_read_b128 v[240:243], v252 offset:10240\n\t" \
    "ds_read_b128 v[244:247], v252 offset:11264\n\t" \
    "ds_read_b128 v[248:251], v252 offset:12288\n\t" \
    "s_waitcnt lgkmcnt(2)\n\t" \
    "v_smfmac_f32_16x16x64_bf16 v[232:235], v[240:243], v[200:207], v255\n\t" \
    "ds_read_b128 v[200:203], v253 offset:32768\n\t" \
    "ds_read_b128 v[204:207], v253 offset:33792\n\t" \
    "v_smfmac_f32_16x16x64_bf16 v[236:239], v[240:243], v[208:215], v255\n\t" \
    "ds_read_b128 v[208:211], v253 offset:38912\n\t" \
    "ds_read_b128 v[212:215], v253 offset:39936\n\t" \
    "s_waitcnt lgkmcnt(5)\n\t" \
    "v_smfmac_f32_16x16x64_bf16 v[232:235], v[244:247], v[216:223], v255\n\t" \
    "ds_read_b128 v[216:219], v254 offset:13312\n\t" \
    "ds_read_b128 v[220:223], v254 offset:13824\n\t" \
    "v_smfmac_f32_16x16x64_bf16 v[236:239], v[244:247], v[224:231], v255\n\t" \
    "ds_read_b128 v[224:227], v254 offset:14336\n\t" \
    "ds_read_b128 v[228:231], v254 offset:14848\n\t" \
    "s_waitcnt lgkmcnt(6)\n\t" \
    "v_smfmac_f32_16x16x64_bf16 v[232:235], v[248:251], v[200:207], v255\n\t" \
    "s_waitcnt lgkmcnt(4)\n\t" \
    "v_smfmac_f32_16x16x64_bf16 v[236:239], v[248:251], v[208:215], v255\n\t" \
    "ds_read_b128 v[208:211], v254 offset:15360\n\t" \
    "ds_read_b128 v[212:215], v254 offset:15872\n\t" \
    "v_mov_b64 v[204:205], 0\n\t" \
    "v_mov_b64 v[206:207], 0\n\t" \
    "s_nop 1\n\t" \
    "s_waitcnt lgkmcnt(4)\n\t" \
    "v_smfmac_f32_16x16x64_bf16 v[204:207], v[240:243], v[216:223], v255\n\t" \
    "s_waitcnt lgkmcnt(2)\n\t" \
    "v_smfmac_f32_16x16x64_bf16 v[204:207], v[244:247], v[224:231], v255\n\t" \
    "v_bfe_u32 v253, v252, 5, 1\n\t" \
    "v_bfe_i32 v202, v252, 4, 1\n\t" \
    "v_lshlrev_b32 v200, 1, v252\n\t" \
    "v_and_b32 v200, 0x180, v200\n\t" \
    "v_permlane32_swap_b32 v232, v234\n\t" \
    "v_permlane32_swap_b32 v233, v235\n\t" \
    "v_permlane32_swap_b32 v236, v238\n\t" \
    "v_permlane32_swap_b32 v237, v239\n\t" \
    "v_lshrrev_b32 v201, 2, v252\n\t" \
    "v_lshrrev_b32 v203, 4, v252\n\t" \
    "v_and_b32 v201, 0x40, v201\n\t" \
    "v_and_or_b32 v201, v203, 32, v201\n\t" \
    "v_and_or_b32 v201, v252, 16, v201\n\t" \
    "v_pk_add_f32 v[232:233], v[232:233], v[234:235]\n\t" \
    "v_pk_add_f32 v[234:235], v[236:237], v[238:239]\n\t" \
    "v_or_b32 v200, v200, v201\n\t" \
    "v_mad_u32_u24 v200, v253, %[cs], v200\n\t" \
    "v_mad_u32_u24 v201, v253, %[cs], v201\n\t" \
    "v_lshrrev_b32 v203, 3, v252\n\t" \
    "v_bfe_u32 v253, v252, 7, 1\n\t" \
    "s_waitcnt lgkmcnt(0)\n\t" \
    "v_smfmac_f32_16x16x64_bf16 v[204:207], v[248:251], v[208:215], v255\n\t" \
    "v_and_or_b32 v201, v203, 8, v201\n\t" \
    "v_and_b32 v202, 0x06060606, v202\n\t" \
    "v_mad_u32_u24 v201, v253, %[cp], v201\n\t" \
    "v_xor_b32 v202, 0x01000504, v202\n\t" \
    "v_max_f32 v236, v232, v234\n\t" \
    "v_max_f32 v237, v233, v235\n\t" \
    "s_nop 1\n\t" \
    "v_permlane32_swap_b32 v204, v206\n\t" \
    "v_permlane32_swap_b32 v205, v207\n\t" \
    "v_add_f32 v204, v204, v206\n\t" \
    "v_add_f32 v205, v205, v207\n\t" \
    "v_max_f32 v236, v236, v204\n\t" \
    "v_max_f32 v237, v237, v205\n\t" \
    "s_nop 0\n\t" \
    "v_max_f32_dpp v236, v236, v236 quad_perm:[1,0,3,2] row_mask:0xf bank_mask:0xf\n\t" \
    "v_max_f32_dpp v237, v237, v237 quad_perm:[1,0,3,2] row_mask:0xf bank_mask:0xf\n\t" \
    "s_nop 0\n\t" \
    "v_max_f32_dpp v236, v236, v236 quad_perm:[2,3,0,1] row_mask:0xf bank_mask:0xf\n\t" \
    "v_max_f32_dpp v237, v237, v237 quad_perm:[2,3,0,1] row_mask:0xf bank_mask:0xf\n\t" \
    "s_nop 0\n\t" \
    "v_max_f32_dpp v236, v236, v236 row_half_mirror row_mask:0xf bank_mask:0xf\n\t" \
    "v_max_f32_dpp v237, v237, v237 row_half_mirror row_mask:0xf bank_mask:0xf\n\t" \
    "s_nop 0\n\t" \
    "v_max_f32_dpp v236, v236, v236 row_mirror row_mask:0xf bank_mask:0xf\n\t" \
    "v_max_f32_dpp v237, v237, v237 row_mirror row_mask:0xf bank_mask:0xf\n\t" \
    "v_mul_f32 v236, %[scl], v236\n\t" \
    "v_mul_f32 v237, %[scl], v237\n\t" \
    "v_fma_f32 v232, v232, %[scl], -v236\n\t" \
    "v_fma_f32 v233, v233, %[scl], -v237\n\t" \
    "v_fma_f32 v234, v234, %[scl], -v236\n\t" \
    "v_fma_f32 v235, v235, %[scl], -v237\n\t" \
    "v_exp_f32 v232, v232\n\t" \
    "v_exp_f32 v233, v233\n\t" \
    "v_fma_f32 v204, v204, %[scl], -v236\n\t" \
    "v_fma_f32 v205, v205, %[scl], -v237\n\t" \
    "v_exp_f32 v234, v234\n\t" \
    "v_exp_f32 v235, v235\n\t" \
    "v_exp_f32 v204, v204\n\t" \
    "v_exp_f32 v205, v205\n\t" \
    "v_add_f32 v238, v232, v234\n\t" \
    "v_add_f32 v239, v233, v235\n\t" \
    "v_fma_f32 v238, v204, 0.5, v238\n\t" \
    "v_fma_f32 v239, v205, 0.5, v239\n\t" \
    "s_nop 0\n\t" \
    "v_add_f32_dpp v238, v238, v238 quad_perm:[1,0,3,2] row_mask:0xf bank_mask:0xf\n\t" \
    "v_add_f32_dpp v239, v239, v239 quad_perm:[1,0,3,2] row_mask:0xf bank_mask:0xf\n\t" \
    "s_nop 0\n\t" \
    "v_add_f32_dpp v238, v238, v238 quad_perm:[2,3,0,1] row_mask:0xf bank_mask:0xf\n\t" \
    "v_add_f32_dpp v239, v239, v239 quad_perm:[2,3,0,1] row_mask:0xf bank_mask:0xf\n\t" \
    "s_nop 0\n\t" \
    "v_add_f32_dpp v238, v238, v238 row_half_mirror row_mask:0xf bank_mask:0xf\n\t" \
    "v_add_f32_dpp v239, v239, v239 row_half_mirror row_mask:0xf bank_mask:0xf\n\t" \
    "s_nop 0\n\t" \
    "v_add_f32_dpp v238, v238, v238 row_mirror row_mask:0xf bank_mask:0xf\n\t" \
    "v_add_f32_dpp v239, v239, v239 row_mirror row_mask:0xf bank_mask:0xf\n\t" \
    "v_rcp_f32 v238, v238\n\t" \
    "v_rcp_f32 v239, v239\n\t" \
    "s_nop 0\n\t" \
    "v_pk_mul_f32 v[232:233], v[232:233], v[238:239]\n\t" \
    "v_pk_mul_f32 v[234:235], v[234:235], v[238:239]\n\t" \
    "v_pk_mul_f32 v[204:205], v[204:205], v[238:239]\n\t" \
    "v_cvt_pk_bf16_f32 v232, |v232|, |v233|\n\t" \
    "v_cvt_pk_bf16_f32 v234, |v234|, |v235|\n\t" \
    "v_cvt_pk_bf16_f32 v204, |v204|, |v205|\n\t" \
    "v_mov_b32_dpp v233, v232 quad_perm:[1,0,3,2] row_mask:0xf bank_mask:0xf\n\t" \
    "v_mov_b32_dpp v235, v234 quad_perm:[1,0,3,2] row_mask:0xf bank_mask:0xf\n\t" \
    "v_mov_b32_dpp v205, v204 quad_perm:[1,0,3,2] row_mask:0xf bank_mask:0xf\n\t" \
    "v_perm_b32 v232, v232, v233, v202\n\t" \
    "v_perm_b32 v234, v234, v235, v202\n\t" \
    "v_perm_b32 v204, v204, v205, v202\n\t" \
    "v_mov_b32_dpp v240, v232 quad_perm:[0,1,0,1] row_mask:0xf bank_mask:0xf\n\t" \
    "v_mov_b32_dpp v241, v234 quad_perm:[0,1,0,1] row_mask:0xf bank_mask:0xf\n\t" \
    "v_mov_b32_dpp v242, v232 quad_perm:[2,3,2,3] row_mask:0xf bank_mask:0xf\n\t" \
    "v_mov_b32_dpp v243, v234 quad_perm:[2,3,2,3] row_mask:0xf bank_mask:0xf\n\t" \
    "buffer_store_dwordx4 v[240:243], v200, %[rr], %[sx] offen sc1\n\t" \
    "v_mov_b32_dpp v244, v204 quad_perm:[0,1,0,1] row_mask:0xf bank_mask:0xf\n\t" \
    "v_mov_b32_dpp v245, v204 quad_perm:[2,3,2,3] row_mask:0xf bank_mask:0xf\n\t" \
    "buffer_store_dwordx4 v[240:243], v200, %[rr], %[sy] offen sc1\n\t" \
    "buffer_store_dwordx2 v[244:245], v201, %[rr], %[sm] offen sc1\n\t" \
    "s_wakeup\n\t" \
    PRISM
#define ASM_P4_0 \
    "ds_read_b128 v[200:203], %[qa] offset:0\n\t" \
    "ds_read_b128 v[204:207], %[qa] offset:1024\n\t" \
    "ds_read_b128 v[208:211], %[qa] offset:2048\n\t" \
    "ds_read_b128 v[212:215], %[qa] offset:3072\n\t" \
    "ds_read_b128 v[216:219], %[qa] offset:4096\n\t" \
    "ds_read_b128 v[220:223], %[qa] offset:5120\n\t" \
    "ds_read_b128 v[224:227], %[qa] offset:12288\n\t" \
    "ds_read_b128 v[228:231], %[qa] offset:13312\n\t" \
    "ds_read_b128 v[232:235], %[qa] offset:14336\n\t" \
    "ds_read_b128 v[236:239], %[qa] offset:15360\n\t" \
    "s_waitcnt lgkmcnt(0)\n\t" \
    "s_barrier\n\t" ASM_ST \
    "v_mfma_f32_16x16x32_bf16 v[240:243], v[224:227], v[200:203], v[240:243]\n\t" \
    "ds_read_b128 v[224:227], %[qa] offset:16384\n\t" \
    "v_mfma_f32_16x16x32_bf16 v[240:243], v[228:231], v[204:207], v[240:243]\n\t" \
    "ds_read_b128 v[228:231], %[qa] offset:17408\n\t" \
    "v_mfma_f32_16x16x32_bf16 v[240:243], v[232:235], v[208:211], v[240:243]\n\t" \
    "ds_read_b128 v[232:235], %[km] offset:0\n\t" \
    "v_mfma_f32_16x16x32_bf16 v[240:243], v[236:239], v[212:215], v[240:243]\n\t" \
    "ds_read_b128 v[236:239], %[km] offset:512\n\t" \
    "s_waitcnt lgkmcnt(3)\n\t" \
    "v_mfma_f32_16x16x32_bf16 v[240:243], v[224:227], v[216:219], v[240:243]\n\t" \
    "ds_read_b128 v[224:227], %[km] offset:1024\n\t" \
    "s_waitcnt lgkmcnt(3)\n\t" \
    "v_mfma_f32_16x16x32_bf16 v[240:243], v[228:231], v[220:223], v[240:243]\n\t" \
    "ds_read_b128 v[228:231], %[km] offset:1536\n\t" \
    "s_waitcnt lgkmcnt(3)\n\t" \
    "v_mfma_f32_16x16x32_bf16 v[248:251], v[232:235], v[200:203], 0\n\t" \
    "ds_read_b128 v[232:235], %[km] offset:2048\n\t" \
    "s_waitcnt lgkmcnt(3)\n\t" \
    "v_mfma_f32_16x16x32_bf16 v[248:251], v[236:239], v[204:207], v[248:251]\n\t" \
    "ds_read_b128 v[236:239], %[km] offset:2560\n\t" \
    "s_waitcnt lgkmcnt(3)\n\t" \
    "v_mfma_f32_16x16x32_bf16 v[248:251], v[224:227], v[208:211], v[248:251]\n\t" \
    "s_waitcnt lgkmcnt(2)\n\t" \
    "v_mfma_f32_16x16x32_bf16 v[248:251], v[228:231], v[212:215], v[248:251]\n\t" \
    "s_waitcnt lgkmcnt(1)\n\t" \
    "v_mfma_f32_16x16x32_bf16 v[248:251], v[232:235], v[216:219], v[248:251]\n\t" \
    "s_waitcnt lgkmcnt(0)\n\t" \
    "v_mfma_f32_16x16x32_bf16 v[248:251], v[236:239], v[220:223], v[248:251]\n\t" \
    "v_max3_f32 v224, v240, v241, v242\n\t" \
    "v_mov_b32 v225, v243\n\t" \
    "v_lshlrev_b32 v201, 23, %[qa]\n\t" \
    "v_and_b32 v201, 0x80000000, v201\n\t" \
    "v_and_b32 v200, 0x3ff, %[qa]\n\t" \
    "v_or_b32 v200, v201, v200\n\t" \
    "s_nop 1\n\t" \
    "v_max3_f32 v224, v224, v248, v249\n\t" \
    "v_max3_f32 v225, v225, v250, v251\n\t" \
    "v_max_f32 v224, v224, v225\n\t" \
    "v_mov_b32 v225, v224\n\t" \
    "s_nop 1\n\t" \
    "v_permlane16_swap_b32 v224, v225\n\t" \
    "v_max_f32 v224, v224, v225\n\t" \
    "v_mov_b32 v225, v224\n\t" \
    "s_nop 1\n\t" \
    "v_permlane32_swap_b32 v224, v225\n\t" \
    "v_max_f32 v224, v224, v225\n\t" \
    "v_mul_f32 v224, %[scl], v224\n\t" \
    "v_fma_f32 v240, v240, %[scl], -v224\n\t" \
    "v_fma_f32 v241, v241, %[scl], -v224\n\t" \
    "v_exp_f32 v240, v240\n\t" \
    "v_fma_f32 v242, v242, %[scl], -v224\n\t" \
    "v_exp_f32 v241, v241\n\t" \
    "v_fma_f32 v243, v243, %[scl], -v224\n\t" \
    "v_exp_f32 v242, v242\n\t" \
    "v_fma_f32 v248, v248, %[scl], -v224\n\t" \
    "v_exp_f32 v243, v243\n\t" \
    "v_fma_f32 v249, v249, %[scl], -v224\n\t" \
    "v_exp_f32 v248, v248\n\t" \
    "v_fma_f32 v250, v250, %[scl], -v224\n\t" \
    "v_exp_f32 v249, v249\n\t" \
    "v_fma_f32 v251, v251, %[scl], -v224\n\t" \
    "v_exp_f32 v250, v250\n\t" \
    "v_exp_f32 v251, v251\n\t" \
    "v_add_f32 v224, v240, v241\n\t" \
    "v_add_f32 v225, v242, v243\n\t" \
    "v_add_f32 v232, v248, v249\n\t" \
    "v_add_f32 v233, v250, v251\n\t" \
    "v_add_f32 v224, v224, v225\n\t" \
    "v_add_f32 v232, v232, v233\n\t" \
    "v_fma_f32 v224, v232, 0.5, v224\n\t" \
    "v_mov_b32 v225, v224\n\t" \
    "s_nop 1\n\t" \
    "v_permlane16_swap_b32 v224, v225\n\t" \
    "v_add_f32 v224, v224, v225\n\t" \
    "v_mov_b32 v225, v224\n\t" \
    "s_nop 1\n\t" \
    "v_permlane32_swap_b32 v224, v225\n\t" \
    "v_add_f32 v224, v224, v225\n\t" \
    "v_rcp_f32 v224, v224\n\t" \
    "s_nop 0\n\t" \
    "v_mul_f32 v240, v240, v224\n\t" \
    "v_mul_f32 v241, v241, v224\n\t" \
    "v_mul_f32 v242, v242, v224\n\t" \
    "v_mul_f32 v243, v243, v224\n\t" \
    "v_mul_f32 v248, v248, v224\n\t" \
    "v_mul_f32 v249, v249, v224\n\t" \
    "v_mul_f32 v250, v250, v224\n\t" \
    "v_mul_f32 v251, v251, v224\n\t" \
    "v_cvt_pk_bf16_f32 v228, |v240|, |v241|\n\t" \
    "v_cvt_pk_bf16_f32 v229, |v242|, |v243|\n\t" \
    "v_cvt_pk_bf16_f32 v232, |v248|, |v249|\n\t" \
    "v_cvt_pk_bf16_f32 v233, |v250|, |v251|\n\t" \
    "v_permlane16_swap_b32 v228, v230\n\t" \
    "v_permlane16_swap_b32 v229, v231\n\t" \
    "buffer_store_dwordx4 v[228:231], v200, %[rr], %[so] offen sc1\n\t" \
    "buffer_store_dwordx2 v[232:233], %[pm], %[rr], %[so] offen sc1\n\t" \
    "s_nop 1\n\t"
#define ASM_P4_1 \
    "ds_read_b128 v[200:203], %[qa] offset:0\n\t" \
    "ds_read_b128 v[204:207], %[qa] offset:1024\n\t" \
    "ds_read_b128 v[208:211], %[qa] offset:2048\n\t" \
    "ds_read_b128 v[212:215], %[qa] offset:3072\n\t" \
    "ds_read_b128 v[216:219], %[qa] offset:4096\n\t" \
    "ds_read_b128 v[220:223], %[qa] offset:5120\n\t" \
    "ds_read_b128 v[224:227], %[qa] offset:12288\n\t" \
    "ds_read_b128 v[228:231], %[qa] offset:13312\n\t" \
    "ds_read_b128 v[232:235], %[qa] offset:14336\n\t" \
    "ds_read_b128 v[236:239], %[qa] offset:15360\n\t" \
    "s_waitcnt lgkmcnt(0)\n\t" \
    "s_barrier\n\t" ASM_ST \
    "v_mfma_f32_16x16x32_bf16 v[240:243], v[224:227], v[200:203], v[240:243]\n\t" \
    "ds_read_b128 v[224:227], %[qa] offset:16384\n\t" \
    "v_mfma_f32_16x16x32_bf16 v[240:243], v[228:231], v[204:207], v[240:243]\n\t" \
    "ds_read_b128 v[228:231], %[qa] offset:17408\n\t" \
    "v_mfma_f32_16x16x32_bf16 v[240:243], v[232:235], v[208:211], v[240:243]\n\t" \
    "ds_read_b128 v[232:235], %[qa] offset:6144\n\t" \
    "v_mfma_f32_16x16x32_bf16 v[240:243], v[236:239], v[212:215], v[240:243]\n\t" \
    "ds_read_b128 v[236:239], %[qa] offset:7168\n\t" \
    "s_waitcnt lgkmcnt(3)\n\t" \
    "v_mfma_f32_16x16x32_bf16 v[240:243], v[224:227], v[216:219], v[240:243]\n\t" \
    "ds_read_b128 v[224:227], %[qa] offset:8192\n\t" \
    "s_waitcnt lgkmcnt(3)\n\t" \
    "v_mfma_f32_16x16x32_bf16 v[240:243], v[228:231], v[220:223], v[240:243]\n\t" \
    "ds_read_b128 v[228:231], %[qa] offset:9216\n\t" \
    "s_waitcnt lgkmcnt(3)\n\t" \
    "v_mfma_f32_16x16x32_bf16 v[244:247], v[232:235], v[200:203], 0\n\t" \
    "ds_read_b128 v[232:235], %[qa] offset:10240\n\t" \
    "s_waitcnt lgkmcnt(3)\n\t" \
    "v_mfma_f32_16x16x32_bf16 v[244:247], v[236:239], v[204:207], v[244:247]\n\t" \
    "ds_read_b128 v[236:239], %[qa] offset:11264\n\t" \
    "s_waitcnt lgkmcnt(3)\n\t" \
    "v_mfma_f32_16x16x32_bf16 v[244:247], v[224:227], v[208:211], v[244:247]\n\t" \
    "ds_read_b128 v[224:227], %[km] offset:0\n\t" \
    "s_waitcnt lgkmcnt(3)\n\t" \
    "v_mfma_f32_16x16x32_bf16 v[244:247], v[228:231], v[212:215], v[244:247]\n\t" \
    "ds_read_b128 v[228:231], %[km] offset:512\n\t" \
    "s_waitcnt lgkmcnt(3)\n\t" \
    "v_mfma_f32_16x16x32_bf16 v[244:247], v[232:235], v[216:219], v[244:247]\n\t" \
    "ds_read_b128 v[232:235], %[km] offset:1024\n\t" \
    "s_waitcnt lgkmcnt(3)\n\t" \
    "v_mfma_f32_16x16x32_bf16 v[244:247], v[236:239], v[220:223], v[244:247]\n\t" \
    "ds_read_b128 v[236:239], %[km] offset:1536\n\t" \
    "s_waitcnt lgkmcnt(3)\n\t" \
    "v_mfma_f32_16x16x32_bf16 v[248:251], v[224:227], v[200:203], 0\n\t" \
    "ds_read_b128 v[224:227], %[km] offset:2048\n\t" \
    "s_waitcnt lgkmcnt(3)\n\t" \
    "v_mfma_f32_16x16x32_bf16 v[248:251], v[228:231], v[204:207], v[248:251]\n\t" \
    "ds_read_b128 v[228:231], %[km] offset:2560\n\t" \
    "s_waitcnt lgkmcnt(3)\n\t" \
    "v_mfma_f32_16x16x32_bf16 v[248:251], v[232:235], v[208:211], v[248:251]\n\t" \
    "s_waitcnt lgkmcnt(2)\n\t" \
    "v_mfma_f32_16x16x32_bf16 v[248:251], v[236:239], v[212:215], v[248:251]\n\t" \
    "s_waitcnt lgkmcnt(1)\n\t" \
    "v_mfma_f32_16x16x32_bf16 v[248:251], v[224:227], v[216:219], v[248:251]\n\t" \
    "s_waitcnt lgkmcnt(0)\n\t" \
    "v_mfma_f32_16x16x32_bf16 v[248:251], v[228:231], v[220:223], v[248:251]\n\t" \
    "v_max3_f32 v224, v240, v241, v242\n\t" \
    "v_max3_f32 v225, v244, v245, v246\n\t" \
    "v_max3_f32 v224, v224, v243, v247\n\t" \
    "v_and_b32 v200, 0x3ff, %[qa]\n\t" \
    "s_nop 3\n\t" \
    "v_max3_f32 v224, v224, v248, v249\n\t" \
    "v_max3_f32 v225, v225, v250, v251\n\t" \
    "v_max_f32 v224, v224, v225\n\t" \
    "v_mov_b32 v225, v224\n\t" \
    "s_nop 1\n\t" \
    "v_permlane16_swap_b32 v224, v225\n\t" \
    "v_max_f32 v224, v224, v225\n\t" \
    "v_mov_b32 v225, v224\n\t" \
    "s_nop 1\n\t" \
    "v_permlane32_swap_b32 v224, v225\n\t" \
    "v_max_f32 v224, v224, v225\n\t" \
    "v_mul_f32 v224, %[scl], v224\n\t" \
    "v_fma_f32 v240, v240, %[scl], -v224\n\t" \
    "v_fma_f32 v241, v241, %[scl], -v224\n\t" \
    "v_exp_f32 v240, v240\n\t" \
    "v_fma_f32 v242, v242, %[scl], -v224\n\t" \
    "v_exp_f32 v241, v241\n\t" \
    "v_fma_f32 v243, v243, %[scl], -v224\n\t" \
    "v_exp_f32 v242, v242\n\t" \
    "v_fma_f32 v244, v244, %[scl], -v224\n\t" \
    "v_exp_f32 v243, v243\n\t" \
    "v_fma_f32 v245, v245, %[scl], -v224\n\t" \
    "v_exp_f32 v244, v244\n\t" \
    "v_fma_f32 v246, v246, %[scl], -v224\n\t" \
    "v_exp_f32 v245, v245\n\t" \
    "v_fma_f32 v247, v247, %[scl], -v224\n\t" \
    "v_exp_f32 v246, v246\n\t" \
    "v_fma_f32 v248, v248, %[scl], -v224\n\t" \
    "v_exp_f32 v247, v247\n\t" \
    "v_fma_f32 v249, v249, %[scl], -v224\n\t" \
    "v_exp_f32 v248, v248\n\t" \
    "v_fma_f32 v250, v250, %[scl], -v224\n\t" \
    "v_exp_f32 v249, v249\n\t" \
    "v_fma_f32 v251, v251, %[scl], -v224\n\t" \
    "v_exp_f32 v250, v250\n\t" \
    "v_exp_f32 v251, v251\n\t" \
    "v_add_f32 v224, v240, v241\n\t" \
    "v_add_f32 v225, v242, v243\n\t" \
    "v_add_f32 v226, v244, v245\n\t" \
    "v_add_f32 v227, v246, v247\n\t" \
    "v_add_f32 v232, v248, v249\n\t" \
    "v_add_f32 v233, v250, v251\n\t" \
    "v_add_f32 v224, v224, v225\n\t" \
    "v_add_f32 v226, v226, v227\n\t" \
    "v_add_f32 v232, v232, v233\n\t" \
    "v_add_f32 v224, v224, v226\n\t" \
    "v_fma_f32 v224, v232, 0.5, v224\n\t" \
    "v_mov_b32 v225, v224\n\t" \
    "s_nop 1\n\t" \
    "v_permlane16_swap_b32 v224, v225\n\t" \
    "v_add_f32 v224, v224, v225\n\t" \
    "v_mov_b32 v225, v224\n\t" \
    "s_nop 1\n\t" \
    "v_permlane32_swap_b32 v224, v225\n\t" \
    "v_add_f32 v224, v224, v225\n\t" \
    "v_rcp_f32 v224, v224\n\t" \
    "s_nop 0\n\t" \
    "v_mul_f32 v240, v240, v224\n\t" \
    "v_mul_f32 v241, v241, v224\n\t" \
    "v_mul_f32 v242, v242, v224\n\t" \
    "v_mul_f32 v243, v243, v224\n\t" \
    "v_mul_f32 v244, v244, v224\n\t" \
    "v_mul_f32 v245, v245, v224\n\t" \
    "v_mul_f32 v246, v246, v224\n\t" \
    "v_mul_f32 v247, v247, v224\n\t" \
    "v_mul_f32 v248, v248, v224\n\t" \
    "v_mul_f32 v249, v249, v224\n\t" \
    "v_mul_f32 v250, v250, v224\n\t" \
    "v_mul_f32 v251, v251, v224\n\t" \
    "v_cvt_pk_bf16_f32 v230, |v240|, |v241|\n\t" \
    "v_cvt_pk_bf16_f32 v231, |v242|, |v243|\n\t" \
    "v_cvt_pk_bf16_f32 v228, |v244|, |v245|\n\t" \
    "v_cvt_pk_bf16_f32 v229, |v246|, |v247|\n\t" \
    "v_cvt_pk_bf16_f32 v232, |v248|, |v249|\n\t" \
    "v_cvt_pk_bf16_f32 v233, |v250|, |v251|\n\t" \
    "buffer_store_dwordx4 v[228:231], v200, %[rr], %[so] offen sc1\n\t" \
    "buffer_store_dwordx2 v[232:233], %[pm], %[rr], %[so] offen sc1\n\t" \
    "s_nop 1\n\t"
#define ASM_RCRIT \
    "v_mov_b64 v[176:177], -1\n\t" \
    "v_mov_b64 v[178:179], -1\n\t" \
    "v_mov_b64 v[180:181], -1\n\t" \
    "v_mov_b64 v[182:183], -1\n\t" \
    "v_mov_b64 v[184:185], -1\n\t" \
    "v_mov_b64 v[186:187], -1\n\t" \
    "v_mov_b64 v[188:189], -1\n\t" \
    "v_mov_b64 v[190:191], -1\n\t" \
    "v_mov_b64 v[192:193], -1\n\t" \
    "v_mov_b64 v[194:195], -1\n\t" \
    ".Lfa%=:\n\t" \
    "s_mov_b32 %[a], 0\n\t" \
    "v_max_u32 v196, v176, v177\n\t" \
    "v_max3_u32 v196, v178, v179, v196\n\t" \
    "v_cmp_eq_u32 vcc, -1, v196\n\t" \
    "s_cbranch_vccz .Lga0_%=\n\t" \
    "buffer_load_dwordx4 v[176:179], %[ov], %[rs], %[sv] offen sc1\n\t" \
    "s_mov_b32 %[a], 1\n\t" \
    ".Lga0_%=:\n\t" \
    "v_max_u32 v196, v180, v181\n\t" \
    "v_max3_u32 v196, v182, v183, v196\n\t" \
    "v_cmp_eq_u32 vcc, -1, v196\n\t" \
    "s_cbranch_vccz .Lga1_%=\n\t" \
    "buffer_load_dwordx4 v[180:183], %[ov], %[rs], %[sv] offen offset:1024 sc1\n\t" \
    "s_mov_b32 %[a], 1\n\t" \
    ".Lga1_%=:\n\t" \
    "s_cmp_eq_u32 %[a], 0\n\t" \
    "s_cbranch_scc1 .Lha%=\n\t" \
    "s_waitcnt vmcnt(12)\n\t" \
    ".Lia%=:\n\t" \
    "v_max_u32 v196, v176, v177\n\t" \
    "v_max3_u32 v196, v178, v179, v196\n\t" \
    "v_max3_u32 v196, v180, v181, v196\n\t" \
    "v_max3_u32 v196, v182, v183, v196\n\t" \
    "v_cmp_eq_u32 vcc, -1, v196\n\t" \
    "s_cbranch_vccz .Lha%=\n\t" \
    "s_nop 7\n\t" \
    "v_max_u32 v196, v176, v177\n\t" \
    "v_max3_u32 v196, v178, v179, v196\n\t" \
    "v_max3_u32 v196, v180, v181, v196\n\t" \
    "v_max3_u32 v196, v182, v183, v196\n\t" \
    "v_cmp_eq_u32 vcc, -1, v196\n\t" \
    "s_cbranch_vccz .Lha%=\n\t" \
    "s_nop 7\n\t" \
    "v_max_u32 v196, v176, v177\n\t" \
    "v_max3_u32 v196, v178, v179, v196\n\t" \
    "v_max3_u32 v196, v180, v181, v196\n\t" \
    "v_max3_u32 v196, v182, v183, v196\n\t" \
    "v_cmp_eq_u32 vcc, -1, v196\n\t" \
    "s_cbranch_vccz .Lha%=\n\t" \
    "s_nop 7\n\t" \
    "v_max_u32 v196, v176, v177\n\t" \
    "v_max3_u32 v196, v178, v179, v196\n\t" \
    "v_max3_u32 v196, v180, v181, v196\n\t" \
    "v_max3_u32 v196, v182, v183, v196\n\t" \
    "v_cmp_eq_u32 vcc, -1, v196\n\t" \
    "s_cbranch_vccz .Lha%=\n\t" \
    "s_nop 7\n\t" \
    "s_sub_u32 %[n], %[n], 1\n\t" \
    "s_cmp_gt_i32 %[n], 0\n\t" \
    "s_cbranch_scc1 .Lfa%=\n\t" \
    ".Lha%=:\n\t" \
    ASM_ST3 \
    "buffer_load_dwordx4 v[184:187], %[o0], %[rs], %[sp] offen sc1\n\t" \
    "buffer_load_dwordx4 v[188:191], %[o0], %[rs], %[sp] offen offset:1024 sc1\n\t" \
    "buffer_load_dwordx4 v[192:195], %[o2], %[rs], %[sp] offen sc1\n\t" \
    "s_waitcnt vmcnt(0)\n\t" \
    "v_max_u32 v196, v184, v185\n\t" \
    "v_max3_u32 v196, v186, v187, v196\n\t" \
    "v_max3_u32 v196, v188, v189, v196\n\t" \
    "v_max3_u32 v196, v190, v191, v196\n\t" \
    "v_max3_u32 v196, v192, v193, v196\n\t" \
    "v_max3_u32 v196, v194, v195, v196\n\t" \
    "v_cmp_eq_u32 vcc, -1, v196\n\t" \
    "s_cbranch_vccz .Lhb%=\n\t" \
    ".Lfb%=:\n\t" \
    "s_mov_b32 %[a], 0\n\t" \
    "v_max_u32 v196, v184, v185\n\t" \
    "v_max3_u32 v196, v186, v187, v196\n\t" \
    "v_cmp_eq_u32 vcc, -1, v196\n\t" \
    "s_cbranch_vccz .Lgb0_%=\n\t" \
    "buffer_load_dwordx4 v[184:187], %[o0], %[rs], %[sp] offen sc1\n\t" \
    "s_mov_b32 %[a], 1\n\t" \
    ".Lgb0_%=:\n\t" \
    "v_max_u32 v196, v188, v189\n\t" \
    "v_max3_u32 v196, v190, v191, v196\n\t" \
    "v_cmp_eq_u32 vcc, -1, v196\n\t" \
    "s_cbranch_vccz .Lgb1_%=\n\t" \
    "buffer_load_dwordx4 v[188:191], %[o0], %[rs], %[sp] offen offset:1024 sc1\n\t" \
    "s_mov_b32 %[a], 1\n\t" \
    ".Lgb1_%=:\n\t" \
    "v_max_u32 v196, v192, v193\n\t" \
    "v_max3_u32 v196, v194, v195, v196\n\t" \
    "v_cmp_eq_u32 vcc, -1, v196\n\t" \
    "s_cbranch_vccz .Lgb2_%=\n\t" \
    "buffer_load_dwordx4 v[192:195], %[o2], %[rs], %[sp] offen sc1\n\t" \
    "s_mov_b32 %[a], 1\n\t" \
    ".Lgb2_%=:\n\t" \
    "s_cmp_eq_u32 %[a], 0\n\t" \
    "s_cbranch_scc1 .Lhb%=\n\t" \
    "s_waitcnt vmcnt(12)\n\t" \
    ".Lib%=:\n\t" \
    "v_max_u32 v196, v184, v185\n\t" \
    "v_max3_u32 v196, v186, v187, v196\n\t" \
    "v_max3_u32 v196, v188, v189, v196\n\t" \
    "v_max3_u32 v196, v190, v191, v196\n\t" \
    "v_max3_u32 v196, v192, v193, v196\n\t" \
    "v_max3_u32 v196, v194, v195, v196\n\t" \
    "v_cmp_eq_u32 vcc, -1, v196\n\t" \
    "s_cbranch_vccz .Lhb%=\n\t" \
    "s_nop 7\n\t" \
    "v_max_u32 v196, v184, v185\n\t" \
    "v_max3_u32 v196, v186, v187, v196\n\t" \
    "v_max3_u32 v196, v188, v189, v196\n\t" \
    "v_max3_u32 v196, v190, v191, v196\n\t" \
    "v_max3_u32 v196, v192, v193, v196\n\t" \
    "v_max3_u32 v196, v194, v195, v196\n\t" \
    "v_cmp_eq_u32 vcc, -1, v196\n\t" \
    "s_cbranch_vccz .Lhb%=\n\t" \
    "s_nop 7\n\t" \
    "v_max_u32 v196, v184, v185\n\t" \
    "v_max3_u32 v196, v186, v187, v196\n\t" \
    "v_max3_u32 v196, v188, v189, v196\n\t" \
    "v_max3_u32 v196, v190, v191, v196\n\t" \
    "v_max3_u32 v196, v192, v193, v196\n\t" \
    "v_max3_u32 v196, v194, v195, v196\n\t" \
    "v_cmp_eq_u32 vcc, -1, v196\n\t" \
    "s_cbranch_vccz .Lhb%=\n\t" \
    "s_nop 7\n\t" \
    "v_max_u32 v196, v184, v185\n\t" \
    "v_max3_u32 v196, v186, v187, v196\n\t" \
    "v_max3_u32 v196, v188, v189, v196\n\t" \
    "v_max3_u32 v196, v190, v191, v196\n\t" \
    "v_max3_u32 v196, v192, v193, v196\n\t" \
    "v_max3_u32 v196, v194, v195, v196\n\t" \
    "v_cmp_eq_u32 vcc, -1, v196\n\t" \
    "s_cbranch_vccz .Lhb%=\n\t" \
    "s_nop 7\n\t" \
    "s_sub_u32 %[n], %[n], 1\n\t" \
    "s_cmp_gt_i32 %[n], 0\n\t" \
    "s_cbranch_scc1 .Lfb%=\n\t" \
    ".Lhb%=:\n\t" \
    ASM_ST2 \
    "v_mfma_f32_16x16x32_bf16 v[160:163], v[192:195], v[176:179], 0\n\t" \
    "v_mfma_f32_16x16x32_bf16 v[164:167], v[192:195], v[180:183], 0\n\t" \
    "v_smfmac_f32_16x16x64_bf16 v[160:163], v[184:187], v[128:135], %[sidx]\n\t" \
    "v_smfmac_f32_16x16x64_bf16 v[164:167], v[184:187], v[144:151], %[sidx]\n\t" \
    "v_smfmac_f32_16x16x64_bf16 v[160:163], v[188:191], v[136:143], %[sidx]\n\t" \
    "v_smfmac_f32_16x16x64_bf16 v[164:167], v[188:191], v[152:159], %[sidx]\n\t" \
    "s_cmp_eq_u32 %[pub], 0\n\t" \
    "s_nop 7\n\t" \
    "v_permlane32_swap_b32 v160, v164\n\t" \
    "v_permlane32_swap_b32 v161, v165\n\t" \
    "v_permlane32_swap_b32 v162, v166\n\t" \
    "v_permlane32_swap_b32 v163, v167\n\t" \
    "v_add_f32 v160, v160, v164\n\t" \
    "v_add_f32 v161, v161, v165\n\t" \
    "ds_write2_b32 %[rw], v160, v161 offset1:80\n\t" \
    "v_add_f32 v162, v162, v166\n\t" \
    "v_add_f32 v163, v163, v167\n\t" \
    "ds_write2_b32 %[rw], v162, v163 offset0:160 offset1:240\n\t" \
    "s_waitcnt lgkmcnt(0)\n\t" \
    "s_barrier\n\t" ASM_ST \
    "s_cbranch_scc1 .Lre%=\n\t" \
    "ds_read_b128 v[200:203], %[rd] offset:0\n\t" \
    "ds_read_b128 v[204:207], %[rd] offset:16\n\t" \
    "s_waitcnt lgkmcnt(1)\n\t" \
    "v_pk_add_f32 v[200:201], v[200:201], v[202:203]\n\t" \
    "s_waitcnt lgkmcnt(0)\n\t" \
    "v_pk_add_f32 v[204:205], v[204:205], v[206:207]\n\t" \
    "v_pk_add_f32 v[200:201], v[200:201], v[204:205]\n\t" \
    "s_nop 1\n\t" \
    "v_permlane32_swap_b32 v200, v201\n\t" \
    "v_add_f32 v202, v200, v201\n\t" \
    "v_add_f32 v203, v200, v201\n\t" \
    "s_nop 1\n\t" \
    "v_permlane32_swap_b32 v202, v203\n\t" \
    "v_cvt_pk_bf16_f32 v200, v202, v203\n\t" \
    "v_min_u32 v200, -2, v200\n\t" \
    "buffer_store_dword v200, %[oo], %[rs], %[so] offen sc1\n\t" \
    ".Lre%=:\n\t"
#define ASM_RX \
    "s_cmp_eq_u32 %[fin], 0\n\t" \
    "s_cbranch_scc1 .Lx0%=\n\t" \
    "buffer_load_dwordx2 v[250:251], %[xo], %[rx], %[sx] offen sc1\n\t" \
    ".Lx0%=:\n\t" \
    "buffer_load_dword v248, %[fo], %[rs], %[fs] offen sc1\n\t" \
    "buffer_load_dwordx4 v[200:203], %[l16], %[rs], %[s0] offen offset:0 sc1\n\t" \
    "buffer_load_dwordx4 v[204:207], %[l16], %[rs], %[s0] offen offset:1024 sc1\n\t" \
    "buffer_load_dwordx4 v[208:211], %[l16], %[rs], %[s0] offen offset:2048 sc1\n\t" \
    "buffer_load_dwordx4 v[212:215], %[l16], %[rs], %[s0] offen offset:3072 sc1\n\t" \
    "buffer_load_dwordx4 v[216:219], %[l16], %[rs], %[s1] offen offset:0 sc1\n\t" \
    "buffer_load_dwordx4 v[220:223], %[l16], %[rs], %[s1] offen offset:1024 sc1\n\t" \
    "buffer_load_dwordx4 v[224:227], %[l16], %[rs], %[s1] offen offset:2048 sc1\n\t" \
    "buffer_load_dwordx4 v[228:231], %[l16], %[rs], %[s1] offen offset:3072 sc1\n\t" \
    "v_mov_b64 v[96:97], v[128:129]\n\t" \
    "v_mov_b64 v[98:99], v[132:133]\n\t" \
    "v_mov_b64 v[100:101], v[136:137]\n\t" \
    "v_mov_b64 v[102:103], v[140:141]\n\t" \
    "v_mov_b64 v[104:105], v[144:145]\n\t" \
    "v_mov_b64 v[106:107], v[148:149]\n\t" \
    "v_mov_b64 v[108:109], v[152:153]\n\t" \
    "v_mov_b64 v[110:111], v[156:157]\n\t" \
    "s_movk_i32 %[n], 0x200\n\t" \
    "s_nop 1\n\t" \
    "v_permlane16_swap_b32 v96, v98\n\t" \
    "v_permlane16_swap_b32 v97, v99\n\t" \
    "v_permlane16_swap_b32 v100, v102\n\t" \
    "v_permlane16_swap_b32 v101, v103\n\t" \
    "v_permlane16_swap_b32 v104, v106\n\t" \
    "v_permlane16_swap_b32 v105, v107\n\t" \
    "v_permlane16_swap_b32 v108, v110\n\t" \
    "v_permlane16_swap_b32 v109, v111\n\t" \
    "s_waitcnt vmcnt(7)\n\t" \
    "v_max_u32 v196, v200, v201\n\t" \
    "v_max3_u32 v196, v202, v203, v196\n\t" \
    "s_waitcnt vmcnt(6)\n\t" \
    "v_max3_u32 v196, v204, v205, v196\n\t" \
    "v_max3_u32 v196, v206, v207, v196\n\t" \
    "s_waitcnt vmcnt(5)\n\t" \
    "v_max3_u32 v196, v208, v209, v196\n\t" \
    "v_max3_u32 v196, v210, v211, v196\n\t" \
    "s_waitcnt vmcnt(4)\n\t" \
    "v_max3_u32 v196, v212, v213, v196\n\t" \
    "v_max3_u32 v196, v214, v215, v196\n\t" \
    "s_waitcnt vmcnt(3)\n\t" \
    "v_max3_u32 v196, v216, v217, v196\n\t" \
    "v_max3_u32 v196, v218, v219, v196\n\t" \
    "s_waitcnt vmcnt(2)\n\t" \
    "v_max3_u32 v196, v220, v221, v196\n\t" \
    "v_max3_u32 v196, v222, v223, v196\n\t" \
    "s_waitcnt vmcnt(1)\n\t" \
    "v_max3_u32 v196, v224, v225, v196\n\t" \
    "v_max3_u32 v196, v226, v227, v196\n\t" \
    "s_waitcnt vmcnt(0)\n\t" \
    "v_max3_u32 v196, v228, v229, v196\n\t" \
    "v_max3_u32 v196, v230, v231, v196\n\t" \
    "v_cmp_eq_u32 vcc, -1, v196\n\t" \
    "s_cbranch_vccz .Lxd%=\n\t" \
    ".Lxl%=:\n\t" \
    "s_mov_b32 %[a], 0\n\t" \
    "v_max_u32 v196, v200, v201\n\t" \
    "v_max3_u32 v196, v202, v203, v196\n\t" \
    "v_cmp_eq_u32 vcc, -1, v196\n\t" \
    "s_cbranch_vccz .Lxs0_%=\n\t" \
    "buffer_load_dwordx4 v[200:203], %[l16], %[rs], %[s0] offen offset:0 sc1\n\t" \
    "s_mov_b32 %[a], 1\n\t" \
    ".Lxs0_%=:\n\t" \
    "v_max_u32 v196, v204, v205\n\t" \
    "v_max3_u32 v196, v206, v207, v196\n\t" \
    "v_cmp_eq_u32 vcc, -1, v196\n\t" \
    "s_cbranch_vccz .Lxs1_%=\n\t" \
    "buffer_load_dwordx4 v[204:207], %[l16], %[rs], %[s0] offen offset:1024 sc1\n\t" \
    "s_mov_b32 %[a], 1\n\t" \
    ".Lxs1_%=:\n\t" \
    "v_max_u32 v196, v208, v209\n\t" \
    "v_max3_u32 v196, v210, v211, v196\n\t" \
    "v_cmp_eq_u32 vcc, -1, v196\n\t" \
    "s_cbranch_vccz .Lxs2_%=\n\t" \
    "buffer_load_dwordx4 v[208:211], %[l16], %[rs], %[s0] offen offset:2048 sc1\n\t" \
    "s_mov_b32 %[a], 1\n\t" \
    ".Lxs2_%=:\n\t" \
    "v_max_u32 v196, v212, v213\n\t" \
    "v_max3_u32 v196, v214, v215, v196\n\t" \
    "v_cmp_eq_u32 vcc, -1, v196\n\t" \
    "s_cbranch_vccz .Lxs3_%=\n\t" \
    "buffer_load_dwordx4 v[212:215], %[l16], %[rs], %[s0] offen offset:3072 sc1\n\t" \
    "s_mov_b32 %[a], 1\n\t" \
    ".Lxs3_%=:\n\t" \
    "v_max_u32 v196, v216, v217\n\t" \
    "v_max3_u32 v196, v218, v219, v196\n\t" \
    "v_cmp_eq_u32 vcc, -1, v196\n\t" \
    "s_cbranch_vccz .Lxs4_%=\n\t" \
    "buffer_load_dwordx4 v[216:219], %[l16], %[rs], %[s1] offen offset:0 sc1\n\t" \
    "s_mov_b32 %[a], 1\n\t" \
    ".Lxs4_%=:\n\t" \
    "v_max_u32 v196, v220, v221\n\t" \
    "v_max3_u32 v196, v222, v223, v196\n\t" \
    "v_cmp_eq_u32 vcc, -1, v196\n\t" \
    "s_cbranch_vccz .Lxs5_%=\n\t" \
    "buffer_load_dwordx4 v[220:223], %[l16], %[rs], %[s1] offen offset:1024 sc1\n\t" \
    "s_mov_b32 %[a], 1\n\t" \
    ".Lxs5_%=:\n\t" \
    "v_max_u32 v196, v224, v225\n\t" \
    "v_max3_u32 v196, v226, v227, v196\n\t" \
    "v_cmp_eq_u32 vcc, -1, v196\n\t" \
    "s_cbranch_vccz .Lxs6_%=\n\t" \
    "buffer_load_dwordx4 v[224:227], %[l16], %[rs], %[s1] offen offset:2048 sc1\n\t" \
    "s_mov_b32 %[a], 1\n\t" \
    ".Lxs6_%=:\n\t" \
    "v_max_u32 v196, v228, v229\n\t" \
    "v_max3_u32 v196, v230, v231, v196\n\t" \
    "v_cmp_eq_u32 vcc, -1, v196\n\t" \
    "s_cbranch_vccz .Lxs7_%=\n\t" \
    "buffer_load_dwordx4 v[228:231], %[l16], %[rs], %[s1] offen offset:3072 sc1\n\t" \
    "s_mov_b32 %[a], 1\n\t" \
    ".Lxs7_%=:\n\t" \
    "s_cmp_eq_u32 %[a], 0\n\t" \
    "s_cbranch_scc1 .Lxd%=\n\t" \
    "s_waitcnt vmcnt(0)\n\t" \
    "s_sub_u32 %[n], %[n], 1\n\t" \
    "s_cmp_lg_u32 %[n], 0\n\t" \
    "s_cbranch_scc1 .Lxl%=\n\t" \
    "s_branch .Lxe%=\n\t" \
    ".Lxd%=:\n\t" \
    ASM_ST2 \
    "v_mfma_f32_16x16x32_bf16 v[120:123], v[128:131], v[200:203], 0\n\t" \
    "v_mfma_f32_16x16x32_bf16 v[124:127], v[144:147], v[200:203], 0\n\t" \
    "v_mfma_f32_16x16x32_bf16 v[120:123], v[132:135], v[204:207], v[120:123]\n\t" \
    "v_mfma_f32_16x16x32_bf16 v[124:127], v[148:151], v[204:207], v[124:127]\n\t" \
    "v_mfma_f32_16x16x32_bf16 v[120:123], v[136:139], v[208:211], v[120:123]\n\t" \
    "v_mfma_f32_16x16x32_bf16 v[124:127], v[152:155], v[208:211], v[124:127]\n\t" \
    "v_mfma_f32_16x16x32_bf16 v[120:123], v[140:143], v[212:215], v[120:123]\n\t" \
    "v_mfma_f32_16x16x32_bf16 v[124:127], v[156:159], v[212:215], v[124:127]\n\t" \
    "v_mfma_f32_16x16x32_bf16 v[112:115], v[96:99], v[220:223], 0\n\t" \
    "v_mfma_f32_16x16x32_bf16 v[116:119], v[104:107], v[220:223], 0\n\t" \
    "v_mfma_f32_16x16x32_bf16 v[120:123], v[176:179], v[216:219], v[120:123]\n\t" \
    "v_mfma_f32_16x16x32_bf16 v[124:127], v[180:183], v[216:219], v[124:127]\n\t" \
    "v_mfma_f32_16x16x32_bf16 v[112:115], v[100:103], v[224:227], v[112:115]\n\t" \
    "v_mfma_f32_16x16x32_bf16 v[116:119], v[108:111], v[224:227], v[116:119]\n\t" \
    "v_mfma_f32_16x16x32_bf16 v[112:115], v[176:179], v[228:231], v[112:115]\n\t" \
    "v_mfma_f32_16x16x32_bf16 v[116:119], v[180:183], v[228:231], v[116:119]\n\t" \
    "s_nop 1\n\t" \
    "ds_write_b128 %[pw], v[120:123] offset:16384\n\t" \
    "ds_write_b128 %[pw], v[124:127] offset:24576\n\t" \
    "s_nop 3\n\t" \
    "ds_write_b128 %[pw], v[112:115] offset:0\n\t" \
    "ds_write_b128 %[pw], v[116:119] offset:8192\n\t" \
    "v_cmp_ge_u32 vcc, %[tn], v248\n\t" \
    "s_mov_b32 %[a], 0\n\t" \
    "s_cbranch_vccnz .Lxv%=\n\t" \
    "buffer_load_dwordx4 v[128:131], %[l16], %[rp], %[sv0] offen offset:0 sc1\n\t" \
    "buffer_load_dwordx4 v[132:135], %[l16], %[rp], %[sv0] offen offset:1024 sc1\n\t" \
    "buffer_load_dwordx4 v[136:139], %[l16], %[rp], %[sv0] offen offset:2048 sc1\n\t" \
    "buffer_load_dwordx4 v[140:143], %[l16], %[rp], %[sv0] offen offset:3072 sc1\n\t" \
    "buffer_load_dwordx4 v[144:147], %[l16], %[rp], %[sv1] offen offset:0 sc1\n\t" \
    "buffer_load_dwordx4 v[148:151], %[l16], %[rp], %[sv1] offen offset:1024 sc1\n\t" \
    "buffer_load_dwordx4 v[152:155], %[l16], %[rp], %[sv1] offen offset:2048 sc1\n\t" \
    "buffer_load_dwordx4 v[156:159], %[l16], %[rp], %[sv1] offen offset:3072 sc1\n\t" \
    "s_mov_b32 %[a], 1\n\t" \
    ".Lxv%=:\n\t" \
    "s_cmp_eq_u32 %[fin], 0\n\t" \
    "s_waitcnt lgkmcnt(0)\n\t" \
    "s_barrier\n\t" ASM_ST \
    "s_cbranch_scc1 .Lxe%=\n\t" \
    "ds_read_b128 v[200:203], %[pr] offset:0\n\t" \
    "ds_read_b128 v[204:207], %[pr] offset:1024\n\t" \
    "ds_read_b128 v[208:211], %[pr] offset:2048\n\t" \
    "ds_read_b128 v[212:215], %[pr] offset:3072\n\t" \
    "ds_read_b128 v[216:219], %[pr] offset:4096\n\t" \
    "ds_read_b128 v[220:223], %[pr] offset:5120\n\t" \
    "ds_read_b128 v[224:227], %[pr] offset:6144\n\t" \
    "ds_read_b128 v[228:231], %[pr] offset:7168\n\t" \
    "v_lshlrev_b32 v240, 16, v250\n\t" \
    "v_and_b32 v241, 0xffff0000, v250\n\t" \
    "v_lshlrev_b32 v242, 16, v251\n\t" \
    "v_and_b32 v243, 0xffff0000, v251\n\t" \
    "s_waitcnt lgkmcnt(6)\n\t" \
    "v_pk_add_f32 v[200:201], v[200:201], v[204:205]\n\t" \
    "v_pk_add_f32 v[202:203], v[202:203], v[206:207]\n\t" \
    "s_waitcnt lgkmcnt(4)\n\t" \
    "v_pk_add_f32 v[208:209], v[208:209], v[212:213]\n\t" \
    "v_pk_add_f32 v[210:211], v[210:211], v[214:215]\n\t" \
    "v_pk_add_f32 v[200:201], v[200:201], v[208:209]\n\t" \
    "v_pk_add_f32 v[202:203], v[202:203], v[210:211]\n\t" \
    "s_waitcnt lgkmcnt(2)\n\t" \
    "v_pk_add_f32 v[216:217], v[216:217], v[220:221]\n\t" \
    "v_pk_add_f32 v[218:219], v[218:219], v[222:223]\n\t" \
    "s_waitcnt lgkmcnt(0)\n\t" \
    "v_pk_add_f32 v[224:225], v[224:225], v[228:229]\n\t" \
    "v_pk_add_f32 v[226:227], v[226:227], v[230:231]\n\t" \
    "v_pk_add_f32 v[216:217], v[216:217], v[224:225]\n\t" \
    "v_pk_add_f32 v[218:219], v[218:219], v[226:227]\n\t" \
    "v_pk_add_f32 v[200:201], v[200:201], v[216:217]\n\t" \
    "v_pk_add_f32 v[202:203], v[202:203], v[218:219]\n\t" \
    "v_pk_add_f32 v[200:201], v[200:201], v[240:241]\n\t" \
    "v_pk_add_f32 v[202:203], v[202:203], v[242:243]\n\t" \
    "v_cvt_pk_bf16_f32 v200, v200, v201\n\t" \
    "v_cvt_pk_bf16_f32 v201, v202, v203\n\t" \
    "v_min_u32 v200, -2, v200\n\t" \
    "v_min_u32 v201, -2, v201\n\t" \
    "buffer_store_dwordx2 v[200:201], %[xo], %[rd], %[sa] offen sc1\n\t" \
    ".Lxe%=:\n\t"
#define ASM_W4_M \
    ".Lc7_%=:\n\t" \
    "s_waitcnt vmcnt(0)\n\t" \
    "v_max_u32 v212, v188, v189\n\t" \
    "v_max3_u32 v212, v190, v191, v212\n\t" \
    "v_cmp_eq_u32 vcc, -1, v212\n\t" \
    "s_cbranch_vccnz .Ls7_%=\n\t" \
    ".Lk7_%=:\n\t" \
    ASM_ST2 \
    "buffer_load_dwordx4 v[184:187], %[l16], %[rs], %[s1] offen offset:2048 sc1\n\t" \
    "buffer_load_dwordx4 v[180:183], %[l16], %[rs], %[s1] offen offset:1024 sc1\n\t" \
    "buffer_load_dwordx4 v[176:179], %[l16], %[rs], %[s1] offen offset:0 sc1\n\t" \
    "buffer_load_dwordx4 v[172:175], %[l16], %[rs], %[s0] offen offset:3072 sc1\n\t" \
    "buffer_load_dwordx4 v[168:171], %[l16], %[rs], %[s0] offen offset:2048 sc1\n\t" \
    "buffer_load_dwordx4 v[164:167], %[l16], %[rs], %[s0] offen offset:1024 sc1\n\t" \
    "buffer_load_dwordx4 v[160:163], %[l16], %[rs], %[s0] offen offset:0 sc1\n\t" \
    "v_mfma_f32_16x16x32_bf16 v[208:211], v[156:159], v[188:191], 0\n\t" \
    "v_mfma_f32_16x16x32_bf16 v[204:207], v[124:127], v[188:191], 0\n\t" \
    "v_mfma_f32_16x16x32_bf16 v[200:203], v[92:95], v[188:191], 0\n\t" \
    "v_mfma_f32_16x16x32_bf16 v[196:199], v[60:63], v[188:191], 0\n\t" \
    "v_mfma_f32_16x16x32_bf16 v[192:195], v[28:31], v[188:191], 0\n\t" \
    ".Lc6_%=:\n\t" \
    "s_waitcnt vmcnt(6)\n\t" \
    "v_max_u32 v212, v184, v185\n\t" \
    "v_max3_u32 v212, v186, v187, v212\n\t" \
    "v_cmp_eq_u32 vcc, -1, v212\n\t" \
    "s_cbranch_vccnz .Ls6_%=\n\t" \
    ".Lk6_%=:\n\t" \
    ".Lc5_%=:\n\t" \
    "s_waitcnt vmcnt(5)\n\t" \
    "v_max_u32 v212, v180, v181\n\t" \
    "v_max3_u32 v212, v182, v183, v212\n\t" \
    "v_cmp_eq_u32 vcc, -1, v212\n\t" \
    "s_cbranch_vccnz .Ls5_%=\n\t" \
    ".Lk5_%=:\n\t" \
    "v_mfma_f32_16x16x32_bf16 v[192:195], v[24:27], v[184:187], v[192:195]\n\t" \
    "v_mfma_f32_16x16x32_bf16 v[192:195], v[20:23], v[180:183], v[192:195]\n\t" \
    "v_mfma_f32_16x16x32_bf16 v[196:199], v[56:59], v[184:187], v[196:199]\n\t" \
    "v_mfma_f32_16x16x32_bf16 v[196:199], v[52:55], v[180:183], v[196:199]\n\t" \
    "v_mfma_f32_16x16x32_bf16 v[200:203], v[88:91], v[184:187], v[200:203]\n\t" \
    "v_mfma_f32_16x16x32_bf16 v[200:203], v[84:87], v[180:183], v[200:203]\n\t" \
    "v_mfma_f32_16x16x32_bf16 v[204:207], v[120:123], v[184:187], v[204:207]\n\t" \
    "v_mfma_f32_16x16x32_bf16 v[204:207], v[116:119], v[180:183], v[204:207]\n\t" \
    "v_mfma_f32_16x16x32_bf16 v[208:211], v[152:155], v[184:187], v[208:211]\n\t" \
    "v_mfma_f32_16x16x32_bf16 v[208:211], v[148:151], v[180:183], v[208:211]\n\t" \
    ".Lc4_%=:\n\t" \
    "s_waitcnt vmcnt(4)\n\t" \
    "v_max_u32 v212, v176, v177\n\t" \
    "v_max3_u32 v212, v178, v179, v212\n\t" \
    "v_cmp_eq_u32 vcc, -1, v212\n\t" \
    "s_cbranch_vccnz .Ls4_%=\n\t" \
    ".Lk4_%=:\n\t" \
    ".Lc3_%=:\n\t" \
    "s_waitcnt vmcnt(3)\n\t" \
    "v_max_u32 v212, v172, v173\n\t" \
    "v_max3_u32 v212, v174, v175, v212\n\t" \
    "v_cmp_eq_u32 vcc, -1, v212\n\t" \
    "s_cbranch_vccnz .Ls3_%=\n\t" \
    ".Lk3_%=:\n\t" \
    "v_mfma_f32_16x16x32_bf16 v[208:211], v[144:147], v[176:179], v[208:211]\n\t" \
    "v_mfma_f32_16x16x32_bf16 v[208:211], v[140:143], v[172:175], v[208:211]\n\t" \
    "v_mfma_f32_16x16x32_bf16 v[204:207], v[112:115], v[176:179], v[204:207]\n\t" \
    "v_mfma_f32_16x16x32_bf16 v[204:207], v[108:111], v[172:175], v[204:207]\n\t" \
    "v_mfma_f32_16x16x32_bf16 v[200:203], v[80:83], v[176:179], v[200:203]\n\t" \
    "v_mfma_f32_16x16x32_bf16 v[200:203], v[76:79], v[172:175], v[200:203]\n\t" \
    "v_mfma_f32_16x16x32_bf16 v[196:199], v[48:51], v[176:179], v[196:199]\n\t" \
    "v_mfma_f32_16x16x32_bf16 v[196:199], v[44:47], v[172:175], v[196:199]\n\t" \
    "v_mfma_f32_16x16x32_bf16 v[192:195], v[16:19], v[176:179], v[192:195]\n\t" \
    "v_mfma_f32_16x16x32_bf16 v[192:195], v[12:15], v[172:175], v[192:195]\n\t" \
    ".Lc2_%=:\n\t" \
    "s_waitcnt vmcnt(2)\n\t" \
    "v_max_u32 v212, v168, v169\n\t" \
    "v_max3_u32 v212, v170, v171, v212\n\t" \
    "v_cmp_eq_u32 vcc, -1, v212\n\t" \
    "s_cbranch_vccnz .Ls2_%=\n\t" \
    ".Lk2_%=:\n\t" \
    "buffer_load_dwordx4 v[188:191], %[l16], %[rs], %[n1] offen offset:3072 sc1\n\t" \
    ".Lc1_%=:\n\t" \
    "s_waitcnt vmcnt(2)\n\t" \
    "v_max_u32 v212, v164, v165\n\t" \
    "v_max3_u32 v212, v166, v167, v212\n\t" \
    "v_cmp_eq_u32 vcc, -1, v212\n\t" \
    "s_cbranch_vccnz .Ls1_%=\n\t" \
    ".Lk1_%=:\n\t" \
    ".Lc0_%=:\n\t" \
    "s_waitcnt vmcnt(1)\n\t" \
    "v_max_u32 v212, v160, v161\n\t" \
    "v_max3_u32 v212, v162, v163, v212\n\t" \
    "v_cmp_eq_u32 vcc, -1, v212\n\t" \
    "s_cbranch_vccnz .Ls0_%=\n\t" \
    ".Lk0_%=:\n\t" \
    "v_mfma_f32_16x16x32_bf16 v[192:195], v[8:11], v[168:171], v[192:195]\n\t" \
    "v_mfma_f32_16x16x32_bf16 v[192:195], v[4:7], v[164:167], v[192:195]\n\t" \
    "v_mfma_f32_16x16x32_bf16 v[192:195], v[0:3], v[160:163], v[192:195]\n\t" \
    "v_mfma_f32_16x16x32_bf16 v[196:199], v[40:43], v[168:171], v[196:199]\n\t" \
    "v_mfma_f32_16x16x32_bf16 v[196:199], v[36:39], v[164:167], v[196:199]\n\t" \
    "v_mfma_f32_16x16x32_bf16 v[196:199], v[32:35], v[160:163], v[196:199]\n\t" \
    "v_mfma_f32_16x16x32_bf16 v[200:203], v[72:75], v[168:171], v[200:203]\n\t" \
    "v_mfma_f32_16x16x32_bf16 v[200:203], v[68:71], v[164:167], v[200:203]\n\t" \
    "v_mfma_f32_16x16x32_bf16 v[200:203], v[64:67], v[160:163], v[200:203]\n\t" \
    "v_mfma_f32_16x16x32_bf16 v[204:207], v[104:107], v[168:171], v[204:207]\n\t" \
    "v_mfma_f32_16x16x32_bf16 v[204:207], v[100:103], v[164:167], v[204:207]\n\t" \
    "v_mfma_f32_16x16x32_bf16 v[204:207], v[96:99], v[160:163], v[204:207]\n\t" \
    "v_mfma_f32_16x16x32_bf16 v[208:211], v[136:139], v[168:171], v[208:211]\n\t" \
    "v_mfma_f32_16x16x32_bf16 v[208:211], v[132:135], v[164:167], v[208:211]\n\t" \
    "v_mfma_f32_16x16x32_bf16 v[208:211], v[128:131], v[160:163], v[208:211]\n\t" \
    "v_mov_b32 v212, %[un]\n\t" \
    "ds_write_b128 %[ld], v[192:195] offset:0\n\t" \
    "ds_write_b128 %[ld], v[196:199] offset:6144\n\t" \
    "ds_write_b128 %[ld], v[200:203] offset:12288\n\t" \
    "ds_write_b128 %[ld], v[204:207] offset:18432\n\t" \
    "s_nop 2\n\t" \
    "ds_write_b128 %[ld], v[208:211] offset:24576\n\t" \
    "ds_write_b32 %[fl], v212\n\t" \
    "s_branch .Lme%=\n\t" \
    ".Ls7_%=:\n\t" \
    "s_cmp_eq_u32 %[n], 0\n\t" \
    "s_cbranch_scc1 .Lk7_%=\n\t" \
    "s_sub_u32 %[n], %[n], 1\n\t" \
    "s_sleep 1\n\t" \
    "buffer_load_dwordx4 v[188:191], %[l16], %[rs], %[s1] offen offset:3072 sc1\n\t" \
    "s_branch .Lc7_%=\n\t" \
    ".Ls6_%=:\n\t" \
    "s_cmp_eq_u32 %[n], 0\n\t" \
    "s_cbranch_scc1 .Lk6_%=\n\t" \
    "s_sub_u32 %[n], %[n], 1\n\t" \
    "buffer_load_dwordx4 v[184:187], %[l16], %[rs], %[s1] offen offset:2048 sc1\n\t" \
    "s_waitcnt vmcnt(0)\n\t" \
    "s_branch .Lc6_%=\n\t" \
    ".Ls5_%=:\n\t" \
    "s_cmp_eq_u32 %[n], 0\n\t" \
    "s_cbranch_scc1 .Lk5_%=\n\t" \
    "s_sub_u32 %[n], %[n], 1\n\t" \
    "buffer_load_dwordx4 v[180:183], %[l16], %[rs], %[s1] offen offset:1024 sc1\n\t" \
    "s_waitcnt vmcnt(0)\n\t" \
    "s_branch .Lc5_%=\n\t" \
    ".Ls4_%=:\n\t" \
    "s_cmp_eq_u32 %[n], 0\n\t" \
    "s_cbranch_scc1 .Lk4_%=\n\t" \
    "s_sub_u32 %[n], %[n], 1\n\t" \
    "buffer_load_dwordx4 v[176:179], %[l16], %[rs], %[s1] offen offset:0 sc1\n\t" \
    "s_waitcnt vmcnt(0)\n\t" \
    "s_branch .Lc4_%=\n\t" \
    ".Ls3_%=:\n\t" \
    "s_cmp_eq_u32 %[n], 0\n\t" \
    "s_cbranch_scc1 .Lk3_%=\n\t" \
    "s_sub_u32 %[n], %[n], 1\n\t" \
    "buffer_load_dwordx4 v[172:175], %[l16], %[rs], %[s0] offen offset:3072 sc1\n\t" \
    "s_waitcnt vmcnt(0)\n\t" \
    "s_branch .Lc3_%=\n\t" \
    ".Ls2_%=:\n\t" \
    "s_cmp_eq_u32 %[n], 0\n\t" \
    "s_cbranch_scc1 .Lk2_%=\n\t" \
    "s_sub_u32 %[n], %[n], 1\n\t" \
    "buffer_load_dwordx4 v[168:171], %[l16], %[rs], %[s0] offen offset:2048 sc1\n\t" \
    "s_waitcnt vmcnt(0)\n\t" \
    "s_branch .Lc2_%=\n\t" \
    ".Ls1_%=:\n\t" \
    "s_cmp_eq_u32 %[n], 0\n\t" \
    "s_cbranch_scc1 .Lk1_%=\n\t" \
    "s_sub_u32 %[n], %[n], 1\n\t" \
    "buffer_load_dwordx4 v[164:167], %[l16], %[rs], %[s0] offen offset:1024 sc1\n\t" \
    "s_waitcnt vmcnt(0)\n\t" \
    "s_branch .Lc1_%=\n\t" \
    ".Ls0_%=:\n\t" \
    "s_cmp_eq_u32 %[n], 0\n\t" \
    "s_cbranch_scc1 .Lk0_%=\n\t" \
    "s_sub_u32 %[n], %[n], 1\n\t" \
    "buffer_load_dwordx4 v[160:163], %[l16], %[rs], %[s0] offen offset:0 sc1\n\t" \
    "s_waitcnt vmcnt(0)\n\t" \
    "s_branch .Lc0_%=\n\t" \
    ".Lme%=:\n\t"
#define ASM_W4F_0 \
    "s_branch .Lf0%=\n\t" \
    ".Lfr%=:\n\t" \
    "s_cmp_eq_u32 %[n], 0\n\t" \
    "s_cbranch_scc1 .Lfb%=\n\t" \
    "s_sub_u32 %[n], %[n], 1\n\t" \
    "s_sleep 6\n\t" \
    "buffer_load_dwordx2 v[164:165], %[o0], %[rd], %[c1] offen sc1\n\t" \
    "buffer_load_dwordx2 v[166:167], %[o1], %[rd], %[c1] offen sc1\n\t" \
    "buffer_load_dwordx2 v[168:169], %[o2], %[rd], %[c1] offen sc1\n\t" \
    "buffer_load_dwordx2 v[170:171], %[o3], %[rd], %[c1] offen sc1\n\t" \
    "buffer_load_dwordx2 v[172:173], %[o4], %[rd], %[c1] offen sc1\n\t" \
    "buffer_load_dwordx2 v[174:175], %[o0], %[rd], %[ca] offen sc1\n\t" \
    "buffer_load_dwordx2 v[176:177], %[o1], %[rd], %[ca] offen sc1\n\t" \
    "buffer_load_dwordx2 v[178:179], %[o2], %[rd], %[ca] offen sc1\n\t" \
    "buffer_load_dwordx2 v[180:181], %[o3], %[rd], %[ca] offen sc1\n\t" \
    "buffer_load_dwordx2 v[182:183], %[o4], %[rd], %[ca] offen sc1\n\t" \
    "s_waitcnt vmcnt(0)\n\t" \
    "s_branch .Lfa%=\n\t" \
    ".Lfs0_%=:\n\t" \
    "s_cmp_eq_u32 %[n], 0\n\t" \
    "s_cbranch_scc1 .Lfu0_%=\n\t" \
    "s_sub_u32 %[n], %[n], 1\n\t" \
    "s_sleep 1\n\t" \
    "s_branch .Lft0_%=\n\t" \
    ".Lfs1_%=:\n\t" \
    "s_cmp_eq_u32 %[n], 0\n\t" \
    "s_cbranch_scc1 .Lfu1_%=\n\t" \
    "s_sub_u32 %[n], %[n], 1\n\t" \
    "s_sleep 1\n\t" \
    "s_branch .Lft1_%=\n\t" \
    ".Lfx%=:\n\t" \
    "s_cmp_eq_u32 %[n], 0\n\t" \
    "s_cbranch_scc1 .Lfz%=\n\t" \
    "s_sub_u32 %[n], %[n], 1\n\t" \
    "s_branch .Lfy%=\n\t" \
    ".Lf0%=:\n\t" \
    "s_waitcnt vmcnt(1)\n\t" \
    ".Lfa%=:\n\t" \
    "v_max_u32 v194, v164, v165\n\t" \
    "v_max3_u32 v194, v166, v167, v194\n\t" \
    "v_max3_u32 v194, v168, v169, v194\n\t" \
    "v_max3_u32 v194, v170, v171, v194\n\t" \
    "v_max3_u32 v194, v172, v173, v194\n\t" \
    "v_max3_u32 v194, v174, v175, v194\n\t" \
    "v_max3_u32 v194, v176, v177, v194\n\t" \
    "v_max3_u32 v194, v178, v179, v194\n\t" \
    "v_max3_u32 v194, v180, v181, v194\n\t" \
    "v_max3_u32 v194, v182, v183, v194\n\t" \
    "v_cmp_eq_u32 vcc, -1, v194\n\t" \
    "s_cbranch_vccnz .Lfr%=\n\t" \
    ".Lfb%=:\n\t" \
    "v_lshlrev_b32 v64, 16, v164\n\t" \
    "v_and_b32 v65, 0xffff0000, v164\n\t" \
    "v_lshlrev_b32 v84, 16, v174\n\t" \
    "v_and_b32 v85, 0xffff0000, v174\n\t" \
    "v_lshlrev_b32 v66, 16, v165\n\t" \
    "v_and_b32 v67, 0xffff0000, v165\n\t" \
    "v_lshlrev_b32 v86, 16, v175\n\t" \
    "v_and_b32 v87, 0xffff0000, v175\n\t" \
    "v_lshlrev_b32 v68, 16, v166\n\t" \
    "v_and_b32 v69, 0xffff0000, v166\n\t" \
    "v_lshlrev_b32 v88, 16, v176\n\t" \
    "v_and_b32 v89, 0xffff0000, v176\n\t" \
    "v_lshlrev_b32 v70, 16, v167\n\t" \
    "v_and_b32 v71, 0xffff0000, v167\n\t" \
    "v_lshlrev_b32 v90, 16, v177\n\t" \
    "v_and_b32 v91, 0xffff0000, v177\n\t" \
    "v_lshlrev_b32 v72, 16, v168\n\t" \
    "v_and_b32 v73, 0xffff0000, v168\n\t" \
    "v_lshlrev_b32 v92, 16, v178\n\t" \
    "v_and_b32 v93, 0xffff0000, v178\n\t" \
    "v_lshlrev_b32 v74, 16, v169\n\t" \
    "v_and_b32 v75, 0xffff0000, v169\n\t" \
    "v_lshlrev_b32 v94, 16, v179\n\t" \
    "v_and_b32 v95, 0xffff0000, v179\n\t" \
    "v_lshlrev_b32 v76, 16, v170\n\t" \
    "v_and_b32 v77, 0xffff0000, v170\n\t" \
    "v_lshlrev_b32 v96, 16, v180\n\t" \
    "v_and_b32 v97, 0xffff0000, v180\n\t" \
    "v_lshlrev_b32 v78, 16, v171\n\t" \
    "v_and_b32 v79, 0xffff0000, v171\n\t" \
    "v_lshlrev_b32 v98, 16, v181\n\t" \
    "v_and_b32 v99, 0xffff0000, v181\n\t" \
    "v_lshlrev_b32 v80, 16, v172\n\t" \
    "v_and_b32 v81, 0xffff0000, v172\n\t" \
    "v_lshlrev_b32 v100, 16, v182\n\t" \
    "v_and_b32 v101, 0xffff0000, v182\n\t" \
    "v_lshlrev_b32 v82, 16, v173\n\t" \
    "v_and_b32 v83, 0xffff0000, v173\n\t" \
    "v_lshlrev_b32 v102, 16, v183\n\t" \
    "v_and_b32 v103, 0xffff0000, v183\n\t" \
    "v_pk_add_f32 v[64:65], v[64:65], v[84:85]\n\t" \
    "v_pk_add_f32 v[66:67], v[66:67], v[86:87]\n\t" \
    "v_pk_add_f32 v[68:69], v[68:69], v[88:89]\n\t" \
    "v_pk_add_f32 v[70:71], v[70:71], v[90:91]\n\t" \
    "v_pk_add_f32 v[72:73], v[72:73], v[92:93]\n\t" \
    "v_pk_add_f32 v[74:75], v[74:75], v[94:95]\n\t" \
    "v_pk_add_f32 v[76:77], v[76:77], v[96:97]\n\t" \
    "v_pk_add_f32 v[78:79], v[78:79], v[98:99]\n\t" \
    "v_pk_add_f32 v[80:81], v[80:81], v[100:101]\n\t" \
    "v_pk_add_f32 v[82:83], v[82:83], v[102:103]\n\t" \
    ".Lft0_%=:\n\t" \
    "ds_read_b32 v194, %[f0]\n\t" \
    "s_waitcnt lgkmcnt(0)\n\t" \
    "v_cmp_ge_u32 vcc, %[u], v194\n\t" \
    "s_cbranch_vccnz .Lfs0_%=\n\t" \
    ".Lfu0_%=:\n\t" \
    "ds_read_b128 v[84:87], %[la] offset:0\n\t" \
    "ds_read_b128 v[88:91], %[la] offset:6144\n\t" \
    "ds_read_b128 v[92:95], %[la] offset:12288\n\t" \
    "ds_read_b128 v[96:99], %[la] offset:18432\n\t" \
    "ds_read_b128 v[100:103], %[la] offset:24576\n\t" \
    "ds_read_b128 v[104:107], %[la] offset:1024\n\t" \
    "ds_read_b128 v[108:111], %[la] offset:7168\n\t" \
    "ds_read_b128 v[112:115], %[la] offset:13312\n\t" \
    "ds_read_b128 v[116:119], %[la] offset:19456\n\t" \
    "ds_read_b128 v[120:123], %[la] offset:25600\n\t" \
    "ds_read_b128 v[124:127], %[la] offset:2048\n\t" \
    "s_waitcnt lgkmcnt(10)\n\t" \
    "v_pk_add_f32 v[64:65], v[64:65], v[84:85]\n\t" \
    "v_pk_add_f32 v[66:67], v[66:67], v[86:87]\n\t" \
    "ds_read_b128 v[128:131], %[la] offset:8192\n\t" \
    "s_waitcnt lgkmcnt(10)\n\t" \
    "v_pk_add_f32 v[68:69], v[68:69], v[88:89]\n\t" \
    "v_pk_add_f32 v[70:71], v[70:71], v[90:91]\n\t" \
    "ds_read_b128 v[132:135], %[la] offset:14336\n\t" \
    "s_waitcnt lgkmcnt(10)\n\t" \
    "v_pk_add_f32 v[72:73], v[72:73], v[92:93]\n\t" \
    "v_pk_add_f32 v[74:75], v[74:75], v[94:95]\n\t" \
    "ds_read_b128 v[136:139], %[la] offset:20480\n\t" \
    "s_waitcnt lgkmcnt(10)\n\t" \
    "v_pk_add_f32 v[76:77], v[76:77], v[96:97]\n\t" \
    "v_pk_add_f32 v[78:79], v[78:79], v[98:99]\n\t" \
    "ds_read_b128 v[140:143], %[la] offset:26624\n\t" \
    "s_waitcnt lgkmcnt(10)\n\t" \
    "v_pk_add_f32 v[80:81], v[80:81], v[100:101]\n\t" \
    "v_pk_add_f32 v[82:83], v[82:83], v[102:103]\n\t" \
    "ds_read_b128 v[144:147], %[la] offset:3072\n\t" \
    "s_waitcnt lgkmcnt(10)\n\t" \
    "v_pk_add_f32 v[64:65], v[64:65], v[104:105]\n\t" \
    "v_pk_add_f32 v[66:67], v[66:67], v[106:107]\n\t" \
    "ds_read_b128 v[148:151], %[la] offset:9216\n\t" \
    "s_waitcnt lgkmcnt(10)\n\t" \
    "v_pk_add_f32 v[68:69], v[68:69], v[108:109]\n\t" \
    "v_pk_add_f32 v[70:71], v[70:71], v[110:111]\n\t" \
    "ds_read_b128 v[152:155], %[la] offset:15360\n\t" \
    "s_waitcnt lgkmcnt(10)\n\t" \
    "v_pk_add_f32 v[72:73], v[72:73], v[112:113]\n\t" \
    "v_pk_add_f32 v[74:75], v[74:75], v[114:115]\n\t" \
    "ds_read_b128 v[156:159], %[la] offset:21504\n\t" \
    "s_waitcnt lgkmcnt(10)\n\t" \
    "v_pk_add_f32 v[76:77], v[76:77], v[116:117]\n\t" \
    "v_pk_add_f32 v[78:79], v[78:79], v[118:119]\n\t" \
    "ds_read_b128 v[160:163], %[la] offset:27648\n\t" \
    "s_waitcnt lgkmcnt(10)\n\t" \
    "v_pk_add_f32 v[80:81], v[80:81], v[120:121]\n\t" \
    "v_pk_add_f32 v[82:83], v[82:83], v[122:123]\n\t" \
    "s_waitcnt lgkmcnt(9)\n\t" \
    "v_pk_add_f32 v[64:65], v[64:65], v[124:125]\n\t" \
    "v_pk_add_f32 v[66:67], v[66:67], v[126:127]\n\t" \
    "s_waitcnt lgkmcnt(8)\n\t" \
    "v_pk_add_f32 v[68:69], v[68:69], v[128:129]\n\t" \
    "v_pk_add_f32 v[70:71], v[70:71], v[130:131]\n\t" \
    "s_waitcnt lgkmcnt(7)\n\t" \
    "v_pk_add_f32 v[72:73], v[72:73], v[132:133]\n\t" \
    "v_pk_add_f32 v[74:75], v[74:75], v[134:135]\n\t" \
    "s_waitcnt lgkmcnt(6)\n\t" \
    "v_pk_add_f32 v[76:77], v[76:77], v[136:137]\n\t" \
    "v_pk_add_f32 v[78:79], v[78:79], v[138:139]\n\t" \
    "s_waitcnt lgkmcnt(5)\n\t" \
    "v_pk_add_f32 v[80:81], v[80:81], v[140:141]\n\t" \
    "v_pk_add_f32 v[82:83], v[82:83], v[142:143]\n\t" \
    "s_waitcnt lgkmcnt(4)\n\t" \
    "v_pk_add_f32 v[64:65], v[64:65], v[144:145]\n\t" \
    "v_pk_add_f32 v[66:67], v[66:67], v[146:147]\n\t" \
    "s_waitcnt lgkmcnt(3)\n\t" \
    "v_pk_add_f32 v[68:69], v[68:69], v[148:149]\n\t" \
    "v_pk_add_f32 v[70:71], v[70:71], v[150:151]\n\t" \
    "s_waitcnt lgkmcnt(2)\n\t" \
    "v_pk_add_f32 v[72:73], v[72:73], v[152:153]\n\t" \
    "v_pk_add_f32 v[74:75], v[74:75], v[154:155]\n\t" \
    "s_waitcnt lgkmcnt(1)\n\t" \
    "v_pk_add_f32 v[76:77], v[76:77], v[156:157]\n\t" \
    "v_pk_add_f32 v[78:79], v[78:79], v[158:159]\n\t" \
    "s_waitcnt lgkmcnt(0)\n\t" \
    "v_pk_add_f32 v[80:81], v[80:81], v[160:161]\n\t" \
    "v_pk_add_f32 v[82:83], v[82:83], v[162:163]\n\t" \
    ".Lft1_%=:\n\t" \
    "ds_read_b32 v194, %[f1]\n\t" \
    "s_waitcnt lgkmcnt(0)\n\t" \
    "v_cmp_ge_u32 vcc, %[u], v194\n\t" \
    "s_cbranch_vccnz .Lfs1_%=\n\t" \
    ".Lfu1_%=:\n\t" \
    ASM_ST2 \
    "ds_read_b128 v[84:87], %[la] offset:4096\n\t" \
    "ds_read_b128 v[88:91], %[la] offset:10240\n\t" \
    "ds_read_b128 v[92:95], %[la] offset:16384\n\t" \
    "ds_read_b128 v[96:99], %[la] offset:22528\n\t" \
    "ds_read_b128 v[100:103], %[la] offset:28672\n\t" \
    "ds_read_b128 v[104:107], %[la] offset:5120\n\t" \
    "ds_read_b128 v[108:111], %[la] offset:11264\n\t" \
    "ds_read_b128 v[112:115], %[la] offset:17408\n\t" \
    "ds_read_b128 v[116:119], %[la] offset:23552\n\t" \
    "ds_read_b128 v[120:123], %[la] offset:29696\n\t" \
    "v_mov_b32 v194, %[un]\n\t" \
    "ds_write_b32 %[fd], v194\n\t" \
    "s_waitcnt lgkmcnt(10)\n\t" \
    "v_pk_add_f32 v[64:65], v[64:65], v[84:85]\n\t" \
    "v_pk_add_f32 v[66:67], v[66:67], v[86:87]\n\t" \
    "s_waitcnt lgkmcnt(9)\n\t" \
    "v_pk_add_f32 v[68:69], v[68:69], v[88:89]\n\t" \
    "v_pk_add_f32 v[70:71], v[70:71], v[90:91]\n\t" \
    "s_waitcnt lgkmcnt(8)\n\t" \
    "v_pk_add_f32 v[72:73], v[72:73], v[92:93]\n\t" \
    "v_pk_add_f32 v[74:75], v[74:75], v[94:95]\n\t" \
    "s_waitcnt lgkmcnt(7)\n\t" \
    "v_pk_add_f32 v[76:77], v[76:77], v[96:97]\n\t" \
    "v_pk_add_f32 v[78:79], v[78:79], v[98:99]\n\t" \
    "s_waitcnt lgkmcnt(6)\n\t" \
    "v_pk_add_f32 v[80:81], v[80:81], v[100:101]\n\t" \
    "v_pk_add_f32 v[82:83], v[82:83], v[102:103]\n\t" \
    "s_waitcnt lgkmcnt(5)\n\t" \
    "v_pk_add_f32 v[64:65], v[64:65], v[104:105]\n\t" \
    "v_pk_add_f32 v[66:67], v[66:67], v[106:107]\n\t" \
    "s_waitcnt lgkmcnt(4)\n\t" \
    "v_pk_add_f32 v[68:69], v[68:69], v[108:109]\n\t" \
    "v_pk_add_f32 v[70:71], v[70:71], v[110:111]\n\t" \
    "s_waitcnt lgkmcnt(3)\n\t" \
    "v_pk_add_f32 v[72:73], v[72:73], v[112:113]\n\t" \
    "v_pk_add_f32 v[74:75], v[74:75], v[114:115]\n\t" \
    "s_waitcnt lgkmcnt(2)\n\t" \
    "v_pk_add_f32 v[76:77], v[76:77], v[116:117]\n\t" \
    "v_pk_add_f32 v[78:79], v[78:79], v[118:119]\n\t" \
    "s_waitcnt lgkmcnt(1)\n\t" \
    "v_pk_add_f32 v[80:81], v[80:81], v[120:121]\n\t" \
    "v_pk_add_f32 v[82:83], v[82:83], v[122:123]\n\t" \
    "v_pk_add_f32 v[84:85], v[64:65], v[66:67]\n\t" \
    "v_pk_mul_f32 v[88:89], v[64:65], v[64:65]\n\t" \
    "v_pk_add_f32 v[86:87], v[68:69], v[70:71]\n\t" \
    "v_pk_mul_f32 v[90:91], v[66:67], v[66:67]\n\t" \
    "v_pk_fma_f32 v[88:89], v[68:69], v[68:69], v[88:89]\n\t" \
    "v_pk_fma_f32 v[90:91], v[70:71], v[70:71], v[90:91]\n\t" \
    "v_pk_add_f32 v[84:85], v[84:85], v[72:73]\n\t" \
    "v_pk_fma_f32 v[88:89], v[72:73], v[72:73], v[88:89]\n\t" \
    "v_pk_add_f32 v[86:87], v[86:87], v[74:75]\n\t" \
    "v_pk_fma_f32 v[90:91], v[74:75], v[74:75], v[90:91]\n\t" \
    "v_pk_add_f32 v[84:85], v[84:85], v[76:77]\n\t" \
    "v_pk_fma_f32 v[88:89], v[76:77], v[76:77], v[88:89]\n\t" \
    "v_pk_add_f32 v[86:87], v[86:87], v[78:79]\n\t" \
    "v_pk_fma_f32 v[90:91], v[78:79], v[78:79], v[90:91]\n\t" \
    "v_pk_add_f32 v[84:85], v[84:85], v[80:81]\n\t" \
    "v_pk_fma_f32 v[88:89], v[80:81], v[80:81], v[88:89]\n\t" \
    "v_pk_add_f32 v[86:87], v[86:87], v[82:83]\n\t" \
    "v_pk_fma_f32 v[90:91], v[82:83], v[82:83], v[90:91]\n\t" \
    "v_pk_add_f32 v[84:85], v[84:85], v[86:87]\n\t" \
    "v_pk_add_f32 v[88:89], v[88:89], v[90:91]\n\t" \
    "v_add_f32 v194, v84, v85\n\t" \
    "v_add_f32 v195, v88, v89\n\t" \
    "s_nop 1\n\t" \
    "v_permlane16_swap_b32 v194, v195\n\t" \
    "v_add_f32 v196, v194, v195\n\t" \
    "v_add_f32 v197, v194, v195\n\t" \
    "s_nop 1\n\t" \
    "v_permlane32_swap_b32 v196, v197\n\t" \
    "v_add_f32 v194, v196, v197\n\t" \
    "v_add_f32 v195, v196, v197\n\t" \
    "s_nop 1\n\t" \
    "v_permlane16_swap_b32 v194, v195\n\t" \
    "v_min_u32 v194, -2, v194\n\t" \
    "v_min_u32 v195, -2, v195\n\t" \
    "buffer_store_dwordx2 v[194:195], %[so], %[rr], %[sp] offen sc1\n\t" \
    ASM_ST3 \
    "v_cvt_pk_bf16_f32 v184, v64, v65\n\t" \
    "v_cvt_pk_bf16_f32 v185, v66, v67\n\t" \
    "v_cvt_pk_bf16_f32 v186, v68, v69\n\t" \
    "v_cvt_pk_bf16_f32 v187, v70, v71\n\t" \
    "v_cvt_pk_bf16_f32 v188, v72, v73\n\t" \
    "v_cvt_pk_bf16_f32 v189, v74, v75\n\t" \
    "v_cvt_pk_bf16_f32 v190, v76, v77\n\t" \
    "v_cvt_pk_bf16_f32 v191, v78, v79\n\t" \
    "v_cvt_pk_bf16_f32 v192, v80, v81\n\t" \
    "v_cvt_pk_bf16_f32 v193, v82, v83\n\t" \
    "v_permlane16_swap_b32 v184, v186\n\t" \
    "v_permlane16_swap_b32 v185, v187\n\t" \
    "v_permlane16_swap_b32 v188, v190\n\t" \
    "v_permlane16_swap_b32 v189, v191\n\t" \
    W4SLEEP \
    ".Lfy%=:\n\t" \
    "buffer_load_dwordx2 v[196:197], %[si], %[rr], %[sq] offen sc1\n\t" \
    "buffer_load_dwordx2 v[198:199], %[si], %[rr], %[sq] offen offset:512 sc1\n\t" \
    "s_waitcnt vmcnt(0)\n\t" \
    "v_max_u32 v200, v196, v197\n\t" \
    "v_max3_u32 v200, v198, v199, v200\n\t" \
    "v_cmp_eq_u32 vcc, -1, v200\n\t" \
    "s_cbranch_vccnz .Lfx%=\n\t" \
    ".Lfz%=:\n\t" \
    ASM_ST \
    "v_add_f32 v194, v196, v198\n\t" \
    "v_add_f32 v195, v197, v199\n\t" \
    "buffer_store_dwordx4 v[184:187], %[pa], %[rd], %[xr] offen sc1\n\t" \
    "buffer_store_dwordx4 v[188:191], %[pb], %[rd], %[xr] offen sc1\n\t" \
    "buffer_store_dwordx2 v[192:193], %[o4], %[rd], %[xr] offen sc1\n\t" \
    "s_nop 1\n\t" \
    "v_permlane16_swap_b32 v194, v195\n\t" \
    "v_add_f32 v196, v194, v195\n\t" \
    "v_add_f32 v197, v194, v195\n\t" \
    "s_nop 1\n\t" \
    "v_permlane32_swap_b32 v196, v197\n\t" \
    "v_add_f32 v194, v196, v197\n\t" \
    "v_add_f32 v195, v196, v197\n\t" \
    "s_nop 1\n\t" \
    "v_permlane16_swap_b32 v194, v195\n\t" \
    "v_mul_f32 v194, %[kd], v194\n\t" \
    "v_mul_f32 v195, %[kd], v195\n\t" \
    "v_fma_f32 v195, -v194, v194, v195\n\t" \
    "v_max_f32 v195, 0, v195\n\t" \
    "v_cvt_pk_bf16_f32 v195, v195, v195\n\t" \
    "v_lshlrev_b32 v195, 16, v195\n\t" \
    "v_add_f32 v195, %[ke], v195\n\t" \
    "v_cvt_pk_bf16_f32 v195, v195, v195\n\t" \
    "v_lshlrev_b32 v195, 16, v195\n\t" \
    "v_sqrt_f32 v195, v195\n\t" \
    "v_cvt_pk_bf16_f32 v194, v194, v194\n\t" \
    "v_lshlrev_b32 v194, 16, v194\n\t" \
    "v_cvt_pk_bf16_f32 v195, v195, v195\n\t" \
    "v_lshlrev_b32 v195, 16, v195\n\t" \
    "v_rcp_f32 v196, v195\n\t" \
    "v_rcp_f32 v197, v195\n\t" \
    "s_nop 0\n\t" \
    "v_mul_f32 v198, -v194, v196\n\t" \
    "v_mul_f32 v199, -v194, v196\n\t" \
    "v_pk_fma_f32 v[64:65], v[64:65], v[196:197], v[198:199]\n\t" \
    "v_pk_fma_f32 v[66:67], v[66:67], v[196:197], v[198:199]\n\t" \
    "v_pk_fma_f32 v[68:69], v[68:69], v[196:197], v[198:199]\n\t" \
    "v_pk_fma_f32 v[70:71], v[70:71], v[196:197], v[198:199]\n\t" \
    "v_pk_fma_f32 v[72:73], v[72:73], v[196:197], v[198:199]\n\t" \
    "v_pk_fma_f32 v[74:75], v[74:75], v[196:197], v[198:199]\n\t" \
    "v_pk_fma_f32 v[76:77], v[76:77], v[196:197], v[198:199]\n\t" \
    "v_pk_fma_f32 v[78:79], v[78:79], v[196:197], v[198:199]\n\t" \
    "v_pk_fma_f32 v[80:81], v[80:81], v[196:197], v[198:199]\n\t" \
    "v_pk_fma_f32 v[82:83], v[82:83], v[196:197], v[198:199]\n\t" \
    "v_cvt_pk_bf16_f32 v184, v64, v65\n\t" \
    "v_cvt_pk_bf16_f32 v185, v66, v67\n\t" \
    "v_cvt_pk_bf16_f32 v186, v68, v69\n\t" \
    "v_cvt_pk_bf16_f32 v187, v70, v71\n\t" \
    "v_cvt_pk_bf16_f32 v188, v72, v73\n\t" \
    "v_cvt_pk_bf16_f32 v189, v74, v75\n\t" \
    "v_cvt_pk_bf16_f32 v190, v76, v77\n\t" \
    "v_cvt_pk_bf16_f32 v191, v78, v79\n\t" \
    "v_cvt_pk_bf16_f32 v192, v80, v81\n\t" \
    "v_cvt_pk_bf16_f32 v193, v82, v83\n\t" \
    "v_permlane16_swap_b32 v184, v186\n\t" \
    "v_permlane16_swap_b32 v185, v187\n\t" \
    "v_permlane16_swap_b32 v188, v190\n\t" \
    "v_permlane16_swap_b32 v189, v191\n\t" \
    "buffer_store_dwordx4 v[184:187], %[pa], %[rd], %[xn] offen sc1\n\t" \
    "buffer_store_dwordx4 v[188:191], %[pb], %[rd], %[xn] offen sc1\n\t" \
    "buffer_store_dwordx2 v[192:193], %[o4], %[rd], %[xn] offen sc1\n\t" \
    "buffer_load_dwordx2 v[164:165], %[o0], %[rd], %[n1] offen sc1\n\t" \
    "buffer_load_dwordx2 v[166:167], %[o1], %[rd], %[n1] offen sc1\n\t" \
    "buffer_load_dwordx2 v[168:169], %[o2], %[rd], %[n1] offen sc1\n\t" \
    "buffer_load_dwordx2 v[170:171], %[o3], %[rd], %[n1] offen sc1\n\t" \
    "buffer_load_dwordx2 v[172:173], %[o4], %[rd], %[n1] offen sc1\n\t" \
    "buffer_load_dwordx2 v[174:175], %[o0], %[rd], %[na] offen sc1\n\t" \
    "buffer_load_dwordx2 v[176:177], %[o1], %[rd], %[na] offen sc1\n\t" \
    "buffer_load_dwordx2 v[178:179], %[o2], %[rd], %[na] offen sc1\n\t" \
    "buffer_load_dwordx2 v[180:181], %[o3], %[rd], %[na] offen sc1\n\t" \
    "buffer_load_dwordx2 v[182:183], %[o4], %[rd], %[na] offen sc1\n\t" \
    "v_mov_b32 v196, %[tn]\n\t" \
    "v_mov_b32 v197, %[tn]\n\t" \
    "v_mov_b32 v198, %[tn]\n\t" \
    "v_mov_b32 v199, %[tn]\n\t" \
    "s_waitcnt vmcnt(10)\n\t" \
    "buffer_store_dwordx4 v[196:199], %[fo], %[rn], %[fs] offen sc1\n\t" \
    "s_nop 1\n\t"
#define ASM_W4F_1 \
    "s_branch .Lf0%=\n\t" \
    ".Lfr%=:\n\t" \
    "s_cmp_eq_u32 %[n], 0\n\t" \
    "s_cbranch_scc1 .Lfb%=\n\t" \
    "s_sub_u32 %[n], %[n], 1\n\t" \
    "s_sleep 6\n\t" \
    "buffer_load_dwordx2 v[164:165], %[o0], %[rd], %[c1] offen sc1\n\t" \
    "buffer_load_dwordx2 v[166:167], %[o1], %[rd], %[c1] offen sc1\n\t" \
    "buffer_load_dwordx2 v[168:169], %[o2], %[rd], %[c1] offen sc1\n\t" \
    "buffer_load_dwordx2 v[170:171], %[o3], %[rd], %[c1] offen sc1\n\t" \
    "buffer_load_dwordx2 v[172:173], %[o4], %[rd], %[c1] offen sc1\n\t" \
    "buffer_load_dwordx2 v[174:175], %[o0], %[rd], %[ca] offen sc1\n\t" \
    "buffer_load_dwordx2 v[176:177], %[o1], %[rd], %[ca] offen sc1\n\t" \
    "buffer_load_dwordx2 v[178:179], %[o2], %[rd], %[ca] offen sc1\n\t" \
    "buffer_load_dwordx2 v[180:181], %[o3], %[rd], %[ca] offen sc1\n\t" \
    "buffer_load_dwordx2 v[182:183], %[o4], %[rd], %[ca] offen sc1\n\t" \
    "s_waitcnt vmcnt(0)\n\t" \
    "s_branch .Lfa%=\n\t" \
    ".Lfs0_%=:\n\t" \
    "s_cmp_eq_u32 %[n], 0\n\t" \
    "s_cbranch_scc1 .Lfu0_%=\n\t" \
    "s_sub_u32 %[n], %[n], 1\n\t" \
    "s_sleep 1\n\t" \
    "s_branch .Lft0_%=\n\t" \
    ".Lfs1_%=:\n\t" \
    "s_cmp_eq_u32 %[n], 0\n\t" \
    "s_cbranch_scc1 .Lfu1_%=\n\t" \
    "s_sub_u32 %[n], %[n], 1\n\t" \
    "s_sleep 1\n\t" \
    "s_branch .Lft1_%=\n\t" \
    ".Lf0%=:\n\t" \
    "s_waitcnt vmcnt(0)\n\t" \
    ".Lfa%=:\n\t" \
    "v_max_u32 v194, v164, v165\n\t" \
    "v_max3_u32 v194, v166, v167, v194\n\t" \
    "v_max3_u32 v194, v168, v169, v194\n\t" \
    "v_max3_u32 v194, v170, v171, v194\n\t" \
    "v_max3_u32 v194, v172, v173, v194\n\t" \
    "v_max3_u32 v194, v174, v175, v194\n\t" \
    "v_max3_u32 v194, v176, v177, v194\n\t" \
    "v_max3_u32 v194, v178, v179, v194\n\t" \
    "v_max3_u32 v194, v180, v181, v194\n\t" \
    "v_max3_u32 v194, v182, v183, v194\n\t" \
    "v_cmp_eq_u32 vcc, -1, v194\n\t" \
    "s_cbranch_vccnz .Lfr%=\n\t" \
    ".Lfb%=:\n\t" \
    "v_lshlrev_b32 v64, 16, v164\n\t" \
    "v_and_b32 v65, 0xffff0000, v164\n\t" \
    "v_lshlrev_b32 v84, 16, v174\n\t" \
    "v_and_b32 v85, 0xffff0000, v174\n\t" \
    "v_lshlrev_b32 v66, 16, v165\n\t" \
    "v_and_b32 v67, 0xffff0000, v165\n\t" \
    "v_lshlrev_b32 v86, 16, v175\n\t" \
    "v_and_b32 v87, 0xffff0000, v175\n\t" \
    "v_lshlrev_b32 v68, 16, v166\n\t" \
    "v_and_b32 v69, 0xffff0000, v166\n\t" \
    "v_lshlrev_b32 v88, 16, v176\n\t" \
    "v_and_b32 v89, 0xffff0000, v176\n\t" \
    "v_lshlrev_b32 v70, 16, v167\n\t" \
    "v_and_b32 v71, 0xffff0000, v167\n\t" \
    "v_lshlrev_b32 v90, 16, v177\n\t" \
    "v_and_b32 v91, 0xffff0000, v177\n\t" \
    "v_lshlrev_b32 v72, 16, v168\n\t" \
    "v_and_b32 v73, 0xffff0000, v168\n\t" \
    "v_lshlrev_b32 v92, 16, v178\n\t" \
    "v_and_b32 v93, 0xffff0000, v178\n\t" \
    "v_lshlrev_b32 v74, 16, v169\n\t" \
    "v_and_b32 v75, 0xffff0000, v169\n\t" \
    "v_lshlrev_b32 v94, 16, v179\n\t" \
    "v_and_b32 v95, 0xffff0000, v179\n\t" \
    "v_lshlrev_b32 v76, 16, v170\n\t" \
    "v_and_b32 v77, 0xffff0000, v170\n\t" \
    "v_lshlrev_b32 v96, 16, v180\n\t" \
    "v_and_b32 v97, 0xffff0000, v180\n\t" \
    "v_lshlrev_b32 v78, 16, v171\n\t" \
    "v_and_b32 v79, 0xffff0000, v171\n\t" \
    "v_lshlrev_b32 v98, 16, v181\n\t" \
    "v_and_b32 v99, 0xffff0000, v181\n\t" \
    "v_lshlrev_b32 v80, 16, v172\n\t" \
    "v_and_b32 v81, 0xffff0000, v172\n\t" \
    "v_lshlrev_b32 v100, 16, v182\n\t" \
    "v_and_b32 v101, 0xffff0000, v182\n\t" \
    "v_lshlrev_b32 v82, 16, v173\n\t" \
    "v_and_b32 v83, 0xffff0000, v173\n\t" \
    "v_lshlrev_b32 v102, 16, v183\n\t" \
    "v_and_b32 v103, 0xffff0000, v183\n\t" \
    "v_pk_add_f32 v[64:65], v[64:65], v[84:85]\n\t" \
    "v_pk_add_f32 v[66:67], v[66:67], v[86:87]\n\t" \
    "v_pk_add_f32 v[68:69], v[68:69], v[88:89]\n\t" \
    "v_pk_add_f32 v[70:71], v[70:71], v[90:91]\n\t" \
    "v_pk_add_f32 v[72:73], v[72:73], v[92:93]\n\t" \
    "v_pk_add_f32 v[74:75], v[74:75], v[94:95]\n\t" \
    "v_pk_add_f32 v[76:77], v[76:77], v[96:97]\n\t" \
    "v_pk_add_f32 v[78:79], v[78:79], v[98:99]\n\t" \
    "v_pk_add_f32 v[80:81], v[80:81], v[100:101]\n\t" \
    "v_pk_add_f32 v[82:83], v[82:83], v[102:103]\n\t" \
    ".Lft0_%=:\n\t" \
    "ds_read_b32 v194, %[f0]\n\t" \
    "s_waitcnt lgkmcnt(0)\n\t" \
    "v_cmp_ge_u32 vcc, %[u], v194\n\t" \
    "s_cbranch_vccnz .Lfs0_%=\n\t" \
    ".Lfu0_%=:\n\t" \
    "ds_read_b128 v[84:87], %[la] offset:0\n\t" \
    "ds_read_b128 v[88:91], %[la] offset:6144\n\t" \
    "ds_read_b128 v[92:95], %[la] offset:12288\n\t" \
    "ds_read_b128 v[96:99], %[la] offset:18432\n\t" \
    "ds_read_b128 v[100:103], %[la] offset:24576\n\t" \
    "ds_read_b128 v[104:107], %[la] offset:1024\n\t" \
    "ds_read_b128 v[108:111], %[la] offset:7168\n\t" \
    "ds_read_b128 v[112:115], %[la] offset:13312\n\t" \
    "ds_read_b128 v[116:119], %[la] offset:19456\n\t" \
    "ds_read_b128 v[120:123], %[la] offset:25600\n\t" \
    "ds_read_b128 v[124:127], %[la] offset:2048\n\t" \
    "s_waitcnt lgkmcnt(10)\n\t" \
    "v_pk_add_f32 v[64:65], v[64:65], v[84:85]\n\t" \
    "v_pk_add_f32 v[66:67], v[66:67], v[86:87]\n\t" \
    "ds_read_b128 v[128:131], %[la] offset:8192\n\t" \
    "s_waitcnt lgkmcnt(10)\n\t" \
    "v_pk_add_f32 v[68:69], v[68:69], v[88:89]\n\t" \
    "v_pk_add_f32 v[70:71], v[70:71], v[90:91]\n\t" \
    "ds_read_b128 v[132:135], %[la] offset:14336\n\t" \
    "s_waitcnt lgkmcnt(10)\n\t" \
    "v_pk_add_f32 v[72:73], v[72:73], v[92:93]\n\t" \
    "v_pk_add_f32 v[74:75], v[74:75], v[94:95]\n\t" \
    "ds_read_b128 v[136:139], %[la] offset:20480\n\t" \
    "s_waitcnt lgkmcnt(10)\n\t" \
    "v_pk_add_f32 v[76:77], v[76:77], v[96:97]\n\t" \
    "v_pk_add_f32 v[78:79], v[78:79], v[98:99]\n\t" \
    "ds_read_b128 v[140:143], %[la] offset:26624\n\t" \
    "s_waitcnt lgkmcnt(10)\n\t" \
    "v_pk_add_f32 v[80:81], v[80:81], v[100:101]\n\t" \
    "v_pk_add_f32 v[82:83], v[82:83], v[102:103]\n\t" \
    "ds_read_b128 v[144:147], %[la] offset:3072\n\t" \
    "s_waitcnt lgkmcnt(10)\n\t" \
    "v_pk_add_f32 v[64:65], v[64:65], v[104:105]\n\t" \
    "v_pk_add_f32 v[66:67], v[66:67], v[106:107]\n\t" \
    "ds_read_b128 v[148:151], %[la] offset:9216\n\t" \
    "s_waitcnt lgkmcnt(10)\n\t" \
    "v_pk_add_f32 v[68:69], v[68:69], v[108:109]\n\t" \
    "v_pk_add_f32 v[70:71], v[70:71], v[110:111]\n\t" \
    "ds_read_b128 v[152:155], %[la] offset:15360\n\t" \
    "s_waitcnt lgkmcnt(10)\n\t" \
    "v_pk_add_f32 v[72:73], v[72:73], v[112:113]\n\t" \
    "v_pk_add_f32 v[74:75], v[74:75], v[114:115]\n\t" \
    "ds_read_b128 v[156:159], %[la] offset:21504\n\t" \
    "s_waitcnt lgkmcnt(10)\n\t" \
    "v_pk_add_f32 v[76:77], v[76:77], v[116:117]\n\t" \
    "v_pk_add_f32 v[78:79], v[78:79], v[118:119]\n\t" \
    "ds_read_b128 v[160:163], %[la] offset:27648\n\t" \
    "s_waitcnt lgkmcnt(10)\n\t" \
    "v_pk_add_f32 v[80:81], v[80:81], v[120:121]\n\t" \
    "v_pk_add_f32 v[82:83], v[82:83], v[122:123]\n\t" \
    "s_waitcnt lgkmcnt(9)\n\t" \
    "v_pk_add_f32 v[64:65], v[64:65], v[124:125]\n\t" \
    "v_pk_add_f32 v[66:67], v[66:67], v[126:127]\n\t" \
    "s_waitcnt lgkmcnt(8)\n\t" \
    "v_pk_add_f32 v[68:69], v[68:69], v[128:129]\n\t" \
    "v_pk_add_f32 v[70:71], v[70:71], v[130:131]\n\t" \
    "s_waitcnt lgkmcnt(7)\n\t" \
    "v_pk_add_f32 v[72:73], v[72:73], v[132:133]\n\t" \
    "v_pk_add_f32 v[74:75], v[74:75], v[134:135]\n\t" \
    "s_waitcnt lgkmcnt(6)\n\t" \
    "v_pk_add_f32 v[76:77], v[76:77], v[136:137]\n\t" \
    "v_pk_add_f32 v[78:79], v[78:79], v[138:139]\n\t" \
    "s_waitcnt lgkmcnt(5)\n\t" \
    "v_pk_add_f32 v[80:81], v[80:81], v[140:141]\n\t" \
    "v_pk_add_f32 v[82:83], v[82:83], v[142:143]\n\t" \
    "s_waitcnt lgkmcnt(4)\n\t" \
    "v_pk_add_f32 v[64:65], v[64:65], v[144:145]\n\t" \
    "v_pk_add_f32 v[66:67], v[66:67], v[146:147]\n\t" \
    "s_waitcnt lgkmcnt(3)\n\t" \
    "v_pk_add_f32 v[68:69], v[68:69], v[148:149]\n\t" \
    "v_pk_add_f32 v[70:71], v[70:71], v[150:151]\n\t" \
    "s_waitcnt lgkmcnt(2)\n\t" \
    "v_pk_add_f32 v[72:73], v[72:73], v[152:153]\n\t" \
    "v_pk_add_f32 v[74:75], v[74:75], v[154:155]\n\t" \
    "s_waitcnt lgkmcnt(1)\n\t" \
    "v_pk_add_f32 v[76:77], v[76:77], v[156:157]\n\t" \
    "v_pk_add_f32 v[78:79], v[78:79], v[158:159]\n\t" \
    "s_waitcnt lgkmcnt(0)\n\t" \
    "v_pk_add_f32 v[80:81], v[80:81], v[160:161]\n\t" \
    "v_pk_add_f32 v[82:83], v[82:83], v[162:163]\n\t" \
    ".Lft1_%=:\n\t" \
    "ds_read_b32 v194, %[f1]\n\t" \
    "s_waitcnt lgkmcnt(0)\n\t" \
    "v_cmp_ge_u32 vcc, %[u], v194\n\t" \
    "s_cbranch_vccnz .Lfs1_%=\n\t" \
    ".Lfu1_%=:\n\t" \
    ASM_ST2 \
    "ds_read_b128 v[84:87], %[la] offset:4096\n\t" \
    "ds_read_b128 v[88:91], %[la] offset:10240\n\t" \
    "ds_read_b128 v[92:95], %[la] offset:16384\n\t" \
    "ds_read_b128 v[96:99], %[la] offset:22528\n\t" \
    "ds_read_b128 v[100:103], %[la] offset:28672\n\t" \
    "ds_read_b128 v[104:107], %[la] offset:5120\n\t" \
    "ds_read_b128 v[108:111], %[la] offset:11264\n\t" \
    "ds_read_b128 v[112:115], %[la] offset:17408\n\t" \
    "ds_read_b128 v[116:119], %[la] offset:23552\n\t" \
    "ds_read_b128 v[120:123], %[la] offset:29696\n\t" \
    "v_mov_b32 v194, %[un]\n\t" \
    "ds_write_b32 %[fd], v194\n\t" \
    "s_waitcnt lgkmcnt(10)\n\t" \
    "v_pk_add_f32 v[64:65], v[64:65], v[84:85]\n\t" \
    "v_pk_add_f32 v[66:67], v[66:67], v[86:87]\n\t" \
    "s_waitcnt lgkmcnt(9)\n\t" \
    "v_pk_add_f32 v[68:69], v[68:69], v[88:89]\n\t" \
    "v_pk_add_f32 v[70:71], v[70:71], v[90:91]\n\t" \
    "s_waitcnt lgkmcnt(8)\n\t" \
    "v_pk_add_f32 v[72:73], v[72:73], v[92:93]\n\t" \
    "v_pk_add_f32 v[74:75], v[74:75], v[94:95]\n\t" \
    "s_waitcnt lgkmcnt(7)\n\t" \
    "v_pk_add_f32 v[76:77], v[76:77], v[96:97]\n\t" \
    "v_pk_add_f32 v[78:79], v[78:79], v[98:99]\n\t" \
    "s_waitcnt lgkmcnt(6)\n\t" \
    "v_pk_add_f32 v[80:81], v[80:81], v[100:101]\n\t" \
    "v_pk_add_f32 v[82:83], v[82:83], v[102:103]\n\t" \
    "s_waitcnt lgkmcnt(5)\n\t" \
    "v_pk_add_f32 v[64:65], v[64:65], v[104:105]\n\t" \
    "v_pk_add_f32 v[66:67], v[66:67], v[106:107]\n\t" \
    "s_waitcnt lgkmcnt(4)\n\t" \
    "v_pk_add_f32 v[68:69], v[68:69], v[108:109]\n\t" \
    "v_pk_add_f32 v[70:71], v[70:71], v[110:111]\n\t" \
    "s_waitcnt lgkmcnt(3)\n\t" \
    "v_pk_add_f32 v[72:73], v[72:73], v[112:113]\n\t" \
    "v_pk_add_f32 v[74:75], v[74:75], v[114:115]\n\t" \
    "s_waitcnt lgkmcnt(2)\n\t" \
    "v_pk_add_f32 v[76:77], v[76:77], v[116:117]\n\t" \
    "v_pk_add_f32 v[78:79], v[78:79], v[118:119]\n\t" \
    "s_waitcnt lgkmcnt(1)\n\t" \
    "v_pk_add_f32 v[80:81], v[80:81], v[120:121]\n\t" \
    "v_pk_add_f32 v[82:83], v[82:83], v[122:123]\n\t" \
    "v_cvt_pk_bf16_f32 v184, v64, v65\n\t" \
    "v_cvt_pk_bf16_f32 v185, v66, v67\n\t" \
    "v_cvt_pk_bf16_f32 v186, v68, v69\n\t" \
    "v_cvt_pk_bf16_f32 v187, v70, v71\n\t" \
    "v_cvt_pk_bf16_f32 v188, v72, v73\n\t" \
    "v_cvt_pk_bf16_f32 v189, v74, v75\n\t" \
    "v_cvt_pk_bf16_f32 v190, v76, v77\n\t" \
    "v_cvt_pk_bf16_f32 v191, v78, v79\n\t" \
    "v_cvt_pk_bf16_f32 v192, v80, v81\n\t" \
    "v_cvt_pk_bf16_f32 v193, v82, v83\n\t" \
    "v_permlane16_swap_b32 v184, v186\n\t" \
    "v_permlane16_swap_b32 v185, v187\n\t" \
    "v_permlane16_swap_b32 v188, v190\n\t" \
    "v_permlane16_swap_b32 v189, v191\n\t" \
    "buffer_store_dwordx4 v[184:187], %[pa], %[ro], %[so] offen nt\n\t" \
    "buffer_store_dwordx4 v[188:191], %[pa], %[ro], %[so] offen offset:64 nt\n\t" \
    "buffer_store_dwordx2 v[192:193], %[pb], %[ro], %[so] offen offset:128 nt\n\t" \
    "buffer_load_dwordx2 v[164:165], %[o0], %[rd], %[n1] offen sc1\n\t" \
    "buffer_load_dwordx2 v[166:167], %[o1], %[rd], %[n1] offen sc1\n\t" \
    "buffer_load_dwordx2 v[168:169], %[o2], %[rd], %[n1] offen sc1\n\t" \
    "buffer_load_dwordx2 v[170:171], %[o3], %[rd], %[n1] offen sc1\n\t" \
    "buffer_load_dwordx2 v[172:173], %[o4], %[rd], %[n1] offen sc1\n\t" \
    "buffer_load_dwordx2 v[174:175], %[o0], %[rd], %[na] offen sc1\n\t" \
    "buffer_load_dwordx2 v[176:177], %[o1], %[rd], %[na] offen sc1\n\t" \
    "buffer_load_dwordx2 v[178:179], %[o2], %[rd], %[na] offen sc1\n\t" \
    "buffer_load_dwordx2 v[180:181], %[o3], %[rd], %[na] offen sc1\n\t" \
    "buffer_load_dwordx2 v[182:183], %[o4], %[rd], %[na] offen sc1\n\t"
#define ASM_WCS \
    "v_mfma_f32_16x16x32_bf16 v[196:199], v[0:3], v[216:219], 0\n\t" \
    "v_mfma_f32_16x16x32_bf16 v[200:203], v[80:83], v[216:219], 0\n\t" \
    "v_mfma_f32_16x16x32_bf16 v[196:199], v[4:7], v[216:219], v[196:199]\n\t" \
    "v_mfma_f32_16x16x32_bf16 v[200:203], v[84:87], v[216:219], v[200:203]\n\t" \
    "v_mfma_f32_16x16x32_bf16 v[196:199], v[8:11], v[216:219], v[196:199]\n\t" \
    "v_mfma_f32_16x16x32_bf16 v[200:203], v[88:91], v[216:219], v[200:203]\n\t" \
    "v_mfma_f32_16x16x32_bf16 v[196:199], v[12:15], v[216:219], v[196:199]\n\t" \
    "v_mfma_f32_16x16x32_bf16 v[200:203], v[92:95], v[216:219], v[200:203]\n\t" \
    "v_mfma_f32_16x16x32_bf16 v[196:199], v[16:19], v[216:219], v[196:199]\n\t" \
    "v_mfma_f32_16x16x32_bf16 v[200:203], v[96:99], v[216:219], v[200:203]\n\t" \
    "v_mfma_f32_16x16x32_bf16 v[196:199], v[20:23], v[216:219], v[196:199]\n\t" \
    "v_mfma_f32_16x16x32_bf16 v[200:203], v[100:103], v[216:219], v[200:203]\n\t" \
    "v_mfma_f32_16x16x32_bf16 v[196:199], v[24:27], v[216:219], v[196:199]\n\t" \
    "v_mfma_f32_16x16x32_bf16 v[200:203], v[104:107], v[216:219], v[200:203]\n\t" \
    "v_mfma_f32_16x16x32_bf16 v[196:199], v[28:31], v[216:219], v[196:199]\n\t" \
    "v_mfma_f32_16x16x32_bf16 v[200:203], v[108:111], v[216:219], v[200:203]\n\t" \
    "v_mfma_f32_16x16x32_bf16 v[196:199], v[32:35], v[216:219], v[196:199]\n\t" \
    "v_mfma_f32_16x16x32_bf16 v[200:203], v[112:115], v[216:219], v[200:203]\n\t" \
    "v_mfma_f32_16x16x32_bf16 v[196:199], v[36:39], v[216:219], v[196:199]\n\t" \
    "v_mfma_f32_16x16x32_bf16 v[200:203], v[116:119], v[216:219], v[200:203]\n\t" \
    "v_mfma_f32_16x16x32_bf16 v[196:199], v[40:43], v[216:219], v[196:199]\n\t" \
    "v_mfma_f32_16x16x32_bf16 v[200:203], v[120:123], v[216:219], v[200:203]\n\t" \
    "v_mfma_f32_16x16x32_bf16 v[196:199], v[44:47], v[216:219], v[196:199]\n\t" \
    "v_mfma_f32_16x16x32_bf16 v[200:203], v[124:127], v[216:219], v[200:203]\n\t" \
    "v_mfma_f32_16x16x32_bf16 v[196:199], v[48:51], v[216:219], v[196:199]\n\t" \
    "v_mfma_f32_16x16x32_bf16 v[200:203], v[128:131], v[216:219], v[200:203]\n\t" \
    "v_mfma_f32_16x16x32_bf16 v[196:199], v[52:55], v[216:219], v[196:199]\n\t" \
    "v_mfma_f32_16x16x32_bf16 v[200:203], v[132:135], v[216:219], v[200:203]\n\t" \
    "v_mfma_f32_16x16x32_bf16 v[196:199], v[56:59], v[216:219], v[196:199]\n\t" \
    "v_mfma_f32_16x16x32_bf16 v[200:203], v[136:139], v[216:219], v[200:203]\n\t" \
    "v_mfma_f32_16x16x32_bf16 v[196:199], v[60:63], v[216:219], v[196:199]\n\t" \
    "v_mfma_f32_16x16x32_bf16 v[200:203], v[140:143], v[216:219], v[200:203]\n\t" \
    "v_mfma_f32_16x16x32_bf16 v[196:199], v[64:67], v[216:219], v[196:199]\n\t" \
    "v_mfma_f32_16x16x32_bf16 v[200:203], v[144:147], v[216:219], v[200:203]\n\t" \
    "v_mfma_f32_16x16x32_bf16 v[196:199], v[68:71], v[216:219], v[196:199]\n\t" \
    "v_mfma_f32_16x16x32_bf16 v[200:203], v[148:151], v[216:219], v[200:203]\n\t" \
    "v_mfma_f32_16x16x32_bf16 v[196:199], v[72:75], v[216:219], v[196:199]\n\t" \
    "v_mfma_f32_16x16x32_bf16 v[200:203], v[152:155], v[216:219], v[200:203]\n\t" \
    "v_mfma_f32_16x16x32_bf16 v[196:199], v[76:79], v[216:219], v[196:199]\n\t" \
    "v_mfma_f32_16x16x32_bf16 v[200:203], v[156:159], v[216:219], v[200:203]\n\t" \
    "s_nop 7\n\t"
#define ASM_WT1_LO \
    "s_branch .Lt0%=\n\t" \
    ".Ltr%=:\n\t" \
    "s_cmp_gt_i32 %[n], 0\n\t" \
    "s_cbranch_scc0 .Ltg%=\n\t" \
    ".Lfw%=:\n\t" \
    "s_mov_b32 %[a], 0\n\t" \
    "v_max_u32 v252, v232, v233\n\t" \
    "v_max3_u32 v252, v234, v235, v252\n\t" \
    "v_cmp_eq_u32 vcc, -1, v252\n\t" \
    "s_cbranch_vccz .Lgw0_%=\n\t" \
    "buffer_load_dwordx4 v[232:235], %[l16], %[rs], %[i0] offen offset:0 sc1\n\t" \
    "s_mov_b32 %[a], 1\n\t" \
    ".Lgw0_%=:\n\t" \
    "v_max_u32 v252, v236, v237\n\t" \
    "v_max3_u32 v252, v238, v239, v252\n\t" \
    "v_cmp_eq_u32 vcc, -1, v252\n\t" \
    "s_cbranch_vccz .Lgw1_%=\n\t" \
    "buffer_load_dwordx4 v[236:239], %[l16], %[rs], %[i0] offen offset:1024 sc1\n\t" \
    "s_mov_b32 %[a], 1\n\t" \
    ".Lgw1_%=:\n\t" \
    "v_max_u32 v252, v240, v241\n\t" \
    "v_max3_u32 v252, v242, v243, v252\n\t" \
    "v_cmp_eq_u32 vcc, -1, v252\n\t" \
    "s_cbranch_vccz .Lgw2_%=\n\t" \
    "buffer_load_dwordx4 v[240:243], %[l16], %[rs], %[i0] offen offset:2048 sc1\n\t" \
    "s_mov_b32 %[a], 1\n\t" \
    ".Lgw2_%=:\n\t" \
    "v_max_u32 v252, v244, v245\n\t" \
    "v_max3_u32 v252, v246, v247, v252\n\t" \
    "v_cmp_eq_u32 vcc, -1, v252\n\t" \
    "s_cbranch_vccz .Lgw3_%=\n\t" \
    "buffer_load_dwordx4 v[244:247], %[l16], %[rs], %[i0] offen offset:3072 sc1\n\t" \
    "s_mov_b32 %[a], 1\n\t" \
    ".Lgw3_%=:\n\t" \
    "v_max_u32 v252, v248, v249\n\t" \
    "v_max3_u32 v252, v250, v251, v252\n\t" \
    "v_cmp_eq_u32 vcc, -1, v252\n\t" \
    "s_cbranch_vccz .Lgw4_%=\n\t" \
    "buffer_load_dwordx4 v[248:251], %[l16], %[rs], %[i1] offen offset:0 sc1\n\t" \
    "s_mov_b32 %[a], 1\n\t" \
    ".Lgw4_%=:\n\t" \
    "s_cmp_eq_u32 %[a], 0\n\t" \
    "s_cbranch_scc1 .Lhw%=\n\t" \
    "s_waitcnt vmcnt(20)\n\t" \
    ".Liw%=:\n\t" \
    "v_max_u32 v252, v232, v233\n\t" \
    "v_max3_u32 v252, v234, v235, v252\n\t" \
    "v_max3_u32 v252, v236, v237, v252\n\t" \
    "v_max3_u32 v252, v238, v239, v252\n\t" \
    "v_max3_u32 v252, v240, v241, v252\n\t" \
    "v_max3_u32 v252, v242, v243, v252\n\t" \
    "v_max3_u32 v252, v244, v245, v252\n\t" \
    "v_max3_u32 v252, v246, v247, v252\n\t" \
    "v_max3_u32 v252, v248, v249, v252\n\t" \
    "v_max3_u32 v252, v250, v251, v252\n\t" \
    "v_cmp_eq_u32 vcc, -1, v252\n\t" \
    "s_cbranch_vccz .Lhw%=\n\t" \
    "s_sleep 1\n\t" \
    "v_max_u32 v252, v232, v233\n\t" \
    "v_max3_u32 v252, v234, v235, v252\n\t" \
    "v_max3_u32 v252, v236, v237, v252\n\t" \
    "v_max3_u32 v252, v238, v239, v252\n\t" \
    "v_max3_u32 v252, v240, v241, v252\n\t" \
    "v_max3_u32 v252, v242, v243, v252\n\t" \
    "v_max3_u32 v252, v244, v245, v252\n\t" \
    "v_max3_u32 v252, v246, v247, v252\n\t" \
    "v_max3_u32 v252, v248, v249, v252\n\t" \
    "v_max3_u32 v252, v250, v251, v252\n\t" \
    "v_cmp_eq_u32 vcc, -1, v252\n\t" \
    "s_cbranch_vccz .Lhw%=\n\t" \
    "s_sleep 1\n\t" \
    "v_max_u32 v252, v232, v233\n\t" \
    "v_max3_u32 v252, v234, v235, v252\n\t" \
    "v_max3_u32 v252, v236, v237, v252\n\t" \
    "v_max3_u32 v252, v238, v239, v252\n\t" \
    "v_max3_u32 v252, v240, v241, v252\n\t" \
    "v_max3_u32 v252, v242, v243, v252\n\t" \
    "v_max3_u32 v252, v244, v245, v252\n\t" \
    "v_max3_u32 v252, v246, v247, v252\n\t" \
    "v_max3_u32 v252, v248, v249, v252\n\t" \
    "v_max3_u32 v252, v250, v251, v252\n\t" \
    "v_cmp_eq_u32 vcc, -1, v252\n\t" \
    "s_cbranch_vccz .Lhw%=\n\t" \
    "s_sleep 1\n\t" \
    "v_max_u32 v252, v232, v233\n\t" \
    "v_max3_u32 v252, v234, v235, v252\n\t" \
    "v_max3_u32 v252, v236, v237, v252\n\t" \
    "v_max3_u32 v252, v238, v239, v252\n\t" \
    "v_max3_u32 v252, v240, v241, v252\n\t" \
    "v_max3_u32 v252, v242, v243, v252\n\t" \
    "v_max3_u32 v252, v244, v245, v252\n\t" \
    "v_max3_u32 v252, v246, v247, v252\n\t" \
    "v_max3_u32 v252, v248, v249, v252\n\t" \
    "v_max3_u32 v252, v250, v251, v252\n\t" \
    "v_cmp_eq_u32 vcc, -1, v252\n\t" \
    "s_cbranch_vccz .Lhw%=\n\t" \
    "s_sleep 1\n\t" \
    "v_max_u32 v252, v232, v233\n\t" \
    "v_max3_u32 v252, v234, v235, v252\n\t" \
    "v_max3_u32 v252, v236, v237, v252\n\t" \
    "v_max3_u32 v252, v238, v239, v252\n\t" \
    "v_max3_u32 v252, v240, v241, v252\n\t" \
    "v_max3_u32 v252, v242, v243, v252\n\t" \
    "v_max3_u32 v252, v244, v245, v252\n\t" \
    "v_max3_u32 v252, v246, v247, v252\n\t" \
    "v_max3_u32 v252, v248, v249, v252\n\t" \
    "v_max3_u32 v252, v250, v251, v252\n\t" \
    "v_cmp_eq_u32 vcc, -1, v252\n\t" \
    "s_cbranch_vccz .Lhw%=\n\t" \
    "s_sleep 1\n\t" \
    "v_max_u32 v252, v232, v233\n\t" \
    "v_max3_u32 v252, v234, v235, v252\n\t" \
    "v_max3_u32 v252, v236, v237, v252\n\t" \
    "v_max3_u32 v252, v238, v239, v252\n\t" \
    "v_max3_u32 v252, v240, v241, v252\n\t" \
    "v_max3_u32 v252, v242, v243, v252\n\t" \
    "v_max3_u32 v252, v244, v245, v252\n\t" \
    "v_max3_u32 v252, v246, v247, v252\n\t" \
    "v_max3_u32 v252, v248, v249, v252\n\t" \
    "v_max3_u32 v252, v250, v251, v252\n\t" \
    "v_cmp_eq_u32 vcc, -1, v252\n\t" \
    "s_cbranch_vccz .Lhw%=\n\t" \
    "s_sleep 1\n\t" \
    "s_sub_u32 %[n], %[n], 1\n\t" \
    "s_cmp_gt_i32 %[n], 0\n\t" \
    "s_cbranch_scc1 .Lfw%=\n\t" \
    ".Lhw%=:\n\t" \
    "s_branch .Lta%=\n\t" \
    ".Lt0%=:\n\t" \
    "s_waitcnt vmcnt(1)\n\t" \
    ".Lta%=:\n\t" \
    "ds_write_b128 %[lw], v[232:235] offset:0\n\t" \
    "v_max_u32 v252, v232, v233\n\t" \
    "v_max3_u32 v252, v234, v235, v252\n\t" \
    "v_mfma_f32_16x16x32_bf16 v[188:191], v[232:235], v[232:235], 0\n\t" \
    "ds_write_b128 %[lw], v[236:239] offset:1024\n\t" \
    "v_max3_u32 v252, v236, v237, v252\n\t" \
    "v_max3_u32 v252, v238, v239, v252\n\t" \
    "v_mfma_f32_16x16x32_bf16 v[188:191], v[236:239], v[236:239], v[188:191]\n\t" \
    "ds_write_b128 %[lw], v[240:243] offset:2048\n\t" \
    "v_max3_u32 v252, v240, v241, v252\n\t" \
    "v_max3_u32 v252, v242, v243, v252\n\t" \
    "v_mfma_f32_16x16x32_bf16 v[188:191], v[240:243], v[240:243], v[188:191]\n\t" \
    "ds_write_b128 %[lw], v[244:247] offset:3072\n\t" \
    "v_max3_u32 v252, v244, v245, v252\n\t" \
    "v_max3_u32 v252, v246, v247, v252\n\t" \
    "v_mfma_f32_16x16x32_bf16 v[188:191], v[244:247], v[244:247], v[188:191]\n\t" \
    "ds_write_b128 %[lw], v[248:251] offset:4096\n\t" \
    "v_max3_u32 v252, v248, v249, v252\n\t" \
    "v_max3_u32 v252, v250, v251, v252\n\t" \
    "v_mfma_f32_16x16x32_bf16 v[188:191], v[248:251], v[248:251], v[188:191]\n\t" \
    "v_mfma_f32_16x16x32_bf16 v[192:195], v[216:219], v[232:235], 0\n\t" \
    "v_mfma_f32_16x16x32_bf16 v[192:195], v[216:219], v[236:239], v[192:195]\n\t" \
    "v_mfma_f32_16x16x32_bf16 v[192:195], v[216:219], v[240:243], v[192:195]\n\t" \
    "v_mfma_f32_16x16x32_bf16 v[192:195], v[216:219], v[244:247], v[192:195]\n\t" \
    "v_mfma_f32_16x16x32_bf16 v[192:195], v[216:219], v[248:251], v[192:195]\n\t" \
    "v_cmp_eq_u32 vcc, -1, v252\n\t" \
    "s_cbranch_vccnz .Ltr%=\n\t" \
    ".Ltg%=:\n\t" \
    ASM_ST2 \
    "s_nop 5\n\t" \
    "ds_write_b128 %[lw], v[188:191] offset:5120\n\t" \
    "ds_write_b128 %[lw], v[192:195] offset:6144\n\t" \
    "s_waitcnt lgkmcnt(0)\n\t" \
    "s_setprio 2\n\t" \
    "s_barrier\n\t" ASM_ST \
    "ds_read_b128 v[160:163], %[la] offset:0\n\t" \
    "ds_read_b128 v[164:167], %[la] offset:1024\n\t" \
    "ds_read_b128 v[168:171], %[la] offset:2048\n\t" \
    "ds_read2st64_b32 v[204:205], %[sr] offset0:20 offset1:24\n\t" \
    "ds_read_b128 v[172:175], %[la] offset:3072\n\t" \
    "ds_read_b128 v[176:179], %[la] offset:8192\n\t" \
    "ds_read_b128 v[180:183], %[la] offset:9216\n\t" \
    "ds_read_b128 v[184:187], %[la] offset:10240\n\t" \
    "s_waitcnt lgkmcnt(7)\n\t" \
    "v_mfma_f32_16x16x32_bf16 v[192:195], v[80:83], v[160:163], 0\n\t" \
    "s_waitcnt lgkmcnt(6)\n\t" \
    "v_mfma_f32_16x16x32_bf16 v[192:195], v[84:87], v[164:167], v[192:195]\n\t" \
    "v_mfma_f32_16x16x32_bf16 v[188:191], v[0:3], v[160:163], 0\n\t" \
    "ds_read_b128 v[160:163], %[la] offset:11264\n\t" \
    "v_mfma_f32_16x16x32_bf16 v[188:191], v[4:7], v[164:167], v[188:191]\n\t" \
    "ds_read_b128 v[164:167], %[la] offset:16384\n\t" \
    "s_waitcnt lgkmcnt(7)\n\t" \
    "v_mfma_f32_16x16x32_bf16 v[188:191], v[8:11], v[168:171], v[188:191]\n\t" \
    "s_waitcnt lgkmcnt(6)\n\t" \
    "s_waitcnt lgkmcnt(5)\n\t" \
    "v_mfma_f32_16x16x32_bf16 v[188:191], v[12:15], v[172:175], v[188:191]\n\t" \
    "v_permlane16_swap_b32 v204, v205\n\t" \
    "v_mfma_f32_16x16x32_bf16 v[192:195], v[88:91], v[168:171], v[192:195]\n\t" \
    "ds_read_b128 v[168:171], %[la] offset:17408\n\t" \
    "v_mfma_f32_16x16x32_bf16 v[192:195], v[92:95], v[172:175], v[192:195]\n\t" \
    "ds_read_b128 v[172:175], %[la] offset:18432\n\t" \
    "v_add_f32 v206, v204, v205\n\t" \
    "s_waitcnt lgkmcnt(6)\n\t" \
    "v_mfma_f32_16x16x32_bf16 v[192:195], v[96:99], v[176:179], v[192:195]\n\t" \
    "v_add_f32 v207, v204, v205\n\t" \
    "s_waitcnt lgkmcnt(5)\n\t" \
    "v_mfma_f32_16x16x32_bf16 v[192:195], v[100:103], v[180:183], v[192:195]\n\t" \
    "v_mfma_f32_16x16x32_bf16 v[188:191], v[16:19], v[176:179], v[188:191]\n\t" \
    "ds_read_b128 v[176:179], %[la] offset:19456\n\t" \
    "v_permlane32_swap_b32 v206, v207\n\t" \
    "v_mfma_f32_16x16x32_bf16 v[188:191], v[20:23], v[180:183], v[188:191]\n\t" \
    "ds_read_b128 v[180:183], %[la] offset:24576\n\t" \
    "s_waitcnt lgkmcnt(6)\n\t" \
    "v_mfma_f32_16x16x32_bf16 v[188:191], v[24:27], v[184:187], v[188:191]\n\t" \
    "v_add_f32 v204, v206, v207\n\t" \
    "s_waitcnt lgkmcnt(5)\n\t" \
    "v_mfma_f32_16x16x32_bf16 v[188:191], v[28:31], v[160:163], v[188:191]\n\t" \
    "v_add_f32 v205, v206, v207\n\t" \
    "v_mfma_f32_16x16x32_bf16 v[192:195], v[104:107], v[184:187], v[192:195]\n\t" \
    "ds_read_b128 v[184:187], %[la] offset:25600\n\t" \
    "v_mfma_f32_16x16x32_bf16 v[192:195], v[108:111], v[160:163], v[192:195]\n\t" \
    "ds_read_b128 v[160:163], %[la] offset:26624\n\t" \
    "v_permlane16_swap_b32 v204, v205\n\t" \
    "s_waitcnt lgkmcnt(6)\n\t" \
    "v_mfma_f32_16x16x32_bf16 v[192:195], v[112:115], v[164:167], v[192:195]\n\t" \
    "s_waitcnt lgkmcnt(5)\n\t" \
    "v_mfma_f32_16x16x32_bf16 v[192:195], v[116:119], v[168:171], v[192:195]\n\t" \
    "v_mul_f32 v205, %[kd], v205\n\t" \
    "v_mfma_f32_16x16x32_bf16 v[188:191], v[32:35], v[164:167], v[188:191]\n\t" \
    "ds_read_b128 v[164:167], %[la] offset:27648\n\t" \
    "v_mul_f32 v204, %[kd], v204\n\t" \
    "v_mfma_f32_16x16x32_bf16 v[188:191], v[36:39], v[168:171], v[188:191]\n\t" \
    "ds_read_b128 v[168:171], %[la] offset:4096\n\t" \
    "v_fma_f32 v204, -v205, v205, v204\n\t" \
    "s_waitcnt lgkmcnt(6)\n\t" \
    "v_mfma_f32_16x16x32_bf16 v[188:191], v[40:43], v[172:175], v[188:191]\n\t" \
    "v_max_f32 v204, 0, v204\n\t" \
    "s_waitcnt lgkmcnt(5)\n\t" \
    "v_mfma_f32_16x16x32_bf16 v[188:191], v[44:47], v[176:179], v[188:191]\n\t" \
    "v_add_f32 v206, %[ke], v204\n\t" \
    "v_mfma_f32_16x16x32_bf16 v[192:195], v[120:123], v[172:175], v[192:195]\n\t" \
    "ds_read_b128 v[172:175], %[la] offset:12288\n\t" \
    "v_rsq_f32 v206, v206\n\t" \
    "v_mfma_f32_16x16x32_bf16 v[192:195], v[124:127], v[176:179], v[192:195]\n\t" \
    "ds_read_b128 v[176:179], %[la] offset:20480\n\t" \
    "s_waitcnt lgkmcnt(6)\n\t" \
    "v_mfma_f32_16x16x32_bf16 v[192:195], v[128:131], v[180:183], v[192:195]\n\t" \
    "v_mul_f32 v207, v204, v206\n\t" \
    "s_waitcnt lgkmcnt(5)\n\t" \
    "v_mfma_f32_16x16x32_bf16 v[192:195], v[132:135], v[184:187], v[192:195]\n\t" \
    "v_fma_f32 v207, v207, v206, %[ke]\n\t" \
    "v_mfma_f32_16x16x32_bf16 v[188:191], v[48:51], v[180:183], v[188:191]\n\t" \
    "ds_read_b128 v[180:183], %[la] offset:28672\n\t" \
    "v_rsq_f32 v207, v207\n\t" \
    "v_mfma_f32_16x16x32_bf16 v[188:191], v[52:55], v[184:187], v[188:191]\n\t" \
    "v_mul_f32 v206, v206, v207\n\t" \
    "v_mul_f32 v207, -v205, v206\n\t" \
    "s_waitcnt lgkmcnt(5)\n\t" \
    "v_mfma_f32_16x16x32_bf16 v[188:191], v[56:59], v[160:163], v[188:191]\n\t" \
    "v_mul_f32 v208, v196, v207\n\t" \
    "v_mul_f32 v209, v197, v207\n\t" \
    "s_waitcnt lgkmcnt(4)\n\t" \
    "v_mfma_f32_16x16x32_bf16 v[188:191], v[60:63], v[164:167], v[188:191]\n\t" \
    "v_mul_f32 v210, v198, v207\n\t" \
    "v_mul_f32 v211, v199, v207\n\t" \
    "v_mfma_f32_16x16x32_bf16 v[192:195], v[136:139], v[160:163], v[192:195]\n\t" \
    "v_mul_f32 v212, v200, v207\n\t" \
    "v_mul_f32 v213, v201, v207\n\t" \
    "v_mfma_f32_16x16x32_bf16 v[192:195], v[140:143], v[164:167], v[192:195]\n\t" \
    "v_mul_f32 v214, v202, v207\n\t" \
    "v_mul_f32 v215, v203, v207\n\t" \
    "s_waitcnt lgkmcnt(3)\n\t" \
    "v_mfma_f32_16x16x32_bf16 v[192:195], v[144:147], v[168:171], v[192:195]\n\t" \
    "s_cmp_lg_u32 %[sw], 0\n\t" \
    "s_cbranch_scc1 .Lts%=\n\t" \
    "v_cvt_pk_bf16_f32 v217, v204, v204\n\t" \
    "v_lshlrev_b32 v217, 16, v217\n\t" \
    "v_add_f32 v217, %[ke], v217\n\t" \
    "v_cvt_pk_bf16_f32 v217, v217, v217\n\t" \
    "v_lshlrev_b32 v217, 16, v217\n\t" \
    "v_sqrt_f32 v217, v217\n\t" \
    "v_cvt_pk_bf16_f32 v216, v205, v205\n\t" \
    "v_lshlrev_b32 v216, 16, v216\n\t" \
    "v_cvt_pk_bf16_f32 v217, v217, v217\n\t" \
    "v_lshlrev_b32 v217, 16, v217\n\t" \
    "v_rcp_f32 v217, v217\n\t" \
    "v_lshrrev_b32 v218, 1, %[l16]\n\t" \
    "v_and_b32 v218, 0x78, v218\n\t" \
    "buffer_store_dwordx2 v[216:217], v218, %[rs], %[ss] offen sc1\n\t" \
    ".Lts%=:\n\t" \
    "v_mov_b64 v[216:217], -1\n\t" \
    "s_waitcnt lgkmcnt(2)\n\t" \
    "v_mfma_f32_16x16x32_bf16 v[192:195], v[148:151], v[172:175], v[192:195]\n\t" \
    "v_mov_b64 v[218:219], -1\n\t" \
    "buffer_store_dwordx4 v[216:219], %[ob], %[rs], %[sa] offen sc1\n\t" \
    "v_mfma_f32_16x16x32_bf16 v[188:191], v[64:67], v[168:171], v[188:191]\n\t" \
    "v_mfma_f32_16x16x32_bf16 v[188:191], v[68:71], v[172:175], v[188:191]\n\t" \
    "s_waitcnt lgkmcnt(1)\n\t" \
    "v_mfma_f32_16x16x32_bf16 v[188:191], v[72:75], v[176:179], v[188:191]\n\t" \
    "s_waitcnt lgkmcnt(0)\n\t" \
    "v_mfma_f32_16x16x32_bf16 v[188:191], v[76:79], v[180:183], v[188:191]\n\t" \
    "v_mfma_f32_16x16x32_bf16 v[192:195], v[152:155], v[176:179], v[192:195]\n\t" \
    "v_mfma_f32_16x16x32_bf16 v[192:195], v[156:159], v[180:183], v[192:195]\n\t" \
    "s_setprio 2\n\t" \
    "buffer_load_dwordx4 v[232:235], %[l16], %[rs], %[n0] offen offset:0 sc1\n\t" \
    "buffer_load_dwordx4 v[236:239], %[l16], %[rs], %[n0] offen offset:1024 sc1\n\t" \
    "buffer_load_dwordx4 v[240:243], %[l16], %[rs], %[n0] offen offset:2048 sc1\n\t" \
    "buffer_load_dwordx4 v[244:247], %[l16], %[rs], %[n0] offen offset:3072 sc1\n\t" \
    "buffer_load_dwordx4 v[248:251], %[l16], %[rs], %[n1] offen offset:0 sc1\n\t" \
    ASM_ST3 \
    "v_fma_f32 v188, v188, v206, v208\n\t" \
    "v_fma_f32 v189, v189, v206, v209\n\t" \
    "v_fma_f32 v190, v190, v206, v210\n\t" \
    "v_fma_f32 v191, v191, v206, v211\n\t" \
    "v_mul_f32 v160, v188, v188\n\t" \
    "v_mul_f32 v161, v189, v189\n\t" \
    "v_mul_f32 v162, v190, v190\n\t" \
    "v_mul_f32 v163, v191, v191\n\t" \
    "v_fma_f32 v160, v160, %[k1], v220\n\t" \
    "v_fma_f32 v161, v161, %[k1], v220\n\t" \
    "v_fma_f32 v162, v162, %[k1], v220\n\t" \
    "v_fma_f32 v163, v163, %[k1], v220\n\t" \
    "v_mul_f32 v160, v188, v160\n\t" \
    "v_mul_f32 v161, v189, v161\n\t" \
    "v_mul_f32 v162, v190, v162\n\t" \
    "v_mul_f32 v163, v191, v163\n\t" \
    "v_fma_f32 v192, v192, v206, v212\n\t" \
    "v_fma_f32 v193, v193, v206, v213\n\t" \
    "v_fma_f32 v194, v194, v206, v214\n\t" \
    "v_fma_f32 v195, v195, v206, v215\n\t" \
    "v_mul_f32 v164, v192, v192\n\t" \
    "v_mul_f32 v165, v193, v193\n\t" \
    "v_mul_f32 v166, v194, v194\n\t" \
    "v_mul_f32 v167, v195, v195\n\t" \
    "v_fma_f32 v164, v164, %[k1], v220\n\t" \
    "v_fma_f32 v165, v165, %[k1], v220\n\t" \
    "v_fma_f32 v166, v166, %[k1], v220\n\t" \
    "v_fma_f32 v167, v167, %[k1], v220\n\t" \
    "v_mul_f32 v164, v192, v164\n\t" \
    "v_mul_f32 v165, v193, v165\n\t" \
    "v_mul_f32 v166, v194, v166\n\t" \
    "v_mul_f32 v167, v195, v167\n\t" \
    "v_exp_f32 v160, v160\n\t" \
    "v_exp_f32 v161, v161\n\t" \
    "v_exp_f32 v162, v162\n\t" \
    "v_exp_f32 v163, v163\n\t" \
    "v_exp_f32 v164, v164\n\t" \
    "v_exp_f32 v165, v165\n\t" \
    "v_exp_f32 v166, v166\n\t" \
    "v_exp_f32 v167, v167\n\t" \
    "v_add_f32 v160, 1.0, v160\n\t" \
    "v_add_f32 v161, 1.0, v161\n\t" \
    "v_add_f32 v162, 1.0, v162\n\t" \
    "v_add_f32 v163, 1.0, v163\n\t" \
    "v_add_f32 v164, 1.0, v164\n\t" \
    "v_add_f32 v165, 1.0, v165\n\t" \
    "v_add_f32 v166, 1.0, v166\n\t" \
    "v_add_f32 v167, 1.0, v167\n\t" \
    "v_rcp_f32 v160, v160\n\t" \
    "v_rcp_f32 v161, v161\n\t" \
    "v_rcp_f32 v162, v162\n\t" \
    "v_rcp_f32 v163, v163\n\t" \
    "v_rcp_f32 v164, v164\n\t" \
    "v_rcp_f32 v165, v165\n\t" \
    "v_rcp_f32 v166, v166\n\t" \
    "v_rcp_f32 v167, v167\n\t" \
    "v_mul_f32 v188, v188, v160\n\t" \
    "v_mul_f32 v189, v189, v161\n\t" \
    "v_mul_f32 v190, v190, v162\n\t" \
    "v_mul_f32 v191, v191, v163\n\t" \
    "v_mul_f32 v192, v192, v164\n\t" \
    "v_mul_f32 v193, v193, v165\n\t" \
    "v_mul_f32 v194, v194, v166\n\t" \
    "v_mul_f32 v195, v195, v167\n\t" \
    "v_cvt_pk_bf16_f32 v188, v188, v189\n\t" \
    "v_cvt_pk_bf16_f32 v189, v190, v191\n\t" \
    "v_cvt_pk_bf16_f32 v190, v192, v193\n\t" \
    "v_cvt_pk_bf16_f32 v191, v194, v195\n\t" \
    "v_min_u32 v188, -2, v188\n\t" \
    "v_min_u32 v189, -2, v189\n\t" \
    "v_min_u32 v190, -2, v190\n\t" \
    "v_min_u32 v191, -2, v191\n\t" \
    "v_mov_b32 v216, 0x3f803f80\n\t" \
    "v_mov_b32 v217, 0x3f803f80\n\t" \
    "v_mov_b32 v218, 0x3f803f80\n\t" \
    "v_mov_b32 v219, 0x3f803f80\n\t" \
    "v_permlane16_swap_b32 v188, v190\n\t" \
    "v_permlane16_swap_b32 v189, v191\n\t" \
    "s_waitcnt vmcnt(6)\n\t" \
    "buffer_store_dwordx4 v[188:191], %[oa], %[rs], %[so] offen sc1\n\t" \
    "s_nop 1\n\t" \
    "s_setprio 0\n\t"
#define ASM_WT1_HI \
    "s_setprio 1\n\t" \
    "s_barrier\n\t" ASM_ST \
    "ds_read_b128 v[160:163], %[la] offset:0\n\t" \
    "ds_read_b128 v[164:167], %[la] offset:1024\n\t" \
    "ds_read_b128 v[168:171], %[la] offset:2048\n\t" \
    "ds_read2st64_b32 v[204:205], %[sr] offset0:20 offset1:24\n\t" \
    "ds_read_b128 v[172:175], %[la] offset:3072\n\t" \
    "ds_read_b128 v[176:179], %[la] offset:8192\n\t" \
    "ds_read_b128 v[180:183], %[la] offset:9216\n\t" \
    "ds_read_b128 v[184:187], %[la] offset:10240\n\t" \
    "s_waitcnt lgkmcnt(7)\n\t" \
    "v_mfma_f32_16x16x32_bf16 v[192:195], v[80:83], v[160:163], 0\n\t" \
    "s_waitcnt lgkmcnt(6)\n\t" \
    "v_mfma_f32_16x16x32_bf16 v[192:195], v[84:87], v[164:167], v[192:195]\n\t" \
    "v_mfma_f32_16x16x32_bf16 v[188:191], v[0:3], v[160:163], 0\n\t" \
    "ds_read_b128 v[160:163], %[la] offset:11264\n\t" \
    "v_mfma_f32_16x16x32_bf16 v[188:191], v[4:7], v[164:167], v[188:191]\n\t" \
    "ds_read_b128 v[164:167], %[la] offset:16384\n\t" \
    "s_waitcnt lgkmcnt(7)\n\t" \
    "v_mfma_f32_16x16x32_bf16 v[188:191], v[8:11], v[168:171], v[188:191]\n\t" \
    "s_waitcnt lgkmcnt(6)\n\t" \
    "s_waitcnt lgkmcnt(5)\n\t" \
    "v_mfma_f32_16x16x32_bf16 v[188:191], v[12:15], v[172:175], v[188:191]\n\t" \
    "v_permlane16_swap_b32 v204, v205\n\t" \
    "v_mfma_f32_16x16x32_bf16 v[192:195], v[88:91], v[168:171], v[192:195]\n\t" \
    "ds_read_b128 v[168:171], %[la] offset:17408\n\t" \
    "v_mfma_f32_16x16x32_bf16 v[192:195], v[92:95], v[172:175], v[192:195]\n\t" \
    "ds_read_b128 v[172:175], %[la] offset:18432\n\t" \
    "v_add_f32 v206, v204, v205\n\t" \
    "s_waitcnt lgkmcnt(6)\n\t" \
    "v_mfma_f32_16x16x32_bf16 v[192:195], v[96:99], v[176:179], v[192:195]\n\t" \
    "v_add_f32 v207, v204, v205\n\t" \
    "s_waitcnt lgkmcnt(5)\n\t" \
    "v_mfma_f32_16x16x32_bf16 v[192:195], v[100:103], v[180:183], v[192:195]\n\t" \
    "v_mfma_f32_16x16x32_bf16 v[188:191], v[16:19], v[176:179], v[188:191]\n\t" \
    "ds_read_b128 v[176:179], %[la] offset:19456\n\t" \
    "v_permlane32_swap_b32 v206, v207\n\t" \
    "v_mfma_f32_16x16x32_bf16 v[188:191], v[20:23], v[180:183], v[188:191]\n\t" \
    "ds_read_b128 v[180:183], %[la] offset:24576\n\t" \
    "s_waitcnt lgkmcnt(6)\n\t" \
    "v_mfma_f32_16x16x32_bf16 v[188:191], v[24:27], v[184:187], v[188:191]\n\t" \
    "v_add_f32 v204, v206, v207\n\t" \
    "s_waitcnt lgkmcnt(5)\n\t" \
    "v_mfma_f32_16x16x32_bf16 v[188:191], v[28:31], v[160:163], v[188:191]\n\t" \
    "v_add_f32 v205, v206, v207\n\t" \
    "v_mfma_f32_16x16x32_bf16 v[192:195], v[104:107], v[184:187], v[192:195]\n\t" \
    "ds_read_b128 v[184:187], %[la] offset:25600\n\t" \
    "v_mfma_f32_16x16x32_bf16 v[192:195], v[108:111], v[160:163], v[192:195]\n\t" \
    "ds_read_b128 v[160:163], %[la] offset:26624\n\t" \
    "v_permlane16_swap_b32 v204, v205\n\t" \
    "s_waitcnt lgkmcnt(6)\n\t" \
    "v_mfma_f32_16x16x32_bf16 v[192:195], v[112:115], v[164:167], v[192:195]\n\t" \
    "s_waitcnt lgkmcnt(5)\n\t" \
    "v_mfma_f32_16x16x32_bf16 v[192:195], v[116:119], v[168:171], v[192:195]\n\t" \
    "v_mul_f32 v205, %[kd], v205\n\t" \
    "v_mfma_f32_16x16x32_bf16 v[188:191], v[32:35], v[164:167], v[188:191]\n\t" \
    "ds_read_b128 v[164:167], %[la] offset:27648\n\t" \
    "v_mul_f32 v204, %[kd], v204\n\t" \
    "v_mfma_f32_16x16x32_bf16 v[188:191], v[36:39], v[168:171], v[188:191]\n\t" \
    "ds_read_b128 v[168:171], %[la] offset:4096\n\t" \
    "v_fma_f32 v204, -v205, v205, v204\n\t" \
    "s_waitcnt lgkmcnt(6)\n\t" \
    "v_mfma_f32_16x16x32_bf16 v[188:191], v[40:43], v[172:175], v[188:191]\n\t" \
    "v_max_f32 v204, 0, v204\n\t" \
    "s_waitcnt lgkmcnt(5)\n\t" \
    "v_mfma_f32_16x16x32_bf16 v[188:191], v[44:47], v[176:179], v[188:191]\n\t" \
    "v_add_f32 v206, %[ke], v204\n\t" \
    "v_mfma_f32_16x16x32_bf16 v[192:195], v[120:123], v[172:175], v[192:195]\n\t" \
    "ds_read_b128 v[172:175], %[la] offset:12288\n\t" \
    "v_rsq_f32 v206, v206\n\t" \
    "v_mfma_f32_16x16x32_bf16 v[192:195], v[124:127], v[176:179], v[192:195]\n\t" \
    "ds_read_b128 v[176:179], %[la] offset:20480\n\t" \
    "s_waitcnt lgkmcnt(6)\n\t" \
    "v_mfma_f32_16x16x32_bf16 v[192:195], v[128:131], v[180:183], v[192:195]\n\t" \
    "v_mul_f32 v207, v204, v206\n\t" \
    "s_waitcnt lgkmcnt(5)\n\t" \
    "v_mfma_f32_16x16x32_bf16 v[192:195], v[132:135], v[184:187], v[192:195]\n\t" \
    "v_fma_f32 v207, v207, v206, %[ke]\n\t" \
    "v_mfma_f32_16x16x32_bf16 v[188:191], v[48:51], v[180:183], v[188:191]\n\t" \
    "ds_read_b128 v[180:183], %[la] offset:28672\n\t" \
    "v_rsq_f32 v207, v207\n\t" \
    "v_mfma_f32_16x16x32_bf16 v[188:191], v[52:55], v[184:187], v[188:191]\n\t" \
    "v_mul_f32 v206, v206, v207\n\t" \
    "v_mul_f32 v207, -v205, v206\n\t" \
    "s_waitcnt lgkmcnt(5)\n\t" \
    "v_mfma_f32_16x16x32_bf16 v[188:191], v[56:59], v[160:163], v[188:191]\n\t" \
    "v_mul_f32 v208, v196, v207\n\t" \
    "v_mul_f32 v209, v197, v207\n\t" \
    "s_waitcnt lgkmcnt(4)\n\t" \
    "v_mfma_f32_16x16x32_bf16 v[188:191], v[60:63], v[164:167], v[188:191]\n\t" \
    "v_mul_f32 v210, v198, v207\n\t" \
    "v_mul_f32 v211, v199, v207\n\t" \
    "v_mfma_f32_16x16x32_bf16 v[192:195], v[136:139], v[160:163], v[192:195]\n\t" \
    "v_mul_f32 v212, v200, v207\n\t" \
    "v_mul_f32 v213, v201, v207\n\t" \
    "v_mfma_f32_16x16x32_bf16 v[192:195], v[140:143], v[164:167], v[192:195]\n\t" \
    "v_mul_f32 v214, v202, v207\n\t" \
    "v_mul_f32 v215, v203, v207\n\t" \
    "s_waitcnt lgkmcnt(3)\n\t" \
    "v_mfma_f32_16x16x32_bf16 v[192:195], v[144:147], v[168:171], v[192:195]\n\t" \
    "v_mov_b64 v[216:217], -1\n\t" \
    "v_mov_b64 v[218:219], -1\n\t" \
    "s_waitcnt lgkmcnt(2)\n\t" \
    "v_mfma_f32_16x16x32_bf16 v[192:195], v[148:151], v[172:175], v[192:195]\n\t" \
    "buffer_store_dwordx4 v[216:219], %[ob], %[rs], %[sa] offen sc1\n\t" \
    "v_mfma_f32_16x16x32_bf16 v[188:191], v[64:67], v[168:171], v[188:191]\n\t" \
    "v_mfma_f32_16x16x32_bf16 v[188:191], v[68:71], v[172:175], v[188:191]\n\t" \
    "s_waitcnt lgkmcnt(1)\n\t" \
    "v_mfma_f32_16x16x32_bf16 v[188:191], v[72:75], v[176:179], v[188:191]\n\t" \
    "s_waitcnt lgkmcnt(0)\n\t" \
    "v_mfma_f32_16x16x32_bf16 v[188:191], v[76:79], v[180:183], v[188:191]\n\t" \
    "v_mfma_f32_16x16x32_bf16 v[192:195], v[152:155], v[176:179], v[192:195]\n\t" \
    "v_mfma_f32_16x16x32_bf16 v[192:195], v[156:159], v[180:183], v[192:195]\n\t" \
    ASM_ST3 \
    "s_nop 1\n\t" \
    "v_fma_f32 v188, v188, v206, v208\n\t" \
    "v_fma_f32 v189, v189, v206, v209\n\t" \
    "v_fma_f32 v190, v190, v206, v210\n\t" \
    "v_fma_f32 v191, v191, v206, v211\n\t" \
    "v_mul_f32 v160, v188, v188\n\t" \
    "v_mul_f32 v161, v189, v189\n\t" \
    "v_mul_f32 v162, v190, v190\n\t" \
    "v_mul_f32 v163, v191, v191\n\t" \
    "v_fma_f32 v160, v160, %[k1], v220\n\t" \
    "v_fma_f32 v161, v161, %[k1], v220\n\t" \
    "v_fma_f32 v162, v162, %[k1], v220\n\t" \
    "v_fma_f32 v163, v163, %[k1], v220\n\t" \
    "v_mul_f32 v160, v188, v160\n\t" \
    "v_mul_f32 v161, v189, v161\n\t" \
    "v_mul_f32 v162, v190, v162\n\t" \
    "v_mul_f32 v163, v191, v163\n\t" \
    "v_fma_f32 v192, v192, v206, v212\n\t" \
    "v_fma_f32 v193, v193, v206, v213\n\t" \
    "v_fma_f32 v194, v194, v206, v214\n\t" \
    "v_fma_f32 v195, v195, v206, v215\n\t" \
    "v_mul_f32 v164, v192, v192\n\t" \
    "v_mul_f32 v165, v193, v193\n\t" \
    "v_mul_f32 v166, v194, v194\n\t" \
    "v_mul_f32 v167, v195, v195\n\t" \
    "v_fma_f32 v164, v164, %[k1], v220\n\t" \
    "v_fma_f32 v165, v165, %[k1], v220\n\t" \
    "v_fma_f32 v166, v166, %[k1], v220\n\t" \
    "v_fma_f32 v167, v167, %[k1], v220\n\t" \
    "v_mul_f32 v164, v192, v164\n\t" \
    "v_mul_f32 v165, v193, v165\n\t" \
    "v_mul_f32 v166, v194, v166\n\t" \
    "v_mul_f32 v167, v195, v167\n\t" \
    "v_exp_f32 v160, v160\n\t" \
    "v_exp_f32 v161, v161\n\t" \
    "v_exp_f32 v162, v162\n\t" \
    "v_exp_f32 v163, v163\n\t" \
    "v_exp_f32 v164, v164\n\t" \
    "v_exp_f32 v165, v165\n\t" \
    "v_exp_f32 v166, v166\n\t" \
    "v_exp_f32 v167, v167\n\t" \
    "v_add_f32 v160, 1.0, v160\n\t" \
    "v_add_f32 v161, 1.0, v161\n\t" \
    "v_add_f32 v162, 1.0, v162\n\t" \
    "v_add_f32 v163, 1.0, v163\n\t" \
    "v_add_f32 v164, 1.0, v164\n\t" \
    "v_add_f32 v165, 1.0, v165\n\t" \
    "v_add_f32 v166, 1.0, v166\n\t" \
    "v_add_f32 v167, 1.0, v167\n\t" \
    "v_rcp_f32 v160, v160\n\t" \
    "v_rcp_f32 v161, v161\n\t" \
    "v_rcp_f32 v162, v162\n\t" \
    "v_rcp_f32 v163, v163\n\t" \
    "v_rcp_f32 v164, v164\n\t" \
    "v_rcp_f32 v165, v165\n\t" \
    "v_rcp_f32 v166, v166\n\t" \
    "v_rcp_f32 v167, v167\n\t" \
    "v_mul_f32 v188, v188, v160\n\t" \
    "v_mul_f32 v189, v189, v161\n\t" \
    "v_mul_f32 v190, v190, v162\n\t" \
    "v_mul_f32 v191, v191, v163\n\t" \
    "v_mul_f32 v192, v192, v164\n\t" \
    "v_mul_f32 v193, v193, v165\n\t" \
    "v_mul_f32 v194, v194, v166\n\t" \
    "v_mul_f32 v195, v195, v167\n\t" \
    "v_cvt_pk_bf16_f32 v188, v188, v189\n\t" \
    "v_cvt_pk_bf16_f32 v189, v190, v191\n\t" \
    "v_cvt_pk_bf16_f32 v190, v192, v193\n\t" \
    "v_cvt_pk_bf16_f32 v191, v194, v195\n\t" \
    "v_min_u32 v188, -2, v188\n\t" \
    "v_min_u32 v189, -2, v189\n\t" \
    "v_min_u32 v190, -2, v190\n\t" \
    "v_min_u32 v191, -2, v191\n\t" \
    "s_nop 1\n\t" \
    "v_permlane16_swap_b32 v188, v190\n\t" \
    "v_permlane16_swap_b32 v189, v191\n\t" \
    "s_waitcnt vmcnt(1)\n\t" \
    "buffer_store_dwordx4 v[188:191], %[oa], %[rs], %[so] offen sc1\n\t" \
    "s_nop 1\n\t"
#define ASM_WT2_LO \
    "s_branch .Lt0%=\n\t" \
    ".Ltr%=:\n\t" \
    "s_cmp_gt_i32 %[n], 0\n\t" \
    "s_cbranch_scc0 .Ltg%=\n\t" \
    ".Lfw%=:\n\t" \
    "s_mov_b32 %[a], 0\n\t" \
    "v_max_u32 v252, v232, v233\n\t" \
    "v_max3_u32 v252, v234, v235, v252\n\t" \
    "v_cmp_eq_u32 vcc, -1, v252\n\t" \
    "s_cbranch_vccz .Lgw0_%=\n\t" \
    "buffer_load_dwordx4 v[232:235], %[l16], %[rs], %[i0] offen offset:0 sc1\n\t" \
    "s_mov_b32 %[a], 1\n\t" \
    ".Lgw0_%=:\n\t" \
    "v_max_u32 v252, v236, v237\n\t" \
    "v_max3_u32 v252, v238, v239, v252\n\t" \
    "v_cmp_eq_u32 vcc, -1, v252\n\t" \
    "s_cbranch_vccz .Lgw1_%=\n\t" \
    "buffer_load_dwordx4 v[236:239], %[l16], %[rs], %[i0] offen offset:1024 sc1\n\t" \
    "s_mov_b32 %[a], 1\n\t" \
    ".Lgw1_%=:\n\t" \
    "v_max_u32 v252, v240, v241\n\t" \
    "v_max3_u32 v252, v242, v243, v252\n\t" \
    "v_cmp_eq_u32 vcc, -1, v252\n\t" \
    "s_cbranch_vccz .Lgw2_%=\n\t" \
    "buffer_load_dwordx4 v[240:243], %[l16], %[rs], %[i0] offen offset:2048 sc1\n\t" \
    "s_mov_b32 %[a], 1\n\t" \
    ".Lgw2_%=:\n\t" \
    "v_max_u32 v252, v244, v245\n\t" \
    "v_max3_u32 v252, v246, v247, v252\n\t" \
    "v_cmp_eq_u32 vcc, -1, v252\n\t" \
    "s_cbranch_vccz .Lgw3_%=\n\t" \
    "buffer_load_dwordx4 v[244:247], %[l16], %[rs], %[i0] offen offset:3072 sc1\n\t" \
    "s_mov_b32 %[a], 1\n\t" \
    ".Lgw3_%=:\n\t" \
    "v_max_u32 v252, v248, v249\n\t" \
    "v_max3_u32 v252, v250, v251, v252\n\t" \
    "v_cmp_eq_u32 vcc, -1, v252\n\t" \
    "s_cbranch_vccz .Lgw4_%=\n\t" \
    "buffer_load_dwordx4 v[248:251], %[l16], %[rs], %[i1] offen offset:0 sc1\n\t" \
    "s_mov_b32 %[a], 1\n\t" \
    ".Lgw4_%=:\n\t" \
    "s_cmp_eq_u32 %[a], 0\n\t" \
    "s_cbranch_scc1 .Lhw%=\n\t" \
    "s_waitcnt vmcnt(20)\n\t" \
    ".Liw%=:\n\t" \
    "v_max_u32 v252, v232, v233\n\t" \
    "v_max3_u32 v252, v234, v235, v252\n\t" \
    "v_max3_u32 v252, v236, v237, v252\n\t" \
    "v_max3_u32 v252, v238, v239, v252\n\t" \
    "v_max3_u32 v252, v240, v241, v252\n\t" \
    "v_max3_u32 v252, v242, v243, v252\n\t" \
    "v_max3_u32 v252, v244, v245, v252\n\t" \
    "v_max3_u32 v252, v246, v247, v252\n\t" \
    "v_max3_u32 v252, v248, v249, v252\n\t" \
    "v_max3_u32 v252, v250, v251, v252\n\t" \
    "v_cmp_eq_u32 vcc, -1, v252\n\t" \
    "s_cbranch_vccz .Lhw%=\n\t" \
    "s_sleep 1\n\t" \
    "v_max_u32 v252, v232, v233\n\t" \
    "v_max3_u32 v252, v234, v235, v252\n\t" \
    "v_max3_u32 v252, v236, v237, v252\n\t" \
    "v_max3_u32 v252, v238, v239, v252\n\t" \
    "v_max3_u32 v252, v240, v241, v252\n\t" \
    "v_max3_u32 v252, v242, v243, v252\n\t" \
    "v_max3_u32 v252, v244, v245, v252\n\t" \
    "v_max3_u32 v252, v246, v247, v252\n\t" \
    "v_max3_u32 v252, v248, v249, v252\n\t" \
    "v_max3_u32 v252, v250, v251, v252\n\t" \
    "v_cmp_eq_u32 vcc, -1, v252\n\t" \
    "s_cbranch_vccz .Lhw%=\n\t" \
    "s_sleep 1\n\t" \
    "v_max_u32 v252, v232, v233\n\t" \
    "v_max3_u32 v252, v234, v235, v252\n\t" \
    "v_max3_u32 v252, v236, v237, v252\n\t" \
    "v_max3_u32 v252, v238, v239, v252\n\t" \
    "v_max3_u32 v252, v240, v241, v252\n\t" \
    "v_max3_u32 v252, v242, v243, v252\n\t" \
    "v_max3_u32 v252, v244, v245, v252\n\t" \
    "v_max3_u32 v252, v246, v247, v252\n\t" \
    "v_max3_u32 v252, v248, v249, v252\n\t" \
    "v_max3_u32 v252, v250, v251, v252\n\t" \
    "v_cmp_eq_u32 vcc, -1, v252\n\t" \
    "s_cbranch_vccz .Lhw%=\n\t" \
    "s_sleep 1\n\t" \
    "v_max_u32 v252, v232, v233\n\t" \
    "v_max3_u32 v252, v234, v235, v252\n\t" \
    "v_max3_u32 v252, v236, v237, v252\n\t" \
    "v_max3_u32 v252, v238, v239, v252\n\t" \
    "v_max3_u32 v252, v240, v241, v252\n\t" \
    "v_max3_u32 v252, v242, v243, v252\n\t" \
    "v_max3_u32 v252, v244, v245, v252\n\t" \
    "v_max3_u32 v252, v246, v247, v252\n\t" \
    "v_max3_u32 v252, v248, v249, v252\n\t" \
    "v_max3_u32 v252, v250, v251, v252\n\t" \
    "v_cmp_eq_u32 vcc, -1, v252\n\t" \
    "s_cbranch_vccz .Lhw%=\n\t" \
    "s_sleep 1\n\t" \
    "v_max_u32 v252, v232, v233\n\t" \
    "v_max3_u32 v252, v234, v235, v252\n\t" \
    "v_max3_u32 v252, v236, v237, v252\n\t" \
    "v_max3_u32 v252, v238, v239, v252\n\t" \
    "v_max3_u32 v252, v240, v241, v252\n\t" \
    "v_max3_u32 v252, v242, v243, v252\n\t" \
    "v_max3_u32 v252, v244, v245, v252\n\t" \
    "v_max3_u32 v252, v246, v247, v252\n\t" \
    "v_max3_u32 v252, v248, v249, v252\n\t" \
    "v_max3_u32 v252, v250, v251, v252\n\t" \
    "v_cmp_eq_u32 vcc, -1, v252\n\t" \
    "s_cbranch_vccz .Lhw%=\n\t" \
    "s_sleep 1\n\t" \
    "v_max_u32 v252, v232, v233\n\t" \
    "v_max3_u32 v252, v234, v235, v252\n\t" \
    "v_max3_u32 v252, v236, v237, v252\n\t" \
    "v_max3_u32 v252, v238, v239, v252\n\t" \
    "v_max3_u32 v252, v240, v241, v252\n\t" \
    "v_max3_u32 v252, v242, v243, v252\n\t" \
    "v_max3_u32 v252, v244, v245, v252\n\t" \
    "v_max3_u32 v252, v246, v247, v252\n\t" \
    "v_max3_u32 v252, v248, v249, v252\n\t" \
    "v_max3_u32 v252, v250, v251, v252\n\t" \
    "v_cmp_eq_u32 vcc, -1, v252\n\t" \
    "s_cbranch_vccz .Lhw%=\n\t" \
    "s_sleep 1\n\t" \
    "s_sub_u32 %[n], %[n], 1\n\t" \
    "s_cmp_gt_i32 %[n], 0\n\t" \
    "s_cbranch_scc1 .Lfw%=\n\t" \
    ".Lhw%=:\n\t" \
    "s_branch .Lta%=\n\t" \
    ".Lt0%=:\n\t" \
    "s_waitcnt vmcnt(1)\n\t" \
    ".Lta%=:\n\t" \
    "ds_write_b128 %[lw], v[232:235] offset:0\n\t" \
    "v_max_u32 v252, v232, v233\n\t" \
    "v_max3_u32 v252, v234, v235, v252\n\t" \
    "ds_write_b128 %[lw], v[236:239] offset:1024\n\t" \
    "v_max3_u32 v252, v236, v237, v252\n\t" \
    "v_max3_u32 v252, v238, v239, v252\n\t" \
    "ds_write_b128 %[lw], v[240:243] offset:2048\n\t" \
    "v_max3_u32 v252, v240, v241, v252\n\t" \
    "v_max3_u32 v252, v242, v243, v252\n\t" \
    "ds_write_b128 %[lw], v[244:247] offset:3072\n\t" \
    "v_max3_u32 v252, v244, v245, v252\n\t" \
    "v_max3_u32 v252, v246, v247, v252\n\t" \
    "ds_write_b128 %[lw], v[248:251] offset:4096\n\t" \
    "v_max3_u32 v252, v248, v249, v252\n\t" \
    "v_max3_u32 v252, v250, v251, v252\n\t" \
    "v_cmp_eq_u32 vcc, -1, v252\n\t" \
    "s_cbranch_vccnz .Ltr%=\n\t" \
    ".Ltg%=:\n\t" \
    ASM_ST2 \
    "s_waitcnt lgkmcnt(0)\n\t" \
    "s_setprio 2\n\t" \
    "s_barrier\n\t" ASM_ST \
    "ds_read_b128 v[160:163], %[la] offset:0\n\t" \
    "ds_read_b128 v[164:167], %[la] offset:1024\n\t" \
    "ds_read_b128 v[168:171], %[la] offset:2048\n\t" \
    "ds_read_b128 v[172:175], %[la] offset:3072\n\t" \
    "ds_read_b128 v[176:179], %[la] offset:8192\n\t" \
    "ds_read_b128 v[180:183], %[la] offset:9216\n\t" \
    "ds_read_b128 v[184:187], %[la] offset:10240\n\t" \
    "s_waitcnt lgkmcnt(6)\n\t" \
    "v_mfma_f32_16x16x32_bf16 v[192:195], v[80:83], v[160:163], 0\n\t" \
    "buffer_load_dwordx2 v[196:197], %[ro], %[rs], %[sx] offen sc1\n\t" \
    "s_waitcnt lgkmcnt(5)\n\t" \
    "v_mfma_f32_16x16x32_bf16 v[192:195], v[84:87], v[164:167], v[192:195]\n\t" \
    "buffer_load_dwordx2 v[198:199], %[ro], %[rs], %[sx] offen offset:512 sc1\n\t" \
    "v_mfma_f32_16x16x32_bf16 v[188:191], v[0:3], v[160:163], 0\n\t" \
    "ds_read_b128 v[160:163], %[la] offset:11264\n\t" \
    "v_lshrrev_b32 v204, 1, %[l16]\n\t" \
    "v_mfma_f32_16x16x32_bf16 v[188:191], v[4:7], v[164:167], v[188:191]\n\t" \
    "ds_read_b128 v[164:167], %[la] offset:16384\n\t" \
    "v_and_b32 v204, 0x78, v204\n\t" \
    "s_waitcnt lgkmcnt(6)\n\t" \
    "v_mfma_f32_16x16x32_bf16 v[188:191], v[8:11], v[168:171], v[188:191]\n\t" \
    "buffer_load_dwordx2 v[200:201], v204, %[rs], %[ss] offen sc1\n\t" \
    "s_waitcnt lgkmcnt(5)\n\t" \
    "v_mfma_f32_16x16x32_bf16 v[188:191], v[12:15], v[172:175], v[188:191]\n\t" \
    "v_mov_b64 v[216:217], -1\n\t" \
    "v_mfma_f32_16x16x32_bf16 v[192:195], v[88:91], v[168:171], v[192:195]\n\t" \
    "ds_read_b128 v[168:171], %[la] offset:17408\n\t" \
    "v_mov_b64 v[218:219], -1\n\t" \
    "v_mfma_f32_16x16x32_bf16 v[192:195], v[92:95], v[172:175], v[192:195]\n\t" \
    "ds_read_b128 v[172:175], %[la] offset:18432\n\t" \
    "buffer_store_dwordx4 v[216:219], %[l16], %[rs], %[sa] offen sc1\n\t" \
    "s_waitcnt lgkmcnt(6)\n\t" \
    "v_mfma_f32_16x16x32_bf16 v[192:195], v[96:99], v[176:179], v[192:195]\n\t" \
    "s_waitcnt lgkmcnt(5)\n\t" \
    "v_mfma_f32_16x16x32_bf16 v[192:195], v[100:103], v[180:183], v[192:195]\n\t" \
    "v_mfma_f32_16x16x32_bf16 v[188:191], v[16:19], v[176:179], v[188:191]\n\t" \
    "ds_read_b128 v[176:179], %[la] offset:19456\n\t" \
    "v_mfma_f32_16x16x32_bf16 v[188:191], v[20:23], v[180:183], v[188:191]\n\t" \
    "ds_read_b128 v[180:183], %[la] offset:24576\n\t" \
    "s_waitcnt lgkmcnt(6)\n\t" \
    "v_mfma_f32_16x16x32_bf16 v[188:191], v[24:27], v[184:187], v[188:191]\n\t" \
    "s_waitcnt lgkmcnt(5)\n\t" \
    "v_mfma_f32_16x16x32_bf16 v[188:191], v[28:31], v[160:163], v[188:191]\n\t" \
    "v_mfma_f32_16x16x32_bf16 v[192:195], v[104:107], v[184:187], v[192:195]\n\t" \
    "ds_read_b128 v[184:187], %[la] offset:25600\n\t" \
    "v_mfma_f32_16x16x32_bf16 v[192:195], v[108:111], v[160:163], v[192:195]\n\t" \
    "ds_read_b128 v[160:163], %[la] offset:26624\n\t" \
    "s_waitcnt lgkmcnt(6)\n\t" \
    "v_mfma_f32_16x16x32_bf16 v[192:195], v[112:115], v[164:167], v[192:195]\n\t" \
    "s_waitcnt lgkmcnt(5)\n\t" \
    "v_mfma_f32_16x16x32_bf16 v[192:195], v[116:119], v[168:171], v[192:195]\n\t" \
    "v_mfma_f32_16x16x32_bf16 v[188:191], v[32:35], v[164:167], v[188:191]\n\t" \
    "ds_read_b128 v[164:167], %[la] offset:27648\n\t" \
    "v_mfma_f32_16x16x32_bf16 v[188:191], v[36:39], v[168:171], v[188:191]\n\t" \
    "ds_read_b128 v[168:171], %[la] offset:4096\n\t" \
    "s_waitcnt lgkmcnt(6)\n\t" \
    "v_mfma_f32_16x16x32_bf16 v[188:191], v[40:43], v[172:175], v[188:191]\n\t" \
    "s_waitcnt lgkmcnt(5)\n\t" \
    "v_mfma_f32_16x16x32_bf16 v[188:191], v[44:47], v[176:179], v[188:191]\n\t" \
    "v_mfma_f32_16x16x32_bf16 v[192:195], v[120:123], v[172:175], v[192:195]\n\t" \
    "ds_read_b128 v[172:175], %[la] offset:12288\n\t" \
    "v_mfma_f32_16x16x32_bf16 v[192:195], v[124:127], v[176:179], v[192:195]\n\t" \
    "ds_read_b128 v[176:179], %[la] offset:20480\n\t" \
    "s_waitcnt lgkmcnt(6)\n\t" \
    "v_mfma_f32_16x16x32_bf16 v[192:195], v[128:131], v[180:183], v[192:195]\n\t" \
    "s_waitcnt lgkmcnt(5)\n\t" \
    "v_mfma_f32_16x16x32_bf16 v[192:195], v[132:135], v[184:187], v[192:195]\n\t" \
    "v_mfma_f32_16x16x32_bf16 v[188:191], v[48:51], v[180:183], v[188:191]\n\t" \
    "ds_read_b128 v[180:183], %[la] offset:28672\n\t" \
    "v_mfma_f32_16x16x32_bf16 v[188:191], v[52:55], v[184:187], v[188:191]\n\t" \
    "s_waitcnt lgkmcnt(5)\n\t" \
    "v_mfma_f32_16x16x32_bf16 v[188:191], v[56:59], v[160:163], v[188:191]\n\t" \
    "s_waitcnt lgkmcnt(4)\n\t" \
    "v_mfma_f32_16x16x32_bf16 v[188:191], v[60:63], v[164:167], v[188:191]\n\t" \
    "v_mfma_f32_16x16x32_bf16 v[192:195], v[136:139], v[160:163], v[192:195]\n\t" \
    "v_mfma_f32_16x16x32_bf16 v[192:195], v[140:143], v[164:167], v[192:195]\n\t" \
    "s_waitcnt lgkmcnt(3)\n\t" \
    "v_mfma_f32_16x16x32_bf16 v[192:195], v[144:147], v[168:171], v[192:195]\n\t" \
    "s_waitcnt lgkmcnt(2)\n\t" \
    "v_mfma_f32_16x16x32_bf16 v[192:195], v[148:151], v[172:175], v[192:195]\n\t" \
    "v_mfma_f32_16x16x32_bf16 v[188:191], v[64:67], v[168:171], v[188:191]\n\t" \
    "v_mfma_f32_16x16x32_bf16 v[188:191], v[68:71], v[172:175], v[188:191]\n\t" \
    "s_waitcnt lgkmcnt(1)\n\t" \
    "v_mfma_f32_16x16x32_bf16 v[188:191], v[72:75], v[176:179], v[188:191]\n\t" \
    "s_waitcnt lgkmcnt(0)\n\t" \
    "v_mfma_f32_16x16x32_bf16 v[188:191], v[76:79], v[180:183], v[188:191]\n\t" \
    "v_mfma_f32_16x16x32_bf16 v[192:195], v[152:155], v[176:179], v[192:195]\n\t" \
    "v_mfma_f32_16x16x32_bf16 v[192:195], v[156:159], v[180:183], v[192:195]\n\t" \
    "s_setprio 2\n\t" \
    "buffer_load_dwordx4 v[232:235], %[l16], %[rs], %[n0] offen offset:0 sc1\n\t" \
    "buffer_load_dwordx4 v[236:239], %[l16], %[rs], %[n0] offen offset:1024 sc1\n\t" \
    "buffer_load_dwordx4 v[240:243], %[l16], %[rs], %[n0] offen offset:2048 sc1\n\t" \
    "buffer_load_dwordx4 v[244:247], %[l16], %[rs], %[n0] offen offset:3072 sc1\n\t" \
    "buffer_load_dwordx4 v[248:251], %[l16], %[rs], %[n1] offen offset:0 sc1\n\t" \
    ASM_ST3 \
    "s_waitcnt vmcnt(6)\n\t" \
    "v_lshlrev_b32 v160, 16, v196\n\t" \
    "v_and_b32 v161, 0xffff0000, v196\n\t" \
    "v_lshlrev_b32 v162, 16, v197\n\t" \
    "v_and_b32 v163, 0xffff0000, v197\n\t" \
    "v_lshlrev_b32 v164, 16, v198\n\t" \
    "v_and_b32 v165, 0xffff0000, v198\n\t" \
    "v_lshlrev_b32 v166, 16, v199\n\t" \
    "v_and_b32 v167, 0xffff0000, v199\n\t" \
    "v_sub_f32 v160, v160, v200\n\t" \
    "v_sub_f32 v161, v161, v200\n\t" \
    "v_sub_f32 v162, v162, v200\n\t" \
    "v_sub_f32 v163, v163, v200\n\t" \
    "v_sub_f32 v164, v164, v200\n\t" \
    "v_sub_f32 v165, v165, v200\n\t" \
    "v_sub_f32 v166, v166, v200\n\t" \
    "v_sub_f32 v167, v167, v200\n\t" \
    "v_mul_f32 v160, v160, v201\n\t" \
    "v_mul_f32 v161, v161, v201\n\t" \
    "v_mul_f32 v162, v162, v201\n\t" \
    "v_mul_f32 v163, v163, v201\n\t" \
    "v_mul_f32 v164, v164, v201\n\t" \
    "v_mul_f32 v165, v165, v201\n\t" \
    "v_mul_f32 v166, v166, v201\n\t" \
    "v_mul_f32 v167, v167, v201\n\t" \
    "v_cvt_pk_bf16_f32 v160, v160, v161\n\t" \
    "v_cvt_pk_bf16_f32 v162, v162, v163\n\t" \
    "v_cvt_pk_bf16_f32 v164, v164, v165\n\t" \
    "v_cvt_pk_bf16_f32 v166, v166, v167\n\t" \
    "v_and_b32 v161, 0xffff0000, v160\n\t" \
    "v_lshlrev_b32 v160, 16, v160\n\t" \
    "v_and_b32 v163, 0xffff0000, v162\n\t" \
    "v_lshlrev_b32 v162, 16, v162\n\t" \
    "v_and_b32 v165, 0xffff0000, v164\n\t" \
    "v_lshlrev_b32 v164, 16, v164\n\t" \
    "v_and_b32 v167, 0xffff0000, v166\n\t" \
    "v_lshlrev_b32 v166, 16, v166\n\t" \
    "v_add_f32 v188, v188, v160\n\t" \
    "v_add_f32 v189, v189, v161\n\t" \
    "v_add_f32 v190, v190, v162\n\t" \
    "v_add_f32 v191, v191, v163\n\t" \
    "v_add_f32 v192, v192, v164\n\t" \
    "v_add_f32 v193, v193, v165\n\t" \
    "v_add_f32 v194, v194, v166\n\t" \
    "v_add_f32 v195, v195, v167\n\t" \
    "v_cvt_pk_bf16_f32 v188, v188, v189\n\t" \
    "v_cvt_pk_bf16_f32 v189, v190, v191\n\t" \
    "v_cvt_pk_bf16_f32 v190, v192, v193\n\t" \
    "v_cvt_pk_bf16_f32 v191, v194, v195\n\t" \
    "v_min_u32 v188, -2, v188\n\t" \
    "v_min_u32 v189, -2, v189\n\t" \
    "v_min_u32 v190, -2, v190\n\t" \
    "v_min_u32 v191, -2, v191\n\t" \
    "s_nop 1\n\t" \
    "v_permlane16_swap_b32 v188, v190\n\t" \
    "v_permlane16_swap_b32 v189, v191\n\t" \
    "s_waitcnt vmcnt(6)\n\t" \
    "buffer_store_dwordx4 v[188:191], %[oa], %[rs], %[so] offen sc1\n\t" \
    "s_nop 1\n\t" \
    "s_setprio 0\n\t"
#define ASM_WT2_HI \
    "s_setprio 1\n\t" \
    "s_barrier\n\t" ASM_ST \
    "ds_read_b128 v[160:163], %[la] offset:0\n\t" \
    "ds_read_b128 v[164:167], %[la] offset:1024\n\t" \
    "ds_read_b128 v[168:171], %[la] offset:2048\n\t" \
    "ds_read_b128 v[172:175], %[la] offset:3072\n\t" \
    "ds_read_b128 v[176:179], %[la] offset:8192\n\t" \
    "ds_read_b128 v[180:183], %[la] offset:9216\n\t" \
    "ds_read_b128 v[184:187], %[la] offset:10240\n\t" \
    "s_waitcnt lgkmcnt(6)\n\t" \
    "v_mfma_f32_16x16x32_bf16 v[192:195], v[80:83], v[160:163], 0\n\t" \
    "buffer_load_dwordx2 v[196:197], %[ro], %[rs], %[sx] offen sc1\n\t" \
    "s_waitcnt lgkmcnt(5)\n\t" \
    "v_mfma_f32_16x16x32_bf16 v[192:195], v[84:87], v[164:167], v[192:195]\n\t" \
    "buffer_load_dwordx2 v[198:199], %[ro], %[rs], %[sx] offen offset:512 sc1\n\t" \
    "v_mfma_f32_16x16x32_bf16 v[188:191], v[0:3], v[160:163], 0\n\t" \
    "ds_read_b128 v[160:163], %[la] offset:11264\n\t" \
    "v_lshrrev_b32 v204, 1, %[l16]\n\t" \
    "v_mfma_f32_16x16x32_bf16 v[188:191], v[4:7], v[164:167], v[188:191]\n\t" \
    "ds_read_b128 v[164:167], %[la] offset:16384\n\t" \
    "v_and_b32 v204, 0x78, v204\n\t" \
    "s_waitcnt lgkmcnt(6)\n\t" \
    "v_mfma_f32_16x16x32_bf16 v[188:191], v[8:11], v[168:171], v[188:191]\n\t" \
    "buffer_load_dwordx2 v[200:201], v204, %[rs], %[ss] offen sc1\n\t" \
    "s_waitcnt lgkmcnt(5)\n\t" \
    "v_mfma_f32_16x16x32_bf16 v[188:191], v[12:15], v[172:175], v[188:191]\n\t" \
    "v_mov_b64 v[216:217], -1\n\t" \
    "v_mfma_f32_16x16x32_bf16 v[192:195], v[88:91], v[168:171], v[192:195]\n\t" \
    "ds_read_b128 v[168:171], %[la] offset:17408\n\t" \
    "v_mov_b64 v[218:219], -1\n\t" \
    "v_mfma_f32_16x16x32_bf16 v[192:195], v[92:95], v[172:175], v[192:195]\n\t" \
    "ds_read_b128 v[172:175], %[la] offset:18432\n\t" \
    "buffer_store_dwordx4 v[216:219], %[l16], %[rs], %[sa] offen sc1\n\t" \
    "s_waitcnt lgkmcnt(6)\n\t" \
    "v_mfma_f32_16x16x32_bf16 v[192:195], v[96:99], v[176:179], v[192:195]\n\t" \
    "s_waitcnt lgkmcnt(5)\n\t" \
    "v_mfma_f32_16x16x32_bf16 v[192:195], v[100:103], v[180:183], v[192:195]\n\t" \
    "v_mfma_f32_16x16x32_bf16 v[188:191], v[16:19], v[176:179], v[188:191]\n\t" \
    "ds_read_b128 v[176:179], %[la] offset:19456\n\t" \
    "v_mfma_f32_16x16x32_bf16 v[188:191], v[20:23], v[180:183], v[188:191]\n\t" \
    "ds_read_b128 v[180:183], %[la] offset:24576\n\t" \
    "s_waitcnt lgkmcnt(6)\n\t" \
    "v_mfma_f32_16x16x32_bf16 v[188:191], v[24:27], v[184:187], v[188:191]\n\t" \
    "s_waitcnt lgkmcnt(5)\n\t" \
    "v_mfma_f32_16x16x32_bf16 v[188:191], v[28:31], v[160:163], v[188:191]\n\t" \
    "v_mfma_f32_16x16x32_bf16 v[192:195], v[104:107], v[184:187], v[192:195]\n\t" \
    "ds_read_b128 v[184:187], %[la] offset:25600\n\t" \
    "v_mfma_f32_16x16x32_bf16 v[192:195], v[108:111], v[160:163], v[192:195]\n\t" \
    "ds_read_b128 v[160:163], %[la] offset:26624\n\t" \
    "s_waitcnt lgkmcnt(6)\n\t" \
    "v_mfma_f32_16x16x32_bf16 v[192:195], v[112:115], v[164:167], v[192:195]\n\t" \
    "s_waitcnt lgkmcnt(5)\n\t" \
    "v_mfma_f32_16x16x32_bf16 v[192:195], v[116:119], v[168:171], v[192:195]\n\t" \
    "v_mfma_f32_16x16x32_bf16 v[188:191], v[32:35], v[164:167], v[188:191]\n\t" \
    "ds_read_b128 v[164:167], %[la] offset:27648\n\t" \
    "v_mfma_f32_16x16x32_bf16 v[188:191], v[36:39], v[168:171], v[188:191]\n\t" \
    "ds_read_b128 v[168:171], %[la] offset:4096\n\t" \
    "s_waitcnt lgkmcnt(6)\n\t" \
    "v_mfma_f32_16x16x32_bf16 v[188:191], v[40:43], v[172:175], v[188:191]\n\t" \
    "s_waitcnt lgkmcnt(5)\n\t" \
    "v_mfma_f32_16x16x32_bf16 v[188:191], v[44:47], v[176:179], v[188:191]\n\t" \
    "v_mfma_f32_16x16x32_bf16 v[192:195], v[120:123], v[172:175], v[192:195]\n\t" \
    "ds_read_b128 v[172:175], %[la] offset:12288\n\t" \
    "v_mfma_f32_16x16x32_bf16 v[192:195], v[124:127], v[176:179], v[192:195]\n\t" \
    "ds_read_b128 v[176:179], %[la] offset:20480\n\t" \
    "s_waitcnt lgkmcnt(6)\n\t" \
    "v_mfma_f32_16x16x32_bf16 v[192:195], v[128:131], v[180:183], v[192:195]\n\t" \
    "s_waitcnt lgkmcnt(5)\n\t" \
    "v_mfma_f32_16x16x32_bf16 v[192:195], v[132:135], v[184:187], v[192:195]\n\t" \
    "v_mfma_f32_16x16x32_bf16 v[188:191], v[48:51], v[180:183], v[188:191]\n\t" \
    "ds_read_b128 v[180:183], %[la] offset:28672\n\t" \
    "v_mfma_f32_16x16x32_bf16 v[188:191], v[52:55], v[184:187], v[188:191]\n\t" \
    "s_waitcnt lgkmcnt(5)\n\t" \
    "v_mfma_f32_16x16x32_bf16 v[188:191], v[56:59], v[160:163], v[188:191]\n\t" \
    "s_waitcnt lgkmcnt(4)\n\t" \
    "v_mfma_f32_16x16x32_bf16 v[188:191], v[60:63], v[164:167], v[188:191]\n\t" \
    "v_mfma_f32_16x16x32_bf16 v[192:195], v[136:139], v[160:163], v[192:195]\n\t" \
    "v_mfma_f32_16x16x32_bf16 v[192:195], v[140:143], v[164:167], v[192:195]\n\t" \
    "s_waitcnt lgkmcnt(3)\n\t" \
    "v_mfma_f32_16x16x32_bf16 v[192:195], v[144:147], v[168:171], v[192:195]\n\t" \
    "s_waitcnt lgkmcnt(2)\n\t" \
    "v_mfma_f32_16x16x32_bf16 v[192:195], v[148:151], v[172:175], v[192:195]\n\t" \
    "v_mfma_f32_16x16x32_bf16 v[188:191], v[64:67], v[168:171], v[188:191]\n\t" \
    "v_mfma_f32_16x16x32_bf16 v[188:191], v[68:71], v[172:175], v[188:191]\n\t" \
    "s_waitcnt lgkmcnt(1)\n\t" \
    "v_mfma_f32_16x16x32_bf16 v[188:191], v[72:75], v[176:179], v[188:191]\n\t" \
    "s_waitcnt lgkmcnt(0)\n\t" \
    "v_mfma_f32_16x16x32_bf16 v[188:191], v[76:79], v[180:183], v[188:191]\n\t" \
    "v_mfma_f32_16x16x32_bf16 v[192:195], v[152:155], v[176:179], v[192:195]\n\t" \
    "v_mfma_f32_16x16x32_bf16 v[192:195], v[156:159], v[180:183], v[192:195]\n\t" \
    ASM_ST3 \
    "s_waitcnt vmcnt(1)\n\t" \
    "v_lshlrev_b32 v160, 16, v196\n\t" \
    "v_and_b32 v161, 0xffff0000, v196\n\t" \
    "v_lshlrev_b32 v162, 16, v197\n\t" \
    "v_and_b32 v163, 0xffff0000, v197\n\t" \
    "v_lshlrev_b32 v164, 16, v198\n\t" \
    "v_and_b32 v165, 0xffff0000, v198\n\t" \
    "v_lshlrev_b32 v166, 16, v199\n\t" \
    "v_and_b32 v167, 0xffff0000, v199\n\t" \
    "v_sub_f32 v160, v160, v200\n\t" \
    "v_sub_f32 v161, v161, v200\n\t" \
    "v_sub_f32 v162, v162, v200\n\t" \
    "v_sub_f32 v163, v163, v200\n\t" \
    "v_sub_f32 v164, v164, v200\n\t" \
    "v_sub_f32 v165, v165, v200\n\t" \
    "v_sub_f32 v166, v166, v200\n\t" \
    "v_sub_f32 v167, v167, v200\n\t" \
    "v_mul_f32 v160, v160, v201\n\t" \
    "v_mul_f32 v161, v161, v201\n\t" \
    "v_mul_f32 v162, v162, v201\n\t" \
    "v_mul_f32 v163, v163, v201\n\t" \
    "v_mul_f32 v164, v164, v201\n\t" \
    "v_mul_f32 v165, v165, v201\n\t" \
    "v_mul_f32 v166, v166, v201\n\t" \
    "v_mul_f32 v167, v167, v201\n\t" \
    "v_cvt_pk_bf16_f32 v160, v160, v161\n\t" \
    "v_cvt_pk_bf16_f32 v162, v162, v163\n\t" \
    "v_cvt_pk_bf16_f32 v164, v164, v165\n\t" \
    "v_cvt_pk_bf16_f32 v166, v166, v167\n\t" \
    "v_and_b32 v161, 0xffff0000, v160\n\t" \
    "v_lshlrev_b32 v160, 16, v160\n\t" \
    "v_and_b32 v163, 0xffff0000, v162\n\t" \
    "v_lshlrev_b32 v162, 16, v162\n\t" \
    "v_and_b32 v165, 0xffff0000, v164\n\t" \
    "v_lshlrev_b32 v164, 16, v164\n\t" \
    "v_and_b32 v167, 0xffff0000, v166\n\t" \
    "v_lshlrev_b32 v166, 16, v166\n\t" \
    "v_add_f32 v188, v188, v160\n\t" \
    "v_add_f32 v189, v189, v161\n\t" \
    "v_add_f32 v190, v190, v162\n\t" \
    "v_add_f32 v191, v191, v163\n\t" \
    "v_add_f32 v192, v192, v164\n\t" \
    "v_add_f32 v193, v193, v165\n\t" \
    "v_add_f32 v194, v194, v166\n\t" \
    "v_add_f32 v195, v195, v167\n\t" \
    "v_cvt_pk_bf16_f32 v188, v188, v189\n\t" \
    "v_cvt_pk_bf16_f32 v189, v190, v191\n\t" \
    "v_cvt_pk_bf16_f32 v190, v192, v193\n\t" \
    "v_cvt_pk_bf16_f32 v191, v194, v195\n\t" \
    "v_min_u32 v188, -2, v188\n\t" \
    "v_min_u32 v189, -2, v189\n\t" \
    "v_min_u32 v190, -2, v190\n\t" \
    "v_min_u32 v191, -2, v191\n\t" \
    "s_nop 1\n\t" \
    "v_permlane16_swap_b32 v188, v190\n\t" \
    "v_permlane16_swap_b32 v189, v191\n\t" \
    "s_waitcnt vmcnt(1)\n\t" \
    "buffer_store_dwordx4 v[188:191], %[oa], %[rs], %[so] offen sc1\n\t" \
    "s_nop 1\n\t"
#define ASM_WT3_LO \
    "s_branch .Lt0%=\n\t" \
    ".Ltr%=:\n\t" \
    "s_cmp_gt_i32 %[n], 0\n\t" \
    "s_cbranch_scc0 .Ltg%=\n\t" \
    ".Lfw%=:\n\t" \
    "s_mov_b32 %[a], 0\n\t" \
    "v_max_u32 v252, v232, v233\n\t" \
    "v_max3_u32 v252, v234, v235, v252\n\t" \
    "v_cmp_eq_u32 vcc, -1, v252\n\t" \
    "s_cbranch_vccz .Lgw0_%=\n\t" \
    "buffer_load_dwordx4 v[232:235], %[l16], %[rs], %[i0] offen offset:0 sc1\n\t" \
    "s_mov_b32 %[a], 1\n\t" \
    ".Lgw0_%=:\n\t" \
    "v_max_u32 v252, v236, v237\n\t" \
    "v_max3_u32 v252, v238, v239, v252\n\t" \
    "v_cmp_eq_u32 vcc, -1, v252\n\t" \
    "s_cbranch_vccz .Lgw1_%=\n\t" \
    "buffer_load_dwordx4 v[236:239], %[l16], %[rs], %[i0] offen offset:1024 sc1\n\t" \
    "s_mov_b32 %[a], 1\n\t" \
    ".Lgw1_%=:\n\t" \
    "v_max_u32 v252, v240, v241\n\t" \
    "v_max3_u32 v252, v242, v243, v252\n\t" \
    "v_cmp_eq_u32 vcc, -1, v252\n\t" \
    "s_cbranch_vccz .Lgw2_%=\n\t" \
    "buffer_load_dwordx4 v[240:243], %[l16], %[rs], %[i0] offen offset:2048 sc1\n\t" \
    "s_mov_b32 %[a], 1\n\t" \
    ".Lgw2_%=:\n\t" \
    "v_max_u32 v252, v244, v245\n\t" \
    "v_max3_u32 v252, v246, v247, v252\n\t" \
    "v_cmp_eq_u32 vcc, -1, v252\n\t" \
    "s_cbranch_vccz .Lgw3_%=\n\t" \
    "buffer_load_dwordx4 v[244:247], %[l16], %[rs], %[i0] offen offset:3072 sc1\n\t" \
    "s_mov_b32 %[a], 1\n\t" \
    ".Lgw3_%=:\n\t" \
    "v_max_u32 v252, v248, v249\n\t" \
    "v_max3_u32 v252, v250, v251, v252\n\t" \
    "v_cmp_eq_u32 vcc, -1, v252\n\t" \
    "s_cbranch_vccz .Lgw4_%=\n\t" \
    "buffer_load_dwordx4 v[248:251], %[l16], %[rs], %[i1] offen offset:0 sc1\n\t" \
    "s_mov_b32 %[a], 1\n\t" \
    ".Lgw4_%=:\n\t" \
    "s_cmp_eq_u32 %[a], 0\n\t" \
    "s_cbranch_scc1 .Lhw%=\n\t" \
    "s_waitcnt vmcnt(20)\n\t" \
    ".Liw%=:\n\t" \
    "v_max_u32 v252, v232, v233\n\t" \
    "v_max3_u32 v252, v234, v235, v252\n\t" \
    "v_max3_u32 v252, v236, v237, v252\n\t" \
    "v_max3_u32 v252, v238, v239, v252\n\t" \
    "v_max3_u32 v252, v240, v241, v252\n\t" \
    "v_max3_u32 v252, v242, v243, v252\n\t" \
    "v_max3_u32 v252, v244, v245, v252\n\t" \
    "v_max3_u32 v252, v246, v247, v252\n\t" \
    "v_max3_u32 v252, v248, v249, v252\n\t" \
    "v_max3_u32 v252, v250, v251, v252\n\t" \
    "v_cmp_eq_u32 vcc, -1, v252\n\t" \
    "s_cbranch_vccz .Lhw%=\n\t" \
    "s_sleep 1\n\t" \
    "v_max_u32 v252, v232, v233\n\t" \
    "v_max3_u32 v252, v234, v235, v252\n\t" \
    "v_max3_u32 v252, v236, v237, v252\n\t" \
    "v_max3_u32 v252, v238, v239, v252\n\t" \
    "v_max3_u32 v252, v240, v241, v252\n\t" \
    "v_max3_u32 v252, v242, v243, v252\n\t" \
    "v_max3_u32 v252, v244, v245, v252\n\t" \
    "v_max3_u32 v252, v246, v247, v252\n\t" \
    "v_max3_u32 v252, v248, v249, v252\n\t" \
    "v_max3_u32 v252, v250, v251, v252\n\t" \
    "v_cmp_eq_u32 vcc, -1, v252\n\t" \
    "s_cbranch_vccz .Lhw%=\n\t" \
    "s_sleep 1\n\t" \
    "v_max_u32 v252, v232, v233\n\t" \
    "v_max3_u32 v252, v234, v235, v252\n\t" \
    "v_max3_u32 v252, v236, v237, v252\n\t" \
    "v_max3_u32 v252, v238, v239, v252\n\t" \
    "v_max3_u32 v252, v240, v241, v252\n\t" \
    "v_max3_u32 v252, v242, v243, v252\n\t" \
    "v_max3_u32 v252, v244, v245, v252\n\t" \
    "v_max3_u32 v252, v246, v247, v252\n\t" \
    "v_max3_u32 v252, v248, v249, v252\n\t" \
    "v_max3_u32 v252, v250, v251, v252\n\t" \
    "v_cmp_eq_u32 vcc, -1, v252\n\t" \
    "s_cbranch_vccz .Lhw%=\n\t" \
    "s_sleep 1\n\t" \
    "v_max_u32 v252, v232, v233\n\t" \
    "v_max3_u32 v252, v234, v235, v252\n\t" \
    "v_max3_u32 v252, v236, v237, v252\n\t" \
    "v_max3_u32 v252, v238, v239, v252\n\t" \
    "v_max3_u32 v252, v240, v241, v252\n\t" \
    "v_max3_u32 v252, v242, v243, v252\n\t" \
    "v_max3_u32 v252, v244, v245, v252\n\t" \
    "v_max3_u32 v252, v246, v247, v252\n\t" \
    "v_max3_u32 v252, v248, v249, v252\n\t" \
    "v_max3_u32 v252, v250, v251, v252\n\t" \
    "v_cmp_eq_u32 vcc, -1, v252\n\t" \
    "s_cbranch_vccz .Lhw%=\n\t" \
    "s_sleep 1\n\t" \
    "v_max_u32 v252, v232, v233\n\t" \
    "v_max3_u32 v252, v234, v235, v252\n\t" \
    "v_max3_u32 v252, v236, v237, v252\n\t" \
    "v_max3_u32 v252, v238, v239, v252\n\t" \
    "v_max3_u32 v252, v240, v241, v252\n\t" \
    "v_max3_u32 v252, v242, v243, v252\n\t" \
    "v_max3_u32 v252, v244, v245, v252\n\t" \
    "v_max3_u32 v252, v246, v247, v252\n\t" \
    "v_max3_u32 v252, v248, v249, v252\n\t" \
    "v_max3_u32 v252, v250, v251, v252\n\t" \
    "v_cmp_eq_u32 vcc, -1, v252\n\t" \
    "s_cbranch_vccz .Lhw%=\n\t" \
    "s_sleep 1\n\t" \
    "v_max_u32 v252, v232, v233\n\t" \
    "v_max3_u32 v252, v234, v235, v252\n\t" \
    "v_max3_u32 v252, v236, v237, v252\n\t" \
    "v_max3_u32 v252, v238, v239, v252\n\t" \
    "v_max3_u32 v252, v240, v241, v252\n\t" \
    "v_max3_u32 v252, v242, v243, v252\n\t" \
    "v_max3_u32 v252, v244, v245, v252\n\t" \
    "v_max3_u32 v252, v246, v247, v252\n\t" \
    "v_max3_u32 v252, v248, v249, v252\n\t" \
    "v_max3_u32 v252, v250, v251, v252\n\t" \
    "v_cmp_eq_u32 vcc, -1, v252\n\t" \
    "s_cbranch_vccz .Lhw%=\n\t" \
    "s_sleep 1\n\t" \
    "s_sub_u32 %[n], %[n], 1\n\t" \
    "s_cmp_gt_i32 %[n], 0\n\t" \
    "s_cbranch_scc1 .Lfw%=\n\t" \
    ".Lhw%=:\n\t" \
    "s_branch .Lta%=\n\t" \
    ".Lt0%=:\n\t" \
    "s_waitcnt vmcnt(1)\n\t" \
    ".Lta%=:\n\t" \
    "ds_write_b128 %[lw], v[232:235] offset:0\n\t" \
    "v_max_u32 v252, v232, v233\n\t" \
    "v_max3_u32 v252, v234, v235, v252\n\t" \
    "v_mfma_f32_16x16x32_bf16 v[188:191], v[232:235], v[232:235], 0\n\t" \
    "ds_write_b128 %[lw], v[236:239] offset:1024\n\t" \
    "v_max3_u32 v252, v236, v237, v252\n\t" \
    "v_max3_u32 v252, v238, v239, v252\n\t" \
    "v_mfma_f32_16x16x32_bf16 v[188:191], v[236:239], v[236:239], v[188:191]\n\t" \
    "ds_write_b128 %[lw], v[240:243] offset:2048\n\t" \
    "v_max3_u32 v252, v240, v241, v252\n\t" \
    "v_max3_u32 v252, v242, v243, v252\n\t" \
    "v_mfma_f32_16x16x32_bf16 v[188:191], v[240:243], v[240:243], v[188:191]\n\t" \
    "ds_write_b128 %[lw], v[244:247] offset:3072\n\t" \
    "v_max3_u32 v252, v244, v245, v252\n\t" \
    "v_max3_u32 v252, v246, v247, v252\n\t" \
    "v_mfma_f32_16x16x32_bf16 v[188:191], v[244:247], v[244:247], v[188:191]\n\t" \
    "ds_write_b128 %[lw], v[248:251] offset:4096\n\t" \
    "v_max3_u32 v252, v248, v249, v252\n\t" \
    "v_max3_u32 v252, v250, v251, v252\n\t" \
    "v_mfma_f32_16x16x32_bf16 v[188:191], v[248:251], v[248:251], v[188:191]\n\t" \
    "v_mfma_f32_16x16x32_bf16 v[192:195], v[216:219], v[232:235], 0\n\t" \
    "v_mfma_f32_16x16x32_bf16 v[192:195], v[216:219], v[236:239], v[192:195]\n\t" \
    "v_mfma_f32_16x16x32_bf16 v[192:195], v[216:219], v[240:243], v[192:195]\n\t" \
    "v_mfma_f32_16x16x32_bf16 v[192:195], v[216:219], v[244:247], v[192:195]\n\t" \
    "v_mfma_f32_16x16x32_bf16 v[192:195], v[216:219], v[248:251], v[192:195]\n\t" \
    "v_cmp_eq_u32 vcc, -1, v252\n\t" \
    "s_cbranch_vccnz .Ltr%=\n\t" \
    ".Ltg%=:\n\t" \
    ASM_ST2 \
    "s_nop 5\n\t" \
    "ds_write_b128 %[lw], v[188:191] offset:5120\n\t" \
    "ds_write_b128 %[lw], v[192:195] offset:6144\n\t" \
    "s_waitcnt lgkmcnt(0)\n\t" \
    "s_setprio 2\n\t" \
    "s_barrier\n\t" ASM_ST \
    "ds_read_b128 v[160:163], %[la] offset:0\n\t" \
    "ds_read_b128 v[164:167], %[la] offset:1024\n\t" \
    "ds_read_b128 v[168:171], %[la] offset:2048\n\t" \
    "ds_read2st64_b32 v[204:205], %[sr] offset0:20 offset1:24\n\t" \
    "ds_read_b128 v[172:175], %[la] offset:3072\n\t" \
    "ds_read_b128 v[176:179], %[la] offset:8192\n\t" \
    "ds_read_b128 v[180:183], %[la] offset:9216\n\t" \
    "ds_read_b128 v[184:187], %[la] offset:10240\n\t" \
    "s_waitcnt lgkmcnt(7)\n\t" \
    "v_mfma_f32_16x16x32_bf16 v[192:195], v[80:83], v[160:163], 0\n\t" \
    "s_waitcnt lgkmcnt(6)\n\t" \
    "v_mfma_f32_16x16x32_bf16 v[192:195], v[84:87], v[164:167], v[192:195]\n\t" \
    "v_mfma_f32_16x16x32_bf16 v[188:191], v[0:3], v[160:163], 0\n\t" \
    "ds_read_b128 v[160:163], %[la] offset:11264\n\t" \
    "v_mfma_f32_16x16x32_bf16 v[188:191], v[4:7], v[164:167], v[188:191]\n\t" \
    "ds_read_b128 v[164:167], %[la] offset:16384\n\t" \
    "s_waitcnt lgkmcnt(7)\n\t" \
    "v_mfma_f32_16x16x32_bf16 v[188:191], v[8:11], v[168:171], v[188:191]\n\t" \
    "s_waitcnt lgkmcnt(6)\n\t" \
    "s_waitcnt lgkmcnt(5)\n\t" \
    "v_mfma_f32_16x16x32_bf16 v[188:191], v[12:15], v[172:175], v[188:191]\n\t" \
    "v_permlane16_swap_b32 v204, v205\n\t" \
    "v_mfma_f32_16x16x32_bf16 v[192:195], v[88:91], v[168:171], v[192:195]\n\t" \
    "ds_read_b128 v[168:171], %[la] offset:17408\n\t" \
    "v_mfma_f32_16x16x32_bf16 v[192:195], v[92:95], v[172:175], v[192:195]\n\t" \
    "ds_read_b128 v[172:175], %[la] offset:18432\n\t" \
    "v_add_f32 v206, v204, v205\n\t" \
    "s_waitcnt lgkmcnt(6)\n\t" \
    "v_mfma_f32_16x16x32_bf16 v[192:195], v[96:99], v[176:179], v[192:195]\n\t" \
    "v_add_f32 v207, v204, v205\n\t" \
    "s_waitcnt lgkmcnt(5)\n\t" \
    "v_mfma_f32_16x16x32_bf16 v[192:195], v[100:103], v[180:183], v[192:195]\n\t" \
    "v_mfma_f32_16x16x32_bf16 v[188:191], v[16:19], v[176:179], v[188:191]\n\t" \
    "ds_read_b128 v[176:179], %[la] offset:19456\n\t" \
    "v_permlane32_swap_b32 v206, v207\n\t" \
    "v_mfma_f32_16x16x32_bf16 v[188:191], v[20:23], v[180:183], v[188:191]\n\t" \
    "ds_read_b128 v[180:183], %[la] offset:24576\n\t" \
    "s_waitcnt lgkmcnt(6)\n\t" \
    "v_mfma_f32_16x16x32_bf16 v[188:191], v[24:27], v[184:187], v[188:191]\n\t" \
    "v_add_f32 v204, v206, v207\n\t" \
    "s_waitcnt lgkmcnt(5)\n\t" \
    "v_mfma_f32_16x16x32_bf16 v[188:191], v[28:31], v[160:163], v[188:191]\n\t" \
    "v_add_f32 v205, v206, v207\n\t" \
    "v_mfma_f32_16x16x32_bf16 v[192:195], v[104:107], v[184:187], v[192:195]\n\t" \
    "ds_read_b128 v[184:187], %[la] offset:25600\n\t" \
    "v_mfma_f32_16x16x32_bf16 v[192:195], v[108:111], v[160:163], v[192:195]\n\t" \
    "ds_read_b128 v[160:163], %[la] offset:26624\n\t" \
    "v_permlane16_swap_b32 v204, v205\n\t" \
    "s_waitcnt lgkmcnt(6)\n\t" \
    "v_mfma_f32_16x16x32_bf16 v[192:195], v[112:115], v[164:167], v[192:195]\n\t" \
    "s_waitcnt lgkmcnt(5)\n\t" \
    "v_mfma_f32_16x16x32_bf16 v[192:195], v[116:119], v[168:171], v[192:195]\n\t" \
    "v_mul_f32 v205, %[kd], v205\n\t" \
    "v_mfma_f32_16x16x32_bf16 v[188:191], v[32:35], v[164:167], v[188:191]\n\t" \
    "ds_read_b128 v[164:167], %[la] offset:27648\n\t" \
    "v_mul_f32 v204, %[kd], v204\n\t" \
    "v_mfma_f32_16x16x32_bf16 v[188:191], v[36:39], v[168:171], v[188:191]\n\t" \
    "ds_read_b128 v[168:171], %[la] offset:4096\n\t" \
    "v_fma_f32 v204, -v205, v205, v204\n\t" \
    "s_waitcnt lgkmcnt(6)\n\t" \
    "v_mfma_f32_16x16x32_bf16 v[188:191], v[40:43], v[172:175], v[188:191]\n\t" \
    "v_max_f32 v204, 0, v204\n\t" \
    "s_waitcnt lgkmcnt(5)\n\t" \
    "v_mfma_f32_16x16x32_bf16 v[188:191], v[44:47], v[176:179], v[188:191]\n\t" \
    "v_add_f32 v206, %[ke], v204\n\t" \
    "v_mfma_f32_16x16x32_bf16 v[192:195], v[120:123], v[172:175], v[192:195]\n\t" \
    "ds_read_b128 v[172:175], %[la] offset:12288\n\t" \
    "v_rsq_f32 v206, v206\n\t" \
    "v_mfma_f32_16x16x32_bf16 v[192:195], v[124:127], v[176:179], v[192:195]\n\t" \
    "ds_read_b128 v[176:179], %[la] offset:20480\n\t" \
    "s_waitcnt lgkmcnt(6)\n\t" \
    "v_mfma_f32_16x16x32_bf16 v[192:195], v[128:131], v[180:183], v[192:195]\n\t" \
    "v_mul_f32 v207, -v205, v206\n\t" \
    "s_waitcnt lgkmcnt(5)\n\t" \
    "v_mfma_f32_16x16x32_bf16 v[192:195], v[132:135], v[184:187], v[192:195]\n\t" \
    "v_mul_f32 v208, v196, v207\n\t" \
    "v_mfma_f32_16x16x32_bf16 v[188:191], v[48:51], v[180:183], v[188:191]\n\t" \
    "ds_read_b128 v[180:183], %[la] offset:28672\n\t" \
    "v_mul_f32 v209, v197, v207\n\t" \
    "v_mul_f32 v210, v198, v207\n\t" \
    "v_mfma_f32_16x16x32_bf16 v[188:191], v[52:55], v[184:187], v[188:191]\n\t" \
    "v_mul_f32 v211, v199, v207\n\t" \
    "v_mul_f32 v212, v200, v207\n\t" \
    "s_waitcnt lgkmcnt(5)\n\t" \
    "v_mfma_f32_16x16x32_bf16 v[188:191], v[56:59], v[160:163], v[188:191]\n\t" \
    "v_mul_f32 v213, v201, v207\n\t" \
    "v_mul_f32 v214, v202, v207\n\t" \
    "s_waitcnt lgkmcnt(4)\n\t" \
    "v_mfma_f32_16x16x32_bf16 v[188:191], v[60:63], v[164:167], v[188:191]\n\t" \
    "v_mul_f32 v215, v203, v207\n\t" \
    "v_mov_b64 v[216:217], -1\n\t" \
    "v_mfma_f32_16x16x32_bf16 v[192:195], v[136:139], v[160:163], v[192:195]\n\t" \
    "v_mov_b64 v[218:219], -1\n\t" \
    "buffer_store_dwordx4 v[216:219], %[l16], %[rs], %[sa] offen sc1\n\t" \
    "v_mfma_f32_16x16x32_bf16 v[192:195], v[140:143], v[164:167], v[192:195]\n\t" \
    "s_waitcnt lgkmcnt(3)\n\t" \
    "v_mfma_f32_16x16x32_bf16 v[192:195], v[144:147], v[168:171], v[192:195]\n\t" \
    "s_waitcnt lgkmcnt(2)\n\t" \
    "v_mfma_f32_16x16x32_bf16 v[192:195], v[148:151], v[172:175], v[192:195]\n\t" \
    "v_mfma_f32_16x16x32_bf16 v[188:191], v[64:67], v[168:171], v[188:191]\n\t" \
    "v_mfma_f32_16x16x32_bf16 v[188:191], v[68:71], v[172:175], v[188:191]\n\t" \
    "s_waitcnt lgkmcnt(1)\n\t" \
    "v_mfma_f32_16x16x32_bf16 v[188:191], v[72:75], v[176:179], v[188:191]\n\t" \
    "s_waitcnt lgkmcnt(0)\n\t" \
    "v_mfma_f32_16x16x32_bf16 v[188:191], v[76:79], v[180:183], v[188:191]\n\t" \
    "v_mfma_f32_16x16x32_bf16 v[192:195], v[152:155], v[176:179], v[192:195]\n\t" \
    "v_mfma_f32_16x16x32_bf16 v[192:195], v[156:159], v[180:183], v[192:195]\n\t" \
    "s_setprio 2\n\t" \
    "buffer_load_dwordx4 v[232:235], %[l16], %[rs], %[n0] offen offset:0 sc1\n\t" \
    "buffer_load_dwordx4 v[236:239], %[l16], %[rs], %[n0] offen offset:1024 sc1\n\t" \
    "buffer_load_dwordx4 v[240:243], %[l16], %[rs], %[n0] offen offset:2048 sc1\n\t" \
    "buffer_load_dwordx4 v[244:247], %[l16], %[rs], %[n0] offen offset:3072 sc1\n\t" \
    "buffer_load_dwordx4 v[248:251], %[l16], %[rs], %[n1] offen offset:0 sc1\n\t" \
    ASM_ST3 \
    "v_fma_f32 v188, v188, v206, v208\n\t" \
    "v_fma_f32 v189, v189, v206, v209\n\t" \
    "v_fma_f32 v190, v190, v206, v210\n\t" \
    "v_fma_f32 v191, v191, v206, v211\n\t" \
    "v_mul_f32 v160, v188, v188\n\t" \
    "v_mul_f32 v161, v189, v189\n\t" \
    "v_mul_f32 v162, v190, v190\n\t" \
    "v_mul_f32 v163, v191, v191\n\t" \
    "v_fma_f32 v160, v160, %[k1], v220\n\t" \
    "v_fma_f32 v161, v161, %[k1], v220\n\t" \
    "v_fma_f32 v162, v162, %[k1], v220\n\t" \
    "v_fma_f32 v163, v163, %[k1], v220\n\t" \
    "v_mul_f32 v160, v188, v160\n\t" \
    "v_mul_f32 v161, v189, v161\n\t" \
    "v_mul_f32 v162, v190, v162\n\t" \
    "v_mul_f32 v163, v191, v163\n\t" \
    "v_fma_f32 v192, v192, v206, v212\n\t" \
    "v_fma_f32 v193, v193, v206, v213\n\t" \
    "v_fma_f32 v194, v194, v206, v214\n\t" \
    "v_fma_f32 v195, v195, v206, v215\n\t" \
    "v_mul_f32 v164, v192, v192\n\t" \
    "v_mul_f32 v165, v193, v193\n\t" \
    "v_mul_f32 v166, v194, v194\n\t" \
    "v_mul_f32 v167, v195, v195\n\t" \
    "v_fma_f32 v164, v164, %[k1], v220\n\t" \
    "v_fma_f32 v165, v165, %[k1], v220\n\t" \
    "v_fma_f32 v166, v166, %[k1], v220\n\t" \
    "v_fma_f32 v167, v167, %[k1], v220\n\t" \
    "v_mul_f32 v164, v192, v164\n\t" \
    "v_mul_f32 v165, v193, v165\n\t" \
    "v_mul_f32 v166, v194, v166\n\t" \
    "v_mul_f32 v167, v195, v167\n\t" \
    "v_exp_f32 v160, v160\n\t" \
    "v_exp_f32 v161, v161\n\t" \
    "v_exp_f32 v162, v162\n\t" \
    "v_exp_f32 v163, v163\n\t" \
    "v_exp_f32 v164, v164\n\t" \
    "v_exp_f32 v165, v165\n\t" \
    "v_exp_f32 v166, v166\n\t" \
    "v_exp_f32 v167, v167\n\t" \
    "v_add_f32 v160, 1.0, v160\n\t" \
    "v_add_f32 v161, 1.0, v161\n\t" \
    "v_add_f32 v162, 1.0, v162\n\t" \
    "v_add_f32 v163, 1.0, v163\n\t" \
    "v_add_f32 v164, 1.0, v164\n\t" \
    "v_add_f32 v165, 1.0, v165\n\t" \
    "v_add_f32 v166, 1.0, v166\n\t" \
    "v_add_f32 v167, 1.0, v167\n\t" \
    "v_rcp_f32 v160, v160\n\t" \
    "v_rcp_f32 v161, v161\n\t" \
    "v_rcp_f32 v162, v162\n\t" \
    "v_rcp_f32 v163, v163\n\t" \
    "v_rcp_f32 v164, v164\n\t" \
    "v_rcp_f32 v165, v165\n\t" \
    "v_rcp_f32 v166, v166\n\t" \
    "v_rcp_f32 v167, v167\n\t" \
    "v_mul_f32 v188, v188, v160\n\t" \
    "v_mul_f32 v189, v189, v161\n\t" \
    "v_mul_f32 v190, v190, v162\n\t" \
    "v_mul_f32 v191, v191, v163\n\t" \
    "v_mul_f32 v192, v192, v164\n\t" \
    "v_mul_f32 v193, v193, v165\n\t" \
    "v_mul_f32 v194, v194, v166\n\t" \
    "v_mul_f32 v195, v195, v167\n\t" \
    "v_cvt_pk_bf16_f32 v188, v188, v189\n\t" \
    "v_cvt_pk_bf16_f32 v189, v190, v191\n\t" \
    "v_cvt_pk_bf16_f32 v190, v192, v193\n\t" \
    "v_cvt_pk_bf16_f32 v191, v194, v195\n\t" \
    "v_min_u32 v188, -2, v188\n\t" \
    "v_min_u32 v189, -2, v189\n\t" \
    "v_min_u32 v190, -2, v190\n\t" \
    "v_min_u32 v191, -2, v191\n\t" \
    "v_mov_b32 v216, 0x3f803f80\n\t" \
    "v_mov_b32 v217, 0x3f803f80\n\t" \
    "v_mov_b32 v218, 0x3f803f80\n\t" \
    "v_mov_b32 v219, 0x3f803f80\n\t" \
    "v_permlane16_swap_b32 v188, v190\n\t" \
    "v_permlane16_swap_b32 v189, v191\n\t" \
    "s_waitcnt vmcnt(6)\n\t" \
    "buffer_store_dwordx4 v[188:191], %[oa], %[rs], %[so] offen sc1\n\t" \
    "s_nop 1\n\t" \
    "s_setprio 0\n\t"
#define ASM_WT3_HI \
    "s_setprio 1\n\t" \
    "s_barrier\n\t" ASM_ST \
    "ds_read_b128 v[160:163], %[la] offset:0\n\t" \
    "ds_read_b128 v[164:167], %[la] offset:1024\n\t" \
    "ds_read_b128 v[168:171], %[la] offset:2048\n\t" \
    "ds_read2st64_b32 v[204:205], %[sr] offset0:20 offset1:24\n\t" \
    "ds_read_b128 v[172:175], %[la] offset:3072\n\t" \
    "ds_read_b128 v[176:179], %[la] offset:8192\n\t" \
    "ds_read_b128 v[180:183], %[la] offset:9216\n\t" \
    "ds_read_b128 v[184:187], %[la] offset:10240\n\t" \
    "s_waitcnt lgkmcnt(7)\n\t" \
    "v_mfma_f32_16x16x32_bf16 v[192:195], v[80:83], v[160:163], 0\n\t" \
    "s_waitcnt lgkmcnt(6)\n\t" \
    "v_mfma_f32_16x16x32_bf16 v[192:195], v[84:87], v[164:167], v[192:195]\n\t" \
    "v_mfma_f32_16x16x32_bf16 v[188:191], v[0:3], v[160:163], 0\n\t" \
    "ds_read_b128 v[160:163], %[la] offset:11264\n\t" \
    "v_mfma_f32_16x16x32_bf16 v[188:191], v[4:7], v[164:167], v[188:191]\n\t" \
    "ds_read_b128 v[164:167], %[la] offset:16384\n\t" \
    "s_waitcnt lgkmcnt(7)\n\t" \
    "v_mfma_f32_16x16x32_bf16 v[188:191], v[8:11], v[168:171], v[188:191]\n\t" \
    "s_waitcnt lgkmcnt(6)\n\t" \
    "s_waitcnt lgkmcnt(5)\n\t" \
    "v_mfma_f32_16x16x32_bf16 v[188:191], v[12:15], v[172:175], v[188:191]\n\t" \
    "v_permlane16_swap_b32 v204, v205\n\t" \
    "v_mfma_f32_16x16x32_bf16 v[192:195], v[88:91], v[168:171], v[192:195]\n\t" \
    "ds_read_b128 v[168:171], %[la] offset:17408\n\t" \
    "v_mfma_f32_16x16x32_bf16 v[192:195], v[92:95], v[172:175], v[192:195]\n\t" \
    "ds_read_b128 v[172:175], %[la] offset:18432\n\t" \
    "v_add_f32 v206, v204, v205\n\t" \
    "s_waitcnt lgkmcnt(6)\n\t" \
    "v_mfma_f32_16x16x32_bf16 v[192:195], v[96:99], v[176:179], v[192:195]\n\t" \
    "v_add_f32 v207, v204, v205\n\t" \
    "s_waitcnt lgkmcnt(5)\n\t" \
    "v_mfma_f32_16x16x32_bf16 v[192:195], v[100:103], v[180:183], v[192:195]\n\t" \
    "v_mfma_f32_16x16x32_bf16 v[188:191], v[16:19], v[176:179], v[188:191]\n\t" \
    "ds_read_b128 v[176:179], %[la] offset:19456\n\t" \
    "v_permlane32_swap_b32 v206, v207\n\t" \
    "v_mfma_f32_16x16x32_bf16 v[188:191], v[20:23], v[180:183], v[188:191]\n\t" \
    "ds_read_b128 v[180:183], %[la] offset:24576\n\t" \
    "s_waitcnt lgkmcnt(6)\n\t" \
    "v_mfma_f32_16x16x32_bf16 v[188:191], v[24:27], v[184:187], v[188:191]\n\t" \
    "v_add_f32 v204, v206, v207\n\t" \
    "s_waitcnt lgkmcnt(5)\n\t" \
    "v_mfma_f32_16x16x32_bf16 v[188:191], v[28:31], v[160:163], v[188:191]\n\t" \
    "v_add_f32 v205, v206, v207\n\t" \
    "v_mfma_f32_16x16x32_bf16 v[192:195], v[104:107], v[184:187], v[192:195]\n\t" \
    "ds_read_b128 v[184:187], %[la] offset:25600\n\t" \
    "v_mfma_f32_16x16x32_bf16 v[192:195], v[108:111], v[160:163], v[192:195]\n\t" \
    "ds_read_b128 v[160:163], %[la] offset:26624\n\t" \
    "v_permlane16_swap_b32 v204, v205\n\t" \
    "s_waitcnt lgkmcnt(6)\n\t" \
    "v_mfma_f32_16x16x32_bf16 v[192:195], v[112:115], v[164:167], v[192:195]\n\t" \
    "s_waitcnt lgkmcnt(5)\n\t" \
    "v_mfma_f32_16x16x32_bf16 v[192:195], v[116:119], v[168:171], v[192:195]\n\t" \
    "v_mul_f32 v205, %[kd], v205\n\t" \
    "v_mfma_f32_16x16x32_bf16 v[188:191], v[32:35], v[164:167], v[188:191]\n\t" \
    "ds_read_b128 v[164:167], %[la] offset:27648\n\t" \
    "v_mul_f32 v204, %[kd], v204\n\t" \
    "v_mfma_f32_16x16x32_bf16 v[188:191], v[36:39], v[168:171], v[188:191]\n\t" \
    "ds_read_b128 v[168:171], %[la] offset:4096\n\t" \
    "v_fma_f32 v204, -v205, v205, v204\n\t" \
    "s_waitcnt lgkmcnt(6)\n\t" \
    "v_mfma_f32_16x16x32_bf16 v[188:191], v[40:43], v[172:175], v[188:191]\n\t" \
    "v_max_f32 v204, 0, v204\n\t" \
    "s_waitcnt lgkmcnt(5)\n\t" \
    "v_mfma_f32_16x16x32_bf16 v[188:191], v[44:47], v[176:179], v[188:191]\n\t" \
    "v_add_f32 v206, %[ke], v204\n\t" \
    "v_mfma_f32_16x16x32_bf16 v[192:195], v[120:123], v[172:175], v[192:195]\n\t" \
    "ds_read_b128 v[172:175], %[la] offset:12288\n\t" \
    "v_rsq_f32 v206, v206\n\t" \
    "v_mfma_f32_16x16x32_bf16 v[192:195], v[124:127], v[176:179], v[192:195]\n\t" \
    "ds_read_b128 v[176:179], %[la] offset:20480\n\t" \
    "s_waitcnt lgkmcnt(6)\n\t" \
    "v_mfma_f32_16x16x32_bf16 v[192:195], v[128:131], v[180:183], v[192:195]\n\t" \
    "v_mul_f32 v207, -v205, v206\n\t" \
    "s_waitcnt lgkmcnt(5)\n\t" \
    "v_mfma_f32_16x16x32_bf16 v[192:195], v[132:135], v[184:187], v[192:195]\n\t" \
    "v_mul_f32 v208, v196, v207\n\t" \
    "v_mfma_f32_16x16x32_bf16 v[188:191], v[48:51], v[180:183], v[188:191]\n\t" \
    "ds_read_b128 v[180:183], %[la] offset:28672\n\t" \
    "v_mul_f32 v209, v197, v207\n\t" \
    "v_mul_f32 v210, v198, v207\n\t" \
    "v_mfma_f32_16x16x32_bf16 v[188:191], v[52:55], v[184:187], v[188:191]\n\t" \
    "v_mul_f32 v211, v199, v207\n\t" \
    "v_mul_f32 v212, v200, v207\n\t" \
    "s_waitcnt lgkmcnt(5)\n\t" \
    "v_mfma_f32_16x16x32_bf16 v[188:191], v[56:59], v[160:163], v[188:191]\n\t" \
    "v_mul_f32 v213, v201, v207\n\t" \
    "v_mul_f32 v214, v202, v207\n\t" \
    "s_waitcnt lgkmcnt(4)\n\t" \
    "v_mfma_f32_16x16x32_bf16 v[188:191], v[60:63], v[164:167], v[188:191]\n\t" \
    "v_mul_f32 v215, v203, v207\n\t" \
    "v_mov_b64 v[216:217], -1\n\t" \
    "v_mfma_f32_16x16x32_bf16 v[192:195], v[136:139], v[160:163], v[192:195]\n\t" \
    "v_mov_b64 v[218:219], -1\n\t" \
    "buffer_store_dwordx4 v[216:219], %[l16], %[rs], %[sa] offen sc1\n\t" \
    "v_mfma_f32_16x16x32_bf16 v[192:195], v[140:143], v[164:167], v[192:195]\n\t" \
    "s_waitcnt lgkmcnt(3)\n\t" \
    "v_mfma_f32_16x16x32_bf16 v[192:195], v[144:147], v[168:171], v[192:195]\n\t" \
    "s_waitcnt lgkmcnt(2)\n\t" \
    "v_mfma_f32_16x16x32_bf16 v[192:195], v[148:151], v[172:175], v[192:195]\n\t" \
    "v_mfma_f32_16x16x32_bf16 v[188:191], v[64:67], v[168:171], v[188:191]\n\t" \
    "v_mfma_f32_16x16x32_bf16 v[188:191], v[68:71], v[172:175], v[188:191]\n\t" \
    "s_waitcnt lgkmcnt(1)\n\t" \
    "v_mfma_f32_16x16x32_bf16 v[188:191], v[72:75], v[176:179], v[188:191]\n\t" \
    "s_waitcnt lgkmcnt(0)\n\t" \
    "v_mfma_f32_16x16x32_bf16 v[188:191], v[76:79], v[180:183], v[188:191]\n\t" \
    "v_mfma_f32_16x16x32_bf16 v[192:195], v[152:155], v[176:179], v[192:195]\n\t" \
    "v_mfma_f32_16x16x32_bf16 v[192:195], v[156:159], v[180:183], v[192:195]\n\t" \
    ASM_ST3 \
    "s_nop 1\n\t" \
    "v_fma_f32 v188, v188, v206, v208\n\t" \
    "v_fma_f32 v189, v189, v206, v209\n\t" \
    "v_fma_f32 v190, v190, v206, v210\n\t" \
    "v_fma_f32 v191, v191, v206, v211\n\t" \
    "v_mul_f32 v160, v188, v188\n\t" \
    "v_mul_f32 v161, v189, v189\n\t" \
    "v_mul_f32 v162, v190, v190\n\t" \
    "v_mul_f32 v163, v191, v191\n\t" \
    "v_fma_f32 v160, v160, %[k1], v220\n\t" \
    "v_fma_f32 v161, v161, %[k1], v220\n\t" \
    "v_fma_f32 v162, v162, %[k1], v220\n\t" \
    "v_fma_f32 v163, v163, %[k1], v220\n\t" \
    "v_mul_f32 v160, v188, v160\n\t" \
    "v_mul_f32 v161, v189, v161\n\t" \
    "v_mul_f32 v162, v190, v162\n\t" \
    "v_mul_f32 v163, v191, v163\n\t" \
    "v_fma_f32 v192, v192, v206, v212\n\t" \
    "v_fma_f32 v193, v193, v206, v213\n\t" \
    "v_fma_f32 v194, v194, v206, v214\n\t" \
    "v_fma_f32 v195, v195, v206, v215\n\t" \
    "v_mul_f32 v164, v192, v192\n\t" \
    "v_mul_f32 v165, v193, v193\n\t" \
    "v_mul_f32 v166, v194, v194\n\t" \
    "v_mul_f32 v167, v195, v195\n\t" \
    "v_fma_f32 v164, v164, %[k1], v220\n\t" \
    "v_fma_f32 v165, v165, %[k1], v220\n\t" \
    "v_fma_f32 v166, v166, %[k1], v220\n\t" \
    "v_fma_f32 v167, v167, %[k1], v220\n\t" \
    "v_mul_f32 v164, v192, v164\n\t" \
    "v_mul_f32 v165, v193, v165\n\t" \
    "v_mul_f32 v166, v194, v166\n\t" \
    "v_mul_f32 v167, v195, v167\n\t" \
    "v_exp_f32 v160, v160\n\t" \
    "v_exp_f32 v161, v161\n\t" \
    "v_exp_f32 v162, v162\n\t" \
    "v_exp_f32 v163, v163\n\t" \
    "v_exp_f32 v164, v164\n\t" \
    "v_exp_f32 v165, v165\n\t" \
    "v_exp_f32 v166, v166\n\t" \
    "v_exp_f32 v167, v167\n\t" \
    "v_add_f32 v160, 1.0, v160\n\t" \
    "v_add_f32 v161, 1.0, v161\n\t" \
    "v_add_f32 v162, 1.0, v162\n\t" \
    "v_add_f32 v163, 1.0, v163\n\t" \
    "v_add_f32 v164, 1.0, v164\n\t" \
    "v_add_f32 v165, 1.0, v165\n\t" \
    "v_add_f32 v166, 1.0, v166\n\t" \
    "v_add_f32 v167, 1.0, v167\n\t" \
    "v_rcp_f32 v160, v160\n\t" \
    "v_rcp_f32 v161, v161\n\t" \
    "v_rcp_f32 v162, v162\n\t" \
    "v_rcp_f32 v163, v163\n\t" \
    "v_rcp_f32 v164, v164\n\t" \
    "v_rcp_f32 v165, v165\n\t" \
    "v_rcp_f32 v166, v166\n\t" \
    "v_rcp_f32 v167, v167\n\t" \
    "v_mul_f32 v188, v188, v160\n\t" \
    "v_mul_f32 v189, v189, v161\n\t" \
    "v_mul_f32 v190, v190, v162\n\t" \
    "v_mul_f32 v191, v191, v163\n\t" \
    "v_mul_f32 v192, v192, v164\n\t" \
    "v_mul_f32 v193, v193, v165\n\t" \
    "v_mul_f32 v194, v194, v166\n\t" \
    "v_mul_f32 v195, v195, v167\n\t" \
    "v_cvt_pk_bf16_f32 v188, v188, v189\n\t" \
    "v_cvt_pk_bf16_f32 v189, v190, v191\n\t" \
    "v_cvt_pk_bf16_f32 v190, v192, v193\n\t" \
    "v_cvt_pk_bf16_f32 v191, v194, v195\n\t" \
    "v_min_u32 v188, -2, v188\n\t" \
    "v_min_u32 v189, -2, v189\n\t" \
    "v_min_u32 v190, -2, v190\n\t" \
    "v_min_u32 v191, -2, v191\n\t" \
    "s_nop 1\n\t" \
    "v_permlane16_swap_b32 v188, v190\n\t" \
    "v_permlane16_swap_b32 v189, v191\n\t" \
    "s_waitcnt vmcnt(1)\n\t" \
    "buffer_store_dwordx4 v[188:191], %[oa], %[rs], %[so] offen sc1\n\t" \
    "s_nop 1\n\t"
#define ASM_POLL5_I \
    "buffer_load_dwordx4 v[232:235], %[l16], %[rs], %[s0] offen offset:0 sc1\n\t" \
    "buffer_load_dwordx4 v[236:239], %[l16], %[rs], %[s0] offen offset:1024 sc1\n\t" \
    "buffer_load_dwordx4 v[240:243], %[l16], %[rs], %[s0] offen offset:2048 sc1\n\t" \
    "buffer_load_dwordx4 v[244:247], %[l16], %[rs], %[s0] offen offset:3072 sc1\n\t" \
    "buffer_load_dwordx4 v[248:251], %[l16], %[rs], %[s1] offen offset:0 sc1\n\t"
using u32x8 = __attribute__((ext_vector_type(8))) unsigned;
using u32x16 = __attribute__((ext_vector_type(16))) unsigned;
using u32x32 = __attribute__((ext_vector_type(32))) unsigned;
struct WRegs { u32x32 w[6]; u32x8 t; };
#define WREGS(W) "{v[0:31]}"(W.w[0]), "{v[32:63]}"(W.w[1]), "{v[64:95]}"(W.w[2]), "{v[96:127]}"(W.w[3]), "{v[128:159]}"(W.w[4]), "{v[160:191]}"(W.w[5]), "{v[192:199]}"(W.t)
__device__ __forceinline__ u32x32 cat32(u32x8 a, u32x8 b, u32x8 c, u32x8 d) {
    const u32x16 lo = __builtin_shufflevector(a, b, 0, 1, 2, 3, 4, 5, 6, 7, 8, 9, 10, 11, 12, 13, 14, 15);
    const u32x16 hi = __builtin_shufflevector(c, d, 0, 1, 2, 3, 4, 5, 6, 7, 8, 9, 10, 11, 12, 13, 14, 15);
    return __builtin_shufflevector(lo, hi, 0, 1, 2, 3, 4, 5, 6, 7, 8, 9, 10, 11, 12, 13, 14, 15, 16, 17, 18, 19, 20, 21, 22, 23, 24, 25, 26, 27, 28, 29, 30, 31);
}
#define WOUT(W) "=&{v[0:31]}"(W.w[0]), "=&{v[32:63]}"(W.w[1]), "=&{v[64:95]}"(W.w[2]), "=&{v[96:127]}"(W.w[3]), "=&{v[128:159]}"(W.w[4]), "=&{v[160:191]}"(W.w[5]), "=&{v[192:199]}"(W.t)
#define WTMP "memory", "scc", "v200", "v201", "v202", "v203", "v204", "v205", "v206", "v207", "v208", "v209", "v210", "v211", "v212", "v213", "v214", "v215", "v216", "v217", "v218", "v219", "v220", "v221", "v222", "v223", "v224", "v225", "v226", "v227", "v228", "v229", "v230", "v231", "v232", "v233", "v234", "v235", "v236", "v237", "v238", "v239"
__device__ __forceinline__ void load_w3(WRegs& W, const bf16_t* m, int wave, unsigned c0, bool hq) {
    const int ln = lane_id();
    unsigned o[3], s[3], t;
#pragma unroll
    for (int k = 0; k < 3; ++k) {
        const int row = (k * 64 + ln) / 24, sg = (k * 64 + ln) % 24;
        o[k] = row * (HD * 2) + c0 + (hq ? ((sg >> 1) & 1) * 384 + ((0x140 >> ((sg >> 3) * 4)) & 15) * 32 + (sg & 5) * 16 : sg * 16); s[k] = wave * 13824 + row * 416 + sg * 16;
    }
    const unsigned tr = (ln >> 4) * 3456 + ((ln >> 2) & 3) * 416 + (wave & 3) * 32 + (ln & 3) * 8;
    asm volatile(ASM_WLOAD3 : WOUT(W), [t] "=&s"(t) : [rs] "s"(mkrsrc(m, DM * HD * 2)), [wx] "s"(wave), [o0] "v"(o[0]), [o1] "v"(o[1]), [o2] "v"(o[2]), [s0] "v"(s[0]), [s1] "v"(s[1]), [s2] "v"(s[2]),
                 [t0] "v"(tr + wave * 13824), [t1] "v"(tr + (wave ^ 1) * 13824), [t2] "v"(tr + (wave ^ 2) * 13824), [t3] "v"(tr + (wave ^ 3) * 13824), [t4] "v"(tr + (wave & 4) * 13824),
                 [wl] "v"(L_W + wave * 10240 + ln * 16) : WTMP);
}
__device__ __forceinline__ void load_wo(WRegs& W, const bf16_t* m, int wave, int h) {
    const int ln = lane_id();
    unsigned o[5], s[5], t;
#pragma unroll
    for (int k = 0; k < 5; ++k) {
        const int row = (k * 64 + ln) / 20, sg = (k * 64 + ln) % 20;
        o[k] = (h * DH + row) * (DM * 2) + sg * 16 + (wave & 3) * 320; s[k] = wave * 13824 + row * 352 + sg * 16;
    }
    asm volatile(ASM_WLOADO : WOUT(W), [t] "=&s"(t) : [rs] "s"(mkrsrc(m, DM * HD * 2)), [o0] "v"(o[0]), [o1] "v"(o[1]), [o2] "v"(o[2]), [o3] "v"(o[3]), [o4] "v"(o[4]),
                 [s0] "v"(s[0]), [s1] "v"(s[1]), [s2] "v"(s[2]), [s3] "v"(s[3]), [s4] "v"(s[4]), [tr] "v"(wave * 13824 + (ln >> 2) * 352 + (ln & 3) * 8), [wl] "v"(L_W + wave * 10240 + ln * 16) : WTMP);
}
using s16x4 = __attribute__((ext_vector_type(4))) short;
template <int NT, int KS, int N>
__device__ __forceinline__ void load_wn(u32x4 (&f)[NT * KS], const bf16_t* m, int rows, int wave, int c0, int k0) {
    const int ln = lane_id();
    const rsrc_t rs = mkrsrc(m, rows * N * 2);
#pragma unroll
    for (int k = 0; k < KS; ++k)
#pragma unroll
        for (int n = 0; n < NT; ++n) f[n * KS + k] = __builtin_amdgcn_raw_buffer_load_b128(rs, ((n * 64 + ln) / (2 * NT)) * (N * 2) + ((n * 64 + ln) % (2 * NT)) * 16, (k0 + k) * (N * 64) + c0 * 32, 0);
    const unsigned st = wave * 10240 + ln * 16, tr = wave * 10240 + ((ln >> 4) * 8 + ((ln >> 2) & 3)) * (NT * 32) + (ln & 3) * 8;
#pragma unroll
    for (int k = 0; k < KS; ++k) {
#pragma unroll
        for (int n = 0; n < NT; ++n) lds_st<u32x4>(st + n * 1024, f[n * KS + k]);
        asm volatile("" ::: "memory");
#pragma unroll
        for (int n = 0; n < NT; ++n) {
            const u32x2 a = __builtin_bit_cast(u32x2, __builtin_amdgcn_ds_read_tr16_b64_v4i16((LAS s16x4*)(tr + n * 32)));
            const u32x2 b = __builtin_bit_cast(u32x2, __builtin_amdgcn_ds_read_tr16_b64_v4i16((LAS s16x4*)(tr + n * 32 + NT * 128)));
            f[n * KS + k] = (u32x4){a[0], a[1], b[0], b[1]};
        }
        asm volatile("" ::: "memory");
    }
}
struct C1Regs { u32x8 lw0, lw1, z1; u32x4 z0; };
#define XG_OUT "=&{v[200:207]}"(C.lw0), "=&{v[224:227]}"(C.z0), "=&{v[228:235]}"(C.lw1), "=&{v[244:251]}"(C.z1)
#define XG_IN WREGS(W), [xa] "v"(xa), [wl] "v"(wl), [qs] "s"(qs)
#define XG_CLOB "memory", "scc", "v208", "v209", "v210", "v211", "v212", "v213", "v214", "v215", "v216", "v217", "v218", "v219", "v220", "v221", "v222", "v223", "v236", "v237", "v238", "v239", "v240", "v241", "v242", "v243", "v252"
constexpr int XS0 = 8;
__device__ __forceinline__ unsigned init_wait(const Params& P, int c, int ph, unsigned need = 0x220080u) {
    unsigned w;
    for (int spins = 0;; ++spins) {
        unsigned o;
        asm volatile("v_mov_b32 %0, 0" : "=v"(o));
        w = __builtin_amdgcn_readfirstlane(__builtin_amdgcn_raw_buffer_load_b32(mkrsrc(P.ws + FL0, FL_BYTES), o, FL_IN + (ph * 8 + (c & 7)) * 128, 16));
        if ((w & need) == need) break;
        __builtin_amdgcn_s_sleep(8);
        if (spin_fail(spins, P)) break;
    }
    asm volatile("" ::: "memory");
    return w;
}
#define PLACE() const unsigned pw_ = init_wait(P, wave, L); const int par = (L ^ (pw_ >> 16)) & 1, cpy = x ^ ((pw_ >> 20) & 1); const rsrc_t rr = mkrsrc(P.ws + par * 8192, RG_L); (void)cpy; (void)rr
__device__ __forceinline__ void xn_wait(const Params& P, rsrc_t rr, int seg, int cpy) {
    for (int spins = 0;; ++spins) {
        const unsigned f = __builtin_amdgcn_raw_buffer_load_b32(rr, (lane_id() & 15) * 128, RG_FX + (cpy & 3) * 2048, 16);
        if (__all(f > (unsigned)seg)) break;
        __builtin_amdgcn_s_sleep(4);
        if (spin_fail(spins, P)) break;
    }
    asm volatile("" ::: "memory");
}
__device__ __forceinline__ unsigned c1_dst(int wave, bool ktype) {
    const int ln = lane_id(), l15 = ln & 15, hf = (wave >> 1) & 1;
    const int rb16 = ((ln >> 2) & 4) * 16 + (ln >> 5) * 32;
    return (ktype ? L_KM + (l15 >> 2) * 128 + hf * 8 + (l15 & 3) * 2
                  : L_Q + (l15 >> 3) * 256 + ((l15 >> 1) & 1) * 128 + ((l15 >> 2) & 1) * 8 + hf * 4 + (l15 & 1) * 2) + (wave & 1) * 1536 + rb16;
}
template <int NP>
__device__ __forceinline__ void crit1(const WRegs& W, C1Regs& C, rsrc_t rr, unsigned sbase, int wave, unsigned wl, unsigned dsto, unsigned& budget, unsigned& fv, unsigned fs
) {
    unsigned abase;
    asm volatile("v_and_b32 %0, 0x3ff, %1\n\tv_or_b32 %0, %2, %0" : "=&v"(abase) : "v"(wl), "s"(wave * 1024));
    ASM_ST_DECL;
#define C1_OUT "+{v[224:227]}"(C.z0), "+{v[244:251]}"(C.z1), "+{v[200:207]}"(C.lw0), "+{v[228:235]}"(C.lw1)
#define C1_IN WREGS(W), [ab] "v"(abase), [wl] "v"(wl), [dst] "v"(dsto)
#define C1_CLOB "memory", "vcc", "scc", "v208", "v209", "v210", "v211", "v212", "v213", "v214", "v215", "v216", "v217", "v218", "v219", "v220", "v221", "v222", "v223", "v236", "v237", "v238", "v239", "v240", "v241", "v242", "v243"
#define C1_POLL [rs] "s"(rr), [s0] "s"(sbase), [s1] "s"(sbase + 4096), [s2] "s"(sbase + 2 * BLK)
    ST(1);
    if constexpr (NP == 13) asm volatile(ASM_CRIT1_V3 "s_waitcnt vmcnt(0)" : C1_OUT, [n] "+s"(budget), [fv] "+v"(fv) ASM_ST_OUT ASM_ST2_OUT : C1_IN, C1_POLL, [fs] "s"(fs) : C1_CLOB);
    else if constexpr (NP == 12) asm volatile(ASM_CRIT1_V2 "s_waitcnt vmcnt(0)" : C1_OUT, [n] "+s"(budget), [fv] "+v"(fv) ASM_ST_OUT ASM_ST2_OUT : C1_IN, C1_POLL, [fs] "s"(fs) : C1_CLOB);
    else if constexpr (NP == 3) asm volatile(ASM_CRIT1_3 : C1_OUT, [n] "+s"(budget) ASM_ST_OUT ASM_ST2_OUT : C1_IN, C1_POLL : C1_CLOB);
    else if constexpr (NP == 2) asm volatile(ASM_CRIT1_2 : C1_OUT, [n] "+s"(budget) ASM_ST_OUT ASM_ST2_OUT : C1_IN, C1_POLL : C1_CLOB);
    else asm volatile(ASM_CRIT1_N : C1_OUT ASM_ST_OUT : C1_IN : C1_CLOB);
    SB();
    ASM_ST_SAVE(3);
    if constexpr (NP != 0) { ASM_ST2_SAVE(2); }
}
#define STARG

#define SRG 5
#undef STROLE
#define STROLE 1
__device__ __forceinline__ void s_role(const Params& P, int wave, int L, int h, int x) {
    const char* xn = P.ws + PG0 + PG_XN + L * PG_LX;
    auto xdma = [&](int seg, int hb, int par) {
        const unsigned l16 = lane_id() * 16;
        const int mt = (wave >> 1) & 1;
        const char* g = xn + pgb(seg, par);
        if (hb == 0) {
            dma4(g + blin(mt * 20480 + (wave & 1) * 4096), l16, L_XA + mt * 16384 + (wave & 1) * 4096);
            dmaq<0>(g + blin(mt * 20480 + 16384) + (wave & 1) * 1024, l16, L_XB + mt * 4096 + (wave & 1) * 1024);
        } else {
            dma4(g + blin(mt * 20480 + 8192), l16, L_XA + mt * 16384 + 8192); dma4(g + blin(mt * 20480 + 12288), l16, L_XA + mt * 16384 + 12288);
            dmaq<2>(g + blin(mt * 20480 + 16384), l16, L_XB + mt * 4096); dmaq<3>(g + blin(mt * 20480 + 16384), l16, L_XB + mt * 4096);
        }
    };
    const float SCL = 0.07216878364870322f * 1.4426950408889634f;
    auto run = [&](auto Cc) {
    constexpr int CLS = decltype(Cc)::value;
    WRegs wf;
    MARK(!L && h == 0 && wave == 0, 0);
    load_w3(wf, P.w[CLS == 3], wave, h * 384, false);
    MARKX(!L && wave == 0, 1);
    PLACE();
    if (CLS == 3) {
        if (L) xn_wait(P, rr, 0, cpy);
        xdma(0, 0, par);
        if (wave == 4 || wave == 7) xdma(0, 1, par);
        wait_vm<0>();
    }
    bar();
    if (CLS == 3 && wave == 7 && L) xn_wait(P, rr, 1, cpy);
    const unsigned qs = (CLS == 3 ? L_KX : L_QX) + (wave & 1) * 3072 + ((wave >> 1) & 1) * 8;
    unsigned xa, wl, dsto;
    if (CLS == 3) { xa = L_XA + wave * 2048 + lane_id() * 16; wl = L_W + wave * 10240 + lane_id() * 16; dsto = c1_dst(wave, true); }
    __builtin_amdgcn_s_setprio(CLS == 3 ? 1 : 2);
    unsigned budget = 1u << 18, tb2 = 0;
    STDECL;
    LDECL;
#pragma unroll 1
    for (int t = 0; t < NSEG; ++t) {
        const unsigned slot = t & (NSLOT - 1);
        C1Regs C;
        {
            const WRegs& W = wf;
            ASM_ST_DECL;
            if constexpr (CLS == 3) {
                const int mt = (wave >> 1) & 1;
                const char* g = xn + pgb(t + 1 < NSEG ? t + 1 : t, par);
                asm volatile(ASM_XG_S_HI : XG_OUT ASM_ST_OUT : XG_IN, [slp] "s"(SLP4), [g0] "s"(g + blin(mt * 20480 + (wave & 1) * 4096)), [d0] "s"(L_XA + mt * 16384 + (wave & 1) * 4096),
                             [g1] "s"(g + blin(mt * 20480 + 16384) + (wave & 1) * 1024), [d1] "s"(L_XB + mt * 4096 + (wave & 1) * 1024) : XG_CLOB);
                wait_vm<0>();
                LB(t);
            } else {
                const unsigned l16 = lane_id() * 16;
                xa = L_XA + wave * 2048 + l16; wl = L_W + wave * 10240 + l16;
#define XG_Q(A) asm volatile("s_cmp_lt_u32 %[t], 2\n\ts_cbranch_scc1 .Lq3%=\n\t" A "s_branch .Lqe%=\n\t.Lq3%=:\n\t" ASM_XG_S_LO ".Lqe%=:" : XG_OUT ASM_ST_OUT : XG_IN, [t] "s"(t) : XG_CLOB)
                if constexpr (CLS == 0 || CLS == 4) XG_Q(ASM_XG_S_LO1); else XG_Q(ASM_XG_S_LO2);
                __builtin_amdgcn_s_setprio(0);
                if constexpr (CLS == 0) {
                    const int ln = lane_id();
                    const u32x4 s4 = sent4();
                    const unsigned poff = (ln * 16 + ((ln < 8) ? h * 128 : (1 + (h >> 4)) * 2 * BLK - 128 + (h & 15) * 512)) | ((ln >= 40 || t < 2) ? 0x40000000u : 0u);
#pragma unroll
                    for (int c = 0; c < (int)NCP; ++c) st128(s4, rr, poff, RG_XP + ((t - 2) & (NSLOT - 1)) * XP_SLOT + (c >> 1) * XP_PAIR + (c & 1) * BLK);
                    const unsigned pbase = RG_PX + ((t - 3) & (NSLOT - 1)) * PX_SLOT + (h >> 2) * BLK;
                    const unsigned o2 = ((ln & 16) ? 5120 + ((h >> 1) & 1) * 1024 + (h & 1) * 256 + (ln >> 5) * 512 : 4096 + (h & 3) * 256 + (ln >> 5) * 3072) + (ln & 15) * 16;
                    if (t >= 3) { st128(s4, rr, ln * 16, pbase + (h & 3) * 1024); st128(s4, rr, o2, pbase); }
                }
                if (CLS == 4 && t >= HT0) {
                    const int ln = lane_id(), u = h >> 2;
                    for (int spins = 0;; ++spins) {
                        const unsigned fl = __builtin_amdgcn_raw_buffer_load_b32(rr, (ln & 3) * 128, RG_FH + BLK + u * 512, 16);
                        if (__all(fl > (unsigned)t)) break;
                        __builtin_amdgcn_s_sleep(2);
                        if (spin_fail(spins, P)) break;
                    }
                    asm volatile("" ::: "memory");
                    const char* g = P.ws + PG0 + pgb(t, par) + PG_HV + (16 + u) * 3 * BLK;
                    unsigned t0, t1, vo;
                    static_assert(L_QX == 16384, "");
                    asm volatile("s_add_u32 %0, %4, 0\n\ts_and_b32 %1, %0, 0xe000\n\ts_add_u32 %0, %0, %1\n\tv_add_u32 %2, %0, %5\n\ts_mov_b32 m0, 16384\n\ts_nop 0\n\tglobal_load_lds_dwordx4 %2, %3 sc1\n\t"
                                 "s_add_u32 %0, %4, 1024\n\ts_and_b32 %1, %0, 0xe000\n\ts_add_u32 %0, %0, %1\n\tv_add_u32 %2, %0, %5\n\ts_mov_b32 m0, 17408\n\ts_nop 0\n\tglobal_load_lds_dwordx4 %2, %3 sc1\n\t"
                                 "s_add_u32 %0, %4, 2048\n\ts_and_b32 %1, %0, 0xe000\n\ts_add_u32 %0, %0, %1\n\tv_add_u32 %2, %0, %5\n\ts_mov_b32 m0, 19456\n\ts_nop 0\n\tglobal_load_lds_dwordx4 %2, %3 sc1\n\t"
                                 "s_add_u32 %0, %4, 6144\n\ts_and_b32 %1, %0, 0xe000\n\ts_add_u32 %0, %0, %1\n\tv_add_u32 %2, %0, %5\n\ts_mov_b32 m0, 22528\n\ts_nop 0\n\tglobal_load_lds_dwordx4 %2, %3 sc1\n\t"
                                 "s_add_u32 %0, %4, 7168\n\ts_and_b32 %1, %0, 0xe000\n\ts_add_u32 %0, %0, %1\n\tv_add_u32 %2, %0, %5\n\ts_mov_b32 m0, 23552\n\ts_nop 0\n\tglobal_load_lds_dwordx4 %2, %3 sc1\n\t"
                                 "s_add_u32 %0, %4, 8192\n\ts_and_b32 %1, %0, 0xe000\n\ts_add_u32 %0, %0, %1\n\tv_add_u32 %2, %0, %5\n\ts_mov_b32 m0, 25600\n\ts_nop 0\n\tglobal_load_lds_dwordx4 %2, %3 sc1\n\t"
                                 : "=&s"(t0), "=&s"(t1), "=&v"(vo) : "s"(g), "s"((((h >> 1) & 1) * 12 + (h & 1) * 3) * 1024), "v"(ln * 16) : "memory", "scc");
                }
                dsto = c1_dst(wave, false);
                while ((unsigned)__builtin_amdgcn_s_memtime() - tb2 < STD) __builtin_amdgcn_s_sleep(2);
            }
            ASM_ST_SAVE(7);
            ST(6);
        }
        ST(0);
        unsigned fv = 0;
        if (CLS == 2) fv = (lane_id() & 15) * 128;
        crit1<CLS == 3 ? 0 : CLS < 2 ? 3 : CLS == 2 ? 12 : 2>(wf, C, rr, RG_XM + slot * XM_SLOT + (cpy >> 1) * XM_PAIR + (cpy & 1) * BLK, wave, wl, dsto, budget, fv, RG_FX + (cpy & 3) * 2048 STARG);
        if (CLS != 3) tb2 = (unsigned)__builtin_amdgcn_s_memtime();
        if constexpr (CLS == 0) {
            const unsigned sp = RG_XP + slot * XP_SLOT, sx = sp + (1 + (h >> 4)) * 2 * BLK + (h & 15) * 512;
            ASM_ST_DECL;
            asm volatile(ASM_SM : [c0] "+s"(budget) ASM_ST_OUT : WREGS(wf), [rr] "s"(rr), [sx] "s"(sx), [sy] "s"(sx + XP_PAIR), [sm] "s"(sp + h * 128), [cs] "s"(BLK), [cp] "s"(XP_PAIR), [c70] "s"(0x70), [scl] "s"(SCL)
                         : "memory", "v200", "v201", "v202", "v203", "v204", "v205", "v206", "v207", "v208", "v209", "v210", "v211", "v212", "v213", "v214", "v215", "v216", "v217", "v218", "v219", "v220", "v221", "v222", "v223", "v224", "v225", "v226", "v227", "v228", "v229", "v230", "v231", "v232", "v233", "v234", "v235", "v236", "v237", "v238", "v239", "v240", "v241", "v242", "v243", "v244", "v245", "v246", "v247", "v248", "v249", "v250", "v251", "v252", "v253", "v254", "v255");
            ASM_ST_SAVE(4);
            ST(5);
        } else if constexpr (CLS == 1 || CLS == 4) {
            const int ln = lane_id(), l15 = ln & 15, l4 = ln >> 4;
            constexpr int mt = (CLS == 4);
            const unsigned qa = L_QX + mt * 6144 + ln * 16, kmo = L_KM + l4 * 128 + (ln & 7) * 16;
            const unsigned xo = mt ? (h & 3) * 1024 : 5120 + ((h >> 1) & 1) * 1024 + (h & 1) * 256;
            const unsigned pm = l4 < 2 ? (mt ? 4096 : 7168) + (h & 3) * 256 - xo + l15 * 16 + l4 * 8 : OOB;
            const unsigned so = RG_PX + slot * PX_SLOT + (h >> 2) * BLK + xo;
            f32x4 sd;
#pragma unroll
            for (int r = 0; r < 4; ++r) sd[r] = (l4 * 4 + r > l15) ? -3.0e38f : 0.f;
            ASM_ST_DECL;
#define P4_ARGS : "+{v[240:243]}"(sd) ASM_ST_OUT : WREGS(wf), [qa] "v"(qa), [km] "v"(kmo), [pm] "v"(pm), [rr] "s"(rr), [so] "s"(so), [scl] "s"(SCL) : "memory", "v200", "v201", "v202", "v203", "v204", "v205", "v206", "v207", "v208", "v209", "v210", "v211", "v212", "v213", "v214", "v215", "v216", "v217", "v218", "v219", "v220", "v221", "v222", "v223", "v224", "v225", "v226", "v227", "v228", "v229", "v230", "v231", "v232", "v233", "v234", "v235", "v236", "v237", "v238", "v239", "v244", "v245", "v246", "v247", "v248", "v249", "v250", "v251"
            if constexpr (mt) asm volatile(ASM_P4_1 P4_ARGS); else asm volatile(ASM_P4_0 P4_ARGS);
            ASM_ST_SAVE(4);
            ST(5);
        } else {
            bar();
            ST(4);
            if (CLS == 2 && L && t + 2 < NSEG && !__all(fv > (unsigned)t + 2)) xn_wait(P, rr, t + 2, cpy);
            if (CLS == 2) { int tt = t; asm volatile("" : "+s"(tt)); if (tt + 2 == XS0) init_wait(P, h, 1); }
            if (CLS == 3) LA();
            {
                if (CLS == 3 && (wave == 4 || wave == 7)) {
                    if (t + 1 < NSEG) xdma(t + 1, 1, par);
                }
                ST(5);
            }
        }
        STFLUSH(t);
    }
    LF();
    };
    if (wave == 0) run(std::integral_constant<int, 0>{});
    else if (wave == 1) run(std::integral_constant<int, 1>{});
    else if (wave == 2) run(std::integral_constant<int, 4>{});
    else if (wave == 3) run(std::integral_constant<int, 2>{});
    else run(std::integral_constant<int, 3>{});
}

#undef STROLE
#define STROLE 8
__device__ __forceinline__ void h_role(const Params& P, int wave, int L, int i, int x) {
    const rsrc_t rp = mkrsrc(P.ws + PG0, PG_L);
    const char* xn = P.ws + PG0 + PG_XN + L * PG_LX;
    auto run = [&](auto Cc) {
    constexpr bool HI = decltype(Cc)::value;
    const int mt = (wave >> 1) & 1;
    WRegs wf;
    load_w3(wf, i < 16 ? P.w[2] : P.w[0], wave, (i < 16 ? i * 2 + HI : (i - 16) * 4 + HI * 2) * 384, i >= 16);
    PLACE();
    auto xdma = [&](int seg, int hb) {
        const unsigned l16 = lane_id() * 16;
        const char* g = xn + pgb(seg, par);
        dma4(g + blin(mt * 20480 + hb * 8192 + (wave & 1) * 4096), l16, L_XA + mt * 16384 + hb * 8192 + (wave & 1) * 4096);
        dmaq<0>(g + blin(mt * 20480 + 16384) + hb * 2048 + (wave & 1) * 1024, l16, L_XB + mt * 4096 + hb * 2048 + (wave & 1) * 1024);
    };
    const unsigned qs = (HI ? L_KX : L_QX) + (wave & 1) * 3072 + mt * 8;
    const unsigned xa = L_XA + wave * 2048 + lane_id() * 16, wl = L_W + wave * 10240 + lane_id() * 16;
    const unsigned fp = RG_FH + (i >> 4) * BLK + ((i & 15) * 4 + (wave & 3)) * 128;
    const unsigned fb = L ? RG_FX + (cpy & 3) * 2048 : RG_FV + (i & 3) * 2048, fa = L ? 0 : HAH;
    unsigned go = 0, all = 0;
    STDECL;
    LDECL;
    for (int t = HT0; t <= NSEG; ++t) {
        ST(0);
        if constexpr (HI) {
            const unsigned l16 = lane_id() * 16;
            if (t > HT0) {
                u32x4 v[6];
                const int w4 = wave - 4;
#pragma unroll
                for (int k = 0; k < 6; ++k) v[k] = lds_ld<u32x4>(L_QX + ((k >> 1) * 8 + w4 * 2 + (k & 1)) * 1024 + l16);
                const unsigned so = pgb(t - 1, par) + PG_HV + i * 3 * BLK + w4 * 2048;
#pragma unroll
                for (int k = 0; k < 6; ++k) st128(v[k], rp, l16 + (k & 1) * 1024, so + (k >> 1) * BLK);
                if (!all) {
                    wait_vm<0>();
                    if (l16 < 512) __builtin_amdgcn_raw_buffer_store_b32(t, rr, l16 >> 2, fp, 16);
                }
            }
            ST(1);
            if (t == NSEG) break;
            if (!all) bar();
            xdma(t, 1);
        } else {
            if (t == NSEG) break;
            if (!go) {
                for (int spins = 0;; ++spins) {
                    if (__all(__builtin_amdgcn_raw_buffer_load_b32(rr, (lane_id() & 15) * 128, fb, 16) + fa > (unsigned)t)) break;
                    __builtin_amdgcn_s_sleep(HSL);
                    if (spin_fail(spins, P)) break;
                }
                asm volatile("" ::: "memory");
                xdma(t, 0); wait_vm<0>();
            }
            if (!all) bar();
        }
        ST(2);
        C1Regs C;
        const WRegs& W = wf;
        ASM_ST_DECL;
        if constexpr (HI) {
            const unsigned l4 = lane_id() * 4;
            asm volatile(ASM_XG_H_HI : XG_OUT ASM_ST_OUT : XG_IN, [rr] "s"(rr), [fp] "s"(fp), [tv] "s"(t), [fv] "v"((all && l4 < 128) ? l4 : OOB) : XG_CLOB);
        } else {
            asm volatile(ASM_XG_H_LO : XG_OUT ASM_ST_OUT : XG_IN : XG_CLOB);
            if (t + 1 == XS0) init_wait(P, wave, 1);
            const unsigned fv = __builtin_amdgcn_raw_buffer_load_b32(rr, (lane_id() & 15) * 128, fb, 16) + fa;
            go = __builtin_amdgcn_readfirstlane((t + 1 < NSEG && __all(fv > (unsigned)t + 1)) ? 1u : 0u);
            if (go) xdma(t + 1, 0);
            lds_st<unsigned>(L_MEM + wave * 4, go);
            wait_vm<0>();
        }
        ASM_ST_SAVE(3);
        ST(4);
        if (!HI) { LB(t); LA(); }
        PMARK(L && wave == 7, 6, t);
        bar();
        const u32x4 gv = lds_ld<u32x4>(L_MEM);
        all = __builtin_amdgcn_readfirstlane(gv[0] & gv[1] & gv[2] & gv[3]);
        ST(5);
        STFLUSH(t);
    }
    LF();
    };
    if (wave < 4) run(std::false_type{}); else run(std::true_type{});
}

#undef STROLE
#define STROLE 2
__device__ __forceinline__ void v_role(const Params& P, int wave, int L, int h, int x) {
    WRegs wf;
    STDECL;
    auto runv = [&](auto Cc) {
        load_w3(wf, P.w[2], wave, h * 384, false);
        MARKX(!L && wave == 0, 6);
        PLACE();
        unsigned budget = 1u << 18;
        const unsigned wl = L_W + wave * 10240 + lane_id() * 16, dsto = c1_dst(wave, false);
        auto vfetch = [&](int seg, int tries) {
            const int ln = lane_id();
            for (int spins = 0; tries != -2; ++spins) {
                if (spins == tries) return false;
                const unsigned f = __builtin_amdgcn_raw_buffer_load_b32(rr, (ln & 3) * 128, RG_FH + (h >> 1) * 512, 16);
                if (__all(f > (unsigned)seg)) break;
                __builtin_amdgcn_s_sleep(4);
                if (spin_fail(spins, P)) break;
            }
            asm volatile("" ::: "memory");
            const char* g = P.ws + PG0 + pgb(seg, par) + PG_HV + (h >> 1) * 3 * BLK;
#pragma unroll
            for (int k = 0; k < 3; ++k) {
                const int f = (h & 1) * 12 + wave * 3 + k;
                dmaq<0>(g + (f >> 3) * BLK + (f & 7) * 1024, ln * 16, L_QX + (seg & 1) * 12288 + (wave * 3 + k) * 1024);
            }
            return true;
        };
#pragma unroll 1
        for (int t = 0; t < HT0; ++t) {
            if (L) xn_wait(P, rr, t, cpy);
            const unsigned l16 = lane_id() * 16;
            const char* g = P.ws + PG0 + PG_XN + L * PG_LX + pgb(t, par);
            dma4(g + blin(wave * 4096), l16, L_XA + wave * 4096); dma4(g + blin(20480 + wave * 4096), l16, L_XA + 16384 + wave * 4096);
            dmaq<0>(g + blin(16384) + wave * 1024, l16, L_XB + wave * 1024); dmaq<0>(g + blin(20480 + 16384) + wave * 1024, l16, L_XB + 4096 + wave * 1024);
            wait_vm<0>();
            bar();
            C1Regs C;
            const WRegs& W = wf;
            const unsigned xa = L_XA + wave * 2048 + l16, qs = L_QX + (wave & 1) * 3072 + ((wave >> 1) & 1) * 8 + t * 12288;
            asm volatile(ASM_XG_V : XG_OUT : XG_IN : XG_CLOB);
            if (t == 0) bar();
        }
        bool seen = false;
        unsigned tb2 = 0;
        for (int t = 0; t < NSEG; ++t) {
            if (t + 1 - V1A >= HT0 && t + 1 - V1A < NSEG) { vfetch(t + 1 - V1A, seen ? -2 : -1); wait_vm<0>(); }
            ST(7);
            bar();
            C1Regs C;
            asm volatile("ds_read_b128 v[200:203], %4\n\tds_read_b128 v[204:207], %4 offset:1024\n\tds_read_b128 v[228:231], %4 offset:2048\n\tds_read_b128 v[232:235], %4 offset:3072\n\t"
                         "v_mov_b64 v[224:225], 0\n\tv_mov_b64 v[226:227], 0\n\tv_mov_b64 v[244:245], 0\n\tv_mov_b64 v[246:247], 0\n\tv_mov_b64 v[248:249], 0\n\tv_mov_b64 v[250:251], 0\n\ts_waitcnt lgkmcnt(0)"
                         : XG_OUT : "v"(wl) : "memory");
            ST(6);
            while ((unsigned)__builtin_amdgcn_s_memtime() - tb2 < VTD) __builtin_amdgcn_s_sleep(1);
            unsigned fv = (lane_id() & 3) * 128;
            crit1<10 + decltype(Cc)::value>(wf, C, rr, RG_XM + (t & (NSLOT - 1)) * XM_SLOT + (cpy >> 1) * XM_PAIR + (cpy & 1) * BLK, wave, wl, dsto, budget, fv, RG_FH + (h >> 1) * 512 STARG);
            __builtin_amdgcn_s_setprio(0);
            bar();
            seen = __all(fv > (unsigned)t + 2 - V1A);
            tb2 = (unsigned)__builtin_amdgcn_s_memtime();
            ST(4);
            STC(5);
            STFLUSH(t);
        }
    };
    if (wave < 2) runv(std::integral_constant<int, 3>{});
    else if (wave < 4) runv(std::integral_constant<int, 2>{});
    else {
        const int w4 = wave - 4;
        const rsrc_t rd = mkrsrc(P.ws + PG0, PG_L);
        load_wo(wf, P.w[3], wave, h);
        PLACE();
        bar();
        bar();
        for (int t = -1; t < NSEG; ++t) {
            const unsigned slot = t & (NSLOT - 1);
            bar();
            ST(7);
#pragma unroll 1
            for (int n = V1A ? t + (t <= 0) : t + (t != 1) + (t == 0); n <= (V1A && t >= 0 ? t : t + 1) && n < NSEG; ++n) {
                const unsigned l16 = lane_id() * 16;
                const unsigned wl = L_W + wave * 10240 + l16, va = L_QX + (n & 1) * 12288 + l16;
                unsigned so = ((n & 7) << 22) + pgb(64 + (w4 >> 1), par) + ((w4 & 1) * 40 + (h >> 3)) * BLK + (h & 7) * 1024;
                asm volatile(ASM_VOX : [so] "+s"(so) : WREGS(wf), [l16] "v"(l16), [wl] "v"(wl), [va] "v"(va), [rd] "s"(rd) : "memory", "scc", "v200", "v201", "v202", "v203", "v204", "v205", "v206", "v207", "v208", "v209", "v210", "v211", "v212", "v213", "v214", "v215", "v216", "v217", "v218", "v219", "v220", "v221", "v222", "v223", "v224", "v225", "v226", "v227", "v228", "v229", "v230", "v231", "v232", "v233", "v234", "v235", "v236", "v237", "v238", "v239", "v240", "v241", "v242", "v243", "v244", "v245", "v246", "v247", "v248", "v249", "v250", "v251");
                if (V1A || n < 2) {
                    wait_vm<0>();
                    const unsigned l4 = lane_id() * 4;
                    if (l4 < 128) __builtin_amdgcn_raw_buffer_store_b32(n + 1, rr, l4, RG_FV + (w4 >> 1) * BLK + ((w4 & 1) * 32 + h) * 128, 16);
                }
            }
            if (t < 0) continue;
            ST(0);
            unsigned wl = L_W + wave * 10240 + lane_id() * 16;
            bf16x16 lw = lds_frag(wl, 1024);
            u32x32 cz;
            asm volatile("v_mov_b64 v[220:221], 0\n\tv_mov_b64 v[222:223], 0\n\tv_mov_b64 v[224:225], 0\n\tv_mov_b64 v[226:227], 0\n\tv_mov_b64 v[228:229], 0\n\tv_mov_b64 v[230:231], 0\n\tv_mov_b64 v[232:233], 0\n\tv_mov_b64 v[234:235], 0\n\tv_mov_b64 v[236:237], 0\n\tv_mov_b64 v[238:239], 0\n\tv_mov_b64 v[240:241], 0\n\tv_mov_b64 v[242:243], 0\n\tv_mov_b64 v[244:245], 0\n\tv_mov_b64 v[246:247], 0\n\tv_mov_b64 v[248:249], 0\n\tv_mov_b64 v[250:251], 0" : "={v[220:251]}"(cz));
            unsigned vo = ((wl >> 8) & 1) * (1 + (w4 & 1) * 2) * BLK + ((wl >> 9) & 1) * 1024 + (wl & 255);
            asm volatile("" : "+{v[212:219]}"(lw), "+{v[220:251]}"(cz), "+v"(wl), "+v"(vo));
            unsigned l16 = wl & 1023u;
            bar();
            wait_vm<0>();
            if (!V1A && l16 < 512 && t && t + 1 < NSEG) __builtin_amdgcn_raw_buffer_store_b32(t + 2, rr, l16 >> 2, RG_FV + (w4 >> 1) * BLK + ((w4 & 1) * 32 + h) * 128, 16);
            ST(1);
            const int sidx = (wl & 128u) ? 0xEEEE : 0x4444;
            unsigned so = RG_VM + slot * VM_SLOT + (unsigned)((w4 * 10 - (w4 & 1) + (h >> 4) * 2) * BLK + ((h >> 2) & 3) * 2048 + (h & 3) * 256);
            ST(3);
            ASM_ST_DECL;
            asm volatile(ASM_CRIT2 : [so] "+s"(so), "+{v[212:219]}"(lw), "+{v200}"(l16), "+{v[220:251]}"(cz) ASM_ST_OUT
                         : WREGS(wf), [wl] "v"(wl), [vo] "v"(vo), [sidx] "v"(sidx), [rr] "s"(rr)
                         : "memory", "scc", "v201", "v202", "v203", "v204", "v205", "v206", "v207", "v208", "v209", "v210", "v211");
            ASM_ST_SAVE(4);
            ST(5);
            STFLUSH(t);
        }
    }
}

#undef STROLE
#define STROLE 4
constexpr unsigned R_RED = 0;
constexpr unsigned R_PART = 16384;
__device__ __forceinline__ void r_role(const Params& P, int wave, int L, int r5, int x) {
    PLACE();
    const int jj = (r5 * 4 + cpy) ^ (r5 * 4 + x >= 6 ? 1 : 0);
    const int lane = lane_id(), l15 = lane & 15, l4 = lane >> 4;
    const unsigned l16 = lane * 16;
    const rsrc_t rd = mkrsrc(P.ws + PG0, PG_L);
    const int sidx = (l15 < 8) ? 0x4444 : 0xEEEE;
    unsigned offp[3];
    offp[0] = RG_XP + (cpy >> 1) * XP_PAIR + (cpy & 1) * BLK + (1 + (wave >> 2)) * 2 * BLK + (wave & 3) * 2048 + (PLAY ? (l4 >> 1) * 512 + (((l15 >> 2) & 1) + (l15 & 2)) * 128 + (l4 & 1) * 64 + (l15 >> 3) * 32 + (l15 & 1) * 16 : l16);
    offp[2] = (l15 < 8) ? RG_XP + (cpy >> 1) * XP_PAIR + (cpy & 1) * BLK + wave * 512 + l4 * 128 + l15 * 16 : OOB;
    const unsigned offv = RG_VM + ((jj >> 1) * 4 + (wave >> 2) * 2 + (jj & 1)) * BLK + (wave & 3) * 2048 + l16;
    const int j = jj * 2 + ((wave >> 1) & 1), fl = (wave & 1) * 32 + (lane & 31);
    const unsigned offo = RG_XM + (wave >> 2) * XM_PAIR + (lane >> 5) * BLK + (j & 31) * 256 + (j >> 5) * 2 * BLK + fl * 4;
    MARK(!L && jj == 0 && wave == 0, 5);
    {
        const int row = (lane >> 2) & 7, col = 16 * j + 4 * (lane & 3) + 2 * (wave & 1);
        __builtin_amdgcn_raw_buffer_store_b32(nosent(*(const unsigned*)(P.mem0 + row * DM + col)), rr, offo, 0, 16);
    }
    auto red_addr = [](int fl) { return R_RED + fl * 80 + (fl >> 4) * 16; };
    const unsigned rw0 = (lane >> 5) * 5248 + red_addr((((l15 >> 1) & 1) * 8 + 4 * (l4 & 1)) * 4 + (l15 >> 2)) + (l15 & 1) * 4 + (wave ^ (l15 >> 3)) * 8;
    const unsigned rrd = ((wave >> 1) & 1) * 5248 + red_addr(fl) + (lane >> 5) * 32;
    u32x32 vx;
    const unsigned fo = (lane & 3) * 128, fs = RG_FV + (jj / 10) * BLK + (((jj / 5) & 1) * 32 + wave * 4) * 128, vxo = pgb(64 + jj / 10, par) + (unsigned)((jj % 10) * 8 + (wave >> 1)) * BLK + (wave & 1) * 4096;
    const rsrc_t rp = mkrsrc(P.ws + PG0, PG_L);
    const unsigned xo = (l4 >> 1) * 256 + l15 * 16 + (l4 & 1) * 8, pw = R_PART + wave * 1024 + l16, pr = R_PART + (wave & 3) * 8192 + l16;
    const int fin = wave >> 2;
    asm volatile("" : "={v[128:159]}"(vx));
    auto vx_fetch = [&](int t) {
        for (int spins = 0;; ++spins) {
            const unsigned f = __builtin_amdgcn_raw_buffer_load_b32(rr, fo, fs, 16);
            if (__all(f > (unsigned)t)) break;
            __builtin_amdgcn_s_sleep(2);
            if (spin_fail(spins, P)) break;
        }
        const unsigned sv = ((t & 7) << 22) + vxo;
        asm volatile("buffer_load_dwordx4 v[128:131], %1, %2, %3 offen sc1\n\tbuffer_load_dwordx4 v[132:135], %1, %2, %3 offen offset:1024 sc1\n\t"
                     "buffer_load_dwordx4 v[136:139], %1, %2, %3 offen offset:2048 sc1\n\tbuffer_load_dwordx4 v[140:143], %1, %2, %3 offen offset:3072 sc1\n\t"
                     "buffer_load_dwordx4 v[144:147], %1, %2, %4 offen sc1\n\tbuffer_load_dwordx4 v[148:151], %1, %2, %4 offen offset:1024 sc1\n\t"
                     "buffer_load_dwordx4 v[152:155], %1, %2, %4 offen offset:2048 sc1\n\tbuffer_load_dwordx4 v[156:159], %1, %2, %4 offen offset:3072 sc1"
                     : "+{v[128:159]}"(vx) : "v"(l16), "s"(rp), "s"(sv), "s"(sv + 4 * BLK) : "memory");
    };
    vx_fetch(0);
    unsigned budget = 1u << 18, tpub = 0;
    STDECL;
    LDECL;
    for (int t = 0; t < NSEG; ++t) {
        const unsigned slot = t & (NSLOT - 1);
        ST(0);
        while ((unsigned)__builtin_amdgcn_s_memtime() - tpub < RTD) __builtin_amdgcn_s_sleep(1);
        u32x8 vm, pv;
        u32x4 pm;
        {
            unsigned a;
            ASM_ST_DECL;
            asm volatile(ASM_RCRIT : "=&{v[176:183]}"(vm), "=&{v[184:191]}"(pv), "=&{v[192:195]}"(pm), [a] "=&s"(a), [n] "+s"(budget) ASM_ST_OUT ASM_ST2_OUT ASM_ST3_OUT
                         : [rs] "s"(rr), [ov] "v"(offv), [sv] "s"(slot * VM_SLOT), [o0] "v"(offp[0]), [o2] "v"(offp[2]), [sp] "s"(slot * XP_SLOT),
                           "{v[128:159]}"(vx), [sidx] "v"(sidx), [rw] "v"(rw0), [rd] "v"(rrd), [oo] "v"(offo),
                           [so] "s"(((t + 1) & (NSLOT - 1)) * XM_SLOT), [pub] "s"(NSEG - 1 - t)
                         : "memory", "vcc", "scc", "v160", "v161", "v162", "v163", "v164", "v165", "v166", "v167", "v196", "v197", "v200", "v201", "v202", "v203", "v204", "v205", "v206", "v207");
            ASM_ST_SAVE(2); ASM_ST2_SAVE(1); ASM_ST3_SAVE(6);
        }
        ST(3);
        LA();
        MARK(!L && jj == 0 && wave == 0 && t < 4, 10 + t);
        PMARK(wave == 0, L * 7, t - 1 + L);
        tpub = (unsigned)__builtin_amdgcn_s_memtime();
        __builtin_amdgcn_s_sleep(RXDEL);
        unsigned early;
#pragma unroll 1
        for (int spins = 1023;; spins += 1024) {
            unsigned n;
            const unsigned sx = pgb(t, par) + blin(((wave >> 1) & 1) * 20480 + jj * 1024) + (wave & 1) * 512;
            const unsigned pxb = RG_PX + slot * PX_SLOT + wave * BLK, sv = (((t + 1) & 7) << 22) + vxo;
            ASM_ST_DECL;
            asm volatile(ASM_RX : "+{v[128:159]}"(vx), [n] "=&s"(n), [a] "=&s"(early) ASM_ST_OUT ASM_ST2_OUT
                         : "{v[176:183]}"(vm), "{v[184:191]}"(pv), "{v[192:195]}"(pm), [l16] "v"(l16), [rs] "s"(rr), [s0] "s"(pxb), [s1] "s"(pxb + 4096), [rd] "s"(rd), [rx] "s"(rd), [xo] "v"(xo),
                           [fo] "v"(fo), [fs] "s"(fs), [tn] "s"(t + 1), [sv0] "s"(sv), [sv1] "s"(sv + 4 * BLK), [rp] "s"(rp), [sx] "s"(PG_XR + L * PG_LX + sx), [sa] "s"(PG_XA + sx),
                           [pw] "v"(pw), [pr] "v"(pr), [fin] "s"(fin)
                         : "memory", "vcc", "scc", "v96", "v97", "v98", "v99", "v100", "v101", "v102", "v103", "v104", "v105", "v106", "v107", "v108", "v109", "v110", "v111", "v112", "v113", "v114", "v115", "v116", "v117", "v118", "v119", "v120", "v121", "v122", "v123", "v124", "v125", "v126", "v127", "v196", "v200", "v201", "v202", "v203", "v204", "v205", "v206", "v207", "v208", "v209", "v210", "v211", "v212", "v213", "v214", "v215", "v216", "v217", "v218", "v219", "v220", "v221", "v222", "v223", "v224", "v225", "v226", "v227", "v228", "v229", "v230", "v231", "v240", "v241", "v242", "v243", "v248", "v250", "v251");
            ASM_ST_SAVE(4); ASM_ST2_SAVE(5);
            if (__builtin_amdgcn_readfirstlane(n)) break;
            early = 0;
            if (spin_fail(spins, P)) { bar(); break; }
        }
        if (t + 1 < NSEG && !__builtin_amdgcn_readfirstlane(early)) vx_fetch(t + 1);
        PMARK(!L && wave == 7 && PMK == 1, 1, t - 1);
        st128(sent4(), rr, offv, slot * VM_SLOT); st128(sent4(), rr, offv + 1024, slot * VM_SLOT);
        __builtin_amdgcn_raw_buffer_store_b32(SENT, rr, offo, slot * XM_SLOT, 16);
        if (wave < 2 && t + AH < NSEG) st128(sent4(), rd, l16, PG_XA + pgb(t + AH, par) + blin(wave * 20480 + jj * 1024));
        ST(7);
        LB(t);
        STFLUSH(t);
    }
    LF();
}

#undef STROLE
#define STROLE 16
template <int WHICH>
__device__ __forceinline__ void w_role(const Params& P, int wave, int L, int cu, int x) {
    constexpr int KS = 20, N = (WHICH == 3) ? FF : DM, NU = 2 * NSEG;
    constexpr unsigned D_IN = WHICH == 1 ? PG_XA : WHICH == 2 ? PG_HS : PG_X1;
    constexpr unsigned D_OUT = WHICH == 1 ? PG_HS : WHICH == 2 ? PG_X1 : PG_H2;
    if (WHICH != 3 && cu * 8 + wave >= 20) {
        for (int u = 0; u <= NU; ++u) bar();
        return;
    }
    auto run = [&](auto Cc) {
    constexpr bool LO = decltype(Cc)::value;
    const int ln = lane_id(), l15 = ln & 15, l4 = ln >> 4;
    const unsigned l16 = ln * 16;
    const rsrc_t rd = mkrsrc(P.ws + PG0, PG_L);
    const int nt0 = (cu * 8 + wave) * 2;
    u32x32 w[5];
    {
        u32x4 f[2 * KS];
        load_wn<2, KS, N>(f, P.w[3 + WHICH], DM, wave, nt0, 0);
#pragma unroll
        for (int n = 0; n < 5; ++n) {
            u32x8 q[4];
#pragma unroll
            for (int i = 0; i < 4; ++i) q[i] = __builtin_shufflevector(f[n * 8 + 2 * i], f[n * 8 + 2 * i + 1], 0, 1, 2, 3, 4, 5, 6, 7);
            w[n] = cat32(q[0], q[1], q[2], q[3]);
        }
        asm volatile("" : "+{v[0:31]}"(w[0]), "+{v[32:63]}"(w[1]), "+{v[64:95]}"(w[2]), "+{v[96:127]}"(w[3]), "+{v[128:159]}"(w[4]));
    }
    PLACE();
    bar();
    const int ocol = nt0 * 16 + (l4 & 1) * 16 + (l4 >> 1) * 8;
    const unsigned o3 = (unsigned)((l4 & 1) * 512 + (l4 >> 1) * 256 + l15 * 16), ro = (l4 >> 1) * 256 + l15 * 16 + (l4 & 1) * 8;
    const int grp = nt0 / 10, sh = ((grp & 1) ? -1 : 1) * ((grp >> 1) + 1);
    if (WHICH == 1) {
        unsigned zv;
        asm volatile("v_mov_b32 %0, 0" : "=v"(zv));
        st128(((u32x4){zv, zv, zv, zv}), rd, l15 <= (grp >> 1) ? toff((grp & 1) ? S_ - 1 - l15 : l15, ocol, par) : OOB, PG_HS);
    }
    auto in0 = [&](int u) { return D_IN + pgb(u >> 1, par) + blin((u & 1) * 20480 + wave * 4096); };
    auto in1 = [&](int u) { return D_IN + pgb(u >> 1, par) + blin((u & 1) * 20480 + 16384) + wave * 1024; };
    u32x16 va;
    u32x4 vb;
    u32x8 cs, k8;
#define WT_W "{v[0:31]}"(w[0]), "{v[32:63]}"(w[1]), "{v[64:95]}"(w[2]), "{v[96:127]}"(w[3]), "{v[128:159]}"(w[4])
    asm volatile("v_mov_b32 v216, 0x3f803f80\n\tv_mov_b32 v217, v216\n\tv_mov_b32 v218, v216\n\tv_mov_b32 v219, v216\n\tv_mov_b32 v220, %[c0]\n\tv_mov_b32 v232, -1\n\tv_mov_b32 v236, -1\n\t"
                 "v_mov_b32 v240, -1\n\tv_mov_b32 v244, -1\n\tv_mov_b32 v248, -1\n\t" ASM_WCS
                 : "=&{v[196:203]}"(cs), "=&{v[216:223]}"(k8), "=&{v[232:247]}"(va), "=&{v[248:251]}"(vb) : WT_W, [c0] "s"(-2.0f * 0.7978845608028654f * 1.4426950408889634f));
    unsigned la = l16, lw = wave * 8192 + l16, sr = l4 * 8192 + ((l15 >> 2) * 16 + l15) * 16 + (l15 & 3) * 4, budget = 1u << 18;
    wait_vm<0>();
    STDECL;
    LDECL;
#pragma unroll 1
    for (int u = 0; u < NU; ++u) {
        const int mt = u & 1;
        const unsigned pg = pgb(u >> 1, par), c3 = D_OUT + blin(mt * (N * 32) + (nt0 >> 1) * 1024);
        unsigned oa = o3, ob = 0, so = pg + c3, sa = u + 2 * AH < NU ? pgb((u >> 1) + AH, par) + c3 : pg + PG_SS + 4096, a;
        if (WHICH == 1) {
            const int orow = u * 16 + l15 + sh;
            ob = toff(orow + AH * SEG, ocol, par);
            oa = (unsigned)orow < (unsigned)S_ ? (ob ^ 8192u) - (AH << 21) : OOB;
            if (orow + AH * SEG >= S_ + (sh < 0 ? sh : 0)) ob = OOB;
            so = sa = D_OUT;
        }
        if (mt) { ST(0); LB(u >> 1); } else { LA(); }
        ASM_ST_DECL;
#define WT_A WT_W, [la] "v"(la), [l16] "v"(l16), [oa] "v"(oa), [rs] "s"(rd), [so] "s"(so), [sa] "s"(sa)
#define WT_G [sr] "v"(sr), [kd] "s"(1.0f / DM), [ke] "s"(1e-5f), [k1] "s"(-2.0f * 0.7978845608028654f * 1.4426950408889634f * 0.044715f)
#define WT_1 WT_G, [ob] "v"(ob), [ss] "s"(pg + PG_SS + mt * 128), [sw] "s"(nt0)
#define WT_2 [ro] "v"(ro), [sx] "s"(pg + PG_XA + blin(mt * 20480 + nt0 * 512)), [ss] "s"(pg + PG_SS + mt * 128)
#define WT_C "memory", "vcc", "scc", "v160", "v161", "v162", "v163", "v164", "v165", "v166", "v167", "v168", "v169", "v170", "v171", "v172", "v173", "v174", "v175", "v176", "v177", "v178", "v179", "v180", "v181", "v182", "v183", "v184", "v185", "v186", "v187", "v188", "v189", "v190", "v191", "v192", "v193", "v194", "v195", "v204", "v205", "v206", "v207", "v208", "v209", "v210", "v211", "v212", "v213", "v214", "v215", "v252"
#define WT_LO(A, ...) asm volatile(A : "+{v[196:203]}"(cs), "+{v[216:223]}"(k8), "+{v[232:247]}"(va), "+{v[248:251]}"(vb), [n] "+s"(budget), [a] "=&s"(a) ASM_ST_OUT ASM_ST2_OUT ASM_ST3_OUT \
                              : WT_A, __VA_ARGS__, [lw] "v"(lw), [i0] "s"(in0(u)), [i1] "s"(in1(u)), [n0] "s"(in0(u + 1)), [n1] "s"(in1(u + 1)) : WT_C)
#define WT_HI(A, ...) asm volatile(A : "+{v[196:203]}"(cs), "+{v[216:223]}"(k8) ASM_ST_OUT ASM_ST3_OUT : WT_A, __VA_ARGS__ : WT_C)
        if constexpr (LO) { if constexpr (WHICH == 1) WT_LO(ASM_WT1_LO, WT_1); else if constexpr (WHICH == 2) WT_LO(ASM_WT2_LO, WT_2); else WT_LO(ASM_WT3_LO, WT_G); }
        else { if constexpr (WHICH == 1) WT_HI(ASM_WT1_HI, WT_1); else if constexpr (WHICH == 2) WT_HI(ASM_WT2_HI, WT_2); else WT_HI(ASM_WT3_HI, WT_G); }
        la ^= 32768u; lw ^= 32768u; sr ^= 32768u;
        PMARK(!L && mt == (WHICH != 1) && (PMK == 1 || WHICH == 3), PMK == 1 ? 1 + WHICH : 1, (u >> 1) - (WHICH == 1));
    }
    wait_vm<0>();
    if constexpr (LO) asm volatile("" :: "{v[232:247]}"(va), "{v[248:251]}"(vb));
    LF();
    };
    if (wave < 4) run(std::true_type{}); else run(std::false_type{});
}

__device__ __forceinline__ void w4_role(const Params& P, int wave, int L, int cu, int x) {
    constexpr unsigned W_FL = 122880;
    const int ln = lane_id(), l15 = ln & 15, l4 = ln >> 4;
    const unsigned l16 = ln * 16;
    const rsrc_t rd = mkrsrc(P.ws + PG0, PG_L);
    lds_st<unsigned>(W_FL + wave * 4, 0u);
    STDECL;
    if (wave < 6) {
        u32x32 w[5];
        {
            u32x4 g[40];
            load_wn<5, 8, DM>(g, P.w[7], FF, wave, cu * 5, wave * 8);
#pragma unroll
            for (int n = 0; n < 5; ++n) {
                u32x8 f[4];
#pragma unroll
                for (int i = 0; i < 4; ++i) f[i] = __builtin_shufflevector(g[n * 8 + 2 * i], g[n * 8 + 2 * i + 1], 0, 1, 2, 3, 4, 5, 6, 7);
                w[n] = cat32(f[0], f[1], f[2], f[3]);
            }
            asm volatile("" : "+{v[0:31]}"(w[0]), "+{v[32:63]}"(w[1]), "+{v[64:95]}"(w[2]), "+{v[96:127]}"(w[3]), "+{v[128:159]}"(w[4]));
        }
        PLACE();
        bar();
        unsigned budget = 1u << 18;
        u32x32 av;
        asm volatile("v_mov_b32 v160, -1\n\tv_mov_b32 v164, -1\n\tv_mov_b32 v168, -1\n\tv_mov_b32 v172, -1\n\tv_mov_b32 v176, -1\n\tv_mov_b32 v180, -1\n\tv_mov_b32 v184, -1\n\tv_mov_b32 v188, -1" : "={v[160:191]}"(av));
        for (int u = 0; u < 2 * NSEG; ++u) {
            if (u & 1) ST(0);
            for (int spins = 0;; ++spins) {
                asm volatile("" ::: "memory");
                if ((int)__builtin_amdgcn_readfirstlane(lds_ld<unsigned>(W_FL + 24 + (u & 1) * 4)) + 3 >= u) break;
                __builtin_amdgcn_s_sleep(2);
                if (spin_fail(spins, P)) break;
            }
            const unsigned gb = PG_H2 + pgb(u >> 1, par) + ((u & 1) * 6 + wave) * BLK, gn = PG_H2 + pgb((u + 1) >> 1, par) + ((~u & 1) * 6 + wave) * BLK;
            ASM_ST_DECL;
            asm volatile(ASM_W4_M : [n] "+s"(budget), "+{v[160:191]}"(av) ASM_ST2_OUT
                         : "{v[0:31]}"(w[0]), "{v[32:63]}"(w[1]), "{v[64:95]}"(w[2]), "{v[96:127]}"(w[3]), "{v[128:159]}"(w[4]),
                           [ld] "v"(l16 + wave * 1024 + (u & 3) * 30720), [l16] "v"(l16), [rs] "s"(rd), [s0] "s"(gb), [s1] "s"(gb + 4096), [n0] "s"(gn), [n1] "s"(gn + 4096), [un] "s"(u + 1), [fl] "v"(W_FL + wave * 4)
                         : "memory", "vcc", "scc", "v192", "v193", "v194", "v195", "v196", "v197", "v198", "v199", "v200", "v201", "v202", "v203", "v204", "v205", "v206", "v207", "v208", "v209", "v210", "v211", "v212");
            PMARK(!L && (u & 1) && PMK == 2, 2, u >> 1);
        }
        asm volatile("s_waitcnt vmcnt(0)" :: "{v[160:191]}"(av) : "memory");
    } else {
        const int mt = wave - 6;
        PLACE();
        bar();
        const unsigned c0 = mt * 20480 + cu * 2560, o8 = (l4 >> 1) * 256 + l15 * 16 + (l4 & 1) * 8;
        unsigned budget = 1u << 18;
        u32x16 ra;
        u32x4 rb;
        asm volatile("v_mov_b32 v164, -1" : "={v[164:179]}"(ra), "={v[180:183]}"(rb));
        __builtin_amdgcn_s_setprio(W4P);
#define W4F_OUT "+{v[164:179]}"(ra), "+{v[180:183]}"(rb), [n] "+s"(budget)
#define W4F_IN [o0] "v"(o8 + blin(c0)), [o1] "v"(o8 + blin(c0 + 512)), [o2] "v"(o8 + blin(c0 + 1024)), [o3] "v"(o8 + blin(c0 + 1536)), [o4] "v"(o8 + blin(c0 + 2048)), [rd] "s"(rd), \
               [c1] "s"(PG_X1 + pg), [ca] "s"(PG_XA + pg), [n1] "s"(PG_X1 + pn), [na] "s"(PG_XA + pn), [f0] "v"(W_FL + (ln & 3) * 4), [f1] "v"(W_FL + 16 + (ln & 1) * 4), [fd] "v"(W_FL + wave * 4), \
               [la] "v"((u & 3) * 30720 + l16), [u] "s"(u), [un] "s"(u + 1)
#define W4F_CLOB "memory", "vcc", "scc", "v64", "v65", "v66", "v67", "v68", "v69", "v70", "v71", "v72", "v73", "v74", "v75", "v76", "v77", "v78", "v79", "v80", "v81", "v82", "v83", "v84", "v85", "v86", "v87", "v88", "v89", "v90", "v91", "v92", "v93", "v94", "v95", "v96", "v97", "v98", "v99", "v100", "v101", "v102", "v103", "v104", "v105", "v106", "v107", "v108", "v109", "v110", "v111", "v112", "v113", "v114", "v115", "v116", "v117", "v118", "v119", "v120", "v121", "v122", "v123", "v124", "v125", "v126", "v127", "v128", "v129", "v130", "v131", "v132", "v133", "v134", "v135", "v136", "v137", "v138", "v139", "v140", "v141", "v142", "v143", "v144", "v145", "v146", "v147", "v148", "v149", "v150", "v151", "v152", "v153", "v154", "v155", "v156", "v157", "v158", "v159", "v160", "v161", "v162", "v163", "v184", "v185", "v186", "v187", "v188", "v189", "v190", "v191", "v192", "v193", "v194", "v195", "v196", "v197", "v198", "v199", "v200"
        if (L) {
            const rsrc_t ro = mkrsrc(P.out, ACT);
#pragma unroll 1
            for (int t = 0; t < NSEG; ++t) {
                const unsigned u = t * 2 + mt, pg = pgb(t, par), pn = pgb(t + 1, par);
                ASM_ST_DECL;
                asm volatile(ASM_W4F_1 : W4F_OUT ASM_ST2_OUT : W4F_IN, [pa] "v"(l15 * (DM * 2) + (l4 & 1) * 32 + (l4 >> 1) * 16), [pb] "v"(l15 * (DM * 2) + l4 * 8), [ro] "s"(ro), [so] "s"(u * 20480 + cu * 160) : W4F_CLOB);
            }
        } else {
            const rsrc_t rn = mkrsrc(P.ws + (par ^ 1) * 8192, RG_L);
#pragma unroll 1
            for (int t = 0; t < NSEG; ++t) {
                const unsigned u = t * 2 + mt, pg = pgb(t, par), pn = pgb(t + 1, par), sn = (pg ^ 8192) + PG_LX, st = RG_ST + (u >> 3) * BLK + (u & 7) * 1024;
                ASM_ST_DECL;
                asm volatile(ASM_W4F_0 : W4F_OUT ASM_ST_OUT ASM_ST2_OUT ASM_ST3_OUT
                             : W4F_IN, [pa] "v"(blin(c0 + (l4 & 1) * 512) + (l4 >> 1) * 256 + l15 * 16), [pb] "v"(blin(c0 + 1024 + (l4 & 1) * 512) + (l4 >> 1) * 256 + l15 * 16),
                               [so] "v"(ln < 16 ? ln * 8 : OOB), [si] "v"(ln * 8), [rr] "s"(rr), [sp] "s"(st + cu * 128), [sq] "s"(st), [xr] "s"(PG_XR + sn), [xn] "s"(PG_XN + sn),
                               [kd] "s"(1.0f / DM), [ke] "s"(1e-5f), [tn] "s"(t + 1), [fo] "v"(ln < 32 ? (ln >> 3) * 2048 + (ln & 7) * 16 : OOB), [rn] "s"(rn), [fs] "s"(RG_FX + (cu * 2 + mt) * 128) : W4F_CLOB);
            }
        }
        asm volatile("s_waitcnt vmcnt(0)" :: "{v[164:179]}"(ra), "{v[180:183]}"(rb) : "memory");
    }
}

__device__ __forceinline__ unsigned probe(char* ws, unsigned base, unsigned bit, int n) {
    const rsrc_t r = mkrsrc(ws, RG_SLOT);
    unsigned m0 = ~0u, m1 = ~0u;
    for (int i = 0; i < n; ++i) {
        const unsigned long long t0 = __builtin_amdgcn_s_memtime();
        __builtin_amdgcn_raw_buffer_store_b32(0u, r, 96 * BLK + base + (i & 1) * bit + ((i >> 2) & 1) * 4096 + (i >> 2) * 768, 0, 16);
        asm volatile("s_waitcnt vmcnt(0)" ::: "memory");
        const unsigned d = (unsigned)(__builtin_amdgcn_s_memtime() - t0);
        if (i & 1) m1 = d < m1 ? d : m1; else m0 = d < m0 ? d : m0;
    }
    return m0 > m1 ? 1u : 0u;
}
__device__ __forceinline__ void setup(const Params& P, int wave, int mt, int par0, int ph) {
    const bool xw = wave < 4 && (mt >= 2 * XS0) == (ph != 0);
    const int tid = threadIdx.x & 255, r15 = tid & 15, slot = tid >> 4;
    const rsrc_t rw = mkrsrc(P.ws, WS_END);
    u32x4 v[5];
    float s = 0.f, q = 0.f;
    if (xw) {
#pragma unroll
        for (int c = 0; c < 5; ++c) v[c] = *(const u32x4*)(P.x + (size_t)(mt * 16 + r15) * DM + (slot * 5 + c) * 8);
#pragma unroll
        for (int c = 0; c < 5; ++c)
#pragma unroll
            for (int e = 0; e < 4; ++e) s += lo2f(v[c][e]) + hi2f(v[c][e]);
        lds_st<float>((slot * 17 + r15) * 4, s);
    } else if (wave >= 4) {
        constexpr unsigned N0 = RG_ST / BLK, N1 = 4 * N0 + 16, N2 = PG_SS / BLK;
        const u32x4 s4 = sent4();
        for (unsigned k = mt; k < 2 * (N1 + 4 * N2); k += 128) {
            const unsigned i = k >> 1;
            const unsigned o = i < N1 ? (i < 4 * N0 ? (i / N0) * RG_SLOT + (i % N0) * BLK : (i - 3 * N0) * BLK) + (par0 ^ ph) * 8192 : PG0 + pgb((i - N1) / N2, par0 ^ ph) + ((i - N1) % N2) * BLK;
            st128(s4, rw, tid * 16, o + (k & 1) * 4096);
        }
    }
    bar();
    float mean = 0.f;
    if (xw) {
        s = 0.f;
#pragma unroll
        for (int k = 0; k < 16; ++k) s += lds_ld<float>((k * 17 + r15) * 4);
        mean = s * (1.0f / DM);
#pragma unroll
        for (int c = 0; c < 5; ++c)
#pragma unroll
            for (int e = 0; e < 4; ++e) { const float a = lo2f(v[c][e]) - mean, b = hi2f(v[c][e]) - mean; q += a * a + b * b; }
        lds_st<float>(2048 + (slot * 17 + r15) * 4, q);
    }
    bar();
    if (xw) {
        q = 0.f;
#pragma unroll
        for (int k = 0; k < 16; ++k) q += lds_ld<float>(2048 + (k * 17 + r15) * 4);
        const float m1 = rbf(mean), r = 1.0f / ln_den(q * (1.0f / DM));
#pragma unroll
        for (int c = 0; c < 5; ++c) {
            const unsigned o = toff(mt * 16 + r15, (slot * 5 + c) * 8, par0);
            st128(v[c], rw, o, PG0 + PG_XR);
            u32x4 n;
#pragma unroll
            for (int e = 0; e < 4; ++e) n[e] = pack2(rbf(lo2f(v[c][e]) - m1) * r, rbf(hi2f(v[c][e]) - m1) * r);
            st128(n, rw, o, PG0 + PG_XN);
        }
    }
    wait_vm<0>();
    bar();
    if (threadIdx.x < 8) __hip_atomic_fetch_add((unsigned*)(P.ws + FL0 + FL_IN + (ph * 8 + threadIdx.x) * 128), 1u, RLX_AGENT);
}

__global__ __launch_bounds__(512, 2) void mega_kernel(Params P) {
    const int wave = __builtin_amdgcn_readfirstlane(threadIdx.x >> 6);
    u32x4 cpf;
    {
        unsigned long long pc;
        unsigned len;
        asm volatile("s_getpc_b64 %0\n\ts_mov_b32 %1, _Z12prep1_kernel5Prep1-_Z11mega_kernel6Params" : "=s"(pc), "=s"(len));
        cpf = __builtin_amdgcn_raw_buffer_load_b128(mkrsrc((const void*)(pc & ~255ull), len), (blockIdx.x >> 3) * 8192 + threadIdx.x * 16, 0, 0);
    }
    if (threadIdx.x == 0) {
        unsigned g = (unsigned)__builtin_amdgcn_s_getreg((3 << 11) | 20) & 7u, k;
        while ((k = __hip_atomic_fetch_add((unsigned*)(P.ws + FL0 + FL_TK + g * 128), 1u, RLX_AGENT)) >= 32u) g = (g + 1) & 7u;
        lds_st<unsigned>(0, g << 5 | k);
    }
    bar();
    const unsigned tk = __builtin_amdgcn_readfirstlane(lds_ld<unsigned>(0));
    asm volatile("" :: "v"(cpf));
    bar();
    const int g4 = (tk >> 5) & 3, k = tk & 31;
    const int L = tk >> 7, x = g4, r = k;
    if (L) {
        const int par0 = (init_wait(P, wave, 1, 0u) >> 16) & 1;
        if (tk == 255 && wave == 0) {
            const unsigned xc = (unsigned)__builtin_amdgcn_s_getreg((3 << 11) | 20);
            const unsigned f = (probe(P.ws, ((par0 ^ (xc >> 2)) & 1) * 8192, BLK, CAL2) ^ 1u ^ xc) & 1u;
            if (threadIdx.x < 16) __hip_atomic_fetch_add((unsigned*)(P.ws + FL0 + FL_IN + threadIdx.x * 128), (2u + f) << 20, RLX_AGENT);
        }
#pragma unroll 1
        for (int ph = 0; ph < 2; ++ph) {
            setup(P, wave, (tk & 31) * 4 + g4, par0, ph);
            for (int k = ph * DLY_L; k < DLY_L; ++k) __builtin_amdgcn_s_sleep(32);
        }
    }
    {
        const int i = (r - 26) * 4 + x;
        const int d = (r < 8 ? DLY_S : r < 21 ? 0 : i < 4 ? DLY_H : DLY_W);
        for (int k = 0; k < d; ++k) __builtin_amdgcn_s_sleep(32);
    }
    if (r < 8) s_role(P, wave, L, x * 8 + r, x);
    else if (r < 16) v_role(P, wave ^ 4, L, x * 8 + r - 8, x);
    else if (r < 21) r_role(P, wave, L, r - 16, x);
    else {
        const int i = (r - 26) * 4 + x;
        if (i < 4) h_role(P, wave, L, r < 25 ? x * 4 + r - 21 : 16 + x * 2 + r - 25, x);
        else if (i < 7) w_role<1>(P, wave, L, i - 4, x);
        else if (i < 10) w_role<2>(P, wave, L, i - 7, x);
        else if (i < 16) w_role<3>(P, wave, L, i - 10, x);
        else w4_role(P, wave, L, i - 16, x);
    }
}

struct Prep1 { char* ws; };
__global__ __launch_bounds__(256) void prep1_kernel(Prep1 A) {
    int b = blockIdx.x;
    if (b == 0) {
        if (threadIdx.x >= 64) return;
        const unsigned h = (probe(A.ws, 0, 8192, CAL1) ^ ((unsigned)__builtin_amdgcn_s_getreg((3 << 11) | 20) >> 2)) & 1u;
        if (threadIdx.x < 16) *(unsigned*)(A.ws + FL0 + FL_IN + threadIdx.x * 128) = (2u + h) << 16;
        return;
    }
    b -= 1;
    ((u32x4*)(A.ws + RG_FV))[b * 256 + threadIdx.x] = (u32x4){0u, 0u, 0u, 0u};
    if (b == 0 && threadIdx.x < FL_IN / 16) ((u32x4*)(A.ws + FL0))[threadIdx.x] = (u32x4){0u, 0u, 0u, 0u};
}

extern "C" void kernel_launch(void* const* d_in, const int* in_sizes, int n_in, void* d_out, int out_size, void* d_ws,
                              size_t ws_size, hipStream_t stream) {
    char* ws = (char*)d_ws;
    Params P;
    P.ws = ws; P.x = (const bf16_t*)d_in[0]; P.mem0 = (const bf16_t*)d_in[1]; P.out = (bf16_t*)d_out;
    for (int i = 0; i < 8; ++i) P.w[i] = (const bf16_t*)d_in[i < 4 ? 2 + i : 4 + i];
    (void)hipFuncSetAttribute((const void*)mega_kernel, hipFuncAttributeMaxDynamicSharedMemorySize, MEGA_LDS);

    Prep1 A1;
    A1.ws = ws;
    prep1_kernel<<<21, 256, 0, stream>>>(A1);
    mega_kernel<<<256, 512, MEGA_LDS, stream>>>(P);
}
```

```cpp
#define HM_L 0
#define HM_T 99
#define HM_C 0
#define HM_H 0
#include <hip/hip_runtime.h>
#include <stdint.h>
#include <type_traits>

typedef unsigned short bf16_t;
using bf16x2 = __attribute__((ext_vector_type(2))) __bf16;
using bf16x8 = __attribute__((ext_vector_type(8))) __bf16;
using bf16x16 = __attribute__((ext_vector_type(16))) __bf16;
using f32x2 = __attribute__((ext_vector_type(2))) float;
using f32x4 = __attribute__((ext_vector_type(4))) float;
using u32x2 = __attribute__((ext_vector_type(2))) unsigned;
using u32x4 = __attribute__((ext_vector_type(4))) unsigned;
typedef __amdgpu_buffer_rsrc_t rsrc_t;

__device__ __forceinline__ unsigned pack2(float a, float b) {
    bf16x2 r = __builtin_convertvector((f32x2){a, b}, bf16x2);
    return __builtin_bit_cast(unsigned, r);
}
__device__ __forceinline__ float rbf(float f) { return __uint_as_float(pack2(f, 0.f) << 16); }
__device__ __forceinline__ float lo2f(unsigned u) { return __uint_as_float(u << 16); }
__device__ __forceinline__ float hi2f(unsigned u) { return __uint_as_float(u & 0xFFFF0000u); }
__device__ __forceinline__ rsrc_t mkrsrc(const void* p, unsigned bytes) {
    return __builtin_amdgcn_make_buffer_rsrc((void*)p, 0, bytes, 0x00020000);
}

constexpr int S_ = 2048, DM = 640, NH = 32, DH = 192, HD = NH * DH  , SEG = 32, NSEG = 64;
constexpr int FF = 1536;

#define XMAP
#define NCPY 4
constexpr unsigned NSLOT = 4, NCP = NCPY;
#define FL_TK_DEF
#define DLY_L 12
#define DLY_S 1
#define DLY_W 10
#define DLY_H 2
#define EWAKE "s_nop 0"
#define LWAKE "s_wakeup\n\t"
#define SLP4 (wave == 4 ? t : 0)
#define WPF 12
#define WPRI 1
#define WPRI2 0
#define RTD 3500
#define VTD 3900
#define HAH 100
#define WEAK 0
#define BAL 0
#define HSL 8
#define OAUX 2
#define W4P 3
#define STD 3800
#define CAL1 4
#define CAL2 12
#define CALN 12
#define RSL 9
#define VTRY 0
#define VSL 1
#define NRR 5
constexpr unsigned BLK = 16384, RG_SLOT = 4u << 20;
constexpr unsigned XP_PAIR = 6 * BLK, XP_SLOT = RG_SLOT;
constexpr unsigned XM_PAIR = 4 * BLK, XM_SLOT = RG_SLOT;
constexpr unsigned VM_SLOT = RG_SLOT;
constexpr unsigned PX_SLOT = RG_SLOT;
constexpr unsigned RG_XP = 0, RG_XM = 12 * BLK, RG_VM = 20 * BLK, RG_PX = 60 * BLK;
constexpr unsigned RG_ST = 68 * BLK, RG_FV = 84 * BLK, RG_FH = 86 * BLK, RG_FX = 88 * BLK, RG_L = NSLOT * RG_SLOT;
constexpr unsigned FL0 = 89 * BLK, FL_TK = 512, FL_IN = FL_TK + 8 * 128, FL_BYTES = FL_IN + 16 * 128;
constexpr unsigned ACT = S_ * DM * 2;
#define PXROT 5
#define W4SL 5
#define RXDEL 1
#define VXD 32
#define VXAUX 16
constexpr unsigned PG0 = RG_L, PG_L = 80u << 21;
constexpr unsigned PG_XA = 0, PG_HS = 5 * BLK, PG_X1 = 10 * BLK, PG_H2 = 15 * BLK, PG_SS = 27 * BLK, PG_XR = 28 * BLK, PG_XN = 33 * BLK, PG_LX = 10 * BLK, PG_HV = 48 * BLK;
__device__ __forceinline__ unsigned pgb(unsigned p, int par) { return p << 21 | ((p ^ par) & 1) << 13; }
__device__ __forceinline__ constexpr unsigned blin(unsigned o) { return o + (o & ~8191u); }
constexpr int HT0 = 2;
constexpr int AH = 3;
constexpr unsigned WS_END = PG0 + PG_L;

#define SB() __builtin_amdgcn_sched_barrier(0)
#define LAS __attribute__((address_space(3)))
template <class T> __device__ __forceinline__ T lds_ld(unsigned a) { return *(const LAS T*)(a); }
template <class T> __device__ __forceinline__ void lds_st(unsigned a, T v) { *(LAS T*)(a) = v; }
constexpr unsigned OOB = 0x7FFFFF00u;
template <int N> __device__ __forceinline__ void wait_vm() { asm volatile("s_waitcnt vmcnt(%0)" :: "i"(N) : "memory"); }
__device__ __forceinline__ void bar() {
    asm volatile("s_waitcnt lgkmcnt(0)" ::: "memory");
    __builtin_amdgcn_s_barrier();
    asm volatile("" ::: "memory");
}
__device__ __forceinline__ int lane_id() {
    int ln;
    asm volatile("v_mbcnt_lo_u32_b32 %0, -1, 0\n\tv_mbcnt_hi_u32_b32 %0, -1, %0" : "=v"(ln));
    return ln;
}
__device__ __forceinline__ void swap16(unsigned& a, unsigned& b) { asm volatile("s_nop 3\n\tv_permlane16_swap_b32 %0, %1\n\ts_nop 1" : "+v"(a), "+v"(b)); }
__device__ __forceinline__ void swap32(unsigned& a, unsigned& b) { asm volatile("s_nop 3\n\tv_permlane32_swap_b32 %0, %1\n\ts_nop 1" : "+v"(a), "+v"(b)); }
__device__ __forceinline__ float sum_x16(float x) { unsigned a = __float_as_uint(x), b = a; swap16(a, b); return __uint_as_float(a) + __uint_as_float(b); }
__device__ __forceinline__ float sum_x32(float x) { unsigned a = __float_as_uint(x), b = a; swap32(a, b); return __uint_as_float(a) + __uint_as_float(b); }
__device__ __forceinline__ float max_x16(float x) { unsigned a = __float_as_uint(x), b = a; swap16(a, b); return __builtin_fmaxf(__uint_as_float(a), __uint_as_float(b)); }
__device__ __forceinline__ float max_x32(float x) { unsigned a = __float_as_uint(x), b = a; swap32(a, b); return __builtin_fmaxf(__uint_as_float(a), __uint_as_float(b)); }
__device__ __forceinline__ void sum2_rows(float& s, float& q) {
    unsigned a = __float_as_uint(s), b = a, c = __float_as_uint(q), d = c;
    asm volatile("s_nop 2\n\tv_permlane16_swap_b32 %0, %1\n\tv_permlane16_swap_b32 %2, %3\n\tv_add_f32 %0, %0, %1\n\tv_add_f32 %2, %2, %3\n\tv_mov_b32 %1, %0\n\tv_mov_b32 %3, %2\n\t"
                 "s_nop 1\n\tv_permlane32_swap_b32 %0, %1\n\tv_permlane32_swap_b32 %2, %3\n\tv_add_f32 %0, %0, %1\n\tv_add_f32 %2, %2, %3" : "+v"(a), "+v"(b), "+v"(c), "+v"(d));
    s = __uint_as_float(a); q = __uint_as_float(c);
}
__device__ __forceinline__ u32x4 merge16(u32x2 a, u32x2 b) {
    unsigned a0 = a[0], a1 = a[1], b0 = b[0], b1 = b[1];
    asm volatile("s_nop 3\n\tv_permlane16_swap_b32 %0, %2\n\tv_permlane16_swap_b32 %1, %3\n\ts_nop 1" : "+v"(a0), "+v"(a1), "+v"(b0), "+v"(b1));
    return (u32x4){a0, a1, b0, b1};
}
template <int AUXV>
__device__ __forceinline__ void st128_(u32x4 v, rsrc_t r, unsigned voff, unsigned soff) {
    __builtin_amdgcn_raw_buffer_store_b128(v, r, voff, soff, AUXV);
    asm volatile("s_nop 1" :: "v"(v) : "memory");
}
#define st128(V, R, VO_, SO_) st128_<16>(V, R, VO_, SO_)
#define LD128(R, VO_, SO_) __builtin_amdgcn_raw_buffer_load_b128(R, VO_, SO_, 16)
#define LD64(R, VO_, SO_) __builtin_amdgcn_raw_buffer_load_b64(R, VO_, SO_, 16)
#define BF8(X) __builtin_bit_cast(bf16x8, X)
__device__ __forceinline__ float gelu_tanh(float x) {
    const float c0 = -2.0f * 0.7978845608028654f * 1.4426950408889634f, c1 = c0 * 0.044715f;
    const float e = __builtin_amdgcn_exp2f(x * __builtin_fmaf(x * x, c1, c0));
    return x * __builtin_amdgcn_rcpf(1.0f + e);
}
#define MFMA(A_, B_, C_) C_ = __builtin_amdgcn_mfma_f32_16x16x32_bf16(A_, B_, C_, 0, 0, 0)
#define SMF(A_, B_, C_) C_ = __builtin_amdgcn_smfmac_f32_16x16x64_bf16(A_, B_, C_, sidx, 0, 0)
__device__ __forceinline__ float ln_den(float var) { return rbf(__builtin_amdgcn_sqrtf(rbf(rbf(var) + 1e-5f))); }
__device__ __forceinline__ float rcp(float x) { return __builtin_amdgcn_rcpf(x); }

struct Params {
    char* ws;
    const bf16_t* x;
    const bf16_t* mem0;
    const bf16_t* w[8];
    bf16_t* out;
};

#define RLX_AGENT __ATOMIC_RELAXED, __HIP_MEMORY_SCOPE_AGENT
#define SENT 0xFFFFFFFFu
constexpr int SPIN_CAP = 400000;
__device__ __forceinline__ bool spin_fail(int spins, const Params& P) {
    if ((spins & 1023) == 1023) {
        const rsrc_t rt = mkrsrc(P.ws + FL0, FL_BYTES);
        unsigned o;
        asm volatile("v_mov_b32 %0, 0x100" : "=v"(o));
        if (__builtin_amdgcn_readfirstlane(__builtin_amdgcn_raw_buffer_load_b32(rt, o, 0, 16)) != 0u) return true;
        if (spins > SPIN_CAP) { __builtin_amdgcn_raw_buffer_store_b32(o, rt, o, 0, 16); return true; }
    }
    return false;
}
__device__ __forceinline__ bool ok4(u32x4 v) { return v[0] != SENT && v[1] != SENT && v[2] != SENT && v[3] != SENT; }
__device__ __forceinline__ unsigned nosent(unsigned d) { return d < 0xFFFFFFFEu ? d : 0xFFFFFFFEu; }
__device__ __forceinline__ u32x4 nosent4(u32x4 v) { return (u32x4){nosent(v[0]), nosent(v[1]), nosent(v[2]), nosent(v[3])}; }
__device__ __forceinline__ unsigned umax3(unsigned a, unsigned b, unsigned c) { const unsigned m = a > b ? a : b; return m > c ? m : c; }
__device__ __forceinline__ unsigned umax4(unsigned m, u32x4 v) { return umax3(umax3(m, v[0], v[1]), v[2], v[3]); }
__device__ __forceinline__ u32x4 sent4() { unsigned sv; asm volatile("v_mov_b32 %0, -1" : "=v"(sv)); return (u32x4){sv, sv, sv, sv}; }
template <int N>
__device__ __forceinline__ void poll(rsrc_t rs, unsigned base, const unsigned (&off)[N], u32x4 (&v)[N], const Params& P) {
    for (int spins = 0;; ++spins) {
        unsigned m = 0;
#pragma unroll
        for (int k = 0; k < N; ++k) v[k] = LD128(rs, off[k], base);
#pragma unroll
        for (int k = 0; k < N; ++k) m = umax4(m, v[k]);
        if (__all(m != SENT)) return;
        asm volatile("" ::: "memory");
        if (spin_fail(spins, P)) return;
    }
}
__device__ __forceinline__ bf16x16 cat16(bf16x8 a, bf16x8 b) {
    return __builtin_shufflevector(a, b, 0, 1, 2, 3, 4, 5, 6, 7, 8, 9, 10, 11, 12, 13, 14, 15);
}
__device__ __forceinline__ bf16x8 half8(bf16x16 v, int h) {
    return h ? __builtin_shufflevector(v, v, 8, 9, 10, 11, 12, 13, 14, 15) : __builtin_shufflevector(v, v, 0, 1, 2, 3, 4, 5, 6, 7);
}
__device__ __forceinline__ bf16x16 lds_frag(unsigned a, unsigned step) { return cat16(lds_ld<bf16x8>(a), lds_ld<bf16x8>(a + step)); }
template <int I>
__device__ __forceinline__ void dmaq(const char* g, unsigned l16, unsigned dst) {
    asm volatile("s_mov_b32 m0, %2\n\ts_nop 0\n\tglobal_load_lds_dwordx4 %0, %1 offset:%3 sc1" :: "v"(l16), "s"(g), "s"(dst), "i"(I * 1024) : "memory");
}
__device__ __forceinline__ void dma4(const char* g, unsigned l16, unsigned dst) {
    asm volatile("s_mov_b32 m0, %2\n\ts_nop 0\n\tglobal_load_lds_dwordx4 %0, %1 sc1\n\tglobal_load_lds_dwordx4 %0, %1 offset:1024 sc1\n\t"
                 "global_load_lds_dwordx4 %0, %1 offset:2048 sc1\n\tglobal_load_lds_dwordx4 %0, %1 offset:3072 sc1" :: "v"(l16), "s"(g), "s"(dst) : "memory");
}
__device__ __forceinline__ unsigned toff(int row, int col, int par) {
    return pgb(row >> 5, par) + blin((unsigned)((row >> 4) & 1) * 20480u + (unsigned)(col >> 3) * 256u) + (unsigned)(row & 15) * 16u;
}

#define ASM_ST
#define ASM_ST_DECL
#define ASM_ST_OUT
#define ASM_ST_SAVE(k)
#define ASM_ST2
#define ASM_ST3
#define ASM_ST3_OUT
#define ASM_ST3_SAVE(k)
#define ASM_ST2_OUT
#define ASM_ST2_SAVE(k)
#define STDECL
#define ST(k)
#define STC(k)
#define STFLUSH(t)

#define MARK(c, k)
#define MARKX(c, k)
#define PMARK(c, st, seg)
#define LDECL
#define LA()
#define LB(t)
#define LF()
#define STROLE 3
constexpr unsigned L_MEM = 0;
constexpr unsigned L_Q = 10240;
constexpr unsigned L_KM = 13312;
constexpr unsigned L_QX = 16384;
constexpr unsigned L_KX = 28672;
constexpr unsigned L_XB = 40960;
constexpr unsigned L_XA = 49152;
constexpr unsigned L_W = 81920;
constexpr int MEGA_LDS = 163840;
constexpr int wi(int i, int j) { return (i >> 1) * 5 + ((i & 1) ? 2 + j : j - 1); }

#define RPUB2
#define WNT ""
#define PRIUP "s_setprio 2\n\t"
#define PRISM PRIUP
#define STR2(x) #x
#define STRX(x) STR2(x)
#define W4SLEEP "s_sleep " STRX(W4SL) "\n\t"
#define PLAY 0
#define V1A 0
#define ASM_WLOAD3 \
    "s_xor_b32 %[t], %[wx], 0\n\t" \
    "s_mul_i32 %[t], %[t], 0xc0000\n\t" \
    "buffer_load_dwordx4 v[200:203], %[o0], %[rs], %[t] offen" WNT "\n\t" \
    "buffer_load_dwordx4 v[0:3], %[o1], %[rs], %[t] offen" WNT "\n\t" \
    "buffer_load_dwordx4 v[8:11], %[o2], %[rs], %[t] offen" WNT "\n\t" \
    "s_add_u32 %[t], %[t], 0x18000\n\t" \
    "buffer_load_dwordx4 v[16:19], %[o0], %[rs], %[t] offen" WNT "\n\t" \
    "buffer_load_dwordx4 v[24:27], %[o1], %[rs], %[t] offen" WNT "\n\t" \
    "buffer_load_dwordx4 v[32:35], %[o2], %[rs], %[t] offen" WNT "\n\t" \
    "s_add_u32 %[t], %[t], 0x18000\n\t" \
    "buffer_load_dwordx4 v[208:211], %[o0], %[rs], %[t] offen" WNT "\n\t" \
    "buffer_load_dwordx4 v[40:43], %[o1], %[rs], %[t] offen" WNT "\n\t" \
    "buffer_load_dwordx4 v[48:51], %[o2], %[rs], %[t] offen" WNT "\n\t" \
    "s_add_u32 %[t], %[t], 0x18000\n\t" \
    "buffer_load_dwordx4 v[56:59], %[o0], %[rs], %[t] offen" WNT "\n\t" \
    "buffer_load_dwordx4 v[64:67], %[o1], %[rs], %[t] offen" WNT "\n\t" \
    "buffer_load_dwordx4 v[72:75], %[o2], %[rs], %[t] offen" WNT "\n\t" \
    "s_add_u32 %[t], %[t], 0x18000\n\t" \
    "buffer_load_dwordx4 v[204:207], %[o0], %[rs], %[t] offen" WNT "\n\t" \
    "buffer_load_dwordx4 v[4:7], %[o1], %[rs], %[t] offen" WNT "\n\t" \
    "buffer_load_dwordx4 v[12:15], %[o2], %[rs], %[t] offen" WNT "\n\t" \
    "s_add_u32 %[t], %[t], 0x18000\n\t" \
    "buffer_load_dwordx4 v[20:23], %[o0], %[rs], %[t] offen" WNT "\n\t" \
    "buffer_load_dwordx4 v[28:31], %[o1], %[rs], %[t] offen" WNT "\n\t" \
    "buffer_load_dwordx4 v[36:39], %[o2], %[rs], %[t] offen" WNT "\n\t" \
    "s_add_u32 %[t], %[t], 0x18000\n\t" \
    "buffer_load_dwordx4 v[212:215], %[o0], %[rs], %[t] offen" WNT "\n\t" \
    "buffer_load_dwordx4 v[44:47], %[o1], %[rs], %[t] offen" WNT "\n\t" \
    "buffer_load_dwordx4 v[52:55], %[o2], %[rs], %[t] offen" WNT "\n\t" \
    "s_add_u32 %[t], %[t], 0x18000\n\t" \
    "buffer_load_dwordx4 v[60:63], %[o0], %[rs], %[t] offen" WNT "\n\t" \
    "buffer_load_dwordx4 v[68:71], %[o1], %[rs], %[t] offen" WNT "\n\t" \
    "buffer_load_dwordx4 v[76:79], %[o2], %[rs], %[t] offen" WNT "\n\t" \
    "s_add_u32 %[t], %[t], 0x18000\n\t" \
    "s_waitcnt vmcnt(21)\n\t" \
    "ds_write_b128 %[s0], v[200:203] offset:0\n\t" \
    "ds_write_b128 %[s1], v[0:3] offset:0\n\t" \
    "ds_write_b128 %[s2], v[8:11] offset:0\n\t" \
    "s_waitcnt vmcnt(18)\n\t" \
    "ds_write_b128 %[s0], v[16:19] offset:3456\n\t" \
    "ds_write_b128 %[s1], v[24:27] offset:3456\n\t" \
    "ds_write_b128 %[s2], v[32:35] offset:3456\n\t" \
    "s_waitcnt vmcnt(15)\n\t" \
    "ds_write_b128 %[s0], v[208:211] offset:6912\n\t" \
    "ds_write_b128 %[s1], v[40:43] offset:6912\n\t" \
    "ds_write_b128 %[s2], v[48:51] offset:6912\n\t" \
    "s_waitcnt vmcnt(12)\n\t" \
    "ds_write_b128 %[s0], v[56:59] offset:10368\n\t" \
    "ds_write_b128 %[s1], v[64:67] offset:10368\n\t" \
    "ds_write_b128 %[s2], v[72:75] offset:10368\n\t" \
    "s_waitcnt lgkmcnt(0)\n\t" \
    "s_barrier \n\t" \
    "ds_read_b64_tr_b16 v[200:201], %[t0] offset:0\n\t" \
    "ds_read_b64_tr_b16 v[202:203], %[t0] offset:1664\n\t" \
    "ds_read_b64_tr_b16 v[0:1], %[t0] offset:128\n\t" \
    "ds_read_b64_tr_b16 v[2:3], %[t0] offset:1792\n\t" \
    "ds_read_b64_tr_b16 v[8:9], %[t0] offset:256\n\t" \
    "ds_read_b64_tr_b16 v[10:11], %[t0] offset:1920\n\t" \
    "ds_read_b64_tr_b16 v[16:17], %[t1] offset:0\n\t" \
    "ds_read_b64_tr_b16 v[18:19], %[t1] offset:1664\n\t" \
    "ds_read_b64_tr_b16 v[24:25], %[t1] offset:128\n\t" \
    "ds_read_b64_tr_b16 v[26:27], %[t1] offset:1792\n\t" \
    "ds_read_b64_tr_b16 v[32:33], %[t1] offset:256\n\t" \
    "ds_read_b64_tr_b16 v[34:35], %[t1] offset:1920\n\t" \
    "ds_read_b64_tr_b16 v[208:209], %[t2] offset:0\n\t" \
    "ds_read_b64_tr_b16 v[210:211], %[t2] offset:1664\n\t" \
    "ds_read_b64_tr_b16 v[40:41], %[t2] offset:128\n\t" \
    "ds_read_b64_tr_b16 v[42:43], %[t2] offset:1792\n\t" \
    "ds_read_b64_tr_b16 v[48:49], %[t2] offset:256\n\t" \
    "ds_read_b64_tr_b16 v[50:51], %[t2] offset:1920\n\t" \
    "ds_read_b64_tr_b16 v[56:57], %[t3] offset:0\n\t" \
    "ds_read_b64_tr_b16 v[58:59], %[t3] offset:1664\n\t" \
    "ds_read_b64_tr_b16 v[64:65], %[t3] offset:128\n\t" \
    "ds_read_b64_tr_b16 v[66:67], %[t3] offset:1792\n\t" \
    "ds_read_b64_tr_b16 v[72:73], %[t3] offset:256\n\t" \
    "ds_read_b64_tr_b16 v[74:75], %[t3] offset:1920\n\t" \
    "s_xor_b32 %[t], %[wx], 4\n\t" \
    "s_mul_i32 %[t], %[t], 0xc0000\n\t" \
    "buffer_load_dwordx4 v[216:219], %[o0], %[rs], %[t] offen" WNT "\n\t" \
    "buffer_load_dwordx4 v[80:83], %[o1], %[rs], %[t] offen" WNT "\n\t" \
    "buffer_load_dwordx4 v[88:91], %[o2], %[rs], %[t] offen" WNT "\n\t" \
    "s_add_u32 %[t], %[t], 0x18000\n\t" \
    "buffer_load_dwordx4 v[96:99], %[o0], %[rs], %[t] offen" WNT "\n\t" \
    "buffer_load_dwordx4 v[104:107], %[o1], %[rs], %[t] offen" WNT "\n\t" \
    "buffer_load_dwordx4 v[112:115], %[o2], %[rs], %[t] offen" WNT "\n\t" \
    "s_add_u32 %[t], %[t], 0x18000\n\t" \
    "buffer_load_dwordx4 v[224:227], %[o0], %[rs], %[t] offen" WNT "\n\t" \
    "buffer_load_dwordx4 v[120:123], %[o1], %[rs], %[t] offen" WNT "\n\t" \
    "buffer_load_dwordx4 v[128:131], %[o2], %[rs], %[t] offen" WNT "\n\t" \
    "s_add_u32 %[t], %[t], 0x18000\n\t" \
    "buffer_load_dwordx4 v[136:139], %[o0], %[rs], %[t] offen" WNT "\n\t" \
    "buffer_load_dwordx4 v[144:147], %[o1], %[rs], %[t] offen" WNT "\n\t" \
    "buffer_load_dwordx4 v[152:155], %[o2], %[rs], %[t] offen" WNT "\n\t" \
    "s_add_u32 %[t], %[t], 0x18000\n\t" \
    "s_waitcnt lgkmcnt(0)\n\t" \
    "s_barrier \n\t" \
    "s_waitcnt vmcnt(21)\n\t" \
    "ds_write_b128 %[s0], v[204:207] offset:0\n\t" \
    "ds_write_b128 %[s1], v[4:7] offset:0\n\t" \
    "ds_write_b128 %[s2], v[12:15] offset:0\n\t" \
    "s_waitcnt vmcnt(18)\n\t" \
    "ds_write_b128 %[s0], v[20:23] offset:3456\n\t" \
    "ds_write_b128 %[s1], v[28:31] offset:3456\n\t" \
    "ds_write_b128 %[s2], v[36:39] offset:3456\n\t" \
    "s_waitcnt vmcnt(15)\n\t" \
    "ds_write_b128 %[s0], v[212:215] offset:6912\n\t" \
    "ds_write_b128 %[s1], v[44:47] offset:6912\n\t" \
    "ds_write_b128 %[s2], v[52:55] offset:6912\n\t" \
    "s_waitcnt vmcnt(12)\n\t" \
    "ds_write_b128 %[s0], v[60:63] offset:10368\n\t" \
    "ds_write_b128 %[s1], v[68:71] offset:10368\n\t" \
    "ds_write_b128 %[s2], v[76:79] offset:10368\n\t" \
    "s_waitcnt lgkmcnt(0)\n\t" \
    "s_barrier \n\t" \
    "ds_read_b64_tr_b16 v[204:205], %[t0] offset:0\n\t" \
    "ds_read_b64_tr_b16 v[206:207], %[t0] offset:1664\n\t" \
    "ds_read_b64_tr_b16 v[4:5], %[t0] offset:128\n\t" \
    "ds_read_b64_tr_b16 v[6:7], %[t0] offset:1792\n\t" \
    "ds_read_b64_tr_b16 v[12:13], %[t0] offset:256\n\t" \
    "ds_read_b64_tr_b16 v[14:15], %[t0] offset:1920\n\t" \
    "ds_read_b64_tr_b16 v[20:21], %[t1] offset:0\n\t" \
    "ds_read_b64_tr_b16 v[22:23], %[t1] offset:1664\n\t" \
    "ds_read_b64_tr_b16 v[28:29], %[t1] offset:128\n\t" \
    "ds_read_b64_tr_b16 v[30:31], %[t1] offset:1792\n\t" \
    "ds_read_b64_tr_b16 v[36:37], %[t1] offset:256\n\t" \
    "ds_read_b64_tr_b16 v[38:39], %[t1] offset:1920\n\t" \
    "ds_read_b64_tr_b16 v[212:213], %[t2] offset:0\n\t" \
    "ds_read_b64_tr_b16 v[214:215], %[t2] offset:1664\n\t" \
    "ds_read_b64_tr_b16 v[44:45], %[t2] offset:128\n\t" \
    "ds_read_b64_tr_b16 v[46:47], %[t2] offset:1792\n\t" \
    "ds_read_b64_tr_b16 v[52:53], %[t2] offset:256\n\t" \
    "ds_read_b64_tr_b16 v[54:55], %[t2] offset:1920\n\t" \
    "ds_read_b64_tr_b16 v[60:61], %[t3] offset:0\n\t" \
    "ds_read_b64_tr_b16 v[62:63], %[t3] offset:1664\n\t" \
    "ds_read_b64_tr_b16 v[68:69], %[t3] offset:128\n\t" \
    "ds_read_b64_tr_b16 v[70:71], %[t3] offset:1792\n\t" \
    "ds_read_b64_tr_b16 v[76:77], %[t3] offset:256\n\t" \
    "ds_read_b64_tr_b16 v[78:79], %[t3] offset:1920\n\t" \
    "buffer_load_dwordx4 v[220:223], %[o0], %[rs], %[t] offen" WNT "\n\t" \
    "buffer_load_dwordx4 v[84:87], %[o1], %[rs], %[t] offen" WNT "\n\t" \
    "buffer_load_dwordx4 v[92:95], %[o2], %[rs], %[t] offen" WNT "\n\t" \
    "s_add_u32 %[t], %[t], 0x18000\n\t" \
    "buffer_load_dwordx4 v[100:103], %[o0], %[rs], %[t] offen" WNT "\n\t" \
    "buffer_load_dwordx4 v[108:111], %[o1], %[rs], %[t] offen" WNT "\n\t" \
    "buffer_load_dwordx4 v[116:119], %[o2], %[rs], %[t] offen" WNT "\n\t" \
    "s_add_u32 %[t], %[t], 0x18000\n\t" \
    "buffer_load_dwordx4 v[228:231], %[o0], %[rs], %[t] offen" WNT "\n\t" \
    "buffer_load_dwordx4 v[124:127], %[o1], %[rs], %[t] offen" WNT "\n\t" \
    "buffer_load_dwordx4 v[132:135], %[o2], %[rs], %[t] offen" WNT "\n\t" \
    "s_add_u32 %[t], %[t], 0x18000\n\t" \
    "buffer_load_dwordx4 v[140:143], %[o0], %[rs], %[t] offen" WNT "\n\t" \
    "buffer_load_dwordx4 v[148:151], %[o1], %[rs], %[t] offen" WNT "\n\t" \
    "buffer_load_dwordx4 v[156:159], %[o2], %[rs], %[t] offen" WNT "\n\t" \
    "s_add_u32 %[t], %[t], 0x18000\n\t" \
    "s_waitcnt lgkmcnt(0)\n\t" \
    "s_barrier \n\t" \
    "s_waitcnt vmcnt(21)\n\t" \
    "ds_write_b128 %[s0], v[216:219] offset:0\n\t" \
    "ds_write_b128 %[s1], v[80:83] offset:0\n\t" \
    "ds_write_b128 %[s2], v[88:91] offset:0\n\t" \
    "s_waitcnt vmcnt(18)\n\t" \
    "ds_write_b128 %[s0], v[96:99] offset:3456\n\t" \
    "ds_write_b128 %[s1], v[104:107] offset:3456\n\t" \
    "ds_write_b128 %[s2], v[112:115] offset:3456\n\t" \
    "s_waitcnt vmcnt(15)\n\t" \
    "ds_write_b128 %[s0], v[224:227] offset:6912\n\t" \
    "ds_write_b128 %[s1], v[120:123] offset:6912\n\t" \
    "ds_write_b128 %[s2], v[128:131] offset:6912\n\t" \
    "s_waitcnt vmcnt(12)\n\t" \
    "ds_write_b128 %[s0], v[136:139] offset:10368\n\t" \
    "ds_write_b128 %[s1], v[144:147] offset:10368\n\t" \
    "ds_write_b128 %[s2], v[152:155] offset:10368\n\t" \
    "s_waitcnt lgkmcnt(0)\n\t" \
    "s_barrier \n\t" \
    "ds_read_b64_tr_b16 v[216:217], %[t0] offset:0\n\t" \
    "ds_read_b64_tr_b16 v[218:219], %[t0] offset:1664\n\t" \
    "ds_read_b64_tr_b16 v[80:81], %[t0] offset:128\n\t" \
    "ds_read_b64_tr_b16 v[82:83], %[t0] offset:1792\n\t" \
    "ds_read_b64_tr_b16 v[88:89], %[t0] offset:256\n\t" \
    "ds_read_b64_tr_b16 v[90:91], %[t0] offset:1920\n\t" \
    "ds_read_b64_tr_b16 v[96:97], %[t1] offset:0\n\t" \
    "ds_read_b64_tr_b16 v[98:99], %[t1] offset:1664\n\t" \
    "ds_read_b64_tr_b16 v[104:105], %[t1] offset:128\n\t" \
    "ds_read_b64_tr_b16 v[106:107], %[t1] offset:1792\n\t" \
    "ds_read_b64_tr_b16 v[112:113], %[t1] offset:256\n\t" \
    "ds_read_b64_tr_b16 v[114:115], %[t1] offset:1920\n\t" \
    "ds_read_b64_tr_b16 v[224:225], %[t2] offset:0\n\t" \
    "ds_read_b64_tr_b16 v[226:227], %[t2] offset:1664\n\t" \
    "ds_read_b64_tr_b16 v[120:121], %[t2] offset:128\n\t" \
    "ds_read_b64_tr_b16 v[122:123], %[t2] offset:1792\n\t" \
    "ds_read_b64_tr_b16 v[128:129], %[t2] offset:256\n\t" \
    "ds_read_b64_tr_b16 v[130:131], %[t2] offset:1920\n\t" \
    "ds_read_b64_tr_b16 v[136:137], %[t3] offset:0\n\t" \
    "ds_read_b64_tr_b16 v[138:139], %[t3] offset:1664\n\t" \
    "ds_read_b64_tr_b16 v[144:145], %[t3] offset:128\n\t" \
    "ds_read_b64_tr_b16 v[146:147], %[t3] offset:1792\n\t" \
    "ds_read_b64_tr_b16 v[152:153], %[t3] offset:256\n\t" \
    "ds_read_b64_tr_b16 v[154:155], %[t3] offset:1920\n\t" \
    "s_and_b32 %[t], %[wx], 3\n\t" \
    "s_mul_i32 %[t], %[t], 0x60000\n\t" \
    "s_add_u32 %[t], %[t], 0x600000\n\t" \
    "buffer_load_dwordx4 v[232:235], %[o0], %[rs], %[t] offen" WNT "\n\t" \
    "buffer_load_dwordx4 v[160:163], %[o1], %[rs], %[t] offen" WNT "\n\t" \
    "buffer_load_dwordx4 v[168:171], %[o2], %[rs], %[t] offen" WNT "\n\t" \
    "s_add_u32 %[t], %[t], 0x18000\n\t" \
    "buffer_load_dwordx4 v[236:239], %[o0], %[rs], %[t] offen" WNT "\n\t" \
    "buffer_load_dwordx4 v[164:167], %[o1], %[rs], %[t] offen" WNT "\n\t" \
    "buffer_load_dwordx4 v[172:175], %[o2], %[rs], %[t] offen" WNT "\n\t" \
    "s_add_u32 %[t], %[t], 0x18000\n\t" \
    "buffer_load_dwordx4 v[176:179], %[o0], %[rs], %[t] offen" WNT "\n\t" \
    "buffer_load_dwordx4 v[184:187], %[o1], %[rs], %[t] offen" WNT "\n\t" \
    "buffer_load_dwordx4 v[192:195], %[o2], %[rs], %[t] offen" WNT "\n\t" \
    "s_add_u32 %[t], %[t], 0x18000\n\t" \
    "buffer_load_dwordx4 v[180:183], %[o0], %[rs], %[t] offen" WNT "\n\t" \
    "buffer_load_dwordx4 v[188:191], %[o1], %[rs], %[t] offen" WNT "\n\t" \
    "buffer_load_dwordx4 v[196:199], %[o2], %[rs], %[t] offen" WNT "\n\t" \
    "s_add_u32 %[t], %[t], 0x18000\n\t" \
    "s_waitcnt lgkmcnt(0)\n\t" \
    "s_barrier \n\t" \
    "s_waitcnt vmcnt(21)\n\t" \
    "ds_write_b128 %[s0], v[220:223] offset:0\n\t" \
    "ds_write_b128 %[s1], v[84:87] offset:0\n\t" \
    "ds_write_b128 %[s2], v[92:95] offset:0\n\t" \
    "s_waitcnt vmcnt(18)\n\t" \
    "ds_write_b128 %[s0], v[100:103] offset:3456\n\t" \
    "ds_write_b128 %[s1], v[108:111] offset:3456\n\t" \
    "ds_write_b128 %[s2], v[116:119] offset:3456\n\t" \
    "s_waitcnt vmcnt(15)\n\t" \
    "ds_write_b128 %[s0], v[228:231] offset:6912\n\t" \
    "ds_write_b128 %[s1], v[124:127] offset:6912\n\t" \
    "ds_write_b128 %[s2], v[132:135] offset:6912\n\t" \
    "s_waitcnt vmcnt(12)\n\t" \
    "ds_write_b128 %[s0], v[140:143] offset:10368\n\t" \
    "ds_write_b128 %[s1], v[148:151] offset:10368\n\t" \
    "ds_write_b128 %[s2], v[156:159] offset:10368\n\t" \
    "s_waitcnt lgkmcnt(0)\n\t" \
    "s_barrier \n\t" \
    "ds_read_b64_tr_b16 v[220:221], %[t0] offset:0\n\t" \
    "ds_read_b64_tr_b16 v[222:223], %[t0] offset:1664\n\t" \
    "ds_read_b64_tr_b16 v[84:85], %[t0] offset:128\n\t" \
    "ds_read_b64_tr_b16 v[86:87], %[t0] offset:1792\n\t" \
    "ds_read_b64_tr_b16 v[92:93], %[t0] offset:256\n\t" \
    "ds_read_b64_tr_b16 v[94:95], %[t0] offset:1920\n\t" \
    "ds_read_b64_tr_b16 v[100:101], %[t1] offset:0\n\t" \
    "ds_read_b64_tr_b16 v[102:103], %[t1] offset:1664\n\t" \
    "ds_read_b64_tr_b16 v[108:109], %[t1] offset:128\n\t" \
    "ds_read_b64_tr_b16 v[110:111], %[t1] offset:1792\n\t" \
    "ds_read_b64_tr_b16 v[116:117], %[t1] offset:256\n\t" \
    "ds_read_b64_tr_b16 v[118:119], %[t1] offset:1920\n\t" \
    "ds_read_b64_tr_b16 v[228:229], %[t2] offset:0\n\t" \
    "ds_read_b64_tr_b16 v[230:231], %[t2] offset:1664\n\t" \
    "ds_read_b64_tr_b16 v[124:125], %[t2] offset:128\n\t" \
    "ds_read_b64_tr_b16 v[126:127], %[t2] offset:1792\n\t" \
    "ds_read_b64_tr_b16 v[132:133], %[t2] offset:256\n\t" \
    "ds_read_b64_tr_b16 v[134:135], %[t2] offset:1920\n\t" \
    "ds_read_b64_tr_b16 v[140:141], %[t3] offset:0\n\t" \
    "ds_read_b64_tr_b16 v[142:143], %[t3] offset:1664\n\t" \
    "ds_read_b64_tr_b16 v[148:149], %[t3] offset:128\n\t" \
    "ds_read_b64_tr_b16 v[150:151], %[t3] offset:1792\n\t" \
    "ds_read_b64_tr_b16 v[156:157], %[t3] offset:256\n\t" \
    "ds_read_b64_tr_b16 v[158:159], %[t3] offset:1920\n\t" \
    "s_waitcnt lgkmcnt(0)\n\t" \
    "s_barrier \n\t" \
    "s_waitcnt vmcnt(9)\n\t" \
    "ds_write_b128 %[s0], v[232:235] offset:0\n\t" \
    "ds_write_b128 %[s1], v[160:163] offset:0\n\t" \
    "ds_write_b128 %[s2], v[168:171] offset:0\n\t" \
    "s_waitcnt vmcnt(6)\n\t" \
    "ds_write_b128 %[s0], v[236:239] offset:3456\n\t" \
    "ds_write_b128 %[s1], v[164:167] offset:3456\n\t" \
    "ds_write_b128 %[s2], v[172:175] offset:3456\n\t" \
    "s_waitcnt vmcnt(3)\n\t" \
    "ds_write_b128 %[s0], v[176:179] offset:6912\n\t" \
    "ds_write_b128 %[s1], v[184:187] offset:6912\n\t" \
    "ds_write_b128 %[s2], v[192:195] offset:6912\n\t" \
    "s_waitcnt vmcnt(0)\n\t" \
    "ds_write_b128 %[s0], v[180:183] offset:10368\n\t" \
    "ds_write_b128 %[s1], v[188:191] offset:10368\n\t" \
    "ds_write_b128 %[s2], v[196:199] offset:10368\n\t" \
    "s_waitcnt lgkmcnt(0)\n\t" \
    "s_barrier \n\t" \
    "ds_read_b64_tr_b16 v[232:233], %[t4] offset:0\n\t" \
    "ds_read_b64_tr_b16 v[234:235], %[t4] offset:1664\n\t" \
    "ds_read_b64_tr_b16 v[160:161], %[t4] offset:128\n\t" \
    "ds_read_b64_tr_b16 v[162:163], %[t4] offset:1792\n\t" \
    "ds_read_b64_tr_b16 v[168:169], %[t4] offset:256\n\t" \
    "ds_read_b64_tr_b16 v[170:171], %[t4] offset:1920\n\t" \
    "ds_read_b64_tr_b16 v[236:237], %[t4] offset:13824\n\t" \
    "ds_read_b64_tr_b16 v[238:239], %[t4] offset:15488\n\t" \
    "ds_read_b64_tr_b16 v[164:165], %[t4] offset:13952\n\t" \
    "ds_read_b64_tr_b16 v[166:167], %[t4] offset:15616\n\t" \
    "ds_read_b64_tr_b16 v[172:173], %[t4] offset:14080\n\t" \
    "ds_read_b64_tr_b16 v[174:175], %[t4] offset:15744\n\t" \
    "ds_read_b64_tr_b16 v[176:177], %[t4] offset:27648\n\t" \
    "ds_read_b64_tr_b16 v[178:179], %[t4] offset:29312\n\t" \
    "ds_read_b64_tr_b16 v[184:185], %[t4] offset:27776\n\t" \
    "ds_read_b64_tr_b16 v[186:187], %[t4] offset:29440\n\t" \
    "ds_read_b64_tr_b16 v[192:193], %[t4] offset:27904\n\t" \
    "ds_read_b64_tr_b16 v[194:195], %[t4] offset:29568\n\t" \
    "ds_read_b64_tr_b16 v[180:181], %[t4] offset:41472\n\t" \
    "ds_read_b64_tr_b16 v[182:183], %[t4] offset:43136\n\t" \
    "ds_read_b64_tr_b16 v[188:189], %[t4] offset:41600\n\t" \
    "ds_read_b64_tr_b16 v[190:191], %[t4] offset:43264\n\t" \
    "ds_read_b64_tr_b16 v[196:197], %[t4] offset:41728\n\t" \
    "ds_read_b64_tr_b16 v[198:199], %[t4] offset:43392\n\t" \
    "s_waitcnt lgkmcnt(0)\n\t" \
    "s_barrier \n\t" \
    "ds_write_b128 %[wl], v[200:203] offset:0\n\t" \
    "ds_write_b128 %[wl], v[204:207] offset:1024\n\t" \
    "ds_write_b128 %[wl], v[208:211] offset:2048\n\t" \
    "ds_write_b128 %[wl], v[212:215] offset:3072\n\t" \
    "ds_write_b128 %[wl], v[216:219] offset:4096\n\t" \
    "ds_write_b128 %[wl], v[220:223] offset:5120\n\t" \
    "ds_write_b128 %[wl], v[224:227] offset:6144\n\t" \
    "ds_write_b128 %[wl], v[228:231] offset:7168\n\t" \
    "ds_write_b128 %[wl], v[232:235] offset:8192\n\t" \
    "ds_write_b128 %[wl], v[236:239] offset:9216\n\t" \
    "s_waitcnt lgkmcnt(0)\n\t"
#define ASM_WLOADO \
    "s_mov_b32 %[t], 0x0\n\t" \
    "buffer_load_dwordx4 v[200:203], %[o0], %[rs], %[t] offen" WNT "\n\t" \
    "buffer_load_dwordx4 v[16:19], %[o1], %[rs], %[t] offen" WNT "\n\t" \
    "buffer_load_dwordx4 v[208:211], %[o2], %[rs], %[t] offen" WNT "\n\t" \
    "buffer_load_dwordx4 v[56:59], %[o3], %[rs], %[t] offen" WNT "\n\t" \
    "buffer_load_dwordx4 v[216:219], %[o4], %[rs], %[t] offen" WNT "\n\t" \
    "s_mov_b32 %[t], 0xa000\n\t" \
    "buffer_load_dwordx4 v[96:99], %[o0], %[rs], %[t] offen" WNT "\n\t" \
    "buffer_load_dwordx4 v[224:227], %[o1], %[rs], %[t] offen" WNT "\n\t" \
    "buffer_load_dwordx4 v[136:139], %[o2], %[rs], %[t] offen" WNT "\n\t" \
    "buffer_load_dwordx4 v[232:235], %[o3], %[rs], %[t] offen" WNT "\n\t" \
    "buffer_load_dwordx4 v[176:179], %[o4], %[rs], %[t] offen" WNT "\n\t" \
    "s_mov_b32 %[t], 0x14000\n\t" \
    "buffer_load_dwordx4 v[204:207], %[o0], %[rs], %[t] offen" WNT "\n\t" \
    "buffer_load_dwordx4 v[20:23], %[o1], %[rs], %[t] offen" WNT "\n\t" \
    "buffer_load_dwordx4 v[212:215], %[o2], %[rs], %[t] offen" WNT "\n\t" \
    "buffer_load_dwordx4 v[60:63], %[o3], %[rs], %[t] offen" WNT "\n\t" \
    "buffer_load_dwordx4 v[220:223], %[o4], %[rs], %[t] offen" WNT "\n\t" \
    "s_mov_b32 %[t], 0x1e000\n\t" \
    "buffer_load_dwordx4 v[100:103], %[o0], %[rs], %[t] offen" WNT "\n\t" \
    "buffer_load_dwordx4 v[228:231], %[o1], %[rs], %[t] offen" WNT "\n\t" \
    "buffer_load_dwordx4 v[140:143], %[o2], %[rs], %[t] offen" WNT "\n\t" \
    "buffer_load_dwordx4 v[236:239], %[o3], %[rs], %[t] offen" WNT "\n\t" \
    "buffer_load_dwordx4 v[180:183], %[o4], %[rs], %[t] offen" WNT "\n\t" \
    "s_waitcnt vmcnt(15)\n\t" \
    "ds_write_b128 %[s0], v[200:203] offset:0\n\t" \
    "ds_write_b128 %[s1], v[16:19] offset:0\n\t" \
    "ds_write_b128 %[s2], v[208:211] offset:0\n\t" \
    "ds_write_b128 %[s3], v[56:59] offset:0\n\t" \
    "ds_write_b128 %[s4], v[216:219] offset:0\n\t" \
    "s_waitcnt vmcnt(10)\n\t" \
    "ds_write_b128 %[s0], v[96:99] offset:5632\n\t" \
    "ds_write_b128 %[s1], v[224:227] offset:5632\n\t" \
    "ds_write_b128 %[s2], v[136:139] offset:5632\n\t" \
    "ds_write_b128 %[s3], v[232:235] offset:5632\n\t" \
    "ds_write_b128 %[s4], v[176:179] offset:5632\n\t" \
    "ds_read_b64_tr_b16 v[200:201], %[tr] offset:0\n\t" \
    "ds_read_b64_tr_b16 v[202:203], %[tr] offset:5632\n\t" \
    "ds_read_b64_tr_b16 v[16:17], %[tr] offset:32\n\t" \
    "ds_read_b64_tr_b16 v[18:19], %[tr] offset:5664\n\t" \
    "ds_read_b64_tr_b16 v[208:209], %[tr] offset:64\n\t" \
    "ds_read_b64_tr_b16 v[210:211], %[tr] offset:5696\n\t" \
    "ds_read_b64_tr_b16 v[56:57], %[tr] offset:96\n\t" \
    "ds_read_b64_tr_b16 v[58:59], %[tr] offset:5728\n\t" \
    "ds_read_b64_tr_b16 v[216:217], %[tr] offset:128\n\t" \
    "ds_read_b64_tr_b16 v[218:219], %[tr] offset:5760\n\t" \
    "ds_read_b64_tr_b16 v[96:97], %[tr] offset:160\n\t" \
    "ds_read_b64_tr_b16 v[98:99], %[tr] offset:5792\n\t" \
    "ds_read_b64_tr_b16 v[224:225], %[tr] offset:192\n\t" \
    "ds_read_b64_tr_b16 v[226:227], %[tr] offset:5824\n\t" \
    "ds_read_b64_tr_b16 v[136:137], %[tr] offset:224\n\t" \
    "ds_read_b64_tr_b16 v[138:139], %[tr] offset:5856\n\t" \
    "ds_read_b64_tr_b16 v[232:233], %[tr] offset:256\n\t" \
    "ds_read_b64_tr_b16 v[234:235], %[tr] offset:5888\n\t" \
    "ds_read_b64_tr_b16 v[176:177], %[tr] offset:288\n\t" \
    "ds_read_b64_tr_b16 v[178:179], %[tr] offset:5920\n\t" \
    "s_barrier \n\t" \
    "s_mov_b32 %[t], 0x28000\n\t" \
    "buffer_load_dwordx4 v[0:3], %[o0], %[rs], %[t] offen" WNT "\n\t" \
    "buffer_load_dwordx4 v[24:27], %[o1], %[rs], %[t] offen" WNT "\n\t" \
    "buffer_load_dwordx4 v[40:43], %[o2], %[rs], %[t] offen" WNT "\n\t" \
    "buffer_load_dwordx4 v[64:67], %[o3], %[rs], %[t] offen" WNT "\n\t" \
    "buffer_load_dwordx4 v[80:83], %[o4], %[rs], %[t] offen" WNT "\n\t" \
    "s_mov_b32 %[t], 0x32000\n\t" \
    "buffer_load_dwordx4 v[104:107], %[o0], %[rs], %[t] offen" WNT "\n\t" \
    "buffer_load_dwordx4 v[120:123], %[o1], %[rs], %[t] offen" WNT "\n\t" \
    "buffer_load_dwordx4 v[144:147], %[o2], %[rs], %[t] offen" WNT "\n\t" \
    "buffer_load_dwordx4 v[160:163], %[o3], %[rs], %[t] offen" WNT "\n\t" \
    "buffer_load_dwordx4 v[184:187], %[o4], %[rs], %[t] offen" WNT "\n\t" \
    "s_waitcnt lgkmcnt(0)\n\t" \
    "s_barrier \n\t" \
    "s_waitcnt vmcnt(15)\n\t" \
    "ds_write_b128 %[s0], v[204:207] offset:0\n\t" \
    "ds_write_b128 %[s1], v[20:23] offset:0\n\t" \
    "ds_write_b128 %[s2], v[212:215] offset:0\n\t" \
    "ds_write_b128 %[s3], v[60:63] offset:0\n\t" \
    "ds_write_b128 %[s4], v[220:223] offset:0\n\t" \
    "s_waitcnt vmcnt(10)\n\t" \
    "ds_write_b128 %[s0], v[100:103] offset:5632\n\t" \
    "ds_write_b128 %[s1], v[228:231] offset:5632\n\t" \
    "ds_write_b128 %[s2], v[140:143] offset:5632\n\t" \
    "ds_write_b128 %[s3], v[236:239] offset:5632\n\t" \
    "ds_write_b128 %[s4], v[180:183] offset:5632\n\t" \
    "ds_read_b64_tr_b16 v[204:205], %[tr] offset:0\n\t" \
    "ds_read_b64_tr_b16 v[206:207], %[tr] offset:5632\n\t" \
    "ds_read_b64_tr_b16 v[20:21], %[tr] offset:32\n\t" \
    "ds_read_b64_tr_b16 v[22:23], %[tr] offset:5664\n\t" \
    "ds_read_b64_tr_b16 v[212:213], %[tr] offset:64\n\t" \
    "ds_read_b64_tr_b16 v[214:215], %[tr] offset:5696\n\t" \
    "ds_read_b64_tr_b16 v[60:61], %[tr] offset:96\n\t" \
    "ds_read_b64_tr_b16 v[62:63], %[tr] offset:5728\n\t" \
    "ds_read_b64_tr_b16 v[220:221], %[tr] offset:128\n\t" \
    "ds_read_b64_tr_b16 v[222:223], %[tr] offset:5760\n\t" \
    "ds_read_b64_tr_b16 v[100:101], %[tr] offset:160\n\t" \
    "ds_read_b64_tr_b16 v[102:103], %[tr] offset:5792\n\t" \
    "ds_read_b64_tr_b16 v[228:229], %[tr] offset:192\n\t" \
    "ds_read_b64_tr_b16 v[230:231], %[tr] offset:5824\n\t" \
    "ds_read_b64_tr_b16 v[140:141], %[tr] offset:224\n\t" \
    "ds_read_b64_tr_b16 v[142:143], %[tr] offset:5856\n\t" \
    "ds_read_b64_tr_b16 v[236:237], %[tr] offset:256\n\t" \
    "ds_read_b64_tr_b16 v[238:239], %[tr] offset:5888\n\t" \
    "ds_read_b64_tr_b16 v[180:181], %[tr] offset:288\n\t" \
    "ds_read_b64_tr_b16 v[182:183], %[tr] offset:5920\n\t" \
    "s_barrier \n\t" \
    "s_mov_b32 %[t], 0x5000\n\t" \
    "buffer_load_dwordx4 v[4:7], %[o0], %[rs], %[t] offen" WNT "\n\t" \
    "buffer_load_dwordx4 v[28:31], %[o1], %[rs], %[t] offen" WNT "\n\t" \
    "buffer_load_dwordx4 v[44:47], %[o2], %[rs], %[t] offen" WNT "\n\t" \
    "buffer_load_dwordx4 v[68:71], %[o3], %[rs], %[t] offen" WNT "\n\t" \
    "buffer_load_dwordx4 v[84:87], %[o4], %[rs], %[t] offen" WNT "\n\t" \
    "s_mov_b32 %[t], 0xf000\n\t" \
    "buffer_load_dwordx4 v[108:111], %[o0], %[rs], %[t] offen" WNT "\n\t" \
    "buffer_load_dwordx4 v[124:127], %[o1], %[rs], %[t] offen" WNT "\n\t" \
    "buffer_load_dwordx4 v[148:151], %[o2], %[rs], %[t] offen" WNT "\n\t" \
    "buffer_load_dwordx4 v[164:167], %[o3], %[rs], %[t] offen" WNT "\n\t" \
    "buffer_load_dwordx4 v[188:191], %[o4], %[rs], %[t] offen" WNT "\n\t" \
    "s_waitcnt lgkmcnt(0)\n\t" \
    "s_barrier \n\t" \
    "s_waitcnt vmcnt(15)\n\t" \
    "ds_write_b128 %[s0], v[0:3] offset:0\n\t" \
    "ds_write_b128 %[s1], v[24:27] offset:0\n\t" \
    "ds_write_b128 %[s2], v[40:43] offset:0\n\t" \
    "ds_write_b128 %[s3], v[64:67] offset:0\n\t" \
    "ds_write_b128 %[s4], v[80:83] offset:0\n\t" \
    "s_waitcnt vmcnt(10)\n\t" \
    "ds_write_b128 %[s0], v[104:107] offset:5632\n\t" \
    "ds_write_b128 %[s1], v[120:123] offset:5632\n\t" \
    "ds_write_b128 %[s2], v[144:147] offset:5632\n\t" \
    "ds_write_b128 %[s3], v[160:163] offset:5632\n\t" \
    "ds_write_b128 %[s4], v[184:187] offset:5632\n\t" \
    "ds_read_b64_tr_b16 v[0:1], %[tr] offset:0\n\t" \
    "ds_read_b64_tr_b16 v[2:3], %[tr] offset:5632\n\t" \
    "ds_read_b64_tr_b16 v[24:25], %[tr] offset:32\n\t" \
    "ds_read_b64_tr_b16 v[26:27], %[tr] offset:5664\n\t" \
    "ds_read_b64_tr_b16 v[40:41], %[tr] offset:64\n\t" \
    "ds_read_b64_tr_b16 v[42:43], %[tr] offset:5696\n\t" \
    "ds_read_b64_tr_b16 v[64:65], %[tr] offset:96\n\t" \
    "ds_read_b64_tr_b16 v[66:67], %[tr] offset:5728\n\t" \
    "ds_read_b64_tr_b16 v[80:81], %[tr] offset:128\n\t" \
    "ds_read_b64_tr_b16 v[82:83], %[tr] offset:5760\n\t" \
    "ds_read_b64_tr_b16 v[104:105], %[tr] offset:160\n\t" \
    "ds_read_b64_tr_b16 v[106:107], %[tr] offset:5792\n\t" \
    "ds_read_b64_tr_b16 v[120:121], %[tr] offset:192\n\t" \
    "ds_read_b64_tr_b16 v[122:123], %[tr] offset:5824\n\t" \
    "ds_read_b64_tr_b16 v[144:145], %[tr] offset:224\n\t" \
    "ds_read_b64_tr_b16 v[146:147], %[tr] offset:5856\n\t" \
    "ds_read_b64_tr_b16 v[160:161], %[tr] offset:256\n\t" \
    "ds_read_b64_tr_b16 v[162:163], %[tr] offset:5888\n\t" \
    "ds_read_b64_tr_b16 v[184:185], %[tr] offset:288\n\t" \
    "ds_read_b64_tr_b16 v[186:187], %[tr] offset:5920\n\t" \
    "s_barrier \n\t" \
    "s_mov_b32 %[t], 0x19000\n\t" \
    "buffer_load_dwordx4 v[8:11], %[o0], %[rs], %[t] offen" WNT "\n\t" \
    "buffer_load_dwordx4 v[32:35], %[o1], %[rs], %[t] offen" WNT "\n\t" \
    "buffer_load_dwordx4 v[48:51], %[o2], %[rs], %[t] offen" WNT "\n\t" \
    "buffer_load_dwordx4 v[72:75], %[o3], %[rs], %[t] offen" WNT "\n\t" \
    "buffer_load_dwordx4 v[88:91], %[o4], %[rs], %[t] offen" WNT "\n\t" \
    "s_mov_b32 %[t], 0x23000\n\t" \
    "buffer_load_dwordx4 v[112:115], %[o0], %[rs], %[t] offen" WNT "\n\t" \
    "buffer_load_dwordx4 v[128:131], %[o1], %[rs], %[t] offen" WNT "\n\t" \
    "buffer_load_dwordx4 v[152:155], %[o2], %[rs], %[t] offen" WNT "\n\t" \
    "buffer_load_dwordx4 v[168:171], %[o3], %[rs], %[t] offen" WNT "\n\t" \
    "buffer_load_dwordx4 v[192:195], %[o4], %[rs], %[t] offen" WNT "\n\t" \
    "s_waitcnt lgkmcnt(0)\n\t" \
    "s_barrier \n\t" \
    "s_waitcnt vmcnt(15)\n\t" \
    "ds_write_b128 %[s0], v[4:7] offset:0\n\t" \
    "ds_write_b128 %[s1], v[28:31] offset:0\n\t" \
    "ds_write_b128 %[s2], v[44:47] offset:0\n\t" \
    "ds_write_b128 %[s3], v[68:71] offset:0\n\t" \
    "ds_write_b128 %[s4], v[84:87] offset:0\n\t" \
    "s_waitcnt vmcnt(10)\n\t" \
    "ds_write_b128 %[s0], v[108:111] offset:5632\n\t" \
    "ds_write_b128 %[s1], v[124:127] offset:5632\n\t" \
    "ds_write_b128 %[s2], v[148:151] offset:5632\n\t" \
    "ds_write_b128 %[s3], v[164:167] offset:5632\n\t" \
    "ds_write_b128 %[s4], v[188:191] offset:5632\n\t" \
    "ds_read_b64_tr_b16 v[4:5], %[tr] offset:0\n\t" \
    "ds_read_b64_tr_b16 v[6:7], %[tr] offset:5632\n\t" \
    "ds_read_b64_tr_b16 v[28:29], %[tr] offset:32\n\t" \
    "ds_read_b64_tr_b16 v[30:31], %[tr] offset:5664\n\t" \
    "ds_read_b64_tr_b16 v[44:45], %[tr] offset:64\n\t" \
    "ds_read_b64_tr_b16 v[46:47], %[tr] offset:5696\n\t" \
    "ds_read_b64_tr_b16 v[68:69], %[tr] offset:96\n\t" \
    "ds_read_b64_tr_b16 v[70:71], %[tr] offset:5728\n\t" \
    "ds_read_b64_tr_b16 v[84:85], %[tr] offset:128\n\t" \
    "ds_read_b64_tr_b16 v[86:87], %[tr] offset:5760\n\t" \
    "ds_read_b64_tr_b16 v[108:109], %[tr] offset:160\n\t" \
    "ds_read_b64_tr_b16 v[110:111], %[tr] offset:5792\n\t" \
    "ds_read_b64_tr_b16 v[124:125], %[tr] offset:192\n\t" \
    "ds_read_b64_tr_b16 v[126:127], %[tr] offset:5824\n\t" \
    "ds_read_b64_tr_b16 v[148:149], %[tr] offset:224\n\t" \
    "ds_read_b64_tr_b16 v[150:151], %[tr] offset:5856\n\t" \
    "ds_read_b64_tr_b16 v[164:165], %[tr] offset:256\n\t" \
    "ds_read_b64_tr_b16 v[166:167], %[tr] offset:5888\n\t" \
    "ds_read_b64_tr_b16 v[188:189], %[tr] offset:288\n\t" \
    "ds_read_b64_tr_b16 v[190:191], %[tr] offset:5920\n\t" \
    "s_barrier \n\t" \
    "s_mov_b32 %[t], 0x2d000\n\t" \
    "buffer_load_dwordx4 v[12:15], %[o0], %[rs], %[t] offen" WNT "\n\t" \
    "buffer_load_dwordx4 v[36:39], %[o1], %[rs], %[t] offen" WNT "\n\t" \
    "buffer_load_dwordx4 v[52:55], %[o2], %[rs], %[t] offen" WNT "\n\t" \
    "buffer_load_dwordx4 v[76:79], %[o3], %[rs], %[t] offen" WNT "\n\t" \
    "buffer_load_dwordx4 v[92:95], %[o4], %[rs], %[t] offen" WNT "\n\t" \
    "s_mov_b32 %[t], 0x37000\n\t" \
    "buffer_load_dwordx4 v[116:119], %[o0], %[rs], %[t] offen" WNT "\n\t" \
    "buffer_load_dwordx4 v[132:135], %[o1], %[rs], %[t] offen" WNT "\n\t" \
    "buffer_load_dwordx4 v[156:159], %[o2], %[rs], %[t] offen" WNT "\n\t" \
    "buffer_load_dwordx4 v[172:175], %[o3], %[rs], %[t] offen" WNT "\n\t" \
    "buffer_load_dwordx4 v[196:199], %[o4], %[rs], %[t] offen" WNT "\n\t" \
    "s_waitcnt lgkmcnt(0)\n\t" \
    "s_barrier \n\t" \
    "s_waitcnt vmcnt(15)\n\t" \
    "ds_write_b128 %[s0], v[8:11] offset:0\n\t" \
    "ds_write_b128 %[s1], v[32:35] offset:0\n\t" \
    "ds_write_b128 %[s2], v[48:51] offset:0\n\t" \
    "ds_write_b128 %[s3], v[72:75] offset:0\n\t" \
    "ds_write_b128 %[s4], v[88:91] offset:0\n\t" \
    "s_waitcnt vmcnt(10)\n\t" \
    "ds_write_b128 %[s0], v[112:115] offset:5632\n\t" \
    "ds_write_b128 %[s1], v[128:131] offset:5632\n\t" \
    "ds_write_b128 %[s2], v[152:155] offset:5632\n\t" \
    "ds_write_b128 %[s3], v[168:171] offset:5632\n\t" \
    "ds_write_b128 %[s4], v[192:195] offset:5632\n\t" \
    "ds_read_b64_tr_b16 v[8:9], %[tr] offset:0\n\t" \
    "ds_read_b64_tr_b16 v[10:11], %[tr] offset:5632\n\t" \
    "ds_read_b64_tr_b16 v[32:33], %[tr] offset:32\n\t" \
    "ds_read_b64_tr_b16 v[34:35], %[tr] offset:5664\n\t" \
    "ds_read_b64_tr_b16 v[48:49], %[tr] offset:64\n\t" \
    "ds_read_b64_tr_b16 v[50:51], %[tr] offset:5696\n\t" \
    "ds_read_b64_tr_b16 v[72:73], %[tr] offset:96\n\t" \
    "ds_read_b64_tr_b16 v[74:75], %[tr] offset:5728\n\t" \
    "ds_read_b64_tr_b16 v[88:89], %[tr] offset:128\n\t" \
    "ds_read_b64_tr_b16 v[90:91], %[tr] offset:5760\n\t" \
    "ds_read_b64_tr_b16 v[112:113], %[tr] offset:160\n\t" \
    "ds_read_b64_tr_b16 v[114:115], %[tr] offset:5792\n\t" \
    "ds_read_b64_tr_b16 v[128:129], %[tr] offset:192\n\t" \
    "ds_read_b64_tr_b16 v[130:131], %[tr] offset:5824\n\t" \
    "ds_read_b64_tr_b16 v[152:153], %[tr] offset:224\n\t" \
    "ds_read_b64_tr_b16 v[154:155], %[tr] offset:5856\n\t" \
    "ds_read_b64_tr_b16 v[168:169], %[tr] offset:256\n\t" \
    "ds_read_b64_tr_b16 v[170:171], %[tr] offset:5888\n\t" \
    "ds_read_b64_tr_b16 v[192:193], %[tr] offset:288\n\t" \
    "ds_read_b64_tr_b16 v[194:195], %[tr] offset:5920\n\t" \
    "s_waitcnt lgkmcnt(0)\n\t" \
    "s_barrier \n\t" \
    "s_waitcnt vmcnt(5)\n\t" \
    "ds_write_b128 %[s0], v[12:15] offset:0\n\t" \
    "ds_write_b128 %[s1], v[36:39] offset:0\n\t" \
    "ds_write_b128 %[s2], v[52:55] offset:0\n\t" \
    "ds_write_b128 %[s3], v[76:79] offset:0\n\t" \
    "ds_write_b128 %[s4], v[92:95] offset:0\n\t" \
    "s_waitcnt vmcnt(0)\n\t" \
    "ds_write_b128 %[s0], v[116:119] offset:5632\n\t" \
    "ds_write_b128 %[s1], v[132:135] offset:5632\n\t" \
    "ds_write_b128 %[s2], v[156:159] offset:5632\n\t" \
    "ds_write_b128 %[s3], v[172:175] offset:5632\n\t" \
    "ds_write_b128 %[s4], v[196:199] offset:5632\n\t" \
    "ds_read_b64_tr_b16 v[12:13], %[tr] offset:0\n\t" \
    "ds_read_b64_tr_b16 v[14:15], %[tr] offset:5632\n\t" \
    "ds_read_b64_tr_b16 v[36:37], %[tr] offset:32\n\t" \
    "ds_read_b64_tr_b16 v[38:39], %[tr] offset:5664\n\t" \
    "ds_read_b64_tr_b16 v[52:53], %[tr] offset:64\n\t" \
    "ds_read_b64_tr_b16 v[54:55], %[tr] offset:5696\n\t" \
    "ds_read_b64_tr_b16 v[76:77], %[tr] offset:96\n\t" \
    "ds_read_b64_tr_b16 v[78:79], %[tr] offset:5728\n\t" \
    "ds_read_b64_tr_b16 v[92:93], %[tr] offset:128\n\t" \
    "ds_read_b64_tr_b16 v[94:95], %[tr] offset:5760\n\t" \
    "ds_read_b64_tr_b16 v[116:117], %[tr] offset:160\n\t" \
    "ds_read_b64_tr_b16 v[118:119], %[tr] offset:5792\n\t" \
    "ds_read_b64_tr_b16 v[132:133], %[tr] offset:192\n\t" \
    "ds_read_b64_tr_b16 v[134:135], %[tr] offset:5824\n\t" \
    "ds_read_b64_tr_b16 v[156:157], %[tr] offset:224\n\t" \
    "ds_read_b64_tr_b16 v[158:159], %[tr] offset:5856\n\t" \
    "ds_read_b64_tr_b16 v[172:173], %[tr] offset:256\n\t" \
    "ds_read_b64_tr_b16 v[174:175], %[tr] offset:5888\n\t" \
    "ds_read_b64_tr_b16 v[196:197], %[tr] offset:288\n\t" \
    "ds_read_b64_tr_b16 v[198:199], %[tr] offset:5920\n\t" \
    "s_waitcnt lgkmcnt(0)\n\t" \
    "s_barrier \n\t" \
    "ds_write_b128 %[wl], v[200:203] offset:0\n\t" \
    "ds_write_b128 %[wl], v[204:207] offset:1024\n\t" \
    "ds_write_b128 %[wl], v[208:211] offset:2048\n\t" \
    "ds_write_b128 %[wl], v[212:215] offset:3072\n\t" \
    "ds_write_b128 %[wl], v[216:219] offset:4096\n\t" \
    "ds_write_b128 %[wl], v[220:223] offset:5120\n\t" \
    "ds_write_b128 %[wl], v[224:227] offset:6144\n\t" \
    "ds_write_b128 %[wl], v[228:231] offset:7168\n\t" \
    "ds_write_b128 %[wl], v[232:235] offset:8192\n\t" \
    "ds_write_b128 %[wl], v[236:239] offset:9216\n\t" \
    "s_waitcnt lgkmcnt(0)\n\t"
#define ASM_XG_S_LO \
    "s_waitcnt lgkmcnt(0)\n\t" \
    "ds_read_b128 v[224:227], %[xa] offset:16384\n\t" \
    "ds_read_b128 v[228:231], %[xa] offset:17408\n\t" \
    "ds_read_b128 v[244:247], %[wl] offset:0\n\t" \
    "ds_read_b128 v[248:251], %[wl] offset:1024\n\t" \
    "ds_read_b128 v[232:235], %[xa] offset:0\n\t" \
    "ds_read_b128 v[236:239], %[xa] offset:1024\n\t" \
    "v_xor_b32 v252, 0x800, %[xa]\n\t" \
    "ds_read_b128 v[240:243], v252 offset:0\n\t" \
    "s_waitcnt lgkmcnt(6)\n\t" \
    "v_mfma_f32_16x16x32_bf16 v[220:223], v[8:11], v[224:227], 0\n\t" \
    "s_waitcnt lgkmcnt(5)\n\t" \
    "v_mfma_f32_16x16x32_bf16 v[220:223], v[12:15], v[228:231], v[220:223]\n\t" \
    "v_mfma_f32_16x16x32_bf16 v[216:219], v[0:3], v[224:227], 0\n\t" \
    "v_mfma_f32_16x16x32_bf16 v[216:219], v[4:7], v[228:231], v[216:219]\n\t" \
    "s_waitcnt lgkmcnt(4)\n\t" \
    "v_mfma_f32_16x16x32_bf16 v[212:215], v[244:247], v[224:227], 0\n\t" \
    "ds_read_b128 v[224:227], v252 offset:1024\n\t" \
    "s_waitcnt lgkmcnt(4)\n\t" \
    "v_mfma_f32_16x16x32_bf16 v[212:215], v[248:251], v[228:231], v[212:215]\n\t" \
    "ds_read_b128 v[228:231], v252 offset:16384\n\t" \
    "s_waitcnt lgkmcnt(4)\n\t" \
    "v_mfma_f32_16x16x32_bf16 v[200:203], v[244:247], v[232:235], 0\n\t" \
    "ds_read_b128 v[244:247], %[wl] offset:2048\n\t" \
    "s_waitcnt lgkmcnt(4)\n\t" \
    "v_mfma_f32_16x16x32_bf16 v[200:203], v[248:251], v[236:239], v[200:203]\n\t" \
    "ds_read_b128 v[248:251], %[wl] offset:3072\n\t" \
    "v_mfma_f32_16x16x32_bf16 v[204:207], v[0:3], v[232:235], 0\n\t" \
    "v_mfma_f32_16x16x32_bf16 v[204:207], v[4:7], v[236:239], v[204:207]\n\t" \
    "v_mfma_f32_16x16x32_bf16 v[208:211], v[8:11], v[232:235], 0\n\t" \
    "ds_read_b128 v[232:235], v252 offset:17408\n\t" \
    "v_mfma_f32_16x16x32_bf16 v[208:211], v[12:15], v[236:239], v[208:211]\n\t" \
    "v_xor_b32 v252, 0x1000, %[xa]\n\t" \
    "ds_read_b128 v[236:239], v252 offset:16384\n\t" \
    "s_waitcnt lgkmcnt(6)\n\t" \
    "v_mfma_f32_16x16x32_bf16 v[208:211], v[32:35], v[240:243], v[208:211]\n\t" \
    "s_waitcnt lgkmcnt(5)\n\t" \
    "v_mfma_f32_16x16x32_bf16 v[208:211], v[36:39], v[224:227], v[208:211]\n\t" \
    "v_mfma_f32_16x16x32_bf16 v[204:207], v[24:27], v[240:243], v[204:207]\n\t" \
    "v_mfma_f32_16x16x32_bf16 v[204:207], v[28:31], v[224:227], v[204:207]\n\t" \
    "v_mfma_f32_16x16x32_bf16 v[200:203], v[16:19], v[240:243], v[200:203]\n\t" \
    "ds_read_b128 v[240:243], v252 offset:17408\n\t" \
    "v_mfma_f32_16x16x32_bf16 v[200:203], v[20:23], v[224:227], v[200:203]\n\t" \
    "ds_read_b128 v[224:227], v252 offset:0\n\t" \
    "s_waitcnt lgkmcnt(6)\n\t" \
    "v_mfma_f32_16x16x32_bf16 v[212:215], v[16:19], v[228:231], v[212:215]\n\t" \
    "s_waitcnt lgkmcnt(3)\n\t" \
    "v_mfma_f32_16x16x32_bf16 v[212:215], v[20:23], v[232:235], v[212:215]\n\t" \
    "v_mfma_f32_16x16x32_bf16 v[216:219], v[24:27], v[228:231], v[216:219]\n\t" \
    "v_mfma_f32_16x16x32_bf16 v[216:219], v[28:31], v[232:235], v[216:219]\n\t" \
    "v_mfma_f32_16x16x32_bf16 v[220:223], v[32:35], v[228:231], v[220:223]\n\t" \
    "ds_read_b128 v[228:231], v252 offset:1024\n\t" \
    "v_mfma_f32_16x16x32_bf16 v[220:223], v[36:39], v[232:235], v[220:223]\n\t" \
    "v_xor_b32 v252, 0x1800, %[xa]\n\t" \
    "ds_read_b128 v[232:235], v252 offset:0\n\t" \
    "s_waitcnt lgkmcnt(4)\n\t" \
    "v_mfma_f32_16x16x32_bf16 v[220:223], v[48:51], v[236:239], v[220:223]\n\t" \
    "s_waitcnt lgkmcnt(3)\n\t" \
    "v_mfma_f32_16x16x32_bf16 v[220:223], v[52:55], v[240:243], v[220:223]\n\t" \
    "v_mfma_f32_16x16x32_bf16 v[216:219], v[40:43], v[236:239], v[216:219]\n\t" \
    "v_mfma_f32_16x16x32_bf16 v[216:219], v[44:47], v[240:243], v[216:219]\n\t" \
    "v_mfma_f32_16x16x32_bf16 v[212:215], v[244:247], v[236:239], v[212:215]\n\t" \
    "ds_read_b128 v[236:239], v252 offset:1024\n\t" \
    "v_mfma_f32_16x16x32_bf16 v[212:215], v[248:251], v[240:243], v[212:215]\n\t" \
    "ds_read_b128 v[240:243], v252 offset:16384\n\t" \
    "s_waitcnt lgkmcnt(4)\n\t" \
    "v_mfma_f32_16x16x32_bf16 v[200:203], v[244:247], v[224:227], v[200:203]\n\t" \
    "ds_read_b128 v[244:247], %[wl] offset:8192\n\t" \
    "s_waitcnt lgkmcnt(4)\n\t" \
    "v_mfma_f32_16x16x32_bf16 v[200:203], v[248:251], v[228:231], v[200:203]\n\t" \
    "ds_read_b128 v[248:251], %[wl] offset:9216\n\t" \
    "v_mfma_f32_16x16x32_bf16 v[204:207], v[40:43], v[224:227], v[204:207]\n\t" \
    "v_mfma_f32_16x16x32_bf16 v[204:207], v[44:47], v[228:231], v[204:207]\n\t" \
    "v_mfma_f32_16x16x32_bf16 v[208:211], v[48:51], v[224:227], v[208:211]\n\t" \
    "ds_read_b128 v[224:227], v252 offset:17408\n\t" \
    "v_mfma_f32_16x16x32_bf16 v[208:211], v[52:55], v[228:231], v[208:211]\n\t" \
    "v_and_b32 v252, 0x3ff, %[xa]\n\t" \
    "ds_read_b128 v[228:231], v252 offset:45056\n\t" \
    "s_waitcnt lgkmcnt(6)\n\t" \
    "v_mfma_f32_16x16x32_bf16 v[208:211], v[72:75], v[232:235], v[208:211]\n\t" \
    "s_waitcnt lgkmcnt(5)\n\t" \
    "v_mfma_f32_16x16x32_bf16 v[208:211], v[76:79], v[236:239], v[208:211]\n\t" \
    "v_mfma_f32_16x16x32_bf16 v[204:207], v[64:67], v[232:235], v[204:207]\n\t" \
    "v_mfma_f32_16x16x32_bf16 v[204:207], v[68:71], v[236:239], v[204:207]\n\t" \
    "v_mfma_f32_16x16x32_bf16 v[200:203], v[56:59], v[232:235], v[200:203]\n\t" \
    "ds_read_b128 v[232:235], v252 offset:46080\n\t" \
    "v_mfma_f32_16x16x32_bf16 v[200:203], v[60:63], v[236:239], v[200:203]\n\t" \
    "ds_read_b128 v[236:239], v252 offset:40960\n\t" \
    "s_waitcnt lgkmcnt(6)\n\t" \
    "v_mfma_f32_16x16x32_bf16 v[212:215], v[56:59], v[240:243], v[212:215]\n\t" \
    "s_waitcnt lgkmcnt(3)\n\t" \
    "v_mfma_f32_16x16x32_bf16 v[212:215], v[60:63], v[224:227], v[212:215]\n\t" \
    "v_mfma_f32_16x16x32_bf16 v[216:219], v[64:67], v[240:243], v[216:219]\n\t" \
    "v_mfma_f32_16x16x32_bf16 v[216:219], v[68:71], v[224:227], v[216:219]\n\t" \
    "v_mfma_f32_16x16x32_bf16 v[220:223], v[72:75], v[240:243], v[220:223]\n\t" \
    "ds_read_b128 v[240:243], v252 offset:41984\n\t" \
    "v_mfma_f32_16x16x32_bf16 v[220:223], v[76:79], v[224:227], v[220:223]\n\t" \
    "s_waitcnt lgkmcnt(0)\n\t" \
    "s_barrier\n\t" ASM_ST \
    "v_xor_b32 v252, 0x2000, %[xa]\n\t" \
    "ds_read_b128 v[224:227], v252 offset:0\n\t" \
    "v_mfma_f32_16x16x32_bf16 v[220:223], v[168:171], v[228:231], v[220:223]\n\t" \
    "v_mfma_f32_16x16x32_bf16 v[220:223], v[172:175], v[232:235], v[220:223]\n\t" \
    "v_mfma_f32_16x16x32_bf16 v[216:219], v[160:163], v[228:231], v[216:219]\n\t" \
    "v_mfma_f32_16x16x32_bf16 v[216:219], v[164:167], v[232:235], v[216:219]\n\t" \
    "v_mfma_f32_16x16x32_bf16 v[212:215], v[244:247], v[228:231], v[212:215]\n\t" \
    "ds_read_b128 v[228:231], v252 offset:1024\n\t" \
    "v_mfma_f32_16x16x32_bf16 v[212:215], v[248:251], v[232:235], v[212:215]\n\t" \
    "ds_read_b128 v[232:235], v252 offset:16384\n\t" \
    "v_mfma_f32_16x16x32_bf16 v[200:203], v[244:247], v[236:239], v[200:203]\n\t" \
    "ds_read_b128 v[244:247], %[wl] offset:4096\n\t" \
    "v_mfma_f32_16x16x32_bf16 v[200:203], v[248:251], v[240:243], v[200:203]\n\t" \
    "ds_read_b128 v[248:251], %[wl] offset:5120\n\t" \
    "v_mfma_f32_16x16x32_bf16 v[204:207], v[160:163], v[236:239], v[204:207]\n\t" \
    "v_mfma_f32_16x16x32_bf16 v[204:207], v[164:167], v[240:243], v[204:207]\n\t" \
    "v_mfma_f32_16x16x32_bf16 v[208:211], v[168:171], v[236:239], v[208:211]\n\t" \
    "ds_read_b128 v[236:239], v252 offset:17408\n\t" \
    "v_mfma_f32_16x16x32_bf16 v[208:211], v[172:175], v[240:243], v[208:211]\n\t" \
    "v_xor_b32 v252, 0x2800, %[xa]\n\t" \
    "ds_read_b128 v[240:243], v252 offset:16384\n\t" \
    "s_waitcnt lgkmcnt(6)\n\t" \
    "v_mfma_f32_16x16x32_bf16 v[208:211], v[88:91], v[224:227], v[208:211]\n\t" \
    "s_waitcnt lgkmcnt(5)\n\t" \
    "v_mfma_f32_16x16x32_bf16 v[208:211], v[92:95], v[228:231], v[208:211]\n\t" \
    "v_mfma_f32_16x16x32_bf16 v[204:207], v[80:83], v[224:227], v[204:207]\n\t" \
    "v_mfma_f32_16x16x32_bf16 v[204:207], v[84:87], v[228:231], v[204:207]\n\t" \
    "s_waitcnt lgkmcnt(3)\n\t" \
    "v_mfma_f32_16x16x32_bf16 v[200:203], v[244:247], v[224:227], v[200:203]\n\t" \
    "ds_read_b128 v[224:227], v252 offset:17408\n\t" \
    "s_waitcnt lgkmcnt(3)\n\t" \
    "v_mfma_f32_16x16x32_bf16 v[200:203], v[248:251], v[228:231], v[200:203]\n\t" \
    "ds_read_b128 v[228:231], v252 offset:0\n\t" \
    "v_mfma_f32_16x16x32_bf16 v[212:215], v[244:247], v[232:235], v[212:215]\n\t" \
    "ds_read_b128 v[244:247], %[wl] offset:6144\n\t" \
    "s_waitcnt lgkmcnt(4)\n\t" \
    "v_mfma_f32_16x16x32_bf16 v[212:215], v[248:251], v[236:239], v[212:215]\n\t" \
    "ds_read_b128 v[248:251], %[wl] offset:7168\n\t" \
    "v_mfma_f32_16x16x32_bf16 v[216:219], v[80:83], v[232:235], v[216:219]\n\t" \
    "v_mfma_f32_16x16x32_bf16 v[216:219], v[84:87], v[236:239], v[216:219]\n\t" \
    "v_mfma_f32_16x16x32_bf16 v[220:223], v[88:91], v[232:235], v[220:223]\n\t" \
    "ds_read_b128 v[232:235], v252 offset:1024\n\t" \
    "v_mfma_f32_16x16x32_bf16 v[220:223], v[92:95], v[236:239], v[220:223]\n\t" \
    "v_xor_b32 v252, 0x3000, %[xa]\n\t" \
    "ds_read_b128 v[236:239], v252 offset:0\n\t" \
    "s_waitcnt lgkmcnt(6)\n\t" \
    "v_mfma_f32_16x16x32_bf16 v[220:223], v[112:115], v[240:243], v[220:223]\n\t" \
    "s_waitcnt lgkmcnt(5)\n\t" \
    "v_mfma_f32_16x16x32_bf16 v[220:223], v[116:119], v[224:227], v[220:223]\n\t" \
    "v_mfma_f32_16x16x32_bf16 v[216:219], v[104:107], v[240:243], v[216:219]\n\t" \
    "v_mfma_f32_16x16x32_bf16 v[216:219], v[108:111], v[224:227], v[216:219]\n\t" \
    "v_mfma_f32_16x16x32_bf16 v[212:215], v[96:99], v[240:243], v[212:215]\n\t" \
    "ds_read_b128 v[240:243], v252 offset:1024\n\t" \
    "v_mfma_f32_16x16x32_bf16 v[212:215], v[100:103], v[224:227], v[212:215]\n\t" \
    "ds_read_b128 v[224:227], v252 offset:16384\n\t" \
    "s_waitcnt lgkmcnt(6)\n\t" \
    "v_mfma_f32_16x16x32_bf16 v[200:203], v[96:99], v[228:231], v[200:203]\n\t" \
    "s_waitcnt lgkmcnt(3)\n\t" \
    "v_mfma_f32_16x16x32_bf16 v[200:203], v[100:103], v[232:235], v[200:203]\n\t" \
    "v_mfma_f32_16x16x32_bf16 v[204:207], v[104:107], v[228:231], v[204:207]\n\t" \
    "v_mfma_f32_16x16x32_bf16 v[204:207], v[108:111], v[232:235], v[204:207]\n\t" \
    "v_mfma_f32_16x16x32_bf16 v[208:211], v[112:115], v[228:231], v[208:211]\n\t" \
    "ds_read_b128 v[228:231], v252 offset:17408\n\t" \
    "v_mfma_f32_16x16x32_bf16 v[208:211], v[116:119], v[232:235], v[208:211]\n\t" \
    "v_xor_b32 v252, 0x3800, %[xa]\n\t" \
    "ds_read_b128 v[232:235], v252 offset:16384\n\t" \
    "s_waitcnt lgkmcnt(4)\n\t" \
    "v_mfma_f32_16x16x32_bf16 v[208:211], v[128:131], v[236:239], v[208:211]\n\t" \
    "s_waitcnt lgkmcnt(3)\n\t" \
    "v_mfma_f32_16x16x32_bf16 v[208:211], v[132:135], v[240:243], v[208:211]\n\t" \
    "v_mfma_f32_16x16x32_bf16 v[204:207], v[120:123], v[236:239], v[204:207]\n\t" \
    "v_mfma_f32_16x16x32_bf16 v[204:207], v[124:127], v[240:243], v[204:207]\n\t" \
    "v_mfma_f32_16x16x32_bf16 v[200:203], v[244:247], v[236:239], v[200:203]\n\t" \
    "ds_read_b128 v[236:239], v252 offset:17408\n\t" \
    "v_mfma_f32_16x16x32_bf16 v[200:203], v[248:251], v[240:243], v[200:203]\n\t" \
    "ds_read_b128 v[240:243], v252 offset:0\n\t" \
    "s_waitcnt lgkmcnt(4)\n\t" \
    "v_mfma_f32_16x16x32_bf16 v[212:215], v[244:247], v[224:227], v[212:215]\n\t" \
    "s_waitcnt lgkmcnt(3)\n\t" \
    "v_mfma_f32_16x16x32_bf16 v[212:215], v[248:251], v[228:231], v[212:215]\n\t" \
    "v_mfma_f32_16x16x32_bf16 v[216:219], v[120:123], v[224:227], v[216:219]\n\t" \
    "v_mfma_f32_16x16x32_bf16 v[216:219], v[124:127], v[228:231], v[216:219]\n\t" \
    "v_mfma_f32_16x16x32_bf16 v[220:223], v[128:131], v[224:227], v[220:223]\n\t" \
    "ds_read_b128 v[224:227], v252 offset:1024\n\t" \
    "v_mfma_f32_16x16x32_bf16 v[220:223], v[132:135], v[228:231], v[220:223]\n\t" \
    "v_and_b32 v252, 0x3ff, %[xa]\n\t" \
    "ds_read_b128 v[228:231], v252 offset:43008\n\t" \
    "s_waitcnt lgkmcnt(4)\n\t" \
    "v_mfma_f32_16x16x32_bf16 v[220:223], v[152:155], v[232:235], v[220:223]\n\t" \
    "v_mov_b64 v[244:245], 0\n\t" \
    "s_waitcnt lgkmcnt(3)\n\t" \
    "v_mfma_f32_16x16x32_bf16 v[220:223], v[156:159], v[236:239], v[220:223]\n\t" \
    "v_mov_b64 v[246:247], 0\n\t" \
    "v_mfma_f32_16x16x32_bf16 v[216:219], v[144:147], v[232:235], v[216:219]\n\t" \
    "v_mov_b64 v[248:249], 0\n\t" \
    "v_mfma_f32_16x16x32_bf16 v[216:219], v[148:151], v[236:239], v[216:219]\n\t" \
    "v_mov_b64 v[250:251], 0\n\t" \
    "v_mfma_f32_16x16x32_bf16 v[212:215], v[136:139], v[232:235], v[212:215]\n\t" \
    "ds_read_b128 v[232:235], v252 offset:44032\n\t" \
    "v_mfma_f32_16x16x32_bf16 v[212:215], v[140:143], v[236:239], v[212:215]\n\t" \
    "ds_read_b128 v[236:239], v252 offset:47104\n\t" \
    "s_waitcnt lgkmcnt(4)\n\t" \
    "v_mfma_f32_16x16x32_bf16 v[200:203], v[136:139], v[240:243], v[200:203]\n\t" \
    "s_waitcnt lgkmcnt(3)\n\t" \
    "v_mfma_f32_16x16x32_bf16 v[200:203], v[140:143], v[224:227], v[200:203]\n\t" \
    "v_mfma_f32_16x16x32_bf16 v[204:207], v[144:147], v[240:243], v[204:207]\n\t" \
    "v_mfma_f32_16x16x32_bf16 v[204:207], v[148:151], v[224:227], v[204:207]\n\t" \
    "v_mfma_f32_16x16x32_bf16 v[208:211], v[152:155], v[240:243], v[208:211]\n\t" \
    "ds_read_b128 v[240:243], v252 offset:48128\n\t" \
    "v_mfma_f32_16x16x32_bf16 v[208:211], v[156:159], v[224:227], v[208:211]\n\t" \
    "s_waitcnt lgkmcnt(3)\n\t" \
    "v_mfma_f32_16x16x32_bf16 v[208:211], v[192:195], v[228:231], v[208:211]\n\t" \
    "v_add_u32 v252, %[qs], v252\n\t" \
    "v_mov_b64 v[224:225], 0\n\t" \
    "s_waitcnt lgkmcnt(2)\n\t" \
    "v_mfma_f32_16x16x32_bf16 v[208:211], v[196:199], v[232:235], v[208:211]\n\t" \
    "v_mov_b64 v[226:227], 0\n\t" \
    "v_mfma_f32_16x16x32_bf16 v[204:207], v[184:187], v[228:231], v[204:207]\n\t" \
    "v_mfma_f32_16x16x32_bf16 v[204:207], v[188:191], v[232:235], v[204:207]\n\t" \
    "v_mfma_f32_16x16x32_bf16 v[200:203], v[176:179], v[228:231], v[200:203]\n\t" \
    "v_mfma_f32_16x16x32_bf16 v[200:203], v[180:183], v[232:235], v[200:203]\n\t" \
    "s_waitcnt lgkmcnt(1)\n\t" \
    "v_mfma_f32_16x16x32_bf16 v[212:215], v[176:179], v[236:239], v[212:215]\n\t" \
    "ds_read_b128 v[228:231], %[wl] offset:2048\n\t" \
    "ds_read_b128 v[232:235], %[wl] offset:3072\n\t" \
    "s_waitcnt lgkmcnt(2)\n\t" \
    "v_mfma_f32_16x16x32_bf16 v[212:215], v[180:183], v[240:243], v[212:215]\n\t" \
    "v_cvt_pk_bf16_f32 v208, v208, v209\n\t" \
    "v_cvt_pk_bf16_f32 v209, v210, v211\n\t" \
    "v_mfma_f32_16x16x32_bf16 v[216:219], v[184:187], v[236:239], v[216:219]\n\t" \
    "ds_write_b64 v252, v[208:209] offset:2048\n\t" \
    "v_cvt_pk_bf16_f32 v204, v204, v205\n\t" \
    "v_mfma_f32_16x16x32_bf16 v[216:219], v[188:191], v[240:243], v[216:219]\n\t" \
    "v_cvt_pk_bf16_f32 v205, v206, v207\n\t" \
    "ds_write_b64 v252, v[204:205] offset:1024\n\t" \
    "v_mfma_f32_16x16x32_bf16 v[220:223], v[192:195], v[236:239], v[220:223]\n\t" \
    "v_cvt_pk_bf16_f32 v200, v200, v201\n\t" \
    "v_cvt_pk_bf16_f32 v201, v202, v203\n\t" \
    "v_mfma_f32_16x16x32_bf16 v[220:223], v[196:199], v[240:243], v[220:223]\n\t" \
    "ds_write_b64 v252, v[200:201] offset:0\n\t" \
    "ds_read_b128 v[200:203], %[wl] offset:0\n\t" \
    "ds_read_b128 v[204:207], %[wl] offset:1024\n\t" \
    "v_cvt_pk_bf16_f32 v212, v212, v213\n\t" \
    "v_cvt_pk_bf16_f32 v213, v214, v215\n\t" \
    "ds_write_b64 v252, v[212:213] offset:6144\n\t" \
    "v_cvt_pk_bf16_f32 v216, v216, v217\n\t" \
    "v_cvt_pk_bf16_f32 v217, v218, v219\n\t" \
    "ds_write_b64 v252, v[216:217] offset:7168\n\t" \
    "v_cvt_pk_bf16_f32 v220, v220, v221\n\t" \
    "v_cvt_pk_bf16_f32 v221, v222, v223\n\t" \
    "ds_write_b64 v252, v[220:221] offset:8192\n\t" \
    "s_waitcnt lgkmcnt(0)\n\t"
#define ASM_XG_S_HI \
    "s_waitcnt lgkmcnt(0)\n\t" \
    "v_xor_b32 v252, 0x2000, %[xa]\n\t" \
    "ds_read_b128 v[224:227], v252 offset:16384\n\t" \
    "ds_read_b128 v[228:231], v252 offset:17408\n\t" \
    "ds_read_b128 v[244:247], %[wl] offset:4096\n\t" \
    "ds_read_b128 v[248:251], %[wl] offset:5120\n\t" \
    "ds_read_b128 v[232:235], v252 offset:0\n\t" \
    "ds_read_b128 v[236:239], v252 offset:1024\n\t" \
    "v_xor_b32 v252, 0x2800, %[xa]\n\t" \
    "ds_read_b128 v[240:243], v252 offset:0\n\t" \
    "s_cmp_eq_u32 %[slp], 0\n\t" \
    "s_cbranch_scc1 .Lxs%=\n\t" \
    "s_sleep 10\n\t" \
    ".Lxs%=:\n\t" \
    "s_waitcnt lgkmcnt(6)\n\t" \
    "v_mfma_f32_16x16x32_bf16 v[220:223], v[88:91], v[224:227], 0\n\t" \
    "s_waitcnt lgkmcnt(5)\n\t" \
    "v_mfma_f32_16x16x32_bf16 v[220:223], v[92:95], v[228:231], v[220:223]\n\t" \
    "v_mfma_f32_16x16x32_bf16 v[216:219], v[80:83], v[224:227], 0\n\t" \
    "v_mfma_f32_16x16x32_bf16 v[216:219], v[84:87], v[228:231], v[216:219]\n\t" \
    "s_waitcnt lgkmcnt(4)\n\t" \
    "v_mfma_f32_16x16x32_bf16 v[212:215], v[244:247], v[224:227], 0\n\t" \
    "ds_read_b128 v[224:227], v252 offset:1024\n\t" \
    "s_waitcnt lgkmcnt(4)\n\t" \
    "v_mfma_f32_16x16x32_bf16 v[212:215], v[248:251], v[228:231], v[212:215]\n\t" \
    "ds_read_b128 v[228:231], v252 offset:16384\n\t" \
    "s_waitcnt lgkmcnt(4)\n\t" \
    "v_mfma_f32_16x16x32_bf16 v[200:203], v[244:247], v[232:235], 0\n\t" \
    "ds_read_b128 v[244:247], %[wl] offset:6144\n\t" \
    "s_waitcnt lgkmcnt(4)\n\t" \
    "v_mfma_f32_16x16x32_bf16 v[200:203], v[248:251], v[236:239], v[200:203]\n\t" \
    "ds_read_b128 v[248:251], %[wl] offset:7168\n\t" \
    "v_mfma_f32_16x16x32_bf16 v[204:207], v[80:83], v[232:235], 0\n\t" \
    "v_mfma_f32_16x16x32_bf16 v[204:207], v[84:87], v[236:239], v[204:207]\n\t" \
    "v_mfma_f32_16x16x32_bf16 v[208:211], v[88:91], v[232:235], 0\n\t" \
    "ds_read_b128 v[232:235], v252 offset:17408\n\t" \
    "v_mfma_f32_16x16x32_bf16 v[208:211], v[92:95], v[236:239], v[208:211]\n\t" \
    "v_xor_b32 v252, 0x3000, %[xa]\n\t" \
    "ds_read_b128 v[236:239], v252 offset:16384\n\t" \
    "s_waitcnt lgkmcnt(6)\n\t" \
    "v_mfma_f32_16x16x32_bf16 v[208:211], v[112:115], v[240:243], v[208:211]\n\t" \
    "s_waitcnt lgkmcnt(5)\n\t" \
    "v_mfma_f32_16x16x32_bf16 v[208:211], v[116:119], v[224:227], v[208:211]\n\t" \
    "v_mfma_f32_16x16x32_bf16 v[204:207], v[104:107], v[240:243], v[204:207]\n\t" \
    "v_mfma_f32_16x16x32_bf16 v[204:207], v[108:111], v[224:227], v[204:207]\n\t" \
    "v_mfma_f32_16x16x32_bf16 v[200:203], v[96:99], v[240:243], v[200:203]\n\t" \
    "ds_read_b128 v[240:243], v252 offset:17408\n\t" \
    "v_mfma_f32_16x16x32_bf16 v[200:203], v[100:103], v[224:227], v[200:203]\n\t" \
    "ds_read_b128 v[224:227], v252 offset:0\n\t" \
    "s_waitcnt lgkmcnt(6)\n\t" \
    "v_mfma_f32_16x16x32_bf16 v[212:215], v[96:99], v[228:231], v[212:215]\n\t" \
    "s_waitcnt lgkmcnt(3)\n\t" \
    "v_mfma_f32_16x16x32_bf16 v[212:215], v[100:103], v[232:235], v[212:215]\n\t" \
    "v_mfma_f32_16x16x32_bf16 v[216:219], v[104:107], v[228:231], v[216:219]\n\t" \
    "v_mfma_f32_16x16x32_bf16 v[216:219], v[108:111], v[232:235], v[216:219]\n\t" \
    "v_mfma_f32_16x16x32_bf16 v[220:223], v[112:115], v[228:231], v[220:223]\n\t" \
    "ds_read_b128 v[228:231], v252 offset:1024\n\t" \
    "v_mfma_f32_16x16x32_bf16 v[220:223], v[116:119], v[232:235], v[220:223]\n\t" \
    "v_xor_b32 v252, 0x3800, %[xa]\n\t" \
    "ds_read_b128 v[232:235], v252 offset:0\n\t" \
    "s_waitcnt lgkmcnt(4)\n\t" \
    "v_mfma_f32_16x16x32_bf16 v[220:223], v[128:131], v[236:239], v[220:223]\n\t" \
    "s_waitcnt lgkmcnt(3)\n\t" \
    "v_mfma_f32_16x16x32_bf16 v[220:223], v[132:135], v[240:243], v[220:223]\n\t" \
    "v_mfma_f32_16x16x32_bf16 v[216:219], v[120:123], v[236:239], v[216:219]\n\t" \
    "v_mfma_f32_16x16x32_bf16 v[216:219], v[124:127], v[240:243], v[216:219]\n\t" \
    "v_mfma_f32_16x16x32_bf16 v[212:215], v[244:247], v[236:239], v[212:215]\n\t" \
    "ds_read_b128 v[236:239], v252 offset:1024\n\t" \
    "v_mfma_f32_16x16x32_bf16 v[212:215], v[248:251], v[240:243], v[212:215]\n\t" \
    "ds_read_b128 v[240:243], v252 offset:16384\n\t" \
    "s_waitcnt lgkmcnt(4)\n\t" \
    "v_mfma_f32_16x16x32_bf16 v[200:203], v[244:247], v[224:227], v[200:203]\n\t" \
    "ds_read_b128 v[244:247], %[wl] offset:8192\n\t" \
    "s_waitcnt lgkmcnt(4)\n\t" \
    "v_mfma_f32_16x16x32_bf16 v[200:203], v[248:251], v[228:231], v[200:203]\n\t" \
    "ds_read_b128 v[248:251], %[wl] offset:9216\n\t" \
    "v_mfma_f32_16x16x32_bf16 v[204:207], v[120:123], v[224:227], v[204:207]\n\t" \
    "v_mfma_f32_16x16x32_bf16 v[204:207], v[124:127], v[228:231], v[204:207]\n\t" \
    "v_mfma_f32_16x16x32_bf16 v[208:211], v[128:131], v[224:227], v[208:211]\n\t" \
    "ds_read_b128 v[224:227], v252 offset:17408\n\t" \
    "v_mfma_f32_16x16x32_bf16 v[208:211], v[132:135], v[228:231], v[208:211]\n\t" \
    "v_and_b32 v252, 0x3ff, %[xa]\n\t" \
    "ds_read_b128 v[228:231], v252 offset:45056\n\t" \
    "s_waitcnt lgkmcnt(6)\n\t" \
    "v_mfma_f32_16x16x32_bf16 v[208:211], v[152:155], v[232:235], v[208:211]\n\t" \
    "s_waitcnt lgkmcnt(5)\n\t" \
    "v_mfma_f32_16x16x32_bf16 v[208:211], v[156:159], v[236:239], v[208:211]\n\t" \
    "v_mfma_f32_16x16x32_bf16 v[204:207], v[144:147], v[232:235], v[204:207]\n\t" \
    "v_mfma_f32_16x16x32_bf16 v[204:207], v[148:151], v[236:239], v[204:207]\n\t" \
    "v_mfma_f32_16x16x32_bf16 v[200:203], v[136:139], v[232:235], v[200:203]\n\t" \
    "ds_read_b128 v[232:235], v252 offset:46080\n\t" \
    "v_mfma_f32_16x16x32_bf16 v[200:203], v[140:143], v[236:239], v[200:203]\n\t" \
    "ds_read_b128 v[236:239], v252 offset:40960\n\t" \
    "s_waitcnt lgkmcnt(6)\n\t" \
    "v_mfma_f32_16x16x32_bf16 v[212:215], v[136:139], v[240:243], v[212:215]\n\t" \
    "s_waitcnt lgkmcnt(3)\n\t" \
    "v_mfma_f32_16x16x32_bf16 v[212:215], v[140:143], v[224:227], v[212:215]\n\t" \
    "v_mfma_f32_16x16x32_bf16 v[216:219], v[144:147], v[240:243], v[216:219]\n\t" \
    "v_mfma_f32_16x16x32_bf16 v[216:219], v[148:151], v[224:227], v[216:219]\n\t" \
    "v_mfma_f32_16x16x32_bf16 v[220:223], v[152:155], v[240:243], v[220:223]\n\t" \
    "ds_read_b128 v[240:243], v252 offset:41984\n\t" \
    "v_mfma_f32_16x16x32_bf16 v[220:223], v[156:159], v[224:227], v[220:223]\n\t" \
    "s_waitcnt vmcnt(0) lgkmcnt(0)\n\t" \
    "s_barrier\n\t" ASM_ST \
    "s_mov_b32 m0, %[d0]\n\t" \
    "s_nop 0\n\t" \
    "global_load_lds_dwordx4 v252, %[g0] offset:0 sc1\n\t" \
    "global_load_lds_dwordx4 v252, %[g0] offset:1024 sc1\n\t" \
    "global_load_lds_dwordx4 v252, %[g0] offset:2048 sc1\n\t" \
    "global_load_lds_dwordx4 v252, %[g0] offset:3072 sc1\n\t" \
    "s_mov_b32 m0, %[d1]\n\t" \
    "s_nop 0\n\t" \
    "global_load_lds_dwordx4 v252, %[g1] sc1\n\t" \
    "ds_read_b128 v[224:227], %[xa] offset:0\n\t" \
    "v_mfma_f32_16x16x32_bf16 v[220:223], v[168:171], v[228:231], v[220:223]\n\t" \
    "v_mfma_f32_16x16x32_bf16 v[220:223], v[172:175], v[232:235], v[220:223]\n\t" \
    "v_mfma_f32_16x16x32_bf16 v[216:219], v[160:163], v[228:231], v[216:219]\n\t" \
    "v_mfma_f32_16x16x32_bf16 v[216:219], v[164:167], v[232:235], v[216:219]\n\t" \
    "v_mfma_f32_16x16x32_bf16 v[212:215], v[244:247], v[228:231], v[212:215]\n\t" \
    "ds_read_b128 v[228:231], %[xa] offset:1024\n\t" \
    "v_mfma_f32_16x16x32_bf16 v[212:215], v[248:251], v[232:235], v[212:215]\n\t" \
    "ds_read_b128 v[232:235], %[xa] offset:16384\n\t" \
    "v_mfma_f32_16x16x32_bf16 v[200:203], v[244:247], v[236:239], v[200:203]\n\t" \
    "ds_read_b128 v[244:247], %[wl] offset:0\n\t" \
    "v_mfma_f32_16x16x32_bf16 v[200:203], v[248:251], v[240:243], v[200:203]\n\t" \
    "ds_read_b128 v[248:251], %[wl] offset:1024\n\t" \
    "v_mfma_f32_16x16x32_bf16 v[204:207], v[160:163], v[236:239], v[204:207]\n\t" \
    "v_mfma_f32_16x16x32_bf16 v[204:207], v[164:167], v[240:243], v[204:207]\n\t" \
    "v_mfma_f32_16x16x32_bf16 v[208:211], v[168:171], v[236:239], v[208:211]\n\t" \
    "ds_read_b128 v[236:239], %[xa] offset:17408\n\t" \
    "v_mfma_f32_16x16x32_bf16 v[208:211], v[172:175], v[240:243], v[208:211]\n\t" \
    "v_xor_b32 v252, 0x800, %[xa]\n\t" \
    "ds_read_b128 v[240:243], v252 offset:16384\n\t" \
    "s_waitcnt lgkmcnt(6)\n\t" \
    "v_mfma_f32_16x16x32_bf16 v[208:211], v[8:11], v[224:227], v[208:211]\n\t" \
    "s_waitcnt lgkmcnt(5)\n\t" \
    "v_mfma_f32_16x16x32_bf16 v[208:211], v[12:15], v[228:231], v[208:211]\n\t" \
    "v_mfma_f32_16x16x32_bf16 v[204:207], v[0:3], v[224:227], v[204:207]\n\t" \
    "v_mfma_f32_16x16x32_bf16 v[204:207], v[4:7], v[228:231], v[204:207]\n\t" \
    "s_waitcnt lgkmcnt(3)\n\t" \
    "v_mfma_f32_16x16x32_bf16 v[200:203], v[244:247], v[224:227], v[200:203]\n\t" \
    "ds_read_b128 v[224:227], v252 offset:17408\n\t" \
    "s_waitcnt lgkmcnt(3)\n\t" \
    "v_mfma_f32_16x16x32_bf16 v[200:203], v[248:251], v[228:231], v[200:203]\n\t" \
    "ds_read_b128 v[228:231], v252 offset:0\n\t" \
    "v_mfma_f32_16x16x32_bf16 v[212:215], v[244:247], v[232:235], v[212:215]\n\t" \
    "ds_read_b128 v[244:247], %[wl] offset:2048\n\t" \
    "s_waitcnt lgkmcnt(4)\n\t" \
    "v_mfma_f32_16x16x32_bf16 v[212:215], v[248:251], v[236:239], v[212:215]\n\t" \
    "ds_read_b128 v[248:251], %[wl] offset:3072\n\t" \
    "v_mfma_f32_16x16x32_bf16 v[216:219], v[0:3], v[232:235], v[216:219]\n\t" \
    "v_mfma_f32_16x16x32_bf16 v[216:219], v[4:7], v[236:239], v[216:219]\n\t" \
    "v_mfma_f32_16x16x32_bf16 v[220:223], v[8:11], v[232:235], v[220:223]\n\t" \
    "ds_read_b128 v[232:235], v252 offset:1024\n\t" \
    "v_mfma_f32_16x16x32_bf16 v[220:223], v[12:15], v[236:239], v[220:223]\n\t" \
    "v_xor_b32 v252, 0x1000, %[xa]\n\t" \
    "ds_read_b128 v[236:239], v252 offset:0\n\t" \
    "s_waitcnt lgkmcnt(6)\n\t" \
    "v_mfma_f32_16x16x32_bf16 v[220:223], v[32:35], v[240:243], v[220:223]\n\t" \
    "s_waitcnt lgkmcnt(5)\n\t" \
    "v_mfma_f32_16x16x32_bf16 v[220:223], v[36:39], v[224:227], v[220:223]\n\t" \
    "v_mfma_f32_16x16x32_bf16 v[216:219], v[24:27], v[240:243], v[216:219]\n\t" \
    "v_mfma_f32_16x16x32_bf16 v[216:219], v[28:31], v[224:227], v[216:219]\n\t" \
    "v_mfma_f32_16x16x32_bf16 v[212:215], v[16:19], v[240:243], v[212:215]\n\t" \
    "ds_read_b128 v[240:243], v252 offset:1024\n\t" \
    "v_mfma_f32_16x16x32_bf16 v[212:215], v[20:23], v[224:227], v[212:215]\n\t" \
    "ds_read_b128 v[224:227], v252 offset:16384\n\t" \
    "s_waitcnt lgkmcnt(6)\n\t" \
    "v_mfma_f32_16x16x32_bf16 v[200:203], v[16:19], v[228:231], v[200:203]\n\t" \
    "s_waitcnt lgkmcnt(3)\n\t" \
    "v_mfma_f32_16x16x32_bf16 v[200:203], v[20:23], v[232:235], v[200:203]\n\t" \
    "v_mfma_f32_16x16x32_bf16 v[204:207], v[24:27], v[228:231], v[204:207]\n\t" \
    "v_mfma_f32_16x16x32_bf16 v[204:207], v[28:31], v[232:235], v[204:207]\n\t" \
    "v_mfma_f32_16x16x32_bf16 v[208:211], v[32:35], v[228:231], v[208:211]\n\t" \
    "ds_read_b128 v[228:231], v252 offset:17408\n\t" \
    "v_mfma_f32_16x16x32_bf16 v[208:211], v[36:39], v[232:235], v[208:211]\n\t" \
    "v_xor_b32 v252, 0x1800, %[xa]\n\t" \
    "ds_read_b128 v[232:235], v252 offset:16384\n\t" \
    "s_waitcnt lgkmcnt(4)\n\t" \
    "v_mfma_f32_16x16x32_bf16 v[208:211], v[48:51], v[236:239], v[208:211]\n\t" \
    "s_waitcnt lgkmcnt(3)\n\t" \
    "v_mfma_f32_16x16x32_bf16 v[208:211], v[52:55], v[240:243], v[208:211]\n\t" \
    "v_mfma_f32_16x16x32_bf16 v[204:207], v[40:43], v[236:239], v[204:207]\n\t" \
    "v_mfma_f32_16x16x32_bf16 v[204:207], v[44:47], v[240:243], v[204:207]\n\t" \
    "v_mfma_f32_16x16x32_bf16 v[200:203], v[244:247], v[236:239], v[200:203]\n\t" \
    "ds_read_b128 v[236:239], v252 offset:17408\n\t" \
    "v_mfma_f32_16x16x32_bf16 v[200:203], v[248:251], v[240:243], v[200:203]\n\t" \
    "ds_read_b128 v[240:243], v252 offset:0\n\t" \
    "s_waitcnt lgkmcnt(4)\n\t" \
    "v_mfma_f32_16x16x32_bf16 v[212:215], v[244:247], v[224:227], v[212:215]\n\t" \
    "s_waitcnt lgkmcnt(3)\n\t" \
    "v_mfma_f32_16x16x32_bf16 v[212:215], v[248:251], v[228:231], v[212:215]\n\t" \
    "v_mfma_f32_16x16x32_bf16 v[216:219], v[40:43], v[224:227], v[216:219]\n\t" \
    "v_mfma_f32_16x16x32_bf16 v[216:219], v[44:47], v[228:231], v[216:219]\n\t" \
    "v_mfma_f32_16x16x32_bf16 v[220:223], v[48:51], v[224:227], v[220:223]\n\t" \
    "ds_read_b128 v[224:227], v252 offset:1024\n\t" \
    "v_mfma_f32_16x16x32_bf16 v[220:223], v[52:55], v[228:231], v[220:223]\n\t" \
    "v_and_b32 v252, 0x3ff, %[xa]\n\t" \
    "ds_read_b128 v[228:231], v252 offset:43008\n\t" \
    "s_waitcnt lgkmcnt(4)\n\t" \
    "v_mfma_f32_16x16x32_bf16 v[220:223], v[72:75], v[232:235], v[220:223]\n\t" \
    "v_mov_b64 v[244:245], 0\n\t" \
    "s_waitcnt lgkmcnt(3)\n\t" \
    "v_mfma_f32_16x16x32_bf16 v[220:223], v[76:79], v[236:239], v[220:223]\n\t" \
    "v_mov_b64 v[246:247], 0\n\t" \
    "v_mfma_f32_16x16x32_bf16 v[216:219], v[64:67], v[232:235], v[216:219]\n\t" \
    "v_mov_b64 v[248:249], 0\n\t" \
    "v_mfma_f32_16x16x32_bf16 v[216:219], v[68:71], v[236:239], v[216:219]\n\t" \
    "v_mov_b64 v[250:251], 0\n\t" \
    "v_mfma_f32_16x16x32_bf16 v[212:215], v[56:59], v[232:235], v[212:215]\n\t" \
    "ds_read_b128 v[232:235], v252 offset:44032\n\t" \
    "v_mfma_f32_16x16x32_bf16 v[212:215], v[60:63], v[236:239], v[212:215]\n\t" \
    "ds_read_b128 v[236:239], v252 offset:47104\n\t" \
    "s_waitcnt lgkmcnt(4)\n\t" \
    "v_mfma_f32_16x16x32_bf16 v[200:203], v[56:59], v[240:243], v[200:203]\n\t" \
    "s_waitcnt lgkmcnt(3)\n\t" \
    "v_mfma_f32_16x16x32_bf16 v[200:203], v[60:63], v[224:227], v[200:203]\n\t" \
    "v_mfma_f32_16x16x32_bf16 v[204:207], v[64:67], v[240:243], v[204:207]\n\t" \
    "v_mfma_f32_16x16x32_bf16 v[204:207], v[68:71], v[224:227], v[204:207]\n\t" \
    "v_mfma_f32_16x16x32_bf16 v[208:211], v[72:75], v[240:243], v[208:211]\n\t" \
    "ds_read_b128 v[240:243], v252 offset:48128\n\t" \
    "v_mfma_f32_16x16x32_bf16 v[208:211], v[76:79], v[224:227], v[208:211]\n\t" \
    "s_waitcnt lgkmcnt(3)\n\t" \
    "v_mfma_f32_16x16x32_bf16 v[208:211], v[192:195], v[228:231], v[208:211]\n\t" \
    "v_add_u32 v252, %[qs], v252\n\t" \
    "v_mov_b64 v[224:225], 0\n\t" \
    "s_waitcnt lgkmcnt(2)\n\t" \
    "v_mfma_f32_16x16x32_bf16 v[208:211], v[196:199], v[232:235], v[208:211]\n\t" \
    "v_mov_b64 v[226:227], 0\n\t" \
    "v_mfma_f32_16x16x32_bf16 v[204:207], v[184:187], v[228:231], v[204:207]\n\t" \
    "v_mfma_f32_16x16x32_bf16 v[204:207], v[188:191], v[232:235], v[204:207]\n\t" \
    "v_mfma_f32_16x16x32_bf16 v[200:203], v[176:179], v[228:231], v[200:203]\n\t" \
    "v_mfma_f32_16x16x32_bf16 v[200:203], v[180:183], v[232:235], v[200:203]\n\t" \
    "s_waitcnt lgkmcnt(1)\n\t" \
    "v_mfma_f32_16x16x32_bf16 v[212:215], v[176:179], v[236:239], v[212:215]\n\t" \
    "ds_read_b128 v[228:231], %[wl] offset:2048\n\t" \
    "ds_read_b128 v[232:235], %[wl] offset:3072\n\t" \
    "s_waitcnt lgkmcnt(2)\n\t" \
    "v_mfma_f32_16x16x32_bf16 v[212:215], v[180:183], v[240:243], v[212:215]\n\t" \
    "v_cvt_pk_bf16_f32 v208, v208, v209\n\t" \
    "v_cvt_pk_bf16_f32 v209, v210, v211\n\t" \
    "v_mfma_f32_16x16x32_bf16 v[216:219], v[184:187], v[236:239], v[216:219]\n\t" \
    "ds_write_b64 v252, v[208:209] offset:2048\n\t" \
    "v_cvt_pk_bf16_f32 v204, v204, v205\n\t" \
    "v_mfma_f32_16x16x32_bf16 v[216:219], v[188:191], v[240:243], v[216:219]\n\t" \
    "v_cvt_pk_bf16_f32 v205, v206, v207\n\t" \
    "ds_write_b64 v252, v[204:205] offset:1024\n\t" \
    "v_mfma_f32_16x16x32_bf16 v[220:223], v[192:195], v[236:239], v[220:223]\n\t" \
    "v_cvt_pk_bf16_f32 v200, v200, v201\n\t" \
    "v_cvt_pk_bf16_f32 v201, v202, v203\n\t" \
    "v_mfma_f32_16x16x32_bf16 v[220:223], v[196:199], v[240:243], v[220:223]\n\t" \
    "ds_write_b64 v252, v[200:201] offset:0\n\t" \
    "ds_read_b128 v[200:203], %[wl] offset:0\n\t" \
    "ds_read_b128 v[204:207], %[wl] offset:1024\n\t" \
    "v_cvt_pk_bf16_f32 v212, v212, v213\n\t" \
    "v_cvt_pk_bf16_f32 v213, v214, v215\n\t" \
    "ds_write_b64 v252, v[212:213] offset:6144\n\t" \
    "v_cvt_pk_bf16_f32 v216, v216, v217\n\t" \
    "v_cvt_pk_bf16_f32 v217, v218, v219\n\t" \
    "ds_write_b64 v252, v[216:217] offset:7168\n\t" \
    "v_cvt_pk_bf16_f32 v220, v220, v221\n\t" \
    "v_cvt_pk_bf16_f32 v221, v222, v223\n\t" \
    "ds_write_b64 v252, v[220:221] offset:8192\n\t" \
    "s_waitcnt lgkmcnt(0)\n\t"
#define ASM_XG_S_LO2 \
    "s_waitcnt lgkmcnt(0)\n\t" \
    "ds_read_b128 v[224:227], %[xa] offset:16384\n\t" \
    "ds_read_b128 v[228:231], %[xa] offset:17408\n\t" \
    "v_xor_b32 v252, 0x800, %[xa]\n\t" \
    "ds_read_b128 v[232:235], v252 offset:16384\n\t" \
    "ds_read_b128 v[236:239], v252 offset:17408\n\t" \
    "v_xor_b32 v252, 0x1000, %[xa]\n\t" \
    "ds_read_b128 v[240:243], v252 offset:16384\n\t" \
    "ds_read_b128 v[212:215], v252 offset:17408\n\t" \
    "v_xor_b32 v252, 0x1800, %[xa]\n\t" \
    "ds_read_b128 v[200:203], v252 offset:16384\n\t" \
    "s_waitcnt lgkmcnt(6)\n\t" \
    "v_mfma_f32_16x16x32_bf16 v[220:223], v[8:11], v[224:227], 0\n\t" \
    "s_waitcnt lgkmcnt(5)\n\t" \
    "v_mfma_f32_16x16x32_bf16 v[220:223], v[12:15], v[228:231], v[220:223]\n\t" \
    "v_mfma_f32_16x16x32_bf16 v[216:219], v[0:3], v[224:227], 0\n\t" \
    "ds_read_b128 v[224:227], v252 offset:17408\n\t" \
    "v_mfma_f32_16x16x32_bf16 v[216:219], v[4:7], v[228:231], v[216:219]\n\t" \
    "v_and_b32 v252, 0x3ff, %[xa]\n\t" \
    "ds_read_b128 v[228:231], v252 offset:45056\n\t" \
    "s_waitcnt lgkmcnt(6)\n\t" \
    "v_mfma_f32_16x16x32_bf16 v[216:219], v[24:27], v[232:235], v[216:219]\n\t" \
    "s_waitcnt lgkmcnt(5)\n\t" \
    "v_mfma_f32_16x16x32_bf16 v[216:219], v[28:31], v[236:239], v[216:219]\n\t" \
    "v_mfma_f32_16x16x32_bf16 v[220:223], v[32:35], v[232:235], v[220:223]\n\t" \
    "ds_read_b128 v[232:235], v252 offset:46080\n\t" \
    "v_mfma_f32_16x16x32_bf16 v[220:223], v[36:39], v[236:239], v[220:223]\n\t" \
    "ds_read_b128 v[236:239], %[xa] offset:0\n\t" \
    "s_waitcnt lgkmcnt(6)\n\t" \
    "v_mfma_f32_16x16x32_bf16 v[220:223], v[48:51], v[240:243], v[220:223]\n\t" \
    "s_waitcnt lgkmcnt(5)\n\t" \
    "v_mfma_f32_16x16x32_bf16 v[220:223], v[52:55], v[212:215], v[220:223]\n\t" \
    "v_mfma_f32_16x16x32_bf16 v[216:219], v[40:43], v[240:243], v[216:219]\n\t" \
    "ds_read_b128 v[240:243], %[xa] offset:1024\n\t" \
    "v_mfma_f32_16x16x32_bf16 v[216:219], v[44:47], v[212:215], v[216:219]\n\t" \
    "v_xor_b32 v252, 0x800, %[xa]\n\t" \
    "ds_read_b128 v[212:215], v252 offset:0\n\t" \
    "s_waitcnt lgkmcnt(6)\n\t" \
    "v_mfma_f32_16x16x32_bf16 v[216:219], v[64:67], v[200:203], v[216:219]\n\t" \
    "s_waitcnt lgkmcnt(5)\n\t" \
    "v_mfma_f32_16x16x32_bf16 v[216:219], v[68:71], v[224:227], v[216:219]\n\t" \
    "v_mfma_f32_16x16x32_bf16 v[220:223], v[72:75], v[200:203], v[220:223]\n\t" \
    "ds_read_b128 v[200:203], v252 offset:1024\n\t" \
    "v_mfma_f32_16x16x32_bf16 v[220:223], v[76:79], v[224:227], v[220:223]\n\t" \
    "v_xor_b32 v252, 0x1000, %[xa]\n\t" \
    "ds_read_b128 v[224:227], v252 offset:0\n\t" \
    "s_waitcnt lgkmcnt(6)\n\t" \
    "v_mfma_f32_16x16x32_bf16 v[220:223], v[168:171], v[228:231], v[220:223]\n\t" \
    "s_waitcnt lgkmcnt(5)\n\t" \
    "v_mfma_f32_16x16x32_bf16 v[220:223], v[172:175], v[232:235], v[220:223]\n\t" \
    "v_mfma_f32_16x16x32_bf16 v[216:219], v[160:163], v[228:231], v[216:219]\n\t" \
    "ds_read_b128 v[228:231], v252 offset:1024\n\t" \
    "v_mfma_f32_16x16x32_bf16 v[216:219], v[164:167], v[232:235], v[216:219]\n\t" \
    "v_xor_b32 v252, 0x1800, %[xa]\n\t" \
    "ds_read_b128 v[232:235], v252 offset:0\n\t" \
    "s_waitcnt lgkmcnt(6)\n\t" \
    "v_mfma_f32_16x16x32_bf16 v[204:207], v[0:3], v[236:239], 0\n\t" \
    "s_waitcnt lgkmcnt(5)\n\t" \
    "v_mfma_f32_16x16x32_bf16 v[204:207], v[4:7], v[240:243], v[204:207]\n\t" \
    "v_mfma_f32_16x16x32_bf16 v[208:211], v[8:11], v[236:239], 0\n\t" \
    "ds_read_b128 v[236:239], v252 offset:1024\n\t" \
    "v_mfma_f32_16x16x32_bf16 v[208:211], v[12:15], v[240:243], v[208:211]\n\t" \
    "v_and_b32 v252, 0x3ff, %[xa]\n\t" \
    "ds_read_b128 v[240:243], v252 offset:40960\n\t" \
    "s_waitcnt lgkmcnt(6)\n\t" \
    "v_mfma_f32_16x16x32_bf16 v[208:211], v[32:35], v[212:215], v[208:211]\n\t" \
    "s_waitcnt lgkmcnt(5)\n\t" \
    "v_mfma_f32_16x16x32_bf16 v[208:211], v[36:39], v[200:203], v[208:211]\n\t" \
    "v_mfma_f32_16x16x32_bf16 v[204:207], v[24:27], v[212:215], v[204:207]\n\t" \
    "ds_read_b128 v[212:215], v252 offset:41984\n\t" \
    "v_mfma_f32_16x16x32_bf16 v[204:207], v[28:31], v[200:203], v[204:207]\n\t" \
    "s_waitcnt lgkmcnt(0)\n\t" \
    "s_barrier\n\t" ASM_ST \
    "v_xor_b32 v252, 0x2000, %[xa]\n\t" \
    "ds_read_b128 v[200:203], v252 offset:0\n\t" \
    "v_mfma_f32_16x16x32_bf16 v[204:207], v[40:43], v[224:227], v[204:207]\n\t" \
    "v_mfma_f32_16x16x32_bf16 v[204:207], v[44:47], v[228:231], v[204:207]\n\t" \
    "v_mfma_f32_16x16x32_bf16 v[208:211], v[48:51], v[224:227], v[208:211]\n\t" \
    "ds_read_b128 v[224:227], v252 offset:1024\n\t" \
    "v_mfma_f32_16x16x32_bf16 v[208:211], v[52:55], v[228:231], v[208:211]\n\t" \
    "v_xor_b32 v252, 0x2800, %[xa]\n\t" \
    "ds_read_b128 v[228:231], v252 offset:0\n\t" \
    "v_mfma_f32_16x16x32_bf16 v[208:211], v[72:75], v[232:235], v[208:211]\n\t" \
    "v_mfma_f32_16x16x32_bf16 v[208:211], v[76:79], v[236:239], v[208:211]\n\t" \
    "v_mfma_f32_16x16x32_bf16 v[204:207], v[64:67], v[232:235], v[204:207]\n\t" \
    "ds_read_b128 v[232:235], v252 offset:1024\n\t" \
    "v_mfma_f32_16x16x32_bf16 v[204:207], v[68:71], v[236:239], v[204:207]\n\t" \
    "v_xor_b32 v252, 0x3000, %[xa]\n\t" \
    "ds_read_b128 v[236:239], v252 offset:0\n\t" \
    "v_mfma_f32_16x16x32_bf16 v[204:207], v[160:163], v[240:243], v[204:207]\n\t" \
    "v_mfma_f32_16x16x32_bf16 v[204:207], v[164:167], v[212:215], v[204:207]\n\t" \
    "v_mfma_f32_16x16x32_bf16 v[208:211], v[168:171], v[240:243], v[208:211]\n\t" \
    "ds_read_b128 v[240:243], v252 offset:1024\n\t" \
    "v_mfma_f32_16x16x32_bf16 v[208:211], v[172:175], v[212:215], v[208:211]\n\t" \
    "v_xor_b32 v252, 0x3800, %[xa]\n\t" \
    "ds_read_b128 v[212:215], v252 offset:0\n\t" \
    "s_waitcnt lgkmcnt(6)\n\t" \
    "v_mfma_f32_16x16x32_bf16 v[208:211], v[88:91], v[200:203], v[208:211]\n\t" \
    "s_waitcnt lgkmcnt(5)\n\t" \
    "v_mfma_f32_16x16x32_bf16 v[208:211], v[92:95], v[224:227], v[208:211]\n\t" \
    "v_mfma_f32_16x16x32_bf16 v[204:207], v[80:83], v[200:203], v[204:207]\n\t" \
    "ds_read_b128 v[200:203], v252 offset:1024\n\t" \
    "v_mfma_f32_16x16x32_bf16 v[204:207], v[84:87], v[224:227], v[204:207]\n\t" \
    "v_and_b32 v252, 0x3ff, %[xa]\n\t" \
    "ds_read_b128 v[224:227], v252 offset:43008\n\t" \
    "s_waitcnt lgkmcnt(6)\n\t" \
    "v_mfma_f32_16x16x32_bf16 v[204:207], v[104:107], v[228:231], v[204:207]\n\t" \
    "s_waitcnt lgkmcnt(5)\n\t" \
    "v_mfma_f32_16x16x32_bf16 v[204:207], v[108:111], v[232:235], v[204:207]\n\t" \
    "v_mfma_f32_16x16x32_bf16 v[208:211], v[112:115], v[228:231], v[208:211]\n\t" \
    "ds_read_b128 v[228:231], v252 offset:44032\n\t" \
    "v_mfma_f32_16x16x32_bf16 v[208:211], v[116:119], v[232:235], v[208:211]\n\t" \
    "v_xor_b32 v252, 0x2000, %[xa]\n\t" \
    "ds_read_b128 v[232:235], v252 offset:16384\n\t" \
    "s_waitcnt lgkmcnt(6)\n\t" \
    "v_mfma_f32_16x16x32_bf16 v[208:211], v[128:131], v[236:239], v[208:211]\n\t" \
    "s_waitcnt lgkmcnt(5)\n\t" \
    "v_mfma_f32_16x16x32_bf16 v[208:211], v[132:135], v[240:243], v[208:211]\n\t" \
    "v_mfma_f32_16x16x32_bf16 v[204:207], v[120:123], v[236:239], v[204:207]\n\t" \
    "ds_read_b128 v[236:239], v252 offset:17408\n\t" \
    "v_mfma_f32_16x16x32_bf16 v[204:207], v[124:127], v[240:243], v[204:207]\n\t" \
    "v_xor_b32 v252, 0x2800, %[xa]\n\t" \
    "ds_read_b128 v[240:243], v252 offset:16384\n\t" \
    "s_waitcnt lgkmcnt(6)\n\t" \
    "v_mfma_f32_16x16x32_bf16 v[204:207], v[144:147], v[212:215], v[204:207]\n\t" \
    "s_waitcnt lgkmcnt(5)\n\t" \
    "v_mfma_f32_16x16x32_bf16 v[204:207], v[148:151], v[200:203], v[204:207]\n\t" \
    "v_mfma_f32_16x16x32_bf16 v[208:211], v[152:155], v[212:215], v[208:211]\n\t" \
    "ds_read_b128 v[212:215], v252 offset:17408\n\t" \
    "v_mfma_f32_16x16x32_bf16 v[208:211], v[156:159], v[200:203], v[208:211]\n\t" \
    "v_xor_b32 v252, 0x3000, %[xa]\n\t" \
    "ds_read_b128 v[200:203], v252 offset:16384\n\t" \
    "s_waitcnt lgkmcnt(6)\n\t" \
    "v_mfma_f32_16x16x32_bf16 v[208:211], v[192:195], v[224:227], v[208:211]\n\t" \
    "s_waitcnt lgkmcnt(5)\n\t" \
    "v_mfma_f32_16x16x32_bf16 v[208:211], v[196:199], v[228:231], v[208:211]\n\t" \
    "v_mfma_f32_16x16x32_bf16 v[204:207], v[184:187], v[224:227], v[204:207]\n\t" \
    "ds_read_b128 v[224:227], v252 offset:17408\n\t" \
    "v_mfma_f32_16x16x32_bf16 v[204:207], v[188:191], v[228:231], v[204:207]\n\t" \
    "v_xor_b32 v252, 0x3800, %[xa]\n\t" \
    "ds_read_b128 v[228:231], v252 offset:16384\n\t" \
    "s_waitcnt lgkmcnt(6)\n\t" \
    "v_mfma_f32_16x16x32_bf16 v[216:219], v[80:83], v[232:235], v[216:219]\n\t" \
    "s_waitcnt lgkmcnt(5)\n\t" \
    "v_mfma_f32_16x16x32_bf16 v[216:219], v[84:87], v[236:239], v[216:219]\n\t" \
    "v_mfma_f32_16x16x32_bf16 v[220:223], v[88:91], v[232:235], v[220:223]\n\t" \
    "ds_read_b128 v[232:235], v252 offset:17408\n\t" \
    "v_mfma_f32_16x16x32_bf16 v[220:223], v[92:95], v[236:239], v[220:223]\n\t" \
    "v_and_b32 v252, 0x3ff, %[xa]\n\t" \
    "ds_read_b128 v[236:239], v252 offset:47104\n\t" \
    "s_waitcnt lgkmcnt(6)\n\t" \
    "v_mfma_f32_16x16x32_bf16 v[220:223], v[112:115], v[240:243], v[220:223]\n\t" \
    "v_mov_b64 v[244:245], 0\n\t" \
    "s_waitcnt lgkmcnt(5)\n\t" \
    "v_mfma_f32_16x16x32_bf16 v[220:223], v[116:119], v[212:215], v[220:223]\n\t" \
    "v_mov_b64 v[246:247], 0\n\t" \
    "v_mfma_f32_16x16x32_bf16 v[216:219], v[104:107], v[240:243], v[216:219]\n\t" \
    "ds_read_b128 v[240:243], v252 offset:48128\n\t" \
    "v_mov_b64 v[248:249], 0\n\t" \
    "v_mfma_f32_16x16x32_bf16 v[216:219], v[108:111], v[212:215], v[216:219]\n\t" \
    "v_mov_b64 v[250:251], 0\n\t" \
    "s_waitcnt lgkmcnt(5)\n\t" \
    "v_mfma_f32_16x16x32_bf16 v[216:219], v[120:123], v[200:203], v[216:219]\n\t" \
    "s_waitcnt lgkmcnt(4)\n\t" \
    "v_mfma_f32_16x16x32_bf16 v[216:219], v[124:127], v[224:227], v[216:219]\n\t" \
    "v_mfma_f32_16x16x32_bf16 v[220:223], v[128:131], v[200:203], v[220:223]\n\t" \
    "v_mfma_f32_16x16x32_bf16 v[220:223], v[132:135], v[224:227], v[220:223]\n\t" \
    "s_waitcnt lgkmcnt(3)\n\t" \
    "v_mfma_f32_16x16x32_bf16 v[220:223], v[152:155], v[228:231], v[220:223]\n\t" \
    "v_add_u32 v252, %[qs], v252\n\t" \
    "v_mov_b64 v[224:225], 0\n\t" \
    "s_waitcnt lgkmcnt(2)\n\t" \
    "v_mfma_f32_16x16x32_bf16 v[220:223], v[156:159], v[232:235], v[220:223]\n\t" \
    "v_mov_b64 v[226:227], 0\n\t" \
    "v_cvt_pk_bf16_f32 v208, v208, v209\n\t" \
    "v_mfma_f32_16x16x32_bf16 v[216:219], v[144:147], v[228:231], v[216:219]\n\t" \
    "v_cvt_pk_bf16_f32 v209, v210, v211\n\t" \
    "ds_write_b64 v252, v[208:209] offset:2048\n\t" \
    "v_mfma_f32_16x16x32_bf16 v[216:219], v[148:151], v[232:235], v[216:219]\n\t" \
    "v_cvt_pk_bf16_f32 v204, v204, v205\n\t" \
    "v_cvt_pk_bf16_f32 v205, v206, v207\n\t" \
    "s_waitcnt lgkmcnt(2)\n\t" \
    "v_mfma_f32_16x16x32_bf16 v[216:219], v[184:187], v[236:239], v[216:219]\n\t" \
    "ds_write_b64 v252, v[204:205] offset:1024\n\t" \
    "ds_read_b128 v[228:231], %[wl] offset:2048\n\t" \
    "s_waitcnt lgkmcnt(3)\n\t" \
    "v_mfma_f32_16x16x32_bf16 v[216:219], v[188:191], v[240:243], v[216:219]\n\t" \
    "ds_read_b128 v[232:235], %[wl] offset:3072\n\t" \
    "v_mfma_f32_16x16x32_bf16 v[220:223], v[192:195], v[236:239], v[220:223]\n\t" \
    "v_mfma_f32_16x16x32_bf16 v[220:223], v[196:199], v[240:243], v[220:223]\n\t" \
    "ds_read_b128 v[200:203], %[wl] offset:0\n\t" \
    "ds_read_b128 v[204:207], %[wl] offset:1024\n\t" \
    "v_cvt_pk_bf16_f32 v216, v216, v217\n\t" \
    "v_cvt_pk_bf16_f32 v217, v218, v219\n\t" \
    "ds_write_b64 v252, v[216:217] offset:7168\n\t" \
    "s_nop 2\n\t" \
    "v_cvt_pk_bf16_f32 v220, v220, v221\n\t" \
    "v_cvt_pk_bf16_f32 v221, v222, v223\n\t" \
    "ds_write_b64 v252, v[220:221] offset:8192\n\t" \
    "s_waitcnt lgkmcnt(0)\n\t"
#define ASM_XG_S_LO1 \
    "s_waitcnt lgkmcnt(0)\n\t" \
    "ds_read_b128 v[224:227], %[xa] offset:16384\n\t" \
    "ds_read_b128 v[228:231], %[xa] offset:17408\n\t" \
    "v_xor_b32 v252, 0x800, %[xa]\n\t" \
    "ds_read_b128 v[232:235], v252 offset:16384\n\t" \
    "ds_read_b128 v[236:239], v252 offset:17408\n\t" \
    "v_xor_b32 v252, 0x1000, %[xa]\n\t" \
    "ds_read_b128 v[240:243], v252 offset:16384\n\t" \
    "ds_read_b128 v[212:215], v252 offset:17408\n\t" \
    "v_xor_b32 v252, 0x1800, %[xa]\n\t" \
    "ds_read_b128 v[200:203], v252 offset:16384\n\t" \
    "s_waitcnt lgkmcnt(6)\n\t" \
    "v_mfma_f32_16x16x32_bf16 v[220:223], v[8:11], v[224:227], 0\n\t" \
    "ds_read_b128 v[224:227], v252 offset:17408\n\t" \
    "s_waitcnt lgkmcnt(6)\n\t" \
    "v_mfma_f32_16x16x32_bf16 v[220:223], v[12:15], v[228:231], v[220:223]\n\t" \
    "v_and_b32 v252, 0x3ff, %[xa]\n\t" \
    "ds_read_b128 v[228:231], v252 offset:45056\n\t" \
    "s_waitcnt lgkmcnt(6)\n\t" \
    "v_mfma_f32_16x16x32_bf16 v[220:223], v[32:35], v[232:235], v[220:223]\n\t" \
    "ds_read_b128 v[232:235], v252 offset:46080\n\t" \
    "s_waitcnt lgkmcnt(6)\n\t" \
    "v_mfma_f32_16x16x32_bf16 v[220:223], v[36:39], v[236:239], v[220:223]\n\t" \
    "ds_read_b128 v[236:239], %[xa] offset:0\n\t" \
    "s_waitcnt lgkmcnt(6)\n\t" \
    "v_mfma_f32_16x16x32_bf16 v[220:223], v[48:51], v[240:243], v[220:223]\n\t" \
    "ds_read_b128 v[240:243], %[xa] offset:1024\n\t" \
    "s_waitcnt lgkmcnt(6)\n\t" \
    "v_mfma_f32_16x16x32_bf16 v[220:223], v[52:55], v[212:215], v[220:223]\n\t" \
    "v_xor_b32 v252, 0x800, %[xa]\n\t" \
    "ds_read_b128 v[212:215], v252 offset:0\n\t" \
    "s_waitcnt lgkmcnt(6)\n\t" \
    "v_mfma_f32_16x16x32_bf16 v[220:223], v[72:75], v[200:203], v[220:223]\n\t" \
    "ds_read_b128 v[200:203], v252 offset:1024\n\t" \
    "s_waitcnt lgkmcnt(6)\n\t" \
    "v_mfma_f32_16x16x32_bf16 v[220:223], v[76:79], v[224:227], v[220:223]\n\t" \
    "v_xor_b32 v252, 0x1000, %[xa]\n\t" \
    "ds_read_b128 v[224:227], v252 offset:0\n\t" \
    "s_waitcnt lgkmcnt(6)\n\t" \
    "v_mfma_f32_16x16x32_bf16 v[220:223], v[168:171], v[228:231], v[220:223]\n\t" \
    "ds_read_b128 v[228:231], v252 offset:1024\n\t" \
    "s_waitcnt lgkmcnt(6)\n\t" \
    "v_mfma_f32_16x16x32_bf16 v[220:223], v[172:175], v[232:235], v[220:223]\n\t" \
    "v_xor_b32 v252, 0x1800, %[xa]\n\t" \
    "ds_read_b128 v[232:235], v252 offset:0\n\t" \
    "s_waitcnt lgkmcnt(6)\n\t" \
    "v_mfma_f32_16x16x32_bf16 v[208:211], v[8:11], v[236:239], 0\n\t" \
    "ds_read_b128 v[236:239], v252 offset:1024\n\t" \
    "s_waitcnt lgkmcnt(6)\n\t" \
    "v_mfma_f32_16x16x32_bf16 v[208:211], v[12:15], v[240:243], v[208:211]\n\t" \
    "v_and_b32 v252, 0x3ff, %[xa]\n\t" \
    "ds_read_b128 v[240:243], v252 offset:40960\n\t" \
    "s_waitcnt lgkmcnt(6)\n\t" \
    "v_mfma_f32_16x16x32_bf16 v[208:211], v[32:35], v[212:215], v[208:211]\n\t" \
    "ds_read_b128 v[212:215], v252 offset:41984\n\t" \
    "s_waitcnt lgkmcnt(6)\n\t" \
    "v_mfma_f32_16x16x32_bf16 v[208:211], v[36:39], v[200:203], v[208:211]\n\t" \
    "s_waitcnt lgkmcnt(0)\n\t" \
    "s_barrier\n\t" ASM_ST \
    "v_xor_b32 v252, 0x2000, %[xa]\n\t" \
    "ds_read_b128 v[200:203], v252 offset:0\n\t" \
    "v_mfma_f32_16x16x32_bf16 v[208:211], v[48:51], v[224:227], v[208:211]\n\t" \
    "ds_read_b128 v[224:227], v252 offset:1024\n\t" \
    "v_mfma_f32_16x16x32_bf16 v[208:211], v[52:55], v[228:231], v[208:211]\n\t" \
    "v_xor_b32 v252, 0x2800, %[xa]\n\t" \
    "ds_read_b128 v[228:231], v252 offset:0\n\t" \
    "v_mfma_f32_16x16x32_bf16 v[208:211], v[72:75], v[232:235], v[208:211]\n\t" \
    "ds_read_b128 v[232:235], v252 offset:1024\n\t" \
    "v_mfma_f32_16x16x32_bf16 v[208:211], v[76:79], v[236:239], v[208:211]\n\t" \
    "v_xor_b32 v252, 0x3000, %[xa]\n\t" \
    "ds_read_b128 v[236:239], v252 offset:0\n\t" \
    "v_mfma_f32_16x16x32_bf16 v[208:211], v[168:171], v[240:243], v[208:211]\n\t" \
    "ds_read_b128 v[240:243], v252 offset:1024\n\t" \
    "v_mfma_f32_16x16x32_bf16 v[208:211], v[172:175], v[212:215], v[208:211]\n\t" \
    "v_xor_b32 v252, 0x3800, %[xa]\n\t" \
    "ds_read_b128 v[212:215], v252 offset:0\n\t" \
    "s_waitcnt lgkmcnt(6)\n\t" \
    "v_mfma_f32_16x16x32_bf16 v[208:211], v[88:91], v[200:203], v[208:211]\n\t" \
    "ds_read_b128 v[200:203], v252 offset:1024\n\t" \
    "s_waitcnt lgkmcnt(6)\n\t" \
    "v_mfma_f32_16x16x32_bf16 v[208:211], v[92:95], v[224:227], v[208:211]\n\t" \
    "v_and_b32 v252, 0x3ff, %[xa]\n\t" \
    "ds_read_b128 v[224:227], v252 offset:43008\n\t" \
    "s_waitcnt lgkmcnt(6)\n\t" \
    "v_mfma_f32_16x16x32_bf16 v[208:211], v[112:115], v[228:231], v[208:211]\n\t" \
    "ds_read_b128 v[228:231], v252 offset:44032\n\t" \
    "s_waitcnt lgkmcnt(6)\n\t" \
    "v_mfma_f32_16x16x32_bf16 v[208:211], v[116:119], v[232:235], v[208:211]\n\t" \
    "v_xor_b32 v252, 0x2000, %[xa]\n\t" \
    "ds_read_b128 v[232:235], v252 offset:16384\n\t" \
    "s_waitcnt lgkmcnt(6)\n\t" \
    "v_mfma_f32_16x16x32_bf16 v[208:211], v[128:131], v[236:239], v[208:211]\n\t" \
    "ds_read_b128 v[236:239], v252 offset:17408\n\t" \
    "s_waitcnt lgkmcnt(6)\n\t" \
    "v_mfma_f32_16x16x32_bf16 v[208:211], v[132:135], v[240:243], v[208:211]\n\t" \
    "v_xor_b32 v252, 0x2800, %[xa]\n\t" \
    "ds_read_b128 v[240:243], v252 offset:16384\n\t" \
    "s_waitcnt lgkmcnt(6)\n\t" \
    "v_mfma_f32_16x16x32_bf16 v[208:211], v[152:155], v[212:215], v[208:211]\n\t" \
    "ds_read_b128 v[212:215], v252 offset:17408\n\t" \
    "s_waitcnt lgkmcnt(6)\n\t" \
    "v_mfma_f32_16x16x32_bf16 v[208:211], v[156:159], v[200:203], v[208:211]\n\t" \
    "v_xor_b32 v252, 0x3000, %[xa]\n\t" \
    "ds_read_b128 v[200:203], v252 offset:16384\n\t" \
    "s_waitcnt lgkmcnt(6)\n\t" \
    "v_mfma_f32_16x16x32_bf16 v[208:211], v[192:195], v[224:227], v[208:211]\n\t" \
    "ds_read_b128 v[224:227], v252 offset:17408\n\t" \
    "s_waitcnt lgkmcnt(6)\n\t" \
    "v_mfma_f32_16x16x32_bf16 v[208:211], v[196:199], v[228:231], v[208:211]\n\t" \
    "v_xor_b32 v252, 0x3800, %[xa]\n\t" \
    "ds_read_b128 v[228:231], v252 offset:16384\n\t" \
    "s_waitcnt lgkmcnt(6)\n\t" \
    "v_mfma_f32_16x16x32_bf16 v[220:223], v[88:91], v[232:235], v[220:223]\n\t" \
    "ds_read_b128 v[232:235], v252 offset:17408\n\t" \
    "s_waitcnt lgkmcnt(6)\n\t" \
    "v_mfma_f32_16x16x32_bf16 v[220:223], v[92:95], v[236:239], v[220:223]\n\t" \
    "v_and_b32 v252, 0x3ff, %[xa]\n\t" \
    "ds_read_b128 v[236:239], v252 offset:47104\n\t" \
    "s_waitcnt lgkmcnt(6)\n\t" \
    "v_mfma_f32_16x16x32_bf16 v[220:223], v[112:115], v[240:243], v[220:223]\n\t" \
    "ds_read_b128 v[240:243], v252 offset:48128\n\t" \
    "v_mov_b64 v[244:245], 0\n\t" \
    "s_waitcnt lgkmcnt(6)\n\t" \
    "v_mfma_f32_16x16x32_bf16 v[220:223], v[116:119], v[212:215], v[220:223]\n\t" \
    "v_mov_b64 v[246:247], 0\n\t" \
    "s_waitcnt lgkmcnt(5)\n\t" \
    "v_mfma_f32_16x16x32_bf16 v[220:223], v[128:131], v[200:203], v[220:223]\n\t" \
    "v_mov_b64 v[248:249], 0\n\t" \
    "s_waitcnt lgkmcnt(4)\n\t" \
    "v_mfma_f32_16x16x32_bf16 v[220:223], v[132:135], v[224:227], v[220:223]\n\t" \
    "v_mov_b64 v[250:251], 0\n\t" \
    "s_waitcnt lgkmcnt(3)\n\t" \
    "v_mfma_f32_16x16x32_bf16 v[220:223], v[152:155], v[228:231], v[220:223]\n\t" \
    "v_add_u32 v252, %[qs], v252\n\t" \
    "v_mov_b64 v[224:225], 0\n\t" \
    "s_waitcnt lgkmcnt(2)\n\t" \
    "v_mfma_f32_16x16x32_bf16 v[220:223], v[156:159], v[232:235], v[220:223]\n\t" \
    "v_mov_b64 v[226:227], 0\n\t" \
    "v_cvt_pk_bf16_f32 v208, v208, v209\n\t" \
    "s_waitcnt lgkmcnt(1)\n\t" \
    "v_mfma_f32_16x16x32_bf16 v[220:223], v[192:195], v[236:239], v[220:223]\n\t" \
    "v_cvt_pk_bf16_f32 v209, v210, v211\n\t" \
    "ds_write_b64 v252, v[208:209] offset:2048\n\t" \
    "s_waitcnt lgkmcnt(1)\n\t" \
    "v_mfma_f32_16x16x32_bf16 v[220:223], v[196:199], v[240:243], v[220:223]\n\t" \
    "ds_read_b128 v[228:231], %[wl] offset:2048\n\t" \
    "ds_read_b128 v[232:235], %[wl] offset:3072\n\t" \
    "ds_read_b128 v[200:203], %[wl] offset:0\n\t" \
    "ds_read_b128 v[204:207], %[wl] offset:1024\n\t" \
    "s_nop 3\n\t" \
    "v_cvt_pk_bf16_f32 v220, v220, v221\n\t" \
    "v_cvt_pk_bf16_f32 v221, v222, v223\n\t" \
    "ds_write_b64 v252, v[220:221] offset:8192\n\t" \
    "s_waitcnt lgkmcnt(0)\n\t"
#define ASM_XG_V \
    "s_waitcnt lgkmcnt(0)\n\t" \
    "ds_read_b128 v[224:227], %[xa] offset:16384\n\t" \
    "ds_read_b128 v[228:231], %[xa] offset:17408\n\t" \
    "ds_read_b128 v[244:247], %[wl] offset:0\n\t" \
    "ds_read_b128 v[248:251], %[wl] offset:1024\n\t" \
    "ds_read_b128 v[232:235], %[xa] offset:0\n\t" \
    "ds_read_b128 v[236:239], %[xa] offset:1024\n\t" \
    "v_xor_b32 v252, 0x800, %[xa]\n\t" \
    "ds_read_b128 v[240:243], v252 offset:0\n\t" \
    "s_waitcnt lgkmcnt(6)\n\t" \
    "v_mfma_f32_16x16x32_bf16 v[220:223], v[8:11], v[224:227], 0\n\t" \
    "s_waitcnt lgkmcnt(5)\n\t" \
    "v_mfma_f32_16x16x32_bf16 v[220:223], v[12:15], v[228:231], v[220:223]\n\t" \
    "v_mfma_f32_16x16x32_bf16 v[216:219], v[0:3], v[224:227], 0\n\t" \
    "v_mfma_f32_16x16x32_bf16 v[216:219], v[4:7], v[228:231], v[216:219]\n\t" \
    "s_waitcnt lgkmcnt(4)\n\t" \
    "v_mfma_f32_16x16x32_bf16 v[212:215], v[244:247], v[224:227], 0\n\t" \
    "ds_read_b128 v[224:227], v252 offset:1024\n\t" \
    "s_waitcnt lgkmcnt(4)\n\t" \
    "v_mfma_f32_16x16x32_bf16 v[212:215], v[248:251], v[228:231], v[212:215]\n\t" \
    "ds_read_b128 v[228:231], v252 offset:16384\n\t" \
    "s_waitcnt lgkmcnt(4)\n\t" \
    "v_mfma_f32_16x16x32_bf16 v[200:203], v[244:247], v[232:235], 0\n\t" \
    "ds_read_b128 v[244:247], %[wl] offset:2048\n\t" \
    "s_waitcnt lgkmcnt(4)\n\t" \
    "v_mfma_f32_16x16x32_bf16 v[200:203], v[248:251], v[236:239], v[200:203]\n\t" \
    "ds_read_b128 v[248:251], %[wl] offset:3072\n\t" \
    "v_mfma_f32_16x16x32_bf16 v[204:207], v[0:3], v[232:235], 0\n\t" \
    "v_mfma_f32_16x16x32_bf16 v[204:207], v[4:7], v[236:239], v[204:207]\n\t" \
    "v_mfma_f32_16x16x32_bf16 v[208:211], v[8:11], v[232:235], 0\n\t" \
    "ds_read_b128 v[232:235], v252 offset:17408\n\t" \
    "v_mfma_f32_16x16x32_bf16 v[208:211], v[12:15], v[236:239], v[208:211]\n\t" \
    "v_xor_b32 v252, 0x1000, %[xa]\n\t" \
    "ds_read_b128 v[236:239], v252 offset:16384\n\t" \
    "s_waitcnt lgkmcnt(6)\n\t" \
    "v_mfma_f32_16x16x32_bf16 v[208:211], v[32:35], v[240:243], v[208:211]\n\t" \
    "s_waitcnt lgkmcnt(5)\n\t" \
    "v_mfma_f32_16x16x32_bf16 v[208:211], v[36:39], v[224:227], v[208:211]\n\t" \
    "v_mfma_f32_16x16x32_bf16 v[204:207], v[24:27], v[240:243], v[204:207]\n\t" \
    "v_mfma_f32_16x16x32_bf16 v[204:207], v[28:31], v[224:227], v[204:207]\n\t" \
    "v_mfma_f32_16x16x32_bf16 v[200:203], v[16:19], v[240:243], v[200:203]\n\t" \
    "ds_read_b128 v[240:243], v252 offset:17408\n\t" \
    "v_mfma_f32_16x16x32_bf16 v[200:203], v[20:23], v[224:227], v[200:203]\n\t" \
    "ds_read_b128 v[224:227], v252 offset:0\n\t" \
    "s_waitcnt lgkmcnt(6)\n\t" \
    "v_mfma_f32_16x16x32_bf16 v[212:215], v[16:19], v[228:231], v[212:215]\n\t" \
    "s_waitcnt lgkmcnt(3)\n\t" \
    "v_mfma_f32_16x16x32_bf16 v[212:215], v[20:23], v[232:235], v[212:215]\n\t" \
    "v_mfma_f32_16x16x32_bf16 v[216:219], v[24:27], v[228:231], v[216:219]\n\t" \
    "v_mfma_f32_16x16x32_bf16 v[216:219], v[28:31], v[232:235], v[216:219]\n\t" \
    "v_mfma_f32_16x16x32_bf16 v[220:223], v[32:35], v[228:231], v[220:223]\n\t" \
    "ds_read_b128 v[228:231], v252 offset:1024\n\t" \
    "v_mfma_f32_16x16x32_bf16 v[220:223], v[36:39], v[232:235], v[220:223]\n\t" \
    "v_xor_b32 v252, 0x1800, %[xa]\n\t" \
    "ds_read_b128 v[232:235], v252 offset:0\n\t" \
    "s_waitcnt lgkmcnt(4)\n\t" \
    "v_mfma_f32_16x16x32_bf16 v[220:223], v[48:51], v[236:239], v[220:223]\n\t" \
    "s_waitcnt lgkmcnt(3)\n\t" \
    "v_mfma_f32_16x16x32_bf16 v[220:223], v[52:55], v[240:243], v[220:223]\n\t" \
    "v_mfma_f32_16x16x32_bf16 v[216:219], v[40:43], v[236:239], v[216:219]\n\t" \
    "v_mfma_f32_16x16x32_bf16 v[216:219], v[44:47], v[240:243], v[216:219]\n\t" \
    "v_mfma_f32_16x16x32_bf16 v[212:215], v[244:247], v[236:239], v[212:215]\n\t" \
    "ds_read_b128 v[236:239], v252 offset:1024\n\t" \
    "v_mfma_f32_16x16x32_bf16 v[212:215], v[248:251], v[240:243], v[212:215]\n\t" \
    "ds_read_b128 v[240:243], v252 offset:16384\n\t" \
    "s_waitcnt lgkmcnt(4)\n\t" \
    "v_mfma_f32_16x16x32_bf16 v[200:203], v[244:247], v[224:227], v[200:203]\n\t" \
    "ds_read_b128 v[244:247], %[wl] offset:8192\n\t" \
    "s_waitcnt lgkmcnt(4)\n\t" \
    "v_mfma_f32_16x16x32_bf16 v[200:203], v[248:251], v[228:231], v[200:203]\n\t" \
    "ds_read_b128 v[248:251], %[wl] offset:9216\n\t" \
    "v_mfma_f32_16x16x32_bf16 v[204:207], v[40:43], v[224:227], v[204:207]\n\t" \
    "v_mfma_f32_16x16x32_bf16 v[204:207], v[44:47], v[228:231], v[204:207]\n\t" \
    "v_mfma_f32_16x16x32_bf16 v[208:211], v[48:51], v[224:227], v[208:211]\n\t" \
    "ds_read_b128 v[224:227], v252 offset:17408\n\t" \
    "v_mfma_f32_16x16x32_bf16 v[208:211], v[52:55], v[228:231], v[208:211]\n\t" \
    "v_and_b32 v252, 0x3ff, %[xa]\n\t" \
    "ds_read_b128 v[228:231], v252 offset:45056\n\t" \
    "s_waitcnt lgkmcnt(6)\n\t" \
    "v_mfma_f32_16x16x32_bf16 v[208:211], v[72:75], v[232:235], v[208:211]\n\t" \
    "s_waitcnt lgkmcnt(5)\n\t" \
    "v_mfma_f32_16x16x32_bf16 v[208:211], v[76:79], v[236:239], v[208:211]\n\t" \
    "v_mfma_f32_16x16x32_bf16 v[204:207], v[64:67], v[232:235], v[204:207]\n\t" \
    "v_mfma_f32_16x16x32_bf16 v[204:207], v[68:71], v[236:239], v[204:207]\n\t" \
    "v_mfma_f32_16x16x32_bf16 v[200:203], v[56:59], v[232:235], v[200:203]\n\t" \
    "ds_read_b128 v[232:235], v252 offset:46080\n\t" \
    "v_mfma_f32_16x16x32_bf16 v[200:203], v[60:63], v[236:239], v[200:203]\n\t" \
    "ds_read_b128 v[236:239], v252 offset:40960\n\t" \
    "s_waitcnt lgkmcnt(6)\n\t" \
    "v_mfma_f32_16x16x32_bf16 v[212:215], v[56:59], v[240:243], v[212:215]\n\t" \
    "s_waitcnt lgkmcnt(3)\n\t" \
    "v_mfma_f32_16x16x32_bf16 v[212:215], v[60:63], v[224:227], v[212:215]\n\t" \
    "v_mfma_f32_16x16x32_bf16 v[216:219], v[64:67], v[240:243], v[216:219]\n\t" \
    "v_mfma_f32_16x16x32_bf16 v[216:219], v[68:71], v[224:227], v[216:219]\n\t" \
    "v_mfma_f32_16x16x32_bf16 v[220:223], v[72:75], v[240:243], v[220:223]\n\t" \
    "ds_read_b128 v[240:243], v252 offset:41984\n\t" \
    "v_mfma_f32_16x16x32_bf16 v[220:223], v[76:79], v[224:227], v[220:223]\n\t" \
    "v_xor_b32 v252, 0x2000, %[xa]\n\t" \
    "ds_read_b128 v[224:227], v252 offset:0\n\t" \
    "s_waitcnt lgkmcnt(4)\n\t" \
    "v_mfma_f32_16x16x32_bf16 v[220:223], v[168:171], v[228:231], v[220:223]\n\t" \
    "s_waitcnt lgkmcnt(3)\n\t" \
    "v_mfma_f32_16x16x32_bf16 v[220:223], v[172:175], v[232:235], v[220:223]\n\t" \
    "v_mfma_f32_16x16x32_bf16 v[216:219], v[160:163], v[228:231], v[216:219]\n\t" \
    "v_mfma_f32_16x16x32_bf16 v[216:219], v[164:167], v[232:235], v[216:219]\n\t" \
    "v_mfma_f32_16x16x32_bf16 v[212:215], v[244:247], v[228:231], v[212:215]\n\t" \
    "ds_read_b128 v[228:231], v252 offset:1024\n\t" \
    "v_mfma_f32_16x16x32_bf16 v[212:215], v[248:251], v[232:235], v[212:215]\n\t" \
    "ds_read_b128 v[232:235], v252 offset:16384\n\t" \
    "s_waitcnt lgkmcnt(4)\n\t" \
    "v_mfma_f32_16x16x32_bf16 v[200:203], v[244:247], v[236:239], v[200:203]\n\t" \
    "ds_read_b128 v[244:247], %[wl] offset:4096\n\t" \
    "s_waitcnt lgkmcnt(4)\n\t" \
    "v_mfma_f32_16x16x32_bf16 v[200:203], v[248:251], v[240:243], v[200:203]\n\t" \
    "ds_read_b128 v[248:251], %[wl] offset:5120\n\t" \
    "v_mfma_f32_16x16x32_bf16 v[204:207], v[160:163], v[236:239], v[204:207]\n\t" \
    "v_mfma_f32_16x16x32_bf16 v[204:207], v[164:167], v[240:243], v[204:207]\n\t" \
    "v_mfma_f32_16x16x32_bf16 v[208:211], v[168:171], v[236:239], v[208:211]\n\t" \
    "ds_read_b128 v[236:239], v252 offset:17408\n\t" \
    "v_mfma_f32_16x16x32_bf16 v[208:211], v[172:175], v[240:243], v[208:211]\n\t" \
    "v_xor_b32 v252, 0x2800, %[xa]\n\t" \
    "ds_read_b128 v[240:243], v252 offset:16384\n\t" \
    "s_waitcnt lgkmcnt(6)\n\t" \
    "v_mfma_f32_16x16x32_bf16 v[208:211], v[88:91], v[224:227], v[208:211]\n\t" \
    "s_waitcnt lgkmcnt(5)\n\t" \
    "v_mfma_f32_16x16x32_bf16 v[208:211], v[92:95], v[228:231], v[208:211]\n\t" \
    "v_mfma_f32_16x16x32_bf16 v[204:207], v[80:83], v[224:227], v[204:207]\n\t" \
    "v_mfma_f32_16x16x32_bf16 v[204:207], v[84:87], v[228:231], v[204:207]\n\t" \
    "s_waitcnt lgkmcnt(3)\n\t" \
    "v_mfma_f32_16x16x32_bf16 v[200:203], v[244:247], v[224:227], v[200:203]\n\t" \
    "ds_read_b128 v[224:227], v252 offset:17408\n\t" \
    "s_waitcnt lgkmcnt(3)\n\t" \
    "v_mfma_f32_16x16x32_bf16 v[200:203], v[248:251], v[228:231], v[200:203]\n\t" \
    "ds_read_b128 v[228:231], v252 offset:0\n\t" \
    "v_mfma_f32_16x16x32_bf16 v[212:215], v[244:247], v[232:235], v[212:215]\n\t" \
    "ds_read_b128 v[244:247], %[wl] offset:6144\n\t" \
    "s_waitcnt lgkmcnt(4)\n\t" \
    "v_mfma_f32_16x16x32_bf16 v[212:215], v[248:251], v[236:239], v[212:215]\n\t" \
    "ds_read_b128 v[248:251], %[wl] offset:7168\n\t" \
    "v_mfma_f32_16x16x32_bf16 v[216:219], v[80:83], v[232:235], v[216:219]\n\t" \
    "v_mfma_f32_16x16x32_bf16 v[216:219], v[84:87], v[236:239], v[216:219]\n\t" \
    "v_mfma_f32_16x16x32_bf16 v[220:223], v[88:91], v[232:235], v[220:223]\n\t" \
    "ds_read_b128 v[232:235], v252 offset:1024\n\t" \
    "v_mfma_f32_16x16x32_bf16 v[220:223], v[92:95], v[236:239], v[220:223]\n\t" \
    "v_xor_b32 v252, 0x3000, %[xa]\n\t" \
    "ds_read_b128 v[236:239], v252 offset:0\n\t" \
    "s_waitcnt lgkmcnt(6)\n\t" \
    "v_mfma_f32_16x16x32_bf16 v[220:223], v[112:115], v[240:243], v[220:223]\n\t" \
    "s_waitcnt lgkmcnt(5)\n\t" \
    "v_mfma_f32_16x16x32_bf16 v[220:223], v[116:119], v[224:227], v[220:223]\n\t" \
    "v_mfma_f32_16x16x32_bf16 v[216:219], v[104:107], v[240:243], v[216:219]\n\t" \
    "v_mfma_f32_16x16x32_bf16 v[216:219], v[108:111], v[224:227], v[216:219]\n\t" \
    "v_mfma_f32_16x16x32_bf16 v[212:215], v[96:99], v[240:243], v[212:215]\n\t" \
    "ds_read_b128 v[240:243], v252 offset:1024\n\t" \
    "v_mfma_f32_16x16x32_bf16 v[212:215], v[100:103], v[224:227], v[212:215]\n\t" \
    "ds_read_b128 v[224:227], v252 offset:16384\n\t" \
    "s_waitcnt lgkmcnt(6)\n\t" \
    "v_mfma_f32_16x16x32_bf16 v[200:203], v[96:99], v[228:231], v[200:203]\n\t" \
    "s_waitcnt lgkmcnt(3)\n\t" \
    "v_mfma_f32_16x16x32_bf16 v[200:203], v[100:103], v[232:235], v[200:203]\n\t" \
    "v_mfma_f32_16x16x32_bf16 v[204:207], v[104:107], v[228:231], v[204:207]\n\t" \
    "v_mfma_f32_16x16x32_bf16 v[204:207], v[108:111], v[232:235], v[204:207]\n\t" \
    "v_mfma_f32_16x16x32_bf16 v[208:211], v[112:115], v[228:231], v[208:211]\n\t" \
    "ds_read_b128 v[228:231], v252 offset:17408\n\t" \
    "v_mfma_f32_16x16x32_bf16 v[208:211], v[116:119], v[232:235], v[208:211]\n\t" \
    "v_xor_b32 v252, 0x3800, %[xa]\n\t" \
    "ds_read_b128 v[232:235], v252 offset:16384\n\t" \
    "s_waitcnt lgkmcnt(4)\n\t" \
    "v_mfma_f32_16x16x32_bf16 v[208:211], v[128:131], v[236:239], v[208:211]\n\t" \
    "s_waitcnt lgkmcnt(3)\n\t" \
    "v_mfma_f32_16x16x32_bf16 v[208:211], v[132:135], v[240:243], v[208:211]\n\t" \
    "v_mfma_f32_16x16x32_bf16 v[204:207], v[120:123], v[236:239], v[204:207]\n\t" \
    "v_mfma_f32_16x16x32_bf16 v[204:207], v[124:127], v[240:243], v[204:207]\n\t" \
    "v_mfma_f32_16x16x32_bf16 v[200:203], v[244:247], v[236:239], v[200:203]\n\t" \
    "ds_read_b128 v[236:239], v252 offset:17408\n\t" \
    "v_mfma_f32_16x16x32_bf16 v[200:203], v[248:251], v[240:243], v[200:203]\n\t" \
    "ds_read_b128 v[240:243], v252 offset:0\n\t" \
    "s_waitcnt lgkmcnt(4)\n\t" \
    "v_mfma_f32_16x16x32_bf16 v[212:215], v[244:247], v[224:227], v[212:215]\n\t" \
    "s_waitcnt lgkmcnt(3)\n\t" \
    "v_mfma_f32_16x16x32_bf16 v[212:215], v[248:251], v[228:231], v[212:215]\n\t" \
    "v_mfma_f32_16x16x32_bf16 v[216:219], v[120:123], v[224:227], v[216:219]\n\t" \
    "v_mfma_f32_16x16x32_bf16 v[216:219], v[124:127], v[228:231], v[216:219]\n\t" \
    "v_mfma_f32_16x16x32_bf16 v[220:223], v[128:131], v[224:227], v[220:223]\n\t" \
    "ds_read_b128 v[224:227], v252 offset:1024\n\t" \
    "v_mfma_f32_16x16x32_bf16 v[220:223], v[132:135], v[228:231], v[220:223]\n\t" \
    "v_and_b32 v252, 0x3ff, %[xa]\n\t" \
    "ds_read_b128 v[228:231], v252 offset:43008\n\t" \
    "s_waitcnt lgkmcnt(4)\n\t" \
    "v_mfma_f32_16x16x32_bf16 v[220:223], v[152:155], v[232:235], v[220:223]\n\t" \
    "s_waitcnt lgkmcnt(3)\n\t" \
    "v_mfma_f32_16x16x32_bf16 v[220:223], v[156:159], v[236:239], v[220:223]\n\t" \
    "v_mfma_f32_16x16x32_bf16 v[216:219], v[144:147], v[232:235], v[216:219]\n\t" \
    "v_mfma_f32_16x16x32_bf16 v[216:219], v[148:151], v[236:239], v[216:219]\n\t" \
    "v_mfma_f32_16x16x32_bf16 v[212:215], v[136:139], v[232:235], v[212:215]\n\t" \
    "ds_read_b128 v[232:235], v252 offset:44032\n\t" \
    "v_mfma_f32_16x16x32_bf16 v[212:215], v[140:143], v[236:239], v[212:215]\n\t" \
    "ds_read_b128 v[236:239], v252 offset:47104\n\t" \
    "s_waitcnt lgkmcnt(4)\n\t" \
    "v_mfma_f32_16x16x32_bf16 v[200:203], v[136:139], v[240:243], v[200:203]\n\t" \
    "s_waitcnt lgkmcnt(3)\n\t" \
    "v_mfma_f32_16x16x32_bf16 v[200:203], v[140:143], v[224:227], v[200:203]\n\t" \
    "v_mfma_f32_16x16x32_bf16 v[204:207], v[144:147], v[240:243], v[204:207]\n\t" \
    "v_mfma_f32_16x16x32_bf16 v[204:207], v[148:151], v[224:227], v[204:207]\n\t" \
    "v_mfma_f32_16x16x32_bf16 v[208:211], v[152:155], v[240:243], v[208:211]\n\t" \
    "ds_read_b128 v[240:243], v252 offset:48128\n\t" \
    "v_mfma_f32_16x16x32_bf16 v[208:211], v[156:159], v[224:227], v[208:211]\n\t" \
    "s_waitcnt lgkmcnt(3)\n\t" \
    "v_mfma_f32_16x16x32_bf16 v[208:211], v[192:195], v[228:231], v[208:211]\n\t" \
    "v_add_u32 v252, %[qs], v252\n\t" \
    "s_waitcnt lgkmcnt(2)\n\t" \
    "v_mfma_f32_16x16x32_bf16 v[208:211], v[196:199], v[232:235], v[208:211]\n\t" \
    "v_mfma_f32_16x16x32_bf16 v[204:207], v[184:187], v[228:231], v[204:207]\n\t" \
    "v_mfma_f32_16x16x32_bf16 v[204:207], v[188:191], v[232:235], v[204:207]\n\t" \
    "v_mfma_f32_16x16x32_bf16 v[200:203], v[176:179], v[228:231], v[200:203]\n\t" \
    "v_mfma_f32_16x16x32_bf16 v[200:203], v[180:183], v[232:235], v[200:203]\n\t" \
    "s_waitcnt lgkmcnt(1)\n\t" \
    "v_mfma_f32_16x16x32_bf16 v[212:215], v[176:179], v[236:239], v[212:215]\n\t" \
    "v_cvt_pk_bf16_f32 v208, v208, v209\n\t" \
    "v_cvt_pk_bf16_f32 v209, v210, v211\n\t" \
    "s_waitcnt lgkmcnt(0)\n\t" \
    "v_mfma_f32_16x16x32_bf16 v[212:215], v[180:183], v[240:243], v[212:215]\n\t" \
    "ds_write_b64 v252, v[208:209] offset:2048\n\t" \
    "v_cvt_pk_bf16_f32 v204, v204, v205\n\t" \
    "v_mfma_f32_16x16x32_bf16 v[216:219], v[184:187], v[236:239], v[216:219]\n\t" \
    "v_cvt_pk_bf16_f32 v205, v206, v207\n\t" \
    "ds_write_b64 v252, v[204:205] offset:1024\n\t" \
    "v_mfma_f32_16x16x32_bf16 v[216:219], v[188:191], v[240:243], v[216:219]\n\t" \
    "v_cvt_pk_bf16_f32 v200, v200, v201\n\t" \
    "v_cvt_pk_bf16_f32 v201, v202, v203\n\t" \
    "v_mfma_f32_16x16x32_bf16 v[220:223], v[192:195], v[236:239], v[220:223]\n\t" \
    "ds_write_b64 v252, v[200:201] offset:0\n\t" \
    "v_mfma_f32_16x16x32_bf16 v[220:223], v[196:199], v[240:243], v[220:223]\n\t" \
    "v_cvt_pk_bf16_f32 v212, v212, v213\n\t" \
    "v_cvt_pk_bf16_f32 v213, v214, v215\n\t" \
    "ds_write_b64 v252, v[212:213] offset:6144\n\t" \
    "v_cvt_pk_bf16_f32 v216, v216, v217\n\t" \
    "v_cvt_pk_bf16_f32 v217, v218, v219\n\t" \
    "ds_write_b64 v252, v[216:217] offset:7168\n\t" \
    "s_nop 1\n\t" \
    "v_cvt_pk_bf16_f32 v220, v220, v221\n\t" \
    "v_cvt_pk_bf16_f32 v221, v222, v223\n\t" \
    "ds_write_b64 v252, v[220:221] offset:8192\n\t" \
    "s_waitcnt lgkmcnt(0)\n\t"
#define ASM_XG_H_LO \
    "s_waitcnt lgkmcnt(0)\n\t" \
    "ds_read_b128 v[224:227], %[xa] offset:16384\n\t" \
    "ds_read_b128 v[228:231], %[xa] offset:17408\n\t" \
    "ds_read_b128 v[244:247], %[wl] offset:0\n\t" \
    "ds_read_b128 v[248:251], %[wl] offset:1024\n\t" \
    "ds_read_b128 v[232:235], %[xa] offset:0\n\t" \
    "ds_read_b128 v[236:239], %[xa] offset:1024\n\t" \
    "v_xor_b32 v252, 0x800, %[xa]\n\t" \
    "ds_read_b128 v[240:243], v252 offset:0\n\t" \
    "s_waitcnt lgkmcnt(6)\n\t" \
    "v_mfma_f32_16x16x32_bf16 v[220:223], v[8:11], v[224:227], 0\n\t" \
    "s_waitcnt lgkmcnt(5)\n\t" \
    "v_mfma_f32_16x16x32_bf16 v[220:223], v[12:15], v[228:231], v[220:223]\n\t" \
    "v_mfma_f32_16x16x32_bf16 v[216:219], v[0:3], v[224:227], 0\n\t" \
    "v_mfma_f32_16x16x32_bf16 v[216:219], v[4:7], v[228:231], v[216:219]\n\t" \
    "s_waitcnt lgkmcnt(4)\n\t" \
    "v_mfma_f32_16x16x32_bf16 v[212:215], v[244:247], v[224:227], 0\n\t" \
    "ds_read_b128 v[224:227], v252 offset:1024\n\t" \
    "s_waitcnt lgkmcnt(4)\n\t" \
    "v_mfma_f32_16x16x32_bf16 v[212:215], v[248:251], v[228:231], v[212:215]\n\t" \
    "ds_read_b128 v[228:231], v252 offset:16384\n\t" \
    "s_waitcnt lgkmcnt(4)\n\t" \
    "v_mfma_f32_16x16x32_bf16 v[200:203], v[244:247], v[232:235], 0\n\t" \
    "ds_read_b128 v[244:247], %[wl] offset:2048\n\t" \
    "s_waitcnt lgkmcnt(4)\n\t" \
    "v_mfma_f32_16x16x32_bf16 v[200:203], v[248:251], v[236:239], v[200:203]\n\t" \
    "ds_read_b128 v[248:251], %[wl] offset:3072\n\t" \
    "v_mfma_f32_16x16x32_bf16 v[204:207], v[0:3], v[232:235], 0\n\t" \
    "v_mfma_f32_16x16x32_bf16 v[204:207], v[4:7], v[236:239], v[204:207]\n\t" \
    "v_mfma_f32_16x16x32_bf16 v[208:211], v[8:11], v[232:235], 0\n\t" \
    "ds_read_b128 v[232:235], v252 offset:17408\n\t" \
    "v_mfma_f32_16x16x32_bf16 v[208:211], v[12:15], v[236:239], v[208:211]\n\t" \
    "v_xor_b32 v252, 0x1000, %[xa]\n\t" \
    "ds_read_b128 v[236:239], v252 offset:16384\n\t" \
    "s_waitcnt lgkmcnt(6)\n\t" \
    "v_mfma_f32_16x16x32_bf16 v[208:211], v[32:35], v[240:243], v[208:211]\n\t" \
    "s_waitcnt lgkmcnt(5)\n\t" \
    "v_mfma_f32_16x16x32_bf16 v[208:211], v[36:39], v[224:227], v[208:211]\n\t" \
    "v_mfma_f32_16x16x32_bf16 v[204:207], v[24:27], v[240:243], v[204:207]\n\t" \
    "v_mfma_f32_16x16x32_bf16 v[204:207], v[28:31], v[224:227], v[204:207]\n\t" \
    "v_mfma_f32_16x16x32_bf16 v[200:203], v[16:19], v[240:243], v[200:203]\n\t" \
    "ds_read_b128 v[240:243], v252 offset:17408\n\t" \
    "v_mfma_f32_16x16x32_bf16 v[200:203], v[20:23], v[224:227], v[200:203]\n\t" \
    "ds_read_b128 v[224:227], v252 offset:0\n\t" \
    "s_waitcnt lgkmcnt(6)\n\t" \
    "v_mfma_f32_16x16x32_bf16 v[212:215], v[16:19], v[228:231], v[212:215]\n\t" \
    "s_waitcnt lgkmcnt(3)\n\t" \
    "v_mfma_f32_16x16x32_bf16 v[212:215], v[20:23], v[232:235], v[212:215]\n\t" \
    "v_mfma_f32_16x16x32_bf16 v[216:219], v[24:27], v[228:231], v[216:219]\n\t" \
    "v_mfma_f32_16x16x32_bf16 v[216:219], v[28:31], v[232:235], v[216:219]\n\t" \
    "v_mfma_f32_16x16x32_bf16 v[220:223], v[32:35], v[228:231], v[220:223]\n\t" \
    "ds_read_b128 v[228:231], v252 offset:1024\n\t" \
    "v_mfma_f32_16x16x32_bf16 v[220:223], v[36:39], v[232:235], v[220:223]\n\t" \
    "v_xor_b32 v252, 0x1800, %[xa]\n\t" \
    "ds_read_b128 v[232:235], v252 offset:0\n\t" \
    "s_waitcnt lgkmcnt(4)\n\t" \
    "v_mfma_f32_16x16x32_bf16 v[220:223], v[48:51], v[236:239], v[220:223]\n\t" \
    "s_waitcnt lgkmcnt(3)\n\t" \
    "v_mfma_f32_16x16x32_bf16 v[220:223], v[52:55], v[240:243], v[220:223]\n\t" \
    "v_mfma_f32_16x16x32_bf16 v[216:219], v[40:43], v[236:239], v[216:219]\n\t" \
    "v_mfma_f32_16x16x32_bf16 v[216:219], v[44:47], v[240:243], v[216:219]\n\t" \
    "v_mfma_f32_16x16x32_bf16 v[212:215], v[244:247], v[236:239], v[212:215]\n\t" \
    "ds_read_b128 v[236:239], v252 offset:1024\n\t" \
    "v_mfma_f32_16x16x32_bf16 v[212:215], v[248:251], v[240:243], v[212:215]\n\t" \
    "ds_read_b128 v[240:243], v252 offset:16384\n\t" \
    "s_waitcnt lgkmcnt(4)\n\t" \
    "v_mfma_f32_16x16x32_bf16 v[200:203], v[244:247], v[224:227], v[200:203]\n\t" \
    "ds_read_b128 v[244:247], %[wl] offset:8192\n\t" \
    "s_waitcnt lgkmcnt(4)\n\t" \
    "v_mfma_f32_16x16x32_bf16 v[200:203], v[248:251], v[228:231], v[200:203]\n\t" \
    "ds_read_b128 v[248:251], %[wl] offset:9216\n\t" \
    "v_mfma_f32_16x16x32_bf16 v[204:207], v[40:43], v[224:227], v[204:207]\n\t" \
    "v_mfma_f32_16x16x32_bf16 v[204:207], v[44:47], v[228:231], v[204:207]\n\t" \
    "v_mfma_f32_16x16x32_bf16 v[208:211], v[48:51], v[224:227], v[208:211]\n\t" \
    "ds_read_b128 v[224:227], v252 offset:17408\n\t" \
    "v_mfma_f32_16x16x32_bf16 v[208:211], v[52:55], v[228:231], v[208:211]\n\t" \
    "v_and_b32 v252, 0x3ff, %[xa]\n\t" \
    "ds_read_b128 v[228:231], v252 offset:45056\n\t" \
    "s_waitcnt lgkmcnt(6)\n\t" \
    "v_mfma_f32_16x16x32_bf16 v[208:211], v[72:75], v[232:235], v[208:211]\n\t" \
    "s_waitcnt lgkmcnt(5)\n\t" \
    "v_mfma_f32_16x16x32_bf16 v[208:211], v[76:79], v[236:239], v[208:211]\n\t" \
    "v_mfma_f32_16x16x32_bf16 v[204:207], v[64:67], v[232:235], v[204:207]\n\t" \
    "v_mfma_f32_16x16x32_bf16 v[204:207], v[68:71], v[236:239], v[204:207]\n\t" \
    "v_mfma_f32_16x16x32_bf16 v[200:203], v[56:59], v[232:235], v[200:203]\n\t" \
    "ds_read_b128 v[232:235], v252 offset:46080\n\t" \
    "v_mfma_f32_16x16x32_bf16 v[200:203], v[60:63], v[236:239], v[200:203]\n\t" \
    "ds_read_b128 v[236:239], v252 offset:40960\n\t" \
    "s_waitcnt lgkmcnt(6)\n\t" \
    "v_mfma_f32_16x16x32_bf16 v[212:215], v[56:59], v[240:243], v[212:215]\n\t" \
    "s_waitcnt lgkmcnt(3)\n\t" \
    "v_mfma_f32_16x16x32_bf16 v[212:215], v[60:63], v[224:227], v[212:215]\n\t" \
    "v_mfma_f32_16x16x32_bf16 v[216:219], v[64:67], v[240:243], v[216:219]\n\t" \
    "v_mfma_f32_16x16x32_bf16 v[216:219], v[68:71], v[224:227], v[216:219]\n\t" \
    "v_mfma_f32_16x16x32_bf16 v[220:223], v[72:75], v[240:243], v[220:223]\n\t" \
    "ds_read_b128 v[240:243], v252 offset:41984\n\t" \
    "v_mfma_f32_16x16x32_bf16 v[220:223], v[76:79], v[224:227], v[220:223]\n\t" \
    "s_waitcnt lgkmcnt(0)\n\t" \
    "s_barrier\n\t" ASM_ST \
    "v_xor_b32 v252, 0x2000, %[xa]\n\t" \
    "ds_read_b128 v[224:227], v252 offset:0\n\t" \
    "v_mfma_f32_16x16x32_bf16 v[220:223], v[168:171], v[228:231], v[220:223]\n\t" \
    "v_mfma_f32_16x16x32_bf16 v[220:223], v[172:175], v[232:235], v[220:223]\n\t" \
    "v_mfma_f32_16x16x32_bf16 v[216:219], v[160:163], v[228:231], v[216:219]\n\t" \
    "v_mfma_f32_16x16x32_bf16 v[216:219], v[164:167], v[232:235], v[216:219]\n\t" \
    "v_mfma_f32_16x16x32_bf16 v[212:215], v[244:247], v[228:231], v[212:215]\n\t" \
    "ds_read_b128 v[228:231], v252 offset:1024\n\t" \
    "v_mfma_f32_16x16x32_bf16 v[212:215], v[248:251], v[232:235], v[212:215]\n\t" \
    "ds_read_b128 v[232:235], v252 offset:16384\n\t" \
    "v_mfma_f32_16x16x32_bf16 v[200:203], v[244:247], v[236:239], v[200:203]\n\t" \
    "ds_read_b128 v[244:247], %[wl] offset:4096\n\t" \
    "v_mfma_f32_16x16x32_bf16 v[200:203], v[248:251], v[240:243], v[200:203]\n\t" \
    "ds_read_b128 v[248:251], %[wl] offset:5120\n\t" \
    "v_mfma_f32_16x16x32_bf16 v[204:207], v[160:163], v[236:239], v[204:207]\n\t" \
    "v_mfma_f32_16x16x32_bf16 v[204:207], v[164:167], v[240:243], v[204:207]\n\t" \
    "v_mfma_f32_16x16x32_bf16 v[208:211], v[168:171], v[236:239], v[208:211]\n\t" \
    "ds_read_b128 v[236:239], v252 offset:17408\n\t" \
    "v_mfma_f32_16x16x32_bf16 v[208:211], v[172:175], v[240:243], v[208:211]\n\t" \
    "v_xor_b32 v252, 0x2800, %[xa]\n\t" \
    "ds_read_b128 v[240:243], v252 offset:16384\n\t" \
    "s_waitcnt lgkmcnt(6)\n\t" \
    "v_mfma_f32_16x16x32_bf16 v[208:211], v[88:91], v[224:227], v[208:211]\n\t" \
    "s_waitcnt lgkmcnt(5)\n\t" \
    "v_mfma_f32_16x16x32_bf16 v[208:211], v[92:95], v[228:231], v[208:211]\n\t" \
    "v_mfma_f32_16x16x32_bf16 v[204:207], v[80:83], v[224:227], v[204:207]\n\t" \
    "v_mfma_f32_16x16x32_bf16 v[204:207], v[84:87], v[228:231], v[204:207]\n\t" \
    "s_waitcnt lgkmcnt(3)\n\t" \
    "v_mfma_f32_16x16x32_bf16 v[200:203], v[244:247], v[224:227], v[200:203]\n\t" \
    "ds_read_b128 v[224:227], v252 offset:17408\n\t" \
    "s_waitcnt lgkmcnt(3)\n\t" \
    "v_mfma_f32_16x16x32_bf16 v[200:203], v[248:251], v[228:231], v[200:203]\n\t" \
    "ds_read_b128 v[228:231], v252 offset:0\n\t" \
    "v_mfma_f32_16x16x32_bf16 v[212:215], v[244:247], v[232:235], v[212:215]\n\t" \
    "ds_read_b128 v[244:247], %[wl] offset:6144\n\t" \
    "s_waitcnt lgkmcnt(4)\n\t" \
    "v_mfma_f32_16x16x32_bf16 v[212:215], v[248:251], v[236:239], v[212:215]\n\t" \
    "ds_read_b128 v[248:251], %[wl] offset:7168\n\t" \
    "v_mfma_f32_16x16x32_bf16 v[216:219], v[80:83], v[232:235], v[216:219]\n\t" \
    "v_mfma_f32_16x16x32_bf16 v[216:219], v[84:87], v[236:239], v[216:219]\n\t" \
    "v_mfma_f32_16x16x32_bf16 v[220:223], v[88:91], v[232:235], v[220:223]\n\t" \
    "ds_read_b128 v[232:235], v252 offset:1024\n\t" \
    "v_mfma_f32_16x16x32_bf16 v[220:223], v[92:95], v[236:239], v[220:223]\n\t" \
    "v_xor_b32 v252, 0x3000, %[xa]\n\t" \
    "ds_read_b128 v[236:239], v252 offset:0\n\t" \
    "s_waitcnt lgkmcnt(6)\n\t" \
    "v_mfma_f32_16x16x32_bf16 v[220:223], v[112:115], v[240:243], v[220:223]\n\t" \
    "s_waitcnt lgkmcnt(5)\n\t" \
    "v_mfma_f32_16x16x32_bf16 v[220:223], v[116:119], v[224:227], v[220:223]\n\t" \
    "v_mfma_f32_16x16x32_bf16 v[216:219], v[104:107], v[240:243], v[216:219]\n\t" \
    "v_mfma_f32_16x16x32_bf16 v[216:219], v[108:111], v[224:227], v[216:219]\n\t" \
    "v_mfma_f32_16x16x32_bf16 v[212:215], v[96:99], v[240:243], v[212:215]\n\t" \
    "ds_read_b128 v[240:243], v252 offset:1024\n\t" \
    "v_mfma_f32_16x16x32_bf16 v[212:215], v[100:103], v[224:227], v[212:215]\n\t" \
    "ds_read_b128 v[224:227], v252 offset:16384\n\t" \
    "s_waitcnt lgkmcnt(6)\n\t" \
    "v_mfma_f32_16x16x32_bf16 v[200:203], v[96:99], v[228:231], v[200:203]\n\t" \
    "s_waitcnt lgkmcnt(3)\n\t" \
    "v_mfma_f32_16x16x32_bf16 v[200:203], v[100:103], v[232:235], v[200:203]\n\t" \
    "v_mfma_f32_16x16x32_bf16 v[204:207], v[104:107], v[228:231], v[204:207]\n\t" \
    "v_mfma_f32_16x16x32_bf16 v[204:207], v[108:111], v[232:235], v[204:207]\n\t" \
    "v_mfma_f32_16x16x32_bf16 v[208:211], v[112:115], v[228:231], v[208:211]\n\t" \
    "ds_read_b128 v[228:231], v252 offset:17408\n\t" \
    "v_mfma_f32_16x16x32_bf16 v[208:211], v[116:119], v[232:235], v[208:211]\n\t" \
    "v_xor_b32 v252, 0x3800, %[xa]\n\t" \
    "ds_read_b128 v[232:235], v252 offset:16384\n\t" \
    "s_waitcnt lgkmcnt(4)\n\t" \
    "v_mfma_f32_16x16x32_bf16 v[208:211], v[128:131], v[236:239], v[208:211]\n\t" \
    "s_waitcnt lgkmcnt(3)\n\t" \
    "v_mfma_f32_16x16x32_bf16 v[208:211], v[132:135], v[240:243], v[208:211]\n\t" \
    "v_mfma_f32_16x16x32_bf16 v[204:207], v[120:123], v[236:239], v[204:207]\n\t" \
    "v_mfma_f32_16x16x32_bf16 v[204:207], v[124:127], v[240:243], v[204:207]\n\t" \
    "v_mfma_f32_16x16x32_bf16 v[200:203], v[244:247], v[236:239], v[200:203]\n\t" \
    "ds_read_b128 v[236:239], v252 offset:17408\n\t" \
    "v_mfma_f32_16x16x32_bf16 v[200:203], v[248:251], v[240:243], v[200:203]\n\t" \
    "ds_read_b128 v[240:243], v252 offset:0\n\t" \
    "s_waitcnt lgkmcnt(4)\n\t" \
    "v_mfma_f32_16x16x32_bf16 v[212:215], v[244:247], v[224:227], v[212:215]\n\t" \
    "s_waitcnt lgkmcnt(3)\n\t" \
    "v_mfma_f32_16x16x32_bf16 v[212:215], v[248:251], v[228:231], v[212:215]\n\t" \
    "v_mfma_f32_16x16x32_bf16 v[216:219], v[120:123], v[224:227], v[216:219]\n\t" \
    "v_mfma_f32_16x16x32_bf16 v[216:219], v[124:127], v[228:231], v[216:219]\n\t" \
    "v_mfma_f32_16x16x32_bf16 v[220:223], v[128:131], v[224:227], v[220:223]\n\t" \
    "ds_read_b128 v[224:227], v252 offset:1024\n\t" \
    "v_mfma_f32_16x16x32_bf16 v[220:223], v[132:135], v[228:231], v[220:223]\n\t" \
    "v_and_b32 v252, 0x3ff, %[xa]\n\t" \
    "ds_read_b128 v[228:231], v252 offset:43008\n\t" \
    "s_waitcnt lgkmcnt(4)\n\t" \
    "v_mfma_f32_16x16x32_bf16 v[220:223], v[152:155], v[232:235], v[220:223]\n\t" \
    "s_waitcnt lgkmcnt(3)\n\t" \
    "v_mfma_f32_16x16x32_bf16 v[220:223], v[156:159], v[236:239], v[220:223]\n\t" \
    "v_mfma_f32_16x16x32_bf16 v[216:219], v[144:147], v[232:235], v[216:219]\n\t" \
    "v_mfma_f32_16x16x32_bf16 v[216:219], v[148:151], v[236:239], v[216:219]\n\t" \
    "v_mfma_f32_16x16x32_bf16 v[212:215], v[136:139], v[232:235], v[212:215]\n\t" \
    "ds_read_b128 v[232:235], v252 offset:44032\n\t" \
    "v_mfma_f32_16x16x32_bf16 v[212:215], v[140:143], v[236:239], v[212:215]\n\t" \
    "ds_read_b128 v[236:239], v252 offset:47104\n\t" \
    "s_waitcnt lgkmcnt(4)\n\t" \
    "v_mfma_f32_16x16x32_bf16 v[200:203], v[136:139], v[240:243], v[200:203]\n\t" \
    "s_waitcnt lgkmcnt(3)\n\t" \
    "v_mfma_f32_16x16x32_bf16 v[200:203], v[140:143], v[224:227], v[200:203]\n\t" \
    "v_mfma_f32_16x16x32_bf16 v[204:207], v[144:147], v[240:243], v[204:207]\n\t" \
    "v_mfma_f32_16x16x32_bf16 v[204:207], v[148:151], v[224:227], v[204:207]\n\t" \
    "v_mfma_f32_16x16x32_bf16 v[208:211], v[152:155], v[240:243], v[208:211]\n\t" \
    "ds_read_b128 v[240:243], v252 offset:48128\n\t" \
    "v_mfma_f32_16x16x32_bf16 v[208:211], v[156:159], v[224:227], v[208:211]\n\t" \
    "s_waitcnt lgkmcnt(3)\n\t" \
    "v_mfma_f32_16x16x32_bf16 v[208:211], v[192:195], v[228:231], v[208:211]\n\t" \
    "v_add_u32 v252, %[qs], v252\n\t" \
    "s_waitcnt lgkmcnt(2)\n\t" \
    "v_mfma_f32_16x16x32_bf16 v[208:211], v[196:199], v[232:235], v[208:211]\n\t" \
    "v_mfma_f32_16x16x32_bf16 v[204:207], v[184:187], v[228:231], v[204:207]\n\t" \
    "v_mfma_f32_16x16x32_bf16 v[204:207], v[188:191], v[232:235], v[204:207]\n\t" \
    "v_mfma_f32_16x16x32_bf16 v[200:203], v[176:179], v[228:231], v[200:203]\n\t" \
    "v_mfma_f32_16x16x32_bf16 v[200:203], v[180:183], v[232:235], v[200:203]\n\t" \
    "s_waitcnt lgkmcnt(1)\n\t" \
    "v_mfma_f32_16x16x32_bf16 v[212:215], v[176:179], v[236:239], v[212:215]\n\t" \
    "v_cvt_pk_bf16_f32 v208, v208, v209\n\t" \
    "v_cvt_pk_bf16_f32 v209, v210, v211\n\t" \
    "s_waitcnt lgkmcnt(0)\n\t" \
    "v_mfma_f32_16x16x32_bf16 v[212:215], v[180:183], v[240:243], v[212:215]\n\t" \
    "ds_write_b64 v252, v[208:209] offset:2048\n\t" \
    "v_cvt_pk_bf16_f32 v204, v204, v205\n\t" \
    "v_mfma_f32_16x16x32_bf16 v[216:219], v[184:187], v[236:239], v[216:219]\n\t" \
    "v_cvt_pk_bf16_f32 v205, v206, v207\n\t" \
    "ds_write_b64 v252, v[204:205] offset:1024\n\t" \
    "v_mfma_f32_16x16x32_bf16 v[216:219], v[188:191], v[240:243], v[216:219]\n\t" \
    "v_cvt_pk_bf16_f32 v200, v200, v201\n\t" \
    "v_cvt_pk_bf16_f32 v201, v202, v203\n\t" \
    "v_mfma_f32_16x16x32_bf16 v[220:223], v[192:195], v[236:239], v[220:223]\n\t" \
    "ds_write_b64 v252, v[200:201] offset:0\n\t" \
    "v_mfma_f32_16x16x32_bf16 v[220:223], v[196:199], v[240:243], v[220:223]\n\t" \
    "v_cvt_pk_bf16_f32 v212, v212, v213\n\t" \
    "v_cvt_pk_bf16_f32 v213, v214, v215\n\t" \
    "ds_write_b64 v252, v[212:213] offset:6144\n\t" \
    "v_cvt_pk_bf16_f32 v216, v216, v217\n\t" \
    "v_cvt_pk_bf16_f32 v217, v218, v219\n\t" \
    "ds_write_b64 v252, v[216:217] offset:7168\n\t" \
    "s_nop 1\n\t" \
    "v_cvt_pk_bf16_f32 v220, v220, v221\n\t" \
    "v_cvt_pk_bf16_f32 v221, v222, v223\n\t" \
    "ds_write_b64 v252, v[220:221] offset:8192\n\t" \
    "s_waitcnt lgkmcnt(0)\n\t"
#define ASM_XG_H_HI \
    "s_waitcnt lgkmcnt(0)\n\t" \
    "v_xor_b32 v252, 0x2000, %[xa]\n\t" \
    "ds_read_b128 v[224:227], v252 offset:16384\n\t" \
    "ds_read_b128 v[228:231], v252 offset:17408\n\t" \
    "ds_read_b128 v[244:247], %[wl] offset:4096\n\t" \
    "ds_read_b128 v[248:251], %[wl] offset:5120\n\t" \
    "ds_read_b128 v[232:235], v252 offset:0\n\t" \
    "ds_read_b128 v[236:239], v252 offset:1024\n\t" \
    "v_xor_b32 v252, 0x2800, %[xa]\n\t" \
    "ds_read_b128 v[240:243], v252 offset:0\n\t" \
    "s_waitcnt lgkmcnt(6)\n\t" \
    "v_mfma_f32_16x16x32_bf16 v[220:223], v[88:91], v[224:227], 0\n\t" \
    "s_waitcnt lgkmcnt(5)\n\t" \
    "v_mfma_f32_16x16x32_bf16 v[220:223], v[92:95], v[228:231], v[220:223]\n\t" \
    "v_mfma_f32_16x16x32_bf16 v[216:219], v[80:83], v[224:227], 0\n\t" \
    "v_mfma_f32_16x16x32_bf16 v[216:219], v[84:87], v[228:231], v[216:219]\n\t" \
    "s_waitcnt lgkmcnt(4)\n\t" \
    "v_mfma_f32_16x16x32_bf16 v[212:215], v[244:247], v[224:227], 0\n\t" \
    "ds_read_b128 v[224:227], v252 offset:1024\n\t" \
    "s_waitcnt lgkmcnt(4)\n\t" \
    "v_mfma_f32_16x16x32_bf16 v[212:215], v[248:251], v[228:231], v[212:215]\n\t" \
    "ds_read_b128 v[228:231], v252 offset:16384\n\t" \
    "s_waitcnt lgkmcnt(4)\n\t" \
    "v_mfma_f32_16x16x32_bf16 v[200:203], v[244:247], v[232:235], 0\n\t" \
    "ds_read_b128 v[244:247], %[wl] offset:6144\n\t" \
    "s_waitcnt lgkmcnt(4)\n\t" \
    "v_mfma_f32_16x16x32_bf16 v[200:203], v[248:251], v[236:239], v[200:203]\n\t" \
    "ds_read_b128 v[248:251], %[wl] offset:7168\n\t" \
    "v_mfma_f32_16x16x32_bf16 v[204:207], v[80:83], v[232:235], 0\n\t" \
    "v_mfma_f32_16x16x32_bf16 v[204:207], v[84:87], v[236:239], v[204:207]\n\t" \
    "v_mfma_f32_16x16x32_bf16 v[208:211], v[88:91], v[232:235], 0\n\t" \
    "ds_read_b128 v[232:235], v252 offset:17408\n\t" \
    "v_mfma_f32_16x16x32_bf16 v[208:211], v[92:95], v[236:239], v[208:211]\n\t" \
    "v_xor_b32 v252, 0x3000, %[xa]\n\t" \
    "ds_read_b128 v[236:239], v252 offset:16384\n\t" \
    "s_waitcnt lgkmcnt(6)\n\t" \
    "v_mfma_f32_16x16x32_bf16 v[208:211], v[112:115], v[240:243], v[208:211]\n\t" \
    "s_waitcnt lgkmcnt(5)\n\t" \
    "v_mfma_f32_16x16x32_bf16 v[208:211], v[116:119], v[224:227], v[208:211]\n\t" \
    "v_mfma_f32_16x16x32_bf16 v[204:207], v[104:107], v[240:243], v[204:207]\n\t" \
    "v_mfma_f32_16x16x32_bf16 v[204:207], v[108:111], v[224:227], v[204:207]\n\t" \
    "v_mfma_f32_16x16x32_bf16 v[200:203], v[96:99], v[240:243], v[200:203]\n\t" \
    "ds_read_b128 v[240:243], v252 offset:17408\n\t" \
    "v_mfma_f32_16x16x32_bf16 v[200:203], v[100:103], v[224:227], v[200:203]\n\t" \
    "s_nop 3\n\t" \
    "v_mov_b32 v224, %[tv]\n\t" \
    "s_waitcnt vmcnt(5)\n\t" \
    "buffer_store_dword v224, %[fv], %[rr], %[fp] offen sc1\n\t" \
    "ds_read_b128 v[224:227], v252 offset:0\n\t" \
    "s_waitcnt lgkmcnt(6)\n\t" \
    "v_mfma_f32_16x16x32_bf16 v[212:215], v[96:99], v[228:231], v[212:215]\n\t" \
    "s_waitcnt lgkmcnt(3)\n\t" \
    "v_mfma_f32_16x16x32_bf16 v[212:215], v[100:103], v[232:235], v[212:215]\n\t" \
    "v_mfma_f32_16x16x32_bf16 v[216:219], v[104:107], v[228:231], v[216:219]\n\t" \
    "v_mfma_f32_16x16x32_bf16 v[216:219], v[108:111], v[232:235], v[216:219]\n\t" \
    "v_mfma_f32_16x16x32_bf16 v[220:223], v[112:115], v[228:231], v[220:223]\n\t" \
    "ds_read_b128 v[228:231], v252 offset:1024\n\t" \
    "v_mfma_f32_16x16x32_bf16 v[220:223], v[116:119], v[232:235], v[220:223]\n\t" \
    "v_xor_b32 v252, 0x3800, %[xa]\n\t" \
    "ds_read_b128 v[232:235], v252 offset:0\n\t" \
    "s_waitcnt lgkmcnt(4)\n\t" \
    "v_mfma_f32_16x16x32_bf16 v[220:223], v[128:131], v[236:239], v[220:223]\n\t" \
    "s_waitcnt lgkmcnt(3)\n\t" \
    "v_mfma_f32_16x16x32_bf16 v[220:223], v[132:135], v[240:243], v[220:223]\n\t" \
    "v_mfma_f32_16x16x32_bf16 v[216:219], v[120:123], v[236:239], v[216:219]\n\t" \
    "v_mfma_f32_16x16x32_bf16 v[216:219], v[124:127], v[240:243], v[216:219]\n\t" \
    "v_mfma_f32_16x16x32_bf16 v[212:215], v[244:247], v[236:239], v[212:215]\n\t" \
    "ds_read_b128 v[236:239], v252 offset:1024\n\t" \
    "v_mfma_f32_16x16x32_bf16 v[212:215], v[248:251], v[240:243], v[212:215]\n\t" \
    "ds_read_b128 v[240:243], v252 offset:16384\n\t" \
    "s_waitcnt lgkmcnt(4)\n\t" \
    "v_mfma_f32_16x16x32_bf16 v[200:203], v[244:247], v[224:227], v[200:203]\n\t" \
    "ds_read_b128 v[244:247], %[wl] offset:8192\n\t" \
    "s_waitcnt lgkmcnt(4)\n\t" \
    "v_mfma_f32_16x16x32_bf16 v[200:203], v[248:251], v[228:231], v[200:203]\n\t" \
    "ds_read_b128 v[248:251], %[wl] offset:9216\n\t" \
    "v_mfma_f32_16x16x32_bf16 v[204:207], v[120:123], v[224:227], v[204:207]\n\t" \
    "v_mfma_f32_16x16x32_bf16 v[204:207], v[124:127], v[228:231], v[204:207]\n\t" \
    "v_mfma_f32_16x16x32_bf16 v[208:211], v[128:131], v[224:227], v[208:211]\n\t" \
    "ds_read_b128 v[224:227], v252 offset:17408\n\t" \
    "v_mfma_f32_16x16x32_bf16 v[208:211], v[132:135], v[228:231], v[208:211]\n\t" \
    "v_and_b32 v252, 0x3ff, %[xa]\n\t" \
    "ds_read_b128 v[228:231], v252 offset:45056\n\t" \
    "s_waitcnt lgkmcnt(6)\n\t" \
    "v_mfma_f32_16x16x32_bf16 v[208:211], v[152:155], v[232:235], v[208:211]\n\t" \
    "s_waitcnt lgkmcnt(5)\n\t" \
    "v_mfma_f32_16x16x32_bf16 v[208:211], v[156:159], v[236:239], v[208:211]\n\t" \
    "v_mfma_f32_16x16x32_bf16 v[204:207], v[144:147], v[232:235], v[204:207]\n\t" \
    "v_mfma_f32_16x16x32_bf16 v[204:207], v[148:151], v[236:239], v[204:207]\n\t" \
    "v_mfma_f32_16x16x32_bf16 v[200:203], v[136:139], v[232:235], v[200:203]\n\t" \
    "ds_read_b128 v[232:235], v252 offset:46080\n\t" \
    "v_mfma_f32_16x16x32_bf16 v[200:203], v[140:143], v[236:239], v[200:203]\n\t" \
    "ds_read_b128 v[236:239], v252 offset:40960\n\t" \
    "s_waitcnt lgkmcnt(6)\n\t" \
    "v_mfma_f32_16x16x32_bf16 v[212:215], v[136:139], v[240:243], v[212:215]\n\t" \
    "s_waitcnt lgkmcnt(3)\n\t" \
    "v_mfma_f32_16x16x32_bf16 v[212:215], v[140:143], v[224:227], v[212:215]\n\t" \
    "v_mfma_f32_16x16x32_bf16 v[216:219], v[144:147], v[240:243], v[216:219]\n\t" \
    "v_mfma_f32_16x16x32_bf16 v[216:219], v[148:151], v[224:227], v[216:219]\n\t" \
    "v_mfma_f32_16x16x32_bf16 v[220:223], v[152:155], v[240:243], v[220:223]\n\t" \
    "ds_read_b128 v[240:243], v252 offset:41984\n\t" \
    "v_mfma_f32_16x16x32_bf16 v[220:223], v[156:159], v[224:227], v[220:223]\n\t" \
    "s_waitcnt vmcnt(1) lgkmcnt(0)\n\t" \
    "s_barrier\n\t" ASM_ST \
    "ds_read_b128 v[224:227], %[xa] offset:0\n\t" \
    "v_mfma_f32_16x16x32_bf16 v[220:223], v[168:171], v[228:231], v[220:223]\n\t" \
    "v_mfma_f32_16x16x32_bf16 v[220:223], v[172:175], v[232:235], v[220:223]\n\t" \
    "v_mfma_f32_16x16x32_bf16 v[216:219], v[160:163], v[228:231], v[216:219]\n\t" \
    "v_mfma_f32_16x16x32_bf16 v[216:219], v[164:167], v[232:235], v[216:219]\n\t" \
    "v_mfma_f32_16x16x32_bf16 v[212:215], v[244:247], v[228:231], v[212:215]\n\t" \
    "ds_read_b128 v[228:231], %[xa] offset:1024\n\t" \
    "v_mfma_f32_16x16x32_bf16 v[212:215], v[248:251], v[232:235], v[212:215]\n\t" \
    "ds_read_b128 v[232:235], %[xa] offset:16384\n\t" \
    "v_mfma_f32_16x16x32_bf16 v[200:203], v[244:247], v[236:239], v[200:203]\n\t" \
    "ds_read_b128 v[244:247], %[wl] offset:0\n\t" \
    "v_mfma_f32_16x16x32_bf16 v[200:203], v[248:251], v[240:243], v[200:203]\n\t" \
    "ds_read_b128 v[248:251], %[wl] offset:1024\n\t" \
    "v_mfma_f32_16x16x32_bf16 v[204:207], v[160:163], v[236:239], v[204:207]\n\t" \
    "v_mfma_f32_16x16x32_bf16 v[204:207], v[164:167], v[240:243], v[204:207]\n\t" \
    "v_mfma_f32_16x16x32_bf16 v[208:211], v[168:171], v[236:239], v[208:211]\n\t" \
    "ds_read_b128 v[236:239], %[xa] offset:17408\n\t" \
    "v_mfma_f32_16x16x32_bf16 v[208:211], v[172:175], v[240:243], v[208:211]\n\t" \
    "v_xor_b32 v252, 0x800, %[xa]\n\t" \
    "ds_read_b128 v[240:243], v252 offset:16384\n\t" \
    "s_waitcnt lgkmcnt(6)\n\t" \
    "v_mfma_f32_16x16x32_bf16 v[208:211], v[8:11], v[224:227], v[208:211]\n\t" \
    "s_waitcnt lgkmcnt(5)\n\t" \
    "v_mfma_f32_16x16x32_bf16 v[208:211], v[12:15], v[228:231], v[208:211]\n\t" \
    "v_mfma_f32_16x16x32_bf16 v[204:207], v[0:3], v[224:227], v[204:207]\n\t" \
    "v_mfma_f32_16x16x32_bf16 v[204:207], v[4:7], v[228:231], v[204:207]\n\t" \
    "s_waitcnt lgkmcnt(3)\n\t" \
    "v_mfma_f32_16x16x32_bf16 v[200:203], v[244:247], v[224:227], v[200:203]\n\t" \
    "ds_read_b128 v[224:227], v252 offset:17408\n\t" \
    "s_waitcnt lgkmcnt(3)\n\t" \
    "v_mfma_f32_16x16x32_bf16 v[200:203], v[248:251], v[228:231], v[200:203]\n\t" \
    "ds_read_b128 v[228:231], v252 offset:0\n\t" \
    "v_mfma_f32_16x16x32_bf16 v[212:215], v[244:247], v[232:235], v[212:215]\n\t" \
    "ds_read_b128 v[244:247], %[wl] offset:2048\n\t" \
    "s_waitcnt lgkmcnt(4)\n\t" \
    "v_mfma_f32_16x16x32_bf16 v[212:215], v[248:251], v[236:239], v[212:215]\n\t" \
    "ds_read_b128 v[248:251], %[wl] offset:3072\n\t" \
    "v_mfma_f32_16x16x32_bf16 v[216:219], v[0:3], v[232:235], v[216:219]\n\t" \
    "v_mfma_f32_16x16x32_bf16 v[216:219], v[4:7], v[236:239], v[216:219]\n\t" \
    "v_mfma_f32_16x16x32_bf16 v[220:223], v[8:11], v[232:235], v[220:223]\n\t" \
    "ds_read_b128 v[232:235], v252 offset:1024\n\t" \
    "v_mfma_f32_16x16x32_bf16 v[220:223], v[12:15], v[236:239], v[220:223]\n\t" \
    "v_xor_b32 v252, 0x1000, %[xa]\n\t" \
    "ds_read_b128 v[236:239], v252 offset:0\n\t" \
    "s_waitcnt lgkmcnt(6)\n\t" \
    "v_mfma_f32_16x16x32_bf16 v[220:223], v[32:35], v[240:243], v[220:223]\n\t" \
    "s_waitcnt lgkmcnt(5)\n\t" \
    "v_mfma_f32_16x16x32_bf16 v[220:223], v[36:39], v[224:227], v[220:223]\n\t" \
    "v_mfma_f32_16x16x32_bf16 v[216:219], v[24:27], v[240:243], v[216:219]\n\t" \
    "v_mfma_f32_16x16x32_bf16 v[216:219], v[28:31], v[224:227], v[216:219]\n\t" \
    "v_mfma_f32_16x16x32_bf16 v[212:215], v[16:19], v[240:243], v[212:215]\n\t" \
    "ds_read_b128 v[240:243], v252 offset:1024\n\t" \
    "v_mfma_f32_16x16x32_bf16 v[212:215], v[20:23], v[224:227], v[212:215]\n\t" \
    "ds_read_b128 v[224:227], v252 offset:16384\n\t" \
    "s_waitcnt lgkmcnt(6)\n\t" \
    "v_mfma_f32_16x16x32_bf16 v[200:203], v[16:19], v[228:231], v[200:203]\n\t" \
    "s_waitcnt lgkmcnt(3)\n\t" \
    "v_mfma_f32_16x16x32_bf16 v[200:203], v[20:23], v[232:235], v[200:203]\n\t" \
    "v_mfma_f32_16x16x32_bf16 v[204:207], v[24:27], v[228:231], v[204:207]\n\t" \
    "v_mfma_f32_16x16x32_bf16 v[204:207], v[28:31], v[232:235], v[204:207]\n\t" \
    "v_mfma_f32_16x16x32_bf16 v[208:211], v[32:35], v[228:231], v[208:211]\n\t" \
    "ds_read_b128 v[228:231], v252 offset:17408\n\t" \
    "v_mfma_f32_16x16x32_bf16 v[208:211], v[36:39], v[232:235], v[208:211]\n\t" \
    "v_xor_b32 v252, 0x1800, %[xa]\n\t" \
    "ds_read_b128 v[232:235], v252 offset:16384\n\t" \
    "s_waitcnt lgkmcnt(4)\n\t" \
    "v_mfma_f32_16x16x32_bf16 v[208:211], v[48:51], v[236:239], v[208:211]\n\t" \
    "s_waitcnt lgkmcnt(3)\n\t" \
    "v_mfma_f32_16x16x32_bf16 v[208:211], v[52:55], v[240:243], v[208:211]\n\t" \
    "v_mfma_f32_16x16x32_bf16 v[204:207], v[40:43], v[236:239], v[204:207]\n\t" \
    "v_mfma_f32_16x16x32_bf16 v[204:207], v[44:47], v[240:243], v[204:207]\n\t" \
    "v_mfma_f32_16x16x32_bf16 v[200:203], v[244:247], v[236:239], v[200:203]\n\t" \
    "ds_read_b128 v[236:239], v252 offset:17408\n\t" \
    "v_mfma_f32_16x16x32_bf16 v[200:203], v[248:251], v[240:243], v[200:203]\n\t" \
    "ds_read_b128 v[240:243], v252 offset:0\n\t" \
    "s_waitcnt lgkmcnt(4)\n\t" \
    "v_mfma_f32_16x16x32_bf16 v[212:215], v[244:247], v[224:227], v[212:215]\n\t" \
    "s_waitcnt lgkmcnt(3)\n\t" \
    "v_mfma_f32_16x16x32_bf16 v[212:215], v[248:251], v[228:231], v[212:215]\n\t" \
    "v_mfma_f32_16x16x32_bf16 v[216:219], v[40:43], v[224:227], v[216:219]\n\t" \
    "v_mfma_f32_16x16x32_bf16 v[216:219], v[44:47], v[228:231], v[216:219]\n\t" \
    "v_mfma_f32_16x16x32_bf16 v[220:223], v[48:51], v[224:227], v[220:223]\n\t" \
    "ds_read_b128 v[224:227], v252 offset:1024\n\t" \
    "v_mfma_f32_16x16x32_bf16 v[220:223], v[52:55], v[228:231], v[220:223]\n\t" \
    "v_and_b32 v252, 0x3ff, %[xa]\n\t" \
    "ds_read_b128 v[228:231], v252 offset:43008\n\t" \
    "s_waitcnt lgkmcnt(4)\n\t" \
    "v_mfma_f32_16x16x32_bf16 v[220:223], v[72:75], v[232:235], v[220:223]\n\t" \
    "s_waitcnt lgkmcnt(3)\n\t" \
    "v_mfma_f32_16x16x32_bf16 v[220:223], v[76:79], v[236:239], v[220:223]\n\t" \
    "v_mfma_f32_16x16x32_bf16 v[216:219], v[64:67], v[232:235], v[216:219]\n\t" \
    "v_mfma_f32_16x16x32_bf16 v[216:219], v[68:71], v[236:239], v[216:219]\n\t" \
    "v_mfma_f32_16x16x32_bf16 v[212:215], v[56:59], v[232:235], v[212:215]\n\t" \
    "ds_read_b128 v[232:235], v252 offset:44032\n\t" \
    "v_mfma_f32_16x16x32_bf16 v[212:215], v[60:63], v[236:239], v[212:215]\n\t" \
    "ds_read_b128 v[236:239], v252 offset:47104\n\t" \
    "s_waitcnt lgkmcnt(4)\n\t" \
    "v_mfma_f32_16x16x32_bf16 v[200:203], v[56:59], v[240:243], v[200:203]\n\t" \
    "s_waitcnt lgkmcnt(3)\n\t" \
    "v_mfma_f32_16x16x32_bf16 v[200:203], v[60:63], v[224:227], v[200:203]\n\t" \
    "v_mfma_f32_16x16x32_bf16 v[204:207], v[64:67], v[240:243], v[204:207]\n\t" \
    "v_mfma_f32_16x16x32_bf16 v[204:207], v[68:71], v[224:227], v[204:207]\n\t" \
    "v_mfma_f32_16x16x32_bf16 v[208:211], v[72:75], v[240:243], v[208:211]\n\t" \
    "ds_read_b128 v[240:243], v252 offset:48128\n\t" \
    "v_mfma_f32_16x16x32_bf16 v[208:211], v[76:79], v[224:227], v[208:211]\n\t" \
    "s_waitcnt lgkmcnt(3)\n\t" \
    "v_mfma_f32_16x16x32_bf16 v[208:211], v[192:195], v[228:231], v[208:211]\n\t" \
    "v_add_u32 v252, %[qs], v252\n\t" \
    "s_waitcnt lgkmcnt(2)\n\t" \
    "v_mfma_f32_16x16x32_bf16 v[208:211], v[196:199], v[232:235], v[208:211]\n\t" \
    "v_mfma_f32_16x16x32_bf16 v[204:207], v[184:187], v[228:231], v[204:207]\n\t" \
    "v_mfma_f32_16x16x32_bf16 v[204:207], v[188:191], v[232:235], v[204:207]\n\t" \
    "v_mfma_f32_16x16x32_bf16 v[200:203], v[176:179], v[228:231], v[200:203]\n\t" \
    "v_mfma_f32_16x16x32_bf16 v[200:203], v[180:183], v[232:235], v[200:203]\n\t" \
    "s_waitcnt lgkmcnt(1)\n\t" \
    "v_mfma_f32_16x16x32_bf16 v[212:215], v[176:179], v[236:239], v[212:215]\n\t" \
    "v_cvt_pk_bf16_f32 v208, v208, v209\n\t" \
    "v_cvt_pk_bf16_f32 v209, v210, v211\n\t" \
    "s_waitcnt lgkmcnt(0)\n\t" \
    "v_mfma_f32_16x16x32_bf16 v[212:215], v[180:183], v[240:243], v[212:215]\n\t" \
    "ds_write_b64 v252, v[208:209] offset:2048\n\t" \
    "v_cvt_pk_bf16_f32 v204, v204, v205\n\t" \
    "v_mfma_f32_16x16x32_bf16 v[216:219], v[184:187], v[236:239], v[216:219]\n\t" \
    "v_cvt_pk_bf16_f32 v205, v206, v207\n\t" \
    "ds_write_b64 v252, v[204:205] offset:1024\n\t" \
    "v_mfma_f32_16x16x32_bf16 v[216:219], v[188:191], v[240:243], v[216:219]\n\t" \
    "v_cvt_pk_bf16_f32 v200, v200, v201\n\t" \
    "v_cvt_pk_bf16_f32 v201, v202, v203\n\t" \
    "v_mfma_f32_16x16x32_bf16 v[220:223], v[192:195], v[236:239], v[220:223]\n\t" \
    "ds_write_b64 v252, v[200:201] offset:0\n\t" \
    "v_mfma_f32_16x16x32_bf16 v[220:223], v[196:199], v[240:243], v[220:223]\n\t" \
    "v_cvt_pk_bf16_f32 v212, v212, v213\n\t" \
    "v_cvt_pk_bf16_f32 v213, v214, v215\n\t" \
    "ds_write_b64 v252, v[212:213] offset:6144\n\t" \
    "v_cvt_pk_bf16_f32 v216, v216, v217\n\t" \
    "v_cvt_pk_bf16_f32 v217, v218, v219\n\t" \
    "ds_write_b64 v252, v[216:217] offset:7168\n\t" \
    "s_nop 1\n\t" \
    "v_cvt_pk_bf16_f32 v220, v220, v221\n\t" \
    "v_cvt_pk_bf16_f32 v221, v222, v223\n\t" \
    "ds_write_b64 v252, v[220:221] offset:8192\n\t" \
    "s_waitcnt lgkmcnt(0)\n\t"
#define ASM_CRIT1_3 \
    "s_branch .Lc0%=\n\t" \
    ".Lcr%=:\n\t" \
    "s_cmp_eq_u32 %[n], 0\n\t" \
    "s_cbranch_scc1 .Lcb%=\n\t" \
    "s_sub_u32 %[n], %[n], 1\n\t" \
    "v_max_u32 v243, v208, v209\n\t" \
    "v_max3_u32 v243, v210, v211, v243\n\t" \
    "v_cmp_eq_u32 vcc, -1, v243\n\t" \
    "s_cbranch_vccz .Lcs0_%=\n\t" \
    "buffer_load_dwordx4 v[208:211], %[ab], %[rs], %[s0] offen sc1\n\t" \
    ".Lcs0_%=:\n\t" \
    "v_max_u32 v243, v236, v237\n\t" \
    "v_max3_u32 v243, v238, v239, v243\n\t" \
    "v_cmp_eq_u32 vcc, -1, v243\n\t" \
    "s_cbranch_vccz .Lcs1_%=\n\t" \
    "buffer_load_dwordx4 v[236:239], %[ab], %[rs], %[s1] offen sc1\n\t" \
    ".Lcs1_%=:\n\t" \
    "v_max_u32 v243, v220, v221\n\t" \
    "v_max3_u32 v243, v222, v223, v243\n\t" \
    "v_cmp_eq_u32 vcc, -1, v243\n\t" \
    "s_cbranch_vccz .Lca%=\n\t" \
    "buffer_load_dwordx4 v[220:223], %[ab], %[rs], %[s2] offen sc1\n\t" \
    "s_branch .Lca%=\n\t" \
    ".Lc0%=:\n\t" \
    "buffer_load_dwordx4 v[208:211], %[ab], %[rs], %[s0] offen sc1\n\t" \
    "buffer_load_dwordx4 v[236:239], %[ab], %[rs], %[s1] offen sc1\n\t" \
    "buffer_load_dwordx4 v[220:223], %[ab], %[rs], %[s2] offen sc1\n\t" \
    "v_bfe_i32 v241, %[ab], 7, 1\n\t" \
    "v_and_b32 v241, 0xaaaa, v241\n\t" \
    "v_or_b32 v241, 0x4444, v241\n\t" \
    "v_xor_b32 v212, 0x400, %[ab]\n\t" \
    "v_xor_b32 v216, 0x800, %[ab]\n\t" \
    "v_xor_b32 v242, 0xc00, %[ab]\n\t" \
    "v_xor_b32 v240, 0x1400, %[ab]\n\t" \
    ".Lca%=:\n\t" \
    "s_waitcnt vmcnt(0)\n\t" \
    "ds_write_b128 %[ab], v[208:211] offset:0\n\t" \
    "ds_write_b128 %[ab], v[236:239] offset:4096\n\t" \
    "ds_write_b128 %[ab], v[220:223] offset:8192\n\t" \
    "v_max_u32 v243, v208, v209\n\t" \
    "v_max3_u32 v243, v210, v211, v243\n\t" \
    "v_max3_u32 v243, v236, v237, v243\n\t" \
    "v_max3_u32 v243, v238, v239, v243\n\t" \
    "v_max3_u32 v243, v220, v221, v243\n\t" \
    "v_max3_u32 v243, v222, v223, v243\n\t" \
    "v_cmp_eq_u32 vcc, -1, v243\n\t" \
    "s_cbranch_vccnz .Lcr%=\n\t" \
    ".Lcb%=:\n\t" \
    PRIUP \
    ASM_ST2 \
    "v_smfmac_f32_16x16x64_bf16 v[224:227], v[208:211], v[200:207], v241\n\t" \
    "ds_read_b128 v[200:203], %[wl] offset:4096\n\t" \
    "ds_read_b128 v[204:207], %[wl] offset:5120\n\t" \
    "v_smfmac_f32_16x16x64_bf16 v[244:247], v[208:211], v[0:7], v241\n\t" \
    "v_smfmac_f32_16x16x64_bf16 v[248:251], v[208:211], v[8:15], v241\n\t" \
    "s_waitcnt lgkmcnt(2)\n\t" \
    "s_barrier\n\t" ASM_ST \
    "ds_read_b128 v[212:215], v212 offset:0\n\t" \
    "ds_read_b128 v[216:219], v216 offset:0\n\t" \
    "ds_read_b128 v[220:223], v242 offset:0\n\t" \
    "ds_read_b128 v[208:211], v240 offset:0\n\t" \
    "v_smfmac_f32_16x16x64_bf16 v[244:247], v[236:239], v[80:87], v241\n\t" \
    "v_xor_b32 v240, 0x1800, %[ab]\n\t" \
    "v_smfmac_f32_16x16x64_bf16 v[248:251], v[236:239], v[88:95], v241\n\t" \
    "s_waitcnt lgkmcnt(4)\n\t" \
    "v_smfmac_f32_16x16x64_bf16 v[224:227], v[236:239], v[200:207], v241\n\t" \
    "ds_read_b128 v[200:203], %[wl] offset:6144\n\t" \
    "ds_read_b128 v[204:207], %[wl] offset:7168\n\t" \
    "ds_read_b128 v[236:239], v240 offset:0\n\t" \
    "s_waitcnt lgkmcnt(6)\n\t" \
    "v_smfmac_f32_16x16x64_bf16 v[224:227], v[212:215], v[16:23], v241\n\t" \
    "v_smfmac_f32_16x16x64_bf16 v[244:247], v[212:215], v[24:31], v241\n\t" \
    "v_xor_b32 v240, 0x1c00, %[ab]\n\t" \
    "v_smfmac_f32_16x16x64_bf16 v[248:251], v[212:215], v[32:39], v241\n\t" \
    "ds_read_b128 v[212:215], v240 offset:0\n\t" \
    "s_waitcnt lgkmcnt(6)\n\t" \
    "v_smfmac_f32_16x16x64_bf16 v[224:227], v[216:219], v[228:235], v241\n\t" \
    "ds_read_b128 v[228:231], %[wl] offset:8192\n\t" \
    "ds_read_b128 v[232:235], %[wl] offset:9216\n\t" \
    "v_smfmac_f32_16x16x64_bf16 v[244:247], v[216:219], v[40:47], v241\n\t" \
    "v_and_b32 v240, 0x3ff, %[ab]\n\t" \
    "v_smfmac_f32_16x16x64_bf16 v[248:251], v[216:219], v[48:55], v241\n\t" \
    "ds_read_b128 v[216:219], v240 offset:8192\n\t" \
    "s_waitcnt lgkmcnt(8)\n\t" \
    "v_smfmac_f32_16x16x64_bf16 v[224:227], v[220:223], v[56:63], v241\n\t" \
    "v_smfmac_f32_16x16x64_bf16 v[244:247], v[220:223], v[64:71], v241\n\t" \
    "v_smfmac_f32_16x16x64_bf16 v[248:251], v[220:223], v[72:79], v241\n\t" \
    "ds_read_b128 v[220:223], v240 offset:9216\n\t" \
    "s_waitcnt lgkmcnt(8)\n\t" \
    "v_smfmac_f32_16x16x64_bf16 v[244:247], v[208:211], v[104:111], v241\n\t" \
    "s_waitcnt lgkmcnt(5)\n\t" \
    "v_smfmac_f32_16x16x64_bf16 v[244:247], v[236:239], v[120:127], v241\n\t" \
    "s_waitcnt lgkmcnt(4)\n\t" \
    "v_smfmac_f32_16x16x64_bf16 v[244:247], v[212:215], v[144:151], v241\n\t" \
    "s_waitcnt lgkmcnt(1)\n\t" \
    "v_smfmac_f32_16x16x64_bf16 v[244:247], v[216:219], v[160:167], v241\n\t" \
    "s_waitcnt lgkmcnt(0)\n\t" \
    "v_smfmac_f32_16x16x64_bf16 v[244:247], v[220:223], v[184:191], v241\n\t" \
    "v_smfmac_f32_16x16x64_bf16 v[248:251], v[208:211], v[112:119], v241\n\t" \
    "v_smfmac_f32_16x16x64_bf16 v[248:251], v[236:239], v[128:135], v241\n\t" \
    "v_smfmac_f32_16x16x64_bf16 v[248:251], v[212:215], v[152:159], v241\n\t" \
    "v_permlane32_swap_b32 v244, v246\n\t" \
    "v_permlane32_swap_b32 v245, v247\n\t" \
    "v_smfmac_f32_16x16x64_bf16 v[248:251], v[216:219], v[168:175], v241\n\t" \
    "v_add_f32 v244, v244, v246\n\t" \
    "v_add_f32 v245, v245, v247\n\t" \
    "v_smfmac_f32_16x16x64_bf16 v[248:251], v[220:223], v[192:199], v241\n\t" \
    "v_cvt_pk_bf16_f32 v244, v244, v245\n\t" \
    "ds_write_b16 %[dst], v244 offset:512\n\t" \
    "ds_write_b16_d16_hi %[dst], v244 offset:528\n\t" \
    "v_smfmac_f32_16x16x64_bf16 v[224:227], v[208:211], v[96:103], v241\n\t" \
    "v_smfmac_f32_16x16x64_bf16 v[224:227], v[236:239], v[200:207], v241\n\t" \
    "v_smfmac_f32_16x16x64_bf16 v[224:227], v[212:215], v[136:143], v241\n\t" \
    "v_permlane32_swap_b32 v248, v250\n\t" \
    "v_permlane32_swap_b32 v249, v251\n\t" \
    "v_smfmac_f32_16x16x64_bf16 v[224:227], v[216:219], v[228:235], v241\n\t" \
    "v_add_f32 v248, v248, v250\n\t" \
    "v_add_f32 v249, v249, v251\n\t" \
    "v_smfmac_f32_16x16x64_bf16 v[224:227], v[220:223], v[176:183], v241\n\t" \
    "v_cvt_pk_bf16_f32 v248, v248, v249\n\t" \
    "ds_write_b16 %[dst], v248 offset:1024\n\t" \
    "ds_write_b16_d16_hi %[dst], v248 offset:1040\n\t" \
    "s_nop 4\n\t" \
    "v_permlane32_swap_b32 v224, v226\n\t" \
    "v_permlane32_swap_b32 v225, v227\n\t" \
    "v_add_f32 v224, v224, v226\n\t" \
    "v_add_f32 v225, v225, v227\n\t" \
    "v_cvt_pk_bf16_f32 v224, v224, v225\n\t" \
    "ds_write_b16 %[dst], v224 offset:0\n\t" \
    "ds_write_b16_d16_hi %[dst], v224 offset:16\n\t"
#define ASM_CRIT1_2 \
    "s_branch .Lc0%=\n\t" \
    ".Lcr%=:\n\t" \
    "s_cmp_eq_u32 %[n], 0\n\t" \
    "s_cbranch_scc1 .Lcb%=\n\t" \
    "s_sub_u32 %[n], %[n], 1\n\t" \
    "v_max_u32 v243, v208, v209\n\t" \
    "v_max3_u32 v243, v210, v211, v243\n\t" \
    "v_cmp_eq_u32 vcc, -1, v243\n\t" \
    "s_cbranch_vccz .Lcs0_%=\n\t" \
    "buffer_load_dwordx4 v[208:211], %[ab], %[rs], %[s0] offen sc1\n\t" \
    ".Lcs0_%=:\n\t" \
    "v_max_u32 v243, v236, v237\n\t" \
    "v_max3_u32 v243, v238, v239, v243\n\t" \
    "v_cmp_eq_u32 vcc, -1, v243\n\t" \
    "s_cbranch_vccz .Lca%=\n\t" \
    "buffer_load_dwordx4 v[236:239], %[ab], %[rs], %[s1] offen sc1\n\t" \
    "s_branch .Lca%=\n\t" \
    ".Lc0%=:\n\t" \
    "buffer_load_dwordx4 v[208:211], %[ab], %[rs], %[s0] offen sc1\n\t" \
    "buffer_load_dwordx4 v[236:239], %[ab], %[rs], %[s1] offen sc1\n\t" \
    "v_bfe_i32 v241, %[ab], 7, 1\n\t" \
    "v_and_b32 v241, 0xaaaa, v241\n\t" \
    "v_or_b32 v241, 0x4444, v241\n\t" \
    "v_xor_b32 v212, 0x400, %[ab]\n\t" \
    "v_xor_b32 v216, 0x800, %[ab]\n\t" \
    "v_xor_b32 v242, 0xc00, %[ab]\n\t" \
    "v_xor_b32 v240, 0x1400, %[ab]\n\t" \
    ".Lca%=:\n\t" \
    "s_waitcnt vmcnt(0)\n\t" \
    "ds_write_b128 %[ab], v[208:211] offset:0\n\t" \
    "ds_write_b128 %[ab], v[236:239] offset:4096\n\t" \
    "v_max_u32 v243, v208, v209\n\t" \
    "v_max3_u32 v243, v210, v211, v243\n\t" \
    "v_max3_u32 v243, v236, v237, v243\n\t" \
    "v_max3_u32 v243, v238, v239, v243\n\t" \
    "v_cmp_eq_u32 vcc, -1, v243\n\t" \
    "s_cbranch_vccnz .Lcr%=\n\t" \
    ".Lcb%=:\n\t" \
    PRIUP \
    ASM_ST2 \
    "v_smfmac_f32_16x16x64_bf16 v[224:227], v[208:211], v[200:207], v241\n\t" \
    "ds_read_b128 v[200:203], %[wl] offset:4096\n\t" \
    "ds_read_b128 v[204:207], %[wl] offset:5120\n\t" \
    "v_smfmac_f32_16x16x64_bf16 v[244:247], v[208:211], v[0:7], v241\n\t" \
    "v_smfmac_f32_16x16x64_bf16 v[248:251], v[208:211], v[8:15], v241\n\t" \
    "s_waitcnt lgkmcnt(2)\n\t" \
    "s_barrier\n\t" ASM_ST \
    "ds_read_b128 v[212:215], v212 offset:0\n\t" \
    "ds_read_b128 v[216:219], v216 offset:0\n\t" \
    "ds_read_b128 v[220:223], v242 offset:0\n\t" \
    "ds_read_b128 v[208:211], v240 offset:0\n\t" \
    "v_smfmac_f32_16x16x64_bf16 v[244:247], v[236:239], v[80:87], v241\n\t" \
    "v_xor_b32 v240, 0x1800, %[ab]\n\t" \
    "v_smfmac_f32_16x16x64_bf16 v[248:251], v[236:239], v[88:95], v241\n\t" \
    "s_waitcnt lgkmcnt(4)\n\t" \
    "v_smfmac_f32_16x16x64_bf16 v[224:227], v[236:239], v[200:207], v241\n\t" \
    "ds_read_b128 v[200:203], %[wl] offset:6144\n\t" \
    "ds_read_b128 v[204:207], %[wl] offset:7168\n\t" \
    "ds_read_b128 v[236:239], v240 offset:0\n\t" \
    "s_waitcnt lgkmcnt(6)\n\t" \
    "v_smfmac_f32_16x16x64_bf16 v[224:227], v[212:215], v[16:23], v241\n\t" \
    "v_smfmac_f32_16x16x64_bf16 v[244:247], v[212:215], v[24:31], v241\n\t" \
    "v_xor_b32 v240, 0x1c00, %[ab]\n\t" \
    "v_smfmac_f32_16x16x64_bf16 v[248:251], v[212:215], v[32:39], v241\n\t" \
    "ds_read_b128 v[212:215], v240 offset:0\n\t" \
    "s_waitcnt lgkmcnt(6)\n\t" \
    "v_smfmac_f32_16x16x64_bf16 v[224:227], v[216:219], v[228:235], v241\n\t" \
    "ds_read_b128 v[228:231], %[wl] offset:8192\n\t" \
    "ds_read_b128 v[232:235], %[wl] offset:9216\n\t" \
    "v_smfmac_f32_16x16x64_bf16 v[244:247], v[216:219], v[40:47], v241\n\t" \
    "v_and_b32 v240, 0x3ff, %[ab]\n\t" \
    "v_smfmac_f32_16x16x64_bf16 v[248:251], v[216:219], v[48:55], v241\n\t" \
    "ds_read_b128 v[216:219], v240 offset:8192\n\t" \
    "s_waitcnt lgkmcnt(8)\n\t" \
    "v_smfmac_f32_16x16x64_bf16 v[224:227], v[220:223], v[56:63], v241\n\t" \
    "v_smfmac_f32_16x16x64_bf16 v[244:247], v[220:223], v[64:71], v241\n\t" \
    "v_smfmac_f32_16x16x64_bf16 v[248:251], v[220:223], v[72:79], v241\n\t" \
    "ds_read_b128 v[220:223], v240 offset:9216\n\t" \
    "s_waitcnt lgkmcnt(8)\n\t" \
    "v_smfmac_f32_16x16x64_bf16 v[244:247], v[208:211], v[104:111], v241\n\t" \
    "s_waitcnt lgkmcnt(5)\n\t" \
    "v_smfmac_f32_16x16x64_bf16 v[244:247], v[236:239], v[120:127], v241\n\t" \
    "s_waitcnt lgkmcnt(4)\n\t" \
    "v_smfmac_f32_16x16x64_bf16 v[244:247], v[212:215], v[144:151], v241\n\t" \
    "s_waitcnt lgkmcnt(1)\n\t" \
    "v_smfmac_f32_16x16x64_bf16 v[244:247], v[216:219], v[160:167], v241\n\t" \
    "s_waitcnt lgkmcnt(0)\n\t" \
    "v_smfmac_f32_16x16x64_bf16 v[244:247], v[220:223], v[184:191], v241\n\t" \
    "v_smfmac_f32_16x16x64_bf16 v[248:251], v[208:211], v[112:119], v241\n\t" \
    "v_smfmac_f32_16x16x64_bf16 v[248:251], v[236:239], v[128:135], v241\n\t" \
    "v_smfmac_f32_16x16x64_bf16 v[248:251], v[212:215], v[152:159], v241\n\t" \
    "v_permlane32_swap_b32 v244, v246\n\t" \
    "v_permlane32_swap_b32 v245, v247\n\t" \
    "v_smfmac_f32_16x16x64_bf16 v[248:251], v[216:219], v[168:175], v241\n\t" \
    "v_add_f32 v244, v244, v246\n\t" \
    "v_add_f32 v245, v245, v247\n\t" \
    "v_smfmac_f32_16x16x64_bf16 v[248:251], v[220:223], v[192:199], v241\n\t" \
    "v_cvt_pk_bf16_f32 v244, v244, v245\n\t" \
    "ds_write_b16 %[dst], v244 offset:512\n\t" \
    "ds_write_b16_d16_hi %[dst], v244 offset:528\n\t" \
    "v_smfmac_f32_16x16x64_bf16 v[224:227], v[208:211], v[96:103], v241\n\t" \
    "v_smfmac_f32_16x16x64_bf16 v[224:227], v[236:239], v[200:207], v241\n\t" \
    "v_smfmac_f32_16x16x64_bf16 v[224:227], v[212:215], v[136:143], v241\n\t" \
    "v_permlane32_swap_b32 v248, v250\n\t" \
    "v_permlane32_swap_b32 v249, v251\n\t" \
    "v_smfmac_f32_16x16x64_bf16 v[224:227], v[216:219], v[228:235], v241\n\t" \
    "v_add_f32 v248, v248, v250\n\t" \
    "v_add_f32 v249, v249, v251\n\t" \
    "v_smfmac_f32_16x16x64_bf16 v[224:227], v[220:223], v[176:183], v241\n\t" \
    "v_cvt_pk_bf16_f32 v248, v248, v249\n\t" \
    "ds_write_b16 %[dst], v248 offset:1024\n\t" \
    "ds_write_b16_d16_hi %[dst], v248 offset:1040\n\t" \
    "s_nop 4\n\t" \
    "v_permlane32_swap_b32 v224, v226\n\t" \
    "v_permlane32_swap_b32 v225, v227\n\t" \
    "v_add_f32 v224, v224, v226\n\t" \
    "v_add_f32 v225, v225, v227\n\t" \
    "v_cvt_pk_bf16_f32 v224, v224, v225\n\t" \
    "ds_write_b16 %[dst], v224 offset:0\n\t" \
    "ds_write_b16_d16_hi %[dst], v224 offset:16\n\t"
#define ASM_CRIT1_N \
    "v_bfe_i32 v241, %[ab], 7, 1\n\t" \
    "v_and_b32 v241, 0xaaaa, v241\n\t" \
    "v_or_b32 v241, 0x4444, v241\n\t" \
    "v_xor_b32 v212, 0x400, %[ab]\n\t" \
    "v_xor_b32 v216, 0x800, %[ab]\n\t" \
    "v_xor_b32 v242, 0xc00, %[ab]\n\t" \
    "v_xor_b32 v240, 0x1400, %[ab]\n\t" \
    "v_xor_b32 v236, 0x1000, %[ab]\n\t" \
    "s_waitcnt lgkmcnt(0)\n\t" \
    "s_barrier\n\t" ASM_ST \
    "ds_read_b128 v[208:211], %[ab] offset:0\n\t" \
    "ds_read_b128 v[212:215], v212 offset:0\n\t" \
    "ds_read_b128 v[216:219], v216 offset:0\n\t" \
    "ds_read_b128 v[220:223], v242 offset:0\n\t" \
    "ds_read_b128 v[236:239], v236 offset:0\n\t" \
    "s_waitcnt lgkmcnt(4)\n\t" \
    "v_smfmac_f32_16x16x64_bf16 v[224:227], v[208:211], v[200:207], v241\n\t" \
    "ds_read_b128 v[200:203], %[wl] offset:4096\n\t" \
    "ds_read_b128 v[204:207], %[wl] offset:5120\n\t" \
    "v_smfmac_f32_16x16x64_bf16 v[244:247], v[208:211], v[0:7], v241\n\t" \
    "v_smfmac_f32_16x16x64_bf16 v[248:251], v[208:211], v[8:15], v241\n\t" \
    "ds_read_b128 v[208:211], v240 offset:0\n\t" \
    "s_waitcnt lgkmcnt(6)\n\t" \
    "v_smfmac_f32_16x16x64_bf16 v[224:227], v[212:215], v[16:23], v241\n\t" \
    "v_smfmac_f32_16x16x64_bf16 v[244:247], v[212:215], v[24:31], v241\n\t" \
    "v_xor_b32 v240, 0x1800, %[ab]\n\t" \
    "v_smfmac_f32_16x16x64_bf16 v[248:251], v[212:215], v[32:39], v241\n\t" \
    "ds_read_b128 v[212:215], v240 offset:0\n\t" \
    "s_waitcnt lgkmcnt(6)\n\t" \
    "v_smfmac_f32_16x16x64_bf16 v[224:227], v[216:219], v[228:235], v241\n\t" \
    "ds_read_b128 v[228:231], %[wl] offset:6144\n\t" \
    "ds_read_b128 v[232:235], %[wl] offset:7168\n\t" \
    "v_smfmac_f32_16x16x64_bf16 v[244:247], v[216:219], v[40:47], v241\n\t" \
    "v_xor_b32 v240, 0x1c00, %[ab]\n\t" \
    "v_smfmac_f32_16x16x64_bf16 v[248:251], v[216:219], v[48:55], v241\n\t" \
    "ds_read_b128 v[216:219], v240 offset:0\n\t" \
    "s_waitcnt lgkmcnt(8)\n\t" \
    "v_smfmac_f32_16x16x64_bf16 v[224:227], v[220:223], v[56:63], v241\n\t" \
    "v_smfmac_f32_16x16x64_bf16 v[244:247], v[220:223], v[64:71], v241\n\t" \
    "v_and_b32 v240, 0x3ff, %[ab]\n\t" \
    "v_smfmac_f32_16x16x64_bf16 v[248:251], v[220:223], v[72:79], v241\n\t" \
    "ds_read_b128 v[220:223], v240 offset:8192\n\t" \
    "s_waitcnt lgkmcnt(6)\n\t" \
    "v_smfmac_f32_16x16x64_bf16 v[224:227], v[236:239], v[200:207], v241\n\t" \
    "ds_read_b128 v[200:203], %[wl] offset:8192\n\t" \
    "ds_read_b128 v[204:207], %[wl] offset:9216\n\t" \
    "v_smfmac_f32_16x16x64_bf16 v[244:247], v[236:239], v[80:87], v241\n\t" \
    "v_smfmac_f32_16x16x64_bf16 v[248:251], v[236:239], v[88:95], v241\n\t" \
    "ds_read_b128 v[236:239], v240 offset:9216\n\t" \
    "s_waitcnt lgkmcnt(8)\n\t" \
    "v_smfmac_f32_16x16x64_bf16 v[244:247], v[208:211], v[104:111], v241\n\t" \
    "s_waitcnt lgkmcnt(7)\n\t" \
    "v_smfmac_f32_16x16x64_bf16 v[244:247], v[212:215], v[120:127], v241\n\t" \
    "s_waitcnt lgkmcnt(4)\n\t" \
    "v_smfmac_f32_16x16x64_bf16 v[244:247], v[216:219], v[144:151], v241\n\t" \
    "s_waitcnt lgkmcnt(3)\n\t" \
    "v_smfmac_f32_16x16x64_bf16 v[244:247], v[220:223], v[160:167], v241\n\t" \
    "s_waitcnt lgkmcnt(0)\n\t" \
    "v_smfmac_f32_16x16x64_bf16 v[244:247], v[236:239], v[184:191], v241\n\t" \
    "v_smfmac_f32_16x16x64_bf16 v[248:251], v[208:211], v[112:119], v241\n\t" \
    "v_smfmac_f32_16x16x64_bf16 v[248:251], v[212:215], v[128:135], v241\n\t" \
    "v_smfmac_f32_16x16x64_bf16 v[248:251], v[216:219], v[152:159], v241\n\t" \
    "v_permlane32_swap_b32 v244, v246\n\t" \
    "v_permlane32_swap_b32 v245, v247\n\t" \
    "v_smfmac_f32_16x16x64_bf16 v[248:251], v[220:223], v[168:175], v241\n\t" \
    "v_add_f32 v244, v244, v246\n\t" \
    "v_add_f32 v245, v245, v247\n\t" \
    "v_smfmac_f32_16x16x64_bf16 v[248:251], v[236:239], v[192:199], v241\n\t" \
    "v_cvt_pk_bf16_f32 v244, v244, v245\n\t" \
    "ds_write_b16 %[dst], v244 offset:512\n\t" \
    "ds_write_b16_d16_hi %[dst], v244 offset:528\n\t" \
    "v_smfmac_f32_16x16x64_bf16 v[224:227], v[208:211], v[96:103], v241\n\t" \
    "v_smfmac_f32_16x16x64_bf16 v[224:227], v[212:215], v[228:235], v241\n\t" \
    "v_smfmac_f32_16x16x64_bf16 v[224:227], v[216:219], v[136:143], v241\n\t" \
    "v_permlane32_swap_b32 v248, v250\n\t" \
    "v_permlane32_swap_b32 v249, v251\n\t" \
    "v_smfmac_f32_16x16x64_bf16 v[224:227], v[220:223], v[200:207], v241\n\t" \
    "v_add_f32 v248, v248, v250\n\t" \
    "v_add_f32 v249, v249, v251\n\t" \
    "v_smfmac_f32_16x16x64_bf16 v[224:227], v[236:239], v[176:183], v241\n\t" \
    "v_cvt_pk_bf16_f32 v248, v248, v249\n\t" \
    "ds_write_b16 %[dst], v248 offset:1024\n\t" \
    "ds_write_b16_d16_hi %[dst], v248 offset:1040\n\t" \
    "s_nop 4\n\t" \
    "v_permlane32_swap_b32 v224, v226\n\t" \
    "v_permlane32_swap_b32 v225, v227\n\t" \
    "v_add_f32 v224, v224, v226\n\t" \
    "v_add_f32 v225, v225, v227\n\t" \
    "v_cvt_pk_bf16_f32 v224, v224, v225\n\t" \
    "ds_write_b16 %[dst], v224 offset:0\n\t" \
    "ds_write_b16_d16_hi %[dst], v224 offset:16\n\t"
#define ASM_CRIT1_V3 \
    "s_branch .Lc0%=\n\t" \
    ".Lcr%=:\n\t" \
    "s_cmp_eq_u32 %[n], 0\n\t" \
    "s_cbranch_scc1 .Lcb%=\n\t" \
    "s_sub_u32 %[n], %[n], 1\n\t" \
    "v_max_u32 v243, v208, v209\n\t" \
    "v_max3_u32 v243, v210, v211, v243\n\t" \
    "v_cmp_eq_u32 vcc, -1, v243\n\t" \
    "s_cbranch_vccz .Lcs0_%=\n\t" \
    "buffer_load_dwordx4 v[208:211], %[ab], %[rs], %[s0] offen sc1\n\t" \
    ".Lcs0_%=:\n\t" \
    "v_max_u32 v243, v236, v237\n\t" \
    "v_max3_u32 v243, v238, v239, v243\n\t" \
    "v_cmp_eq_u32 vcc, -1, v243\n\t" \
    "s_cbranch_vccz .Lcs1_%=\n\t" \
    "buffer_load_dwordx4 v[236:239], %[ab], %[rs], %[s1] offen sc1\n\t" \
    ".Lcs1_%=:\n\t" \
    "v_max_u32 v243, v220, v221\n\t" \
    "v_max3_u32 v243, v222, v223, v243\n\t" \
    "v_cmp_eq_u32 vcc, -1, v243\n\t" \
    "s_cbranch_vccz .Lca%=\n\t" \
    "buffer_load_dwordx4 v[220:223], %[ab], %[rs], %[s2] offen sc1\n\t" \
    "s_branch .Lca%=\n\t" \
    ".Lc0%=:\n\t" \
    "buffer_load_dwordx4 v[208:211], %[ab], %[rs], %[s0] offen sc1\n\t" \
    "buffer_load_dwordx4 v[236:239], %[ab], %[rs], %[s1] offen sc1\n\t" \
    "buffer_load_dwordx4 v[220:223], %[ab], %[rs], %[s2] offen sc1\n\t" \
    "v_bfe_i32 v241, %[ab], 7, 1\n\t" \
    "v_and_b32 v241, 0xaaaa, v241\n\t" \
    "v_or_b32 v241, 0x4444, v241\n\t" \
    "v_xor_b32 v212, 0x400, %[ab]\n\t" \
    "v_xor_b32 v216, 0x800, %[ab]\n\t" \
    "v_xor_b32 v242, 0xc00, %[ab]\n\t" \
    "v_xor_b32 v240, 0x1400, %[ab]\n\t" \
    ".Lca%=:\n\t" \
    "s_waitcnt vmcnt(0)\n\t" \
    "ds_write_b128 %[ab], v[208:211] offset:0\n\t" \
    "ds_write_b128 %[ab], v[236:239] offset:4096\n\t" \
    "ds_write_b128 %[ab], v[220:223] offset:8192\n\t" \
    "v_max_u32 v243, v208, v209\n\t" \
    "v_max3_u32 v243, v210, v211, v243\n\t" \
    "v_max3_u32 v243, v236, v237, v243\n\t" \
    "v_max3_u32 v243, v238, v239, v243\n\t" \
    "v_max3_u32 v243, v220, v221, v243\n\t" \
    "v_max3_u32 v243, v222, v223, v243\n\t" \
    "v_cmp_eq_u32 vcc, -1, v243\n\t" \
    "s_cbranch_vccnz .Lcr%=\n\t" \
    ".Lcb%=:\n\t" \
    PRIUP \
    ASM_ST2 \
    "v_smfmac_f32_16x16x64_bf16 v[224:227], v[208:211], v[200:207], v241\n\t" \
    "ds_read_b128 v[200:203], %[wl] offset:4096\n\t" \
    "ds_read_b128 v[204:207], %[wl] offset:5120\n\t" \
    "v_smfmac_f32_16x16x64_bf16 v[244:247], v[208:211], v[0:7], v241\n\t" \
    "v_smfmac_f32_16x16x64_bf16 v[248:251], v[208:211], v[8:15], v241\n\t" \
    "s_waitcnt lgkmcnt(2)\n\t" \
    "buffer_load_dword %[fv], %[fv], %[rs], %[fs] offen sc1\n\t" \
    "s_barrier\n\t" ASM_ST \
    "ds_read_b128 v[212:215], v212 offset:0\n\t" \
    "ds_read_b128 v[216:219], v216 offset:0\n\t" \
    "ds_read_b128 v[220:223], v242 offset:0\n\t" \
    "ds_read_b128 v[208:211], v240 offset:0\n\t" \
    "v_smfmac_f32_16x16x64_bf16 v[244:247], v[236:239], v[80:87], v241\n\t" \
    "v_xor_b32 v240, 0x1800, %[ab]\n\t" \
    "v_smfmac_f32_16x16x64_bf16 v[248:251], v[236:239], v[88:95], v241\n\t" \
    "s_waitcnt lgkmcnt(4)\n\t" \
    "v_smfmac_f32_16x16x64_bf16 v[224:227], v[236:239], v[200:207], v241\n\t" \
    "ds_read_b128 v[200:203], %[wl] offset:6144\n\t" \
    "ds_read_b128 v[204:207], %[wl] offset:7168\n\t" \
    "ds_read_b128 v[236:239], v240 offset:0\n\t" \
    "s_waitcnt lgkmcnt(6)\n\t" \
    "v_smfmac_f32_16x16x64_bf16 v[224:227], v[212:215], v[16:23], v241\n\t" \
    "v_smfmac_f32_16x16x64_bf16 v[244:247], v[212:215], v[24:31], v241\n\t" \
    "v_xor_b32 v240, 0x1c00, %[ab]\n\t" \
    "v_smfmac_f32_16x16x64_bf16 v[248:251], v[212:215], v[32:39], v241\n\t" \
    "ds_read_b128 v[212:215], v240 offset:0\n\t" \
    "s_waitcnt lgkmcnt(6)\n\t" \
    "v_smfmac_f32_16x16x64_bf16 v[224:227], v[216:219], v[228:235], v241\n\t" \
    "ds_read_b128 v[228:231], %[wl] offset:8192\n\t" \
    "ds_read_b128 v[232:235], %[wl] offset:9216\n\t" \
    "v_smfmac_f32_16x16x64_bf16 v[244:247], v[216:219], v[40:47], v241\n\t" \
    "v_and_b32 v240, 0x3ff, %[ab]\n\t" \
    "v_smfmac_f32_16x16x64_bf16 v[248:251], v[216:219], v[48:55], v241\n\t" \
    "ds_read_b128 v[216:219], v240 offset:8192\n\t" \
    "s_waitcnt lgkmcnt(8)\n\t" \
    "v_smfmac_f32_16x16x64_bf16 v[224:227], v[220:223], v[56:63], v241\n\t" \
    "v_smfmac_f32_16x16x64_bf16 v[244:247], v[220:223], v[64:71], v241\n\t" \
    "v_smfmac_f32_16x16x64_bf16 v[248:251], v[220:223], v[72:79], v241\n\t" \
    "ds_read_b128 v[220:223], v240 offset:9216\n\t" \
    "s_waitcnt lgkmcnt(8)\n\t" \
    "v_smfmac_f32_16x16x64_bf16 v[244:247], v[208:211], v[104:111], v241\n\t" \
    "s_waitcnt lgkmcnt(5)\n\t" \
    "v_smfmac_f32_16x16x64_bf16 v[244:247], v[236:239], v[120:127], v241\n\t" \
    "s_waitcnt lgkmcnt(4)\n\t" \
    "v_smfmac_f32_16x16x64_bf16 v[244:247], v[212:215], v[144:151], v241\n\t" \
    "s_waitcnt lgkmcnt(1)\n\t" \
    "v_smfmac_f32_16x16x64_bf16 v[244:247], v[216:219], v[160:167], v241\n\t" \
    "s_waitcnt lgkmcnt(0)\n\t" \
    "v_smfmac_f32_16x16x64_bf16 v[244:247], v[220:223], v[184:191], v241\n\t" \
    "v_smfmac_f32_16x16x64_bf16 v[248:251], v[208:211], v[112:119], v241\n\t" \
    "v_smfmac_f32_16x16x64_bf16 v[248:251], v[236:239], v[128:135], v241\n\t" \
    "v_smfmac_f32_16x16x64_bf16 v[248:251], v[212:215], v[152:159], v241\n\t" \
    "v_permlane32_swap_b32 v244, v246\n\t" \
    "v_permlane32_swap_b32 v245, v247\n\t" \
    "v_smfmac_f32_16x16x64_bf16 v[248:251], v[216:219], v[168:175], v241\n\t" \
    "v_add_f32 v244, v244, v246\n\t" \
    "v_add_f32 v245, v245, v247\n\t" \
    "v_smfmac_f32_16x16x64_bf16 v[248:251], v[220:223], v[192:199], v241\n\t" \
    "v_cvt_pk_bf16_f32 v244, v244, v245\n\t" \
    "ds_write_b16 %[dst], v244 offset:512\n\t" \
    "ds_write_b16_d16_hi %[dst], v244 offset:528\n\t" \
    "v_smfmac_f32_16x16x64_bf16 v[224:227], v[208:211], v[96:103], v241\n\t" \
    "v_smfmac_f32_16x16x64_bf16 v[224:227], v[236:239], v[200:207], v241\n\t" \
    "v_smfmac_f32_16x16x64_bf16 v[224:227], v[212:215], v[136:143], v241\n\t" \
    "v_permlane32_swap_b32 v248, v250\n\t" \
    "v_permlane32_swap_b32 v249, v251\n\t" \
    "v_smfmac_f32_16x16x64_bf16 v[224:227], v[216:219], v[228:235], v241\n\t" \
    "v_add_f32 v248, v248, v250\n\t" \
    "v_add_f32 v249, v249, v251\n\t" \
    "v_smfmac_f32_16x16x64_bf16 v[224:227], v[220:223], v[176:183], v241\n\t" \
    "v_cvt_pk_bf16_f32 v248, v248, v249\n\t" \
    "ds_write_b16 %[dst], v248 offset:1024\n\t" \
    "ds_write_b16_d16_hi %[dst], v248 offset:1040\n\t" \
    "s_nop 4\n\t" \
    "v_permlane32_swap_b32 v224, v226\n\t" \
    "v_permlane32_swap_b32 v225, v227\n\t" \
    "v_add_f32 v224, v224, v226\n\t" \
    "v_add_f32 v225, v225, v227\n\t" \
    "v_cvt_pk_bf16_f32 v224, v224, v225\n\t" \
    "ds_write_b16 %[dst], v224 offset:0\n\t" \
    "ds_write_b16_d16_hi %[dst], v224 offset:16\n\t"
#define ASM_CRIT1_V2 \
    "s_branch .Lc0%=\n\t" \
    ".Lcr%=:\n\t" \
    "s_cmp_eq_u32 %[n], 0\n\t" \
    "s_cbranch_scc1 .Lcb%=\n\t" \
    "s_sub_u32 %[n], %[n], 1\n\t" \
    "v_max_u32 v243, v208, v209\n\t" \
    "v_max3_u32 v243, v210, v211, v243\n\t" \
    "v_cmp_eq_u32 vcc, -1, v243\n\t" \
    "s_cbranch_vccz .Lcs0_%=\n\t" \
    "buffer_load_dwordx4 v[208:211], %[ab], %[rs], %[s0] offen sc1\n\t" \
    ".Lcs0_%=:\n\t" \
    "v_max_u32 v243, v236, v237\n\t" \
    "v_max3_u32 v243, v238, v239, v243\n\t" \
    "v_cmp_eq_u32 vcc, -1, v243\n\t" \
    "s_cbranch_vccz .Lca%=\n\t" \
    "buffer_load_dwordx4 v[236:239], %[ab], %[rs], %[s1] offen sc1\n\t" \
    "s_branch .Lca%=\n\t" \
    ".Lc0%=:\n\t" \
    "buffer_load_dwordx4 v[208:211], %[ab], %[rs], %[s0] offen sc1\n\t" \
    "buffer_load_dwordx4 v[236:239], %[ab], %[rs], %[s1] offen sc1\n\t" \
    "v_bfe_i32 v241, %[ab], 7, 1\n\t" \
    "v_and_b32 v241, 0xaaaa, v241\n\t" \
    "v_or_b32 v241, 0x4444, v241\n\t" \
    "v_xor_b32 v212, 0x400, %[ab]\n\t" \
    "v_xor_b32 v216, 0x800, %[ab]\n\t" \
    "v_xor_b32 v242, 0xc00, %[ab]\n\t" \
    "v_xor_b32 v240, 0x1400, %[ab]\n\t" \
    ".Lca%=:\n\t" \
    "s_waitcnt vmcnt(0)\n\t" \
    "ds_write_b128 %[ab], v[208:211] offset:0\n\t" \
    "ds_write_b128 %[ab], v[236:239] offset:4096\n\t" \
    "v_max_u32 v243, v208, v209\n\t" \
    "v_max3_u32 v243, v210, v211, v243\n\t" \
    "v_max3_u32 v243, v236, v237, v243\n\t" \
    "v_max3_u32 v243, v238, v239, v243\n\t" \
    "v_cmp_eq_u32 vcc, -1, v243\n\t" \
    "s_cbranch_vccnz .Lcr%=\n\t" \
    ".Lcb%=:\n\t" \
    PRIUP \
    ASM_ST2 \
    "v_smfmac_f32_16x16x64_bf16 v[224:227], v[208:211], v[200:207], v241\n\t" \
    "ds_read_b128 v[200:203], %[wl] offset:4096\n\t" \
    "ds_read_b128 v[204:207], %[wl] offset:5120\n\t" \
    "v_smfmac_f32_16x16x64_bf16 v[244:247], v[208:211], v[0:7], v241\n\t" \
    "v_smfmac_f32_16x16x64_bf16 v[248:251], v[208:211], v[8:15], v241\n\t" \
    "s_waitcnt lgkmcnt(2)\n\t" \
    "buffer_load_dword %[fv], %[fv], %[rs], %[fs] offen sc1\n\t" \
    "s_barrier\n\t" ASM_ST \
    "ds_read_b128 v[212:215], v212 offset:0\n\t" \
    "ds_read_b128 v[216:219], v216 offset:0\n\t" \
    "ds_read_b128 v[220:223], v242 offset:0\n\t" \
    "ds_read_b128 v[208:211], v240 offset:0\n\t" \
    "v_smfmac_f32_16x16x64_bf16 v[244:247], v[236:239], v[80:87], v241\n\t" \
    "v_xor_b32 v240, 0x1800, %[ab]\n\t" \
    "v_smfmac_f32_16x16x64_bf16 v[248:251], v[236:239], v[88:95], v241\n\t" \
    "s_waitcnt lgkmcnt(4)\n\t" \
    "v_smfmac_f32_16x16x64_bf16 v[224:227], v[236:239], v[200:207], v241\n\t" \
    "ds_read_b128 v[200:203], %[wl] offset:6144\n\t" \
    "ds_read_b128 v[204:207], %[wl] offset:7168\n\t" \
    "ds_read_b128 v[236:239], v240 offset:0\n\t" \
    "s_waitcnt lgkmcnt(6)\n\t" \
    "v_smfmac_f32_16x16x64_bf16 v[224:227], v[212:215], v[16:23], v241\n\t" \
    "v_smfmac_f32_16x16x64_bf16 v[244:247], v[212:215], v[24:31], v241\n\t" \
    "v_xor_b32 v240, 0x1c00, %[ab]\n\t" \
    "v_smfmac_f32_16x16x64_bf16 v[248:251], v[212:215], v[32:39], v241\n\t" \
    "ds_read_b128 v[212:215], v240 offset:0\n\t" \
    "s_waitcnt lgkmcnt(6)\n\t" \
    "v_smfmac_f32_16x16x64_bf16 v[224:227], v[216:219], v[228:235], v241\n\t" \
    "ds_read_b128 v[228:231], %[wl] offset:8192\n\t" \
    "ds_read_b128 v[232:235], %[wl] offset:9216\n\t" \
    "v_smfmac_f32_16x16x64_bf16 v[244:247], v[216:219], v[40:47], v241\n\t" \
    "v_and_b32 v240, 0x3ff, %[ab]\n\t" \
    "v_smfmac_f32_16x16x64_bf16 v[248:251], v[216:219], v[48:55], v241\n\t" \
    "ds_read_b128 v[216:219], v240 offset:8192\n\t" \
    "s_waitcnt lgkmcnt(8)\n\t" \
    "v_smfmac_f32_16x16x64_bf16 v[224:227], v[220:223], v[56:63], v241\n\t" \
    "v_smfmac_f32_16x16x64_bf16 v[244:247], v[220:223], v[64:71], v241\n\t" \
    "v_smfmac_f32_16x16x64_bf16 v[248:251], v[220:223], v[72:79], v241\n\t" \
    "ds_read_b128 v[220:223], v240 offset:9216\n\t" \
    "s_waitcnt lgkmcnt(8)\n\t" \
    "v_smfmac_f32_16x16x64_bf16 v[244:247], v[208:211], v[104:111], v241\n\t" \
    "s_waitcnt lgkmcnt(5)\n\t" \
    "v_smfmac_f32_16x16x64_bf16 v[244:247], v[236:239], v[120:127], v241\n\t" \
    "s_waitcnt lgkmcnt(4)\n\t" \
    "v_smfmac_f32_16x16x64_bf16 v[244:247], v[212:215], v[144:151], v241\n\t" \
    "s_waitcnt lgkmcnt(1)\n\t" \
    "v_smfmac_f32_16x16x64_bf16 v[244:247], v[216:219], v[160:167], v241\n\t" \
    "s_waitcnt lgkmcnt(0)\n\t" \
    "v_smfmac_f32_16x16x64_bf16 v[244:247], v[220:223], v[184:191], v241\n\t" \
    "v_smfmac_f32_16x16x64_bf16 v[248:251], v[208:211], v[112:119], v241\n\t" \
    "v_smfmac_f32_16x16x64_bf16 v[248:251], v[236:239], v[128:135], v241\n\t" \
    "v_smfmac_f32_16x16x64_bf16 v[248:251], v[212:215], v[152:159], v241\n\t" \
    "v_permlane32_swap_b32 v244, v246\n\t" \
    "v_permlane32_swap_b32 v245, v247\n\t" \
    "v_smfmac_f32_16x16x64_bf16 v[248:251], v[216:219], v[168:175], v241\n\t" \
    "v_add_f32 v244, v244, v246\n\t" \
    "v_add_f32 v245, v245, v247\n\t" \
    "v_smfmac_f32_16x16x64_bf16 v[248:251], v[220:223], v[192:199], v241\n\t" \
    "v_cvt_pk_bf16_f32 v244, v244, v245\n\t" \
    "ds_write_b16 %[dst], v244 offset:512\n\t" \
    "ds_write_b16_d16_hi %[dst], v244 offset:528\n\t" \
    "v_smfmac_f32_16x16x64_bf16 v[224:227], v[208:211], v[96:103], v241\n\t" \
    "v_smfmac_f32_16x16x64_bf16 v[224:227], v[236:239], v[200:207], v241\n\t" \
    "v_smfmac_f32_16x16x64_bf16 v[224:227], v[212:215], v[136:143], v241\n\t" \
    "v_permlane32_swap_b32 v248, v250\n\t" \
    "v_permlane32_swap_b32 v249, v251\n\t" \
    "v_smfmac_f32_16x16x64_bf16 v[224:227], v[216:219], v[228:235], v241\n\t" \
    "v_add_f32 v248, v248, v250\n\t" \
    "v_add_f32 v249, v249, v251\n\t" \
    "v_smfmac_f32_16x16x64_bf16 v[224:227], v[220:223], v[176:183], v241\n\t" \
    "v_cvt_pk_bf16_f32 v248, v248, v249\n\t" \
    "ds_write_b16 %[dst], v248 offset:1024\n\t" \
    "ds_write_b16_d16_hi %[dst], v248 offset:1040\n\t" \
    "s_nop 4\n\t" \
    "v_permlane32_swap_b32 v224, v226\n\t" \
    "v_permlane32_swap_b32 v225, v227\n\t" \
    "v_add_f32 v224, v224, v226\n\t" \
    "v_add_f32 v225, v225, v227\n\t" \
    "v_cvt_pk_bf16_f32 v224, v224, v225\n\t" \
    "ds_write_b16 %[dst], v224 offset:0\n\t" \
    "ds_write_b16_d16_hi %[dst], v224 offset:16\n\t"
#define ASM_VOX \
    "s_waitcnt lgkmcnt(0)\n\t" \
    "ds_read_b128 v[208:211], %[va] offset:2048\n\t" \
    "ds_read_b128 v[212:215], %[va] offset:3072\n\t" \
    "ds_read_b128 v[216:219], %[va] offset:4096\n\t" \
    "ds_read_b128 v[220:223], %[va] offset:5120\n\t" \
    "ds_read_b128 v[200:203], %[va] offset:0\n\t" \
    "ds_read_b128 v[204:207], %[va] offset:1024\n\t" \
    "ds_read_b128 v[236:239], %[wl] offset:0\n\t" \
    "ds_read_b128 v[240:243], %[wl] offset:1024\n\t" \
    "s_waitcnt lgkmcnt(7)\n\t" \
    "v_mfma_f32_16x16x32_bf16 v[244:247], v[208:211], v[0:3], 0\n\t" \
    "s_waitcnt lgkmcnt(6)\n\t" \
    "v_mfma_f32_16x16x32_bf16 v[244:247], v[212:215], v[4:7], v[244:247]\n\t" \
    "s_waitcnt lgkmcnt(5)\n\t" \
    "v_mfma_f32_16x16x32_bf16 v[244:247], v[216:219], v[8:11], v[244:247]\n\t" \
    "s_waitcnt lgkmcnt(4)\n\t" \
    "v_mfma_f32_16x16x32_bf16 v[244:247], v[220:223], v[12:15], v[244:247]\n\t" \
    "s_waitcnt lgkmcnt(1)\n\t" \
    "v_mfma_f32_16x16x32_bf16 v[244:247], v[200:203], v[236:239], v[244:247]\n\t" \
    "ds_read_b128 v[236:239], %[wl] offset:2048\n\t" \
    "s_waitcnt lgkmcnt(1)\n\t" \
    "v_mfma_f32_16x16x32_bf16 v[244:247], v[204:207], v[240:243], v[244:247]\n\t" \
    "ds_read_b128 v[240:243], %[wl] offset:3072\n\t" \
    "v_mfma_f32_16x16x32_bf16 v[248:251], v[208:211], v[40:43], 0\n\t" \
    "v_mfma_f32_16x16x32_bf16 v[248:251], v[212:215], v[44:47], v[248:251]\n\t" \
    "v_mfma_f32_16x16x32_bf16 v[248:251], v[216:219], v[48:51], v[248:251]\n\t" \
    "v_cvt_pk_bf16_f32 v224, v244, v245\n\t" \
    "v_mfma_f32_16x16x32_bf16 v[248:251], v[220:223], v[52:55], v[248:251]\n\t" \
    "v_cvt_pk_bf16_f32 v225, v246, v247\n\t" \
    "s_waitcnt lgkmcnt(1)\n\t" \
    "v_mfma_f32_16x16x32_bf16 v[248:251], v[200:203], v[236:239], v[248:251]\n\t" \
    "ds_read_b128 v[236:239], %[wl] offset:4096\n\t" \
    "s_waitcnt lgkmcnt(1)\n\t" \
    "v_mfma_f32_16x16x32_bf16 v[248:251], v[204:207], v[240:243], v[248:251]\n\t" \
    "ds_read_b128 v[240:243], %[wl] offset:5120\n\t" \
    "v_mfma_f32_16x16x32_bf16 v[244:247], v[208:211], v[80:83], 0\n\t" \
    "v_mfma_f32_16x16x32_bf16 v[244:247], v[212:215], v[84:87], v[244:247]\n\t" \
    "v_mfma_f32_16x16x32_bf16 v[244:247], v[216:219], v[88:91], v[244:247]\n\t" \
    "v_cvt_pk_bf16_f32 v226, v248, v249\n\t" \
    "v_mfma_f32_16x16x32_bf16 v[244:247], v[220:223], v[92:95], v[244:247]\n\t" \
    "v_cvt_pk_bf16_f32 v227, v250, v251\n\t" \
    "s_waitcnt lgkmcnt(1)\n\t" \
    "v_mfma_f32_16x16x32_bf16 v[244:247], v[200:203], v[236:239], v[244:247]\n\t" \
    "ds_read_b128 v[236:239], %[wl] offset:6144\n\t" \
    "s_waitcnt lgkmcnt(1)\n\t" \
    "v_mfma_f32_16x16x32_bf16 v[244:247], v[204:207], v[240:243], v[244:247]\n\t" \
    "ds_read_b128 v[240:243], %[wl] offset:7168\n\t" \
    "v_mfma_f32_16x16x32_bf16 v[248:251], v[208:211], v[120:123], 0\n\t" \
    "v_mfma_f32_16x16x32_bf16 v[248:251], v[212:215], v[124:127], v[248:251]\n\t" \
    "v_mfma_f32_16x16x32_bf16 v[248:251], v[216:219], v[128:131], v[248:251]\n\t" \
    "v_cvt_pk_bf16_f32 v228, v244, v245\n\t" \
    "v_mfma_f32_16x16x32_bf16 v[248:251], v[220:223], v[132:135], v[248:251]\n\t" \
    "v_cvt_pk_bf16_f32 v229, v246, v247\n\t" \
    "s_waitcnt lgkmcnt(1)\n\t" \
    "v_mfma_f32_16x16x32_bf16 v[248:251], v[200:203], v[236:239], v[248:251]\n\t" \
    "ds_read_b128 v[236:239], %[wl] offset:8192\n\t" \
    "s_waitcnt lgkmcnt(1)\n\t" \
    "v_mfma_f32_16x16x32_bf16 v[248:251], v[204:207], v[240:243], v[248:251]\n\t" \
    "ds_read_b128 v[240:243], %[wl] offset:9216\n\t" \
    "v_mfma_f32_16x16x32_bf16 v[244:247], v[208:211], v[160:163], 0\n\t" \
    "v_mfma_f32_16x16x32_bf16 v[244:247], v[212:215], v[164:167], v[244:247]\n\t" \
    "v_mfma_f32_16x16x32_bf16 v[244:247], v[216:219], v[168:171], v[244:247]\n\t" \
    "v_cvt_pk_bf16_f32 v230, v248, v249\n\t" \
    "v_mfma_f32_16x16x32_bf16 v[244:247], v[220:223], v[172:175], v[244:247]\n\t" \
    "v_cvt_pk_bf16_f32 v231, v250, v251\n\t" \
    "s_waitcnt lgkmcnt(1)\n\t" \
    "v_mfma_f32_16x16x32_bf16 v[244:247], v[200:203], v[236:239], v[244:247]\n\t" \
    "s_waitcnt lgkmcnt(0)\n\t" \
    "v_mfma_f32_16x16x32_bf16 v[244:247], v[204:207], v[240:243], v[244:247]\n\t" \
    "v_mfma_f32_16x16x32_bf16 v[248:251], v[208:211], v[24:27], 0\n\t" \
    "v_mfma_f32_16x16x32_bf16 v[248:251], v[212:215], v[28:31], v[248:251]\n\t" \
    "v_mfma_f32_16x16x32_bf16 v[248:251], v[216:219], v[32:35], v[248:251]\n\t" \
    "v_cvt_pk_bf16_f32 v232, v244, v245\n\t" \
    "v_mfma_f32_16x16x32_bf16 v[248:251], v[220:223], v[36:39], v[248:251]\n\t" \
    "v_cvt_pk_bf16_f32 v233, v246, v247\n\t" \
    "v_mfma_f32_16x16x32_bf16 v[248:251], v[200:203], v[16:19], v[248:251]\n\t" \
    "v_mfma_f32_16x16x32_bf16 v[248:251], v[204:207], v[20:23], v[248:251]\n\t" \
    "v_mfma_f32_16x16x32_bf16 v[244:247], v[208:211], v[64:67], 0\n\t" \
    "v_mfma_f32_16x16x32_bf16 v[244:247], v[212:215], v[68:71], v[244:247]\n\t" \
    "v_mfma_f32_16x16x32_bf16 v[244:247], v[216:219], v[72:75], v[244:247]\n\t" \
    "v_cvt_pk_bf16_f32 v236, v248, v249\n\t" \
    "v_mfma_f32_16x16x32_bf16 v[244:247], v[220:223], v[76:79], v[244:247]\n\t" \
    "v_cvt_pk_bf16_f32 v237, v250, v251\n\t" \
    "v_mfma_f32_16x16x32_bf16 v[244:247], v[200:203], v[56:59], v[244:247]\n\t" \
    "v_mfma_f32_16x16x32_bf16 v[244:247], v[204:207], v[60:63], v[244:247]\n\t" \
    "v_mfma_f32_16x16x32_bf16 v[248:251], v[208:211], v[104:107], 0\n\t" \
    "v_mfma_f32_16x16x32_bf16 v[248:251], v[212:215], v[108:111], v[248:251]\n\t" \
    "v_mfma_f32_16x16x32_bf16 v[248:251], v[216:219], v[112:115], v[248:251]\n\t" \
    "v_cvt_pk_bf16_f32 v238, v244, v245\n\t" \
    "v_mfma_f32_16x16x32_bf16 v[248:251], v[220:223], v[116:119], v[248:251]\n\t" \
    "v_cvt_pk_bf16_f32 v239, v246, v247\n\t" \
    "v_mfma_f32_16x16x32_bf16 v[248:251], v[200:203], v[96:99], v[248:251]\n\t" \
    "v_mfma_f32_16x16x32_bf16 v[248:251], v[204:207], v[100:103], v[248:251]\n\t" \
    "v_mfma_f32_16x16x32_bf16 v[244:247], v[208:211], v[144:147], 0\n\t" \
    "v_mfma_f32_16x16x32_bf16 v[244:247], v[212:215], v[148:151], v[244:247]\n\t" \
    "v_mfma_f32_16x16x32_bf16 v[244:247], v[216:219], v[152:155], v[244:247]\n\t" \
    "v_cvt_pk_bf16_f32 v240, v248, v249\n\t" \
    "v_mfma_f32_16x16x32_bf16 v[244:247], v[220:223], v[156:159], v[244:247]\n\t" \
    "v_cvt_pk_bf16_f32 v241, v250, v251\n\t" \
    "v_mfma_f32_16x16x32_bf16 v[244:247], v[200:203], v[136:139], v[244:247]\n\t" \
    "v_mfma_f32_16x16x32_bf16 v[244:247], v[204:207], v[140:143], v[244:247]\n\t" \
    "v_mfma_f32_16x16x32_bf16 v[248:251], v[208:211], v[184:187], 0\n\t" \
    "ds_read_b128 v[208:211], %[va] offset:8192\n\t" \
    "v_mfma_f32_16x16x32_bf16 v[248:251], v[212:215], v[188:191], v[248:251]\n\t" \
    "ds_read_b128 v[212:215], %[va] offset:9216\n\t" \
    "v_mfma_f32_16x16x32_bf16 v[248:251], v[216:219], v[192:195], v[248:251]\n\t" \
    "ds_read_b128 v[216:219], %[va] offset:10240\n\t" \
    "v_cvt_pk_bf16_f32 v242, v244, v245\n\t" \
    "v_mfma_f32_16x16x32_bf16 v[248:251], v[220:223], v[196:199], v[248:251]\n\t" \
    "ds_read_b128 v[220:223], %[va] offset:11264\n\t" \
    "v_cvt_pk_bf16_f32 v243, v246, v247\n\t" \
    "v_mfma_f32_16x16x32_bf16 v[248:251], v[200:203], v[176:179], v[248:251]\n\t" \
    "ds_read_b128 v[200:203], %[va] offset:6144\n\t" \
    "v_mfma_f32_16x16x32_bf16 v[248:251], v[204:207], v[180:183], v[248:251]\n\t" \
    "ds_read_b128 v[204:207], %[va] offset:7168\n\t" \
    "s_waitcnt lgkmcnt(5)\n\t" \
    "v_mfma_f32_16x16x32_bf16 v[244:247], v[208:211], v[24:27], 0\n\t" \
    "s_waitcnt lgkmcnt(4)\n\t" \
    "v_mfma_f32_16x16x32_bf16 v[244:247], v[212:215], v[28:31], v[244:247]\n\t" \
    "s_waitcnt lgkmcnt(3)\n\t" \
    "v_mfma_f32_16x16x32_bf16 v[244:247], v[216:219], v[32:35], v[244:247]\n\t" \
    "v_cvt_pk_bf16_f32 v234, v248, v249\n\t" \
    "s_waitcnt lgkmcnt(2)\n\t" \
    "v_mfma_f32_16x16x32_bf16 v[244:247], v[220:223], v[36:39], v[244:247]\n\t" \
    "v_cvt_pk_bf16_f32 v235, v250, v251\n\t" \
    "s_waitcnt lgkmcnt(1)\n\t" \
    "v_mfma_f32_16x16x32_bf16 v[244:247], v[200:203], v[16:19], v[244:247]\n\t" \
    "s_waitcnt lgkmcnt(0)\n\t" \
    "v_mfma_f32_16x16x32_bf16 v[244:247], v[204:207], v[20:23], v[244:247]\n\t" \
    "v_mfma_f32_16x16x32_bf16 v[248:251], v[208:211], v[64:67], 0\n\t" \
    "s_add_i32 %[so], %[so], 65536\n\t" \
    "v_mfma_f32_16x16x32_bf16 v[248:251], v[212:215], v[68:71], v[248:251]\n\t" \
    "s_nop 1\n\t" \
    "v_cvt_pk_bf16_f32 v247, v246, v247\n\t" \
    "v_mfma_f32_16x16x32_bf16 v[248:251], v[216:219], v[72:75], v[248:251]\n\t" \
    "v_cvt_pk_bf16_f32 v246, v244, v245\n\t" \
    "v_mfma_f32_16x16x32_bf16 v[248:251], v[220:223], v[76:79], v[248:251]\n\t" \
    "v_mov_b64 v[244:245], v[236:237]\n\t" \
    "buffer_store_dwordx4 v[244:247], %[l16], %[rd], %[so] offen sc1\n\t" \
    "v_mfma_f32_16x16x32_bf16 v[248:251], v[200:203], v[56:59], v[248:251]\n\t" \
    "v_mfma_f32_16x16x32_bf16 v[248:251], v[204:207], v[60:63], v[248:251]\n\t" \
    "v_mfma_f32_16x16x32_bf16 v[244:247], v[208:211], v[104:107], 0\n\t" \
    "s_add_i32 %[so], %[so], 131072\n\t" \
    "v_mfma_f32_16x16x32_bf16 v[244:247], v[212:215], v[108:111], v[244:247]\n\t" \
    "s_nop 1\n\t" \
    "v_cvt_pk_bf16_f32 v251, v250, v251\n\t" \
    "v_mfma_f32_16x16x32_bf16 v[244:247], v[216:219], v[112:115], v[244:247]\n\t" \
    "v_cvt_pk_bf16_f32 v250, v248, v249\n\t" \
    "v_mfma_f32_16x16x32_bf16 v[244:247], v[220:223], v[116:119], v[244:247]\n\t" \
    "v_mov_b64 v[248:249], v[238:239]\n\t" \
    "buffer_store_dwordx4 v[248:251], %[l16], %[rd], %[so] offen sc1\n\t" \
    "v_mfma_f32_16x16x32_bf16 v[244:247], v[200:203], v[96:99], v[244:247]\n\t" \
    "v_mfma_f32_16x16x32_bf16 v[244:247], v[204:207], v[100:103], v[244:247]\n\t" \
    "v_mfma_f32_16x16x32_bf16 v[248:251], v[208:211], v[144:147], 0\n\t" \
    "s_add_i32 %[so], %[so], 131072\n\t" \
    "v_mfma_f32_16x16x32_bf16 v[248:251], v[212:215], v[148:151], v[248:251]\n\t" \
    "s_nop 1\n\t" \
    "v_cvt_pk_bf16_f32 v247, v246, v247\n\t" \
    "v_mfma_f32_16x16x32_bf16 v[248:251], v[216:219], v[152:155], v[248:251]\n\t" \
    "v_cvt_pk_bf16_f32 v246, v244, v245\n\t" \
    "v_mfma_f32_16x16x32_bf16 v[248:251], v[220:223], v[156:159], v[248:251]\n\t" \
    "v_mov_b64 v[244:245], v[240:241]\n\t" \
    "buffer_store_dwordx4 v[244:247], %[l16], %[rd], %[so] offen sc1\n\t" \
    "v_mfma_f32_16x16x32_bf16 v[248:251], v[200:203], v[136:139], v[248:251]\n\t" \
    "v_mfma_f32_16x16x32_bf16 v[248:251], v[204:207], v[140:143], v[248:251]\n\t" \
    "v_mfma_f32_16x16x32_bf16 v[244:247], v[208:211], v[184:187], 0\n\t" \
    "s_add_i32 %[so], %[so], 131072\n\t" \
    "v_mfma_f32_16x16x32_bf16 v[244:247], v[212:215], v[188:191], v[244:247]\n\t" \
    "s_nop 1\n\t" \
    "v_cvt_pk_bf16_f32 v251, v250, v251\n\t" \
    "v_mfma_f32_16x16x32_bf16 v[244:247], v[216:219], v[192:195], v[244:247]\n\t" \
    "v_cvt_pk_bf16_f32 v250, v248, v249\n\t" \
    "v_mfma_f32_16x16x32_bf16 v[244:247], v[220:223], v[196:199], v[244:247]\n\t" \
    "v_mov_b64 v[248:249], v[242:243]\n\t" \
    "buffer_store_dwordx4 v[248:251], %[l16], %[rd], %[so] offen sc1\n\t" \
    "v_mfma_f32_16x16x32_bf16 v[244:247], v[200:203], v[176:179], v[244:247]\n\t" \
    "v_mfma_f32_16x16x32_bf16 v[244:247], v[204:207], v[180:183], v[244:247]\n\t" \
    "ds_read_b128 v[236:239], %[wl] offset:0\n\t" \
    "ds_read_b128 v[240:243], %[wl] offset:1024\n\t" \
    "v_mfma_f32_16x16x32_bf16 v[248:251], v[208:211], v[0:3], 0\n\t" \
    "s_add_i32 %[so], %[so], 131072\n\t" \
    "v_mfma_f32_16x16x32_bf16 v[248:251], v[212:215], v[4:7], v[248:251]\n\t" \
    "v_cvt_pk_bf16_f32 v247, v246, v247\n\t" \
    "v_mfma_f32_16x16x32_bf16 v[248:251], v[216:219], v[8:11], v[248:251]\n\t" \
    "v_cvt_pk_bf16_f32 v246, v244, v245\n\t" \
    "v_mfma_f32_16x16x32_bf16 v[248:251], v[220:223], v[12:15], v[248:251]\n\t" \
    "v_mov_b64 v[244:245], v[234:235]\n\t" \
    "buffer_store_dwordx4 v[244:247], %[l16], %[rd], %[so] offen sc1\n\t" \
    "s_waitcnt lgkmcnt(1)\n\t" \
    "v_mfma_f32_16x16x32_bf16 v[248:251], v[200:203], v[236:239], v[248:251]\n\t" \
    "ds_read_b128 v[236:239], %[wl] offset:2048\n\t" \
    "s_waitcnt lgkmcnt(1)\n\t" \
    "v_mfma_f32_16x16x32_bf16 v[248:251], v[204:207], v[240:243], v[248:251]\n\t" \
    "ds_read_b128 v[240:243], %[wl] offset:3072\n\t" \
    "v_mfma_f32_16x16x32_bf16 v[244:247], v[208:211], v[40:43], 0\n\t" \
    "s_add_i32 %[so], %[so], -589824\n\t" \
    "v_mfma_f32_16x16x32_bf16 v[244:247], v[212:215], v[44:47], v[244:247]\n\t" \
    "s_nop 0\n\t" \
    "v_cvt_pk_bf16_f32 v251, v250, v251\n\t" \
    "v_mfma_f32_16x16x32_bf16 v[244:247], v[216:219], v[48:51], v[244:247]\n\t" \
    "v_cvt_pk_bf16_f32 v250, v248, v249\n\t" \
    "v_mfma_f32_16x16x32_bf16 v[244:247], v[220:223], v[52:55], v[244:247]\n\t" \
    "v_mov_b64 v[248:249], v[224:225]\n\t" \
    "buffer_store_dwordx4 v[248:251], %[l16], %[rd], %[so] offen sc1\n\t" \
    "s_waitcnt lgkmcnt(1)\n\t" \
    "v_mfma_f32_16x16x32_bf16 v[244:247], v[200:203], v[236:239], v[244:247]\n\t" \
    "ds_read_b128 v[236:239], %[wl] offset:4096\n\t" \
    "s_waitcnt lgkmcnt(1)\n\t" \
    "v_mfma_f32_16x16x32_bf16 v[244:247], v[204:207], v[240:243], v[244:247]\n\t" \
    "ds_read_b128 v[240:243], %[wl] offset:5120\n\t" \
    "v_mfma_f32_16x16x32_bf16 v[248:251], v[208:211], v[80:83], 0\n\t" \
    "s_add_i32 %[so], %[so], 131072\n\t" \
    "v_mfma_f32_16x16x32_bf16 v[248:251], v[212:215], v[84:87], v[248:251]\n\t" \
    "s_nop 0\n\t" \
    "v_cvt_pk_bf16_f32 v247, v246, v247\n\t" \
    "v_mfma_f32_16x16x32_bf16 v[248:251], v[216:219], v[88:91], v[248:251]\n\t" \
    "v_cvt_pk_bf16_f32 v246, v244, v245\n\t" \
    "v_mfma_f32_16x16x32_bf16 v[248:251], v[220:223], v[92:95], v[248:251]\n\t" \
    "v_mov_b64 v[244:245], v[226:227]\n\t" \
    "buffer_store_dwordx4 v[244:247], %[l16], %[rd], %[so] offen sc1\n\t" \
    "s_waitcnt lgkmcnt(1)\n\t" \
    "v_mfma_f32_16x16x32_bf16 v[248:251], v[200:203], v[236:239], v[248:251]\n\t" \
    "ds_read_b128 v[236:239], %[wl] offset:6144\n\t" \
    "s_waitcnt lgkmcnt(1)\n\t" \
    "v_mfma_f32_16x16x32_bf16 v[248:251], v[204:207], v[240:243], v[248:251]\n\t" \
    "ds_read_b128 v[240:243], %[wl] offset:7168\n\t" \
    "v_mfma_f32_16x16x32_bf16 v[244:247], v[208:211], v[120:123], 0\n\t" \
    "s_add_i32 %[so], %[so], 131072\n\t" \
    "v_mfma_f32_16x16x32_bf16 v[244:247], v[212:215], v[124:127], v[244:247]\n\t" \
    "s_nop 0\n\t" \
    "v_cvt_pk_bf16_f32 v251, v250, v251\n\t" \
    "v_mfma_f32_16x16x32_bf16 v[244:247], v[216:219], v[128:131], v[244:247]\n\t" \
    "v_cvt_pk_bf16_f32 v250, v248, v249\n\t" \
    "v_mfma_f32_16x16x32_bf16 v[244:247], v[220:223], v[132:135], v[244:247]\n\t" \
    "v_mov_b64 v[248:249], v[228:229]\n\t" \
    "buffer_store_dwordx4 v[248:251], %[l16], %[rd], %[so] offen sc1\n\t" \
    "s_waitcnt lgkmcnt(1)\n\t" \
    "v_mfma_f32_16x16x32_bf16 v[244:247], v[200:203], v[236:239], v[244:247]\n\t" \
    "ds_read_b128 v[236:239], %[wl] offset:8192\n\t" \
    "s_waitcnt lgkmcnt(1)\n\t" \
    "v_mfma_f32_16x16x32_bf16 v[244:247], v[204:207], v[240:243], v[244:247]\n\t" \
    "ds_read_b128 v[240:243], %[wl] offset:9216\n\t" \
    "v_mfma_f32_16x16x32_bf16 v[248:251], v[208:211], v[160:163], 0\n\t" \
    "s_add_i32 %[so], %[so], 131072\n\t" \
    "v_mfma_f32_16x16x32_bf16 v[248:251], v[212:215], v[164:167], v[248:251]\n\t" \
    "s_nop 0\n\t" \
    "v_cvt_pk_bf16_f32 v247, v246, v247\n\t" \
    "v_mfma_f32_16x16x32_bf16 v[248:251], v[216:219], v[168:171], v[248:251]\n\t" \
    "v_cvt_pk_bf16_f32 v246, v244, v245\n\t" \
    "v_mfma_f32_16x16x32_bf16 v[248:251], v[220:223], v[172:175], v[248:251]\n\t" \
    "v_mov_b64 v[244:245], v[230:231]\n\t" \
    "buffer_store_dwordx4 v[244:247], %[l16], %[rd], %[so] offen sc1\n\t" \
    "s_waitcnt lgkmcnt(1)\n\t" \
    "v_mfma_f32_16x16x32_bf16 v[248:251], v[200:203], v[236:239], v[248:251]\n\t" \
    "s_waitcnt lgkmcnt(0)\n\t" \
    "v_mfma_f32_16x16x32_bf16 v[248:251], v[204:207], v[240:243], v[248:251]\n\t" \
    "s_add_i32 %[so], %[so], 131072\n\t" \
    "s_nop 7\n\t" \
    "v_cvt_pk_bf16_f32 v251, v250, v251\n\t" \
    "v_cvt_pk_bf16_f32 v250, v248, v249\n\t" \
    "v_mov_b64 v[248:249], v[232:233]\n\t" \
    "buffer_store_dwordx4 v[248:251], %[l16], %[rd], %[so] offen sc1\n\t" \
    "s_nop 1\n\t"
#define ASM_CRIT2 \
    "s_waitcnt lgkmcnt(0)\n\t" \
    "s_barrier\n\t" ASM_ST \
    "ds_read_b128 v[204:207], v200 offset:11264\n\t" \
    "ds_read_b128 v[208:211], v200 offset:12288\n\t" \
    "ds_read_b128 v[200:203], v200 offset:10240\n\t" \
    "s_waitcnt lgkmcnt(2)\n\t" \
    "v_smfmac_f32_16x16x64_bf16 v[224:227], v[204:207], v[24:31], %[sidx]\n\t" \
    "s_waitcnt lgkmcnt(1)\n\t" \
    "v_smfmac_f32_16x16x64_bf16 v[224:227], v[208:211], v[32:39], %[sidx]\n\t" \
    "s_waitcnt lgkmcnt(0)\n\t" \
    "v_smfmac_f32_16x16x64_bf16 v[224:227], v[200:203], v[16:23], %[sidx]\n\t" \
    "v_smfmac_f32_16x16x64_bf16 v[220:223], v[204:207], v[0:7], %[sidx]\n\t" \
    "v_smfmac_f32_16x16x64_bf16 v[220:223], v[208:211], v[8:15], %[sidx]\n\t" \
    "v_smfmac_f32_16x16x64_bf16 v[220:223], v[200:203], v[212:219], %[sidx]\n\t" \
    "ds_read_b128 v[212:215], %[wl] offset:2048\n\t" \
    "ds_read_b128 v[216:219], %[wl] offset:3072\n\t" \
    "v_smfmac_f32_16x16x64_bf16 v[232:235], v[204:207], v[64:71], %[sidx]\n\t" \
    "v_smfmac_f32_16x16x64_bf16 v[232:235], v[208:211], v[72:79], %[sidx]\n\t" \
    "v_smfmac_f32_16x16x64_bf16 v[232:235], v[200:203], v[56:63], %[sidx]\n\t" \
    "v_permlane32_swap_b32 v220, v224\n\t" \
    "v_permlane32_swap_b32 v221, v225\n\t" \
    "v_permlane32_swap_b32 v222, v226\n\t" \
    "v_smfmac_f32_16x16x64_bf16 v[228:231], v[204:207], v[40:47], %[sidx]\n\t" \
    "v_permlane32_swap_b32 v223, v227\n\t" \
    "v_add_f32 v220, v220, v224\n\t" \
    "v_add_f32 v221, v221, v225\n\t" \
    "v_smfmac_f32_16x16x64_bf16 v[228:231], v[208:211], v[48:55], %[sidx]\n\t" \
    "v_add_f32 v222, v222, v226\n\t" \
    "v_add_f32 v223, v223, v227\n\t" \
    "v_cvt_pk_bf16_f32 v226, v220, v221\n\t" \
    "s_waitcnt lgkmcnt(0)\n\t" \
    "v_smfmac_f32_16x16x64_bf16 v[228:231], v[200:203], v[212:219], %[sidx]\n\t" \
    "ds_read_b128 v[212:215], %[wl] offset:4096\n\t" \
    "ds_read_b128 v[216:219], %[wl] offset:5120\n\t" \
    "v_cvt_pk_bf16_f32 v227, v222, v223\n\t" \
    "v_min_u32 v226, -2, v226\n\t" \
    "v_min_u32 v227, -2, v227\n\t" \
    "v_smfmac_f32_16x16x64_bf16 v[240:243], v[204:207], v[104:111], %[sidx]\n\t" \
    "v_smfmac_f32_16x16x64_bf16 v[240:243], v[208:211], v[112:119], %[sidx]\n\t" \
    "v_smfmac_f32_16x16x64_bf16 v[240:243], v[200:203], v[96:103], %[sidx]\n\t" \
    "v_permlane32_swap_b32 v228, v232\n\t" \
    "v_permlane32_swap_b32 v229, v233\n\t" \
    "v_permlane32_swap_b32 v230, v234\n\t" \
    "v_smfmac_f32_16x16x64_bf16 v[236:239], v[204:207], v[80:87], %[sidx]\n\t" \
    "v_permlane32_swap_b32 v231, v235\n\t" \
    "v_add_f32 v228, v228, v232\n\t" \
    "v_add_f32 v229, v229, v233\n\t" \
    "v_smfmac_f32_16x16x64_bf16 v[236:239], v[208:211], v[88:95], %[sidx]\n\t" \
    "v_add_f32 v230, v230, v234\n\t" \
    "v_add_f32 v231, v231, v235\n\t" \
    "v_cvt_pk_bf16_f32 v228, v228, v229\n\t" \
    "s_waitcnt lgkmcnt(0)\n\t" \
    "v_smfmac_f32_16x16x64_bf16 v[236:239], v[200:203], v[212:219], %[sidx]\n\t" \
    "ds_read_b128 v[212:215], %[wl] offset:6144\n\t" \
    "ds_read_b128 v[216:219], %[wl] offset:7168\n\t" \
    "v_cvt_pk_bf16_f32 v229, v230, v231\n\t" \
    "v_min_u32 v228, -2, v228\n\t" \
    "v_min_u32 v229, -2, v229\n\t" \
    "s_nop 1\n\t" \
    "v_permlane16_swap_b32 v226, v228\n\t" \
    "v_permlane16_swap_b32 v227, v229\n\t" \
    "buffer_store_dwordx4 v[226:229], %[vo], %[rr], %[so] offen sc1\n\t" \
    "s_add_u32 %[so], %[so], 0x10000\n\t" \
    "v_smfmac_f32_16x16x64_bf16 v[248:251], v[204:207], v[144:151], %[sidx]\n\t" \
    "v_mov_b64 v[220:221], 0\n\t" \
    "v_mov_b64 v[222:223], 0\n\t" \
    "v_smfmac_f32_16x16x64_bf16 v[248:251], v[208:211], v[152:159], %[sidx]\n\t" \
    "v_mov_b64 v[224:225], 0\n\t" \
    "v_mov_b64 v[226:227], 0\n\t" \
    "v_smfmac_f32_16x16x64_bf16 v[248:251], v[200:203], v[136:143], %[sidx]\n\t" \
    "v_permlane32_swap_b32 v236, v240\n\t" \
    "v_permlane32_swap_b32 v237, v241\n\t" \
    "v_permlane32_swap_b32 v238, v242\n\t" \
    "v_smfmac_f32_16x16x64_bf16 v[244:247], v[204:207], v[120:127], %[sidx]\n\t" \
    "v_permlane32_swap_b32 v239, v243\n\t" \
    "v_add_f32 v236, v236, v240\n\t" \
    "v_add_f32 v237, v237, v241\n\t" \
    "v_smfmac_f32_16x16x64_bf16 v[244:247], v[208:211], v[128:135], %[sidx]\n\t" \
    "v_add_f32 v238, v238, v242\n\t" \
    "v_add_f32 v239, v239, v243\n\t" \
    "v_cvt_pk_bf16_f32 v242, v236, v237\n\t" \
    "s_waitcnt lgkmcnt(0)\n\t" \
    "v_smfmac_f32_16x16x64_bf16 v[244:247], v[200:203], v[212:219], %[sidx]\n\t" \
    "ds_read_b128 v[212:215], %[wl] offset:8192\n\t" \
    "ds_read_b128 v[216:219], %[wl] offset:9216\n\t" \
    "v_cvt_pk_bf16_f32 v243, v238, v239\n\t" \
    "v_min_u32 v242, -2, v242\n\t" \
    "v_min_u32 v243, -2, v243\n\t" \
    "v_smfmac_f32_16x16x64_bf16 v[224:227], v[204:207], v[184:191], %[sidx]\n\t" \
    "v_and_b32 v232, 0x3fff, %[vo]\n\t" \
    "v_bfe_u32 v233, %[vo], 14, 1\n\t" \
    "v_smfmac_f32_16x16x64_bf16 v[224:227], v[208:211], v[192:199], %[sidx]\n\t" \
    "v_lshl_add_u32 v232, v233, 3, v232\n\t" \
    "v_permlane32_swap_b32 v244, v248\n\t" \
    "v_smfmac_f32_16x16x64_bf16 v[224:227], v[200:203], v[176:183], %[sidx]\n\t" \
    "v_permlane32_swap_b32 v245, v249\n\t" \
    "v_permlane32_swap_b32 v246, v250\n\t" \
    "v_permlane32_swap_b32 v247, v251\n\t" \
    "v_smfmac_f32_16x16x64_bf16 v[220:223], v[204:207], v[160:167], %[sidx]\n\t" \
    "v_add_f32 v244, v244, v248\n\t" \
    "v_add_f32 v245, v245, v249\n\t" \
    "v_add_f32 v246, v246, v250\n\t" \
    "v_smfmac_f32_16x16x64_bf16 v[220:223], v[208:211], v[168:175], %[sidx]\n\t" \
    "v_add_f32 v247, v247, v251\n\t" \
    "v_cvt_pk_bf16_f32 v244, v244, v245\n\t" \
    "v_cvt_pk_bf16_f32 v245, v246, v247\n\t" \
    "s_waitcnt lgkmcnt(0)\n\t" \
    "v_smfmac_f32_16x16x64_bf16 v[220:223], v[200:203], v[212:219], %[sidx]\n\t" \
    "v_min_u32 v244, -2, v244\n\t" \
    "v_min_u32 v245, -2, v245\n\t" \
    "s_nop 1\n\t" \
    "v_permlane16_swap_b32 v242, v244\n\t" \
    "v_permlane16_swap_b32 v243, v245\n\t" \
    "buffer_store_dwordx4 v[242:245], %[vo], %[rr], %[so] offen sc1\n\t" \
    "s_add_u32 %[so], %[so], 0x10000\n\t" \
    "s_nop 0\n\t" \
    "v_permlane32_swap_b32 v220, v224\n\t" \
    "v_permlane32_swap_b32 v221, v225\n\t" \
    "v_permlane32_swap_b32 v222, v226\n\t" \
    "v_permlane32_swap_b32 v223, v227\n\t" \
    "v_add_f32 v220, v220, v224\n\t" \
    "v_add_f32 v221, v221, v225\n\t" \
    "v_add_f32 v222, v222, v226\n\t" \
    "v_add_f32 v223, v223, v227\n\t" \
    "v_cvt_pk_bf16_f32 v220, v220, v221\n\t" \
    "v_cvt_pk_bf16_f32 v221, v222, v223\n\t" \
    "v_min_u32 v220, -2, v220\n\t" \
    "v_min_u32 v221, -2, v221\n\t" \
    "buffer_store_dwordx2 v[220:221], v232, %[rr], %[so] offen sc1\n\t" \
    "s_nop 1\n\t"
#define ASM_SM \
    "v_mbcnt_lo_u32_b32 v252, -1, 0\n\t" \
    "v_mbcnt_hi_u32_b32 v252, -1, v252\n\t" \
    "v_lshlrev_b32 v252, 4, v252\n\t" \
    "v_lshrrev_b32 v253, 1, v252\n\t" \
    "v_xor_b32 v253, v253, v252\n\t" \
    "v_and_b32 v253, 0x20, v253\n\t" \
    "v_mul_u32_u24 v253, 3, v253\n\t" \
    "v_xor_b32 v253, v253, v252\n\t" \
    "ds_read_b128 v[200:203], v253 offset:28672\n\t" \
    "ds_read_b128 v[204:207], v253 offset:29696\n\t" \
    "ds_read_b128 v[208:211], v253 offset:34816\n\t" \
    "ds_read_b128 v[212:215], v253 offset:35840\n\t" \
    "ds_read_b128 v[216:219], v253 offset:30720\n\t" \
    "ds_read_b128 v[220:223], v253 offset:31744\n\t" \
    "ds_read_b128 v[224:227], v253 offset:36864\n\t" \
    "ds_read_b128 v[228:231], v253 offset:37888\n\t" \
    "v_lshrrev_b32 v254, 1, v252\n\t" \
    "v_and_b32 v254, 0x180, v254\n\t" \
    "v_and_or_b32 v254, v252, %[c70], v254\n\t" \
    "v_bfe_i32 v255, v252, 7, 1\n\t" \
    "v_and_b32 v255, 0xaaaa, v255\n\t" \
    "v_or_b32 v255, 0x4444, v255\n\t" \
    "v_mov_b64 v[232:233], 0\n\t" \
    "v_mov_b64 v[234:235], 0\n\t" \
    "v_mov_b64 v[236:237], 0\n\t" \
    "v_mov_b64 v[238:239], 0\n\t" \
    "s_waitcnt lgkmcnt(0)\n\t" \
    "s_barrier\n\t" ASM_ST \
    "s_setprio 3\n\t" \
    "ds_read_b128 v[240:243], v252 offset:10240\n\t" \
    "ds_read_b128 v[244:247], v252 offset:11264\n\t" \
    "ds_read_b128 v[248:251], v252 offset:12288\n\t" \
    "s_waitcnt lgkmcnt(2)\n\t" \
    "v_smfmac_f32_16x16x64_bf16 v[232:235], v[240:243], v[200:207], v255\n\t" \
    "ds_read_b128 v[200:203], v253 offset:32768\n\t" \
    "ds_read_b128 v[204:207], v253 offset:33792\n\t" \
    "v_smfmac_f32_16x16x64_bf16 v[236:239], v[240:243], v[208:215], v255\n\t" \
    "ds_read_b128 v[208:211], v253 offset:38912\n\t" \
    "ds_read_b128 v[212:215], v253 offset:39936\n\t" \
    "s_waitcnt lgkmcnt(5)\n\t" \
    "v_smfmac_f32_16x16x64_bf16 v[232:235], v[244:247], v[216:223], v255\n\t" \
    "ds_read_b128 v[216:219], v254 offset:13312\n\t" \
    "ds_read_b128 v[220:223], v254 offset:13824\n\t" \
    "v_smfmac_f32_16x16x64_bf16 v[236:239], v[244:247], v[224:231], v255\n\t" \
    "ds_read_b128 v[224:227], v254 offset:14336\n\t" \
    "ds_read_b128 v[228:231], v254 offset:14848\n\t" \
    "s_waitcnt lgkmcnt(6)\n\t" \
    "v_smfmac_f32_16x16x64_bf16 v[232:235], v[248:251], v[200:207], v255\n\t" \
    "s_waitcnt lgkmcnt(4)\n\t" \
    "v_smfmac_f32_16x16x64_bf16 v[236:239], v[248:251], v[208:215], v255\n\t" \
    "ds_read_b128 v[208:211], v254 offset:15360\n\t" \
    "ds_read_b128 v[212:215], v254 offset:15872\n\t" \
    "v_mov_b64 v[204:205], 0\n\t" \
    "v_mov_b64 v[206:207], 0\n\t" \
    "s_nop 1\n\t" \
    "s_waitcnt lgkmcnt(4)\n\t" \
    "v_smfmac_f32_16x16x64_bf16 v[204:207], v[240:243], v[216:223], v255\n\t" \
    "s_waitcnt lgkmcnt(2)\n\t" \
    "v_smfmac_f32_16x16x64_bf16 v[204:207], v[244:247], v[224:231], v255\n\t" \
    "v_bfe_u32 v253, v252, 5, 1\n\t" \
    "v_bfe_i32 v202, v252, 4, 1\n\t" \
    "v_lshlrev_b32 v200, 1, v252\n\t" \
    "v_and_b32 v200, 0x180, v200\n\t" \
    "v_permlane32_swap_b32 v232, v234\n\t" \
    "v_permlane32_swap_b32 v233, v235\n\t" \
    "v_permlane32_swap_b32 v236, v238\n\t" \
    "v_permlane32_swap_b32 v237, v239\n\t" \
    "v_lshrrev_b32 v201, 2, v252\n\t" \
    "v_lshrrev_b32 v203, 4, v252\n\t" \
    "v_and_b32 v201, 0x40, v201\n\t" \
    "v_and_or_b32 v201, v203, 32, v201\n\t" \
    "v_and_or_b32 v201, v252, 16, v201\n\t" \
    "v_pk_add_f32 v[232:233], v[232:233], v[234:235]\n\t" \
    "v_pk_add_f32 v[234:235], v[236:237], v[238:239]\n\t" \
    "v_or_b32 v200, v200, v201\n\t" \
    "v_mad_u32_u24 v200, v253, %[cs], v200\n\t" \
    "v_mad_u32_u24 v201, v253, %[cs], v201\n\t" \
    "v_lshrrev_b32 v203, 3, v252\n\t" \
    "v_bfe_u32 v253, v252, 7, 1\n\t" \
    "s_waitcnt lgkmcnt(0)\n\t" \
    "v_smfmac_f32_16x16x64_bf16 v[204:207], v[248:251], v[208:215], v255\n\t" \
    "v_and_or_b32 v201, v203, 8, v201\n\t" \
    "v_and_b32 v202, 0x06060606, v202\n\t" \
    "v_mad_u32_u24 v201, v253, %[cp], v201\n\t" \
    "v_xor_b32 v202, 0x01000504, v202\n\t" \
    "v_max_f32 v236, v232, v234\n\t" \
    "v_max_f32 v237, v233, v235\n\t" \
    "s_nop 1\n\t" \
    "v_permlane32_swap_b32 v204, v206\n\t" \
    "v_permlane32_swap_b32 v205, v207\n\t" \
    "v_add_f32 v204, v204, v206\n\t" \
    "v_add_f32 v205, v205, v207\n\t" \
    "v_max_f32 v236, v236, v204\n\t" \
    "v_max_f32 v237, v237, v205\n\t" \
    "s_nop 0\n\t" \
    "v_max_f32_dpp v236, v236, v236 quad_perm:[1,0,3,2] row_mask:0xf bank_mask:0xf\n\t" \
    "v_max_f32_dpp v237, v237, v237 quad_perm:[1,0,3,2] row_mask:0xf bank_mask:0xf\n\t" \
    "s_nop 0\n\t" \
    "v_max_f32_dpp v236, v236, v236 quad_perm:[2,3,0,1] row_mask:0xf bank_mask:0xf\n\t" \
    "v_max_f32_dpp v237, v237, v237 quad_perm:[2,3,0,1] row_mask:0xf bank_mask:0xf\n\t" \
    "s_nop 0\n\t" \
    "v_max_f32_dpp v236, v236, v236 row_half_mirror row_mask:0xf bank_mask:0xf\n\t" \
    "v_max_f32_dpp v237, v237, v237 row_half_mirror row_mask:0xf bank_mask:0xf\n\t" \
    "s_nop 0\n\t" \
    "v_max_f32_dpp v236, v236, v236 row_mirror row_mask:0xf bank_mask:0xf\n\t" \
    "v_max_f32_dpp v237, v237, v237 row_mirror row_mask:0xf bank_mask:0xf\n\t" \
    "v_mul_f32 v236, %[scl], v236\n\t" \
    "v_mul_f32 v237, %[scl], v237\n\t" \
    "v_fma_f32 v232, v232, %[scl], -v236\n\t" \
    "v_fma_f32 v233, v233, %[scl], -v237\n\t" \
    "v_fma_f32 v234, v234, %[scl], -v236\n\t" \
    "v_fma_f32 v235, v235, %[scl], -v237\n\t" \
    "v_exp_f32 v232, v232\n\t" \
    "v_exp_f32 v233, v233\n\t" \
    "v_fma_f32 v204, v204, %[scl], -v236\n\t" \
    "v_fma_f32 v205, v205, %[scl], -v237\n\t" \
    "v_exp_f32 v234, v234\n\t" \
    "v_exp_f32 v235, v235\n\t" \
    "v_exp_f32 v204, v204\n\t" \
    "v_exp_f32 v205, v205\n\t" \
    "v_add_f32 v238, v232, v234\n\t" \
    "v_add_f32 v239, v233, v235\n\t" \
    "v_fma_f32 v238, v204, 0.5, v238\n\t" \
    "v_fma_f32 v239, v205, 0.5, v239\n\t" \
    "s_nop 0\n\t" \
    "v_add_f32_dpp v238, v238, v238 quad_perm:[1,0,3,2] row_mask:0xf bank_mask:0xf\n\t" \
    "v_add_f32_dpp v239, v239, v239 quad_perm:[1,0,3,2] row_mask:0xf bank_mask:0xf\n\t" \
    "s_nop 0\n\t" \
    "v_add_f32_dpp v238, v238, v238 quad_perm:[2,3,0,1] row_mask:0xf bank_mask:0xf\n\t" \
    "v_add_f32_dpp v239, v239, v239 quad_perm:[2,3,0,1] row_mask:0xf bank_mask:0xf\n\t" \
    "s_nop 0\n\t" \
    "v_add_f32_dpp v238, v238, v238 row_half_mirror row_mask:0xf bank_mask:0xf\n\t" \
    "v_add_f32_dpp v239, v239, v239 row_half_mirror row_mask:0xf bank_mask:0xf\n\t" \
    "s_nop 0\n\t" \
    "v_add_f32_dpp v238, v238, v238 row_mirror row_mask:0xf bank_mask:0xf\n\t" \
    "v_add_f32_dpp v239, v239, v239 row_mirror row_mask:0xf bank_mask:0xf\n\t" \
    "v_rcp_f32 v238, v238\n\t" \
    "v_rcp_f32 v239, v239\n\t" \
    "s_nop 0\n\t" \
    "v_pk_mul_f32 v[232:233], v[232:233], v[238:239]\n\t" \
    "v_pk_mul_f32 v[234:235], v[234:235], v[238:239]\n\t" \
    "v_pk_mul_f32 v[204:205], v[204:205], v[238:239]\n\t" \
    "v_cvt_pk_bf16_f32 v232, |v232|, |v233|\n\t" \
    "v_cvt_pk_bf16_f32 v234, |v234|, |v235|\n\t" \
    "v_cvt_pk_bf16_f32 v204, |v204|, |v205|\n\t" \
    "v_mov_b32_dpp v233, v232 quad_perm:[1,0,3,2] row_mask:0xf bank_mask:0xf\n\t" \
    "v_mov_b32_dpp v235, v234 quad_perm:[1,0,3,2] row_mask:0xf bank_mask:0xf\n\t" \
    "v_mov_b32_dpp v205, v204 quad_perm:[1,0,3,2] row_mask:0xf bank_mask:0xf\n\t" \
    "v_perm_b32 v232, v232, v233, v202\n\t" \
    "v_perm_b32 v234, v234, v235, v202\n\t" \
    "v_perm_b32 v204, v204, v205, v202\n\t" \
    "v_mov_b32_dpp v240, v232 quad_perm:[0,1,0,1] row_mask:0xf bank_mask:0xf\n\t" \
    "v_mov_b32_dpp v241, v234 quad_perm:[0,1,0,1] row_mask:0xf bank_mask:0xf\n\t" \
    "v_mov_b32_dpp v242, v232 quad_perm:[2,3,2,3] row_mask:0xf bank_mask:0xf\n\t" \
    "v_mov_b32_dpp v243, v234 quad_perm:[2,3,2,3] row_mask:0xf bank_mask:0xf\n\t" \
    "buffer_store_dwordx4 v[240:243], v200, %[rr], %[sx] offen sc1\n\t" \
    "v_mov_b32_dpp v244, v204 quad_perm:[0,1,0,1] row_mask:0xf bank_mask:0xf\n\t" \
    "v_mov_b32_dpp v245, v204 quad_perm:[2,3,2,3] row_mask:0xf bank_mask:0xf\n\t" \
    "buffer_store_dwordx4 v[240:243], v200, %[rr], %[sy] offen sc1\n\t" \
    "buffer_store_dwordx2 v[244:245], v201, %[rr], %[sm] offen sc1\n\t" \
    "s_wakeup\n\t" \
    PRISM
#define ASM_P4_0 \
    "ds_read_b128 v[200:203], %[qa] offset:0\n\t" \
    "ds_read_b128 v[204:207], %[qa] offset:1024\n\t" \
    "ds_read_b128 v[208:211], %[qa] offset:2048\n\t" \
    "ds_read_b128 v[212:215], %[qa] offset:3072\n\t" \
    "ds_read_b128 v[216:219], %[qa] offset:4096\n\t" \
    "ds_read_b128 v[220:223], %[qa] offset:5120\n\t" \
    "ds_read_b128 v[224:227], %[qa] offset:12288\n\t" \
    "ds_read_b128 v[228:231], %[qa] offset:13312\n\t" \
    "ds_read_b128 v[232:235], %[qa] offset:14336\n\t" \
    "ds_read_b128 v[236:239], %[qa] offset:15360\n\t" \
    "s_waitcnt lgkmcnt(0)\n\t" \
    "s_barrier\n\t" ASM_ST \
    "v_mfma_f32_16x16x32_bf16 v[240:243], v[224:227], v[200:203], v[240:243]\n\t" \
    "ds_read_b128 v[224:227], %[qa] offset:16384\n\t" \
    "v_mfma_f32_16x16x32_bf16 v[240:243], v[228:231], v[204:207], v[240:243]\n\t" \
    "ds_read_b128 v[228:231], %[qa] offset:17408\n\t" \
    "v_mfma_f32_16x16x32_bf16 v[240:243], v[232:235], v[208:211], v[240:243]\n\t" \
    "ds_read_b128 v[232:235], %[km] offset:0\n\t" \
    "v_mfma_f32_16x16x32_bf16 v[240:243], v[236:239], v[212:215], v[240:243]\n\t" \
    "ds_read_b128 v[236:239], %[km] offset:512\n\t" \
    "s_waitcnt lgkmcnt(3)\n\t" \
    "v_mfma_f32_16x16x32_bf16 v[240:243], v[224:227], v[216:219], v[240:243]\n\t" \
    "ds_read_b128 v[224:227], %[km] offset:1024\n\t" \
    "s_waitcnt lgkmcnt(3)\n\t" \
    "v_mfma_f32_16x16x32_bf16 v[240:243], v[228:231], v[220:223], v[240:243]\n\t" \
    "ds_read_b128 v[228:231], %[km] offset:1536\n\t" \
    "s_waitcnt lgkmcnt(3)\n\t" \
    "v_mfma_f32_16x16x32_bf16 v[248:251], v[232:235], v[200:203], 0\n\t" \
    "ds_read_b128 v[232:235], %[km] offset:2048\n\t" \
    "s_waitcnt lgkmcnt(3)\n\t" \
    "v_mfma_f32_16x16x32_bf16 v[248:251], v[236:239], v[204:207], v[248:251]\n\t" \
    "ds_read_b128 v[236:239], %[km] offset:2560\n\t" \
    "s_waitcnt lgkmcnt(3)\n\t" \
    "v_mfma_f32_16x16x32_bf16 v[248:251], v[224:227], v[208:211], v[248:251]\n\t" \
    "s_waitcnt lgkmcnt(2)\n\t" \
    "v_mfma_f32_16x16x32_bf16 v[248:251], v[228:231], v[212:215], v[248:251]\n\t" \
    "s_waitcnt lgkmcnt(1)\n\t" \
    "v_mfma_f32_16x16x32_bf16 v[248:251], v[232:235], v[216:219], v[248:251]\n\t" \
    "s_waitcnt lgkmcnt(0)\n\t" \
    "v_mfma_f32_16x16x32_bf16 v[248:251], v[236:239], v[220:223], v[248:251]\n\t" \
    "v_max3_f32 v224, v240, v241, v242\n\t" \
    "v_mov_b32 v225, v243\n\t" \
    "v_lshlrev_b32 v201, 23, %[qa]\n\t" \
    "v_and_b32 v201, 0x80000000, v201\n\t" \
    "v_and_b32 v200, 0x3ff, %[qa]\n\t" \
    "v_or_b32 v200, v201, v200\n\t" \
    "s_nop 1\n\t" \
    "v_max3_f32 v224, v224, v248, v249\n\t" \
    "v_max3_f32 v225, v225, v250, v251\n\t" \
    "v_max_f32 v224, v224, v225\n\t" \
    "v_mov_b32 v225, v224\n\t" \
    "s_nop 1\n\t" \
    "v_permlane16_swap_b32 v224, v225\n\t" \
    "v_max_f32 v224, v224, v225\n\t" \
    "v_mov_b32 v225, v224\n\t" \
    "s_nop 1\n\t" \
    "v_permlane32_swap_b32 v224, v225\n\t" \
    "v_max_f32 v224, v224, v225\n\t" \
    "v_mul_f32 v224, %[scl], v224\n\t" \
    "v_fma_f32 v240, v240, %[scl], -v224\n\t" \
    "v_fma_f32 v241, v241, %[scl], -v224\n\t" \
    "v_exp_f32 v240, v240\n\t" \
    "v_fma_f32 v242, v242, %[scl], -v224\n\t" \
    "v_exp_f32 v241, v241\n\t" \
    "v_fma_f32 v243, v243, %[scl], -v224\n\t" \
    "v_exp_f32 v242, v242\n\t" \
    "v_fma_f32 v248, v248, %[scl], -v224\n\t" \
    "v_exp_f32 v243, v243\n\t" \
    "v_fma_f32 v249, v249, %[scl], -v224\n\t" \
    "v_exp_f32 v248, v248\n\t" \
    "v_fma_f32 v250, v250, %[scl], -v224\n\t" \
    "v_exp_f32 v249, v249\n\t" \
    "v_fma_f32 v251, v251, %[scl], -v224\n\t" \
    "v_exp_f32 v250, v250\n\t" \
    "v_exp_f32 v251, v251\n\t" \
    "v_add_f32 v224, v240, v241\n\t" \
    "v_add_f32 v225, v242, v243\n\t" \
    "v_add_f32 v232, v248, v249\n\t" \
    "v_add_f32 v233, v250, v251\n\t" \
    "v_add_f32 v224, v224, v225\n\t" \
    "v_add_f32 v232, v232, v233\n\t" \
    "v_fma_f32 v224, v232, 0.5, v224\n\t" \
    "v_mov_b32 v225, v224\n\t" \
    "s_nop 1\n\t" \
    "v_permlane16_swap_b32 v224, v225\n\t" \
    "v_add_f32 v224, v224, v225\n\t" \
    "v_mov_b32 v225, v224\n\t" \
    "s_nop 1\n\t" \
    "v_permlane32_swap_b32 v224, v225\n\t" \
    "v_add_f32 v224, v224, v225\n\t" \
    "v_rcp_f32 v224, v224\n\t" \
    "s_nop 0\n\t" \
    "v_mul_f32 v240, v240, v224\n\t" \
    "v_mul_f32 v241, v241, v224\n\t" \
    "v_mul_f32 v242, v242, v224\n\t" \
    "v_mul_f32 v243, v243, v224\n\t" \
    "v_mul_f32 v248, v248, v224\n\t" \
    "v_mul_f32 v249, v249, v224\n\t" \
    "v_mul_f32 v250, v250, v224\n\t" \
    "v_mul_f32 v251, v251, v224\n\t" \
    "v_cvt_pk_bf16_f32 v228, |v240|, |v241|\n\t" \
    "v_cvt_pk_bf16_f32 v229, |v242|, |v243|\n\t" \
    "v_cvt_pk_bf16_f32 v232, |v248|, |v249|\n\t" \
    "v_cvt_pk_bf16_f32 v233, |v250|, |v251|\n\t" \
    "v_permlane16_swap_b32 v228, v230\n\t" \
    "v_permlane16_swap_b32 v229, v231\n\t" \
    "buffer_store_dwordx4 v[228:231], v200, %[rr], %[so] offen sc1\n\t" \
    "buffer_store_dwordx2 v[232:233], %[pm], %[rr], %[so] offen sc1\n\t" \
    "s_nop 1\n\t"
#define ASM_P4_1 \
    "ds_read_b128 v[200:203], %[qa] offset:0\n\t" \
    "ds_read_b128 v[204:207], %[qa] offset:1024\n\t" \
    "ds_read_b128 v[208:211], %[qa] offset:2048\n\t" \
    "ds_read_b128 v[212:215], %[qa] offset:3072\n\t" \
    "ds_read_b128 v[216:219], %[qa] offset:4096\n\t" \
    "ds_read_b128 v[220:223], %[qa] offset:5120\n\t" \
    "ds_read_b128 v[224:227], %[qa] offset:12288\n\t" \
    "ds_read_b128 v[228:231], %[qa] offset:13312\n\t" \
    "ds_read_b128 v[232:235], %[qa] offset:14336\n\t" \
    "ds_read_b128 v[236:239], %[qa] offset:15360\n\t" \
    "s_waitcnt lgkmcnt(0)\n\t" \
    "s_barrier\n\t" ASM_ST \
    "v_mfma_f32_16x16x32_bf16 v[240:243], v[224:227], v[200:203], v[240:243]\n\t" \
    "ds_read_b128 v[224:227], %[qa] offset:16384\n\t" \
    "v_mfma_f32_16x16x32_bf16 v[240:243], v[228:231], v[204:207], v[240:243]\n\t" \
    "ds_read_b128 v[228:231], %[qa] offset:17408\n\t" \
    "v_mfma_f32_16x16x32_bf16 v[240:243], v[232:235], v[208:211], v[240:243]\n\t" \
    "ds_read_b128 v[232:235], %[qa] offset:6144\n\t" \
    "v_mfma_f32_16x16x32_bf16 v[240:243], v[236:239], v[212:215], v[240:243]\n\t" \
    "ds_read_b128 v[236:239], %[qa] offset:7168\n\t" \
    "s_waitcnt lgkmcnt(3)\n\t" \
    "v_mfma_f32_16x16x32_bf16 v[240:243], v[224:227], v[216:219], v[240:243]\n\t" \
    "ds_read_b128 v[224:227], %[qa] offset:8192\n\t" \
    "s_waitcnt lgkmcnt(3)\n\t" \
    "v_mfma_f32_16x16x32_bf16 v[240:243], v[228:231], v[220:223], v[240:243]\n\t" \
    "ds_read_b128 v[228:231], %[qa] offset:9216\n\t" \
    "s_waitcnt lgkmcnt(3)\n\t" \
    "v_mfma_f32_16x16x32_bf16 v[244:247], v[232:235], v[200:203], 0\n\t" \
    "ds_read_b128 v[232:235], %[qa] offset:10240\n\t" \
    "s_waitcnt lgkmcnt(3)\n\t" \
    "v_mfma_f32_16x16x32_bf16 v[244:247], v[236:239], v[204:207], v[244:247]\n\t" \
    "ds_read_b128 v[236:239], %[qa] offset:11264\n\t" \
    "s_waitcnt lgkmcnt(3)\n\t" \
    "v_mfma_f32_16x16x32_bf16 v[244:247], v[224:227], v[208:211], v[244:247]\n\t" \
    "ds_read_b128 v[224:227], %[km] offset:0\n\t" \
    "s_waitcnt lgkmcnt(3)\n\t" \
    "v_mfma_f32_16x16x32_bf16 v[244:247], v[228:231], v[212:215], v[244:247]\n\t" \
    "ds_read_b128 v[228:231], %[km] offset:512\n\t" \
    "s_waitcnt lgkmcnt(3)\n\t" \
    "v_mfma_f32_16x16x32_bf16 v[244:247], v[232:235], v[216:219], v[244:247]\n\t" \
    "ds_read_b128 v[232:235], %[km] offset:1024\n\t" \
    "s_waitcnt lgkmcnt(3)\n\t" \
    "v_mfma_f32_16x16x32_bf16 v[244:247], v[236:239], v[220:223], v[244:247]\n\t" \
    "ds_read_b128 v[236:239], %[km] offset:1536\n\t" \
    "s_waitcnt lgkmcnt(3)\n\t" \
    "v_mfma_f32_16x16x32_bf16 v[248:251], v[224:227], v[200:203], 0\n\t" \
    "ds_read_b128 v[224:227], %[km] offset:2048\n\t" \
    "s_waitcnt lgkmcnt(3)\n\t" \
    "v_mfma_f32_16x16x32_bf16 v[248:251], v[228:231], v[204:207], v[248:251]\n\t" \
    "ds_read_b128 v[228:231], %[km] offset:2560\n\t" \
    "s_waitcnt lgkmcnt(3)\n\t" \
    "v_mfma_f32_16x16x32_bf16 v[248:251], v[232:235], v[208:211], v[248:251]\n\t" \
    "s_waitcnt lgkmcnt(2)\n\t" \
    "v_mfma_f32_16x16x32_bf16 v[248:251], v[236:239], v[212:215], v[248:251]\n\t" \
    "s_waitcnt lgkmcnt(1)\n\t" \
    "v_mfma_f32_16x16x32_bf16 v[248:251], v[224:227], v[216:219], v[248:251]\n\t" \
    "s_waitcnt lgkmcnt(0)\n\t" \
    "v_mfma_f32_16x16x32_bf16 v[248:251], v[228:231], v[220:223], v[248:251]\n\t" \
    "v_max3_f32 v224, v240, v241, v242\n\t" \
    "v_max3_f32 v225, v244, v245, v246\n\t" \
    "v_max3_f32 v224, v224, v243, v247\n\t" \
    "v_and_b32 v200, 0x3ff, %[qa]\n\t" \
    "s_nop 3\n\t" \
    "v_max3_f32 v224, v224, v248, v249\n\t" \
    "v_max3_f32 v225, v225, v250, v251\n\t" \
    "v_max_f32 v224, v224, v225\n\t" \
    "v_mov_b32 v225, v224\n\t" \
    "s_nop 1\n\t" \
    "v_permlane16_swap_b32 v224, v225\n\t" \
    "v_max_f32 v224, v224, v225\n\t" \
    "v_mov_b32 v225, v224\n\t" \
    "s_nop 1\n\t" \
    "v_permlane32_swap_b32 v224, v225\n\t" \
    "v_max_f32 v224, v224, v225\n\t" \
    "v_mul_f32 v224, %[scl], v224\n\t" \
    "v_fma_f32 v240, v240, %[scl], -v224\n\t" \
    "v_fma_f32 v241, v241, %[scl], -v224\n\t" \
    "v_exp_f32 v240, v240\n\t" \
    "v_fma_f32 v242, v242, %[scl], -v224\n\t" \
    "v_exp_f32 v241, v241\n\t" \
    "v_fma_f32 v243, v243, %[scl], -v224\n\t" \
    "v_exp_f32 v242, v242\n\t" \
    "v_fma_f32 v244, v244, %[scl], -v224\n\t" \
    "v_exp_f32 v243, v243\n\t" \
    "v_fma_f32 v245, v245, %[scl], -v224\n\t" \
    "v_exp_f32 v244, v244\n\t" \
    "v_fma_f32 v246, v246, %[scl], -v224\n\t" \
    "v_exp_f32 v245, v245\n\t" \
    "v_fma_f32 v247, v247, %[scl], -v224\n\t" \
    "v_exp_f32 v246, v246\n\t" \
    "v_fma_f32 v248, v248, %[scl], -v224\n\t" \
    "v_exp_f32 v247, v247\n\t" \
    "v_fma_f32 v249, v249, %[scl], -v224\n\t" \
    "v_exp_f32 v248, v248\n\t" \
    "v_fma_f32 v250, v250, %[scl], -v224\n\t" \
    "v_exp_f32 v249, v249\n\t" \
    "v_fma_f32 v251, v251, %[scl], -v224\n\t" \
    "v_exp_f32 v250, v250\n\t" \
    "v_exp_f32 v251, v251\n\t" \
    "v_add_f32 v224, v240, v241\n\t" \
    "v_add_f32 v225, v242, v243\n\t" \
    "v_add_f32 v226, v244, v245\n\t" \
    "v_add_f32 v227, v246, v247\n\t" \
    "v_add_f32 v232, v248, v249\n\t" \
    "v_add_f32 v233, v250, v251\n\t" \
    "v_add_f32 v224, v224, v225\n\t" \
    "v_add_f32 v226, v226, v227\n\t" \
    "v_add_f32 v232, v232, v233\n\t" \
    "v_add_f32 v224, v224, v226\n\t" \
    "v_fma_f32 v224, v232, 0.5, v224\n\t" \
    "v_mov_b32 v225, v224\n\t" \
    "s_nop 1\n\t" \
    "v_permlane16_swap_b32 v224, v225\n\t" \
    "v_add_f32 v224, v224, v225\n\t" \
    "v_mov_b32 v225, v224\n\t" \
    "s_nop 1\n\t" \
    "v_permlane32_swap_b32 v224, v225\n\t" \
    "v_add_f32 v224, v224, v225\n\t" \
    "v_rcp_f32 v224, v224\n\t" \
    "s_nop 0\n\t" \
    "v_mul_f32 v240, v240, v224\n\t" \
    "v_mul_f32 v241, v241, v224\n\t" \
    "v_mul_f32 v242, v242, v224\n\t" \
    "v_mul_f32 v243, v243, v224\n\t" \
    "v_mul_f32 v244, v244, v224\n\t" \
    "v_mul_f32 v245, v245, v224\n\t" \
    "v_mul_f32 v246, v246, v224\n\t" \
    "v_mul_f32 v247, v247, v224\n\t" \
    "v_mul_f32 v248, v248, v224\n\t" \
    "v_mul_f32 v249, v249, v224\n\t" \
    "v_mul_f32 v250, v250, v224\n\t" \
    "v_mul_f32 v251, v251, v224\n\t" \
    "v_cvt_pk_bf16_f32 v230, |v240|, |v241|\n\t" \
    "v_cvt_pk_bf16_f32 v231, |v242|, |v243|\n\t" \
    "v_cvt_pk_bf16_f32 v228, |v244|, |v245|\n\t" \
    "v_cvt_pk_bf16_f32 v229, |v246|, |v247|\n\t" \
    "v_cvt_pk_bf16_f32 v232, |v248|, |v249|\n\t" \
    "v_cvt_pk_bf16_f32 v233, |v250|, |v251|\n\t" \
    "buffer_store_dwordx4 v[228:231], v200, %[rr], %[so] offen sc1\n\t" \
    "buffer_store_dwordx2 v[232:233], %[pm], %[rr], %[so] offen sc1\n\t" \
    "s_nop 1\n\t"
#define ASM_RCRIT \
    "v_mov_b64 v[176:177], -1\n\t" \
    "v_mov_b64 v[178:179], -1\n\t" \
    "v_mov_b64 v[180:181], -1\n\t" \
    "v_mov_b64 v[182:183], -1\n\t" \
    "v_mov_b64 v[184:185], -1\n\t" \
    "v_mov_b64 v[186:187], -1\n\t" \
    "v_mov_b64 v[188:189], -1\n\t" \
    "v_mov_b64 v[190:191], -1\n\t" \
    "v_mov_b64 v[192:193], -1\n\t" \
    "v_mov_b64 v[194:195], -1\n\t" \
    ".Lfa%=:\n\t" \
    "s_mov_b32 %[a], 0\n\t" \
    "v_max_u32 v196, v176, v177\n\t" \
    "v_max3_u32 v196, v178, v179, v196\n\t" \
    "v_cmp_eq_u32 vcc, -1, v196\n\t" \
    "s_cbranch_vccz .Lga0_%=\n\t" \
    "buffer_load_dwordx4 v[176:179], %[ov], %[rs], %[sv] offen sc1\n\t" \
    "s_mov_b32 %[a], 1\n\t" \
    ".Lga0_%=:\n\t" \
    "v_max_u32 v196, v180, v181\n\t" \
    "v_max3_u32 v196, v182, v183, v196\n\t" \
    "v_cmp_eq_u32 vcc, -1, v196\n\t" \
    "s_cbranch_vccz .Lga1_%=\n\t" \
    "buffer_load_dwordx4 v[180:183], %[ov], %[rs], %[sv] offen offset:1024 sc1\n\t" \
    "s_mov_b32 %[a], 1\n\t" \
    ".Lga1_%=:\n\t" \
    "s_cmp_eq_u32 %[a], 0\n\t" \
    "s_cbranch_scc1 .Lha%=\n\t" \
    "s_waitcnt vmcnt(12)\n\t" \
    ".Lia%=:\n\t" \
    "v_max_u32 v196, v176, v177\n\t" \
    "v_max3_u32 v196, v178, v179, v196\n\t" \
    "v_max3_u32 v196, v180, v181, v196\n\t" \
    "v_max3_u32 v196, v182, v183, v196\n\t" \
    "v_cmp_eq_u32 vcc, -1, v196\n\t" \
    "s_cbranch_vccz .Lha%=\n\t" \
    "s_nop 7\n\t" \
    "v_max_u32 v196, v176, v177\n\t" \
    "v_max3_u32 v196, v178, v179, v196\n\t" \
    "v_max3_u32 v196, v180, v181, v196\n\t" \
    "v_max3_u32 v196, v182, v183, v196\n\t" \
    "v_cmp_eq_u32 vcc, -1, v196\n\t" \
    "s_cbranch_vccz .Lha%=\n\t" \
    "s_nop 7\n\t" \
    "v_max_u32 v196, v176, v177\n\t" \
    "v_max3_u32 v196, v178, v179, v196\n\t" \
    "v_max3_u32 v196, v180, v181, v196\n\t" \
    "v_max3_u32 v196, v182, v183, v196\n\t" \
    "v_cmp_eq_u32 vcc, -1, v196\n\t" \
    "s_cbranch_vccz .Lha%=\n\t" \
    "s_nop 7\n\t" \
    "v_max_u32 v196, v176, v177\n\t" \
    "v_max3_u32 v196, v178, v179, v196\n\t" \
    "v_max3_u32 v196, v180, v181, v196\n\t" \
    "v_max3_u32 v196, v182, v183, v196\n\t" \
    "v_cmp_eq_u32 vcc, -1, v196\n\t" \
    "s_cbranch_vccz .Lha%=\n\t" \
    "s_nop 7\n\t" \
    "s_sub_u32 %[n], %[n], 1\n\t" \
    "s_cmp_gt_i32 %[n], 0\n\t" \
    "s_cbranch_scc1 .Lfa%=\n\t" \
    ".Lha%=:\n\t" \
    ASM_ST3 \
    "buffer_load_dwordx4 v[184:187], %[o0], %[rs], %[sp] offen sc1\n\t" \
    "buffer_load_dwordx4 v[188:191], %[o0], %[rs], %[sp] offen offset:1024 sc1\n\t" \
    "buffer_load_dwordx4 v[192:195], %[o2], %[rs], %[sp] offen sc1\n\t" \
    "s_waitcnt vmcnt(0)\n\t" \
    "v_max_u32 v196, v184, v185\n\t" \
    "v_max3_u32 v196, v186, v187, v196\n\t" \
    "v_max3_u32 v196, v188, v189, v196\n\t" \
    "v_max3_u32 v196, v190, v191, v196\n\t" \
    "v_max3_u32 v196, v192, v193, v196\n\t" \
    "v_max3_u32 v196, v194, v195, v196\n\t" \
    "v_cmp_eq_u32 vcc, -1, v196\n\t" \
    "s_cbranch_vccz .Lhb%=\n\t" \
    ".Lfb%=:\n\t" \
    "s_mov_b32 %[a], 0\n\t" \
    "v_max_u32 v196, v184, v185\n\t" \
    "v_max3_u32 v196, v186, v187, v196\n\t" \
    "v_cmp_eq_u32 vcc, -1, v196\n\t" \
    "s_cbranch_vccz .Lgb0_%=\n\t" \
    "buffer_load_dwordx4 v[184:187], %[o0], %[rs], %[sp] offen sc1\n\t" \
    "s_mov_b32 %[a], 1\n\t" \
    ".Lgb0_%=:\n\t" \
    "v_max_u32 v196, v188, v189\n\t" \
    "v_max3_u32 v196, v190, v191, v196\n\t" \
    "v_cmp_eq_u32 vcc, -1, v196\n\t" \
    "s_cbranch_vccz .Lgb1_%=\n\t" \
    "buffer_load_dwordx4 v[188:191], %[o0], %[rs], %[sp] offen offset:1024 sc1\n\t" \
    "s_mov_b32 %[a], 1\n\t" \
    ".Lgb1_%=:\n\t" \
    "v_max_u32 v196, v192, v193\n\t" \
    "v_max3_u32 v196, v194, v195, v196\n\t" \
    "v_cmp_eq_u32 vcc, -1, v196\n\t" \
    "s_cbranch_vccz .Lgb2_%=\n\t" \
    "buffer_load_dwordx4 v[192:195], %[o2], %[rs], %[sp] offen sc1\n\t" \
    "s_mov_b32 %[a], 1\n\t" \
    ".Lgb2_%=:\n\t" \
    "s_cmp_eq_u32 %[a], 0\n\t" \
    "s_cbranch_scc1 .Lhb%=\n\t" \
    "s_waitcnt vmcnt(12)\n\t" \
    ".Lib%=:\n\t" \
    "v_max_u32 v196, v184, v185\n\t" \
    "v_max3_u32 v196, v186, v187, v196\n\t" \
    "v_max3_u32 v196, v188, v189, v196\n\t" \
    "v_max3_u32 v196, v190, v191, v196\n\t" \
    "v_max3_u32 v196, v192, v193, v196\n\t" \
    "v_max3_u32 v196, v194, v195, v196\n\t" \
    "v_cmp_eq_u32 vcc, -1, v196\n\t" \
    "s_cbranch_vccz .Lhb%=\n\t" \
    "s_nop 7\n\t" \
    "v_max_u32 v196, v184, v185\n\t" \
    "v_max3_u32 v196, v186, v187, v196\n\t" \
    "v_max3_u32 v196, v188, v189, v196\n\t" \
    "v_max3_u32 v196, v190, v191, v196\n\t" \
    "v_max3_u32 v196, v192, v193, v196\n\t" \
    "v_max3_u32 v196, v194, v195, v196\n\t" \
    "v_cmp_eq_u32 vcc, -1, v196\n\t" \
    "s_cbranch_vccz .Lhb%=\n\t" \
    "s_nop 7\n\t" \
    "v_max_u32 v196, v184, v185\n\t" \
    "v_max3_u32 v196, v186, v187, v196\n\t" \
    "v_max3_u32 v196, v188, v189, v196\n\t" \
    "v_max3_u32 v196, v190, v191, v196\n\t" \
    "v_max3_u32 v196, v192, v193, v196\n\t" \
    "v_max3_u32 v196, v194, v195, v196\n\t" \
    "v_cmp_eq_u32 vcc, -1, v196\n\t" \
    "s_cbranch_vccz .Lhb%=\n\t" \
    "s_nop 7\n\t" \
    "v_max_u32 v196, v184, v185\n\t" \
    "v_max3_u32 v196, v186, v187, v196\n\t" \
    "v_max3_u32 v196, v188, v189, v196\n\t" \
    "v_max3_u32 v196, v190, v191, v196\n\t" \
    "v_max3_u32 v196, v192, v193, v196\n\t" \
    "v_max3_u32 v196, v194, v195, v196\n\t" \
    "v_cmp_eq_u32 vcc, -1, v196\n\t" \
    "s_cbranch_vccz .Lhb%=\n\t" \
    "s_nop 7\n\t" \
    "s_sub_u32 %[n], %[n], 1\n\t" \
    "s_cmp_gt_i32 %[n], 0\n\t" \
    "s_cbranch_scc1 .Lfb%=\n\t" \
    ".Lhb%=:\n\t" \
    ASM_ST2 \
    "v_mfma_f32_16x16x32_bf16 v[160:163], v[192:195], v[176:179], 0\n\t" \
    "v_mfma_f32_16x16x32_bf16 v[164:167], v[192:195], v[180:183], 0\n\t" \
    "v_smfmac_f32_16x16x64_bf16 v[160:163], v[184:187], v[128:135], %[sidx]\n\t" \
    "v_smfmac_f32_16x16x64_bf16 v[164:167], v[184:187], v[144:151], %[sidx]\n\t" \
    "v_smfmac_f32_16x16x64_bf16 v[160:163], v[188:191], v[136:143], %[sidx]\n\t" \
    "v_smfmac_f32_16x16x64_bf16 v[164:167], v[188:191], v[152:159], %[sidx]\n\t" \
    "s_cmp_eq_u32 %[pub], 0\n\t" \
    "s_nop 7\n\t" \
    "v_permlane32_swap_b32 v160, v164\n\t" \
    "v_permlane32_swap_b32 v161, v165\n\t" \
    "v_permlane32_swap_b32 v162, v166\n\t" \
    "v_permlane32_swap_b32 v163, v167\n\t" \
    "v_add_f32 v160, v160, v164\n\t" \
    "v_add_f32 v161, v161, v165\n\t" \
    "ds_write2_b32 %[rw], v160, v161 offset1:80\n\t" \
    "v_add_f32 v162, v162, v166\n\t" \
    "v_add_f32 v163, v163, v167\n\t" \
    "ds_write2_b32 %[rw], v162, v163 offset0:160 offset1:240\n\t" \
    "s_waitcnt lgkmcnt(0)\n\t" \
    "s_barrier\n\t" ASM_ST \
    "s_cbranch_scc1 .Lre%=\n\t" \
    "ds_read_b128 v[200:203], %[rd] offset:0\n\t" \
    "ds_read_b128 v[204:207], %[rd] offset:16\n\t" \
    "s_waitcnt lgkmcnt(1)\n\t" \
    "v_pk_add_f32 v[200:201], v[200:201], v[202:203]\n\t" \
    "s_waitcnt lgkmcnt(0)\n\t" \
    "v_pk_add_f32 v[204:205], v[204:205], v[206:207]\n\t" \
    "v_pk_add_f32 v[200:201], v[200:201], v[204:205]\n\t" \
    "s_nop 1\n\t" \
    "v_permlane32_swap_b32 v200, v201\n\t" \
    "v_add_f32 v202, v200, v201\n\t" \
    "v_add_f32 v203, v200, v201\n\t" \
    "s_nop 1\n\t" \
    "v_permlane32_swap_b32 v202, v203\n\t" \
    "v_cvt_pk_bf16_f32 v200, v202, v203\n\t" \
    "v_min_u32 v200, -2, v200\n\t" \
    "buffer_store_dword v200, %[oo], %[rs], %[so] offen sc1\n\t" \
    ".Lre%=:\n\t"
#define ASM_RX \
    "s_cmp_eq_u32 %[fin], 0\n\t" \
    "s_cbranch_scc1 .Lx0%=\n\t" \
    "buffer_load_dwordx2 v[250:251], %[xo], %[rx], %[sx] offen sc1\n\t" \
    ".Lx0%=:\n\t" \
    "buffer_load_dword v248, %[fo], %[rs], %[fs] offen sc1\n\t" \
    "buffer_load_dwordx4 v[200:203], %[l16], %[rs], %[s0] offen offset:0 sc1\n\t" \
    "buffer_load_dwordx4 v[204:207], %[l16], %[rs], %[s0] offen offset:1024 sc1\n\t" \
    "buffer_load_dwordx4 v[208:211], %[l16], %[rs], %[s0] offen offset:2048 sc1\n\t" \
    "buffer_load_dwordx4 v[212:215], %[l16], %[rs], %[s0] offen offset:3072 sc1\n\t" \
    "buffer_load_dwordx4 v[216:219], %[l16], %[rs], %[s1] offen offset:0 sc1\n\t" \
    "buffer_load_dwordx4 v[220:223], %[l16], %[rs], %[s1] offen offset:1024 sc1\n\t" \
    "buffer_load_dwordx4 v[224:227], %[l16], %[rs], %[s1] offen offset:2048 sc1\n\t" \
    "buffer_load_dwordx4 v[228:231], %[l16], %[rs], %[s1] offen offset:3072 sc1\n\t" \
    "v_mov_b64 v[96:97], v[128:129]\n\t" \
    "v_mov_b64 v[98:99], v[132:133]\n\t" \
    "v_mov_b64 v[100:101], v[136:137]\n\t" \
    "v_mov_b64 v[102:103], v[140:141]\n\t" \
    "v_mov_b64 v[104:105], v[144:145]\n\t" \
    "v_mov_b64 v[106:107], v[148:149]\n\t" \
    "v_mov_b64 v[108:109], v[152:153]\n\t" \
    "v_mov_b64 v[110:111], v[156:157]\n\t" \
    "s_movk_i32 %[n], 0x200\n\t" \
    "s_nop 1\n\t" \
    "v_permlane16_swap_b32 v96, v98\n\t" \
    "v_permlane16_swap_b32 v97, v99\n\t" \
    "v_permlane16_swap_b32 v100, v102\n\t" \
    "v_permlane16_swap_b32 v101, v103\n\t" \
    "v_permlane16_swap_b32 v104, v106\n\t" \
    "v_permlane16_swap_b32 v105, v107\n\t" \
    "v_permlane16_swap_b32 v108, v110\n\t" \
    "v_permlane16_swap_b32 v109, v111\n\t" \
    "s_waitcnt vmcnt(7)\n\t" \
    "v_max_u32 v196, v200, v201\n\t" \
    "v_max3_u32 v196, v202, v203, v196\n\t" \
    "s_waitcnt vmcnt(6)\n\t" \
    "v_max3_u32 v196, v204, v205, v196\n\t" \
    "v_max3_u32 v196, v206, v207, v196\n\t" \
    "s_waitcnt vmcnt(5)\n\t" \
    "v_max3_u32 v196, v208, v209, v196\n\t" \
    "v_max3_u32 v196, v210, v211, v196\n\t" \
    "s_waitcnt vmcnt(4)\n\t" \
    "v_max3_u32 v196, v212, v213, v196\n\t" \
    "v_max3_u32 v196, v214, v215, v196\n\t" \
    "s_waitcnt vmcnt(3)\n\t" \
    "v_max3_u32 v196, v216, v217, v196\n\t" \
    "v_max3_u32 v196, v218, v219, v196\n\t" \
    "s_waitcnt vmcnt(2)\n\t" \
    "v_max3_u32 v196, v220, v221, v196\n\t" \
    "v_max3_u32 v196, v222, v223, v196\n\t" \
    "s_waitcnt vmcnt(1)\n\t" \
    "v_max3_u32 v196, v224, v225, v196\n\t" \
    "v_max3_u32 v196, v226, v227, v196\n\t" \
    "s_waitcnt vmcnt(0)\n\t" \
    "v_max3_u32 v196, v228, v229, v196\n\t" \
    "v_max3_u32 v196, v230, v231, v196\n\t" \
    "v_cmp_eq_u32 vcc, -1, v196\n\t" \
    "s_cbranch_vccz .Lxd%=\n\t" \
    ".Lxl%=:\n\t" \
    "s_mov_b32 %[a], 0\n\t" \
    "v_max_u32 v196, v200, v201\n\t" \
    "v_max3_u32 v196, v202, v203, v196\n\t" \
    "v_cmp_eq_u32 vcc, -1, v196\n\t" \
    "s_cbranch_vccz .Lxs0_%=\n\t" \
    "buffer_load_dwordx4 v[200:203], %[l16], %[rs], %[s0] offen offset:0 sc1\n\t" \
    "s_mov_b32 %[a], 1\n\t" \
    ".Lxs0_%=:\n\t" \
    "v_max_u32 v196, v204, v205\n\t" \
    "v_max3_u32 v196, v206, v207, v196\n\t" \
    "v_cmp_eq_u32 vcc, -1, v196\n\t" \
    "s_cbranch_vccz .Lxs1_%=\n\t" \
    "buffer_load_dwordx4 v[204:207], %[l16], %[rs], %[s0] offen offset:1024 sc1\n\t" \
    "s_mov_b32 %[a], 1\n\t" \
    ".Lxs1_%=:\n\t" \
    "v_max_u32 v196, v208, v209\n\t" \
    "v_max3_u32 v196, v210, v211, v196\n\t" \
    "v_cmp_eq_u32 vcc, -1, v196\n\t" \
    "s_cbranch_vccz .Lxs2_%=\n\t" \
    "buffer_load_dwordx4 v[208:211], %[l16], %[rs], %[s0] offen offset:2048 sc1\n\t" \
    "s_mov_b32 %[a], 1\n\t" \
    ".Lxs2_%=:\n\t" \
    "v_max_u32 v196, v212, v213\n\t" \
    "v_max3_u32 v196, v214, v215, v196\n\t" \
    "v_cmp_eq_u32 vcc, -1, v196\n\t" \
    "s_cbranch_vccz .Lxs3_%=\n\t" \
    "buffer_load_dwordx4 v[212:215], %[l16], %[rs], %[s0] offen offset:3072 sc1\n\t" \
    "s_mov_b32 %[a], 1\n\t" \
    ".Lxs3_%=:\n\t" \
    "v_max_u32 v196, v216, v217\n\t" \
    "v_max3_u32 v196, v218, v219, v196\n\t" \
    "v_cmp_eq_u32 vcc, -1, v196\n\t" \
    "s_cbranch_vccz .Lxs4_%=\n\t" \
    "buffer_load_dwordx4 v[216:219], %[l16], %[rs], %[s1] offen offset:0 sc1\n\t" \
    "s_mov_b32 %[a], 1\n\t" \
    ".Lxs4_%=:\n\t" \
    "v_max_u32 v196, v220, v221\n\t" \
    "v_max3_u32 v196, v222, v223, v196\n\t" \
    "v_cmp_eq_u32 vcc, -1, v196\n\t" \
    "s_cbranch_vccz .Lxs5_%=\n\t" \
    "buffer_load_dwordx4 v[220:223], %[l16], %[rs], %[s1] offen offset:1024 sc1\n\t" \
    "s_mov_b32 %[a], 1\n\t" \
    ".Lxs5_%=:\n\t" \
    "v_max_u32 v196, v224, v225\n\t" \
    "v_max3_u32 v196, v226, v227, v196\n\t" \
    "v_cmp_eq_u32 vcc, -1, v196\n\t" \
    "s_cbranch_vccz .Lxs6_%=\n\t" \
    "buffer_load_dwordx4 v[224:227], %[l16], %[rs], %[s1] offen offset:2048 sc1\n\t" \
    "s_mov_b32 %[a], 1\n\t" \
    ".Lxs6_%=:\n\t" \
    "v_max_u32 v196, v228, v229\n\t" \
    "v_max3_u32 v196, v230, v231, v196\n\t" \
    "v_cmp_eq_u32 vcc, -1, v196\n\t" \
    "s_cbranch_vccz .Lxs7_%=\n\t" \
    "buffer_load_dwordx4 v[228:231], %[l16], %[rs], %[s1] offen offset:3072 sc1\n\t" \
    "s_mov_b32 %[a], 1\n\t" \
    ".Lxs7_%=:\n\t" \
    "s_cmp_eq_u32 %[a], 0\n\t" \
    "s_cbranch_scc1 .Lxd%=\n\t" \
    "s_waitcnt vmcnt(0)\n\t" \
    "s_sub_u32 %[n], %[n], 1\n\t" \
    "s_cmp_lg_u32 %[n], 0\n\t" \
    "s_cbranch_scc1 .Lxl%=\n\t" \
    "s_branch .Lxe%=\n\t" \
    ".Lxd%=:\n\t" \
    ASM_ST2 \
    "v_mfma_f32_16x16x32_bf16 v[120:123], v[128:131], v[200:203], 0\n\t" \
    "v_mfma_f32_16x16x32_bf16 v[124:127], v[144:147], v[200:203], 0\n\t" \
    "v_mfma_f32_16x16x32_bf16 v[120:123], v[132:135], v[204:207], v[120:123]\n\t" \
    "v_mfma_f32_16x16x32_bf16 v[124:127], v[148:151], v[204:207], v[124:127]\n\t" \
    "v_mfma_f32_16x16x32_bf16 v[120:123], v[136:139], v[208:211], v[120:123]\n\t" \
    "v_mfma_f32_16x16x32_bf16 v[124:127], v[152:155], v[208:211], v[124:127]\n\t" \
    "v_mfma_f32_16x16x32_bf16 v[120:123], v[140:143], v[212:215], v[120:123]\n\t" \
    "v_mfma_f32_16x16x32_bf16 v[124:127], v[156:159], v[212:215], v[124:127]\n\t" \
    "v_mfma_f32_16x16x32_bf16 v[112:115], v[96:99], v[220:223], 0\n\t" \
    "v_mfma_f32_16x16x32_bf16 v[116:119], v[104:107], v[220:223], 0\n\t" \
    "v_mfma_f32_16x16x32_bf16 v[120:123], v[176:179], v[216:219], v[120:123]\n\t" \
    "v_mfma_f32_16x16x32_bf16 v[124:127], v[180:183], v[216:219], v[124:127]\n\t" \
    "v_mfma_f32_16x16x32_bf16 v[112:115], v[100:103], v[224:227], v[112:115]\n\t" \
    "v_mfma_f32_16x16x32_bf16 v[116:119], v[108:111], v[224:227], v[116:119]\n\t" \
    "v_mfma_f32_16x16x32_bf16 v[112:115], v[176:179], v[228:231], v[112:115]\n\t" \
    "v_mfma_f32_16x16x32_bf16 v[116:119], v[180:183], v[228:231], v[116:119]\n\t" \
    "s_nop 1\n\t" \
    "ds_write_b128 %[pw], v[120:123] offset:16384\n\t" \
    "ds_write_b128 %[pw], v[124:127] offset:24576\n\t" \
    "s_nop 3\n\t" \
    "ds_write_b128 %[pw], v[112:115] offset:0\n\t" \
    "ds_write_b128 %[pw], v[116:119] offset:8192\n\t" \
    "v_cmp_ge_u32 vcc, %[tn], v248\n\t" \
    "s_mov_b32 %[a], 0\n\t" \
    "s_cbranch_vccnz .Lxv%=\n\t" \
    "buffer_load_dwordx4 v[128:131], %[l16], %[rp], %[sv0] offen offset:0 sc1\n\t" \
    "buffer_load_dwordx4 v[132:135], %[l16], %[rp], %[sv0] offen offset:1024 sc1\n\t" \
    "buffer_load_dwordx4 v[136:139], %[l16], %[rp], %[sv0] offen offset:2048 sc1\n\t" \
    "buffer_load_dwordx4 v[140:143], %[l16], %[rp], %[sv0] offen offset:3072 sc1\n\t" \
    "buffer_load_dwordx4 v[144:147], %[l16], %[rp], %[sv1] offen offset:0 sc1\n\t" \
    "buffer_load_dwordx4 v[148:151], %[l16], %[rp], %[sv1] offen offset:1024 sc1\n\t" \
    "buffer_load_dwordx4 v[152:155], %[l16], %[rp], %[sv1] offen offset:2048 sc1\n\t" \
    "buffer_load_dwordx4 v[156:159], %[l16], %[rp], %[sv1] offen offset:3072 sc1\n\t" \
    "s_mov_b32 %[a], 1\n\t" \
    ".Lxv%=:\n\t" \
    "s_cmp_eq_u32 %[fin], 0\n\t" \
    "s_waitcnt lgkmcnt(0)\n\t" \
    "s_barrier\n\t" ASM_ST \
    "s_cbranch_scc1 .Lxe%=\n\t" \
    "ds_read_b128 v[200:203], %[pr] offset:0\n\t" \
    "ds_read_b128 v[204:207], %[pr] offset:1024\n\t" \
    "ds_read_b128 v[208:211], %[pr] offset:2048\n\t" \
    "ds_read_b128 v[212:215], %[pr] offset:3072\n\t" \
    "ds_read_b128 v[216:219], %[pr] offset:4096\n\t" \
    "ds_read_b128 v[220:223], %[pr] offset:5120\n\t" \
    "ds_read_b128 v[224:227], %[pr] offset:6144\n\t" \
    "ds_read_b128 v[228:231], %[pr] offset:7168\n\t" \
    "v_lshlrev_b32 v240, 16, v250\n\t" \
    "v_and_b32 v241, 0xffff0000, v250\n\t" \
    "v_lshlrev_b32 v242, 16, v251\n\t" \
    "v_and_b32 v243, 0xffff0000, v251\n\t" \
    "s_waitcnt lgkmcnt(6)\n\t" \
    "v_pk_add_f32 v[200:201], v[200:201], v[204:205]\n\t" \
    "v_pk_add_f32 v[202:203], v[202:203], v[206:207]\n\t" \
    "s_waitcnt lgkmcnt(4)\n\t" \
    "v_pk_add_f32 v[208:209], v[208:209], v[212:213]\n\t" \
    "v_pk_add_f32 v[210:211], v[210:211], v[214:215]\n\t" \
    "v_pk_add_f32 v[200:201], v[200:201], v[208:209]\n\t" \
    "v_pk_add_f32 v[202:203], v[202:203], v[210:211]\n\t" \
    "s_waitcnt lgkmcnt(2)\n\t" \
    "v_pk_add_f32 v[216:217], v[216:217], v[220:221]\n\t" \
    "v_pk_add_f32 v[218:219], v[218:219], v[222:223]\n\t" \
    "s_waitcnt lgkmcnt(0)\n\t" \
    "v_pk_add_f32 v[224:225], v[224:225], v[228:229]\n\t" \
    "v_pk_add_f32 v[226:227], v[226:227], v[230:231]\n\t" \
    "v_pk_add_f32 v[216:217], v[216:217], v[224:225]\n\t" \
    "v_pk_add_f32 v[218:219], v[218:219], v[226:227]\n\t" \
    "v_pk_add_f32 v[200:201], v[200:201], v[216:217]\n\t" \
    "v_pk_add_f32 v[202:203], v[202:203], v[218:219]\n\t" \
    "v_pk_add_f32 v[200:201], v[200:201], v[240:241]\n\t" \
    "v_pk_add_f32 v[202:203], v[202:203], v[242:243]\n\t" \
    "v_cvt_pk_bf16_f32 v200, v200, v201\n\t" \
    "v_cvt_pk_bf16_f32 v201, v202, v203\n\t" \
    "v_min_u32 v200, -2, v200\n\t" \
    "v_min_u32 v201, -2, v201\n\t" \
    "buffer_store_dwordx2 v[200:201], %[xo], %[rd], %[sa] offen sc1\n\t" \
    ".Lxe%=:\n\t"
#define ASM_W4_M \
    ".Lc7_%=:\n\t" \
    "s_waitcnt vmcnt(0)\n\t" \
    "v_max_u32 v212, v188, v189\n\t" \
    "v_max3_u32 v212, v190, v191, v212\n\t" \
    "v_cmp_eq_u32 vcc, -1, v212\n\t" \
    "s_cbranch_vccnz .Ls7_%=\n\t" \
    ".Lk7_%=:\n\t" \
    ASM_ST2 \
    "buffer_load_dwordx4 v[184:187], %[l16], %[rs], %[s1] offen offset:2048 sc1\n\t" \
    "buffer_load_dwordx4 v[180:183], %[l16], %[rs], %[s1] offen offset:1024 sc1\n\t" \
    "buffer_load_dwordx4 v[176:179], %[l16], %[rs], %[s1] offen offset:0 sc1\n\t" \
    "buffer_load_dwordx4 v[172:175], %[l16], %[rs], %[s0] offen offset:3072 sc1\n\t" \
    "buffer_load_dwordx4 v[168:171], %[l16], %[rs], %[s0] offen offset:2048 sc1\n\t" \
    "buffer_load_dwordx4 v[164:167], %[l16], %[rs], %[s0] offen offset:1024 sc1\n\t" \
    "buffer_load_dwordx4 v[160:163], %[l16], %[rs], %[s0] offen offset:0 sc1\n\t" \
    "v_mfma_f32_16x16x32_bf16 v[208:211], v[156:159], v[188:191], 0\n\t" \
    "v_mfma_f32_16x16x32_bf16 v[204:207], v[124:127], v[188:191], 0\n\t" \
    "v_mfma_f32_16x16x32_bf16 v[200:203], v[92:95], v[188:191], 0\n\t" \
    "v_mfma_f32_16x16x32_bf16 v[196:199], v[60:63], v[188:191], 0\n\t" \
    "v_mfma_f32_16x16x32_bf16 v[192:195], v[28:31], v[188:191], 0\n\t" \
    ".Lc6_%=:\n\t" \
    "s_waitcnt vmcnt(6)\n\t" \
    "v_max_u32 v212, v184, v185\n\t" \
    "v_max3_u32 v212, v186, v187, v212\n\t" \
    "v_cmp_eq_u32 vcc, -1, v212\n\t" \
    "s_cbranch_vccnz .Ls6_%=\n\t" \
    ".Lk6_%=:\n\t" \
    ".Lc5_%=:\n\t" \
    "s_waitcnt vmcnt(5)\n\t" \
    "v_max_u32 v212, v180, v181\n\t" \
    "v_max3_u32 v212, v182, v183, v212\n\t" \
    "v_cmp_eq_u32 vcc, -1, v212\n\t" \
    "s_cbranch_vccnz .Ls5_%=\n\t" \
    ".Lk5_%=:\n\t" \
    "v_mfma_f32_16x16x32_bf16 v[192:195], v[24:27], v[184:187], v[192:195]\n\t" \
    "v_mfma_f32_16x16x32_bf16 v[192:195], v[20:23], v[180:183], v[192:195]\n\t" \
    "v_mfma_f32_16x16x32_bf16 v[196:199], v[56:59], v[184:187], v[196:199]\n\t" \
    "v_mfma_f32_16x16x32_bf16 v[196:199], v[52:55], v[180:183], v[196:199]\n\t" \
    "v_mfma_f32_16x16x32_bf16 v[200:203], v[88:91], v[184:187], v[200:203]\n\t" \
    "v_mfma_f32_16x16x32_bf16 v[200:203], v[84:87], v[180:183], v[200:203]\n\t" \
    "v_mfma_f32_16x16x32_bf16 v[204:207], v[120:123], v[184:187], v[204:207]\n\t" \
    "v_mfma_f32_16x16x32_bf16 v[204:207], v[116:119], v[180:183], v[204:207]\n\t" \
    "v_mfma_f32_16x16x32_bf16 v[208:211], v[152:155], v[184:187], v[208:211]\n\t" \
    "v_mfma_f32_16x16x32_bf16 v[208:211], v[148:151], v[180:183], v[208:211]\n\t" \
    ".Lc4_%=:\n\t" \
    "s_waitcnt vmcnt(4)\n\t" \
    "v_max_u32 v212, v176, v177\n\t" \
    "v_max3_u32 v212, v178, v179, v212\n\t" \
    "v_cmp_eq_u32 vcc, -1, v212\n\t" \
    "s_cbranch_vccnz .Ls4_%=\n\t" \
    ".Lk4_%=:\n\t" \
    ".Lc3_%=:\n\t" \
    "s_waitcnt vmcnt(3)\n\t" \
    "v_max_u32 v212, v172, v173\n\t" \
    "v_max3_u32 v212, v174, v175, v212\n\t" \
    "v_cmp_eq_u32 vcc, -1, v212\n\t" \
    "s_cbranch_vccnz .Ls3_%=\n\t" \
    ".Lk3_%=:\n\t" \
    "v_mfma_f32_16x16x32_bf16 v[208:211], v[144:147], v[176:179], v[208:211]\n\t" \
    "v_mfma_f32_16x16x32_bf16 v[208:211], v[140:143], v[172:175], v[208:211]\n\t" \
    "v_mfma_f32_16x16x32_bf16 v[204:207], v[112:115], v[176:179], v[204:207]\n\t" \
    "v_mfma_f32_16x16x32_bf16 v[204:207], v[108:111], v[172:175], v[204:207]\n\t" \
    "v_mfma_f32_16x16x32_bf16 v[200:203], v[80:83], v[176:179], v[200:203]\n\t" \
    "v_mfma_f32_16x16x32_bf16 v[200:203], v[76:79], v[172:175], v[200:203]\n\t" \
    "v_mfma_f32_16x16x32_bf16 v[196:199], v[48:51], v[176:179], v[196:199]\n\t" \
    "v_mfma_f32_16x16x32_bf16 v[196:199], v[44:47], v[172:175], v[196:199]\n\t" \
    "v_mfma_f32_16x16x32_bf16 v[192:195], v[16:19], v[176:179], v[192:195]\n\t" \
    "v_mfma_f32_16x16x32_bf16 v[192:195], v[12:15], v[172:175], v[192:195]\n\t" \
    ".Lc2_%=:\n\t" \
    "s_waitcnt vmcnt(2)\n\t" \
    "v_max_u32 v212, v168, v169\n\t" \
    "v_max3_u32 v212, v170, v171, v212\n\t" \
    "v_cmp_eq_u32 vcc, -1, v212\n\t" \
    "s_cbranch_vccnz .Ls2_%=\n\t" \
    ".Lk2_%=:\n\t" \
    "buffer_load_dwordx4 v[188:191], %[l16], %[rs], %[n1] offen offset:3072 sc1\n\t" \
    ".Lc1_%=:\n\t" \
    "s_waitcnt vmcnt(2)\n\t" \
    "v_max_u32 v212, v164, v165\n\t" \
    "v_max3_u32 v212, v166, v167, v212\n\t" \
    "v_cmp_eq_u32 vcc, -1, v212\n\t" \
    "s_cbranch_vccnz .Ls1_%=\n\t" \
    ".Lk1_%=:\n\t" \
    ".Lc0_%=:\n\t" \
    "s_waitcnt vmcnt(1)\n\t" \
    "v_max_u32 v212, v160, v161\n\t" \
    "v_max3_u32 v212, v162, v163, v212\n\t" \
    "v_cmp_eq_u32 vcc, -1, v212\n\t" \
    "s_cbranch_vccnz .Ls0_%=\n\t" \
    ".Lk0_%=:\n\t" \
    "v_mfma_f32_16x16x32_bf16 v[192:195], v[8:11], v[168:171], v[192:195]\n\t" \
    "v_mfma_f32_16x16x32_bf16 v[192:195], v[4:7], v[164:167], v[192:195]\n\t" \
    "v_mfma_f32_16x16x32_bf16 v[192:195], v[0:3], v[160:163], v[192:195]\n\t" \
    "v_mfma_f32_16x16x32_bf16 v[196:199], v[40:43], v[168:171], v[196:199]\n\t" \
    "v_mfma_f32_16x16x32_bf16 v[196:199], v[36:39], v[164:167], v[196:199]\n\t" \
    "v_mfma_f32_16x16x32_bf16 v[196:199], v[32:35], v[160:163], v[196:199]\n\t" \
    "v_mfma_f32_16x16x32_bf16 v[200:203], v[72:75], v[168:171], v[200:203]\n\t" \
    "v_mfma_f32_16x16x32_bf16 v[200:203], v[68:71], v[164:167], v[200:203]\n\t" \
    "v_mfma_f32_16x16x32_bf16 v[200:203], v[64:67], v[160:163], v[200:203]\n\t" \
    "v_mfma_f32_16x16x32_bf16 v[204:207], v[104:107], v[168:171], v[204:207]\n\t" \
    "v_mfma_f32_16x16x32_bf16 v[204:207], v[100:103], v[164:167], v[204:207]\n\t" \
    "v_mfma_f32_16x16x32_bf16 v[204:207], v[96:99], v[160:163], v[204:207]\n\t" \
    "v_mfma_f32_16x16x32_bf16 v[208:211], v[136:139], v[168:171], v[208:211]\n\t" \
    "v_mfma_f32_16x16x32_bf16 v[208:211], v[132:135], v[164:167], v[208:211]\n\t" \
    "v_mfma_f32_16x16x32_bf16 v[208:211], v[128:131], v[160:163], v[208:211]\n\t" \
    "v_mov_b32 v212, %[un]\n\t" \
    "ds_write_b128 %[ld], v[192:195] offset:0\n\t" \
    "ds_write_b128 %[ld], v[196:199] offset:6144\n\t" \
    "ds_write_b128 %[ld], v[200:203] offset:12288\n\t" \
    "ds_write_b128 %[ld], v[204:207] offset:18432\n\t" \
    "s_nop 2\n\t" \
    "ds_write_b128 %[ld], v[208:211] offset:24576\n\t" \
    "ds_write_b32 %[fl], v212\n\t" \
    "s_branch .Lme%=\n\t" \
    ".Ls7_%=:\n\t" \
    "s_cmp_eq_u32 %[n], 0\n\t" \
    "s_cbranch_scc1 .Lk7_%=\n\t" \
    "s_sub_u32 %[n], %[n], 1\n\t" \
    "s_sleep 1\n\t" \
    "buffer_load_dwordx4 v[188:191], %[l16], %[rs], %[s1] offen offset:3072 sc1\n\t" \
    "s_branch .Lc7_%=\n\t" \
    ".Ls6_%=:\n\t" \
    "s_cmp_eq_u32 %[n], 0\n\t" \
    "s_cbranch_scc1 .Lk6_%=\n\t" \
    "s_sub_u32 %[n], %[n], 1\n\t" \
    "buffer_load_dwordx4 v[184:187], %[l16], %[rs], %[s1] offen offset:2048 sc1\n\t" \
    "s_waitcnt vmcnt(0)\n\t" \
    "s_branch .Lc6_%=\n\t" \
    ".Ls5_%=:\n\t" \
    "s_cmp_eq_u32 %[n], 0\n\t" \
    "s_cbranch_scc1 .Lk5_%=\n\t" \
    "s_sub_u32 %[n], %[n], 1\n\t" \
    "buffer_load_dwordx4 v[180:183], %[l16], %[rs], %[s1] offen offset:1024 sc1\n\t" \
    "s_waitcnt vmcnt(0)\n\t" \
    "s_branch .Lc5_%=\n\t" \
    ".Ls4_%=:\n\t" \
    "s_cmp_eq_u32 %[n], 0\n\t" \
    "s_cbranch_scc1 .Lk4_%=\n\t" \
    "s_sub_u32 %[n], %[n], 1\n\t" \
    "buffer_load_dwordx4 v[176:179], %[l16], %[rs], %[s1] offen offset:0 sc1\n\t" \
    "s_waitcnt vmcnt(0)\n\t" \
    "s_branch .Lc4_%=\n\t" \
    ".Ls3_%=:\n\t" \
    "s_cmp_eq_u32 %[n], 0\n\t" \
    "s_cbranch_scc1 .Lk3_%=\n\t" \
    "s_sub_u32 %[n], %[n], 1\n\t" \
    "buffer_load_dwordx4 v[172:175], %[l16], %[rs], %[s0] offen offset:3072 sc1\n\t" \
    "s_waitcnt vmcnt(0)\n\t" \
    "s_branch .Lc3_%=\n\t" \
    ".Ls2_%=:\n\t" \
    "s_cmp_eq_u32 %[n], 0\n\t" \
    "s_cbranch_scc1 .Lk2_%=\n\t" \
    "s_sub_u32 %[n], %[n], 1\n\t" \
    "buffer_load_dwordx4 v[168:171], %[l16], %[rs], %[s0] offen offset:2048 sc1\n\t" \
    "s_waitcnt vmcnt(0)\n\t" \
    "s_branch .Lc2_%=\n\t" \
    ".Ls1_%=:\n\t" \
    "s_cmp_eq_u32 %[n], 0\n\t" \
    "s_cbranch_scc1 .Lk1_%=\n\t" \
    "s_sub_u32 %[n], %[n], 1\n\t" \
    "buffer_load_dwordx4 v[164:167], %[l16], %[rs], %[s0] offen offset:1024 sc1\n\t" \
    "s_waitcnt vmcnt(0)\n\t" \
    "s_branch .Lc1_%=\n\t" \
    ".Ls0_%=:\n\t" \
    "s_cmp_eq_u32 %[n], 0\n\t" \
    "s_cbranch_scc1 .Lk0_%=\n\t" \
    "s_sub_u32 %[n], %[n], 1\n\t" \
    "buffer_load_dwordx4 v[160:163], %[l16], %[rs], %[s0] offen offset:0 sc1\n\t" \
    "s_waitcnt vmcnt(0)\n\t" \
    "s_branch .Lc0_%=\n\t" \
    ".Lme%=:\n\t"
#define ASM_W4F_0 \
    "s_branch .Lf0%=\n\t" \
    ".Lfr%=:\n\t" \
    "s_cmp_eq_u32 %[n], 0\n\t" \
    "s_cbranch_scc1 .Lfb%=\n\t" \
    "s_sub_u32 %[n], %[n], 1\n\t" \
    "s_sleep 6\n\t" \
    "buffer_load_dwordx2 v[164:165], %[o0], %[rd], %[c1] offen sc1\n\t" \
    "buffer_load_dwordx2 v[166:167], %[o1], %[rd], %[c1] offen sc1\n\t" \
    "buffer_load_dwordx2 v[168:169], %[o2], %[rd], %[c1] offen sc1\n\t" \
    "buffer_load_dwordx2 v[170:171], %[o3], %[rd], %[c1] offen sc1\n\t" \
    "buffer_load_dwordx2 v[172:173], %[o4], %[rd], %[c1] offen sc1\n\t" \
    "buffer_load_dwordx2 v[174:175], %[o0], %[rd], %[ca] offen sc1\n\t" \
    "buffer_load_dwordx2 v[176:177], %[o1], %[rd], %[ca] offen sc1\n\t" \
    "buffer_load_dwordx2 v[178:179], %[o2], %[rd], %[ca] offen sc1\n\t" \
    "buffer_load_dwordx2 v[180:181], %[o3], %[rd], %[ca] offen sc1\n\t" \
    "buffer_load_dwordx2 v[182:183], %[o4], %[rd], %[ca] offen sc1\n\t" \
    "s_waitcnt vmcnt(0)\n\t" \
    "s_branch .Lfa%=\n\t" \
    ".Lfs0_%=:\n\t" \
    "s_cmp_eq_u32 %[n], 0\n\t" \
    "s_cbranch_scc1 .Lfu0_%=\n\t" \
    "s_sub_u32 %[n], %[n], 1\n\t" \
    "s_sleep 1\n\t" \
    "s_branch .Lft0_%=\n\t" \
    ".Lfs1_%=:\n\t" \
    "s_cmp_eq_u32 %[n], 0\n\t" \
    "s_cbranch_scc1 .Lfu1_%=\n\t" \
    "s_sub_u32 %[n], %[n], 1\n\t" \
    "s_sleep 1\n\t" \
    "s_branch .Lft1_%=\n\t" \
    ".Lfx%=:\n\t" \
    "s_cmp_eq_u32 %[n], 0\n\t" \
    "s_cbranch_scc1 .Lfz%=\n\t" \
    "s_sub_u32 %[n], %[n], 1\n\t" \
    "s_branch .Lfy%=\n\t" \
    ".Lf0%=:\n\t" \
    "s_waitcnt vmcnt(1)\n\t" \
    ".Lfa%=:\n\t" \
    "v_max_u32 v194, v164, v165\n\t" \
    "v_max3_u32 v194, v166, v167, v194\n\t" \
    "v_max3_u32 v194, v168, v169, v194\n\t" \
    "v_max3_u32 v194, v170, v171, v194\n\t" \
    "v_max3_u32 v194, v172, v173, v194\n\t" \
    "v_max3_u32 v194, v174, v175, v194\n\t" \
    "v_max3_u32 v194, v176, v177, v194\n\t" \
    "v_max3_u32 v194, v178, v179, v194\n\t" \
    "v_max3_u32 v194, v180, v181, v194\n\t" \
    "v_max3_u32 v194, v182, v183, v194\n\t" \
    "v_cmp_eq_u32 vcc, -1, v194\n\t" \
    "s_cbranch_vccnz .Lfr%=\n\t" \
    ".Lfb%=:\n\t" \
    "v_lshlrev_b32 v64, 16, v164\n\t" \
    "v_and_b32 v65, 0xffff0000, v164\n\t" \
    "v_lshlrev_b32 v84, 16, v174\n\t" \
    "v_and_b32 v85, 0xffff0000, v174\n\t" \
    "v_lshlrev_b32 v66, 16, v165\n\t" \
    "v_and_b32 v67, 0xffff0000, v165\n\t" \
    "v_lshlrev_b32 v86, 16, v175\n\t" \
    "v_and_b32 v87, 0xffff0000, v175\n\t" \
    "v_lshlrev_b32 v68, 16, v166\n\t" \
    "v_and_b32 v69, 0xffff0000, v166\n\t" \
    "v_lshlrev_b32 v88, 16, v176\n\t" \
    "v_and_b32 v89, 0xffff0000, v176\n\t" \
    "v_lshlrev_b32 v70, 16, v167\n\t" \
    "v_and_b32 v71, 0xffff0000, v167\n\t" \
    "v_lshlrev_b32 v90, 16, v177\n\t" \
    "v_and_b32 v91, 0xffff0000, v177\n\t" \
    "v_lshlrev_b32 v72, 16, v168\n\t" \
    "v_and_b32 v73, 0xffff0000, v168\n\t" \
    "v_lshlrev_b32 v92, 16, v178\n\t" \
    "v_and_b32 v93, 0xffff0000, v178\n\t" \
    "v_lshlrev_b32 v74, 16, v169\n\t" \
    "v_and_b32 v75, 0xffff0000, v169\n\t" \
    "v_lshlrev_b32 v94, 16, v179\n\t" \
    "v_and_b32 v95, 0xffff0000, v179\n\t" \
    "v_lshlrev_b32 v76, 16, v170\n\t" \
    "v_and_b32 v77, 0xffff0000, v170\n\t" \
    "v_lshlrev_b32 v96, 16, v180\n\t" \
    "v_and_b32 v97, 0xffff0000, v180\n\t" \
    "v_lshlrev_b32 v78, 16, v171\n\t" \
    "v_and_b32 v79, 0xffff0000, v171\n\t" \
    "v_lshlrev_b32 v98, 16, v181\n\t" \
    "v_and_b32 v99, 0xffff0000, v181\n\t" \
    "v_lshlrev_b32 v80, 16, v172\n\t" \
    "v_and_b32 v81, 0xffff0000, v172\n\t" \
    "v_lshlrev_b32 v100, 16, v182\n\t" \
    "v_and_b32 v101, 0xffff0000, v182\n\t" \
    "v_lshlrev_b32 v82, 16, v173\n\t" \
    "v_and_b32 v83, 0xffff0000, v173\n\t" \
    "v_lshlrev_b32 v102, 16, v183\n\t" \
    "v_and_b32 v103, 0xffff0000, v183\n\t" \
    "v_pk_add_f32 v[64:65], v[64:65], v[84:85]\n\t" \
    "v_pk_add_f32 v[66:67], v[66:67], v[86:87]\n\t" \
    "v_pk_add_f32 v[68:69], v[68:69], v[88:89]\n\t" \
    "v_pk_add_f32 v[70:71], v[70:71], v[90:91]\n\t" \
    "v_pk_add_f32 v[72:73], v[72:73], v[92:93]\n\t" \
    "v_pk_add_f32 v[74:75], v[74:75], v[94:95]\n\t" \
    "v_pk_add_f32 v[76:77], v[76:77], v[96:97]\n\t" \
    "v_pk_add_f32 v[78:79], v[78:79], v[98:99]\n\t" \
    "v_pk_add_f32 v[80:81], v[80:81], v[100:101]\n\t" \
    "v_pk_add_f32 v[82:83], v[82:83], v[102:103]\n\t" \
    ".Lft0_%=:\n\t" \
    "ds_read_b32 v194, %[f0]\n\t" \
    "s_waitcnt lgkmcnt(0)\n\t" \
    "v_cmp_ge_u32 vcc, %[u], v194\n\t" \
    "s_cbranch_vccnz .Lfs0_%=\n\t" \
    ".Lfu0_%=:\n\t" \
    "ds_read_b128 v[84:87], %[la] offset:0\n\t" \
    "ds_read_b128 v[88:91], %[la] offset:6144\n\t" \
    "ds_read_b128 v[92:95], %[la] offset:12288\n\t" \
    "ds_read_b128 v[96:99], %[la] offset:18432\n\t" \
    "ds_read_b128 v[100:103], %[la] offset:24576\n\t" \
    "ds_read_b128 v[104:107], %[la] offset:1024\n\t" \
    "ds_read_b128 v[108:111], %[la] offset:7168\n\t" \
    "ds_read_b128 v[112:115], %[la] offset:13312\n\t" \
    "ds_read_b128 v[116:119], %[la] offset:19456\n\t" \
    "ds_read_b128 v[120:123], %[la] offset:25600\n\t" \
    "ds_read_b128 v[124:127], %[la] offset:2048\n\t" \
    "s_waitcnt lgkmcnt(10)\n\t" \
    "v_pk_add_f32 v[64:65], v[64:65], v[84:85]\n\t" \
    "v_pk_add_f32 v[66:67], v[66:67], v[86:87]\n\t" \
    "ds_read_b128 v[128:131], %[la] offset:8192\n\t" \
    "s_waitcnt lgkmcnt(10)\n\t" \
    "v_pk_add_f32 v[68:69], v[68:69], v[88:89]\n\t" \
    "v_pk_add_f32 v[70:71], v[70:71], v[90:91]\n\t" \
    "ds_read_b128 v[132:135], %[la] offset:14336\n\t" \
    "s_waitcnt lgkmcnt(10)\n\t" \
    "v_pk_add_f32 v[72:73], v[72:73], v[92:93]\n\t" \
    "v_pk_add_f32 v[74:75], v[74:75], v[94:95]\n\t" \
    "ds_read_b128 v[136:139], %[la] offset:20480\n\t" \
    "s_waitcnt lgkmcnt(10)\n\t" \
    "v_pk_add_f32 v[76:77], v[76:77], v[96:97]\n\t" \
    "v_pk_add_f32 v[78:79], v[78:79], v[98:99]\n\t" \
    "ds_read_b128 v[140:143], %[la] offset:26624\n\t" \
    "s_waitcnt lgkmcnt(10)\n\t" \
    "v_pk_add_f32 v[80:81], v[80:81], v[100:101]\n\t" \
    "v_pk_add_f32 v[82:83], v[82:83], v[102:103]\n\t" \
    "ds_read_b128 v[144:147], %[la] offset:3072\n\t" \
    "s_waitcnt lgkmcnt(10)\n\t" \
    "v_pk_add_f32 v[64:65], v[64:65], v[104:105]\n\t" \
    "v_pk_add_f32 v[66:67], v[66:67], v[106:107]\n\t" \
    "ds_read_b128 v[148:151], %[la] offset:9216\n\t" \
    "s_waitcnt lgkmcnt(10)\n\t" \
    "v_pk_add_f32 v[68:69], v[68:69], v[108:109]\n\t" \
    "v_pk_add_f32 v[70:71], v[70:71], v[110:111]\n\t" \
    "ds_read_b128 v[152:155], %[la] offset:15360\n\t" \
    "s_waitcnt lgkmcnt(10)\n\t" \
    "v_pk_add_f32 v[72:73], v[72:73], v[112:113]\n\t" \
    "v_pk_add_f32 v[74:75], v[74:75], v[114:115]\n\t" \
    "ds_read_b128 v[156:159], %[la] offset:21504\n\t" \
    "s_waitcnt lgkmcnt(10)\n\t" \
    "v_pk_add_f32 v[76:77], v[76:77], v[116:117]\n\t" \
    "v_pk_add_f32 v[78:79], v[78:79], v[118:119]\n\t" \
    "ds_read_b128 v[160:163], %[la] offset:27648\n\t" \
    "s_waitcnt lgkmcnt(10)\n\t" \
    "v_pk_add_f32 v[80:81], v[80:81], v[120:121]\n\t" \
    "v_pk_add_f32 v[82:83], v[82:83], v[122:123]\n\t" \
    "s_waitcnt lgkmcnt(9)\n\t" \
    "v_pk_add_f32 v[64:65], v[64:65], v[124:125]\n\t" \
    "v_pk_add_f32 v[66:67], v[66:67], v[126:127]\n\t" \
    "s_waitcnt lgkmcnt(8)\n\t" \
    "v_pk_add_f32 v[68:69], v[68:69], v[128:129]\n\t" \
    "v_pk_add_f32 v[70:71], v[70:71], v[130:131]\n\t" \
    "s_waitcnt lgkmcnt(7)\n\t" \
    "v_pk_add_f32 v[72:73], v[72:73], v[132:133]\n\t" \
    "v_pk_add_f32 v[74:75], v[74:75], v[134:135]\n\t" \
    "s_waitcnt lgkmcnt(6)\n\t" \
    "v_pk_add_f32 v[76:77], v[76:77], v[136:137]\n\t" \
    "v_pk_add_f32 v[78:79], v[78:79], v[138:139]\n\t" \
    "s_waitcnt lgkmcnt(5)\n\t" \
    "v_pk_add_f32 v[80:81], v[80:81], v[140:141]\n\t" \
    "v_pk_add_f32 v[82:83], v[82:83], v[142:143]\n\t" \
    "s_waitcnt lgkmcnt(4)\n\t" \
    "v_pk_add_f32 v[64:65], v[64:65], v[144:145]\n\t" \
    "v_pk_add_f32 v[66:67], v[66:67], v[146:147]\n\t" \
    "s_waitcnt lgkmcnt(3)\n\t" \
    "v_pk_add_f32 v[68:69], v[68:69], v[148:149]\n\t" \
    "v_pk_add_f32 v[70:71], v[70:71], v[150:151]\n\t" \
    "s_waitcnt lgkmcnt(2)\n\t" \
    "v_pk_add_f32 v[72:73], v[72:73], v[152:153]\n\t" \
    "v_pk_add_f32 v[74:75], v[74:75], v[154:155]\n\t" \
    "s_waitcnt lgkmcnt(1)\n\t" \
    "v_pk_add_f32 v[76:77], v[76:77], v[156:157]\n\t" \
    "v_pk_add_f32 v[78:79], v[78:79], v[158:159]\n\t" \
    "s_waitcnt lgkmcnt(0)\n\t" \
    "v_pk_add_f32 v[80:81], v[80:81], v[160:161]\n\t" \
    "v_pk_add_f32 v[82:83], v[82:83], v[162:163]\n\t" \
    ".Lft1_%=:\n\t" \
    "ds_read_b32 v194, %[f1]\n\t" \
    "s_waitcnt lgkmcnt(0)\n\t" \
    "v_cmp_ge_u32 vcc, %[u], v194\n\t" \
    "s_cbranch_vccnz .Lfs1_%=\n\t" \
    ".Lfu1_%=:\n\t" \
    ASM_ST2 \
    "ds_read_b128 v[84:87], %[la] offset:4096\n\t" \
    "ds_read_b128 v[88:91], %[la] offset:10240\n\t" \
    "ds_read_b128 v[92:95], %[la] offset:16384\n\t" \
    "ds_read_b128 v[96:99], %[la] offset:22528\n\t" \
    "ds_read_b128 v[100:103], %[la] offset:28672\n\t" \
    "ds_read_b128 v[104:107], %[la] offset:5120\n\t" \
    "ds_read_b128 v[108:111], %[la] offset:11264\n\t" \
    "ds_read_b128 v[112:115], %[la] offset:17408\n\t" \
    "ds_read_b128 v[116:119], %[la] offset:23552\n\t" \
    "ds_read_b128 v[120:123], %[la] offset:29696\n\t" \
    "v_mov_b32 v194, %[un]\n\t" \
    "ds_write_b32 %[fd], v194\n\t" \
    "s_waitcnt lgkmcnt(10)\n\t" \
    "v_pk_add_f32 v[64:65], v[64:65], v[84:85]\n\t" \
    "v_pk_add_f32 v[66:67], v[66:67], v[86:87]\n\t" \
    "s_waitcnt lgkmcnt(9)\n\t" \
    "v_pk_add_f32 v[68:69], v[68:69], v[88:89]\n\t" \
    "v_pk_add_f32 v[70:71], v[70:71], v[90:91]\n\t" \
    "s_waitcnt lgkmcnt(8)\n\t" \
    "v_pk_add_f32 v[72:73], v[72:73], v[92:93]\n\t" \
    "v_pk_add_f32 v[74:75], v[74:75], v[94:95]\n\t" \
    "s_waitcnt lgkmcnt(7)\n\t" \
    "v_pk_add_f32 v[76:77], v[76:77], v[96:97]\n\t" \
    "v_pk_add_f32 v[78:79], v[78:79], v[98:99]\n\t" \
    "s_waitcnt lgkmcnt(6)\n\t" \
    "v_pk_add_f32 v[80:81], v[80:81], v[100:101]\n\t" \
    "v_pk_add_f32 v[82:83], v[82:83], v[102:103]\n\t" \
    "s_waitcnt lgkmcnt(5)\n\t" \
    "v_pk_add_f32 v[64:65], v[64:65], v[104:105]\n\t" \
    "v_pk_add_f32 v[66:67], v[66:67], v[106:107]\n\t" \
    "s_waitcnt lgkmcnt(4)\n\t" \
    "v_pk_add_f32 v[68:69], v[68:69], v[108:109]\n\t" \
    "v_pk_add_f32 v[70:71], v[70:71], v[110:111]\n\t" \
    "s_waitcnt lgkmcnt(3)\n\t" \
    "v_pk_add_f32 v[72:73], v[72:73], v[112:113]\n\t" \
    "v_pk_add_f32 v[74:75], v[74:75], v[114:115]\n\t" \
    "s_waitcnt lgkmcnt(2)\n\t" \
    "v_pk_add_f32 v[76:77], v[76:77], v[116:117]\n\t" \
    "v_pk_add_f32 v[78:79], v[78:79], v[118:119]\n\t" \
    "s_waitcnt lgkmcnt(1)\n\t" \
    "v_pk_add_f32 v[80:81], v[80:81], v[120:121]\n\t" \
    "v_pk_add_f32 v[82:83], v[82:83], v[122:123]\n\t" \
    "v_pk_add_f32 v[84:85], v[64:65], v[66:67]\n\t" \
    "v_pk_mul_f32 v[88:89], v[64:65], v[64:65]\n\t" \
    "v_pk_add_f32 v[86:87], v[68:69], v[70:71]\n\t" \
    "v_pk_mul_f32 v[90:91], v[66:67], v[66:67]\n\t" \
    "v_pk_fma_f32 v[88:89], v[68:69], v[68:69], v[88:89]\n\t" \
    "v_pk_fma_f32 v[90:91], v[70:71], v[70:71], v[90:91]\n\t" \
    "v_pk_add_f32 v[84:85], v[84:85], v[72:73]\n\t" \
    "v_pk_fma_f32 v[88:89], v[72:73], v[72:73], v[88:89]\n\t" \
    "v_pk_add_f32 v[86:87], v[86:87], v[74:75]\n\t" \
    "v_pk_fma_f32 v[90:91], v[74:75], v[74:75], v[90:91]\n\t" \
    "v_pk_add_f32 v[84:85], v[84:85], v[76:77]\n\t" \
    "v_pk_fma_f32 v[88:89], v[76:77], v[76:77], v[88:89]\n\t" \
    "v_pk_add_f32 v[86:87], v[86:87], v[78:79]\n\t" \
    "v_pk_fma_f32 v[90:91], v[78:79], v[78:79], v[90:91]\n\t" \
    "v_pk_add_f32 v[84:85], v[84:85], v[80:81]\n\t" \
    "v_pk_fma_f32 v[88:89], v[80:81], v[80:81], v[88:89]\n\t" \
    "v_pk_add_f32 v[86:87], v[86:87], v[82:83]\n\t" \
    "v_pk_fma_f32 v[90:91], v[82:83], v[82:83], v[90:91]\n\t" \
    "v_pk_add_f32 v[84:85], v[84:85], v[86:87]\n\t" \
    "v_pk_add_f32 v[88:89], v[88:89], v[90:91]\n\t" \
    "v_add_f32 v194, v84, v85\n\t" \
    "v_add_f32 v195, v88, v89\n\t" \
    "s_nop 1\n\t" \
    "v_permlane16_swap_b32 v194, v195\n\t" \
    "v_add_f32 v196, v194, v195\n\t" \
    "v_add_f32 v197, v194, v195\n\t" \
    "s_nop 1\n\t" \
    "v_permlane32_swap_b32 v196, v197\n\t" \
    "v_add_f32 v194, v196, v197\n\t" \
    "v_add_f32 v195, v196, v197\n\t" \
    "s_nop 1\n\t" \
    "v_permlane16_swap_b32 v194, v195\n\t" \
    "v_min_u32 v194, -2, v194\n\t" \
    "v_min_u32 v195, -2, v195\n\t" \
    "buffer_store_dwordx2 v[194:195], %[so], %[rr], %[sp] offen sc1\n\t" \
    ASM_ST3 \
    "v_cvt_pk_bf16_f32 v184, v64, v65\n\t" \
    "v_cvt_pk_bf16_f32 v185, v66, v67\n\t" \
    "v_cvt_pk_bf16_f32 v186, v68, v69\n\t" \
    "v_cvt_pk_bf16_f32 v187, v70, v71\n\t" \
    "v_cvt_pk_bf16_f32 v188, v72, v73\n\t" \
    "v_cvt_pk_bf16_f32 v189, v74, v75\n\t" \
    "v_cvt_pk_bf16_f32 v190, v76, v77\n\t" \
    "v_cvt_pk_bf16_f32 v191, v78, v79\n\t" \
    "v_cvt_pk_bf16_f32 v192, v80, v81\n\t" \
    "v_cvt_pk_bf16_f32 v193, v82, v83\n\t" \
    "v_permlane16_swap_b32 v184, v186\n\t" \
    "v_permlane16_swap_b32 v185, v187\n\t" \
    "v_permlane16_swap_b32 v188, v190\n\t" \
    "v_permlane16_swap_b32 v189, v191\n\t" \
    W4SLEEP \
    ".Lfy%=:\n\t" \
    "buffer_load_dwordx2 v[196:197], %[si], %[rr], %[sq] offen sc1\n\t" \
    "buffer_load_dwordx2 v[198:199], %[si], %[rr], %[sq] offen offset:512 sc1\n\t" \
    "s_waitcnt vmcnt(0)\n\t" \
    "v_max_u32 v200, v196, v197\n\t" \
    "v_max3_u32 v200, v198, v199, v200\n\t" \
    "v_cmp_eq_u32 vcc, -1, v200\n\t" \
    "s_cbranch_vccnz .Lfx%=\n\t" \
    ".Lfz%=:\n\t" \
    ASM_ST \
    "v_add_f32 v194, v196, v198\n\t" \
    "v_add_f32 v195, v197, v199\n\t" \
    "buffer_store_dwordx4 v[184:187], %[pa], %[rd], %[xr] offen sc1\n\t" \
    "buffer_store_dwordx4 v[188:191], %[pb], %[rd], %[xr] offen sc1\n\t" \
    "buffer_store_dwordx2 v[192:193], %[o4], %[rd], %[xr] offen sc1\n\t" \
    "s_nop 1\n\t" \
    "v_permlane16_swap_b32 v194, v195\n\t" \
    "v_add_f32 v196, v194, v195\n\t" \
    "v_add_f32 v197, v194, v195\n\t" \
    "s_nop 1\n\t" \
    "v_permlane32_swap_b32 v196, v197\n\t" \
    "v_add_f32 v194, v196, v197\n\t" \
    "v_add_f32 v195, v196, v197\n\t" \
    "s_nop 1\n\t" \
    "v_permlane16_swap_b32 v194, v195\n\t" \
    "v_mul_f32 v194, %[kd], v194\n\t" \
    "v_mul_f32 v195, %[kd], v195\n\t" \
    "v_fma_f32 v195, -v194, v194, v195\n\t" \
    "v_max_f32 v195, 0, v195\n\t" \
    "v_cvt_pk_bf16_f32 v195, v195, v195\n\t" \
    "v_lshlrev_b32 v195, 16, v195\n\t" \
    "v_add_f32 v195, %[ke], v195\n\t" \
    "v_cvt_pk_bf16_f32 v195, v195, v195\n\t" \
    "v_lshlrev_b32 v195, 16, v195\n\t" \
    "v_sqrt_f32 v195, v195\n\t" \
    "v_cvt_pk_bf16_f32 v194, v194, v194\n\t" \
    "v_lshlrev_b32 v194, 16, v194\n\t" \
    "v_cvt_pk_bf16_f32 v195, v195, v195\n\t" \
    "v_lshlrev_b32 v195, 16, v195\n\t" \
    "v_rcp_f32 v196, v195\n\t" \
    "v_rcp_f32 v197, v195\n\t" \
    "s_nop 0\n\t" \
    "v_mul_f32 v198, -v194, v196\n\t" \
    "v_mul_f32 v199, -v194, v196\n\t" \
    "v_pk_fma_f32 v[64:65], v[64:65], v[196:197], v[198:199]\n\t" \
    "v_pk_fma_f32 v[66:67], v[66:67], v[196:197], v[198:199]\n\t" \
    "v_pk_fma_f32 v[68:69], v[68:69], v[196:197], v[198:199]\n\t" \
    "v_pk_fma_f32 v[70:71], v[70:71], v[196:197], v[198:199]\n\t" \
    "v_pk_fma_f32 v[72:73], v[72:73], v[196:197], v[198:199]\n\t" \
    "v_pk_fma_f32 v[74:75], v[74:75], v[196:197], v[198:199]\n\t" \
    "v_pk_fma_f32 v[76:77], v[76:77], v[196:197], v[198:199]\n\t" \
    "v_pk_fma_f32 v[78:79], v[78:79], v[196:197], v[198:199]\n\t" \
    "v_pk_fma_f32 v[80:81], v[80:81], v[196:197], v[198:199]\n\t" \
    "v_pk_fma_f32 v[82:83], v[82:83], v[196:197], v[198:199]\n\t" \
    "v_cvt_pk_bf16_f32 v184, v64, v65\n\t" \
    "v_cvt_pk_bf16_f32 v185, v66, v67\n\t" \
    "v_cvt_pk_bf16_f32 v186, v68, v69\n\t" \
    "v_cvt_pk_bf16_f32 v187, v70, v71\n\t" \
    "v_cvt_pk_bf16_f32 v188, v72, v73\n\t" \
    "v_cvt_pk_bf16_f32 v189, v74, v75\n\t" \
    "v_cvt_pk_bf16_f32 v190, v76, v77\n\t" \
    "v_cvt_pk_bf16_f32 v191, v78, v79\n\t" \
    "v_cvt_pk_bf16_f32 v192, v80, v81\n\t" \
    "v_cvt_pk_bf16_f32 v193, v82, v83\n\t" \
    "v_permlane16_swap_b32 v184, v186\n\t" \
    "v_permlane16_swap_b32 v185, v187\n\t" \
    "v_permlane16_swap_b32 v188, v190\n\t" \
    "v_permlane16_swap_b32 v189, v191\n\t" \
    "buffer_store_dwordx4 v[184:187], %[pa], %[rd], %[xn] offen sc1\n\t" \
    "buffer_store_dwordx4 v[188:191], %[pb], %[rd], %[xn] offen sc1\n\t" \
    "buffer_store_dwordx2 v[192:193], %[o4], %[rd], %[xn] offen sc1\n\t" \
    "buffer_load_dwordx2 v[164:165], %[o0], %[rd], %[n1] offen sc1\n\t" \
    "buffer_load_dwordx2 v[166:167], %[o1], %[rd], %[n1] offen sc1\n\t" \
    "buffer_load_dwordx2 v[168:169], %[o2], %[rd], %[n1] offen sc1\n\t" \
    "buffer_load_dwordx2 v[170:171], %[o3], %[rd], %[n1] offen sc1\n\t" \
    "buffer_load_dwordx2 v[172:173], %[o4], %[rd], %[n1] offen sc1\n\t" \
    "buffer_load_dwordx2 v[174:175], %[o0], %[rd], %[na] offen sc1\n\t" \
    "buffer_load_dwordx2 v[176:177], %[o1], %[rd], %[na] offen sc1\n\t" \
    "buffer_load_dwordx2 v[178:179], %[o2], %[rd], %[na] offen sc1\n\t" \
    "buffer_load_dwordx2 v[180:181], %[o3], %[rd], %[na] offen sc1\n\t" \
    "buffer_load_dwordx2 v[182:183], %[o4], %[rd], %[na] offen sc1\n\t" \
    "v_mov_b32 v196, %[tn]\n\t" \
    "v_mov_b32 v197, %[tn]\n\t" \
    "v_mov_b32 v198, %[tn]\n\t" \
    "v_mov_b32 v199, %[tn]\n\t" \
    "s_waitcnt vmcnt(10)\n\t" \
    "buffer_store_dwordx4 v[196:199], %[fo], %[rn], %[fs] offen sc1\n\t" \
    "s_nop 1\n\t"
#define ASM_W4F_1 \
    "s_branch .Lf0%=\n\t" \
    ".Lfr%=:\n\t" \
    "s_cmp_eq_u32 %[n], 0\n\t" \
    "s_cbranch_scc1 .Lfb%=\n\t" \
    "s_sub_u32 %[n], %[n], 1\n\t" \
    "s_sleep 6\n\t" \
    "buffer_load_dwordx2 v[164:165], %[o0], %[rd], %[c1] offen sc1\n\t" \
    "buffer_load_dwordx2 v[166:167], %[o1], %[rd], %[c1] offen sc1\n\t" \
    "buffer_load_dwordx2 v[168:169], %[o2], %[rd], %[c1] offen sc1\n\t" \
    "buffer_load_dwordx2 v[170:171], %[o3], %[rd], %[c1] offen sc1\n\t" \
    "buffer_load_dwordx2 v[172:173], %[o4], %[rd], %[c1] offen sc1\n\t" \
    "buffer_load_dwordx2 v[174:175], %[o0], %[rd], %[ca] offen sc1\n\t" \
    "buffer_load_dwordx2 v[176:177], %[o1], %[rd], %[ca] offen sc1\n\t" \
    "buffer_load_dwordx2 v[178:179], %[o2], %[rd], %[ca] offen sc1\n\t" \
    "buffer_load_dwordx2 v[180:181], %[o3], %[rd], %[ca] offen sc1\n\t" \
    "buffer_load_dwordx2 v[182:183], %[o4], %[rd], %[ca] offen sc1\n\t" \
    "s_waitcnt vmcnt(0)\n\t" \
    "s_branch .Lfa%=\n\t" \
    ".Lfs0_%=:\n\t" \
    "s_cmp_eq_u32 %[n], 0\n\t" \
    "s_cbranch_scc1 .Lfu0_%=\n\t" \
    "s_sub_u32 %[n], %[n], 1\n\t" \
    "s_sleep 1\n\t" \
    "s_branch .Lft0_%=\n\t" \
    ".Lfs1_%=:\n\t" \
    "s_cmp_eq_u32 %[n], 0\n\t" \
    "s_cbranch_scc1 .Lfu1_%=\n\t" \
    "s_sub_u32 %[n], %[n], 1\n\t" \
    "s_sleep 1\n\t" \
    "s_branch .Lft1_%=\n\t" \
    ".Lf0%=:\n\t" \
    "s_waitcnt vmcnt(0)\n\t" \
    ".Lfa%=:\n\t" \
    "v_max_u32 v194, v164, v165\n\t" \
    "v_max3_u32 v194, v166, v167, v194\n\t" \
    "v_max3_u32 v194, v168, v169, v194\n\t" \
    "v_max3_u32 v194, v170, v171, v194\n\t" \
    "v_max3_u32 v194, v172, v173, v194\n\t" \
    "v_max3_u32 v194, v174, v175, v194\n\t" \
    "v_max3_u32 v194, v176, v177, v194\n\t" \
    "v_max3_u32 v194, v178, v179, v194\n\t" \
    "v_max3_u32 v194, v180, v181, v194\n\t" \
    "v_max3_u32 v194, v182, v183, v194\n\t" \
    "v_cmp_eq_u32 vcc, -1, v194\n\t" \
    "s_cbranch_vccnz .Lfr%=\n\t" \
    ".Lfb%=:\n\t" \
    "v_lshlrev_b32 v64, 16, v164\n\t" \
    "v_and_b32 v65, 0xffff0000, v164\n\t" \
    "v_lshlrev_b32 v84, 16, v174\n\t" \
    "v_and_b32 v85, 0xffff0000, v174\n\t" \
    "v_lshlrev_b32 v66, 16, v165\n\t" \
    "v_and_b32 v67, 0xffff0000, v165\n\t" \
    "v_lshlrev_b32 v86, 16, v175\n\t" \
    "v_and_b32 v87, 0xffff0000, v175\n\t" \
    "v_lshlrev_b32 v68, 16, v166\n\t" \
    "v_and_b32 v69, 0xffff0000, v166\n\t" \
    "v_lshlrev_b32 v88, 16, v176\n\t" \
    "v_and_b32 v89, 0xffff0000, v176\n\t" \
    "v_lshlrev_b32 v70, 16, v167\n\t" \
    "v_and_b32 v71, 0xffff0000, v167\n\t" \
    "v_lshlrev_b32 v90, 16, v177\n\t" \
    "v_and_b32 v91, 0xffff0000, v177\n\t" \
    "v_lshlrev_b32 v72, 16, v168\n\t" \
    "v_and_b32 v73, 0xffff0000, v168\n\t" \
    "v_lshlrev_b32 v92, 16, v178\n\t" \
    "v_and_b32 v93, 0xffff0000, v178\n\t" \
    "v_lshlrev_b32 v74, 16, v169\n\t" \
    "v_and_b32 v75, 0xffff0000, v169\n\t" \
    "v_lshlrev_b32 v94, 16, v179\n\t" \
    "v_and_b32 v95, 0xffff0000, v179\n\t" \
    "v_lshlrev_b32 v76, 16, v170\n\t" \
    "v_and_b32 v77, 0xffff0000, v170\n\t" \
    "v_lshlrev_b32 v96, 16, v180\n\t" \
    "v_and_b32 v97, 0xffff0000, v180\n\t" \
    "v_lshlrev_b32 v78, 16, v171\n\t" \
    "v_and_b32 v79, 0xffff0000, v171\n\t" \
    "v_lshlrev_b32 v98, 16, v181\n\t" \
    "v_and_b32 v99, 0xffff0000, v181\n\t" \
    "v_lshlrev_b32 v80, 16, v172\n\t" \
    "v_and_b32 v81, 0xffff0000, v172\n\t" \
    "v_lshlrev_b32 v100, 16, v182\n\t" \
    "v_and_b32 v101, 0xffff0000, v182\n\t" \
    "v_lshlrev_b32 v82, 16, v173\n\t" \
    "v_and_b32 v83, 0xffff0000, v173\n\t" \
    "v_lshlrev_b32 v102, 16, v183\n\t" \
    "v_and_b32 v103, 0xffff0000, v183\n\t" \
    "v_pk_add_f32 v[64:65], v[64:65], v[84:85]\n\t" \
    "v_pk_add_f32 v[66:67], v[66:67], v[86:87]\n\t" \
    "v_pk_add_f32 v[68:69], v[68:69], v[88:89]\n\t" \
    "v_pk_add_f32 v[70:71], v[70:71], v[90:91]\n\t" \
    "v_pk_add_f32 v[72:73], v[72:73], v[92:93]\n\t" \
    "v_pk_add_f32 v[74:75], v[74:75], v[94:95]\n\t" \
    "v_pk_add_f32 v[76:77], v[76:77], v[96:97]\n\t" \
    "v_pk_add_f32 v[78:79], v[78:79], v[98:99]\n\t" \
    "v_pk_add_f32 v[80:81], v[80:81], v[100:101]\n\t" \
    "v_pk_add_f32 v[82:83], v[82:83], v[102:103]\n\t" \
    ".Lft0_%=:\n\t" \
    "ds_read_b32 v194, %[f0]\n\t" \
    "s_waitcnt lgkmcnt(0)\n\t" \
    "v_cmp_ge_u32 vcc, %[u], v194\n\t" \
    "s_cbranch_vccnz .Lfs0_%=\n\t" \
    ".Lfu0_%=:\n\t" \
    "ds_read_b128 v[84:87], %[la] offset:0\n\t" \
    "ds_read_b128 v[88:91], %[la] offset:6144\n\t" \
    "ds_read_b128 v[92:95], %[la] offset:12288\n\t" \
    "ds_read_b128 v[96:99], %[la] offset:18432\n\t" \
    "ds_read_b128 v[100:103], %[la] offset:24576\n\t" \
    "ds_read_b128 v[104:107], %[la] offset:1024\n\t" \
    "ds_read_b128 v[108:111], %[la] offset:7168\n\t" \
    "ds_read_b128 v[112:115], %[la] offset:13312\n\t" \
    "ds_read_b128 v[116:119], %[la] offset:19456\n\t" \
    "ds_read_b128 v[120:123], %[la] offset:25600\n\t" \
    "ds_read_b128 v[124:127], %[la] offset:2048\n\t" \
    "s_waitcnt lgkmcnt(10)\n\t" \
    "v_pk_add_f32 v[64:65], v[64:65], v[84:85]\n\t" \
    "v_pk_add_f32 v[66:67], v[66:67], v[86:87]\n\t" \
    "ds_read_b128 v[128:131], %[la] offset:8192\n\t" \
    "s_waitcnt lgkmcnt(10)\n\t" \
    "v_pk_add_f32 v[68:69], v[68:69], v[88:89]\n\t" \
    "v_pk_add_f32 v[70:71], v[70:71], v[90:91]\n\t" \
    "ds_read_b128 v[132:135], %[la] offset:14336\n\t" \
    "s_waitcnt lgkmcnt(10)\n\t" \
    "v_pk_add_f32 v[72:73], v[72:73], v[92:93]\n\t" \
    "v_pk_add_f32 v[74:75], v[74:75], v[94:95]\n\t" \
    "ds_read_b128 v[136:139], %[la] offset:20480\n\t" \
    "s_waitcnt lgkmcnt(10)\n\t" \
    "v_pk_add_f32 v[76:77], v[76:77], v[96:97]\n\t" \
    "v_pk_add_f32 v[78:79], v[78:79], v[98:99]\n\t" \
    "ds_read_b128 v[140:143], %[la] offset:26624\n\t" \
    "s_waitcnt lgkmcnt(10)\n\t" \
    "v_pk_add_f32 v[80:81], v[80:81], v[100:101]\n\t" \
    "v_pk_add_f32 v[82:83], v[82:83], v[102:103]\n\t" \
    "ds_read_b128 v[144:147], %[la] offset:3072\n\t" \
    "s_waitcnt lgkmcnt(10)\n\t" \
    "v_pk_add_f32 v[64:65], v[64:65], v[104:105]\n\t" \
    "v_pk_add_f32 v[66:67], v[66:67], v[106:107]\n\t" \
    "ds_read_b128 v[148:151], %[la] offset:9216\n\t" \
    "s_waitcnt lgkmcnt(10)\n\t" \
    "v_pk_add_f32 v[68:69], v[68:69], v[108:109]\n\t" \
    "v_pk_add_f32 v[70:71], v[70:71], v[110:111]\n\t" \
    "ds_read_b128 v[152:155], %[la] offset:15360\n\t" \
    "s_waitcnt lgkmcnt(10)\n\t" \
    "v_pk_add_f32 v[72:73], v[72:73], v[112:113]\n\t" \
    "v_pk_add_f32 v[74:75], v[74:75], v[114:115]\n\t" \
    "ds_read_b128 v[156:159], %[la] offset:21504\n\t" \
    "s_waitcnt lgkmcnt(10)\n\t" \
    "v_pk_add_f32 v[76:77], v[76:77], v[116:117]\n\t" \
    "v_pk_add_f32 v[78:79], v[78:79], v[118:119]\n\t" \
    "ds_read_b128 v[160:163], %[la] offset:27648\n\t" \
    "s_waitcnt lgkmcnt(10)\n\t" \
    "v_pk_add_f32 v[80:81], v[80:81], v[120:121]\n\t" \
    "v_pk_add_f32 v[82:83], v[82:83], v[122:123]\n\t" \
    "s_waitcnt lgkmcnt(9)\n\t" \
    "v_pk_add_f32 v[64:65], v[64:65], v[124:125]\n\t" \
    "v_pk_add_f32 v[66:67], v[66:67], v[126:127]\n\t" \
    "s_waitcnt lgkmcnt(8)\n\t" \
    "v_pk_add_f32 v[68:69], v[68:69], v[128:129]\n\t" \
    "v_pk_add_f32 v[70:71], v[70:71], v[130:131]\n\t" \
    "s_waitcnt lgkmcnt(7)\n\t" \
    "v_pk_add_f32 v[72:73], v[72:73], v[132:133]\n\t" \
    "v_pk_add_f32 v[74:75], v[74:75], v[134:135]\n\t" \
    "s_waitcnt lgkmcnt(6)\n\t" \
    "v_pk_add_f32 v[76:77], v[76:77], v[136:137]\n\t" \
    "v_pk_add_f32 v[78:79], v[78:79], v[138:139]\n\t" \
    "s_waitcnt lgkmcnt(5)\n\t" \
    "v_pk_add_f32 v[80:81], v[80:81], v[140:141]\n\t" \
    "v_pk_add_f32 v[82:83], v[82:83], v[142:143]\n\t" \
    "s_waitcnt lgkmcnt(4)\n\t" \
    "v_pk_add_f32 v[64:65], v[64:65], v[144:145]\n\t" \
    "v_pk_add_f32 v[66:67], v[66:67], v[146:147]\n\t" \
    "s_waitcnt lgkmcnt(3)\n\t" \
    "v_pk_add_f32 v[68:69], v[68:69], v[148:149]\n\t" \
    "v_pk_add_f32 v[70:71], v[70:71], v[150:151]\n\t" \
    "s_waitcnt lgkmcnt(2)\n\t" \
    "v_pk_add_f32 v[72:73], v[72:73], v[152:153]\n\t" \
    "v_pk_add_f32 v[74:75], v[74:75], v[154:155]\n\t" \
    "s_waitcnt lgkmcnt(1)\n\t" \
    "v_pk_add_f32 v[76:77], v[76:77], v[156:157]\n\t" \
    "v_pk_add_f32 v[78:79], v[78:79], v[158:159]\n\t" \
    "s_waitcnt lgkmcnt(0)\n\t" \
    "v_pk_add_f32 v[80:81], v[80:81], v[160:161]\n\t" \
    "v_pk_add_f32 v[82:83], v[82:83], v[162:163]\n\t" \
    ".Lft1_%=:\n\t" \
    "ds_read_b32 v194, %[f1]\n\t" \
    "s_waitcnt lgkmcnt(0)\n\t" \
    "v_cmp_ge_u32 vcc, %[u], v194\n\t" \
    "s_cbranch_vccnz .Lfs1_%=\n\t" \
    ".Lfu1_%=:\n\t" \
    ASM_ST2 \
    "ds_read_b128 v[84:87], %[la] offset:4096\n\t" \
    "ds_read_b128 v[88:91], %[la] offset:10240\n\t" \
    "ds_read_b128 v[92:95], %[la] offset:16384\n\t" \
    "ds_read_b128 v[96:99], %[la] offset:22528\n\t" \
    "ds_read_b128 v[100:103], %[la] offset:28672\n\t" \
    "ds_read_b128 v[104:107], %[la] offset:5120\n\t" \
    "ds_read_b128 v[108:111], %[la] offset:11264\n\t" \
    "ds_read_b128 v[112:115], %[la] offset:17408\n\t" \
    "ds_read_b128 v[116:119], %[la] offset:23552\n\t" \
    "ds_read_b128 v[120:123], %[la] offset:29696\n\t" \
    "v_mov_b32 v194, %[un]\n\t" \
    "ds_write_b32 %[fd], v194\n\t" \
    "s_waitcnt lgkmcnt(10)\n\t" \
    "v_pk_add_f32 v[64:65], v[64:65], v[84:85]\n\t" \
    "v_pk_add_f32 v[66:67], v[66:67], v[86:87]\n\t" \
    "s_waitcnt lgkmcnt(9)\n\t" \
    "v_pk_add_f32 v[68:69], v[68:69], v[88:89]\n\t" \
    "v_pk_add_f32 v[70:71], v[70:71], v[90:91]\n\t" \
    "s_waitcnt lgkmcnt(8)\n\t" \
    "v_pk_add_f32 v[72:73], v[72:73], v[92:93]\n\t" \
    "v_pk_add_f32 v[74:75], v[74:75], v[94:95]\n\t" \
    "s_waitcnt lgkmcnt(7)\n\t" \
    "v_pk_add_f32 v[76:77], v[76:77], v[96:97]\n\t" \
    "v_pk_add_f32 v[78:79], v[78:79], v[98:99]\n\t" \
    "s_waitcnt lgkmcnt(6)\n\t" \
    "v_pk_add_f32 v[80:81], v[80:81], v[100:101]\n\t" \
    "v_pk_add_f32 v[82:83], v[82:83], v[102:103]\n\t" \
    "s_waitcnt lgkmcnt(5)\n\t" \
    "v_pk_add_f32 v[64:65], v[64:65], v[104:105]\n\t" \
    "v_pk_add_f32 v[66:67], v[66:67], v[106:107]\n\t" \
    "s_waitcnt lgkmcnt(4)\n\t" \
    "v_pk_add_f32 v[68:69], v[68:69], v[108:109]\n\t" \
    "v_pk_add_f32 v[70:71], v[70:71], v[110:111]\n\t" \
    "s_waitcnt lgkmcnt(3)\n\t" \
    "v_pk_add_f32 v[72:73], v[72:73], v[112:113]\n\t" \
    "v_pk_add_f32 v[74:75], v[74:75], v[114:115]\n\t" \
    "s_waitcnt lgkmcnt(2)\n\t" \
    "v_pk_add_f32 v[76:77], v[76:77], v[116:117]\n\t" \
    "v_pk_add_f32 v[78:79], v[78:79], v[118:119]\n\t" \
    "s_waitcnt lgkmcnt(1)\n\t" \
    "v_pk_add_f32 v[80:81], v[80:81], v[120:121]\n\t" \
    "v_pk_add_f32 v[82:83], v[82:83], v[122:123]\n\t" \
    "v_cvt_pk_bf16_f32 v184, v64, v65\n\t" \
    "v_cvt_pk_bf16_f32 v185, v66, v67\n\t" \
    "v_cvt_pk_bf16_f32 v186, v68, v69\n\t" \
    "v_cvt_pk_bf16_f32 v187, v70, v71\n\t" \
    "v_cvt_pk_bf16_f32 v188, v72, v73\n\t" \
    "v_cvt_pk_bf16_f32 v189, v74, v75\n\t" \
    "v_cvt_pk_bf16_f32 v190, v76, v77\n\t" \
    "v_cvt_pk_bf16_f32 v191, v78, v79\n\t" \
    "v_cvt_pk_bf16_f32 v192, v80, v81\n\t" \
    "v_cvt_pk_bf16_f32 v193, v82, v83\n\t" \
    "v_permlane16_swap_b32 v184, v186\n\t" \
    "v_permlane16_swap_b32 v185, v187\n\t" \
    "v_permlane16_swap_b32 v188, v190\n\t" \
    "v_permlane16_swap_b32 v189, v191\n\t" \
    "buffer_store_dwordx4 v[184:187], %[pa], %[ro], %[so] offen nt\n\t" \
    "buffer_store_dwordx4 v[188:191], %[pa], %[ro], %[so] offen offset:64 nt\n\t" \
    "buffer_store_dwordx2 v[192:193], %[pb], %[ro], %[so] offen offset:128 nt\n\t" \
    "buffer_load_dwordx2 v[164:165], %[o0], %[rd], %[n1] offen sc1\n\t" \
    "buffer_load_dwordx2 v[166:167], %[o1], %[rd], %[n1] offen sc1\n\t" \
    "buffer_load_dwordx2 v[168:169], %[o2], %[rd], %[n1] offen sc1\n\t" \
    "buffer_load_dwordx2 v[170:171], %[o3], %[rd], %[n1] offen sc1\n\t" \
    "buffer_load_dwordx2 v[172:173], %[o4], %[rd], %[n1] offen sc1\n\t" \
    "buffer_load_dwordx2 v[174:175], %[o0], %[rd], %[na] offen sc1\n\t" \
    "buffer_load_dwordx2 v[176:177], %[o1], %[rd], %[na] offen sc1\n\t" \
    "buffer_load_dwordx2 v[178:179], %[o2], %[rd], %[na] offen sc1\n\t" \
    "buffer_load_dwordx2 v[180:181], %[o3], %[rd], %[na] offen sc1\n\t" \
    "buffer_load_dwordx2 v[182:183], %[o4], %[rd], %[na] offen sc1\n\t"
#define ASM_WCS \
    "v_mfma_f32_16x16x32_bf16 v[196:199], v[0:3], v[216:219], 0\n\t" \
    "v_mfma_f32_16x16x32_bf16 v[200:203], v[80:83], v[216:219], 0\n\t" \
    "v_mfma_f32_16x16x32_bf16 v[196:199], v[4:7], v[216:219], v[196:199]\n\t" \
    "v_mfma_f32_16x16x32_bf16 v[200:203], v[84:87], v[216:219], v[200:203]\n\t" \
    "v_mfma_f32_16x16x32_bf16 v[196:199], v[8:11], v[216:219], v[196:199]\n\t" \
    "v_mfma_f32_16x16x32_bf16 v[200:203], v[88:91], v[216:219], v[200:203]\n\t" \
    "v_mfma_f32_16x16x32_bf16 v[196:199], v[12:15], v[216:219], v[196:199]\n\t" \
    "v_mfma_f32_16x16x32_bf16 v[200:203], v[92:95], v[216:219], v[200:203]\n\t" \
    "v_mfma_f32_16x16x32_bf16 v[196:199], v[16:19], v[216:219], v[196:199]\n\t" \
    "v_mfma_f32_16x16x32_bf16 v[200:203], v[96:99], v[216:219], v[200:203]\n\t" \
    "v_mfma_f32_16x16x32_bf16 v[196:199], v[20:23], v[216:219], v[196:199]\n\t" \
    "v_mfma_f32_16x16x32_bf16 v[200:203], v[100:103], v[216:219], v[200:203]\n\t" \
    "v_mfma_f32_16x16x32_bf16 v[196:199], v[24:27], v[216:219], v[196:199]\n\t" \
    "v_mfma_f32_16x16x32_bf16 v[200:203], v[104:107], v[216:219], v[200:203]\n\t" \
    "v_mfma_f32_16x16x32_bf16 v[196:199], v[28:31], v[216:219], v[196:199]\n\t" \
    "v_mfma_f32_16x16x32_bf16 v[200:203], v[108:111], v[216:219], v[200:203]\n\t" \
    "v_mfma_f32_16x16x32_bf16 v[196:199], v[32:35], v[216:219], v[196:199]\n\t" \
    "v_mfma_f32_16x16x32_bf16 v[200:203], v[112:115], v[216:219], v[200:203]\n\t" \
    "v_mfma_f32_16x16x32_bf16 v[196:199], v[36:39], v[216:219], v[196:199]\n\t" \
    "v_mfma_f32_16x16x32_bf16 v[200:203], v[116:119], v[216:219], v[200:203]\n\t" \
    "v_mfma_f32_16x16x32_bf16 v[196:199], v[40:43], v[216:219], v[196:199]\n\t" \
    "v_mfma_f32_16x16x32_bf16 v[200:203], v[120:123], v[216:219], v[200:203]\n\t" \
    "v_mfma_f32_16x16x32_bf16 v[196:199], v[44:47], v[216:219], v[196:199]\n\t" \
    "v_mfma_f32_16x16x32_bf16 v[200:203], v[124:127], v[216:219], v[200:203]\n\t" \
    "v_mfma_f32_16x16x32_bf16 v[196:199], v[48:51], v[216:219], v[196:199]\n\t" \
    "v_mfma_f32_16x16x32_bf16 v[200:203], v[128:131], v[216:219], v[200:203]\n\t" \
    "v_mfma_f32_16x16x32_bf16 v[196:199], v[52:55], v[216:219], v[196:199]\n\t" \
    "v_mfma_f32_16x16x32_bf16 v[200:203], v[132:135], v[216:219], v[200:203]\n\t" \
    "v_mfma_f32_16x16x32_bf16 v[196:199], v[56:59], v[216:219], v[196:199]\n\t" \
    "v_mfma_f32_16x16x32_bf16 v[200:203], v[136:139], v[216:219], v[200:203]\n\t" \
    "v_mfma_f32_16x16x32_bf16 v[196:199], v[60:63], v[216:219], v[196:199]\n\t" \
    "v_mfma_f32_16x16x32_bf16 v[200:203], v[140:143], v[216:219], v[200:203]\n\t" \
    "v_mfma_f32_16x16x32_bf16 v[196:199], v[64:67], v[216:219], v[196:199]\n\t" \
    "v_mfma_f32_16x16x32_bf16 v[200:203], v[144:147], v[216:219], v[200:203]\n\t" \
    "v_mfma_f32_16x16x32_bf16 v[196:199], v[68:71], v[216:219], v[196:199]\n\t" \
    "v_mfma_f32_16x16x32_bf16 v[200:203], v[148:151], v[216:219], v[200:203]\n\t" \
    "v_mfma_f32_16x16x32_bf16 v[196:199], v[72:75], v[216:219], v[196:199]\n\t" \
    "v_mfma_f32_16x16x32_bf16 v[200:203], v[152:155], v[216:219], v[200:203]\n\t" \
    "v_mfma_f32_16x16x32_bf16 v[196:199], v[76:79], v[216:219], v[196:199]\n\t" \
    "v_mfma_f32_16x16x32_bf16 v[200:203], v[156:159], v[216:219], v[200:203]\n\t" \
    "s_nop 7\n\t"
#define ASM_WT1_LO \
    "s_branch .Lt0%=\n\t" \
    ".Ltr%=:\n\t" \
    "s_cmp_gt_i32 %[n], 0\n\t" \
    "s_cbranch_scc0 .Ltg%=\n\t" \
    ".Lfw%=:\n\t" \
    "s_mov_b32 %[a], 0\n\t" \
    "v_max_u32 v252, v232, v233\n\t" \
    "v_max3_u32 v252, v234, v235, v252\n\t" \
    "v_cmp_eq_u32 vcc, -1, v252\n\t" \
    "s_cbranch_vccz .Lgw0_%=\n\t" \
    "buffer_load_dwordx4 v[232:235], %[l16], %[rs], %[i0] offen offset:0 sc1\n\t" \
    "s_mov_b32 %[a], 1\n\t" \
    ".Lgw0_%=:\n\t" \
    "v_max_u32 v252, v236, v237\n\t" \
    "v_max3_u32 v252, v238, v239, v252\n\t" \
    "v_cmp_eq_u32 vcc, -1, v252\n\t" \
    "s_cbranch_vccz .Lgw1_%=\n\t" \
    "buffer_load_dwordx4 v[236:239], %[l16], %[rs], %[i0] offen offset:1024 sc1\n\t" \
    "s_mov_b32 %[a], 1\n\t" \
    ".Lgw1_%=:\n\t" \
    "v_max_u32 v252, v240, v241\n\t" \
    "v_max3_u32 v252, v242, v243, v252\n\t" \
    "v_cmp_eq_u32 vcc, -1, v252\n\t" \
    "s_cbranch_vccz .Lgw2_%=\n\t" \
    "buffer_load_dwordx4 v[240:243], %[l16], %[rs], %[i0] offen offset:2048 sc1\n\t" \
    "s_mov_b32 %[a], 1\n\t" \
    ".Lgw2_%=:\n\t" \
    "v_max_u32 v252, v244, v245\n\t" \
    "v_max3_u32 v252, v246, v247, v252\n\t" \
    "v_cmp_eq_u32 vcc, -1, v252\n\t" \
    "s_cbranch_vccz .Lgw3_%=\n\t" \
    "buffer_load_dwordx4 v[244:247], %[l16], %[rs], %[i0] offen offset:3072 sc1\n\t" \
    "s_mov_b32 %[a], 1\n\t" \
    ".Lgw3_%=:\n\t" \
    "v_max_u32 v252, v248, v249\n\t" \
    "v_max3_u32 v252, v250, v251, v252\n\t" \
    "v_cmp_eq_u32 vcc, -1, v252\n\t" \
    "s_cbranch_vccz .Lgw4_%=\n\t" \
    "buffer_load_dwordx4 v[248:251], %[l16], %[rs], %[i1] offen offset:0 sc1\n\t" \
    "s_mov_b32 %[a], 1\n\t" \
    ".Lgw4_%=:\n\t" \
    "s_cmp_eq_u32 %[a], 0\n\t" \
    "s_cbranch_scc1 .Lhw%=\n\t" \
    "s_waitcnt vmcnt(20)\n\t" \
    ".Liw%=:\n\t" \
    "v_max_u32 v252, v232, v233\n\t" \
    "v_max3_u32 v252, v234, v235, v252\n\t" \
    "v_max3_u32 v252, v236, v237, v252\n\t" \
    "v_max3_u32 v252, v238, v239, v252\n\t" \
    "v_max3_u32 v252, v240, v241, v252\n\t" \
    "v_max3_u32 v252, v242, v243, v252\n\t" \
    "v_max3_u32 v252, v244, v245, v252\n\t" \
    "v_max3_u32 v252, v246, v247, v252\n\t" \
    "v_max3_u32 v252, v248, v249, v252\n\t" \
    "v_max3_u32 v252, v250, v251, v252\n\t" \
    "v_cmp_eq_u32 vcc, -1, v252\n\t" \
    "s_cbranch_vccz .Lhw%=\n\t" \
    "s_sleep 1\n\t" \
    "v_max_u32 v252, v232, v233\n\t" \
    "v_max3_u32 v252, v234, v235, v252\n\t" \
    "v_max3_u32 v252, v236, v237, v252\n\t" \
    "v_max3_u32 v252, v238, v239, v252\n\t" \
    "v_max3_u32 v252, v240, v241, v252\n\t" \
    "v_max3_u32 v252, v242, v243, v252\n\t" \
    "v_max3_u32 v252, v244, v245, v252\n\t" \
    "v_max3_u32 v252, v246, v247, v252\n\t" \
    "v_max3_u32 v252, v248, v249, v252\n\t" \
    "v_max3_u32 v252, v250, v251, v252\n\t" \
    "v_cmp_eq_u32 vcc, -1, v252\n\t" \
    "s_cbranch_vccz .Lhw%=\n\t" \
    "s_sleep 1\n\t" \
    "v_max_u32 v252, v232, v233\n\t" \
    "v_max3_u32 v252, v234, v235, v252\n\t" \
    "v_max3_u32 v252, v236, v237, v252\n\t" \
    "v_max3_u32 v252, v238, v239, v252\n\t" \
    "v_max3_u32 v252, v240, v241, v252\n\t" \
    "v_max3_u32 v252, v242, v243, v252\n\t" \
    "v_max3_u32 v252, v244, v245, v252\n\t" \
    "v_max3_u32 v252, v246, v247, v252\n\t" \
    "v_max3_u32 v252, v248, v249, v252\n\t" \
    "v_max3_u32 v252, v250, v251, v252\n\t" \
    "v_cmp_eq_u32 vcc, -1, v252\n\t" \
    "s_cbranch_vccz .Lhw%=\n\t" \
    "s_sleep 1\n\t" \
    "v_max_u32 v252, v232, v233\n\t" \
    "v_max3_u32 v252, v234, v235, v252\n\t" \
    "v_max3_u32 v252, v236, v237, v252\n\t" \
    "v_max3_u32 v252, v238, v239, v252\n\t" \
    "v_max3_u32 v252, v240, v241, v252\n\t" \
    "v_max3_u32 v252, v242, v243, v252\n\t" \
    "v_max3_u32 v252, v244, v245, v252\n\t" \
    "v_max3_u32 v252, v246, v247, v252\n\t" \
    "v_max3_u32 v252, v248, v249, v252\n\t" \
    "v_max3_u32 v252, v250, v251, v252\n\t" \
    "v_cmp_eq_u32 vcc, -1, v252\n\t" \
    "s_cbranch_vccz .Lhw%=\n\t" \
    "s_sleep 1\n\t" \
    "v_max_u32 v252, v232, v233\n\t" \
    "v_max3_u32 v252, v234, v235, v252\n\t" \
    "v_max3_u32 v252, v236, v237, v252\n\t" \
    "v_max3_u32 v252, v238, v239, v252\n\t" \
    "v_max3_u32 v252, v240, v241, v252\n\t" \
    "v_max3_u32 v252, v242, v243, v252\n\t" \
    "v_max3_u32 v252, v244, v245, v252\n\t" \
    "v_max3_u32 v252, v246, v247, v252\n\t" \
    "v_max3_u32 v252, v248, v249, v252\n\t" \
    "v_max3_u32 v252, v250, v251, v252\n\t" \
    "v_cmp_eq_u32 vcc, -1, v252\n\t" \
    "s_cbranch_vccz .Lhw%=\n\t" \
    "s_sleep 1\n\t" \
    "v_max_u32 v252, v232, v233\n\t" \
    "v_max3_u32 v252, v234, v235, v252\n\t" \
    "v_max3_u32 v252, v236, v237, v252\n\t" \
    "v_max3_u32 v252, v238, v239, v252\n\t" \
    "v_max3_u32 v252, v240, v241, v252\n\t" \
    "v_max3_u32 v252, v242, v243, v252\n\t" \
    "v_max3_u32 v252, v244, v245, v252\n\t" \
    "v_max3_u32 v252, v246, v247, v252\n\t" \
    "v_max3_u32 v252, v248, v249, v252\n\t" \
    "v_max3_u32 v252, v250, v251, v252\n\t" \
    "v_cmp_eq_u32 vcc, -1, v252\n\t" \
    "s_cbranch_vccz .Lhw%=\n\t" \
    "s_sleep 1\n\t" \
    "s_sub_u32 %[n], %[n], 1\n\t" \
    "s_cmp_gt_i32 %[n], 0\n\t" \
    "s_cbranch_scc1 .Lfw%=\n\t" \
    ".Lhw%=:\n\t" \
    "s_branch .Lta%=\n\t" \
    ".Lt0%=:\n\t" \
    "s_waitcnt vmcnt(1)\n\t" \
    ".Lta%=:\n\t" \
    "ds_write_b128 %[lw], v[232:235] offset:0\n\t" \
    "v_max_u32 v252, v232, v233\n\t" \
    "v_max3_u32 v252, v234, v235, v252\n\t" \
    "v_mfma_f32_16x16x32_bf16 v[188:191], v[232:235], v[232:235], 0\n\t" \
    "ds_write_b128 %[lw], v[236:239] offset:1024\n\t" \
    "v_max3_u32 v252, v236, v237, v252\n\t" \
    "v_max3_u32 v252, v238, v239, v252\n\t" \
    "v_mfma_f32_16x16x32_bf16 v[188:191], v[236:239], v[236:239], v[188:191]\n\t" \
    "ds_write_b128 %[lw], v[240:243] offset:2048\n\t" \
    "v_max3_u32 v252, v240, v241, v252\n\t" \
    "v_max3_u32 v252, v242, v243, v252\n\t" \
    "v_mfma_f32_16x16x32_bf16 v[188:191], v[240:243], v[240:243], v[188:191]\n\t" \
    "ds_write_b128 %[lw], v[244:247] offset:3072\n\t" \
    "v_max3_u32 v252, v244, v245, v252\n\t" \
    "v_max3_u32 v252, v246, v247, v252\n\t" \
    "v_mfma_f32_16x16x32_bf16 v[188:191], v[244:247], v[244:247], v[188:191]\n\t" \
    "ds_write_b128 %[lw], v[248:251] offset:4096\n\t" \
    "v_max3_u32 v252, v248, v249, v252\n\t" \
    "v_max3_u32 v252, v250, v251, v252\n\t" \
    "v_mfma_f32_16x16x32_bf16 v[188:191], v[248:251], v[248:251], v[188:191]\n\t" \
    "v_mfma_f32_16x16x32_bf16 v[192:195], v[216:219], v[232:235], 0\n\t" \
    "v_mfma_f32_16x16x32_bf16 v[192:195], v[216:219], v[236:239], v[192:195]\n\t" \
    "v_mfma_f32_16x16x32_bf16 v[192:195], v[216:219], v[240:243], v[192:195]\n\t" \
    "v_mfma_f32_16x16x32_bf16 v[192:195], v[216:219], v[244:247], v[192:195]\n\t" \
    "v_mfma_f32_16x16x32_bf16 v[192:195], v[216:219], v[248:251], v[192:195]\n\t" \
    "v_cmp_eq_u32 vcc, -1, v252\n\t" \
    "s_cbranch_vccnz .Ltr%=\n\t" \
    ".Ltg%=:\n\t" \
    ASM_ST2 \
    "s_nop 5\n\t" \
    "ds_write_b128 %[lw], v[188:191] offset:5120\n\t" \
    "ds_write_b128 %[lw], v[192:195] offset:6144\n\t" \
    "s_waitcnt lgkmcnt(0)\n\t" \
    "s_setprio 2\n\t" \
    "s_barrier\n\t" ASM_ST \
    "ds_read_b128 v[160:163], %[la] offset:0\n\t" \
    "ds_read_b128 v[164:167], %[la] offset:1024\n\t" \
    "ds_read_b128 v[168:171], %[la] offset:2048\n\t" \
    "ds_read2st64_b32 v[204:205], %[sr] offset0:20 offset1:24\n\t" \
    "ds_read_b128 v[172:175], %[la] offset:3072\n\t" \
    "ds_read_b128 v[176:179], %[la] offset:8192\n\t" \
    "ds_read_b128 v[180:183], %[la] offset:9216\n\t" \
    "ds_read_b128 v[184:187], %[la] offset:10240\n\t" \
    "s_waitcnt lgkmcnt(7)\n\t" \
    "v_mfma_f32_16x16x32_bf16 v[192:195], v[80:83], v[160:163], 0\n\t" \
    "s_waitcnt lgkmcnt(6)\n\t" \
    "v_mfma_f32_16x16x32_bf16 v[192:195], v[84:87], v[164:167], v[192:195]\n\t" \
    "v_mfma_f32_16x16x32_bf16 v[188:191], v[0:3], v[160:163], 0\n\t" \
    "ds_read_b128 v[160:163], %[la] offset:11264\n\t" \
    "v_mfma_f32_16x16x32_bf16 v[188:191], v[4:7], v[164:167], v[188:191]\n\t" \
    "ds_read_b128 v[164:167], %[la] offset:16384\n\t" \
    "s_waitcnt lgkmcnt(7)\n\t" \
    "v_mfma_f32_16x16x32_bf16 v[188:191], v[8:11], v[168:171], v[188:191]\n\t" \
    "s_waitcnt lgkmcnt(6)\n\t" \
    "s_waitcnt lgkmcnt(5)\n\t" \
    "v_mfma_f32_16x16x32_bf16 v[188:191], v[12:15], v[172:175], v[188:191]\n\t" \
    "v_permlane16_swap_b32 v204, v205\n\t" \
    "v_mfma_f32_16x16x32_bf16 v[192:195], v[88:91], v[168:171], v[192:195]\n\t" \
    "ds_read_b128 v[168:171], %[la] offset:17408\n\t" \
    "v_mfma_f32_16x16x32_bf16 v[192:195], v[92:95], v[172:175], v[192:195]\n\t" \
    "ds_read_b128 v[172:175], %[la] offset:18432\n\t" \
    "v_add_f32 v206, v204, v205\n\t" \
    "s_waitcnt lgkmcnt(6)\n\t" \
    "v_mfma_f32_16x16x32_bf16 v[192:195], v[96:99], v[176:179], v[192:195]\n\t" \
    "v_add_f32 v207, v204, v205\n\t" \
    "s_waitcnt lgkmcnt(5)\n\t" \
    "v_mfma_f32_16x16x32_bf16 v[192:195], v[100:103], v[180:183], v[192:195]\n\t" \
    "v_mfma_f32_16x16x32_bf16 v[188:191], v[16:19], v[176:179], v[188:191]\n\t" \
    "ds_read_b128 v[176:179], %[la] offset:19456\n\t" \
    "v_permlane32_swap_b32 v206, v207\n\t" \
    "v_mfma_f32_16x16x32_bf16 v[188:191], v[20:23], v[180:183], v[188:191]\n\t" \
    "ds_read_b128 v[180:183], %[la] offset:24576\n\t" \
    "s_waitcnt lgkmcnt(6)\n\t" \
    "v_mfma_f32_16x16x32_bf16 v[188:191], v[24:27], v[184:187], v[188:191]\n\t" \
    "v_add_f32 v204, v206, v207\n\t" \
    "s_waitcnt lgkmcnt(5)\n\t" \
    "v_mfma_f32_16x16x32_bf16 v[188:191], v[28:31], v[160:163], v[188:191]\n\t" \
    "v_add_f32 v205, v206, v207\n\t" \
    "v_mfma_f32_16x16x32_bf16 v[192:195], v[104:107], v[184:187], v[192:195]\n\t" \
    "ds_read_b128 v[184:187], %[la] offset:25600\n\t" \
    "v_mfma_f32_16x16x32_bf16 v[192:195], v[108:111], v[160:163], v[192:195]\n\t" \
    "ds_read_b128 v[160:163], %[la] offset:26624\n\t" \
    "v_permlane16_swap_b32 v204, v205\n\t" \
    "s_waitcnt lgkmcnt(6)\n\t" \
    "v_mfma_f32_16x16x32_bf16 v[192:195], v[112:115], v[164:167], v[192:195]\n\t" \
    "s_waitcnt lgkmcnt(5)\n\t" \
    "v_mfma_f32_16x16x32_bf16 v[192:195], v[116:119], v[168:171], v[192:195]\n\t" \
    "v_mul_f32 v205, %[kd], v205\n\t" \
    "v_mfma_f32_16x16x32_bf16 v[188:191], v[32:35], v[164:167], v[188:191]\n\t" \
    "ds_read_b128 v[164:167], %[la] offset:27648\n\t" \
    "v_mul_f32 v204, %[kd], v204\n\t" \
    "v_mfma_f32_16x16x32_bf16 v[188:191], v[36:39], v[168:171], v[188:191]\n\t" \
    "ds_read_b128 v[168:171], %[la] offset:4096\n\t" \
    "v_fma_f32 v204, -v205, v205, v204\n\t" \
    "s_waitcnt lgkmcnt(6)\n\t" \
    "v_mfma_f32_16x16x32_bf16 v[188:191], v[40:43], v[172:175], v[188:191]\n\t" \
    "v_max_f32 v204, 0, v204\n\t" \
    "s_waitcnt lgkmcnt(5)\n\t" \
    "v_mfma_f32_16x16x32_bf16 v[188:191], v[44:47], v[176:179], v[188:191]\n\t" \
    "v_add_f32 v206, %[ke], v204\n\t" \
    "v_mfma_f32_16x16x32_bf16 v[192:195], v[120:123], v[172:175], v[192:195]\n\t" \
    "ds_read_b128 v[172:175], %[la] offset:12288\n\t" \
    "v_rsq_f32 v206, v206\n\t" \
    "v_mfma_f32_16x16x32_bf16 v[192:195], v[124:127], v[176:179], v[192:195]\n\t" \
    "ds_read_b128 v[176:179], %[la] offset:20480\n\t" \
    "s_waitcnt lgkmcnt(6)\n\t" \
    "v_mfma_f32_16x16x32_bf16 v[192:195], v[128:131], v[180:183], v[192:195]\n\t" \
    "v_mul_f32 v207, v204, v206\n\t" \
    "s_waitcnt lgkmcnt(5)\n\t" \
    "v_mfma_f32_16x16x32_bf16 v[192:195], v[132:135], v[184:187], v[192:195]\n\t" \
    "v_fma_f32 v207, v207, v206, %[ke]\n\t" \
    "v_mfma_f32_16x16x32_bf16 v[188:191], v[48:51], v[180:183], v[188:191]\n\t" \
    "ds_read_b128 v[180:183], %[la] offset:28672\n\t" \
    "v_rsq_f32 v207, v207\n\t" \
    "v_mfma_f32_16x16x32_bf16 v[188:191], v[52:55], v[184:187], v[188:191]\n\t" \
    "v_mul_f32 v206, v206, v207\n\t" \
    "v_mul_f32 v207, -v205, v206\n\t" \
    "s_waitcnt lgkmcnt(5)\n\t" \
    "v_mfma_f32_16x16x32_bf16 v[188:191], v[56:59], v[160:163], v[188:191]\n\t" \
    "v_mul_f32 v208, v196, v207\n\t" \
    "v_mul_f32 v209, v197, v207\n\t" \
    "s_waitcnt lgkmcnt(4)\n\t" \
    "v_mfma_f32_16x16x32_bf16 v[188:191], v[60:63], v[164:167], v[188:191]\n\t" \
    "v_mul_f32 v210, v198, v207\n\t" \
    "v_mul_f32 v211, v199, v207\n\t" \
    "v_mfma_f32_16x16x32_bf16 v[192:195], v[136:139], v[160:163], v[192:195]\n\t" \
    "v_mul_f32 v212, v200, v207\n\t" \
    "v_mul_f32 v213, v201, v207\n\t" \
    "v_mfma_f32_16x16x32_bf16 v[192:195], v[140:143], v[164:167], v[192:195]\n\t" \
    "v_mul_f32 v214, v202, v207\n\t" \
    "v_mul_f32 v215, v203, v207\n\t" \
    "s_waitcnt lgkmcnt(3)\n\t" \
    "v_mfma_f32_16x16x32_bf16 v[192:195], v[144:147], v[168:171], v[192:195]\n\t" \
    "s_cmp_lg_u32 %[sw], 0\n\t" \
    "s_cbranch_scc1 .Lts%=\n\t" \
    "v_cvt_pk_bf16_f32 v217, v204, v204\n\t" \
    "v_lshlrev_b32 v217, 16, v217\n\t" \
    "v_add_f32 v217, %[ke], v217\n\t" \
    "v_cvt_pk_bf16_f32 v217, v217, v217\n\t" \
    "v_lshlrev_b32 v217, 16, v217\n\t" \
    "v_sqrt_f32 v217, v217\n\t" \
    "v_cvt_pk_bf16_f32 v216, v205, v205\n\t" \
    "v_lshlrev_b32 v216, 16, v216\n\t" \
    "v_cvt_pk_bf16_f32 v217, v217, v217\n\t" \
    "v_lshlrev_b32 v217, 16, v217\n\t" \
    "v_rcp_f32 v217, v217\n\t" \
    "v_lshrrev_b32 v218, 1, %[l16]\n\t" \
    "v_and_b32 v218, 0x78, v218\n\t" \
    "buffer_store_dwordx2 v[216:217], v218, %[rs], %[ss] offen sc1\n\t" \
    ".Lts%=:\n\t" \
    "v_mov_b64 v[216:217], -1\n\t" \
    "s_waitcnt lgkmcnt(2)\n\t" \
    "v_mfma_f32_16x16x32_bf16 v[192:195], v[148:151], v[172:175], v[192:195]\n\t" \
    "v_mov_b64 v[218:219], -1\n\t" \
    "buffer_store_dwordx4 v[216:219], %[ob], %[rs], %[sa] offen sc1\n\t" \
    "v_mfma_f32_16x16x32_bf16 v[188:191], v[64:67], v[168:171], v[188:191]\n\t" \
    "v_mfma_f32_16x16x32_bf16 v[188:191], v[68:71], v[172:175], v[188:191]\n\t" \
    "s_waitcnt lgkmcnt(1)\n\t" \
    "v_mfma_f32_16x16x32_bf16 v[188:191], v[72:75], v[176:179], v[188:191]\n\t" \
    "s_waitcnt lgkmcnt(0)\n\t" \
    "v_mfma_f32_16x16x32_bf16 v[188:191], v[76:79], v[180:183], v[188:191]\n\t" \
    "v_mfma_f32_16x16x32_bf16 v[192:195], v[152:155], v[176:179], v[192:195]\n\t" \
    "v_mfma_f32_16x16x32_bf16 v[192:195], v[156:159], v[180:183], v[192:195]\n\t" \
    "s_setprio 2\n\t" \
    "buffer_load_dwordx4 v[232:235], %[l16], %[rs], %[n0] offen offset:0 sc1\n\t" \
    "buffer_load_dwordx4 v[236:239], %[l16], %[rs], %[n0] offen offset:1024 sc1\n\t" \
    "buffer_load_dwordx4 v[240:243], %[l16], %[rs], %[n0] offen offset:2048 sc1\n\t" \
    "buffer_load_dwordx4 v[244:247], %[l16], %[rs], %[n0] offen offset:3072 sc1\n\t" \
    "buffer_load_dwordx4 v[248:251], %[l16], %[rs], %[n1] offen offset:0 sc1\n\t" \
    ASM_ST3 \
    "v_fma_f32 v188, v188, v206, v208\n\t" \
    "v_fma_f32 v189, v189, v206, v209\n\t" \
    "v_fma_f32 v190, v190, v206, v210\n\t" \
    "v_fma_f32 v191, v191, v206, v211\n\t" \
    "v_mul_f32 v160, v188, v188\n\t" \
    "v_mul_f32 v161, v189, v189\n\t" \
    "v_mul_f32 v162, v190, v190\n\t" \
    "v_mul_f32 v163, v191, v191\n\t" \
    "v_fma_f32 v160, v160, %[k1], v220\n\t" \
    "v_fma_f32 v161, v161, %[k1], v220\n\t" \
    "v_fma_f32 v162, v162, %[k1], v220\n\t" \
    "v_fma_f32 v163, v163, %[k1], v220\n\t" \
    "v_mul_f32 v160, v188, v160\n\t" \
    "v_mul_f32 v161, v189, v161\n\t" \
    "v_mul_f32 v162, v190, v162\n\t" \
    "v_mul_f32 v163, v191, v163\n\t" \
    "v_fma_f32 v192, v192, v206, v212\n\t" \
    "v_fma_f32 v193, v193, v206, v213\n\t" \
    "v_fma_f32 v194, v194, v206, v214\n\t" \
    "v_fma_f32 v195, v195, v206, v215\n\t" \
    "v_mul_f32 v164, v192, v192\n\t" \
    "v_mul_f32 v165, v193, v193\n\t" \
    "v_mul_f32 v166, v194, v194\n\t" \
    "v_mul_f32 v167, v195, v195\n\t" \
    "v_fma_f32 v164, v164, %[k1], v220\n\t" \
    "v_fma_f32 v165, v165, %[k1], v220\n\t" \
    "v_fma_f32 v166, v166, %[k1], v220\n\t" \
    "v_fma_f32 v167, v167, %[k1], v220\n\t" \
    "v_mul_f32 v164, v192, v164\n\t" \
    "v_mul_f32 v165, v193, v165\n\t" \
    "v_mul_f32 v166, v194, v166\n\t" \
    "v_mul_f32 v167, v195, v167\n\t" \
    "v_exp_f32 v160, v160\n\t" \
    "v_exp_f32 v161, v161\n\t" \
    "v_exp_f32 v162, v162\n\t" \
    "v_exp_f32 v163, v163\n\t" \
    "v_exp_f32 v164, v164\n\t" \
    "v_exp_f32 v165, v165\n\t" \
    "v_exp_f32 v166, v166\n\t" \
    "v_exp_f32 v167, v167\n\t" \
    "v_add_f32 v160, 1.0, v160\n\t" \
    "v_add_f32 v161, 1.0, v161\n\t" \
    "v_add_f32 v162, 1.0, v162\n\t" \
    "v_add_f32 v163, 1.0, v163\n\t" \
    "v_add_f32 v164, 1.0, v164\n\t" \
    "v_add_f32 v165, 1.0, v165\n\t" \
    "v_add_f32 v166, 1.0, v166\n\t" \
    "v_add_f32 v167, 1.0, v167\n\t" \
    "v_rcp_f32 v160, v160\n\t" \
    "v_rcp_f32 v161, v161\n\t" \
    "v_rcp_f32 v162, v162\n\t" \
    "v_rcp_f32 v163, v163\n\t" \
    "v_rcp_f32 v164, v164\n\t" \
    "v_rcp_f32 v165, v165\n\t" \
    "v_rcp_f32 v166, v166\n\t" \
    "v_rcp_f32 v167, v167\n\t" \
    "v_mul_f32 v188, v188, v160\n\t" \
    "v_mul_f32 v189, v189, v161\n\t" \
    "v_mul_f32 v190, v190, v162\n\t" \
    "v_mul_f32 v191, v191, v163\n\t" \
    "v_mul_f32 v192, v192, v164\n\t" \
    "v_mul_f32 v193, v193, v165\n\t" \
    "v_mul_f32 v194, v194, v166\n\t" \
    "v_mul_f32 v195, v195, v167\n\t" \
    "v_cvt_pk_bf16_f32 v188, v188, v189\n\t" \
    "v_cvt_pk_bf16_f32 v189, v190, v191\n\t" \
    "v_cvt_pk_bf16_f32 v190, v192, v193\n\t" \
    "v_cvt_pk_bf16_f32 v191, v194, v195\n\t" \
    "v_min_u32 v188, -2, v188\n\t" \
    "v_min_u32 v189, -2, v189\n\t" \
    "v_min_u32 v190, -2, v190\n\t" \
    "v_min_u32 v191, -2, v191\n\t" \
    "v_mov_b32 v216, 0x3f803f80\n\t" \
    "v_mov_b32 v217, 0x3f803f80\n\t" \
    "v_mov_b32 v218, 0x3f803f80\n\t" \
    "v_mov_b32 v219, 0x3f803f80\n\t" \
    "v_permlane16_swap_b32 v188, v190\n\t" \
    "v_permlane16_swap_b32 v189, v191\n\t" \
    "s_waitcnt vmcnt(6)\n\t" \
    "buffer_store_dwordx4 v[188:191], %[oa], %[rs], %[so] offen sc1\n\t" \
    "s_nop 1\n\t" \
    "s_setprio 0\n\t"
#define ASM_WT1_HI \
    "s_setprio 1\n\t" \
    "s_barrier\n\t" ASM_ST \
    "ds_read_b128 v[160:163], %[la] offset:0\n\t" \
    "ds_read_b128 v[164:167], %[la] offset:1024\n\t" \
    "ds_read_b128 v[168:171], %[la] offset:2048\n\t" \
    "ds_read2st64_b32 v[204:205], %[sr] offset0:20 offset1:24\n\t" \
    "ds_read_b128 v[172:175], %[la] offset:3072\n\t" \
    "ds_read_b128 v[176:179], %[la] offset:8192\n\t" \
    "ds_read_b128 v[180:183], %[la] offset:9216\n\t" \
    "ds_read_b128 v[184:187], %[la] offset:10240\n\t" \
    "s_waitcnt lgkmcnt(7)\n\t" \
    "v_mfma_f32_16x16x32_bf16 v[192:195], v[80:83], v[160:163], 0\n\t" \
    "s_waitcnt lgkmcnt(6)\n\t" \
    "v_mfma_f32_16x16x32_bf16 v[192:195], v[84:87], v[164:167], v[192:195]\n\t" \
    "v_mfma_f32_16x16x32_bf16 v[188:191], v[0:3], v[160:163], 0\n\t" \
    "ds_read_b128 v[160:163], %[la] offset:11264\n\t" \
    "v_mfma_f32_16x16x32_bf16 v[188:191], v[4:7], v[164:167], v[188:191]\n\t" \
    "ds_read_b128 v[164:167], %[la] offset:16384\n\t" \
    "s_waitcnt lgkmcnt(7)\n\t" \
    "v_mfma_f32_16x16x32_bf16 v[188:191], v[8:11], v[168:171], v[188:191]\n\t" \
    "s_waitcnt lgkmcnt(6)\n\t" \
    "s_waitcnt lgkmcnt(5)\n\t" \
    "v_mfma_f32_16x16x32_bf16 v[188:191], v[12:15], v[172:175], v[188:191]\n\t" \
    "v_permlane16_swap_b32 v204, v205\n\t" \
    "v_mfma_f32_16x16x32_bf16 v[192:195], v[88:91], v[168:171], v[192:195]\n\t" \
    "ds_read_b128 v[168:171], %[la] offset:17408\n\t" \
    "v_mfma_f32_16x16x32_bf16 v[192:195], v[92:95], v[172:175], v[192:195]\n\t" \
    "ds_read_b128 v[172:175], %[la] offset:18432\n\t" \
    "v_add_f32 v206, v204, v205\n\t" \
    "s_waitcnt lgkmcnt(6)\n\t" \
    "v_mfma_f32_16x16x32_bf16 v[192:195], v[96:99], v[176:179], v[192:195]\n\t" \
    "v_add_f32 v207, v204, v205\n\t" \
    "s_waitcnt lgkmcnt(5)\n\t" \
    "v_mfma_f32_16x16x32_bf16 v[192:195], v[100:103], v[180:183], v[192:195]\n\t" \
    "v_mfma_f32_16x16x32_bf16 v[188:191], v[16:19], v[176:179], v[188:191]\n\t" \
    "ds_read_b128 v[176:179], %[la] offset:19456\n\t" \
    "v_permlane32_swap_b32 v206, v207\n\t" \
    "v_mfma_f32_16x16x32_bf16 v[188:191], v[20:23], v[180:183], v[188:191]\n\t" \
    "ds_read_b128 v[180:183], %[la] offset:24576\n\t" \
    "s_waitcnt lgkmcnt(6)\n\t" \
    "v_mfma_f32_16x16x32_bf16 v[188:191], v[24:27], v[184:187], v[188:191]\n\t" \
    "v_add_f32 v204, v206, v207\n\t" \
    "s_waitcnt lgkmcnt(5)\n\t" \
    "v_mfma_f32_16x16x32_bf16 v[188:191], v[28:31], v[160:163], v[188:191]\n\t" \
    "v_add_f32 v205, v206, v207\n\t" \
    "v_mfma_f32_16x16x32_bf16 v[192:195], v[104:107], v[184:187], v[192:195]\n\t" \
    "ds_read_b128 v[184:187], %[la] offset:25600\n\t" \
    "v_mfma_f32_16x16x32_bf16 v[192:195], v[108:111], v[160:163], v[192:195]\n\t" \
    "ds_read_b128 v[160:163], %[la] offset:26624\n\t" \
    "v_permlane16_swap_b32 v204, v205\n\t" \
    "s_waitcnt lgkmcnt(6)\n\t" \
    "v_mfma_f32_16x16x32_bf16 v[192:195], v[112:115], v[164:167], v[192:195]\n\t" \
    "s_waitcnt lgkmcnt(5)\n\t" \
    "v_mfma_f32_16x16x32_bf16 v[192:195], v[116:119], v[168:171], v[192:195]\n\t" \
    "v_mul_f32 v205, %[kd], v205\n\t" \
    "v_mfma_f32_16x16x32_bf16 v[188:191], v[32:35], v[164:167], v[188:191]\n\t" \
    "ds_read_b128 v[164:167], %[la] offset:27648\n\t" \
    "v_mul_f32 v204, %[kd], v204\n\t" \
    "v_mfma_f32_16x16x32_bf16 v[188:191], v[36:39], v[168:171], v[188:191]\n\t" \
    "ds_read_b128 v[168:171], %[la] offset:4096\n\t" \
    "v_fma_f32 v204, -v205, v205, v204\n\t" \
    "s_waitcnt lgkmcnt(6)\n\t" \
    "v_mfma_f32_16x16x32_bf16 v[188:191], v[40:43], v[172:175], v[188:191]\n\t" \
    "v_max_f32 v204, 0, v204\n\t" \
    "s_waitcnt lgkmcnt(5)\n\t" \
    "v_mfma_f32_16x16x32_bf16 v[188:191], v[44:47], v[176:179], v[188:191]\n\t" \
    "v_add_f32 v206, %[ke], v204\n\t" \
    "v_mfma_f32_16x16x32_bf16 v[192:195], v[120:123], v[172:175], v[192:195]\n\t" \
    "ds_read_b128 v[172:175], %[la] offset:12288\n\t" \
    "v_rsq_f32 v206, v206\n\t" \
    "v_mfma_f32_16x16x32_bf16 v[192:195], v[124:127], v[176:179], v[192:195]\n\t" \
    "ds_read_b128 v[176:179], %[la] offset:20480\n\t" \
    "s_waitcnt lgkmcnt(6)\n\t" \
    "v_mfma_f32_16x16x32_bf16 v[192:195], v[128:131], v[180:183], v[192:195]\n\t" \
    "v_mul_f32 v207, v204, v206\n\t" \
    "s_waitcnt lgkmcnt(5)\n\t" \
    "v_mfma_f32_16x16x32_bf16 v[192:195], v[132:135], v[184:187], v[192:195]\n\t" \
    "v_fma_f32 v207, v207, v206, %[ke]\n\t" \
    "v_mfma_f32_16x16x32_bf16 v[188:191], v[48:51], v[180:183], v[188:191]\n\t" \
    "ds_read_b128 v[180:183], %[la] offset:28672\n\t" \
    "v_rsq_f32 v207, v207\n\t" \
    "v_mfma_f32_16x16x32_bf16 v[188:191], v[52:55], v[184:187], v[188:191]\n\t" \
    "v_mul_f32 v206, v206, v207\n\t" \
    "v_mul_f32 v207, -v205, v206\n\t" \
    "s_waitcnt lgkmcnt(5)\n\t" \
    "v_mfma_f32_16x16x32_bf16 v[188:191], v[56:59], v[160:163], v[188:191]\n\t" \
    "v_mul_f32 v208, v196, v207\n\t" \
    "v_mul_f32 v209, v197, v207\n\t" \
    "s_waitcnt lgkmcnt(4)\n\t" \
    "v_mfma_f32_16x16x32_bf16 v[188:191], v[60:63], v[164:167], v[188:191]\n\t" \
    "v_mul_f32 v210, v198, v207\n\t" \
    "v_mul_f32 v211, v199, v207\n\t" \
    "v_mfma_f32_16x16x32_bf16 v[192:195], v[136:139], v[160:163], v[192:195]\n\t" \
    "v_mul_f32 v212, v200, v207\n\t" \
    "v_mul_f32 v213, v201, v207\n\t" \
    "v_mfma_f32_16x16x32_bf16 v[192:195], v[140:143], v[164:167], v[192:195]\n\t" \
    "v_mul_f32 v214, v202, v207\n\t" \
    "v_mul_f32 v215, v203, v207\n\t" \
    "s_waitcnt lgkmcnt(3)\n\t" \
    "v_mfma_f32_16x16x32_bf16 v[192:195], v[144:147], v[168:171], v[192:195]\n\t" \
    "v_mov_b64 v[216:217], -1\n\t" \
    "v_mov_b64 v[218:219], -1\n\t" \
    "s_waitcnt lgkmcnt(2)\n\t" \
    "v_mfma_f32_16x16x32_bf16 v[192:195], v[148:151], v[172:175], v[192:195]\n\t" \
    "buffer_store_dwordx4 v[216:219], %[ob], %[rs], %[sa] offen sc1\n\t" \
    "v_mfma_f32_16x16x32_bf16 v[188:191], v[64:67], v[168:171], v[188:191]\n\t" \
    "v_mfma_f32_16x16x32_bf16 v[188:191], v[68:71], v[172:175], v[188:191]\n\t" \
    "s_waitcnt lgkmcnt(1)\n\t" \
    "v_mfma_f32_16x16x32_bf16 v[188:191], v[72:75], v[176:179], v[188:191]\n\t" \
    "s_waitcnt lgkmcnt(0)\n\t" \
    "v_mfma_f32_16x16x32_bf16 v[188:191], v[76:79], v[180:183], v[188:191]\n\t" \
    "v_mfma_f32_16x16x32_bf16 v[192:195], v[152:155], v[176:179], v[192:195]\n\t" \
    "v_mfma_f32_16x16x32_bf16 v[192:195], v[156:159], v[180:183], v[192:195]\n\t" \
    ASM_ST3 \
    "s_nop 1\n\t" \
    "v_fma_f32 v188, v188, v206, v208\n\t" \
    "v_fma_f32 v189, v189, v206, v209\n\t" \
    "v_fma_f32 v190, v190, v206, v210\n\t" \
    "v_fma_f32 v191, v191, v206, v211\n\t" \
    "v_mul_f32 v160, v188, v188\n\t" \
    "v_mul_f32 v161, v189, v189\n\t" \
    "v_mul_f32 v162, v190, v190\n\t" \
    "v_mul_f32 v163, v191, v191\n\t" \
    "v_fma_f32 v160, v160, %[k1], v220\n\t" \
    "v_fma_f32 v161, v161, %[k1], v220\n\t" \
    "v_fma_f32 v162, v162, %[k1], v220\n\t" \
    "v_fma_f32 v163, v163, %[k1], v220\n\t" \
    "v_mul_f32 v160, v188, v160\n\t" \
    "v_mul_f32 v161, v189, v161\n\t" \
    "v_mul_f32 v162, v190, v162\n\t" \
    "v_mul_f32 v163, v191, v163\n\t" \
    "v_fma_f32 v192, v192, v206, v212\n\t" \
    "v_fma_f32 v193, v193, v206, v213\n\t" \
    "v_fma_f32 v194, v194, v206, v214\n\t" \
    "v_fma_f32 v195, v195, v206, v215\n\t" \
    "v_mul_f32 v164, v192, v192\n\t" \
    "v_mul_f32 v165, v193, v193\n\t" \
    "v_mul_f32 v166, v194, v194\n\t" \
    "v_mul_f32 v167, v195, v195\n\t" \
    "v_fma_f32 v164, v164, %[k1], v220\n\t" \
    "v_fma_f32 v165, v165, %[k1], v220\n\t" \
    "v_fma_f32 v166, v166, %[k1], v220\n\t" \
    "v_fma_f32 v167, v167, %[k1], v220\n\t" \
    "v_mul_f32 v164, v192, v164\n\t" \
    "v_mul_f32 v165, v193, v165\n\t" \
    "v_mul_f32 v166, v194, v166\n\t" \
    "v_mul_f32 v167, v195, v167\n\t" \
    "v_exp_f32 v160, v160\n\t" \
    "v_exp_f32 v161, v161\n\t" \
    "v_exp_f32 v162, v162\n\t" \
    "v_exp_f32 v163, v163\n\t" \
    "v_exp_f32 v164, v164\n\t" \
    "v_exp_f32 v165, v165\n\t" \
    "v_exp_f32 v166, v166\n\t" \
    "v_exp_f32 v167, v167\n\t" \
    "v_add_f32 v160, 1.0, v160\n\t" \
    "v_add_f32 v161, 1.0, v161\n\t" \
    "v_add_f32 v162, 1.0, v162\n\t" \
    "v_add_f32 v163, 1.0, v163\n\t" \
    "v_add_f32 v164, 1.0, v164\n\t" \
    "v_add_f32 v165, 1.0, v165\n\t" \
    "v_add_f32 v166, 1.0, v166\n\t" \
    "v_add_f32 v167, 1.0, v167\n\t" \
    "v_rcp_f32 v160, v160\n\t" \
    "v_rcp_f32 v161, v161\n\t" \
    "v_rcp_f32 v162, v162\n\t" \
    "v_rcp_f32 v163, v163\n\t" \
    "v_rcp_f32 v164, v164\n\t" \
    "v_rcp_f32 v165, v165\n\t" \
    "v_rcp_f32 v166, v166\n\t" \
    "v_rcp_f32 v167, v167\n\t" \
    "v_mul_f32 v188, v188, v160\n\t" \
    "v_mul_f32 v189, v189, v161\n\t" \
    "v_mul_f32 v190, v190, v162\n\t" \
    "v_mul_f32 v191, v191, v163\n\t" \
    "v_mul_f32 v192, v192, v164\n\t" \
    "v_mul_f32 v193, v193, v165\n\t" \
    "v_mul_f32 v194, v194, v166\n\t" \
    "v_mul_f32 v195, v195, v167\n\t" \
    "v_cvt_pk_bf16_f32 v188, v188, v189\n\t" \
    "v_cvt_pk_bf16_f32 v189, v190, v191\n\t" \
    "v_cvt_pk_bf16_f32 v190, v192, v193\n\t" \
    "v_cvt_pk_bf16_f32 v191, v194, v195\n\t" \
    "v_min_u32 v188, -2, v188\n\t" \
    "v_min_u32 v189, -2, v189\n\t" \
    "v_min_u32 v190, -2, v190\n\t" \
    "v_min_u32 v191, -2, v191\n\t" \
    "s_nop 1\n\t" \
    "v_permlane16_swap_b32 v188, v190\n\t" \
    "v_permlane16_swap_b32 v189, v191\n\t" \
    "s_waitcnt vmcnt(1)\n\t" \
    "buffer_store_dwordx4 v[188:191], %[oa], %[rs], %[so] offen sc1\n\t" \
    "s_nop 1\n\t"
#define ASM_WT2_LO \
    "s_branch .Lt0%=\n\t" \
    ".Ltr%=:\n\t" \
    "s_cmp_gt_i32 %[n], 0\n\t" \
    "s_cbranch_scc0 .Ltg%=\n\t" \
    ".Lfw%=:\n\t" \
    "s_mov_b32 %[a], 0\n\t" \
    "v_max_u32 v252, v232, v233\n\t" \
    "v_max3_u32 v252, v234, v235, v252\n\t" \
    "v_cmp_eq_u32 vcc, -1, v252\n\t" \
    "s_cbranch_vccz .Lgw0_%=\n\t" \
    "buffer_load_dwordx4 v[232:235], %[l16], %[rs], %[i0] offen offset:0 sc1\n\t" \
    "s_mov_b32 %[a], 1\n\t" \
    ".Lgw0_%=:\n\t" \
    "v_max_u32 v252, v236, v237\n\t" \
    "v_max3_u32 v252, v238, v239, v252\n\t" \
    "v_cmp_eq_u32 vcc, -1, v252\n\t" \
    "s_cbranch_vccz .Lgw1_%=\n\t" \
    "buffer_load_dwordx4 v[236:239], %[l16], %[rs], %[i0] offen offset:1024 sc1\n\t" \
    "s_mov_b32 %[a], 1\n\t" \
    ".Lgw1_%=:\n\t" \
    "v_max_u32 v252, v240, v241\n\t" \
    "v_max3_u32 v252, v242, v243, v252\n\t" \
    "v_cmp_eq_u32 vcc, -1, v252\n\t" \
    "s_cbranch_vccz .Lgw2_%=\n\t" \
    "buffer_load_dwordx4 v[240:243], %[l16], %[rs], %[i0] offen offset:2048 sc1\n\t" \
    "s_mov_b32 %[a], 1\n\t" \
    ".Lgw2_%=:\n\t" \
    "v_max_u32 v252, v244, v245\n\t" \
    "v_max3_u32 v252, v246, v247, v252\n\t" \
    "v_cmp_eq_u32 vcc, -1, v252\n\t" \
    "s_cbranch_vccz .Lgw3_%=\n\t" \
    "buffer_load_dwordx4 v[244:247], %[l16], %[rs], %[i0] offen offset:3072 sc1\n\t" \
    "s_mov_b32 %[a], 1\n\t" \
    ".Lgw3_%=:\n\t" \
    "v_max_u32 v252, v248, v249\n\t" \
    "v_max3_u32 v252, v250, v251, v252\n\t" \
    "v_cmp_eq_u32 vcc, -1, v252\n\t" \
    "s_cbranch_vccz .Lgw4_%=\n\t" \
    "buffer_load_dwordx4 v[248:251], %[l16], %[rs], %[i1] offen offset:0 sc1\n\t" \
    "s_mov_b32 %[a], 1\n\t" \
    ".Lgw4_%=:\n\t" \
    "s_cmp_eq_u32 %[a], 0\n\t" \
    "s_cbranch_scc1 .Lhw%=\n\t" \
    "s_waitcnt vmcnt(20)\n\t" \
    ".Liw%=:\n\t" \
    "v_max_u32 v252, v232, v233\n\t" \
    "v_max3_u32 v252, v234, v235, v252\n\t" \
    "v_max3_u32 v252, v236, v237, v252\n\t" \
    "v_max3_u32 v252, v238, v239, v252\n\t" \
    "v_max3_u32 v252, v240, v241, v252\n\t" \
    "v_max3_u32 v252, v242, v243, v252\n\t" \
    "v_max3_u32 v252, v244, v245, v252\n\t" \
    "v_max3_u32 v252, v246, v247, v252\n\t" \
    "v_max3_u32 v252, v248, v249, v252\n\t" \
    "v_max3_u32 v252, v250, v251, v252\n\t" \
    "v_cmp_eq_u32 vcc, -1, v252\n\t" \
    "s_cbranch_vccz .Lhw%=\n\t" \
    "s_sleep 1\n\t" \
    "v_max_u32 v252, v232, v233\n\t" \
    "v_max3_u32 v252, v234, v235, v252\n\t" \
    "v_max3_u32 v252, v236, v237, v252\n\t" \
    "v_max3_u32 v252, v238, v239, v252\n\t" \
    "v_max3_u32 v252, v240, v241, v252\n\t" \
    "v_max3_u32 v252, v242, v243, v252\n\t" \
    "v_max3_u32 v252, v244, v245, v252\n\t" \
    "v_max3_u32 v252, v246, v247, v252\n\t" \
    "v_max3_u32 v252, v248, v249, v252\n\t" \
    "v_max3_u32 v252, v250, v251, v252\n\t" \
    "v_cmp_eq_u32 vcc, -1, v252\n\t" \
    "s_cbranch_vccz .Lhw%=\n\t" \
    "s_sleep 1\n\t" \
    "v_max_u32 v252, v232, v233\n\t" \
    "v_max3_u32 v252, v234, v235, v252\n\t" \
    "v_max3_u32 v252, v236, v237, v252\n\t" \
    "v_max3_u32 v252, v238, v239, v252\n\t" \
    "v_max3_u32 v252, v240, v241, v252\n\t" \
    "v_max3_u32 v252, v242, v243, v252\n\t" \
    "v_max3_u32 v252, v244, v245, v252\n\t" \
    "v_max3_u32 v252, v246, v247, v252\n\t" \
    "v_max3_u32 v252, v248, v249, v252\n\t" \
    "v_max3_u32 v252, v250, v251, v252\n\t" \
    "v_cmp_eq_u32 vcc, -1, v252\n\t" \
    "s_cbranch_vccz .Lhw%=\n\t" \
    "s_sleep 1\n\t" \
    "v_max_u32 v252, v232, v233\n\t" \
    "v_max3_u32 v252, v234, v235, v252\n\t" \
    "v_max3_u32 v252, v236, v237, v252\n\t" \
    "v_max3_u32 v252, v238, v239, v252\n\t" \
    "v_max3_u32 v252, v240, v241, v252\n\t" \
    "v_max3_u32 v252, v242, v243, v252\n\t" \
    "v_max3_u32 v252, v244, v245, v252\n\t" \
    "v_max3_u32 v252, v246, v247, v252\n\t" \
    "v_max3_u32 v252, v248, v249, v252\n\t" \
    "v_max3_u32 v252, v250, v251, v252\n\t" \
    "v_cmp_eq_u32 vcc, -1, v252\n\t" \
    "s_cbranch_vccz .Lhw%=\n\t" \
    "s_sleep 1\n\t" \
    "v_max_u32 v252, v232, v233\n\t" \
    "v_max3_u32 v252, v234, v235, v252\n\t" \
    "v_max3_u32 v252, v236, v237, v252\n\t" \
    "v_max3_u32 v252, v238, v239, v252\n\t" \
    "v_max3_u32 v252, v240, v241, v252\n\t" \
    "v_max3_u32 v252, v242, v243, v252\n\t" \
    "v_max3_u32 v252, v244, v245, v252\n\t" \
    "v_max3_u32 v252, v246, v247, v252\n\t" \
    "v_max3_u32 v252, v248, v249, v252\n\t" \
    "v_max3_u32 v252, v250, v251, v252\n\t" \
    "v_cmp_eq_u32 vcc, -1, v252\n\t" \
    "s_cbranch_vccz .Lhw%=\n\t" \
    "s_sleep 1\n\t" \
    "v_max_u32 v252, v232, v233\n\t" \
    "v_max3_u32 v252, v234, v235, v252\n\t" \
    "v_max3_u32 v252, v236, v237, v252\n\t" \
    "v_max3_u32 v252, v238, v239, v252\n\t" \
    "v_max3_u32 v252, v240, v241, v252\n\t" \
    "v_max3_u32 v252, v242, v243, v252\n\t" \
    "v_max3_u32 v252, v244, v245, v252\n\t" \
    "v_max3_u32 v252, v246, v247, v252\n\t" \
    "v_max3_u32 v252, v248, v249, v252\n\t" \
    "v_max3_u32 v252, v250, v251, v252\n\t" \
    "v_cmp_eq_u32 vcc, -1, v252\n\t" \
    "s_cbranch_vccz .Lhw%=\n\t" \
    "s_sleep 1\n\t" \
    "s_sub_u32 %[n], %[n], 1\n\t" \
    "s_cmp_gt_i32 %[n], 0\n\t" \
    "s_cbranch_scc1 .Lfw%=\n\t" \
    ".Lhw%=:\n\t" \
    "s_branch .Lta%=\n\t" \
    ".Lt0%=:\n\t" \
    "s_waitcnt vmcnt(1)\n\t" \
    ".Lta%=:\n\t" \
    "ds_write_b128 %[lw], v[232:235] offset:0\n\t" \
    "v_max_u32 v252, v232, v233\n\t" \
    "v_max3_u32 v252, v234, v235, v252\n\t" \
    "ds_write_b128 %[lw], v[236:239] offset:1024\n\t" \
    "v_max3_u32 v252, v236, v237, v252\n\t" \
    "v_max3_u32 v252, v238, v239, v252\n\t" \
    "ds_write_b128 %[lw], v[240:243] offset:2048\n\t" \
    "v_max3_u32 v252, v240, v241, v252\n\t" \
    "v_max3_u32 v252, v242, v243, v252\n\t" \
    "ds_write_b128 %[lw], v[244:247] offset:3072\n\t" \
    "v_max3_u32 v252, v244, v245, v252\n\t" \
    "v_max3_u32 v252, v246, v247, v252\n\t" \
    "ds_write_b128 %[lw], v[248:251] offset:4096\n\t" \
    "v_max3_u32 v252, v248, v249, v252\n\t" \
    "v_max3_u32 v252, v250, v251, v252\n\t" \
    "v_cmp_eq_u32 vcc, -1, v252\n\t" \
    "s_cbranch_vccnz .Ltr%=\n\t" \
    ".Ltg%=:\n\t" \
    ASM_ST2 \
    "s_waitcnt lgkmcnt(0)\n\t" \
    "s_setprio 2\n\t" \
    "s_barrier\n\t" ASM_ST \
    "ds_read_b128 v[160:163], %[la] offset:0\n\t" \
    "ds_read_b128 v[164:167], %[la] offset:1024\n\t" \
    "ds_read_b128 v[168:171], %[la] offset:2048\n\t" \
    "ds_read_b128 v[172:175], %[la] offset:3072\n\t" \
    "ds_read_b128 v[176:179], %[la] offset:8192\n\t" \
    "ds_read_b128 v[180:183], %[la] offset:9216\n\t" \
    "ds_read_b128 v[184:187], %[la] offset:10240\n\t" \
    "s_waitcnt lgkmcnt(6)\n\t" \
    "v_mfma_f32_16x16x32_bf16 v[192:195], v[80:83], v[160:163], 0\n\t" \
    "buffer_load_dwordx2 v[196:197], %[ro], %[rs], %[sx] offen sc1\n\t" \
    "s_waitcnt lgkmcnt(5)\n\t" \
    "v_mfma_f32_16x16x32_bf16 v[192:195], v[84:87], v[164:167], v[192:195]\n\t" \
    "buffer_load_dwordx2 v[198:199], %[ro], %[rs], %[sx] offen offset:512 sc1\n\t" \
    "v_mfma_f32_16x16x32_bf16 v[188:191], v[0:3], v[160:163], 0\n\t" \
    "ds_read_b128 v[160:163], %[la] offset:11264\n\t" \
    "v_lshrrev_b32 v204, 1, %[l16]\n\t" \
    "v_mfma_f32_16x16x32_bf16 v[188:191], v[4:7], v[164:167], v[188:191]\n\t" \
    "ds_read_b128 v[164:167], %[la] offset:16384\n\t" \
    "v_and_b32 v204, 0x78, v204\n\t" \
    "s_waitcnt lgkmcnt(6)\n\t" \
    "v_mfma_f32_16x16x32_bf16 v[188:191], v[8:11], v[168:171], v[188:191]\n\t" \
    "buffer_load_dwordx2 v[200:201], v204, %[rs], %[ss] offen sc1\n\t" \
    "s_waitcnt lgkmcnt(5)\n\t" \
    "v_mfma_f32_16x16x32_bf16 v[188:191], v[12:15], v[172:175], v[188:191]\n\t" \
    "v_mov_b64 v[216:217], -1\n\t" \
    "v_mfma_f32_16x16x32_bf16 v[192:195], v[88:91], v[168:171], v[192:195]\n\t" \
    "ds_read_b128 v[168:171], %[la] offset:17408\n\t" \
    "v_mov_b64 v[218:219], -1\n\t" \
    "v_mfma_f32_16x16x32_bf16 v[192:195], v[92:95], v[172:175], v[192:195]\n\t" \
    "ds_read_b128 v[172:175], %[la] offset:18432\n\t" \
    "buffer_store_dwordx4 v[216:219], %[l16], %[rs], %[sa] offen sc1\n\t" \
    "s_waitcnt lgkmcnt(6)\n\t" \
    "v_mfma_f32_16x16x32_bf16 v[192:195], v[96:99], v[176:179], v[192:195]\n\t" \
    "s_waitcnt lgkmcnt(5)\n\t" \
    "v_mfma_f32_16x16x32_bf16 v[192:195], v[100:103], v[180:183], v[192:195]\n\t" \
    "v_mfma_f32_16x16x32_bf16 v[188:191], v[16:19], v[176:179], v[188:191]\n\t" \
    "ds_read_b128 v[176:179], %[la] offset:19456\n\t" \
    "v_mfma_f32_16x16x32_bf16 v[188:191], v[20:23], v[180:183], v[188:191]\n\t" \
    "ds_read_b128 v[180:183], %[la] offset:24576\n\t" \
    "s_waitcnt lgkmcnt(6)\n\t" \
    "v_mfma_f32_16x16x32_bf16 v[188:191], v[24:27], v[184:187], v[188:191]\n\t" \
    "s_waitcnt lgkmcnt(5)\n\t" \
    "v_mfma_f32_16x16x32_bf16 v[188:191], v[28:31], v[160:163], v[188:191]\n\t" \
    "v_mfma_f32_16x16x32_bf16 v[192:195], v[104:107], v[184:187], v[192:195]\n\t" \
    "ds_read_b128 v[184:187], %[la] offset:25600\n\t" \
    "v_mfma_f32_16x16x32_bf16 v[192:195], v[108:111], v[160:163], v[192:195]\n\t" \
    "ds_read_b128 v[160:163], %[la] offset:26624\n\t" \
    "s_waitcnt lgkmcnt(6)\n\t" \
    "v_mfma_f32_16x16x32_bf16 v[192:195], v[112:115], v[164:167], v[192:195]\n\t" \
    "s_waitcnt lgkmcnt(5)\n\t" \
    "v_mfma_f32_16x16x32_bf16 v[192:195], v[116:119], v[168:171], v[192:195]\n\t" \
    "v_mfma_f32_16x16x32_bf16 v[188:191], v[32:35], v[164:167], v[188:191]\n\t" \
    "ds_read_b128 v[164:167], %[la] offset:27648\n\t" \
    "v_mfma_f32_16x16x32_bf16 v[188:191], v[36:39], v[168:171], v[188:191]\n\t" \
    "ds_read_b128 v[168:171], %[la] offset:4096\n\t" \
    "s_waitcnt lgkmcnt(6)\n\t" \
    "v_mfma_f32_16x16x32_bf16 v[188:191], v[40:43], v[172:175], v[188:191]\n\t" \
    "s_waitcnt lgkmcnt(5)\n\t" \
    "v_mfma_f32_16x16x32_bf16 v[188:191], v[44:47], v[176:179], v[188:191]\n\t" \
    "v_mfma_f32_16x16x32_bf16 v[192:195], v[120:123], v[172:175], v[192:195]\n\t" \
    "ds_read_b128 v[172:175], %[la] offset:12288\n\t" \
    "v_mfma_f32_16x16x32_bf16 v[192:195], v[124:127], v[176:179], v[192:195]\n\t" \
    "ds_read_b128 v[176:179], %[la] offset:20480\n\t" \
    "s_waitcnt lgkmcnt(6)\n\t" \
    "v_mfma_f32_16x16x32_bf16 v[192:195], v[128:131], v[180:183], v[192:195]\n\t" \
    "s_waitcnt lgkmcnt(5)\n\t" \
    "v_mfma_f32_16x16x32_bf16 v[192:195], v[132:135], v[184:187], v[192:195]\n\t" \
    "v_mfma_f32_16x16x32_bf16 v[188:191], v[48:51], v[180:183], v[188:191]\n\t" \
    "ds_read_b128 v[180:183], %[la] offset:28672\n\t" \
    "v_mfma_f32_16x16x32_bf16 v[188:191], v[52:55], v[184:187], v[188:191]\n\t" \
    "s_waitcnt lgkmcnt(5)\n\t" \
    "v_mfma_f32_16x16x32_bf16 v[188:191], v[56:59], v[160:163], v[188:191]\n\t" \
    "s_waitcnt lgkmcnt(4)\n\t" \
    "v_mfma_f32_16x16x32_bf16 v[188:191], v[60:63], v[164:167], v[188:191]\n\t" \
    "v_mfma_f32_16x16x32_bf16 v[192:195], v[136:139], v[160:163], v[192:195]\n\t" \
    "v_mfma_f32_16x16x32_bf16 v[192:195], v[140:143], v[164:167], v[192:195]\n\t" \
    "s_waitcnt lgkmcnt(3)\n\t" \
    "v_mfma_f32_16x16x32_bf16 v[192:195], v[144:147], v[168:171], v[192:195]\n\t" \
    "s_waitcnt lgkmcnt(2)\n\t" \
    "v_mfma_f32_16x16x32_bf16 v[192:195], v[148:151], v[172:175], v[192:195]\n\t" \
    "v_mfma_f32_16x16x32_bf16 v[188:191], v[64:67], v[168:171], v[188:191]\n\t" \
    "v_mfma_f32_16x16x32_bf16 v[188:191], v[68:71], v[172:175], v[188:191]\n\t" \
    "s_waitcnt lgkmcnt(1)\n\t" \
    "v_mfma_f32_16x16x32_bf16 v[188:191], v[72:75], v[176:179], v[188:191]\n\t" \
    "s_waitcnt lgkmcnt(0)\n\t" \
    "v_mfma_f32_16x16x32_bf16 v[188:191], v[76:79], v[180:183], v[188:191]\n\t" \
    "v_mfma_f32_16x16x32_bf16 v[192:195], v[152:155], v[176:179], v[192:195]\n\t" \
    "v_mfma_f32_16x16x32_bf16 v[192:195], v[156:159], v[180:183], v[192:195]\n\t" \
    "s_setprio 2\n\t" \
    "buffer_load_dwordx4 v[232:235], %[l16], %[rs], %[n0] offen offset:0 sc1\n\t" \
    "buffer_load_dwordx4 v[236:239], %[l16], %[rs], %[n0] offen offset:1024 sc1\n\t" \
    "buffer_load_dwordx4 v[240:243], %[l16], %[rs], %[n0] offen offset:2048 sc1\n\t" \
    "buffer_load_dwordx4 v[244:247], %[l16], %[rs], %[n0] offen offset:3072 sc1\n\t" \
    "buffer_load_dwordx4 v[248:251], %[l16], %[rs], %[n1] offen offset:0 sc1\n\t" \
    ASM_ST3 \
    "s_waitcnt vmcnt(6)\n\t" \
    "v_lshlrev_b32 v160, 16, v196\n\t" \
    "v_and_b32 v161, 0xffff0000, v196\n\t" \
    "v_lshlrev_b32 v162, 16, v197\n\t" \
    "v_and_b32 v163, 0xffff0000, v197\n\t" \
    "v_lshlrev_b32 v164, 16, v198\n\t" \
    "v_and_b32 v165, 0xffff0000, v198\n\t" \
    "v_lshlrev_b32 v166, 16, v199\n\t" \
    "v_and_b32 v167, 0xffff0000, v199\n\t" \
    "v_sub_f32 v160, v160, v200\n\t" \
    "v_sub_f32 v161, v161, v200\n\t" \
    "v_sub_f32 v162, v162, v200\n\t" \
    "v_sub_f32 v163, v163, v200\n\t" \
    "v_sub_f32 v164, v164, v200\n\t" \
    "v_sub_f32 v165, v165, v200\n\t" \
    "v_sub_f32 v166, v166, v200\n\t" \
    "v_sub_f32 v167, v167, v200\n\t" \
    "v_mul_f32 v160, v160, v201\n\t" \
    "v_mul_f32 v161, v161, v201\n\t" \
    "v_mul_f32 v162, v162, v201\n\t" \
    "v_mul_f32 v163, v163, v201\n\t" \
    "v_mul_f32 v164, v164, v201\n\t" \
    "v_mul_f32 v165, v165, v201\n\t" \
    "v_mul_f32 v166, v166, v201\n\t" \
    "v_mul_f32 v167, v167, v201\n\t" \
    "v_cvt_pk_bf16_f32 v160, v160, v161\n\t" \
    "v_cvt_pk_bf16_f32 v162, v162, v163\n\t" \
    "v_cvt_pk_bf16_f32 v164, v164, v165\n\t" \
    "v_cvt_pk_bf16_f32 v166, v166, v167\n\t" \
    "v_and_b32 v161, 0xffff0000, v160\n\t" \
    "v_lshlrev_b32 v160, 16, v160\n\t" \
    "v_and_b32 v163, 0xffff0000, v162\n\t" \
    "v_lshlrev_b32 v162, 16, v162\n\t" \
    "v_and_b32 v165, 0xffff0000, v164\n\t" \
    "v_lshlrev_b32 v164, 16, v164\n\t" \
    "v_and_b32 v167, 0xffff0000, v166\n\t" \
    "v_lshlrev_b32 v166, 16, v166\n\t" \
    "v_add_f32 v188, v188, v160\n\t" \
    "v_add_f32 v189, v189, v161\n\t" \
    "v_add_f32 v190, v190, v162\n\t" \
    "v_add_f32 v191, v191, v163\n\t" \
    "v_add_f32 v192, v192, v164\n\t" \
    "v_add_f32 v193, v193, v165\n\t" \
    "v_add_f32 v194, v194, v166\n\t" \
    "v_add_f32 v195, v195, v167\n\t" \
    "v_cvt_pk_bf16_f32 v188, v188, v189\n\t" \
    "v_cvt_pk_bf16_f32 v189, v190, v191\n\t" \
    "v_cvt_pk_bf16_f32 v190, v192, v193\n\t" \
    "v_cvt_pk_bf16_f32 v191, v194, v195\n\t" \
    "v_min_u32 v188, -2, v188\n\t" \
    "v_min_u32 v189, -2, v189\n\t" \
    "v_min_u32 v190, -2, v190\n\t" \
    "v_min_u32 v191, -2, v191\n\t" \
    "s_nop 1\n\t" \
    "v_permlane16_swap_b32 v188, v190\n\t" \
    "v_permlane16_swap_b32 v189, v191\n\t" \
    "s_waitcnt vmcnt(6)\n\t" \
    "buffer_store_dwordx4 v[188:191], %[oa], %[rs], %[so] offen sc1\n\t" \
    "s_nop 1\n\t" \
    "s_setprio 0\n\t"
#define ASM_WT2_HI \
    "s_setprio 1\n\t" \
    "s_barrier\n\t" ASM_ST \
    "ds_read_b128 v[160:163], %[la] offset:0\n\t" \
    "ds_read_b128 v[164:167], %[la] offset:1024\n\t" \
    "ds_read_b128 v[168:171], %[la] offset:2048\n\t" \
    "ds_read_b128 v[172:175], %[la] offset:3072\n\t" \
    "ds_read_b128 v[176:179], %[la] offset:8192\n\t" \
    "ds_read_b128 v[180:183], %[la] offset:9216\n\t" \
    "ds_read_b128 v[184:187], %[la] offset:10240\n\t" \
    "s_waitcnt lgkmcnt(6)\n\t" \
    "v_mfma_f32_16x16x32_bf16 v[192:195], v[80:83], v[160:163], 0\n\t" \
    "buffer_load_dwordx2 v[196:197], %[ro], %[rs], %[sx] offen sc1\n\t" \
    "s_waitcnt lgkmcnt(5)\n\t" \
    "v_mfma_f32_16x16x32_bf16 v[192:195], v[84:87], v[164:167], v[192:195]\n\t" \
    "buffer_load_dwordx2 v[198:199], %[ro], %[rs], %[sx] offen offset:512 sc1\n\t" \
    "v_mfma_f32_16x16x32_bf16 v[188:191], v[0:3], v[160:163], 0\n\t" \
    "ds_read_b128 v[160:163], %[la] offset:11264\n\t" \
    "v_lshrrev_b32 v204, 1, %[l16]\n\t" \
    "v_mfma_f32_16x16x32_bf16 v[188:191], v[4:7], v[164:167], v[188:191]\n\t" \
    "ds_read_b128 v[164:167], %[la] offset:16384\n\t" \
    "v_and_b32 v204, 0x78, v204\n\t" \
    "s_waitcnt lgkmcnt(6)\n\t" \
    "v_mfma_f32_16x16x32_bf16 v[188:191], v[8:11], v[168:171], v[188:191]\n\t" \
    "buffer_load_dwordx2 v[200:201], v204, %[rs], %[ss] offen sc1\n\t" \
    "s_waitcnt lgkmcnt(5)\n\t" \
    "v_mfma_f32_16x16x32_bf16 v[188:191], v[12:15], v[172:175], v[188:191]\n\t" \
    "v_mov_b64 v[216:217], -1\n\t" \
    "v_mfma_f32_16x16x32_bf16 v[192:195], v[88:91], v[168:171], v[192:195]\n\t" \
    "ds_read_b128 v[168:171], %[la] offset:17408\n\t" \
    "v_mov_b64 v[218:219], -1\n\t" \
    "v_mfma_f32_16x16x32_bf16 v[192:195], v[92:95], v[172:175], v[192:195]\n\t" \
    "ds_read_b128 v[172:175], %[la] offset:18432\n\t" \
    "buffer_store_dwordx4 v[216:219], %[l16], %[rs], %[sa] offen sc1\n\t" \
    "s_waitcnt lgkmcnt(6)\n\t" \
    "v_mfma_f32_16x16x32_bf16 v[192:195], v[96:99], v[176:179], v[192:195]\n\t" \
    "s_waitcnt lgkmcnt(5)\n\t" \
    "v_mfma_f32_16x16x32_bf16 v[192:195], v[100:103], v[180:183], v[192:195]\n\t" \
    "v_mfma_f32_16x16x32_bf16 v[188:191], v[16:19], v[176:179], v[188:191]\n\t" \
    "ds_read_b128 v[176:179], %[la] offset:19456\n\t" \
    "v_mfma_f32_16x16x32_bf16 v[188:191], v[20:23], v[180:183], v[188:191]\n\t" \
    "ds_read_b128 v[180:183], %[la] offset:24576\n\t" \
    "s_waitcnt lgkmcnt(6)\n\t" \
    "v_mfma_f32_16x16x32_bf16 v[188:191], v[24:27], v[184:187], v[188:191]\n\t" \
    "s_waitcnt lgkmcnt(5)\n\t" \
    "v_mfma_f32_16x16x32_bf16 v[188:191], v[28:31], v[160:163], v[188:191]\n\t" \
    "v_mfma_f32_16x16x32_bf16 v[192:195], v[104:107], v[184:187], v[192:195]\n\t" \
    "ds_read_b128 v[184:187], %[la] offset:25600\n\t" \
    "v_mfma_f32_16x16x32_bf16 v[192:195], v[108:111], v[160:163], v[192:195]\n\t" \
    "ds_read_b128 v[160:163], %[la] offset:26624\n\t" \
    "s_waitcnt lgkmcnt(6)\n\t" \
    "v_mfma_f32_16x16x32_bf16 v[192:195], v[112:115], v[164:167], v[192:195]\n\t" \
    "s_waitcnt lgkmcnt(5)\n\t" \
    "v_mfma_f32_16x16x32_bf16 v[192:195], v[116:119], v[168:171], v[192:195]\n\t" \
    "v_mfma_f32_16x16x32_bf16 v[188:191], v[32:35], v[164:167], v[188:191]\n\t" \
    "ds_read_b128 v[164:167], %[la] offset:27648\n\t" \
    "v_mfma_f32_16x16x32_bf16 v[188:191], v[36:39], v[168:171], v[188:191]\n\t" \
    "ds_read_b128 v[168:171], %[la] offset:4096\n\t" \
    "s_waitcnt lgkmcnt(6)\n\t" \
    "v_mfma_f32_16x16x32_bf16 v[188:191], v[40:43], v[172:175], v[188:191]\n\t" \
    "s_waitcnt lgkmcnt(5)\n\t" \
    "v_mfma_f32_16x16x32_bf16 v[188:191], v[44:47], v[176:179], v[188:191]\n\t" \
    "v_mfma_f32_16x16x32_bf16 v[192:195], v[120:123], v[172:175], v[192:195]\n\t" \
    "ds_read_b128 v[172:175], %[la] offset:12288\n\t" \
    "v_mfma_f32_16x16x32_bf16 v[192:195], v[124:127], v[176:179], v[192:195]\n\t" \
    "ds_read_b128 v[176:179], %[la] offset:20480\n\t" \
    "s_waitcnt lgkmcnt(6)\n\t" \
    "v_mfma_f32_16x16x32_bf16 v[192:195], v[128:131], v[180:183], v[192:195]\n\t" \
    "s_waitcnt lgkmcnt(5)\n\t" \
    "v_mfma_f32_16x16x32_bf16 v[192:195], v[132:135], v[184:187], v[192:195]\n\t" \
    "v_mfma_f32_16x16x32_bf16 v[188:191], v[48:51], v[180:183], v[188:191]\n\t" \
    "ds_read_b128 v[180:183], %[la] offset:28672\n\t" \
    "v_mfma_f32_16x16x32_bf16 v[188:191], v[52:55], v[184:187], v[188:191]\n\t" \
    "s_waitcnt lgkmcnt(5)\n\t" \
    "v_mfma_f32_16x16x32_bf16 v[188:191], v[56:59], v[160:163], v[188:191]\n\t" \
    "s_waitcnt lgkmcnt(4)\n\t" \
    "v_mfma_f32_16x16x32_bf16 v[188:191], v[60:63], v[164:167], v[188:191]\n\t" \
    "v_mfma_f32_16x16x32_bf16 v[192:195], v[136:139], v[160:163], v[192:195]\n\t" \
    "v_mfma_f32_16x16x32_bf16 v[192:195], v[140:143], v[164:167], v[192:195]\n\t" \
    "s_waitcnt lgkmcnt(3)\n\t" \
    "v_mfma_f32_16x16x32_bf16 v[192:195], v[144:147], v[168:171], v[192:195]\n\t" \
    "s_waitcnt lgkmcnt(2)\n\t" \
    "v_mfma_f32_16x16x32_bf16 v[192:195], v[148:151], v[172:175], v[192:195]\n\t" \
    "v_mfma_f32_16x16x32_bf16 v[188:191], v[64:67], v[168:171], v[188:191]\n\t" \
    "v_mfma_f32_16x16x32_bf16 v[188:191], v[68:71], v[172:175], v[188:191]\n\t" \
    "s_waitcnt lgkmcnt(1)\n\t" \
    "v_mfma_f32_16x16x32_bf16 v[188:191], v[72:75], v[176:179], v[188:191]\n\t" \
    "s_waitcnt lgkmcnt(0)\n\t" \
    "v_mfma_f32_16x16x32_bf16 v[188:191], v[76:79], v[180:183], v[188:191]\n\t" \
    "v_mfma_f32_16x16x32_bf16 v[192:195], v[152:155], v[176:179], v[192:195]\n\t" \
    "v_mfma_f32_16x16x32_bf16 v[192:195], v[156:159], v[180:183], v[192:195]\n\t" \
    ASM_ST3 \
    "s_waitcnt vmcnt(1)\n\t" \
    "v_lshlrev_b32 v160, 16, v196\n\t" \
    "v_and_b32 v161, 0xffff0000, v196\n\t" \
    "v_lshlrev_b32 v162, 16, v197\n\t" \
    "v_and_b32 v163, 0xffff0000, v197\n\t" \
    "v_lshlrev_b32 v164, 16, v198\n\t" \
    "v_and_b32 v165, 0xffff0000, v198\n\t" \
    "v_lshlrev_b32 v166, 16, v199\n\t" \
    "v_and_b32 v167, 0xffff0000, v199\n\t" \
    "v_sub_f32 v160, v160, v200\n\t" \
    "v_sub_f32 v161, v161, v200\n\t" \
    "v_sub_f32 v162, v162, v200\n\t" \
    "v_sub_f32 v163, v163, v200\n\t" \
    "v_sub_f32 v164, v164, v200\n\t" \
    "v_sub_f32 v165, v165, v200\n\t" \
    "v_sub_f32 v166, v166, v200\n\t" \
    "v_sub_f32 v167, v167, v200\n\t" \
    "v_mul_f32 v160, v160, v201\n\t" \
    "v_mul_f32 v161, v161, v201\n\t" \
    "v_mul_f32 v162, v162, v201\n\t" \
    "v_mul_f32 v163, v163, v201\n\t" \
    "v_mul_f32 v164, v164, v201\n\t" \
    "v_mul_f32 v165, v165, v201\n\t" \
    "v_mul_f32 v166, v166, v201\n\t" \
    "v_mul_f32 v167, v167, v201\n\t" \
    "v_cvt_pk_bf16_f32 v160, v160, v161\n\t" \
    "v_cvt_pk_bf16_f32 v162, v162, v163\n\t" \
    "v_cvt_pk_bf16_f32 v164, v164, v165\n\t" \
    "v_cvt_pk_bf16_f32 v166, v166, v167\n\t" \
    "v_and_b32 v161, 0xffff0000, v160\n\t" \
    "v_lshlrev_b32 v160, 16, v160\n\t" \
    "v_and_b32 v163, 0xffff0000, v162\n\t" \
    "v_lshlrev_b32 v162, 16, v162\n\t" \
    "v_and_b32 v165, 0xffff0000, v164\n\t" \
    "v_lshlrev_b32 v164, 16, v164\n\t" \
    "v_and_b32 v167, 0xffff0000, v166\n\t" \
    "v_lshlrev_b32 v166, 16, v166\n\t" \
    "v_add_f32 v188, v188, v160\n\t" \
    "v_add_f32 v189, v189, v161\n\t" \
    "v_add_f32 v190, v190, v162\n\t" \
    "v_add_f32 v191, v191, v163\n\t" \
    "v_add_f32 v192, v192, v164\n\t" \
    "v_add_f32 v193, v193, v165\n\t" \
    "v_add_f32 v194, v194, v166\n\t" \
    "v_add_f32 v195, v195, v167\n\t" \
    "v_cvt_pk_bf16_f32 v188, v188, v189\n\t" \
    "v_cvt_pk_bf16_f32 v189, v190, v191\n\t" \
    "v_cvt_pk_bf16_f32 v190, v192, v193\n\t" \
    "v_cvt_pk_bf16_f32 v191, v194, v195\n\t" \
    "v_min_u32 v188, -2, v188\n\t" \
    "v_min_u32 v189, -2, v189\n\t" \
    "v_min_u32 v190, -2, v190\n\t" \
    "v_min_u32 v191, -2, v191\n\t" \
    "s_nop 1\n\t" \
    "v_permlane16_swap_b32 v188, v190\n\t" \
    "v_permlane16_swap_b32 v189, v191\n\t" \
    "s_waitcnt vmcnt(1)\n\t" \
    "buffer_store_dwordx4 v[188:191], %[oa], %[rs], %[so] offen sc1\n\t" \
    "s_nop 1\n\t"
#define ASM_WT3_LO \
    "s_branch .Lt0%=\n\t" \
    ".Ltr%=:\n\t" \
    "s_cmp_gt_i32 %[n], 0\n\t" \
    "s_cbranch_scc0 .Ltg%=\n\t" \
    ".Lfw%=:\n\t" \
    "s_mov_b32 %[a], 0\n\t" \
    "v_max_u32 v252, v232, v233\n\t" \
    "v_max3_u32 v252, v234, v235, v252\n\t" \
    "v_cmp_eq_u32 vcc, -1, v252\n\t" \
    "s_cbranch_vccz .Lgw0_%=\n\t" \
    "buffer_load_dwordx4 v[232:235], %[l16], %[rs], %[i0] offen offset:0 sc1\n\t" \
    "s_mov_b32 %[a], 1\n\t" \
    ".Lgw0_%=:\n\t" \
    "v_max_u32 v252, v236, v237\n\t" \
    "v_max3_u32 v252, v238, v239, v252\n\t" \
    "v_cmp_eq_u32 vcc, -1, v252\n\t" \
    "s_cbranch_vccz .Lgw1_%=\n\t" \
    "buffer_load_dwordx4 v[236:239], %[l16], %[rs], %[i0] offen offset:1024 sc1\n\t" \
    "s_mov_b32 %[a], 1\n\t" \
    ".Lgw1_%=:\n\t" \
    "v_max_u32 v252, v240, v241\n\t" \
    "v_max3_u32 v252, v242, v243, v252\n\t" \
    "v_cmp_eq_u32 vcc, -1, v252\n\t" \
    "s_cbranch_vccz .Lgw2_%=\n\t" \
    "buffer_load_dwordx4 v[240:243], %[l16], %[rs], %[i0] offen offset:2048 sc1\n\t" \
    "s_mov_b32 %[a], 1\n\t" \
    ".Lgw2_%=:\n\t" \
    "v_max_u32 v252, v244, v245\n\t" \
    "v_max3_u32 v252, v246, v247, v252\n\t" \
    "v_cmp_eq_u32 vcc, -1, v252\n\t" \
    "s_cbranch_vccz .Lgw3_%=\n\t" \
    "buffer_load_dwordx4 v[244:247], %[l16], %[rs], %[i0] offen offset:3072 sc1\n\t" \
    "s_mov_b32 %[a], 1\n\t" \
    ".Lgw3_%=:\n\t" \
    "v_max_u32 v252, v248, v249\n\t" \
    "v_max3_u32 v252, v250, v251, v252\n\t" \
    "v_cmp_eq_u32 vcc, -1, v252\n\t" \
    "s_cbranch_vccz .Lgw4_%=\n\t" \
    "buffer_load_dwordx4 v[248:251], %[l16], %[rs], %[i1] offen offset:0 sc1\n\t" \
    "s_mov_b32 %[a], 1\n\t" \
    ".Lgw4_%=:\n\t" \
    "s_cmp_eq_u32 %[a], 0\n\t" \
    "s_cbranch_scc1 .Lhw%=\n\t" \
    "s_waitcnt vmcnt(20)\n\t" \
    ".Liw%=:\n\t" \
    "v_max_u32 v252, v232, v233\n\t" \
    "v_max3_u32 v252, v234, v235, v252\n\t" \
    "v_max3_u32 v252, v236, v237, v252\n\t" \
    "v_max3_u32 v252, v238, v239, v252\n\t" \
    "v_max3_u32 v252, v240, v241, v252\n\t" \
    "v_max3_u32 v252, v242, v243, v252\n\t" \
    "v_max3_u32 v252, v244, v245, v252\n\t" \
    "v_max3_u32 v252, v246, v247, v252\n\t" \
    "v_max3_u32 v252, v248, v249, v252\n\t" \
    "v_max3_u32 v252, v250, v251, v252\n\t" \
    "v_cmp_eq_u32 vcc, -1, v252\n\t" \
    "s_cbranch_vccz .Lhw%=\n\t" \
    "s_sleep 1\n\t" \
    "v_max_u32 v252, v232, v233\n\t" \
    "v_max3_u32 v252, v234, v235, v252\n\t" \
    "v_max3_u32 v252, v236, v237, v252\n\t" \
    "v_max3_u32 v252, v238, v239, v252\n\t" \
    "v_max3_u32 v252, v240, v241, v252\n\t" \
    "v_max3_u32 v252, v242, v243, v252\n\t" \
    "v_max3_u32 v252, v244, v245, v252\n\t" \
    "v_max3_u32 v252, v246, v247, v252\n\t" \
    "v_max3_u32 v252, v248, v249, v252\n\t" \
    "v_max3_u32 v252, v250, v251, v252\n\t" \
    "v_cmp_eq_u32 vcc, -1, v252\n\t" \
    "s_cbranch_vccz .Lhw%=\n\t" \
    "s_sleep 1\n\t" \
    "v_max_u32 v252, v232, v233\n\t" \
    "v_max3_u32 v252, v234, v235, v252\n\t" \
    "v_max3_u32 v252, v236, v237, v252\n\t" \
    "v_max3_u32 v252, v238, v239, v252\n\t" \
    "v_max3_u32 v252, v240, v241, v252\n\t" \
    "v_max3_u32 v252, v242, v243, v252\n\t" \
    "v_max3_u32 v252, v244, v245, v252\n\t" \
    "v_max3_u32 v252, v246, v247, v252\n\t" \
    "v_max3_u32 v252, v248, v249, v252\n\t" \
    "v_max3_u32 v252, v250, v251, v252\n\t" \
    "v_cmp_eq_u32 vcc, -1, v252\n\t" \
    "s_cbranch_vccz .Lhw%=\n\t" \
    "s_sleep 1\n\t" \
    "v_max_u32 v252, v232, v233\n\t" \
    "v_max3_u32 v252, v234, v235, v252\n\t" \
    "v_max3_u32 v252, v236, v237, v252\n\t" \
    "v_max3_u32 v252, v238, v239, v252\n\t" \
    "v_max3_u32 v252, v240, v241, v252\n\t" \
    "v_max3_u32 v252, v242, v243, v252\n\t" \
    "v_max3_u32 v252, v244, v245, v252\n\t" \
    "v_max3_u32 v252, v246, v247, v252\n\t" \
    "v_max3_u32 v252, v248, v249, v252\n\t" \
    "v_max3_u32 v252, v250, v251, v252\n\t" \
    "v_cmp_eq_u32 vcc, -1, v252\n\t" \
    "s_cbranch_vccz .Lhw%=\n\t" \
    "s_sleep 1\n\t" \
    "v_max_u32 v252, v232, v233\n\t" \
    "v_max3_u32 v252, v234, v235, v252\n\t" \
    "v_max3_u32 v252, v236, v237, v252\n\t" \
    "v_max3_u32 v252, v238, v239, v252\n\t" \
    "v_max3_u32 v252, v240, v241, v252\n\t" \
    "v_max3_u32 v252, v242, v243, v252\n\t" \
    "v_max3_u32 v252, v244, v245, v252\n\t" \
    "v_max3_u32 v252, v246, v247, v252\n\t" \
    "v_max3_u32 v252, v248, v249, v252\n\t" \
    "v_max3_u32 v252, v250, v251, v252\n\t" \
    "v_cmp_eq_u32 vcc, -1, v252\n\t" \
    "s_cbranch_vccz .Lhw%=\n\t" \
    "s_sleep 1\n\t" \
    "v_max_u32 v252, v232, v233\n\t" \
    "v_max3_u32 v252, v234, v235, v252\n\t" \
    "v_max3_u32 v252, v236, v237, v252\n\t" \
    "v_max3_u32 v252, v238, v239, v252\n\t" \
    "v_max3_u32 v252, v240, v241, v252\n\t" \
    "v_max3_u32 v252, v242, v243, v252\n\t" \
    "v_max3_u32 v252, v244, v245, v252\n\t" \
    "v_max3_u32 v252, v246, v247, v252\n\t" \
    "v_max3_u32 v252, v248, v249, v252\n\t" \
    "v_max3_u32 v252, v250, v251, v252\n\t" \
    "v_cmp_eq_u32 vcc, -1, v252\n\t" \
    "s_cbranch_vccz .Lhw%=\n\t" \
    "s_sleep 1\n\t" \
    "s_sub_u32 %[n], %[n], 1\n\t" \
    "s_cmp_gt_i32 %[n], 0\n\t" \
    "s_cbranch_scc1 .Lfw%=\n\t" \
    ".Lhw%=:\n\t" \
    "s_branch .Lta%=\n\t" \
    ".Lt0%=:\n\t" \
    "s_waitcnt vmcnt(1)\n\t" \
    ".Lta%=:\n\t" \
    "ds_write_b128 %[lw], v[232:235] offset:0\n\t" \
    "v_max_u32 v252, v232, v233\n\t" \
    "v_max3_u32 v252, v234, v235, v252\n\t" \
    "v_mfma_f32_16x16x32_bf16 v[188:191], v[232:235], v[232:235], 0\n\t" \
    "ds_write_b128 %[lw], v[236:239] offset:1024\n\t" \
    "v_max3_u32 v252, v236, v237, v252\n\t" \
    "v_max3_u32 v252, v238, v239, v252\n\t" \
    "v_mfma_f32_16x16x32_bf16 v[188:191], v[236:239], v[236:239], v[188:191]\n\t" \
    "ds_write_b128 %[lw], v[240:243] offset:2048\n\t" \
    "v_max3_u32 v252, v240, v241, v252\n\t" \
    "v_max3_u32 v252, v242, v243, v252\n\t" \
    "v_mfma_f32_16x16x32_bf16 v[188:191], v[240:243], v[240:243], v[188:191]\n\t" \
    "ds_write_b128 %[lw], v[244:247] offset:3072\n\t" \
    "v_max3_u32 v252, v244, v245, v252\n\t" \
    "v_max3_u32 v252, v246, v247, v252\n\t" \
    "v_mfma_f32_16x16x32_bf16 v[188:191], v[244:247], v[244:247], v[188:191]\n\t" \
    "ds_write_b128 %[lw], v[248:251] offset:4096\n\t" \
    "v_max3_u32 v252, v248, v249, v252\n\t" \
    "v_max3_u32 v252, v250, v251, v252\n\t" \
    "v_mfma_f32_16x16x32_bf16 v[188:191], v[248:251], v[248:251], v[188:191]\n\t" \
    "v_mfma_f32_16x16x32_bf16 v[192:195], v[216:219], v[232:235], 0\n\t" \
    "v_mfma_f32_16x16x32_bf16 v[192:195], v[216:219], v[236:239], v[192:195]\n\t" \
    "v_mfma_f32_16x16x32_bf16 v[192:195], v[216:219], v[240:243], v[192:195]\n\t" \
    "v_mfma_f32_16x16x32_bf16 v[192:195], v[216:219], v[244:247], v[192:195]\n\t" \
    "v_mfma_f32_16x16x32_bf16 v[192:195], v[216:219], v[248:251], v[192:195]\n\t" \
    "v_cmp_eq_u32 vcc, -1, v252\n\t" \
    "s_cbranch_vccnz .Ltr%=\n\t" \
    ".Ltg%=:\n\t" \
    ASM_ST2 \
    "s_nop 5\n\t" \
    "ds_write_b128 %[lw], v[188:191] offset:5120\n\t" \
    "ds_write_b128 %[lw], v[192:195] offset:6144\n\t" \
    "s_waitcnt lgkmcnt(0)\n\t" \
    "s_setprio 2\n\t" \
    "s_barrier\n\t" ASM_ST \
    "ds_read_b128 v[160:163], %[la] offset:0\n\t" \
    "ds_read_b128 v[164:167], %[la] offset:1024\n\t" \
    "ds_read_b128 v[168:171], %[la] offset:2048\n\t" \
    "ds_read2st64_b32 v[204:205], %[sr] offset0:20 offset1:24\n\t" \
    "ds_read_b128 v[172:175], %[la] offset:3072\n\t" \
    "ds_read_b128 v[176:179], %[la] offset:8192\n\t" \
    "ds_read_b128 v[180:183], %[la] offset:9216\n\t" \
    "ds_read_b128 v[184:187], %[la] offset:10240\n\t" \
    "s_waitcnt lgkmcnt(7)\n\t" \
    "v_mfma_f32_16x16x32_bf16 v[192:195], v[80:83], v[160:163], 0\n\t" \
    "s_waitcnt lgkmcnt(6)\n\t" \
    "v_mfma_f32_16x16x32_bf16 v[192:195], v[84:87], v[164:167], v[192:195]\n\t" \
    "v_mfma_f32_16x16x32_bf16 v[188:191], v[0:3], v[160:163], 0\n\t" \
    "ds_read_b128 v[160:163], %[la] offset:11264\n\t" \
    "v_mfma_f32_16x16x32_bf16 v[188:191], v[4:7], v[164:167], v[188:191]\n\t" \
    "ds_read_b128 v[164:167], %[la] offset:16384\n\t" \
    "s_waitcnt lgkmcnt(7)\n\t" \
    "v_mfma_f32_16x16x32_bf16 v[188:191], v[8:11], v[168:171], v[188:191]\n\t" \
    "s_waitcnt lgkmcnt(6)\n\t" \
    "s_waitcnt lgkmcnt(5)\n\t" \
    "v_mfma_f32_16x16x32_bf16 v[188:191], v[12:15], v[172:175], v[188:191]\n\t" \
    "v_permlane16_swap_b32 v204, v205\n\t" \
    "v_mfma_f32_16x16x32_bf16 v[192:195], v[88:91], v[168:171], v[192:195]\n\t" \
    "ds_read_b128 v[168:171], %[la] offset:17408\n\t" \
    "v_mfma_f32_16x16x32_bf16 v[192:195], v[92:95], v[172:175], v[192:195]\n\t" \
    "ds_read_b128 v[172:175], %[la] offset:18432\n\t" \
    "v_add_f32 v206, v204, v205\n\t" \
    "s_waitcnt lgkmcnt(6)\n\t" \
    "v_mfma_f32_16x16x32_bf16 v[192:195], v[96:99], v[176:179], v[192:195]\n\t" \
    "v_add_f32 v207, v204, v205\n\t" \
    "s_waitcnt lgkmcnt(5)\n\t" \
    "v_mfma_f32_16x16x32_bf16 v[192:195], v[100:103], v[180:183], v[192:195]\n\t" \
    "v_mfma_f32_16x16x32_bf16 v[188:191], v[16:19], v[176:179], v[188:191]\n\t" \
    "ds_read_b128 v[176:179], %[la] offset:19456\n\t" \
    "v_permlane32_swap_b32 v206, v207\n\t" \
    "v_mfma_f32_16x16x32_bf16 v[188:191], v[20:23], v[180:183], v[188:191]\n\t" \
    "ds_read_b128 v[180:183], %[la] offset:24576\n\t" \
    "s_waitcnt lgkmcnt(6)\n\t" \
    "v_mfma_f32_16x16x32_bf16 v[188:191], v[24:27], v[184:187], v[188:191]\n\t" \
    "v_add_f32 v204, v206, v207\n\t" \
    "s_waitcnt lgkmcnt(5)\n\t" \
    "v_mfma_f32_16x16x32_bf16 v[188:191], v[28:31], v[160:163], v[188:191]\n\t" \
    "v_add_f32 v205, v206, v207\n\t" \
    "v_mfma_f32_16x16x32_bf16 v[192:195], v[104:107], v[184:187], v[192:195]\n\t" \
    "ds_read_b128 v[184:187], %[la] offset:25600\n\t" \
    "v_mfma_f32_16x16x32_bf16 v[192:195], v[108:111], v[160:163], v[192:195]\n\t" \
    "ds_read_b128 v[160:163], %[la] offset:26624\n\t" \
    "v_permlane16_swap_b32 v204, v205\n\t" \
    "s_waitcnt lgkmcnt(6)\n\t" \
    "v_mfma_f32_16x16x32_bf16 v[192:195], v[112:115], v[164:167], v[192:195]\n\t" \
    "s_waitcnt lgkmcnt(5)\n\t" \
    "v_mfma_f32_16x16x32_bf16 v[192:195], v[116:119], v[168:171], v[192:195]\n\t" \
    "v_mul_f32 v205, %[kd], v205\n\t" \
    "v_mfma_f32_16x16x32_bf16 v[188:191], v[32:35], v[164:167], v[188:191]\n\t" \
    "ds_read_b128 v[164:167], %[la] offset:27648\n\t" \
    "v_mul_f32 v204, %[kd], v204\n\t" \
    "v_mfma_f32_16x16x32_bf16 v[188:191], v[36:39], v[168:171], v[188:191]\n\t" \
    "ds_read_b128 v[168:171], %[la] offset:4096\n\t" \
    "v_fma_f32 v204, -v205, v205, v204\n\t" \
    "s_waitcnt lgkmcnt(6)\n\t" \
    "v_mfma_f32_16x16x32_bf16 v[188:191], v[40:43], v[172:175], v[188:191]\n\t" \
    "v_max_f32 v204, 0, v204\n\t" \
    "s_waitcnt lgkmcnt(5)\n\t" \
    "v_mfma_f32_16x16x32_bf16 v[188:191], v[44:47], v[176:179], v[188:191]\n\t" \
    "v_add_f32 v206, %[ke], v204\n\t" \
    "v_mfma_f32_16x16x32_bf16 v[192:195], v[120:123], v[172:175], v[192:195]\n\t" \
    "ds_read_b128 v[172:175], %[la] offset:12288\n\t" \
    "v_rsq_f32 v206, v206\n\t" \
    "v_mfma_f32_16x16x32_bf16 v[192:195], v[124:127], v[176:179], v[192:195]\n\t" \
    "ds_read_b128 v[176:179], %[la] offset:20480\n\t" \
    "s_waitcnt lgkmcnt(6)\n\t" \
    "v_mfma_f32_16x16x32_bf16 v[192:195], v[128:131], v[180:183], v[192:195]\n\t" \
    "v_mul_f32 v207, -v205, v206\n\t" \
    "s_waitcnt lgkmcnt(5)\n\t" \
    "v_mfma_f32_16x16x32_bf16 v[192:195], v[132:135], v[184:187], v[192:195]\n\t" \
    "v_mul_f32 v208, v196, v207\n\t" \
    "v_mfma_f32_16x16x32_bf16 v[188:191], v[48:51], v[180:183], v[188:191]\n\t" \
    "ds_read_b128 v[180:183], %[la] offset:28672\n\t" \
    "v_mul_f32 v209, v197, v207\n\t" \
    "v_mul_f32 v210, v198, v207\n\t" \
    "v_mfma_f32_16x16x32_bf16 v[188:191], v[52:55], v[184:187], v[188:191]\n\t" \
    "v_mul_f32 v211, v199, v207\n\t" \
    "v_mul_f32 v212, v200, v207\n\t" \
    "s_waitcnt lgkmcnt(5)\n\t" \
    "v_mfma_f32_16x16x32_bf16 v[188:191], v[56:59], v[160:163], v[188:191]\n\t" \
    "v_mul_f32 v213, v201, v207\n\t" \
    "v_mul_f32 v214, v202, v207\n\t" \
    "s_waitcnt lgkmcnt(4)\n\t" \
    "v_mfma_f32_16x16x32_bf16 v[188:191], v[60:63], v[164:167], v[188:191]\n\t" \
    "v_mul_f32 v215, v203, v207\n\t" \
    "v_mov_b64 v[216:217], -1\n\t" \
    "v_mfma_f32_16x16x32_bf16 v[192:195], v[136:139], v[160:163], v[192:195]\n\t" \
    "v_mov_b64 v[218:219], -1\n\t" \
    "buffer_store_dwordx4 v[216:219], %[l16], %[rs], %[sa] offen sc1\n\t" \
    "v_mfma_f32_16x16x32_bf16 v[192:195], v[140:143], v[164:167], v[192:195]\n\t" \
    "s_waitcnt lgkmcnt(3)\n\t" \
    "v_mfma_f32_16x16x32_bf16 v[192:195], v[144:147], v[168:171], v[192:195]\n\t" \
    "s_waitcnt lgkmcnt(2)\n\t" \
    "v_mfma_f32_16x16x32_bf16 v[192:195], v[148:151], v[172:175], v[192:195]\n\t" \
    "v_mfma_f32_16x16x32_bf16 v[188:191], v[64:67], v[168:171], v[188:191]\n\t" \
    "v_mfma_f32_16x16x32_bf16 v[188:191], v[68:71], v[172:175], v[188:191]\n\t" \
    "s_waitcnt lgkmcnt(1)\n\t" \
    "v_mfma_f32_16x16x32_bf16 v[188:191], v[72:75], v[176:179], v[188:191]\n\t" \
    "s_waitcnt lgkmcnt(0)\n\t" \
    "v_mfma_f32_16x16x32_bf16 v[188:191], v[76:79], v[180:183], v[188:191]\n\t" \
    "v_mfma_f32_16x16x32_bf16 v[192:195], v[152:155], v[176:179], v[192:195]\n\t" \
    "v_mfma_f32_16x16x32_bf16 v[192:195], v[156:159], v[180:183], v[192:195]\n\t" \
    "s_setprio 2\n\t" \
    "buffer_load_dwordx4 v[232:235], %[l16], %[rs], %[n0] offen offset:0 sc1\n\t" \
    "buffer_load_dwordx4 v[236:239], %[l16], %[rs], %[n0] offen offset:1024 sc1\n\t" \
    "buffer_load_dwordx4 v[240:243], %[l16], %[rs], %[n0] offen offset:2048 sc1\n\t" \
    "buffer_load_dwordx4 v[244:247], %[l16], %[rs], %[n0] offen offset:3072 sc1\n\t" \
    "buffer_load_dwordx4 v[248:251], %[l16], %[rs], %[n1] offen offset:0 sc1\n\t" \
    ASM_ST3 \
    "v_fma_f32 v188, v188, v206, v208\n\t" \
    "v_fma_f32 v189, v189, v206, v209\n\t" \
    "v_fma_f32 v190, v190, v206, v210\n\t" \
    "v_fma_f32 v191, v191, v206, v211\n\t" \
    "v_mul_f32 v160, v188, v188\n\t" \
    "v_mul_f32 v161, v189, v189\n\t" \
    "v_mul_f32 v162, v190, v190\n\t" \
    "v_mul_f32 v163, v191, v191\n\t" \
    "v_fma_f32 v160, v160, %[k1], v220\n\t" \
    "v_fma_f32 v161, v161, %[k1], v220\n\t" \
    "v_fma_f32 v162, v162, %[k1], v220\n\t" \
    "v_fma_f32 v163, v163, %[k1], v220\n\t" \
    "v_mul_f32 v160, v188, v160\n\t" \
    "v_mul_f32 v161, v189, v161\n\t" \
    "v_mul_f32 v162, v190, v162\n\t" \
    "v_mul_f32 v163, v191, v163\n\t" \
    "v_fma_f32 v192, v192, v206, v212\n\t" \
    "v_fma_f32 v193, v193, v206, v213\n\t" \
    "v_fma_f32 v194, v194, v206, v214\n\t" \
    "v_fma_f32 v195, v195, v206, v215\n\t" \
    "v_mul_f32 v164, v192, v192\n\t" \
    "v_mul_f32 v165, v193, v193\n\t" \
    "v_mul_f32 v166, v194, v194\n\t" \
    "v_mul_f32 v167, v195, v195\n\t" \
    "v_fma_f32 v164, v164, %[k1], v220\n\t" \
    "v_fma_f32 v165, v165, %[k1], v220\n\t" \
    "v_fma_f32 v166, v166, %[k1], v220\n\t" \
    "v_fma_f32 v167, v167, %[k1], v220\n\t" \
    "v_mul_f32 v164, v192, v164\n\t" \
    "v_mul_f32 v165, v193, v165\n\t" \
    "v_mul_f32 v166, v194, v166\n\t" \
    "v_mul_f32 v167, v195, v167\n\t" \
    "v_exp_f32 v160, v160\n\t" \
    "v_exp_f32 v161, v161\n\t" \
    "v_exp_f32 v162, v162\n\t" \
    "v_exp_f32 v163, v163\n\t" \
    "v_exp_f32 v164, v164\n\t" \
    "v_exp_f32 v165, v165\n\t" \
    "v_exp_f32 v166, v166\n\t" \
    "v_exp_f32 v167, v167\n\t" \
    "v_add_f32 v160, 1.0, v160\n\t" \
    "v_add_f32 v161, 1.0, v161\n\t" \
    "v_add_f32 v162, 1.0, v162\n\t" \
    "v_add_f32 v163, 1.0, v163\n\t" \
    "v_add_f32 v164, 1.0, v164\n\t" \
    "v_add_f32 v165, 1.0, v165\n\t" \
    "v_add_f32 v166, 1.0, v166\n\t" \
    "v_add_f32 v167, 1.0, v167\n\t" \
    "v_rcp_f32 v160, v160\n\t" \
    "v_rcp_f32 v161, v161\n\t" \
    "v_rcp_f32 v162, v162\n\t" \
    "v_rcp_f32 v163, v163\n\t" \
    "v_rcp_f32 v164, v164\n\t" \
    "v_rcp_f32 v165, v165\n\t" \
    "v_rcp_f32 v166, v166\n\t" \
    "v_rcp_f32 v167, v167\n\t" \
    "v_mul_f32 v188, v188, v160\n\t" \
    "v_mul_f32 v189, v189, v161\n\t" \
    "v_mul_f32 v190, v190, v162\n\t" \
    "v_mul_f32 v191, v191, v163\n\t" \
    "v_mul_f32 v192, v192, v164\n\t" \
    "v_mul_f32 v193, v193, v165\n\t" \
    "v_mul_f32 v194, v194, v166\n\t" \
    "v_mul_f32 v195, v195, v167\n\t" \
    "v_cvt_pk_bf16_f32 v188, v188, v189\n\t" \
    "v_cvt_pk_bf16_f32 v189, v190, v191\n\t" \
    "v_cvt_pk_bf16_f32 v190, v192, v193\n\t" \
    "v_cvt_pk_bf16_f32 v191, v194, v195\n\t" \
    "v_min_u32 v188, -2, v188\n\t" \
    "v_min_u32 v189, -2, v189\n\t" \
    "v_min_u32 v190, -2, v190\n\t" \
    "v_min_u32 v191, -2, v191\n\t" \
    "v_mov_b32 v216, 0x3f803f80\n\t" \
    "v_mov_b32 v217, 0x3f803f80\n\t" \
    "v_mov_b32 v218, 0x3f803f80\n\t" \
    "v_mov_b32 v219, 0x3f803f80\n\t" \
    "v_permlane16_swap_b32 v188, v190\n\t" \
    "v_permlane16_swap_b32 v189, v191\n\t" \
    "s_waitcnt vmcnt(6)\n\t" \
    "buffer_store_dwordx4 v[188:191], %[oa], %[rs], %[so] offen sc1\n\t" \
    "s_nop 1\n\t" \
    "s_setprio 0\n\t"
#define ASM_WT3_HI \
    "s_setprio 1\n\t" \
    "s_barrier\n\t" ASM_ST \
    "ds_read_b128 v[160:163], %[la] offset:0\n\t" \
    "ds_read_b128 v[164:167], %[la] offset:1024\n\t" \
    "ds_read_b128 v[168:171], %[la] offset:2048\n\t" \
    "ds_read2st64_b32 v[204:205], %[sr] offset0:20 offset1:24\n\t" \
    "ds_read_b128 v[172:175], %[la] offset:3072\n\t" \
    "ds_read_b128 v[176:179], %[la] offset:8192\n\t" \
    "ds_read_b128 v[180:183], %[la] offset:9216\n\t" \
    "ds_read_b128 v[184:187], %[la] offset:10240\n\t" \
    "s_waitcnt lgkmcnt(7)\n\t" \
    "v_mfma_f32_16x16x32_bf16 v[192:195], v[80:83], v[160:163], 0\n\t" \
    "s_waitcnt lgkmcnt(6)\n\t" \
    "v_mfma_f32_16x16x32_bf16 v[192:195], v[84:87], v[164:167], v[192:195]\n\t" \
    "v_mfma_f32_16x16x32_bf16 v[188:191], v[0:3], v[160:163], 0\n\t" \
    "ds_read_b128 v[160:163], %[la] offset:11264\n\t" \
    "v_mfma_f32_16x16x32_bf16 v[188:191], v[4:7], v[164:167], v[188:191]\n\t" \
    "ds_read_b128 v[164:167], %[la] offset:16384\n\t" \
    "s_waitcnt lgkmcnt(7)\n\t" \
    "v_mfma_f32_16x16x32_bf16 v[188:191], v[8:11], v[168:171], v[188:191]\n\t" \
    "s_waitcnt lgkmcnt(6)\n\t" \
    "s_waitcnt lgkmcnt(5)\n\t" \
    "v_mfma_f32_16x16x32_bf16 v[188:191], v[12:15], v[172:175], v[188:191]\n\t" \
    "v_permlane16_swap_b32 v204, v205\n\t" \
    "v_mfma_f32_16x16x32_bf16 v[192:195], v[88:91], v[168:171], v[192:195]\n\t" \
    "ds_read_b128 v[168:171], %[la] offset:17408\n\t" \
    "v_mfma_f32_16x16x32_bf16 v[192:195], v[92:95], v[172:175], v[192:195]\n\t" \
    "ds_read_b128 v[172:175], %[la] offset:18432\n\t" \
    "v_add_f32 v206, v204, v205\n\t" \
    "s_waitcnt lgkmcnt(6)\n\t" \
    "v_mfma_f32_16x16x32_bf16 v[192:195], v[96:99], v[176:179], v[192:195]\n\t" \
    "v_add_f32 v207, v204, v205\n\t" \
    "s_waitcnt lgkmcnt(5)\n\t" \
    "v_mfma_f32_16x16x32_bf16 v[192:195], v[100:103], v[180:183], v[192:195]\n\t" \
    "v_mfma_f32_16x16x32_bf16 v[188:191], v[16:19], v[176:179], v[188:191]\n\t" \
    "ds_read_b128 v[176:179], %[la] offset:19456\n\t" \
    "v_permlane32_swap_b32 v206, v207\n\t" \
    "v_mfma_f32_16x16x32_bf16 v[188:191], v[20:23], v[180:183], v[188:191]\n\t" \
    "ds_read_b128 v[180:183], %[la] offset:24576\n\t" \
    "s_waitcnt lgkmcnt(6)\n\t" \
    "v_mfma_f32_16x16x32_bf16 v[188:191], v[24:27], v[184:187], v[188:191]\n\t" \
    "v_add_f32 v204, v206, v207\n\t" \
    "s_waitcnt lgkmcnt(5)\n\t" \
    "v_mfma_f32_16x16x32_bf16 v[188:191], v[28:31], v[160:163], v[188:191]\n\t" \
    "v_add_f32 v205, v206, v207\n\t" \
    "v_mfma_f32_16x16x32_bf16 v[192:195], v[104:107], v[184:187], v[192:195]\n\t" \
    "ds_read_b128 v[184:187], %[la] offset:25600\n\t" \
    "v_mfma_f32_16x16x32_bf16 v[192:195], v[108:111], v[160:163], v[192:195]\n\t" \
    "ds_read_b128 v[160:163], %[la] offset:26624\n\t" \
    "v_permlane16_swap_b32 v204, v205\n\t" \
    "s_waitcnt lgkmcnt(6)\n\t" \
    "v_mfma_f32_16x16x32_bf16 v[192:195], v[112:115], v[164:167], v[192:195]\n\t" \
    "s_waitcnt lgkmcnt(5)\n\t" \
    "v_mfma_f32_16x16x32_bf16 v[192:195], v[116:119], v[168:171], v[192:195]\n\t" \
    "v_mul_f32 v205, %[kd], v205\n\t" \
    "v_mfma_f32_16x16x32_bf16 v[188:191], v[32:35], v[164:167], v[188:191]\n\t" \
    "ds_read_b128 v[164:167], %[la] offset:27648\n\t" \
    "v_mul_f32 v204, %[kd], v204\n\t" \
    "v_mfma_f32_16x16x32_bf16 v[188:191], v[36:39], v[168:171], v[188:191]\n\t" \
    "ds_read_b128 v[168:171], %[la] offset:4096\n\t" \
    "v_fma_f32 v204, -v205, v205, v204\n\t" \
    "s_waitcnt lgkmcnt(6)\n\t" \
    "v_mfma_f32_16x16x32_bf16 v[188:191], v[40:43], v[172:175], v[188:191]\n\t" \
    "v_max_f32 v204, 0, v204\n\t" \
    "s_waitcnt lgkmcnt(5)\n\t" \
    "v_mfma_f32_16x16x32_bf16 v[188:191], v[44:47], v[176:179], v[188:191]\n\t" \
    "v_add_f32 v206, %[ke], v204\n\t" \
    "v_mfma_f32_16x16x32_bf16 v[192:195], v[120:123], v[172:175], v[192:195]\n\t" \
    "ds_read_b128 v[172:175], %[la] offset:12288\n\t" \
    "v_rsq_f32 v206, v206\n\t" \
    "v_mfma_f32_16x16x32_bf16 v[192:195], v[124:127], v[176:179], v[192:195]\n\t" \
    "ds_read_b128 v[176:179], %[la] offset:20480\n\t" \
    "s_waitcnt lgkmcnt(6)\n\t" \
    "v_mfma_f32_16x16x32_bf16 v[192:195], v[128:131], v[180:183], v[192:195]\n\t" \
    "v_mul_f32 v207, -v205, v206\n\t" \
    "s_waitcnt lgkmcnt(5)\n\t" \
    "v_mfma_f32_16x16x32_bf16 v[192:195], v[132:135], v[184:187], v[192:195]\n\t" \
    "v_mul_f32 v208, v196, v207\n\t" \
    "v_mfma_f32_16x16x32_bf16 v[188:191], v[48:51], v[180:183], v[188:191]\n\t" \
    "ds_read_b128 v[180:183], %[la] offset:28672\n\t" \
    "v_mul_f32 v209, v197, v207\n\t" \
    "v_mul_f32 v210, v198, v207\n\t" \
    "v_mfma_f32_16x16x32_bf16 v[188:191], v[52:55], v[184:187], v[188:191]\n\t" \
    "v_mul_f32 v211, v199, v207\n\t" \
    "v_mul_f32 v212, v200, v207\n\t" \
    "s_waitcnt lgkmcnt(5)\n\t" \
    "v_mfma_f32_16x16x32_bf16 v[188:191], v[56:59], v[160:163], v[188:191]\n\t" \
    "v_mul_f32 v213, v201, v207\n\t" \
    "v_mul_f32 v214, v202, v207\n\t" \
    "s_waitcnt lgkmcnt(4)\n\t" \
    "v_mfma_f32_16x16x32_bf16 v[188:191], v[60:63], v[164:167], v[188:191]\n\t" \
    "v_mul_f32 v215, v203, v207\n\t" \
    "v_mov_b64 v[216:217], -1\n\t" \
    "v_mfma_f32_16x16x32_bf16 v[192:195], v[136:139], v[160:163], v[192:195]\n\t" \
    "v_mov_b64 v[218:219], -1\n\t" \
    "buffer_store_dwordx4 v[216:219], %[l16], %[rs], %[sa] offen sc1\n\t" \
    "v_mfma_f32_16x16x32_bf16 v[192:195], v[140:143], v[164:167], v[192:195]\n\t" \
    "s_waitcnt lgkmcnt(3)\n\t" \
    "v_mfma_f32_16x16x32_bf16 v[192:195], v[144:147], v[168:171], v[192:195]\n\t" \
    "s_waitcnt lgkmcnt(2)\n\t" \
    "v_mfma_f32_16x16x32_bf16 v[192:195], v[148:151], v[172:175], v[192:195]\n\t" \
    "v_mfma_f32_16x16x32_bf16 v[188:191], v[64:67], v[168:171], v[188:191]\n\t" \
    "v_mfma_f32_16x16x32_bf16 v[188:191], v[68:71], v[172:175], v[188:191]\n\t" \
    "s_waitcnt lgkmcnt(1)\n\t" \
    "v_mfma_f32_16x16x32_bf16 v[188:191], v[72:75], v[176:179], v[188:191]\n\t" \
    "s_waitcnt lgkmcnt(0)\n\t" \
    "v_mfma_f32_16x16x32_bf16 v[188:191], v[76:79], v[180:183], v[188:191]\n\t" \
    "v_mfma_f32_16x16x32_bf16 v[192:195], v[152:155], v[176:179], v[192:195]\n\t" \
    "v_mfma_f32_16x16x32_bf16 v[192:195], v[156:159], v[180:183], v[192:195]\n\t" \
    ASM_ST3 \
    "s_nop 1\n\t" \
    "v_fma_f32 v188, v188, v206, v208\n\t" \
    "v_fma_f32 v189, v189, v206, v209\n\t" \
    "v_fma_f32 v190, v190, v206, v210\n\t" \
    "v_fma_f32 v191, v191, v206, v211\n\t" \
    "v_mul_f32 v160, v188, v188\n\t" \
    "v_mul_f32 v161, v189, v189\n\t" \
    "v_mul_f32 v162, v190, v190\n\t" \
    "v_mul_f32 v163, v191, v191\n\t" \
    "v_fma_f32 v160, v160, %[k1], v220\n\t" \
    "v_fma_f32 v161, v161, %[k1], v220\n\t" \
    "v_fma_f32 v162, v162, %[k1], v220\n\t" \
    "v_fma_f32 v163, v163, %[k1], v220\n\t" \
    "v_mul_f32 v160, v188, v160\n\t" \
    "v_mul_f32 v161, v189, v161\n\t" \
    "v_mul_f32 v162, v190, v162\n\t" \
    "v_mul_f32 v163, v191, v163\n\t" \
    "v_fma_f32 v192, v192, v206, v212\n\t" \
    "v_fma_f32 v193, v193, v206, v213\n\t" \
    "v_fma_f32 v194, v194, v206, v214\n\t" \
    "v_fma_f32 v195, v195, v206, v215\n\t" \
    "v_mul_f32 v164, v192, v192\n\t" \
    "v_mul_f32 v165, v193, v193\n\t" \
    "v_mul_f32 v166, v194, v194\n\t" \
    "v_mul_f32 v167, v195, v195\n\t" \
    "v_fma_f32 v164, v164, %[k1], v220\n\t" \
    "v_fma_f32 v165, v165, %[k1], v220\n\t" \
    "v_fma_f32 v166, v166, %[k1], v220\n\t" \
    "v_fma_f32 v167, v167, %[k1], v220\n\t" \
    "v_mul_f32 v164, v192, v164\n\t" \
    "v_mul_f32 v165, v193, v165\n\t" \
    "v_mul_f32 v166, v194, v166\n\t" \
    "v_mul_f32 v167, v195, v167\n\t" \
    "v_exp_f32 v160, v160\n\t" \
    "v_exp_f32 v161, v161\n\t" \
    "v_exp_f32 v162, v162\n\t" \
    "v_exp_f32 v163, v163\n\t" \
    "v_exp_f32 v164, v164\n\t" \
    "v_exp_f32 v165, v165\n\t" \
    "v_exp_f32 v166, v166\n\t" \
    "v_exp_f32 v167, v167\n\t" \
    "v_add_f32 v160, 1.0, v160\n\t" \
    "v_add_f32 v161, 1.0, v161\n\t" \
    "v_add_f32 v162, 1.0, v162\n\t" \
    "v_add_f32 v163, 1.0, v163\n\t" \
    "v_add_f32 v164, 1.0, v164\n\t" \
    "v_add_f32 v165, 1.0, v165\n\t" \
    "v_add_f32 v166, 1.0, v166\n\t" \
    "v_add_f32 v167, 1.0, v167\n\t" \
    "v_rcp_f32 v160, v160\n\t" \
    "v_rcp_f32 v161, v161\n\t" \
    "v_rcp_f32 v162, v162\n\t" \
    "v_rcp_f32 v163, v163\n\t" \
    "v_rcp_f32 v164, v164\n\t" \
    "v_rcp_f32 v165, v165\n\t" \
    "v_rcp_f32 v166, v166\n\t" \
    "v_rcp_f32 v167, v167\n\t" \
    "v_mul_f32 v188, v188, v160\n\t" \
    "v_mul_f32 v189, v189, v161\n\t" \
    "v_mul_f32 v190, v190, v162\n\t" \
    "v_mul_f32 v191, v191, v163\n\t" \
    "v_mul_f32 v192, v192, v164\n\t" \
    "v_mul_f32 v193, v193, v165\n\t" \
    "v_mul_f32 v194, v194, v166\n\t" \
    "v_mul_f32 v195, v195, v167\n\t" \
    "v_cvt_pk_bf16_f32 v188, v188, v189\n\t" \
    "v_cvt_pk_bf16_f32 v189, v190, v191\n\t" \
    "v_cvt_pk_bf16_f32 v190, v192, v193\n\t" \
    "v_cvt_pk_bf16_f32 v191, v194, v195\n\t" \
    "v_min_u32 v188, -2, v188\n\t" \
    "v_min_u32 v189, -2, v189\n\t" \
    "v_min_u32 v190, -2, v190\n\t" \
    "v_min_u32 v191, -2, v191\n\t" \
    "s_nop 1\n\t" \
    "v_permlane16_swap_b32 v188, v190\n\t" \
    "v_permlane16_swap_b32 v189, v191\n\t" \
    "s_waitcnt vmcnt(1)\n\t" \
    "buffer_store_dwordx4 v[188:191], %[oa], %[rs], %[so] offen sc1\n\t" \
    "s_nop 1\n\t"
#define ASM_POLL5_I \
    "buffer_load_dwordx4 v[232:235], %[l16], %[rs], %[s0] offen offset:0 sc1\n\t" \
    "buffer_load_dwordx4 v[236:239], %[l16], %[rs], %[s0] offen offset:1024 sc1\n\t" \
    "buffer_load_dwordx4 v[240:243], %[l16], %[rs], %[s0] offen offset:2048 sc1\n\t" \
    "buffer_load_dwordx4 v[244:247], %[l16], %[rs], %[s0] offen offset:3072 sc1\n\t" \
    "buffer_load_dwordx4 v[248:251], %[l16], %[rs], %[s1] offen offset:0 sc1\n\t"
using u32x8 = __attribute__((ext_vector_type(8))) unsigned;
using u32x16 = __attribute__((ext_vector_type(16))) unsigned;
using u32x32 = __attribute__((ext_vector_type(32))) unsigned;
struct WRegs { u32x32 w[6]; u32x8 t; };
#define WREGS(W) "{v[0:31]}"(W.w[0]), "{v[32:63]}"(W.w[1]), "{v[64:95]}"(W.w[2]), "{v[96:127]}"(W.w[3]), "{v[128:159]}"(W.w[4]), "{v[160:191]}"(W.w[5]), "{v[192:199]}"(W.t)
__device__ __forceinline__ u32x32 cat32(u32x8 a, u32x8 b, u32x8 c, u32x8 d) {
    const u32x16 lo = __builtin_shufflevector(a, b, 0, 1, 2, 3, 4, 5, 6, 7, 8, 9, 10, 11, 12, 13, 14, 15);
    const u32x16 hi = __builtin_shufflevector(c, d, 0, 1, 2, 3, 4, 5, 6, 7, 8, 9, 10, 11, 12, 13, 14, 15);
    return __builtin_shufflevector(lo, hi, 0, 1, 2, 3, 4, 5, 6, 7, 8, 9, 10, 11, 12, 13, 14, 15, 16, 17, 18, 19, 20, 21, 22, 23, 24, 25, 26, 27, 28, 29, 30, 31);
}
#define WOUT(W) "=&{v[0:31]}"(W.w[0]), "=&{v[32:63]}"(W.w[1]), "=&{v[64:95]}"(W.w[2]), "=&{v[96:127]}"(W.w[3]), "=&{v[128:159]}"(W.w[4]), "=&{v[160:191]}"(W.w[5]), "=&{v[192:199]}"(W.t)
#define WTMP "memory", "scc", "v200", "v201", "v202", "v203", "v204", "v205", "v206", "v207", "v208", "v209", "v210", "v211", "v212", "v213", "v214", "v215", "v216", "v217", "v218", "v219", "v220", "v221", "v222", "v223", "v224", "v225", "v226", "v227", "v228", "v229", "v230", "v231", "v232", "v233", "v234", "v235", "v236", "v237", "v238", "v239"
__device__ __forceinline__ void load_w3(WRegs& W, const bf16_t* m, int wave, unsigned c0, bool hq) {
    const int ln = lane_id();
    unsigned o[3], s[3], t;
#pragma unroll
    for (int k = 0; k < 3; ++k) {
        const int row = (k * 64 + ln) / 24, sg = (k * 64 + ln) % 24;
        o[k] = row * (HD * 2) + c0 + (hq ? ((sg >> 1) & 1) * 384 + ((0x140 >> ((sg >> 3) * 4)) & 15) * 32 + (sg & 5) * 16 : sg * 16); s[k] = wave * 13824 + row * 416 + sg * 16;
    }
    const unsigned tr = (ln >> 4) * 3456 + ((ln >> 2) & 3) * 416 + (wave & 3) * 32 + (ln & 3) * 8;
    asm volatile(ASM_WLOAD3 : WOUT(W), [t] "=&s"(t) : [rs] "s"(mkrsrc(m, DM * HD * 2)), [wx] "s"(wave), [o0] "v"(o[0]), [o1] "v"(o[1]), [o2] "v"(o[2]), [s0] "v"(s[0]), [s1] "v"(s[1]), [s2] "v"(s[2]),
                 [t0] "v"(tr + wave * 13824), [t1] "v"(tr + (wave ^ 1) * 13824), [t2] "v"(tr + (wave ^ 2) * 13824), [t3] "v"(tr + (wave ^ 3) * 13824), [t4] "v"(tr + (wave & 4) * 13824),
                 [wl] "v"(L_W + wave * 10240 + ln * 16) : WTMP);
}
__device__ __forceinline__ void load_wo(WRegs& W, const bf16_t* m, int wave, int h) {
    const int ln = lane_id();
    unsigned o[5], s[5], t;
#pragma unroll
    for (int k = 0; k < 5; ++k) {
        const int row = (k * 64 + ln) / 20, sg = (k * 64 + ln) % 20;
        o[k] = (h * DH + row) * (DM * 2) + sg * 16 + (wave & 3) * 320; s[k] = wave * 13824 + row * 352 + sg * 16;
    }
    asm volatile(ASM_WLOADO : WOUT(W), [t] "=&s"(t) : [rs] "s"(mkrsrc(m, DM * HD * 2)), [o0] "v"(o[0]), [o1] "v"(o[1]), [o2] "v"(o[2]), [o3] "v"(o[3]), [o4] "v"(o[4]),
                 [s0] "v"(s[0]), [s1] "v"(s[1]), [s2] "v"(s[2]), [s3] "v"(s[3]), [s4] "v"(s[4]), [tr] "v"(wave * 13824 + (ln >> 2) * 352 + (ln & 3) * 8), [wl] "v"(L_W + wave * 10240 + ln * 16) : WTMP);
}
using s16x4 = __attribute__((ext_vector_type(4))) short;
template <int NT, int KS, int N>
__device__ __forceinline__ void load_wn(u32x4 (&f)[NT * KS], const bf16_t* m, int rows, int wave, int c0, int k0) {
    const int ln = lane_id();
    const rsrc_t rs = mkrsrc(m, rows * N * 2);
#pragma unroll
    for (int k = 0; k < KS; ++k)
#pragma unroll
        for (int n = 0; n < NT; ++n) f[n * KS + k] = __builtin_amdgcn_raw_buffer_load_b128(rs, ((n * 64 + ln) / (2 * NT)) * (N * 2) + ((n * 64 + ln) % (2 * NT)) * 16, (k0 + k) * (N * 64) + c0 * 32, 0);
    const unsigned st = wave * 10240 + ln * 16, tr = wave * 10240 + ((ln >> 4) * 8 + ((ln >> 2) & 3)) * (NT * 32) + (ln & 3) * 8;
#pragma unroll
    for (int k = 0; k < KS; ++k) {
#pragma unroll
        for (int n = 0; n < NT; ++n) lds_st<u32x4>(st + n * 1024, f[n * KS + k]);
        asm volatile("" ::: "memory");
#pragma unroll
        for (int n = 0; n < NT; ++n) {
            const u32x2 a = __builtin_bit_cast(u32x2, __builtin_amdgcn_ds_read_tr16_b64_v4i16((LAS s16x4*)(tr + n * 32)));
            const u32x2 b = __builtin_bit_cast(u32x2, __builtin_amdgcn_ds_read_tr16_b64_v4i16((LAS s16x4*)(tr + n * 32 + NT * 128)));
            f[n * KS + k] = (u32x4){a[0], a[1], b[0], b[1]};
        }
        asm volatile("" ::: "memory");
    }
}
struct C1Regs { u32x8 lw0, lw1, z1; u32x4 z0; };
#define XG_OUT "=&{v[200:207]}"(C.lw0), "=&{v[224:227]}"(C.z0), "=&{v[228:235]}"(C.lw1), "=&{v[244:251]}"(C.z1)
#define XG_IN WREGS(W), [xa] "v"(xa), [wl] "v"(wl), [qs] "s"(qs)
#define XG_CLOB "memory", "scc", "v208", "v209", "v210", "v211", "v212", "v213", "v214", "v215", "v216", "v217", "v218", "v219", "v220", "v221", "v222", "v223", "v236", "v237", "v238", "v239", "v240", "v241", "v242", "v243", "v252"
constexpr int XS0 = 8;
__device__ __forceinline__ unsigned init_wait(const Params& P, int c, int ph, unsigned need = 0x220080u) {
    unsigned w;
    for (int spins = 0;; ++spins) {
        unsigned o;
        asm volatile("v_mov_b32 %0, 0" : "=v"(o));
        w = __builtin_amdgcn_readfirstlane(__builtin_amdgcn_raw_buffer_load_b32(mkrsrc(P.ws + FL0, FL_BYTES), o, FL_IN + (ph * 8 + (c & 7)) * 128, 16));
        if ((w & need) == need) break;
        __builtin_amdgcn_s_sleep(8);
        if (spin_fail(spins, P)) break;
    }
    asm volatile("" ::: "memory");
    return w;
}
#define PLACE() const unsigned pw_ = init_wait(P, wave, L); const int par = (L ^ (pw_ >> 16)) & 1, cpy = x ^ ((pw_ >> 20) & 1); const rsrc_t rr = mkrsrc(P.ws + par * 8192, RG_L); (void)cpy; (void)rr
__device__ __forceinline__ void xn_wait(const Params& P, rsrc_t rr, int seg, int cpy) {
    for (int spins = 0;; ++spins) {
        const unsigned f = __builtin_amdgcn_raw_buffer_load_b32(rr, (lane_id() & 15) * 128, RG_FX + (cpy & 3) * 2048, 16);
        if (__all(f > (unsigned)seg)) break;
        __builtin_amdgcn_s_sleep(4);
        if (spin_fail(spins, P)) break;
    }
    asm volatile("" ::: "memory");
}
__device__ __forceinline__ unsigned c1_dst(int wave, bool ktype) {
    const int ln = lane_id(), l15 = ln & 15, hf = (wave >> 1) & 1;
    const int rb16 = ((ln >> 2) & 4) * 16 + (ln >> 5) * 32;
    return (ktype ? L_KM + (l15 >> 2) * 128 + hf * 8 + (l15 & 3) * 2
                  : L_Q + (l15 >> 3) * 256 + ((l15 >> 1) & 1) * 128 + ((l15 >> 2) & 1) * 8 + hf * 4 + (l15 & 1) * 2) + (wave & 1) * 1536 + rb16;
}
template <int NP>
__device__ __forceinline__ void crit1(const WRegs& W, C1Regs& C, rsrc_t rr, unsigned sbase, int wave, unsigned wl, unsigned dsto, unsigned& budget, unsigned& fv, unsigned fs
) {
    unsigned abase;
    asm volatile("v_and_b32 %0, 0x3ff, %1\n\tv_or_b32 %0, %2, %0" : "=&v"(abase) : "v"(wl), "s"(wave * 1024));
    ASM_ST_DECL;
#define C1_OUT "+{v[224:227]}"(C.z0), "+{v[244:251]}"(C.z1), "+{v[200:207]}"(C.lw0), "+{v[228:235]}"(C.lw1)
#define C1_IN WREGS(W), [ab] "v"(abase), [wl] "v"(wl), [dst] "v"(dsto)
#define C1_CLOB "memory", "vcc", "scc", "v208", "v209", "v210", "v211", "v212", "v213", "v214", "v215", "v216", "v217", "v218", "v219", "v220", "v221", "v222", "v223", "v236", "v237", "v238", "v239", "v240", "v241", "v242", "v243"
#define C1_POLL [rs] "s"(rr), [s0] "s"(sbase), [s1] "s"(sbase + 4096), [s2] "s"(sbase + 2 * BLK)
    ST(1);
    if constexpr (NP == 13) asm volatile(ASM_CRIT1_V3 "s_waitcnt vmcnt(0)" : C1_OUT, [n] "+s"(budget), [fv] "+v"(fv) ASM_ST_OUT ASM_ST2_OUT : C1_IN, C1_POLL, [fs] "s"(fs) : C1_CLOB);
    else if constexpr (NP == 12) asm volatile(ASM_CRIT1_V2 "s_waitcnt vmcnt(0)" : C1_OUT, [n] "+s"(budget), [fv] "+v"(fv) ASM_ST_OUT ASM_ST2_OUT : C1_IN, C1_POLL, [fs] "s"(fs) : C1_CLOB);
    else if constexpr (NP == 3) asm volatile(ASM_CRIT1_3 : C1_OUT, [n] "+s"(budget) ASM_ST_OUT ASM_ST2_OUT : C1_IN, C1_POLL : C1_CLOB);
    else if constexpr (NP == 2) asm volatile(ASM_CRIT1_2 : C1_OUT, [n] "+s"(budget) ASM_ST_OUT ASM_ST2_OUT : C1_IN, C1_POLL : C1_CLOB);
    else asm volatile(ASM_CRIT1_N : C1_OUT ASM_ST_OUT : C1_IN : C1_CLOB);
    SB();
    ASM_ST_SAVE(3);
    if constexpr (NP != 0) { ASM_ST2_SAVE(2); }
}
#define STARG

#define SRG 5
#undef STROLE
#define STROLE 1
__device__ __forceinline__ void s_role(const Params& P, int wave, int L, int h, int x, unsigned t0_) {
    const char* xn = P.ws + PG0 + PG_XN + L * PG_LX;
    auto xdma = [&](int seg, int hb, int par) {
        const unsigned l16 = lane_id() * 16;
        const int mt = (wave >> 1) & 1;
        const char* g = xn + pgb(seg, par);
        if (hb == 0) {
            dma4(g + blin(mt * 20480 + (wave & 1) * 4096), l16, L_XA + mt * 16384 + (wave & 1) * 4096);
            dmaq<0>(g + blin(mt * 20480 + 16384) + (wave & 1) * 1024, l16, L_XB + mt * 4096 + (wave & 1) * 1024);
        } else {
            dma4(g + blin(mt * 20480 + 8192), l16, L_XA + mt * 16384 + 8192); dma4(g + blin(mt * 20480 + 12288), l16, L_XA + mt * 16384 + 12288);
            dmaq<2>(g + blin(mt * 20480 + 16384), l16, L_XB + mt * 4096); dmaq<3>(g + blin(mt * 20480 + 16384), l16, L_XB + mt * 4096);
        }
    };
    const float SCL = 0.07216878364870322f * 1.4426950408889634f;
    auto run = [&](auto Cc) {
    constexpr int CLS = decltype(Cc)::value;
    WRegs wf;
    MARK(!L && h == 0 && wave == 0, 0);
    unsigned hm_ = 0, hc_ = 0;
    load_w3(wf, P.w[CLS == 3], wave, h * 384, false);
    MARKX(!L && wave == 0, 1);
    PLACE();
    if (CLS == 3) {
        if (L) xn_wait(P, rr, 0, cpy);
        xdma(0, 0, par);
        if (wave == 4 || wave == 7) xdma(0, 1, par);
        wait_vm<0>();
    }
    bar();
    if (CLS == 3 && wave == 7 && L) xn_wait(P, rr, 1, cpy);
    const unsigned qs = (CLS == 3 ? L_KX : L_QX) + (wave & 1) * 3072 + ((wave >> 1) & 1) * 8;
    unsigned xa, wl, dsto;
    if (CLS == 3) { xa = L_XA + wave * 2048 + lane_id() * 16; wl = L_W + wave * 10240 + lane_id() * 16; dsto = c1_dst(wave, true); }
    __builtin_amdgcn_s_setprio(CLS == 3 ? 1 : 2);
    unsigned budget = 1u << 18, tb2 = 0;
    STDECL;
    LDECL;
#pragma unroll 1
    for (int t = 0; t < NSEG; ++t) {
        const unsigned slot = t & (NSLOT - 1);
        C1Regs C;
        {
            const WRegs& W = wf;
            ASM_ST_DECL;
            if constexpr (CLS == 3) {
                const int mt = (wave >> 1) & 1;
                const char* g = xn + pgb(t + 1 < NSEG ? t + 1 : t, par);
                asm volatile(ASM_XG_S_HI : XG_OUT ASM_ST_OUT : XG_IN, [slp] "s"(SLP4), [g0] "s"(g + blin(mt * 20480 + (wave & 1) * 4096)), [d0] "s"(L_XA + mt * 16384 + (wave & 1) * 4096),
                             [g1] "s"(g + blin(mt * 20480 + 16384) + (wave & 1) * 1024), [d1] "s"(L_XB + mt * 4096 + (wave & 1) * 1024) : XG_CLOB);
                wait_vm<0>();
                LB(t);
            } else {
                const unsigned l16 = lane_id() * 16;
                xa = L_XA + wave * 2048 + l16; wl = L_W + wave * 10240 + l16;
#define XG_Q(A) asm volatile("s_cmp_lt_u32 %[t], 2\n\ts_cbranch_scc1 .Lq3%=\n\t" A "s_branch .Lqe%=\n\t.Lq3%=:\n\t" ASM_XG_S_LO ".Lqe%=:" : XG_OUT ASM_ST_OUT : XG_IN, [t] "s"(t) : XG_CLOB)
                if constexpr (CLS == 0 || CLS == 4) XG_Q(ASM_XG_S_LO1); else XG_Q(ASM_XG_S_LO2);
                __builtin_amdgcn_s_setprio(0);
                if constexpr (CLS == 0) {
                    const int ln = lane_id();
                    const u32x4 s4 = sent4();
                    const unsigned poff = (ln * 16 + ((ln < 8) ? h * 128 : (1 + (h >> 4)) * 2 * BLK - 128 + (h & 15) * 512)) | ((ln >= 40 || t < 2) ? 0x40000000u : 0u);
#pragma unroll
                    for (int c = 0; c < (int)NCP; ++c) st128(s4, rr, poff, RG_XP + ((t - 2) & (NSLOT - 1)) * XP_SLOT + (c >> 1) * XP_PAIR + (c & 1) * BLK);
                    const unsigned pbase = RG_PX + ((t - 3) & (NSLOT - 1)) * PX_SLOT + (h >> 2) * BLK;
                    const unsigned o2 = ((ln & 16) ? 5120 + ((h >> 1) & 1) * 1024 + (h & 1) * 256 + (ln >> 5) * 512 : 4096 + (h & 3) * 256 + (ln >> 5) * 3072) + (ln & 15) * 16;
                    if (t >= 3) { st128(s4, rr, ln * 16, pbase + (h & 3) * 1024); st128(s4, rr, o2, pbase); }
                }
                if (CLS == 4 && t >= HT0) {
                    const int ln = lane_id(), u = h >> 2;
                    for (int spins = 0;; ++spins) {
                        const unsigned fl = __builtin_amdgcn_raw_buffer_load_b32(rr, (ln & 3) * 128, RG_FH + BLK + u * 512, 16);
                        if (__all(fl > (unsigned)t)) break;
                        __builtin_amdgcn_s_sleep(2);
                        if (spin_fail(spins, P)) break;
                    }
                    asm volatile("" ::: "memory");
                    const char* g = P.ws + PG0 + pgb(t, par) + PG_HV + (16 + u) * 3 * BLK;
                    unsigned t0, t1, vo;
                    static_assert(L_QX == 16384, "");
                    asm volatile("s_add_u32 %0, %4, 0\n\ts_and_b32 %1, %0, 0xe000\n\ts_add_u32 %0, %0, %1\n\tv_add_u32 %2, %0, %5\n\ts_mov_b32 m0, 16384\n\ts_nop 0\n\tglobal_load_lds_dwordx4 %2, %3 sc1\n\t"
                                 "s_add_u32 %0, %4, 1024\n\ts_and_b32 %1, %0, 0xe000\n\ts_add_u32 %0, %0, %1\n\tv_add_u32 %2, %0, %5\n\ts_mov_b32 m0, 17408\n\ts_nop 0\n\tglobal_load_lds_dwordx4 %2, %3 sc1\n\t"
                                 "s_add_u32 %0, %4, 2048\n\ts_and_b32 %1, %0, 0xe000\n\ts_add_u32 %0, %0, %1\n\tv_add_u32 %2, %0, %5\n\ts_mov_b32 m0, 19456\n\ts_nop 0\n\tglobal_load_lds_dwordx4 %2, %3 sc1\n\t"
                                 "s_add_u32 %0, %4, 6144\n\ts_and_b32 %1, %0, 0xe000\n\ts_add_u32 %0, %0, %1\n\tv_add_u32 %2, %0, %5\n\ts_mov_b32 m0, 22528\n\ts_nop 0\n\tglobal_load_lds_dwordx4 %2, %3 sc1\n\t"
                                 "s_add_u32 %0, %4, 7168\n\ts_and_b32 %1, %0, 0xe000\n\ts_add_u32 %0, %0, %1\n\tv_add_u32 %2, %0, %5\n\ts_mov_b32 m0, 23552\n\ts_nop 0\n\tglobal_load_lds_dwordx4 %2, %3 sc1\n\t"
                                 "s_add_u32 %0, %4, 8192\n\ts_and_b32 %1, %0, 0xe000\n\ts_add_u32 %0, %0, %1\n\tv_add_u32 %2, %0, %5\n\ts_mov_b32 m0, 25600\n\ts_nop 0\n\tglobal_load_lds_dwordx4 %2, %3 sc1\n\t"
                                 : "=&s"(t0), "=&s"(t1), "=&v"(vo) : "s"(g), "s"((((h >> 1) & 1) * 12 + (h & 1) * 3) * 1024), "v"(ln * 16) : "memory", "scc");
                }
                dsto = c1_dst(wave, false);
                while ((unsigned)__builtin_amdgcn_s_memtime() - tb2 < STD) __builtin_amdgcn_s_sleep(2);
            }
            ASM_ST_SAVE(7);
            ST(6);
        }
        ST(0);
        unsigned fv = 0;
        if (CLS == 2) fv = (lane_id() & 15) * 128;
        crit1<CLS == 3 ? 0 : CLS < 2 ? 3 : CLS == 2 ? 12 : 2>(wf, C, rr, RG_XM + slot * XM_SLOT + (cpy >> 1) * XM_PAIR + (cpy & 1) * BLK, wave, wl, dsto, budget, fv, RG_FX + (cpy & 3) * 2048 STARG);
        if (CLS != 3) tb2 = (unsigned)__builtin_amdgcn_s_memtime();
        if constexpr (CLS == 0) {
            const unsigned sp = RG_XP + slot * XP_SLOT, sx = sp + (1 + (h >> 4)) * 2 * BLK + (h & 15) * 512;
            ASM_ST_DECL;
            asm volatile(ASM_SM : [c0] "+s"(budget) ASM_ST_OUT : WREGS(wf), [rr] "s"(rr), [sx] "s"(sx), [sy] "s"(sx + XP_PAIR), [sm] "s"(sp + h * 128), [cs] "s"(BLK), [cp] "s"(XP_PAIR), [c70] "s"(0x70), [scl] "s"(SCL)
                         : "memory", "v200", "v201", "v202", "v203", "v204", "v205", "v206", "v207", "v208", "v209", "v210", "v211", "v212", "v213", "v214", "v215", "v216", "v217", "v218", "v219", "v220", "v221", "v222", "v223", "v224", "v225", "v226", "v227", "v228", "v229", "v230", "v231", "v232", "v233", "v234", "v235", "v236", "v237", "v238", "v239", "v240", "v241", "v242", "v243", "v244", "v245", "v246", "v247", "v248", "v249", "v250", "v251", "v252", "v253", "v254", "v255");
            ASM_ST_SAVE(4);
            ST(5);
        } else if constexpr (CLS == 1 || CLS == 4) {
            const int ln = lane_id(), l15 = ln & 15, l4 = ln >> 4;
            constexpr int mt = (CLS == 4);
            const unsigned qa = L_QX + mt * 6144 + ln * 16, kmo = L_KM + l4 * 128 + (ln & 7) * 16;
            const unsigned xo = mt ? (h & 3) * 1024 : 5120 + ((h >> 1) & 1) * 1024 + (h & 1) * 256;
            const unsigned pm = l4 < 2 ? (mt ? 4096 : 7168) + (h & 3) * 256 - xo + l15 * 16 + l4 * 8 : OOB;
            const unsigned so = RG_PX + slot * PX_SLOT + (h >> 2) * BLK + xo;
            f32x4 sd;
#pragma unroll
            for (int r = 0; r < 4; ++r) sd[r] = (l4 * 4 + r > l15) ? -3.0e38f : 0.f;
            ASM_ST_DECL;
#define P4_ARGS : "+{v[240:243]}"(sd) ASM_ST_OUT : WREGS(wf), [qa] "v"(qa), [km] "v"(kmo), [pm] "v"(pm), [rr] "s"(rr), [so] "s"(so), [scl] "s"(SCL) : "memory", "v200", "v201", "v202", "v203", "v204", "v205", "v206", "v207", "v208", "v209", "v210", "v211", "v212", "v213", "v214", "v215", "v216", "v217", "v218", "v219", "v220", "v221", "v222", "v223", "v224", "v225", "v226", "v227", "v228", "v229", "v230", "v231", "v232", "v233", "v234", "v235", "v236", "v237", "v238", "v239", "v244", "v245", "v246", "v247", "v248", "v249", "v250", "v251"
            if constexpr (mt) asm volatile(ASM_P4_1 P4_ARGS); else asm volatile(ASM_P4_0 P4_ARGS);
            ASM_ST_SAVE(4);
            ST(5);
        } else {
            bar();
            ST(4);
            if (CLS == 2 && L && t + 2 < NSEG && !__all(fv > (unsigned)t + 2)) xn_wait(P, rr, t + 2, cpy);
            if (CLS == 2) { int tt = t; asm volatile("" : "+s"(tt)); if (tt + 2 == XS0) init_wait(P, h, 1); }
            if (CLS == 3) LA();
            if (CLS == 3 && t == HM_T) hm_ = (unsigned)__builtin_amdgcn_s_memrealtime() - t0_;
            if (CLS == 3 && HM_C && t == 8) hc_ = (unsigned)__builtin_amdgcn_s_memtime();
            if (CLS == 3 && HM_C && t == 56) hm_ = ((unsigned)__builtin_amdgcn_s_memtime() - hc_) / 20u;
            {
                if (CLS == 3 && (wave == 4 || wave == 7)) {
                    if (t + 1 < NSEG) xdma(t + 1, 1, par);
                }
                ST(5);
            }
        }
        STFLUSH(t);
    }
    LF();
    if (CLS == 3 && wave == 5 && h == HM_H && L == HM_L) while ((unsigned)__builtin_amdgcn_s_memrealtime() - t0_ < 30000u + hm_) __builtin_amdgcn_s_sleep(1);
    };
    if (wave == 0) run(std::integral_constant<int, 0>{});
    else if (wave == 1) run(std::integral_constant<int, 1>{});
    else if (wave == 2) run(std::integral_constant<int, 4>{});
    else if (wave == 3) run(std::integral_constant<int, 2>{});
    else run(std::integral_constant<int, 3>{});
}

#undef STROLE
#define STROLE 8
__device__ __forceinline__ void h_role(const Params& P, int wave, int L, int i, int x) {
    const rsrc_t rp = mkrsrc(P.ws + PG0, PG_L);
    const char* xn = P.ws + PG0 + PG_XN + L * PG_LX;
    auto run = [&](auto Cc) {
    constexpr bool HI = decltype(Cc)::value;
    const int mt = (wave >> 1) & 1;
    WRegs wf;
    load_w3(wf, i < 16 ? P.w[2] : P.w[0], wave, (i < 16 ? i * 2 + HI : (i - 16) * 4 + HI * 2) * 384, i >= 16);
    PLACE();
    auto xdma = [&](int seg, int hb) {
        const unsigned l16 = lane_id() * 16;
        const char* g = xn + pgb(seg, par);
        dma4(g + blin(mt * 20480 + hb * 8192 + (wave & 1) * 4096), l16, L_XA + mt * 16384 + hb * 8192 + (wave & 1) * 4096);
        dmaq<0>(g + blin(mt * 20480 + 16384) + hb * 2048 + (wave & 1) * 1024, l16, L_XB + mt * 4096 + hb * 2048 + (wave & 1) * 1024);
    };
    const unsigned qs = (HI ? L_KX : L_QX) + (wave & 1) * 3072 + mt * 8;
    const unsigned xa = L_XA + wave * 2048 + lane_id() * 16, wl = L_W + wave * 10240 + lane_id() * 16;
    const unsigned fp = RG_FH + (i >> 4) * BLK + ((i & 15) * 4 + (wave & 3)) * 128;
    const unsigned fb = L ? RG_FX + (cpy & 3) * 2048 : RG_FV + (i & 3) * 2048, fa = L ? 0 : HAH;
    unsigned go = 0, all = 0;
    STDECL;
    LDECL;
    for (int t = HT0; t <= NSEG; ++t) {
        ST(0);
        if constexpr (HI) {
            const unsigned l16 = lane_id() * 16;
            if (t > HT0) {
                u32x4 v[6];
                const int w4 = wave - 4;
#pragma unroll
                for (int k = 0; k < 6; ++k) v[k] = lds_ld<u32x4>(L_QX + ((k >> 1) * 8 + w4 * 2 + (k & 1)) * 1024 + l16);
                const unsigned so = pgb(t - 1, par) + PG_HV + i * 3 * BLK + w4 * 2048;
#pragma unroll
                for (int k = 0; k < 6; ++k) st128(v[k], rp, l16 + (k & 1) * 1024, so + (k >> 1) * BLK);
                if (!all) {
                    wait_vm<0>();
                    if (l16 < 512) __builtin_amdgcn_raw_buffer_store_b32(t, rr, l16 >> 2, fp, 16);
                }
            }
            ST(1);
            if (t == NSEG) break;
            if (!all) bar();
            xdma(t, 1);
        } else {
            if (t == NSEG) break;
            if (!go) {
                for (int spins = 0;; ++spins) {
                    if (__all(__builtin_amdgcn_raw_buffer_load_b32(rr, (lane_id() & 15) * 128, fb, 16) + fa > (unsigned)t)) break;
                    __builtin_amdgcn_s_sleep(HSL);
                    if (spin_fail(spins, P)) break;
                }
                asm volatile("" ::: "memory");
                xdma(t, 0); wait_vm<0>();
            }
            if (!all) bar();
        }
        ST(2);
        C1Regs C;
        const WRegs& W = wf;
        ASM_ST_DECL;
        if constexpr (HI) {
            const unsigned l4 = lane_id() * 4;
            asm volatile(ASM_XG_H_HI : XG_OUT ASM_ST_OUT : XG_IN, [rr] "s"(rr), [fp] "s"(fp), [tv] "s"(t), [fv] "v"((all && l4 < 128) ? l4 : OOB) : XG_CLOB);
        } else {
            asm volatile(ASM_XG_H_LO : XG_OUT ASM_ST_OUT : XG_IN : XG_CLOB);
            if (t + 1 == XS0) init_wait(P, wave, 1);
            const unsigned fv = __builtin_amdgcn_raw_buffer_load_b32(rr, (lane_id() & 15) * 128, fb, 16) + fa;
            go = __builtin_amdgcn_readfirstlane((t + 1 < NSEG && __all(fv > (unsigned)t + 1)) ? 1u : 0u);
            if (go) xdma(t + 1, 0);
            lds_st<unsigned>(L_MEM + wave * 4, go);
            wait_vm<0>();
        }
        ASM_ST_SAVE(3);
        ST(4);
        if (!HI) { LB(t); LA(); }
        PMARK(L && wave == 7, 6, t);
        bar();
        const u32x4 gv = lds_ld<u32x4>(L_MEM);
        all = __builtin_amdgcn_readfirstlane(gv[0] & gv[1] & gv[2] & gv[3]);
        ST(5);
        STFLUSH(t);
    }
    LF();
    };
    if (wave < 4) run(std::false_type{}); else run(std::true_type{});
}

#undef STROLE
#define STROLE 2
__device__ __forceinline__ void v_role(const Params& P, int wave, int L, int h, int x) {
    WRegs wf;
    STDECL;
    auto runv = [&](auto Cc) {
        load_w3(wf, P.w[2], wave, h * 384, false);
        MARKX(!L && wave == 0, 6);
        PLACE();
        unsigned budget = 1u << 18;
        const unsigned wl = L_W + wave * 10240 + lane_id() * 16, dsto = c1_dst(wave, false);
        auto vfetch = [&](int seg, int tries) {
            const int ln = lane_id();
            for (int spins = 0; tries != -2; ++spins) {
                if (spins == tries) return false;
                const unsigned f = __builtin_amdgcn_raw_buffer_load_b32(rr, (ln & 3) * 128, RG_FH + (h >> 1) * 512, 16);
                if (__all(f > (unsigned)seg)) break;
                __builtin_amdgcn_s_sleep(4);
                if (spin_fail(spins, P)) break;
            }
            asm volatile("" ::: "memory");
            const char* g = P.ws + PG0 + pgb(seg, par) + PG_HV + (h >> 1) * 3 * BLK;
#pragma unroll
            for (int k = 0; k < 3; ++k) {
                const int f = (h & 1) * 12 + wave * 3 + k;
                dmaq<0>(g + (f >> 3) * BLK + (f & 7) * 1024, ln * 16, L_QX + (seg & 1) * 12288 + (wave * 3 + k) * 1024);
            }
            return true;
        };
#pragma unroll 1
        for (int t = 0; t < HT0; ++t) {
            if (L) xn_wait(P, rr, t, cpy);
            const unsigned l16 = lane_id() * 16;
            const char* g = P.ws + PG0 + PG_XN + L * PG_LX + pgb(t, par);
            dma4(g + blin(wave * 4096), l16, L_XA + wave * 4096); dma4(g + blin(20480 + wave * 4096), l16, L_XA + 16384 + wave * 4096);
            dmaq<0>(g + blin(16384) + wave * 1024, l16, L_XB + wave * 1024); dmaq<0>(g + blin(20480 + 16384) + wave * 1024, l16, L_XB + 4096 + wave * 1024);
            wait_vm<0>();
            bar();
            C1Regs C;
            const WRegs& W = wf;
            const unsigned xa = L_XA + wave * 2048 + l16, qs = L_QX + (wave & 1) * 3072 + ((wave >> 1) & 1) * 8 + t * 12288;
            asm volatile(ASM_XG_V : XG_OUT : XG_IN : XG_CLOB);
            if (t == 0) bar();
        }
        bool seen = false;
        unsigned tb2 = 0;
        for (int t = 0; t < NSEG; ++t) {
            if (t + 1 - V1A >= HT0 && t + 1 - V1A < NSEG) { vfetch(t + 1 - V1A, seen ? -2 : -1); wait_vm<0>(); }
            ST(7);
            bar();
            C1Regs C;
            asm volatile("ds_read_b128 v[200:203], %4\n\tds_read_b128 v[204:207], %4 offset:1024\n\tds_read_b128 v[228:231], %4 offset:2048\n\tds_read_b128 v[232:235], %4 offset:3072\n\t"
                         "v_mov_b64 v[224:225], 0\n\tv_mov_b64 v[226:227], 0\n\tv_mov_b64 v[244:245], 0\n\tv_mov_b64 v[246:247], 0\n\tv_mov_b64 v[248:249], 0\n\tv_mov_b64 v[250:251], 0\n\ts_waitcnt lgkmcnt(0)"
                         : XG_OUT : "v"(wl) : "memory");
            ST(6);
            while ((unsigned)__builtin_amdgcn_s_memtime() - tb2 < VTD) __builtin_amdgcn_s_sleep(1);
            unsigned fv = (lane_id() & 3) * 128;
            crit1<10 + decltype(Cc)::value>(wf, C, rr, RG_XM + (t & (NSLOT - 1)) * XM_SLOT + (cpy >> 1) * XM_PAIR + (cpy & 1) * BLK, wave, wl, dsto, budget, fv, RG_FH + (h >> 1) * 512 STARG);
            __builtin_amdgcn_s_setprio(0);
            bar();
            seen = __all(fv > (unsigned)t + 2 - V1A);
            tb2 = (unsigned)__builtin_amdgcn_s_memtime();
            ST(4);
            STC(5);
            STFLUSH(t);
        }
    };
    if (wave < 2) runv(std::integral_constant<int, 3>{});
    else if (wave < 4) runv(std::integral_constant<int, 2>{});
    else {
        const int w4 = wave - 4;
        const rsrc_t rd = mkrsrc(P.ws + PG0, PG_L);
        load_wo(wf, P.w[3], wave, h);
        PLACE();
        bar();
        bar();
        for (int t = -1; t < NSEG; ++t) {
            const unsigned slot = t & (NSLOT - 1);
            bar();
            ST(7);
#pragma unroll 1
            for (int n = V1A ? t + (t <= 0) : t + (t != 1) + (t == 0); n <= (V1A && t >= 0 ? t : t + 1) && n < NSEG; ++n) {
                const unsigned l16 = lane_id() * 16;
                const unsigned wl = L_W + wave * 10240 + l16, va = L_QX + (n & 1) * 12288 + l16;
                unsigned so = ((n & 7) << 22) + pgb(64 + (w4 >> 1), par) + ((w4 & 1) * 40 + (h >> 3)) * BLK + (h & 7) * 1024;
                asm volatile(ASM_VOX : [so] "+s"(so) : WREGS(wf), [l16] "v"(l16), [wl] "v"(wl), [va] "v"(va), [rd] "s"(rd) : "memory", "scc", "v200", "v201", "v202", "v203", "v204", "v205", "v206", "v207", "v208", "v209", "v210", "v211", "v212", "v213", "v214", "v215", "v216", "v217", "v218", "v219", "v220", "v221", "v222", "v223", "v224", "v225", "v226", "v227", "v228", "v229", "v230", "v231", "v232", "v233", "v234", "v235", "v236", "v237", "v238", "v239", "v240", "v241", "v242", "v243", "v244", "v245", "v246", "v247", "v248", "v249", "v250", "v251");
                if (V1A || n < 2) {
                    wait_vm<0>();
                    const unsigned l4 = lane_id() * 4;
                    if (l4 < 128) __builtin_amdgcn_raw_buffer_store_b32(n + 1, rr, l4, RG_FV + (w4 >> 1) * BLK + ((w4 & 1) * 32 + h) * 128, 16);
                }
            }
            if (t < 0) continue;
            ST(0);
            unsigned wl = L_W + wave * 10240 + lane_id() * 16;
            bf16x16 lw = lds_frag(wl, 1024);
            u32x32 cz;
            asm volatile("v_mov_b64 v[220:221], 0\n\tv_mov_b64 v[222:223], 0\n\tv_mov_b64 v[224:225], 0\n\tv_mov_b64 v[226:227], 0\n\tv_mov_b64 v[228:229], 0\n\tv_mov_b64 v[230:231], 0\n\tv_mov_b64 v[232:233], 0\n\tv_mov_b64 v[234:235], 0\n\tv_mov_b64 v[236:237], 0\n\tv_mov_b64 v[238:239], 0\n\tv_mov_b64 v[240:241], 0\n\tv_mov_b64 v[242:243], 0\n\tv_mov_b64 v[244:245], 0\n\tv_mov_b64 v[246:247], 0\n\tv_mov_b64 v[248:249], 0\n\tv_mov_b64 v[250:251], 0" : "={v[220:251]}"(cz));
            unsigned vo = ((wl >> 8) & 1) * (1 + (w4 & 1) * 2) * BLK + ((wl >> 9) & 1) * 1024 + (wl & 255);
            asm volatile("" : "+{v[212:219]}"(lw), "+{v[220:251]}"(cz), "+v"(wl), "+v"(vo));
            unsigned l16 = wl & 1023u;
            bar();
            wait_vm<0>();
            if (!V1A && l16 < 512 && t && t + 1 < NSEG) __builtin_amdgcn_raw_buffer_store_b32(t + 2, rr, l16 >> 2, RG_FV + (w4 >> 1) * BLK + ((w4 & 1) * 32 + h) * 128, 16);
            ST(1);
            const int sidx = (wl & 128u) ? 0xEEEE : 0x4444;
            unsigned so = RG_VM + slot * VM_SLOT + (unsigned)((w4 * 10 - (w4 & 1) + (h >> 4) * 2) * BLK + ((h >> 2) & 3) * 2048 + (h & 3) * 256);
            ST(3);
            ASM_ST_DECL;
            asm volatile(ASM_CRIT2 : [so] "+s"(so), "+{v[212:219]}"(lw), "+{v200}"(l16), "+{v[220:251]}"(cz) ASM_ST_OUT
                         : WREGS(wf), [wl] "v"(wl), [vo] "v"(vo), [sidx] "v"(sidx), [rr] "s"(rr)
                         : "memory", "scc", "v201", "v202", "v203", "v204", "v205", "v206", "v207", "v208", "v209", "v210", "v211");
            ASM_ST_SAVE(4);
            ST(5);
            STFLUSH(t);
        }
    }
}

#undef STROLE
#define STROLE 4
constexpr unsigned R_RED = 0;
constexpr unsigned R_PART = 16384;
__device__ __forceinline__ void r_role(const Params& P, int wave, int L, int r5, int x) {
    PLACE();
    const int jj = (r5 * 4 + cpy) ^ (r5 * 4 + x >= 6 ? 1 : 0);
    const int lane = lane_id(), l15 = lane & 15, l4 = lane >> 4;
    const unsigned l16 = lane * 16;
    const rsrc_t rd = mkrsrc(P.ws + PG0, PG_L);
    const int sidx = (l15 < 8) ? 0x4444 : 0xEEEE;
    unsigned offp[3];
    offp[0] = RG_XP + (cpy >> 1) * XP_PAIR + (cpy & 1) * BLK + (1 + (wave >> 2)) * 2 * BLK + (wave & 3) * 2048 + (PLAY ? (l4 >> 1) * 512 + (((l15 >> 2) & 1) + (l15 & 2)) * 128 + (l4 & 1) * 64 + (l15 >> 3) * 32 + (l15 & 1) * 16 : l16);
    offp[2] = (l15 < 8) ? RG_XP + (cpy >> 1) * XP_PAIR + (cpy & 1) * BLK + wave * 512 + l4 * 128 + l15 * 16 : OOB;
    const unsigned offv = RG_VM + ((jj >> 1) * 4 + (wave >> 2) * 2 + (jj & 1)) * BLK + (wave & 3) * 2048 + l16;
    const int j = jj * 2 + ((wave >> 1) & 1), fl = (wave & 1) * 32 + (lane & 31);
    const unsigned offo = RG_XM + (wave >> 2) * XM_PAIR + (lane >> 5) * BLK + (j & 31) * 256 + (j >> 5) * 2 * BLK + fl * 4;
    MARK(!L && jj == 0 && wave == 0, 5);
    {
        const int row = (lane >> 2) & 7, col = 16 * j + 4 * (lane & 3) + 2 * (wave & 1);
        __builtin_amdgcn_raw_buffer_store_b32(nosent(*(const unsigned*)(P.mem0 + row * DM + col)), rr, offo, 0, 16);
    }
    auto red_addr = [](int fl) { return R_RED + fl * 80 + (fl >> 4) * 16; };
    const unsigned rw0 = (lane >> 5) * 5248 + red_addr((((l15 >> 1) & 1) * 8 + 4 * (l4 & 1)) * 4 + (l15 >> 2)) + (l15 & 1) * 4 + (wave ^ (l15 >> 3)) * 8;
    const unsigned rrd = ((wave >> 1) & 1) * 5248 + red_addr(fl) + (lane >> 5) * 32;
    u32x32 vx;
    const unsigned fo = (lane & 3) * 128, fs = RG_FV + (jj / 10) * BLK + (((jj / 5) & 1) * 32 + wave * 4) * 128, vxo = pgb(64 + jj / 10, par) + (unsigned)((jj % 10) * 8 + (wave >> 1)) * BLK + (wave & 1) * 4096;
    const rsrc_t rp = mkrsrc(P.ws + PG0, PG_L);
    const unsigned xo = (l4 >> 1) * 256 + l15 * 16 + (l4 & 1) * 8, pw = R_PART + wave * 1024 + l16, pr = R_PART + (wave & 3) * 8192 + l16;
    const int fin = wave >> 2;
    asm volatile("" : "={v[128:159]}"(vx));
    auto vx_fetch = [&](int t) {
        for (int spins = 0;; ++spins) {
            const unsigned f = __builtin_amdgcn_raw_buffer_load_b32(rr, fo, fs, 16);
            if (__all(f > (unsigned)t)) break;
            __builtin_amdgcn_s_sleep(2);
            if (spin_fail(spins, P)) break;
        }
        const unsigned sv = ((t & 7) << 22) + vxo;
        asm volatile("buffer_load_dwordx4 v[128:131], %1, %2, %3 offen sc1\n\tbuffer_load_dwordx4 v[132:135], %1, %2, %3 offen offset:1024 sc1\n\t"
                     "buffer_load_dwordx4 v[136:139], %1, %2, %3 offen offset:2048 sc1\n\tbuffer_load_dwordx4 v[140:143], %1, %2, %3 offen offset:3072 sc1\n\t"
                     "buffer_load_dwordx4 v[144:147], %1, %2, %4 offen sc1\n\tbuffer_load_dwordx4 v[148:151], %1, %2, %4 offen offset:1024 sc1\n\t"
                     "buffer_load_dwordx4 v[152:155], %1, %2, %4 offen offset:2048 sc1\n\tbuffer_load_dwordx4 v[156:159], %1, %2, %4 offen offset:3072 sc1"
                     : "+{v[128:159]}"(vx) : "v"(l16), "s"(rp), "s"(sv), "s"(sv + 4 * BLK) : "memory");
    };
    vx_fetch(0);
    unsigned budget = 1u << 18, tpub = 0;
    STDECL;
    LDECL;
    for (int t = 0; t < NSEG; ++t) {
        const unsigned slot = t & (NSLOT - 1);
        ST(0);
        while ((unsigned)__builtin_amdgcn_s_memtime() - tpub < RTD) __builtin_amdgcn_s_sleep(1);
        u32x8 vm, pv;
        u32x4 pm;
        {
            unsigned a;
            ASM_ST_DECL;
            asm volatile(ASM_RCRIT : "=&{v[176:183]}"(vm), "=&{v[184:191]}"(pv), "=&{v[192:195]}"(pm), [a] "=&s"(a), [n] "+s"(budget) ASM_ST_OUT ASM_ST2_OUT ASM_ST3_OUT
                         : [rs] "s"(rr), [ov] "v"(offv), [sv] "s"(slot * VM_SLOT), [o0] "v"(offp[0]), [o2] "v"(offp[2]), [sp] "s"(slot * XP_SLOT),
                           "{v[128:159]}"(vx), [sidx] "v"(sidx), [rw] "v"(rw0), [rd] "v"(rrd), [oo] "v"(offo),
                           [so] "s"(((t + 1) & (NSLOT - 1)) * XM_SLOT), [pub] "s"(NSEG - 1 - t)
                         : "memory", "vcc", "scc", "v160", "v161", "v162", "v163", "v164", "v165", "v166", "v167", "v196", "v197", "v200", "v201", "v202", "v203", "v204", "v205", "v206", "v207");
            ASM_ST_SAVE(2); ASM_ST2_SAVE(1); ASM_ST3_SAVE(6);
        }
        ST(3);
        LA();
        MARK(!L && jj == 0 && wave == 0 && t < 4, 10 + t);
        PMARK(wave == 0, L * 7, t - 1 + L);
        tpub = (unsigned)__builtin_amdgcn_s_memtime();
        __builtin_amdgcn_s_sleep(RXDEL);
        unsigned early;
#pragma unroll 1
        for (int spins = 1023;; spins += 1024) {
            unsigned n;
            const unsigned sx = pgb(t, par) + blin(((wave >> 1) & 1) * 20480 + jj * 1024) + (wave & 1) * 512;
            const unsigned pxb = RG_PX + slot * PX_SLOT + wave * BLK, sv = (((t + 1) & 7) << 22) + vxo;
            ASM_ST_DECL;
            asm volatile(ASM_RX : "+{v[128:159]}"(vx), [n] "=&s"(n), [a] "=&s"(early) ASM_ST_OUT ASM_ST2_OUT
                         : "{v[176:183]}"(vm), "{v[184:191]}"(pv), "{v[192:195]}"(pm), [l16] "v"(l16), [rs] "s"(rr), [s0] "s"(pxb), [s1] "s"(pxb + 4096), [rd] "s"(rd), [rx] "s"(rd), [xo] "v"(xo),
                           [fo] "v"(fo), [fs] "s"(fs), [tn] "s"(t + 1), [sv0] "s"(sv), [sv1] "s"(sv + 4 * BLK), [rp] "s"(rp), [sx] "s"(PG_XR + L * PG_LX + sx), [sa] "s"(PG_XA + sx),
                           [pw] "v"(pw), [pr] "v"(pr), [fin] "s"(fin)
                         : "memory", "vcc", "scc", "v96", "v97", "v98", "v99", "v100", "v101", "v102", "v103", "v104", "v105", "v106", "v107", "v108", "v109", "v110", "v111", "v112", "v113", "v114", "v115", "v116", "v117", "v118", "v119", "v120", "v121", "v122", "v123", "v124", "v125", "v126", "v127", "v196", "v200", "v201", "v202", "v203", "v204", "v205", "v206", "v207", "v208", "v209", "v210", "v211", "v212", "v213", "v214", "v215", "v216", "v217", "v218", "v219", "v220", "v221", "v222", "v223", "v224", "v225", "v226", "v227", "v228", "v229", "v230", "v231", "v240", "v241", "v242", "v243", "v248", "v250", "v251");
            ASM_ST_SAVE(4); ASM_ST2_SAVE(5);
            if (__builtin_amdgcn_readfirstlane(n)) break;
            early = 0;
            if (spin_fail(spins, P)) { bar(); break; }
        }
        if (t + 1 < NSEG && !__builtin_amdgcn_readfirstlane(early)) vx_fetch(t + 1);
        PMARK(!L && wave == 7 && PMK == 1, 1, t - 1);
        st128(sent4(), rr, offv, slot * VM_SLOT); st128(sent4(), rr, offv + 1024, slot * VM_SLOT);
        __builtin_amdgcn_raw_buffer_store_b32(SENT, rr, offo, slot * XM_SLOT, 16);
        if (wave < 2 && t + AH < NSEG) st128(sent4(), rd, l16, PG_XA + pgb(t + AH, par) + blin(wave * 20480 + jj * 1024));
        ST(7);
        LB(t);
        STFLUSH(t);
    }
    LF();
}

#undef STROLE
#define STROLE 16
template <int WHICH>
__device__ __forceinline__ void w_role(const Params& P, int wave, int L, int cu, int x) {
    constexpr int KS = 20, N = (WHICH == 3) ? FF : DM, NU = 2 * NSEG;
    constexpr unsigned D_IN = WHICH == 1 ? PG_XA : WHICH == 2 ? PG_HS : PG_X1;
    constexpr unsigned D_OUT = WHICH == 1 ? PG_HS : WHICH == 2 ? PG_X1 : PG_H2;
    if (WHICH != 3 && cu * 8 + wave >= 20) {
        for (int u = 0; u <= NU; ++u) bar();
        return;
    }
    auto run = [&](auto Cc) {
    constexpr bool LO = decltype(Cc)::value;
    const int ln = lane_id(), l15 = ln & 15, l4 = ln >> 4;
    const unsigned l16 = ln * 16;
    const rsrc_t rd = mkrsrc(P.ws + PG0, PG_L);
    const int nt0 = (cu * 8 + wave) * 2;
    u32x32 w[5];
    {
        u32x4 f[2 * KS];
        load_wn<2, KS, N>(f, P.w[3 + WHICH], DM, wave, nt0, 0);
#pragma unroll
        for (int n = 0; n < 5; ++n) {
            u32x8 q[4];
#pragma unroll
            for (int i = 0; i < 4; ++i) q[i] = __builtin_shufflevector(f[n * 8 + 2 * i], f[n * 8 + 2 * i + 1], 0, 1, 2, 3, 4, 5, 6, 7);
            w[n] = cat32(q[0], q[1], q[2], q[3]);
        }
        asm volatile("" : "+{v[0:31]}"(w[0]), "+{v[32:63]}"(w[1]), "+{v[64:95]}"(w[2]), "+{v[96:127]}"(w[3]), "+{v[128:159]}"(w[4]));
    }
    PLACE();
    bar();
    const int ocol = nt0 * 16 + (l4 & 1) * 16 + (l4 >> 1) * 8;
    const unsigned o3 = (unsigned)((l4 & 1) * 512 + (l4 >> 1) * 256 + l15 * 16), ro = (l4 >> 1) * 256 + l15 * 16 + (l4 & 1) * 8;
    const int grp = nt0 / 10, sh = ((grp & 1) ? -1 : 1) * ((grp >> 1) + 1);
    if (WHICH == 1) {
        unsigned zv;
        asm volatile("v_mov_b32 %0, 0" : "=v"(zv));
        st128(((u32x4){zv, zv, zv, zv}), rd, l15 <= (grp >> 1) ? toff((grp & 1) ? S_ - 1 - l15 : l15, ocol, par) : OOB, PG_HS);
    }
    auto in0 = [&](int u) { return D_IN + pgb(u >> 1, par) + blin((u & 1) * 20480 + wave * 4096); };
    auto in1 = [&](int u) { return D_IN + pgb(u >> 1, par) + blin((u & 1) * 20480 + 16384) + wave * 1024; };
    u32x16 va;
    u32x4 vb;
    u32x8 cs, k8;
#define WT_W "{v[0:31]}"(w[0]), "{v[32:63]}"(w[1]), "{v[64:95]}"(w[2]), "{v[96:127]}"(w[3]), "{v[128:159]}"(w[4])
    asm volatile("v_mov_b32 v216, 0x3f803f80\n\tv_mov_b32 v217, v216\n\tv_mov_b32 v218, v216\n\tv_mov_b32 v219, v216\n\tv_mov_b32 v220, %[c0]\n\tv_mov_b32 v232, -1\n\tv_mov_b32 v236, -1\n\t"
                 "v_mov_b32 v240, -1\n\tv_mov_b32 v244, -1\n\tv_mov_b32 v248, -1\n\t" ASM_WCS
                 : "=&{v[196:203]}"(cs), "=&{v[216:223]}"(k8), "=&{v[232:247]}"(va), "=&{v[248:251]}"(vb) : WT_W, [c0] "s"(-2.0f * 0.7978845608028654f * 1.4426950408889634f));
    unsigned la = l16, lw = wave * 8192 + l16, sr = l4 * 8192 + ((l15 >> 2) * 16 + l15) * 16 + (l15 & 3) * 4, budget = 1u << 18;
    wait_vm<0>();
    STDECL;
    LDECL;
#pragma unroll 1
    for (int u = 0; u < NU; ++u) {
        const int mt = u & 1;
        const unsigned pg = pgb(u >> 1, par), c3 = D_OUT + blin(mt * (N * 32) + (nt0 >> 1) * 1024);
        unsigned oa = o3, ob = 0, so = pg + c3, sa = u + 2 * AH < NU ? pgb((u >> 1) + AH, par) + c3 : pg + PG_SS + 4096, a;
        if (WHICH == 1) {
            const int orow = u * 16 + l15 + sh;
            ob = toff(orow + AH * SEG, ocol, par);
            oa = (unsigned)orow < (unsigned)S_ ? (ob ^ 8192u) - (AH << 21) : OOB;
            if (orow + AH * SEG >= S_ + (sh < 0 ? sh : 0)) ob = OOB;
            so = sa = D_OUT;
        }
        if (mt) { ST(0); LB(u >> 1); } else { LA(); }
        ASM_ST_DECL;
#define WT_A WT_W, [la] "v"(la), [l16] "v"(l16), [oa] "v"(oa), [rs] "s"(rd), [so] "s"(so), [sa] "s"(sa)
#define WT_G [sr] "v"(sr), [kd] "s"(1.0f / DM), [ke] "s"(1e-5f), [k1] "s"(-2.0f * 0.7978845608028654f * 1.4426950408889634f * 0.044715f)
#define WT_1 WT_G, [ob] "v"(ob), [ss] "s"(pg + PG_SS + mt * 128), [sw] "s"(nt0)
#define WT_2 [ro] "v"(ro), [sx] "s"(pg + PG_XA + blin(mt * 20480 + nt0 * 512)), [ss] "s"(pg + PG_SS + mt * 128)
#define WT_C "memory", "vcc", "scc", "v160", "v161", "v162", "v163", "v164", "v165", "v166", "v167", "v168", "v169", "v170", "v171", "v172", "v173", "v174", "v175", "v176", "v177", "v178", "v179", "v180", "v181", "v182", "v183", "v184", "v185", "v186", "v187", "v188", "v189", "v190", "v191", "v192", "v193", "v194", "v195", "v204", "v205", "v206", "v207", "v208", "v209", "v210", "v211", "v212", "v213", "v214", "v215", "v252"
#define WT_LO(A, ...) asm volatile(A : "+{v[196:203]}"(cs), "+{v[216:223]}"(k8), "+{v[232:247]}"(va), "+{v[248:251]}"(vb), [n] "+s"(budget), [a] "=&s"(a) ASM_ST_OUT ASM_ST2_OUT ASM_ST3_OUT \
                              : WT_A, __VA_ARGS__, [lw] "v"(lw), [i0] "s"(in0(u)), [i1] "s"(in1(u)), [n0] "s"(in0(u + 1)), [n1] "s"(in1(u + 1)) : WT_C)
#define WT_HI(A, ...) asm volatile(A : "+{v[196:203]}"(cs), "+{v[216:223]}"(k8) ASM_ST_OUT ASM_ST3_OUT : WT_A, __VA_ARGS__ : WT_C)
        if constexpr (LO) { if constexpr (WHICH == 1) WT_LO(ASM_WT1_LO, WT_1); else if constexpr (WHICH == 2) WT_LO(ASM_WT2_LO, WT_2); else WT_LO(ASM_WT3_LO, WT_G); }
        else { if constexpr (WHICH == 1) WT_HI(ASM_WT1_HI, WT_1); else if constexpr (WHICH == 2) WT_HI(ASM_WT2_HI, WT_2); else WT_HI(ASM_WT3_HI, WT_G); }
        la ^= 32768u; lw ^= 32768u; sr ^= 32768u;
        PMARK(!L && mt == (WHICH != 1) && (PMK == 1 || WHICH == 3), PMK == 1 ? 1 + WHICH : 1, (u >> 1) - (WHICH == 1));
    }
    wait_vm<0>();
    if constexpr (LO) asm volatile("" :: "{v[232:247]}"(va), "{v[248:251]}"(vb));
    LF();
    };
    if (wave < 4) run(std::true_type{}); else run(std::false_type{});
}

__device__ __forceinline__ void w4_role(const Params& P, int wave, int L, int cu, int x) {
    constexpr unsigned W_FL = 122880;
    const int ln = lane_id(), l15 = ln & 15, l4 = ln >> 4;
    const unsigned l16 = ln * 16;
    const rsrc_t rd = mkrsrc(P.ws + PG0, PG_L);
    lds_st<unsigned>(W_FL + wave * 4, 0u);
    STDECL;
    if (wave < 6) {
        u32x32 w[5];
        {
            u32x4 g[40];
            load_wn<5, 8, DM>(g, P.w[7], FF, wave, cu * 5, wave * 8);
#pragma unroll
            for (int n = 0; n < 5; ++n) {
                u32x8 f[4];
#pragma unroll
                for (int i = 0; i < 4; ++i) f[i] = __builtin_shufflevector(g[n * 8 + 2 * i], g[n * 8 + 2 * i + 1], 0, 1, 2, 3, 4, 5, 6, 7);
                w[n] = cat32(f[0], f[1], f[2], f[3]);
            }
            asm volatile("" : "+{v[0:31]}"(w[0]), "+{v[32:63]}"(w[1]), "+{v[64:95]}"(w[2]), "+{v[96:127]}"(w[3]), "+{v[128:159]}"(w[4]));
        }
        PLACE();
        bar();
        unsigned budget = 1u << 18;
        u32x32 av;
        asm volatile("v_mov_b32 v160, -1\n\tv_mov_b32 v164, -1\n\tv_mov_b32 v168, -1\n\tv_mov_b32 v172, -1\n\tv_mov_b32 v176, -1\n\tv_mov_b32 v180, -1\n\tv_mov_b32 v184, -1\n\tv_mov_b32 v188, -1" : "={v[160:191]}"(av));
        for (int u = 0; u < 2 * NSEG; ++u) {
            if (u & 1) ST(0);
            for (int spins = 0;; ++spins) {
                asm volatile("" ::: "memory");
                if ((int)__builtin_amdgcn_readfirstlane(lds_ld<unsigned>(W_FL + 24 + (u & 1) * 4)) + 3 >= u) break;
                __builtin_amdgcn_s_sleep(2);
                if (spin_fail(spins, P)) break;
            }
            const unsigned gb = PG_H2 + pgb(u >> 1, par) + ((u & 1) * 6 + wave) * BLK, gn = PG_H2 + pgb((u + 1) >> 1, par) + ((~u & 1) * 6 + wave) * BLK;
            ASM_ST_DECL;
            asm volatile(ASM_W4_M : [n] "+s"(budget), "+{v[160:191]}"(av) ASM_ST2_OUT
                         : "{v[0:31]}"(w[0]), "{v[32:63]}"(w[1]), "{v[64:95]}"(w[2]), "{v[96:127]}"(w[3]), "{v[128:159]}"(w[4]),
                           [ld] "v"(l16 + wave * 1024 + (u & 3) * 30720), [l16] "v"(l16), [rs] "s"(rd), [s0] "s"(gb), [s1] "s"(gb + 4096), [n0] "s"(gn), [n1] "s"(gn + 4096), [un] "s"(u + 1), [fl] "v"(W_FL + wave * 4)
                         : "memory", "vcc", "scc", "v192", "v193", "v194", "v195", "v196", "v197", "v198", "v199", "v200", "v201", "v202", "v203", "v204", "v205", "v206", "v207", "v208", "v209", "v210", "v211", "v212");
            PMARK(!L && (u & 1) && PMK == 2, 2, u >> 1);
        }
        asm volatile("s_waitcnt vmcnt(0)" :: "{v[160:191]}"(av) : "memory");
    } else {
        const int mt = wave - 6;
        PLACE();
        bar();
        const unsigned c0 = mt * 20480 + cu * 2560, o8 = (l4 >> 1) * 256 + l15 * 16 + (l4 & 1) * 8;
        unsigned budget = 1u << 18;
        u32x16 ra;
        u32x4 rb;
        asm volatile("v_mov_b32 v164, -1" : "={v[164:179]}"(ra), "={v[180:183]}"(rb));
        __builtin_amdgcn_s_setprio(W4P);
#define W4F_OUT "+{v[164:179]}"(ra), "+{v[180:183]}"(rb), [n] "+s"(budget)
#define W4F_IN [o0] "v"(o8 + blin(c0)), [o1] "v"(o8 + blin(c0 + 512)), [o2] "v"(o8 + blin(c0 + 1024)), [o3] "v"(o8 + blin(c0 + 1536)), [o4] "v"(o8 + blin(c0 + 2048)), [rd] "s"(rd), \
               [c1] "s"(PG_X1 + pg), [ca] "s"(PG_XA + pg), [n1] "s"(PG_X1 + pn), [na] "s"(PG_XA + pn), [f0] "v"(W_FL + (ln & 3) * 4), [f1] "v"(W_FL + 16 + (ln & 1) * 4), [fd] "v"(W_FL + wave * 4), \
               [la] "v"((u & 3) * 30720 + l16), [u] "s"(u), [un] "s"(u + 1)
#define W4F_CLOB "memory", "vcc", "scc", "v64", "v65", "v66", "v67", "v68", "v69", "v70", "v71", "v72", "v73", "v74", "v75", "v76", "v77", "v78", "v79", "v80", "v81", "v82", "v83", "v84", "v85", "v86", "v87", "v88", "v89", "v90", "v91", "v92", "v93", "v94", "v95", "v96", "v97", "v98", "v99", "v100", "v101", "v102", "v103", "v104", "v105", "v106", "v107", "v108", "v109", "v110", "v111", "v112", "v113", "v114", "v115", "v116", "v117", "v118", "v119", "v120", "v121", "v122", "v123", "v124", "v125", "v126", "v127", "v128", "v129", "v130", "v131", "v132", "v133", "v134", "v135", "v136", "v137", "v138", "v139", "v140", "v141", "v142", "v143", "v144", "v145", "v146", "v147", "v148", "v149", "v150", "v151", "v152", "v153", "v154", "v155", "v156", "v157", "v158", "v159", "v160", "v161", "v162", "v163", "v184", "v185", "v186", "v187", "v188", "v189", "v190", "v191", "v192", "v193", "v194", "v195", "v196", "v197", "v198", "v199", "v200"
        if (L) {
            const rsrc_t ro = mkrsrc(P.out, ACT);
#pragma unroll 1
            for (int t = 0; t < NSEG; ++t) {
                const unsigned u = t * 2 + mt, pg = pgb(t, par), pn = pgb(t + 1, par);
                ASM_ST_DECL;
                asm volatile(ASM_W4F_1 : W4F_OUT ASM_ST2_OUT : W4F_IN, [pa] "v"(l15 * (DM * 2) + (l4 & 1) * 32 + (l4 >> 1) * 16), [pb] "v"(l15 * (DM * 2) + l4 * 8), [ro] "s"(ro), [so] "s"(u * 20480 + cu * 160) : W4F_CLOB);
            }
        } else {
            const rsrc_t rn = mkrsrc(P.ws + (par ^ 1) * 8192, RG_L);
#pragma unroll 1
            for (int t = 0; t < NSEG; ++t) {
                const unsigned u = t * 2 + mt, pg = pgb(t, par), pn = pgb(t + 1, par), sn = (pg ^ 8192) + PG_LX, st = RG_ST + (u >> 3) * BLK + (u & 7) * 1024;
                ASM_ST_DECL;
                asm volatile(ASM_W4F_0 : W4F_OUT ASM_ST_OUT ASM_ST2_OUT ASM_ST3_OUT
                             : W4F_IN, [pa] "v"(blin(c0 + (l4 & 1) * 512) + (l4 >> 1) * 256 + l15 * 16), [pb] "v"(blin(c0 + 1024 + (l4 & 1) * 512) + (l4 >> 1) * 256 + l15 * 16),
                               [so] "v"(ln < 16 ? ln * 8 : OOB), [si] "v"(ln * 8), [rr] "s"(rr), [sp] "s"(st + cu * 128), [sq] "s"(st), [xr] "s"(PG_XR + sn), [xn] "s"(PG_XN + sn),
                               [kd] "s"(1.0f / DM), [ke] "s"(1e-5f), [tn] "s"(t + 1), [fo] "v"(ln < 32 ? (ln >> 3) * 2048 + (ln & 7) * 16 : OOB), [rn] "s"(rn), [fs] "s"(RG_FX + (cu * 2 + mt) * 128) : W4F_CLOB);
            }
        }
        asm volatile("s_waitcnt vmcnt(0)" :: "{v[164:179]}"(ra), "{v[180:183]}"(rb) : "memory");
    }
}

__device__ __forceinline__ unsigned probe(char* ws, unsigned base, unsigned bit, int n) {
    const rsrc_t r = mkrsrc(ws, RG_SLOT);
    unsigned m0 = ~0u, m1 = ~0u;
    for (int i = 0; i < n; ++i) {
        const unsigned long long t0 = __builtin_amdgcn_s_memtime();
        __builtin_amdgcn_raw_buffer_store_b32(0u, r, 96 * BLK + base + (i & 1) * bit + ((i >> 2) & 1) * 4096 + (i >> 2) * 768, 0, 16);
        asm volatile("s_waitcnt vmcnt(0)" ::: "memory");
        const unsigned d = (unsigned)(__builtin_amdgcn_s_memtime() - t0);
        if (i & 1) m1 = d < m1 ? d : m1; else m0 = d < m0 ? d : m0;
    }
    return m0 > m1 ? 1u : 0u;
}
__device__ __forceinline__ void setup(const Params& P, int wave, int mt, int par0, int ph) {
    const bool xw = wave < 4 && (mt >= 2 * XS0) == (ph != 0);
    const int tid = threadIdx.x & 255, r15 = tid & 15, slot = tid >> 4;
    const rsrc_t rw = mkrsrc(P.ws, WS_END);
    u32x4 v[5];
    float s = 0.f, q = 0.f;
    if (xw) {
#pragma unroll
        for (int c = 0; c < 5; ++c) v[c] = *(const u32x4*)(P.x + (size_t)(mt * 16 + r15) * DM + (slot * 5 + c) * 8);
#pragma unroll
        for (int c = 0; c < 5; ++c)
#pragma unroll
            for (int e = 0; e < 4; ++e) s += lo2f(v[c][e]) + hi2f(v[c][e]);
        lds_st<float>((slot * 17 + r15) * 4, s);
    } else if (wave >= 4) {
        constexpr unsigned N0 = RG_ST / BLK, N1 = 4 * N0 + 16, N2 = PG_SS / BLK;
        const u32x4 s4 = sent4();
        for (unsigned k = mt; k < 2 * (N1 + 4 * N2); k += 128) {
            const unsigned i = k >> 1;
            const unsigned o = i < N1 ? (i < 4 * N0 ? (i / N0) * RG_SLOT + (i % N0) * BLK : (i - 3 * N0) * BLK) + (par0 ^ ph) * 8192 : PG0 + pgb((i - N1) / N2, par0 ^ ph) + ((i - N1) % N2) * BLK;
            st128(s4, rw, tid * 16, o + (k & 1) * 4096);
        }
    }
    bar();
    float mean = 0.f;
    if (xw) {
        s = 0.f;
#pragma unroll
        for (int k = 0; k < 16; ++k) s += lds_ld<float>((k * 17 + r15) * 4);
        mean = s * (1.0f / DM);
#pragma unroll
        for (int c = 0; c < 5; ++c)
#pragma unroll
            for (int e = 0; e < 4; ++e) { const float a = lo2f(v[c][e]) - mean, b = hi2f(v[c][e]) - mean; q += a * a + b * b; }
        lds_st<float>(2048 + (slot * 17 + r15) * 4, q);
    }
    bar();
    if (xw) {
        q = 0.f;
#pragma unroll
        for (int k = 0; k < 16; ++k) q += lds_ld<float>(2048 + (k * 17 + r15) * 4);
        const float m1 = rbf(mean), r = 1.0f / ln_den(q * (1.0f / DM));
#pragma unroll
        for (int c = 0; c < 5; ++c) {
            const unsigned o = toff(mt * 16 + r15, (slot * 5 + c) * 8, par0);
            st128(v[c], rw, o, PG0 + PG_XR);
            u32x4 n;
#pragma unroll
            for (int e = 0; e < 4; ++e) n[e] = pack2(rbf(lo2f(v[c][e]) - m1) * r, rbf(hi2f(v[c][e]) - m1) * r);
            st128(n, rw, o, PG0 + PG_XN);
        }
    }
    wait_vm<0>();
    bar();
    if (threadIdx.x < 8) __hip_atomic_fetch_add((unsigned*)(P.ws + FL0 + FL_IN + (ph * 8 + threadIdx.x) * 128), 1u, RLX_AGENT);
}

__global__ __launch_bounds__(512, 2) void mega_kernel(Params P) {
    const unsigned t0_ = (unsigned)__builtin_amdgcn_s_memrealtime();
    const int wave = __builtin_amdgcn_readfirstlane(threadIdx.x >> 6);
    u32x4 cpf;
    {
        unsigned long long pc;
        unsigned len;
        asm volatile("s_getpc_b64 %0\n\ts_mov_b32 %1, _Z12prep1_kernel5Prep1-_Z11mega_kernel6Params" : "=s"(pc), "=s"(len));
        cpf = __builtin_amdgcn_raw_buffer_load_b128(mkrsrc((const void*)(pc & ~255ull), len), (blockIdx.x >> 3) * 8192 + threadIdx.x * 16, 0, 0);
    }
    if (threadIdx.x == 0) {
        unsigned g = (unsigned)__builtin_amdgcn_s_getreg((3 << 11) | 20) & 7u, k;
        while ((k = __hip_atomic_fetch_add((unsigned*)(P.ws + FL0 + FL_TK + g * 128), 1u, RLX_AGENT)) >= 32u) g = (g + 1) & 7u;
        lds_st<unsigned>(0, g << 5 | k);
    }
    bar();
    const unsigned tk = __builtin_amdgcn_readfirstlane(lds_ld<unsigned>(0));
    asm volatile("" :: "v"(cpf));
    bar();
    const int g4 = (tk >> 5) & 3, k = tk & 31;
    const int L = tk >> 7, x = g4, r = k;
    if (L) {
        const int par0 = (init_wait(P, wave, 1, 0u) >> 16) & 1;
        if (tk == 255 && wave == 0) {
            const unsigned xc = (unsigned)__builtin_amdgcn_s_getreg((3 << 11) | 20);
            const unsigned f = (probe(P.ws, ((par0 ^ (xc >> 2)) & 1) * 8192, BLK, CAL2) ^ 1u ^ xc) & 1u;
            if (threadIdx.x < 16) __hip_atomic_fetch_add((unsigned*)(P.ws + FL0 + FL_IN + threadIdx.x * 128), (2u + f) << 20, RLX_AGENT);
        }
#pragma unroll 1
        for (int ph = 0; ph < 2; ++ph) {
            setup(P, wave, (tk & 31) * 4 + g4, par0, ph);
            for (int k = ph * DLY_L; k < DLY_L; ++k) __builtin_amdgcn_s_sleep(32);
        }
    }
    {
        const int i = (r - 26) * 4 + x;
        const int d = (r < 8 ? DLY_S : r < 21 ? 0 : i < 4 ? DLY_H : DLY_W);
        for (int k = 0; k < d; ++k) __builtin_amdgcn_s_sleep(32);
    }
    if (r < 8) s_role(P, wave, L, x * 8 + r, x, t0_);
    else if (r < 16) v_role(P, wave ^ 4, L, x * 8 + r - 8, x);
    else if (r < 21) r_role(P, wave, L, r - 16, x);
    else {
        const int i = (r - 26) * 4 + x;
        if (i < 4) h_role(P, wave, L, r < 25 ? x * 4 + r - 21 : 16 + x * 2 + r - 25, x);
        else if (i < 7) w_role<1>(P, wave, L, i - 4, x);
        else if (i < 10) w_role<2>(P, wave, L, i - 7, x);
        else if (i < 16) w_role<3>(P, wave, L, i - 10, x);
        else w4_role(P, wave, L, i - 16, x);
    }
}

struct Prep1 { char* ws; };
__global__ __launch_bounds__(256) void prep1_kernel(Prep1 A) {
    int b = blockIdx.x;
    if (b == 0) {
        if (threadIdx.x >= 64) return;
        const unsigned h = (probe(A.ws, 0, 8192, CAL1) ^ ((unsigned)__builtin_amdgcn_s_getreg((3 << 11) | 20) >> 2)) & 1u;
        if (threadIdx.x < 16) *(unsigned*)(A.ws + FL0 + FL_IN + threadIdx.x * 128) = (2u + h) << 16;
        return;
    }
    b -= 1;
    ((u32x4*)(A.ws + RG_FV))[b * 256 + threadIdx.x] = (u32x4){0u, 0u, 0u, 0u};
    if (b == 0 && threadIdx.x < FL_IN / 16) ((u32x4*)(A.ws + FL0))[threadIdx.x] = (u32x4){0u, 0u, 0u, 0u};
}

extern "C" void kernel_launch(void* const* d_in, const int* in_sizes, int n_in, void* d_out, int out_size, void* d_ws,
                              size_t ws_size, hipStream_t stream) {
    char* ws = (char*)d_ws;
    Params P;
    P.ws = ws; P.x = (const bf16_t*)d_in[0]; P.mem0 = (const bf16_t*)d_in[1]; P.out = (bf16_t*)d_out;
    for (int i = 0; i < 8; ++i) P.w[i] = (const bf16_t*)d_in[i < 4 ? 2 + i : 4 + i];
    (void)hipFuncSetAttribute((const void*)mega_kernel, hipFuncAttributeMaxDynamicSharedMemorySize, MEGA_LDS);

    Prep1 A1;
    A1.ws = ws;
    prep1_kernel<<<21, 256, 0, stream>>>(A1);
    mega_kernel<<<256, 512, MEGA_LDS, stream>>>(P);
}
```

```cpp
#include <hip/hip_runtime.h>
#include <stdint.h>
#include <type_traits>

typedef unsigned short bf16_t;
using bf16x2 = __attribute__((ext_vector_type(2))) __bf16;
using bf16x8 = __attribute__((ext_vector_type(8))) __bf16;
using bf16x16 = __attribute__((ext_vector_type(16))) __bf16;
using f32x2 = __attribute__((ext_vector_type(2))) float;
using f32x4 = __attribute__((ext_vector_type(4))) float;
using u32x2 = __attribute__((ext_vector_type(2))) unsigned;
using u32x4 = __attribute__((ext_vector_type(4))) unsigned;
typedef __amdgpu_buffer_rsrc_t rsrc_t;

__device__ __forceinline__ unsigned pack2(float a, float b) {
    bf16x2 r = __builtin_convertvector((f32x2){a, b}, bf16x2);
    return __builtin_bit_cast(unsigned, r);
}
__device__ __forceinline__ float rbf(float f) { return __uint_as_float(pack2(f, 0.f) << 16); }
__device__ __forceinline__ float lo2f(unsigned u) { return __uint_as_float(u << 16); }
__device__ __forceinline__ float hi2f(unsigned u) { return __uint_as_float(u & 0xFFFF0000u); }
__device__ __forceinline__ rsrc_t mkrsrc(const void* p, unsigned bytes) {
    return __builtin_amdgcn_make_buffer_rsrc((void*)p, 0, bytes, 0x00020000);
}

constexpr int S_ = 2048, DM = 640, NH = 32, DH = 192, HD = NH * DH  , SEG = 32, NSEG = 64;
constexpr int FF = 1536;

#define XMAP
#define NCPY 4
constexpr unsigned NSLOT = 4, NCP = NCPY;
#define FL_TK_DEF
#define DLY_L 11
#define DLY_S 1
#define DLY_W 10
#define DLY_H 2
#define EWAKE "s_nop 0"
#define LWAKE "s_wakeup\n\t"
#define SLP4 (wave == 4 ? t : 0)
#define WPF 12
#define WPRI 1
#define WPRI2 0
#define RTD 3500
#define VTD 3900
#define HAH 100
#define WEAK 0
#define BAL 0
#define HSL 8
#define OAUX 2
#define W4P 3
#define STD 3800
#define CAL1 4
#define CAL2 12
#define CALN 12
#define RSL 9
#define VTRY 0
#define VSL 1
#define NRR 5
constexpr unsigned BLK = 16384, RG_SLOT = 4u << 20;
constexpr unsigned XP_PAIR = 6 * BLK, XP_SLOT = RG_SLOT;
constexpr unsigned XM_PAIR = 4 * BLK, XM_SLOT = RG_SLOT;
constexpr unsigned VM_SLOT = RG_SLOT;
constexpr unsigned PX_SLOT = RG_SLOT;
constexpr unsigned RG_XP = 0, RG_XM = 12 * BLK, RG_VM = 20 * BLK, RG_PX = 60 * BLK;
constexpr unsigned RG_ST = 68 * BLK, RG_FV = 84 * BLK, RG_FH = 86 * BLK, RG_FX = 88 * BLK, RG_L = NSLOT * RG_SLOT;
constexpr unsigned FL0 = 89 * BLK, FL_TK = 512, FL_IN = FL_TK + 8 * 128, FL_HB = FL_IN + 16 * 128, FL_BYTES = FL_HB + 8 * 128;
constexpr unsigned ACT = S_ * DM * 2;
#define PXROT 5
#define W4SL 5
#define RXDEL 1
#define VXD 32
#define VXAUX 16
constexpr unsigned PG0 = RG_L, PG_L = 80u << 21;
constexpr unsigned PG_XA = 0, PG_HS = 5 * BLK, PG_X1 = 10 * BLK, PG_H2 = 15 * BLK, PG_SS = 27 * BLK, PG_XR = 28 * BLK, PG_XN = 33 * BLK, PG_LX = 10 * BLK, PG_HV = 48 * BLK;
__device__ __forceinline__ unsigned pgb(unsigned p, int par) { return p << 21 | ((p ^ par) & 1) << 13; }
__device__ __forceinline__ constexpr unsigned blin(unsigned o) { return o + (o & ~8191u); }
constexpr int HT0 = 2;
constexpr int AH = 3;
constexpr unsigned WS_END = PG0 + PG_L;

#define SB() __builtin_amdgcn_sched_barrier(0)
#define LAS __attribute__((address_space(3)))
template <class T> __device__ __forceinline__ T lds_ld(unsigned a) { return *(const LAS T*)(a); }
template <class T> __device__ __forceinline__ void lds_st(unsigned a, T v) { *(LAS T*)(a) = v; }
constexpr unsigned OOB = 0x7FFFFF00u;
template <int N> __device__ __forceinline__ void wait_vm() { asm volatile("s_waitcnt vmcnt(%0)" :: "i"(N) : "memory"); }
__device__ __forceinline__ void bar() {
    asm volatile("s_waitcnt lgkmcnt(0)" ::: "memory");
    __builtin_amdgcn_s_barrier();
    asm volatile("" ::: "memory");
}
__device__ __forceinline__ int lane_id() {
    int ln;
    asm volatile("v_mbcnt_lo_u32_b32 %0, -1, 0\n\tv_mbcnt_hi_u32_b32 %0, -1, %0" : "=v"(ln));
    return ln;
}
__device__ __forceinline__ void swap16(unsigned& a, unsigned& b) { asm volatile("s_nop 3\n\tv_permlane16_swap_b32 %0, %1\n\ts_nop 1" : "+v"(a), "+v"(b)); }
__device__ __forceinline__ void swap32(unsigned& a, unsigned& b) { asm volatile("s_nop 3\n\tv_permlane32_swap_b32 %0, %1\n\ts_nop 1" : "+v"(a), "+v"(b)); }
__device__ __forceinline__ float sum_x16(float x) { unsigned a = __float_as_uint(x), b = a; swap16(a, b); return __uint_as_float(a) + __uint_as_float(b); }
__device__ __forceinline__ float sum_x32(float x) { unsigned a = __float_as_uint(x), b = a; swap32(a, b); return __uint_as_float(a) + __uint_as_float(b); }
__device__ __forceinline__ float max_x16(float x) { unsigned a = __float_as_uint(x), b = a; swap16(a, b); return __builtin_fmaxf(__uint_as_float(a), __uint_as_float(b)); }
__device__ __forceinline__ float max_x32(float x) { unsigned a = __float_as_uint(x), b = a; swap32(a, b); return __builtin_fmaxf(__uint_as_float(a), __uint_as_float(b)); }
__device__ __forceinline__ void sum2_rows(float& s, float& q) {
    unsigned a = __float_as_uint(s), b = a, c = __float_as_uint(q), d = c;
    asm volatile("s_nop 2\n\tv_permlane16_swap_b32 %0, %1\n\tv_permlane16_swap_b32 %2, %3\n\tv_add_f32 %0, %0, %1\n\tv_add_f32 %2, %2, %3\n\tv_mov_b32 %1, %0\n\tv_mov_b32 %3, %2\n\t"
                 "s_nop 1\n\tv_permlane32_swap_b32 %0, %1\n\tv_permlane32_swap_b32 %2, %3\n\tv_add_f32 %0, %0, %1\n\tv_add_f32 %2, %2, %3" : "+v"(a), "+v"(b), "+v"(c), "+v"(d));
    s = __uint_as_float(a); q = __uint_as_float(c);
}
__device__ __forceinline__ u32x4 merge16(u32x2 a, u32x2 b) {
    unsigned a0 = a[0], a1 = a[1], b0 = b[0], b1 = b[1];
    asm volatile("s_nop 3\n\tv_permlane16_swap_b32 %0, %2\n\tv_permlane16_swap_b32 %1, %3\n\ts_nop 1" : "+v"(a0), "+v"(a1), "+v"(b0), "+v"(b1));
    return (u32x4){a0, a1, b0, b1};
}
template <int AUXV>
__device__ __forceinline__ void st128_(u32x4 v, rsrc_t r, unsigned voff, unsigned soff) {
    __builtin_amdgcn_raw_buffer_store_b128(v, r, voff, soff, AUXV);
    asm volatile("s_nop 1" :: "v"(v) : "memory");
}
#define st128(V, R, VO_, SO_) st128_<16>(V, R, VO_, SO_)
#define LD128(R, VO_, SO_) __builtin_amdgcn_raw_buffer_load_b128(R, VO_, SO_, 16)
#define LD64(R, VO_, SO_) __builtin_amdgcn_raw_buffer_load_b64(R, VO_, SO_, 16)
#define BF8(X) __builtin_bit_cast(bf16x8, X)
__device__ __forceinline__ float gelu_tanh(float x) {
    const float c0 = -2.0f * 0.7978845608028654f * 1.4426950408889634f, c1 = c0 * 0.044715f;
    const float e = __builtin_amdgcn_exp2f(x * __builtin_fmaf(x * x, c1, c0));
    return x * __builtin_amdgcn_rcpf(1.0f + e);
}
#define MFMA(A_, B_, C_) C_ = __builtin_amdgcn_mfma_f32_16x16x32_bf16(A_, B_, C_, 0, 0, 0)
#define SMF(A_, B_, C_) C_ = __builtin_amdgcn_smfmac_f32_16x16x64_bf16(A_, B_, C_, sidx, 0, 0)
__device__ __forceinline__ float ln_den(float var) { return rbf(__builtin_amdgcn_sqrtf(rbf(rbf(var) + 1e-5f))); }
__device__ __forceinline__ float rcp(float x) { return __builtin_amdgcn_rcpf(x); }

struct Params {
    char* ws;
    const bf16_t* x;
    const bf16_t* mem0;
    const bf16_t* w[8];
    bf16_t* out;
};

#define RLX_AGENT __ATOMIC_RELAXED, __HIP_MEMORY_SCOPE_AGENT
#define SENT 0xFFFFFFFFu
constexpr int SPIN_CAP = 400000;
__device__ __forceinline__ bool spin_fail(int spins, const Params& P) {
    if ((spins & 1023) == 1023) {
        const rsrc_t rt = mkrsrc(P.ws + FL0, FL_BYTES);
        unsigned o;
        asm volatile("v_mov_b32 %0, 0x100" : "=v"(o));
        if (__builtin_amdgcn_readfirstlane(__builtin_amdgcn_raw_buffer_load_b32(rt, o, 0, 16)) != 0u) return true;
        if (spins > SPIN_CAP) { __builtin_amdgcn_raw_buffer_store_b32(o, rt, o, 0, 16); return true; }
    }
    return false;
}
__device__ __forceinline__ bool ok4(u32x4 v) { return v[0] != SENT && v[1] != SENT && v[2] != SENT && v[3] != SENT; }
__device__ __forceinline__ unsigned nosent(unsigned d) { return d < 0xFFFFFFFEu ? d : 0xFFFFFFFEu; }
__device__ __forceinline__ u32x4 nosent4(u32x4 v) { return (u32x4){nosent(v[0]), nosent(v[1]), nosent(v[2]), nosent(v[3])}; }
__device__ __forceinline__ unsigned umax3(unsigned a, unsigned b, unsigned c) { const unsigned m = a > b ? a : b; return m > c ? m : c; }
__device__ __forceinline__ unsigned umax4(unsigned m, u32x4 v) { return umax3(umax3(m, v[0], v[1]), v[2], v[3]); }
__device__ __forceinline__ u32x4 sent4() { unsigned sv; asm volatile("v_mov_b32 %0, -1" : "=v"(sv)); return (u32x4){sv, sv, sv, sv}; }
template <int N>
__device__ __forceinline__ void poll(rsrc_t rs, unsigned base, const unsigned (&off)[N], u32x4 (&v)[N], const Params& P) {
    for (int spins = 0;; ++spins) {
        unsigned m = 0;
#pragma unroll
        for (int k = 0; k < N; ++k) v[k] = LD128(rs, off[k], base);
#pragma unroll
        for (int k = 0; k < N; ++k) m = umax4(m, v[k]);
        if (__all(m != SENT)) return;
        asm volatile("" ::: "memory");
        if (spin_fail(spins, P)) return;
    }
}
__device__ __forceinline__ bf16x16 cat16(bf16x8 a, bf16x8 b) {
    return __builtin_shufflevector(a, b, 0, 1, 2, 3, 4, 5, 6, 7, 8, 9, 10, 11, 12, 13, 14, 15);
}
__device__ __forceinline__ bf16x8 half8(bf16x16 v, int h) {
    return h ? __builtin_shufflevector(v, v, 8, 9, 10, 11, 12, 13, 14, 15) : __builtin_shufflevector(v, v, 0, 1, 2, 3, 4, 5, 6, 7);
}
__device__ __forceinline__ bf16x16 lds_frag(unsigned a, unsigned step) { return cat16(lds_ld<bf16x8>(a), lds_ld<bf16x8>(a + step)); }
template <int I>
__device__ __forceinline__ void dmaq(const char* g, unsigned l16, unsigned dst) {
    asm volatile("s_mov_b32 m0, %2\n\ts_nop 0\n\tglobal_load_lds_dwordx4 %0, %1 offset:%3 sc1" :: "v"(l16), "s"(g), "s"(dst), "i"(I * 1024) : "memory");
}
__device__ __forceinline__ void dma4(const char* g, unsigned l16, unsigned dst) {
    asm volatile("s_mov_b32 m0, %2\n\ts_nop 0\n\tglobal_load_lds_dwordx4 %0, %1 sc1\n\tglobal_load_lds_dwordx4 %0, %1 offset:1024 sc1\n\t"
                 "global_load_lds_dwordx4 %0, %1 offset:2048 sc1\n\tglobal_load_lds_dwordx4 %0, %1 offset:3072 sc1" :: "v"(l16), "s"(g), "s"(dst) : "memory");
}
__device__ __forceinline__ unsigned toff(int row, int col, int par) {
    return pgb(row >> 5, par) + blin((unsigned)((row >> 4) & 1) * 20480u + (unsigned)(col >> 3) * 256u) + (unsigned)(row & 15) * 16u;
}

#define ASM_ST
#define ASM_ST_DECL
#define ASM_ST_OUT
#define ASM_ST_SAVE(k)
#define ASM_ST2
#define ASM_ST3
#define ASM_ST3_OUT
#define ASM_ST3_SAVE(k)
#define ASM_ST2_OUT
#define ASM_ST2_SAVE(k)
#define STDECL
#define ST(k)
#define STC(k)
#define STFLUSH(t)

#define MARK(c, k)
#define MARKX(c, k)
#define PMARK(c, st, seg)
#define LDECL
#define LA()
#define LB(t)
#define LF()
#define STROLE 3
constexpr unsigned L_MEM = 0;
constexpr unsigned L_Q = 10240;
constexpr unsigned L_KM = 13312;
constexpr unsigned L_QX = 16384;
constexpr unsigned L_KX = 28672;
constexpr unsigned L_XB = 40960;
constexpr unsigned L_XA = 49152;
constexpr unsigned L_W = 81920;
constexpr int MEGA_LDS = 163840;
constexpr int wi(int i, int j) { return (i >> 1) * 5 + ((i & 1) ? 2 + j : j - 1); }

#define RPUB2
#define WNT ""
#define PRIUP "s_setprio 2\n\t"
#define PRISM PRIUP
#define STR2(x) #x
#define STRX(x) STR2(x)
#define W4SLEEP "s_sleep " STRX(W4SL) "\n\t"
#define PLAY 0
#define V1A 0
#define ASM_WLOAD3 \
    "s_xor_b32 %[t], %[wx], 0\n\t" \
    "s_mul_i32 %[t], %[t], 0xc0000\n\t" \
    "buffer_load_dwordx4 v[200:203], %[o0], %[rs], %[t] offen" WNT "\n\t" \
    "buffer_load_dwordx4 v[0:3], %[o1], %[rs], %[t] offen" WNT "\n\t" \
    "buffer_load_dwordx4 v[8:11], %[o2], %[rs], %[t] offen" WNT "\n\t" \
    "s_add_u32 %[t], %[t], 0x18000\n\t" \
    "buffer_load_dwordx4 v[16:19], %[o0], %[rs], %[t] offen" WNT "\n\t" \
    "buffer_load_dwordx4 v[24:27], %[o1], %[rs], %[t] offen" WNT "\n\t" \
    "buffer_load_dwordx4 v[32:35], %[o2], %[rs], %[t] offen" WNT "\n\t" \
    "s_add_u32 %[t], %[t], 0x18000\n\t" \
    "buffer_load_dwordx4 v[208:211], %[o0], %[rs], %[t] offen" WNT "\n\t" \
    "buffer_load_dwordx4 v[40:43], %[o1], %[rs], %[t] offen" WNT "\n\t" \
    "buffer_load_dwordx4 v[48:51], %[o2], %[rs], %[t] offen" WNT "\n\t" \
    "s_add_u32 %[t], %[t], 0x18000\n\t" \
    "buffer_load_dwordx4 v[56:59], %[o0], %[rs], %[t] offen" WNT "\n\t" \
    "buffer_load_dwordx4 v[64:67], %[o1], %[rs], %[t] offen" WNT "\n\t" \
    "buffer_load_dwordx4 v[72:75], %[o2], %[rs], %[t] offen" WNT "\n\t" \
    "s_add_u32 %[t], %[t], 0x18000\n\t" \
    "buffer_load_dwordx4 v[204:207], %[o0], %[rs], %[t] offen" WNT "\n\t" \
    "buffer_load_dwordx4 v[4:7], %[o1], %[rs], %[t] offen" WNT "\n\t" \
    "buffer_load_dwordx4 v[12:15], %[o2], %[rs], %[t] offen" WNT "\n\t" \
    "s_add_u32 %[t], %[t], 0x18000\n\t" \
    "buffer_load_dwordx4 v[20:23], %[o0], %[rs], %[t] offen" WNT "\n\t" \
    "buffer_load_dwordx4 v[28:31], %[o1], %[rs], %[t] offen" WNT "\n\t" \
    "buffer_load_dwordx4 v[36:39], %[o2], %[rs], %[t] offen" WNT "\n\t" \
    "s_add_u32 %[t], %[t], 0x18000\n\t" \
    "buffer_load_dwordx4 v[212:215], %[o0], %[rs], %[t] offen" WNT "\n\t" \
    "buffer_load_dwordx4 v[44:47], %[o1], %[rs], %[t] offen" WNT "\n\t" \
    "buffer_load_dwordx4 v[52:55], %[o2], %[rs], %[t] offen" WNT "\n\t" \
    "s_add_u32 %[t], %[t], 0x18000\n\t" \
    "buffer_load_dwordx4 v[60:63], %[o0], %[rs], %[t] offen" WNT "\n\t" \
    "buffer_load_dwordx4 v[68:71], %[o1], %[rs], %[t] offen" WNT "\n\t" \
    "buffer_load_dwordx4 v[76:79], %[o2], %[rs], %[t] offen" WNT "\n\t" \
    "s_add_u32 %[t], %[t], 0x18000\n\t" \
    "s_waitcnt vmcnt(21)\n\t" \
    "ds_write_b128 %[s0], v[200:203] offset:0\n\t" \
    "ds_write_b128 %[s1], v[0:3] offset:0\n\t" \
    "ds_write_b128 %[s2], v[8:11] offset:0\n\t" \
    "s_waitcnt vmcnt(18)\n\t" \
    "ds_write_b128 %[s0], v[16:19] offset:3456\n\t" \
    "ds_write_b128 %[s1], v[24:27] offset:3456\n\t" \
    "ds_write_b128 %[s2], v[32:35] offset:3456\n\t" \
    "s_waitcnt vmcnt(15)\n\t" \
    "ds_write_b128 %[s0], v[208:211] offset:6912\n\t" \
    "ds_write_b128 %[s1], v[40:43] offset:6912\n\t" \
    "ds_write_b128 %[s2], v[48:51] offset:6912\n\t" \
    "s_waitcnt vmcnt(12)\n\t" \
    "ds_write_b128 %[s0], v[56:59] offset:10368\n\t" \
    "ds_write_b128 %[s1], v[64:67] offset:10368\n\t" \
    "ds_write_b128 %[s2], v[72:75] offset:10368\n\t" \
    "s_waitcnt lgkmcnt(0)\n\t" \
    "s_barrier \n\t" \
    "ds_read_b64_tr_b16 v[200:201], %[t0] offset:0\n\t" \
    "ds_read_b64_tr_b16 v[202:203], %[t0] offset:1664\n\t" \
    "ds_read_b64_tr_b16 v[0:1], %[t0] offset:128\n\t" \
    "ds_read_b64_tr_b16 v[2:3], %[t0] offset:1792\n\t" \
    "ds_read_b64_tr_b16 v[8:9], %[t0] offset:256\n\t" \
    "ds_read_b64_tr_b16 v[10:11], %[t0] offset:1920\n\t" \
    "ds_read_b64_tr_b16 v[16:17], %[t1] offset:0\n\t" \
    "ds_read_b64_tr_b16 v[18:19], %[t1] offset:1664\n\t" \
    "ds_read_b64_tr_b16 v[24:25], %[t1] offset:128\n\t" \
    "ds_read_b64_tr_b16 v[26:27], %[t1] offset:1792\n\t" \
    "ds_read_b64_tr_b16 v[32:33], %[t1] offset:256\n\t" \
    "ds_read_b64_tr_b16 v[34:35], %[t1] offset:1920\n\t" \
    "ds_read_b64_tr_b16 v[208:209], %[t2] offset:0\n\t" \
    "ds_read_b64_tr_b16 v[210:211], %[t2] offset:1664\n\t" \
    "ds_read_b64_tr_b16 v[40:41], %[t2] offset:128\n\t" \
    "ds_read_b64_tr_b16 v[42:43], %[t2] offset:1792\n\t" \
    "ds_read_b64_tr_b16 v[48:49], %[t2] offset:256\n\t" \
    "ds_read_b64_tr_b16 v[50:51], %[t2] offset:1920\n\t" \
    "ds_read_b64_tr_b16 v[56:57], %[t3] offset:0\n\t" \
    "ds_read_b64_tr_b16 v[58:59], %[t3] offset:1664\n\t" \
    "ds_read_b64_tr_b16 v[64:65], %[t3] offset:128\n\t" \
    "ds_read_b64_tr_b16 v[66:67], %[t3] offset:1792\n\t" \
    "ds_read_b64_tr_b16 v[72:73], %[t3] offset:256\n\t" \
    "ds_read_b64_tr_b16 v[74:75], %[t3] offset:1920\n\t" \
    "s_xor_b32 %[t], %[wx], 4\n\t" \
    "s_mul_i32 %[t], %[t], 0xc0000\n\t" \
    "buffer_load_dwordx4 v[216:219], %[o0], %[rs], %[t] offen" WNT "\n\t" \
    "buffer_load_dwordx4 v[80:83], %[o1], %[rs], %[t] offen" WNT "\n\t" \
    "buffer_load_dwordx4 v[88:91], %[o2], %[rs], %[t] offen" WNT "\n\t" \
    "s_add_u32 %[t], %[t], 0x18000\n\t" \
    "buffer_load_dwordx4 v[96:99], %[o0], %[rs], %[t] offen" WNT "\n\t" \
    "buffer_load_dwordx4 v[104:107], %[o1], %[rs], %[t] offen" WNT "\n\t" \
    "buffer_load_dwordx4 v[112:115], %[o2], %[rs], %[t] offen" WNT "\n\t" \
    "s_add_u32 %[t], %[t], 0x18000\n\t" \
    "buffer_load_dwordx4 v[224:227], %[o0], %[rs], %[t] offen" WNT "\n\t" \
    "buffer_load_dwordx4 v[120:123], %[o1], %[rs], %[t] offen" WNT "\n\t" \
    "buffer_load_dwordx4 v[128:131], %[o2], %[rs], %[t] offen" WNT "\n\t" \
    "s_add_u32 %[t], %[t], 0x18000\n\t" \
    "buffer_load_dwordx4 v[136:139], %[o0], %[rs], %[t] offen" WNT "\n\t" \
    "buffer_load_dwordx4 v[144:147], %[o1], %[rs], %[t] offen" WNT "\n\t" \
    "buffer_load_dwordx4 v[152:155], %[o2], %[rs], %[t] offen" WNT "\n\t" \
    "s_add_u32 %[t], %[t], 0x18000\n\t" \
    "s_waitcnt lgkmcnt(0)\n\t" \
    "s_barrier \n\t" \
    "s_waitcnt vmcnt(21)\n\t" \
    "ds_write_b128 %[s0], v[204:207] offset:0\n\t" \
    "ds_write_b128 %[s1], v[4:7] offset:0\n\t" \
    "ds_write_b128 %[s2], v[12:15] offset:0\n\t" \
    "s_waitcnt vmcnt(18)\n\t" \
    "ds_write_b128 %[s0], v[20:23] offset:3456\n\t" \
    "ds_write_b128 %[s1], v[28:31] offset:3456\n\t" \
    "ds_write_b128 %[s2], v[36:39] offset:3456\n\t" \
    "s_waitcnt vmcnt(15)\n\t" \
    "ds_write_b128 %[s0], v[212:215] offset:6912\n\t" \
    "ds_write_b128 %[s1], v[44:47] offset:6912\n\t" \
    "ds_write_b128 %[s2], v[52:55] offset:6912\n\t" \
    "s_waitcnt vmcnt(12)\n\t" \
    "ds_write_b128 %[s0], v[60:63] offset:10368\n\t" \
    "ds_write_b128 %[s1], v[68:71] offset:10368\n\t" \
    "ds_write_b128 %[s2], v[76:79] offset:10368\n\t" \
    "s_waitcnt lgkmcnt(0)\n\t" \
    "s_barrier \n\t" \
    "ds_read_b64_tr_b16 v[204:205], %[t0] offset:0\n\t" \
    "ds_read_b64_tr_b16 v[206:207], %[t0] offset:1664\n\t" \
    "ds_read_b64_tr_b16 v[4:5], %[t0] offset:128\n\t" \
    "ds_read_b64_tr_b16 v[6:7], %[t0] offset:1792\n\t" \
    "ds_read_b64_tr_b16 v[12:13], %[t0] offset:256\n\t" \
    "ds_read_b64_tr_b16 v[14:15], %[t0] offset:1920\n\t" \
    "ds_read_b64_tr_b16 v[20:21], %[t1] offset:0\n\t" \
    "ds_read_b64_tr_b16 v[22:23], %[t1] offset:1664\n\t" \
    "ds_read_b64_tr_b16 v[28:29], %[t1] offset:128\n\t" \
    "ds_read_b64_tr_b16 v[30:31], %[t1] offset:1792\n\t" \
    "ds_read_b64_tr_b16 v[36:37], %[t1] offset:256\n\t" \
    "ds_read_b64_tr_b16 v[38:39], %[t1] offset:1920\n\t" \
    "ds_read_b64_tr_b16 v[212:213], %[t2] offset:0\n\t" \
    "ds_read_b64_tr_b16 v[214:215], %[t2] offset:1664\n\t" \
    "ds_read_b64_tr_b16 v[44:45], %[t2] offset:128\n\t" \
    "ds_read_b64_tr_b16 v[46:47], %[t2] offset:1792\n\t" \
    "ds_read_b64_tr_b16 v[52:53], %[t2] offset:256\n\t" \
    "ds_read_b64_tr_b16 v[54:55], %[t2] offset:1920\n\t" \
    "ds_read_b64_tr_b16 v[60:61], %[t3] offset:0\n\t" \
    "ds_read_b64_tr_b16 v[62:63], %[t3] offset:1664\n\t" \
    "ds_read_b64_tr_b16 v[68:69], %[t3] offset:128\n\t" \
    "ds_read_b64_tr_b16 v[70:71], %[t3] offset:1792\n\t" \
    "ds_read_b64_tr_b16 v[76:77], %[t3] offset:256\n\t" \
    "ds_read_b64_tr_b16 v[78:79], %[t3] offset:1920\n\t" \
    "buffer_load_dwordx4 v[220:223], %[o0], %[rs], %[t] offen" WNT "\n\t" \
    "buffer_load_dwordx4 v[84:87], %[o1], %[rs], %[t] offen" WNT "\n\t" \
    "buffer_load_dwordx4 v[92:95], %[o2], %[rs], %[t] offen" WNT "\n\t" \
    "s_add_u32 %[t], %[t], 0x18000\n\t" \
    "buffer_load_dwordx4 v[100:103], %[o0], %[rs], %[t] offen" WNT "\n\t" \
    "buffer_load_dwordx4 v[108:111], %[o1], %[rs], %[t] offen" WNT "\n\t" \
    "buffer_load_dwordx4 v[116:119], %[o2], %[rs], %[t] offen" WNT "\n\t" \
    "s_add_u32 %[t], %[t], 0x18000\n\t" \
    "buffer_load_dwordx4 v[228:231], %[o0], %[rs], %[t] offen" WNT "\n\t" \
    "buffer_load_dwordx4 v[124:127], %[o1], %[rs], %[t] offen" WNT "\n\t" \
    "buffer_load_dwordx4 v[132:135], %[o2], %[rs], %[t] offen" WNT "\n\t" \
    "s_add_u32 %[t], %[t], 0x18000\n\t" \
    "buffer_load_dwordx4 v[140:143], %[o0], %[rs], %[t] offen" WNT "\n\t" \
    "buffer_load_dwordx4 v[148:151], %[o1], %[rs], %[t] offen" WNT "\n\t" \
    "buffer_load_dwordx4 v[156:159], %[o2], %[rs], %[t] offen" WNT "\n\t" \
    "s_add_u32 %[t], %[t], 0x18000\n\t" \
    "s_waitcnt lgkmcnt(0)\n\t" \
    "s_barrier \n\t" \
    "s_waitcnt vmcnt(21)\n\t" \
    "ds_write_b128 %[s0], v[216:219] offset:0\n\t" \
    "ds_write_b128 %[s1], v[80:83] offset:0\n\t" \
    "ds_write_b128 %[s2], v[88:91] offset:0\n\t" \
    "s_waitcnt vmcnt(18)\n\t" \
    "ds_write_b128 %[s0], v[96:99] offset:3456\n\t" \
    "ds_write_b128 %[s1], v[104:107] offset:3456\n\t" \
    "ds_write_b128 %[s2], v[112:115] offset:3456\n\t" \
    "s_waitcnt vmcnt(15)\n\t" \
    "ds_write_b128 %[s0], v[224:227] offset:6912\n\t" \
    "ds_write_b128 %[s1], v[120:123] offset:6912\n\t" \
    "ds_write_b128 %[s2], v[128:131] offset:6912\n\t" \
    "s_waitcnt vmcnt(12)\n\t" \
    "ds_write_b128 %[s0], v[136:139] offset:10368\n\t" \
    "ds_write_b128 %[s1], v[144:147] offset:10368\n\t" \
    "ds_write_b128 %[s2], v[152:155] offset:10368\n\t" \
    "s_waitcnt lgkmcnt(0)\n\t" \
    "s_barrier \n\t" \
    "ds_read_b64_tr_b16 v[216:217], %[t0] offset:0\n\t" \
    "ds_read_b64_tr_b16 v[218:219], %[t0] offset:1664\n\t" \
    "ds_read_b64_tr_b16 v[80:81], %[t0] offset:128\n\t" \
    "ds_read_b64_tr_b16 v[82:83], %[t0] offset:1792\n\t" \
    "ds_read_b64_tr_b16 v[88:89], %[t0] offset:256\n\t" \
    "ds_read_b64_tr_b16 v[90:91], %[t0] offset:1920\n\t" \
    "ds_read_b64_tr_b16 v[96:97], %[t1] offset:0\n\t" \
    "ds_read_b64_tr_b16 v[98:99], %[t1] offset:1664\n\t" \
    "ds_read_b64_tr_b16 v[104:105], %[t1] offset:128\n\t" \
    "ds_read_b64_tr_b16 v[106:107], %[t1] offset:1792\n\t" \
    "ds_read_b64_tr_b16 v[112:113], %[t1] offset:256\n\t" \
    "ds_read_b64_tr_b16 v[114:115], %[t1] offset:1920\n\t" \
    "ds_read_b64_tr_b16 v[224:225], %[t2] offset:0\n\t" \
    "ds_read_b64_tr_b16 v[226:227], %[t2] offset:1664\n\t" \
    "ds_read_b64_tr_b16 v[120:121], %[t2] offset:128\n\t" \
    "ds_read_b64_tr_b16 v[122:123], %[t2] offset:1792\n\t" \
    "ds_read_b64_tr_b16 v[128:129], %[t2] offset:256\n\t" \
    "ds_read_b64_tr_b16 v[130:131], %[t2] offset:1920\n\t" \
    "ds_read_b64_tr_b16 v[136:137], %[t3] offset:0\n\t" \
    "ds_read_b64_tr_b16 v[138:139], %[t3] offset:1664\n\t" \
    "ds_read_b64_tr_b16 v[144:145], %[t3] offset:128\n\t" \
    "ds_read_b64_tr_b16 v[146:147], %[t3] offset:1792\n\t" \
    "ds_read_b64_tr_b16 v[152:153], %[t3] offset:256\n\t" \
    "ds_read_b64_tr_b16 v[154:155], %[t3] offset:1920\n\t" \
    "s_and_b32 %[t], %[wx], 3\n\t" \
    "s_mul_i32 %[t], %[t], 0x60000\n\t" \
    "s_add_u32 %[t], %[t], 0x600000\n\t" \
    "buffer_load_dwordx4 v[232:235], %[o0], %[rs], %[t] offen" WNT "\n\t" \
    "buffer_load_dwordx4 v[160:163], %[o1], %[rs], %[t] offen" WNT "\n\t" \
    "buffer_load_dwordx4 v[168:171], %[o2], %[rs], %[t] offen" WNT "\n\t" \
    "s_add_u32 %[t], %[t], 0x18000\n\t" \
    "buffer_load_dwordx4 v[236:239], %[o0], %[rs], %[t] offen" WNT "\n\t" \
    "buffer_load_dwordx4 v[164:167], %[o1], %[rs], %[t] offen" WNT "\n\t" \
    "buffer_load_dwordx4 v[172:175], %[o2], %[rs], %[t] offen" WNT "\n\t" \
    "s_add_u32 %[t], %[t], 0x18000\n\t" \
    "buffer_load_dwordx4 v[176:179], %[o0], %[rs], %[t] offen" WNT "\n\t" \
    "buffer_load_dwordx4 v[184:187], %[o1], %[rs], %[t] offen" WNT "\n\t" \
    "buffer_load_dwordx4 v[192:195], %[o2], %[rs], %[t] offen" WNT "\n\t" \
    "s_add_u32 %[t], %[t], 0x18000\n\t" \
    "buffer_load_dwordx4 v[180:183], %[o0], %[rs], %[t] offen" WNT "\n\t" \
    "buffer_load_dwordx4 v[188:191], %[o1], %[rs], %[t] offen" WNT "\n\t" \
    "buffer_load_dwordx4 v[196:199], %[o2], %[rs], %[t] offen" WNT "\n\t" \
    "s_add_u32 %[t], %[t], 0x18000\n\t" \
    "s_waitcnt lgkmcnt(0)\n\t" \
    "s_barrier \n\t" \
    "s_waitcnt vmcnt(21)\n\t" \
    "ds_write_b128 %[s0], v[220:223] offset:0\n\t" \
    "ds_write_b128 %[s1], v[84:87] offset:0\n\t" \
    "ds_write_b128 %[s2], v[92:95] offset:0\n\t" \
    "s_waitcnt vmcnt(18)\n\t" \
    "ds_write_b128 %[s0], v[100:103] offset:3456\n\t" \
    "ds_write_b128 %[s1], v[108:111] offset:3456\n\t" \
    "ds_write_b128 %[s2], v[116:119] offset:3456\n\t" \
    "s_waitcnt vmcnt(15)\n\t" \
    "ds_write_b128 %[s0], v[228:231] offset:6912\n\t" \
    "ds_write_b128 %[s1], v[124:127] offset:6912\n\t" \
    "ds_write_b128 %[s2], v[132:135] offset:6912\n\t" \
    "s_waitcnt vmcnt(12)\n\t" \
    "ds_write_b128 %[s0], v[140:143] offset:10368\n\t" \
    "ds_write_b128 %[s1], v[148:151] offset:10368\n\t" \
    "ds_write_b128 %[s2], v[156:159] offset:10368\n\t" \
    "s_waitcnt lgkmcnt(0)\n\t" \
    "s_barrier \n\t" \
    "ds_read_b64_tr_b16 v[220:221], %[t0] offset:0\n\t" \
    "ds_read_b64_tr_b16 v[222:223], %[t0] offset:1664\n\t" \
    "ds_read_b64_tr_b16 v[84:85], %[t0] offset:128\n\t" \
    "ds_read_b64_tr_b16 v[86:87], %[t0] offset:1792\n\t" \
    "ds_read_b64_tr_b16 v[92:93], %[t0] offset:256\n\t" \
    "ds_read_b64_tr_b16 v[94:95], %[t0] offset:1920\n\t" \
    "ds_read_b64_tr_b16 v[100:101], %[t1] offset:0\n\t" \
    "ds_read_b64_tr_b16 v[102:103], %[t1] offset:1664\n\t" \
    "ds_read_b64_tr_b16 v[108:109], %[t1] offset:128\n\t" \
    "ds_read_b64_tr_b16 v[110:111], %[t1] offset:1792\n\t" \
    "ds_read_b64_tr_b16 v[116:117], %[t1] offset:256\n\t" \
    "ds_read_b64_tr_b16 v[118:119], %[t1] offset:1920\n\t" \
    "ds_read_b64_tr_b16 v[228:229], %[t2] offset:0\n\t" \
    "ds_read_b64_tr_b16 v[230:231], %[t2] offset:1664\n\t" \
    "ds_read_b64_tr_b16 v[124:125], %[t2] offset:128\n\t" \
    "ds_read_b64_tr_b16 v[126:127], %[t2] offset:1792\n\t" \
    "ds_read_b64_tr_b16 v[132:133], %[t2] offset:256\n\t" \
    "ds_read_b64_tr_b16 v[134:135], %[t2] offset:1920\n\t" \
    "ds_read_b64_tr_b16 v[140:141], %[t3] offset:0\n\t" \
    "ds_read_b64_tr_b16 v[142:143], %[t3] offset:1664\n\t" \
    "ds_read_b64_tr_b16 v[148:149], %[t3] offset:128\n\t" \
    "ds_read_b64_tr_b16 v[150:151], %[t3] offset:1792\n\t" \
    "ds_read_b64_tr_b16 v[156:157], %[t3] offset:256\n\t" \
    "ds_read_b64_tr_b16 v[158:159], %[t3] offset:1920\n\t" \
    "s_waitcnt lgkmcnt(0)\n\t" \
    "s_barrier \n\t" \
    "s_waitcnt vmcnt(9)\n\t" \
    "ds_write_b128 %[s0], v[232:235] offset:0\n\t" \
    "ds_write_b128 %[s1], v[160:163] offset:0\n\t" \
    "ds_write_b128 %[s2], v[168:171] offset:0\n\t" \
    "s_waitcnt vmcnt(6)\n\t" \
    "ds_write_b128 %[s0], v[236:239] offset:3456\n\t" \
    "ds_write_b128 %[s1], v[164:167] offset:3456\n\t" \
    "ds_write_b128 %[s2], v[172:175] offset:3456\n\t" \
    "s_waitcnt vmcnt(3)\n\t" \
    "ds_write_b128 %[s0], v[176:179] offset:6912\n\t" \
    "ds_write_b128 %[s1], v[184:187] offset:6912\n\t" \
    "ds_write_b128 %[s2], v[192:195] offset:6912\n\t" \
    "s_waitcnt vmcnt(0)\n\t" \
    "ds_write_b128 %[s0], v[180:183] offset:10368\n\t" \
    "ds_write_b128 %[s1], v[188:191] offset:10368\n\t" \
    "ds_write_b128 %[s2], v[196:199] offset:10368\n\t" \
    "s_waitcnt lgkmcnt(0)\n\t" \
    "s_barrier \n\t" \
    "ds_read_b64_tr_b16 v[232:233], %[t4] offset:0\n\t" \
    "ds_read_b64_tr_b16 v[234:235], %[t4] offset:1664\n\t" \
    "ds_read_b64_tr_b16 v[160:161], %[t4] offset:128\n\t" \
    "ds_read_b64_tr_b16 v[162:163], %[t4] offset:1792\n\t" \
    "ds_read_b64_tr_b16 v[168:169], %[t4] offset:256\n\t" \
    "ds_read_b64_tr_b16 v[170:171], %[t4] offset:1920\n\t" \
    "ds_read_b64_tr_b16 v[236:237], %[t4] offset:13824\n\t" \
    "ds_read_b64_tr_b16 v[238:239], %[t4] offset:15488\n\t" \
    "ds_read_b64_tr_b16 v[164:165], %[t4] offset:13952\n\t" \
    "ds_read_b64_tr_b16 v[166:167], %[t4] offset:15616\n\t" \
    "ds_read_b64_tr_b16 v[172:173], %[t4] offset:14080\n\t" \
    "ds_read_b64_tr_b16 v[174:175], %[t4] offset:15744\n\t" \
    "ds_read_b64_tr_b16 v[176:177], %[t4] offset:27648\n\t" \
    "ds_read_b64_tr_b16 v[178:179], %[t4] offset:29312\n\t" \
    "ds_read_b64_tr_b16 v[184:185], %[t4] offset:27776\n\t" \
    "ds_read_b64_tr_b16 v[186:187], %[t4] offset:29440\n\t" \
    "ds_read_b64_tr_b16 v[192:193], %[t4] offset:27904\n\t" \
    "ds_read_b64_tr_b16 v[194:195], %[t4] offset:29568\n\t" \
    "ds_read_b64_tr_b16 v[180:181], %[t4] offset:41472\n\t" \
    "ds_read_b64_tr_b16 v[182:183], %[t4] offset:43136\n\t" \
    "ds_read_b64_tr_b16 v[188:189], %[t4] offset:41600\n\t" \
    "ds_read_b64_tr_b16 v[190:191], %[t4] offset:43264\n\t" \
    "ds_read_b64_tr_b16 v[196:197], %[t4] offset:41728\n\t" \
    "ds_read_b64_tr_b16 v[198:199], %[t4] offset:43392\n\t" \
    "s_waitcnt lgkmcnt(0)\n\t" \
    "s_barrier \n\t" \
    "ds_write_b128 %[wl], v[200:203] offset:0\n\t" \
    "ds_write_b128 %[wl], v[204:207] offset:1024\n\t" \
    "ds_write_b128 %[wl], v[208:211] offset:2048\n\t" \
    "ds_write_b128 %[wl], v[212:215] offset:3072\n\t" \
    "ds_write_b128 %[wl], v[216:219] offset:4096\n\t" \
    "ds_write_b128 %[wl], v[220:223] offset:5120\n\t" \
    "ds_write_b128 %[wl], v[224:227] offset:6144\n\t" \
    "ds_write_b128 %[wl], v[228:231] offset:7168\n\t" \
    "ds_write_b128 %[wl], v[232:235] offset:8192\n\t" \
    "ds_write_b128 %[wl], v[236:239] offset:9216\n\t" \
    "s_waitcnt lgkmcnt(0)\n\t"
#define ASM_WLOADO \
    "s_mov_b32 %[t], 0x0\n\t" \
    "buffer_load_dwordx4 v[200:203], %[o0], %[rs], %[t] offen" WNT "\n\t" \
    "buffer_load_dwordx4 v[16:19], %[o1], %[rs], %[t] offen" WNT "\n\t" \
    "buffer_load_dwordx4 v[208:211], %[o2], %[rs], %[t] offen" WNT "\n\t" \
    "buffer_load_dwordx4 v[56:59], %[o3], %[rs], %[t] offen" WNT "\n\t" \
    "buffer_load_dwordx4 v[216:219], %[o4], %[rs], %[t] offen" WNT "\n\t" \
    "s_mov_b32 %[t], 0xa000\n\t" \
    "buffer_load_dwordx4 v[96:99], %[o0], %[rs], %[t] offen" WNT "\n\t" \
    "buffer_load_dwordx4 v[224:227], %[o1], %[rs], %[t] offen" WNT "\n\t" \
    "buffer_load_dwordx4 v[136:139], %[o2], %[rs], %[t] offen" WNT "\n\t" \
    "buffer_load_dwordx4 v[232:235], %[o3], %[rs], %[t] offen" WNT "\n\t" \
    "buffer_load_dwordx4 v[176:179], %[o4], %[rs], %[t] offen" WNT "\n\t" \
    "s_mov_b32 %[t], 0x14000\n\t" \
    "buffer_load_dwordx4 v[204:207], %[o0], %[rs], %[t] offen" WNT "\n\t" \
    "buffer_load_dwordx4 v[20:23], %[o1], %[rs], %[t] offen" WNT "\n\t" \
    "buffer_load_dwordx4 v[212:215], %[o2], %[rs], %[t] offen" WNT "\n\t" \
    "buffer_load_dwordx4 v[60:63], %[o3], %[rs], %[t] offen" WNT "\n\t" \
    "buffer_load_dwordx4 v[220:223], %[o4], %[rs], %[t] offen" WNT "\n\t" \
    "s_mov_b32 %[t], 0x1e000\n\t" \
    "buffer_load_dwordx4 v[100:103], %[o0], %[rs], %[t] offen" WNT "\n\t" \
    "buffer_load_dwordx4 v[228:231], %[o1], %[rs], %[t] offen" WNT "\n\t" \
    "buffer_load_dwordx4 v[140:143], %[o2], %[rs], %[t] offen" WNT "\n\t" \
    "buffer_load_dwordx4 v[236:239], %[o3], %[rs], %[t] offen" WNT "\n\t" \
    "buffer_load_dwordx4 v[180:183], %[o4], %[rs], %[t] offen" WNT "\n\t" \
    "s_waitcnt vmcnt(15)\n\t" \
    "ds_write_b128 %[s0], v[200:203] offset:0\n\t" \
    "ds_write_b128 %[s1], v[16:19] offset:0\n\t" \
    "ds_write_b128 %[s2], v[208:211] offset:0\n\t" \
    "ds_write_b128 %[s3], v[56:59] offset:0\n\t" \
    "ds_write_b128 %[s4], v[216:219] offset:0\n\t" \
    "s_waitcnt vmcnt(10)\n\t" \
    "ds_write_b128 %[s0], v[96:99] offset:5632\n\t" \
    "ds_write_b128 %[s1], v[224:227] offset:5632\n\t" \
    "ds_write_b128 %[s2], v[136:139] offset:5632\n\t" \
    "ds_write_b128 %[s3], v[232:235] offset:5632\n\t" \
    "ds_write_b128 %[s4], v[176:179] offset:5632\n\t" \
    "ds_read_b64_tr_b16 v[200:201], %[tr] offset:0\n\t" \
    "ds_read_b64_tr_b16 v[202:203], %[tr] offset:5632\n\t" \
    "ds_read_b64_tr_b16 v[16:17], %[tr] offset:32\n\t" \
    "ds_read_b64_tr_b16 v[18:19], %[tr] offset:5664\n\t" \
    "ds_read_b64_tr_b16 v[208:209], %[tr] offset:64\n\t" \
    "ds_read_b64_tr_b16 v[210:211], %[tr] offset:5696\n\t" \
    "ds_read_b64_tr_b16 v[56:57], %[tr] offset:96\n\t" \
    "ds_read_b64_tr_b16 v[58:59], %[tr] offset:5728\n\t" \
    "ds_read_b64_tr_b16 v[216:217], %[tr] offset:128\n\t" \
    "ds_read_b64_tr_b16 v[218:219], %[tr] offset:5760\n\t" \
    "ds_read_b64_tr_b16 v[96:97], %[tr] offset:160\n\t" \
    "ds_read_b64_tr_b16 v[98:99], %[tr] offset:5792\n\t" \
    "ds_read_b64_tr_b16 v[224:225], %[tr] offset:192\n\t" \
    "ds_read_b64_tr_b16 v[226:227], %[tr] offset:5824\n\t" \
    "ds_read_b64_tr_b16 v[136:137], %[tr] offset:224\n\t" \
    "ds_read_b64_tr_b16 v[138:139], %[tr] offset:5856\n\t" \
    "ds_read_b64_tr_b16 v[232:233], %[tr] offset:256\n\t" \
    "ds_read_b64_tr_b16 v[234:235], %[tr] offset:5888\n\t" \
    "ds_read_b64_tr_b16 v[176:177], %[tr] offset:288\n\t" \
    "ds_read_b64_tr_b16 v[178:179], %[tr] offset:5920\n\t" \
    "s_barrier \n\t" \
    "s_mov_b32 %[t], 0x28000\n\t" \
    "buffer_load_dwordx4 v[0:3], %[o0], %[rs], %[t] offen" WNT "\n\t" \
    "buffer_load_dwordx4 v[24:27], %[o1], %[rs], %[t] offen" WNT "\n\t" \
    "buffer_load_dwordx4 v[40:43], %[o2], %[rs], %[t] offen" WNT "\n\t" \
    "buffer_load_dwordx4 v[64:67], %[o3], %[rs], %[t] offen" WNT "\n\t" \
    "buffer_load_dwordx4 v[80:83], %[o4], %[rs], %[t] offen" WNT "\n\t" \
    "s_mov_b32 %[t], 0x32000\n\t" \
    "buffer_load_dwordx4 v[104:107], %[o0], %[rs], %[t] offen" WNT "\n\t" \
    "buffer_load_dwordx4 v[120:123], %[o1], %[rs], %[t] offen" WNT "\n\t" \
    "buffer_load_dwordx4 v[144:147], %[o2], %[rs], %[t] offen" WNT "\n\t" \
    "buffer_load_dwordx4 v[160:163], %[o3], %[rs], %[t] offen" WNT "\n\t" \
    "buffer_load_dwordx4 v[184:187], %[o4], %[rs], %[t] offen" WNT "\n\t" \
    "s_waitcnt lgkmcnt(0)\n\t" \
    "s_barrier \n\t" \
    "s_waitcnt vmcnt(15)\n\t" \
    "ds_write_b128 %[s0], v[204:207] offset:0\n\t" \
    "ds_write_b128 %[s1], v[20:23] offset:0\n\t" \
    "ds_write_b128 %[s2], v[212:215] offset:0\n\t" \
    "ds_write_b128 %[s3], v[60:63] offset:0\n\t" \
    "ds_write_b128 %[s4], v[220:223] offset:0\n\t" \
    "s_waitcnt vmcnt(10)\n\t" \
    "ds_write_b128 %[s0], v[100:103] offset:5632\n\t" \
    "ds_write_b128 %[s1], v[228:231] offset:5632\n\t" \
    "ds_write_b128 %[s2], v[140:143] offset:5632\n\t" \
    "ds_write_b128 %[s3], v[236:239] offset:5632\n\t" \
    "ds_write_b128 %[s4], v[180:183] offset:5632\n\t" \
    "ds_read_b64_tr_b16 v[204:205], %[tr] offset:0\n\t" \
    "ds_read_b64_tr_b16 v[206:207], %[tr] offset:5632\n\t" \
    "ds_read_b64_tr_b16 v[20:21], %[tr] offset:32\n\t" \
    "ds_read_b64_tr_b16 v[22:23], %[tr] offset:5664\n\t" \
    "ds_read_b64_tr_b16 v[212:213], %[tr] offset:64\n\t" \
    "ds_read_b64_tr_b16 v[214:215], %[tr] offset:5696\n\t" \
    "ds_read_b64_tr_b16 v[60:61], %[tr] offset:96\n\t" \
    "ds_read_b64_tr_b16 v[62:63], %[tr] offset:5728\n\t" \
    "ds_read_b64_tr_b16 v[220:221], %[tr] offset:128\n\t" \
    "ds_read_b64_tr_b16 v[222:223], %[tr] offset:5760\n\t" \
    "ds_read_b64_tr_b16 v[100:101], %[tr] offset:160\n\t" \
    "ds_read_b64_tr_b16 v[102:103], %[tr] offset:5792\n\t" \
    "ds_read_b64_tr_b16 v[228:229], %[tr] offset:192\n\t" \
    "ds_read_b64_tr_b16 v[230:231], %[tr] offset:5824\n\t" \
    "ds_read_b64_tr_b16 v[140:141], %[tr] offset:224\n\t" \
    "ds_read_b64_tr_b16 v[142:143], %[tr] offset:5856\n\t" \
    "ds_read_b64_tr_b16 v[236:237], %[tr] offset:256\n\t" \
    "ds_read_b64_tr_b16 v[238:239], %[tr] offset:5888\n\t" \
    "ds_read_b64_tr_b16 v[180:181], %[tr] offset:288\n\t" \
    "ds_read_b64_tr_b16 v[182:183], %[tr] offset:5920\n\t" \
    "s_barrier \n\t" \
    "s_mov_b32 %[t], 0x5000\n\t" \
    "buffer_load_dwordx4 v[4:7], %[o0], %[rs], %[t] offen" WNT "\n\t" \
    "buffer_load_dwordx4 v[28:31], %[o1], %[rs], %[t] offen" WNT "\n\t" \
    "buffer_load_dwordx4 v[44:47], %[o2], %[rs], %[t] offen" WNT "\n\t" \
    "buffer_load_dwordx4 v[68:71], %[o3], %[rs], %[t] offen" WNT "\n\t" \
    "buffer_load_dwordx4 v[84:87], %[o4], %[rs], %[t] offen" WNT "\n\t" \
    "s_mov_b32 %[t], 0xf000\n\t" \
    "buffer_load_dwordx4 v[108:111], %[o0], %[rs], %[t] offen" WNT "\n\t" \
    "buffer_load_dwordx4 v[124:127], %[o1], %[rs], %[t] offen" WNT "\n\t" \
    "buffer_load_dwordx4 v[148:151], %[o2], %[rs], %[t] offen" WNT "\n\t" \
    "buffer_load_dwordx4 v[164:167], %[o3], %[rs], %[t] offen" WNT "\n\t" \
    "buffer_load_dwordx4 v[188:191], %[o4], %[rs], %[t] offen" WNT "\n\t" \
    "s_waitcnt lgkmcnt(0)\n\t" \
    "s_barrier \n\t" \
    "s_waitcnt vmcnt(15)\n\t" \
    "ds_write_b128 %[s0], v[0:3] offset:0\n\t" \
    "ds_write_b128 %[s1], v[24:27] offset:0\n\t" \
    "ds_write_b128 %[s2], v[40:43] offset:0\n\t" \
    "ds_write_b128 %[s3], v[64:67] offset:0\n\t" \
    "ds_write_b128 %[s4], v[80:83] offset:0\n\t" \
    "s_waitcnt vmcnt(10)\n\t" \
    "ds_write_b128 %[s0], v[104:107] offset:5632\n\t" \
    "ds_write_b128 %[s1], v[120:123] offset:5632\n\t" \
    "ds_write_b128 %[s2], v[144:147] offset:5632\n\t" \
    "ds_write_b128 %[s3], v[160:163] offset:5632\n\t" \
    "ds_write_b128 %[s4], v[184:187] offset:5632\n\t" \
    "ds_read_b64_tr_b16 v[0:1], %[tr] offset:0\n\t" \
    "ds_read_b64_tr_b16 v[2:3], %[tr] offset:5632\n\t" \
    "ds_read_b64_tr_b16 v[24:25], %[tr] offset:32\n\t" \
    "ds_read_b64_tr_b16 v[26:27], %[tr] offset:5664\n\t" \
    "ds_read_b64_tr_b16 v[40:41], %[tr] offset:64\n\t" \
    "ds_read_b64_tr_b16 v[42:43], %[tr] offset:5696\n\t" \
    "ds_read_b64_tr_b16 v[64:65], %[tr] offset:96\n\t" \
    "ds_read_b64_tr_b16 v[66:67], %[tr] offset:5728\n\t" \
    "ds_read_b64_tr_b16 v[80:81], %[tr] offset:128\n\t" \
    "ds_read_b64_tr_b16 v[82:83], %[tr] offset:5760\n\t" \
    "ds_read_b64_tr_b16 v[104:105], %[tr] offset:160\n\t" \
    "ds_read_b64_tr_b16 v[106:107], %[tr] offset:5792\n\t" \
    "ds_read_b64_tr_b16 v[120:121], %[tr] offset:192\n\t" \
    "ds_read_b64_tr_b16 v[122:123], %[tr] offset:5824\n\t" \
    "ds_read_b64_tr_b16 v[144:145], %[tr] offset:224\n\t" \
    "ds_read_b64_tr_b16 v[146:147], %[tr] offset:5856\n\t" \
    "ds_read_b64_tr_b16 v[160:161], %[tr] offset:256\n\t" \
    "ds_read_b64_tr_b16 v[162:163], %[tr] offset:5888\n\t" \
    "ds_read_b64_tr_b16 v[184:185], %[tr] offset:288\n\t" \
    "ds_read_b64_tr_b16 v[186:187], %[tr] offset:5920\n\t" \
    "s_barrier \n\t" \
    "s_mov_b32 %[t], 0x19000\n\t" \
    "buffer_load_dwordx4 v[8:11], %[o0], %[rs], %[t] offen" WNT "\n\t" \
    "buffer_load_dwordx4 v[32:35], %[o1], %[rs], %[t] offen" WNT "\n\t" \
    "buffer_load_dwordx4 v[48:51], %[o2], %[rs], %[t] offen" WNT "\n\t" \
    "buffer_load_dwordx4 v[72:75], %[o3], %[rs], %[t] offen" WNT "\n\t" \
    "buffer_load_dwordx4 v[88:91], %[o4], %[rs], %[t] offen" WNT "\n\t" \
    "s_mov_b32 %[t], 0x23000\n\t" \
    "buffer_load_dwordx4 v[112:115], %[o0], %[rs], %[t] offen" WNT "\n\t" \
    "buffer_load_dwordx4 v[128:131], %[o1], %[rs], %[t] offen" WNT "\n\t" \
    "buffer_load_dwordx4 v[152:155], %[o2], %[rs], %[t] offen" WNT "\n\t" \
    "buffer_load_dwordx4 v[168:171], %[o3], %[rs], %[t] offen" WNT "\n\t" \
    "buffer_load_dwordx4 v[192:195], %[o4], %[rs], %[t] offen" WNT "\n\t" \
    "s_waitcnt lgkmcnt(0)\n\t" \
    "s_barrier \n\t" \
    "s_waitcnt vmcnt(15)\n\t" \
    "ds_write_b128 %[s0], v[4:7] offset:0\n\t" \
    "ds_write_b128 %[s1], v[28:31] offset:0\n\t" \
    "ds_write_b128 %[s2], v[44:47] offset:0\n\t" \
    "ds_write_b128 %[s3], v[68:71] offset:0\n\t" \
    "ds_write_b128 %[s4], v[84:87] offset:0\n\t" \
    "s_waitcnt vmcnt(10)\n\t" \
    "ds_write_b128 %[s0], v[108:111] offset:5632\n\t" \
    "ds_write_b128 %[s1], v[124:127] offset:5632\n\t" \
    "ds_write_b128 %[s2], v[148:151] offset:5632\n\t" \
    "ds_write_b128 %[s3], v[164:167] offset:5632\n\t" \
    "ds_write_b128 %[s4], v[188:191] offset:5632\n\t" \
    "ds_read_b64_tr_b16 v[4:5], %[tr] offset:0\n\t" \
    "ds_read_b64_tr_b16 v[6:7], %[tr] offset:5632\n\t" \
    "ds_read_b64_tr_b16 v[28:29], %[tr] offset:32\n\t" \
    "ds_read_b64_tr_b16 v[30:31], %[tr] offset:5664\n\t" \
    "ds_read_b64_tr_b16 v[44:45], %[tr] offset:64\n\t" \
    "ds_read_b64_tr_b16 v[46:47], %[tr] offset:5696\n\t" \
    "ds_read_b64_tr_b16 v[68:69], %[tr] offset:96\n\t" \
    "ds_read_b64_tr_b16 v[70:71], %[tr] offset:5728\n\t" \
    "ds_read_b64_tr_b16 v[84:85], %[tr] offset:128\n\t" \
    "ds_read_b64_tr_b16 v[86:87], %[tr] offset:5760\n\t" \
    "ds_read_b64_tr_b16 v[108:109], %[tr] offset:160\n\t" \
    "ds_read_b64_tr_b16 v[110:111], %[tr] offset:5792\n\t" \
    "ds_read_b64_tr_b16 v[124:125], %[tr] offset:192\n\t" \
    "ds_read_b64_tr_b16 v[126:127], %[tr] offset:5824\n\t" \
    "ds_read_b64_tr_b16 v[148:149], %[tr] offset:224\n\t" \
    "ds_read_b64_tr_b16 v[150:151], %[tr] offset:5856\n\t" \
    "ds_read_b64_tr_b16 v[164:165], %[tr] offset:256\n\t" \
    "ds_read_b64_tr_b16 v[166:167], %[tr] offset:5888\n\t" \
    "ds_read_b64_tr_b16 v[188:189], %[tr] offset:288\n\t" \
    "ds_read_b64_tr_b16 v[190:191], %[tr] offset:5920\n\t" \
    "s_barrier \n\t" \
    "s_mov_b32 %[t], 0x2d000\n\t" \
    "buffer_load_dwordx4 v[12:15], %[o0], %[rs], %[t] offen" WNT "\n\t" \
    "buffer_load_dwordx4 v[36:39], %[o1], %[rs], %[t] offen" WNT "\n\t" \
    "buffer_load_dwordx4 v[52:55], %[o2], %[rs], %[t] offen" WNT "\n\t" \
    "buffer_load_dwordx4 v[76:79], %[o3], %[rs], %[t] offen" WNT "\n\t" \
    "buffer_load_dwordx4 v[92:95], %[o4], %[rs], %[t] offen" WNT "\n\t" \
    "s_mov_b32 %[t], 0x37000\n\t" \
    "buffer_load_dwordx4 v[116:119], %[o0], %[rs], %[t] offen" WNT "\n\t" \
    "buffer_load_dwordx4 v[132:135], %[o1], %[rs], %[t] offen" WNT "\n\t" \
    "buffer_load_dwordx4 v[156:159], %[o2], %[rs], %[t] offen" WNT "\n\t" \
    "buffer_load_dwordx4 v[172:175], %[o3], %[rs], %[t] offen" WNT "\n\t" \
    "buffer_load_dwordx4 v[196:199], %[o4], %[rs], %[t] offen" WNT "\n\t" \
    "s_waitcnt lgkmcnt(0)\n\t" \
    "s_barrier \n\t" \
    "s_waitcnt vmcnt(15)\n\t" \
    "ds_write_b128 %[s0], v[8:11] offset:0\n\t" \
    "ds_write_b128 %[s1], v[32:35] offset:0\n\t" \
    "ds_write_b128 %[s2], v[48:51] offset:0\n\t" \
    "ds_write_b128 %[s3], v[72:75] offset:0\n\t" \
    "ds_write_b128 %[s4], v[88:91] offset:0\n\t" \
    "s_waitcnt vmcnt(10)\n\t" \
    "ds_write_b128 %[s0], v[112:115] offset:5632\n\t" \
    "ds_write_b128 %[s1], v[128:131] offset:5632\n\t" \
    "ds_write_b128 %[s2], v[152:155] offset:5632\n\t" \
    "ds_write_b128 %[s3], v[168:171] offset:5632\n\t" \
    "ds_write_b128 %[s4], v[192:195] offset:5632\n\t" \
    "ds_read_b64_tr_b16 v[8:9], %[tr] offset:0\n\t" \
    "ds_read_b64_tr_b16 v[10:11], %[tr] offset:5632\n\t" \
    "ds_read_b64_tr_b16 v[32:33], %[tr] offset:32\n\t" \
    "ds_read_b64_tr_b16 v[34:35], %[tr] offset:5664\n\t" \
    "ds_read_b64_tr_b16 v[48:49], %[tr] offset:64\n\t" \
    "ds_read_b64_tr_b16 v[50:51], %[tr] offset:5696\n\t" \
    "ds_read_b64_tr_b16 v[72:73], %[tr] offset:96\n\t" \
    "ds_read_b64_tr_b16 v[74:75], %[tr] offset:5728\n\t" \
    "ds_read_b64_tr_b16 v[88:89], %[tr] offset:128\n\t" \
    "ds_read_b64_tr_b16 v[90:91], %[tr] offset:5760\n\t" \
    "ds_read_b64_tr_b16 v[112:113], %[tr] offset:160\n\t" \
    "ds_read_b64_tr_b16 v[114:115], %[tr] offset:5792\n\t" \
    "ds_read_b64_tr_b16 v[128:129], %[tr] offset:192\n\t" \
    "ds_read_b64_tr_b16 v[130:131], %[tr] offset:5824\n\t" \
    "ds_read_b64_tr_b16 v[152:153], %[tr] offset:224\n\t" \
    "ds_read_b64_tr_b16 v[154:155], %[tr] offset:5856\n\t" \
    "ds_read_b64_tr_b16 v[168:169], %[tr] offset:256\n\t" \
    "ds_read_b64_tr_b16 v[170:171], %[tr] offset:5888\n\t" \
    "ds_read_b64_tr_b16 v[192:193], %[tr] offset:288\n\t" \
    "ds_read_b64_tr_b16 v[194:195], %[tr] offset:5920\n\t" \
    "s_waitcnt lgkmcnt(0)\n\t" \
    "s_barrier \n\t" \
    "s_waitcnt vmcnt(5)\n\t" \
    "ds_write_b128 %[s0], v[12:15] offset:0\n\t" \
    "ds_write_b128 %[s1], v[36:39] offset:0\n\t" \
    "ds_write_b128 %[s2], v[52:55] offset:0\n\t" \
    "ds_write_b128 %[s3], v[76:79] offset:0\n\t" \
    "ds_write_b128 %[s4], v[92:95] offset:0\n\t" \
    "s_waitcnt vmcnt(0)\n\t" \
    "ds_write_b128 %[s0], v[116:119] offset:5632\n\t" \
    "ds_write_b128 %[s1], v[132:135] offset:5632\n\t" \
    "ds_write_b128 %[s2], v[156:159] offset:5632\n\t" \
    "ds_write_b128 %[s3], v[172:175] offset:5632\n\t" \
    "ds_write_b128 %[s4], v[196:199] offset:5632\n\t" \
    "ds_read_b64_tr_b16 v[12:13], %[tr] offset:0\n\t" \
    "ds_read_b64_tr_b16 v[14:15], %[tr] offset:5632\n\t" \
    "ds_read_b64_tr_b16 v[36:37], %[tr] offset:32\n\t" \
    "ds_read_b64_tr_b16 v[38:39], %[tr] offset:5664\n\t" \
    "ds_read_b64_tr_b16 v[52:53], %[tr] offset:64\n\t" \
    "ds_read_b64_tr_b16 v[54:55], %[tr] offset:5696\n\t" \
    "ds_read_b64_tr_b16 v[76:77], %[tr] offset:96\n\t" \
    "ds_read_b64_tr_b16 v[78:79], %[tr] offset:5728\n\t" \
    "ds_read_b64_tr_b16 v[92:93], %[tr] offset:128\n\t" \
    "ds_read_b64_tr_b16 v[94:95], %[tr] offset:5760\n\t" \
    "ds_read_b64_tr_b16 v[116:117], %[tr] offset:160\n\t" \
    "ds_read_b64_tr_b16 v[118:119], %[tr] offset:5792\n\t" \
    "ds_read_b64_tr_b16 v[132:133], %[tr] offset:192\n\t" \
    "ds_read_b64_tr_b16 v[134:135], %[tr] offset:5824\n\t" \
    "ds_read_b64_tr_b16 v[156:157], %[tr] offset:224\n\t" \
    "ds_read_b64_tr_b16 v[158:159], %[tr] offset:5856\n\t" \
    "ds_read_b64_tr_b16 v[172:173], %[tr] offset:256\n\t" \
    "ds_read_b64_tr_b16 v[174:175], %[tr] offset:5888\n\t" \
    "ds_read_b64_tr_b16 v[196:197], %[tr] offset:288\n\t" \
    "ds_read_b64_tr_b16 v[198:199], %[tr] offset:5920\n\t" \
    "s_waitcnt lgkmcnt(0)\n\t" \
    "s_barrier \n\t" \
    "ds_write_b128 %[wl], v[200:203] offset:0\n\t" \
    "ds_write_b128 %[wl], v[204:207] offset:1024\n\t" \
    "ds_write_b128 %[wl], v[208:211] offset:2048\n\t" \
    "ds_write_b128 %[wl], v[212:215] offset:3072\n\t" \
    "ds_write_b128 %[wl], v[216:219] offset:4096\n\t" \
    "ds_write_b128 %[wl], v[220:223] offset:5120\n\t" \
    "ds_write_b128 %[wl], v[224:227] offset:6144\n\t" \
    "ds_write_b128 %[wl], v[228:231] offset:7168\n\t" \
    "ds_write_b128 %[wl], v[232:235] offset:8192\n\t" \
    "ds_write_b128 %[wl], v[236:239] offset:9216\n\t" \
    "s_waitcnt lgkmcnt(0)\n\t"
#define ASM_XG_S_LO \
    "s_waitcnt lgkmcnt(0)\n\t" \
    "ds_read_b128 v[224:227], %[xa] offset:16384\n\t" \
    "ds_read_b128 v[228:231], %[xa] offset:17408\n\t" \
    "ds_read_b128 v[244:247], %[wl] offset:0\n\t" \
    "ds_read_b128 v[248:251], %[wl] offset:1024\n\t" \
    "ds_read_b128 v[232:235], %[xa] offset:0\n\t" \
    "ds_read_b128 v[236:239], %[xa] offset:1024\n\t" \
    "v_xor_b32 v252, 0x800, %[xa]\n\t" \
    "ds_read_b128 v[240:243], v252 offset:0\n\t" \
    "s_waitcnt lgkmcnt(6)\n\t" \
    "v_mfma_f32_16x16x32_bf16 v[220:223], v[8:11], v[224:227], 0\n\t" \
    "s_waitcnt lgkmcnt(5)\n\t" \
    "v_mfma_f32_16x16x32_bf16 v[220:223], v[12:15], v[228:231], v[220:223]\n\t" \
    "v_mfma_f32_16x16x32_bf16 v[216:219], v[0:3], v[224:227], 0\n\t" \
    "v_mfma_f32_16x16x32_bf16 v[216:219], v[4:7], v[228:231], v[216:219]\n\t" \
    "s_waitcnt lgkmcnt(4)\n\t" \
    "v_mfma_f32_16x16x32_bf16 v[212:215], v[244:247], v[224:227], 0\n\t" \
    "ds_read_b128 v[224:227], v252 offset:1024\n\t" \
    "s_waitcnt lgkmcnt(4)\n\t" \
    "v_mfma_f32_16x16x32_bf16 v[212:215], v[248:251], v[228:231], v[212:215]\n\t" \
    "ds_read_b128 v[228:231], v252 offset:16384\n\t" \
    "s_waitcnt lgkmcnt(4)\n\t" \
    "v_mfma_f32_16x16x32_bf16 v[200:203], v[244:247], v[232:235], 0\n\t" \
    "ds_read_b128 v[244:247], %[wl] offset:2048\n\t" \
    "s_waitcnt lgkmcnt(4)\n\t" \
    "v_mfma_f32_16x16x32_bf16 v[200:203], v[248:251], v[236:239], v[200:203]\n\t" \
    "ds_read_b128 v[248:251], %[wl] offset:3072\n\t" \
    "v_mfma_f32_16x16x32_bf16 v[204:207], v[0:3], v[232:235], 0\n\t" \
    "v_mfma_f32_16x16x32_bf16 v[204:207], v[4:7], v[236:239], v[204:207]\n\t" \
    "v_mfma_f32_16x16x32_bf16 v[208:211], v[8:11], v[232:235], 0\n\t" \
    "ds_read_b128 v[232:235], v252 offset:17408\n\t" \
    "v_mfma_f32_16x16x32_bf16 v[208:211], v[12:15], v[236:239], v[208:211]\n\t" \
    "v_xor_b32 v252, 0x1000, %[xa]\n\t" \
    "ds_read_b128 v[236:239], v252 offset:16384\n\t" \
    "s_waitcnt lgkmcnt(6)\n\t" \
    "v_mfma_f32_16x16x32_bf16 v[208:211], v[32:35], v[240:243], v[208:211]\n\t" \
    "s_waitcnt lgkmcnt(5)\n\t" \
    "v_mfma_f32_16x16x32_bf16 v[208:211], v[36:39], v[224:227], v[208:211]\n\t" \
    "v_mfma_f32_16x16x32_bf16 v[204:207], v[24:27], v[240:243], v[204:207]\n\t" \
    "v_mfma_f32_16x16x32_bf16 v[204:207], v[28:31], v[224:227], v[204:207]\n\t" \
    "v_mfma_f32_16x16x32_bf16 v[200:203], v[16:19], v[240:243], v[200:203]\n\t" \
    "ds_read_b128 v[240:243], v252 offset:17408\n\t" \
    "v_mfma_f32_16x16x32_bf16 v[200:203], v[20:23], v[224:227], v[200:203]\n\t" \
    "ds_read_b128 v[224:227], v252 offset:0\n\t" \
    "s_waitcnt lgkmcnt(6)\n\t" \
    "v_mfma_f32_16x16x32_bf16 v[212:215], v[16:19], v[228:231], v[212:215]\n\t" \
    "s_waitcnt lgkmcnt(3)\n\t" \
    "v_mfma_f32_16x16x32_bf16 v[212:215], v[20:23], v[232:235], v[212:215]\n\t" \
    "v_mfma_f32_16x16x32_bf16 v[216:219], v[24:27], v[228:231], v[216:219]\n\t" \
    "v_mfma_f32_16x16x32_bf16 v[216:219], v[28:31], v[232:235], v[216:219]\n\t" \
    "v_mfma_f32_16x16x32_bf16 v[220:223], v[32:35], v[228:231], v[220:223]\n\t" \
    "ds_read_b128 v[228:231], v252 offset:1024\n\t" \
    "v_mfma_f32_16x16x32_bf16 v[220:223], v[36:39], v[232:235], v[220:223]\n\t" \
    "v_xor_b32 v252, 0x1800, %[xa]\n\t" \
    "ds_read_b128 v[232:235], v252 offset:0\n\t" \
    "s_waitcnt lgkmcnt(4)\n\t" \
    "v_mfma_f32_16x16x32_bf16 v[220:223], v[48:51], v[236:239], v[220:223]\n\t" \
    "s_waitcnt lgkmcnt(3)\n\t" \
    "v_mfma_f32_16x16x32_bf16 v[220:223], v[52:55], v[240:243], v[220:223]\n\t" \
    "v_mfma_f32_16x16x32_bf16 v[216:219], v[40:43], v[236:239], v[216:219]\n\t" \
    "v_mfma_f32_16x16x32_bf16 v[216:219], v[44:47], v[240:243], v[216:219]\n\t" \
    "v_mfma_f32_16x16x32_bf16 v[212:215], v[244:247], v[236:239], v[212:215]\n\t" \
    "ds_read_b128 v[236:239], v252 offset:1024\n\t" \
    "v_mfma_f32_16x16x32_bf16 v[212:215], v[248:251], v[240:243], v[212:215]\n\t" \
    "ds_read_b128 v[240:243], v252 offset:16384\n\t" \
    "s_waitcnt lgkmcnt(4)\n\t" \
    "v_mfma_f32_16x16x32_bf16 v[200:203], v[244:247], v[224:227], v[200:203]\n\t" \
    "ds_read_b128 v[244:247], %[wl] offset:8192\n\t" \
    "s_waitcnt lgkmcnt(4)\n\t" \
    "v_mfma_f32_16x16x32_bf16 v[200:203], v[248:251], v[228:231], v[200:203]\n\t" \
    "ds_read_b128 v[248:251], %[wl] offset:9216\n\t" \
    "v_mfma_f32_16x16x32_bf16 v[204:207], v[40:43], v[224:227], v[204:207]\n\t" \
    "v_mfma_f32_16x16x32_bf16 v[204:207], v[44:47], v[228:231], v[204:207]\n\t" \
    "v_mfma_f32_16x16x32_bf16 v[208:211], v[48:51], v[224:227], v[208:211]\n\t" \
    "ds_read_b128 v[224:227], v252 offset:17408\n\t" \
    "v_mfma_f32_16x16x32_bf16 v[208:211], v[52:55], v[228:231], v[208:211]\n\t" \
    "v_and_b32 v252, 0x3ff, %[xa]\n\t" \
    "ds_read_b128 v[228:231], v252 offset:45056\n\t" \
    "s_waitcnt lgkmcnt(6)\n\t" \
    "v_mfma_f32_16x16x32_bf16 v[208:211], v[72:75], v[232:235], v[208:211]\n\t" \
    "s_waitcnt lgkmcnt(5)\n\t" \
    "v_mfma_f32_16x16x32_bf16 v[208:211], v[76:79], v[236:239], v[208:211]\n\t" \
    "v_mfma_f32_16x16x32_bf16 v[204:207], v[64:67], v[232:235], v[204:207]\n\t" \
    "v_mfma_f32_16x16x32_bf16 v[204:207], v[68:71], v[236:239], v[204:207]\n\t" \
    "v_mfma_f32_16x16x32_bf16 v[200:203], v[56:59], v[232:235], v[200:203]\n\t" \
    "ds_read_b128 v[232:235], v252 offset:46080\n\t" \
    "v_mfma_f32_16x16x32_bf16 v[200:203], v[60:63], v[236:239], v[200:203]\n\t" \
    "ds_read_b128 v[236:239], v252 offset:40960\n\t" \
    "s_waitcnt lgkmcnt(6)\n\t" \
    "v_mfma_f32_16x16x32_bf16 v[212:215], v[56:59], v[240:243], v[212:215]\n\t" \
    "s_waitcnt lgkmcnt(3)\n\t" \
    "v_mfma_f32_16x16x32_bf16 v[212:215], v[60:63], v[224:227], v[212:215]\n\t" \
    "v_mfma_f32_16x16x32_bf16 v[216:219], v[64:67], v[240:243], v[216:219]\n\t" \
    "v_mfma_f32_16x16x32_bf16 v[216:219], v[68:71], v[224:227], v[216:219]\n\t" \
    "v_mfma_f32_16x16x32_bf16 v[220:223], v[72:75], v[240:243], v[220:223]\n\t" \
    "ds_read_b128 v[240:243], v252 offset:41984\n\t" \
    "v_mfma_f32_16x16x32_bf16 v[220:223], v[76:79], v[224:227], v[220:223]\n\t" \
    "s_waitcnt lgkmcnt(0)\n\t" \
    "s_barrier\n\t" ASM_ST \
    "v_xor_b32 v252, 0x2000, %[xa]\n\t" \
    "ds_read_b128 v[224:227], v252 offset:0\n\t" \
    "v_mfma_f32_16x16x32_bf16 v[220:223], v[168:171], v[228:231], v[220:223]\n\t" \
    "v_mfma_f32_16x16x32_bf16 v[220:223], v[172:175], v[232:235], v[220:223]\n\t" \
    "v_mfma_f32_16x16x32_bf16 v[216:219], v[160:163], v[228:231], v[216:219]\n\t" \
    "v_mfma_f32_16x16x32_bf16 v[216:219], v[164:167], v[232:235], v[216:219]\n\t" \
    "v_mfma_f32_16x16x32_bf16 v[212:215], v[244:247], v[228:231], v[212:215]\n\t" \
    "ds_read_b128 v[228:231], v252 offset:1024\n\t" \
    "v_mfma_f32_16x16x32_bf16 v[212:215], v[248:251], v[232:235], v[212:215]\n\t" \
    "ds_read_b128 v[232:235], v252 offset:16384\n\t" \
    "v_mfma_f32_16x16x32_bf16 v[200:203], v[244:247], v[236:239], v[200:203]\n\t" \
    "ds_read_b128 v[244:247], %[wl] offset:4096\n\t" \
    "v_mfma_f32_16x16x32_bf16 v[200:203], v[248:251], v[240:243], v[200:203]\n\t" \
    "ds_read_b128 v[248:251], %[wl] offset:5120\n\t" \
    "v_mfma_f32_16x16x32_bf16 v[204:207], v[160:163], v[236:239], v[204:207]\n\t" \
    "v_mfma_f32_16x16x32_bf16 v[204:207], v[164:167], v[240:243], v[204:207]\n\t" \
    "v_mfma_f32_16x16x32_bf16 v[208:211], v[168:171], v[236:239], v[208:211]\n\t" \
    "ds_read_b128 v[236:239], v252 offset:17408\n\t" \
    "v_mfma_f32_16x16x32_bf16 v[208:211], v[172:175], v[240:243], v[208:211]\n\t" \
    "v_xor_b32 v252, 0x2800, %[xa]\n\t" \
    "ds_read_b128 v[240:243], v252 offset:16384\n\t" \
    "s_waitcnt lgkmcnt(6)\n\t" \
    "v_mfma_f32_16x16x32_bf16 v[208:211], v[88:91], v[224:227], v[208:211]\n\t" \
    "s_waitcnt lgkmcnt(5)\n\t" \
    "v_mfma_f32_16x16x32_bf16 v[208:211], v[92:95], v[228:231], v[208:211]\n\t" \
    "v_mfma_f32_16x16x32_bf16 v[204:207], v[80:83], v[224:227], v[204:207]\n\t" \
    "v_mfma_f32_16x16x32_bf16 v[204:207], v[84:87], v[228:231], v[204:207]\n\t" \
    "s_waitcnt lgkmcnt(3)\n\t" \
    "v_mfma_f32_16x16x32_bf16 v[200:203], v[244:247], v[224:227], v[200:203]\n\t" \
    "ds_read_b128 v[224:227], v252 offset:17408\n\t" \
    "s_waitcnt lgkmcnt(3)\n\t" \
    "v_mfma_f32_16x16x32_bf16 v[200:203], v[248:251], v[228:231], v[200:203]\n\t" \
    "ds_read_b128 v[228:231], v252 offset:0\n\t" \
    "v_mfma_f32_16x16x32_bf16 v[212:215], v[244:247], v[232:235], v[212:215]\n\t" \
    "ds_read_b128 v[244:247], %[wl] offset:6144\n\t" \
    "s_waitcnt lgkmcnt(4)\n\t" \
    "v_mfma_f32_16x16x32_bf16 v[212:215], v[248:251], v[236:239], v[212:215]\n\t" \
    "ds_read_b128 v[248:251], %[wl] offset:7168\n\t" \
    "v_mfma_f32_16x16x32_bf16 v[216:219], v[80:83], v[232:235], v[216:219]\n\t" \
    "v_mfma_f32_16x16x32_bf16 v[216:219], v[84:87], v[236:239], v[216:219]\n\t" \
    "v_mfma_f32_16x16x32_bf16 v[220:223], v[88:91], v[232:235], v[220:223]\n\t" \
    "ds_read_b128 v[232:235], v252 offset:1024\n\t" \
    "v_mfma_f32_16x16x32_bf16 v[220:223], v[92:95], v[236:239], v[220:223]\n\t" \
    "v_xor_b32 v252, 0x3000, %[xa]\n\t" \
    "ds_read_b128 v[236:239], v252 offset:0\n\t" \
    "s_waitcnt lgkmcnt(6)\n\t" \
    "v_mfma_f32_16x16x32_bf16 v[220:223], v[112:115], v[240:243], v[220:223]\n\t" \
    "s_waitcnt lgkmcnt(5)\n\t" \
    "v_mfma_f32_16x16x32_bf16 v[220:223], v[116:119], v[224:227], v[220:223]\n\t" \
    "v_mfma_f32_16x16x32_bf16 v[216:219], v[104:107], v[240:243], v[216:219]\n\t" \
    "v_mfma_f32_16x16x32_bf16 v[216:219], v[108:111], v[224:227], v[216:219]\n\t" \
    "v_mfma_f32_16x16x32_bf16 v[212:215], v[96:99], v[240:243], v[212:215]\n\t" \
    "ds_read_b128 v[240:243], v252 offset:1024\n\t" \
    "v_mfma_f32_16x16x32_bf16 v[212:215], v[100:103], v[224:227], v[212:215]\n\t" \
    "ds_read_b128 v[224:227], v252 offset:16384\n\t" \
    "s_waitcnt lgkmcnt(6)\n\t" \
    "v_mfma_f32_16x16x32_bf16 v[200:203], v[96:99], v[228:231], v[200:203]\n\t" \
    "s_waitcnt lgkmcnt(3)\n\t" \
    "v_mfma_f32_16x16x32_bf16 v[200:203], v[100:103], v[232:235], v[200:203]\n\t" \
    "v_mfma_f32_16x16x32_bf16 v[204:207], v[104:107], v[228:231], v[204:207]\n\t" \
    "v_mfma_f32_16x16x32_bf16 v[204:207], v[108:111], v[232:235], v[204:207]\n\t" \
    "v_mfma_f32_16x16x32_bf16 v[208:211], v[112:115], v[228:231], v[208:211]\n\t" \
    "ds_read_b128 v[228:231], v252 offset:17408\n\t" \
    "v_mfma_f32_16x16x32_bf16 v[208:211], v[116:119], v[232:235], v[208:211]\n\t" \
    "v_xor_b32 v252, 0x3800, %[xa]\n\t" \
    "ds_read_b128 v[232:235], v252 offset:16384\n\t" \
    "s_waitcnt lgkmcnt(4)\n\t" \
    "v_mfma_f32_16x16x32_bf16 v[208:211], v[128:131], v[236:239], v[208:211]\n\t" \
    "s_waitcnt lgkmcnt(3)\n\t" \
    "v_mfma_f32_16x16x32_bf16 v[208:211], v[132:135], v[240:243], v[208:211]\n\t" \
    "v_mfma_f32_16x16x32_bf16 v[204:207], v[120:123], v[236:239], v[204:207]\n\t" \
    "v_mfma_f32_16x16x32_bf16 v[204:207], v[124:127], v[240:243], v[204:207]\n\t" \
    "v_mfma_f32_16x16x32_bf16 v[200:203], v[244:247], v[236:239], v[200:203]\n\t" \
    "ds_read_b128 v[236:239], v252 offset:17408\n\t" \
    "v_mfma_f32_16x16x32_bf16 v[200:203], v[248:251], v[240:243], v[200:203]\n\t" \
    "ds_read_b128 v[240:243], v252 offset:0\n\t" \
    "s_waitcnt lgkmcnt(4)\n\t" \
    "v_mfma_f32_16x16x32_bf16 v[212:215], v[244:247], v[224:227], v[212:215]\n\t" \
    "s_waitcnt lgkmcnt(3)\n\t" \
    "v_mfma_f32_16x16x32_bf16 v[212:215], v[248:251], v[228:231], v[212:215]\n\t" \
    "v_mfma_f32_16x16x32_bf16 v[216:219], v[120:123], v[224:227], v[216:219]\n\t" \
    "v_mfma_f32_16x16x32_bf16 v[216:219], v[124:127], v[228:231], v[216:219]\n\t" \
    "v_mfma_f32_16x16x32_bf16 v[220:223], v[128:131], v[224:227], v[220:223]\n\t" \
    "ds_read_b128 v[224:227], v252 offset:1024\n\t" \
    "v_mfma_f32_16x16x32_bf16 v[220:223], v[132:135], v[228:231], v[220:223]\n\t" \
    "v_and_b32 v252, 0x3ff, %[xa]\n\t" \
    "ds_read_b128 v[228:231], v252 offset:43008\n\t" \
    "s_waitcnt lgkmcnt(4)\n\t" \
    "v_mfma_f32_16x16x32_bf16 v[220:223], v[152:155], v[232:235], v[220:223]\n\t" \
    "v_mov_b64 v[244:245], 0\n\t" \
    "s_waitcnt lgkmcnt(3)\n\t" \
    "v_mfma_f32_16x16x32_bf16 v[220:223], v[156:159], v[236:239], v[220:223]\n\t" \
    "v_mov_b64 v[246:247], 0\n\t" \
    "v_mfma_f32_16x16x32_bf16 v[216:219], v[144:147], v[232:235], v[216:219]\n\t" \
    "v_mov_b64 v[248:249], 0\n\t" \
    "v_mfma_f32_16x16x32_bf16 v[216:219], v[148:151], v[236:239], v[216:219]\n\t" \
    "v_mov_b64 v[250:251], 0\n\t" \
    "v_mfma_f32_16x16x32_bf16 v[212:215], v[136:139], v[232:235], v[212:215]\n\t" \
    "ds_read_b128 v[232:235], v252 offset:44032\n\t" \
    "v_mfma_f32_16x16x32_bf16 v[212:215], v[140:143], v[236:239], v[212:215]\n\t" \
    "ds_read_b128 v[236:239], v252 offset:47104\n\t" \
    "s_waitcnt lgkmcnt(4)\n\t" \
    "v_mfma_f32_16x16x32_bf16 v[200:203], v[136:139], v[240:243], v[200:203]\n\t" \
    "s_waitcnt lgkmcnt(3)\n\t" \
    "v_mfma_f32_16x16x32_bf16 v[200:203], v[140:143], v[224:227], v[200:203]\n\t" \
    "v_mfma_f32_16x16x32_bf16 v[204:207], v[144:147], v[240:243], v[204:207]\n\t" \
    "v_mfma_f32_16x16x32_bf16 v[204:207], v[148:151], v[224:227], v[204:207]\n\t" \
    "v_mfma_f32_16x16x32_bf16 v[208:211], v[152:155], v[240:243], v[208:211]\n\t" \
    "ds_read_b128 v[240:243], v252 offset:48128\n\t" \
    "v_mfma_f32_16x16x32_bf16 v[208:211], v[156:159], v[224:227], v[208:211]\n\t" \
    "s_waitcnt lgkmcnt(3)\n\t" \
    "v_mfma_f32_16x16x32_bf16 v[208:211], v[192:195], v[228:231], v[208:211]\n\t" \
    "v_add_u32 v252, %[qs], v252\n\t" \
    "v_mov_b64 v[224:225], 0\n\t" \
    "s_waitcnt lgkmcnt(2)\n\t" \
    "v_mfma_f32_16x16x32_bf16 v[208:211], v[196:199], v[232:235], v[208:211]\n\t" \
    "v_mov_b64 v[226:227], 0\n\t" \
    "v_mfma_f32_16x16x32_bf16 v[204:207], v[184:187], v[228:231], v[204:207]\n\t" \
    "v_mfma_f32_16x16x32_bf16 v[204:207], v[188:191], v[232:235], v[204:207]\n\t" \
    "v_mfma_f32_16x16x32_bf16 v[200:203], v[176:179], v[228:231], v[200:203]\n\t" \
    "v_mfma_f32_16x16x32_bf16 v[200:203], v[180:183], v[232:235], v[200:203]\n\t" \
    "s_waitcnt lgkmcnt(1)\n\t" \
    "v_mfma_f32_16x16x32_bf16 v[212:215], v[176:179], v[236:239], v[212:215]\n\t" \
    "ds_read_b128 v[228:231], %[wl] offset:2048\n\t" \
    "ds_read_b128 v[232:235], %[wl] offset:3072\n\t" \
    "s_waitcnt lgkmcnt(2)\n\t" \
    "v_mfma_f32_16x16x32_bf16 v[212:215], v[180:183], v[240:243], v[212:215]\n\t" \
    "v_cvt_pk_bf16_f32 v208, v208, v209\n\t" \
    "v_cvt_pk_bf16_f32 v209, v210, v211\n\t" \
    "v_mfma_f32_16x16x32_bf16 v[216:219], v[184:187], v[236:239], v[216:219]\n\t" \
    "ds_write_b64 v252, v[208:209] offset:2048\n\t" \
    "v_cvt_pk_bf16_f32 v204, v204, v205\n\t" \
    "v_mfma_f32_16x16x32_bf16 v[216:219], v[188:191], v[240:243], v[216:219]\n\t" \
    "v_cvt_pk_bf16_f32 v205, v206, v207\n\t" \
    "ds_write_b64 v252, v[204:205] offset:1024\n\t" \
    "v_mfma_f32_16x16x32_bf16 v[220:223], v[192:195], v[236:239], v[220:223]\n\t" \
    "v_cvt_pk_bf16_f32 v200, v200, v201\n\t" \
    "v_cvt_pk_bf16_f32 v201, v202, v203\n\t" \
    "v_mfma_f32_16x16x32_bf16 v[220:223], v[196:199], v[240:243], v[220:223]\n\t" \
    "ds_write_b64 v252, v[200:201] offset:0\n\t" \
    "ds_read_b128 v[200:203], %[wl] offset:0\n\t" \
    "ds_read_b128 v[204:207], %[wl] offset:1024\n\t" \
    "v_cvt_pk_bf16_f32 v212, v212, v213\n\t" \
    "v_cvt_pk_bf16_f32 v213, v214, v215\n\t" \
    "ds_write_b64 v252, v[212:213] offset:6144\n\t" \
    "v_cvt_pk_bf16_f32 v216, v216, v217\n\t" \
    "v_cvt_pk_bf16_f32 v217, v218, v219\n\t" \
    "ds_write_b64 v252, v[216:217] offset:7168\n\t" \
    "v_cvt_pk_bf16_f32 v220, v220, v221\n\t" \
    "v_cvt_pk_bf16_f32 v221, v222, v223\n\t" \
    "ds_write_b64 v252, v[220:221] offset:8192\n\t" \
    "s_waitcnt lgkmcnt(0)\n\t"
#define ASM_XG_S_HI \
    "s_waitcnt lgkmcnt(0)\n\t" \
    "v_xor_b32 v252, 0x2000, %[xa]\n\t" \
    "ds_read_b128 v[224:227], v252 offset:16384\n\t" \
    "ds_read_b128 v[228:231], v252 offset:17408\n\t" \
    "ds_read_b128 v[244:247], %[wl] offset:4096\n\t" \
    "ds_read_b128 v[248:251], %[wl] offset:5120\n\t" \
    "ds_read_b128 v[232:235], v252 offset:0\n\t" \
    "ds_read_b128 v[236:239], v252 offset:1024\n\t" \
    "v_xor_b32 v252, 0x2800, %[xa]\n\t" \
    "ds_read_b128 v[240:243], v252 offset:0\n\t" \
    "s_cmp_eq_u32 %[slp], 0\n\t" \
    "s_cbranch_scc1 .Lxs%=\n\t" \
    "s_sleep 10\n\t" \
    ".Lxs%=:\n\t" \
    "s_waitcnt lgkmcnt(6)\n\t" \
    "v_mfma_f32_16x16x32_bf16 v[220:223], v[88:91], v[224:227], 0\n\t" \
    "s_waitcnt lgkmcnt(5)\n\t" \
    "v_mfma_f32_16x16x32_bf16 v[220:223], v[92:95], v[228:231], v[220:223]\n\t" \
    "v_mfma_f32_16x16x32_bf16 v[216:219], v[80:83], v[224:227], 0\n\t" \
    "v_mfma_f32_16x16x32_bf16 v[216:219], v[84:87], v[228:231], v[216:219]\n\t" \
    "s_waitcnt lgkmcnt(4)\n\t" \
    "v_mfma_f32_16x16x32_bf16 v[212:215], v[244:247], v[224:227], 0\n\t" \
    "ds_read_b128 v[224:227], v252 offset:1024\n\t" \
    "s_waitcnt lgkmcnt(4)\n\t" \
    "v_mfma_f32_16x16x32_bf16 v[212:215], v[248:251], v[228:231], v[212:215]\n\t" \
    "ds_read_b128 v[228:231], v252 offset:16384\n\t" \
    "s_waitcnt lgkmcnt(4)\n\t" \
    "v_mfma_f32_16x16x32_bf16 v[200:203], v[244:247], v[232:235], 0\n\t" \
    "ds_read_b128 v[244:247], %[wl] offset:6144\n\t" \
    "s_waitcnt lgkmcnt(4)\n\t" \
    "v_mfma_f32_16x16x32_bf16 v[200:203], v[248:251], v[236:239], v[200:203]\n\t" \
    "ds_read_b128 v[248:251], %[wl] offset:7168\n\t" \
    "v_mfma_f32_16x16x32_bf16 v[204:207], v[80:83], v[232:235], 0\n\t" \
    "v_mfma_f32_16x16x32_bf16 v[204:207], v[84:87], v[236:239], v[204:207]\n\t" \
    "v_mfma_f32_16x16x32_bf16 v[208:211], v[88:91], v[232:235], 0\n\t" \
    "ds_read_b128 v[232:235], v252 offset:17408\n\t" \
    "v_mfma_f32_16x16x32_bf16 v[208:211], v[92:95], v[236:239], v[208:211]\n\t" \
    "v_xor_b32 v252, 0x3000, %[xa]\n\t" \
    "ds_read_b128 v[236:239], v252 offset:16384\n\t" \
    "s_waitcnt lgkmcnt(6)\n\t" \
    "v_mfma_f32_16x16x32_bf16 v[208:211], v[112:115], v[240:243], v[208:211]\n\t" \
    "s_waitcnt lgkmcnt(5)\n\t" \
    "v_mfma_f32_16x16x32_bf16 v[208:211], v[116:119], v[224:227], v[208:211]\n\t" \
    "v_mfma_f32_16x16x32_bf16 v[204:207], v[104:107], v[240:243], v[204:207]\n\t" \
    "v_mfma_f32_16x16x32_bf16 v[204:207], v[108:111], v[224:227], v[204:207]\n\t" \
    "v_mfma_f32_16x16x32_bf16 v[200:203], v[96:99], v[240:243], v[200:203]\n\t" \
    "ds_read_b128 v[240:243], v252 offset:17408\n\t" \
    "v_mfma_f32_16x16x32_bf16 v[200:203], v[100:103], v[224:227], v[200:203]\n\t" \
    "ds_read_b128 v[224:227], v252 offset:0\n\t" \
    "s_waitcnt lgkmcnt(6)\n\t" \
    "v_mfma_f32_16x16x32_bf16 v[212:215], v[96:99], v[228:231], v[212:215]\n\t" \
    "s_waitcnt lgkmcnt(3)\n\t" \
    "v_mfma_f32_16x16x32_bf16 v[212:215], v[100:103], v[232:235], v[212:215]\n\t" \
    "v_mfma_f32_16x16x32_bf16 v[216:219], v[104:107], v[228:231], v[216:219]\n\t" \
    "v_mfma_f32_16x16x32_bf16 v[216:219], v[108:111], v[232:235], v[216:219]\n\t" \
    "v_mfma_f32_16x16x32_bf16 v[220:223], v[112:115], v[228:231], v[220:223]\n\t" \
    "ds_read_b128 v[228:231], v252 offset:1024\n\t" \
    "v_mfma_f32_16x16x32_bf16 v[220:223], v[116:119], v[232:235], v[220:223]\n\t" \
    "v_xor_b32 v252, 0x3800, %[xa]\n\t" \
    "ds_read_b128 v[232:235], v252 offset:0\n\t" \
    "s_waitcnt lgkmcnt(4)\n\t" \
    "v_mfma_f32_16x16x32_bf16 v[220:223], v[128:131], v[236:239], v[220:223]\n\t" \
    "s_waitcnt lgkmcnt(3)\n\t" \
    "v_mfma_f32_16x16x32_bf16 v[220:223], v[132:135], v[240:243], v[220:223]\n\t" \
    "v_mfma_f32_16x16x32_bf16 v[216:219], v[120:123], v[236:239], v[216:219]\n\t" \
    "v_mfma_f32_16x16x32_bf16 v[216:219], v[124:127], v[240:243], v[216:219]\n\t" \
    "v_mfma_f32_16x16x32_bf16 v[212:215], v[244:247], v[236:239], v[212:215]\n\t" \
    "ds_read_b128 v[236:239], v252 offset:1024\n\t" \
    "v_mfma_f32_16x16x32_bf16 v[212:215], v[248:251], v[240:243], v[212:215]\n\t" \
    "ds_read_b128 v[240:243], v252 offset:16384\n\t" \
    "s_waitcnt lgkmcnt(4)\n\t" \
    "v_mfma_f32_16x16x32_bf16 v[200:203], v[244:247], v[224:227], v[200:203]\n\t" \
    "ds_read_b128 v[244:247], %[wl] offset:8192\n\t" \
    "s_waitcnt lgkmcnt(4)\n\t" \
    "v_mfma_f32_16x16x32_bf16 v[200:203], v[248:251], v[228:231], v[200:203]\n\t" \
    "ds_read_b128 v[248:251], %[wl] offset:9216\n\t" \
    "v_mfma_f32_16x16x32_bf16 v[204:207], v[120:123], v[224:227], v[204:207]\n\t" \
    "v_mfma_f32_16x16x32_bf16 v[204:207], v[124:127], v[228:231], v[204:207]\n\t" \
    "v_mfma_f32_16x16x32_bf16 v[208:211], v[128:131], v[224:227], v[208:211]\n\t" \
    "ds_read_b128 v[224:227], v252 offset:17408\n\t" \
    "v_mfma_f32_16x16x32_bf16 v[208:211], v[132:135], v[228:231], v[208:211]\n\t" \
    "v_and_b32 v252, 0x3ff, %[xa]\n\t" \
    "ds_read_b128 v[228:231], v252 offset:45056\n\t" \
    "s_waitcnt lgkmcnt(6)\n\t" \
    "v_mfma_f32_16x16x32_bf16 v[208:211], v[152:155], v[232:235], v[208:211]\n\t" \
    "s_waitcnt lgkmcnt(5)\n\t" \
    "v_mfma_f32_16x16x32_bf16 v[208:211], v[156:159], v[236:239], v[208:211]\n\t" \
    "v_mfma_f32_16x16x32_bf16 v[204:207], v[144:147], v[232:235], v[204:207]\n\t" \
    "v_mfma_f32_16x16x32_bf16 v[204:207], v[148:151], v[236:239], v[204:207]\n\t" \
    "v_mfma_f32_16x16x32_bf16 v[200:203], v[136:139], v[232:235], v[200:203]\n\t" \
    "ds_read_b128 v[232:235], v252 offset:46080\n\t" \
    "v_mfma_f32_16x16x32_bf16 v[200:203], v[140:143], v[236:239], v[200:203]\n\t" \
    "ds_read_b128 v[236:239], v252 offset:40960\n\t" \
    "s_waitcnt lgkmcnt(6)\n\t" \
    "v_mfma_f32_16x16x32_bf16 v[212:215], v[136:139], v[240:243], v[212:215]\n\t" \
    "s_waitcnt lgkmcnt(3)\n\t" \
    "v_mfma_f32_16x16x32_bf16 v[212:215], v[140:143], v[224:227], v[212:215]\n\t" \
    "v_mfma_f32_16x16x32_bf16 v[216:219], v[144:147], v[240:243], v[216:219]\n\t" \
    "v_mfma_f32_16x16x32_bf16 v[216:219], v[148:151], v[224:227], v[216:219]\n\t" \
    "v_mfma_f32_16x16x32_bf16 v[220:223], v[152:155], v[240:243], v[220:223]\n\t" \
    "ds_read_b128 v[240:243], v252 offset:41984\n\t" \
    "v_mfma_f32_16x16x32_bf16 v[220:223], v[156:159], v[224:227], v[220:223]\n\t" \
    "s_waitcnt vmcnt(0) lgkmcnt(0)\n\t" \
    "s_barrier\n\t" ASM_ST \
    "s_mov_b32 m0, %[d0]\n\t" \
    "s_nop 0\n\t" \
    "global_load_lds_dwordx4 v252, %[g0] offset:0 sc1\n\t" \
    "global_load_lds_dwordx4 v252, %[g0] offset:1024 sc1\n\t" \
    "global_load_lds_dwordx4 v252, %[g0] offset:2048 sc1\n\t" \
    "global_load_lds_dwordx4 v252, %[g0] offset:3072 sc1\n\t" \
    "s_mov_b32 m0, %[d1]\n\t" \
    "s_nop 0\n\t" \
    "global_load_lds_dwordx4 v252, %[g1] sc1\n\t" \
    "ds_read_b128 v[224:227], %[xa] offset:0\n\t" \
    "v_mfma_f32_16x16x32_bf16 v[220:223], v[168:171], v[228:231], v[220:223]\n\t" \
    "v_mfma_f32_16x16x32_bf16 v[220:223], v[172:175], v[232:235], v[220:223]\n\t" \
    "v_mfma_f32_16x16x32_bf16 v[216:219], v[160:163], v[228:231], v[216:219]\n\t" \
    "v_mfma_f32_16x16x32_bf16 v[216:219], v[164:167], v[232:235], v[216:219]\n\t" \
    "v_mfma_f32_16x16x32_bf16 v[212:215], v[244:247], v[228:231], v[212:215]\n\t" \
    "ds_read_b128 v[228:231], %[xa] offset:1024\n\t" \
    "v_mfma_f32_16x16x32_bf16 v[212:215], v[248:251], v[232:235], v[212:215]\n\t" \
    "ds_read_b128 v[232:235], %[xa] offset:16384\n\t" \
    "v_mfma_f32_16x16x32_bf16 v[200:203], v[244:247], v[236:239], v[200:203]\n\t" \
    "ds_read_b128 v[244:247], %[wl] offset:0\n\t" \
    "v_mfma_f32_16x16x32_bf16 v[200:203], v[248:251], v[240:243], v[200:203]\n\t" \
    "ds_read_b128 v[248:251], %[wl] offset:1024\n\t" \
    "v_mfma_f32_16x16x32_bf16 v[204:207], v[160:163], v[236:239], v[204:207]\n\t" \
    "v_mfma_f32_16x16x32_bf16 v[204:207], v[164:167], v[240:243], v[204:207]\n\t" \
    "v_mfma_f32_16x16x32_bf16 v[208:211], v[168:171], v[236:239], v[208:211]\n\t" \
    "ds_read_b128 v[236:239], %[xa] offset:17408\n\t" \
    "v_mfma_f32_16x16x32_bf16 v[208:211], v[172:175], v[240:243], v[208:211]\n\t" \
    "v_xor_b32 v252, 0x800, %[xa]\n\t" \
    "ds_read_b128 v[240:243], v252 offset:16384\n\t" \
    "s_waitcnt lgkmcnt(6)\n\t" \
    "v_mfma_f32_16x16x32_bf16 v[208:211], v[8:11], v[224:227], v[208:211]\n\t" \
    "s_waitcnt lgkmcnt(5)\n\t" \
    "v_mfma_f32_16x16x32_bf16 v[208:211], v[12:15], v[228:231], v[208:211]\n\t" \
    "v_mfma_f32_16x16x32_bf16 v[204:207], v[0:3], v[224:227], v[204:207]\n\t" \
    "v_mfma_f32_16x16x32_bf16 v[204:207], v[4:7], v[228:231], v[204:207]\n\t" \
    "s_waitcnt lgkmcnt(3)\n\t" \
    "v_mfma_f32_16x16x32_bf16 v[200:203], v[244:247], v[224:227], v[200:203]\n\t" \
    "ds_read_b128 v[224:227], v252 offset:17408\n\t" \
    "s_waitcnt lgkmcnt(3)\n\t" \
    "v_mfma_f32_16x16x32_bf16 v[200:203], v[248:251], v[228:231], v[200:203]\n\t" \
    "ds_read_b128 v[228:231], v252 offset:0\n\t" \
    "v_mfma_f32_16x16x32_bf16 v[212:215], v[244:247], v[232:235], v[212:215]\n\t" \
    "ds_read_b128 v[244:247], %[wl] offset:2048\n\t" \
    "s_waitcnt lgkmcnt(4)\n\t" \
    "v_mfma_f32_16x16x32_bf16 v[212:215], v[248:251], v[236:239], v[212:215]\n\t" \
    "ds_read_b128 v[248:251], %[wl] offset:3072\n\t" \
    "v_mfma_f32_16x16x32_bf16 v[216:219], v[0:3], v[232:235], v[216:219]\n\t" \
    "v_mfma_f32_16x16x32_bf16 v[216:219], v[4:7], v[236:239], v[216:219]\n\t" \
    "v_mfma_f32_16x16x32_bf16 v[220:223], v[8:11], v[232:235], v[220:223]\n\t" \
    "ds_read_b128 v[232:235], v252 offset:1024\n\t" \
    "v_mfma_f32_16x16x32_bf16 v[220:223], v[12:15], v[236:239], v[220:223]\n\t" \
    "v_xor_b32 v252, 0x1000, %[xa]\n\t" \
    "ds_read_b128 v[236:239], v252 offset:0\n\t" \
    "s_waitcnt lgkmcnt(6)\n\t" \
    "v_mfma_f32_16x16x32_bf16 v[220:223], v[32:35], v[240:243], v[220:223]\n\t" \
    "s_waitcnt lgkmcnt(5)\n\t" \
    "v_mfma_f32_16x16x32_bf16 v[220:223], v[36:39], v[224:227], v[220:223]\n\t" \
    "v_mfma_f32_16x16x32_bf16 v[216:219], v[24:27], v[240:243], v[216:219]\n\t" \
    "v_mfma_f32_16x16x32_bf16 v[216:219], v[28:31], v[224:227], v[216:219]\n\t" \
    "v_mfma_f32_16x16x32_bf16 v[212:215], v[16:19], v[240:243], v[212:215]\n\t" \
    "ds_read_b128 v[240:243], v252 offset:1024\n\t" \
    "v_mfma_f32_16x16x32_bf16 v[212:215], v[20:23], v[224:227], v[212:215]\n\t" \
    "ds_read_b128 v[224:227], v252 offset:16384\n\t" \
    "s_waitcnt lgkmcnt(6)\n\t" \
    "v_mfma_f32_16x16x32_bf16 v[200:203], v[16:19], v[228:231], v[200:203]\n\t" \
    "s_waitcnt lgkmcnt(3)\n\t" \
    "v_mfma_f32_16x16x32_bf16 v[200:203], v[20:23], v[232:235], v[200:203]\n\t" \
    "v_mfma_f32_16x16x32_bf16 v[204:207], v[24:27], v[228:231], v[204:207]\n\t" \
    "v_mfma_f32_16x16x32_bf16 v[204:207], v[28:31], v[232:235], v[204:207]\n\t" \
    "v_mfma_f32_16x16x32_bf16 v[208:211], v[32:35], v[228:231], v[208:211]\n\t" \
    "ds_read_b128 v[228:231], v252 offset:17408\n\t" \
    "v_mfma_f32_16x16x32_bf16 v[208:211], v[36:39], v[232:235], v[208:211]\n\t" \
    "v_xor_b32 v252, 0x1800, %[xa]\n\t" \
    "ds_read_b128 v[232:235], v252 offset:16384\n\t" \
    "s_waitcnt lgkmcnt(4)\n\t" \
    "v_mfma_f32_16x16x32_bf16 v[208:211], v[48:51], v[236:239], v[208:211]\n\t" \
    "s_waitcnt lgkmcnt(3)\n\t" \
    "v_mfma_f32_16x16x32_bf16 v[208:211], v[52:55], v[240:243], v[208:211]\n\t" \
    "v_mfma_f32_16x16x32_bf16 v[204:207], v[40:43], v[236:239], v[204:207]\n\t" \
    "v_mfma_f32_16x16x32_bf16 v[204:207], v[44:47], v[240:243], v[204:207]\n\t" \
    "v_mfma_f32_16x16x32_bf16 v[200:203], v[244:247], v[236:239], v[200:203]\n\t" \
    "ds_read_b128 v[236:239], v252 offset:17408\n\t" \
    "v_mfma_f32_16x16x32_bf16 v[200:203], v[248:251], v[240:243], v[200:203]\n\t" \
    "ds_read_b128 v[240:243], v252 offset:0\n\t" \
    "s_waitcnt lgkmcnt(4)\n\t" \
    "v_mfma_f32_16x16x32_bf16 v[212:215], v[244:247], v[224:227], v[212:215]\n\t" \
    "s_waitcnt lgkmcnt(3)\n\t" \
    "v_mfma_f32_16x16x32_bf16 v[212:215], v[248:251], v[228:231], v[212:215]\n\t" \
    "v_mfma_f32_16x16x32_bf16 v[216:219], v[40:43], v[224:227], v[216:219]\n\t" \
    "v_mfma_f32_16x16x32_bf16 v[216:219], v[44:47], v[228:231], v[216:219]\n\t" \
    "v_mfma_f32_16x16x32_bf16 v[220:223], v[48:51], v[224:227], v[220:223]\n\t" \
    "ds_read_b128 v[224:227], v252 offset:1024\n\t" \
    "v_mfma_f32_16x16x32_bf16 v[220:223], v[52:55], v[228:231], v[220:223]\n\t" \
    "v_and_b32 v252, 0x3ff, %[xa]\n\t" \
    "ds_read_b128 v[228:231], v252 offset:43008\n\t" \
    "s_waitcnt lgkmcnt(4)\n\t" \
    "v_mfma_f32_16x16x32_bf16 v[220:223], v[72:75], v[232:235], v[220:223]\n\t" \
    "v_mov_b64 v[244:245], 0\n\t" \
    "s_waitcnt lgkmcnt(3)\n\t" \
    "v_mfma_f32_16x16x32_bf16 v[220:223], v[76:79], v[236:239], v[220:223]\n\t" \
    "v_mov_b64 v[246:247], 0\n\t" \
    "v_mfma_f32_16x16x32_bf16 v[216:219], v[64:67], v[232:235], v[216:219]\n\t" \
    "v_mov_b64 v[248:249], 0\n\t" \
    "v_mfma_f32_16x16x32_bf16 v[216:219], v[68:71], v[236:239], v[216:219]\n\t" \
    "v_mov_b64 v[250:251], 0\n\t" \
    "v_mfma_f32_16x16x32_bf16 v[212:215], v[56:59], v[232:235], v[212:215]\n\t" \
    "ds_read_b128 v[232:235], v252 offset:44032\n\t" \
    "v_mfma_f32_16x16x32_bf16 v[212:215], v[60:63], v[236:239], v[212:215]\n\t" \
    "ds_read_b128 v[236:239], v252 offset:47104\n\t" \
    "s_waitcnt lgkmcnt(4)\n\t" \
    "v_mfma_f32_16x16x32_bf16 v[200:203], v[56:59], v[240:243], v[200:203]\n\t" \
    "s_waitcnt lgkmcnt(3)\n\t" \
    "v_mfma_f32_16x16x32_bf16 v[200:203], v[60:63], v[224:227], v[200:203]\n\t" \
    "v_mfma_f32_16x16x32_bf16 v[204:207], v[64:67], v[240:243], v[204:207]\n\t" \
    "v_mfma_f32_16x16x32_bf16 v[204:207], v[68:71], v[224:227], v[204:207]\n\t" \
    "v_mfma_f32_16x16x32_bf16 v[208:211], v[72:75], v[240:243], v[208:211]\n\t" \
    "ds_read_b128 v[240:243], v252 offset:48128\n\t" \
    "v_mfma_f32_16x16x32_bf16 v[208:211], v[76:79], v[224:227], v[208:211]\n\t" \
    "s_waitcnt lgkmcnt(3)\n\t" \
    "v_mfma_f32_16x16x32_bf16 v[208:211], v[192:195], v[228:231], v[208:211]\n\t" \
    "v_add_u32 v252, %[qs], v252\n\t" \
    "v_mov_b64 v[224:225], 0\n\t" \
    "s_waitcnt lgkmcnt(2)\n\t" \
    "v_mfma_f32_16x16x32_bf16 v[208:211], v[196:199], v[232:235], v[208:211]\n\t" \
    "v_mov_b64 v[226:227], 0\n\t" \
    "v_mfma_f32_16x16x32_bf16 v[204:207], v[184:187], v[228:231], v[204:207]\n\t" \
    "v_mfma_f32_16x16x32_bf16 v[204:207], v[188:191], v[232:235], v[204:207]\n\t" \
    "v_mfma_f32_16x16x32_bf16 v[200:203], v[176:179], v[228:231], v[200:203]\n\t" \
    "v_mfma_f32_16x16x32_bf16 v[200:203], v[180:183], v[232:235], v[200:203]\n\t" \
    "s_waitcnt lgkmcnt(1)\n\t" \
    "v_mfma_f32_16x16x32_bf16 v[212:215], v[176:179], v[236:239], v[212:215]\n\t" \
    "ds_read_b128 v[228:231], %[wl] offset:2048\n\t" \
    "ds_read_b128 v[232:235], %[wl] offset:3072\n\t" \
    "s_waitcnt lgkmcnt(2)\n\t" \
    "v_mfma_f32_16x16x32_bf16 v[212:215], v[180:183], v[240:243], v[212:215]\n\t" \
    "v_cvt_pk_bf16_f32 v208, v208, v209\n\t" \
    "v_cvt_pk_bf16_f32 v209, v210, v211\n\t" \
    "v_mfma_f32_16x16x32_bf16 v[216:219], v[184:187], v[236:239], v[216:219]\n\t" \
    "ds_write_b64 v252, v[208:209] offset:2048\n\t" \
    "v_cvt_pk_bf16_f32 v204, v204, v205\n\t" \
    "v_mfma_f32_16x16x32_bf16 v[216:219], v[188:191], v[240:243], v[216:219]\n\t" \
    "v_cvt_pk_bf16_f32 v205, v206, v207\n\t" \
    "ds_write_b64 v252, v[204:205] offset:1024\n\t" \
    "v_mfma_f32_16x16x32_bf16 v[220:223], v[192:195], v[236:239], v[220:223]\n\t" \
    "v_cvt_pk_bf16_f32 v200, v200, v201\n\t" \
    "v_cvt_pk_bf16_f32 v201, v202, v203\n\t" \
    "v_mfma_f32_16x16x32_bf16 v[220:223], v[196:199], v[240:243], v[220:223]\n\t" \
    "ds_write_b64 v252, v[200:201] offset:0\n\t" \
    "ds_read_b128 v[200:203], %[wl] offset:0\n\t" \
    "ds_read_b128 v[204:207], %[wl] offset:1024\n\t" \
    "v_cvt_pk_bf16_f32 v212, v212, v213\n\t" \
    "v_cvt_pk_bf16_f32 v213, v214, v215\n\t" \
    "ds_write_b64 v252, v[212:213] offset:6144\n\t" \
    "v_cvt_pk_bf16_f32 v216, v216, v217\n\t" \
    "v_cvt_pk_bf16_f32 v217, v218, v219\n\t" \
    "ds_write_b64 v252, v[216:217] offset:7168\n\t" \
    "v_cvt_pk_bf16_f32 v220, v220, v221\n\t" \
    "v_cvt_pk_bf16_f32 v221, v222, v223\n\t" \
    "ds_write_b64 v252, v[220:221] offset:8192\n\t" \
    "s_waitcnt lgkmcnt(0)\n\t"
#define ASM_XG_S_LO2 \
    "s_waitcnt lgkmcnt(0)\n\t" \
    "ds_read_b128 v[224:227], %[xa] offset:16384\n\t" \
    "ds_read_b128 v[228:231], %[xa] offset:17408\n\t" \
    "v_xor_b32 v252, 0x800, %[xa]\n\t" \
    "ds_read_b128 v[232:235], v252 offset:16384\n\t" \
    "ds_read_b128 v[236:239], v252 offset:17408\n\t" \
    "v_xor_b32 v252, 0x1000, %[xa]\n\t" \
    "ds_read_b128 v[240:243], v252 offset:16384\n\t" \
    "ds_read_b128 v[212:215], v252 offset:17408\n\t" \
    "v_xor_b32 v252, 0x1800, %[xa]\n\t" \
    "ds_read_b128 v[200:203], v252 offset:16384\n\t" \
    "s_waitcnt lgkmcnt(6)\n\t" \
    "v_mfma_f32_16x16x32_bf16 v[220:223], v[8:11], v[224:227], 0\n\t" \
    "s_waitcnt lgkmcnt(5)\n\t" \
    "v_mfma_f32_16x16x32_bf16 v[220:223], v[12:15], v[228:231], v[220:223]\n\t" \
    "v_mfma_f32_16x16x32_bf16 v[216:219], v[0:3], v[224:227], 0\n\t" \
    "ds_read_b128 v[224:227], v252 offset:17408\n\t" \
    "v_mfma_f32_16x16x32_bf16 v[216:219], v[4:7], v[228:231], v[216:219]\n\t" \
    "v_and_b32 v252, 0x3ff, %[xa]\n\t" \
    "ds_read_b128 v[228:231], v252 offset:45056\n\t" \
    "s_waitcnt lgkmcnt(6)\n\t" \
    "v_mfma_f32_16x16x32_bf16 v[216:219], v[24:27], v[232:235], v[216:219]\n\t" \
    "s_waitcnt lgkmcnt(5)\n\t" \
    "v_mfma_f32_16x16x32_bf16 v[216:219], v[28:31], v[236:239], v[216:219]\n\t" \
    "v_mfma_f32_16x16x32_bf16 v[220:223], v[32:35], v[232:235], v[220:223]\n\t" \
    "ds_read_b128 v[232:235], v252 offset:46080\n\t" \
    "v_mfma_f32_16x16x32_bf16 v[220:223], v[36:39], v[236:239], v[220:223]\n\t" \
    "ds_read_b128 v[236:239], %[xa] offset:0\n\t" \
    "s_waitcnt lgkmcnt(6)\n\t" \
    "v_mfma_f32_16x16x32_bf16 v[220:223], v[48:51], v[240:243], v[220:223]\n\t" \
    "s_waitcnt lgkmcnt(5)\n\t" \
    "v_mfma_f32_16x16x32_bf16 v[220:223], v[52:55], v[212:215], v[220:223]\n\t" \
    "v_mfma_f32_16x16x32_bf16 v[216:219], v[40:43], v[240:243], v[216:219]\n\t" \
    "ds_read_b128 v[240:243], %[xa] offset:1024\n\t" \
    "v_mfma_f32_16x16x32_bf16 v[216:219], v[44:47], v[212:215], v[216:219]\n\t" \
    "v_xor_b32 v252, 0x800, %[xa]\n\t" \
    "ds_read_b128 v[212:215], v252 offset:0\n\t" \
    "s_waitcnt lgkmcnt(6)\n\t" \
    "v_mfma_f32_16x16x32_bf16 v[216:219], v[64:67], v[200:203], v[216:219]\n\t" \
    "s_waitcnt lgkmcnt(5)\n\t" \
    "v_mfma_f32_16x16x32_bf16 v[216:219], v[68:71], v[224:227], v[216:219]\n\t" \
    "v_mfma_f32_16x16x32_bf16 v[220:223], v[72:75], v[200:203], v[220:223]\n\t" \
    "ds_read_b128 v[200:203], v252 offset:1024\n\t" \
    "v_mfma_f32_16x16x32_bf16 v[220:223], v[76:79], v[224:227], v[220:223]\n\t" \
    "v_xor_b32 v252, 0x1000, %[xa]\n\t" \
    "ds_read_b128 v[224:227], v252 offset:0\n\t" \
    "s_waitcnt lgkmcnt(6)\n\t" \
    "v_mfma_f32_16x16x32_bf16 v[220:223], v[168:171], v[228:231], v[220:223]\n\t" \
    "s_waitcnt lgkmcnt(5)\n\t" \
    "v_mfma_f32_16x16x32_bf16 v[220:223], v[172:175], v[232:235], v[220:223]\n\t" \
    "v_mfma_f32_16x16x32_bf16 v[216:219], v[160:163], v[228:231], v[216:219]\n\t" \
    "ds_read_b128 v[228:231], v252 offset:1024\n\t" \
    "v_mfma_f32_16x16x32_bf16 v[216:219], v[164:167], v[232:235], v[216:219]\n\t" \
    "v_xor_b32 v252, 0x1800, %[xa]\n\t" \
    "ds_read_b128 v[232:235], v252 offset:0\n\t" \
    "s_waitcnt lgkmcnt(6)\n\t" \
    "v_mfma_f32_16x16x32_bf16 v[204:207], v[0:3], v[236:239], 0\n\t" \
    "s_waitcnt lgkmcnt(5)\n\t" \
    "v_mfma_f32_16x16x32_bf16 v[204:207], v[4:7], v[240:243], v[204:207]\n\t" \
    "v_mfma_f32_16x16x32_bf16 v[208:211], v[8:11], v[236:239], 0\n\t" \
    "ds_read_b128 v[236:239], v252 offset:1024\n\t" \
    "v_mfma_f32_16x16x32_bf16 v[208:211], v[12:15], v[240:243], v[208:211]\n\t" \
    "v_and_b32 v252, 0x3ff, %[xa]\n\t" \
    "ds_read_b128 v[240:243], v252 offset:40960\n\t" \
    "s_waitcnt lgkmcnt(6)\n\t" \
    "v_mfma_f32_16x16x32_bf16 v[208:211], v[32:35], v[212:215], v[208:211]\n\t" \
    "s_waitcnt lgkmcnt(5)\n\t" \
    "v_mfma_f32_16x16x32_bf16 v[208:211], v[36:39], v[200:203], v[208:211]\n\t" \
    "v_mfma_f32_16x16x32_bf16 v[204:207], v[24:27], v[212:215], v[204:207]\n\t" \
    "ds_read_b128 v[212:215], v252 offset:41984\n\t" \
    "v_mfma_f32_16x16x32_bf16 v[204:207], v[28:31], v[200:203], v[204:207]\n\t" \
    "s_waitcnt lgkmcnt(0)\n\t" \
    "s_barrier\n\t" ASM_ST \
    "v_xor_b32 v252, 0x2000, %[xa]\n\t" \
    "ds_read_b128 v[200:203], v252 offset:0\n\t" \
    "v_mfma_f32_16x16x32_bf16 v[204:207], v[40:43], v[224:227], v[204:207]\n\t" \
    "v_mfma_f32_16x16x32_bf16 v[204:207], v[44:47], v[228:231], v[204:207]\n\t" \
    "v_mfma_f32_16x16x32_bf16 v[208:211], v[48:51], v[224:227], v[208:211]\n\t" \
    "ds_read_b128 v[224:227], v252 offset:1024\n\t" \
    "v_mfma_f32_16x16x32_bf16 v[208:211], v[52:55], v[228:231], v[208:211]\n\t" \
    "v_xor_b32 v252, 0x2800, %[xa]\n\t" \
    "ds_read_b128 v[228:231], v252 offset:0\n\t" \
    "v_mfma_f32_16x16x32_bf16 v[208:211], v[72:75], v[232:235], v[208:211]\n\t" \
    "v_mfma_f32_16x16x32_bf16 v[208:211], v[76:79], v[236:239], v[208:211]\n\t" \
    "v_mfma_f32_16x16x32_bf16 v[204:207], v[64:67], v[232:235], v[204:207]\n\t" \
    "ds_read_b128 v[232:235], v252 offset:1024\n\t" \
    "v_mfma_f32_16x16x32_bf16 v[204:207], v[68:71], v[236:239], v[204:207]\n\t" \
    "v_xor_b32 v252, 0x3000, %[xa]\n\t" \
    "ds_read_b128 v[236:239], v252 offset:0\n\t" \
    "v_mfma_f32_16x16x32_bf16 v[204:207], v[160:163], v[240:243], v[204:207]\n\t" \
    "v_mfma_f32_16x16x32_bf16 v[204:207], v[164:167], v[212:215], v[204:207]\n\t" \
    "v_mfma_f32_16x16x32_bf16 v[208:211], v[168:171], v[240:243], v[208:211]\n\t" \
    "ds_read_b128 v[240:243], v252 offset:1024\n\t" \
    "v_mfma_f32_16x16x32_bf16 v[208:211], v[172:175], v[212:215], v[208:211]\n\t" \
    "v_xor_b32 v252, 0x3800, %[xa]\n\t" \
    "ds_read_b128 v[212:215], v252 offset:0\n\t" \
    "s_waitcnt lgkmcnt(6)\n\t" \
    "v_mfma_f32_16x16x32_bf16 v[208:211], v[88:91], v[200:203], v[208:211]\n\t" \
    "s_waitcnt lgkmcnt(5)\n\t" \
    "v_mfma_f32_16x16x32_bf16 v[208:211], v[92:95], v[224:227], v[208:211]\n\t" \
    "v_mfma_f32_16x16x32_bf16 v[204:207], v[80:83], v[200:203], v[204:207]\n\t" \
    "ds_read_b128 v[200:203], v252 offset:1024\n\t" \
    "v_mfma_f32_16x16x32_bf16 v[204:207], v[84:87], v[224:227], v[204:207]\n\t" \
    "v_and_b32 v252, 0x3ff, %[xa]\n\t" \
    "ds_read_b128 v[224:227], v252 offset:43008\n\t" \
    "s_waitcnt lgkmcnt(6)\n\t" \
    "v_mfma_f32_16x16x32_bf16 v[204:207], v[104:107], v[228:231], v[204:207]\n\t" \
    "s_waitcnt lgkmcnt(5)\n\t" \
    "v_mfma_f32_16x16x32_bf16 v[204:207], v[108:111], v[232:235], v[204:207]\n\t" \
    "v_mfma_f32_16x16x32_bf16 v[208:211], v[112:115], v[228:231], v[208:211]\n\t" \
    "ds_read_b128 v[228:231], v252 offset:44032\n\t" \
    "v_mfma_f32_16x16x32_bf16 v[208:211], v[116:119], v[232:235], v[208:211]\n\t" \
    "v_xor_b32 v252, 0x2000, %[xa]\n\t" \
    "ds_read_b128 v[232:235], v252 offset:16384\n\t" \
    "s_waitcnt lgkmcnt(6)\n\t" \
    "v_mfma_f32_16x16x32_bf16 v[208:211], v[128:131], v[236:239], v[208:211]\n\t" \
    "s_waitcnt lgkmcnt(5)\n\t" \
    "v_mfma_f32_16x16x32_bf16 v[208:211], v[132:135], v[240:243], v[208:211]\n\t" \
    "v_mfma_f32_16x16x32_bf16 v[204:207], v[120:123], v[236:239], v[204:207]\n\t" \
    "ds_read_b128 v[236:239], v252 offset:17408\n\t" \
    "v_mfma_f32_16x16x32_bf16 v[204:207], v[124:127], v[240:243], v[204:207]\n\t" \
    "v_xor_b32 v252, 0x2800, %[xa]\n\t" \
    "ds_read_b128 v[240:243], v252 offset:16384\n\t" \
    "s_waitcnt lgkmcnt(6)\n\t" \
    "v_mfma_f32_16x16x32_bf16 v[204:207], v[144:147], v[212:215], v[204:207]\n\t" \
    "s_waitcnt lgkmcnt(5)\n\t" \
    "v_mfma_f32_16x16x32_bf16 v[204:207], v[148:151], v[200:203], v[204:207]\n\t" \
    "v_mfma_f32_16x16x32_bf16 v[208:211], v[152:155], v[212:215], v[208:211]\n\t" \
    "ds_read_b128 v[212:215], v252 offset:17408\n\t" \
    "v_mfma_f32_16x16x32_bf16 v[208:211], v[156:159], v[200:203], v[208:211]\n\t" \
    "v_xor_b32 v252, 0x3000, %[xa]\n\t" \
    "ds_read_b128 v[200:203], v252 offset:16384\n\t" \
    "s_waitcnt lgkmcnt(6)\n\t" \
    "v_mfma_f32_16x16x32_bf16 v[208:211], v[192:195], v[224:227], v[208:211]\n\t" \
    "s_waitcnt lgkmcnt(5)\n\t" \
    "v_mfma_f32_16x16x32_bf16 v[208:211], v[196:199], v[228:231], v[208:211]\n\t" \
    "v_mfma_f32_16x16x32_bf16 v[204:207], v[184:187], v[224:227], v[204:207]\n\t" \
    "ds_read_b128 v[224:227], v252 offset:17408\n\t" \
    "v_mfma_f32_16x16x32_bf16 v[204:207], v[188:191], v[228:231], v[204:207]\n\t" \
    "v_xor_b32 v252, 0x3800, %[xa]\n\t" \
    "ds_read_b128 v[228:231], v252 offset:16384\n\t" \
    "s_waitcnt lgkmcnt(6)\n\t" \
    "v_mfma_f32_16x16x32_bf16 v[216:219], v[80:83], v[232:235], v[216:219]\n\t" \
    "s_waitcnt lgkmcnt(5)\n\t" \
    "v_mfma_f32_16x16x32_bf16 v[216:219], v[84:87], v[236:239], v[216:219]\n\t" \
    "v_mfma_f32_16x16x32_bf16 v[220:223], v[88:91], v[232:235], v[220:223]\n\t" \
    "ds_read_b128 v[232:235], v252 offset:17408\n\t" \
    "v_mfma_f32_16x16x32_bf16 v[220:223], v[92:95], v[236:239], v[220:223]\n\t" \
    "v_and_b32 v252, 0x3ff, %[xa]\n\t" \
    "ds_read_b128 v[236:239], v252 offset:47104\n\t" \
    "s_waitcnt lgkmcnt(6)\n\t" \
    "v_mfma_f32_16x16x32_bf16 v[220:223], v[112:115], v[240:243], v[220:223]\n\t" \
    "v_mov_b64 v[244:245], 0\n\t" \
    "s_waitcnt lgkmcnt(5)\n\t" \
    "v_mfma_f32_16x16x32_bf16 v[220:223], v[116:119], v[212:215], v[220:223]\n\t" \
    "v_mov_b64 v[246:247], 0\n\t" \
    "v_mfma_f32_16x16x32_bf16 v[216:219], v[104:107], v[240:243], v[216:219]\n\t" \
    "ds_read_b128 v[240:243], v252 offset:48128\n\t" \
    "v_mov_b64 v[248:249], 0\n\t" \
    "v_mfma_f32_16x16x32_bf16 v[216:219], v[108:111], v[212:215], v[216:219]\n\t" \
    "v_mov_b64 v[250:251], 0\n\t" \
    "s_waitcnt lgkmcnt(5)\n\t" \
    "v_mfma_f32_16x16x32_bf16 v[216:219], v[120:123], v[200:203], v[216:219]\n\t" \
    "s_waitcnt lgkmcnt(4)\n\t" \
    "v_mfma_f32_16x16x32_bf16 v[216:219], v[124:127], v[224:227], v[216:219]\n\t" \
    "v_mfma_f32_16x16x32_bf16 v[220:223], v[128:131], v[200:203], v[220:223]\n\t" \
    "v_mfma_f32_16x16x32_bf16 v[220:223], v[132:135], v[224:227], v[220:223]\n\t" \
    "s_waitcnt lgkmcnt(3)\n\t" \
    "v_mfma_f32_16x16x32_bf16 v[220:223], v[152:155], v[228:231], v[220:223]\n\t" \
    "v_add_u32 v252, %[qs], v252\n\t" \
    "v_mov_b64 v[224:225], 0\n\t" \
    "s_waitcnt lgkmcnt(2)\n\t" \
    "v_mfma_f32_16x16x32_bf16 v[220:223], v[156:159], v[232:235], v[220:223]\n\t" \
    "v_mov_b64 v[226:227], 0\n\t" \
    "v_cvt_pk_bf16_f32 v208, v208, v209\n\t" \
    "v_mfma_f32_16x16x32_bf16 v[216:219], v[144:147], v[228:231], v[216:219]\n\t" \
    "v_cvt_pk_bf16_f32 v209, v210, v211\n\t" \
    "ds_write_b64 v252, v[208:209] offset:2048\n\t" \
    "v_mfma_f32_16x16x32_bf16 v[216:219], v[148:151], v[232:235], v[216:219]\n\t" \
    "v_cvt_pk_bf16_f32 v204, v204, v205\n\t" \
    "v_cvt_pk_bf16_f32 v205, v206, v207\n\t" \
    "s_waitcnt lgkmcnt(2)\n\t" \
    "v_mfma_f32_16x16x32_bf16 v[216:219], v[184:187], v[236:239], v[216:219]\n\t" \
    "ds_write_b64 v252, v[204:205] offset:1024\n\t" \
    "ds_read_b128 v[228:231], %[wl] offset:2048\n\t" \
    "s_waitcnt lgkmcnt(3)\n\t" \
    "v_mfma_f32_16x16x32_bf16 v[216:219], v[188:191], v[240:243], v[216:219]\n\t" \
    "ds_read_b128 v[232:235], %[wl] offset:3072\n\t" \
    "v_mfma_f32_16x16x32_bf16 v[220:223], v[192:195], v[236:239], v[220:223]\n\t" \
    "v_mfma_f32_16x16x32_bf16 v[220:223], v[196:199], v[240:243], v[220:223]\n\t" \
    "ds_read_b128 v[200:203], %[wl] offset:0\n\t" \
    "ds_read_b128 v[204:207], %[wl] offset:1024\n\t" \
    "v_cvt_pk_bf16_f32 v216, v216, v217\n\t" \
    "v_cvt_pk_bf16_f32 v217, v218, v219\n\t" \
    "ds_write_b64 v252, v[216:217] offset:7168\n\t" \
    "s_nop 2\n\t" \
    "v_cvt_pk_bf16_f32 v220, v220, v221\n\t" \
    "v_cvt_pk_bf16_f32 v221, v222, v223\n\t" \
    "ds_write_b64 v252, v[220:221] offset:8192\n\t" \
    "s_waitcnt lgkmcnt(0)\n\t"
#define ASM_XG_S_LO1 \
    "s_waitcnt lgkmcnt(0)\n\t" \
    "ds_read_b128 v[224:227], %[xa] offset:16384\n\t" \
    "ds_read_b128 v[228:231], %[xa] offset:17408\n\t" \
    "v_xor_b32 v252, 0x800, %[xa]\n\t" \
    "ds_read_b128 v[232:235], v252 offset:16384\n\t" \
    "ds_read_b128 v[236:239], v252 offset:17408\n\t" \
    "v_xor_b32 v252, 0x1000, %[xa]\n\t" \
    "ds_read_b128 v[240:243], v252 offset:16384\n\t" \
    "ds_read_b128 v[212:215], v252 offset:17408\n\t" \
    "v_xor_b32 v252, 0x1800, %[xa]\n\t" \
    "ds_read_b128 v[200:203], v252 offset:16384\n\t" \
    "s_waitcnt lgkmcnt(6)\n\t" \
    "v_mfma_f32_16x16x32_bf16 v[220:223], v[8:11], v[224:227], 0\n\t" \
    "ds_read_b128 v[224:227], v252 offset:17408\n\t" \
    "s_waitcnt lgkmcnt(6)\n\t" \
    "v_mfma_f32_16x16x32_bf16 v[220:223], v[12:15], v[228:231], v[220:223]\n\t" \
    "v_and_b32 v252, 0x3ff, %[xa]\n\t" \
    "ds_read_b128 v[228:231], v252 offset:45056\n\t" \
    "s_waitcnt lgkmcnt(6)\n\t" \
    "v_mfma_f32_16x16x32_bf16 v[220:223], v[32:35], v[232:235], v[220:223]\n\t" \
    "ds_read_b128 v[232:235], v252 offset:46080\n\t" \
    "s_waitcnt lgkmcnt(6)\n\t" \
    "v_mfma_f32_16x16x32_bf16 v[220:223], v[36:39], v[236:239], v[220:223]\n\t" \
    "ds_read_b128 v[236:239], %[xa] offset:0\n\t" \
    "s_waitcnt lgkmcnt(6)\n\t" \
    "v_mfma_f32_16x16x32_bf16 v[220:223], v[48:51], v[240:243], v[220:223]\n\t" \
    "ds_read_b128 v[240:243], %[xa] offset:1024\n\t" \
    "s_waitcnt lgkmcnt(6)\n\t" \
    "v_mfma_f32_16x16x32_bf16 v[220:223], v[52:55], v[212:215], v[220:223]\n\t" \
    "v_xor_b32 v252, 0x800, %[xa]\n\t" \
    "ds_read_b128 v[212:215], v252 offset:0\n\t" \
    "s_waitcnt lgkmcnt(6)\n\t" \
    "v_mfma_f32_16x16x32_bf16 v[220:223], v[72:75], v[200:203], v[220:223]\n\t" \
    "ds_read_b128 v[200:203], v252 offset:1024\n\t" \
    "s_waitcnt lgkmcnt(6)\n\t" \
    "v_mfma_f32_16x16x32_bf16 v[220:223], v[76:79], v[224:227], v[220:223]\n\t" \
    "v_xor_b32 v252, 0x1000, %[xa]\n\t" \
    "ds_read_b128 v[224:227], v252 offset:0\n\t" \
    "s_waitcnt lgkmcnt(6)\n\t" \
    "v_mfma_f32_16x16x32_bf16 v[220:223], v[168:171], v[228:231], v[220:223]\n\t" \
    "ds_read_b128 v[228:231], v252 offset:1024\n\t" \
    "s_waitcnt lgkmcnt(6)\n\t" \
    "v_mfma_f32_16x16x32_bf16 v[220:223], v[172:175], v[232:235], v[220:223]\n\t" \
    "v_xor_b32 v252, 0x1800, %[xa]\n\t" \
    "ds_read_b128 v[232:235], v252 offset:0\n\t" \
    "s_waitcnt lgkmcnt(6)\n\t" \
    "v_mfma_f32_16x16x32_bf16 v[208:211], v[8:11], v[236:239], 0\n\t" \
    "ds_read_b128 v[236:239], v252 offset:1024\n\t" \
    "s_waitcnt lgkmcnt(6)\n\t" \
    "v_mfma_f32_16x16x32_bf16 v[208:211], v[12:15], v[240:243], v[208:211]\n\t" \
    "v_and_b32 v252, 0x3ff, %[xa]\n\t" \
    "ds_read_b128 v[240:243], v252 offset:40960\n\t" \
    "s_waitcnt lgkmcnt(6)\n\t" \
    "v_mfma_f32_16x16x32_bf16 v[208:211], v[32:35], v[212:215], v[208:211]\n\t" \
    "ds_read_b128 v[212:215], v252 offset:41984\n\t" \
    "s_waitcnt lgkmcnt(6)\n\t" \
    "v_mfma_f32_16x16x32_bf16 v[208:211], v[36:39], v[200:203], v[208:211]\n\t" \
    "s_waitcnt lgkmcnt(0)\n\t" \
    "s_barrier\n\t" ASM_ST \
    "v_xor_b32 v252, 0x2000, %[xa]\n\t" \
    "ds_read_b128 v[200:203], v252 offset:0\n\t" \
    "v_mfma_f32_16x16x32_bf16 v[208:211], v[48:51], v[224:227], v[208:211]\n\t" \
    "ds_read_b128 v[224:227], v252 offset:1024\n\t" \
    "v_mfma_f32_16x16x32_bf16 v[208:211], v[52:55], v[228:231], v[208:211]\n\t" \
    "v_xor_b32 v252, 0x2800, %[xa]\n\t" \
    "ds_read_b128 v[228:231], v252 offset:0\n\t" \
    "v_mfma_f32_16x16x32_bf16 v[208:211], v[72:75], v[232:235], v[208:211]\n\t" \
    "ds_read_b128 v[232:235], v252 offset:1024\n\t" \
    "v_mfma_f32_16x16x32_bf16 v[208:211], v[76:79], v[236:239], v[208:211]\n\t" \
    "v_xor_b32 v252, 0x3000, %[xa]\n\t" \
    "ds_read_b128 v[236:239], v252 offset:0\n\t" \
    "v_mfma_f32_16x16x32_bf16 v[208:211], v[168:171], v[240:243], v[208:211]\n\t" \
    "ds_read_b128 v[240:243], v252 offset:1024\n\t" \
    "v_mfma_f32_16x16x32_bf16 v[208:211], v[172:175], v[212:215], v[208:211]\n\t" \
    "v_xor_b32 v252, 0x3800, %[xa]\n\t" \
    "ds_read_b128 v[212:215], v252 offset:0\n\t" \
    "s_waitcnt lgkmcnt(6)\n\t" \
    "v_mfma_f32_16x16x32_bf16 v[208:211], v[88:91], v[200:203], v[208:211]\n\t" \
    "ds_read_b128 v[200:203], v252 offset:1024\n\t" \
    "s_waitcnt lgkmcnt(6)\n\t" \
    "v_mfma_f32_16x16x32_bf16 v[208:211], v[92:95], v[224:227], v[208:211]\n\t" \
    "v_and_b32 v252, 0x3ff, %[xa]\n\t" \
    "ds_read_b128 v[224:227], v252 offset:43008\n\t" \
    "s_waitcnt lgkmcnt(6)\n\t" \
    "v_mfma_f32_16x16x32_bf16 v[208:211], v[112:115], v[228:231], v[208:211]\n\t" \
    "ds_read_b128 v[228:231], v252 offset:44032\n\t" \
    "s_waitcnt lgkmcnt(6)\n\t" \
    "v_mfma_f32_16x16x32_bf16 v[208:211], v[116:119], v[232:235], v[208:211]\n\t" \
    "v_xor_b32 v252, 0x2000, %[xa]\n\t" \
    "ds_read_b128 v[232:235], v252 offset:16384\n\t" \
    "s_waitcnt lgkmcnt(6)\n\t" \
    "v_mfma_f32_16x16x32_bf16 v[208:211], v[128:131], v[236:239], v[208:211]\n\t" \
    "ds_read_b128 v[236:239], v252 offset:17408\n\t" \
    "s_waitcnt lgkmcnt(6)\n\t" \
    "v_mfma_f32_16x16x32_bf16 v[208:211], v[132:135], v[240:243], v[208:211]\n\t" \
    "v_xor_b32 v252, 0x2800, %[xa]\n\t" \
    "ds_read_b128 v[240:243], v252 offset:16384\n\t" \
    "s_waitcnt lgkmcnt(6)\n\t" \
    "v_mfma_f32_16x16x32_bf16 v[208:211], v[152:155], v[212:215], v[208:211]\n\t" \
    "ds_read_b128 v[212:215], v252 offset:17408\n\t" \
    "s_waitcnt lgkmcnt(6)\n\t" \
    "v_mfma_f32_16x16x32_bf16 v[208:211], v[156:159], v[200:203], v[208:211]\n\t" \
    "v_xor_b32 v252, 0x3000, %[xa]\n\t" \
    "ds_read_b128 v[200:203], v252 offset:16384\n\t" \
    "s_waitcnt lgkmcnt(6)\n\t" \
    "v_mfma_f32_16x16x32_bf16 v[208:211], v[192:195], v[224:227], v[208:211]\n\t" \
    "ds_read_b128 v[224:227], v252 offset:17408\n\t" \
    "s_waitcnt lgkmcnt(6)\n\t" \
    "v_mfma_f32_16x16x32_bf16 v[208:211], v[196:199], v[228:231], v[208:211]\n\t" \
    "v_xor_b32 v252, 0x3800, %[xa]\n\t" \
    "ds_read_b128 v[228:231], v252 offset:16384\n\t" \
    "s_waitcnt lgkmcnt(6)\n\t" \
    "v_mfma_f32_16x16x32_bf16 v[220:223], v[88:91], v[232:235], v[220:223]\n\t" \
    "ds_read_b128 v[232:235], v252 offset:17408\n\t" \
    "s_waitcnt lgkmcnt(6)\n\t" \
    "v_mfma_f32_16x16x32_bf16 v[220:223], v[92:95], v[236:239], v[220:223]\n\t" \
    "v_and_b32 v252, 0x3ff, %[xa]\n\t" \
    "ds_read_b128 v[236:239], v252 offset:47104\n\t" \
    "s_waitcnt lgkmcnt(6)\n\t" \
    "v_mfma_f32_16x16x32_bf16 v[220:223], v[112:115], v[240:243], v[220:223]\n\t" \
    "ds_read_b128 v[240:243], v252 offset:48128\n\t" \
    "v_mov_b64 v[244:245], 0\n\t" \
    "s_waitcnt lgkmcnt(6)\n\t" \
    "v_mfma_f32_16x16x32_bf16 v[220:223], v[116:119], v[212:215], v[220:223]\n\t" \
    "v_mov_b64 v[246:247], 0\n\t" \
    "s_waitcnt lgkmcnt(5)\n\t" \
    "v_mfma_f32_16x16x32_bf16 v[220:223], v[128:131], v[200:203], v[220:223]\n\t" \
    "v_mov_b64 v[248:249], 0\n\t" \
    "s_waitcnt lgkmcnt(4)\n\t" \
    "v_mfma_f32_16x16x32_bf16 v[220:223], v[132:135], v[224:227], v[220:223]\n\t" \
    "v_mov_b64 v[250:251], 0\n\t" \
    "s_waitcnt lgkmcnt(3)\n\t" \
    "v_mfma_f32_16x16x32_bf16 v[220:223], v[152:155], v[228:231], v[220:223]\n\t" \
    "v_add_u32 v252, %[qs], v252\n\t" \
    "v_mov_b64 v[224:225], 0\n\t" \
    "s_waitcnt lgkmcnt(2)\n\t" \
    "v_mfma_f32_16x16x32_bf16 v[220:223], v[156:159], v[232:235], v[220:223]\n\t" \
    "v_mov_b64 v[226:227], 0\n\t" \
    "v_cvt_pk_bf16_f32 v208, v208, v209\n\t" \
    "s_waitcnt lgkmcnt(1)\n\t" \
    "v_mfma_f32_16x16x32_bf16 v[220:223], v[192:195], v[236:239], v[220:223]\n\t" \
    "v_cvt_pk_bf16_f32 v209, v210, v211\n\t" \
    "ds_write_b64 v252, v[208:209] offset:2048\n\t" \
    "s_waitcnt lgkmcnt(1)\n\t" \
    "v_mfma_f32_16x16x32_bf16 v[220:223], v[196:199], v[240:243], v[220:223]\n\t" \
    "ds_read_b128 v[228:231], %[wl] offset:2048\n\t" \
    "ds_read_b128 v[232:235], %[wl] offset:3072\n\t" \
    "ds_read_b128 v[200:203], %[wl] offset:0\n\t" \
    "ds_read_b128 v[204:207], %[wl] offset:1024\n\t" \
    "s_nop 3\n\t" \
    "v_cvt_pk_bf16_f32 v220, v220, v221\n\t" \
    "v_cvt_pk_bf16_f32 v221, v222, v223\n\t" \
    "ds_write_b64 v252, v[220:221] offset:8192\n\t" \
    "s_waitcnt lgkmcnt(0)\n\t"
#define ASM_XG_V \
    "s_waitcnt lgkmcnt(0)\n\t" \
    "ds_read_b128 v[224:227], %[xa] offset:16384\n\t" \
    "ds_read_b128 v[228:231], %[xa] offset:17408\n\t" \
    "ds_read_b128 v[244:247], %[wl] offset:0\n\t" \
    "ds_read_b128 v[248:251], %[wl] offset:1024\n\t" \
    "ds_read_b128 v[232:235], %[xa] offset:0\n\t" \
    "ds_read_b128 v[236:239], %[xa] offset:1024\n\t" \
    "v_xor_b32 v252, 0x800, %[xa]\n\t" \
    "ds_read_b128 v[240:243], v252 offset:0\n\t" \
    "s_waitcnt lgkmcnt(6)\n\t" \
    "v_mfma_f32_16x16x32_bf16 v[220:223], v[8:11], v[224:227], 0\n\t" \
    "s_waitcnt lgkmcnt(5)\n\t" \
    "v_mfma_f32_16x16x32_bf16 v[220:223], v[12:15], v[228:231], v[220:223]\n\t" \
    "v_mfma_f32_16x16x32_bf16 v[216:219], v[0:3], v[224:227], 0\n\t" \
    "v_mfma_f32_16x16x32_bf16 v[216:219], v[4:7], v[228:231], v[216:219]\n\t" \
    "s_waitcnt lgkmcnt(4)\n\t" \
    "v_mfma_f32_16x16x32_bf16 v[212:215], v[244:247], v[224:227], 0\n\t" \
    "ds_read_b128 v[224:227], v252 offset:1024\n\t" \
    "s_waitcnt lgkmcnt(4)\n\t" \
    "v_mfma_f32_16x16x32_bf16 v[212:215], v[248:251], v[228:231], v[212:215]\n\t" \
    "ds_read_b128 v[228:231], v252 offset:16384\n\t" \
    "s_waitcnt lgkmcnt(4)\n\t" \
    "v_mfma_f32_16x16x32_bf16 v[200:203], v[244:247], v[232:235], 0\n\t" \
    "ds_read_b128 v[244:247], %[wl] offset:2048\n\t" \
    "s_waitcnt lgkmcnt(4)\n\t" \
    "v_mfma_f32_16x16x32_bf16 v[200:203], v[248:251], v[236:239], v[200:203]\n\t" \
    "ds_read_b128 v[248:251], %[wl] offset:3072\n\t" \
    "v_mfma_f32_16x16x32_bf16 v[204:207], v[0:3], v[232:235], 0\n\t" \
    "v_mfma_f32_16x16x32_bf16 v[204:207], v[4:7], v[236:239], v[204:207]\n\t" \
    "v_mfma_f32_16x16x32_bf16 v[208:211], v[8:11], v[232:235], 0\n\t" \
    "ds_read_b128 v[232:235], v252 offset:17408\n\t" \
    "v_mfma_f32_16x16x32_bf16 v[208:211], v[12:15], v[236:239], v[208:211]\n\t" \
    "v_xor_b32 v252, 0x1000, %[xa]\n\t" \
    "ds_read_b128 v[236:239], v252 offset:16384\n\t" \
    "s_waitcnt lgkmcnt(6)\n\t" \
    "v_mfma_f32_16x16x32_bf16 v[208:211], v[32:35], v[240:243], v[208:211]\n\t" \
    "s_waitcnt lgkmcnt(5)\n\t" \
    "v_mfma_f32_16x16x32_bf16 v[208:211], v[36:39], v[224:227], v[208:211]\n\t" \
    "v_mfma_f32_16x16x32_bf16 v[204:207], v[24:27], v[240:243], v[204:207]\n\t" \
    "v_mfma_f32_16x16x32_bf16 v[204:207], v[28:31], v[224:227], v[204:207]\n\t" \
    "v_mfma_f32_16x16x32_bf16 v[200:203], v[16:19], v[240:243], v[200:203]\n\t" \
    "ds_read_b128 v[240:243], v252 offset:17408\n\t" \
    "v_mfma_f32_16x16x32_bf16 v[200:203], v[20:23], v[224:227], v[200:203]\n\t" \
    "ds_read_b128 v[224:227], v252 offset:0\n\t" \
    "s_waitcnt lgkmcnt(6)\n\t" \
    "v_mfma_f32_16x16x32_bf16 v[212:215], v[16:19], v[228:231], v[212:215]\n\t" \
    "s_waitcnt lgkmcnt(3)\n\t" \
    "v_mfma_f32_16x16x32_bf16 v[212:215], v[20:23], v[232:235], v[212:215]\n\t" \
    "v_mfma_f32_16x16x32_bf16 v[216:219], v[24:27], v[228:231], v[216:219]\n\t" \
    "v_mfma_f32_16x16x32_bf16 v[216:219], v[28:31], v[232:235], v[216:219]\n\t" \
    "v_mfma_f32_16x16x32_bf16 v[220:223], v[32:35], v[228:231], v[220:223]\n\t" \
    "ds_read_b128 v[228:231], v252 offset:1024\n\t" \
    "v_mfma_f32_16x16x32_bf16 v[220:223], v[36:39], v[232:235], v[220:223]\n\t" \
    "v_xor_b32 v252, 0x1800, %[xa]\n\t" \
    "ds_read_b128 v[232:235], v252 offset:0\n\t" \
    "s_waitcnt lgkmcnt(4)\n\t" \
    "v_mfma_f32_16x16x32_bf16 v[220:223], v[48:51], v[236:239], v[220:223]\n\t" \
    "s_waitcnt lgkmcnt(3)\n\t" \
    "v_mfma_f32_16x16x32_bf16 v[220:223], v[52:55], v[240:243], v[220:223]\n\t" \
    "v_mfma_f32_16x16x32_bf16 v[216:219], v[40:43], v[236:239], v[216:219]\n\t" \
    "v_mfma_f32_16x16x32_bf16 v[216:219], v[44:47], v[240:243], v[216:219]\n\t" \
    "v_mfma_f32_16x16x32_bf16 v[212:215], v[244:247], v[236:239], v[212:215]\n\t" \
    "ds_read_b128 v[236:239], v252 offset:1024\n\t" \
    "v_mfma_f32_16x16x32_bf16 v[212:215], v[248:251], v[240:243], v[212:215]\n\t" \
    "ds_read_b128 v[240:243], v252 offset:16384\n\t" \
    "s_waitcnt lgkmcnt(4)\n\t" \
    "v_mfma_f32_16x16x32_bf16 v[200:203], v[244:247], v[224:227], v[200:203]\n\t" \
    "ds_read_b128 v[244:247], %[wl] offset:8192\n\t" \
    "s_waitcnt lgkmcnt(4)\n\t" \
    "v_mfma_f32_16x16x32_bf16 v[200:203], v[248:251], v[228:231], v[200:203]\n\t" \
    "ds_read_b128 v[248:251], %[wl] offset:9216\n\t" \
    "v_mfma_f32_16x16x32_bf16 v[204:207], v[40:43], v[224:227], v[204:207]\n\t" \
    "v_mfma_f32_16x16x32_bf16 v[204:207], v[44:47], v[228:231], v[204:207]\n\t" \
    "v_mfma_f32_16x16x32_bf16 v[208:211], v[48:51], v[224:227], v[208:211]\n\t" \
    "ds_read_b128 v[224:227], v252 offset:17408\n\t" \
    "v_mfma_f32_16x16x32_bf16 v[208:211], v[52:55], v[228:231], v[208:211]\n\t" \
    "v_and_b32 v252, 0x3ff, %[xa]\n\t" \
    "ds_read_b128 v[228:231], v252 offset:45056\n\t" \
    "s_waitcnt lgkmcnt(6)\n\t" \
    "v_mfma_f32_16x16x32_bf16 v[208:211], v[72:75], v[232:235], v[208:211]\n\t" \
    "s_waitcnt lgkmcnt(5)\n\t" \
    "v_mfma_f32_16x16x32_bf16 v[208:211], v[76:79], v[236:239], v[208:211]\n\t" \
    "v_mfma_f32_16x16x32_bf16 v[204:207], v[64:67], v[232:235], v[204:207]\n\t" \
    "v_mfma_f32_16x16x32_bf16 v[204:207], v[68:71], v[236:239], v[204:207]\n\t" \
    "v_mfma_f32_16x16x32_bf16 v[200:203], v[56:59], v[232:235], v[200:203]\n\t" \
    "ds_read_b128 v[232:235], v252 offset:46080\n\t" \
    "v_mfma_f32_16x16x32_bf16 v[200:203], v[60:63], v[236:239], v[200:203]\n\t" \
    "ds_read_b128 v[236:239], v252 offset:40960\n\t" \
    "s_waitcnt lgkmcnt(6)\n\t" \
    "v_mfma_f32_16x16x32_bf16 v[212:215], v[56:59], v[240:243], v[212:215]\n\t" \
    "s_waitcnt lgkmcnt(3)\n\t" \
    "v_mfma_f32_16x16x32_bf16 v[212:215], v[60:63], v[224:227], v[212:215]\n\t" \
    "v_mfma_f32_16x16x32_bf16 v[216:219], v[64:67], v[240:243], v[216:219]\n\t" \
    "v_mfma_f32_16x16x32_bf16 v[216:219], v[68:71], v[224:227], v[216:219]\n\t" \
    "v_mfma_f32_16x16x32_bf16 v[220:223], v[72:75], v[240:243], v[220:223]\n\t" \
    "ds_read_b128 v[240:243], v252 offset:41984\n\t" \
    "v_mfma_f32_16x16x32_bf16 v[220:223], v[76:79], v[224:227], v[220:223]\n\t" \
    "v_xor_b32 v252, 0x2000, %[xa]\n\t" \
    "ds_read_b128 v[224:227], v252 offset:0\n\t" \
    "s_waitcnt lgkmcnt(4)\n\t" \
    "v_mfma_f32_16x16x32_bf16 v[220:223], v[168:171], v[228:231], v[220:223]\n\t" \
    "s_waitcnt lgkmcnt(3)\n\t" \
    "v_mfma_f32_16x16x32_bf16 v[220:223], v[172:175], v[232:235], v[220:223]\n\t" \
    "v_mfma_f32_16x16x32_bf16 v[216:219], v[160:163], v[228:231], v[216:219]\n\t" \
    "v_mfma_f32_16x16x32_bf16 v[216:219], v[164:167], v[232:235], v[216:219]\n\t" \
    "v_mfma_f32_16x16x32_bf16 v[212:215], v[244:247], v[228:231], v[212:215]\n\t" \
    "ds_read_b128 v[228:231], v252 offset:1024\n\t" \
    "v_mfma_f32_16x16x32_bf16 v[212:215], v[248:251], v[232:235], v[212:215]\n\t" \
    "ds_read_b128 v[232:235], v252 offset:16384\n\t" \
    "s_waitcnt lgkmcnt(4)\n\t" \
    "v_mfma_f32_16x16x32_bf16 v[200:203], v[244:247], v[236:239], v[200:203]\n\t" \
    "ds_read_b128 v[244:247], %[wl] offset:4096\n\t" \
    "s_waitcnt lgkmcnt(4)\n\t" \
    "v_mfma_f32_16x16x32_bf16 v[200:203], v[248:251], v[240:243], v[200:203]\n\t" \
    "ds_read_b128 v[248:251], %[wl] offset:5120\n\t" \
    "v_mfma_f32_16x16x32_bf16 v[204:207], v[160:163], v[236:239], v[204:207]\n\t" \
    "v_mfma_f32_16x16x32_bf16 v[204:207], v[164:167], v[240:243], v[204:207]\n\t" \
    "v_mfma_f32_16x16x32_bf16 v[208:211], v[168:171], v[236:239], v[208:211]\n\t" \
    "ds_read_b128 v[236:239], v252 offset:17408\n\t" \
    "v_mfma_f32_16x16x32_bf16 v[208:211], v[172:175], v[240:243], v[208:211]\n\t" \
    "v_xor_b32 v252, 0x2800, %[xa]\n\t" \
    "ds_read_b128 v[240:243], v252 offset:16384\n\t" \
    "s_waitcnt lgkmcnt(6)\n\t" \
    "v_mfma_f32_16x16x32_bf16 v[208:211], v[88:91], v[224:227], v[208:211]\n\t" \
    "s_waitcnt lgkmcnt(5)\n\t" \
    "v_mfma_f32_16x16x32_bf16 v[208:211], v[92:95], v[228:231], v[208:211]\n\t" \
    "v_mfma_f32_16x16x32_bf16 v[204:207], v[80:83], v[224:227], v[204:207]\n\t" \
    "v_mfma_f32_16x16x32_bf16 v[204:207], v[84:87], v[228:231], v[204:207]\n\t" \
    "s_waitcnt lgkmcnt(3)\n\t" \
    "v_mfma_f32_16x16x32_bf16 v[200:203], v[244:247], v[224:227], v[200:203]\n\t" \
    "ds_read_b128 v[224:227], v252 offset:17408\n\t" \
    "s_waitcnt lgkmcnt(3)\n\t" \
    "v_mfma_f32_16x16x32_bf16 v[200:203], v[248:251], v[228:231], v[200:203]\n\t" \
    "ds_read_b128 v[228:231], v252 offset:0\n\t" \
    "v_mfma_f32_16x16x32_bf16 v[212:215], v[244:247], v[232:235], v[212:215]\n\t" \
    "ds_read_b128 v[244:247], %[wl] offset:6144\n\t" \
    "s_waitcnt lgkmcnt(4)\n\t" \
    "v_mfma_f32_16x16x32_bf16 v[212:215], v[248:251], v[236:239], v[212:215]\n\t" \
    "ds_read_b128 v[248:251], %[wl] offset:7168\n\t" \
    "v_mfma_f32_16x16x32_bf16 v[216:219], v[80:83], v[232:235], v[216:219]\n\t" \
    "v_mfma_f32_16x16x32_bf16 v[216:219], v[84:87], v[236:239], v[216:219]\n\t" \
    "v_mfma_f32_16x16x32_bf16 v[220:223], v[88:91], v[232:235], v[220:223]\n\t" \
    "ds_read_b128 v[232:235], v252 offset:1024\n\t" \
    "v_mfma_f32_16x16x32_bf16 v[220:223], v[92:95], v[236:239], v[220:223]\n\t" \
    "v_xor_b32 v252, 0x3000, %[xa]\n\t" \
    "ds_read_b128 v[236:239], v252 offset:0\n\t" \
    "s_waitcnt lgkmcnt(6)\n\t" \
    "v_mfma_f32_16x16x32_bf16 v[220:223], v[112:115], v[240:243], v[220:223]\n\t" \
    "s_waitcnt lgkmcnt(5)\n\t" \
    "v_mfma_f32_16x16x32_bf16 v[220:223], v[116:119], v[224:227], v[220:223]\n\t" \
    "v_mfma_f32_16x16x32_bf16 v[216:219], v[104:107], v[240:243], v[216:219]\n\t" \
    "v_mfma_f32_16x16x32_bf16 v[216:219], v[108:111], v[224:227], v[216:219]\n\t" \
    "v_mfma_f32_16x16x32_bf16 v[212:215], v[96:99], v[240:243], v[212:215]\n\t" \
    "ds_read_b128 v[240:243], v252 offset:1024\n\t" \
    "v_mfma_f32_16x16x32_bf16 v[212:215], v[100:103], v[224:227], v[212:215]\n\t" \
    "ds_read_b128 v[224:227], v252 offset:16384\n\t" \
    "s_waitcnt lgkmcnt(6)\n\t" \
    "v_mfma_f32_16x16x32_bf16 v[200:203], v[96:99], v[228:231], v[200:203]\n\t" \
    "s_waitcnt lgkmcnt(3)\n\t" \
    "v_mfma_f32_16x16x32_bf16 v[200:203], v[100:103], v[232:235], v[200:203]\n\t" \
    "v_mfma_f32_16x16x32_bf16 v[204:207], v[104:107], v[228:231], v[204:207]\n\t" \
    "v_mfma_f32_16x16x32_bf16 v[204:207], v[108:111], v[232:235], v[204:207]\n\t" \
    "v_mfma_f32_16x16x32_bf16 v[208:211], v[112:115], v[228:231], v[208:211]\n\t" \
    "ds_read_b128 v[228:231], v252 offset:17408\n\t" \
    "v_mfma_f32_16x16x32_bf16 v[208:211], v[116:119], v[232:235], v[208:211]\n\t" \
    "v_xor_b32 v252, 0x3800, %[xa]\n\t" \
    "ds_read_b128 v[232:235], v252 offset:16384\n\t" \
    "s_waitcnt lgkmcnt(4)\n\t" \
    "v_mfma_f32_16x16x32_bf16 v[208:211], v[128:131], v[236:239], v[208:211]\n\t" \
    "s_waitcnt lgkmcnt(3)\n\t" \
    "v_mfma_f32_16x16x32_bf16 v[208:211], v[132:135], v[240:243], v[208:211]\n\t" \
    "v_mfma_f32_16x16x32_bf16 v[204:207], v[120:123], v[236:239], v[204:207]\n\t" \
    "v_mfma_f32_16x16x32_bf16 v[204:207], v[124:127], v[240:243], v[204:207]\n\t" \
    "v_mfma_f32_16x16x32_bf16 v[200:203], v[244:247], v[236:239], v[200:203]\n\t" \
    "ds_read_b128 v[236:239], v252 offset:17408\n\t" \
    "v_mfma_f32_16x16x32_bf16 v[200:203], v[248:251], v[240:243], v[200:203]\n\t" \
    "ds_read_b128 v[240:243], v252 offset:0\n\t" \
    "s_waitcnt lgkmcnt(4)\n\t" \
    "v_mfma_f32_16x16x32_bf16 v[212:215], v[244:247], v[224:227], v[212:215]\n\t" \
    "s_waitcnt lgkmcnt(3)\n\t" \
    "v_mfma_f32_16x16x32_bf16 v[212:215], v[248:251], v[228:231], v[212:215]\n\t" \
    "v_mfma_f32_16x16x32_bf16 v[216:219], v[120:123], v[224:227], v[216:219]\n\t" \
    "v_mfma_f32_16x16x32_bf16 v[216:219], v[124:127], v[228:231], v[216:219]\n\t" \
    "v_mfma_f32_16x16x32_bf16 v[220:223], v[128:131], v[224:227], v[220:223]\n\t" \
    "ds_read_b128 v[224:227], v252 offset:1024\n\t" \
    "v_mfma_f32_16x16x32_bf16 v[220:223], v[132:135], v[228:231], v[220:223]\n\t" \
    "v_and_b32 v252, 0x3ff, %[xa]\n\t" \
    "ds_read_b128 v[228:231], v252 offset:43008\n\t" \
    "s_waitcnt lgkmcnt(4)\n\t" \
    "v_mfma_f32_16x16x32_bf16 v[220:223], v[152:155], v[232:235], v[220:223]\n\t" \
    "s_waitcnt lgkmcnt(3)\n\t" \
    "v_mfma_f32_16x16x32_bf16 v[220:223], v[156:159], v[236:239], v[220:223]\n\t" \
    "v_mfma_f32_16x16x32_bf16 v[216:219], v[144:147], v[232:235], v[216:219]\n\t" \
    "v_mfma_f32_16x16x32_bf16 v[216:219], v[148:151], v[236:239], v[216:219]\n\t" \
    "v_mfma_f32_16x16x32_bf16 v[212:215], v[136:139], v[232:235], v[212:215]\n\t" \
    "ds_read_b128 v[232:235], v252 offset:44032\n\t" \
    "v_mfma_f32_16x16x32_bf16 v[212:215], v[140:143], v[236:239], v[212:215]\n\t" \
    "ds_read_b128 v[236:239], v252 offset:47104\n\t" \
    "s_waitcnt lgkmcnt(4)\n\t" \
    "v_mfma_f32_16x16x32_bf16 v[200:203], v[136:139], v[240:243], v[200:203]\n\t" \
    "s_waitcnt lgkmcnt(3)\n\t" \
    "v_mfma_f32_16x16x32_bf16 v[200:203], v[140:143], v[224:227], v[200:203]\n\t" \
    "v_mfma_f32_16x16x32_bf16 v[204:207], v[144:147], v[240:243], v[204:207]\n\t" \
    "v_mfma_f32_16x16x32_bf16 v[204:207], v[148:151], v[224:227], v[204:207]\n\t" \
    "v_mfma_f32_16x16x32_bf16 v[208:211], v[152:155], v[240:243], v[208:211]\n\t" \
    "ds_read_b128 v[240:243], v252 offset:48128\n\t" \
    "v_mfma_f32_16x16x32_bf16 v[208:211], v[156:159], v[224:227], v[208:211]\n\t" \
    "s_waitcnt lgkmcnt(3)\n\t" \
    "v_mfma_f32_16x16x32_bf16 v[208:211], v[192:195], v[228:231], v[208:211]\n\t" \
    "v_add_u32 v252, %[qs], v252\n\t" \
    "s_waitcnt lgkmcnt(2)\n\t" \
    "v_mfma_f32_16x16x32_bf16 v[208:211], v[196:199], v[232:235], v[208:211]\n\t" \
    "v_mfma_f32_16x16x32_bf16 v[204:207], v[184:187], v[228:231], v[204:207]\n\t" \
    "v_mfma_f32_16x16x32_bf16 v[204:207], v[188:191], v[232:235], v[204:207]\n\t" \
    "v_mfma_f32_16x16x32_bf16 v[200:203], v[176:179], v[228:231], v[200:203]\n\t" \
    "v_mfma_f32_16x16x32_bf16 v[200:203], v[180:183], v[232:235], v[200:203]\n\t" \
    "s_waitcnt lgkmcnt(1)\n\t" \
    "v_mfma_f32_16x16x32_bf16 v[212:215], v[176:179], v[236:239], v[212:215]\n\t" \
    "v_cvt_pk_bf16_f32 v208, v208, v209\n\t" \
    "v_cvt_pk_bf16_f32 v209, v210, v211\n\t" \
    "s_waitcnt lgkmcnt(0)\n\t" \
    "v_mfma_f32_16x16x32_bf16 v[212:215], v[180:183], v[240:243], v[212:215]\n\t" \
    "ds_write_b64 v252, v[208:209] offset:2048\n\t" \
    "v_cvt_pk_bf16_f32 v204, v204, v205\n\t" \
    "v_mfma_f32_16x16x32_bf16 v[216:219], v[184:187], v[236:239], v[216:219]\n\t" \
    "v_cvt_pk_bf16_f32 v205, v206, v207\n\t" \
    "ds_write_b64 v252, v[204:205] offset:1024\n\t" \
    "v_mfma_f32_16x16x32_bf16 v[216:219], v[188:191], v[240:243], v[216:219]\n\t" \
    "v_cvt_pk_bf16_f32 v200, v200, v201\n\t" \
    "v_cvt_pk_bf16_f32 v201, v202, v203\n\t" \
    "v_mfma_f32_16x16x32_bf16 v[220:223], v[192:195], v[236:239], v[220:223]\n\t" \
    "ds_write_b64 v252, v[200:201] offset:0\n\t" \
    "v_mfma_f32_16x16x32_bf16 v[220:223], v[196:199], v[240:243], v[220:223]\n\t" \
    "v_cvt_pk_bf16_f32 v212, v212, v213\n\t" \
    "v_cvt_pk_bf16_f32 v213, v214, v215\n\t" \
    "ds_write_b64 v252, v[212:213] offset:6144\n\t" \
    "v_cvt_pk_bf16_f32 v216, v216, v217\n\t" \
    "v_cvt_pk_bf16_f32 v217, v218, v219\n\t" \
    "ds_write_b64 v252, v[216:217] offset:7168\n\t" \
    "s_nop 1\n\t" \
    "v_cvt_pk_bf16_f32 v220, v220, v221\n\t" \
    "v_cvt_pk_bf16_f32 v221, v222, v223\n\t" \
    "ds_write_b64 v252, v[220:221] offset:8192\n\t" \
    "s_waitcnt lgkmcnt(0)\n\t"
#define ASM_XG_H_LO \
    "s_waitcnt lgkmcnt(0)\n\t" \
    "ds_read_b128 v[224:227], %[xa] offset:16384\n\t" \
    "ds_read_b128 v[228:231], %[xa] offset:17408\n\t" \
    "ds_read_b128 v[244:247], %[wl] offset:0\n\t" \
    "ds_read_b128 v[248:251], %[wl] offset:1024\n\t" \
    "ds_read_b128 v[232:235], %[xa] offset:0\n\t" \
    "ds_read_b128 v[236:239], %[xa] offset:1024\n\t" \
    "v_xor_b32 v252, 0x800, %[xa]\n\t" \
    "ds_read_b128 v[240:243], v252 offset:0\n\t" \
    "s_waitcnt lgkmcnt(6)\n\t" \
    "v_mfma_f32_16x16x32_bf16 v[220:223], v[8:11], v[224:227], 0\n\t" \
    "s_waitcnt lgkmcnt(5)\n\t" \
    "v_mfma_f32_16x16x32_bf16 v[220:223], v[12:15], v[228:231], v[220:223]\n\t" \
    "v_mfma_f32_16x16x32_bf16 v[216:219], v[0:3], v[224:227], 0\n\t" \
    "v_mfma_f32_16x16x32_bf16 v[216:219], v[4:7], v[228:231], v[216:219]\n\t" \
    "s_waitcnt lgkmcnt(4)\n\t" \
    "v_mfma_f32_16x16x32_bf16 v[212:215], v[244:247], v[224:227], 0\n\t" \
    "ds_read_b128 v[224:227], v252 offset:1024\n\t" \
    "s_waitcnt lgkmcnt(4)\n\t" \
    "v_mfma_f32_16x16x32_bf16 v[212:215], v[248:251], v[228:231], v[212:215]\n\t" \
    "ds_read_b128 v[228:231], v252 offset:16384\n\t" \
    "s_waitcnt lgkmcnt(4)\n\t" \
    "v_mfma_f32_16x16x32_bf16 v[200:203], v[244:247], v[232:235], 0\n\t" \
    "ds_read_b128 v[244:247], %[wl] offset:2048\n\t" \
    "s_waitcnt lgkmcnt(4)\n\t" \
    "v_mfma_f32_16x16x32_bf16 v[200:203], v[248:251], v[236:239], v[200:203]\n\t" \
    "ds_read_b128 v[248:251], %[wl] offset:3072\n\t" \
    "v_mfma_f32_16x16x32_bf16 v[204:207], v[0:3], v[232:235], 0\n\t" \
    "v_mfma_f32_16x16x32_bf16 v[204:207], v[4:7], v[236:239], v[204:207]\n\t" \
    "v_mfma_f32_16x16x32_bf16 v[208:211], v[8:11], v[232:235], 0\n\t" \
    "ds_read_b128 v[232:235], v252 offset:17408\n\t" \
    "v_mfma_f32_16x16x32_bf16 v[208:211], v[12:15], v[236:239], v[208:211]\n\t" \
    "v_xor_b32 v252, 0x1000, %[xa]\n\t" \
    "ds_read_b128 v[236:239], v252 offset:16384\n\t" \
    "s_waitcnt lgkmcnt(6)\n\t" \
    "v_mfma_f32_16x16x32_bf16 v[208:211], v[32:35], v[240:243], v[208:211]\n\t" \
    "s_waitcnt lgkmcnt(5)\n\t" \
    "v_mfma_f32_16x16x32_bf16 v[208:211], v[36:39], v[224:227], v[208:211]\n\t" \
    "v_mfma_f32_16x16x32_bf16 v[204:207], v[24:27], v[240:243], v[204:207]\n\t" \
    "v_mfma_f32_16x16x32_bf16 v[204:207], v[28:31], v[224:227], v[204:207]\n\t" \
    "v_mfma_f32_16x16x32_bf16 v[200:203], v[16:19], v[240:243], v[200:203]\n\t" \
    "ds_read_b128 v[240:243], v252 offset:17408\n\t" \
    "v_mfma_f32_16x16x32_bf16 v[200:203], v[20:23], v[224:227], v[200:203]\n\t" \
    "ds_read_b128 v[224:227], v252 offset:0\n\t" \
    "s_waitcnt lgkmcnt(6)\n\t" \
    "v_mfma_f32_16x16x32_bf16 v[212:215], v[16:19], v[228:231], v[212:215]\n\t" \
    "s_waitcnt lgkmcnt(3)\n\t" \
    "v_mfma_f32_16x16x32_bf16 v[212:215], v[20:23], v[232:235], v[212:215]\n\t" \
    "v_mfma_f32_16x16x32_bf16 v[216:219], v[24:27], v[228:231], v[216:219]\n\t" \
    "v_mfma_f32_16x16x32_bf16 v[216:219], v[28:31], v[232:235], v[216:219]\n\t" \
    "v_mfma_f32_16x16x32_bf16 v[220:223], v[32:35], v[228:231], v[220:223]\n\t" \
    "ds_read_b128 v[228:231], v252 offset:1024\n\t" \
    "v_mfma_f32_16x16x32_bf16 v[220:223], v[36:39], v[232:235], v[220:223]\n\t" \
    "v_xor_b32 v252, 0x1800, %[xa]\n\t" \
    "ds_read_b128 v[232:235], v252 offset:0\n\t" \
    "s_waitcnt lgkmcnt(4)\n\t" \
    "v_mfma_f32_16x16x32_bf16 v[220:223], v[48:51], v[236:239], v[220:223]\n\t" \
    "s_waitcnt lgkmcnt(3)\n\t" \
    "v_mfma_f32_16x16x32_bf16 v[220:223], v[52:55], v[240:243], v[220:223]\n\t" \
    "v_mfma_f32_16x16x32_bf16 v[216:219], v[40:43], v[236:239], v[216:219]\n\t" \
    "v_mfma_f32_16x16x32_bf16 v[216:219], v[44:47], v[240:243], v[216:219]\n\t" \
    "v_mfma_f32_16x16x32_bf16 v[212:215], v[244:247], v[236:239], v[212:215]\n\t" \
    "ds_read_b128 v[236:239], v252 offset:1024\n\t" \
    "v_mfma_f32_16x16x32_bf16 v[212:215], v[248:251], v[240:243], v[212:215]\n\t" \
    "ds_read_b128 v[240:243], v252 offset:16384\n\t" \
    "s_waitcnt lgkmcnt(4)\n\t" \
    "v_mfma_f32_16x16x32_bf16 v[200:203], v[244:247], v[224:227], v[200:203]\n\t" \
    "ds_read_b128 v[244:247], %[wl] offset:8192\n\t" \
    "s_waitcnt lgkmcnt(4)\n\t" \
    "v_mfma_f32_16x16x32_bf16 v[200:203], v[248:251], v[228:231], v[200:203]\n\t" \
    "ds_read_b128 v[248:251], %[wl] offset:9216\n\t" \
    "v_mfma_f32_16x16x32_bf16 v[204:207], v[40:43], v[224:227], v[204:207]\n\t" \
    "v_mfma_f32_16x16x32_bf16 v[204:207], v[44:47], v[228:231], v[204:207]\n\t" \
    "v_mfma_f32_16x16x32_bf16 v[208:211], v[48:51], v[224:227], v[208:211]\n\t" \
    "ds_read_b128 v[224:227], v252 offset:17408\n\t" \
    "v_mfma_f32_16x16x32_bf16 v[208:211], v[52:55], v[228:231], v[208:211]\n\t" \
    "v_and_b32 v252, 0x3ff, %[xa]\n\t" \
    "ds_read_b128 v[228:231], v252 offset:45056\n\t" \
    "s_waitcnt lgkmcnt(6)\n\t" \
    "v_mfma_f32_16x16x32_bf16 v[208:211], v[72:75], v[232:235], v[208:211]\n\t" \
    "s_waitcnt lgkmcnt(5)\n\t" \
    "v_mfma_f32_16x16x32_bf16 v[208:211], v[76:79], v[236:239], v[208:211]\n\t" \
    "v_mfma_f32_16x16x32_bf16 v[204:207], v[64:67], v[232:235], v[204:207]\n\t" \
    "v_mfma_f32_16x16x32_bf16 v[204:207], v[68:71], v[236:239], v[204:207]\n\t" \
    "v_mfma_f32_16x16x32_bf16 v[200:203], v[56:59], v[232:235], v[200:203]\n\t" \
    "ds_read_b128 v[232:235], v252 offset:46080\n\t" \
    "v_mfma_f32_16x16x32_bf16 v[200:203], v[60:63], v[236:239], v[200:203]\n\t" \
    "ds_read_b128 v[236:239], v252 offset:40960\n\t" \
    "s_waitcnt lgkmcnt(6)\n\t" \
    "v_mfma_f32_16x16x32_bf16 v[212:215], v[56:59], v[240:243], v[212:215]\n\t" \
    "s_waitcnt lgkmcnt(3)\n\t" \
    "v_mfma_f32_16x16x32_bf16 v[212:215], v[60:63], v[224:227], v[212:215]\n\t" \
    "v_mfma_f32_16x16x32_bf16 v[216:219], v[64:67], v[240:243], v[216:219]\n\t" \
    "v_mfma_f32_16x16x32_bf16 v[216:219], v[68:71], v[224:227], v[216:219]\n\t" \
    "v_mfma_f32_16x16x32_bf16 v[220:223], v[72:75], v[240:243], v[220:223]\n\t" \
    "ds_read_b128 v[240:243], v252 offset:41984\n\t" \
    "v_mfma_f32_16x16x32_bf16 v[220:223], v[76:79], v[224:227], v[220:223]\n\t" \
    "s_waitcnt lgkmcnt(0)\n\t" \
    "s_barrier\n\t" ASM_ST \
    "v_xor_b32 v252, 0x2000, %[xa]\n\t" \
    "ds_read_b128 v[224:227], v252 offset:0\n\t" \
    "v_mfma_f32_16x16x32_bf16 v[220:223], v[168:171], v[228:231], v[220:223]\n\t" \
    "v_mfma_f32_16x16x32_bf16 v[220:223], v[172:175], v[232:235], v[220:223]\n\t" \
    "v_mfma_f32_16x16x32_bf16 v[216:219], v[160:163], v[228:231], v[216:219]\n\t" \
    "v_mfma_f32_16x16x32_bf16 v[216:219], v[164:167], v[232:235], v[216:219]\n\t" \
    "v_mfma_f32_16x16x32_bf16 v[212:215], v[244:247], v[228:231], v[212:215]\n\t" \
    "ds_read_b128 v[228:231], v252 offset:1024\n\t" \
    "v_mfma_f32_16x16x32_bf16 v[212:215], v[248:251], v[232:235], v[212:215]\n\t" \
    "ds_read_b128 v[232:235], v252 offset:16384\n\t" \
    "v_mfma_f32_16x16x32_bf16 v[200:203], v[244:247], v[236:239], v[200:203]\n\t" \
    "ds_read_b128 v[244:247], %[wl] offset:4096\n\t" \
    "v_mfma_f32_16x16x32_bf16 v[200:203], v[248:251], v[240:243], v[200:203]\n\t" \
    "ds_read_b128 v[248:251], %[wl] offset:5120\n\t" \
    "v_mfma_f32_16x16x32_bf16 v[204:207], v[160:163], v[236:239], v[204:207]\n\t" \
    "v_mfma_f32_16x16x32_bf16 v[204:207], v[164:167], v[240:243], v[204:207]\n\t" \
    "v_mfma_f32_16x16x32_bf16 v[208:211], v[168:171], v[236:239], v[208:211]\n\t" \
    "ds_read_b128 v[236:239], v252 offset:17408\n\t" \
    "v_mfma_f32_16x16x32_bf16 v[208:211], v[172:175], v[240:243], v[208:211]\n\t" \
    "v_xor_b32 v252, 0x2800, %[xa]\n\t" \
    "ds_read_b128 v[240:243], v252 offset:16384\n\t" \
    "s_waitcnt lgkmcnt(6)\n\t" \
    "v_mfma_f32_16x16x32_bf16 v[208:211], v[88:91], v[224:227], v[208:211]\n\t" \
    "s_waitcnt lgkmcnt(5)\n\t" \
    "v_mfma_f32_16x16x32_bf16 v[208:211], v[92:95], v[228:231], v[208:211]\n\t" \
    "v_mfma_f32_16x16x32_bf16 v[204:207], v[80:83], v[224:227], v[204:207]\n\t" \
    "v_mfma_f32_16x16x32_bf16 v[204:207], v[84:87], v[228:231], v[204:207]\n\t" \
    "s_waitcnt lgkmcnt(3)\n\t" \
    "v_mfma_f32_16x16x32_bf16 v[200:203], v[244:247], v[224:227], v[200:203]\n\t" \
    "ds_read_b128 v[224:227], v252 offset:17408\n\t" \
    "s_waitcnt lgkmcnt(3)\n\t" \
    "v_mfma_f32_16x16x32_bf16 v[200:203], v[248:251], v[228:231], v[200:203]\n\t" \
    "ds_read_b128 v[228:231], v252 offset:0\n\t" \
    "v_mfma_f32_16x16x32_bf16 v[212:215], v[244:247], v[232:235], v[212:215]\n\t" \
    "ds_read_b128 v[244:247], %[wl] offset:6144\n\t" \
    "s_waitcnt lgkmcnt(4)\n\t" \
    "v_mfma_f32_16x16x32_bf16 v[212:215], v[248:251], v[236:239], v[212:215]\n\t" \
    "ds_read_b128 v[248:251], %[wl] offset:7168\n\t" \
    "v_mfma_f32_16x16x32_bf16 v[216:219], v[80:83], v[232:235], v[216:219]\n\t" \
    "v_mfma_f32_16x16x32_bf16 v[216:219], v[84:87], v[236:239], v[216:219]\n\t" \
    "v_mfma_f32_16x16x32_bf16 v[220:223], v[88:91], v[232:235], v[220:223]\n\t" \
    "ds_read_b128 v[232:235], v252 offset:1024\n\t" \
    "v_mfma_f32_16x16x32_bf16 v[220:223], v[92:95], v[236:239], v[220:223]\n\t" \
    "v_xor_b32 v252, 0x3000, %[xa]\n\t" \
    "ds_read_b128 v[236:239], v252 offset:0\n\t" \
    "s_waitcnt lgkmcnt(6)\n\t" \
    "v_mfma_f32_16x16x32_bf16 v[220:223], v[112:115], v[240:243], v[220:223]\n\t" \
    "s_waitcnt lgkmcnt(5)\n\t" \
    "v_mfma_f32_16x16x32_bf16 v[220:223], v[116:119], v[224:227], v[220:223]\n\t" \
    "v_mfma_f32_16x16x32_bf16 v[216:219], v[104:107], v[240:243], v[216:219]\n\t" \
    "v_mfma_f32_16x16x32_bf16 v[216:219], v[108:111], v[224:227], v[216:219]\n\t" \
    "v_mfma_f32_16x16x32_bf16 v[212:215], v[96:99], v[240:243], v[212:215]\n\t" \
    "ds_read_b128 v[240:243], v252 offset:1024\n\t" \
    "v_mfma_f32_16x16x32_bf16 v[212:215], v[100:103], v[224:227], v[212:215]\n\t" \
    "ds_read_b128 v[224:227], v252 offset:16384\n\t" \
    "s_waitcnt lgkmcnt(6)\n\t" \
    "v_mfma_f32_16x16x32_bf16 v[200:203], v[96:99], v[228:231], v[200:203]\n\t" \
    "s_waitcnt lgkmcnt(3)\n\t" \
    "v_mfma_f32_16x16x32_bf16 v[200:203], v[100:103], v[232:235], v[200:203]\n\t" \
    "v_mfma_f32_16x16x32_bf16 v[204:207], v[104:107], v[228:231], v[204:207]\n\t" \
    "v_mfma_f32_16x16x32_bf16 v[204:207], v[108:111], v[232:235], v[204:207]\n\t" \
    "v_mfma_f32_16x16x32_bf16 v[208:211], v[112:115], v[228:231], v[208:211]\n\t" \
    "ds_read_b128 v[228:231], v252 offset:17408\n\t" \
    "v_mfma_f32_16x16x32_bf16 v[208:211], v[116:119], v[232:235], v[208:211]\n\t" \
    "v_xor_b32 v252, 0x3800, %[xa]\n\t" \
    "ds_read_b128 v[232:235], v252 offset:16384\n\t" \
    "s_waitcnt lgkmcnt(4)\n\t" \
    "v_mfma_f32_16x16x32_bf16 v[208:211], v[128:131], v[236:239], v[208:211]\n\t" \
    "s_waitcnt lgkmcnt(3)\n\t" \
    "v_mfma_f32_16x16x32_bf16 v[208:211], v[132:135], v[240:243], v[208:211]\n\t" \
    "v_mfma_f32_16x16x32_bf16 v[204:207], v[120:123], v[236:239], v[204:207]\n\t" \
    "v_mfma_f32_16x16x32_bf16 v[204:207], v[124:127], v[240:243], v[204:207]\n\t" \
    "v_mfma_f32_16x16x32_bf16 v[200:203], v[244:247], v[236:239], v[200:203]\n\t" \
    "ds_read_b128 v[236:239], v252 offset:17408\n\t" \
    "v_mfma_f32_16x16x32_bf16 v[200:203], v[248:251], v[240:243], v[200:203]\n\t" \
    "ds_read_b128 v[240:243], v252 offset:0\n\t" \
    "s_waitcnt lgkmcnt(4)\n\t" \
    "v_mfma_f32_16x16x32_bf16 v[212:215], v[244:247], v[224:227], v[212:215]\n\t" \
    "s_waitcnt lgkmcnt(3)\n\t" \
    "v_mfma_f32_16x16x32_bf16 v[212:215], v[248:251], v[228:231], v[212:215]\n\t" \
    "v_mfma_f32_16x16x32_bf16 v[216:219], v[120:123], v[224:227], v[216:219]\n\t" \
    "v_mfma_f32_16x16x32_bf16 v[216:219], v[124:127], v[228:231], v[216:219]\n\t" \
    "v_mfma_f32_16x16x32_bf16 v[220:223], v[128:131], v[224:227], v[220:223]\n\t" \
    "ds_read_b128 v[224:227], v252 offset:1024\n\t" \
    "v_mfma_f32_16x16x32_bf16 v[220:223], v[132:135], v[228:231], v[220:223]\n\t" \
    "v_and_b32 v252, 0x3ff, %[xa]\n\t" \
    "ds_read_b128 v[228:231], v252 offset:43008\n\t" \
    "s_waitcnt lgkmcnt(4)\n\t" \
    "v_mfma_f32_16x16x32_bf16 v[220:223], v[152:155], v[232:235], v[220:223]\n\t" \
    "s_waitcnt lgkmcnt(3)\n\t" \
    "v_mfma_f32_16x16x32_bf16 v[220:223], v[156:159], v[236:239], v[220:223]\n\t" \
    "v_mfma_f32_16x16x32_bf16 v[216:219], v[144:147], v[232:235], v[216:219]\n\t" \
    "v_mfma_f32_16x16x32_bf16 v[216:219], v[148:151], v[236:239], v[216:219]\n\t" \
    "v_mfma_f32_16x16x32_bf16 v[212:215], v[136:139], v[232:235], v[212:215]\n\t" \
    "ds_read_b128 v[232:235], v252 offset:44032\n\t" \
    "v_mfma_f32_16x16x32_bf16 v[212:215], v[140:143], v[236:239], v[212:215]\n\t" \
    "ds_read_b128 v[236:239], v252 offset:47104\n\t" \
    "s_waitcnt lgkmcnt(4)\n\t" \
    "v_mfma_f32_16x16x32_bf16 v[200:203], v[136:139], v[240:243], v[200:203]\n\t" \
    "s_waitcnt lgkmcnt(3)\n\t" \
    "v_mfma_f32_16x16x32_bf16 v[200:203], v[140:143], v[224:227], v[200:203]\n\t" \
    "v_mfma_f32_16x16x32_bf16 v[204:207], v[144:147], v[240:243], v[204:207]\n\t" \
    "v_mfma_f32_16x16x32_bf16 v[204:207], v[148:151], v[224:227], v[204:207]\n\t" \
    "v_mfma_f32_16x16x32_bf16 v[208:211], v[152:155], v[240:243], v[208:211]\n\t" \
    "ds_read_b128 v[240:243], v252 offset:48128\n\t" \
    "v_mfma_f32_16x16x32_bf16 v[208:211], v[156:159], v[224:227], v[208:211]\n\t" \
    "s_waitcnt lgkmcnt(3)\n\t" \
    "v_mfma_f32_16x16x32_bf16 v[208:211], v[192:195], v[228:231], v[208:211]\n\t" \
    "v_add_u32 v252, %[qs], v252\n\t" \
    "s_waitcnt lgkmcnt(2)\n\t" \
    "v_mfma_f32_16x16x32_bf16 v[208:211], v[196:199], v[232:235], v[208:211]\n\t" \
    "v_mfma_f32_16x16x32_bf16 v[204:207], v[184:187], v[228:231], v[204:207]\n\t" \
    "v_mfma_f32_16x16x32_bf16 v[204:207], v[188:191], v[232:235], v[204:207]\n\t" \
    "v_mfma_f32_16x16x32_bf16 v[200:203], v[176:179], v[228:231], v[200:203]\n\t" \
    "v_mfma_f32_16x16x32_bf16 v[200:203], v[180:183], v[232:235], v[200:203]\n\t" \
    "s_waitcnt lgkmcnt(1)\n\t" \
    "v_mfma_f32_16x16x32_bf16 v[212:215], v[176:179], v[236:239], v[212:215]\n\t" \
    "v_cvt_pk_bf16_f32 v208, v208, v209\n\t" \
    "v_cvt_pk_bf16_f32 v209, v210, v211\n\t" \
    "s_waitcnt lgkmcnt(0)\n\t" \
    "v_mfma_f32_16x16x32_bf16 v[212:215], v[180:183], v[240:243], v[212:215]\n\t" \
    "ds_write_b64 v252, v[208:209] offset:2048\n\t" \
    "v_cvt_pk_bf16_f32 v204, v204, v205\n\t" \
    "v_mfma_f32_16x16x32_bf16 v[216:219], v[184:187], v[236:239], v[216:219]\n\t" \
    "v_cvt_pk_bf16_f32 v205, v206, v207\n\t" \
    "ds_write_b64 v252, v[204:205] offset:1024\n\t" \
    "v_mfma_f32_16x16x32_bf16 v[216:219], v[188:191], v[240:243], v[216:219]\n\t" \
    "v_cvt_pk_bf16_f32 v200, v200, v201\n\t" \
    "v_cvt_pk_bf16_f32 v201, v202, v203\n\t" \
    "v_mfma_f32_16x16x32_bf16 v[220:223], v[192:195], v[236:239], v[220:223]\n\t" \
    "ds_write_b64 v252, v[200:201] offset:0\n\t" \
    "v_mfma_f32_16x16x32_bf16 v[220:223], v[196:199], v[240:243], v[220:223]\n\t" \
    "v_cvt_pk_bf16_f32 v212, v212, v213\n\t" \
    "v_cvt_pk_bf16_f32 v213, v214, v215\n\t" \
    "ds_write_b64 v252, v[212:213] offset:6144\n\t" \
    "v_cvt_pk_bf16_f32 v216, v216, v217\n\t" \
    "v_cvt_pk_bf16_f32 v217, v218, v219\n\t" \
    "ds_write_b64 v252, v[216:217] offset:7168\n\t" \
    "s_nop 1\n\t" \
    "v_cvt_pk_bf16_f32 v220, v220, v221\n\t" \
    "v_cvt_pk_bf16_f32 v221, v222, v223\n\t" \
    "ds_write_b64 v252, v[220:221] offset:8192\n\t" \
    "s_waitcnt lgkmcnt(0)\n\t"
#define ASM_XG_H_HI \
    "s_waitcnt lgkmcnt(0)\n\t" \
    "v_xor_b32 v252, 0x2000, %[xa]\n\t" \
    "ds_read_b128 v[224:227], v252 offset:16384\n\t" \
    "ds_read_b128 v[228:231], v252 offset:17408\n\t" \
    "ds_read_b128 v[244:247], %[wl] offset:4096\n\t" \
    "ds_read_b128 v[248:251], %[wl] offset:5120\n\t" \
    "ds_read_b128 v[232:235], v252 offset:0\n\t" \
    "ds_read_b128 v[236:239], v252 offset:1024\n\t" \
    "v_xor_b32 v252, 0x2800, %[xa]\n\t" \
    "ds_read_b128 v[240:243], v252 offset:0\n\t" \
    "s_waitcnt lgkmcnt(6)\n\t" \
    "v_mfma_f32_16x16x32_bf16 v[220:223], v[88:91], v[224:227], 0\n\t" \
    "s_waitcnt lgkmcnt(5)\n\t" \
    "v_mfma_f32_16x16x32_bf16 v[220:223], v[92:95], v[228:231], v[220:223]\n\t" \
    "v_mfma_f32_16x16x32_bf16 v[216:219], v[80:83], v[224:227], 0\n\t" \
    "v_mfma_f32_16x16x32_bf16 v[216:219], v[84:87], v[228:231], v[216:219]\n\t" \
    "s_waitcnt lgkmcnt(4)\n\t" \
    "v_mfma_f32_16x16x32_bf16 v[212:215], v[244:247], v[224:227], 0\n\t" \
    "ds_read_b128 v[224:227], v252 offset:1024\n\t" \
    "s_waitcnt lgkmcnt(4)\n\t" \
    "v_mfma_f32_16x16x32_bf16 v[212:215], v[248:251], v[228:231], v[212:215]\n\t" \
    "ds_read_b128 v[228:231], v252 offset:16384\n\t" \
    "s_waitcnt lgkmcnt(4)\n\t" \
    "v_mfma_f32_16x16x32_bf16 v[200:203], v[244:247], v[232:235], 0\n\t" \
    "ds_read_b128 v[244:247], %[wl] offset:6144\n\t" \
    "s_waitcnt lgkmcnt(4)\n\t" \
    "v_mfma_f32_16x16x32_bf16 v[200:203], v[248:251], v[236:239], v[200:203]\n\t" \
    "ds_read_b128 v[248:251], %[wl] offset:7168\n\t" \
    "v_mfma_f32_16x16x32_bf16 v[204:207], v[80:83], v[232:235], 0\n\t" \
    "v_mfma_f32_16x16x32_bf16 v[204:207], v[84:87], v[236:239], v[204:207]\n\t" \
    "v_mfma_f32_16x16x32_bf16 v[208:211], v[88:91], v[232:235], 0\n\t" \
    "ds_read_b128 v[232:235], v252 offset:17408\n\t" \
    "v_mfma_f32_16x16x32_bf16 v[208:211], v[92:95], v[236:239], v[208:211]\n\t" \
    "v_xor_b32 v252, 0x3000, %[xa]\n\t" \
    "ds_read_b128 v[236:239], v252 offset:16384\n\t" \
    "s_waitcnt lgkmcnt(6)\n\t" \
    "v_mfma_f32_16x16x32_bf16 v[208:211], v[112:115], v[240:243], v[208:211]\n\t" \
    "s_waitcnt lgkmcnt(5)\n\t" \
    "v_mfma_f32_16x16x32_bf16 v[208:211], v[116:119], v[224:227], v[208:211]\n\t" \
    "v_mfma_f32_16x16x32_bf16 v[204:207], v[104:107], v[240:243], v[204:207]\n\t" \
    "v_mfma_f32_16x16x32_bf16 v[204:207], v[108:111], v[224:227], v[204:207]\n\t" \
    "v_mfma_f32_16x16x32_bf16 v[200:203], v[96:99], v[240:243], v[200:203]\n\t" \
    "ds_read_b128 v[240:243], v252 offset:17408\n\t" \
    "v_mfma_f32_16x16x32_bf16 v[200:203], v[100:103], v[224:227], v[200:203]\n\t" \
    "s_nop 3\n\t" \
    "v_mov_b32 v224, %[tv]\n\t" \
    "s_waitcnt vmcnt(5)\n\t" \
    "buffer_store_dword v224, %[fv], %[rr], %[fp] offen sc1\n\t" \
    "ds_read_b128 v[224:227], v252 offset:0\n\t" \
    "s_waitcnt lgkmcnt(6)\n\t" \
    "v_mfma_f32_16x16x32_bf16 v[212:215], v[96:99], v[228:231], v[212:215]\n\t" \
    "s_waitcnt lgkmcnt(3)\n\t" \
    "v_mfma_f32_16x16x32_bf16 v[212:215], v[100:103], v[232:235], v[212:215]\n\t" \
    "v_mfma_f32_16x16x32_bf16 v[216:219], v[104:107], v[228:231], v[216:219]\n\t" \
    "v_mfma_f32_16x16x32_bf16 v[216:219], v[108:111], v[232:235], v[216:219]\n\t" \
    "v_mfma_f32_16x16x32_bf16 v[220:223], v[112:115], v[228:231], v[220:223]\n\t" \
    "ds_read_b128 v[228:231], v252 offset:1024\n\t" \
    "v_mfma_f32_16x16x32_bf16 v[220:223], v[116:119], v[232:235], v[220:223]\n\t" \
    "v_xor_b32 v252, 0x3800, %[xa]\n\t" \
    "ds_read_b128 v[232:235], v252 offset:0\n\t" \
    "s_waitcnt lgkmcnt(4)\n\t" \
    "v_mfma_f32_16x16x32_bf16 v[220:223], v[128:131], v[236:239], v[220:223]\n\t" \
    "s_waitcnt lgkmcnt(3)\n\t" \
    "v_mfma_f32_16x16x32_bf16 v[220:223], v[132:135], v[240:243], v[220:223]\n\t" \
    "v_mfma_f32_16x16x32_bf16 v[216:219], v[120:123], v[236:239], v[216:219]\n\t" \
    "v_mfma_f32_16x16x32_bf16 v[216:219], v[124:127], v[240:243], v[216:219]\n\t" \
    "v_mfma_f32_16x16x32_bf16 v[212:215], v[244:247], v[236:239], v[212:215]\n\t" \
    "ds_read_b128 v[236:239], v252 offset:1024\n\t" \
    "v_mfma_f32_16x16x32_bf16 v[212:215], v[248:251], v[240:243], v[212:215]\n\t" \
    "ds_read_b128 v[240:243], v252 offset:16384\n\t" \
    "s_waitcnt lgkmcnt(4)\n\t" \
    "v_mfma_f32_16x16x32_bf16 v[200:203], v[244:247], v[224:227], v[200:203]\n\t" \
    "ds_read_b128 v[244:247], %[wl] offset:8192\n\t" \
    "s_waitcnt lgkmcnt(4)\n\t" \
    "v_mfma_f32_16x16x32_bf16 v[200:203], v[248:251], v[228:231], v[200:203]\n\t" \
    "ds_read_b128 v[248:251], %[wl] offset:9216\n\t" \
    "v_mfma_f32_16x16x32_bf16 v[204:207], v[120:123], v[224:227], v[204:207]\n\t" \
    "v_mfma_f32_16x16x32_bf16 v[204:207], v[124:127], v[228:231], v[204:207]\n\t" \
    "v_mfma_f32_16x16x32_bf16 v[208:211], v[128:131], v[224:227], v[208:211]\n\t" \
    "ds_read_b128 v[224:227], v252 offset:17408\n\t" \
    "v_mfma_f32_16x16x32_bf16 v[208:211], v[132:135], v[228:231], v[208:211]\n\t" \
    "v_and_b32 v252, 0x3ff, %[xa]\n\t" \
    "ds_read_b128 v[228:231], v252 offset:45056\n\t" \
    "s_waitcnt lgkmcnt(6)\n\t" \
    "v_mfma_f32_16x16x32_bf16 v[208:211], v[152:155], v[232:235], v[208:211]\n\t" \
    "s_waitcnt lgkmcnt(5)\n\t" \
    "v_mfma_f32_16x16x32_bf16 v[208:211], v[156:159], v[236:239], v[208:211]\n\t" \
    "v_mfma_f32_16x16x32_bf16 v[204:207], v[144:147], v[232:235], v[204:207]\n\t" \
    "v_mfma_f32_16x16x32_bf16 v[204:207], v[148:151], v[236:239], v[204:207]\n\t" \
    "v_mfma_f32_16x16x32_bf16 v[200:203], v[136:139], v[232:235], v[200:203]\n\t" \
    "ds_read_b128 v[232:235], v252 offset:46080\n\t" \
    "v_mfma_f32_16x16x32_bf16 v[200:203], v[140:143], v[236:239], v[200:203]\n\t" \
    "ds_read_b128 v[236:239], v252 offset:40960\n\t" \
    "s_waitcnt lgkmcnt(6)\n\t" \
    "v_mfma_f32_16x16x32_bf16 v[212:215], v[136:139], v[240:243], v[212:215]\n\t" \
    "s_waitcnt lgkmcnt(3)\n\t" \
    "v_mfma_f32_16x16x32_bf16 v[212:215], v[140:143], v[224:227], v[212:215]\n\t" \
    "v_mfma_f32_16x16x32_bf16 v[216:219], v[144:147], v[240:243], v[216:219]\n\t" \
    "v_mfma_f32_16x16x32_bf16 v[216:219], v[148:151], v[224:227], v[216:219]\n\t" \
    "v_mfma_f32_16x16x32_bf16 v[220:223], v[152:155], v[240:243], v[220:223]\n\t" \
    "ds_read_b128 v[240:243], v252 offset:41984\n\t" \
    "v_mfma_f32_16x16x32_bf16 v[220:223], v[156:159], v[224:227], v[220:223]\n\t" \
    "s_waitcnt vmcnt(1) lgkmcnt(0)\n\t" \
    "s_barrier\n\t" ASM_ST \
    "ds_read_b128 v[224:227], %[xa] offset:0\n\t" \
    "v_mfma_f32_16x16x32_bf16 v[220:223], v[168:171], v[228:231], v[220:223]\n\t" \
    "v_mfma_f32_16x16x32_bf16 v[220:223], v[172:175], v[232:235], v[220:223]\n\t" \
    "v_mfma_f32_16x16x32_bf16 v[216:219], v[160:163], v[228:231], v[216:219]\n\t" \
    "v_mfma_f32_16x16x32_bf16 v[216:219], v[164:167], v[232:235], v[216:219]\n\t" \
    "v_mfma_f32_16x16x32_bf16 v[212:215], v[244:247], v[228:231], v[212:215]\n\t" \
    "ds_read_b128 v[228:231], %[xa] offset:1024\n\t" \
    "v_mfma_f32_16x16x32_bf16 v[212:215], v[248:251], v[232:235], v[212:215]\n\t" \
    "ds_read_b128 v[232:235], %[xa] offset:16384\n\t" \
    "v_mfma_f32_16x16x32_bf16 v[200:203], v[244:247], v[236:239], v[200:203]\n\t" \
    "ds_read_b128 v[244:247], %[wl] offset:0\n\t" \
    "v_mfma_f32_16x16x32_bf16 v[200:203], v[248:251], v[240:243], v[200:203]\n\t" \
    "ds_read_b128 v[248:251], %[wl] offset:1024\n\t" \
    "v_mfma_f32_16x16x32_bf16 v[204:207], v[160:163], v[236:239], v[204:207]\n\t" \
    "v_mfma_f32_16x16x32_bf16 v[204:207], v[164:167], v[240:243], v[204:207]\n\t" \
    "v_mfma_f32_16x16x32_bf16 v[208:211], v[168:171], v[236:239], v[208:211]\n\t" \
    "ds_read_b128 v[236:239], %[xa] offset:17408\n\t" \
    "v_mfma_f32_16x16x32_bf16 v[208:211], v[172:175], v[240:243], v[208:211]\n\t" \
    "v_xor_b32 v252, 0x800, %[xa]\n\t" \
    "ds_read_b128 v[240:243], v252 offset:16384\n\t" \
    "s_waitcnt lgkmcnt(6)\n\t" \
    "v_mfma_f32_16x16x32_bf16 v[208:211], v[8:11], v[224:227], v[208:211]\n\t" \
    "s_waitcnt lgkmcnt(5)\n\t" \
    "v_mfma_f32_16x16x32_bf16 v[208:211], v[12:15], v[228:231], v[208:211]\n\t" \
    "v_mfma_f32_16x16x32_bf16 v[204:207], v[0:3], v[224:227], v[204:207]\n\t" \
    "v_mfma_f32_16x16x32_bf16 v[204:207], v[4:7], v[228:231], v[204:207]\n\t" \
    "s_waitcnt lgkmcnt(3)\n\t" \
    "v_mfma_f32_16x16x32_bf16 v[200:203], v[244:247], v[224:227], v[200:203]\n\t" \
    "ds_read_b128 v[224:227], v252 offset:17408\n\t" \
    "s_waitcnt lgkmcnt(3)\n\t" \
    "v_mfma_f32_16x16x32_bf16 v[200:203], v[248:251], v[228:231], v[200:203]\n\t" \
    "ds_read_b128 v[228:231], v252 offset:0\n\t" \
    "v_mfma_f32_16x16x32_bf16 v[212:215], v[244:247], v[232:235], v[212:215]\n\t" \
    "ds_read_b128 v[244:247], %[wl] offset:2048\n\t" \
    "s_waitcnt lgkmcnt(4)\n\t" \
    "v_mfma_f32_16x16x32_bf16 v[212:215], v[248:251], v[236:239], v[212:215]\n\t" \
    "ds_read_b128 v[248:251], %[wl] offset:3072\n\t" \
    "v_mfma_f32_16x16x32_bf16 v[216:219], v[0:3], v[232:235], v[216:219]\n\t" \
    "v_mfma_f32_16x16x32_bf16 v[216:219], v[4:7], v[236:239], v[216:219]\n\t" \
    "v_mfma_f32_16x16x32_bf16 v[220:223], v[8:11], v[232:235], v[220:223]\n\t" \
    "ds_read_b128 v[232:235], v252 offset:1024\n\t" \
    "v_mfma_f32_16x16x32_bf16 v[220:223], v[12:15], v[236:239], v[220:223]\n\t" \
    "v_xor_b32 v252, 0x1000, %[xa]\n\t" \
    "ds_read_b128 v[236:239], v252 offset:0\n\t" \
    "s_waitcnt lgkmcnt(6)\n\t" \
    "v_mfma_f32_16x16x32_bf16 v[220:223], v[32:35], v[240:243], v[220:223]\n\t" \
    "s_waitcnt lgkmcnt(5)\n\t" \
    "v_mfma_f32_16x16x32_bf16 v[220:223], v[36:39], v[224:227], v[220:223]\n\t" \
    "v_mfma_f32_16x16x32_bf16 v[216:219], v[24:27], v[240:243], v[216:219]\n\t" \
    "v_mfma_f32_16x16x32_bf16 v[216:219], v[28:31], v[224:227], v[216:219]\n\t" \
    "v_mfma_f32_16x16x32_bf16 v[212:215], v[16:19], v[240:243], v[212:215]\n\t" \
    "ds_read_b128 v[240:243], v252 offset:1024\n\t" \
    "v_mfma_f32_16x16x32_bf16 v[212:215], v[20:23], v[224:227], v[212:215]\n\t" \
    "ds_read_b128 v[224:227], v252 offset:16384\n\t" \
    "s_waitcnt lgkmcnt(6)\n\t" \
    "v_mfma_f32_16x16x32_bf16 v[200:203], v[16:19], v[228:231], v[200:203]\n\t" \
    "s_waitcnt lgkmcnt(3)\n\t" \
    "v_mfma_f32_16x16x32_bf16 v[200:203], v[20:23], v[232:235], v[200:203]\n\t" \
    "v_mfma_f32_16x16x32_bf16 v[204:207], v[24:27], v[228:231], v[204:207]\n\t" \
    "v_mfma_f32_16x16x32_bf16 v[204:207], v[28:31], v[232:235], v[204:207]\n\t" \
    "v_mfma_f32_16x16x32_bf16 v[208:211], v[32:35], v[228:231], v[208:211]\n\t" \
    "ds_read_b128 v[228:231], v252 offset:17408\n\t" \
    "v_mfma_f32_16x16x32_bf16 v[208:211], v[36:39], v[232:235], v[208:211]\n\t" \
    "v_xor_b32 v252, 0x1800, %[xa]\n\t" \
    "ds_read_b128 v[232:235], v252 offset:16384\n\t" \
    "s_waitcnt lgkmcnt(4)\n\t" \
    "v_mfma_f32_16x16x32_bf16 v[208:211], v[48:51], v[236:239], v[208:211]\n\t" \
    "s_waitcnt lgkmcnt(3)\n\t" \
    "v_mfma_f32_16x16x32_bf16 v[208:211], v[52:55], v[240:243], v[208:211]\n\t" \
    "v_mfma_f32_16x16x32_bf16 v[204:207], v[40:43], v[236:239], v[204:207]\n\t" \
    "v_mfma_f32_16x16x32_bf16 v[204:207], v[44:47], v[240:243], v[204:207]\n\t" \
    "v_mfma_f32_16x16x32_bf16 v[200:203], v[244:247], v[236:239], v[200:203]\n\t" \
    "ds_read_b128 v[236:239], v252 offset:17408\n\t" \
    "v_mfma_f32_16x16x32_bf16 v[200:203], v[248:251], v[240:243], v[200:203]\n\t" \
    "ds_read_b128 v[240:243], v252 offset:0\n\t" \
    "s_waitcnt lgkmcnt(4)\n\t" \
    "v_mfma_f32_16x16x32_bf16 v[212:215], v[244:247], v[224:227], v[212:215]\n\t" \
    "s_waitcnt lgkmcnt(3)\n\t" \
    "v_mfma_f32_16x16x32_bf16 v[212:215], v[248:251], v[228:231], v[212:215]\n\t" \
    "v_mfma_f32_16x16x32_bf16 v[216:219], v[40:43], v[224:227], v[216:219]\n\t" \
    "v_mfma_f32_16x16x32_bf16 v[216:219], v[44:47], v[228:231], v[216:219]\n\t" \
    "v_mfma_f32_16x16x32_bf16 v[220:223], v[48:51], v[224:227], v[220:223]\n\t" \
    "ds_read_b128 v[224:227], v252 offset:1024\n\t" \
    "v_mfma_f32_16x16x32_bf16 v[220:223], v[52:55], v[228:231], v[220:223]\n\t" \
    "v_and_b32 v252, 0x3ff, %[xa]\n\t" \
    "ds_read_b128 v[228:231], v252 offset:43008\n\t" \
    "s_waitcnt lgkmcnt(4)\n\t" \
    "v_mfma_f32_16x16x32_bf16 v[220:223], v[72:75], v[232:235], v[220:223]\n\t" \
    "s_waitcnt lgkmcnt(3)\n\t" \
    "v_mfma_f32_16x16x32_bf16 v[220:223], v[76:79], v[236:239], v[220:223]\n\t" \
    "v_mfma_f32_16x16x32_bf16 v[216:219], v[64:67], v[232:235], v[216:219]\n\t" \
    "v_mfma_f32_16x16x32_bf16 v[216:219], v[68:71], v[236:239], v[216:219]\n\t" \
    "v_mfma_f32_16x16x32_bf16 v[212:215], v[56:59], v[232:235], v[212:215]\n\t" \
    "ds_read_b128 v[232:235], v252 offset:44032\n\t" \
    "v_mfma_f32_16x16x32_bf16 v[212:215], v[60:63], v[236:239], v[212:215]\n\t" \
    "ds_read_b128 v[236:239], v252 offset:47104\n\t" \
    "s_waitcnt lgkmcnt(4)\n\t" \
    "v_mfma_f32_16x16x32_bf16 v[200:203], v[56:59], v[240:243], v[200:203]\n\t" \
    "s_waitcnt lgkmcnt(3)\n\t" \
    "v_mfma_f32_16x16x32_bf16 v[200:203], v[60:63], v[224:227], v[200:203]\n\t" \
    "v_mfma_f32_16x16x32_bf16 v[204:207], v[64:67], v[240:243], v[204:207]\n\t" \
    "v_mfma_f32_16x16x32_bf16 v[204:207], v[68:71], v[224:227], v[204:207]\n\t" \
    "v_mfma_f32_16x16x32_bf16 v[208:211], v[72:75], v[240:243], v[208:211]\n\t" \
    "ds_read_b128 v[240:243], v252 offset:48128\n\t" \
    "v_mfma_f32_16x16x32_bf16 v[208:211], v[76:79], v[224:227], v[208:211]\n\t" \
    "s_waitcnt lgkmcnt(3)\n\t" \
    "v_mfma_f32_16x16x32_bf16 v[208:211], v[192:195], v[228:231], v[208:211]\n\t" \
    "v_add_u32 v252, %[qs], v252\n\t" \
    "s_waitcnt lgkmcnt(2)\n\t" \
    "v_mfma_f32_16x16x32_bf16 v[208:211], v[196:199], v[232:235], v[208:211]\n\t" \
    "v_mfma_f32_16x16x32_bf16 v[204:207], v[184:187], v[228:231], v[204:207]\n\t" \
    "v_mfma_f32_16x16x32_bf16 v[204:207], v[188:191], v[232:235], v[204:207]\n\t" \
    "v_mfma_f32_16x16x32_bf16 v[200:203], v[176:179], v[228:231], v[200:203]\n\t" \
    "v_mfma_f32_16x16x32_bf16 v[200:203], v[180:183], v[232:235], v[200:203]\n\t" \
    "s_waitcnt lgkmcnt(1)\n\t" \
    "v_mfma_f32_16x16x32_bf16 v[212:215], v[176:179], v[236:239], v[212:215]\n\t" \
    "v_cvt_pk_bf16_f32 v208, v208, v209\n\t" \
    "v_cvt_pk_bf16_f32 v209, v210, v211\n\t" \
    "s_waitcnt lgkmcnt(0)\n\t" \
    "v_mfma_f32_16x16x32_bf16 v[212:215], v[180:183], v[240:243], v[212:215]\n\t" \
    "ds_write_b64 v252, v[208:209] offset:2048\n\t" \
    "v_cvt_pk_bf16_f32 v204, v204, v205\n\t" \
    "v_mfma_f32_16x16x32_bf16 v[216:219], v[184:187], v[236:239], v[216:219]\n\t" \
    "v_cvt_pk_bf16_f32 v205, v206, v207\n\t" \
    "ds_write_b64 v252, v[204:205] offset:1024\n\t" \
    "v_mfma_f32_16x16x32_bf16 v[216:219], v[188:191], v[240:243], v[216:219]\n\t" \
    "v_cvt_pk_bf16_f32 v200, v200, v201\n\t" \
    "v_cvt_pk_bf16_f32 v201, v202, v203\n\t" \
    "v_mfma_f32_16x16x32_bf16 v[220:223], v[192:195], v[236:239], v[220:223]\n\t" \
    "ds_write_b64 v252, v[200:201] offset:0\n\t" \
    "v_mfma_f32_16x16x32_bf16 v[220:223], v[196:199], v[240:243], v[220:223]\n\t" \
    "v_cvt_pk_bf16_f32 v212, v212, v213\n\t" \
    "v_cvt_pk_bf16_f32 v213, v214, v215\n\t" \
    "ds_write_b64 v252, v[212:213] offset:6144\n\t" \
    "v_cvt_pk_bf16_f32 v216, v216, v217\n\t" \
    "v_cvt_pk_bf16_f32 v217, v218, v219\n\t" \
    "ds_write_b64 v252, v[216:217] offset:7168\n\t" \
    "s_nop 1\n\t" \
    "v_cvt_pk_bf16_f32 v220, v220, v221\n\t" \
    "v_cvt_pk_bf16_f32 v221, v222, v223\n\t" \
    "ds_write_b64 v252, v[220:221] offset:8192\n\t" \
    "s_waitcnt lgkmcnt(0)\n\t"
#define ASM_CRIT1_3 \
    "s_branch .Lc0%=\n\t" \
    ".Lcr%=:\n\t" \
    "s_cmp_eq_u32 %[n], 0\n\t" \
    "s_cbranch_scc1 .Lcb%=\n\t" \
    "s_sub_u32 %[n], %[n], 1\n\t" \
    "v_max_u32 v243, v208, v209\n\t" \
    "v_max3_u32 v243, v210, v211, v243\n\t" \
    "v_cmp_eq_u32 vcc, -1, v243\n\t" \
    "s_cbranch_vccz .Lcs0_%=\n\t" \
    "buffer_load_dwordx4 v[208:211], %[ab], %[rs], %[s0] offen sc1\n\t" \
    ".Lcs0_%=:\n\t" \
    "v_max_u32 v243, v236, v237\n\t" \
    "v_max3_u32 v243, v238, v239, v243\n\t" \
    "v_cmp_eq_u32 vcc, -1, v243\n\t" \
    "s_cbranch_vccz .Lcs1_%=\n\t" \
    "buffer_load_dwordx4 v[236:239], %[ab], %[rs], %[s1] offen sc1\n\t" \
    ".Lcs1_%=:\n\t" \
    "v_max_u32 v243, v220, v221\n\t" \
    "v_max3_u32 v243, v222, v223, v243\n\t" \
    "v_cmp_eq_u32 vcc, -1, v243\n\t" \
    "s_cbranch_vccz .Lca%=\n\t" \
    "buffer_load_dwordx4 v[220:223], %[ab], %[rs], %[s2] offen sc1\n\t" \
    "s_branch .Lca%=\n\t" \
    ".Lc0%=:\n\t" \
    "buffer_load_dwordx4 v[208:211], %[ab], %[rs], %[s0] offen sc1\n\t" \
    "buffer_load_dwordx4 v[236:239], %[ab], %[rs], %[s1] offen sc1\n\t" \
    "buffer_load_dwordx4 v[220:223], %[ab], %[rs], %[s2] offen sc1\n\t" \
    "v_bfe_i32 v241, %[ab], 7, 1\n\t" \
    "v_and_b32 v241, 0xaaaa, v241\n\t" \
    "v_or_b32 v241, 0x4444, v241\n\t" \
    "v_xor_b32 v212, 0x400, %[ab]\n\t" \
    "v_xor_b32 v216, 0x800, %[ab]\n\t" \
    "v_xor_b32 v242, 0xc00, %[ab]\n\t" \
    "v_xor_b32 v240, 0x1400, %[ab]\n\t" \
    ".Lca%=:\n\t" \
    "s_waitcnt vmcnt(0)\n\t" \
    "ds_write_b128 %[ab], v[208:211] offset:0\n\t" \
    "ds_write_b128 %[ab], v[236:239] offset:4096\n\t" \
    "ds_write_b128 %[ab], v[220:223] offset:8192\n\t" \
    "v_max_u32 v243, v208, v209\n\t" \
    "v_max3_u32 v243, v210, v211, v243\n\t" \
    "v_max3_u32 v243, v236, v237, v243\n\t" \
    "v_max3_u32 v243, v238, v239, v243\n\t" \
    "v_max3_u32 v243, v220, v221, v243\n\t" \
    "v_max3_u32 v243, v222, v223, v243\n\t" \
    "v_cmp_eq_u32 vcc, -1, v243\n\t" \
    "s_cbranch_vccnz .Lcr%=\n\t" \
    ".Lcb%=:\n\t" \
    PRIUP \
    ASM_ST2 \
    "v_smfmac_f32_16x16x64_bf16 v[224:227], v[208:211], v[200:207], v241\n\t" \
    "ds_read_b128 v[200:203], %[wl] offset:4096\n\t" \
    "ds_read_b128 v[204:207], %[wl] offset:5120\n\t" \
    "v_smfmac_f32_16x16x64_bf16 v[244:247], v[208:211], v[0:7], v241\n\t" \
    "v_smfmac_f32_16x16x64_bf16 v[248:251], v[208:211], v[8:15], v241\n\t" \
    "s_waitcnt lgkmcnt(2)\n\t" \
    "s_barrier\n\t" ASM_ST \
    "ds_read_b128 v[212:215], v212 offset:0\n\t" \
    "ds_read_b128 v[216:219], v216 offset:0\n\t" \
    "ds_read_b128 v[220:223], v242 offset:0\n\t" \
    "ds_read_b128 v[208:211], v240 offset:0\n\t" \
    "v_smfmac_f32_16x16x64_bf16 v[244:247], v[236:239], v[80:87], v241\n\t" \
    "v_xor_b32 v240, 0x1800, %[ab]\n\t" \
    "v_smfmac_f32_16x16x64_bf16 v[248:251], v[236:239], v[88:95], v241\n\t" \
    "s_waitcnt lgkmcnt(4)\n\t" \
    "v_smfmac_f32_16x16x64_bf16 v[224:227], v[236:239], v[200:207], v241\n\t" \
    "ds_read_b128 v[200:203], %[wl] offset:6144\n\t" \
    "ds_read_b128 v[204:207], %[wl] offset:7168\n\t" \
    "ds_read_b128 v[236:239], v240 offset:0\n\t" \
    "s_waitcnt lgkmcnt(6)\n\t" \
    "v_smfmac_f32_16x16x64_bf16 v[224:227], v[212:215], v[16:23], v241\n\t" \
    "v_smfmac_f32_16x16x64_bf16 v[244:247], v[212:215], v[24:31], v241\n\t" \
    "v_xor_b32 v240, 0x1c00, %[ab]\n\t" \
    "v_smfmac_f32_16x16x64_bf16 v[248:251], v[212:215], v[32:39], v241\n\t" \
    "ds_read_b128 v[212:215], v240 offset:0\n\t" \
    "s_waitcnt lgkmcnt(6)\n\t" \
    "v_smfmac_f32_16x16x64_bf16 v[224:227], v[216:219], v[228:235], v241\n\t" \
    "ds_read_b128 v[228:231], %[wl] offset:8192\n\t" \
    "ds_read_b128 v[232:235], %[wl] offset:9216\n\t" \
    "v_smfmac_f32_16x16x64_bf16 v[244:247], v[216:219], v[40:47], v241\n\t" \
    "v_and_b32 v240, 0x3ff, %[ab]\n\t" \
    "v_smfmac_f32_16x16x64_bf16 v[248:251], v[216:219], v[48:55], v241\n\t" \
    "ds_read_b128 v[216:219], v240 offset:8192\n\t" \
    "s_waitcnt lgkmcnt(8)\n\t" \
    "v_smfmac_f32_16x16x64_bf16 v[224:227], v[220:223], v[56:63], v241\n\t" \
    "v_smfmac_f32_16x16x64_bf16 v[244:247], v[220:223], v[64:71], v241\n\t" \
    "v_smfmac_f32_16x16x64_bf16 v[248:251], v[220:223], v[72:79], v241\n\t" \
    "ds_read_b128 v[220:223], v240 offset:9216\n\t" \
    "s_waitcnt lgkmcnt(8)\n\t" \
    "v_smfmac_f32_16x16x64_bf16 v[244:247], v[208:211], v[104:111], v241\n\t" \
    "s_waitcnt lgkmcnt(5)\n\t" \
    "v_smfmac_f32_16x16x64_bf16 v[244:247], v[236:239], v[120:127], v241\n\t" \
    "s_waitcnt lgkmcnt(4)\n\t" \
    "v_smfmac_f32_16x16x64_bf16 v[244:247], v[212:215], v[144:151], v241\n\t" \
    "s_waitcnt lgkmcnt(1)\n\t" \
    "v_smfmac_f32_16x16x64_bf16 v[244:247], v[216:219], v[160:167], v241\n\t" \
    "s_waitcnt lgkmcnt(0)\n\t" \
    "v_smfmac_f32_16x16x64_bf16 v[244:247], v[220:223], v[184:191], v241\n\t" \
    "v_smfmac_f32_16x16x64_bf16 v[248:251], v[208:211], v[112:119], v241\n\t" \
    "v_smfmac_f32_16x16x64_bf16 v[248:251], v[236:239], v[128:135], v241\n\t" \
    "v_smfmac_f32_16x16x64_bf16 v[248:251], v[212:215], v[152:159], v241\n\t" \
    "v_permlane32_swap_b32 v244, v246\n\t" \
    "v_permlane32_swap_b32 v245, v247\n\t" \
    "v_smfmac_f32_16x16x64_bf16 v[248:251], v[216:219], v[168:175], v241\n\t" \
    "v_add_f32 v244, v244, v246\n\t" \
    "v_add_f32 v245, v245, v247\n\t" \
    "v_smfmac_f32_16x16x64_bf16 v[248:251], v[220:223], v[192:199], v241\n\t" \
    "v_cvt_pk_bf16_f32 v244, v244, v245\n\t" \
    "ds_write_b16 %[dst], v244 offset:512\n\t" \
    "ds_write_b16_d16_hi %[dst], v244 offset:528\n\t" \
    "v_smfmac_f32_16x16x64_bf16 v[224:227], v[208:211], v[96:103], v241\n\t" \
    "v_smfmac_f32_16x16x64_bf16 v[224:227], v[236:239], v[200:207], v241\n\t" \
    "v_smfmac_f32_16x16x64_bf16 v[224:227], v[212:215], v[136:143], v241\n\t" \
    "v_permlane32_swap_b32 v248, v250\n\t" \
    "v_permlane32_swap_b32 v249, v251\n\t" \
    "v_smfmac_f32_16x16x64_bf16 v[224:227], v[216:219], v[228:235], v241\n\t" \
    "v_add_f32 v248, v248, v250\n\t" \
    "v_add_f32 v249, v249, v251\n\t" \
    "v_smfmac_f32_16x16x64_bf16 v[224:227], v[220:223], v[176:183], v241\n\t" \
    "v_cvt_pk_bf16_f32 v248, v248, v249\n\t" \
    "ds_write_b16 %[dst], v248 offset:1024\n\t" \
    "ds_write_b16_d16_hi %[dst], v248 offset:1040\n\t" \
    "s_nop 4\n\t" \
    "v_permlane32_swap_b32 v224, v226\n\t" \
    "v_permlane32_swap_b32 v225, v227\n\t" \
    "v_add_f32 v224, v224, v226\n\t" \
    "v_add_f32 v225, v225, v227\n\t" \
    "v_cvt_pk_bf16_f32 v224, v224, v225\n\t" \
    "ds_write_b16 %[dst], v224 offset:0\n\t" \
    "ds_write_b16_d16_hi %[dst], v224 offset:16\n\t"
#define ASM_CRIT1_2 \
    "s_branch .Lc0%=\n\t" \
    ".Lcr%=:\n\t" \
    "s_cmp_eq_u32 %[n], 0\n\t" \
    "s_cbranch_scc1 .Lcb%=\n\t" \
    "s_sub_u32 %[n], %[n], 1\n\t" \
    "v_max_u32 v243, v208, v209\n\t" \
    "v_max3_u32 v243, v210, v211, v243\n\t" \
    "v_cmp_eq_u32 vcc, -1, v243\n\t" \
    "s_cbranch_vccz .Lcs0_%=\n\t" \
    "buffer_load_dwordx4 v[208:211], %[ab], %[rs], %[s0] offen sc1\n\t" \
    ".Lcs0_%=:\n\t" \
    "v_max_u32 v243, v236, v237\n\t" \
    "v_max3_u32 v243, v238, v239, v243\n\t" \
    "v_cmp_eq_u32 vcc, -1, v243\n\t" \
    "s_cbranch_vccz .Lca%=\n\t" \
    "buffer_load_dwordx4 v[236:239], %[ab], %[rs], %[s1] offen sc1\n\t" \
    "s_branch .Lca%=\n\t" \
    ".Lc0%=:\n\t" \
    "buffer_load_dwordx4 v[208:211], %[ab], %[rs], %[s0] offen sc1\n\t" \
    "buffer_load_dwordx4 v[236:239], %[ab], %[rs], %[s1] offen sc1\n\t" \
    "v_bfe_i32 v241, %[ab], 7, 1\n\t" \
    "v_and_b32 v241, 0xaaaa, v241\n\t" \
    "v_or_b32 v241, 0x4444, v241\n\t" \
    "v_xor_b32 v212, 0x400, %[ab]\n\t" \
    "v_xor_b32 v216, 0x800, %[ab]\n\t" \
    "v_xor_b32 v242, 0xc00, %[ab]\n\t" \
    "v_xor_b32 v240, 0x1400, %[ab]\n\t" \
    ".Lca%=:\n\t" \
    "s_waitcnt vmcnt(0)\n\t" \
    "ds_write_b128 %[ab], v[208:211] offset:0\n\t" \
    "ds_write_b128 %[ab], v[236:239] offset:4096\n\t" \
    "v_max_u32 v243, v208, v209\n\t" \
    "v_max3_u32 v243, v210, v211, v243\n\t" \
    "v_max3_u32 v243, v236, v237, v243\n\t" \
    "v_max3_u32 v243, v238, v239, v243\n\t" \
    "v_cmp_eq_u32 vcc, -1, v243\n\t" \
    "s_cbranch_vccnz .Lcr%=\n\t" \
    ".Lcb%=:\n\t" \
    PRIUP \
    ASM_ST2 \
    "v_smfmac_f32_16x16x64_bf16 v[224:227], v[208:211], v[200:207], v241\n\t" \
    "ds_read_b128 v[200:203], %[wl] offset:4096\n\t" \
    "ds_read_b128 v[204:207], %[wl] offset:5120\n\t" \
    "v_smfmac_f32_16x16x64_bf16 v[244:247], v[208:211], v[0:7], v241\n\t" \
    "v_smfmac_f32_16x16x64_bf16 v[248:251], v[208:211], v[8:15], v241\n\t" \
    "s_waitcnt lgkmcnt(2)\n\t" \
    "s_barrier\n\t" ASM_ST \
    "ds_read_b128 v[212:215], v212 offset:0\n\t" \
    "ds_read_b128 v[216:219], v216 offset:0\n\t" \
    "ds_read_b128 v[220:223], v242 offset:0\n\t" \
    "ds_read_b128 v[208:211], v240 offset:0\n\t" \
    "v_smfmac_f32_16x16x64_bf16 v[244:247], v[236:239], v[80:87], v241\n\t" \
    "v_xor_b32 v240, 0x1800, %[ab]\n\t" \
    "v_smfmac_f32_16x16x64_bf16 v[248:251], v[236:239], v[88:95], v241\n\t" \
    "s_waitcnt lgkmcnt(4)\n\t" \
    "v_smfmac_f32_16x16x64_bf16 v[224:227], v[236:239], v[200:207], v241\n\t" \
    "ds_read_b128 v[200:203], %[wl] offset:6144\n\t" \
    "ds_read_b128 v[204:207], %[wl] offset:7168\n\t" \
    "ds_read_b128 v[236:239], v240 offset:0\n\t" \
    "s_waitcnt lgkmcnt(6)\n\t" \
    "v_smfmac_f32_16x16x64_bf16 v[224:227], v[212:215], v[16:23], v241\n\t" \
    "v_smfmac_f32_16x16x64_bf16 v[244:247], v[212:215], v[24:31], v241\n\t" \
    "v_xor_b32 v240, 0x1c00, %[ab]\n\t" \
    "v_smfmac_f32_16x16x64_bf16 v[248:251], v[212:215], v[32:39], v241\n\t" \
    "ds_read_b128 v[212:215], v240 offset:0\n\t" \
    "s_waitcnt lgkmcnt(6)\n\t" \
    "v_smfmac_f32_16x16x64_bf16 v[224:227], v[216:219], v[228:235], v241\n\t" \
    "ds_read_b128 v[228:231], %[wl] offset:8192\n\t" \
    "ds_read_b128 v[232:235], %[wl] offset:9216\n\t" \
    "v_smfmac_f32_16x16x64_bf16 v[244:247], v[216:219], v[40:47], v241\n\t" \
    "v_and_b32 v240, 0x3ff, %[ab]\n\t" \
    "v_smfmac_f32_16x16x64_bf16 v[248:251], v[216:219], v[48:55], v241\n\t" \
    "ds_read_b128 v[216:219], v240 offset:8192\n\t" \
    "s_waitcnt lgkmcnt(8)\n\t" \
    "v_smfmac_f32_16x16x64_bf16 v[224:227], v[220:223], v[56:63], v241\n\t" \
    "v_smfmac_f32_16x16x64_bf16 v[244:247], v[220:223], v[64:71], v241\n\t" \
    "v_smfmac_f32_16x16x64_bf16 v[248:251], v[220:223], v[72:79], v241\n\t" \
    "ds_read_b128 v[220:223], v240 offset:9216\n\t" \
    "s_waitcnt lgkmcnt(8)\n\t" \
    "v_smfmac_f32_16x16x64_bf16 v[244:247], v[208:211], v[104:111], v241\n\t" \
    "s_waitcnt lgkmcnt(5)\n\t" \
    "v_smfmac_f32_16x16x64_bf16 v[244:247], v[236:239], v[120:127], v241\n\t" \
    "s_waitcnt lgkmcnt(4)\n\t" \
    "v_smfmac_f32_16x16x64_bf16 v[244:247], v[212:215], v[144:151], v241\n\t" \
    "s_waitcnt lgkmcnt(1)\n\t" \
    "v_smfmac_f32_16x16x64_bf16 v[244:247], v[216:219], v[160:167], v241\n\t" \
    "s_waitcnt lgkmcnt(0)\n\t" \
    "v_smfmac_f32_16x16x64_bf16 v[244:247], v[220:223], v[184:191], v241\n\t" \
    "v_smfmac_f32_16x16x64_bf16 v[248:251], v[208:211], v[112:119], v241\n\t" \
    "v_smfmac_f32_16x16x64_bf16 v[248:251], v[236:239], v[128:135], v241\n\t" \
    "v_smfmac_f32_16x16x64_bf16 v[248:251], v[212:215], v[152:159], v241\n\t" \
    "v_permlane32_swap_b32 v244, v246\n\t" \
    "v_permlane32_swap_b32 v245, v247\n\t" \
    "v_smfmac_f32_16x16x64_bf16 v[248:251], v[216:219], v[168:175], v241\n\t" \
    "v_add_f32 v244, v244, v246\n\t" \
    "v_add_f32 v245, v245, v247\n\t" \
    "v_smfmac_f32_16x16x64_bf16 v[248:251], v[220:223], v[192:199], v241\n\t" \
    "v_cvt_pk_bf16_f32 v244, v244, v245\n\t" \
    "ds_write_b16 %[dst], v244 offset:512\n\t" \
    "ds_write_b16_d16_hi %[dst], v244 offset:528\n\t" \
    "v_smfmac_f32_16x16x64_bf16 v[224:227], v[208:211], v[96:103], v241\n\t" \
    "v_smfmac_f32_16x16x64_bf16 v[224:227], v[236:239], v[200:207], v241\n\t" \
    "v_smfmac_f32_16x16x64_bf16 v[224:227], v[212:215], v[136:143], v241\n\t" \
    "v_permlane32_swap_b32 v248, v250\n\t" \
    "v_permlane32_swap_b32 v249, v251\n\t" \
    "v_smfmac_f32_16x16x64_bf16 v[224:227], v[216:219], v[228:235], v241\n\t" \
    "v_add_f32 v248, v248, v250\n\t" \
    "v_add_f32 v249, v249, v251\n\t" \
    "v_smfmac_f32_16x16x64_bf16 v[224:227], v[220:223], v[176:183], v241\n\t" \
    "v_cvt_pk_bf16_f32 v248, v248, v249\n\t" \
    "ds_write_b16 %[dst], v248 offset:1024\n\t" \
    "ds_write_b16_d16_hi %[dst], v248 offset:1040\n\t" \
    "s_nop 4\n\t" \
    "v_permlane32_swap_b32 v224, v226\n\t" \
    "v_permlane32_swap_b32 v225, v227\n\t" \
    "v_add_f32 v224, v224, v226\n\t" \
    "v_add_f32 v225, v225, v227\n\t" \
    "v_cvt_pk_bf16_f32 v224, v224, v225\n\t" \
    "ds_write_b16 %[dst], v224 offset:0\n\t" \
    "ds_write_b16_d16_hi %[dst], v224 offset:16\n\t"
#define ASM_CRIT1_N \
    "v_bfe_i32 v241, %[ab], 7, 1\n\t" \
    "v_and_b32 v241, 0xaaaa, v241\n\t" \
    "v_or_b32 v241, 0x4444, v241\n\t" \
    "v_xor_b32 v212, 0x400, %[ab]\n\t" \
    "v_xor_b32 v216, 0x800, %[ab]\n\t" \
    "v_xor_b32 v242, 0xc00, %[ab]\n\t" \
    "v_xor_b32 v240, 0x1400, %[ab]\n\t" \
    "v_xor_b32 v236, 0x1000, %[ab]\n\t" \
    "s_waitcnt lgkmcnt(0)\n\t" \
    "s_barrier\n\t" ASM_ST \
    "ds_read_b128 v[208:211], %[ab] offset:0\n\t" \
    "ds_read_b128 v[212:215], v212 offset:0\n\t" \
    "ds_read_b128 v[216:219], v216 offset:0\n\t" \
    "ds_read_b128 v[220:223], v242 offset:0\n\t" \
    "ds_read_b128 v[236:239], v236 offset:0\n\t" \
    "s_waitcnt lgkmcnt(4)\n\t" \
    "v_smfmac_f32_16x16x64_bf16 v[224:227], v[208:211], v[200:207], v241\n\t" \
    "ds_read_b128 v[200:203], %[wl] offset:4096\n\t" \
    "ds_read_b128 v[204:207], %[wl] offset:5120\n\t" \
    "v_smfmac_f32_16x16x64_bf16 v[244:247], v[208:211], v[0:7], v241\n\t" \
    "v_smfmac_f32_16x16x64_bf16 v[248:251], v[208:211], v[8:15], v241\n\t" \
    "ds_read_b128 v[208:211], v240 offset:0\n\t" \
    "s_waitcnt lgkmcnt(6)\n\t" \
    "v_smfmac_f32_16x16x64_bf16 v[224:227], v[212:215], v[16:23], v241\n\t" \
    "v_smfmac_f32_16x16x64_bf16 v[244:247], v[212:215], v[24:31], v241\n\t" \
    "v_xor_b32 v240, 0x1800, %[ab]\n\t" \
    "v_smfmac_f32_16x16x64_bf16 v[248:251], v[212:215], v[32:39], v241\n\t" \
    "ds_read_b128 v[212:215], v240 offset:0\n\t" \
    "s_waitcnt lgkmcnt(6)\n\t" \
    "v_smfmac_f32_16x16x64_bf16 v[224:227], v[216:219], v[228:235], v241\n\t" \
    "ds_read_b128 v[228:231], %[wl] offset:6144\n\t" \
    "ds_read_b128 v[232:235], %[wl] offset:7168\n\t" \
    "v_smfmac_f32_16x16x64_bf16 v[244:247], v[216:219], v[40:47], v241\n\t" \
    "v_xor_b32 v240, 0x1c00, %[ab]\n\t" \
    "v_smfmac_f32_16x16x64_bf16 v[248:251], v[216:219], v[48:55], v241\n\t" \
    "ds_read_b128 v[216:219], v240 offset:0\n\t" \
    "s_waitcnt lgkmcnt(8)\n\t" \
    "v_smfmac_f32_16x16x64_bf16 v[224:227], v[220:223], v[56:63], v241\n\t" \
    "v_smfmac_f32_16x16x64_bf16 v[244:247], v[220:223], v[64:71], v241\n\t" \
    "v_and_b32 v240, 0x3ff, %[ab]\n\t" \
    "v_smfmac_f32_16x16x64_bf16 v[248:251], v[220:223], v[72:79], v241\n\t" \
    "ds_read_b128 v[220:223], v240 offset:8192\n\t" \
    "s_waitcnt lgkmcnt(6)\n\t" \
    "v_smfmac_f32_16x16x64_bf16 v[224:227], v[236:239], v[200:207], v241\n\t" \
    "ds_read_b128 v[200:203], %[wl] offset:8192\n\t" \
    "ds_read_b128 v[204:207], %[wl] offset:9216\n\t" \
    "v_smfmac_f32_16x16x64_bf16 v[244:247], v[236:239], v[80:87], v241\n\t" \
    "v_smfmac_f32_16x16x64_bf16 v[248:251], v[236:239], v[88:95], v241\n\t" \
    "ds_read_b128 v[236:239], v240 offset:9216\n\t" \
    "s_waitcnt lgkmcnt(8)\n\t" \
    "v_smfmac_f32_16x16x64_bf16 v[244:247], v[208:211], v[104:111], v241\n\t" \
    "s_waitcnt lgkmcnt(7)\n\t" \
    "v_smfmac_f32_16x16x64_bf16 v[244:247], v[212:215], v[120:127], v241\n\t" \
    "s_waitcnt lgkmcnt(4)\n\t" \
    "v_smfmac_f32_16x16x64_bf16 v[244:247], v[216:219], v[144:151], v241\n\t" \
    "s_waitcnt lgkmcnt(3)\n\t" \
    "v_smfmac_f32_16x16x64_bf16 v[244:247], v[220:223], v[160:167], v241\n\t" \
    "s_waitcnt lgkmcnt(0)\n\t" \
    "v_smfmac_f32_16x16x64_bf16 v[244:247], v[236:239], v[184:191], v241\n\t" \
    "v_smfmac_f32_16x16x64_bf16 v[248:251], v[208:211], v[112:119], v241\n\t" \
    "v_smfmac_f32_16x16x64_bf16 v[248:251], v[212:215], v[128:135], v241\n\t" \
    "v_smfmac_f32_16x16x64_bf16 v[248:251], v[216:219], v[152:159], v241\n\t" \
    "v_permlane32_swap_b32 v244, v246\n\t" \
    "v_permlane32_swap_b32 v245, v247\n\t" \
    "v_smfmac_f32_16x16x64_bf16 v[248:251], v[220:223], v[168:175], v241\n\t" \
    "v_add_f32 v244, v244, v246\n\t" \
    "v_add_f32 v245, v245, v247\n\t" \
    "v_smfmac_f32_16x16x64_bf16 v[248:251], v[236:239], v[192:199], v241\n\t" \
    "v_cvt_pk_bf16_f32 v244, v244, v245\n\t" \
    "ds_write_b16 %[dst], v244 offset:512\n\t" \
    "ds_write_b16_d16_hi %[dst], v244 offset:528\n\t" \
    "v_smfmac_f32_16x16x64_bf16 v[224:227], v[208:211], v[96:103], v241\n\t" \
    "v_smfmac_f32_16x16x64_bf16 v[224:227], v[212:215], v[228:235], v241\n\t" \
    "v_smfmac_f32_16x16x64_bf16 v[224:227], v[216:219], v[136:143], v241\n\t" \
    "v_permlane32_swap_b32 v248, v250\n\t" \
    "v_permlane32_swap_b32 v249, v251\n\t" \
    "v_smfmac_f32_16x16x64_bf16 v[224:227], v[220:223], v[200:207], v241\n\t" \
    "v_add_f32 v248, v248, v250\n\t" \
    "v_add_f32 v249, v249, v251\n\t" \
    "v_smfmac_f32_16x16x64_bf16 v[224:227], v[236:239], v[176:183], v241\n\t" \
    "v_cvt_pk_bf16_f32 v248, v248, v249\n\t" \
    "ds_write_b16 %[dst], v248 offset:1024\n\t" \
    "ds_write_b16_d16_hi %[dst], v248 offset:1040\n\t" \
    "s_nop 4\n\t" \
    "v_permlane32_swap_b32 v224, v226\n\t" \
    "v_permlane32_swap_b32 v225, v227\n\t" \
    "v_add_f32 v224, v224, v226\n\t" \
    "v_add_f32 v225, v225, v227\n\t" \
    "v_cvt_pk_bf16_f32 v224, v224, v225\n\t" \
    "ds_write_b16 %[dst], v224 offset:0\n\t" \
    "ds_write_b16_d16_hi %[dst], v224 offset:16\n\t"
#define ASM_CRIT1_V3 \
    "s_branch .Lc0%=\n\t" \
    ".Lcr%=:\n\t" \
    "s_cmp_eq_u32 %[n], 0\n\t" \
    "s_cbranch_scc1 .Lcb%=\n\t" \
    "s_sub_u32 %[n], %[n], 1\n\t" \
    "v_max_u32 v243, v208, v209\n\t" \
    "v_max3_u32 v243, v210, v211, v243\n\t" \
    "v_cmp_eq_u32 vcc, -1, v243\n\t" \
    "s_cbranch_vccz .Lcs0_%=\n\t" \
    "buffer_load_dwordx4 v[208:211], %[ab], %[rs], %[s0] offen sc1\n\t" \
    ".Lcs0_%=:\n\t" \
    "v_max_u32 v243, v236, v237\n\t" \
    "v_max3_u32 v243, v238, v239, v243\n\t" \
    "v_cmp_eq_u32 vcc, -1, v243\n\t" \
    "s_cbranch_vccz .Lcs1_%=\n\t" \
    "buffer_load_dwordx4 v[236:239], %[ab], %[rs], %[s1] offen sc1\n\t" \
    ".Lcs1_%=:\n\t" \
    "v_max_u32 v243, v220, v221\n\t" \
    "v_max3_u32 v243, v222, v223, v243\n\t" \
    "v_cmp_eq_u32 vcc, -1, v243\n\t" \
    "s_cbranch_vccz .Lca%=\n\t" \
    "buffer_load_dwordx4 v[220:223], %[ab], %[rs], %[s2] offen sc1\n\t" \
    "s_branch .Lca%=\n\t" \
    ".Lc0%=:\n\t" \
    "buffer_load_dwordx4 v[208:211], %[ab], %[rs], %[s0] offen sc1\n\t" \
    "buffer_load_dwordx4 v[236:239], %[ab], %[rs], %[s1] offen sc1\n\t" \
    "buffer_load_dwordx4 v[220:223], %[ab], %[rs], %[s2] offen sc1\n\t" \
    "v_bfe_i32 v241, %[ab], 7, 1\n\t" \
    "v_and_b32 v241, 0xaaaa, v241\n\t" \
    "v_or_b32 v241, 0x4444, v241\n\t" \
    "v_xor_b32 v212, 0x400, %[ab]\n\t" \
    "v_xor_b32 v216, 0x800, %[ab]\n\t" \
    "v_xor_b32 v242, 0xc00, %[ab]\n\t" \
    "v_xor_b32 v240, 0x1400, %[ab]\n\t" \
    ".Lca%=:\n\t" \
    "s_waitcnt vmcnt(0)\n\t" \
    "ds_write_b128 %[ab], v[208:211] offset:0\n\t" \
    "ds_write_b128 %[ab], v[236:239] offset:4096\n\t" \
    "ds_write_b128 %[ab], v[220:223] offset:8192\n\t" \
    "v_max_u32 v243, v208, v209\n\t" \
    "v_max3_u32 v243, v210, v211, v243\n\t" \
    "v_max3_u32 v243, v236, v237, v243\n\t" \
    "v_max3_u32 v243, v238, v239, v243\n\t" \
    "v_max3_u32 v243, v220, v221, v243\n\t" \
    "v_max3_u32 v243, v222, v223, v243\n\t" \
    "v_cmp_eq_u32 vcc, -1, v243\n\t" \
    "s_cbranch_vccnz .Lcr%=\n\t" \
    ".Lcb%=:\n\t" \
    PRIUP \
    ASM_ST2 \
    "v_smfmac_f32_16x16x64_bf16 v[224:227], v[208:211], v[200:207], v241\n\t" \
    "ds_read_b128 v[200:203], %[wl] offset:4096\n\t" \
    "ds_read_b128 v[204:207], %[wl] offset:5120\n\t" \
    "v_smfmac_f32_16x16x64_bf16 v[244:247], v[208:211], v[0:7], v241\n\t" \
    "v_smfmac_f32_16x16x64_bf16 v[248:251], v[208:211], v[8:15], v241\n\t" \
    "s_waitcnt lgkmcnt(2)\n\t" \
    "buffer_load_dword %[fv], %[fv], %[rs], %[fs] offen sc1\n\t" \
    "s_barrier\n\t" ASM_ST \
    "ds_read_b128 v[212:215], v212 offset:0\n\t" \
    "ds_read_b128 v[216:219], v216 offset:0\n\t" \
    "ds_read_b128 v[220:223], v242 offset:0\n\t" \
    "ds_read_b128 v[208:211], v240 offset:0\n\t" \
    "v_smfmac_f32_16x16x64_bf16 v[244:247], v[236:239], v[80:87], v241\n\t" \
    "v_xor_b32 v240, 0x1800, %[ab]\n\t" \
    "v_smfmac_f32_16x16x64_bf16 v[248:251], v[236:239], v[88:95], v241\n\t" \
    "s_waitcnt lgkmcnt(4)\n\t" \
    "v_smfmac_f32_16x16x64_bf16 v[224:227], v[236:239], v[200:207], v241\n\t" \
    "ds_read_b128 v[200:203], %[wl] offset:6144\n\t" \
    "ds_read_b128 v[204:207], %[wl] offset:7168\n\t" \
    "ds_read_b128 v[236:239], v240 offset:0\n\t" \
    "s_waitcnt lgkmcnt(6)\n\t" \
    "v_smfmac_f32_16x16x64_bf16 v[224:227], v[212:215], v[16:23], v241\n\t" \
    "v_smfmac_f32_16x16x64_bf16 v[244:247], v[212:215], v[24:31], v241\n\t" \
    "v_xor_b32 v240, 0x1c00, %[ab]\n\t" \
    "v_smfmac_f32_16x16x64_bf16 v[248:251], v[212:215], v[32:39], v241\n\t" \
    "ds_read_b128 v[212:215], v240 offset:0\n\t" \
    "s_waitcnt lgkmcnt(6)\n\t" \
    "v_smfmac_f32_16x16x64_bf16 v[224:227], v[216:219], v[228:235], v241\n\t" \
    "ds_read_b128 v[228:231], %[wl] offset:8192\n\t" \
    "ds_read_b128 v[232:235], %[wl] offset:9216\n\t" \
    "v_smfmac_f32_16x16x64_bf16 v[244:247], v[216:219], v[40:47], v241\n\t" \
    "v_and_b32 v240, 0x3ff, %[ab]\n\t" \
    "v_smfmac_f32_16x16x64_bf16 v[248:251], v[216:219], v[48:55], v241\n\t" \
    "ds_read_b128 v[216:219], v240 offset:8192\n\t" \
    "s_waitcnt lgkmcnt(8)\n\t" \
    "v_smfmac_f32_16x16x64_bf16 v[224:227], v[220:223], v[56:63], v241\n\t" \
    "v_smfmac_f32_16x16x64_bf16 v[244:247], v[220:223], v[64:71], v241\n\t" \
    "v_smfmac_f32_16x16x64_bf16 v[248:251], v[220:223], v[72:79], v241\n\t" \
    "ds_read_b128 v[220:223], v240 offset:9216\n\t" \
    "s_waitcnt lgkmcnt(8)\n\t" \
    "v_smfmac_f32_16x16x64_bf16 v[244:247], v[208:211], v[104:111], v241\n\t" \
    "s_waitcnt lgkmcnt(5)\n\t" \
    "v_smfmac_f32_16x16x64_bf16 v[244:247], v[236:239], v[120:127], v241\n\t" \
    "s_waitcnt lgkmcnt(4)\n\t" \
    "v_smfmac_f32_16x16x64_bf16 v[244:247], v[212:215], v[144:151], v241\n\t" \
    "s_waitcnt lgkmcnt(1)\n\t" \
    "v_smfmac_f32_16x16x64_bf16 v[244:247], v[216:219], v[160:167], v241\n\t" \
    "s_waitcnt lgkmcnt(0)\n\t" \
    "v_smfmac_f32_16x16x64_bf16 v[244:247], v[220:223], v[184:191], v241\n\t" \
    "v_smfmac_f32_16x16x64_bf16 v[248:251], v[208:211], v[112:119], v241\n\t" \
    "v_smfmac_f32_16x16x64_bf16 v[248:251], v[236:239], v[128:135], v241\n\t" \
    "v_smfmac_f32_16x16x64_bf16 v[248:251], v[212:215], v[152:159], v241\n\t" \
    "v_permlane32_swap_b32 v244, v246\n\t" \
    "v_permlane32_swap_b32 v245, v247\n\t" \
    "v_smfmac_f32_16x16x64_bf16 v[248:251], v[216:219], v[168:175], v241\n\t" \
    "v_add_f32 v244, v244, v246\n\t" \
    "v_add_f32 v245, v245, v247\n\t" \
    "v_smfmac_f32_16x16x64_bf16 v[248:251], v[220:223], v[192:199], v241\n\t" \
    "v_cvt_pk_bf16_f32 v244, v244, v245\n\t" \
    "ds_write_b16 %[dst], v244 offset:512\n\t" \
    "ds_write_b16_d16_hi %[dst], v244 offset:528\n\t" \
    "v_smfmac_f32_16x16x64_bf16 v[224:227], v[208:211], v[96:103], v241\n\t" \
    "v_smfmac_f32_16x16x64_bf16 v[224:227], v[236:239], v[200:207], v241\n\t" \
    "v_smfmac_f32_16x16x64_bf16 v[224:227], v[212:215], v[136:143], v241\n\t" \
    "v_permlane32_swap_b32 v248, v250\n\t" \
    "v_permlane32_swap_b32 v249, v251\n\t" \
    "v_smfmac_f32_16x16x64_bf16 v[224:227], v[216:219], v[228:235], v241\n\t" \
    "v_add_f32 v248, v248, v250\n\t" \
    "v_add_f32 v249, v249, v251\n\t" \
    "v_smfmac_f32_16x16x64_bf16 v[224:227], v[220:223], v[176:183], v241\n\t" \
    "v_cvt_pk_bf16_f32 v248, v248, v249\n\t" \
    "ds_write_b16 %[dst], v248 offset:1024\n\t" \
    "ds_write_b16_d16_hi %[dst], v248 offset:1040\n\t" \
    "s_nop 4\n\t" \
    "v_permlane32_swap_b32 v224, v226\n\t" \
    "v_permlane32_swap_b32 v225, v227\n\t" \
    "v_add_f32 v224, v224, v226\n\t" \
    "v_add_f32 v225, v225, v227\n\t" \
    "v_cvt_pk_bf16_f32 v224, v224, v225\n\t" \
    "ds_write_b16 %[dst], v224 offset:0\n\t" \
    "ds_write_b16_d16_hi %[dst], v224 offset:16\n\t"
#define ASM_CRIT1_V2 \
    "s_branch .Lc0%=\n\t" \
    ".Lcr%=:\n\t" \
    "s_cmp_eq_u32 %[n], 0\n\t" \
    "s_cbranch_scc1 .Lcb%=\n\t" \
    "s_sub_u32 %[n], %[n], 1\n\t" \
    "v_max_u32 v243, v208, v209\n\t" \
    "v_max3_u32 v243, v210, v211, v243\n\t" \
    "v_cmp_eq_u32 vcc, -1, v243\n\t" \
    "s_cbranch_vccz .Lcs0_%=\n\t" \
    "buffer_load_dwordx4 v[208:211], %[ab], %[rs], %[s0] offen sc1\n\t" \
    ".Lcs0_%=:\n\t" \
    "v_max_u32 v243, v236, v237\n\t" \
    "v_max3_u32 v243, v238, v239, v243\n\t" \
    "v_cmp_eq_u32 vcc, -1, v243\n\t" \
    "s_cbranch_vccz .Lca%=\n\t" \
    "buffer_load_dwordx4 v[236:239], %[ab], %[rs], %[s1] offen sc1\n\t" \
    "s_branch .Lca%=\n\t" \
    ".Lc0%=:\n\t" \
    "buffer_load_dwordx4 v[208:211], %[ab], %[rs], %[s0] offen sc1\n\t" \
    "buffer_load_dwordx4 v[236:239], %[ab], %[rs], %[s1] offen sc1\n\t" \
    "v_bfe_i32 v241, %[ab], 7, 1\n\t" \
    "v_and_b32 v241, 0xaaaa, v241\n\t" \
    "v_or_b32 v241, 0x4444, v241\n\t" \
    "v_xor_b32 v212, 0x400, %[ab]\n\t" \
    "v_xor_b32 v216, 0x800, %[ab]\n\t" \
    "v_xor_b32 v242, 0xc00, %[ab]\n\t" \
    "v_xor_b32 v240, 0x1400, %[ab]\n\t" \
    ".Lca%=:\n\t" \
    "s_waitcnt vmcnt(0)\n\t" \
    "ds_write_b128 %[ab], v[208:211] offset:0\n\t" \
    "ds_write_b128 %[ab], v[236:239] offset:4096\n\t" \
    "v_max_u32 v243, v208, v209\n\t" \
    "v_max3_u32 v243, v210, v211, v243\n\t" \
    "v_max3_u32 v243, v236, v237, v243\n\t" \
    "v_max3_u32 v243, v238, v239, v243\n\t" \
    "v_cmp_eq_u32 vcc, -1, v243\n\t" \
    "s_cbranch_vccnz .Lcr%=\n\t" \
    ".Lcb%=:\n\t" \
    PRIUP \
    ASM_ST2 \
    "v_smfmac_f32_16x16x64_bf16 v[224:227], v[208:211], v[200:207], v241\n\t" \
    "ds_read_b128 v[200:203], %[wl] offset:4096\n\t" \
    "ds_read_b128 v[204:207], %[wl] offset:5120\n\t" \
    "v_smfmac_f32_16x16x64_bf16 v[244:247], v[208:211], v[0:7], v241\n\t" \
    "v_smfmac_f32_16x16x64_bf16 v[248:251], v[208:211], v[8:15], v241\n\t" \
    "s_waitcnt lgkmcnt(2)\n\t" \
    "buffer_load_dword %[fv], %[fv], %[rs], %[fs] offen sc1\n\t" \
    "s_barrier\n\t" ASM_ST \
    "ds_read_b128 v[212:215], v212 offset:0\n\t" \
    "ds_read_b128 v[216:219], v216 offset:0\n\t" \
    "ds_read_b128 v[220:223], v242 offset:0\n\t" \
    "ds_read_b128 v[208:211], v240 offset:0\n\t" \
    "v_smfmac_f32_16x16x64_bf16 v[244:247], v[236:239], v[80:87], v241\n\t" \
    "v_xor_b32 v240, 0x1800, %[ab]\n\t" \
    "v_smfmac_f32_16x16x64_bf16 v[248:251], v[236:239], v[88:95], v241\n\t" \
    "s_waitcnt lgkmcnt(4)\n\t" \
    "v_smfmac_f32_16x16x64_bf16 v[224:227], v[236:239], v[200:207], v241\n\t" \
    "ds_read_b128 v[200:203], %[wl] offset:6144\n\t" \
    "ds_read_b128 v[204:207], %[wl] offset:7168\n\t" \
    "ds_read_b128 v[236:239], v240 offset:0\n\t" \
    "s_waitcnt lgkmcnt(6)\n\t" \
    "v_smfmac_f32_16x16x64_bf16 v[224:227], v[212:215], v[16:23], v241\n\t" \
    "v_smfmac_f32_16x16x64_bf16 v[244:247], v[212:215], v[24:31], v241\n\t" \
    "v_xor_b32 v240, 0x1c00, %[ab]\n\t" \
    "v_smfmac_f32_16x16x64_bf16 v[248:251], v[212:215], v[32:39], v241\n\t" \
    "ds_read_b128 v[212:215], v240 offset:0\n\t" \
    "s_waitcnt lgkmcnt(6)\n\t" \
    "v_smfmac_f32_16x16x64_bf16 v[224:227], v[216:219], v[228:235], v241\n\t" \
    "ds_read_b128 v[228:231], %[wl] offset:8192\n\t" \
    "ds_read_b128 v[232:235], %[wl] offset:9216\n\t" \
    "v_smfmac_f32_16x16x64_bf16 v[244:247], v[216:219], v[40:47], v241\n\t" \
    "v_and_b32 v240, 0x3ff, %[ab]\n\t" \
    "v_smfmac_f32_16x16x64_bf16 v[248:251], v[216:219], v[48:55], v241\n\t" \
    "ds_read_b128 v[216:219], v240 offset:8192\n\t" \
    "s_waitcnt lgkmcnt(8)\n\t" \
    "v_smfmac_f32_16x16x64_bf16 v[224:227], v[220:223], v[56:63], v241\n\t" \
    "v_smfmac_f32_16x16x64_bf16 v[244:247], v[220:223], v[64:71], v241\n\t" \
    "v_smfmac_f32_16x16x64_bf16 v[248:251], v[220:223], v[72:79], v241\n\t" \
    "ds_read_b128 v[220:223], v240 offset:9216\n\t" \
    "s_waitcnt lgkmcnt(8)\n\t" \
    "v_smfmac_f32_16x16x64_bf16 v[244:247], v[208:211], v[104:111], v241\n\t" \
    "s_waitcnt lgkmcnt(5)\n\t" \
    "v_smfmac_f32_16x16x64_bf16 v[244:247], v[236:239], v[120:127], v241\n\t" \
    "s_waitcnt lgkmcnt(4)\n\t" \
    "v_smfmac_f32_16x16x64_bf16 v[244:247], v[212:215], v[144:151], v241\n\t" \
    "s_waitcnt lgkmcnt(1)\n\t" \
    "v_smfmac_f32_16x16x64_bf16 v[244:247], v[216:219], v[160:167], v241\n\t" \
    "s_waitcnt lgkmcnt(0)\n\t" \
    "v_smfmac_f32_16x16x64_bf16 v[244:247], v[220:223], v[184:191], v241\n\t" \
    "v_smfmac_f32_16x16x64_bf16 v[248:251], v[208:211], v[112:119], v241\n\t" \
    "v_smfmac_f32_16x16x64_bf16 v[248:251], v[236:239], v[128:135], v241\n\t" \
    "v_smfmac_f32_16x16x64_bf16 v[248:251], v[212:215], v[152:159], v241\n\t" \
    "v_permlane32_swap_b32 v244, v246\n\t" \
    "v_permlane32_swap_b32 v245, v247\n\t" \
    "v_smfmac_f32_16x16x64_bf16 v[248:251], v[216:219], v[168:175], v241\n\t" \
    "v_add_f32 v244, v244, v246\n\t" \
    "v_add_f32 v245, v245, v247\n\t" \
    "v_smfmac_f32_16x16x64_bf16 v[248:251], v[220:223], v[192:199], v241\n\t" \
    "v_cvt_pk_bf16_f32 v244, v244, v245\n\t" \
    "ds_write_b16 %[dst], v244 offset:512\n\t" \
    "ds_write_b16_d16_hi %[dst], v244 offset:528\n\t" \
    "v_smfmac_f32_16x16x64_bf16 v[224:227], v[208:211], v[96:103], v241\n\t" \
    "v_smfmac_f32_16x16x64_bf16 v[224:227], v[236:239], v[200:207], v241\n\t" \
    "v_smfmac_f32_16x16x64_bf16 v[224:227], v[212:215], v[136:143], v241\n\t" \
    "v_permlane32_swap_b32 v248, v250\n\t" \
    "v_permlane32_swap_b32 v249, v251\n\t" \
    "v_smfmac_f32_16x16x64_bf16 v[224:227], v[216:219], v[228:235], v241\n\t" \
    "v_add_f32 v248, v248, v250\n\t" \
    "v_add_f32 v249, v249, v251\n\t" \
    "v_smfmac_f32_16x16x64_bf16 v[224:227], v[220:223], v[176:183], v241\n\t" \
    "v_cvt_pk_bf16_f32 v248, v248, v249\n\t" \
    "ds_write_b16 %[dst], v248 offset:1024\n\t" \
    "ds_write_b16_d16_hi %[dst], v248 offset:1040\n\t" \
    "s_nop 4\n\t" \
    "v_permlane32_swap_b32 v224, v226\n\t" \
    "v_permlane32_swap_b32 v225, v227\n\t" \
    "v_add_f32 v224, v224, v226\n\t" \
    "v_add_f32 v225, v225, v227\n\t" \
    "v_cvt_pk_bf16_f32 v224, v224, v225\n\t" \
    "ds_write_b16 %[dst], v224 offset:0\n\t" \
    "ds_write_b16_d16_hi %[dst], v224 offset:16\n\t"
#define ASM_VOX \
    "s_waitcnt lgkmcnt(0)\n\t" \
    "ds_read_b128 v[208:211], %[va] offset:2048\n\t" \
    "ds_read_b128 v[212:215], %[va] offset:3072\n\t" \
    "ds_read_b128 v[216:219], %[va] offset:4096\n\t" \
    "ds_read_b128 v[220:223], %[va] offset:5120\n\t" \
    "ds_read_b128 v[200:203], %[va] offset:0\n\t" \
    "ds_read_b128 v[204:207], %[va] offset:1024\n\t" \
    "ds_read_b128 v[236:239], %[wl] offset:0\n\t" \
    "ds_read_b128 v[240:243], %[wl] offset:1024\n\t" \
    "s_waitcnt lgkmcnt(7)\n\t" \
    "v_mfma_f32_16x16x32_bf16 v[244:247], v[208:211], v[0:3], 0\n\t" \
    "s_waitcnt lgkmcnt(6)\n\t" \
    "v_mfma_f32_16x16x32_bf16 v[244:247], v[212:215], v[4:7], v[244:247]\n\t" \
    "s_waitcnt lgkmcnt(5)\n\t" \
    "v_mfma_f32_16x16x32_bf16 v[244:247], v[216:219], v[8:11], v[244:247]\n\t" \
    "s_waitcnt lgkmcnt(4)\n\t" \
    "v_mfma_f32_16x16x32_bf16 v[244:247], v[220:223], v[12:15], v[244:247]\n\t" \
    "s_waitcnt lgkmcnt(1)\n\t" \
    "v_mfma_f32_16x16x32_bf16 v[244:247], v[200:203], v[236:239], v[244:247]\n\t" \
    "ds_read_b128 v[236:239], %[wl] offset:2048\n\t" \
    "s_waitcnt lgkmcnt(1)\n\t" \
    "v_mfma_f32_16x16x32_bf16 v[244:247], v[204:207], v[240:243], v[244:247]\n\t" \
    "ds_read_b128 v[240:243], %[wl] offset:3072\n\t" \
    "v_mfma_f32_16x16x32_bf16 v[248:251], v[208:211], v[40:43], 0\n\t" \
    "v_mfma_f32_16x16x32_bf16 v[248:251], v[212:215], v[44:47], v[248:251]\n\t" \
    "v_mfma_f32_16x16x32_bf16 v[248:251], v[216:219], v[48:51], v[248:251]\n\t" \
    "v_cvt_pk_bf16_f32 v224, v244, v245\n\t" \
    "v_mfma_f32_16x16x32_bf16 v[248:251], v[220:223], v[52:55], v[248:251]\n\t" \
    "v_cvt_pk_bf16_f32 v225, v246, v247\n\t" \
    "s_waitcnt lgkmcnt(1)\n\t" \
    "v_mfma_f32_16x16x32_bf16 v[248:251], v[200:203], v[236:239], v[248:251]\n\t" \
    "ds_read_b128 v[236:239], %[wl] offset:4096\n\t" \
    "s_waitcnt lgkmcnt(1)\n\t" \
    "v_mfma_f32_16x16x32_bf16 v[248:251], v[204:207], v[240:243], v[248:251]\n\t" \
    "ds_read_b128 v[240:243], %[wl] offset:5120\n\t" \
    "v_mfma_f32_16x16x32_bf16 v[244:247], v[208:211], v[80:83], 0\n\t" \
    "v_mfma_f32_16x16x32_bf16 v[244:247], v[212:215], v[84:87], v[244:247]\n\t" \
    "v_mfma_f32_16x16x32_bf16 v[244:247], v[216:219], v[88:91], v[244:247]\n\t" \
    "v_cvt_pk_bf16_f32 v226, v248, v249\n\t" \
    "v_mfma_f32_16x16x32_bf16 v[244:247], v[220:223], v[92:95], v[244:247]\n\t" \
    "v_cvt_pk_bf16_f32 v227, v250, v251\n\t" \
    "s_waitcnt lgkmcnt(1)\n\t" \
    "v_mfma_f32_16x16x32_bf16 v[244:247], v[200:203], v[236:239], v[244:247]\n\t" \
    "ds_read_b128 v[236:239], %[wl] offset:6144\n\t" \
    "s_waitcnt lgkmcnt(1)\n\t" \
    "v_mfma_f32_16x16x32_bf16 v[244:247], v[204:207], v[240:243], v[244:247]\n\t" \
    "ds_read_b128 v[240:243], %[wl] offset:7168\n\t" \
    "v_mfma_f32_16x16x32_bf16 v[248:251], v[208:211], v[120:123], 0\n\t" \
    "v_mfma_f32_16x16x32_bf16 v[248:251], v[212:215], v[124:127], v[248:251]\n\t" \
    "v_mfma_f32_16x16x32_bf16 v[248:251], v[216:219], v[128:131], v[248:251]\n\t" \
    "v_cvt_pk_bf16_f32 v228, v244, v245\n\t" \
    "v_mfma_f32_16x16x32_bf16 v[248:251], v[220:223], v[132:135], v[248:251]\n\t" \
    "v_cvt_pk_bf16_f32 v229, v246, v247\n\t" \
    "s_waitcnt lgkmcnt(1)\n\t" \
    "v_mfma_f32_16x16x32_bf16 v[248:251], v[200:203], v[236:239], v[248:251]\n\t" \
    "ds_read_b128 v[236:239], %[wl] offset:8192\n\t" \
    "s_waitcnt lgkmcnt(1)\n\t" \
    "v_mfma_f32_16x16x32_bf16 v[248:251], v[204:207], v[240:243], v[248:251]\n\t" \
    "ds_read_b128 v[240:243], %[wl] offset:9216\n\t" \
    "v_mfma_f32_16x16x32_bf16 v[244:247], v[208:211], v[160:163], 0\n\t" \
    "v_mfma_f32_16x16x32_bf16 v[244:247], v[212:215], v[164:167], v[244:247]\n\t" \
    "v_mfma_f32_16x16x32_bf16 v[244:247], v[216:219], v[168:171], v[244:247]\n\t" \
    "v_cvt_pk_bf16_f32 v230, v248, v249\n\t" \
    "v_mfma_f32_16x16x32_bf16 v[244:247], v[220:223], v[172:175], v[244:247]\n\t" \
    "v_cvt_pk_bf16_f32 v231, v250, v251\n\t" \
    "s_waitcnt lgkmcnt(1)\n\t" \
    "v_mfma_f32_16x16x32_bf16 v[244:247], v[200:203], v[236:239], v[244:247]\n\t" \
    "s_waitcnt lgkmcnt(0)\n\t" \
    "v_mfma_f32_16x16x32_bf16 v[244:247], v[204:207], v[240:243], v[244:247]\n\t" \
    "v_mfma_f32_16x16x32_bf16 v[248:251], v[208:211], v[24:27], 0\n\t" \
    "v_mfma_f32_16x16x32_bf16 v[248:251], v[212:215], v[28:31], v[248:251]\n\t" \
    "v_mfma_f32_16x16x32_bf16 v[248:251], v[216:219], v[32:35], v[248:251]\n\t" \
    "v_cvt_pk_bf16_f32 v232, v244, v245\n\t" \
    "v_mfma_f32_16x16x32_bf16 v[248:251], v[220:223], v[36:39], v[248:251]\n\t" \
    "v_cvt_pk_bf16_f32 v233, v246, v247\n\t" \
    "v_mfma_f32_16x16x32_bf16 v[248:251], v[200:203], v[16:19], v[248:251]\n\t" \
    "v_mfma_f32_16x16x32_bf16 v[248:251], v[204:207], v[20:23], v[248:251]\n\t" \
    "v_mfma_f32_16x16x32_bf16 v[244:247], v[208:211], v[64:67], 0\n\t" \
    "v_mfma_f32_16x16x32_bf16 v[244:247], v[212:215], v[68:71], v[244:247]\n\t" \
    "v_mfma_f32_16x16x32_bf16 v[244:247], v[216:219], v[72:75], v[244:247]\n\t" \
    "v_cvt_pk_bf16_f32 v236, v248, v249\n\t" \
    "v_mfma_f32_16x16x32_bf16 v[244:247], v[220:223], v[76:79], v[244:247]\n\t" \
    "v_cvt_pk_bf16_f32 v237, v250, v251\n\t" \
    "v_mfma_f32_16x16x32_bf16 v[244:247], v[200:203], v[56:59], v[244:247]\n\t" \
    "v_mfma_f32_16x16x32_bf16 v[244:247], v[204:207], v[60:63], v[244:247]\n\t" \
    "v_mfma_f32_16x16x32_bf16 v[248:251], v[208:211], v[104:107], 0\n\t" \
    "v_mfma_f32_16x16x32_bf16 v[248:251], v[212:215], v[108:111], v[248:251]\n\t" \
    "v_mfma_f32_16x16x32_bf16 v[248:251], v[216:219], v[112:115], v[248:251]\n\t" \
    "v_cvt_pk_bf16_f32 v238, v244, v245\n\t" \
    "v_mfma_f32_16x16x32_bf16 v[248:251], v[220:223], v[116:119], v[248:251]\n\t" \
    "v_cvt_pk_bf16_f32 v239, v246, v247\n\t" \
    "v_mfma_f32_16x16x32_bf16 v[248:251], v[200:203], v[96:99], v[248:251]\n\t" \
    "v_mfma_f32_16x16x32_bf16 v[248:251], v[204:207], v[100:103], v[248:251]\n\t" \
    "v_mfma_f32_16x16x32_bf16 v[244:247], v[208:211], v[144:147], 0\n\t" \
    "v_mfma_f32_16x16x32_bf16 v[244:247], v[212:215], v[148:151], v[244:247]\n\t" \
    "v_mfma_f32_16x16x32_bf16 v[244:247], v[216:219], v[152:155], v[244:247]\n\t" \
    "v_cvt_pk_bf16_f32 v240, v248, v249\n\t" \
    "v_mfma_f32_16x16x32_bf16 v[244:247], v[220:223], v[156:159], v[244:247]\n\t" \
    "v_cvt_pk_bf16_f32 v241, v250, v251\n\t" \
    "v_mfma_f32_16x16x32_bf16 v[244:247], v[200:203], v[136:139], v[244:247]\n\t" \
    "v_mfma_f32_16x16x32_bf16 v[244:247], v[204:207], v[140:143], v[244:247]\n\t" \
    "v_mfma_f32_16x16x32_bf16 v[248:251], v[208:211], v[184:187], 0\n\t" \
    "ds_read_b128 v[208:211], %[va] offset:8192\n\t" \
    "v_mfma_f32_16x16x32_bf16 v[248:251], v[212:215], v[188:191], v[248:251]\n\t" \
    "ds_read_b128 v[212:215], %[va] offset:9216\n\t" \
    "v_mfma_f32_16x16x32_bf16 v[248:251], v[216:219], v[192:195], v[248:251]\n\t" \
    "ds_read_b128 v[216:219], %[va] offset:10240\n\t" \
    "v_cvt_pk_bf16_f32 v242, v244, v245\n\t" \
    "v_mfma_f32_16x16x32_bf16 v[248:251], v[220:223], v[196:199], v[248:251]\n\t" \
    "ds_read_b128 v[220:223], %[va] offset:11264\n\t" \
    "v_cvt_pk_bf16_f32 v243, v246, v247\n\t" \
    "v_mfma_f32_16x16x32_bf16 v[248:251], v[200:203], v[176:179], v[248:251]\n\t" \
    "ds_read_b128 v[200:203], %[va] offset:6144\n\t" \
    "v_mfma_f32_16x16x32_bf16 v[248:251], v[204:207], v[180:183], v[248:251]\n\t" \
    "ds_read_b128 v[204:207], %[va] offset:7168\n\t" \
    "s_waitcnt lgkmcnt(5)\n\t" \
    "v_mfma_f32_16x16x32_bf16 v[244:247], v[208:211], v[24:27], 0\n\t" \
    "s_waitcnt lgkmcnt(4)\n\t" \
    "v_mfma_f32_16x16x32_bf16 v[244:247], v[212:215], v[28:31], v[244:247]\n\t" \
    "s_waitcnt lgkmcnt(3)\n\t" \
    "v_mfma_f32_16x16x32_bf16 v[244:247], v[216:219], v[32:35], v[244:247]\n\t" \
    "v_cvt_pk_bf16_f32 v234, v248, v249\n\t" \
    "s_waitcnt lgkmcnt(2)\n\t" \
    "v_mfma_f32_16x16x32_bf16 v[244:247], v[220:223], v[36:39], v[244:247]\n\t" \
    "v_cvt_pk_bf16_f32 v235, v250, v251\n\t" \
    "s_waitcnt lgkmcnt(1)\n\t" \
    "v_mfma_f32_16x16x32_bf16 v[244:247], v[200:203], v[16:19], v[244:247]\n\t" \
    "s_waitcnt lgkmcnt(0)\n\t" \
    "v_mfma_f32_16x16x32_bf16 v[244:247], v[204:207], v[20:23], v[244:247]\n\t" \
    "v_mfma_f32_16x16x32_bf16 v[248:251], v[208:211], v[64:67], 0\n\t" \
    "s_add_i32 %[so], %[so], 65536\n\t" \
    "v_mfma_f32_16x16x32_bf16 v[248:251], v[212:215], v[68:71], v[248:251]\n\t" \
    "s_nop 1\n\t" \
    "v_cvt_pk_bf16_f32 v247, v246, v247\n\t" \
    "v_mfma_f32_16x16x32_bf16 v[248:251], v[216:219], v[72:75], v[248:251]\n\t" \
    "v_cvt_pk_bf16_f32 v246, v244, v245\n\t" \
    "v_mfma_f32_16x16x32_bf16 v[248:251], v[220:223], v[76:79], v[248:251]\n\t" \
    "v_mov_b64 v[244:245], v[236:237]\n\t" \
    "buffer_store_dwordx4 v[244:247], %[l16], %[rd], %[so] offen sc1\n\t" \
    "v_mfma_f32_16x16x32_bf16 v[248:251], v[200:203], v[56:59], v[248:251]\n\t" \
    "v_mfma_f32_16x16x32_bf16 v[248:251], v[204:207], v[60:63], v[248:251]\n\t" \
    "v_mfma_f32_16x16x32_bf16 v[244:247], v[208:211], v[104:107], 0\n\t" \
    "s_add_i32 %[so], %[so], 131072\n\t" \
    "v_mfma_f32_16x16x32_bf16 v[244:247], v[212:215], v[108:111], v[244:247]\n\t" \
    "s_nop 1\n\t" \
    "v_cvt_pk_bf16_f32 v251, v250, v251\n\t" \
    "v_mfma_f32_16x16x32_bf16 v[244:247], v[216:219], v[112:115], v[244:247]\n\t" \
    "v_cvt_pk_bf16_f32 v250, v248, v249\n\t" \
    "v_mfma_f32_16x16x32_bf16 v[244:247], v[220:223], v[116:119], v[244:247]\n\t" \
    "v_mov_b64 v[248:249], v[238:239]\n\t" \
    "buffer_store_dwordx4 v[248:251], %[l16], %[rd], %[so] offen sc1\n\t" \
    "v_mfma_f32_16x16x32_bf16 v[244:247], v[200:203], v[96:99], v[244:247]\n\t" \
    "v_mfma_f32_16x16x32_bf16 v[244:247], v[204:207], v[100:103], v[244:247]\n\t" \
    "v_mfma_f32_16x16x32_bf16 v[248:251], v[208:211], v[144:147], 0\n\t" \
    "s_add_i32 %[so], %[so], 131072\n\t" \
    "v_mfma_f32_16x16x32_bf16 v[248:251], v[212:215], v[148:151], v[248:251]\n\t" \
    "s_nop 1\n\t" \
    "v_cvt_pk_bf16_f32 v247, v246, v247\n\t" \
    "v_mfma_f32_16x16x32_bf16 v[248:251], v[216:219], v[152:155], v[248:251]\n\t" \
    "v_cvt_pk_bf16_f32 v246, v244, v245\n\t" \
    "v_mfma_f32_16x16x32_bf16 v[248:251], v[220:223], v[156:159], v[248:251]\n\t" \
    "v_mov_b64 v[244:245], v[240:241]\n\t" \
    "buffer_store_dwordx4 v[244:247], %[l16], %[rd], %[so] offen sc1\n\t" \
    "v_mfma_f32_16x16x32_bf16 v[248:251], v[200:203], v[136:139], v[248:251]\n\t" \
    "v_mfma_f32_16x16x32_bf16 v[248:251], v[204:207], v[140:143], v[248:251]\n\t" \
    "v_mfma_f32_16x16x32_bf16 v[244:247], v[208:211], v[184:187], 0\n\t" \
    "s_add_i32 %[so], %[so], 131072\n\t" \
    "v_mfma_f32_16x16x32_bf16 v[244:247], v[212:215], v[188:191], v[244:247]\n\t" \
    "s_nop 1\n\t" \
    "v_cvt_pk_bf16_f32 v251, v250, v251\n\t" \
    "v_mfma_f32_16x16x32_bf16 v[244:247], v[216:219], v[192:195], v[244:247]\n\t" \
    "v_cvt_pk_bf16_f32 v250, v248, v249\n\t" \
    "v_mfma_f32_16x16x32_bf16 v[244:247], v[220:223], v[196:199], v[244:247]\n\t" \
    "v_mov_b64 v[248:249], v[242:243]\n\t" \
    "buffer_store_dwordx4 v[248:251], %[l16], %[rd], %[so] offen sc1\n\t" \
    "v_mfma_f32_16x16x32_bf16 v[244:247], v[200:203], v[176:179], v[244:247]\n\t" \
    "v_mfma_f32_16x16x32_bf16 v[244:247], v[204:207], v[180:183], v[244:247]\n\t" \
    "ds_read_b128 v[236:239], %[wl] offset:0\n\t" \
    "ds_read_b128 v[240:243], %[wl] offset:1024\n\t" \
    "v_mfma_f32_16x16x32_bf16 v[248:251], v[208:211], v[0:3], 0\n\t" \
    "s_add_i32 %[so], %[so], 131072\n\t" \
    "v_mfma_f32_16x16x32_bf16 v[248:251], v[212:215], v[4:7], v[248:251]\n\t" \
    "v_cvt_pk_bf16_f32 v247, v246, v247\n\t" \
    "v_mfma_f32_16x16x32_bf16 v[248:251], v[216:219], v[8:11], v[248:251]\n\t" \
    "v_cvt_pk_bf16_f32 v246, v244, v245\n\t" \
    "v_mfma_f32_16x16x32_bf16 v[248:251], v[220:223], v[12:15], v[248:251]\n\t" \
    "v_mov_b64 v[244:245], v[234:235]\n\t" \
    "buffer_store_dwordx4 v[244:247], %[l16], %[rd], %[so] offen sc1\n\t" \
    "s_waitcnt lgkmcnt(1)\n\t" \
    "v_mfma_f32_16x16x32_bf16 v[248:251], v[200:203], v[236:239], v[248:251]\n\t" \
    "ds_read_b128 v[236:239], %[wl] offset:2048\n\t" \
    "s_waitcnt lgkmcnt(1)\n\t" \
    "v_mfma_f32_16x16x32_bf16 v[248:251], v[204:207], v[240:243], v[248:251]\n\t" \
    "ds_read_b128 v[240:243], %[wl] offset:3072\n\t" \
    "v_mfma_f32_16x16x32_bf16 v[244:247], v[208:211], v[40:43], 0\n\t" \
    "s_add_i32 %[so], %[so], -589824\n\t" \
    "v_mfma_f32_16x16x32_bf16 v[244:247], v[212:215], v[44:47], v[244:247]\n\t" \
    "s_nop 0\n\t" \
    "v_cvt_pk_bf16_f32 v251, v250, v251\n\t" \
    "v_mfma_f32_16x16x32_bf16 v[244:247], v[216:219], v[48:51], v[244:247]\n\t" \
    "v_cvt_pk_bf16_f32 v250, v248, v249\n\t" \
    "v_mfma_f32_16x16x32_bf16 v[244:247], v[220:223], v[52:55], v[244:247]\n\t" \
    "v_mov_b64 v[248:249], v[224:225]\n\t" \
    "buffer_store_dwordx4 v[248:251], %[l16], %[rd], %[so] offen sc1\n\t" \
    "s_waitcnt lgkmcnt(1)\n\t" \
    "v_mfma_f32_16x16x32_bf16 v[244:247], v[200:203], v[236:239], v[244:247]\n\t" \
    "ds_read_b128 v[236:239], %[wl] offset:4096\n\t" \
    "s_waitcnt lgkmcnt(1)\n\t" \
    "v_mfma_f32_16x16x32_bf16 v[244:247], v[204:207], v[240:243], v[244:247]\n\t" \
    "ds_read_b128 v[240:243], %[wl] offset:5120\n\t" \
    "v_mfma_f32_16x16x32_bf16 v[248:251], v[208:211], v[80:83], 0\n\t" \
    "s_add_i32 %[so], %[so], 131072\n\t" \
    "v_mfma_f32_16x16x32_bf16 v[248:251], v[212:215], v[84:87], v[248:251]\n\t" \
    "s_nop 0\n\t" \
    "v_cvt_pk_bf16_f32 v247, v246, v247\n\t" \
    "v_mfma_f32_16x16x32_bf16 v[248:251], v[216:219], v[88:91], v[248:251]\n\t" \
    "v_cvt_pk_bf16_f32 v246, v244, v245\n\t" \
    "v_mfma_f32_16x16x32_bf16 v[248:251], v[220:223], v[92:95], v[248:251]\n\t" \
    "v_mov_b64 v[244:245], v[226:227]\n\t" \
    "buffer_store_dwordx4 v[244:247], %[l16], %[rd], %[so] offen sc1\n\t" \
    "s_waitcnt lgkmcnt(1)\n\t" \
    "v_mfma_f32_16x16x32_bf16 v[248:251], v[200:203], v[236:239], v[248:251]\n\t" \
    "ds_read_b128 v[236:239], %[wl] offset:6144\n\t" \
    "s_waitcnt lgkmcnt(1)\n\t" \
    "v_mfma_f32_16x16x32_bf16 v[248:251], v[204:207], v[240:243], v[248:251]\n\t" \
    "ds_read_b128 v[240:243], %[wl] offset:7168\n\t" \
    "v_mfma_f32_16x16x32_bf16 v[244:247], v[208:211], v[120:123], 0\n\t" \
    "s_add_i32 %[so], %[so], 131072\n\t" \
    "v_mfma_f32_16x16x32_bf16 v[244:247], v[212:215], v[124:127], v[244:247]\n\t" \
    "s_nop 0\n\t" \
    "v_cvt_pk_bf16_f32 v251, v250, v251\n\t" \
    "v_mfma_f32_16x16x32_bf16 v[244:247], v[216:219], v[128:131], v[244:247]\n\t" \
    "v_cvt_pk_bf16_f32 v250, v248, v249\n\t" \
    "v_mfma_f32_16x16x32_bf16 v[244:247], v[220:223], v[132:135], v[244:247]\n\t" \
    "v_mov_b64 v[248:249], v[228:229]\n\t" \
    "buffer_store_dwordx4 v[248:251], %[l16], %[rd], %[so] offen sc1\n\t" \
    "s_waitcnt lgkmcnt(1)\n\t" \
    "v_mfma_f32_16x16x32_bf16 v[244:247], v[200:203], v[236:239], v[244:247]\n\t" \
    "ds_read_b128 v[236:239], %[wl] offset:8192\n\t" \
    "s_waitcnt lgkmcnt(1)\n\t" \
    "v_mfma_f32_16x16x32_bf16 v[244:247], v[204:207], v[240:243], v[244:247]\n\t" \
    "ds_read_b128 v[240:243], %[wl] offset:9216\n\t" \
    "v_mfma_f32_16x16x32_bf16 v[248:251], v[208:211], v[160:163], 0\n\t" \
    "s_add_i32 %[so], %[so], 131072\n\t" \
    "v_mfma_f32_16x16x32_bf16 v[248:251], v[212:215], v[164:167], v[248:251]\n\t" \
    "s_nop 0\n\t" \
    "v_cvt_pk_bf16_f32 v247, v246, v247\n\t" \
    "v_mfma_f32_16x16x32_bf16 v[248:251], v[216:219], v[168:171], v[248:251]\n\t" \
    "v_cvt_pk_bf16_f32 v246, v244, v245\n\t" \
    "v_mfma_f32_16x16x32_bf16 v[248:251], v[220:223], v[172:175], v[248:251]\n\t" \
    "v_mov_b64 v[244:245], v[230:231]\n\t" \
    "buffer_store_dwordx4 v[244:247], %[l16], %[rd], %[so] offen sc1\n\t" \
    "s_waitcnt lgkmcnt(1)\n\t" \
    "v_mfma_f32_16x16x32_bf16 v[248:251], v[200:203], v[236:239], v[248:251]\n\t" \
    "s_waitcnt lgkmcnt(0)\n\t" \
    "v_mfma_f32_16x16x32_bf16 v[248:251], v[204:207], v[240:243], v[248:251]\n\t" \
    "s_add_i32 %[so], %[so], 131072\n\t" \
    "s_nop 7\n\t" \
    "v_cvt_pk_bf16_f32 v251, v250, v251\n\t" \
    "v_cvt_pk_bf16_f32 v250, v248, v249\n\t" \
    "v_mov_b64 v[248:249], v[232:233]\n\t" \
    "buffer_store_dwordx4 v[248:251], %[l16], %[rd], %[so] offen sc1\n\t" \
    "s_nop 1\n\t"
#define ASM_CRIT2 \
    "s_waitcnt lgkmcnt(0)\n\t" \
    "s_barrier\n\t" ASM_ST \
    "ds_read_b128 v[204:207], v200 offset:11264\n\t" \
    "ds_read_b128 v[208:211], v200 offset:12288\n\t" \
    "ds_read_b128 v[200:203], v200 offset:10240\n\t" \
    "s_waitcnt lgkmcnt(2)\n\t" \
    "v_smfmac_f32_16x16x64_bf16 v[224:227], v[204:207], v[24:31], %[sidx]\n\t" \
    "s_waitcnt lgkmcnt(1)\n\t" \
    "v_smfmac_f32_16x16x64_bf16 v[224:227], v[208:211], v[32:39], %[sidx]\n\t" \
    "s_waitcnt lgkmcnt(0)\n\t" \
    "v_smfmac_f32_16x16x64_bf16 v[224:227], v[200:203], v[16:23], %[sidx]\n\t" \
    "v_smfmac_f32_16x16x64_bf16 v[220:223], v[204:207], v[0:7], %[sidx]\n\t" \
    "v_smfmac_f32_16x16x64_bf16 v[220:223], v[208:211], v[8:15], %[sidx]\n\t" \
    "v_smfmac_f32_16x16x64_bf16 v[220:223], v[200:203], v[212:219], %[sidx]\n\t" \
    "ds_read_b128 v[212:215], %[wl] offset:2048\n\t" \
    "ds_read_b128 v[216:219], %[wl] offset:3072\n\t" \
    "v_smfmac_f32_16x16x64_bf16 v[232:235], v[204:207], v[64:71], %[sidx]\n\t" \
    "v_smfmac_f32_16x16x64_bf16 v[232:235], v[208:211], v[72:79], %[sidx]\n\t" \
    "v_smfmac_f32_16x16x64_bf16 v[232:235], v[200:203], v[56:63], %[sidx]\n\t" \
    "v_permlane32_swap_b32 v220, v224\n\t" \
    "v_permlane32_swap_b32 v221, v225\n\t" \
    "v_permlane32_swap_b32 v222, v226\n\t" \
    "v_smfmac_f32_16x16x64_bf16 v[228:231], v[204:207], v[40:47], %[sidx]\n\t" \
    "v_permlane32_swap_b32 v223, v227\n\t" \
    "v_add_f32 v220, v220, v224\n\t" \
    "v_add_f32 v221, v221, v225\n\t" \
    "v_smfmac_f32_16x16x64_bf16 v[228:231], v[208:211], v[48:55], %[sidx]\n\t" \
    "v_add_f32 v222, v222, v226\n\t" \
    "v_add_f32 v223, v223, v227\n\t" \
    "v_cvt_pk_bf16_f32 v226, v220, v221\n\t" \
    "s_waitcnt lgkmcnt(0)\n\t" \
    "v_smfmac_f32_16x16x64_bf16 v[228:231], v[200:203], v[212:219], %[sidx]\n\t" \
    "ds_read_b128 v[212:215], %[wl] offset:4096\n\t" \
    "ds_read_b128 v[216:219], %[wl] offset:5120\n\t" \
    "v_cvt_pk_bf16_f32 v227, v222, v223\n\t" \
    "v_min_u32 v226, -2, v226\n\t" \
    "v_min_u32 v227, -2, v227\n\t" \
    "v_smfmac_f32_16x16x64_bf16 v[240:243], v[204:207], v[104:111], %[sidx]\n\t" \
    "v_smfmac_f32_16x16x64_bf16 v[240:243], v[208:211], v[112:119], %[sidx]\n\t" \
    "v_smfmac_f32_16x16x64_bf16 v[240:243], v[200:203], v[96:103], %[sidx]\n\t" \
    "v_permlane32_swap_b32 v228, v232\n\t" \
    "v_permlane32_swap_b32 v229, v233\n\t" \
    "v_permlane32_swap_b32 v230, v234\n\t" \
    "v_smfmac_f32_16x16x64_bf16 v[236:239], v[204:207], v[80:87], %[sidx]\n\t" \
    "v_permlane32_swap_b32 v231, v235\n\t" \
    "v_add_f32 v228, v228, v232\n\t" \
    "v_add_f32 v229, v229, v233\n\t" \
    "v_smfmac_f32_16x16x64_bf16 v[236:239], v[208:211], v[88:95], %[sidx]\n\t" \
    "v_add_f32 v230, v230, v234\n\t" \
    "v_add_f32 v231, v231, v235\n\t" \
    "v_cvt_pk_bf16_f32 v228, v228, v229\n\t" \
    "s_waitcnt lgkmcnt(0)\n\t" \
    "v_smfmac_f32_16x16x64_bf16 v[236:239], v[200:203], v[212:219], %[sidx]\n\t" \
    "ds_read_b128 v[212:215], %[wl] offset:6144\n\t" \
    "ds_read_b128 v[216:219], %[wl] offset:7168\n\t" \
    "v_cvt_pk_bf16_f32 v229, v230, v231\n\t" \
    "v_min_u32 v228, -2, v228\n\t" \
    "v_min_u32 v229, -2, v229\n\t" \
    "s_nop 1\n\t" \
    "v_permlane16_swap_b32 v226, v228\n\t" \
    "v_permlane16_swap_b32 v227, v229\n\t" \
    "buffer_store_dwordx4 v[226:229], %[vo], %[rr], %[so] offen sc1\n\t" \
    "s_add_u32 %[so], %[so], 0x10000\n\t" \
    "v_smfmac_f32_16x16x64_bf16 v[248:251], v[204:207], v[144:151], %[sidx]\n\t" \
    "v_mov_b64 v[220:221], 0\n\t" \
    "v_mov_b64 v[222:223], 0\n\t" \
    "v_smfmac_f32_16x16x64_bf16 v[248:251], v[208:211], v[152:159], %[sidx]\n\t" \
    "v_mov_b64 v[224:225], 0\n\t" \
    "v_mov_b64 v[226:227], 0\n\t" \
    "v_smfmac_f32_16x16x64_bf16 v[248:251], v[200:203], v[136:143], %[sidx]\n\t" \
    "v_permlane32_swap_b32 v236, v240\n\t" \
    "v_permlane32_swap_b32 v237, v241\n\t" \
    "v_permlane32_swap_b32 v238, v242\n\t" \
    "v_smfmac_f32_16x16x64_bf16 v[244:247], v[204:207], v[120:127], %[sidx]\n\t" \
    "v_permlane32_swap_b32 v239, v243\n\t" \
    "v_add_f32 v236, v236, v240\n\t" \
    "v_add_f32 v237, v237, v241\n\t" \
    "v_smfmac_f32_16x16x64_bf16 v[244:247], v[208:211], v[128:135], %[sidx]\n\t" \
    "v_add_f32 v238, v238, v242\n\t" \
    "v_add_f32 v239, v239, v243\n\t" \
    "v_cvt_pk_bf16_f32 v242, v236, v237\n\t" \
    "s_waitcnt lgkmcnt(0)\n\t" \
    "v_smfmac_f32_16x16x64_bf16 v[244:247], v[200:203], v[212:219], %[sidx]\n\t" \
    "ds_read_b128 v[212:215], %[wl] offset:8192\n\t" \
    "ds_read_b128 v[216:219], %[wl] offset:9216\n\t" \
    "v_cvt_pk_bf16_f32 v243, v238, v239\n\t" \
    "v_min_u32 v242, -2, v242\n\t" \
    "v_min_u32 v243, -2, v243\n\t" \
    "v_smfmac_f32_16x16x64_bf16 v[224:227], v[204:207], v[184:191], %[sidx]\n\t" \
    "v_and_b32 v232, 0x3fff, %[vo]\n\t" \
    "v_bfe_u32 v233, %[vo], 14, 1\n\t" \
    "v_smfmac_f32_16x16x64_bf16 v[224:227], v[208:211], v[192:199], %[sidx]\n\t" \
    "v_lshl_add_u32 v232, v233, 3, v232\n\t" \
    "v_permlane32_swap_b32 v244, v248\n\t" \
    "v_smfmac_f32_16x16x64_bf16 v[224:227], v[200:203], v[176:183], %[sidx]\n\t" \
    "v_permlane32_swap_b32 v245, v249\n\t" \
    "v_permlane32_swap_b32 v246, v250\n\t" \
    "v_permlane32_swap_b32 v247, v251\n\t" \
    "v_smfmac_f32_16x16x64_bf16 v[220:223], v[204:207], v[160:167], %[sidx]\n\t" \
    "v_add_f32 v244, v244, v248\n\t" \
    "v_add_f32 v245, v245, v249\n\t" \
    "v_add_f32 v246, v246, v250\n\t" \
    "v_smfmac_f32_16x16x64_bf16 v[220:223], v[208:211], v[168:175], %[sidx]\n\t" \
    "v_add_f32 v247, v247, v251\n\t" \
    "v_cvt_pk_bf16_f32 v244, v244, v245\n\t" \
    "v_cvt_pk_bf16_f32 v245, v246, v247\n\t" \
    "s_waitcnt lgkmcnt(0)\n\t" \
    "v_smfmac_f32_16x16x64_bf16 v[220:223], v[200:203], v[212:219], %[sidx]\n\t" \
    "v_min_u32 v244, -2, v244\n\t" \
    "v_min_u32 v245, -2, v245\n\t" \
    "s_nop 1\n\t" \
    "v_permlane16_swap_b32 v242, v244\n\t" \
    "v_permlane16_swap_b32 v243, v245\n\t" \
    "buffer_store_dwordx4 v[242:245], %[vo], %[rr], %[so] offen sc1\n\t" \
    "s_add_u32 %[so], %[so], 0x10000\n\t" \
    "s_nop 0\n\t" \
    "v_permlane32_swap_b32 v220, v224\n\t" \
    "v_permlane32_swap_b32 v221, v225\n\t" \
    "v_permlane32_swap_b32 v222, v226\n\t" \
    "v_permlane32_swap_b32 v223, v227\n\t" \
    "v_add_f32 v220, v220, v224\n\t" \
    "v_add_f32 v221, v221, v225\n\t" \
    "v_add_f32 v222, v222, v226\n\t" \
    "v_add_f32 v223, v223, v227\n\t" \
    "v_cvt_pk_bf16_f32 v220, v220, v221\n\t" \
    "v_cvt_pk_bf16_f32 v221, v222, v223\n\t" \
    "v_min_u32 v220, -2, v220\n\t" \
    "v_min_u32 v221, -2, v221\n\t" \
    "buffer_store_dwordx2 v[220:221], v232, %[rr], %[so] offen sc1\n\t" \
    "s_nop 1\n\t"
#define ASM_SM \
    "v_mbcnt_lo_u32_b32 v252, -1, 0\n\t" \
    "v_mbcnt_hi_u32_b32 v252, -1, v252\n\t" \
    "v_lshlrev_b32 v252, 4, v252\n\t" \
    "v_lshrrev_b32 v253, 1, v252\n\t" \
    "v_xor_b32 v253, v253, v252\n\t" \
    "v_and_b32 v253, 0x20, v253\n\t" \
    "v_mul_u32_u24 v253, 3, v253\n\t" \
    "v_xor_b32 v253, v253, v252\n\t" \
    "ds_read_b128 v[200:203], v253 offset:28672\n\t" \
    "ds_read_b128 v[204:207], v253 offset:29696\n\t" \
    "ds_read_b128 v[208:211], v253 offset:34816\n\t" \
    "ds_read_b128 v[212:215], v253 offset:35840\n\t" \
    "ds_read_b128 v[216:219], v253 offset:30720\n\t" \
    "ds_read_b128 v[220:223], v253 offset:31744\n\t" \
    "ds_read_b128 v[224:227], v253 offset:36864\n\t" \
    "ds_read_b128 v[228:231], v253 offset:37888\n\t" \
    "v_lshrrev_b32 v254, 1, v252\n\t" \
    "v_and_b32 v254, 0x180, v254\n\t" \
    "v_and_or_b32 v254, v252, %[c70], v254\n\t" \
    "v_bfe_i32 v255, v252, 7, 1\n\t" \
    "v_and_b32 v255, 0xaaaa, v255\n\t" \
    "v_or_b32 v255, 0x4444, v255\n\t" \
    "v_mov_b64 v[232:233], 0\n\t" \
    "v_mov_b64 v[234:235], 0\n\t" \
    "v_mov_b64 v[236:237], 0\n\t" \
    "v_mov_b64 v[238:239], 0\n\t" \
    "s_waitcnt lgkmcnt(0)\n\t" \
    "s_barrier\n\t" ASM_ST \
    "s_setprio 3\n\t" \
    "ds_read_b128 v[240:243], v252 offset:10240\n\t" \
    "ds_read_b128 v[244:247], v252 offset:11264\n\t" \
    "ds_read_b128 v[248:251], v252 offset:12288\n\t" \
    "s_waitcnt lgkmcnt(2)\n\t" \
    "v_smfmac_f32_16x16x64_bf16 v[232:235], v[240:243], v[200:207], v255\n\t" \
    "ds_read_b128 v[200:203], v253 offset:32768\n\t" \
    "ds_read_b128 v[204:207], v253 offset:33792\n\t" \
    "v_smfmac_f32_16x16x64_bf16 v[236:239], v[240:243], v[208:215], v255\n\t" \
    "ds_read_b128 v[208:211], v253 offset:38912\n\t" \
    "ds_read_b128 v[212:215], v253 offset:39936\n\t" \
    "s_waitcnt lgkmcnt(5)\n\t" \
    "v_smfmac_f32_16x16x64_bf16 v[232:235], v[244:247], v[216:223], v255\n\t" \
    "ds_read_b128 v[216:219], v254 offset:13312\n\t" \
    "ds_read_b128 v[220:223], v254 offset:13824\n\t" \
    "v_smfmac_f32_16x16x64_bf16 v[236:239], v[244:247], v[224:231], v255\n\t" \
    "ds_read_b128 v[224:227], v254 offset:14336\n\t" \
    "ds_read_b128 v[228:231], v254 offset:14848\n\t" \
    "s_waitcnt lgkmcnt(6)\n\t" \
    "v_smfmac_f32_16x16x64_bf16 v[232:235], v[248:251], v[200:207], v255\n\t" \
    "s_waitcnt lgkmcnt(4)\n\t" \
    "v_smfmac_f32_16x16x64_bf16 v[236:239], v[248:251], v[208:215], v255\n\t" \
    "ds_read_b128 v[208:211], v254 offset:15360\n\t" \
    "ds_read_b128 v[212:215], v254 offset:15872\n\t" \
    "v_mov_b64 v[204:205], 0\n\t" \
    "v_mov_b64 v[206:207], 0\n\t" \
    "s_nop 1\n\t" \
    "s_waitcnt lgkmcnt(4)\n\t" \
    "v_smfmac_f32_16x16x64_bf16 v[204:207], v[240:243], v[216:223], v255\n\t" \
    "s_waitcnt lgkmcnt(2)\n\t" \
    "v_smfmac_f32_16x16x64_bf16 v[204:207], v[244:247], v[224:231], v255\n\t" \
    "v_bfe_u32 v253, v252, 5, 1\n\t" \
    "v_bfe_i32 v202, v252, 4, 1\n\t" \
    "v_lshlrev_b32 v200, 1, v252\n\t" \
    "v_and_b32 v200, 0x180, v200\n\t" \
    "v_permlane32_swap_b32 v232, v234\n\t" \
    "v_permlane32_swap_b32 v233, v235\n\t" \
    "v_permlane32_swap_b32 v236, v238\n\t" \
    "v_permlane32_swap_b32 v237, v239\n\t" \
    "v_lshrrev_b32 v201, 2, v252\n\t" \
    "v_lshrrev_b32 v203, 4, v252\n\t" \
    "v_and_b32 v201, 0x40, v201\n\t" \
    "v_and_or_b32 v201, v203, 32, v201\n\t" \
    "v_and_or_b32 v201, v252, 16, v201\n\t" \
    "v_pk_add_f32 v[232:233], v[232:233], v[234:235]\n\t" \
    "v_pk_add_f32 v[234:235], v[236:237], v[238:239]\n\t" \
    "v_or_b32 v200, v200, v201\n\t" \
    "v_mad_u32_u24 v200, v253, %[cs], v200\n\t" \
    "v_mad_u32_u24 v201, v253, %[cs], v201\n\t" \
    "v_lshrrev_b32 v203, 3, v252\n\t" \
    "v_bfe_u32 v253, v252, 7, 1\n\t" \
    "s_waitcnt lgkmcnt(0)\n\t" \
    "v_smfmac_f32_16x16x64_bf16 v[204:207], v[248:251], v[208:215], v255\n\t" \
    "v_and_or_b32 v201, v203, 8, v201\n\t" \
    "v_and_b32 v202, 0x06060606, v202\n\t" \
    "v_mad_u32_u24 v201, v253, %[cp], v201\n\t" \
    "v_xor_b32 v202, 0x01000504, v202\n\t" \
    "v_max_f32 v236, v232, v234\n\t" \
    "v_max_f32 v237, v233, v235\n\t" \
    "s_nop 1\n\t" \
    "v_permlane32_swap_b32 v204, v206\n\t" \
    "v_permlane32_swap_b32 v205, v207\n\t" \
    "v_add_f32 v204, v204, v206\n\t" \
    "v_add_f32 v205, v205, v207\n\t" \
    "v_max_f32 v236, v236, v204\n\t" \
    "v_max_f32 v237, v237, v205\n\t" \
    "s_nop 0\n\t" \
    "v_max_f32_dpp v236, v236, v236 quad_perm:[1,0,3,2] row_mask:0xf bank_mask:0xf\n\t" \
    "v_max_f32_dpp v237, v237, v237 quad_perm:[1,0,3,2] row_mask:0xf bank_mask:0xf\n\t" \
    "s_nop 0\n\t" \
    "v_max_f32_dpp v236, v236, v236 quad_perm:[2,3,0,1] row_mask:0xf bank_mask:0xf\n\t" \
    "v_max_f32_dpp v237, v237, v237 quad_perm:[2,3,0,1] row_mask:0xf bank_mask:0xf\n\t" \
    "s_nop 0\n\t" \
    "v_max_f32_dpp v236, v236, v236 row_half_mirror row_mask:0xf bank_mask:0xf\n\t" \
    "v_max_f32_dpp v237, v237, v237 row_half_mirror row_mask:0xf bank_mask:0xf\n\t" \
    "s_nop 0\n\t" \
    "v_max_f32_dpp v236, v236, v236 row_mirror row_mask:0xf bank_mask:0xf\n\t" \
    "v_max_f32_dpp v237, v237, v237 row_mirror row_mask:0xf bank_mask:0xf\n\t" \
    "v_mul_f32 v236, %[scl], v236\n\t" \
    "v_mul_f32 v237, %[scl], v237\n\t" \
    "v_fma_f32 v232, v232, %[scl], -v236\n\t" \
    "v_fma_f32 v233, v233, %[scl], -v237\n\t" \
    "v_fma_f32 v234, v234, %[scl], -v236\n\t" \
    "v_fma_f32 v235, v235, %[scl], -v237\n\t" \
    "v_exp_f32 v232, v232\n\t" \
    "v_exp_f32 v233, v233\n\t" \
    "v_fma_f32 v204, v204, %[scl], -v236\n\t" \
    "v_fma_f32 v205, v205, %[scl], -v237\n\t" \
    "v_exp_f32 v234, v234\n\t" \
    "v_exp_f32 v235, v235\n\t" \
    "v_exp_f32 v204, v204\n\t" \
    "v_exp_f32 v205, v205\n\t" \
    "v_add_f32 v238, v232, v234\n\t" \
    "v_add_f32 v239, v233, v235\n\t" \
    "v_fma_f32 v238, v204, 0.5, v238\n\t" \
    "v_fma_f32 v239, v205, 0.5, v239\n\t" \
    "s_nop 0\n\t" \
    "v_add_f32_dpp v238, v238, v238 quad_perm:[1,0,3,2] row_mask:0xf bank_mask:0xf\n\t" \
    "v_add_f32_dpp v239, v239, v239 quad_perm:[1,0,3,2] row_mask:0xf bank_mask:0xf\n\t" \
    "s_nop 0\n\t" \
    "v_add_f32_dpp v238, v238, v238 quad_perm:[2,3,0,1] row_mask:0xf bank_mask:0xf\n\t" \
    "v_add_f32_dpp v239, v239, v239 quad_perm:[2,3,0,1] row_mask:0xf bank_mask:0xf\n\t" \
    "s_nop 0\n\t" \
    "v_add_f32_dpp v238, v238, v238 row_half_mirror row_mask:0xf bank_mask:0xf\n\t" \
    "v_add_f32_dpp v239, v239, v239 row_half_mirror row_mask:0xf bank_mask:0xf\n\t" \
    "s_nop 0\n\t" \
    "v_add_f32_dpp v238, v238, v238 row_mirror row_mask:0xf bank_mask:0xf\n\t" \
    "v_add_f32_dpp v239, v239, v239 row_mirror row_mask:0xf bank_mask:0xf\n\t" \
    "v_rcp_f32 v238, v238\n\t" \
    "v_rcp_f32 v239, v239\n\t" \
    "s_nop 0\n\t" \
    "v_pk_mul_f32 v[232:233], v[232:233], v[238:239]\n\t" \
    "v_pk_mul_f32 v[234:235], v[234:235], v[238:239]\n\t" \
    "v_pk_mul_f32 v[204:205], v[204:205], v[238:239]\n\t" \
    "v_cvt_pk_bf16_f32 v232, |v232|, |v233|\n\t" \
    "v_cvt_pk_bf16_f32 v234, |v234|, |v235|\n\t" \
    "v_cvt_pk_bf16_f32 v204, |v204|, |v205|\n\t" \
    "v_mov_b32_dpp v233, v232 quad_perm:[1,0,3,2] row_mask:0xf bank_mask:0xf\n\t" \
    "v_mov_b32_dpp v235, v234 quad_perm:[1,0,3,2] row_mask:0xf bank_mask:0xf\n\t" \
    "v_mov_b32_dpp v205, v204 quad_perm:[1,0,3,2] row_mask:0xf bank_mask:0xf\n\t" \
    "v_perm_b32 v232, v232, v233, v202\n\t" \
    "v_perm_b32 v234, v234, v235, v202\n\t" \
    "v_perm_b32 v204, v204, v205, v202\n\t" \
    "v_mov_b32_dpp v240, v232 quad_perm:[0,1,0,1] row_mask:0xf bank_mask:0xf\n\t" \
    "v_mov_b32_dpp v241, v234 quad_perm:[0,1,0,1] row_mask:0xf bank_mask:0xf\n\t" \
    "v_mov_b32_dpp v242, v232 quad_perm:[2,3,2,3] row_mask:0xf bank_mask:0xf\n\t" \
    "v_mov_b32_dpp v243, v234 quad_perm:[2,3,2,3] row_mask:0xf bank_mask:0xf\n\t" \
    "buffer_store_dwordx4 v[240:243], v200, %[rr], %[sx] offen sc1\n\t" \
    "v_mov_b32_dpp v244, v204 quad_perm:[0,1,0,1] row_mask:0xf bank_mask:0xf\n\t" \
    "v_mov_b32_dpp v245, v204 quad_perm:[2,3,2,3] row_mask:0xf bank_mask:0xf\n\t" \
    "buffer_store_dwordx4 v[240:243], v200, %[rr], %[sy] offen sc1\n\t" \
    "buffer_store_dwordx2 v[244:245], v201, %[rr], %[sm] offen sc1\n\t" \
    "s_wakeup\n\t" \
    PRISM
#define ASM_P4_0 \
    "ds_read_b128 v[200:203], %[qa] offset:0\n\t" \
    "ds_read_b128 v[204:207], %[qa] offset:1024\n\t" \
    "ds_read_b128 v[208:211], %[qa] offset:2048\n\t" \
    "ds_read_b128 v[212:215], %[qa] offset:3072\n\t" \
    "ds_read_b128 v[216:219], %[qa] offset:4096\n\t" \
    "ds_read_b128 v[220:223], %[qa] offset:5120\n\t" \
    "ds_read_b128 v[224:227], %[qa] offset:12288\n\t" \
    "ds_read_b128 v[228:231], %[qa] offset:13312\n\t" \
    "ds_read_b128 v[232:235], %[qa] offset:14336\n\t" \
    "ds_read_b128 v[236:239], %[qa] offset:15360\n\t" \
    "s_waitcnt lgkmcnt(0)\n\t" \
    "s_barrier\n\t" ASM_ST \
    "v_mfma_f32_16x16x32_bf16 v[240:243], v[224:227], v[200:203], v[240:243]\n\t" \
    "ds_read_b128 v[224:227], %[qa] offset:16384\n\t" \
    "v_mfma_f32_16x16x32_bf16 v[240:243], v[228:231], v[204:207], v[240:243]\n\t" \
    "ds_read_b128 v[228:231], %[qa] offset:17408\n\t" \
    "v_mfma_f32_16x16x32_bf16 v[240:243], v[232:235], v[208:211], v[240:243]\n\t" \
    "ds_read_b128 v[232:235], %[km] offset:0\n\t" \
    "v_mfma_f32_16x16x32_bf16 v[240:243], v[236:239], v[212:215], v[240:243]\n\t" \
    "ds_read_b128 v[236:239], %[km] offset:512\n\t" \
    "s_waitcnt lgkmcnt(3)\n\t" \
    "v_mfma_f32_16x16x32_bf16 v[240:243], v[224:227], v[216:219], v[240:243]\n\t" \
    "ds_read_b128 v[224:227], %[km] offset:1024\n\t" \
    "s_waitcnt lgkmcnt(3)\n\t" \
    "v_mfma_f32_16x16x32_bf16 v[240:243], v[228:231], v[220:223], v[240:243]\n\t" \
    "ds_read_b128 v[228:231], %[km] offset:1536\n\t" \
    "s_waitcnt lgkmcnt(3)\n\t" \
    "v_mfma_f32_16x16x32_bf16 v[248:251], v[232:235], v[200:203], 0\n\t" \
    "ds_read_b128 v[232:235], %[km] offset:2048\n\t" \
    "s_waitcnt lgkmcnt(3)\n\t" \
    "v_mfma_f32_16x16x32_bf16 v[248:251], v[236:239], v[204:207], v[248:251]\n\t" \
    "ds_read_b128 v[236:239], %[km] offset:2560\n\t" \
    "s_waitcnt lgkmcnt(3)\n\t" \
    "v_mfma_f32_16x16x32_bf16 v[248:251], v[224:227], v[208:211], v[248:251]\n\t" \
    "s_waitcnt lgkmcnt(2)\n\t" \
    "v_mfma_f32_16x16x32_bf16 v[248:251], v[228:231], v[212:215], v[248:251]\n\t" \
    "s_waitcnt lgkmcnt(1)\n\t" \
    "v_mfma_f32_16x16x32_bf16 v[248:251], v[232:235], v[216:219], v[248:251]\n\t" \
    "s_waitcnt lgkmcnt(0)\n\t" \
    "v_mfma_f32_16x16x32_bf16 v[248:251], v[236:239], v[220:223], v[248:251]\n\t" \
    "v_max3_f32 v224, v240, v241, v242\n\t" \
    "v_mov_b32 v225, v243\n\t" \
    "v_lshlrev_b32 v201, 23, %[qa]\n\t" \
    "v_and_b32 v201, 0x80000000, v201\n\t" \
    "v_and_b32 v200, 0x3ff, %[qa]\n\t" \
    "v_or_b32 v200, v201, v200\n\t" \
    "s_nop 1\n\t" \
    "v_max3_f32 v224, v224, v248, v249\n\t" \
    "v_max3_f32 v225, v225, v250, v251\n\t" \
    "v_max_f32 v224, v224, v225\n\t" \
    "v_mov_b32 v225, v224\n\t" \
    "s_nop 1\n\t" \
    "v_permlane16_swap_b32 v224, v225\n\t" \
    "v_max_f32 v224, v224, v225\n\t" \
    "v_mov_b32 v225, v224\n\t" \
    "s_nop 1\n\t" \
    "v_permlane32_swap_b32 v224, v225\n\t" \
    "v_max_f32 v224, v224, v225\n\t" \
    "v_mul_f32 v224, %[scl], v224\n\t" \
    "v_fma_f32 v240, v240, %[scl], -v224\n\t" \
    "v_fma_f32 v241, v241, %[scl], -v224\n\t" \
    "v_exp_f32 v240, v240\n\t" \
    "v_fma_f32 v242, v242, %[scl], -v224\n\t" \
    "v_exp_f32 v241, v241\n\t" \
    "v_fma_f32 v243, v243, %[scl], -v224\n\t" \
    "v_exp_f32 v242, v242\n\t" \
    "v_fma_f32 v248, v248, %[scl], -v224\n\t" \
    "v_exp_f32 v243, v243\n\t" \
    "v_fma_f32 v249, v249, %[scl], -v224\n\t" \
    "v_exp_f32 v248, v248\n\t" \
    "v_fma_f32 v250, v250, %[scl], -v224\n\t" \
    "v_exp_f32 v249, v249\n\t" \
    "v_fma_f32 v251, v251, %[scl], -v224\n\t" \
    "v_exp_f32 v250, v250\n\t" \
    "v_exp_f32 v251, v251\n\t" \
    "v_add_f32 v224, v240, v241\n\t" \
    "v_add_f32 v225, v242, v243\n\t" \
    "v_add_f32 v232, v248, v249\n\t" \
    "v_add_f32 v233, v250, v251\n\t" \
    "v_add_f32 v224, v224, v225\n\t" \
    "v_add_f32 v232, v232, v233\n\t" \
    "v_fma_f32 v224, v232, 0.5, v224\n\t" \
    "v_mov_b32 v225, v224\n\t" \
    "s_nop 1\n\t" \
    "v_permlane16_swap_b32 v224, v225\n\t" \
    "v_add_f32 v224, v224, v225\n\t" \
    "v_mov_b32 v225, v224\n\t" \
    "s_nop 1\n\t" \
    "v_permlane32_swap_b32 v224, v225\n\t" \
    "v_add_f32 v224, v224, v225\n\t" \
    "v_rcp_f32 v224, v224\n\t" \
    "s_nop 0\n\t" \
    "v_mul_f32 v240, v240, v224\n\t" \
    "v_mul_f32 v241, v241, v224\n\t" \
    "v_mul_f32 v242, v242, v224\n\t" \
    "v_mul_f32 v243, v243, v224\n\t" \
    "v_mul_f32 v248, v248, v224\n\t" \
    "v_mul_f32 v249, v249, v224\n\t" \
    "v_mul_f32 v250, v250, v224\n\t" \
    "v_mul_f32 v251, v251, v224\n\t" \
    "v_cvt_pk_bf16_f32 v228, |v240|, |v241|\n\t" \
    "v_cvt_pk_bf16_f32 v229, |v242|, |v243|\n\t" \
    "v_cvt_pk_bf16_f32 v232, |v248|, |v249|\n\t" \
    "v_cvt_pk_bf16_f32 v233, |v250|, |v251|\n\t" \
    "v_permlane16_swap_b32 v228, v230\n\t" \
    "v_permlane16_swap_b32 v229, v231\n\t" \
    "buffer_store_dwordx4 v[228:231], v200, %[rr], %[so] offen sc1\n\t" \
    "buffer_store_dwordx2 v[232:233], %[pm], %[rr], %[so] offen sc1\n\t" \
    "s_nop 1\n\t"
#define ASM_P4_1 \
    "ds_read_b128 v[200:203], %[qa] offset:0\n\t" \
    "ds_read_b128 v[204:207], %[qa] offset:1024\n\t" \
    "ds_read_b128 v[208:211], %[qa] offset:2048\n\t" \
    "ds_read_b128 v[212:215], %[qa] offset:3072\n\t" \
    "ds_read_b128 v[216:219], %[qa] offset:4096\n\t" \
    "ds_read_b128 v[220:223], %[qa] offset:5120\n\t" \
    "ds_read_b128 v[224:227], %[qa] offset:12288\n\t" \
    "ds_read_b128 v[228:231], %[qa] offset:13312\n\t" \
    "ds_read_b128 v[232:235], %[qa] offset:14336\n\t" \
    "ds_read_b128 v[236:239], %[qa] offset:15360\n\t" \
    "s_waitcnt lgkmcnt(0)\n\t" \
    "s_barrier\n\t" ASM_ST \
    "v_mfma_f32_16x16x32_bf16 v[240:243], v[224:227], v[200:203], v[240:243]\n\t" \
    "ds_read_b128 v[224:227], %[qa] offset:16384\n\t" \
    "v_mfma_f32_16x16x32_bf16 v[240:243], v[228:231], v[204:207], v[240:243]\n\t" \
    "ds_read_b128 v[228:231], %[qa] offset:17408\n\t" \
    "v_mfma_f32_16x16x32_bf16 v[240:243], v[232:235], v[208:211], v[240:243]\n\t" \
    "ds_read_b128 v[232:235], %[qa] offset:6144\n\t" \
    "v_mfma_f32_16x16x32_bf16 v[240:243], v[236:239], v[212:215], v[240:243]\n\t" \
    "ds_read_b128 v[236:239], %[qa] offset:7168\n\t" \
    "s_waitcnt lgkmcnt(3)\n\t" \
    "v_mfma_f32_16x16x32_bf16 v[240:243], v[224:227], v[216:219], v[240:243]\n\t" \
    "ds_read_b128 v[224:227], %[qa] offset:8192\n\t" \
    "s_waitcnt lgkmcnt(3)\n\t" \
    "v_mfma_f32_16x16x32_bf16 v[240:243], v[228:231], v[220:223], v[240:243]\n\t" \
    "ds_read_b128 v[228:231], %[qa] offset:9216\n\t" \
    "s_waitcnt lgkmcnt(3)\n\t" \
    "v_mfma_f32_16x16x32_bf16 v[244:247], v[232:235], v[200:203], 0\n\t" \
    "ds_read_b128 v[232:235], %[qa] offset:10240\n\t" \
    "s_waitcnt lgkmcnt(3)\n\t" \
    "v_mfma_f32_16x16x32_bf16 v[244:247], v[236:239], v[204:207], v[244:247]\n\t" \
    "ds_read_b128 v[236:239], %[qa] offset:11264\n\t" \
    "s_waitcnt lgkmcnt(3)\n\t" \
    "v_mfma_f32_16x16x32_bf16 v[244:247], v[224:227], v[208:211], v[244:247]\n\t" \
    "ds_read_b128 v[224:227], %[km] offset:0\n\t" \
    "s_waitcnt lgkmcnt(3)\n\t" \
    "v_mfma_f32_16x16x32_bf16 v[244:247], v[228:231], v[212:215], v[244:247]\n\t" \
    "ds_read_b128 v[228:231], %[km] offset:512\n\t" \
    "s_waitcnt lgkmcnt(3)\n\t" \
    "v_mfma_f32_16x16x32_bf16 v[244:247], v[232:235], v[216:219], v[244:247]\n\t" \
    "ds_read_b128 v[232:235], %[km] offset:1024\n\t" \
    "s_waitcnt lgkmcnt(3)\n\t" \
    "v_mfma_f32_16x16x32_bf16 v[244:247], v[236:239], v[220:223], v[244:247]\n\t" \
    "ds_read_b128 v[236:239], %[km] offset:1536\n\t" \
    "s_waitcnt lgkmcnt(3)\n\t" \
    "v_mfma_f32_16x16x32_bf16 v[248:251], v[224:227], v[200:203], 0\n\t" \
    "ds_read_b128 v[224:227], %[km] offset:2048\n\t" \
    "s_waitcnt lgkmcnt(3)\n\t" \
    "v_mfma_f32_16x16x32_bf16 v[248:251], v[228:231], v[204:207], v[248:251]\n\t" \
    "ds_read_b128 v[228:231], %[km] offset:2560\n\t" \
    "s_waitcnt lgkmcnt(3)\n\t" \
    "v_mfma_f32_16x16x32_bf16 v[248:251], v[232:235], v[208:211], v[248:251]\n\t" \
    "s_waitcnt lgkmcnt(2)\n\t" \
    "v_mfma_f32_16x16x32_bf16 v[248:251], v[236:239], v[212:215], v[248:251]\n\t" \
    "s_waitcnt lgkmcnt(1)\n\t" \
    "v_mfma_f32_16x16x32_bf16 v[248:251], v[224:227], v[216:219], v[248:251]\n\t" \
    "s_waitcnt lgkmcnt(0)\n\t" \
    "v_mfma_f32_16x16x32_bf16 v[248:251], v[228:231], v[220:223], v[248:251]\n\t" \
    "v_max3_f32 v224, v240, v241, v242\n\t" \
    "v_max3_f32 v225, v244, v245, v246\n\t" \
    "v_max3_f32 v224, v224, v243, v247\n\t" \
    "v_and_b32 v200, 0x3ff, %[qa]\n\t" \
    "s_nop 3\n\t" \
    "v_max3_f32 v224, v224, v248, v249\n\t" \
    "v_max3_f32 v225, v225, v250, v251\n\t" \
    "v_max_f32 v224, v224, v225\n\t" \
    "v_mov_b32 v225, v224\n\t" \
    "s_nop 1\n\t" \
    "v_permlane16_swap_b32 v224, v225\n\t" \
    "v_max_f32 v224, v224, v225\n\t" \
    "v_mov_b32 v225, v224\n\t" \
    "s_nop 1\n\t" \
    "v_permlane32_swap_b32 v224, v225\n\t" \
    "v_max_f32 v224, v224, v225\n\t" \
    "v_mul_f32 v224, %[scl], v224\n\t" \
    "v_fma_f32 v240, v240, %[scl], -v224\n\t" \
    "v_fma_f32 v241, v241, %[scl], -v224\n\t" \
    "v_exp_f32 v240, v240\n\t" \
    "v_fma_f32 v242, v242, %[scl], -v224\n\t" \
    "v_exp_f32 v241, v241\n\t" \
    "v_fma_f32 v243, v243, %[scl], -v224\n\t" \
    "v_exp_f32 v242, v242\n\t" \
    "v_fma_f32 v244, v244, %[scl], -v224\n\t" \
    "v_exp_f32 v243, v243\n\t" \
    "v_fma_f32 v245, v245, %[scl], -v224\n\t" \
    "v_exp_f32 v244, v244\n\t" \
    "v_fma_f32 v246, v246, %[scl], -v224\n\t" \
    "v_exp_f32 v245, v245\n\t" \
    "v_fma_f32 v247, v247, %[scl], -v224\n\t" \
    "v_exp_f32 v246, v246\n\t" \
    "v_fma_f32 v248, v248, %[scl], -v224\n\t" \
    "v_exp_f32 v247, v247\n\t" \
    "v_fma_f32 v249, v249, %[scl], -v224\n\t" \
    "v_exp_f32 v248, v248\n\t" \
    "v_fma_f32 v250, v250, %[scl], -v224\n\t" \
    "v_exp_f32 v249, v249\n\t" \
    "v_fma_f32 v251, v251, %[scl], -v224\n\t" \
    "v_exp_f32 v250, v250\n\t" \
    "v_exp_f32 v251, v251\n\t" \
    "v_add_f32 v224, v240, v241\n\t" \
    "v_add_f32 v225, v242, v243\n\t" \
    "v_add_f32 v226, v244, v245\n\t" \
    "v_add_f32 v227, v246, v247\n\t" \
    "v_add_f32 v232, v248, v249\n\t" \
    "v_add_f32 v233, v250, v251\n\t" \
    "v_add_f32 v224, v224, v225\n\t" \
    "v_add_f32 v226, v226, v227\n\t" \
    "v_add_f32 v232, v232, v233\n\t" \
    "v_add_f32 v224, v224, v226\n\t" \
    "v_fma_f32 v224, v232, 0.5, v224\n\t" \
    "v_mov_b32 v225, v224\n\t" \
    "s_nop 1\n\t" \
    "v_permlane16_swap_b32 v224, v225\n\t" \
    "v_add_f32 v224, v224, v225\n\t" \
    "v_mov_b32 v225, v224\n\t" \
    "s_nop 1\n\t" \
    "v_permlane32_swap_b32 v224, v225\n\t" \
    "v_add_f32 v224, v224, v225\n\t" \
    "v_rcp_f32 v224, v224\n\t" \
    "s_nop 0\n\t" \
    "v_mul_f32 v240, v240, v224\n\t" \
    "v_mul_f32 v241, v241, v224\n\t" \
    "v_mul_f32 v242, v242, v224\n\t" \
    "v_mul_f32 v243, v243, v224\n\t" \
    "v_mul_f32 v244, v244, v224\n\t" \
    "v_mul_f32 v245, v245, v224\n\t" \
    "v_mul_f32 v246, v246, v224\n\t" \
    "v_mul_f32 v247, v247, v224\n\t" \
    "v_mul_f32 v248, v248, v224\n\t" \
    "v_mul_f32 v249, v249, v224\n\t" \
    "v_mul_f32 v250, v250, v224\n\t" \
    "v_mul_f32 v251, v251, v224\n\t" \
    "v_cvt_pk_bf16_f32 v230, |v240|, |v241|\n\t" \
    "v_cvt_pk_bf16_f32 v231, |v242|, |v243|\n\t" \
    "v_cvt_pk_bf16_f32 v228, |v244|, |v245|\n\t" \
    "v_cvt_pk_bf16_f32 v229, |v246|, |v247|\n\t" \
    "v_cvt_pk_bf16_f32 v232, |v248|, |v249|\n\t" \
    "v_cvt_pk_bf16_f32 v233, |v250|, |v251|\n\t" \
    "buffer_store_dwordx4 v[228:231], v200, %[rr], %[so] offen sc1\n\t" \
    "buffer_store_dwordx2 v[232:233], %[pm], %[rr], %[so] offen sc1\n\t" \
    "s_nop 1\n\t"
#define ASM_RCRIT \
    "v_mov_b64 v[176:177], -1\n\t" \
    "v_mov_b64 v[178:179], -1\n\t" \
    "v_mov_b64 v[180:181], -1\n\t" \
    "v_mov_b64 v[182:183], -1\n\t" \
    "v_mov_b64 v[184:185], -1\n\t" \
    "v_mov_b64 v[186:187], -1\n\t" \
    "v_mov_b64 v[188:189], -1\n\t" \
    "v_mov_b64 v[190:191], -1\n\t" \
    "v_mov_b64 v[192:193], -1\n\t" \
    "v_mov_b64 v[194:195], -1\n\t" \
    ".Lfa%=:\n\t" \
    "s_mov_b32 %[a], 0\n\t" \
    "v_max_u32 v196, v176, v177\n\t" \
    "v_max3_u32 v196, v178, v179, v196\n\t" \
    "v_cmp_eq_u32 vcc, -1, v196\n\t" \
    "s_cbranch_vccz .Lga0_%=\n\t" \
    "buffer_load_dwordx4 v[176:179], %[ov], %[rs], %[sv] offen sc1\n\t" \
    "s_mov_b32 %[a], 1\n\t" \
    ".Lga0_%=:\n\t" \
    "v_max_u32 v196, v180, v181\n\t" \
    "v_max3_u32 v196, v182, v183, v196\n\t" \
    "v_cmp_eq_u32 vcc, -1, v196\n\t" \
    "s_cbranch_vccz .Lga1_%=\n\t" \
    "buffer_load_dwordx4 v[180:183], %[ov], %[rs], %[sv] offen offset:1024 sc1\n\t" \
    "s_mov_b32 %[a], 1\n\t" \
    ".Lga1_%=:\n\t" \
    "s_cmp_eq_u32 %[a], 0\n\t" \
    "s_cbranch_scc1 .Lha%=\n\t" \
    "s_waitcnt vmcnt(12)\n\t" \
    ".Lia%=:\n\t" \
    "v_max_u32 v196, v176, v177\n\t" \
    "v_max3_u32 v196, v178, v179, v196\n\t" \
    "v_max3_u32 v196, v180, v181, v196\n\t" \
    "v_max3_u32 v196, v182, v183, v196\n\t" \
    "v_cmp_eq_u32 vcc, -1, v196\n\t" \
    "s_cbranch_vccz .Lha%=\n\t" \
    "s_nop 7\n\t" \
    "v_max_u32 v196, v176, v177\n\t" \
    "v_max3_u32 v196, v178, v179, v196\n\t" \
    "v_max3_u32 v196, v180, v181, v196\n\t" \
    "v_max3_u32 v196, v182, v183, v196\n\t" \
    "v_cmp_eq_u32 vcc, -1, v196\n\t" \
    "s_cbranch_vccz .Lha%=\n\t" \
    "s_nop 7\n\t" \
    "v_max_u32 v196, v176, v177\n\t" \
    "v_max3_u32 v196, v178, v179, v196\n\t" \
    "v_max3_u32 v196, v180, v181, v196\n\t" \
    "v_max3_u32 v196, v182, v183, v196\n\t" \
    "v_cmp_eq_u32 vcc, -1, v196\n\t" \
    "s_cbranch_vccz .Lha%=\n\t" \
    "s_nop 7\n\t" \
    "v_max_u32 v196, v176, v177\n\t" \
    "v_max3_u32 v196, v178, v179, v196\n\t" \
    "v_max3_u32 v196, v180, v181, v196\n\t" \
    "v_max3_u32 v196, v182, v183, v196\n\t" \
    "v_cmp_eq_u32 vcc, -1, v196\n\t" \
    "s_cbranch_vccz .Lha%=\n\t" \
    "s_nop 7\n\t" \
    "s_sub_u32 %[n], %[n], 1\n\t" \
    "s_cmp_gt_i32 %[n], 0\n\t" \
    "s_cbranch_scc1 .Lfa%=\n\t" \
    ".Lha%=:\n\t" \
    ASM_ST3 \
    "buffer_load_dwordx4 v[184:187], %[o0], %[rs], %[sp] offen sc1\n\t" \
    "buffer_load_dwordx4 v[188:191], %[o0], %[rs], %[sp] offen offset:1024 sc1\n\t" \
    "buffer_load_dwordx4 v[192:195], %[o2], %[rs], %[sp] offen sc1\n\t" \
    "s_waitcnt vmcnt(0)\n\t" \
    "v_max_u32 v196, v184, v185\n\t" \
    "v_max3_u32 v196, v186, v187, v196\n\t" \
    "v_max3_u32 v196, v188, v189, v196\n\t" \
    "v_max3_u32 v196, v190, v191, v196\n\t" \
    "v_max3_u32 v196, v192, v193, v196\n\t" \
    "v_max3_u32 v196, v194, v195, v196\n\t" \
    "v_cmp_eq_u32 vcc, -1, v196\n\t" \
    "s_cbranch_vccz .Lhb%=\n\t" \
    ".Lfb%=:\n\t" \
    "s_mov_b32 %[a], 0\n\t" \
    "v_max_u32 v196, v184, v185\n\t" \
    "v_max3_u32 v196, v186, v187, v196\n\t" \
    "v_cmp_eq_u32 vcc, -1, v196\n\t" \
    "s_cbranch_vccz .Lgb0_%=\n\t" \
    "buffer_load_dwordx4 v[184:187], %[o0], %[rs], %[sp] offen sc1\n\t" \
    "s_mov_b32 %[a], 1\n\t" \
    ".Lgb0_%=:\n\t" \
    "v_max_u32 v196, v188, v189\n\t" \
    "v_max3_u32 v196, v190, v191, v196\n\t" \
    "v_cmp_eq_u32 vcc, -1, v196\n\t" \
    "s_cbranch_vccz .Lgb1_%=\n\t" \
    "buffer_load_dwordx4 v[188:191], %[o0], %[rs], %[sp] offen offset:1024 sc1\n\t" \
    "s_mov_b32 %[a], 1\n\t" \
    ".Lgb1_%=:\n\t" \
    "v_max_u32 v196, v192, v193\n\t" \
    "v_max3_u32 v196, v194, v195, v196\n\t" \
    "v_cmp_eq_u32 vcc, -1, v196\n\t" \
    "s_cbranch_vccz .Lgb2_%=\n\t" \
    "buffer_load_dwordx4 v[192:195], %[o2], %[rs], %[sp] offen sc1\n\t" \
    "s_mov_b32 %[a], 1\n\t" \
    ".Lgb2_%=:\n\t" \
    "s_cmp_eq_u32 %[a], 0\n\t" \
    "s_cbranch_scc1 .Lhb%=\n\t" \
    "s_waitcnt vmcnt(12)\n\t" \
    ".Lib%=:\n\t" \
    "v_max_u32 v196, v184, v185\n\t" \
    "v_max3_u32 v196, v186, v187, v196\n\t" \
    "v_max3_u32 v196, v188, v189, v196\n\t" \
    "v_max3_u32 v196, v190, v191, v196\n\t" \
    "v_max3_u32 v196, v192, v193, v196\n\t" \
    "v_max3_u32 v196, v194, v195, v196\n\t" \
    "v_cmp_eq_u32 vcc, -1, v196\n\t" \
    "s_cbranch_vccz .Lhb%=\n\t" \
    "s_nop 7\n\t" \
    "v_max_u32 v196, v184, v185\n\t" \
    "v_max3_u32 v196, v186, v187, v196\n\t" \
    "v_max3_u32 v196, v188, v189, v196\n\t" \
    "v_max3_u32 v196, v190, v191, v196\n\t" \
    "v_max3_u32 v196, v192, v193, v196\n\t" \
    "v_max3_u32 v196, v194, v195, v196\n\t" \
    "v_cmp_eq_u32 vcc, -1, v196\n\t" \
    "s_cbranch_vccz .Lhb%=\n\t" \
    "s_nop 7\n\t" \
    "v_max_u32 v196, v184, v185\n\t" \
    "v_max3_u32 v196, v186, v187, v196\n\t" \
    "v_max3_u32 v196, v188, v189, v196\n\t" \
    "v_max3_u32 v196, v190, v191, v196\n\t" \
    "v_max3_u32 v196, v192, v193, v196\n\t" \
    "v_max3_u32 v196, v194, v195, v196\n\t" \
    "v_cmp_eq_u32 vcc, -1, v196\n\t" \
    "s_cbranch_vccz .Lhb%=\n\t" \
    "s_nop 7\n\t" \
    "v_max_u32 v196, v184, v185\n\t" \
    "v_max3_u32 v196, v186, v187, v196\n\t" \
    "v_max3_u32 v196, v188, v189, v196\n\t" \
    "v_max3_u32 v196, v190, v191, v196\n\t" \
    "v_max3_u32 v196, v192, v193, v196\n\t" \
    "v_max3_u32 v196, v194, v195, v196\n\t" \
    "v_cmp_eq_u32 vcc, -1, v196\n\t" \
    "s_cbranch_vccz .Lhb%=\n\t" \
    "s_nop 7\n\t" \
    "s_sub_u32 %[n], %[n], 1\n\t" \
    "s_cmp_gt_i32 %[n], 0\n\t" \
    "s_cbranch_scc1 .Lfb%=\n\t" \
    ".Lhb%=:\n\t" \
    ASM_ST2 \
    "v_mfma_f32_16x16x32_bf16 v[160:163], v[192:195], v[176:179], 0\n\t" \
    "v_mfma_f32_16x16x32_bf16 v[164:167], v[192:195], v[180:183], 0\n\t" \
    "v_smfmac_f32_16x16x64_bf16 v[160:163], v[184:187], v[128:135], %[sidx]\n\t" \
    "v_smfmac_f32_16x16x64_bf16 v[164:167], v[184:187], v[144:151], %[sidx]\n\t" \
    "v_smfmac_f32_16x16x64_bf16 v[160:163], v[188:191], v[136:143], %[sidx]\n\t" \
    "v_smfmac_f32_16x16x64_bf16 v[164:167], v[188:191], v[152:159], %[sidx]\n\t" \
    "s_cmp_eq_u32 %[pub], 0\n\t" \
    "s_nop 7\n\t" \
    "v_permlane32_swap_b32 v160, v164\n\t" \
    "v_permlane32_swap_b32 v161, v165\n\t" \
    "v_permlane32_swap_b32 v162, v166\n\t" \
    "v_permlane32_swap_b32 v163, v167\n\t" \
    "v_add_f32 v160, v160, v164\n\t" \
    "v_add_f32 v161, v161, v165\n\t" \
    "ds_write2_b32 %[rw], v160, v161 offset1:80\n\t" \
    "v_add_f32 v162, v162, v166\n\t" \
    "v_add_f32 v163, v163, v167\n\t" \
    "ds_write2_b32 %[rw], v162, v163 offset0:160 offset1:240\n\t" \
    "s_waitcnt lgkmcnt(0)\n\t" \
    "s_barrier\n\t" ASM_ST \
    "s_cbranch_scc1 .Lre%=\n\t" \
    "ds_read_b128 v[200:203], %[rd] offset:0\n\t" \
    "ds_read_b128 v[204:207], %[rd] offset:16\n\t" \
    "s_waitcnt lgkmcnt(1)\n\t" \
    "v_pk_add_f32 v[200:201], v[200:201], v[202:203]\n\t" \
    "s_waitcnt lgkmcnt(0)\n\t" \
    "v_pk_add_f32 v[204:205], v[204:205], v[206:207]\n\t" \
    "v_pk_add_f32 v[200:201], v[200:201], v[204:205]\n\t" \
    "s_nop 1\n\t" \
    "v_permlane32_swap_b32 v200, v201\n\t" \
    "v_add_f32 v202, v200, v201\n\t" \
    "v_add_f32 v203, v200, v201\n\t" \
    "s_nop 1\n\t" \
    "v_permlane32_swap_b32 v202, v203\n\t" \
    "v_cvt_pk_bf16_f32 v200, v202, v203\n\t" \
    "v_min_u32 v200, -2, v200\n\t" \
    "buffer_store_dword v200, %[oo], %[rs], %[so] offen sc1\n\t" \
    ".Lre%=:\n\t"
#define ASM_RX \
    "s_cmp_eq_u32 %[fin], 0\n\t" \
    "s_cbranch_scc1 .Lx0%=\n\t" \
    "buffer_load_dwordx2 v[250:251], %[xo], %[rx], %[sx] offen sc1\n\t" \
    ".Lx0%=:\n\t" \
    "buffer_load_dword v248, %[fo], %[rs], %[fs] offen sc1\n\t" \
    "buffer_load_dwordx4 v[200:203], %[l16], %[rs], %[s0] offen offset:0 sc1\n\t" \
    "buffer_load_dwordx4 v[204:207], %[l16], %[rs], %[s0] offen offset:1024 sc1\n\t" \
    "buffer_load_dwordx4 v[208:211], %[l16], %[rs], %[s0] offen offset:2048 sc1\n\t" \
    "buffer_load_dwordx4 v[212:215], %[l16], %[rs], %[s0] offen offset:3072 sc1\n\t" \
    "buffer_load_dwordx4 v[216:219], %[l16], %[rs], %[s1] offen offset:0 sc1\n\t" \
    "buffer_load_dwordx4 v[220:223], %[l16], %[rs], %[s1] offen offset:1024 sc1\n\t" \
    "buffer_load_dwordx4 v[224:227], %[l16], %[rs], %[s1] offen offset:2048 sc1\n\t" \
    "buffer_load_dwordx4 v[228:231], %[l16], %[rs], %[s1] offen offset:3072 sc1\n\t" \
    "v_mov_b64 v[96:97], v[128:129]\n\t" \
    "v_mov_b64 v[98:99], v[132:133]\n\t" \
    "v_mov_b64 v[100:101], v[136:137]\n\t" \
    "v_mov_b64 v[102:103], v[140:141]\n\t" \
    "v_mov_b64 v[104:105], v[144:145]\n\t" \
    "v_mov_b64 v[106:107], v[148:149]\n\t" \
    "v_mov_b64 v[108:109], v[152:153]\n\t" \
    "v_mov_b64 v[110:111], v[156:157]\n\t" \
    "s_movk_i32 %[n], 0x200\n\t" \
    "s_nop 1\n\t" \
    "v_permlane16_swap_b32 v96, v98\n\t" \
    "v_permlane16_swap_b32 v97, v99\n\t" \
    "v_permlane16_swap_b32 v100, v102\n\t" \
    "v_permlane16_swap_b32 v101, v103\n\t" \
    "v_permlane16_swap_b32 v104, v106\n\t" \
    "v_permlane16_swap_b32 v105, v107\n\t" \
    "v_permlane16_swap_b32 v108, v110\n\t" \
    "v_permlane16_swap_b32 v109, v111\n\t" \
    "s_waitcnt vmcnt(7)\n\t" \
    "v_max_u32 v196, v200, v201\n\t" \
    "v_max3_u32 v196, v202, v203, v196\n\t" \
    "s_waitcnt vmcnt(6)\n\t" \
    "v_max3_u32 v196, v204, v205, v196\n\t" \
    "v_max3_u32 v196, v206, v207, v196\n\t" \
    "s_waitcnt vmcnt(5)\n\t" \
    "v_max3_u32 v196, v208, v209, v196\n\t" \
    "v_max3_u32 v196, v210, v211, v196\n\t" \
    "s_waitcnt vmcnt(4)\n\t" \
    "v_max3_u32 v196, v212, v213, v196\n\t" \
    "v_max3_u32 v196, v214, v215, v196\n\t" \
    "s_waitcnt vmcnt(3)\n\t" \
    "v_max3_u32 v196, v216, v217, v196\n\t" \
    "v_max3_u32 v196, v218, v219, v196\n\t" \
    "s_waitcnt vmcnt(2)\n\t" \
    "v_max3_u32 v196, v220, v221, v196\n\t" \
    "v_max3_u32 v196, v222, v223, v196\n\t" \
    "s_waitcnt vmcnt(1)\n\t" \
    "v_max3_u32 v196, v224, v225, v196\n\t" \
    "v_max3_u32 v196, v226, v227, v196\n\t" \
    "s_waitcnt vmcnt(0)\n\t" \
    "v_max3_u32 v196, v228, v229, v196\n\t" \
    "v_max3_u32 v196, v230, v231, v196\n\t" \
    "v_cmp_eq_u32 vcc, -1, v196\n\t" \
    "s_cbranch_vccz .Lxd%=\n\t" \
    ".Lxl%=:\n\t" \
    "s_mov_b32 %[a], 0\n\t" \
    "v_max_u32 v196, v200, v201\n\t" \
    "v_max3_u32 v196, v202, v203, v196\n\t" \
    "v_cmp_eq_u32 vcc, -1, v196\n\t" \
    "s_cbranch_vccz .Lxs0_%=\n\t" \
    "buffer_load_dwordx4 v[200:203], %[l16], %[rs], %[s0] offen offset:0 sc1\n\t" \
    "s_mov_b32 %[a], 1\n\t" \
    ".Lxs0_%=:\n\t" \
    "v_max_u32 v196, v204, v205\n\t" \
    "v_max3_u32 v196, v206, v207, v196\n\t" \
    "v_cmp_eq_u32 vcc, -1, v196\n\t" \
    "s_cbranch_vccz .Lxs1_%=\n\t" \
    "buffer_load_dwordx4 v[204:207], %[l16], %[rs], %[s0] offen offset:1024 sc1\n\t" \
    "s_mov_b32 %[a], 1\n\t" \
    ".Lxs1_%=:\n\t" \
    "v_max_u32 v196, v208, v209\n\t" \
    "v_max3_u32 v196, v210, v211, v196\n\t" \
    "v_cmp_eq_u32 vcc, -1, v196\n\t" \
    "s_cbranch_vccz .Lxs2_%=\n\t" \
    "buffer_load_dwordx4 v[208:211], %[l16], %[rs], %[s0] offen offset:2048 sc1\n\t" \
    "s_mov_b32 %[a], 1\n\t" \
    ".Lxs2_%=:\n\t" \
    "v_max_u32 v196, v212, v213\n\t" \
    "v_max3_u32 v196, v214, v215, v196\n\t" \
    "v_cmp_eq_u32 vcc, -1, v196\n\t" \
    "s_cbranch_vccz .Lxs3_%=\n\t" \
    "buffer_load_dwordx4 v[212:215], %[l16], %[rs], %[s0] offen offset:3072 sc1\n\t" \
    "s_mov_b32 %[a], 1\n\t" \
    ".Lxs3_%=:\n\t" \
    "v_max_u32 v196, v216, v217\n\t" \
    "v_max3_u32 v196, v218, v219, v196\n\t" \
    "v_cmp_eq_u32 vcc, -1, v196\n\t" \
    "s_cbranch_vccz .Lxs4_%=\n\t" \
    "buffer_load_dwordx4 v[216:219], %[l16], %[rs], %[s1] offen offset:0 sc1\n\t" \
    "s_mov_b32 %[a], 1\n\t" \
    ".Lxs4_%=:\n\t" \
    "v_max_u32 v196, v220, v221\n\t" \
    "v_max3_u32 v196, v222, v223, v196\n\t" \
    "v_cmp_eq_u32 vcc, -1, v196\n\t" \
    "s_cbranch_vccz .Lxs5_%=\n\t" \
    "buffer_load_dwordx4 v[220:223], %[l16], %[rs], %[s1] offen offset:1024 sc1\n\t" \
    "s_mov_b32 %[a], 1\n\t" \
    ".Lxs5_%=:\n\t" \
    "v_max_u32 v196, v224, v225\n\t" \
    "v_max3_u32 v196, v226, v227, v196\n\t" \
    "v_cmp_eq_u32 vcc, -1, v196\n\t" \
    "s_cbranch_vccz .Lxs6_%=\n\t" \
    "buffer_load_dwordx4 v[224:227], %[l16], %[rs], %[s1] offen offset:2048 sc1\n\t" \
    "s_mov_b32 %[a], 1\n\t" \
    ".Lxs6_%=:\n\t" \
    "v_max_u32 v196, v228, v229\n\t" \
    "v_max3_u32 v196, v230, v231, v196\n\t" \
    "v_cmp_eq_u32 vcc, -1, v196\n\t" \
    "s_cbranch_vccz .Lxs7_%=\n\t" \
    "buffer_load_dwordx4 v[228:231], %[l16], %[rs], %[s1] offen offset:3072 sc1\n\t" \
    "s_mov_b32 %[a], 1\n\t" \
    ".Lxs7_%=:\n\t" \
    "s_cmp_eq_u32 %[a], 0\n\t" \
    "s_cbranch_scc1 .Lxd%=\n\t" \
    "s_waitcnt vmcnt(0)\n\t" \
    "s_sub_u32 %[n], %[n], 1\n\t" \
    "s_cmp_lg_u32 %[n], 0\n\t" \
    "s_cbranch_scc1 .Lxl%=\n\t" \
    "s_branch .Lxe%=\n\t" \
    ".Lxd%=:\n\t" \
    ASM_ST2 \
    "v_mfma_f32_16x16x32_bf16 v[120:123], v[128:131], v[200:203], 0\n\t" \
    "v_mfma_f32_16x16x32_bf16 v[124:127], v[144:147], v[200:203], 0\n\t" \
    "v_mfma_f32_16x16x32_bf16 v[120:123], v[132:135], v[204:207], v[120:123]\n\t" \
    "v_mfma_f32_16x16x32_bf16 v[124:127], v[148:151], v[204:207], v[124:127]\n\t" \
    "v_mfma_f32_16x16x32_bf16 v[120:123], v[136:139], v[208:211], v[120:123]\n\t" \
    "v_mfma_f32_16x16x32_bf16 v[124:127], v[152:155], v[208:211], v[124:127]\n\t" \
    "v_mfma_f32_16x16x32_bf16 v[120:123], v[140:143], v[212:215], v[120:123]\n\t" \
    "v_mfma_f32_16x16x32_bf16 v[124:127], v[156:159], v[212:215], v[124:127]\n\t" \
    "v_mfma_f32_16x16x32_bf16 v[112:115], v[96:99], v[220:223], 0\n\t" \
    "v_mfma_f32_16x16x32_bf16 v[116:119], v[104:107], v[220:223], 0\n\t" \
    "v_mfma_f32_16x16x32_bf16 v[120:123], v[176:179], v[216:219], v[120:123]\n\t" \
    "v_mfma_f32_16x16x32_bf16 v[124:127], v[180:183], v[216:219], v[124:127]\n\t" \
    "v_mfma_f32_16x16x32_bf16 v[112:115], v[100:103], v[224:227], v[112:115]\n\t" \
    "v_mfma_f32_16x16x32_bf16 v[116:119], v[108:111], v[224:227], v[116:119]\n\t" \
    "v_mfma_f32_16x16x32_bf16 v[112:115], v[176:179], v[228:231], v[112:115]\n\t" \
    "v_mfma_f32_16x16x32_bf16 v[116:119], v[180:183], v[228:231], v[116:119]\n\t" \
    "s_nop 1\n\t" \
    "ds_write_b128 %[pw], v[120:123] offset:16384\n\t" \
    "ds_write_b128 %[pw], v[124:127] offset:24576\n\t" \
    "s_nop 3\n\t" \
    "ds_write_b128 %[pw], v[112:115] offset:0\n\t" \
    "ds_write_b128 %[pw], v[116:119] offset:8192\n\t" \
    "v_cmp_ge_u32 vcc, %[tn], v248\n\t" \
    "s_mov_b32 %[a], 0\n\t" \
    "s_cbranch_vccnz .Lxv%=\n\t" \
    "buffer_load_dwordx4 v[128:131], %[l16], %[rp], %[sv0] offen offset:0 sc1\n\t" \
    "buffer_load_dwordx4 v[132:135], %[l16], %[rp], %[sv0] offen offset:1024 sc1\n\t" \
    "buffer_load_dwordx4 v[136:139], %[l16], %[rp], %[sv0] offen offset:2048 sc1\n\t" \
    "buffer_load_dwordx4 v[140:143], %[l16], %[rp], %[sv0] offen offset:3072 sc1\n\t" \
    "buffer_load_dwordx4 v[144:147], %[l16], %[rp], %[sv1] offen offset:0 sc1\n\t" \
    "buffer_load_dwordx4 v[148:151], %[l16], %[rp], %[sv1] offen offset:1024 sc1\n\t" \
    "buffer_load_dwordx4 v[152:155], %[l16], %[rp], %[sv1] offen offset:2048 sc1\n\t" \
    "buffer_load_dwordx4 v[156:159], %[l16], %[rp], %[sv1] offen offset:3072 sc1\n\t" \
    "s_mov_b32 %[a], 1\n\t" \
    ".Lxv%=:\n\t" \
    "s_cmp_eq_u32 %[fin], 0\n\t" \
    "s_waitcnt lgkmcnt(0)\n\t" \
    "s_barrier\n\t" ASM_ST \
    "s_cbranch_scc1 .Lxe%=\n\t" \
    "ds_read_b128 v[200:203], %[pr] offset:0\n\t" \
    "ds_read_b128 v[204:207], %[pr] offset:1024\n\t" \
    "ds_read_b128 v[208:211], %[pr] offset:2048\n\t" \
    "ds_read_b128 v[212:215], %[pr] offset:3072\n\t" \
    "ds_read_b128 v[216:219], %[pr] offset:4096\n\t" \
    "ds_read_b128 v[220:223], %[pr] offset:5120\n\t" \
    "ds_read_b128 v[224:227], %[pr] offset:6144\n\t" \
    "ds_read_b128 v[228:231], %[pr] offset:7168\n\t" \
    "v_lshlrev_b32 v240, 16, v250\n\t" \
    "v_and_b32 v241, 0xffff0000, v250\n\t" \
    "v_lshlrev_b32 v242, 16, v251\n\t" \
    "v_and_b32 v243, 0xffff0000, v251\n\t" \
    "s_waitcnt lgkmcnt(6)\n\t" \
    "v_pk_add_f32 v[200:201], v[200:201], v[204:205]\n\t" \
    "v_pk_add_f32 v[202:203], v[202:203], v[206:207]\n\t" \
    "s_waitcnt lgkmcnt(4)\n\t" \
    "v_pk_add_f32 v[208:209], v[208:209], v[212:213]\n\t" \
    "v_pk_add_f32 v[210:211], v[210:211], v[214:215]\n\t" \
    "v_pk_add_f32 v[200:201], v[200:201], v[208:209]\n\t" \
    "v_pk_add_f32 v[202:203], v[202:203], v[210:211]\n\t" \
    "s_waitcnt lgkmcnt(2)\n\t" \
    "v_pk_add_f32 v[216:217], v[216:217], v[220:221]\n\t" \
    "v_pk_add_f32 v[218:219], v[218:219], v[222:223]\n\t" \
    "s_waitcnt lgkmcnt(0)\n\t" \
    "v_pk_add_f32 v[224:225], v[224:225], v[228:229]\n\t" \
    "v_pk_add_f32 v[226:227], v[226:227], v[230:231]\n\t" \
    "v_pk_add_f32 v[216:217], v[216:217], v[224:225]\n\t" \
    "v_pk_add_f32 v[218:219], v[218:219], v[226:227]\n\t" \
    "v_pk_add_f32 v[200:201], v[200:201], v[216:217]\n\t" \
    "v_pk_add_f32 v[202:203], v[202:203], v[218:219]\n\t" \
    "v_pk_add_f32 v[200:201], v[200:201], v[240:241]\n\t" \
    "v_pk_add_f32 v[202:203], v[202:203], v[242:243]\n\t" \
    "v_cvt_pk_bf16_f32 v200, v200, v201\n\t" \
    "v_cvt_pk_bf16_f32 v201, v202, v203\n\t" \
    "v_min_u32 v200, -2, v200\n\t" \
    "v_min_u32 v201, -2, v201\n\t" \
    "buffer_store_dwordx2 v[200:201], %[xo], %[rd], %[sa] offen sc1\n\t" \
    ".Lxe%=:\n\t"
#define ASM_W4_M \
    ".Lc7_%=:\n\t" \
    "s_waitcnt vmcnt(0)\n\t" \
    "v_max_u32 v212, v188, v189\n\t" \
    "v_max3_u32 v212, v190, v191, v212\n\t" \
    "v_cmp_eq_u32 vcc, -1, v212\n\t" \
    "s_cbranch_vccnz .Ls7_%=\n\t" \
    ".Lk7_%=:\n\t" \
    ASM_ST2 \
    "buffer_load_dwordx4 v[184:187], %[l16], %[rs], %[s1] offen offset:2048 sc1\n\t" \
    "buffer_load_dwordx4 v[180:183], %[l16], %[rs], %[s1] offen offset:1024 sc1\n\t" \
    "buffer_load_dwordx4 v[176:179], %[l16], %[rs], %[s1] offen offset:0 sc1\n\t" \
    "buffer_load_dwordx4 v[172:175], %[l16], %[rs], %[s0] offen offset:3072 sc1\n\t" \
    "buffer_load_dwordx4 v[168:171], %[l16], %[rs], %[s0] offen offset:2048 sc1\n\t" \
    "buffer_load_dwordx4 v[164:167], %[l16], %[rs], %[s0] offen offset:1024 sc1\n\t" \
    "buffer_load_dwordx4 v[160:163], %[l16], %[rs], %[s0] offen offset:0 sc1\n\t" \
    "v_mfma_f32_16x16x32_bf16 v[208:211], v[156:159], v[188:191], 0\n\t" \
    "v_mfma_f32_16x16x32_bf16 v[204:207], v[124:127], v[188:191], 0\n\t" \
    "v_mfma_f32_16x16x32_bf16 v[200:203], v[92:95], v[188:191], 0\n\t" \
    "v_mfma_f32_16x16x32_bf16 v[196:199], v[60:63], v[188:191], 0\n\t" \
    "v_mfma_f32_16x16x32_bf16 v[192:195], v[28:31], v[188:191], 0\n\t" \
    ".Lc6_%=:\n\t" \
    "s_waitcnt vmcnt(6)\n\t" \
    "v_max_u32 v212, v184, v185\n\t" \
    "v_max3_u32 v212, v186, v187, v212\n\t" \
    "v_cmp_eq_u32 vcc, -1, v212\n\t" \
    "s_cbranch_vccnz .Ls6_%=\n\t" \
    ".Lk6_%=:\n\t" \
    ".Lc5_%=:\n\t" \
    "s_waitcnt vmcnt(5)\n\t" \
    "v_max_u32 v212, v180, v181\n\t" \
    "v_max3_u32 v212, v182, v183, v212\n\t" \
    "v_cmp_eq_u32 vcc, -1, v212\n\t" \
    "s_cbranch_vccnz .Ls5_%=\n\t" \
    ".Lk5_%=:\n\t" \
    "v_mfma_f32_16x16x32_bf16 v[192:195], v[24:27], v[184:187], v[192:195]\n\t" \
    "v_mfma_f32_16x16x32_bf16 v[192:195], v[20:23], v[180:183], v[192:195]\n\t" \
    "v_mfma_f32_16x16x32_bf16 v[196:199], v[56:59], v[184:187], v[196:199]\n\t" \
    "v_mfma_f32_16x16x32_bf16 v[196:199], v[52:55], v[180:183], v[196:199]\n\t" \
    "v_mfma_f32_16x16x32_bf16 v[200:203], v[88:91], v[184:187], v[200:203]\n\t" \
    "v_mfma_f32_16x16x32_bf16 v[200:203], v[84:87], v[180:183], v[200:203]\n\t" \
    "v_mfma_f32_16x16x32_bf16 v[204:207], v[120:123], v[184:187], v[204:207]\n\t" \
    "v_mfma_f32_16x16x32_bf16 v[204:207], v[116:119], v[180:183], v[204:207]\n\t" \
    "v_mfma_f32_16x16x32_bf16 v[208:211], v[152:155], v[184:187], v[208:211]\n\t" \
    "v_mfma_f32_16x16x32_bf16 v[208:211], v[148:151], v[180:183], v[208:211]\n\t" \
    ".Lc4_%=:\n\t" \
    "s_waitcnt vmcnt(4)\n\t" \
    "v_max_u32 v212, v176, v177\n\t" \
    "v_max3_u32 v212, v178, v179, v212\n\t" \
    "v_cmp_eq_u32 vcc, -1, v212\n\t" \
    "s_cbranch_vccnz .Ls4_%=\n\t" \
    ".Lk4_%=:\n\t" \
    ".Lc3_%=:\n\t" \
    "s_waitcnt vmcnt(3)\n\t" \
    "v_max_u32 v212, v172, v173\n\t" \
    "v_max3_u32 v212, v174, v175, v212\n\t" \
    "v_cmp_eq_u32 vcc, -1, v212\n\t" \
    "s_cbranch_vccnz .Ls3_%=\n\t" \
    ".Lk3_%=:\n\t" \
    "v_mfma_f32_16x16x32_bf16 v[208:211], v[144:147], v[176:179], v[208:211]\n\t" \
    "v_mfma_f32_16x16x32_bf16 v[208:211], v[140:143], v[172:175], v[208:211]\n\t" \
    "v_mfma_f32_16x16x32_bf16 v[204:207], v[112:115], v[176:179], v[204:207]\n\t" \
    "v_mfma_f32_16x16x32_bf16 v[204:207], v[108:111], v[172:175], v[204:207]\n\t" \
    "v_mfma_f32_16x16x32_bf16 v[200:203], v[80:83], v[176:179], v[200:203]\n\t" \
    "v_mfma_f32_16x16x32_bf16 v[200:203], v[76:79], v[172:175], v[200:203]\n\t" \
    "v_mfma_f32_16x16x32_bf16 v[196:199], v[48:51], v[176:179], v[196:199]\n\t" \
    "v_mfma_f32_16x16x32_bf16 v[196:199], v[44:47], v[172:175], v[196:199]\n\t" \
    "v_mfma_f32_16x16x32_bf16 v[192:195], v[16:19], v[176:179], v[192:195]\n\t" \
    "v_mfma_f32_16x16x32_bf16 v[192:195], v[12:15], v[172:175], v[192:195]\n\t" \
    ".Lc2_%=:\n\t" \
    "s_waitcnt vmcnt(2)\n\t" \
    "v_max_u32 v212, v168, v169\n\t" \
    "v_max3_u32 v212, v170, v171, v212\n\t" \
    "v_cmp_eq_u32 vcc, -1, v212\n\t" \
    "s_cbranch_vccnz .Ls2_%=\n\t" \
    ".Lk2_%=:\n\t" \
    "buffer_load_dwordx4 v[188:191], %[l16], %[rs], %[n1] offen offset:3072 sc1\n\t" \
    ".Lc1_%=:\n\t" \
    "s_waitcnt vmcnt(2)\n\t" \
    "v_max_u32 v212, v164, v165\n\t" \
    "v_max3_u32 v212, v166, v167, v212\n\t" \
    "v_cmp_eq_u32 vcc, -1, v212\n\t" \
    "s_cbranch_vccnz .Ls1_%=\n\t" \
    ".Lk1_%=:\n\t" \
    ".Lc0_%=:\n\t" \
    "s_waitcnt vmcnt(1)\n\t" \
    "v_max_u32 v212, v160, v161\n\t" \
    "v_max3_u32 v212, v162, v163, v212\n\t" \
    "v_cmp_eq_u32 vcc, -1, v212\n\t" \
    "s_cbranch_vccnz .Ls0_%=\n\t" \
    ".Lk0_%=:\n\t" \
    "v_mfma_f32_16x16x32_bf16 v[192:195], v[8:11], v[168:171], v[192:195]\n\t" \
    "v_mfma_f32_16x16x32_bf16 v[192:195], v[4:7], v[164:167], v[192:195]\n\t" \
    "v_mfma_f32_16x16x32_bf16 v[192:195], v[0:3], v[160:163], v[192:195]\n\t" \
    "v_mfma_f32_16x16x32_bf16 v[196:199], v[40:43], v[168:171], v[196:199]\n\t" \
    "v_mfma_f32_16x16x32_bf16 v[196:199], v[36:39], v[164:167], v[196:199]\n\t" \
    "v_mfma_f32_16x16x32_bf16 v[196:199], v[32:35], v[160:163], v[196:199]\n\t" \
    "v_mfma_f32_16x16x32_bf16 v[200:203], v[72:75], v[168:171], v[200:203]\n\t" \
    "v_mfma_f32_16x16x32_bf16 v[200:203], v[68:71], v[164:167], v[200:203]\n\t" \
    "v_mfma_f32_16x16x32_bf16 v[200:203], v[64:67], v[160:163], v[200:203]\n\t" \
    "v_mfma_f32_16x16x32_bf16 v[204:207], v[104:107], v[168:171], v[204:207]\n\t" \
    "v_mfma_f32_16x16x32_bf16 v[204:207], v[100:103], v[164:167], v[204:207]\n\t" \
    "v_mfma_f32_16x16x32_bf16 v[204:207], v[96:99], v[160:163], v[204:207]\n\t" \
    "v_mfma_f32_16x16x32_bf16 v[208:211], v[136:139], v[168:171], v[208:211]\n\t" \
    "v_mfma_f32_16x16x32_bf16 v[208:211], v[132:135], v[164:167], v[208:211]\n\t" \
    "v_mfma_f32_16x16x32_bf16 v[208:211], v[128:131], v[160:163], v[208:211]\n\t" \
    "v_mov_b32 v212, %[un]\n\t" \
    "ds_write_b128 %[ld], v[192:195] offset:0\n\t" \
    "ds_write_b128 %[ld], v[196:199] offset:6144\n\t" \
    "ds_write_b128 %[ld], v[200:203] offset:12288\n\t" \
    "ds_write_b128 %[ld], v[204:207] offset:18432\n\t" \
    "s_nop 2\n\t" \
    "ds_write_b128 %[ld], v[208:211] offset:24576\n\t" \
    "ds_write_b32 %[fl], v212\n\t" \
    "s_branch .Lme%=\n\t" \
    ".Ls7_%=:\n\t" \
    "s_cmp_eq_u32 %[n], 0\n\t" \
    "s_cbranch_scc1 .Lk7_%=\n\t" \
    "s_sub_u32 %[n], %[n], 1\n\t" \
    "s_sleep 1\n\t" \
    "buffer_load_dwordx4 v[188:191], %[l16], %[rs], %[s1] offen offset:3072 sc1\n\t" \
    "s_branch .Lc7_%=\n\t" \
    ".Ls6_%=:\n\t" \
    "s_cmp_eq_u32 %[n], 0\n\t" \
    "s_cbranch_scc1 .Lk6_%=\n\t" \
    "s_sub_u32 %[n], %[n], 1\n\t" \
    "buffer_load_dwordx4 v[184:187], %[l16], %[rs], %[s1] offen offset:2048 sc1\n\t" \
    "s_waitcnt vmcnt(0)\n\t" \
    "s_branch .Lc6_%=\n\t" \
    ".Ls5_%=:\n\t" \
    "s_cmp_eq_u32 %[n], 0\n\t" \
    "s_cbranch_scc1 .Lk5_%=\n\t" \
    "s_sub_u32 %[n], %[n], 1\n\t" \
    "buffer_load_dwordx4 v[180:183], %[l16], %[rs], %[s1] offen offset:1024 sc1\n\t" \
    "s_waitcnt vmcnt(0)\n\t" \
    "s_branch .Lc5_%=\n\t" \
    ".Ls4_%=:\n\t" \
    "s_cmp_eq_u32 %[n], 0\n\t" \
    "s_cbranch_scc1 .Lk4_%=\n\t" \
    "s_sub_u32 %[n], %[n], 1\n\t" \
    "buffer_load_dwordx4 v[176:179], %[l16], %[rs], %[s1] offen offset:0 sc1\n\t" \
    "s_waitcnt vmcnt(0)\n\t" \
    "s_branch .Lc4_%=\n\t" \
    ".Ls3_%=:\n\t" \
    "s_cmp_eq_u32 %[n], 0\n\t" \
    "s_cbranch_scc1 .Lk3_%=\n\t" \
    "s_sub_u32 %[n], %[n], 1\n\t" \
    "buffer_load_dwordx4 v[172:175], %[l16], %[rs], %[s0] offen offset:3072 sc1\n\t" \
    "s_waitcnt vmcnt(0)\n\t" \
    "s_branch .Lc3_%=\n\t" \
    ".Ls2_%=:\n\t" \
    "s_cmp_eq_u32 %[n], 0\n\t" \
    "s_cbranch_scc1 .Lk2_%=\n\t" \
    "s_sub_u32 %[n], %[n], 1\n\t" \
    "buffer_load_dwordx4 v[168:171], %[l16], %[rs], %[s0] offen offset:2048 sc1\n\t" \
    "s_waitcnt vmcnt(0)\n\t" \
    "s_branch .Lc2_%=\n\t" \
    ".Ls1_%=:\n\t" \
    "s_cmp_eq_u32 %[n], 0\n\t" \
    "s_cbranch_scc1 .Lk1_%=\n\t" \
    "s_sub_u32 %[n], %[n], 1\n\t" \
    "buffer_load_dwordx4 v[164:167], %[l16], %[rs], %[s0] offen offset:1024 sc1\n\t" \
    "s_waitcnt vmcnt(0)\n\t" \
    "s_branch .Lc1_%=\n\t" \
    ".Ls0_%=:\n\t" \
    "s_cmp_eq_u32 %[n], 0\n\t" \
    "s_cbranch_scc1 .Lk0_%=\n\t" \
    "s_sub_u32 %[n], %[n], 1\n\t" \
    "buffer_load_dwordx4 v[160:163], %[l16], %[rs], %[s0] offen offset:0 sc1\n\t" \
    "s_waitcnt vmcnt(0)\n\t" \
    "s_branch .Lc0_%=\n\t" \
    ".Lme%=:\n\t"
#define ASM_W4F_0 \
    "s_branch .Lf0%=\n\t" \
    ".Lfr%=:\n\t" \
    "s_cmp_eq_u32 %[n], 0\n\t" \
    "s_cbranch_scc1 .Lfb%=\n\t" \
    "s_sub_u32 %[n], %[n], 1\n\t" \
    "s_sleep 6\n\t" \
    "buffer_load_dwordx2 v[164:165], %[o0], %[rd], %[c1] offen sc1\n\t" \
    "buffer_load_dwordx2 v[166:167], %[o1], %[rd], %[c1] offen sc1\n\t" \
    "buffer_load_dwordx2 v[168:169], %[o2], %[rd], %[c1] offen sc1\n\t" \
    "buffer_load_dwordx2 v[170:171], %[o3], %[rd], %[c1] offen sc1\n\t" \
    "buffer_load_dwordx2 v[172:173], %[o4], %[rd], %[c1] offen sc1\n\t" \
    "buffer_load_dwordx2 v[174:175], %[o0], %[rd], %[ca] offen sc1\n\t" \
    "buffer_load_dwordx2 v[176:177], %[o1], %[rd], %[ca] offen sc1\n\t" \
    "buffer_load_dwordx2 v[178:179], %[o2], %[rd], %[ca] offen sc1\n\t" \
    "buffer_load_dwordx2 v[180:181], %[o3], %[rd], %[ca] offen sc1\n\t" \
    "buffer_load_dwordx2 v[182:183], %[o4], %[rd], %[ca] offen sc1\n\t" \
    "s_waitcnt vmcnt(0)\n\t" \
    "s_branch .Lfa%=\n\t" \
    ".Lfs0_%=:\n\t" \
    "s_cmp_eq_u32 %[n], 0\n\t" \
    "s_cbranch_scc1 .Lfu0_%=\n\t" \
    "s_sub_u32 %[n], %[n], 1\n\t" \
    "s_sleep 1\n\t" \
    "s_branch .Lft0_%=\n\t" \
    ".Lfs1_%=:\n\t" \
    "s_cmp_eq_u32 %[n], 0\n\t" \
    "s_cbranch_scc1 .Lfu1_%=\n\t" \
    "s_sub_u32 %[n], %[n], 1\n\t" \
    "s_sleep 1\n\t" \
    "s_branch .Lft1_%=\n\t" \
    ".Lfx%=:\n\t" \
    "s_cmp_eq_u32 %[n], 0\n\t" \
    "s_cbranch_scc1 .Lfz%=\n\t" \
    "s_sub_u32 %[n], %[n], 1\n\t" \
    "s_branch .Lfy%=\n\t" \
    ".Lf0%=:\n\t" \
    "s_waitcnt vmcnt(1)\n\t" \
    ".Lfa%=:\n\t" \
    "v_max_u32 v194, v164, v165\n\t" \
    "v_max3_u32 v194, v166, v167, v194\n\t" \
    "v_max3_u32 v194, v168, v169, v194\n\t" \
    "v_max3_u32 v194, v170, v171, v194\n\t" \
    "v_max3_u32 v194, v172, v173, v194\n\t" \
    "v_max3_u32 v194, v174, v175, v194\n\t" \
    "v_max3_u32 v194, v176, v177, v194\n\t" \
    "v_max3_u32 v194, v178, v179, v194\n\t" \
    "v_max3_u32 v194, v180, v181, v194\n\t" \
    "v_max3_u32 v194, v182, v183, v194\n\t" \
    "v_cmp_eq_u32 vcc, -1, v194\n\t" \
    "s_cbranch_vccnz .Lfr%=\n\t" \
    ".Lfb%=:\n\t" \
    "v_lshlrev_b32 v64, 16, v164\n\t" \
    "v_and_b32 v65, 0xffff0000, v164\n\t" \
    "v_lshlrev_b32 v84, 16, v174\n\t" \
    "v_and_b32 v85, 0xffff0000, v174\n\t" \
    "v_lshlrev_b32 v66, 16, v165\n\t" \
    "v_and_b32 v67, 0xffff0000, v165\n\t" \
    "v_lshlrev_b32 v86, 16, v175\n\t" \
    "v_and_b32 v87, 0xffff0000, v175\n\t" \
    "v_lshlrev_b32 v68, 16, v166\n\t" \
    "v_and_b32 v69, 0xffff0000, v166\n\t" \
    "v_lshlrev_b32 v88, 16, v176\n\t" \
    "v_and_b32 v89, 0xffff0000, v176\n\t" \
    "v_lshlrev_b32 v70, 16, v167\n\t" \
    "v_and_b32 v71, 0xffff0000, v167\n\t" \
    "v_lshlrev_b32 v90, 16, v177\n\t" \
    "v_and_b32 v91, 0xffff0000, v177\n\t" \
    "v_lshlrev_b32 v72, 16, v168\n\t" \
    "v_and_b32 v73, 0xffff0000, v168\n\t" \
    "v_lshlrev_b32 v92, 16, v178\n\t" \
    "v_and_b32 v93, 0xffff0000, v178\n\t" \
    "v_lshlrev_b32 v74, 16, v169\n\t" \
    "v_and_b32 v75, 0xffff0000, v169\n\t" \
    "v_lshlrev_b32 v94, 16, v179\n\t" \
    "v_and_b32 v95, 0xffff0000, v179\n\t" \
    "v_lshlrev_b32 v76, 16, v170\n\t" \
    "v_and_b32 v77, 0xffff0000, v170\n\t" \
    "v_lshlrev_b32 v96, 16, v180\n\t" \
    "v_and_b32 v97, 0xffff0000, v180\n\t" \
    "v_lshlrev_b32 v78, 16, v171\n\t" \
    "v_and_b32 v79, 0xffff0000, v171\n\t" \
    "v_lshlrev_b32 v98, 16, v181\n\t" \
    "v_and_b32 v99, 0xffff0000, v181\n\t" \
    "v_lshlrev_b32 v80, 16, v172\n\t" \
    "v_and_b32 v81, 0xffff0000, v172\n\t" \
    "v_lshlrev_b32 v100, 16, v182\n\t" \
    "v_and_b32 v101, 0xffff0000, v182\n\t" \
    "v_lshlrev_b32 v82, 16, v173\n\t" \
    "v_and_b32 v83, 0xffff0000, v173\n\t" \
    "v_lshlrev_b32 v102, 16, v183\n\t" \
    "v_and_b32 v103, 0xffff0000, v183\n\t" \
    "v_pk_add_f32 v[64:65], v[64:65], v[84:85]\n\t" \
    "v_pk_add_f32 v[66:67], v[66:67], v[86:87]\n\t" \
    "v_pk_add_f32 v[68:69], v[68:69], v[88:89]\n\t" \
    "v_pk_add_f32 v[70:71], v[70:71], v[90:91]\n\t" \
    "v_pk_add_f32 v[72:73], v[72:73], v[92:93]\n\t" \
    "v_pk_add_f32 v[74:75], v[74:75], v[94:95]\n\t" \
    "v_pk_add_f32 v[76:77], v[76:77], v[96:97]\n\t" \
    "v_pk_add_f32 v[78:79], v[78:79], v[98:99]\n\t" \
    "v_pk_add_f32 v[80:81], v[80:81], v[100:101]\n\t" \
    "v_pk_add_f32 v[82:83], v[82:83], v[102:103]\n\t" \
    ".Lft0_%=:\n\t" \
    "ds_read_b32 v194, %[f0]\n\t" \
    "s_waitcnt lgkmcnt(0)\n\t" \
    "v_cmp_ge_u32 vcc, %[u], v194\n\t" \
    "s_cbranch_vccnz .Lfs0_%=\n\t" \
    ".Lfu0_%=:\n\t" \
    "ds_read_b128 v[84:87], %[la] offset:0\n\t" \
    "ds_read_b128 v[88:91], %[la] offset:6144\n\t" \
    "ds_read_b128 v[92:95], %[la] offset:12288\n\t" \
    "ds_read_b128 v[96:99], %[la] offset:18432\n\t" \
    "ds_read_b128 v[100:103], %[la] offset:24576\n\t" \
    "ds_read_b128 v[104:107], %[la] offset:1024\n\t" \
    "ds_read_b128 v[108:111], %[la] offset:7168\n\t" \
    "ds_read_b128 v[112:115], %[la] offset:13312\n\t" \
    "ds_read_b128 v[116:119], %[la] offset:19456\n\t" \
    "ds_read_b128 v[120:123], %[la] offset:25600\n\t" \
    "ds_read_b128 v[124:127], %[la] offset:2048\n\t" \
    "s_waitcnt lgkmcnt(10)\n\t" \
    "v_pk_add_f32 v[64:65], v[64:65], v[84:85]\n\t" \
    "v_pk_add_f32 v[66:67], v[66:67], v[86:87]\n\t" \
    "ds_read_b128 v[128:131], %[la] offset:8192\n\t" \
    "s_waitcnt lgkmcnt(10)\n\t" \
    "v_pk_add_f32 v[68:69], v[68:69], v[88:89]\n\t" \
    "v_pk_add_f32 v[70:71], v[70:71], v[90:91]\n\t" \
    "ds_read_b128 v[132:135], %[la] offset:14336\n\t" \
    "s_waitcnt lgkmcnt(10)\n\t" \
    "v_pk_add_f32 v[72:73], v[72:73], v[92:93]\n\t" \
    "v_pk_add_f32 v[74:75], v[74:75], v[94:95]\n\t" \
    "ds_read_b128 v[136:139], %[la] offset:20480\n\t" \
    "s_waitcnt lgkmcnt(10)\n\t" \
    "v_pk_add_f32 v[76:77], v[76:77], v[96:97]\n\t" \
    "v_pk_add_f32 v[78:79], v[78:79], v[98:99]\n\t" \
    "ds_read_b128 v[140:143], %[la] offset:26624\n\t" \
    "s_waitcnt lgkmcnt(10)\n\t" \
    "v_pk_add_f32 v[80:81], v[80:81], v[100:101]\n\t" \
    "v_pk_add_f32 v[82:83], v[82:83], v[102:103]\n\t" \
    "ds_read_b128 v[144:147], %[la] offset:3072\n\t" \
    "s_waitcnt lgkmcnt(10)\n\t" \
    "v_pk_add_f32 v[64:65], v[64:65], v[104:105]\n\t" \
    "v_pk_add_f32 v[66:67], v[66:67], v[106:107]\n\t" \
    "ds_read_b128 v[148:151], %[la] offset:9216\n\t" \
    "s_waitcnt lgkmcnt(10)\n\t" \
    "v_pk_add_f32 v[68:69], v[68:69], v[108:109]\n\t" \
    "v_pk_add_f32 v[70:71], v[70:71], v[110:111]\n\t" \
    "ds_read_b128 v[152:155], %[la] offset:15360\n\t" \
    "s_waitcnt lgkmcnt(10)\n\t" \
    "v_pk_add_f32 v[72:73], v[72:73], v[112:113]\n\t" \
    "v_pk_add_f32 v[74:75], v[74:75], v[114:115]\n\t" \
    "ds_read_b128 v[156:159], %[la] offset:21504\n\t" \
    "s_waitcnt lgkmcnt(10)\n\t" \
    "v_pk_add_f32 v[76:77], v[76:77], v[116:117]\n\t" \
    "v_pk_add_f32 v[78:79], v[78:79], v[118:119]\n\t" \
    "ds_read_b128 v[160:163], %[la] offset:27648\n\t" \
    "s_waitcnt lgkmcnt(10)\n\t" \
    "v_pk_add_f32 v[80:81], v[80:81], v[120:121]\n\t" \
    "v_pk_add_f32 v[82:83], v[82:83], v[122:123]\n\t" \
    "s_waitcnt lgkmcnt(9)\n\t" \
    "v_pk_add_f32 v[64:65], v[64:65], v[124:125]\n\t" \
    "v_pk_add_f32 v[66:67], v[66:67], v[126:127]\n\t" \
    "s_waitcnt lgkmcnt(8)\n\t" \
    "v_pk_add_f32 v[68:69], v[68:69], v[128:129]\n\t" \
    "v_pk_add_f32 v[70:71], v[70:71], v[130:131]\n\t" \
    "s_waitcnt lgkmcnt(7)\n\t" \
    "v_pk_add_f32 v[72:73], v[72:73], v[132:133]\n\t" \
    "v_pk_add_f32 v[74:75], v[74:75], v[134:135]\n\t" \
    "s_waitcnt lgkmcnt(6)\n\t" \
    "v_pk_add_f32 v[76:77], v[76:77], v[136:137]\n\t" \
    "v_pk_add_f32 v[78:79], v[78:79], v[138:139]\n\t" \
    "s_waitcnt lgkmcnt(5)\n\t" \
    "v_pk_add_f32 v[80:81], v[80:81], v[140:141]\n\t" \
    "v_pk_add_f32 v[82:83], v[82:83], v[142:143]\n\t" \
    "s_waitcnt lgkmcnt(4)\n\t" \
    "v_pk_add_f32 v[64:65], v[64:65], v[144:145]\n\t" \
    "v_pk_add_f32 v[66:67], v[66:67], v[146:147]\n\t" \
    "s_waitcnt lgkmcnt(3)\n\t" \
    "v_pk_add_f32 v[68:69], v[68:69], v[148:149]\n\t" \
    "v_pk_add_f32 v[70:71], v[70:71], v[150:151]\n\t" \
    "s_waitcnt lgkmcnt(2)\n\t" \
    "v_pk_add_f32 v[72:73], v[72:73], v[152:153]\n\t" \
    "v_pk_add_f32 v[74:75], v[74:75], v[154:155]\n\t" \
    "s_waitcnt lgkmcnt(1)\n\t" \
    "v_pk_add_f32 v[76:77], v[76:77], v[156:157]\n\t" \
    "v_pk_add_f32 v[78:79], v[78:79], v[158:159]\n\t" \
    "s_waitcnt lgkmcnt(0)\n\t" \
    "v_pk_add_f32 v[80:81], v[80:81], v[160:161]\n\t" \
    "v_pk_add_f32 v[82:83], v[82:83], v[162:163]\n\t" \
    ".Lft1_%=:\n\t" \
    "ds_read_b32 v194, %[f1]\n\t" \
    "s_waitcnt lgkmcnt(0)\n\t" \
    "v_cmp_ge_u32 vcc, %[u], v194\n\t" \
    "s_cbranch_vccnz .Lfs1_%=\n\t" \
    ".Lfu1_%=:\n\t" \
    ASM_ST2 \
    "ds_read_b128 v[84:87], %[la] offset:4096\n\t" \
    "ds_read_b128 v[88:91], %[la] offset:10240\n\t" \
    "ds_read_b128 v[92:95], %[la] offset:16384\n\t" \
    "ds_read_b128 v[96:99], %[la] offset:22528\n\t" \
    "ds_read_b128 v[100:103], %[la] offset:28672\n\t" \
    "ds_read_b128 v[104:107], %[la] offset:5120\n\t" \
    "ds_read_b128 v[108:111], %[la] offset:11264\n\t" \
    "ds_read_b128 v[112:115], %[la] offset:17408\n\t" \
    "ds_read_b128 v[116:119], %[la] offset:23552\n\t" \
    "ds_read_b128 v[120:123], %[la] offset:29696\n\t" \
    "v_mov_b32 v194, %[un]\n\t" \
    "ds_write_b32 %[fd], v194\n\t" \
    "s_waitcnt lgkmcnt(10)\n\t" \
    "v_pk_add_f32 v[64:65], v[64:65], v[84:85]\n\t" \
    "v_pk_add_f32 v[66:67], v[66:67], v[86:87]\n\t" \
    "s_waitcnt lgkmcnt(9)\n\t" \
    "v_pk_add_f32 v[68:69], v[68:69], v[88:89]\n\t" \
    "v_pk_add_f32 v[70:71], v[70:71], v[90:91]\n\t" \
    "s_waitcnt lgkmcnt(8)\n\t" \
    "v_pk_add_f32 v[72:73], v[72:73], v[92:93]\n\t" \
    "v_pk_add_f32 v[74:75], v[74:75], v[94:95]\n\t" \
    "s_waitcnt lgkmcnt(7)\n\t" \
    "v_pk_add_f32 v[76:77], v[76:77], v[96:97]\n\t" \
    "v_pk_add_f32 v[78:79], v[78:79], v[98:99]\n\t" \
    "s_waitcnt lgkmcnt(6)\n\t" \
    "v_pk_add_f32 v[80:81], v[80:81], v[100:101]\n\t" \
    "v_pk_add_f32 v[82:83], v[82:83], v[102:103]\n\t" \
    "s_waitcnt lgkmcnt(5)\n\t" \
    "v_pk_add_f32 v[64:65], v[64:65], v[104:105]\n\t" \
    "v_pk_add_f32 v[66:67], v[66:67], v[106:107]\n\t" \
    "s_waitcnt lgkmcnt(4)\n\t" \
    "v_pk_add_f32 v[68:69], v[68:69], v[108:109]\n\t" \
    "v_pk_add_f32 v[70:71], v[70:71], v[110:111]\n\t" \
    "s_waitcnt lgkmcnt(3)\n\t" \
    "v_pk_add_f32 v[72:73], v[72:73], v[112:113]\n\t" \
    "v_pk_add_f32 v[74:75], v[74:75], v[114:115]\n\t" \
    "s_waitcnt lgkmcnt(2)\n\t" \
    "v_pk_add_f32 v[76:77], v[76:77], v[116:117]\n\t" \
    "v_pk_add_f32 v[78:79], v[78:79], v[118:119]\n\t" \
    "s_waitcnt lgkmcnt(1)\n\t" \
    "v_pk_add_f32 v[80:81], v[80:81], v[120:121]\n\t" \
    "v_pk_add_f32 v[82:83], v[82:83], v[122:123]\n\t" \
    "v_pk_add_f32 v[84:85], v[64:65], v[66:67]\n\t" \
    "v_pk_mul_f32 v[88:89], v[64:65], v[64:65]\n\t" \
    "v_pk_add_f32 v[86:87], v[68:69], v[70:71]\n\t" \
    "v_pk_mul_f32 v[90:91], v[66:67], v[66:67]\n\t" \
    "v_pk_fma_f32 v[88:89], v[68:69], v[68:69], v[88:89]\n\t" \
    "v_pk_fma_f32 v[90:91], v[70:71], v[70:71], v[90:91]\n\t" \
    "v_pk_add_f32 v[84:85], v[84:85], v[72:73]\n\t" \
    "v_pk_fma_f32 v[88:89], v[72:73], v[72:73], v[88:89]\n\t" \
    "v_pk_add_f32 v[86:87], v[86:87], v[74:75]\n\t" \
    "v_pk_fma_f32 v[90:91], v[74:75], v[74:75], v[90:91]\n\t" \
    "v_pk_add_f32 v[84:85], v[84:85], v[76:77]\n\t" \
    "v_pk_fma_f32 v[88:89], v[76:77], v[76:77], v[88:89]\n\t" \
    "v_pk_add_f32 v[86:87], v[86:87], v[78:79]\n\t" \
    "v_pk_fma_f32 v[90:91], v[78:79], v[78:79], v[90:91]\n\t" \
    "v_pk_add_f32 v[84:85], v[84:85], v[80:81]\n\t" \
    "v_pk_fma_f32 v[88:89], v[80:81], v[80:81], v[88:89]\n\t" \
    "v_pk_add_f32 v[86:87], v[86:87], v[82:83]\n\t" \
    "v_pk_fma_f32 v[90:91], v[82:83], v[82:83], v[90:91]\n\t" \
    "v_pk_add_f32 v[84:85], v[84:85], v[86:87]\n\t" \
    "v_pk_add_f32 v[88:89], v[88:89], v[90:91]\n\t" \
    "v_add_f32 v194, v84, v85\n\t" \
    "v_add_f32 v195, v88, v89\n\t" \
    "s_nop 1\n\t" \
    "v_permlane16_swap_b32 v194, v195\n\t" \
    "v_add_f32 v196, v194, v195\n\t" \
    "v_add_f32 v197, v194, v195\n\t" \
    "s_nop 1\n\t" \
    "v_permlane32_swap_b32 v196, v197\n\t" \
    "v_add_f32 v194, v196, v197\n\t" \
    "v_add_f32 v195, v196, v197\n\t" \
    "s_nop 1\n\t" \
    "v_permlane16_swap_b32 v194, v195\n\t" \
    "v_min_u32 v194, -2, v194\n\t" \
    "v_min_u32 v195, -2, v195\n\t" \
    "buffer_store_dwordx2 v[194:195], %[so], %[rr], %[sp] offen sc1\n\t" \
    ASM_ST3 \
    "v_cvt_pk_bf16_f32 v184, v64, v65\n\t" \
    "v_cvt_pk_bf16_f32 v185, v66, v67\n\t" \
    "v_cvt_pk_bf16_f32 v186, v68, v69\n\t" \
    "v_cvt_pk_bf16_f32 v187, v70, v71\n\t" \
    "v_cvt_pk_bf16_f32 v188, v72, v73\n\t" \
    "v_cvt_pk_bf16_f32 v189, v74, v75\n\t" \
    "v_cvt_pk_bf16_f32 v190, v76, v77\n\t" \
    "v_cvt_pk_bf16_f32 v191, v78, v79\n\t" \
    "v_cvt_pk_bf16_f32 v192, v80, v81\n\t" \
    "v_cvt_pk_bf16_f32 v193, v82, v83\n\t" \
    "v_permlane16_swap_b32 v184, v186\n\t" \
    "v_permlane16_swap_b32 v185, v187\n\t" \
    "v_permlane16_swap_b32 v188, v190\n\t" \
    "v_permlane16_swap_b32 v189, v191\n\t" \
    W4SLEEP \
    ".Lfy%=:\n\t" \
    "buffer_load_dwordx2 v[196:197], %[si], %[rr], %[sq] offen sc1\n\t" \
    "buffer_load_dwordx2 v[198:199], %[si], %[rr], %[sq] offen offset:512 sc1\n\t" \
    "s_waitcnt vmcnt(0)\n\t" \
    "v_max_u32 v200, v196, v197\n\t" \
    "v_max3_u32 v200, v198, v199, v200\n\t" \
    "v_cmp_eq_u32 vcc, -1, v200\n\t" \
    "s_cbranch_vccnz .Lfx%=\n\t" \
    ".Lfz%=:\n\t" \
    ASM_ST \
    "v_add_f32 v194, v196, v198\n\t" \
    "v_add_f32 v195, v197, v199\n\t" \
    "buffer_store_dwordx4 v[184:187], %[pa], %[rd], %[xr] offen sc1\n\t" \
    "buffer_store_dwordx4 v[188:191], %[pb], %[rd], %[xr] offen sc1\n\t" \
    "buffer_store_dwordx2 v[192:193], %[o4], %[rd], %[xr] offen sc1\n\t" \
    "s_nop 1\n\t" \
    "v_permlane16_swap_b32 v194, v195\n\t" \
    "v_add_f32 v196, v194, v195\n\t" \
    "v_add_f32 v197, v194, v195\n\t" \
    "s_nop 1\n\t" \
    "v_permlane32_swap_b32 v196, v197\n\t" \
    "v_add_f32 v194, v196, v197\n\t" \
    "v_add_f32 v195, v196, v197\n\t" \
    "s_nop 1\n\t" \
    "v_permlane16_swap_b32 v194, v195\n\t" \
    "v_mul_f32 v194, %[kd], v194\n\t" \
    "v_mul_f32 v195, %[kd], v195\n\t" \
    "v_fma_f32 v195, -v194, v194, v195\n\t" \
    "v_max_f32 v195, 0, v195\n\t" \
    "v_cvt_pk_bf16_f32 v195, v195, v195\n\t" \
    "v_lshlrev_b32 v195, 16, v195\n\t" \
    "v_add_f32 v195, %[ke], v195\n\t" \
    "v_cvt_pk_bf16_f32 v195, v195, v195\n\t" \
    "v_lshlrev_b32 v195, 16, v195\n\t" \
    "v_sqrt_f32 v195, v195\n\t" \
    "v_cvt_pk_bf16_f32 v194, v194, v194\n\t" \
    "v_lshlrev_b32 v194, 16, v194\n\t" \
    "v_cvt_pk_bf16_f32 v195, v195, v195\n\t" \
    "v_lshlrev_b32 v195, 16, v195\n\t" \
    "v_rcp_f32 v196, v195\n\t" \
    "v_rcp_f32 v197, v195\n\t" \
    "s_nop 0\n\t" \
    "v_mul_f32 v198, -v194, v196\n\t" \
    "v_mul_f32 v199, -v194, v196\n\t" \
    "v_pk_fma_f32 v[64:65], v[64:65], v[196:197], v[198:199]\n\t" \
    "v_pk_fma_f32 v[66:67], v[66:67], v[196:197], v[198:199]\n\t" \
    "v_pk_fma_f32 v[68:69], v[68:69], v[196:197], v[198:199]\n\t" \
    "v_pk_fma_f32 v[70:71], v[70:71], v[196:197], v[198:199]\n\t" \
    "v_pk_fma_f32 v[72:73], v[72:73], v[196:197], v[198:199]\n\t" \
    "v_pk_fma_f32 v[74:75], v[74:75], v[196:197], v[198:199]\n\t" \
    "v_pk_fma_f32 v[76:77], v[76:77], v[196:197], v[198:199]\n\t" \
    "v_pk_fma_f32 v[78:79], v[78:79], v[196:197], v[198:199]\n\t" \
    "v_pk_fma_f32 v[80:81], v[80:81], v[196:197], v[198:199]\n\t" \
    "v_pk_fma_f32 v[82:83], v[82:83], v[196:197], v[198:199]\n\t" \
    "v_cvt_pk_bf16_f32 v184, v64, v65\n\t" \
    "v_cvt_pk_bf16_f32 v185, v66, v67\n\t" \
    "v_cvt_pk_bf16_f32 v186, v68, v69\n\t" \
    "v_cvt_pk_bf16_f32 v187, v70, v71\n\t" \
    "v_cvt_pk_bf16_f32 v188, v72, v73\n\t" \
    "v_cvt_pk_bf16_f32 v189, v74, v75\n\t" \
    "v_cvt_pk_bf16_f32 v190, v76, v77\n\t" \
    "v_cvt_pk_bf16_f32 v191, v78, v79\n\t" \
    "v_cvt_pk_bf16_f32 v192, v80, v81\n\t" \
    "v_cvt_pk_bf16_f32 v193, v82, v83\n\t" \
    "v_permlane16_swap_b32 v184, v186\n\t" \
    "v_permlane16_swap_b32 v185, v187\n\t" \
    "v_permlane16_swap_b32 v188, v190\n\t" \
    "v_permlane16_swap_b32 v189, v191\n\t" \
    "buffer_store_dwordx4 v[184:187], %[pa], %[rd], %[xn] offen sc1\n\t" \
    "buffer_store_dwordx4 v[188:191], %[pb], %[rd], %[xn] offen sc1\n\t" \
    "buffer_store_dwordx2 v[192:193], %[o4], %[rd], %[xn] offen sc1\n\t" \
    "buffer_load_dwordx2 v[164:165], %[o0], %[rd], %[n1] offen sc1\n\t" \
    "buffer_load_dwordx2 v[166:167], %[o1], %[rd], %[n1] offen sc1\n\t" \
    "buffer_load_dwordx2 v[168:169], %[o2], %[rd], %[n1] offen sc1\n\t" \
    "buffer_load_dwordx2 v[170:171], %[o3], %[rd], %[n1] offen sc1\n\t" \
    "buffer_load_dwordx2 v[172:173], %[o4], %[rd], %[n1] offen sc1\n\t" \
    "buffer_load_dwordx2 v[174:175], %[o0], %[rd], %[na] offen sc1\n\t" \
    "buffer_load_dwordx2 v[176:177], %[o1], %[rd], %[na] offen sc1\n\t" \
    "buffer_load_dwordx2 v[178:179], %[o2], %[rd], %[na] offen sc1\n\t" \
    "buffer_load_dwordx2 v[180:181], %[o3], %[rd], %[na] offen sc1\n\t" \
    "buffer_load_dwordx2 v[182:183], %[o4], %[rd], %[na] offen sc1\n\t" \
    "v_mov_b32 v196, %[tn]\n\t" \
    "v_mov_b32 v197, %[tn]\n\t" \
    "v_mov_b32 v198, %[tn]\n\t" \
    "v_mov_b32 v199, %[tn]\n\t" \
    "s_waitcnt vmcnt(10)\n\t" \
    "buffer_store_dwordx4 v[196:199], %[fo], %[rn], %[fs] offen sc1\n\t" \
    "s_nop 1\n\t"
#define ASM_W4F_1 \
    "s_branch .Lf0%=\n\t" \
    ".Lfr%=:\n\t" \
    "s_cmp_eq_u32 %[n], 0\n\t" \
    "s_cbranch_scc1 .Lfb%=\n\t" \
    "s_sub_u32 %[n], %[n], 1\n\t" \
    "s_sleep 6\n\t" \
    "buffer_load_dwordx2 v[164:165], %[o0], %[rd], %[c1] offen sc1\n\t" \
    "buffer_load_dwordx2 v[166:167], %[o1], %[rd], %[c1] offen sc1\n\t" \
    "buffer_load_dwordx2 v[168:169], %[o2], %[rd], %[c1] offen sc1\n\t" \
    "buffer_load_dwordx2 v[170:171], %[o3], %[rd], %[c1] offen sc1\n\t" \
    "buffer_load_dwordx2 v[172:173], %[o4], %[rd], %[c1] offen sc1\n\t" \
    "buffer_load_dwordx2 v[174:175], %[o0], %[rd], %[ca] offen sc1\n\t" \
    "buffer_load_dwordx2 v[176:177], %[o1], %[rd], %[ca] offen sc1\n\t" \
    "buffer_load_dwordx2 v[178:179], %[o2], %[rd], %[ca] offen sc1\n\t" \
    "buffer_load_dwordx2 v[180:181], %[o3], %[rd], %[ca] offen sc1\n\t" \
    "buffer_load_dwordx2 v[182:183], %[o4], %[rd], %[ca] offen sc1\n\t" \
    "s_waitcnt vmcnt(0)\n\t" \
    "s_branch .Lfa%=\n\t" \
    ".Lfs0_%=:\n\t" \
    "s_cmp_eq_u32 %[n], 0\n\t" \
    "s_cbranch_scc1 .Lfu0_%=\n\t" \
    "s_sub_u32 %[n], %[n], 1\n\t" \
    "s_sleep 1\n\t" \
    "s_branch .Lft0_%=\n\t" \
    ".Lfs1_%=:\n\t" \
    "s_cmp_eq_u32 %[n], 0\n\t" \
    "s_cbranch_scc1 .Lfu1_%=\n\t" \
    "s_sub_u32 %[n], %[n], 1\n\t" \
    "s_sleep 1\n\t" \
    "s_branch .Lft1_%=\n\t" \
    ".Lf0%=:\n\t" \
    "s_waitcnt vmcnt(0)\n\t" \
    ".Lfa%=:\n\t" \
    "v_max_u32 v194, v164, v165\n\t" \
    "v_max3_u32 v194, v166, v167, v194\n\t" \
    "v_max3_u32 v194, v168, v169, v194\n\t" \
    "v_max3_u32 v194, v170, v171, v194\n\t" \
    "v_max3_u32 v194, v172, v173, v194\n\t" \
    "v_max3_u32 v194, v174, v175, v194\n\t" \
    "v_max3_u32 v194, v176, v177, v194\n\t" \
    "v_max3_u32 v194, v178, v179, v194\n\t" \
    "v_max3_u32 v194, v180, v181, v194\n\t" \
    "v_max3_u32 v194, v182, v183, v194\n\t" \
    "v_cmp_eq_u32 vcc, -1, v194\n\t" \
    "s_cbranch_vccnz .Lfr%=\n\t" \
    ".Lfb%=:\n\t" \
    "v_lshlrev_b32 v64, 16, v164\n\t" \
    "v_and_b32 v65, 0xffff0000, v164\n\t" \
    "v_lshlrev_b32 v84, 16, v174\n\t" \
    "v_and_b32 v85, 0xffff0000, v174\n\t" \
    "v_lshlrev_b32 v66, 16, v165\n\t" \
    "v_and_b32 v67, 0xffff0000, v165\n\t" \
    "v_lshlrev_b32 v86, 16, v175\n\t" \
    "v_and_b32 v87, 0xffff0000, v175\n\t" \
    "v_lshlrev_b32 v68, 16, v166\n\t" \
    "v_and_b32 v69, 0xffff0000, v166\n\t" \
    "v_lshlrev_b32 v88, 16, v176\n\t" \
    "v_and_b32 v89, 0xffff0000, v176\n\t" \
    "v_lshlrev_b32 v70, 16, v167\n\t" \
    "v_and_b32 v71, 0xffff0000, v167\n\t" \
    "v_lshlrev_b32 v90, 16, v177\n\t" \
    "v_and_b32 v91, 0xffff0000, v177\n\t" \
    "v_lshlrev_b32 v72, 16, v168\n\t" \
    "v_and_b32 v73, 0xffff0000, v168\n\t" \
    "v_lshlrev_b32 v92, 16, v178\n\t" \
    "v_and_b32 v93, 0xffff0000, v178\n\t" \
    "v_lshlrev_b32 v74, 16, v169\n\t" \
    "v_and_b32 v75, 0xffff0000, v169\n\t" \
    "v_lshlrev_b32 v94, 16, v179\n\t" \
    "v_and_b32 v95, 0xffff0000, v179\n\t" \
    "v_lshlrev_b32 v76, 16, v170\n\t" \
    "v_and_b32 v77, 0xffff0000, v170\n\t" \
    "v_lshlrev_b32 v96, 16, v180\n\t" \
    "v_and_b32 v97, 0xffff0000, v180\n\t" \
    "v_lshlrev_b32 v78, 16, v171\n\t" \
    "v_and_b32 v79, 0xffff0000, v171\n\t" \
    "v_lshlrev_b32 v98, 16, v181\n\t" \
    "v_and_b32 v99, 0xffff0000, v181\n\t" \
    "v_lshlrev_b32 v80, 16, v172\n\t" \
    "v_and_b32 v81, 0xffff0000, v172\n\t" \
    "v_lshlrev_b32 v100, 16, v182\n\t" \
    "v_and_b32 v101, 0xffff0000, v182\n\t" \
    "v_lshlrev_b32 v82, 16, v173\n\t" \
    "v_and_b32 v83, 0xffff0000, v173\n\t" \
    "v_lshlrev_b32 v102, 16, v183\n\t" \
    "v_and_b32 v103, 0xffff0000, v183\n\t" \
    "v_pk_add_f32 v[64:65], v[64:65], v[84:85]\n\t" \
    "v_pk_add_f32 v[66:67], v[66:67], v[86:87]\n\t" \
    "v_pk_add_f32 v[68:69], v[68:69], v[88:89]\n\t" \
    "v_pk_add_f32 v[70:71], v[70:71], v[90:91]\n\t" \
    "v_pk_add_f32 v[72:73], v[72:73], v[92:93]\n\t" \
    "v_pk_add_f32 v[74:75], v[74:75], v[94:95]\n\t" \
    "v_pk_add_f32 v[76:77], v[76:77], v[96:97]\n\t" \
    "v_pk_add_f32 v[78:79], v[78:79], v[98:99]\n\t" \
    "v_pk_add_f32 v[80:81], v[80:81], v[100:101]\n\t" \
    "v_pk_add_f32 v[82:83], v[82:83], v[102:103]\n\t" \
    ".Lft0_%=:\n\t" \
    "ds_read_b32 v194, %[f0]\n\t" \
    "s_waitcnt lgkmcnt(0)\n\t" \
    "v_cmp_ge_u32 vcc, %[u], v194\n\t" \
    "s_cbranch_vccnz .Lfs0_%=\n\t" \
    ".Lfu0_%=:\n\t" \
    "ds_read_b128 v[84:87], %[la] offset:0\n\t" \
    "ds_read_b128 v[88:91], %[la] offset:6144\n\t" \
    "ds_read_b128 v[92:95], %[la] offset:12288\n\t" \
    "ds_read_b128 v[96:99], %[la] offset:18432\n\t" \
    "ds_read_b128 v[100:103], %[la] offset:24576\n\t" \
    "ds_read_b128 v[104:107], %[la] offset:1024\n\t" \
    "ds_read_b128 v[108:111], %[la] offset:7168\n\t" \
    "ds_read_b128 v[112:115], %[la] offset:13312\n\t" \
    "ds_read_b128 v[116:119], %[la] offset:19456\n\t" \
    "ds_read_b128 v[120:123], %[la] offset:25600\n\t" \
    "ds_read_b128 v[124:127], %[la] offset:2048\n\t" \
    "s_waitcnt lgkmcnt(10)\n\t" \
    "v_pk_add_f32 v[64:65], v[64:65], v[84:85]\n\t" \
    "v_pk_add_f32 v[66:67], v[66:67], v[86:87]\n\t" \
    "ds_read_b128 v[128:131], %[la] offset:8192\n\t" \
    "s_waitcnt lgkmcnt(10)\n\t" \
    "v_pk_add_f32 v[68:69], v[68:69], v[88:89]\n\t" \
    "v_pk_add_f32 v[70:71], v[70:71], v[90:91]\n\t" \
    "ds_read_b128 v[132:135], %[la] offset:14336\n\t" \
    "s_waitcnt lgkmcnt(10)\n\t" \
    "v_pk_add_f32 v[72:73], v[72:73], v[92:93]\n\t" \
    "v_pk_add_f32 v[74:75], v[74:75], v[94:95]\n\t" \
    "ds_read_b128 v[136:139], %[la] offset:20480\n\t" \
    "s_waitcnt lgkmcnt(10)\n\t" \
    "v_pk_add_f32 v[76:77], v[76:77], v[96:97]\n\t" \
    "v_pk_add_f32 v[78:79], v[78:79], v[98:99]\n\t" \
    "ds_read_b128 v[140:143], %[la] offset:26624\n\t" \
    "s_waitcnt lgkmcnt(10)\n\t" \
    "v_pk_add_f32 v[80:81], v[80:81], v[100:101]\n\t" \
    "v_pk_add_f32 v[82:83], v[82:83], v[102:103]\n\t" \
    "ds_read_b128 v[144:147], %[la] offset:3072\n\t" \
    "s_waitcnt lgkmcnt(10)\n\t" \
    "v_pk_add_f32 v[64:65], v[64:65], v[104:105]\n\t" \
    "v_pk_add_f32 v[66:67], v[66:67], v[106:107]\n\t" \
    "ds_read_b128 v[148:151], %[la] offset:9216\n\t" \
    "s_waitcnt lgkmcnt(10)\n\t" \
    "v_pk_add_f32 v[68:69], v[68:69], v[108:109]\n\t" \
    "v_pk_add_f32 v[70:71], v[70:71], v[110:111]\n\t" \
    "ds_read_b128 v[152:155], %[la] offset:15360\n\t" \
    "s_waitcnt lgkmcnt(10)\n\t" \
    "v_pk_add_f32 v[72:73], v[72:73], v[112:113]\n\t" \
    "v_pk_add_f32 v[74:75], v[74:75], v[114:115]\n\t" \
    "ds_read_b128 v[156:159], %[la] offset:21504\n\t" \
    "s_waitcnt lgkmcnt(10)\n\t" \
    "v_pk_add_f32 v[76:77], v[76:77], v[116:117]\n\t" \
    "v_pk_add_f32 v[78:79], v[78:79], v[118:119]\n\t" \
    "ds_read_b128 v[160:163], %[la] offset:27648\n\t" \
    "s_waitcnt lgkmcnt(10)\n\t" \
    "v_pk_add_f32 v[80:81], v[80:81], v[120:121]\n\t" \
    "v_pk_add_f32 v[82:83], v[82:83], v[122:123]\n\t" \
    "s_waitcnt lgkmcnt(9)\n\t" \
    "v_pk_add_f32 v[64:65], v[64:65], v[124:125]\n\t" \
    "v_pk_add_f32 v[66:67], v[66:67], v[126:127]\n\t" \
    "s_waitcnt lgkmcnt(8)\n\t" \
    "v_pk_add_f32 v[68:69], v[68:69], v[128:129]\n\t" \
    "v_pk_add_f32 v[70:71], v[70:71], v[130:131]\n\t" \
    "s_waitcnt lgkmcnt(7)\n\t" \
    "v_pk_add_f32 v[72:73], v[72:73], v[132:133]\n\t" \
    "v_pk_add_f32 v[74:75], v[74:75], v[134:135]\n\t" \
    "s_waitcnt lgkmcnt(6)\n\t" \
    "v_pk_add_f32 v[76:77], v[76:77], v[136:137]\n\t" \
    "v_pk_add_f32 v[78:79], v[78:79], v[138:139]\n\t" \
    "s_waitcnt lgkmcnt(5)\n\t" \
    "v_pk_add_f32 v[80:81], v[80:81], v[140:141]\n\t" \
    "v_pk_add_f32 v[82:83], v[82:83], v[142:143]\n\t" \
    "s_waitcnt lgkmcnt(4)\n\t" \
    "v_pk_add_f32 v[64:65], v[64:65], v[144:145]\n\t" \
    "v_pk_add_f32 v[66:67], v[66:67], v[146:147]\n\t" \
    "s_waitcnt lgkmcnt(3)\n\t" \
    "v_pk_add_f32 v[68:69], v[68:69], v[148:149]\n\t" \
    "v_pk_add_f32 v[70:71], v[70:71], v[150:151]\n\t" \
    "s_waitcnt lgkmcnt(2)\n\t" \
    "v_pk_add_f32 v[72:73], v[72:73], v[152:153]\n\t" \
    "v_pk_add_f32 v[74:75], v[74:75], v[154:155]\n\t" \
    "s_waitcnt lgkmcnt(1)\n\t" \
    "v_pk_add_f32 v[76:77], v[76:77], v[156:157]\n\t" \
    "v_pk_add_f32 v[78:79], v[78:79], v[158:159]\n\t" \
    "s_waitcnt lgkmcnt(0)\n\t" \
    "v_pk_add_f32 v[80:81], v[80:81], v[160:161]\n\t" \
    "v_pk_add_f32 v[82:83], v[82:83], v[162:163]\n\t" \
    ".Lft1_%=:\n\t" \
    "ds_read_b32 v194, %[f1]\n\t" \
    "s_waitcnt lgkmcnt(0)\n\t" \
    "v_cmp_ge_u32 vcc, %[u], v194\n\t" \
    "s_cbranch_vccnz .Lfs1_%=\n\t" \
    ".Lfu1_%=:\n\t" \
    ASM_ST2 \
    "ds_read_b128 v[84:87], %[la] offset:4096\n\t" \
    "ds_read_b128 v[88:91], %[la] offset:10240\n\t" \
    "ds_read_b128 v[92:95], %[la] offset:16384\n\t" \
    "ds_read_b128 v[96:99], %[la] offset:22528\n\t" \
    "ds_read_b128 v[100:103], %[la] offset:28672\n\t" \
    "ds_read_b128 v[104:107], %[la] offset:5120\n\t" \
    "ds_read_b128 v[108:111], %[la] offset:11264\n\t" \
    "ds_read_b128 v[112:115], %[la] offset:17408\n\t" \
    "ds_read_b128 v[116:119], %[la] offset:23552\n\t" \
    "ds_read_b128 v[120:123], %[la] offset:29696\n\t" \
    "v_mov_b32 v194, %[un]\n\t" \
    "ds_write_b32 %[fd], v194\n\t" \
    "s_waitcnt lgkmcnt(10)\n\t" \
    "v_pk_add_f32 v[64:65], v[64:65], v[84:85]\n\t" \
    "v_pk_add_f32 v[66:67], v[66:67], v[86:87]\n\t" \
    "s_waitcnt lgkmcnt(9)\n\t" \
    "v_pk_add_f32 v[68:69], v[68:69], v[88:89]\n\t" \
    "v_pk_add_f32 v[70:71], v[70:71], v[90:91]\n\t" \
    "s_waitcnt lgkmcnt(8)\n\t" \
    "v_pk_add_f32 v[72:73], v[72:73], v[92:93]\n\t" \
    "v_pk_add_f32 v[74:75], v[74:75], v[94:95]\n\t" \
    "s_waitcnt lgkmcnt(7)\n\t" \
    "v_pk_add_f32 v[76:77], v[76:77], v[96:97]\n\t" \
    "v_pk_add_f32 v[78:79], v[78:79], v[98:99]\n\t" \
    "s_waitcnt lgkmcnt(6)\n\t" \
    "v_pk_add_f32 v[80:81], v[80:81], v[100:101]\n\t" \
    "v_pk_add_f32 v[82:83], v[82:83], v[102:103]\n\t" \
    "s_waitcnt lgkmcnt(5)\n\t" \
    "v_pk_add_f32 v[64:65], v[64:65], v[104:105]\n\t" \
    "v_pk_add_f32 v[66:67], v[66:67], v[106:107]\n\t" \
    "s_waitcnt lgkmcnt(4)\n\t" \
    "v_pk_add_f32 v[68:69], v[68:69], v[108:109]\n\t" \
    "v_pk_add_f32 v[70:71], v[70:71], v[110:111]\n\t" \
    "s_waitcnt lgkmcnt(3)\n\t" \
    "v_pk_add_f32 v[72:73], v[72:73], v[112:113]\n\t" \
    "v_pk_add_f32 v[74:75], v[74:75], v[114:115]\n\t" \
    "s_waitcnt lgkmcnt(2)\n\t" \
    "v_pk_add_f32 v[76:77], v[76:77], v[116:117]\n\t" \
    "v_pk_add_f32 v[78:79], v[78:79], v[118:119]\n\t" \
    "s_waitcnt lgkmcnt(1)\n\t" \
    "v_pk_add_f32 v[80:81], v[80:81], v[120:121]\n\t" \
    "v_pk_add_f32 v[82:83], v[82:83], v[122:123]\n\t" \
    "v_cvt_pk_bf16_f32 v184, v64, v65\n\t" \
    "v_cvt_pk_bf16_f32 v185, v66, v67\n\t" \
    "v_cvt_pk_bf16_f32 v186, v68, v69\n\t" \
    "v_cvt_pk_bf16_f32 v187, v70, v71\n\t" \
    "v_cvt_pk_bf16_f32 v188, v72, v73\n\t" \
    "v_cvt_pk_bf16_f32 v189, v74, v75\n\t" \
    "v_cvt_pk_bf16_f32 v190, v76, v77\n\t" \
    "v_cvt_pk_bf16_f32 v191, v78, v79\n\t" \
    "v_cvt_pk_bf16_f32 v192, v80, v81\n\t" \
    "v_cvt_pk_bf16_f32 v193, v82, v83\n\t" \
    "v_permlane16_swap_b32 v184, v186\n\t" \
    "v_permlane16_swap_b32 v185, v187\n\t" \
    "v_permlane16_swap_b32 v188, v190\n\t" \
    "v_permlane16_swap_b32 v189, v191\n\t" \
    "buffer_store_dwordx4 v[184:187], %[pa], %[ro], %[so] offen nt\n\t" \
    "buffer_store_dwordx4 v[188:191], %[pa], %[ro], %[so] offen offset:64 nt\n\t" \
    "buffer_store_dwordx2 v[192:193], %[pb], %[ro], %[so] offen offset:128 nt\n\t" \
    "buffer_load_dwordx2 v[164:165], %[o0], %[rd], %[n1] offen sc1\n\t" \
    "buffer_load_dwordx2 v[166:167], %[o1], %[rd], %[n1] offen sc1\n\t" \
    "buffer_load_dwordx2 v[168:169], %[o2], %[rd], %[n1] offen sc1\n\t" \
    "buffer_load_dwordx2 v[170:171], %[o3], %[rd], %[n1] offen sc1\n\t" \
    "buffer_load_dwordx2 v[172:173], %[o4], %[rd], %[n1] offen sc1\n\t" \
    "buffer_load_dwordx2 v[174:175], %[o0], %[rd], %[na] offen sc1\n\t" \
    "buffer_load_dwordx2 v[176:177], %[o1], %[rd], %[na] offen sc1\n\t" \
    "buffer_load_dwordx2 v[178:179], %[o2], %[rd], %[na] offen sc1\n\t" \
    "buffer_load_dwordx2 v[180:181], %[o3], %[rd], %[na] offen sc1\n\t" \
    "buffer_load_dwordx2 v[182:183], %[o4], %[rd], %[na] offen sc1\n\t"
#define ASM_WCS \
    "v_mfma_f32_16x16x32_bf16 v[196:199], v[0:3], v[216:219], 0\n\t" \
    "v_mfma_f32_16x16x32_bf16 v[200:203], v[80:83], v[216:219], 0\n\t" \
    "v_mfma_f32_16x16x32_bf16 v[196:199], v[4:7], v[216:219], v[196:199]\n\t" \
    "v_mfma_f32_16x16x32_bf16 v[200:203], v[84:87], v[216:219], v[200:203]\n\t" \
    "v_mfma_f32_16x16x32_bf16 v[196:199], v[8:11], v[216:219], v[196:199]\n\t" \
    "v_mfma_f32_16x16x32_bf16 v[200:203], v[88:91], v[216:219], v[200:203]\n\t" \
    "v_mfma_f32_16x16x32_bf16 v[196:199], v[12:15], v[216:219], v[196:199]\n\t" \
    "v_mfma_f32_16x16x32_bf16 v[200:203], v[92:95], v[216:219], v[200:203]\n\t" \
    "v_mfma_f32_16x16x32_bf16 v[196:199], v[16:19], v[216:219], v[196:199]\n\t" \
    "v_mfma_f32_16x16x32_bf16 v[200:203], v[96:99], v[216:219], v[200:203]\n\t" \
    "v_mfma_f32_16x16x32_bf16 v[196:199], v[20:23], v[216:219], v[196:199]\n\t" \
    "v_mfma_f32_16x16x32_bf16 v[200:203], v[100:103], v[216:219], v[200:203]\n\t" \
    "v_mfma_f32_16x16x32_bf16 v[196:199], v[24:27], v[216:219], v[196:199]\n\t" \
    "v_mfma_f32_16x16x32_bf16 v[200:203], v[104:107], v[216:219], v[200:203]\n\t" \
    "v_mfma_f32_16x16x32_bf16 v[196:199], v[28:31], v[216:219], v[196:199]\n\t" \
    "v_mfma_f32_16x16x32_bf16 v[200:203], v[108:111], v[216:219], v[200:203]\n\t" \
    "v_mfma_f32_16x16x32_bf16 v[196:199], v[32:35], v[216:219], v[196:199]\n\t" \
    "v_mfma_f32_16x16x32_bf16 v[200:203], v[112:115], v[216:219], v[200:203]\n\t" \
    "v_mfma_f32_16x16x32_bf16 v[196:199], v[36:39], v[216:219], v[196:199]\n\t" \
    "v_mfma_f32_16x16x32_bf16 v[200:203], v[116:119], v[216:219], v[200:203]\n\t" \
    "v_mfma_f32_16x16x32_bf16 v[196:199], v[40:43], v[216:219], v[196:199]\n\t" \
    "v_mfma_f32_16x16x32_bf16 v[200:203], v[120:123], v[216:219], v[200:203]\n\t" \
    "v_mfma_f32_16x16x32_bf16 v[196:199], v[44:47], v[216:219], v[196:199]\n\t" \
    "v_mfma_f32_16x16x32_bf16 v[200:203], v[124:127], v[216:219], v[200:203]\n\t" \
    "v_mfma_f32_16x16x32_bf16 v[196:199], v[48:51], v[216:219], v[196:199]\n\t" \
    "v_mfma_f32_16x16x32_bf16 v[200:203], v[128:131], v[216:219], v[200:203]\n\t" \
    "v_mfma_f32_16x16x32_bf16 v[196:199], v[52:55], v[216:219], v[196:199]\n\t" \
    "v_mfma_f32_16x16x32_bf16 v[200:203], v[132:135], v[216:219], v[200:203]\n\t" \
    "v_mfma_f32_16x16x32_bf16 v[196:199], v[56:59], v[216:219], v[196:199]\n\t" \
    "v_mfma_f32_16x16x32_bf16 v[200:203], v[136:139], v[216:219], v[200:203]\n\t" \
    "v_mfma_f32_16x16x32_bf16 v[196:199], v[60:63], v[216:219], v[196:199]\n\t" \
    "v_mfma_f32_16x16x32_bf16 v[200:203], v[140:143], v[216:219], v[200:203]\n\t" \
    "v_mfma_f32_16x16x32_bf16 v[196:199], v[64:67], v[216:219], v[196:199]\n\t" \
    "v_mfma_f32_16x16x32_bf16 v[200:203], v[144:147], v[216:219], v[200:203]\n\t" \
    "v_mfma_f32_16x16x32_bf16 v[196:199], v[68:71], v[216:219], v[196:199]\n\t" \
    "v_mfma_f32_16x16x32_bf16 v[200:203], v[148:151], v[216:219], v[200:203]\n\t" \
    "v_mfma_f32_16x16x32_bf16 v[196:199], v[72:75], v[216:219], v[196:199]\n\t" \
    "v_mfma_f32_16x16x32_bf16 v[200:203], v[152:155], v[216:219], v[200:203]\n\t" \
    "v_mfma_f32_16x16x32_bf16 v[196:199], v[76:79], v[216:219], v[196:199]\n\t" \
    "v_mfma_f32_16x16x32_bf16 v[200:203], v[156:159], v[216:219], v[200:203]\n\t" \
    "s_nop 7\n\t"
#define ASM_WT1_LO \
    "s_branch .Lt0%=\n\t" \
    ".Ltr%=:\n\t" \
    "s_cmp_gt_i32 %[n], 0\n\t" \
    "s_cbranch_scc0 .Ltg%=\n\t" \
    ".Lfw%=:\n\t" \
    "s_mov_b32 %[a], 0\n\t" \
    "v_max_u32 v252, v232, v233\n\t" \
    "v_max3_u32 v252, v234, v235, v252\n\t" \
    "v_cmp_eq_u32 vcc, -1, v252\n\t" \
    "s_cbranch_vccz .Lgw0_%=\n\t" \
    "buffer_load_dwordx4 v[232:235], %[l16], %[rs], %[i0] offen offset:0 sc1\n\t" \
    "s_mov_b32 %[a], 1\n\t" \
    ".Lgw0_%=:\n\t" \
    "v_max_u32 v252, v236, v237\n\t" \
    "v_max3_u32 v252, v238, v239, v252\n\t" \
    "v_cmp_eq_u32 vcc, -1, v252\n\t" \
    "s_cbranch_vccz .Lgw1_%=\n\t" \
    "buffer_load_dwordx4 v[236:239], %[l16], %[rs], %[i0] offen offset:1024 sc1\n\t" \
    "s_mov_b32 %[a], 1\n\t" \
    ".Lgw1_%=:\n\t" \
    "v_max_u32 v252, v240, v241\n\t" \
    "v_max3_u32 v252, v242, v243, v252\n\t" \
    "v_cmp_eq_u32 vcc, -1, v252\n\t" \
    "s_cbranch_vccz .Lgw2_%=\n\t" \
    "buffer_load_dwordx4 v[240:243], %[l16], %[rs], %[i0] offen offset:2048 sc1\n\t" \
    "s_mov_b32 %[a], 1\n\t" \
    ".Lgw2_%=:\n\t" \
    "v_max_u32 v252, v244, v245\n\t" \
    "v_max3_u32 v252, v246, v247, v252\n\t" \
    "v_cmp_eq_u32 vcc, -1, v252\n\t" \
    "s_cbranch_vccz .Lgw3_%=\n\t" \
    "buffer_load_dwordx4 v[244:247], %[l16], %[rs], %[i0] offen offset:3072 sc1\n\t" \
    "s_mov_b32 %[a], 1\n\t" \
    ".Lgw3_%=:\n\t" \
    "v_max_u32 v252, v248, v249\n\t" \
    "v_max3_u32 v252, v250, v251, v252\n\t" \
    "v_cmp_eq_u32 vcc, -1, v252\n\t" \
    "s_cbranch_vccz .Lgw4_%=\n\t" \
    "buffer_load_dwordx4 v[248:251], %[l16], %[rs], %[i1] offen offset:0 sc1\n\t" \
    "s_mov_b32 %[a], 1\n\t" \
    ".Lgw4_%=:\n\t" \
    "s_cmp_eq_u32 %[a], 0\n\t" \
    "s_cbranch_scc1 .Lhw%=\n\t" \
    "s_waitcnt vmcnt(20)\n\t" \
    ".Liw%=:\n\t" \
    "v_max_u32 v252, v232, v233\n\t" \
    "v_max3_u32 v252, v234, v235, v252\n\t" \
    "v_max3_u32 v252, v236, v237, v252\n\t" \
    "v_max3_u32 v252, v238, v239, v252\n\t" \
    "v_max3_u32 v252, v240, v241, v252\n\t" \
    "v_max3_u32 v252, v242, v243, v252\n\t" \
    "v_max3_u32 v252, v244, v245, v252\n\t" \
    "v_max3_u32 v252, v246, v247, v252\n\t" \
    "v_max3_u32 v252, v248, v249, v252\n\t" \
    "v_max3_u32 v252, v250, v251, v252\n\t" \
    "v_cmp_eq_u32 vcc, -1, v252\n\t" \
    "s_cbranch_vccz .Lhw%=\n\t" \
    "s_sleep 1\n\t" \
    "v_max_u32 v252, v232, v233\n\t" \
    "v_max3_u32 v252, v234, v235, v252\n\t" \
    "v_max3_u32 v252, v236, v237, v252\n\t" \
    "v_max3_u32 v252, v238, v239, v252\n\t" \
    "v_max3_u32 v252, v240, v241, v252\n\t" \
    "v_max3_u32 v252, v242, v243, v252\n\t" \
    "v_max3_u32 v252, v244, v245, v252\n\t" \
    "v_max3_u32 v252, v246, v247, v252\n\t" \
    "v_max3_u32 v252, v248, v249, v252\n\t" \
    "v_max3_u32 v252, v250, v251, v252\n\t" \
    "v_cmp_eq_u32 vcc, -1, v252\n\t" \
    "s_cbranch_vccz .Lhw%=\n\t" \
    "s_sleep 1\n\t" \
    "v_max_u32 v252, v232, v233\n\t" \
    "v_max3_u32 v252, v234, v235, v252\n\t" \
    "v_max3_u32 v252, v236, v237, v252\n\t" \
    "v_max3_u32 v252, v238, v239, v252\n\t" \
    "v_max3_u32 v252, v240, v241, v252\n\t" \
    "v_max3_u32 v252, v242, v243, v252\n\t" \
    "v_max3_u32 v252, v244, v245, v252\n\t" \
    "v_max3_u32 v252, v246, v247, v252\n\t" \
    "v_max3_u32 v252, v248, v249, v252\n\t" \
    "v_max3_u32 v252, v250, v251, v252\n\t" \
    "v_cmp_eq_u32 vcc, -1, v252\n\t" \
    "s_cbranch_vccz .Lhw%=\n\t" \
    "s_sleep 1\n\t" \
    "v_max_u32 v252, v232, v233\n\t" \
    "v_max3_u32 v252, v234, v235, v252\n\t" \
    "v_max3_u32 v252, v236, v237, v252\n\t" \
    "v_max3_u32 v252, v238, v239, v252\n\t" \
    "v_max3_u32 v252, v240, v241, v252\n\t" \
    "v_max3_u32 v252, v242, v243, v252\n\t" \
    "v_max3_u32 v252, v244, v245, v252\n\t" \
    "v_max3_u32 v252, v246, v247, v252\n\t" \
    "v_max3_u32 v252, v248, v249, v252\n\t" \
    "v_max3_u32 v252, v250, v251, v252\n\t" \
    "v_cmp_eq_u32 vcc, -1, v252\n\t" \
    "s_cbranch_vccz .Lhw%=\n\t" \
    "s_sleep 1\n\t" \
    "v_max_u32 v252, v232, v233\n\t" \
    "v_max3_u32 v252, v234, v235, v252\n\t" \
    "v_max3_u32 v252, v236, v237, v252\n\t" \
    "v_max3_u32 v252, v238, v239, v252\n\t" \
    "v_max3_u32 v252, v240, v241, v252\n\t" \
    "v_max3_u32 v252, v242, v243, v252\n\t" \
    "v_max3_u32 v252, v244, v245, v252\n\t" \
    "v_max3_u32 v252, v246, v247, v252\n\t" \
    "v_max3_u32 v252, v248, v249, v252\n\t" \
    "v_max3_u32 v252, v250, v251, v252\n\t" \
    "v_cmp_eq_u32 vcc, -1, v252\n\t" \
    "s_cbranch_vccz .Lhw%=\n\t" \
    "s_sleep 1\n\t" \
    "v_max_u32 v252, v232, v233\n\t" \
    "v_max3_u32 v252, v234, v235, v252\n\t" \
    "v_max3_u32 v252, v236, v237, v252\n\t" \
    "v_max3_u32 v252, v238, v239, v252\n\t" \
    "v_max3_u32 v252, v240, v241, v252\n\t" \
    "v_max3_u32 v252, v242, v243, v252\n\t" \
    "v_max3_u32 v252, v244, v245, v252\n\t" \
    "v_max3_u32 v252, v246, v247, v252\n\t" \
    "v_max3_u32 v252, v248, v249, v252\n\t" \
    "v_max3_u32 v252, v250, v251, v252\n\t" \
    "v_cmp_eq_u32 vcc, -1, v252\n\t" \
    "s_cbranch_vccz .Lhw%=\n\t" \
    "s_sleep 1\n\t" \
    "s_sub_u32 %[n], %[n], 1\n\t" \
    "s_cmp_gt_i32 %[n], 0\n\t" \
    "s_cbranch_scc1 .Lfw%=\n\t" \
    ".Lhw%=:\n\t" \
    "s_branch .Lta%=\n\t" \
    ".Lt0%=:\n\t" \
    "s_waitcnt vmcnt(1)\n\t" \
    ".Lta%=:\n\t" \
    "ds_write_b128 %[lw], v[232:235] offset:0\n\t" \
    "v_max_u32 v252, v232, v233\n\t" \
    "v_max3_u32 v252, v234, v235, v252\n\t" \
    "v_mfma_f32_16x16x32_bf16 v[188:191], v[232:235], v[232:235], 0\n\t" \
    "ds_write_b128 %[lw], v[236:239] offset:1024\n\t" \
    "v_max3_u32 v252, v236, v237, v252\n\t" \
    "v_max3_u32 v252, v238, v239, v252\n\t" \
    "v_mfma_f32_16x16x32_bf16 v[188:191], v[236:239], v[236:239], v[188:191]\n\t" \
    "ds_write_b128 %[lw], v[240:243] offset:2048\n\t" \
    "v_max3_u32 v252, v240, v241, v252\n\t" \
    "v_max3_u32 v252, v242, v243, v252\n\t" \
    "v_mfma_f32_16x16x32_bf16 v[188:191], v[240:243], v[240:243], v[188:191]\n\t" \
    "ds_write_b128 %[lw], v[244:247] offset:3072\n\t" \
    "v_max3_u32 v252, v244, v245, v252\n\t" \
    "v_max3_u32 v252, v246, v247, v252\n\t" \
    "v_mfma_f32_16x16x32_bf16 v[188:191], v[244:247], v[244:247], v[188:191]\n\t" \
    "ds_write_b128 %[lw], v[248:251] offset:4096\n\t" \
    "v_max3_u32 v252, v248, v249, v252\n\t" \
    "v_max3_u32 v252, v250, v251, v252\n\t" \
    "v_mfma_f32_16x16x32_bf16 v[188:191], v[248:251], v[248:251], v[188:191]\n\t" \
    "v_mfma_f32_16x16x32_bf16 v[192:195], v[216:219], v[232:235], 0\n\t" \
    "v_mfma_f32_16x16x32_bf16 v[192:195], v[216:219], v[236:239], v[192:195]\n\t" \
    "v_mfma_f32_16x16x32_bf16 v[192:195], v[216:219], v[240:243], v[192:195]\n\t" \
    "v_mfma_f32_16x16x32_bf16 v[192:195], v[216:219], v[244:247], v[192:195]\n\t" \
    "v_mfma_f32_16x16x32_bf16 v[192:195], v[216:219], v[248:251], v[192:195]\n\t" \
    "v_cmp_eq_u32 vcc, -1, v252\n\t" \
    "s_cbranch_vccnz .Ltr%=\n\t" \
    ".Ltg%=:\n\t" \
    ASM_ST2 \
    "s_nop 5\n\t" \
    "ds_write_b128 %[lw], v[188:191] offset:5120\n\t" \
    "ds_write_b128 %[lw], v[192:195] offset:6144\n\t" \
    "s_waitcnt lgkmcnt(0)\n\t" \
    "s_setprio 2\n\t" \
    "s_barrier\n\t" ASM_ST \
    "ds_read_b128 v[160:163], %[la] offset:0\n\t" \
    "ds_read_b128 v[164:167], %[la] offset:1024\n\t" \
    "ds_read_b128 v[168:171], %[la] offset:2048\n\t" \
    "ds_read2st64_b32 v[204:205], %[sr] offset0:20 offset1:24\n\t" \
    "ds_read_b128 v[172:175], %[la] offset:3072\n\t" \
    "ds_read_b128 v[176:179], %[la] offset:8192\n\t" \
    "ds_read_b128 v[180:183], %[la] offset:9216\n\t" \
    "ds_read_b128 v[184:187], %[la] offset:10240\n\t" \
    "s_waitcnt lgkmcnt(7)\n\t" \
    "v_mfma_f32_16x16x32_bf16 v[192:195], v[80:83], v[160:163], 0\n\t" \
    "s_waitcnt lgkmcnt(6)\n\t" \
    "v_mfma_f32_16x16x32_bf16 v[192:195], v[84:87], v[164:167], v[192:195]\n\t" \
    "v_mfma_f32_16x16x32_bf16 v[188:191], v[0:3], v[160:163], 0\n\t" \
    "ds_read_b128 v[160:163], %[la] offset:11264\n\t" \
    "v_mfma_f32_16x16x32_bf16 v[188:191], v[4:7], v[164:167], v[188:191]\n\t" \
    "ds_read_b128 v[164:167], %[la] offset:16384\n\t" \
    "s_waitcnt lgkmcnt(7)\n\t" \
    "v_mfma_f32_16x16x32_bf16 v[188:191], v[8:11], v[168:171], v[188:191]\n\t" \
    "s_waitcnt lgkmcnt(6)\n\t" \
    "s_waitcnt lgkmcnt(5)\n\t" \
    "v_mfma_f32_16x16x32_bf16 v[188:191], v[12:15], v[172:175], v[188:191]\n\t" \
    "v_permlane16_swap_b32 v204, v205\n\t" \
    "v_mfma_f32_16x16x32_bf16 v[192:195], v[88:91], v[168:171], v[192:195]\n\t" \
    "ds_read_b128 v[168:171], %[la] offset:17408\n\t" \
    "v_mfma_f32_16x16x32_bf16 v[192:195], v[92:95], v[172:175], v[192:195]\n\t" \
    "ds_read_b128 v[172:175], %[la] offset:18432\n\t" \
    "v_add_f32 v206, v204, v205\n\t" \
    "s_waitcnt lgkmcnt(6)\n\t" \
    "v_mfma_f32_16x16x32_bf16 v[192:195], v[96:99], v[176:179], v[192:195]\n\t" \
    "v_add_f32 v207, v204, v205\n\t" \
    "s_waitcnt lgkmcnt(5)\n\t" \
    "v_mfma_f32_16x16x32_bf16 v[192:195], v[100:103], v[180:183], v[192:195]\n\t" \
    "v_mfma_f32_16x16x32_bf16 v[188:191], v[16:19], v[176:179], v[188:191]\n\t" \
    "ds_read_b128 v[176:179], %[la] offset:19456\n\t" \
    "v_permlane32_swap_b32 v206, v207\n\t" \
    "v_mfma_f32_16x16x32_bf16 v[188:191], v[20:23], v[180:183], v[188:191]\n\t" \
    "ds_read_b128 v[180:183], %[la] offset:24576\n\t" \
    "s_waitcnt lgkmcnt(6)\n\t" \
    "v_mfma_f32_16x16x32_bf16 v[188:191], v[24:27], v[184:187], v[188:191]\n\t" \
    "v_add_f32 v204, v206, v207\n\t" \
    "s_waitcnt lgkmcnt(5)\n\t" \
    "v_mfma_f32_16x16x32_bf16 v[188:191], v[28:31], v[160:163], v[188:191]\n\t" \
    "v_add_f32 v205, v206, v207\n\t" \
    "v_mfma_f32_16x16x32_bf16 v[192:195], v[104:107], v[184:187], v[192:195]\n\t" \
    "ds_read_b128 v[184:187], %[la] offset:25600\n\t" \
    "v_mfma_f32_16x16x32_bf16 v[192:195], v[108:111], v[160:163], v[192:195]\n\t" \
    "ds_read_b128 v[160:163], %[la] offset:26624\n\t" \
    "v_permlane16_swap_b32 v204, v205\n\t" \
    "s_waitcnt lgkmcnt(6)\n\t" \
    "v_mfma_f32_16x16x32_bf16 v[192:195], v[112:115], v[164:167], v[192:195]\n\t" \
    "s_waitcnt lgkmcnt(5)\n\t" \
    "v_mfma_f32_16x16x32_bf16 v[192:195], v[116:119], v[168:171], v[192:195]\n\t" \
    "v_mul_f32 v205, %[kd], v205\n\t" \
    "v_mfma_f32_16x16x32_bf16 v[188:191], v[32:35], v[164:167], v[188:191]\n\t" \
    "ds_read_b128 v[164:167], %[la] offset:27648\n\t" \
    "v_mul_f32 v204, %[kd], v204\n\t" \
    "v_mfma_f32_16x16x32_bf16 v[188:191], v[36:39], v[168:171], v[188:191]\n\t" \
    "ds_read_b128 v[168:171], %[la] offset:4096\n\t" \
    "v_fma_f32 v204, -v205, v205, v204\n\t" \
    "s_waitcnt lgkmcnt(6)\n\t" \
    "v_mfma_f32_16x16x32_bf16 v[188:191], v[40:43], v[172:175], v[188:191]\n\t" \
    "v_max_f32 v204, 0, v204\n\t" \
    "s_waitcnt lgkmcnt(5)\n\t" \
    "v_mfma_f32_16x16x32_bf16 v[188:191], v[44:47], v[176:179], v[188:191]\n\t" \
    "v_add_f32 v206, %[ke], v204\n\t" \
    "v_mfma_f32_16x16x32_bf16 v[192:195], v[120:123], v[172:175], v[192:195]\n\t" \
    "ds_read_b128 v[172:175], %[la] offset:12288\n\t" \
    "v_rsq_f32 v206, v206\n\t" \
    "v_mfma_f32_16x16x32_bf16 v[192:195], v[124:127], v[176:179], v[192:195]\n\t" \
    "ds_read_b128 v[176:179], %[la] offset:20480\n\t" \
    "s_waitcnt lgkmcnt(6)\n\t" \
    "v_mfma_f32_16x16x32_bf16 v[192:195], v[128:131], v[180:183], v[192:195]\n\t" \
    "v_mul_f32 v207, v204, v206\n\t" \
    "s_waitcnt lgkmcnt(5)\n\t" \
    "v_mfma_f32_16x16x32_bf16 v[192:195], v[132:135], v[184:187], v[192:195]\n\t" \
    "v_fma_f32 v207, v207, v206, %[ke]\n\t" \
    "v_mfma_f32_16x16x32_bf16 v[188:191], v[48:51], v[180:183], v[188:191]\n\t" \
    "ds_read_b128 v[180:183], %[la] offset:28672\n\t" \
    "v_rsq_f32 v207, v207\n\t" \
    "v_mfma_f32_16x16x32_bf16 v[188:191], v[52:55], v[184:187], v[188:191]\n\t" \
    "v_mul_f32 v206, v206, v207\n\t" \
    "v_mul_f32 v207, -v205, v206\n\t" \
    "s_waitcnt lgkmcnt(5)\n\t" \
    "v_mfma_f32_16x16x32_bf16 v[188:191], v[56:59], v[160:163], v[188:191]\n\t" \
    "v_mul_f32 v208, v196, v207\n\t" \
    "v_mul_f32 v209, v197, v207\n\t" \
    "s_waitcnt lgkmcnt(4)\n\t" \
    "v_mfma_f32_16x16x32_bf16 v[188:191], v[60:63], v[164:167], v[188:191]\n\t" \
    "v_mul_f32 v210, v198, v207\n\t" \
    "v_mul_f32 v211, v199, v207\n\t" \
    "v_mfma_f32_16x16x32_bf16 v[192:195], v[136:139], v[160:163], v[192:195]\n\t" \
    "v_mul_f32 v212, v200, v207\n\t" \
    "v_mul_f32 v213, v201, v207\n\t" \
    "v_mfma_f32_16x16x32_bf16 v[192:195], v[140:143], v[164:167], v[192:195]\n\t" \
    "v_mul_f32 v214, v202, v207\n\t" \
    "v_mul_f32 v215, v203, v207\n\t" \
    "s_waitcnt lgkmcnt(3)\n\t" \
    "v_mfma_f32_16x16x32_bf16 v[192:195], v[144:147], v[168:171], v[192:195]\n\t" \
    "s_cmp_lg_u32 %[sw], 0\n\t" \
    "s_cbranch_scc1 .Lts%=\n\t" \
    "v_cvt_pk_bf16_f32 v217, v204, v204\n\t" \
    "v_lshlrev_b32 v217, 16, v217\n\t" \
    "v_add_f32 v217, %[ke], v217\n\t" \
    "v_cvt_pk_bf16_f32 v217, v217, v217\n\t" \
    "v_lshlrev_b32 v217, 16, v217\n\t" \
    "v_sqrt_f32 v217, v217\n\t" \
    "v_cvt_pk_bf16_f32 v216, v205, v205\n\t" \
    "v_lshlrev_b32 v216, 16, v216\n\t" \
    "v_cvt_pk_bf16_f32 v217, v217, v217\n\t" \
    "v_lshlrev_b32 v217, 16, v217\n\t" \
    "v_rcp_f32 v217, v217\n\t" \
    "v_lshrrev_b32 v218, 1, %[l16]\n\t" \
    "v_and_b32 v218, 0x78, v218\n\t" \
    "buffer_store_dwordx2 v[216:217], v218, %[rs], %[ss] offen sc1\n\t" \
    ".Lts%=:\n\t" \
    "v_mov_b64 v[216:217], -1\n\t" \
    "s_waitcnt lgkmcnt(2)\n\t" \
    "v_mfma_f32_16x16x32_bf16 v[192:195], v[148:151], v[172:175], v[192:195]\n\t" \
    "v_mov_b64 v[218:219], -1\n\t" \
    "buffer_store_dwordx4 v[216:219], %[ob], %[rs], %[sa] offen sc1\n\t" \
    "v_mfma_f32_16x16x32_bf16 v[188:191], v[64:67], v[168:171], v[188:191]\n\t" \
    "v_mfma_f32_16x16x32_bf16 v[188:191], v[68:71], v[172:175], v[188:191]\n\t" \
    "s_waitcnt lgkmcnt(1)\n\t" \
    "v_mfma_f32_16x16x32_bf16 v[188:191], v[72:75], v[176:179], v[188:191]\n\t" \
    "s_waitcnt lgkmcnt(0)\n\t" \
    "v_mfma_f32_16x16x32_bf16 v[188:191], v[76:79], v[180:183], v[188:191]\n\t" \
    "v_mfma_f32_16x16x32_bf16 v[192:195], v[152:155], v[176:179], v[192:195]\n\t" \
    "v_mfma_f32_16x16x32_bf16 v[192:195], v[156:159], v[180:183], v[192:195]\n\t" \
    "s_setprio 2\n\t" \
    "buffer_load_dwordx4 v[232:235], %[l16], %[rs], %[n0] offen offset:0 sc1\n\t" \
    "buffer_load_dwordx4 v[236:239], %[l16], %[rs], %[n0] offen offset:1024 sc1\n\t" \
    "buffer_load_dwordx4 v[240:243], %[l16], %[rs], %[n0] offen offset:2048 sc1\n\t" \
    "buffer_load_dwordx4 v[244:247], %[l16], %[rs], %[n0] offen offset:3072 sc1\n\t" \
    "buffer_load_dwordx4 v[248:251], %[l16], %[rs], %[n1] offen offset:0 sc1\n\t" \
    ASM_ST3 \
    "v_fma_f32 v188, v188, v206, v208\n\t" \
    "v_fma_f32 v189, v189, v206, v209\n\t" \
    "v_fma_f32 v190, v190, v206, v210\n\t" \
    "v_fma_f32 v191, v191, v206, v211\n\t" \
    "v_mul_f32 v160, v188, v188\n\t" \
    "v_mul_f32 v161, v189, v189\n\t" \
    "v_mul_f32 v162, v190, v190\n\t" \
    "v_mul_f32 v163, v191, v191\n\t" \
    "v_fma_f32 v160, v160, %[k1], v220\n\t" \
    "v_fma_f32 v161, v161, %[k1], v220\n\t" \
    "v_fma_f32 v162, v162, %[k1], v220\n\t" \
    "v_fma_f32 v163, v163, %[k1], v220\n\t" \
    "v_mul_f32 v160, v188, v160\n\t" \
    "v_mul_f32 v161, v189, v161\n\t" \
    "v_mul_f32 v162, v190, v162\n\t" \
    "v_mul_f32 v163, v191, v163\n\t" \
    "v_fma_f32 v192, v192, v206, v212\n\t" \
    "v_fma_f32 v193, v193, v206, v213\n\t" \
    "v_fma_f32 v194, v194, v206, v214\n\t" \
    "v_fma_f32 v195, v195, v206, v215\n\t" \
    "v_mul_f32 v164, v192, v192\n\t" \
    "v_mul_f32 v165, v193, v193\n\t" \
    "v_mul_f32 v166, v194, v194\n\t" \
    "v_mul_f32 v167, v195, v195\n\t" \
    "v_fma_f32 v164, v164, %[k1], v220\n\t" \
    "v_fma_f32 v165, v165, %[k1], v220\n\t" \
    "v_fma_f32 v166, v166, %[k1], v220\n\t" \
    "v_fma_f32 v167, v167, %[k1], v220\n\t" \
    "v_mul_f32 v164, v192, v164\n\t" \
    "v_mul_f32 v165, v193, v165\n\t" \
    "v_mul_f32 v166, v194, v166\n\t" \
    "v_mul_f32 v167, v195, v167\n\t" \
    "v_exp_f32 v160, v160\n\t" \
    "v_exp_f32 v161, v161\n\t" \
    "v_exp_f32 v162, v162\n\t" \
    "v_exp_f32 v163, v163\n\t" \
    "v_exp_f32 v164, v164\n\t" \
    "v_exp_f32 v165, v165\n\t" \
    "v_exp_f32 v166, v166\n\t" \
    "v_exp_f32 v167, v167\n\t" \
    "v_add_f32 v160, 1.0, v160\n\t" \
    "v_add_f32 v161, 1.0, v161\n\t" \
    "v_add_f32 v162, 1.0, v162\n\t" \
    "v_add_f32 v163, 1.0, v163\n\t" \
    "v_add_f32 v164, 1.0, v164\n\t" \
    "v_add_f32 v165, 1.0, v165\n\t" \
    "v_add_f32 v166, 1.0, v166\n\t" \
    "v_add_f32 v167, 1.0, v167\n\t" \
    "v_rcp_f32 v160, v160\n\t" \
    "v_rcp_f32 v161, v161\n\t" \
    "v_rcp_f32 v162, v162\n\t" \
    "v_rcp_f32 v163, v163\n\t" \
    "v_rcp_f32 v164, v164\n\t" \
    "v_rcp_f32 v165, v165\n\t" \
    "v_rcp_f32 v166, v166\n\t" \
    "v_rcp_f32 v167, v167\n\t" \
    "v_mul_f32 v188, v188, v160\n\t" \
    "v_mul_f32 v189, v189, v161\n\t" \
    "v_mul_f32 v190, v190, v162\n\t" \
    "v_mul_f32 v191, v191, v163\n\t" \
    "v_mul_f32 v192, v192, v164\n\t" \
    "v_mul_f32 v193, v193, v165\n\t" \
    "v_mul_f32 v194, v194, v166\n\t" \
    "v_mul_f32 v195, v195, v167\n\t" \
    "v_cvt_pk_bf16_f32 v188, v188, v189\n\t" \
    "v_cvt_pk_bf16_f32 v189, v190, v191\n\t" \
    "v_cvt_pk_bf16_f32 v190, v192, v193\n\t" \
    "v_cvt_pk_bf16_f32 v191, v194, v195\n\t" \
    "v_min_u32 v188, -2, v188\n\t" \
    "v_min_u32 v189, -2, v189\n\t" \
    "v_min_u32 v190, -2, v190\n\t" \
    "v_min_u32 v191, -2, v191\n\t" \
    "v_mov_b32 v216, 0x3f803f80\n\t" \
    "v_mov_b32 v217, 0x3f803f80\n\t" \
    "v_mov_b32 v218, 0x3f803f80\n\t" \
    "v_mov_b32 v219, 0x3f803f80\n\t" \
    "v_permlane16_swap_b32 v188, v190\n\t" \
    "v_permlane16_swap_b32 v189, v191\n\t" \
    "s_waitcnt vmcnt(6)\n\t" \
    "buffer_store_dwordx4 v[188:191], %[oa], %[rs], %[so] offen sc1\n\t" \
    "s_nop 1\n\t" \
    "s_setprio 0\n\t"
#define ASM_WT1_HI \
    "s_setprio 1\n\t" \
    "s_barrier\n\t" ASM_ST \
    "ds_read_b128 v[160:163], %[la] offset:0\n\t" \
    "ds_read_b128 v[164:167], %[la] offset:1024\n\t" \
    "ds_read_b128 v[168:171], %[la] offset:2048\n\t" \
    "ds_read2st64_b32 v[204:205], %[sr] offset0:20 offset1:24\n\t" \
    "ds_read_b128 v[172:175], %[la] offset:3072\n\t" \
    "ds_read_b128 v[176:179], %[la] offset:8192\n\t" \
    "ds_read_b128 v[180:183], %[la] offset:9216\n\t" \
    "ds_read_b128 v[184:187], %[la] offset:10240\n\t" \
    "s_waitcnt lgkmcnt(7)\n\t" \
    "v_mfma_f32_16x16x32_bf16 v[192:195], v[80:83], v[160:163], 0\n\t" \
    "s_waitcnt lgkmcnt(6)\n\t" \
    "v_mfma_f32_16x16x32_bf16 v[192:195], v[84:87], v[164:167], v[192:195]\n\t" \
    "v_mfma_f32_16x16x32_bf16 v[188:191], v[0:3], v[160:163], 0\n\t" \
    "ds_read_b128 v[160:163], %[la] offset:11264\n\t" \
    "v_mfma_f32_16x16x32_bf16 v[188:191], v[4:7], v[164:167], v[188:191]\n\t" \
    "ds_read_b128 v[164:167], %[la] offset:16384\n\t" \
    "s_waitcnt lgkmcnt(7)\n\t" \
    "v_mfma_f32_16x16x32_bf16 v[188:191], v[8:11], v[168:171], v[188:191]\n\t" \
    "s_waitcnt lgkmcnt(6)\n\t" \
    "s_waitcnt lgkmcnt(5)\n\t" \
    "v_mfma_f32_16x16x32_bf16 v[188:191], v[12:15], v[172:175], v[188:191]\n\t" \
    "v_permlane16_swap_b32 v204, v205\n\t" \
    "v_mfma_f32_16x16x32_bf16 v[192:195], v[88:91], v[168:171], v[192:195]\n\t" \
    "ds_read_b128 v[168:171], %[la] offset:17408\n\t" \
    "v_mfma_f32_16x16x32_bf16 v[192:195], v[92:95], v[172:175], v[192:195]\n\t" \
    "ds_read_b128 v[172:175], %[la] offset:18432\n\t" \
    "v_add_f32 v206, v204, v205\n\t" \
    "s_waitcnt lgkmcnt(6)\n\t" \
    "v_mfma_f32_16x16x32_bf16 v[192:195], v[96:99], v[176:179], v[192:195]\n\t" \
    "v_add_f32 v207, v204, v205\n\t" \
    "s_waitcnt lgkmcnt(5)\n\t" \
    "v_mfma_f32_16x16x32_bf16 v[192:195], v[100:103], v[180:183], v[192:195]\n\t" \
    "v_mfma_f32_16x16x32_bf16 v[188:191], v[16:19], v[176:179], v[188:191]\n\t" \
    "ds_read_b128 v[176:179], %[la] offset:19456\n\t" \
    "v_permlane32_swap_b32 v206, v207\n\t" \
    "v_mfma_f32_16x16x32_bf16 v[188:191], v[20:23], v[180:183], v[188:191]\n\t" \
    "ds_read_b128 v[180:183], %[la] offset:24576\n\t" \
    "s_waitcnt lgkmcnt(6)\n\t" \
    "v_mfma_f32_16x16x32_bf16 v[188:191], v[24:27], v[184:187], v[188:191]\n\t" \
    "v_add_f32 v204, v206, v207\n\t" \
    "s_waitcnt lgkmcnt(5)\n\t" \
    "v_mfma_f32_16x16x32_bf16 v[188:191], v[28:31], v[160:163], v[188:191]\n\t" \
    "v_add_f32 v205, v206, v207\n\t" \
    "v_mfma_f32_16x16x32_bf16 v[192:195], v[104:107], v[184:187], v[192:195]\n\t" \
    "ds_read_b128 v[184:187], %[la] offset:25600\n\t" \
    "v_mfma_f32_16x16x32_bf16 v[192:195], v[108:111], v[160:163], v[192:195]\n\t" \
    "ds_read_b128 v[160:163], %[la] offset:26624\n\t" \
    "v_permlane16_swap_b32 v204, v205\n\t" \
    "s_waitcnt lgkmcnt(6)\n\t" \
    "v_mfma_f32_16x16x32_bf16 v[192:195], v[112:115], v[164:167], v[192:195]\n\t" \
    "s_waitcnt lgkmcnt(5)\n\t" \
    "v_mfma_f32_16x16x32_bf16 v[192:195], v[116:119], v[168:171], v[192:195]\n\t" \
    "v_mul_f32 v205, %[kd], v205\n\t" \
    "v_mfma_f32_16x16x32_bf16 v[188:191], v[32:35], v[164:167], v[188:191]\n\t" \
    "ds_read_b128 v[164:167], %[la] offset:27648\n\t" \
    "v_mul_f32 v204, %[kd], v204\n\t" \
    "v_mfma_f32_16x16x32_bf16 v[188:191], v[36:39], v[168:171], v[188:191]\n\t" \
    "ds_read_b128 v[168:171], %[la] offset:4096\n\t" \
    "v_fma_f32 v204, -v205, v205, v204\n\t" \
    "s_waitcnt lgkmcnt(6)\n\t" \
    "v_mfma_f32_16x16x32_bf16 v[188:191], v[40:43], v[172:175], v[188:191]\n\t" \
    "v_max_f32 v204, 0, v204\n\t" \
    "s_waitcnt lgkmcnt(5)\n\t" \
    "v_mfma_f32_16x16x32_bf16 v[188:191], v[44:47], v[176:179], v[188:191]\n\t" \
    "v_add_f32 v206, %[ke], v204\n\t" \
    "v_mfma_f32_16x16x32_bf16 v[192:195], v[120:123], v[172:175], v[192:195]\n\t" \
    "ds_read_b128 v[172:175], %[la] offset:12288\n\t" \
    "v_rsq_f32 v206, v206\n\t" \
    "v_mfma_f32_16x16x32_bf16 v[192:195], v[124:127], v[176:179], v[192:195]\n\t" \
    "ds_read_b128 v[176:179], %[la] offset:20480\n\t" \
    "s_waitcnt lgkmcnt(6)\n\t" \
    "v_mfma_f32_16x16x32_bf16 v[192:195], v[128:131], v[180:183], v[192:195]\n\t" \
    "v_mul_f32 v207, v204, v206\n\t" \
    "s_waitcnt lgkmcnt(5)\n\t" \
    "v_mfma_f32_16x16x32_bf16 v[192:195], v[132:135], v[184:187], v[192:195]\n\t" \
    "v_fma_f32 v207, v207, v206, %[ke]\n\t" \
    "v_mfma_f32_16x16x32_bf16 v[188:191], v[48:51], v[180:183], v[188:191]\n\t" \
    "ds_read_b128 v[180:183], %[la] offset:28672\n\t" \
    "v_rsq_f32 v207, v207\n\t" \
    "v_mfma_f32_16x16x32_bf16 v[188:191], v[52:55], v[184:187], v[188:191]\n\t" \
    "v_mul_f32 v206, v206, v207\n\t" \
    "v_mul_f32 v207, -v205, v206\n\t" \
    "s_waitcnt lgkmcnt(5)\n\t" \
    "v_mfma_f32_16x16x32_bf16 v[188:191], v[56:59], v[160:163], v[188:191]\n\t" \
    "v_mul_f32 v208, v196, v207\n\t" \
    "v_mul_f32 v209, v197, v207\n\t" \
    "s_waitcnt lgkmcnt(4)\n\t" \
    "v_mfma_f32_16x16x32_bf16 v[188:191], v[60:63], v[164:167], v[188:191]\n\t" \
    "v_mul_f32 v210, v198, v207\n\t" \
    "v_mul_f32 v211, v199, v207\n\t" \
    "v_mfma_f32_16x16x32_bf16 v[192:195], v[136:139], v[160:163], v[192:195]\n\t" \
    "v_mul_f32 v212, v200, v207\n\t" \
    "v_mul_f32 v213, v201, v207\n\t" \
    "v_mfma_f32_16x16x32_bf16 v[192:195], v[140:143], v[164:167], v[192:195]\n\t" \
    "v_mul_f32 v214, v202, v207\n\t" \
    "v_mul_f32 v215, v203, v207\n\t" \
    "s_waitcnt lgkmcnt(3)\n\t" \
    "v_mfma_f32_16x16x32_bf16 v[192:195], v[144:147], v[168:171], v[192:195]\n\t" \
    "v_mov_b64 v[216:217], -1\n\t" \
    "v_mov_b64 v[218:219], -1\n\t" \
    "s_waitcnt lgkmcnt(2)\n\t" \
    "v_mfma_f32_16x16x32_bf16 v[192:195], v[148:151], v[172:175], v[192:195]\n\t" \
    "buffer_store_dwordx4 v[216:219], %[ob], %[rs], %[sa] offen sc1\n\t" \
    "v_mfma_f32_16x16x32_bf16 v[188:191], v[64:67], v[168:171], v[188:191]\n\t" \
    "v_mfma_f32_16x16x32_bf16 v[188:191], v[68:71], v[172:175], v[188:191]\n\t" \
    "s_waitcnt lgkmcnt(1)\n\t" \
    "v_mfma_f32_16x16x32_bf16 v[188:191], v[72:75], v[176:179], v[188:191]\n\t" \
    "s_waitcnt lgkmcnt(0)\n\t" \
    "v_mfma_f32_16x16x32_bf16 v[188:191], v[76:79], v[180:183], v[188:191]\n\t" \
    "v_mfma_f32_16x16x32_bf16 v[192:195], v[152:155], v[176:179], v[192:195]\n\t" \
    "v_mfma_f32_16x16x32_bf16 v[192:195], v[156:159], v[180:183], v[192:195]\n\t" \
    ASM_ST3 \
    "s_nop 1\n\t" \
    "v_fma_f32 v188, v188, v206, v208\n\t" \
    "v_fma_f32 v189, v189, v206, v209\n\t" \
    "v_fma_f32 v190, v190, v206, v210\n\t" \
    "v_fma_f32 v191, v191, v206, v211\n\t" \
    "v_mul_f32 v160, v188, v188\n\t" \
    "v_mul_f32 v161, v189, v189\n\t" \
    "v_mul_f32 v162, v190, v190\n\t" \
    "v_mul_f32 v163, v191, v191\n\t" \
    "v_fma_f32 v160, v160, %[k1], v220\n\t" \
    "v_fma_f32 v161, v161, %[k1], v220\n\t" \
    "v_fma_f32 v162, v162, %[k1], v220\n\t" \
    "v_fma_f32 v163, v163, %[k1], v220\n\t" \
    "v_mul_f32 v160, v188, v160\n\t" \
    "v_mul_f32 v161, v189, v161\n\t" \
    "v_mul_f32 v162, v190, v162\n\t" \
    "v_mul_f32 v163, v191, v163\n\t" \
    "v_fma_f32 v192, v192, v206, v212\n\t" \
    "v_fma_f32 v193, v193, v206, v213\n\t" \
    "v_fma_f32 v194, v194, v206, v214\n\t" \
    "v_fma_f32 v195, v195, v206, v215\n\t" \
    "v_mul_f32 v164, v192, v192\n\t" \
    "v_mul_f32 v165, v193, v193\n\t" \
    "v_mul_f32 v166, v194, v194\n\t" \
    "v_mul_f32 v167, v195, v195\n\t" \
    "v_fma_f32 v164, v164, %[k1], v220\n\t" \
    "v_fma_f32 v165, v165, %[k1], v220\n\t" \
    "v_fma_f32 v166, v166, %[k1], v220\n\t" \
    "v_fma_f32 v167, v167, %[k1], v220\n\t" \
    "v_mul_f32 v164, v192, v164\n\t" \
    "v_mul_f32 v165, v193, v165\n\t" \
    "v_mul_f32 v166, v194, v166\n\t" \
    "v_mul_f32 v167, v195, v167\n\t" \
    "v_exp_f32 v160, v160\n\t" \
    "v_exp_f32 v161, v161\n\t" \
    "v_exp_f32 v162, v162\n\t" \
    "v_exp_f32 v163, v163\n\t" \
    "v_exp_f32 v164, v164\n\t" \
    "v_exp_f32 v165, v165\n\t" \
    "v_exp_f32 v166, v166\n\t" \
    "v_exp_f32 v167, v167\n\t" \
    "v_add_f32 v160, 1.0, v160\n\t" \
    "v_add_f32 v161, 1.0, v161\n\t" \
    "v_add_f32 v162, 1.0, v162\n\t" \
    "v_add_f32 v163, 1.0, v163\n\t" \
    "v_add_f32 v164, 1.0, v164\n\t" \
    "v_add_f32 v165, 1.0, v165\n\t" \
    "v_add_f32 v166, 1.0, v166\n\t" \
    "v_add_f32 v167, 1.0, v167\n\t" \
    "v_rcp_f32 v160, v160\n\t" \
    "v_rcp_f32 v161, v161\n\t" \
    "v_rcp_f32 v162, v162\n\t" \
    "v_rcp_f32 v163, v163\n\t" \
    "v_rcp_f32 v164, v164\n\t" \
    "v_rcp_f32 v165, v165\n\t" \
    "v_rcp_f32 v166, v166\n\t" \
    "v_rcp_f32 v167, v167\n\t" \
    "v_mul_f32 v188, v188, v160\n\t" \
    "v_mul_f32 v189, v189, v161\n\t" \
    "v_mul_f32 v190, v190, v162\n\t" \
    "v_mul_f32 v191, v191, v163\n\t" \
    "v_mul_f32 v192, v192, v164\n\t" \
    "v_mul_f32 v193, v193, v165\n\t" \
    "v_mul_f32 v194, v194, v166\n\t" \
    "v_mul_f32 v195, v195, v167\n\t" \
    "v_cvt_pk_bf16_f32 v188, v188, v189\n\t" \
    "v_cvt_pk_bf16_f32 v189, v190, v191\n\t" \
    "v_cvt_pk_bf16_f32 v190, v192, v193\n\t" \
    "v_cvt_pk_bf16_f32 v191, v194, v195\n\t" \
    "v_min_u32 v188, -2, v188\n\t" \
    "v_min_u32 v189, -2, v189\n\t" \
    "v_min_u32 v190, -2, v190\n\t" \
    "v_min_u32 v191, -2, v191\n\t" \
    "s_nop 1\n\t" \
    "v_permlane16_swap_b32 v188, v190\n\t" \
    "v_permlane16_swap_b32 v189, v191\n\t" \
    "s_waitcnt vmcnt(1)\n\t" \
    "buffer_store_dwordx4 v[188:191], %[oa], %[rs], %[so] offen sc1\n\t" \
    "s_nop 1\n\t"
#define ASM_WT2_LO \
    "s_branch .Lt0%=\n\t" \
    ".Ltr%=:\n\t" \
    "s_cmp_gt_i32 %[n], 0\n\t" \
    "s_cbranch_scc0 .Ltg%=\n\t" \
    ".Lfw%=:\n\t" \
    "s_mov_b32 %[a], 0\n\t" \
    "v_max_u32 v252, v232, v233\n\t" \
    "v_max3_u32 v252, v234, v235, v252\n\t" \
    "v_cmp_eq_u32 vcc, -1, v252\n\t" \
    "s_cbranch_vccz .Lgw0_%=\n\t" \
    "buffer_load_dwordx4 v[232:235], %[l16], %[rs], %[i0] offen offset:0 sc1\n\t" \
    "s_mov_b32 %[a], 1\n\t" \
    ".Lgw0_%=:\n\t" \
    "v_max_u32 v252, v236, v237\n\t" \
    "v_max3_u32 v252, v238, v239, v252\n\t" \
    "v_cmp_eq_u32 vcc, -1, v252\n\t" \
    "s_cbranch_vccz .Lgw1_%=\n\t" \
    "buffer_load_dwordx4 v[236:239], %[l16], %[rs], %[i0] offen offset:1024 sc1\n\t" \
    "s_mov_b32 %[a], 1\n\t" \
    ".Lgw1_%=:\n\t" \
    "v_max_u32 v252, v240, v241\n\t" \
    "v_max3_u32 v252, v242, v243, v252\n\t" \
    "v_cmp_eq_u32 vcc, -1, v252\n\t" \
    "s_cbranch_vccz .Lgw2_%=\n\t" \
    "buffer_load_dwordx4 v[240:243], %[l16], %[rs], %[i0] offen offset:2048 sc1\n\t" \
    "s_mov_b32 %[a], 1\n\t" \
    ".Lgw2_%=:\n\t" \
    "v_max_u32 v252, v244, v245\n\t" \
    "v_max3_u32 v252, v246, v247, v252\n\t" \
    "v_cmp_eq_u32 vcc, -1, v252\n\t" \
    "s_cbranch_vccz .Lgw3_%=\n\t" \
    "buffer_load_dwordx4 v[244:247], %[l16], %[rs], %[i0] offen offset:3072 sc1\n\t" \
    "s_mov_b32 %[a], 1\n\t" \
    ".Lgw3_%=:\n\t" \
    "v_max_u32 v252, v248, v249\n\t" \
    "v_max3_u32 v252, v250, v251, v252\n\t" \
    "v_cmp_eq_u32 vcc, -1, v252\n\t" \
    "s_cbranch_vccz .Lgw4_%=\n\t" \
    "buffer_load_dwordx4 v[248:251], %[l16], %[rs], %[i1] offen offset:0 sc1\n\t" \
    "s_mov_b32 %[a], 1\n\t" \
    ".Lgw4_%=:\n\t" \
    "s_cmp_eq_u32 %[a], 0\n\t" \
    "s_cbranch_scc1 .Lhw%=\n\t" \
    "s_waitcnt vmcnt(20)\n\t" \
    ".Liw%=:\n\t" \
    "v_max_u32 v252, v232, v233\n\t" \
    "v_max3_u32 v252, v234, v235, v252\n\t" \
    "v_max3_u32 v252, v236, v237, v252\n\t" \
    "v_max3_u32 v252, v238, v239, v252\n\t" \
    "v_max3_u32 v252, v240, v241, v252\n\t" \
    "v_max3_u32 v252, v242, v243, v252\n\t" \
    "v_max3_u32 v252, v244, v245, v252\n\t" \
    "v_max3_u32 v252, v246, v247, v252\n\t" \
    "v_max3_u32 v252, v248, v249, v252\n\t" \
    "v_max3_u32 v252, v250, v251, v252\n\t" \
    "v_cmp_eq_u32 vcc, -1, v252\n\t" \
    "s_cbranch_vccz .Lhw%=\n\t" \
    "s_sleep 1\n\t" \
    "v_max_u32 v252, v232, v233\n\t" \
    "v_max3_u32 v252, v234, v235, v252\n\t" \
    "v_max3_u32 v252, v236, v237, v252\n\t" \
    "v_max3_u32 v252, v238, v239, v252\n\t" \
    "v_max3_u32 v252, v240, v241, v252\n\t" \
    "v_max3_u32 v252, v242, v243, v252\n\t" \
    "v_max3_u32 v252, v244, v245, v252\n\t" \
    "v_max3_u32 v252, v246, v247, v252\n\t" \
    "v_max3_u32 v252, v248, v249, v252\n\t" \
    "v_max3_u32 v252, v250, v251, v252\n\t" \
    "v_cmp_eq_u32 vcc, -1, v252\n\t" \
    "s_cbranch_vccz .Lhw%=\n\t" \
    "s_sleep 1\n\t" \
    "v_max_u32 v252, v232, v233\n\t" \
    "v_max3_u32 v252, v234, v235, v252\n\t" \
    "v_max3_u32 v252, v236, v237, v252\n\t" \
    "v_max3_u32 v252, v238, v239, v252\n\t" \
    "v_max3_u32 v252, v240, v241, v252\n\t" \
    "v_max3_u32 v252, v242, v243, v252\n\t" \
    "v_max3_u32 v252, v244, v245, v252\n\t" \
    "v_max3_u32 v252, v246, v247, v252\n\t" \
    "v_max3_u32 v252, v248, v249, v252\n\t" \
    "v_max3_u32 v252, v250, v251, v252\n\t" \
    "v_cmp_eq_u32 vcc, -1, v252\n\t" \
    "s_cbranch_vccz .Lhw%=\n\t" \
    "s_sleep 1\n\t" \
    "v_max_u32 v252, v232, v233\n\t" \
    "v_max3_u32 v252, v234, v235, v252\n\t" \
    "v_max3_u32 v252, v236, v237, v252\n\t" \
    "v_max3_u32 v252, v238, v239, v252\n\t" \
    "v_max3_u32 v252, v240, v241, v252\n\t" \
    "v_max3_u32 v252, v242, v243, v252\n\t" \
    "v_max3_u32 v252, v244, v245, v252\n\t" \
    "v_max3_u32 v252, v246, v247, v252\n\t" \
    "v_max3_u32 v252, v248, v249, v252\n\t" \
    "v_max3_u32 v252, v250, v251, v252\n\t" \
    "v_cmp_eq_u32 vcc, -1, v252\n\t" \
    "s_cbranch_vccz .Lhw%=\n\t" \
    "s_sleep 1\n\t" \
    "v_max_u32 v252, v232, v233\n\t" \
    "v_max3_u32 v252, v234, v235, v252\n\t" \
    "v_max3_u32 v252, v236, v237, v252\n\t" \
    "v_max3_u32 v252, v238, v239, v252\n\t" \
    "v_max3_u32 v252, v240, v241, v252\n\t" \
    "v_max3_u32 v252, v242, v243, v252\n\t" \
    "v_max3_u32 v252, v244, v245, v252\n\t" \
    "v_max3_u32 v252, v246, v247, v252\n\t" \
    "v_max3_u32 v252, v248, v249, v252\n\t" \
    "v_max3_u32 v252, v250, v251, v252\n\t" \
    "v_cmp_eq_u32 vcc, -1, v252\n\t" \
    "s_cbranch_vccz .Lhw%=\n\t" \
    "s_sleep 1\n\t" \
    "v_max_u32 v252, v232, v233\n\t" \
    "v_max3_u32 v252, v234, v235, v252\n\t" \
    "v_max3_u32 v252, v236, v237, v252\n\t" \
    "v_max3_u32 v252, v238, v239, v252\n\t" \
    "v_max3_u32 v252, v240, v241, v252\n\t" \
    "v_max3_u32 v252, v242, v243, v252\n\t" \
    "v_max3_u32 v252, v244, v245, v252\n\t" \
    "v_max3_u32 v252, v246, v247, v252\n\t" \
    "v_max3_u32 v252, v248, v249, v252\n\t" \
    "v_max3_u32 v252, v250, v251, v252\n\t" \
    "v_cmp_eq_u32 vcc, -1, v252\n\t" \
    "s_cbranch_vccz .Lhw%=\n\t" \
    "s_sleep 1\n\t" \
    "s_sub_u32 %[n], %[n], 1\n\t" \
    "s_cmp_gt_i32 %[n], 0\n\t" \
    "s_cbranch_scc1 .Lfw%=\n\t" \
    ".Lhw%=:\n\t" \
    "s_branch .Lta%=\n\t" \
    ".Lt0%=:\n\t" \
    "s_waitcnt vmcnt(1)\n\t" \
    ".Lta%=:\n\t" \
    "ds_write_b128 %[lw], v[232:235] offset:0\n\t" \
    "v_max_u32 v252, v232, v233\n\t" \
    "v_max3_u32 v252, v234, v235, v252\n\t" \
    "ds_write_b128 %[lw], v[236:239] offset:1024\n\t" \
    "v_max3_u32 v252, v236, v237, v252\n\t" \
    "v_max3_u32 v252, v238, v239, v252\n\t" \
    "ds_write_b128 %[lw], v[240:243] offset:2048\n\t" \
    "v_max3_u32 v252, v240, v241, v252\n\t" \
    "v_max3_u32 v252, v242, v243, v252\n\t" \
    "ds_write_b128 %[lw], v[244:247] offset:3072\n\t" \
    "v_max3_u32 v252, v244, v245, v252\n\t" \
    "v_max3_u32 v252, v246, v247, v252\n\t" \
    "ds_write_b128 %[lw], v[248:251] offset:4096\n\t" \
    "v_max3_u32 v252, v248, v249, v252\n\t" \
    "v_max3_u32 v252, v250, v251, v252\n\t" \
    "v_cmp_eq_u32 vcc, -1, v252\n\t" \
    "s_cbranch_vccnz .Ltr%=\n\t" \
    ".Ltg%=:\n\t" \
    ASM_ST2 \
    "s_waitcnt lgkmcnt(0)\n\t" \
    "s_setprio 2\n\t" \
    "s_barrier\n\t" ASM_ST \
    "ds_read_b128 v[160:163], %[la] offset:0\n\t" \
    "ds_read_b128 v[164:167], %[la] offset:1024\n\t" \
    "ds_read_b128 v[168:171], %[la] offset:2048\n\t" \
    "ds_read_b128 v[172:175], %[la] offset:3072\n\t" \
    "ds_read_b128 v[176:179], %[la] offset:8192\n\t" \
    "ds_read_b128 v[180:183], %[la] offset:9216\n\t" \
    "ds_read_b128 v[184:187], %[la] offset:10240\n\t" \
    "s_waitcnt lgkmcnt(6)\n\t" \
    "v_mfma_f32_16x16x32_bf16 v[192:195], v[80:83], v[160:163], 0\n\t" \
    "buffer_load_dwordx2 v[196:197], %[ro], %[rs], %[sx] offen sc1\n\t" \
    "s_waitcnt lgkmcnt(5)\n\t" \
    "v_mfma_f32_16x16x32_bf16 v[192:195], v[84:87], v[164:167], v[192:195]\n\t" \
    "buffer_load_dwordx2 v[198:199], %[ro], %[rs], %[sx] offen offset:512 sc1\n\t" \
    "v_mfma_f32_16x16x32_bf16 v[188:191], v[0:3], v[160:163], 0\n\t" \
    "ds_read_b128 v[160:163], %[la] offset:11264\n\t" \
    "v_lshrrev_b32 v204, 1, %[l16]\n\t" \
    "v_mfma_f32_16x16x32_bf16 v[188:191], v[4:7], v[164:167], v[188:191]\n\t" \
    "ds_read_b128 v[164:167], %[la] offset:16384\n\t" \
    "v_and_b32 v204, 0x78, v204\n\t" \
    "s_waitcnt lgkmcnt(6)\n\t" \
    "v_mfma_f32_16x16x32_bf16 v[188:191], v[8:11], v[168:171], v[188:191]\n\t" \
    "buffer_load_dwordx2 v[200:201], v204, %[rs], %[ss] offen sc1\n\t" \
    "s_waitcnt lgkmcnt(5)\n\t" \
    "v_mfma_f32_16x16x32_bf16 v[188:191], v[12:15], v[172:175], v[188:191]\n\t" \
    "v_mov_b64 v[216:217], -1\n\t" \
    "v_mfma_f32_16x16x32_bf16 v[192:195], v[88:91], v[168:171], v[192:195]\n\t" \
    "ds_read_b128 v[168:171], %[la] offset:17408\n\t" \
    "v_mov_b64 v[218:219], -1\n\t" \
    "v_mfma_f32_16x16x32_bf16 v[192:195], v[92:95], v[172:175], v[192:195]\n\t" \
    "ds_read_b128 v[172:175], %[la] offset:18432\n\t" \
    "buffer_store_dwordx4 v[216:219], %[l16], %[rs], %[sa] offen sc1\n\t" \
    "s_waitcnt lgkmcnt(6)\n\t" \
    "v_mfma_f32_16x16x32_bf16 v[192:195], v[96:99], v[176:179], v[192:195]\n\t" \
    "s_waitcnt lgkmcnt(5)\n\t" \
    "v_mfma_f32_16x16x32_bf16 v[192:195], v[100:103], v[180:183], v[192:195]\n\t" \
    "v_mfma_f32_16x16x32_bf16 v[188:191], v[16:19], v[176:179], v[188:191]\n\t" \
    "ds_read_b128 v[176:179], %[la] offset:19456\n\t" \
    "v_mfma_f32_16x16x32_bf16 v[188:191], v[20:23], v[180:183], v[188:191]\n\t" \
    "ds_read_b128 v[180:183], %[la] offset:24576\n\t" \
    "s_waitcnt lgkmcnt(6)\n\t" \
    "v_mfma_f32_16x16x32_bf16 v[188:191], v[24:27], v[184:187], v[188:191]\n\t" \
    "s_waitcnt lgkmcnt(5)\n\t" \
    "v_mfma_f32_16x16x32_bf16 v[188:191], v[28:31], v[160:163], v[188:191]\n\t" \
    "v_mfma_f32_16x16x32_bf16 v[192:195], v[104:107], v[184:187], v[192:195]\n\t" \
    "ds_read_b128 v[184:187], %[la] offset:25600\n\t" \
    "v_mfma_f32_16x16x32_bf16 v[192:195], v[108:111], v[160:163], v[192:195]\n\t" \
    "ds_read_b128 v[160:163], %[la] offset:26624\n\t" \
    "s_waitcnt lgkmcnt(6)\n\t" \
    "v_mfma_f32_16x16x32_bf16 v[192:195], v[112:115], v[164:167], v[192:195]\n\t" \
    "s_waitcnt lgkmcnt(5)\n\t" \
    "v_mfma_f32_16x16x32_bf16 v[192:195], v[116:119], v[168:171], v[192:195]\n\t" \
    "v_mfma_f32_16x16x32_bf16 v[188:191], v[32:35], v[164:167], v[188:191]\n\t" \
    "ds_read_b128 v[164:167], %[la] offset:27648\n\t" \
    "v_mfma_f32_16x16x32_bf16 v[188:191], v[36:39], v[168:171], v[188:191]\n\t" \
    "ds_read_b128 v[168:171], %[la] offset:4096\n\t" \
    "s_waitcnt lgkmcnt(6)\n\t" \
    "v_mfma_f32_16x16x32_bf16 v[188:191], v[40:43], v[172:175], v[188:191]\n\t" \
    "s_waitcnt lgkmcnt(5)\n\t" \
    "v_mfma_f32_16x16x32_bf16 v[188:191], v[44:47], v[176:179], v[188:191]\n\t" \
    "v_mfma_f32_16x16x32_bf16 v[192:195], v[120:123], v[172:175], v[192:195]\n\t" \
    "ds_read_b128 v[172:175], %[la] offset:12288\n\t" \
    "v_mfma_f32_16x16x32_bf16 v[192:195], v[124:127], v[176:179], v[192:195]\n\t" \
    "ds_read_b128 v[176:179], %[la] offset:20480\n\t" \
    "s_waitcnt lgkmcnt(6)\n\t" \
    "v_mfma_f32_16x16x32_bf16 v[192:195], v[128:131], v[180:183], v[192:195]\n\t" \
    "s_waitcnt lgkmcnt(5)\n\t" \
    "v_mfma_f32_16x16x32_bf16 v[192:195], v[132:135], v[184:187], v[192:195]\n\t" \
    "v_mfma_f32_16x16x32_bf16 v[188:191], v[48:51], v[180:183], v[188:191]\n\t" \
    "ds_read_b128 v[180:183], %[la] offset:28672\n\t" \
    "v_mfma_f32_16x16x32_bf16 v[188:191], v[52:55], v[184:187], v[188:191]\n\t" \
    "s_waitcnt lgkmcnt(5)\n\t" \
    "v_mfma_f32_16x16x32_bf16 v[188:191], v[56:59], v[160:163], v[188:191]\n\t" \
    "s_waitcnt lgkmcnt(4)\n\t" \
    "v_mfma_f32_16x16x32_bf16 v[188:191], v[60:63], v[164:167], v[188:191]\n\t" \
    "v_mfma_f32_16x16x32_bf16 v[192:195], v[136:139], v[160:163], v[192:195]\n\t" \
    "v_mfma_f32_16x16x32_bf16 v[192:195], v[140:143], v[164:167], v[192:195]\n\t" \
    "s_waitcnt lgkmcnt(3)\n\t" \
    "v_mfma_f32_16x16x32_bf16 v[192:195], v[144:147], v[168:171], v[192:195]\n\t" \
    "s_waitcnt lgkmcnt(2)\n\t" \
    "v_mfma_f32_16x16x32_bf16 v[192:195], v[148:151], v[172:175], v[192:195]\n\t" \
    "v_mfma_f32_16x16x32_bf16 v[188:191], v[64:67], v[168:171], v[188:191]\n\t" \
    "v_mfma_f32_16x16x32_bf16 v[188:191], v[68:71], v[172:175], v[188:191]\n\t" \
    "s_waitcnt lgkmcnt(1)\n\t" \
    "v_mfma_f32_16x16x32_bf16 v[188:191], v[72:75], v[176:179], v[188:191]\n\t" \
    "s_waitcnt lgkmcnt(0)\n\t" \
    "v_mfma_f32_16x16x32_bf16 v[188:191], v[76:79], v[180:183], v[188:191]\n\t" \
    "v_mfma_f32_16x16x32_bf16 v[192:195], v[152:155], v[176:179], v[192:195]\n\t" \
    "v_mfma_f32_16x16x32_bf16 v[192:195], v[156:159], v[180:183], v[192:195]\n\t" \
    "s_setprio 2\n\t" \
    "buffer_load_dwordx4 v[232:235], %[l16], %[rs], %[n0] offen offset:0 sc1\n\t" \
    "buffer_load_dwordx4 v[236:239], %[l16], %[rs], %[n0] offen offset:1024 sc1\n\t" \
    "buffer_load_dwordx4 v[240:243], %[l16], %[rs], %[n0] offen offset:2048 sc1\n\t" \
    "buffer_load_dwordx4 v[244:247], %[l16], %[rs], %[n0] offen offset:3072 sc1\n\t" \
    "buffer_load_dwordx4 v[248:251], %[l16], %[rs], %[n1] offen offset:0 sc1\n\t" \
    ASM_ST3 \
    "s_waitcnt vmcnt(6)\n\t" \
    "v_lshlrev_b32 v160, 16, v196\n\t" \
    "v_and_b32 v161, 0xffff0000, v196\n\t" \
    "v_lshlrev_b32 v162, 16, v197\n\t" \
    "v_and_b32 v163, 0xffff0000, v197\n\t" \
    "v_lshlrev_b32 v164, 16, v198\n\t" \
    "v_and_b32 v165, 0xffff0000, v198\n\t" \
    "v_lshlrev_b32 v166, 16, v199\n\t" \
    "v_and_b32 v167, 0xffff0000, v199\n\t" \
    "v_sub_f32 v160, v160, v200\n\t" \
    "v_sub_f32 v161, v161, v200\n\t" \
    "v_sub_f32 v162, v162, v200\n\t" \
    "v_sub_f32 v163, v163, v200\n\t" \
    "v_sub_f32 v164, v164, v200\n\t" \
    "v_sub_f32 v165, v165, v200\n\t" \
    "v_sub_f32 v166, v166, v200\n\t" \
    "v_sub_f32 v167, v167, v200\n\t" \
    "v_mul_f32 v160, v160, v201\n\t" \
    "v_mul_f32 v161, v161, v201\n\t" \
    "v_mul_f32 v162, v162, v201\n\t" \
    "v_mul_f32 v163, v163, v201\n\t" \
    "v_mul_f32 v164, v164, v201\n\t" \
    "v_mul_f32 v165, v165, v201\n\t" \
    "v_mul_f32 v166, v166, v201\n\t" \
    "v_mul_f32 v167, v167, v201\n\t" \
    "v_cvt_pk_bf16_f32 v160, v160, v161\n\t" \
    "v_cvt_pk_bf16_f32 v162, v162, v163\n\t" \
    "v_cvt_pk_bf16_f32 v164, v164, v165\n\t" \
    "v_cvt_pk_bf16_f32 v166, v166, v167\n\t" \
    "v_and_b32 v161, 0xffff0000, v160\n\t" \
    "v_lshlrev_b32 v160, 16, v160\n\t" \
    "v_and_b32 v163, 0xffff0000, v162\n\t" \
    "v_lshlrev_b32 v162, 16, v162\n\t" \
    "v_and_b32 v165, 0xffff0000, v164\n\t" \
    "v_lshlrev_b32 v164, 16, v164\n\t" \
    "v_and_b32 v167, 0xffff0000, v166\n\t" \
    "v_lshlrev_b32 v166, 16, v166\n\t" \
    "v_add_f32 v188, v188, v160\n\t" \
    "v_add_f32 v189, v189, v161\n\t" \
    "v_add_f32 v190, v190, v162\n\t" \
    "v_add_f32 v191, v191, v163\n\t" \
    "v_add_f32 v192, v192, v164\n\t" \
    "v_add_f32 v193, v193, v165\n\t" \
    "v_add_f32 v194, v194, v166\n\t" \
    "v_add_f32 v195, v195, v167\n\t" \
    "v_cvt_pk_bf16_f32 v188, v188, v189\n\t" \
    "v_cvt_pk_bf16_f32 v189, v190, v191\n\t" \
    "v_cvt_pk_bf16_f32 v190, v192, v193\n\t" \
    "v_cvt_pk_bf16_f32 v191, v194, v195\n\t" \
    "v_min_u32 v188, -2, v188\n\t" \
    "v_min_u32 v189, -2, v189\n\t" \
    "v_min_u32 v190, -2, v190\n\t" \
    "v_min_u32 v191, -2, v191\n\t" \
    "s_nop 1\n\t" \
    "v_permlane16_swap_b32 v188, v190\n\t" \
    "v_permlane16_swap_b32 v189, v191\n\t" \
    "s_waitcnt vmcnt(6)\n\t" \
    "buffer_store_dwordx4 v[188:191], %[oa], %[rs], %[so] offen sc1\n\t" \
    "s_nop 1\n\t" \
    "s_setprio 0\n\t"
#define ASM_WT2_HI \
    "s_setprio 1\n\t" \
    "s_barrier\n\t" ASM_ST \
    "ds_read_b128 v[160:163], %[la] offset:0\n\t" \
    "ds_read_b128 v[164:167], %[la] offset:1024\n\t" \
    "ds_read_b128 v[168:171], %[la] offset:2048\n\t" \
    "ds_read_b128 v[172:175], %[la] offset:3072\n\t" \
    "ds_read_b128 v[176:179], %[la] offset:8192\n\t" \
    "ds_read_b128 v[180:183], %[la] offset:9216\n\t" \
    "ds_read_b128 v[184:187], %[la] offset:10240\n\t" \
    "s_waitcnt lgkmcnt(6)\n\t" \
    "v_mfma_f32_16x16x32_bf16 v[192:195], v[80:83], v[160:163], 0\n\t" \
    "buffer_load_dwordx2 v[196:197], %[ro], %[rs], %[sx] offen sc1\n\t" \
    "s_waitcnt lgkmcnt(5)\n\t" \
    "v_mfma_f32_16x16x32_bf16 v[192:195], v[84:87], v[164:167], v[192:195]\n\t" \
    "buffer_load_dwordx2 v[198:199], %[ro], %[rs], %[sx] offen offset:512 sc1\n\t" \
    "v_mfma_f32_16x16x32_bf16 v[188:191], v[0:3], v[160:163], 0\n\t" \
    "ds_read_b128 v[160:163], %[la] offset:11264\n\t" \
    "v_lshrrev_b32 v204, 1, %[l16]\n\t" \
    "v_mfma_f32_16x16x32_bf16 v[188:191], v[4:7], v[164:167], v[188:191]\n\t" \
    "ds_read_b128 v[164:167], %[la] offset:16384\n\t" \
    "v_and_b32 v204, 0x78, v204\n\t" \
    "s_waitcnt lgkmcnt(6)\n\t" \
    "v_mfma_f32_16x16x32_bf16 v[188:191], v[8:11], v[168:171], v[188:191]\n\t" \
    "buffer_load_dwordx2 v[200:201], v204, %[rs], %[ss] offen sc1\n\t" \
    "s_waitcnt lgkmcnt(5)\n\t" \
    "v_mfma_f32_16x16x32_bf16 v[188:191], v[12:15], v[172:175], v[188:191]\n\t" \
    "v_mov_b64 v[216:217], -1\n\t" \
    "v_mfma_f32_16x16x32_bf16 v[192:195], v[88:91], v[168:171], v[192:195]\n\t" \
    "ds_read_b128 v[168:171], %[la] offset:17408\n\t" \
    "v_mov_b64 v[218:219], -1\n\t" \
    "v_mfma_f32_16x16x32_bf16 v[192:195], v[92:95], v[172:175], v[192:195]\n\t" \
    "ds_read_b128 v[172:175], %[la] offset:18432\n\t" \
    "buffer_store_dwordx4 v[216:219], %[l16], %[rs], %[sa] offen sc1\n\t" \
    "s_waitcnt lgkmcnt(6)\n\t" \
    "v_mfma_f32_16x16x32_bf16 v[192:195], v[96:99], v[176:179], v[192:195]\n\t" \
    "s_waitcnt lgkmcnt(5)\n\t" \
    "v_mfma_f32_16x16x32_bf16 v[192:195], v[100:103], v[180:183], v[192:195]\n\t" \
    "v_mfma_f32_16x16x32_bf16 v[188:191], v[16:19], v[176:179], v[188:191]\n\t" \
    "ds_read_b128 v[176:179], %[la] offset:19456\n\t" \
    "v_mfma_f32_16x16x32_bf16 v[188:191], v[20:23], v[180:183], v[188:191]\n\t" \
    "ds_read_b128 v[180:183], %[la] offset:24576\n\t" \
    "s_waitcnt lgkmcnt(6)\n\t" \
    "v_mfma_f32_16x16x32_bf16 v[188:191], v[24:27], v[184:187], v[188:191]\n\t" \
    "s_waitcnt lgkmcnt(5)\n\t" \
    "v_mfma_f32_16x16x32_bf16 v[188:191], v[28:31], v[160:163], v[188:191]\n\t" \
    "v_mfma_f32_16x16x32_bf16 v[192:195], v[104:107], v[184:187], v[192:195]\n\t" \
    "ds_read_b128 v[184:187], %[la] offset:25600\n\t" \
    "v_mfma_f32_16x16x32_bf16 v[192:195], v[108:111], v[160:163], v[192:195]\n\t" \
    "ds_read_b128 v[160:163], %[la] offset:26624\n\t" \
    "s_waitcnt lgkmcnt(6)\n\t" \
    "v_mfma_f32_16x16x32_bf16 v[192:195], v[112:115], v[164:167], v[192:195]\n\t" \
    "s_waitcnt lgkmcnt(5)\n\t" \
    "v_mfma_f32_16x16x32_bf16 v[192:195], v[116:119], v[168:171], v[192:195]\n\t" \
    "v_mfma_f32_16x16x32_bf16 v[188:191], v[32:35], v[164:167], v[188:191]\n\t" \
    "ds_read_b128 v[164:167], %[la] offset:27648\n\t" \
    "v_mfma_f32_16x16x32_bf16 v[188:191], v[36:39], v[168:171], v[188:191]\n\t" \
    "ds_read_b128 v[168:171], %[la] offset:4096\n\t" \
    "s_waitcnt lgkmcnt(6)\n\t" \
    "v_mfma_f32_16x16x32_bf16 v[188:191], v[40:43], v[172:175], v[188:191]\n\t" \
    "s_waitcnt lgkmcnt(5)\n\t" \
    "v_mfma_f32_16x16x32_bf16 v[188:191], v[44:47], v[176:179], v[188:191]\n\t" \
    "v_mfma_f32_16x16x32_bf16 v[192:195], v[120:123], v[172:175], v[192:195]\n\t" \
    "ds_read_b128 v[172:175], %[la] offset:12288\n\t" \
    "v_mfma_f32_16x16x32_bf16 v[192:195], v[124:127], v[176:179], v[192:195]\n\t" \
    "ds_read_b128 v[176:179], %[la] offset:20480\n\t" \
    "s_waitcnt lgkmcnt(6)\n\t" \
    "v_mfma_f32_16x16x32_bf16 v[192:195], v[128:131], v[180:183], v[192:195]\n\t" \
    "s_waitcnt lgkmcnt(5)\n\t" \
    "v_mfma_f32_16x16x32_bf16 v[192:195], v[132:135], v[184:187], v[192:195]\n\t" \
    "v_mfma_f32_16x16x32_bf16 v[188:191], v[48:51], v[180:183], v[188:191]\n\t" \
    "ds_read_b128 v[180:183], %[la] offset:28672\n\t" \
    "v_mfma_f32_16x16x32_bf16 v[188:191], v[52:55], v[184:187], v[188:191]\n\t" \
    "s_waitcnt lgkmcnt(5)\n\t" \
    "v_mfma_f32_16x16x32_bf16 v[188:191], v[56:59], v[160:163], v[188:191]\n\t" \
    "s_waitcnt lgkmcnt(4)\n\t" \
    "v_mfma_f32_16x16x32_bf16 v[188:191], v[60:63], v[164:167], v[188:191]\n\t" \
    "v_mfma_f32_16x16x32_bf16 v[192:195], v[136:139], v[160:163], v[192:195]\n\t" \
    "v_mfma_f32_16x16x32_bf16 v[192:195], v[140:143], v[164:167], v[192:195]\n\t" \
    "s_waitcnt lgkmcnt(3)\n\t" \
    "v_mfma_f32_16x16x32_bf16 v[192:195], v[144:147], v[168:171], v[192:195]\n\t" \
    "s_waitcnt lgkmcnt(2)\n\t" \
    "v_mfma_f32_16x16x32_bf16 v[192:195], v[148:151], v[172:175], v[192:195]\n\t" \
    "v_mfma_f32_16x16x32_bf16 v[188:191], v[64:67], v[168:171], v[188:191]\n\t" \
    "v_mfma_f32_16x16x32_bf16 v[188:191], v[68:71], v[172:175], v[188:191]\n\t" \
    "s_waitcnt lgkmcnt(1)\n\t" \
    "v_mfma_f32_16x16x32_bf16 v[188:191], v[72:75], v[176:179], v[188:191]\n\t" \
    "s_waitcnt lgkmcnt(0)\n\t" \
    "v_mfma_f32_16x16x32_bf16 v[188:191], v[76:79], v[180:183], v[188:191]\n\t" \
    "v_mfma_f32_16x16x32_bf16 v[192:195], v[152:155], v[176:179], v[192:195]\n\t" \
    "v_mfma_f32_16x16x32_bf16 v[192:195], v[156:159], v[180:183], v[192:195]\n\t" \
    ASM_ST3 \
    "s_waitcnt vmcnt(1)\n\t" \
    "v_lshlrev_b32 v160, 16, v196\n\t" \
    "v_and_b32 v161, 0xffff0000, v196\n\t" \
    "v_lshlrev_b32 v162, 16, v197\n\t" \
    "v_and_b32 v163, 0xffff0000, v197\n\t" \
    "v_lshlrev_b32 v164, 16, v198\n\t" \
    "v_and_b32 v165, 0xffff0000, v198\n\t" \
    "v_lshlrev_b32 v166, 16, v199\n\t" \
    "v_and_b32 v167, 0xffff0000, v199\n\t" \
    "v_sub_f32 v160, v160, v200\n\t" \
    "v_sub_f32 v161, v161, v200\n\t" \
    "v_sub_f32 v162, v162, v200\n\t" \
    "v_sub_f32 v163, v163, v200\n\t" \
    "v_sub_f32 v164, v164, v200\n\t" \
    "v_sub_f32 v165, v165, v200\n\t" \
    "v_sub_f32 v166, v166, v200\n\t" \
    "v_sub_f32 v167, v167, v200\n\t" \
    "v_mul_f32 v160, v160, v201\n\t" \
    "v_mul_f32 v161, v161, v201\n\t" \
    "v_mul_f32 v162, v162, v201\n\t" \
    "v_mul_f32 v163, v163, v201\n\t" \
    "v_mul_f32 v164, v164, v201\n\t" \
    "v_mul_f32 v165, v165, v201\n\t" \
    "v_mul_f32 v166, v166, v201\n\t" \
    "v_mul_f32 v167, v167, v201\n\t" \
    "v_cvt_pk_bf16_f32 v160, v160, v161\n\t" \
    "v_cvt_pk_bf16_f32 v162, v162, v163\n\t" \
    "v_cvt_pk_bf16_f32 v164, v164, v165\n\t" \
    "v_cvt_pk_bf16_f32 v166, v166, v167\n\t" \
    "v_and_b32 v161, 0xffff0000, v160\n\t" \
    "v_lshlrev_b32 v160, 16, v160\n\t" \
    "v_and_b32 v163, 0xffff0000, v162\n\t" \
    "v_lshlrev_b32 v162, 16, v162\n\t" \
    "v_and_b32 v165, 0xffff0000, v164\n\t" \
    "v_lshlrev_b32 v164, 16, v164\n\t" \
    "v_and_b32 v167, 0xffff0000, v166\n\t" \
    "v_lshlrev_b32 v166, 16, v166\n\t" \
    "v_add_f32 v188, v188, v160\n\t" \
    "v_add_f32 v189, v189, v161\n\t" \
    "v_add_f32 v190, v190, v162\n\t" \
    "v_add_f32 v191, v191, v163\n\t" \
    "v_add_f32 v192, v192, v164\n\t" \
    "v_add_f32 v193, v193, v165\n\t" \
    "v_add_f32 v194, v194, v166\n\t" \
    "v_add_f32 v195, v195, v167\n\t" \
    "v_cvt_pk_bf16_f32 v188, v188, v189\n\t" \
    "v_cvt_pk_bf16_f32 v189, v190, v191\n\t" \
    "v_cvt_pk_bf16_f32 v190, v192, v193\n\t" \
    "v_cvt_pk_bf16_f32 v191, v194, v195\n\t" \
    "v_min_u32 v188, -2, v188\n\t" \
    "v_min_u32 v189, -2, v189\n\t" \
    "v_min_u32 v190, -2, v190\n\t" \
    "v_min_u32 v191, -2, v191\n\t" \
    "s_nop 1\n\t" \
    "v_permlane16_swap_b32 v188, v190\n\t" \
    "v_permlane16_swap_b32 v189, v191\n\t" \
    "s_waitcnt vmcnt(1)\n\t" \
    "buffer_store_dwordx4 v[188:191], %[oa], %[rs], %[so] offen sc1\n\t" \
    "s_nop 1\n\t"
#define ASM_WT3_LO \
    "s_branch .Lt0%=\n\t" \
    ".Ltr%=:\n\t" \
    "s_cmp_gt_i32 %[n], 0\n\t" \
    "s_cbranch_scc0 .Ltg%=\n\t" \
    ".Lfw%=:\n\t" \
    "s_mov_b32 %[a], 0\n\t" \
    "v_max_u32 v252, v232, v233\n\t" \
    "v_max3_u32 v252, v234, v235, v252\n\t" \
    "v_cmp_eq_u32 vcc, -1, v252\n\t" \
    "s_cbranch_vccz .Lgw0_%=\n\t" \
    "buffer_load_dwordx4 v[232:235], %[l16], %[rs], %[i0] offen offset:0 sc1\n\t" \
    "s_mov_b32 %[a], 1\n\t" \
    ".Lgw0_%=:\n\t" \
    "v_max_u32 v252, v236, v237\n\t" \
    "v_max3_u32 v252, v238, v239, v252\n\t" \
    "v_cmp_eq_u32 vcc, -1, v252\n\t" \
    "s_cbranch_vccz .Lgw1_%=\n\t" \
    "buffer_load_dwordx4 v[236:239], %[l16], %[rs], %[i0] offen offset:1024 sc1\n\t" \
    "s_mov_b32 %[a], 1\n\t" \
    ".Lgw1_%=:\n\t" \
    "v_max_u32 v252, v240, v241\n\t" \
    "v_max3_u32 v252, v242, v243, v252\n\t" \
    "v_cmp_eq_u32 vcc, -1, v252\n\t" \
    "s_cbranch_vccz .Lgw2_%=\n\t" \
    "buffer_load_dwordx4 v[240:243], %[l16], %[rs], %[i0] offen offset:2048 sc1\n\t" \
    "s_mov_b32 %[a], 1\n\t" \
    ".Lgw2_%=:\n\t" \
    "v_max_u32 v252, v244, v245\n\t" \
    "v_max3_u32 v252, v246, v247, v252\n\t" \
    "v_cmp_eq_u32 vcc, -1, v252\n\t" \
    "s_cbranch_vccz .Lgw3_%=\n\t" \
    "buffer_load_dwordx4 v[244:247], %[l16], %[rs], %[i0] offen offset:3072 sc1\n\t" \
    "s_mov_b32 %[a], 1\n\t" \
    ".Lgw3_%=:\n\t" \
    "v_max_u32 v252, v248, v249\n\t" \
    "v_max3_u32 v252, v250, v251, v252\n\t" \
    "v_cmp_eq_u32 vcc, -1, v252\n\t" \
    "s_cbranch_vccz .Lgw4_%=\n\t" \
    "buffer_load_dwordx4 v[248:251], %[l16], %[rs], %[i1] offen offset:0 sc1\n\t" \
    "s_mov_b32 %[a], 1\n\t" \
    ".Lgw4_%=:\n\t" \
    "s_cmp_eq_u32 %[a], 0\n\t" \
    "s_cbranch_scc1 .Lhw%=\n\t" \
    "s_waitcnt vmcnt(20)\n\t" \
    ".Liw%=:\n\t" \
    "v_max_u32 v252, v232, v233\n\t" \
    "v_max3_u32 v252, v234, v235, v252\n\t" \
    "v_max3_u32 v252, v236, v237, v252\n\t" \
    "v_max3_u32 v252, v238, v239, v252\n\t" \
    "v_max3_u32 v252, v240, v241, v252\n\t" \
    "v_max3_u32 v252, v242, v243, v252\n\t" \
    "v_max3_u32 v252, v244, v245, v252\n\t" \
    "v_max3_u32 v252, v246, v247, v252\n\t" \
    "v_max3_u32 v252, v248, v249, v252\n\t" \
    "v_max3_u32 v252, v250, v251, v252\n\t" \
    "v_cmp_eq_u32 vcc, -1, v252\n\t" \
    "s_cbranch_vccz .Lhw%=\n\t" \
    "s_sleep 1\n\t" \
    "v_max_u32 v252, v232, v233\n\t" \
    "v_max3_u32 v252, v234, v235, v252\n\t" \
    "v_max3_u32 v252, v236, v237, v252\n\t" \
    "v_max3_u32 v252, v238, v239, v252\n\t" \
    "v_max3_u32 v252, v240, v241, v252\n\t" \
    "v_max3_u32 v252, v242, v243, v252\n\t" \
    "v_max3_u32 v252, v244, v245, v252\n\t" \
    "v_max3_u32 v252, v246, v247, v252\n\t" \
    "v_max3_u32 v252, v248, v249, v252\n\t" \
    "v_max3_u32 v252, v250, v251, v252\n\t" \
    "v_cmp_eq_u32 vcc, -1, v252\n\t" \
    "s_cbranch_vccz .Lhw%=\n\t" \
    "s_sleep 1\n\t" \
    "v_max_u32 v252, v232, v233\n\t" \
    "v_max3_u32 v252, v234, v235, v252\n\t" \
    "v_max3_u32 v252, v236, v237, v252\n\t" \
    "v_max3_u32 v252, v238, v239, v252\n\t" \
    "v_max3_u32 v252, v240, v241, v252\n\t" \
    "v_max3_u32 v252, v242, v243, v252\n\t" \
    "v_max3_u32 v252, v244, v245, v252\n\t" \
    "v_max3_u32 v252, v246, v247, v252\n\t" \
    "v_max3_u32 v252, v248, v249, v252\n\t" \
    "v_max3_u32 v252, v250, v251, v252\n\t" \
    "v_cmp_eq_u32 vcc, -1, v252\n\t" \
    "s_cbranch_vccz .Lhw%=\n\t" \
    "s_sleep 1\n\t" \
    "v_max_u32 v252, v232, v233\n\t" \
    "v_max3_u32 v252, v234, v235, v252\n\t" \
    "v_max3_u32 v252, v236, v237, v252\n\t" \
    "v_max3_u32 v252, v238, v239, v252\n\t" \
    "v_max3_u32 v252, v240, v241, v252\n\t" \
    "v_max3_u32 v252, v242, v243, v252\n\t" \
    "v_max3_u32 v252, v244, v245, v252\n\t" \
    "v_max3_u32 v252, v246, v247, v252\n\t" \
    "v_max3_u32 v252, v248, v249, v252\n\t" \
    "v_max3_u32 v252, v250, v251, v252\n\t" \
    "v_cmp_eq_u32 vcc, -1, v252\n\t" \
    "s_cbranch_vccz .Lhw%=\n\t" \
    "s_sleep 1\n\t" \
    "v_max_u32 v252, v232, v233\n\t" \
    "v_max3_u32 v252, v234, v235, v252\n\t" \
    "v_max3_u32 v252, v236, v237, v252\n\t" \
    "v_max3_u32 v252, v238, v239, v252\n\t" \
    "v_max3_u32 v252, v240, v241, v252\n\t" \
    "v_max3_u32 v252, v242, v243, v252\n\t" \
    "v_max3_u32 v252, v244, v245, v252\n\t" \
    "v_max3_u32 v252, v246, v247, v252\n\t" \
    "v_max3_u32 v252, v248, v249, v252\n\t" \
    "v_max3_u32 v252, v250, v251, v252\n\t" \
    "v_cmp_eq_u32 vcc, -1, v252\n\t" \
    "s_cbranch_vccz .Lhw%=\n\t" \
    "s_sleep 1\n\t" \
    "v_max_u32 v252, v232, v233\n\t" \
    "v_max3_u32 v252, v234, v235, v252\n\t" \
    "v_max3_u32 v252, v236, v237, v252\n\t" \
    "v_max3_u32 v252, v238, v239, v252\n\t" \
    "v_max3_u32 v252, v240, v241, v252\n\t" \
    "v_max3_u32 v252, v242, v243, v252\n\t" \
    "v_max3_u32 v252, v244, v245, v252\n\t" \
    "v_max3_u32 v252, v246, v247, v252\n\t" \
    "v_max3_u32 v252, v248, v249, v252\n\t" \
    "v_max3_u32 v252, v250, v251, v252\n\t" \
    "v_cmp_eq_u32 vcc, -1, v252\n\t" \
    "s_cbranch_vccz .Lhw%=\n\t" \
    "s_sleep 1\n\t" \
    "s_sub_u32 %[n], %[n], 1\n\t" \
    "s_cmp_gt_i32 %[n], 0\n\t" \
    "s_cbranch_scc1 .Lfw%=\n\t" \
    ".Lhw%=:\n\t" \
    "s_branch .Lta%=\n\t" \
    ".Lt0%=:\n\t" \
    "s_waitcnt vmcnt(1)\n\t" \
    ".Lta%=:\n\t" \
    "ds_write_b128 %[lw], v[232:235] offset:0\n\t" \
    "v_max_u32 v252, v232, v233\n\t" \
    "v_max3_u32 v252, v234, v235, v252\n\t" \
    "v_mfma_f32_16x16x32_bf16 v[188:191], v[232:235], v[232:235], 0\n\t" \
    "ds_write_b128 %[lw], v[236:239] offset:1024\n\t" \
    "v_max3_u32 v252, v236, v237, v252\n\t" \
    "v_max3_u32 v252, v238, v239, v252\n\t" \
    "v_mfma_f32_16x16x32_bf16 v[188:191], v[236:239], v[236:239], v[188:191]\n\t" \
    "ds_write_b128 %[lw], v[240:243] offset:2048\n\t" \
    "v_max3_u32 v252, v240, v241, v252\n\t" \
    "v_max3_u32 v252, v242, v243, v252\n\t" \
    "v_mfma_f32_16x16x32_bf16 v[188:191], v[240:243], v[240:243], v[188:191]\n\t" \
    "ds_write_b128 %[lw], v[244:247] offset:3072\n\t" \
    "v_max3_u32 v252, v244, v245, v252\n\t" \
    "v_max3_u32 v252, v246, v247, v252\n\t" \
    "v_mfma_f32_16x16x32_bf16 v[188:191], v[244:247], v[244:247], v[188:191]\n\t" \
    "ds_write_b128 %[lw], v[248:251] offset:4096\n\t" \
    "v_max3_u32 v252, v248, v249, v252\n\t" \
    "v_max3_u32 v252, v250, v251, v252\n\t" \
    "v_mfma_f32_16x16x32_bf16 v[188:191], v[248:251], v[248:251], v[188:191]\n\t" \
    "v_mfma_f32_16x16x32_bf16 v[192:195], v[216:219], v[232:235], 0\n\t" \
    "v_mfma_f32_16x16x32_bf16 v[192:195], v[216:219], v[236:239], v[192:195]\n\t" \
    "v_mfma_f32_16x16x32_bf16 v[192:195], v[216:219], v[240:243], v[192:195]\n\t" \
    "v_mfma_f32_16x16x32_bf16 v[192:195], v[216:219], v[244:247], v[192:195]\n\t" \
    "v_mfma_f32_16x16x32_bf16 v[192:195], v[216:219], v[248:251], v[192:195]\n\t" \
    "v_cmp_eq_u32 vcc, -1, v252\n\t" \
    "s_cbranch_vccnz .Ltr%=\n\t" \
    ".Ltg%=:\n\t" \
    ASM_ST2 \
    "s_nop 5\n\t" \
    "ds_write_b128 %[lw], v[188:191] offset:5120\n\t" \
    "ds_write_b128 %[lw], v[192:195] offset:6144\n\t" \
    "s_waitcnt lgkmcnt(0)\n\t" \
    "s_setprio 2\n\t" \
    "s_barrier\n\t" ASM_ST \
    "ds_read_b128 v[160:163], %[la] offset:0\n\t" \
    "ds_read_b128 v[164:167], %[la] offset:1024\n\t" \
    "ds_read_b128 v[168:171], %[la] offset:2048\n\t" \
    "ds_read2st64_b32 v[204:205], %[sr] offset0:20 offset1:24\n\t" \
    "ds_read_b128 v[172:175], %[la] offset:3072\n\t" \
    "ds_read_b128 v[176:179], %[la] offset:8192\n\t" \
    "ds_read_b128 v[180:183], %[la] offset:9216\n\t" \
    "ds_read_b128 v[184:187], %[la] offset:10240\n\t" \
    "s_waitcnt lgkmcnt(7)\n\t" \
    "v_mfma_f32_16x16x32_bf16 v[192:195], v[80:83], v[160:163], 0\n\t" \
    "s_waitcnt lgkmcnt(6)\n\t" \
    "v_mfma_f32_16x16x32_bf16 v[192:195], v[84:87], v[164:167], v[192:195]\n\t" \
    "v_mfma_f32_16x16x32_bf16 v[188:191], v[0:3], v[160:163], 0\n\t" \
    "ds_read_b128 v[160:163], %[la] offset:11264\n\t" \
    "v_mfma_f32_16x16x32_bf16 v[188:191], v[4:7], v[164:167], v[188:191]\n\t" \
    "ds_read_b128 v[164:167], %[la] offset:16384\n\t" \
    "s_waitcnt lgkmcnt(7)\n\t" \
    "v_mfma_f32_16x16x32_bf16 v[188:191], v[8:11], v[168:171], v[188:191]\n\t" \
    "s_waitcnt lgkmcnt(6)\n\t" \
    "s_waitcnt lgkmcnt(5)\n\t" \
    "v_mfma_f32_16x16x32_bf16 v[188:191], v[12:15], v[172:175], v[188:191]\n\t" \
    "v_permlane16_swap_b32 v204, v205\n\t" \
    "v_mfma_f32_16x16x32_bf16 v[192:195], v[88:91], v[168:171], v[192:195]\n\t" \
    "ds_read_b128 v[168:171], %[la] offset:17408\n\t" \
    "v_mfma_f32_16x16x32_bf16 v[192:195], v[92:95], v[172:175], v[192:195]\n\t" \
    "ds_read_b128 v[172:175], %[la] offset:18432\n\t" \
    "v_add_f32 v206, v204, v205\n\t" \
    "s_waitcnt lgkmcnt(6)\n\t" \
    "v_mfma_f32_16x16x32_bf16 v[192:195], v[96:99], v[176:179], v[192:195]\n\t" \
    "v_add_f32 v207, v204, v205\n\t" \
    "s_waitcnt lgkmcnt(5)\n\t" \
    "v_mfma_f32_16x16x32_bf16 v[192:195], v[100:103], v[180:183], v[192:195]\n\t" \
    "v_mfma_f32_16x16x32_bf16 v[188:191], v[16:19], v[176:179], v[188:191]\n\t" \
    "ds_read_b128 v[176:179], %[la] offset:19456\n\t" \
    "v_permlane32_swap_b32 v206, v207\n\t" \
    "v_mfma_f32_16x16x32_bf16 v[188:191], v[20:23], v[180:183], v[188:191]\n\t" \
    "ds_read_b128 v[180:183], %[la] offset:24576\n\t" \
    "s_waitcnt lgkmcnt(6)\n\t" \
    "v_mfma_f32_16x16x32_bf16 v[188:191], v[24:27], v[184:187], v[188:191]\n\t" \
    "v_add_f32 v204, v206, v207\n\t" \
    "s_waitcnt lgkmcnt(5)\n\t" \
    "v_mfma_f32_16x16x32_bf16 v[188:191], v[28:31], v[160:163], v[188:191]\n\t" \
    "v_add_f32 v205, v206, v207\n\t" \
    "v_mfma_f32_16x16x32_bf16 v[192:195], v[104:107], v[184:187], v[192:195]\n\t" \
    "ds_read_b128 v[184:187], %[la] offset:25600\n\t" \
    "v_mfma_f32_16x16x32_bf16 v[192:195], v[108:111], v[160:163], v[192:195]\n\t" \
    "ds_read_b128 v[160:163], %[la] offset:26624\n\t" \
    "v_permlane16_swap_b32 v204, v205\n\t" \
    "s_waitcnt lgkmcnt(6)\n\t" \
    "v_mfma_f32_16x16x32_bf16 v[192:195], v[112:115], v[164:167], v[192:195]\n\t" \
    "s_waitcnt lgkmcnt(5)\n\t" \
    "v_mfma_f32_16x16x32_bf16 v[192:195], v[116:119], v[168:171], v[192:195]\n\t" \
    "v_mul_f32 v205, %[kd], v205\n\t" \
    "v_mfma_f32_16x16x32_bf16 v[188:191], v[32:35], v[164:167], v[188:191]\n\t" \
    "ds_read_b128 v[164:167], %[la] offset:27648\n\t" \
    "v_mul_f32 v204, %[kd], v204\n\t" \
    "v_mfma_f32_16x16x32_bf16 v[188:191], v[36:39], v[168:171], v[188:191]\n\t" \
    "ds_read_b128 v[168:171], %[la] offset:4096\n\t" \
    "v_fma_f32 v204, -v205, v205, v204\n\t" \
    "s_waitcnt lgkmcnt(6)\n\t" \
    "v_mfma_f32_16x16x32_bf16 v[188:191], v[40:43], v[172:175], v[188:191]\n\t" \
    "v_max_f32 v204, 0, v204\n\t" \
    "s_waitcnt lgkmcnt(5)\n\t" \
    "v_mfma_f32_16x16x32_bf16 v[188:191], v[44:47], v[176:179], v[188:191]\n\t" \
    "v_add_f32 v206, %[ke], v204\n\t" \
    "v_mfma_f32_16x16x32_bf16 v[192:195], v[120:123], v[172:175], v[192:195]\n\t" \
    "ds_read_b128 v[172:175], %[la] offset:12288\n\t" \
    "v_rsq_f32 v206, v206\n\t" \
    "v_mfma_f32_16x16x32_bf16 v[192:195], v[124:127], v[176:179], v[192:195]\n\t" \
    "ds_read_b128 v[176:179], %[la] offset:20480\n\t" \
    "s_waitcnt lgkmcnt(6)\n\t" \
    "v_mfma_f32_16x16x32_bf16 v[192:195], v[128:131], v[180:183], v[192:195]\n\t" \
    "v_mul_f32 v207, -v205, v206\n\t" \
    "s_waitcnt lgkmcnt(5)\n\t" \
    "v_mfma_f32_16x16x32_bf16 v[192:195], v[132:135], v[184:187], v[192:195]\n\t" \
    "v_mul_f32 v208, v196, v207\n\t" \
    "v_mfma_f32_16x16x32_bf16 v[188:191], v[48:51], v[180:183], v[188:191]\n\t" \
    "ds_read_b128 v[180:183], %[la] offset:28672\n\t" \
    "v_mul_f32 v209, v197, v207\n\t" \
    "v_mul_f32 v210, v198, v207\n\t" \
    "v_mfma_f32_16x16x32_bf16 v[188:191], v[52:55], v[184:187], v[188:191]\n\t" \
    "v_mul_f32 v211, v199, v207\n\t" \
    "v_mul_f32 v212, v200, v207\n\t" \
    "s_waitcnt lgkmcnt(5)\n\t" \
    "v_mfma_f32_16x16x32_bf16 v[188:191], v[56:59], v[160:163], v[188:191]\n\t" \
    "v_mul_f32 v213, v201, v207\n\t" \
    "v_mul_f32 v214, v202, v207\n\t" \
    "s_waitcnt lgkmcnt(4)\n\t" \
    "v_mfma_f32_16x16x32_bf16 v[188:191], v[60:63], v[164:167], v[188:191]\n\t" \
    "v_mul_f32 v215, v203, v207\n\t" \
    "v_mov_b64 v[216:217], -1\n\t" \
    "v_mfma_f32_16x16x32_bf16 v[192:195], v[136:139], v[160:163], v[192:195]\n\t" \
    "v_mov_b64 v[218:219], -1\n\t" \
    "buffer_store_dwordx4 v[216:219], %[l16], %[rs], %[sa] offen sc1\n\t" \
    "v_mfma_f32_16x16x32_bf16 v[192:195], v[140:143], v[164:167], v[192:195]\n\t" \
    "s_waitcnt lgkmcnt(3)\n\t" \
    "v_mfma_f32_16x16x32_bf16 v[192:195], v[144:147], v[168:171], v[192:195]\n\t" \
    "s_waitcnt lgkmcnt(2)\n\t" \
    "v_mfma_f32_16x16x32_bf16 v[192:195], v[148:151], v[172:175], v[192:195]\n\t" \
    "v_mfma_f32_16x16x32_bf16 v[188:191], v[64:67], v[168:171], v[188:191]\n\t" \
    "v_mfma_f32_16x16x32_bf16 v[188:191], v[68:71], v[172:175], v[188:191]\n\t" \
    "s_waitcnt lgkmcnt(1)\n\t" \
    "v_mfma_f32_16x16x32_bf16 v[188:191], v[72:75], v[176:179], v[188:191]\n\t" \
    "s_waitcnt lgkmcnt(0)\n\t" \
    "v_mfma_f32_16x16x32_bf16 v[188:191], v[76:79], v[180:183], v[188:191]\n\t" \
    "v_mfma_f32_16x16x32_bf16 v[192:195], v[152:155], v[176:179], v[192:195]\n\t" \
    "v_mfma_f32_16x16x32_bf16 v[192:195], v[156:159], v[180:183], v[192:195]\n\t" \
    "s_setprio 2\n\t" \
    "buffer_load_dwordx4 v[232:235], %[l16], %[rs], %[n0] offen offset:0 sc1\n\t" \
    "buffer_load_dwordx4 v[236:239], %[l16], %[rs], %[n0] offen offset:1024 sc1\n\t" \
    "buffer_load_dwordx4 v[240:243], %[l16], %[rs], %[n0] offen offset:2048 sc1\n\t" \
    "buffer_load_dwordx4 v[244:247], %[l16], %[rs], %[n0] offen offset:3072 sc1\n\t" \
    "buffer_load_dwordx4 v[248:251], %[l16], %[rs], %[n1] offen offset:0 sc1\n\t" \
    ASM_ST3 \
    "v_fma_f32 v188, v188, v206, v208\n\t" \
    "v_fma_f32 v189, v189, v206, v209\n\t" \
    "v_fma_f32 v190, v190, v206, v210\n\t" \
    "v_fma_f32 v191, v191, v206, v211\n\t" \
    "v_mul_f32 v160, v188, v188\n\t" \
    "v_mul_f32 v161, v189, v189\n\t" \
    "v_mul_f32 v162, v190, v190\n\t" \
    "v_mul_f32 v163, v191, v191\n\t" \
    "v_fma_f32 v160, v160, %[k1], v220\n\t" \
    "v_fma_f32 v161, v161, %[k1], v220\n\t" \
    "v_fma_f32 v162, v162, %[k1], v220\n\t" \
    "v_fma_f32 v163, v163, %[k1], v220\n\t" \
    "v_mul_f32 v160, v188, v160\n\t" \
    "v_mul_f32 v161, v189, v161\n\t" \
    "v_mul_f32 v162, v190, v162\n\t" \
    "v_mul_f32 v163, v191, v163\n\t" \
    "v_fma_f32 v192, v192, v206, v212\n\t" \
    "v_fma_f32 v193, v193, v206, v213\n\t" \
    "v_fma_f32 v194, v194, v206, v214\n\t" \
    "v_fma_f32 v195, v195, v206, v215\n\t" \
    "v_mul_f32 v164, v192, v192\n\t" \
    "v_mul_f32 v165, v193, v193\n\t" \
    "v_mul_f32 v166, v194, v194\n\t" \
    "v_mul_f32 v167, v195, v195\n\t" \
    "v_fma_f32 v164, v164, %[k1], v220\n\t" \
    "v_fma_f32 v165, v165, %[k1], v220\n\t" \
    "v_fma_f32 v166, v166, %[k1], v220\n\t" \
    "v_fma_f32 v167, v167, %[k1], v220\n\t" \
    "v_mul_f32 v164, v192, v164\n\t" \
    "v_mul_f32 v165, v193, v165\n\t" \
    "v_mul_f32 v166, v194, v166\n\t" \
    "v_mul_f32 v167, v195, v167\n\t" \
    "v_exp_f32 v160, v160\n\t" \
    "v_exp_f32 v161, v161\n\t" \
    "v_exp_f32 v162, v162\n\t" \
    "v_exp_f32 v163, v163\n\t" \
    "v_exp_f32 v164, v164\n\t" \
    "v_exp_f32 v165, v165\n\t" \
    "v_exp_f32 v166, v166\n\t" \
    "v_exp_f32 v167, v167\n\t" \
    "v_add_f32 v160, 1.0, v160\n\t" \
    "v_add_f32 v161, 1.0, v161\n\t" \
    "v_add_f32 v162, 1.0, v162\n\t" \
    "v_add_f32 v163, 1.0, v163\n\t" \
    "v_add_f32 v164, 1.0, v164\n\t" \
    "v_add_f32 v165, 1.0, v165\n\t" \
    "v_add_f32 v166, 1.0, v166\n\t" \
    "v_add_f32 v167, 1.0, v167\n\t" \
    "v_rcp_f32 v160, v160\n\t" \
    "v_rcp_f32 v161, v161\n\t" \
    "v_rcp_f32 v162, v162\n\t" \
    "v_rcp_f32 v163, v163\n\t" \
    "v_rcp_f32 v164, v164\n\t" \
    "v_rcp_f32 v165, v165\n\t" \
    "v_rcp_f32 v166, v166\n\t" \
    "v_rcp_f32 v167, v167\n\t" \
    "v_mul_f32 v188, v188, v160\n\t" \
    "v_mul_f32 v189, v189, v161\n\t" \
    "v_mul_f32 v190, v190, v162\n\t" \
    "v_mul_f32 v191, v191, v163\n\t" \
    "v_mul_f32 v192, v192, v164\n\t" \
    "v_mul_f32 v193, v193, v165\n\t" \
    "v_mul_f32 v194, v194, v166\n\t" \
    "v_mul_f32 v195, v195, v167\n\t" \
    "v_cvt_pk_bf16_f32 v188, v188, v189\n\t" \
    "v_cvt_pk_bf16_f32 v189, v190, v191\n\t" \
    "v_cvt_pk_bf16_f32 v190, v192, v193\n\t" \
    "v_cvt_pk_bf16_f32 v191, v194, v195\n\t" \
    "v_min_u32 v188, -2, v188\n\t" \
    "v_min_u32 v189, -2, v189\n\t" \
    "v_min_u32 v190, -2, v190\n\t" \
    "v_min_u32 v191, -2, v191\n\t" \
    "v_mov_b32 v216, 0x3f803f80\n\t" \
    "v_mov_b32 v217, 0x3f803f80\n\t" \
    "v_mov_b32 v218, 0x3f803f80\n\t" \
    "v_mov_b32 v219, 0x3f803f80\n\t" \
    "v_permlane16_swap_b32 v188, v190\n\t" \
    "v_permlane16_swap_b32 v189, v191\n\t" \
    "s_waitcnt vmcnt(6)\n\t" \
    "buffer_store_dwordx4 v[188:191], %[oa], %[rs], %[so] offen sc1\n\t" \
    "s_nop 1\n\t" \
    "s_setprio 0\n\t"
#define ASM_WT3_HI \
    "s_setprio 1\n\t" \
    "s_barrier\n\t" ASM_ST \
    "ds_read_b128 v[160:163], %[la] offset:0\n\t" \
    "ds_read_b128 v[164:167], %[la] offset:1024\n\t" \
    "ds_read_b128 v[168:171], %[la] offset:2048\n\t" \
    "ds_read2st64_b32 v[204:205], %[sr] offset0:20 offset1:24\n\t" \
    "ds_read_b128 v[172:175], %[la] offset:3072\n\t" \
    "ds_read_b128 v[176:179], %[la] offset:8192\n\t" \
    "ds_read_b128 v[180:183], %[la] offset:9216\n\t" \
    "ds_read_b128 v[184:187], %[la] offset:10240\n\t" \
    "s_waitcnt lgkmcnt(7)\n\t" \
    "v_mfma_f32_16x16x32_bf16 v[192:195], v[80:83], v[160:163], 0\n\t" \
    "s_waitcnt lgkmcnt(6)\n\t" \
    "v_mfma_f32_16x16x32_bf16 v[192:195], v[84:87], v[164:167], v[192:195]\n\t" \
    "v_mfma_f32_16x16x32_bf16 v[188:191], v[0:3], v[160:163], 0\n\t" \
    "ds_read_b128 v[160:163], %[la] offset:11264\n\t" \
    "v_mfma_f32_16x16x32_bf16 v[188:191], v[4:7], v[164:167], v[188:191]\n\t" \
    "ds_read_b128 v[164:167], %[la] offset:16384\n\t" \
    "s_waitcnt lgkmcnt(7)\n\t" \
    "v_mfma_f32_16x16x32_bf16 v[188:191], v[8:11], v[168:171], v[188:191]\n\t" \
    "s_waitcnt lgkmcnt(6)\n\t" \
    "s_waitcnt lgkmcnt(5)\n\t" \
    "v_mfma_f32_16x16x32_bf16 v[188:191], v[12:15], v[172:175], v[188:191]\n\t" \
    "v_permlane16_swap_b32 v204, v205\n\t" \
    "v_mfma_f32_16x16x32_bf16 v[192:195], v[88:91], v[168:171], v[192:195]\n\t" \
    "ds_read_b128 v[168:171], %[la] offset:17408\n\t" \
    "v_mfma_f32_16x16x32_bf16 v[192:195], v[92:95], v[172:175], v[192:195]\n\t" \
    "ds_read_b128 v[172:175], %[la] offset:18432\n\t" \
    "v_add_f32 v206, v204, v205\n\t" \
    "s_waitcnt lgkmcnt(6)\n\t" \
    "v_mfma_f32_16x16x32_bf16 v[192:195], v[96:99], v[176:179], v[192:195]\n\t" \
    "v_add_f32 v207, v204, v205\n\t" \
    "s_waitcnt lgkmcnt(5)\n\t" \
    "v_mfma_f32_16x16x32_bf16 v[192:195], v[100:103], v[180:183], v[192:195]\n\t" \
    "v_mfma_f32_16x16x32_bf16 v[188:191], v[16:19], v[176:179], v[188:191]\n\t" \
    "ds_read_b128 v[176:179], %[la] offset:19456\n\t" \
    "v_permlane32_swap_b32 v206, v207\n\t" \
    "v_mfma_f32_16x16x32_bf16 v[188:191], v[20:23], v[180:183], v[188:191]\n\t" \
    "ds_read_b128 v[180:183], %[la] offset:24576\n\t" \
    "s_waitcnt lgkmcnt(6)\n\t" \
    "v_mfma_f32_16x16x32_bf16 v[188:191], v[24:27], v[184:187], v[188:191]\n\t" \
    "v_add_f32 v204, v206, v207\n\t" \
    "s_waitcnt lgkmcnt(5)\n\t" \
    "v_mfma_f32_16x16x32_bf16 v[188:191], v[28:31], v[160:163], v[188:191]\n\t" \
    "v_add_f32 v205, v206, v207\n\t" \
    "v_mfma_f32_16x16x32_bf16 v[192:195], v[104:107], v[184:187], v[192:195]\n\t" \
    "ds_read_b128 v[184:187], %[la] offset:25600\n\t" \
    "v_mfma_f32_16x16x32_bf16 v[192:195], v[108:111], v[160:163], v[192:195]\n\t" \
    "ds_read_b128 v[160:163], %[la] offset:26624\n\t" \
    "v_permlane16_swap_b32 v204, v205\n\t" \
    "s_waitcnt lgkmcnt(6)\n\t" \
    "v_mfma_f32_16x16x32_bf16 v[192:195], v[112:115], v[164:167], v[192:195]\n\t" \
    "s_waitcnt lgkmcnt(5)\n\t" \
    "v_mfma_f32_16x16x32_bf16 v[192:195], v[116:119], v[168:171], v[192:195]\n\t" \
    "v_mul_f32 v205, %[kd], v205\n\t" \
    "v_mfma_f32_16x16x32_bf16 v[188:191], v[32:35], v[164:167], v[188:191]\n\t" \
    "ds_read_b128 v[164:167], %[la] offset:27648\n\t" \
    "v_mul_f32 v204, %[kd], v204\n\t" \
    "v_mfma_f32_16x16x32_bf16 v[188:191], v[36:39], v[168:171], v[188:191]\n\t" \
    "ds_read_b128 v[168:171], %[la] offset:4096\n\t" \
    "v_fma_f32 v204, -v205, v205, v204\n\t" \
    "s_waitcnt lgkmcnt(6)\n\t" \
    "v_mfma_f32_16x16x32_bf16 v[188:191], v[40:43], v[172:175], v[188:191]\n\t" \
    "v_max_f32 v204, 0, v204\n\t" \
    "s_waitcnt lgkmcnt(5)\n\t" \
    "v_mfma_f32_16x16x32_bf16 v[188:191], v[44:47], v[176:179], v[188:191]\n\t" \
    "v_add_f32 v206, %[ke], v204\n\t" \
    "v_mfma_f32_16x16x32_bf16 v[192:195], v[120:123], v[172:175], v[192:195]\n\t" \
    "ds_read_b128 v[172:175], %[la] offset:12288\n\t" \
    "v_rsq_f32 v206, v206\n\t" \
    "v_mfma_f32_16x16x32_bf16 v[192:195], v[124:127], v[176:179], v[192:195]\n\t" \
    "ds_read_b128 v[176:179], %[la] offset:20480\n\t" \
    "s_waitcnt lgkmcnt(6)\n\t" \
    "v_mfma_f32_16x16x32_bf16 v[192:195], v[128:131], v[180:183], v[192:195]\n\t" \
    "v_mul_f32 v207, -v205, v206\n\t" \
    "s_waitcnt lgkmcnt(5)\n\t" \
    "v_mfma_f32_16x16x32_bf16 v[192:195], v[132:135], v[184:187], v[192:195]\n\t" \
    "v_mul_f32 v208, v196, v207\n\t" \
    "v_mfma_f32_16x16x32_bf16 v[188:191], v[48:51], v[180:183], v[188:191]\n\t" \
    "ds_read_b128 v[180:183], %[la] offset:28672\n\t" \
    "v_mul_f32 v209, v197, v207\n\t" \
    "v_mul_f32 v210, v198, v207\n\t" \
    "v_mfma_f32_16x16x32_bf16 v[188:191], v[52:55], v[184:187], v[188:191]\n\t" \
    "v_mul_f32 v211, v199, v207\n\t" \
    "v_mul_f32 v212, v200, v207\n\t" \
    "s_waitcnt lgkmcnt(5)\n\t" \
    "v_mfma_f32_16x16x32_bf16 v[188:191], v[56:59], v[160:163], v[188:191]\n\t" \
    "v_mul_f32 v213, v201, v207\n\t" \
    "v_mul_f32 v214, v202, v207\n\t" \
    "s_waitcnt lgkmcnt(4)\n\t" \
    "v_mfma_f32_16x16x32_bf16 v[188:191], v[60:63], v[164:167], v[188:191]\n\t" \
    "v_mul_f32 v215, v203, v207\n\t" \
    "v_mov_b64 v[216:217], -1\n\t" \
    "v_mfma_f32_16x16x32_bf16 v[192:195], v[136:139], v[160:163], v[192:195]\n\t" \
    "v_mov_b64 v[218:219], -1\n\t" \
    "buffer_store_dwordx4 v[216:219], %[l16], %[rs], %[sa] offen sc1\n\t" \
    "v_mfma_f32_16x16x32_bf16 v[192:195], v[140:143], v[164:167], v[192:195]\n\t" \
    "s_waitcnt lgkmcnt(3)\n\t" \
    "v_mfma_f32_16x16x32_bf16 v[192:195], v[144:147], v[168:171], v[192:195]\n\t" \
    "s_waitcnt lgkmcnt(2)\n\t" \
    "v_mfma_f32_16x16x32_bf16 v[192:195], v[148:151], v[172:175], v[192:195]\n\t" \
    "v_mfma_f32_16x16x32_bf16 v[188:191], v[64:67], v[168:171], v[188:191]\n\t" \
    "v_mfma_f32_16x16x32_bf16 v[188:191], v[68:71], v[172:175], v[188:191]\n\t" \
    "s_waitcnt lgkmcnt(1)\n\t" \
    "v_mfma_f32_16x16x32_bf16 v[188:191], v[72:75], v[176:179], v[188:191]\n\t" \
    "s_waitcnt lgkmcnt(0)\n\t" \
    "v_mfma_f32_16x16x32_bf16 v[188:191], v[76:79], v[180:183], v[188:191]\n\t" \
    "v_mfma_f32_16x16x32_bf16 v[192:195], v[152:155], v[176:179], v[192:195]\n\t" \
    "v_mfma_f32_16x16x32_bf16 v[192:195], v[156:159], v[180:183], v[192:195]\n\t" \
    ASM_ST3 \
    "s_nop 1\n\t" \
    "v_fma_f32 v188, v188, v206, v208\n\t" \
    "v_fma_f32 v189, v189, v206, v209\n\t" \
    "v_fma_f32 v190, v190, v206, v210\n\t" \
    "v_fma_f32 v191, v191, v206, v211\n\t" \
    "v_mul_f32 v160, v188, v188\n\t" \
    "v_mul_f32 v161, v189, v189\n\t" \
    "v_mul_f32 v162, v190, v190\n\t" \
    "v_mul_f32 v163, v191, v191\n\t" \
    "v_fma_f32 v160, v160, %[k1], v220\n\t" \
    "v_fma_f32 v161, v161, %[k1], v220\n\t" \
    "v_fma_f32 v162, v162, %[k1], v220\n\t" \
    "v_fma_f32 v163, v163, %[k1], v220\n\t" \
    "v_mul_f32 v160, v188, v160\n\t" \
    "v_mul_f32 v161, v189, v161\n\t" \
    "v_mul_f32 v162, v190, v162\n\t" \
    "v_mul_f32 v163, v191, v163\n\t" \
    "v_fma_f32 v192, v192, v206, v212\n\t" \
    "v_fma_f32 v193, v193, v206, v213\n\t" \
    "v_fma_f32 v194, v194, v206, v214\n\t" \
    "v_fma_f32 v195, v195, v206, v215\n\t" \
    "v_mul_f32 v164, v192, v192\n\t" \
    "v_mul_f32 v165, v193, v193\n\t" \
    "v_mul_f32 v166, v194, v194\n\t" \
    "v_mul_f32 v167, v195, v195\n\t" \
    "v_fma_f32 v164, v164, %[k1], v220\n\t" \
    "v_fma_f32 v165, v165, %[k1], v220\n\t" \
    "v_fma_f32 v166, v166, %[k1], v220\n\t" \
    "v_fma_f32 v167, v167, %[k1], v220\n\t" \
    "v_mul_f32 v164, v192, v164\n\t" \
    "v_mul_f32 v165, v193, v165\n\t" \
    "v_mul_f32 v166, v194, v166\n\t" \
    "v_mul_f32 v167, v195, v167\n\t" \
    "v_exp_f32 v160, v160\n\t" \
    "v_exp_f32 v161, v161\n\t" \
    "v_exp_f32 v162, v162\n\t" \
    "v_exp_f32 v163, v163\n\t" \
    "v_exp_f32 v164, v164\n\t" \
    "v_exp_f32 v165, v165\n\t" \
    "v_exp_f32 v166, v166\n\t" \
    "v_exp_f32 v167, v167\n\t" \
    "v_add_f32 v160, 1.0, v160\n\t" \
    "v_add_f32 v161, 1.0, v161\n\t" \
    "v_add_f32 v162, 1.0, v162\n\t" \
    "v_add_f32 v163, 1.0, v163\n\t" \
    "v_add_f32 v164, 1.0, v164\n\t" \
    "v_add_f32 v165, 1.0, v165\n\t" \
    "v_add_f32 v166, 1.0, v166\n\t" \
    "v_add_f32 v167, 1.0, v167\n\t" \
    "v_rcp_f32 v160, v160\n\t" \
    "v_rcp_f32 v161, v161\n\t" \
    "v_rcp_f32 v162, v162\n\t" \
    "v_rcp_f32 v163, v163\n\t" \
    "v_rcp_f32 v164, v164\n\t" \
    "v_rcp_f32 v165, v165\n\t" \
    "v_rcp_f32 v166, v166\n\t" \
    "v_rcp_f32 v167, v167\n\t" \
    "v_mul_f32 v188, v188, v160\n\t" \
    "v_mul_f32 v189, v189, v161\n\t" \
    "v_mul_f32 v190, v190, v162\n\t" \
    "v_mul_f32 v191, v191, v163\n\t" \
    "v_mul_f32 v192, v192, v164\n\t" \
    "v_mul_f32 v193, v193, v165\n\t" \
    "v_mul_f32 v194, v194, v166\n\t" \
    "v_mul_f32 v195, v195, v167\n\t" \
    "v_cvt_pk_bf16_f32 v188, v188, v189\n\t" \
    "v_cvt_pk_bf16_f32 v189, v190, v191\n\t" \
    "v_cvt_pk_bf16_f32 v190, v192, v193\n\t" \
    "v_cvt_pk_bf16_f32 v191, v194, v195\n\t" \
    "v_min_u32 v188, -2, v188\n\t" \
    "v_min_u32 v189, -2, v189\n\t" \
    "v_min_u32 v190, -2, v190\n\t" \
    "v_min_u32 v191, -2, v191\n\t" \
    "s_nop 1\n\t" \
    "v_permlane16_swap_b32 v188, v190\n\t" \
    "v_permlane16_swap_b32 v189, v191\n\t" \
    "s_waitcnt vmcnt(1)\n\t" \
    "buffer_store_dwordx4 v[188:191], %[oa], %[rs], %[so] offen sc1\n\t" \
    "s_nop 1\n\t"
#define ASM_POLL5_I \
    "buffer_load_dwordx4 v[232:235], %[l16], %[rs], %[s0] offen offset:0 sc1\n\t" \
    "buffer_load_dwordx4 v[236:239], %[l16], %[rs], %[s0] offen offset:1024 sc1\n\t" \
    "buffer_load_dwordx4 v[240:243], %[l16], %[rs], %[s0] offen offset:2048 sc1\n\t" \
    "buffer_load_dwordx4 v[244:247], %[l16], %[rs], %[s0] offen offset:3072 sc1\n\t" \
    "buffer_load_dwordx4 v[248:251], %[l16], %[rs], %[s1] offen offset:0 sc1\n\t"
using u32x8 = __attribute__((ext_vector_type(8))) unsigned;
using u32x16 = __attribute__((ext_vector_type(16))) unsigned;
using u32x32 = __attribute__((ext_vector_type(32))) unsigned;
struct WRegs { u32x32 w[6]; u32x8 t; };
#define WREGS(W) "{v[0:31]}"(W.w[0]), "{v[32:63]}"(W.w[1]), "{v[64:95]}"(W.w[2]), "{v[96:127]}"(W.w[3]), "{v[128:159]}"(W.w[4]), "{v[160:191]}"(W.w[5]), "{v[192:199]}"(W.t)
__device__ __forceinline__ u32x32 cat32(u32x8 a, u32x8 b, u32x8 c, u32x8 d) {
    const u32x16 lo = __builtin_shufflevector(a, b, 0, 1, 2, 3, 4, 5, 6, 7, 8, 9, 10, 11, 12, 13, 14, 15);
    const u32x16 hi = __builtin_shufflevector(c, d, 0, 1, 2, 3, 4, 5, 6, 7, 8, 9, 10, 11, 12, 13, 14, 15);
    return __builtin_shufflevector(lo, hi, 0, 1, 2, 3, 4, 5, 6, 7, 8, 9, 10, 11, 12, 13, 14, 15, 16, 17, 18, 19, 20, 21, 22, 23, 24, 25, 26, 27, 28, 29, 30, 31);
}
#define WOUT(W) "=&{v[0:31]}"(W.w[0]), "=&{v[32:63]}"(W.w[1]), "=&{v[64:95]}"(W.w[2]), "=&{v[96:127]}"(W.w[3]), "=&{v[128:159]}"(W.w[4]), "=&{v[160:191]}"(W.w[5]), "=&{v[192:199]}"(W.t)
#define WTMP "memory", "scc", "v200", "v201", "v202", "v203", "v204", "v205", "v206", "v207", "v208", "v209", "v210", "v211", "v212", "v213", "v214", "v215", "v216", "v217", "v218", "v219", "v220", "v221", "v222", "v223", "v224", "v225", "v226", "v227", "v228", "v229", "v230", "v231", "v232", "v233", "v234", "v235", "v236", "v237", "v238", "v239"
__device__ __forceinline__ void load_w3(WRegs& W, const bf16_t* m, int wave, unsigned c0, bool hq) {
    const int ln = lane_id();
    unsigned o[3], s[3], t;
#pragma unroll
    for (int k = 0; k < 3; ++k) {
        const int row = (k * 64 + ln) / 24, sg = (k * 64 + ln) % 24;
        o[k] = row * (HD * 2) + c0 + (hq ? ((sg >> 1) & 1) * 384 + ((0x140 >> ((sg >> 3) * 4)) & 15) * 32 + (sg & 5) * 16 : sg * 16); s[k] = wave * 13824 + row * 416 + sg * 16;
    }
    const unsigned tr = (ln >> 4) * 3456 + ((ln >> 2) & 3) * 416 + (wave & 3) * 32 + (ln & 3) * 8;
    asm volatile(ASM_WLOAD3 : WOUT(W), [t] "=&s"(t) : [rs] "s"(mkrsrc(m, DM * HD * 2)), [wx] "s"(wave), [o0] "v"(o[0]), [o1] "v"(o[1]), [o2] "v"(o[2]), [s0] "v"(s[0]), [s1] "v"(s[1]), [s2] "v"(s[2]),
                 [t0] "v"(tr + wave * 13824), [t1] "v"(tr + (wave ^ 1) * 13824), [t2] "v"(tr + (wave ^ 2) * 13824), [t3] "v"(tr + (wave ^ 3) * 13824), [t4] "v"(tr + (wave & 4) * 13824),
                 [wl] "v"(L_W + wave * 10240 + ln * 16) : WTMP);
}
__device__ __forceinline__ void load_wo(WRegs& W, const bf16_t* m, int wave, int h) {
    const int ln = lane_id();
    unsigned o[5], s[5], t;
#pragma unroll
    for (int k = 0; k < 5; ++k) {
        const int row = (k * 64 + ln) / 20, sg = (k * 64 + ln) % 20;
        o[k] = (h * DH + row) * (DM * 2) + sg * 16 + (wave & 3) * 320; s[k] = wave * 13824 + row * 352 + sg * 16;
    }
    asm volatile(ASM_WLOADO : WOUT(W), [t] "=&s"(t) : [rs] "s"(mkrsrc(m, DM * HD * 2)), [o0] "v"(o[0]), [o1] "v"(o[1]), [o2] "v"(o[2]), [o3] "v"(o[3]), [o4] "v"(o[4]),
                 [s0] "v"(s[0]), [s1] "v"(s[1]), [s2] "v"(s[2]), [s3] "v"(s[3]), [s4] "v"(s[4]), [tr] "v"(wave * 13824 + (ln >> 2) * 352 + (ln & 3) * 8), [wl] "v"(L_W + wave * 10240 + ln * 16) : WTMP);
}
using s16x4 = __attribute__((ext_vector_type(4))) short;
template <int NT, int KS, int N>
__device__ __forceinline__ void load_wn(u32x4 (&f)[NT * KS], const bf16_t* m, int rows, int wave, int c0, int k0) {
    const int ln = lane_id();
    const rsrc_t rs = mkrsrc(m, rows * N * 2);
#pragma unroll
    for (int k = 0; k < KS; ++k)
#pragma unroll
        for (int n = 0; n < NT; ++n) f[n * KS + k] = __builtin_amdgcn_raw_buffer_load_b128(rs, ((n * 64 + ln) / (2 * NT)) * (N * 2) + ((n * 64 + ln) % (2 * NT)) * 16, (k0 + k) * (N * 64) + c0 * 32, 0);
    const unsigned st = wave * 10240 + ln * 16, tr = wave * 10240 + ((ln >> 4) * 8 + ((ln >> 2) & 3)) * (NT * 32) + (ln & 3) * 8;
#pragma unroll
    for (int k = 0; k < KS; ++k) {
#pragma unroll
        for (int n = 0; n < NT; ++n) lds_st<u32x4>(st + n * 1024, f[n * KS + k]);
        asm volatile("" ::: "memory");
#pragma unroll
        for (int n = 0; n < NT; ++n) {
            const u32x2 a = __builtin_bit_cast(u32x2, __builtin_amdgcn_ds_read_tr16_b64_v4i16((LAS s16x4*)(tr + n * 32)));
            const u32x2 b = __builtin_bit_cast(u32x2, __builtin_amdgcn_ds_read_tr16_b64_v4i16((LAS s16x4*)(tr + n * 32 + NT * 128)));
            f[n * KS + k] = (u32x4){a[0], a[1], b[0], b[1]};
        }
        asm volatile("" ::: "memory");
    }
}
struct C1Regs { u32x8 lw0, lw1, z1; u32x4 z0; };
#define XG_OUT "=&{v[200:207]}"(C.lw0), "=&{v[224:227]}"(C.z0), "=&{v[228:235]}"(C.lw1), "=&{v[244:251]}"(C.z1)
#define XG_IN WREGS(W), [xa] "v"(xa), [wl] "v"(wl), [qs] "s"(qs)
#define XG_CLOB "memory", "scc", "v208", "v209", "v210", "v211", "v212", "v213", "v214", "v215", "v216", "v217", "v218", "v219", "v220", "v221", "v222", "v223", "v236", "v237", "v238", "v239", "v240", "v241", "v242", "v243", "v252"
constexpr int XS0 = 8;
__device__ __forceinline__ unsigned init_wait(const Params& P, int c, int ph, unsigned need = 0x220080u) {
    unsigned w;
    for (int spins = 0;; ++spins) {
        unsigned o;
        asm volatile("v_mov_b32 %0, 0" : "=v"(o));
        w = __builtin_amdgcn_readfirstlane(__builtin_amdgcn_raw_buffer_load_b32(mkrsrc(P.ws + FL0, FL_BYTES), o, FL_IN + (ph * 8 + (c & 7)) * 128, 16));
        if ((w & need) == need) break;
        __builtin_amdgcn_s_sleep(8);
        if (spin_fail(spins, P)) break;
    }
    asm volatile("" ::: "memory");
    return w;
}
#define PLACE() const unsigned pw_ = init_wait(P, wave, L); const int par = (L ^ (pw_ >> 16)) & 1, cpy = x ^ ((pw_ >> 20) & 1); const rsrc_t rr = mkrsrc(P.ws + par * 8192, RG_L); (void)cpy; (void)rr
__device__ __forceinline__ void xn_wait(const Params& P, rsrc_t rr, int seg, int cpy) {
    for (int spins = 0;; ++spins) {
        const unsigned f = __builtin_amdgcn_raw_buffer_load_b32(rr, (lane_id() & 15) * 128, RG_FX + (cpy & 3) * 2048, 16);
        if (__all(f > (unsigned)seg)) break;
        __builtin_amdgcn_s_sleep(4);
        if (spin_fail(spins, P)) break;
    }
    asm volatile("" ::: "memory");
}
__device__ __forceinline__ unsigned c1_dst(int wave, bool ktype) {
    const int ln = lane_id(), l15 = ln & 15, hf = (wave >> 1) & 1;
    const int rb16 = ((ln >> 2) & 4) * 16 + (ln >> 5) * 32;
    return (ktype ? L_KM + (l15 >> 2) * 128 + hf * 8 + (l15 & 3) * 2
                  : L_Q + (l15 >> 3) * 256 + ((l15 >> 1) & 1) * 128 + ((l15 >> 2) & 1) * 8 + hf * 4 + (l15 & 1) * 2) + (wave & 1) * 1536 + rb16;
}
template <int NP>
__device__ __forceinline__ void crit1(const WRegs& W, C1Regs& C, rsrc_t rr, unsigned sbase, int wave, unsigned wl, unsigned dsto, unsigned& budget, unsigned& fv, unsigned fs
) {
    unsigned abase;
    asm volatile("v_and_b32 %0, 0x3ff, %1\n\tv_or_b32 %0, %2, %0" : "=&v"(abase) : "v"(wl), "s"(wave * 1024));
    ASM_ST_DECL;
#define C1_OUT "+{v[224:227]}"(C.z0), "+{v[244:251]}"(C.z1), "+{v[200:207]}"(C.lw0), "+{v[228:235]}"(C.lw1)
#define C1_IN WREGS(W), [ab] "v"(abase), [wl] "v"(wl), [dst] "v"(dsto)
#define C1_CLOB "memory", "vcc", "scc", "v208", "v209", "v210", "v211", "v212", "v213", "v214", "v215", "v216", "v217", "v218", "v219", "v220", "v221", "v222", "v223", "v236", "v237", "v238", "v239", "v240", "v241", "v242", "v243"
#define C1_POLL [rs] "s"(rr), [s0] "s"(sbase), [s1] "s"(sbase + 4096), [s2] "s"(sbase + 2 * BLK)
    ST(1);
    if constexpr (NP == 13) asm volatile(ASM_CRIT1_V3 "s_waitcnt vmcnt(0)" : C1_OUT, [n] "+s"(budget), [fv] "+v"(fv) ASM_ST_OUT ASM_ST2_OUT : C1_IN, C1_POLL, [fs] "s"(fs) : C1_CLOB);
    else if constexpr (NP == 12) asm volatile(ASM_CRIT1_V2 "s_waitcnt vmcnt(0)" : C1_OUT, [n] "+s"(budget), [fv] "+v"(fv) ASM_ST_OUT ASM_ST2_OUT : C1_IN, C1_POLL, [fs] "s"(fs) : C1_CLOB);
    else if constexpr (NP == 3) asm volatile(ASM_CRIT1_3 : C1_OUT, [n] "+s"(budget) ASM_ST_OUT ASM_ST2_OUT : C1_IN, C1_POLL : C1_CLOB);
    else if constexpr (NP == 2) asm volatile(ASM_CRIT1_2 : C1_OUT, [n] "+s"(budget) ASM_ST_OUT ASM_ST2_OUT : C1_IN, C1_POLL : C1_CLOB);
    else asm volatile(ASM_CRIT1_N : C1_OUT ASM_ST_OUT : C1_IN : C1_CLOB);
    SB();
    ASM_ST_SAVE(3);
    if constexpr (NP != 0) { ASM_ST2_SAVE(2); }
}
#define STARG

#define SRG 5
#undef STROLE
#define STROLE 1
__device__ __forceinline__ void s_role(const Params& P, int wave, int L, int h, int x) {
    const char* xn = P.ws + PG0 + PG_XN + L * PG_LX;
    auto xdma = [&](int seg, int hb, int par) {
        const unsigned l16 = lane_id() * 16;
        const int mt = (wave >> 1) & 1;
        const char* g = xn + pgb(seg, par);
        if (hb == 0) {
            dma4(g + blin(mt * 20480 + (wave & 1) * 4096), l16, L_XA + mt * 16384 + (wave & 1) * 4096);
            dmaq<0>(g + blin(mt * 20480 + 16384) + (wave & 1) * 1024, l16, L_XB + mt * 4096 + (wave & 1) * 1024);
        } else {
            dma4(g + blin(mt * 20480 + 8192), l16, L_XA + mt * 16384 + 8192); dma4(g + blin(mt * 20480 + 12288), l16, L_XA + mt * 16384 + 12288);
            dmaq<2>(g + blin(mt * 20480 + 16384), l16, L_XB + mt * 4096); dmaq<3>(g + blin(mt * 20480 + 16384), l16, L_XB + mt * 4096);
        }
    };
    const float SCL = 0.07216878364870322f * 1.4426950408889634f;
    auto run = [&](auto Cc) {
    constexpr int CLS = decltype(Cc)::value;
    WRegs wf;
    MARK(!L && h == 0 && wave == 0, 0);
    load_w3(wf, P.w[CLS == 3], wave, h * 384, false);
    MARKX(!L && wave == 0, 1);
    PLACE();
    if (CLS == 3) {
        if (L) xn_wait(P, rr, 0, cpy);
        xdma(0, 0, par);
        if (wave == 4 || wave == 7) xdma(0, 1, par);
        wait_vm<0>();
    }
    bar();
    if (CLS == 3 && wave == 7 && L) xn_wait(P, rr, 1, cpy);
    const unsigned qs = (CLS == 3 ? L_KX : L_QX) + (wave & 1) * 3072 + ((wave >> 1) & 1) * 8;
    unsigned xa, wl, dsto;
    if (CLS == 3) { xa = L_XA + wave * 2048 + lane_id() * 16; wl = L_W + wave * 10240 + lane_id() * 16; dsto = c1_dst(wave, true); }
    __builtin_amdgcn_s_setprio(CLS == 3 ? 1 : 2);
    unsigned budget = 1u << 18, tb2 = 0;
    STDECL;
    LDECL;
#pragma unroll 1
    for (int t = 0; t < NSEG; ++t) {
        const unsigned slot = t & (NSLOT - 1);
        C1Regs C;
        {
            const WRegs& W = wf;
            ASM_ST_DECL;
            if constexpr (CLS == 3) {
                const int mt = (wave >> 1) & 1;
                const char* g = xn + pgb(t + 1 < NSEG ? t + 1 : t, par);
                asm volatile(ASM_XG_S_HI : XG_OUT ASM_ST_OUT : XG_IN, [slp] "s"(SLP4), [g0] "s"(g + blin(mt * 20480 + (wave & 1) * 4096)), [d0] "s"(L_XA + mt * 16384 + (wave & 1) * 4096),
                             [g1] "s"(g + blin(mt * 20480 + 16384) + (wave & 1) * 1024), [d1] "s"(L_XB + mt * 4096 + (wave & 1) * 1024) : XG_CLOB);
                wait_vm<0>();
                LB(t);
            } else {
                const unsigned l16 = lane_id() * 16;
                xa = L_XA + wave * 2048 + l16; wl = L_W + wave * 10240 + l16;
#define XG_Q(A) asm volatile("s_cmp_lt_u32 %[t], 2\n\ts_cbranch_scc1 .Lq3%=\n\t" A "s_branch .Lqe%=\n\t.Lq3%=:\n\t" ASM_XG_S_LO ".Lqe%=:" : XG_OUT ASM_ST_OUT : XG_IN, [t] "s"(t) : XG_CLOB)
                if constexpr (CLS == 0 || CLS == 4) XG_Q(ASM_XG_S_LO1); else XG_Q(ASM_XG_S_LO2);
                __builtin_amdgcn_s_setprio(0);
                if constexpr (CLS == 0) {
                    const int ln = lane_id();
                    const u32x4 s4 = sent4();
                    const unsigned poff = (ln * 16 + ((ln < 8) ? h * 128 : (1 + (h >> 4)) * 2 * BLK - 128 + (h & 15) * 512)) | ((ln >= 40 || t < 2) ? 0x40000000u : 0u);
#pragma unroll
                    for (int c = 0; c < (int)NCP; ++c) st128(s4, rr, poff, RG_XP + ((t - 2) & (NSLOT - 1)) * XP_SLOT + (c >> 1) * XP_PAIR + (c & 1) * BLK);
                    const unsigned pbase = RG_PX + ((t - 3) & (NSLOT - 1)) * PX_SLOT + (h >> 2) * BLK;
                    const unsigned o2 = ((ln & 16) ? 5120 + ((h >> 1) & 1) * 1024 + (h & 1) * 256 + (ln >> 5) * 512 : 4096 + (h & 3) * 256 + (ln >> 5) * 3072) + (ln & 15) * 16;
                    if (t >= 3) { st128(s4, rr, ln * 16, pbase + (h & 3) * 1024); st128(s4, rr, o2, pbase); }
                }
                if (CLS == 4 && t >= HT0) {
                    const int ln = lane_id(), u = h >> 2;
                    for (int spins = 0;; ++spins) {
                        const unsigned fl = __builtin_amdgcn_raw_buffer_load_b32(rr, (ln & 3) * 128, RG_FH + BLK + u * 512, 16);
                        if (__all(fl > (unsigned)t)) break;
                        __builtin_amdgcn_s_sleep(2);
                        if (spin_fail(spins, P)) break;
                    }
                    asm volatile("" ::: "memory");
                    const char* g = P.ws + PG0 + pgb(t, par) + PG_HV + (16 + u) * 3 * BLK;
                    unsigned t0, t1, vo;
                    static_assert(L_QX == 16384, "");
                    asm volatile("s_add_u32 %0, %4, 0\n\ts_and_b32 %1, %0, 0xe000\n\ts_add_u32 %0, %0, %1\n\tv_add_u32 %2, %0, %5\n\ts_mov_b32 m0, 16384\n\ts_nop 0\n\tglobal_load_lds_dwordx4 %2, %3 sc1\n\t"
                                 "s_add_u32 %0, %4, 1024\n\ts_and_b32 %1, %0, 0xe000\n\ts_add_u32 %0, %0, %1\n\tv_add_u32 %2, %0, %5\n\ts_mov_b32 m0, 17408\n\ts_nop 0\n\tglobal_load_lds_dwordx4 %2, %3 sc1\n\t"
                                 "s_add_u32 %0, %4, 2048\n\ts_and_b32 %1, %0, 0xe000\n\ts_add_u32 %0, %0, %1\n\tv_add_u32 %2, %0, %5\n\ts_mov_b32 m0, 19456\n\ts_nop 0\n\tglobal_load_lds_dwordx4 %2, %3 sc1\n\t"
                                 "s_add_u32 %0, %4, 6144\n\ts_and_b32 %1, %0, 0xe000\n\ts_add_u32 %0, %0, %1\n\tv_add_u32 %2, %0, %5\n\ts_mov_b32 m0, 22528\n\ts_nop 0\n\tglobal_load_lds_dwordx4 %2, %3 sc1\n\t"
                                 "s_add_u32 %0, %4, 7168\n\ts_and_b32 %1, %0, 0xe000\n\ts_add_u32 %0, %0, %1\n\tv_add_u32 %2, %0, %5\n\ts_mov_b32 m0, 23552\n\ts_nop 0\n\tglobal_load_lds_dwordx4 %2, %3 sc1\n\t"
                                 "s_add_u32 %0, %4, 8192\n\ts_and_b32 %1, %0, 0xe000\n\ts_add_u32 %0, %0, %1\n\tv_add_u32 %2, %0, %5\n\ts_mov_b32 m0, 25600\n\ts_nop 0\n\tglobal_load_lds_dwordx4 %2, %3 sc1\n\t"
                                 : "=&s"(t0), "=&s"(t1), "=&v"(vo) : "s"(g), "s"((((h >> 1) & 1) * 12 + (h & 1) * 3) * 1024), "v"(ln * 16) : "memory", "scc");
                }
                dsto = c1_dst(wave, false);
                while ((unsigned)__builtin_amdgcn_s_memtime() - tb2 < STD) __builtin_amdgcn_s_sleep(2);
            }
            ASM_ST_SAVE(7);
            ST(6);
        }
        ST(0);
        unsigned fv = 0;
        if (CLS == 2) fv = (lane_id() & 15) * 128;
        crit1<CLS == 3 ? 0 : CLS < 2 ? 3 : CLS == 2 ? 12 : 2>(wf, C, rr, RG_XM + slot * XM_SLOT + (cpy >> 1) * XM_PAIR + (cpy & 1) * BLK, wave, wl, dsto, budget, fv, RG_FX + (cpy & 3) * 2048 STARG);
        if (CLS != 3) tb2 = (unsigned)__builtin_amdgcn_s_memtime();
        if constexpr (CLS == 0) {
            const unsigned sp = RG_XP + slot * XP_SLOT, sx = sp + (1 + (h >> 4)) * 2 * BLK + (h & 15) * 512;
            ASM_ST_DECL;
            asm volatile(ASM_SM : [c0] "+s"(budget) ASM_ST_OUT : WREGS(wf), [rr] "s"(rr), [sx] "s"(sx), [sy] "s"(sx + XP_PAIR), [sm] "s"(sp + h * 128), [cs] "s"(BLK), [cp] "s"(XP_PAIR), [c70] "s"(0x70), [scl] "s"(SCL)
                         : "memory", "v200", "v201", "v202", "v203", "v204", "v205", "v206", "v207", "v208", "v209", "v210", "v211", "v212", "v213", "v214", "v215", "v216", "v217", "v218", "v219", "v220", "v221", "v222", "v223", "v224", "v225", "v226", "v227", "v228", "v229", "v230", "v231", "v232", "v233", "v234", "v235", "v236", "v237", "v238", "v239", "v240", "v241", "v242", "v243", "v244", "v245", "v246", "v247", "v248", "v249", "v250", "v251", "v252", "v253", "v254", "v255");
            ASM_ST_SAVE(4);
            ST(5);
        } else if constexpr (CLS == 1 || CLS == 4) {
            const int ln = lane_id(), l15 = ln & 15, l4 = ln >> 4;
            constexpr int mt = (CLS == 4);
            const unsigned qa = L_QX + mt * 6144 + ln * 16, kmo = L_KM + l4 * 128 + (ln & 7) * 16;
            const unsigned xo = mt ? (h & 3) * 1024 : 5120 + ((h >> 1) & 1) * 1024 + (h & 1) * 256;
            const unsigned pm = l4 < 2 ? (mt ? 4096 : 7168) + (h & 3) * 256 - xo + l15 * 16 + l4 * 8 : OOB;
            const unsigned so = RG_PX + slot * PX_SLOT + (h >> 2) * BLK + xo;
            f32x4 sd;
#pragma unroll
            for (int r = 0; r < 4; ++r) sd[r] = (l4 * 4 + r > l15) ? -3.0e38f : 0.f;
            ASM_ST_DECL;
#define P4_ARGS : "+{v[240:243]}"(sd) ASM_ST_OUT : WREGS(wf), [qa] "v"(qa), [km] "v"(kmo), [pm] "v"(pm), [rr] "s"(rr), [so] "s"(so), [scl] "s"(SCL) : "memory", "v200", "v201", "v202", "v203", "v204", "v205", "v206", "v207", "v208", "v209", "v210", "v211", "v212", "v213", "v214", "v215", "v216", "v217", "v218", "v219", "v220", "v221", "v222", "v223", "v224", "v225", "v226", "v227", "v228", "v229", "v230", "v231", "v232", "v233", "v234", "v235", "v236", "v237", "v238", "v239", "v244", "v245", "v246", "v247", "v248", "v249", "v250", "v251"
            if constexpr (mt) asm volatile(ASM_P4_1 P4_ARGS); else asm volatile(ASM_P4_0 P4_ARGS);
            ASM_ST_SAVE(4);
            ST(5);
        } else {
            bar();
            ST(4);
            if (CLS == 2 && L && t + 2 < NSEG && !__all(fv > (unsigned)t + 2)) xn_wait(P, rr, t + 2, cpy);
            if (CLS == 2) { int tt = t; asm volatile("" : "+s"(tt)); if (tt + 2 == XS0) init_wait(P, h, 1); }
            if (CLS == 3) LA();
            {
                if (CLS == 3 && (wave == 4 || wave == 7)) {
                    if (t + 1 < NSEG) xdma(t + 1, 1, par);
                }
                ST(5);
            }
        }
        STFLUSH(t);
    }
    LF();
    };
    if (wave == 0) run(std::integral_constant<int, 0>{});
    else if (wave == 1) run(std::integral_constant<int, 1>{});
    else if (wave == 2) run(std::integral_constant<int, 4>{});
    else if (wave == 3) run(std::integral_constant<int, 2>{});
    else run(std::integral_constant<int, 3>{});
}

#undef STROLE
#define STROLE 8
__device__ __forceinline__ void h_role(const Params& P, int wave, int L, int i, int x) {
    const rsrc_t rp = mkrsrc(P.ws + PG0, PG_L);
    const char* xn = P.ws + PG0 + PG_XN + L * PG_LX;
    auto run = [&](auto Cc) {
    constexpr bool HI = decltype(Cc)::value;
    const int mt = (wave >> 1) & 1;
    WRegs wf;
    load_w3(wf, i < 16 ? P.w[2] : P.w[0], wave, (i < 16 ? i * 2 + HI : (i - 16) * 4 + HI * 2) * 384, i >= 16);
    PLACE();
    auto xdma = [&](int seg, int hb) {
        const unsigned l16 = lane_id() * 16;
        const char* g = xn + pgb(seg, par);
        dma4(g + blin(mt * 20480 + hb * 8192 + (wave & 1) * 4096), l16, L_XA + mt * 16384 + hb * 8192 + (wave & 1) * 4096);
        dmaq<0>(g + blin(mt * 20480 + 16384) + hb * 2048 + (wave & 1) * 1024, l16, L_XB + mt * 4096 + hb * 2048 + (wave & 1) * 1024);
    };
    const unsigned qs = (HI ? L_KX : L_QX) + (wave & 1) * 3072 + mt * 8;
    const unsigned xa = L_XA + wave * 2048 + lane_id() * 16, wl = L_W + wave * 10240 + lane_id() * 16;
    const unsigned fp = RG_FH + (i >> 4) * BLK + ((i & 15) * 4 + (wave & 3)) * 128;
    const unsigned fb = L ? RG_FX + (cpy & 3) * 2048 : RG_FV + (i & 3) * 2048, fa = L ? 0 : HAH;
    unsigned go = 0, all = 0;
    STDECL;
    LDECL;
    for (int t = HT0; t <= NSEG; ++t) {
        ST(0);
        if constexpr (HI) {
            const unsigned l16 = lane_id() * 16;
            if (t > HT0) {
                u32x4 v[6];
                const int w4 = wave - 4;
#pragma unroll
                for (int k = 0; k < 6; ++k) v[k] = lds_ld<u32x4>(L_QX + ((k >> 1) * 8 + w4 * 2 + (k & 1)) * 1024 + l16);
                const unsigned so = pgb(t - 1, par) + PG_HV + i * 3 * BLK + w4 * 2048;
#pragma unroll
                for (int k = 0; k < 6; ++k) st128(v[k], rp, l16 + (k & 1) * 1024, so + (k >> 1) * BLK);
                if (!all) {
                    wait_vm<0>();
                    if (l16 < 512) __builtin_amdgcn_raw_buffer_store_b32(t, rr, l16 >> 2, fp, 16);
                }
            }
            ST(1);
            if (t == NSEG) break;
            if (!all) bar();
            xdma(t, 1);
        } else {
            if (t == NSEG) break;
            if (!go) {
                for (int spins = 0;; ++spins) {
                    if (__all(__builtin_amdgcn_raw_buffer_load_b32(rr, (lane_id() & 15) * 128, fb, 16) + fa > (unsigned)t)) break;
                    __builtin_amdgcn_s_sleep(HSL);
                    if (spin_fail(spins, P)) break;
                }
                asm volatile("" ::: "memory");
                xdma(t, 0); wait_vm<0>();
            }
            if (!all) bar();
        }
        ST(2);
        C1Regs C;
        const WRegs& W = wf;
        ASM_ST_DECL;
        if constexpr (HI) {
            const unsigned l4 = lane_id() * 4;
            asm volatile(ASM_XG_H_HI : XG_OUT ASM_ST_OUT : XG_IN, [rr] "s"(rr), [fp] "s"(fp), [tv] "s"(t), [fv] "v"((all && l4 < 128) ? l4 : OOB) : XG_CLOB);
        } else {
            asm volatile(ASM_XG_H_LO : XG_OUT ASM_ST_OUT : XG_IN : XG_CLOB);
            if (t + 1 == XS0) init_wait(P, wave, 1);
            const unsigned fv = __builtin_amdgcn_raw_buffer_load_b32(rr, (lane_id() & 15) * 128, fb, 16) + fa;
            go = __builtin_amdgcn_readfirstlane((t + 1 < NSEG && __all(fv > (unsigned)t + 1)) ? 1u : 0u);
            if (go) xdma(t + 1, 0);
            lds_st<unsigned>(L_MEM + wave * 4, go);
            wait_vm<0>();
        }
        ASM_ST_SAVE(3);
        ST(4);
        if (!HI) { LB(t); LA(); }
        PMARK(L && wave == 7, 6, t);
        bar();
        const u32x4 gv = lds_ld<u32x4>(L_MEM);
        all = __builtin_amdgcn_readfirstlane(gv[0] & gv[1] & gv[2] & gv[3]);
        ST(5);
        STFLUSH(t);
    }
    LF();
    };
    if (wave < 4) run(std::false_type{}); else run(std::true_type{});
}

#undef STROLE
#define STROLE 2
__device__ __forceinline__ void v_role(const Params& P, int wave, int L, int h, int x) {
    WRegs wf;
    STDECL;
    auto runv = [&](auto Cc) {
        load_w3(wf, P.w[2], wave, h * 384, false);
        MARKX(!L && wave == 0, 6);
        PLACE();
        unsigned budget = 1u << 18;
        const unsigned wl = L_W + wave * 10240 + lane_id() * 16, dsto = c1_dst(wave, false);
        auto vfetch = [&](int seg, int tries) {
            const int ln = lane_id();
            for (int spins = 0; tries != -2; ++spins) {
                if (spins == tries) return false;
                const unsigned f = __builtin_amdgcn_raw_buffer_load_b32(rr, (ln & 3) * 128, RG_FH + (h >> 1) * 512, 16);
                if (__all(f > (unsigned)seg)) break;
                __builtin_amdgcn_s_sleep(4);
                if (spin_fail(spins, P)) break;
            }
            asm volatile("" ::: "memory");
            const char* g = P.ws + PG0 + pgb(seg, par) + PG_HV + (h >> 1) * 3 * BLK;
#pragma unroll
            for (int k = 0; k < 3; ++k) {
                const int f = (h & 1) * 12 + wave * 3 + k;
                dmaq<0>(g + (f >> 3) * BLK + (f & 7) * 1024, ln * 16, L_QX + (seg & 1) * 12288 + (wave * 3 + k) * 1024);
            }
            return true;
        };
#pragma unroll 1
        for (int t = 0; t < HT0; ++t) {
            if (L) xn_wait(P, rr, t, cpy);
            const unsigned l16 = lane_id() * 16;
            const char* g = P.ws + PG0 + PG_XN + L * PG_LX + pgb(t, par);
            dma4(g + blin(wave * 4096), l16, L_XA + wave * 4096); dma4(g + blin(20480 + wave * 4096), l16, L_XA + 16384 + wave * 4096);
            dmaq<0>(g + blin(16384) + wave * 1024, l16, L_XB + wave * 1024); dmaq<0>(g + blin(20480 + 16384) + wave * 1024, l16, L_XB + 4096 + wave * 1024);
            wait_vm<0>();
            bar();
            C1Regs C;
            const WRegs& W = wf;
            const unsigned xa = L_XA + wave * 2048 + l16, qs = L_QX + (wave & 1) * 3072 + ((wave >> 1) & 1) * 8 + t * 12288;
            asm volatile(ASM_XG_V : XG_OUT : XG_IN : XG_CLOB);
            if (t == 0) bar();
        }
        bool seen = false;
        unsigned tb2 = 0;
        for (int t = 0; t < NSEG; ++t) {
            if (t + 1 - V1A >= HT0 && t + 1 - V1A < NSEG) { vfetch(t + 1 - V1A, seen ? -2 : -1); wait_vm<0>(); }
            ST(7);
            bar();
            C1Regs C;
            asm volatile("ds_read_b128 v[200:203], %4\n\tds_read_b128 v[204:207], %4 offset:1024\n\tds_read_b128 v[228:231], %4 offset:2048\n\tds_read_b128 v[232:235], %4 offset:3072\n\t"
                         "v_mov_b64 v[224:225], 0\n\tv_mov_b64 v[226:227], 0\n\tv_mov_b64 v[244:245], 0\n\tv_mov_b64 v[246:247], 0\n\tv_mov_b64 v[248:249], 0\n\tv_mov_b64 v[250:251], 0\n\ts_waitcnt lgkmcnt(0)"
                         : XG_OUT : "v"(wl) : "memory");
            ST(6);
            while ((unsigned)__builtin_amdgcn_s_memtime() - tb2 < VTD) __builtin_amdgcn_s_sleep(1);
            unsigned fv = (lane_id() & 3) * 128;
            crit1<10 + decltype(Cc)::value>(wf, C, rr, RG_XM + (t & (NSLOT - 1)) * XM_SLOT + (cpy >> 1) * XM_PAIR + (cpy & 1) * BLK, wave, wl, dsto, budget, fv, RG_FH + (h >> 1) * 512 STARG);
            __builtin_amdgcn_s_setprio(0);
            bar();
            seen = __all(fv > (unsigned)t + 2 - V1A);
            tb2 = (unsigned)__builtin_amdgcn_s_memtime();
            ST(4);
            STC(5);
            STFLUSH(t);
        }
    };
    if (wave < 2) runv(std::integral_constant<int, 3>{});
    else if (wave < 4) runv(std::integral_constant<int, 2>{});
    else {
        const int w4 = wave - 4;
        const rsrc_t rd = mkrsrc(P.ws + PG0, PG_L);
        load_wo(wf, P.w[3], wave, h);
        PLACE();
        bar();
        bar();
        for (int t = -1; t < NSEG; ++t) {
            const unsigned slot = t & (NSLOT - 1);
            bar();
            ST(7);
#pragma unroll 1
            for (int n = V1A ? t + (t <= 0) : t + (t != 1) + (t == 0); n <= (V1A && t >= 0 ? t : t + 1) && n < NSEG; ++n) {
                const unsigned l16 = lane_id() * 16;
                const unsigned wl = L_W + wave * 10240 + l16, va = L_QX + (n & 1) * 12288 + l16;
                unsigned so = ((n & 7) << 22) + pgb(64 + (w4 >> 1), par) + ((w4 & 1) * 40 + (h >> 3)) * BLK + (h & 7) * 1024;
                asm volatile(ASM_VOX : [so] "+s"(so) : WREGS(wf), [l16] "v"(l16), [wl] "v"(wl), [va] "v"(va), [rd] "s"(rd) : "memory", "scc", "v200", "v201", "v202", "v203", "v204", "v205", "v206", "v207", "v208", "v209", "v210", "v211", "v212", "v213", "v214", "v215", "v216", "v217", "v218", "v219", "v220", "v221", "v222", "v223", "v224", "v225", "v226", "v227", "v228", "v229", "v230", "v231", "v232", "v233", "v234", "v235", "v236", "v237", "v238", "v239", "v240", "v241", "v242", "v243", "v244", "v245", "v246", "v247", "v248", "v249", "v250", "v251");
                if (V1A || n < 2) {
                    wait_vm<0>();
                    const unsigned l4 = lane_id() * 4;
                    if (l4 < 128) __builtin_amdgcn_raw_buffer_store_b32(n + 1, rr, l4, RG_FV + (w4 >> 1) * BLK + ((w4 & 1) * 32 + h) * 128, 16);
                }
            }
            if (t < 0) continue;
            ST(0);
            unsigned wl = L_W + wave * 10240 + lane_id() * 16;
            bf16x16 lw = lds_frag(wl, 1024);
            u32x32 cz;
            asm volatile("v_mov_b64 v[220:221], 0\n\tv_mov_b64 v[222:223], 0\n\tv_mov_b64 v[224:225], 0\n\tv_mov_b64 v[226:227], 0\n\tv_mov_b64 v[228:229], 0\n\tv_mov_b64 v[230:231], 0\n\tv_mov_b64 v[232:233], 0\n\tv_mov_b64 v[234:235], 0\n\tv_mov_b64 v[236:237], 0\n\tv_mov_b64 v[238:239], 0\n\tv_mov_b64 v[240:241], 0\n\tv_mov_b64 v[242:243], 0\n\tv_mov_b64 v[244:245], 0\n\tv_mov_b64 v[246:247], 0\n\tv_mov_b64 v[248:249], 0\n\tv_mov_b64 v[250:251], 0" : "={v[220:251]}"(cz));
            unsigned vo = ((wl >> 8) & 1) * (1 + (w4 & 1) * 2) * BLK + ((wl >> 9) & 1) * 1024 + (wl & 255);
            asm volatile("" : "+{v[212:219]}"(lw), "+{v[220:251]}"(cz), "+v"(wl), "+v"(vo));
            unsigned l16 = wl & 1023u;
            bar();
            wait_vm<0>();
            if (!V1A && l16 < 512 && t && t + 1 < NSEG) __builtin_amdgcn_raw_buffer_store_b32(t + 2, rr, l16 >> 2, RG_FV + (w4 >> 1) * BLK + ((w4 & 1) * 32 + h) * 128, 16);
            ST(1);
            const int sidx = (wl & 128u) ? 0xEEEE : 0x4444;
            unsigned so = RG_VM + slot * VM_SLOT + (unsigned)((w4 * 10 - (w4 & 1) + (h >> 4) * 2) * BLK + ((h >> 2) & 3) * 2048 + (h & 3) * 256);
            ST(3);
            ASM_ST_DECL;
            asm volatile(ASM_CRIT2 : [so] "+s"(so), "+{v[212:219]}"(lw), "+{v200}"(l16), "+{v[220:251]}"(cz) ASM_ST_OUT
                         : WREGS(wf), [wl] "v"(wl), [vo] "v"(vo), [sidx] "v"(sidx), [rr] "s"(rr)
                         : "memory", "scc", "v201", "v202", "v203", "v204", "v205", "v206", "v207", "v208", "v209", "v210", "v211");
            ASM_ST_SAVE(4);
            ST(5);
            STFLUSH(t);
        }
    }
}

#undef STROLE
#define STROLE 4
constexpr unsigned R_RED = 0;
constexpr unsigned R_PART = 16384;
__device__ __forceinline__ void r_role(const Params& P, int wave, int L, int r5, int x) {
    PLACE();
    const int jj = (r5 * 4 + cpy) ^ (r5 * 4 + x >= 6 ? 1 : 0);
    const int lane = lane_id(), l15 = lane & 15, l4 = lane >> 4;
    const unsigned l16 = lane * 16;
    const rsrc_t rd = mkrsrc(P.ws + PG0, PG_L);
    const int sidx = (l15 < 8) ? 0x4444 : 0xEEEE;
    unsigned offp[3];
    offp[0] = RG_XP + (cpy >> 1) * XP_PAIR + (cpy & 1) * BLK + (1 + (wave >> 2)) * 2 * BLK + (wave & 3) * 2048 + (PLAY ? (l4 >> 1) * 512 + (((l15 >> 2) & 1) + (l15 & 2)) * 128 + (l4 & 1) * 64 + (l15 >> 3) * 32 + (l15 & 1) * 16 : l16);
    offp[2] = (l15 < 8) ? RG_XP + (cpy >> 1) * XP_PAIR + (cpy & 1) * BLK + wave * 512 + l4 * 128 + l15 * 16 : OOB;
    const unsigned offv = RG_VM + ((jj >> 1) * 4 + (wave >> 2) * 2 + (jj & 1)) * BLK + (wave & 3) * 2048 + l16;
    const int j = jj * 2 + ((wave >> 1) & 1), fl = (wave & 1) * 32 + (lane & 31);
    const unsigned offo = RG_XM + (wave >> 2) * XM_PAIR + (lane >> 5) * BLK + (j & 31) * 256 + (j >> 5) * 2 * BLK + fl * 4;
    MARK(!L && jj == 0 && wave == 0, 5);
    {
        const int row = (lane >> 2) & 7, col = 16 * j + 4 * (lane & 3) + 2 * (wave & 1);
        __builtin_amdgcn_raw_buffer_store_b32(nosent(*(const unsigned*)(P.mem0 + row * DM + col)), rr, offo, 0, 16);
    }
    auto red_addr = [](int fl) { return R_RED + fl * 80 + (fl >> 4) * 16; };
    const unsigned rw0 = (lane >> 5) * 5248 + red_addr((((l15 >> 1) & 1) * 8 + 4 * (l4 & 1)) * 4 + (l15 >> 2)) + (l15 & 1) * 4 + (wave ^ (l15 >> 3)) * 8;
    const unsigned rrd = ((wave >> 1) & 1) * 5248 + red_addr(fl) + (lane >> 5) * 32;
    u32x32 vx;
    const unsigned fo = (lane & 3) * 128, fs = RG_FV + (jj / 10) * BLK + (((jj / 5) & 1) * 32 + wave * 4) * 128, vxo = pgb(64 + jj / 10, par) + (unsigned)((jj % 10) * 8 + (wave >> 1)) * BLK + (wave & 1) * 4096;
    const rsrc_t rp = mkrsrc(P.ws + PG0, PG_L);
    const unsigned xo = (l4 >> 1) * 256 + l15 * 16 + (l4 & 1) * 8, pw = R_PART + wave * 1024 + l16, pr = R_PART + (wave & 3) * 8192 + l16;
    const int fin = wave >> 2;
    asm volatile("" : "={v[128:159]}"(vx));
    auto vx_fetch = [&](int t) {
        for (int spins = 0;; ++spins) {
            const unsigned f = __builtin_amdgcn_raw_buffer_load_b32(rr, fo, fs, 16);
            if (__all(f > (unsigned)t)) break;
            __builtin_amdgcn_s_sleep(2);
            if (spin_fail(spins, P)) break;
        }
        const unsigned sv = ((t & 7) << 22) + vxo;
        asm volatile("buffer_load_dwordx4 v[128:131], %1, %2, %3 offen sc1\n\tbuffer_load_dwordx4 v[132:135], %1, %2, %3 offen offset:1024 sc1\n\t"
                     "buffer_load_dwordx4 v[136:139], %1, %2, %3 offen offset:2048 sc1\n\tbuffer_load_dwordx4 v[140:143], %1, %2, %3 offen offset:3072 sc1\n\t"
                     "buffer_load_dwordx4 v[144:147], %1, %2, %4 offen sc1\n\tbuffer_load_dwordx4 v[148:151], %1, %2, %4 offen offset:1024 sc1\n\t"
                     "buffer_load_dwordx4 v[152:155], %1, %2, %4 offen offset:2048 sc1\n\tbuffer_load_dwordx4 v[156:159], %1, %2, %4 offen offset:3072 sc1"
                     : "+{v[128:159]}"(vx) : "v"(l16), "s"(rp), "s"(sv), "s"(sv + 4 * BLK) : "memory");
    };
    vx_fetch(0);
    unsigned budget = 1u << 18, tpub = 0;
    STDECL;
    LDECL;
    for (int t = 0; t < NSEG; ++t) {
        const unsigned slot = t & (NSLOT - 1);
        ST(0);
        while ((unsigned)__builtin_amdgcn_s_memtime() - tpub < RTD) __builtin_amdgcn_s_sleep(1);
        u32x8 vm, pv;
        u32x4 pm;
        {
            unsigned a;
            ASM_ST_DECL;
            asm volatile(ASM_RCRIT : "=&{v[176:183]}"(vm), "=&{v[184:191]}"(pv), "=&{v[192:195]}"(pm), [a] "=&s"(a), [n] "+s"(budget) ASM_ST_OUT ASM_ST2_OUT ASM_ST3_OUT
                         : [rs] "s"(rr), [ov] "v"(offv), [sv] "s"(slot * VM_SLOT), [o0] "v"(offp[0]), [o2] "v"(offp[2]), [sp] "s"(slot * XP_SLOT),
                           "{v[128:159]}"(vx), [sidx] "v"(sidx), [rw] "v"(rw0), [rd] "v"(rrd), [oo] "v"(offo),
                           [so] "s"(((t + 1) & (NSLOT - 1)) * XM_SLOT), [pub] "s"(NSEG - 1 - t)
                         : "memory", "vcc", "scc", "v160", "v161", "v162", "v163", "v164", "v165", "v166", "v167", "v196", "v197", "v200", "v201", "v202", "v203", "v204", "v205", "v206", "v207");
            ASM_ST_SAVE(2); ASM_ST2_SAVE(1); ASM_ST3_SAVE(6);
        }
        ST(3);
        LA();
        MARK(!L && jj == 0 && wave == 0 && t < 4, 10 + t);
        PMARK(wave == 0, L * 7, t - 1 + L);
        tpub = (unsigned)__builtin_amdgcn_s_memtime();
        __builtin_amdgcn_s_sleep(RXDEL);
        unsigned early;
#pragma unroll 1
        for (int spins = 1023;; spins += 1024) {
            unsigned n;
            const unsigned sx = pgb(t, par) + blin(((wave >> 1) & 1) * 20480 + jj * 1024) + (wave & 1) * 512;
            const unsigned pxb = RG_PX + slot * PX_SLOT + wave * BLK, sv = (((t + 1) & 7) << 22) + vxo;
            ASM_ST_DECL;
            asm volatile(ASM_RX : "+{v[128:159]}"(vx), [n] "=&s"(n), [a] "=&s"(early) ASM_ST_OUT ASM_ST2_OUT
                         : "{v[176:183]}"(vm), "{v[184:191]}"(pv), "{v[192:195]}"(pm), [l16] "v"(l16), [rs] "s"(rr), [s0] "s"(pxb), [s1] "s"(pxb + 4096), [rd] "s"(rd), [rx] "s"(rd), [xo] "v"(xo),
                           [fo] "v"(fo), [fs] "s"(fs), [tn] "s"(t + 1), [sv0] "s"(sv), [sv1] "s"(sv + 4 * BLK), [rp] "s"(rp), [sx] "s"(PG_XR + L * PG_LX + sx), [sa] "s"(PG_XA + sx),
                           [pw] "v"(pw), [pr] "v"(pr), [fin] "s"(fin)
                         : "memory", "vcc", "scc", "v96", "v97", "v98", "v99", "v100", "v101", "v102", "v103", "v104", "v105", "v106", "v107", "v108", "v109", "v110", "v111", "v112", "v113", "v114", "v115", "v116", "v117", "v118", "v119", "v120", "v121", "v122", "v123", "v124", "v125", "v126", "v127", "v196", "v200", "v201", "v202", "v203", "v204", "v205", "v206", "v207", "v208", "v209", "v210", "v211", "v212", "v213", "v214", "v215", "v216", "v217", "v218", "v219", "v220", "v221", "v222", "v223", "v224", "v225", "v226", "v227", "v228", "v229", "v230", "v231", "v240", "v241", "v242", "v243", "v248", "v250", "v251");
            ASM_ST_SAVE(4); ASM_ST2_SAVE(5);
            if (__builtin_amdgcn_readfirstlane(n)) break;
            early = 0;
            if (spin_fail(spins, P)) { bar(); break; }
        }
        if (t + 1 < NSEG && !__builtin_amdgcn_readfirstlane(early)) vx_fetch(t + 1);
        PMARK(!L && wave == 7 && PMK == 1, 1, t - 1);
        st128(sent4(), rr, offv, slot * VM_SLOT); st128(sent4(), rr, offv + 1024, slot * VM_SLOT);
        __builtin_amdgcn_raw_buffer_store_b32(SENT, rr, offo, slot * XM_SLOT, 16);
        if (wave < 2 && t + AH < NSEG) st128(sent4(), rd, l16, PG_XA + pgb(t + AH, par) + blin(wave * 20480 + jj * 1024));
        ST(7);
        LB(t);
        STFLUSH(t);
    }
    LF();
}

#undef STROLE
#define STROLE 16
template <int WHICH>
__device__ __forceinline__ void w_role(const Params& P, int wave, int L, int cu, int x) {
    constexpr int KS = 20, N = (WHICH == 3) ? FF : DM, NU = 2 * NSEG;
    constexpr unsigned D_IN = WHICH == 1 ? PG_XA : WHICH == 2 ? PG_HS : PG_X1;
    constexpr unsigned D_OUT = WHICH == 1 ? PG_HS : WHICH == 2 ? PG_X1 : PG_H2;
    if (WHICH != 3 && cu * 8 + wave >= 20) {
        for (int u = 0; u <= NU; ++u) bar();
        return;
    }
    auto run = [&](auto Cc) {
    constexpr bool LO = decltype(Cc)::value;
    const int ln = lane_id(), l15 = ln & 15, l4 = ln >> 4;
    const unsigned l16 = ln * 16;
    const rsrc_t rd = mkrsrc(P.ws + PG0, PG_L);
    const int nt0 = (cu * 8 + wave) * 2;
    u32x32 w[5];
    {
        u32x4 f[2 * KS];
        load_wn<2, KS, N>(f, P.w[3 + WHICH], DM, wave, nt0, 0);
#pragma unroll
        for (int n = 0; n < 5; ++n) {
            u32x8 q[4];
#pragma unroll
            for (int i = 0; i < 4; ++i) q[i] = __builtin_shufflevector(f[n * 8 + 2 * i], f[n * 8 + 2 * i + 1], 0, 1, 2, 3, 4, 5, 6, 7);
            w[n] = cat32(q[0], q[1], q[2], q[3]);
        }
        asm volatile("" : "+{v[0:31]}"(w[0]), "+{v[32:63]}"(w[1]), "+{v[64:95]}"(w[2]), "+{v[96:127]}"(w[3]), "+{v[128:159]}"(w[4]));
    }
    PLACE();
    bar();
    const int ocol = nt0 * 16 + (l4 & 1) * 16 + (l4 >> 1) * 8;
    const unsigned o3 = (unsigned)((l4 & 1) * 512 + (l4 >> 1) * 256 + l15 * 16), ro = (l4 >> 1) * 256 + l15 * 16 + (l4 & 1) * 8;
    const int grp = nt0 / 10, sh = ((grp & 1) ? -1 : 1) * ((grp >> 1) + 1);
    if (WHICH == 1) {
        unsigned zv;
        asm volatile("v_mov_b32 %0, 0" : "=v"(zv));
        st128(((u32x4){zv, zv, zv, zv}), rd, l15 <= (grp >> 1) ? toff((grp & 1) ? S_ - 1 - l15 : l15, ocol, par) : OOB, PG_HS);
    }
    auto in0 = [&](int u) { return D_IN + pgb(u >> 1, par) + blin((u & 1) * 20480 + wave * 4096); };
    auto in1 = [&](int u) { return D_IN + pgb(u >> 1, par) + blin((u & 1) * 20480 + 16384) + wave * 1024; };
    u32x16 va;
    u32x4 vb;
    u32x8 cs, k8;
#define WT_W "{v[0:31]}"(w[0]), "{v[32:63]}"(w[1]), "{v[64:95]}"(w[2]), "{v[96:127]}"(w[3]), "{v[128:159]}"(w[4])
    asm volatile("v_mov_b32 v216, 0x3f803f80\n\tv_mov_b32 v217, v216\n\tv_mov_b32 v218, v216\n\tv_mov_b32 v219, v216\n\tv_mov_b32 v220, %[c0]\n\tv_mov_b32 v232, -1\n\tv_mov_b32 v236, -1\n\t"
                 "v_mov_b32 v240, -1\n\tv_mov_b32 v244, -1\n\tv_mov_b32 v248, -1\n\t" ASM_WCS
                 : "=&{v[196:203]}"(cs), "=&{v[216:223]}"(k8), "=&{v[232:247]}"(va), "=&{v[248:251]}"(vb) : WT_W, [c0] "s"(-2.0f * 0.7978845608028654f * 1.4426950408889634f));
    unsigned la = l16, lw = wave * 8192 + l16, sr = l4 * 8192 + ((l15 >> 2) * 16 + l15) * 16 + (l15 & 3) * 4, budget = 1u << 18;
    wait_vm<0>();
    STDECL;
    LDECL;
#pragma unroll 1
    for (int u = 0; u < NU; ++u) {
        const int mt = u & 1;
        const unsigned pg = pgb(u >> 1, par), c3 = D_OUT + blin(mt * (N * 32) + (nt0 >> 1) * 1024);
        unsigned oa = o3, ob = 0, so = pg + c3, sa = u + 2 * AH < NU ? pgb((u >> 1) + AH, par) + c3 : pg + PG_SS + 4096, a;
        if (WHICH == 1) {
            const int orow = u * 16 + l15 + sh;
            ob = toff(orow + AH * SEG, ocol, par);
            oa = (unsigned)orow < (unsigned)S_ ? (ob ^ 8192u) - (AH << 21) : OOB;
            if (orow + AH * SEG >= S_ + (sh < 0 ? sh : 0)) ob = OOB;
            so = sa = D_OUT;
        }
        if (mt) { ST(0); LB(u >> 1); } else { LA(); }
        ASM_ST_DECL;
#define WT_A WT_W, [la] "v"(la), [l16] "v"(l16), [oa] "v"(oa), [rs] "s"(rd), [so] "s"(so), [sa] "s"(sa)
#define WT_G [sr] "v"(sr), [kd] "s"(1.0f / DM), [ke] "s"(1e-5f), [k1] "s"(-2.0f * 0.7978845608028654f * 1.4426950408889634f * 0.044715f)
#define WT_1 WT_G, [ob] "v"(ob), [ss] "s"(pg + PG_SS + mt * 128), [sw] "s"(nt0)
#define WT_2 [ro] "v"(ro), [sx] "s"(pg + PG_XA + blin(mt * 20480 + nt0 * 512)), [ss] "s"(pg + PG_SS + mt * 128)
#define WT_C "memory", "vcc", "scc", "v160", "v161", "v162", "v163", "v164", "v165", "v166", "v167", "v168", "v169", "v170", "v171", "v172", "v173", "v174", "v175", "v176", "v177", "v178", "v179", "v180", "v181", "v182", "v183", "v184", "v185", "v186", "v187", "v188", "v189", "v190", "v191", "v192", "v193", "v194", "v195", "v204", "v205", "v206", "v207", "v208", "v209", "v210", "v211", "v212", "v213", "v214", "v215", "v252"
#define WT_LO(A, ...) asm volatile(A : "+{v[196:203]}"(cs), "+{v[216:223]}"(k8), "+{v[232:247]}"(va), "+{v[248:251]}"(vb), [n] "+s"(budget), [a] "=&s"(a) ASM_ST_OUT ASM_ST2_OUT ASM_ST3_OUT \
                              : WT_A, __VA_ARGS__, [lw] "v"(lw), [i0] "s"(in0(u)), [i1] "s"(in1(u)), [n0] "s"(in0(u + 1)), [n1] "s"(in1(u + 1)) : WT_C)
#define WT_HI(A, ...) asm volatile(A : "+{v[196:203]}"(cs), "+{v[216:223]}"(k8) ASM_ST_OUT ASM_ST3_OUT : WT_A, __VA_ARGS__ : WT_C)
        if constexpr (LO) { if constexpr (WHICH == 1) WT_LO(ASM_WT1_LO, WT_1); else if constexpr (WHICH == 2) WT_LO(ASM_WT2_LO, WT_2); else WT_LO(ASM_WT3_LO, WT_G); }
        else { if constexpr (WHICH == 1) WT_HI(ASM_WT1_HI, WT_1); else if constexpr (WHICH == 2) WT_HI(ASM_WT2_HI, WT_2); else WT_HI(ASM_WT3_HI, WT_G); }
        la ^= 32768u; lw ^= 32768u; sr ^= 32768u;
        PMARK(!L && mt == (WHICH != 1) && (PMK == 1 || WHICH == 3), PMK == 1 ? 1 + WHICH : 1, (u >> 1) - (WHICH == 1));
    }
    wait_vm<0>();
    if constexpr (LO) asm volatile("" :: "{v[232:247]}"(va), "{v[248:251]}"(vb));
    LF();
    };
    if (wave < 4) run(std::true_type{}); else run(std::false_type{});
}

__device__ __forceinline__ void w4_role(const Params& P, int wave, int L, int cu, int x) {
    constexpr unsigned W_FL = 122880;
    const int ln = lane_id(), l15 = ln & 15, l4 = ln >> 4;
    const unsigned l16 = ln * 16;
    const rsrc_t rd = mkrsrc(P.ws + PG0, PG_L);
    lds_st<unsigned>(W_FL + wave * 4, 0u);
    STDECL;
    if (wave < 6) {
        u32x32 w[5];
        {
            u32x4 g[40];
            load_wn<5, 8, DM>(g, P.w[7], FF, wave, cu * 5, wave * 8);
#pragma unroll
            for (int n = 0; n < 5; ++n) {
                u32x8 f[4];
#pragma unroll
                for (int i = 0; i < 4; ++i) f[i] = __builtin_shufflevector(g[n * 8 + 2 * i], g[n * 8 + 2 * i + 1], 0, 1, 2, 3, 4, 5, 6, 7);
                w[n] = cat32(f[0], f[1], f[2], f[3]);
            }
            asm volatile("" : "+{v[0:31]}"(w[0]), "+{v[32:63]}"(w[1]), "+{v[64:95]}"(w[2]), "+{v[96:127]}"(w[3]), "+{v[128:159]}"(w[4]));
        }
        PLACE();
        bar();
        unsigned budget = 1u << 18;
        u32x32 av;
        asm volatile("v_mov_b32 v160, -1\n\tv_mov_b32 v164, -1\n\tv_mov_b32 v168, -1\n\tv_mov_b32 v172, -1\n\tv_mov_b32 v176, -1\n\tv_mov_b32 v180, -1\n\tv_mov_b32 v184, -1\n\tv_mov_b32 v188, -1" : "={v[160:191]}"(av));
        for (int u = 0; u < 2 * NSEG; ++u) {
            if (u & 1) ST(0);
            for (int spins = 0;; ++spins) {
                asm volatile("" ::: "memory");
                if ((int)__builtin_amdgcn_readfirstlane(lds_ld<unsigned>(W_FL + 24 + (u & 1) * 4)) + 3 >= u) break;
                __builtin_amdgcn_s_sleep(2);
                if (spin_fail(spins, P)) break;
            }
            const unsigned gb = PG_H2 + pgb(u >> 1, par) + ((u & 1) * 6 + wave) * BLK, gn = PG_H2 + pgb((u + 1) >> 1, par) + ((~u & 1) * 6 + wave) * BLK;
            ASM_ST_DECL;
            asm volatile(ASM_W4_M : [n] "+s"(budget), "+{v[160:191]}"(av) ASM_ST2_OUT
                         : "{v[0:31]}"(w[0]), "{v[32:63]}"(w[1]), "{v[64:95]}"(w[2]), "{v[96:127]}"(w[3]), "{v[128:159]}"(w[4]),
                           [ld] "v"(l16 + wave * 1024 + (u & 3) * 30720), [l16] "v"(l16), [rs] "s"(rd), [s0] "s"(gb), [s1] "s"(gb + 4096), [n0] "s"(gn), [n1] "s"(gn + 4096), [un] "s"(u + 1), [fl] "v"(W_FL + wave * 4)
                         : "memory", "vcc", "scc", "v192", "v193", "v194", "v195", "v196", "v197", "v198", "v199", "v200", "v201", "v202", "v203", "v204", "v205", "v206", "v207", "v208", "v209", "v210", "v211", "v212");
            PMARK(!L && (u & 1) && PMK == 2, 2, u >> 1);
        }
        asm volatile("s_waitcnt vmcnt(0)" :: "{v[160:191]}"(av) : "memory");
    } else {
        const int mt = wave - 6;
        PLACE();
        bar();
        const unsigned c0 = mt * 20480 + cu * 2560, o8 = (l4 >> 1) * 256 + l15 * 16 + (l4 & 1) * 8;
        unsigned budget = 1u << 18;
        u32x16 ra;
        u32x4 rb;
        asm volatile("v_mov_b32 v164, -1" : "={v[164:179]}"(ra), "={v[180:183]}"(rb));
        __builtin_amdgcn_s_setprio(W4P);
#define W4F_OUT "+{v[164:179]}"(ra), "+{v[180:183]}"(rb), [n] "+s"(budget)
#define W4F_IN [o0] "v"(o8 + blin(c0)), [o1] "v"(o8 + blin(c0 + 512)), [o2] "v"(o8 + blin(c0 + 1024)), [o3] "v"(o8 + blin(c0 + 1536)), [o4] "v"(o8 + blin(c0 + 2048)), [rd] "s"(rd), \
               [c1] "s"(PG_X1 + pg), [ca] "s"(PG_XA + pg), [n1] "s"(PG_X1 + pn), [na] "s"(PG_XA + pn), [f0] "v"(W_FL + (ln & 3) * 4), [f1] "v"(W_FL + 16 + (ln & 1) * 4), [fd] "v"(W_FL + wave * 4), \
               [la] "v"((u & 3) * 30720 + l16), [u] "s"(u), [un] "s"(u + 1)
#define W4F_CLOB "memory", "vcc", "scc", "v64", "v65", "v66", "v67", "v68", "v69", "v70", "v71", "v72", "v73", "v74", "v75", "v76", "v77", "v78", "v79", "v80", "v81", "v82", "v83", "v84", "v85", "v86", "v87", "v88", "v89", "v90", "v91", "v92", "v93", "v94", "v95", "v96", "v97", "v98", "v99", "v100", "v101", "v102", "v103", "v104", "v105", "v106", "v107", "v108", "v109", "v110", "v111", "v112", "v113", "v114", "v115", "v116", "v117", "v118", "v119", "v120", "v121", "v122", "v123", "v124", "v125", "v126", "v127", "v128", "v129", "v130", "v131", "v132", "v133", "v134", "v135", "v136", "v137", "v138", "v139", "v140", "v141", "v142", "v143", "v144", "v145", "v146", "v147", "v148", "v149", "v150", "v151", "v152", "v153", "v154", "v155", "v156", "v157", "v158", "v159", "v160", "v161", "v162", "v163", "v184", "v185", "v186", "v187", "v188", "v189", "v190", "v191", "v192", "v193", "v194", "v195", "v196", "v197", "v198", "v199", "v200"
        if (L) {
            const rsrc_t ro = mkrsrc(P.out, ACT);
#pragma unroll 1
            for (int t = 0; t < NSEG; ++t) {
                const unsigned u = t * 2 + mt, pg = pgb(t, par), pn = pgb(t + 1, par);
                ASM_ST_DECL;
                asm volatile(ASM_W4F_1 : W4F_OUT ASM_ST2_OUT : W4F_IN, [pa] "v"(l15 * (DM * 2) + (l4 & 1) * 32 + (l4 >> 1) * 16), [pb] "v"(l15 * (DM * 2) + l4 * 8), [ro] "s"(ro), [so] "s"(u * 20480 + cu * 160) : W4F_CLOB);
            }
        } else {
            const rsrc_t rn = mkrsrc(P.ws + (par ^ 1) * 8192, RG_L);
#pragma unroll 1
            for (int t = 0; t < NSEG; ++t) {
                const unsigned u = t * 2 + mt, pg = pgb(t, par), pn = pgb(t + 1, par), sn = (pg ^ 8192) + PG_LX, st = RG_ST + (u >> 3) * BLK + (u & 7) * 1024;
                ASM_ST_DECL;
                asm volatile(ASM_W4F_0 : W4F_OUT ASM_ST_OUT ASM_ST2_OUT ASM_ST3_OUT
                             : W4F_IN, [pa] "v"(blin(c0 + (l4 & 1) * 512) + (l4 >> 1) * 256 + l15 * 16), [pb] "v"(blin(c0 + 1024 + (l4 & 1) * 512) + (l4 >> 1) * 256 + l15 * 16),
                               [so] "v"(ln < 16 ? ln * 8 : OOB), [si] "v"(ln * 8), [rr] "s"(rr), [sp] "s"(st + cu * 128), [sq] "s"(st), [xr] "s"(PG_XR + sn), [xn] "s"(PG_XN + sn),
                               [kd] "s"(1.0f / DM), [ke] "s"(1e-5f), [tn] "s"(t + 1), [fo] "v"(ln < 32 ? (ln >> 3) * 2048 + (ln & 7) * 16 : OOB), [rn] "s"(rn), [fs] "s"(RG_FX + (cu * 2 + mt) * 128) : W4F_CLOB);
            }
        }
        asm volatile("s_waitcnt vmcnt(0)" :: "{v[164:179]}"(ra), "{v[180:183]}"(rb) : "memory");
    }
}

__device__ __forceinline__ unsigned probe(char* ws, unsigned base, unsigned bit, int n) {
    const rsrc_t r = mkrsrc(ws, RG_SLOT);
    unsigned m0 = ~0u, m1 = ~0u;
    for (int i = 0; i < n; ++i) {
        const unsigned long long t0 = __builtin_amdgcn_s_memtime();
        __builtin_amdgcn_raw_buffer_store_b32(0u, r, 96 * BLK + base + (i & 1) * bit + ((i >> 2) & 1) * 4096 + (i >> 2) * 768, 0, 16);
        asm volatile("s_waitcnt vmcnt(0)" ::: "memory");
        const unsigned d = (unsigned)(__builtin_amdgcn_s_memtime() - t0);
        if (i & 1) m1 = d < m1 ? d : m1; else m0 = d < m0 ? d : m0;
    }
    return m0 > m1 ? 1u : 0u;
}
__device__ __forceinline__ void setup(const Params& P, int wave, int mt, int par0, int ph) {
    const bool xw = wave < 4 && (mt >= 2 * XS0) == (ph != 0);
    const int tid = threadIdx.x & 255, r15 = tid & 15, slot = tid >> 4;
    const rsrc_t rw = mkrsrc(P.ws, WS_END);
    u32x4 v[5];
    float s = 0.f, q = 0.f;
    if (xw) {
#pragma unroll
        for (int c = 0; c < 5; ++c) v[c] = *(const u32x4*)(P.x + (size_t)(mt * 16 + r15) * DM + (slot * 5 + c) * 8);
#pragma unroll
        for (int c = 0; c < 5; ++c)
#pragma unroll
            for (int e = 0; e < 4; ++e) s += lo2f(v[c][e]) + hi2f(v[c][e]);
        lds_st<float>((slot * 17 + r15) * 4, s);
    } else if (wave >= 4) {
        constexpr unsigned N0 = RG_ST / BLK, N1 = 4 * N0 + 16, N2 = PG_SS / BLK;
        const u32x4 s4 = sent4();
        for (unsigned k = mt; k < 2 * (N1 + 4 * N2); k += 128) {
            const unsigned i = k >> 1;
            const unsigned o = i < N1 ? (i < 4 * N0 ? (i / N0) * RG_SLOT + (i % N0) * BLK : (i - 3 * N0) * BLK) + (par0 ^ ph) * 8192 : PG0 + pgb((i - N1) / N2, par0 ^ ph) + ((i - N1) % N2) * BLK;
            st128(s4, rw, tid * 16, o + (k & 1) * 4096);
        }
        if (ph == 0 && mt >= 108) st128(~s4, rw, tid * 16, RG_FV + (mt - 108) * 4096);
    }
    bar();
    float mean = 0.f;
    if (xw) {
        s = 0.f;
#pragma unroll
        for (int k = 0; k < 16; ++k) s += lds_ld<float>((k * 17 + r15) * 4);
        mean = s * (1.0f / DM);
#pragma unroll
        for (int c = 0; c < 5; ++c)
#pragma unroll
            for (int e = 0; e < 4; ++e) { const float a = lo2f(v[c][e]) - mean, b = hi2f(v[c][e]) - mean; q += a * a + b * b; }
        lds_st<float>(2048 + (slot * 17 + r15) * 4, q);
    }
    bar();
    if (xw) {
        q = 0.f;
#pragma unroll
        for (int k = 0; k < 16; ++k) q += lds_ld<float>(2048 + (k * 17 + r15) * 4);
        const float m1 = rbf(mean), r = 1.0f / ln_den(q * (1.0f / DM));
#pragma unroll
        for (int c = 0; c < 5; ++c) {
            const unsigned o = toff(mt * 16 + r15, (slot * 5 + c) * 8, par0);
            st128(v[c], rw, o, PG0 + PG_XR);
            u32x4 n;
#pragma unroll
            for (int e = 0; e < 4; ++e) n[e] = pack2(rbf(lo2f(v[c][e]) - m1) * r, rbf(hi2f(v[c][e]) - m1) * r);
            st128(n, rw, o, PG0 + PG_XN);
        }
    }
    wait_vm<0>();
    bar();
    if (threadIdx.x < 8) __hip_atomic_fetch_add((unsigned*)(P.ws + FL0 + FL_IN + (ph * 8 + threadIdx.x) * 128), 1u, RLX_AGENT);
}

__global__ __launch_bounds__(512, 2) void mega_kernel(Params P) {
    const int wave = __builtin_amdgcn_readfirstlane(threadIdx.x >> 6);
    u32x4 cpf;
    {
        unsigned long long pc;
        unsigned len;
        asm volatile("s_getpc_b64 %0\n\ts_mov_b32 %1, _Z8code_endv-_Z11mega_kernel6Params" : "=s"(pc), "=s"(len));
        cpf = __builtin_amdgcn_raw_buffer_load_b128(mkrsrc((const void*)(pc & ~255ull), len), (blockIdx.x >> 3) * 8192 + threadIdx.x * 16, 0, 0);
    }
    {
        const rsrc_t rf = mkrsrc(P.ws + FL0, FL_BYTES);
        if (blockIdx.x == 0) {
            unsigned h = 0;
            if (wave == 7) h = (probe(P.ws, 0, 8192, CAL1) ^ ((unsigned)__builtin_amdgcn_s_getreg((3 << 11) | 20) >> 2)) & 1u;
            if (wave == 0) {
                const unsigned l16 = lane_id() * 16;
                u32x4 z = {0u, 0u, 0u, 0u};
                asm volatile("" : "+v"(z));
#pragma unroll
                for (int k = 0; k < 4; ++k) st128(z, rf, k * 1024 + l16 < FL_HB ? k * 1024 + l16 : OOB, 0);
                wait_vm<0>();
                for (unsigned c = 1; c < 200000u; ++c) {
                    __builtin_amdgcn_raw_buffer_store_b32(c, rf, l16 < 128 ? l16 * 8 : OOB, FL_HB, 16);
                    unsigned n = __builtin_amdgcn_raw_buffer_load_b32(rf, (l16 & 127u) * 8, FL_TK, 16), sum = 0;
                    n = n < 32u ? n : 32u;
#pragma unroll
                    for (int k = 0; k < 8; ++k) sum += __builtin_amdgcn_readlane(n, k);
                    if (sum >= 255u) break;
                }
            }
            bar();
            if (wave == 7 && lane_id() < 16) __hip_atomic_fetch_add((unsigned*)(P.ws + FL0 + FL_IN + (threadIdx.x & 15) * 128), (2u + h) << 16, RLX_AGENT);
        } else if (wave == 0) {
            unsigned o;
            asm volatile("v_mov_b32 %0, 0" : "=v"(o));
            const unsigned v0 = __builtin_amdgcn_readfirstlane(__builtin_amdgcn_raw_buffer_load_b32(rf, o, FL_HB + (blockIdx.x & 7) * 128, 16));
            for (int spins = 0; spins < 400000; ++spins) {
                asm volatile("" ::: "memory");
                if (__builtin_amdgcn_readfirstlane(__builtin_amdgcn_raw_buffer_load_b32(rf, o, FL_HB + (blockIdx.x & 7) * 128, 16)) != v0) break;
                __builtin_amdgcn_s_sleep(1);
            }
            asm volatile("" ::: "memory");
        }
    }
    if (threadIdx.x == 0) {
        unsigned g = (unsigned)__builtin_amdgcn_s_getreg((3 << 11) | 20) & 7u, k;
        for (int n = 0; (k = __hip_atomic_fetch_add((unsigned*)(P.ws + FL0 + FL_TK + g * 128), 1u, RLX_AGENT)) >= 32u && n < 8; ++n) g = (g + 1) & 7u;
        lds_st<unsigned>(0, g << 5 | (k & 31u));
    }
    bar();
    const unsigned tk = __builtin_amdgcn_readfirstlane(lds_ld<unsigned>(0));
    asm volatile("" :: "v"(cpf));
    bar();
    const int g4 = (tk >> 5) & 3, k = tk & 31;
    const int L = tk >> 7, x = g4, r = k;
    if (L) {
        const int par0 = (init_wait(P, wave, 1, 0x20000u) >> 16) & 1;
        if (tk == 254 && wave == 0) {
            const unsigned xc = (unsigned)__builtin_amdgcn_s_getreg((3 << 11) | 20);
            const unsigned f = (probe(P.ws, ((par0 ^ (xc >> 2)) & 1) * 8192, BLK, CAL2) ^ 1u ^ xc) & 1u;
            if (threadIdx.x < 16) __hip_atomic_fetch_add((unsigned*)(P.ws + FL0 + FL_IN + threadIdx.x * 128), (2u + f) << 20, RLX_AGENT);
        }
#pragma unroll 1
        for (int ph = 0; ph < 2; ++ph) {
            setup(P, wave, (tk & 31) * 4 + g4, par0, ph);
            for (int k = ph * DLY_L; k < DLY_L; ++k) __builtin_amdgcn_s_sleep(32);
        }
    }
    {
        const int i = (r - 26) * 4 + x;
        const int d = (r < 8 ? DLY_S : r < 21 ? 0 : i < 4 ? DLY_H : DLY_W);
        for (int k = 0; k < d; ++k) __builtin_amdgcn_s_sleep(32);
    }
    if (r < 8) s_role(P, wave, L, x * 8 + r, x);
    else if (r < 16) v_role(P, wave ^ 4, L, x * 8 + r - 8, x);
    else if (r < 21) r_role(P, wave, L, r - 16, x);
    else {
        const int i = (r - 26) * 4 + x;
        if (i < 4) h_role(P, wave, L, r < 25 ? x * 4 + r - 21 : 16 + x * 2 + r - 25, x);
        else if (i < 7) w_role<1>(P, wave, L, i - 4, x);
        else if (i < 10) w_role<2>(P, wave, L, i - 7, x);
        else if (i < 16) w_role<3>(P, wave, L, i - 10, x);
        else w4_role(P, wave, L, i - 16, x);
    }
}

__global__ void code_end() {}

extern "C" void kernel_launch(void* const* d_in, const int* in_sizes, int n_in, void* d_out, int out_size, void* d_ws,
                              size_t ws_size, hipStream_t stream) {
    char* ws = (char*)d_ws;
    Params P;
    P.ws = ws; P.x = (const bf16_t*)d_in[0]; P.mem0 = (const bf16_t*)d_in[1]; P.out = (bf16_t*)d_out;
    for (int i = 0; i < 8; ++i) P.w[i] = (const bf16_t*)d_in[i < 4 ? 2 + i : 4 + i];
    (void)hipFuncSetAttribute((const void*)mega_kernel, hipFuncAttributeMaxDynamicSharedMemorySize, MEGA_LDS);
    mega_kernel<<<256, 512, MEGA_LDS, stream>>>(P);
}
```

```cpp
#include <hip/hip_runtime.h>
#include <stdint.h>
#include <type_traits>

typedef unsigned short bf16_t;
using bf16x2 = __attribute__((ext_vector_type(2))) __bf16;
using bf16x8 = __attribute__((ext_vector_type(8))) __bf16;
using bf16x16 = __attribute__((ext_vector_type(16))) __bf16;
using f32x2 = __attribute__((ext_vector_type(2))) float;
using f32x4 = __attribute__((ext_vector_type(4))) float;
using u32x2 = __attribute__((ext_vector_type(2))) unsigned;
using u32x4 = __attribute__((ext_vector_type(4))) unsigned;
typedef __amdgpu_buffer_rsrc_t rsrc_t;

__device__ __forceinline__ unsigned pack2(float a, float b) {
    bf16x2 r = __builtin_convertvector((f32x2){a, b}, bf16x2);
    return __builtin_bit_cast(unsigned, r);
}
__device__ __forceinline__ float rbf(float f) { return __uint_as_float(pack2(f, 0.f) << 16); }
__device__ __forceinline__ float lo2f(unsigned u) { return __uint_as_float(u << 16); }
__device__ __forceinline__ float hi2f(unsigned u) { return __uint_as_float(u & 0xFFFF0000u); }
__device__ __forceinline__ rsrc_t mkrsrc(const void* p, unsigned bytes) {
    return __builtin_amdgcn_make_buffer_rsrc((void*)p, 0, bytes, 0x00020000);
}

constexpr int S_ = 2048, DM = 640, NH = 32, DH = 192, HD = NH * DH  , SEG = 32, NSEG = 64;
constexpr int FF = 1536;

#define XMAP
#define NCPY 4
constexpr unsigned NSLOT = 4, NCP = NCPY;
#define FL_TK_DEF
#define DLY_L 12
#define DLY_S 0
#define DLY_W 10
#define DLY_H 3
#define EWAKE "s_nop 0"
#define LWAKE "s_wakeup\n\t"
#define SLP4 (wave == 4 ? t : 0)
#define WPF 12
#define WPRI 1
#define WPRI2 0
#define RTD 3500
#define VTD 3900
#define HAH 100
#define WEAK 0
#define BAL 0
#define HSL 8
#define OAUX 2
#define W4P 3
#define STD 3800
#define CAL1 6
#define CAL2 12
#define DLY_X 2
#define CALN 12
#define RSL 9
#define VTRY 0
#define VSL 1
#define NRR 5
constexpr unsigned BLK = 16384, RG_SLOT = 4u << 20;
constexpr unsigned XP_PAIR = 6 * BLK, XP_SLOT = RG_SLOT;
constexpr unsigned XM_PAIR = 4 * BLK, XM_SLOT = RG_SLOT;
constexpr unsigned VM_SLOT = RG_SLOT;
constexpr unsigned PX_SLOT = RG_SLOT;
constexpr unsigned RG_XP = 0, RG_XM = 12 * BLK, RG_VM = 20 * BLK, RG_PX = 60 * BLK;
constexpr unsigned RG_ST = 68 * BLK, RG_FV = 84 * BLK, RG_FH = 86 * BLK, RG_FX = 88 * BLK, RG_L = NSLOT * RG_SLOT;
constexpr unsigned FL0 = 89 * BLK, FL_TK = 512, FL_IN = FL_TK + 8 * 128, FL_HB = FL_IN + 16 * 128, FL_BYTES = FL_HB + 8 * 128;
constexpr unsigned ACT = S_ * DM * 2;
#define PXROT 5
#define W4SL 5
#define RXDEL 1
#define VXD 32
#define VXAUX 16
constexpr unsigned PG0 = RG_L, PG_L = 80u << 21;
constexpr unsigned PG_XA = 0, PG_HS = 5 * BLK, PG_X1 = 10 * BLK, PG_H2 = 15 * BLK, PG_SS = 27 * BLK, PG_XR = 28 * BLK, PG_XN = 33 * BLK, PG_LX = 10 * BLK, PG_HV = 48 * BLK;
__device__ __forceinline__ unsigned pgb(unsigned p, int par) { return p << 21 | ((p ^ par) & 1) << 13; }
__device__ __forceinline__ constexpr unsigned blin(unsigned o) { return o + (o & ~8191u); }
constexpr int HT0 = 2;
constexpr int AH = 3;
constexpr unsigned WS_END = PG0 + PG_L;

#define SB() __builtin_amdgcn_sched_barrier(0)
#define LAS __attribute__((address_space(3)))
template <class T> __device__ __forceinline__ T lds_ld(unsigned a) { return *(const LAS T*)(a); }
template <class T> __device__ __forceinline__ void lds_st(unsigned a, T v) { *(LAS T*)(a) = v; }
constexpr unsigned OOB = 0x7FFFFF00u;
template <int N> __device__ __forceinline__ void wait_vm() { asm volatile("s_waitcnt vmcnt(%0)" :: "i"(N) : "memory"); }
__device__ __forceinline__ void bar() {
    asm volatile("s_waitcnt lgkmcnt(0)" ::: "memory");
    __builtin_amdgcn_s_barrier();
    asm volatile("" ::: "memory");
}
__device__ __forceinline__ int lane_id() {
    int ln;
    asm volatile("v_mbcnt_lo_u32_b32 %0, -1, 0\n\tv_mbcnt_hi_u32_b32 %0, -1, %0" : "=v"(ln));
    return ln;
}
__device__ __forceinline__ void swap16(unsigned& a, unsigned& b) { asm volatile("s_nop 3\n\tv_permlane16_swap_b32 %0, %1\n\ts_nop 1" : "+v"(a), "+v"(b)); }
__device__ __forceinline__ void swap32(unsigned& a, unsigned& b) { asm volatile("s_nop 3\n\tv_permlane32_swap_b32 %0, %1\n\ts_nop 1" : "+v"(a), "+v"(b)); }
__device__ __forceinline__ float sum_x16(float x) { unsigned a = __float_as_uint(x), b = a; swap16(a, b); return __uint_as_float(a) + __uint_as_float(b); }
__device__ __forceinline__ float sum_x32(float x) { unsigned a = __float_as_uint(x), b = a; swap32(a, b); return __uint_as_float(a) + __uint_as_float(b); }
__device__ __forceinline__ float max_x16(float x) { unsigned a = __float_as_uint(x), b = a; swap16(a, b); return __builtin_fmaxf(__uint_as_float(a), __uint_as_float(b)); }
__device__ __forceinline__ float max_x32(float x) { unsigned a = __float_as_uint(x), b = a; swap32(a, b); return __builtin_fmaxf(__uint_as_float(a), __uint_as_float(b)); }
__device__ __forceinline__ void sum2_rows(float& s, float& q) {
    unsigned a = __float_as_uint(s), b = a, c = __float_as_uint(q), d = c;
    asm volatile("s_nop 2\n\tv_permlane16_swap_b32 %0, %1\n\tv_permlane16_swap_b32 %2, %3\n\tv_add_f32 %0, %0, %1\n\tv_add_f32 %2, %2, %3\n\tv_mov_b32 %1, %0\n\tv_mov_b32 %3, %2\n\t"
                 "s_nop 1\n\tv_permlane32_swap_b32 %0, %1\n\tv_permlane32_swap_b32 %2, %3\n\tv_add_f32 %0, %0, %1\n\tv_add_f32 %2, %2, %3" : "+v"(a), "+v"(b), "+v"(c), "+v"(d));
    s = __uint_as_float(a); q = __uint_as_float(c);
}
__device__ __forceinline__ u32x4 merge16(u32x2 a, u32x2 b) {
    unsigned a0 = a[0], a1 = a[1], b0 = b[0], b1 = b[1];
    asm volatile("s_nop 3\n\tv_permlane16_swap_b32 %0, %2\n\tv_permlane16_swap_b32 %1, %3\n\ts_nop 1" : "+v"(a0), "+v"(a1), "+v"(b0), "+v"(b1));
    return (u32x4){a0, a1, b0, b1};
}
template <int AUXV>
__device__ __forceinline__ void st128_(u32x4 v, rsrc_t r, unsigned voff, unsigned soff) {
    __builtin_amdgcn_raw_buffer_store_b128(v, r, voff, soff, AUXV);
    asm volatile("s_nop 1" :: "v"(v) : "memory");
}
#define st128(V, R, VO_, SO_) st128_<16>(V, R, VO_, SO_)
#define LD128(R, VO_, SO_) __builtin_amdgcn_raw_buffer_load_b128(R, VO_, SO_, 16)
#define LD64(R, VO_, SO_) __builtin_amdgcn_raw_buffer_load_b64(R, VO_, SO_, 16)
#define BF8(X) __builtin_bit_cast(bf16x8, X)
__device__ __forceinline__ float gelu_tanh(float x) {
    const float c0 = -2.0f * 0.7978845608028654f * 1.4426950408889634f, c1 = c0 * 0.044715f;
    const float e = __builtin_amdgcn_exp2f(x * __builtin_fmaf(x * x, c1, c0));
    return x * __builtin_amdgcn_rcpf(1.0f + e);
}
#define MFMA(A_, B_, C_) C_ = __builtin_amdgcn_mfma_f32_16x16x32_bf16(A_, B_, C_, 0, 0, 0)
#define SMF(A_, B_, C_) C_ = __builtin_amdgcn_smfmac_f32_16x16x64_bf16(A_, B_, C_, sidx, 0, 0)
__device__ __forceinline__ float ln_den(float var) { return rbf(__builtin_amdgcn_sqrtf(rbf(rbf(var) + 1e-5f))); }
__device__ __forceinline__ float rcp(float x) { return __builtin_amdgcn_rcpf(x); }

struct Params {
    char* ws;
    const bf16_t* x;
    const bf16_t* mem0;
    const bf16_t* w[8];
    bf16_t* out;
};

#define RLX_AGENT __ATOMIC_RELAXED, __HIP_MEMORY_SCOPE_AGENT
#define SENT 0xFFFFFFFFu
constexpr int SPIN_CAP = 400000;
__device__ __forceinline__ bool spin_fail(int spins, const Params& P) {
    if ((spins & 1023) == 1023) {
        const rsrc_t rt = mkrsrc(P.ws + FL0, FL_BYTES);
        unsigned o;
        asm volatile("v_mov_b32 %0, 0x100" : "=v"(o));
        if (__builtin_amdgcn_readfirstlane(__builtin_amdgcn_raw_buffer_load_b32(rt, o, 0, 16)) != 0u) return true;
        if (spins > SPIN_CAP) { __builtin_amdgcn_raw_buffer_store_b32(o, rt, o, 0, 16); return true; }
    }
    return false;
}
__device__ __forceinline__ bool ok4(u32x4 v) { return v[0] != SENT && v[1] != SENT && v[2] != SENT && v[3] != SENT; }
__device__ __forceinline__ unsigned nosent(unsigned d) { return d < 0xFFFFFFFEu ? d : 0xFFFFFFFEu; }
__device__ __forceinline__ u32x4 nosent4(u32x4 v) { return (u32x4){nosent(v[0]), nosent(v[1]), nosent(v[2]), nosent(v[3])}; }
__device__ __forceinline__ unsigned umax3(unsigned a, unsigned b, unsigned c) { const unsigned m = a > b ? a : b; return m > c ? m : c; }
__device__ __forceinline__ unsigned umax4(unsigned m, u32x4 v) { return umax3(umax3(m, v[0], v[1]), v[2], v[3]); }
__device__ __forceinline__ u32x4 sent4() { unsigned sv; asm volatile("v_mov_b32 %0, -1" : "=v"(sv)); return (u32x4){sv, sv, sv, sv}; }
template <int N>
__device__ __forceinline__ void poll(rsrc_t rs, unsigned base, const unsigned (&off)[N], u32x4 (&v)[N], const Params& P) {
    for (int spins = 0;; ++spins) {
        unsigned m = 0;
#pragma unroll
        for (int k = 0; k < N; ++k) v[k] = LD128(rs, off[k], base);
#pragma unroll
        for (int k = 0; k < N; ++k) m = umax4(m, v[k]);
        if (__all(m != SENT)) return;
        asm volatile("" ::: "memory");
        if (spin_fail(spins, P)) return;
    }
}
__device__ __forceinline__ bf16x16 cat16(bf16x8 a, bf16x8 b) {
    return __builtin_shufflevector(a, b, 0, 1, 2, 3, 4, 5, 6, 7, 8, 9, 10, 11, 12, 13, 14, 15);
}
__device__ __forceinline__ bf16x8 half8(bf16x16 v, int h) {
    return h ? __builtin_shufflevector(v, v, 8, 9, 10, 11, 12, 13, 14, 15) : __builtin_shufflevector(v, v, 0, 1, 2, 3, 4, 5, 6, 7);
}
__device__ __forceinline__ bf16x16 lds_frag(unsigned a, unsigned step) { return cat16(lds_ld<bf16x8>(a), lds_ld<bf16x8>(a + step)); }
template <int I>
__device__ __forceinline__ void dmaq(const char* g, unsigned l16, unsigned dst) {
    asm volatile("s_mov_b32 m0, %2\n\ts_nop 0\n\tglobal_load_lds_dwordx4 %0, %1 offset:%3 sc1" :: "v"(l16), "s"(g), "s"(dst), "i"(I * 1024) : "memory");
}
__device__ __forceinline__ void dma4(const char* g, unsigned l16, unsigned dst) {
    asm volatile("s_mov_b32 m0, %2\n\ts_nop 0\n\tglobal_load_lds_dwordx4 %0, %1 sc1\n\tglobal_load_lds_dwordx4 %0, %1 offset:1024 sc1\n\t"
                 "global_load_lds_dwordx4 %0, %1 offset:2048 sc1\n\tglobal_load_lds_dwordx4 %0, %1 offset:3072 sc1" :: "v"(l16), "s"(g), "s"(dst) : "memory");
}
__device__ __forceinline__ unsigned toff(int row, int col, int par) {
    return pgb(row >> 5, par) + blin((unsigned)((row >> 4) & 1) * 20480u + (unsigned)(col >> 3) * 256u) + (unsigned)(row & 15) * 16u;
}

#define ASM_ST
#define ASM_ST_DECL
#define ASM_ST_OUT
#define ASM_ST_SAVE(k)
#define ASM_ST2
#define ASM_ST3
#define ASM_ST3_OUT
#define ASM_ST3_SAVE(k)
#define ASM_ST2_OUT
#define ASM_ST2_SAVE(k)
#define STDECL
#define ST(k)
#define STC(k)
#define STFLUSH(t)

#define MARK(c, k)
#define MARKX(c, k)
#define PMARK(c, st, seg)
#define LDECL
#define LA()
#define LB(t)
#define LF()
#define STROLE 3
constexpr unsigned L_MEM = 0;
constexpr unsigned L_Q = 10240;
constexpr unsigned L_KM = 13312;
constexpr unsigned L_QX = 16384;
constexpr unsigned L_KX = 28672;
constexpr unsigned L_XB = 40960;
constexpr unsigned L_XA = 49152;
constexpr unsigned L_W = 81920;
constexpr int MEGA_LDS = 163840;
constexpr int wi(int i, int j) { return (i >> 1) * 5 + ((i & 1) ? 2 + j : j - 1); }

#define RPUB2
#define WNT ""
#define PRIUP "s_setprio 2\n\t"
#define PRISM PRIUP
#define STR2(x) #x
#define STRX(x) STR2(x)
#define W4SLEEP "s_sleep " STRX(W4SL) "\n\t"
#define PLAY 0
#define V1A 0
#define ASM_WLOAD3 \
    "s_xor_b32 %[t], %[wx], 0\n\t" \
    "s_mul_i32 %[t], %[t], 0xc0000\n\t" \
    "buffer_load_dwordx4 v[200:203], %[o0], %[rs], %[t] offen" WNT "\n\t" \
    "buffer_load_dwordx4 v[0:3], %[o1], %[rs], %[t] offen" WNT "\n\t" \
    "buffer_load_dwordx4 v[8:11], %[o2], %[rs], %[t] offen" WNT "\n\t" \
    "s_add_u32 %[t], %[t], 0x18000\n\t" \
    "buffer_load_dwordx4 v[16:19], %[o0], %[rs], %[t] offen" WNT "\n\t" \
    "buffer_load_dwordx4 v[24:27], %[o1], %[rs], %[t] offen" WNT "\n\t" \
    "buffer_load_dwordx4 v[32:35], %[o2], %[rs], %[t] offen" WNT "\n\t" \
    "s_add_u32 %[t], %[t], 0x18000\n\t" \
    "buffer_load_dwordx4 v[208:211], %[o0], %[rs], %[t] offen" WNT "\n\t" \
    "buffer_load_dwordx4 v[40:43], %[o1], %[rs], %[t] offen" WNT "\n\t" \
    "buffer_load_dwordx4 v[48:51], %[o2], %[rs], %[t] offen" WNT "\n\t" \
    "s_add_u32 %[t], %[t], 0x18000\n\t" \
    "buffer_load_dwordx4 v[56:59], %[o0], %[rs], %[t] offen" WNT "\n\t" \
    "buffer_load_dwordx4 v[64:67], %[o1], %[rs], %[t] offen" WNT "\n\t" \
    "buffer_load_dwordx4 v[72:75], %[o2], %[rs], %[t] offen" WNT "\n\t" \
    "s_add_u32 %[t], %[t], 0x18000\n\t" \
    "buffer_load_dwordx4 v[204:207], %[o0], %[rs], %[t] offen" WNT "\n\t" \
    "buffer_load_dwordx4 v[4:7], %[o1], %[rs], %[t] offen" WNT "\n\t" \
    "buffer_load_dwordx4 v[12:15], %[o2], %[rs], %[t] offen" WNT "\n\t" \
    "s_add_u32 %[t], %[t], 0x18000\n\t" \
    "buffer_load_dwordx4 v[20:23], %[o0], %[rs], %[t] offen" WNT "\n\t" \
    "buffer_load_dwordx4 v[28:31], %[o1], %[rs], %[t] offen" WNT "\n\t" \
    "buffer_load_dwordx4 v[36:39], %[o2], %[rs], %[t] offen" WNT "\n\t" \
    "s_add_u32 %[t], %[t], 0x18000\n\t" \
    "buffer_load_dwordx4 v[212:215], %[o0], %[rs], %[t] offen" WNT "\n\t" \
    "buffer_load_dwordx4 v[44:47], %[o1], %[rs], %[t] offen" WNT "\n\t" \
    "buffer_load_dwordx4 v[52:55], %[o2], %[rs], %[t] offen" WNT "\n\t" \
    "s_add_u32 %[t], %[t], 0x18000\n\t" \
    "buffer_load_dwordx4 v[60:63], %[o0], %[rs], %[t] offen" WNT "\n\t" \
    "buffer_load_dwordx4 v[68:71], %[o1], %[rs], %[t] offen" WNT "\n\t" \
    "buffer_load_dwordx4 v[76:79], %[o2], %[rs], %[t] offen" WNT "\n\t" \
    "s_add_u32 %[t], %[t], 0x18000\n\t" \
    "s_waitcnt vmcnt(21)\n\t" \
    "ds_write_b128 %[s0], v[200:203] offset:0\n\t" \
    "ds_write_b128 %[s1], v[0:3] offset:0\n\t" \
    "ds_write_b128 %[s2], v[8:11] offset:0\n\t" \
    "s_waitcnt vmcnt(18)\n\t" \
    "ds_write_b128 %[s0], v[16:19] offset:3456\n\t" \
    "ds_write_b128 %[s1], v[24:27] offset:3456\n\t" \
    "ds_write_b128 %[s2], v[32:35] offset:3456\n\t" \
    "s_waitcnt vmcnt(15)\n\t" \
    "ds_write_b128 %[s0], v[208:211] offset:6912\n\t" \
    "ds_write_b128 %[s1], v[40:43] offset:6912\n\t" \
    "ds_write_b128 %[s2], v[48:51] offset:6912\n\t" \
    "s_waitcnt vmcnt(12)\n\t" \
    "ds_write_b128 %[s0], v[56:59] offset:10368\n\t" \
    "ds_write_b128 %[s1], v[64:67] offset:10368\n\t" \
    "ds_write_b128 %[s2], v[72:75] offset:10368\n\t" \
    "s_waitcnt lgkmcnt(0)\n\t" \
    "s_barrier \n\t" \
    "ds_read_b64_tr_b16 v[200:201], %[t0] offset:0\n\t" \
    "ds_read_b64_tr_b16 v[202:203], %[t0] offset:1664\n\t" \
    "ds_read_b64_tr_b16 v[0:1], %[t0] offset:128\n\t" \
    "ds_read_b64_tr_b16 v[2:3], %[t0] offset:1792\n\t" \
    "ds_read_b64_tr_b16 v[8:9], %[t0] offset:256\n\t" \
    "ds_read_b64_tr_b16 v[10:11], %[t0] offset:1920\n\t" \
    "ds_read_b64_tr_b16 v[16:17], %[t1] offset:0\n\t" \
    "ds_read_b64_tr_b16 v[18:19], %[t1] offset:1664\n\t" \
    "ds_read_b64_tr_b16 v[24:25], %[t1] offset:128\n\t" \
    "ds_read_b64_tr_b16 v[26:27], %[t1] offset:1792\n\t" \
    "ds_read_b64_tr_b16 v[32:33], %[t1] offset:256\n\t" \
    "ds_read_b64_tr_b16 v[34:35], %[t1] offset:1920\n\t" \
    "ds_read_b64_tr_b16 v[208:209], %[t2] offset:0\n\t" \
    "ds_read_b64_tr_b16 v[210:211], %[t2] offset:1664\n\t" \
    "ds_read_b64_tr_b16 v[40:41], %[t2] offset:128\n\t" \
    "ds_read_b64_tr_b16 v[42:43], %[t2] offset:1792\n\t" \
    "ds_read_b64_tr_b16 v[48:49], %[t2] offset:256\n\t" \
    "ds_read_b64_tr_b16 v[50:51], %[t2] offset:1920\n\t" \
    "ds_read_b64_tr_b16 v[56:57], %[t3] offset:0\n\t" \
    "ds_read_b64_tr_b16 v[58:59], %[t3] offset:1664\n\t" \
    "ds_read_b64_tr_b16 v[64:65], %[t3] offset:128\n\t" \
    "ds_read_b64_tr_b16 v[66:67], %[t3] offset:1792\n\t" \
    "ds_read_b64_tr_b16 v[72:73], %[t3] offset:256\n\t" \
    "ds_read_b64_tr_b16 v[74:75], %[t3] offset:1920\n\t" \
    "s_xor_b32 %[t], %[wx], 4\n\t" \
    "s_mul_i32 %[t], %[t], 0xc0000\n\t" \
    "buffer_load_dwordx4 v[216:219], %[o0], %[rs], %[t] offen" WNT "\n\t" \
    "buffer_load_dwordx4 v[80:83], %[o1], %[rs], %[t] offen" WNT "\n\t" \
    "buffer_load_dwordx4 v[88:91], %[o2], %[rs], %[t] offen" WNT "\n\t" \
    "s_add_u32 %[t], %[t], 0x18000\n\t" \
    "buffer_load_dwordx4 v[96:99], %[o0], %[rs], %[t] offen" WNT "\n\t" \
    "buffer_load_dwordx4 v[104:107], %[o1], %[rs], %[t] offen" WNT "\n\t" \
    "buffer_load_dwordx4 v[112:115], %[o2], %[rs], %[t] offen" WNT "\n\t" \
    "s_add_u32 %[t], %[t], 0x18000\n\t" \
    "buffer_load_dwordx4 v[224:227], %[o0], %[rs], %[t] offen" WNT "\n\t" \
    "buffer_load_dwordx4 v[120:123], %[o1], %[rs], %[t] offen" WNT "\n\t" \
    "buffer_load_dwordx4 v[128:131], %[o2], %[rs], %[t] offen" WNT "\n\t" \
    "s_add_u32 %[t], %[t], 0x18000\n\t" \
    "buffer_load_dwordx4 v[136:139], %[o0], %[rs], %[t] offen" WNT "\n\t" \
    "buffer_load_dwordx4 v[144:147], %[o1], %[rs], %[t] offen" WNT "\n\t" \
    "buffer_load_dwordx4 v[152:155], %[o2], %[rs], %[t] offen" WNT "\n\t" \
    "s_add_u32 %[t], %[t], 0x18000\n\t" \
    "s_waitcnt lgkmcnt(0)\n\t" \
    "s_barrier \n\t" \
    "s_waitcnt vmcnt(21)\n\t" \
    "ds_write_b128 %[s0], v[204:207] offset:0\n\t" \
    "ds_write_b128 %[s1], v[4:7] offset:0\n\t" \
    "ds_write_b128 %[s2], v[12:15] offset:0\n\t" \
    "s_waitcnt vmcnt(18)\n\t" \
    "ds_write_b128 %[s0], v[20:23] offset:3456\n\t" \
    "ds_write_b128 %[s1], v[28:31] offset:3456\n\t" \
    "ds_write_b128 %[s2], v[36:39] offset:3456\n\t" \
    "s_waitcnt vmcnt(15)\n\t" \
    "ds_write_b128 %[s0], v[212:215] offset:6912\n\t" \
    "ds_write_b128 %[s1], v[44:47] offset:6912\n\t" \
    "ds_write_b128 %[s2], v[52:55] offset:6912\n\t" \
    "s_waitcnt vmcnt(12)\n\t" \
    "ds_write_b128 %[s0], v[60:63] offset:10368\n\t" \
    "ds_write_b128 %[s1], v[68:71] offset:10368\n\t" \
    "ds_write_b128 %[s2], v[76:79] offset:10368\n\t" \
    "s_waitcnt lgkmcnt(0)\n\t" \
    "s_barrier \n\t" \
    "ds_read_b64_tr_b16 v[204:205], %[t0] offset:0\n\t" \
    "ds_read_b64_tr_b16 v[206:207], %[t0] offset:1664\n\t" \
    "ds_read_b64_tr_b16 v[4:5], %[t0] offset:128\n\t" \
    "ds_read_b64_tr_b16 v[6:7], %[t0] offset:1792\n\t" \
    "ds_read_b64_tr_b16 v[12:13], %[t0] offset:256\n\t" \
    "ds_read_b64_tr_b16 v[14:15], %[t0] offset:1920\n\t" \
    "ds_read_b64_tr_b16 v[20:21], %[t1] offset:0\n\t" \
    "ds_read_b64_tr_b16 v[22:23], %[t1] offset:1664\n\t" \
    "ds_read_b64_tr_b16 v[28:29], %[t1] offset:128\n\t" \
    "ds_read_b64_tr_b16 v[30:31], %[t1] offset:1792\n\t" \
    "ds_read_b64_tr_b16 v[36:37], %[t1] offset:256\n\t" \
    "ds_read_b64_tr_b16 v[38:39], %[t1] offset:1920\n\t" \
    "ds_read_b64_tr_b16 v[212:213], %[t2] offset:0\n\t" \
    "ds_read_b64_tr_b16 v[214:215], %[t2] offset:1664\n\t" \
    "ds_read_b64_tr_b16 v[44:45], %[t2] offset:128\n\t" \
    "ds_read_b64_tr_b16 v[46:47], %[t2] offset:1792\n\t" \
    "ds_read_b64_tr_b16 v[52:53], %[t2] offset:256\n\t" \
    "ds_read_b64_tr_b16 v[54:55], %[t2] offset:1920\n\t" \
    "ds_read_b64_tr_b16 v[60:61], %[t3] offset:0\n\t" \
    "ds_read_b64_tr_b16 v[62:63], %[t3] offset:1664\n\t" \
    "ds_read_b64_tr_b16 v[68:69], %[t3] offset:128\n\t" \
    "ds_read_b64_tr_b16 v[70:71], %[t3] offset:1792\n\t" \
    "ds_read_b64_tr_b16 v[76:77], %[t3] offset:256\n\t" \
    "ds_read_b64_tr_b16 v[78:79], %[t3] offset:1920\n\t" \
    "buffer_load_dwordx4 v[220:223], %[o0], %[rs], %[t] offen" WNT "\n\t" \
    "buffer_load_dwordx4 v[84:87], %[o1], %[rs], %[t] offen" WNT "\n\t" \
    "buffer_load_dwordx4 v[92:95], %[o2], %[rs], %[t] offen" WNT "\n\t" \
    "s_add_u32 %[t], %[t], 0x18000\n\t" \
    "buffer_load_dwordx4 v[100:103], %[o0], %[rs], %[t] offen" WNT "\n\t" \
    "buffer_load_dwordx4 v[108:111], %[o1], %[rs], %[t] offen" WNT "\n\t" \
    "buffer_load_dwordx4 v[116:119], %[o2], %[rs], %[t] offen" WNT "\n\t" \
    "s_add_u32 %[t], %[t], 0x18000\n\t" \
    "buffer_load_dwordx4 v[228:231], %[o0], %[rs], %[t] offen" WNT "\n\t" \
    "buffer_load_dwordx4 v[124:127], %[o1], %[rs], %[t] offen" WNT "\n\t" \
    "buffer_load_dwordx4 v[132:135], %[o2], %[rs], %[t] offen" WNT "\n\t" \
    "s_add_u32 %[t], %[t], 0x18000\n\t" \
    "buffer_load_dwordx4 v[140:143], %[o0], %[rs], %[t] offen" WNT "\n\t" \
    "buffer_load_dwordx4 v[148:151], %[o1], %[rs], %[t] offen" WNT "\n\t" \
    "buffer_load_dwordx4 v[156:159], %[o2], %[rs], %[t] offen" WNT "\n\t" \
    "s_add_u32 %[t], %[t], 0x18000\n\t" \
    "s_waitcnt lgkmcnt(0)\n\t" \
    "s_barrier \n\t" \
    "s_waitcnt vmcnt(21)\n\t" \
    "ds_write_b128 %[s0], v[216:219] offset:0\n\t" \
    "ds_write_b128 %[s1], v[80:83] offset:0\n\t" \
    "ds_write_b128 %[s2], v[88:91] offset:0\n\t" \
    "s_waitcnt vmcnt(18)\n\t" \
    "ds_write_b128 %[s0], v[96:99] offset:3456\n\t" \
    "ds_write_b128 %[s1], v[104:107] offset:3456\n\t" \
    "ds_write_b128 %[s2], v[112:115] offset:3456\n\t" \
    "s_waitcnt vmcnt(15)\n\t" \
    "ds_write_b128 %[s0], v[224:227] offset:6912\n\t" \
    "ds_write_b128 %[s1], v[120:123] offset:6912\n\t" \
    "ds_write_b128 %[s2], v[128:131] offset:6912\n\t" \
    "s_waitcnt vmcnt(12)\n\t" \
    "ds_write_b128 %[s0], v[136:139] offset:10368\n\t" \
    "ds_write_b128 %[s1], v[144:147] offset:10368\n\t" \
    "ds_write_b128 %[s2], v[152:155] offset:10368\n\t" \
    "s_waitcnt lgkmcnt(0)\n\t" \
    "s_barrier \n\t" \
    "ds_read_b64_tr_b16 v[216:217], %[t0] offset:0\n\t" \
    "ds_read_b64_tr_b16 v[218:219], %[t0] offset:1664\n\t" \
    "ds_read_b64_tr_b16 v[80:81], %[t0] offset:128\n\t" \
    "ds_read_b64_tr_b16 v[82:83], %[t0] offset:1792\n\t" \
    "ds_read_b64_tr_b16 v[88:89], %[t0] offset:256\n\t" \
    "ds_read_b64_tr_b16 v[90:91], %[t0] offset:1920\n\t" \
    "ds_read_b64_tr_b16 v[96:97], %[t1] offset:0\n\t" \
    "ds_read_b64_tr_b16 v[98:99], %[t1] offset:1664\n\t" \
    "ds_read_b64_tr_b16 v[104:105], %[t1] offset:128\n\t" \
    "ds_read_b64_tr_b16 v[106:107], %[t1] offset:1792\n\t" \
    "ds_read_b64_tr_b16 v[112:113], %[t1] offset:256\n\t" \
    "ds_read_b64_tr_b16 v[114:115], %[t1] offset:1920\n\t" \
    "ds_read_b64_tr_b16 v[224:225], %[t2] offset:0\n\t" \
    "ds_read_b64_tr_b16 v[226:227], %[t2] offset:1664\n\t" \
    "ds_read_b64_tr_b16 v[120:121], %[t2] offset:128\n\t" \
    "ds_read_b64_tr_b16 v[122:123], %[t2] offset:1792\n\t" \
    "ds_read_b64_tr_b16 v[128:129], %[t2] offset:256\n\t" \
    "ds_read_b64_tr_b16 v[130:131], %[t2] offset:1920\n\t" \
    "ds_read_b64_tr_b16 v[136:137], %[t3] offset:0\n\t" \
    "ds_read_b64_tr_b16 v[138:139], %[t3] offset:1664\n\t" \
    "ds_read_b64_tr_b16 v[144:145], %[t3] offset:128\n\t" \
    "ds_read_b64_tr_b16 v[146:147], %[t3] offset:1792\n\t" \
    "ds_read_b64_tr_b16 v[152:153], %[t3] offset:256\n\t" \
    "ds_read_b64_tr_b16 v[154:155], %[t3] offset:1920\n\t" \
    "s_and_b32 %[t], %[wx], 3\n\t" \
    "s_mul_i32 %[t], %[t], 0x60000\n\t" \
    "s_add_u32 %[t], %[t], 0x600000\n\t" \
    "buffer_load_dwordx4 v[232:235], %[o0], %[rs], %[t] offen" WNT "\n\t" \
    "buffer_load_dwordx4 v[160:163], %[o1], %[rs], %[t] offen" WNT "\n\t" \
    "buffer_load_dwordx4 v[168:171], %[o2], %[rs], %[t] offen" WNT "\n\t" \
    "s_add_u32 %[t], %[t], 0x18000\n\t" \
    "buffer_load_dwordx4 v[236:239], %[o0], %[rs], %[t] offen" WNT "\n\t" \
    "buffer_load_dwordx4 v[164:167], %[o1], %[rs], %[t] offen" WNT "\n\t" \
    "buffer_load_dwordx4 v[172:175], %[o2], %[rs], %[t] offen" WNT "\n\t" \
    "s_add_u32 %[t], %[t], 0x18000\n\t" \
    "buffer_load_dwordx4 v[176:179], %[o0], %[rs], %[t] offen" WNT "\n\t" \
    "buffer_load_dwordx4 v[184:187], %[o1], %[rs], %[t] offen" WNT "\n\t" \
    "buffer_load_dwordx4 v[192:195], %[o2], %[rs], %[t] offen" WNT "\n\t" \
    "s_add_u32 %[t], %[t], 0x18000\n\t" \
    "buffer_load_dwordx4 v[180:183], %[o0], %[rs], %[t] offen" WNT "\n\t" \
    "buffer_load_dwordx4 v[188:191], %[o1], %[rs], %[t] offen" WNT "\n\t" \
    "buffer_load_dwordx4 v[196:199], %[o2], %[rs], %[t] offen" WNT "\n\t" \
    "s_add_u32 %[t], %[t], 0x18000\n\t" \
    "s_waitcnt lgkmcnt(0)\n\t" \
    "s_barrier \n\t" \
    "s_waitcnt vmcnt(21)\n\t" \
    "ds_write_b128 %[s0], v[220:223] offset:0\n\t" \
    "ds_write_b128 %[s1], v[84:87] offset:0\n\t" \
    "ds_write_b128 %[s2], v[92:95] offset:0\n\t" \
    "s_waitcnt vmcnt(18)\n\t" \
    "ds_write_b128 %[s0], v[100:103] offset:3456\n\t" \
    "ds_write_b128 %[s1], v[108:111] offset:3456\n\t" \
    "ds_write_b128 %[s2], v[116:119] offset:3456\n\t" \
    "s_waitcnt vmcnt(15)\n\t" \
    "ds_write_b128 %[s0], v[228:231] offset:6912\n\t" \
    "ds_write_b128 %[s1], v[124:127] offset:6912\n\t" \
    "ds_write_b128 %[s2], v[132:135] offset:6912\n\t" \
    "s_waitcnt vmcnt(12)\n\t" \
    "ds_write_b128 %[s0], v[140:143] offset:10368\n\t" \
    "ds_write_b128 %[s1], v[148:151] offset:10368\n\t" \
    "ds_write_b128 %[s2], v[156:159] offset:10368\n\t" \
    "s_waitcnt lgkmcnt(0)\n\t" \
    "s_barrier \n\t" \
    "ds_read_b64_tr_b16 v[220:221], %[t0] offset:0\n\t" \
    "ds_read_b64_tr_b16 v[222:223], %[t0] offset:1664\n\t" \
    "ds_read_b64_tr_b16 v[84:85], %[t0] offset:128\n\t" \
    "ds_read_b64_tr_b16 v[86:87], %[t0] offset:1792\n\t" \
    "ds_read_b64_tr_b16 v[92:93], %[t0] offset:256\n\t" \
    "ds_read_b64_tr_b16 v[94:95], %[t0] offset:1920\n\t" \
    "ds_read_b64_tr_b16 v[100:101], %[t1] offset:0\n\t" \
    "ds_read_b64_tr_b16 v[102:103], %[t1] offset:1664\n\t" \
    "ds_read_b64_tr_b16 v[108:109], %[t1] offset:128\n\t" \
    "ds_read_b64_tr_b16 v[110:111], %[t1] offset:1792\n\t" \
    "ds_read_b64_tr_b16 v[116:117], %[t1] offset:256\n\t" \
    "ds_read_b64_tr_b16 v[118:119], %[t1] offset:1920\n\t" \
    "ds_read_b64_tr_b16 v[228:229], %[t2] offset:0\n\t" \
    "ds_read_b64_tr_b16 v[230:231], %[t2] offset:1664\n\t" \
    "ds_read_b64_tr_b16 v[124:125], %[t2] offset:128\n\t" \
    "ds_read_b64_tr_b16 v[126:127], %[t2] offset:1792\n\t" \
    "ds_read_b64_tr_b16 v[132:133], %[t2] offset:256\n\t" \
    "ds_read_b64_tr_b16 v[134:135], %[t2] offset:1920\n\t" \
    "ds_read_b64_tr_b16 v[140:141], %[t3] offset:0\n\t" \
    "ds_read_b64_tr_b16 v[142:143], %[t3] offset:1664\n\t" \
    "ds_read_b64_tr_b16 v[148:149], %[t3] offset:128\n\t" \
    "ds_read_b64_tr_b16 v[150:151], %[t3] offset:1792\n\t" \
    "ds_read_b64_tr_b16 v[156:157], %[t3] offset:256\n\t" \
    "ds_read_b64_tr_b16 v[158:159], %[t3] offset:1920\n\t" \
    "s_waitcnt lgkmcnt(0)\n\t" \
    "s_barrier \n\t" \
    "s_waitcnt vmcnt(9)\n\t" \
    "ds_write_b128 %[s0], v[232:235] offset:0\n\t" \
    "ds_write_b128 %[s1], v[160:163] offset:0\n\t" \
    "ds_write_b128 %[s2], v[168:171] offset:0\n\t" \
    "s_waitcnt vmcnt(6)\n\t" \
    "ds_write_b128 %[s0], v[236:239] offset:3456\n\t" \
    "ds_write_b128 %[s1], v[164:167] offset:3456\n\t" \
    "ds_write_b128 %[s2], v[172:175] offset:3456\n\t" \
    "s_waitcnt vmcnt(3)\n\t" \
    "ds_write_b128 %[s0], v[176:179] offset:6912\n\t" \
    "ds_write_b128 %[s1], v[184:187] offset:6912\n\t" \
    "ds_write_b128 %[s2], v[192:195] offset:6912\n\t" \
    "s_waitcnt vmcnt(0)\n\t" \
    "ds_write_b128 %[s0], v[180:183] offset:10368\n\t" \
    "ds_write_b128 %[s1], v[188:191] offset:10368\n\t" \
    "ds_write_b128 %[s2], v[196:199] offset:10368\n\t" \
    "s_waitcnt lgkmcnt(0)\n\t" \
    "s_barrier \n\t" \
    "ds_read_b64_tr_b16 v[232:233], %[t4] offset:0\n\t" \
    "ds_read_b64_tr_b16 v[234:235], %[t4] offset:1664\n\t" \
    "ds_read_b64_tr_b16 v[160:161], %[t4] offset:128\n\t" \
    "ds_read_b64_tr_b16 v[162:163], %[t4] offset:1792\n\t" \
    "ds_read_b64_tr_b16 v[168:169], %[t4] offset:256\n\t" \
    "ds_read_b64_tr_b16 v[170:171], %[t4] offset:1920\n\t" \
    "ds_read_b64_tr_b16 v[236:237], %[t4] offset:13824\n\t" \
    "ds_read_b64_tr_b16 v[238:239], %[t4] offset:15488\n\t" \
    "ds_read_b64_tr_b16 v[164:165], %[t4] offset:13952\n\t" \
    "ds_read_b64_tr_b16 v[166:167], %[t4] offset:15616\n\t" \
    "ds_read_b64_tr_b16 v[172:173], %[t4] offset:14080\n\t" \
    "ds_read_b64_tr_b16 v[174:175], %[t4] offset:15744\n\t" \
    "ds_read_b64_tr_b16 v[176:177], %[t4] offset:27648\n\t" \
    "ds_read_b64_tr_b16 v[178:179], %[t4] offset:29312\n\t" \
    "ds_read_b64_tr_b16 v[184:185], %[t4] offset:27776\n\t" \
    "ds_read_b64_tr_b16 v[186:187], %[t4] offset:29440\n\t" \
    "ds_read_b64_tr_b16 v[192:193], %[t4] offset:27904\n\t" \
    "ds_read_b64_tr_b16 v[194:195], %[t4] offset:29568\n\t" \
    "ds_read_b64_tr_b16 v[180:181], %[t4] offset:41472\n\t" \
    "ds_read_b64_tr_b16 v[182:183], %[t4] offset:43136\n\t" \
    "ds_read_b64_tr_b16 v[188:189], %[t4] offset:41600\n\t" \
    "ds_read_b64_tr_b16 v[190:191], %[t4] offset:43264\n\t" \
    "ds_read_b64_tr_b16 v[196:197], %[t4] offset:41728\n\t" \
    "ds_read_b64_tr_b16 v[198:199], %[t4] offset:43392\n\t" \
    "s_waitcnt lgkmcnt(0)\n\t" \
    "s_barrier \n\t" \
    "ds_write_b128 %[wl], v[200:203] offset:0\n\t" \
    "ds_write_b128 %[wl], v[204:207] offset:1024\n\t" \
    "ds_write_b128 %[wl], v[208:211] offset:2048\n\t" \
    "ds_write_b128 %[wl], v[212:215] offset:3072\n\t" \
    "ds_write_b128 %[wl], v[216:219] offset:4096\n\t" \
    "ds_write_b128 %[wl], v[220:223] offset:5120\n\t" \
    "ds_write_b128 %[wl], v[224:227] offset:6144\n\t" \
    "ds_write_b128 %[wl], v[228:231] offset:7168\n\t" \
    "ds_write_b128 %[wl], v[232:235] offset:8192\n\t" \
    "ds_write_b128 %[wl], v[236:239] offset:9216\n\t" \
    "s_waitcnt lgkmcnt(0)\n\t"
#define ASM_WLOADO \
    "s_mov_b32 %[t], 0x0\n\t" \
    "buffer_load_dwordx4 v[200:203], %[o0], %[rs], %[t] offen" WNT "\n\t" \
    "buffer_load_dwordx4 v[16:19], %[o1], %[rs], %[t] offen" WNT "\n\t" \
    "buffer_load_dwordx4 v[208:211], %[o2], %[rs], %[t] offen" WNT "\n\t" \
    "buffer_load_dwordx4 v[56:59], %[o3], %[rs], %[t] offen" WNT "\n\t" \
    "buffer_load_dwordx4 v[216:219], %[o4], %[rs], %[t] offen" WNT "\n\t" \
    "s_mov_b32 %[t], 0xa000\n\t" \
    "buffer_load_dwordx4 v[96:99], %[o0], %[rs], %[t] offen" WNT "\n\t" \
    "buffer_load_dwordx4 v[224:227], %[o1], %[rs], %[t] offen" WNT "\n\t" \
    "buffer_load_dwordx4 v[136:139], %[o2], %[rs], %[t] offen" WNT "\n\t" \
    "buffer_load_dwordx4 v[232:235], %[o3], %[rs], %[t] offen" WNT "\n\t" \
    "buffer_load_dwordx4 v[176:179], %[o4], %[rs], %[t] offen" WNT "\n\t" \
    "s_mov_b32 %[t], 0x14000\n\t" \
    "buffer_load_dwordx4 v[204:207], %[o0], %[rs], %[t] offen" WNT "\n\t" \
    "buffer_load_dwordx4 v[20:23], %[o1], %[rs], %[t] offen" WNT "\n\t" \
    "buffer_load_dwordx4 v[212:215], %[o2], %[rs], %[t] offen" WNT "\n\t" \
    "buffer_load_dwordx4 v[60:63], %[o3], %[rs], %[t] offen" WNT "\n\t" \
    "buffer_load_dwordx4 v[220:223], %[o4], %[rs], %[t] offen" WNT "\n\t" \
    "s_mov_b32 %[t], 0x1e000\n\t" \
    "buffer_load_dwordx4 v[100:103], %[o0], %[rs], %[t] offen" WNT "\n\t" \
    "buffer_load_dwordx4 v[228:231], %[o1], %[rs], %[t] offen" WNT "\n\t" \
    "buffer_load_dwordx4 v[140:143], %[o2], %[rs], %[t] offen" WNT "\n\t" \
    "buffer_load_dwordx4 v[236:239], %[o3], %[rs], %[t] offen" WNT "\n\t" \
    "buffer_load_dwordx4 v[180:183], %[o4], %[rs], %[t] offen" WNT "\n\t" \
    "s_waitcnt vmcnt(15)\n\t" \
    "ds_write_b128 %[s0], v[200:203] offset:0\n\t" \
    "ds_write_b128 %[s1], v[16:19] offset:0\n\t" \
    "ds_write_b128 %[s2], v[208:211] offset:0\n\t" \
    "ds_write_b128 %[s3], v[56:59] offset:0\n\t" \
    "ds_write_b128 %[s4], v[216:219] offset:0\n\t" \
    "s_waitcnt vmcnt(10)\n\t" \
    "ds_write_b128 %[s0], v[96:99] offset:5632\n\t" \
    "ds_write_b128 %[s1], v[224:227] offset:5632\n\t" \
    "ds_write_b128 %[s2], v[136:139] offset:5632\n\t" \
    "ds_write_b128 %[s3], v[232:235] offset:5632\n\t" \
    "ds_write_b128 %[s4], v[176:179] offset:5632\n\t" \
    "ds_read_b64_tr_b16 v[200:201], %[tr] offset:0\n\t" \
    "ds_read_b64_tr_b16 v[202:203], %[tr] offset:5632\n\t" \
    "ds_read_b64_tr_b16 v[16:17], %[tr] offset:32\n\t" \
    "ds_read_b64_tr_b16 v[18:19], %[tr] offset:5664\n\t" \
    "ds_read_b64_tr_b16 v[208:209], %[tr] offset:64\n\t" \
    "ds_read_b64_tr_b16 v[210:211], %[tr] offset:5696\n\t" \
    "ds_read_b64_tr_b16 v[56:57], %[tr] offset:96\n\t" \
    "ds_read_b64_tr_b16 v[58:59], %[tr] offset:5728\n\t" \
    "ds_read_b64_tr_b16 v[216:217], %[tr] offset:128\n\t" \
    "ds_read_b64_tr_b16 v[218:219], %[tr] offset:5760\n\t" \
    "ds_read_b64_tr_b16 v[96:97], %[tr] offset:160\n\t" \
    "ds_read_b64_tr_b16 v[98:99], %[tr] offset:5792\n\t" \
    "ds_read_b64_tr_b16 v[224:225], %[tr] offset:192\n\t" \
    "ds_read_b64_tr_b16 v[226:227], %[tr] offset:5824\n\t" \
    "ds_read_b64_tr_b16 v[136:137], %[tr] offset:224\n\t" \
    "ds_read_b64_tr_b16 v[138:139], %[tr] offset:5856\n\t" \
    "ds_read_b64_tr_b16 v[232:233], %[tr] offset:256\n\t" \
    "ds_read_b64_tr_b16 v[234:235], %[tr] offset:5888\n\t" \
    "ds_read_b64_tr_b16 v[176:177], %[tr] offset:288\n\t" \
    "ds_read_b64_tr_b16 v[178:179], %[tr] offset:5920\n\t" \
    "s_barrier \n\t" \
    "s_mov_b32 %[t], 0x28000\n\t" \
    "buffer_load_dwordx4 v[0:3], %[o0], %[rs], %[t] offen" WNT "\n\t" \
    "buffer_load_dwordx4 v[24:27], %[o1], %[rs], %[t] offen" WNT "\n\t" \
    "buffer_load_dwordx4 v[40:43], %[o2], %[rs], %[t] offen" WNT "\n\t" \
    "buffer_load_dwordx4 v[64:67], %[o3], %[rs], %[t] offen" WNT "\n\t" \
    "buffer_load_dwordx4 v[80:83], %[o4], %[rs], %[t] offen" WNT "\n\t" \
    "s_mov_b32 %[t], 0x32000\n\t" \
    "buffer_load_dwordx4 v[104:107], %[o0], %[rs], %[t] offen" WNT "\n\t" \
    "buffer_load_dwordx4 v[120:123], %[o1], %[rs], %[t] offen" WNT "\n\t" \
    "buffer_load_dwordx4 v[144:147], %[o2], %[rs], %[t] offen" WNT "\n\t" \
    "buffer_load_dwordx4 v[160:163], %[o3], %[rs], %[t] offen" WNT "\n\t" \
    "buffer_load_dwordx4 v[184:187], %[o4], %[rs], %[t] offen" WNT "\n\t" \
    "s_waitcnt lgkmcnt(0)\n\t" \
    "s_barrier \n\t" \
    "s_waitcnt vmcnt(15)\n\t" \
    "ds_write_b128 %[s0], v[204:207] offset:0\n\t" \
    "ds_write_b128 %[s1], v[20:23] offset:0\n\t" \
    "ds_write_b128 %[s2], v[212:215] offset:0\n\t" \
    "ds_write_b128 %[s3], v[60:63] offset:0\n\t" \
    "ds_write_b128 %[s4], v[220:223] offset:0\n\t" \
    "s_waitcnt vmcnt(10)\n\t" \
    "ds_write_b128 %[s0], v[100:103] offset:5632\n\t" \
    "ds_write_b128 %[s1], v[228:231] offset:5632\n\t" \
    "ds_write_b128 %[s2], v[140:143] offset:5632\n\t" \
    "ds_write_b128 %[s3], v[236:239] offset:5632\n\t" \
    "ds_write_b128 %[s4], v[180:183] offset:5632\n\t" \
    "ds_read_b64_tr_b16 v[204:205], %[tr] offset:0\n\t" \
    "ds_read_b64_tr_b16 v[206:207], %[tr] offset:5632\n\t" \
    "ds_read_b64_tr_b16 v[20:21], %[tr] offset:32\n\t" \
    "ds_read_b64_tr_b16 v[22:23], %[tr] offset:5664\n\t" \
    "ds_read_b64_tr_b16 v[212:213], %[tr] offset:64\n\t" \
    "ds_read_b64_tr_b16 v[214:215], %[tr] offset:5696\n\t" \
    "ds_read_b64_tr_b16 v[60:61], %[tr] offset:96\n\t" \
    "ds_read_b64_tr_b16 v[62:63], %[tr] offset:5728\n\t" \
    "ds_read_b64_tr_b16 v[220:221], %[tr] offset:128\n\t" \
    "ds_read_b64_tr_b16 v[222:223], %[tr] offset:5760\n\t" \
    "ds_read_b64_tr_b16 v[100:101], %[tr] offset:160\n\t" \
    "ds_read_b64_tr_b16 v[102:103], %[tr] offset:5792\n\t" \
    "ds_read_b64_tr_b16 v[228:229], %[tr] offset:192\n\t" \
    "ds_read_b64_tr_b16 v[230:231], %[tr] offset:5824\n\t" \
    "ds_read_b64_tr_b16 v[140:141], %[tr] offset:224\n\t" \
    "ds_read_b64_tr_b16 v[142:143], %[tr] offset:5856\n\t" \
    "ds_read_b64_tr_b16 v[236:237], %[tr] offset:256\n\t" \
    "ds_read_b64_tr_b16 v[238:239], %[tr] offset:5888\n\t" \
    "ds_read_b64_tr_b16 v[180:181], %[tr] offset:288\n\t" \
    "ds_read_b64_tr_b16 v[182:183], %[tr] offset:5920\n\t" \
    "s_barrier \n\t" \
    "s_mov_b32 %[t], 0x5000\n\t" \
    "buffer_load_dwordx4 v[4:7], %[o0], %[rs], %[t] offen" WNT "\n\t" \
    "buffer_load_dwordx4 v[28:31], %[o1], %[rs], %[t] offen" WNT "\n\t" \
    "buffer_load_dwordx4 v[44:47], %[o2], %[rs], %[t] offen" WNT "\n\t" \
    "buffer_load_dwordx4 v[68:71], %[o3], %[rs], %[t] offen" WNT "\n\t" \
    "buffer_load_dwordx4 v[84:87], %[o4], %[rs], %[t] offen" WNT "\n\t" \
    "s_mov_b32 %[t], 0xf000\n\t" \
    "buffer_load_dwordx4 v[108:111], %[o0], %[rs], %[t] offen" WNT "\n\t" \
    "buffer_load_dwordx4 v[124:127], %[o1], %[rs], %[t] offen" WNT "\n\t" \
    "buffer_load_dwordx4 v[148:151], %[o2], %[rs], %[t] offen" WNT "\n\t" \
    "buffer_load_dwordx4 v[164:167], %[o3], %[rs], %[t] offen" WNT "\n\t" \
    "buffer_load_dwordx4 v[188:191], %[o4], %[rs], %[t] offen" WNT "\n\t" \
    "s_waitcnt lgkmcnt(0)\n\t" \
    "s_barrier \n\t" \
    "s_waitcnt vmcnt(15)\n\t" \
    "ds_write_b128 %[s0], v[0:3] offset:0\n\t" \
    "ds_write_b128 %[s1], v[24:27] offset:0\n\t" \
    "ds_write_b128 %[s2], v[40:43] offset:0\n\t" \
    "ds_write_b128 %[s3], v[64:67] offset:0\n\t" \
    "ds_write_b128 %[s4], v[80:83] offset:0\n\t" \
    "s_waitcnt vmcnt(10)\n\t" \
    "ds_write_b128 %[s0], v[104:107] offset:5632\n\t" \
    "ds_write_b128 %[s1], v[120:123] offset:5632\n\t" \
    "ds_write_b128 %[s2], v[144:147] offset:5632\n\t" \
    "ds_write_b128 %[s3], v[160:163] offset:5632\n\t" \
    "ds_write_b128 %[s4], v[184:187] offset:5632\n\t" \
    "ds_read_b64_tr_b16 v[0:1], %[tr] offset:0\n\t" \
    "ds_read_b64_tr_b16 v[2:3], %[tr] offset:5632\n\t" \
    "ds_read_b64_tr_b16 v[24:25], %[tr] offset:32\n\t" \
    "ds_read_b64_tr_b16 v[26:27], %[tr] offset:5664\n\t" \
    "ds_read_b64_tr_b16 v[40:41], %[tr] offset:64\n\t" \
    "ds_read_b64_tr_b16 v[42:43], %[tr] offset:5696\n\t" \
    "ds_read_b64_tr_b16 v[64:65], %[tr] offset:96\n\t" \
    "ds_read_b64_tr_b16 v[66:67], %[tr] offset:5728\n\t" \
    "ds_read_b64_tr_b16 v[80:81], %[tr] offset:128\n\t" \
    "ds_read_b64_tr_b16 v[82:83], %[tr] offset:5760\n\t" \
    "ds_read_b64_tr_b16 v[104:105], %[tr] offset:160\n\t" \
    "ds_read_b64_tr_b16 v[106:107], %[tr] offset:5792\n\t" \
    "ds_read_b64_tr_b16 v[120:121], %[tr] offset:192\n\t" \
    "ds_read_b64_tr_b16 v[122:123], %[tr] offset:5824\n\t" \
    "ds_read_b64_tr_b16 v[144:145], %[tr] offset:224\n\t" \
    "ds_read_b64_tr_b16 v[146:147], %[tr] offset:5856\n\t" \
    "ds_read_b64_tr_b16 v[160:161], %[tr] offset:256\n\t" \
    "ds_read_b64_tr_b16 v[162:163], %[tr] offset:5888\n\t" \
    "ds_read_b64_tr_b16 v[184:185], %[tr] offset:288\n\t" \
    "ds_read_b64_tr_b16 v[186:187], %[tr] offset:5920\n\t" \
    "s_barrier \n\t" \
    "s_mov_b32 %[t], 0x19000\n\t" \
    "buffer_load_dwordx4 v[8:11], %[o0], %[rs], %[t] offen" WNT "\n\t" \
    "buffer_load_dwordx4 v[32:35], %[o1], %[rs], %[t] offen" WNT "\n\t" \
    "buffer_load_dwordx4 v[48:51], %[o2], %[rs], %[t] offen" WNT "\n\t" \
    "buffer_load_dwordx4 v[72:75], %[o3], %[rs], %[t] offen" WNT "\n\t" \
    "buffer_load_dwordx4 v[88:91], %[o4], %[rs], %[t] offen" WNT "\n\t" \
    "s_mov_b32 %[t], 0x23000\n\t" \
    "buffer_load_dwordx4 v[112:115], %[o0], %[rs], %[t] offen" WNT "\n\t" \
    "buffer_load_dwordx4 v[128:131], %[o1], %[rs], %[t] offen" WNT "\n\t" \
    "buffer_load_dwordx4 v[152:155], %[o2], %[rs], %[t] offen" WNT "\n\t" \
    "buffer_load_dwordx4 v[168:171], %[o3], %[rs], %[t] offen" WNT "\n\t" \
    "buffer_load_dwordx4 v[192:195], %[o4], %[rs], %[t] offen" WNT "\n\t" \
    "s_waitcnt lgkmcnt(0)\n\t" \
    "s_barrier \n\t" \
    "s_waitcnt vmcnt(15)\n\t" \
    "ds_write_b128 %[s0], v[4:7] offset:0\n\t" \
    "ds_write_b128 %[s1], v[28:31] offset:0\n\t" \
    "ds_write_b128 %[s2], v[44:47] offset:0\n\t" \
    "ds_write_b128 %[s3], v[68:71] offset:0\n\t" \
    "ds_write_b128 %[s4], v[84:87] offset:0\n\t" \
    "s_waitcnt vmcnt(10)\n\t" \
    "ds_write_b128 %[s0], v[108:111] offset:5632\n\t" \
    "ds_write_b128 %[s1], v[124:127] offset:5632\n\t" \
    "ds_write_b128 %[s2], v[148:151] offset:5632\n\t" \
    "ds_write_b128 %[s3], v[164:167] offset:5632\n\t" \
    "ds_write_b128 %[s4], v[188:191] offset:5632\n\t" \
    "ds_read_b64_tr_b16 v[4:5], %[tr] offset:0\n\t" \
    "ds_read_b64_tr_b16 v[6:7], %[tr] offset:5632\n\t" \
    "ds_read_b64_tr_b16 v[28:29], %[tr] offset:32\n\t" \
    "ds_read_b64_tr_b16 v[30:31], %[tr] offset:5664\n\t" \
    "ds_read_b64_tr_b16 v[44:45], %[tr] offset:64\n\t" \
    "ds_read_b64_tr_b16 v[46:47], %[tr] offset:5696\n\t" \
    "ds_read_b64_tr_b16 v[68:69], %[tr] offset:96\n\t" \
    "ds_read_b64_tr_b16 v[70:71], %[tr] offset:5728\n\t" \
    "ds_read_b64_tr_b16 v[84:85], %[tr] offset:128\n\t" \
    "ds_read_b64_tr_b16 v[86:87], %[tr] offset:5760\n\t" \
    "ds_read_b64_tr_b16 v[108:109], %[tr] offset:160\n\t" \
    "ds_read_b64_tr_b16 v[110:111], %[tr] offset:5792\n\t" \
    "ds_read_b64_tr_b16 v[124:125], %[tr] offset:192\n\t" \
    "ds_read_b64_tr_b16 v[126:127], %[tr] offset:5824\n\t" \
    "ds_read_b64_tr_b16 v[148:149], %[tr] offset:224\n\t" \
    "ds_read_b64_tr_b16 v[150:151], %[tr] offset:5856\n\t" \
    "ds_read_b64_tr_b16 v[164:165], %[tr] offset:256\n\t" \
    "ds_read_b64_tr_b16 v[166:167], %[tr] offset:5888\n\t" \
    "ds_read_b64_tr_b16 v[188:189], %[tr] offset:288\n\t" \
    "ds_read_b64_tr_b16 v[190:191], %[tr] offset:5920\n\t" \
    "s_barrier \n\t" \
    "s_mov_b32 %[t], 0x2d000\n\t" \
    "buffer_load_dwordx4 v[12:15], %[o0], %[rs], %[t] offen" WNT "\n\t" \
    "buffer_load_dwordx4 v[36:39], %[o1], %[rs], %[t] offen" WNT "\n\t" \
    "buffer_load_dwordx4 v[52:55], %[o2], %[rs], %[t] offen" WNT "\n\t" \
    "buffer_load_dwordx4 v[76:79], %[o3], %[rs], %[t] offen" WNT "\n\t" \
    "buffer_load_dwordx4 v[92:95], %[o4], %[rs], %[t] offen" WNT "\n\t" \
    "s_mov_b32 %[t], 0x37000\n\t" \
    "buffer_load_dwordx4 v[116:119], %[o0], %[rs], %[t] offen" WNT "\n\t" \
    "buffer_load_dwordx4 v[132:135], %[o1], %[rs], %[t] offen" WNT "\n\t" \
    "buffer_load_dwordx4 v[156:159], %[o2], %[rs], %[t] offen" WNT "\n\t" \
    "buffer_load_dwordx4 v[172:175], %[o3], %[rs], %[t] offen" WNT "\n\t" \
    "buffer_load_dwordx4 v[196:199], %[o4], %[rs], %[t] offen" WNT "\n\t" \
    "s_waitcnt lgkmcnt(0)\n\t" \
    "s_barrier \n\t" \
    "s_waitcnt vmcnt(15)\n\t" \
    "ds_write_b128 %[s0], v[8:11] offset:0\n\t" \
    "ds_write_b128 %[s1], v[32:35] offset:0\n\t" \
    "ds_write_b128 %[s2], v[48:51] offset:0\n\t" \
    "ds_write_b128 %[s3], v[72:75] offset:0\n\t" \
    "ds_write_b128 %[s4], v[88:91] offset:0\n\t" \
    "s_waitcnt vmcnt(10)\n\t" \
    "ds_write_b128 %[s0], v[112:115] offset:5632\n\t" \
    "ds_write_b128 %[s1], v[128:131] offset:5632\n\t" \
    "ds_write_b128 %[s2], v[152:155] offset:5632\n\t" \
    "ds_write_b128 %[s3], v[168:171] offset:5632\n\t" \
    "ds_write_b128 %[s4], v[192:195] offset:5632\n\t" \
    "ds_read_b64_tr_b16 v[8:9], %[tr] offset:0\n\t" \
    "ds_read_b64_tr_b16 v[10:11], %[tr] offset:5632\n\t" \
    "ds_read_b64_tr_b16 v[32:33], %[tr] offset:32\n\t" \
    "ds_read_b64_tr_b16 v[34:35], %[tr] offset:5664\n\t" \
    "ds_read_b64_tr_b16 v[48:49], %[tr] offset:64\n\t" \
    "ds_read_b64_tr_b16 v[50:51], %[tr] offset:5696\n\t" \
    "ds_read_b64_tr_b16 v[72:73], %[tr] offset:96\n\t" \
    "ds_read_b64_tr_b16 v[74:75], %[tr] offset:5728\n\t" \
    "ds_read_b64_tr_b16 v[88:89], %[tr] offset:128\n\t" \
    "ds_read_b64_tr_b16 v[90:91], %[tr] offset:5760\n\t" \
    "ds_read_b64_tr_b16 v[112:113], %[tr] offset:160\n\t" \
    "ds_read_b64_tr_b16 v[114:115], %[tr] offset:5792\n\t" \
    "ds_read_b64_tr_b16 v[128:129], %[tr] offset:192\n\t" \
    "ds_read_b64_tr_b16 v[130:131], %[tr] offset:5824\n\t" \
    "ds_read_b64_tr_b16 v[152:153], %[tr] offset:224\n\t" \
    "ds_read_b64_tr_b16 v[154:155], %[tr] offset:5856\n\t" \
    "ds_read_b64_tr_b16 v[168:169], %[tr] offset:256\n\t" \
    "ds_read_b64_tr_b16 v[170:171], %[tr] offset:5888\n\t" \
    "ds_read_b64_tr_b16 v[192:193], %[tr] offset:288\n\t" \
    "ds_read_b64_tr_b16 v[194:195], %[tr] offset:5920\n\t" \
    "s_waitcnt lgkmcnt(0)\n\t" \
    "s_barrier \n\t" \
    "s_waitcnt vmcnt(5)\n\t" \
    "ds_write_b128 %[s0], v[12:15] offset:0\n\t" \
    "ds_write_b128 %[s1], v[36:39] offset:0\n\t" \
    "ds_write_b128 %[s2], v[52:55] offset:0\n\t" \
    "ds_write_b128 %[s3], v[76:79] offset:0\n\t" \
    "ds_write_b128 %[s4], v[92:95] offset:0\n\t" \
    "s_waitcnt vmcnt(0)\n\t" \
    "ds_write_b128 %[s0], v[116:119] offset:5632\n\t" \
    "ds_write_b128 %[s1], v[132:135] offset:5632\n\t" \
    "ds_write_b128 %[s2], v[156:159] offset:5632\n\t" \
    "ds_write_b128 %[s3], v[172:175] offset:5632\n\t" \
    "ds_write_b128 %[s4], v[196:199] offset:5632\n\t" \
    "ds_read_b64_tr_b16 v[12:13], %[tr] offset:0\n\t" \
    "ds_read_b64_tr_b16 v[14:15], %[tr] offset:5632\n\t" \
    "ds_read_b64_tr_b16 v[36:37], %[tr] offset:32\n\t" \
    "ds_read_b64_tr_b16 v[38:39], %[tr] offset:5664\n\t" \
    "ds_read_b64_tr_b16 v[52:53], %[tr] offset:64\n\t" \
    "ds_read_b64_tr_b16 v[54:55], %[tr] offset:5696\n\t" \
    "ds_read_b64_tr_b16 v[76:77], %[tr] offset:96\n\t" \
    "ds_read_b64_tr_b16 v[78:79], %[tr] offset:5728\n\t" \
    "ds_read_b64_tr_b16 v[92:93], %[tr] offset:128\n\t" \
    "ds_read_b64_tr_b16 v[94:95], %[tr] offset:5760\n\t" \
    "ds_read_b64_tr_b16 v[116:117], %[tr] offset:160\n\t" \
    "ds_read_b64_tr_b16 v[118:119], %[tr] offset:5792\n\t" \
    "ds_read_b64_tr_b16 v[132:133], %[tr] offset:192\n\t" \
    "ds_read_b64_tr_b16 v[134:135], %[tr] offset:5824\n\t" \
    "ds_read_b64_tr_b16 v[156:157], %[tr] offset:224\n\t" \
    "ds_read_b64_tr_b16 v[158:159], %[tr] offset:5856\n\t" \
    "ds_read_b64_tr_b16 v[172:173], %[tr] offset:256\n\t" \
    "ds_read_b64_tr_b16 v[174:175], %[tr] offset:5888\n\t" \
    "ds_read_b64_tr_b16 v[196:197], %[tr] offset:288\n\t" \
    "ds_read_b64_tr_b16 v[198:199], %[tr] offset:5920\n\t" \
    "s_waitcnt lgkmcnt(0)\n\t" \
    "s_barrier \n\t" \
    "ds_write_b128 %[wl], v[200:203] offset:0\n\t" \
    "ds_write_b128 %[wl], v[204:207] offset:1024\n\t" \
    "ds_write_b128 %[wl], v[208:211] offset:2048\n\t" \
    "ds_write_b128 %[wl], v[212:215] offset:3072\n\t" \
    "ds_write_b128 %[wl], v[216:219] offset:4096\n\t" \
    "ds_write_b128 %[wl], v[220:223] offset:5120\n\t" \
    "ds_write_b128 %[wl], v[224:227] offset:6144\n\t" \
    "ds_write_b128 %[wl], v[228:231] offset:7168\n\t" \
    "ds_write_b128 %[wl], v[232:235] offset:8192\n\t" \
    "ds_write_b128 %[wl], v[236:239] offset:9216\n\t" \
    "s_waitcnt lgkmcnt(0)\n\t"
#define ASM_XG_S_LO \
    "s_waitcnt lgkmcnt(0)\n\t" \
    "ds_read_b128 v[224:227], %[xa] offset:16384\n\t" \
    "ds_read_b128 v[228:231], %[xa] offset:17408\n\t" \
    "ds_read_b128 v[244:247], %[wl] offset:0\n\t" \
    "ds_read_b128 v[248:251], %[wl] offset:1024\n\t" \
    "ds_read_b128 v[232:235], %[xa] offset:0\n\t" \
    "ds_read_b128 v[236:239], %[xa] offset:1024\n\t" \
    "v_xor_b32 v252, 0x800, %[xa]\n\t" \
    "ds_read_b128 v[240:243], v252 offset:0\n\t" \
    "s_waitcnt lgkmcnt(6)\n\t" \
    "v_mfma_f32_16x16x32_bf16 v[220:223], v[8:11], v[224:227], 0\n\t" \
    "s_waitcnt lgkmcnt(5)\n\t" \
    "v_mfma_f32_16x16x32_bf16 v[220:223], v[12:15], v[228:231], v[220:223]\n\t" \
    "v_mfma_f32_16x16x32_bf16 v[216:219], v[0:3], v[224:227], 0\n\t" \
    "v_mfma_f32_16x16x32_bf16 v[216:219], v[4:7], v[228:231], v[216:219]\n\t" \
    "s_waitcnt lgkmcnt(4)\n\t" \
    "v_mfma_f32_16x16x32_bf16 v[212:215], v[244:247], v[224:227], 0\n\t" \
    "ds_read_b128 v[224:227], v252 offset:1024\n\t" \
    "s_waitcnt lgkmcnt(4)\n\t" \
    "v_mfma_f32_16x16x32_bf16 v[212:215], v[248:251], v[228:231], v[212:215]\n\t" \
    "ds_read_b128 v[228:231], v252 offset:16384\n\t" \
    "s_waitcnt lgkmcnt(4)\n\t" \
    "v_mfma_f32_16x16x32_bf16 v[200:203], v[244:247], v[232:235], 0\n\t" \
    "ds_read_b128 v[244:247], %[wl] offset:2048\n\t" \
    "s_waitcnt lgkmcnt(4)\n\t" \
    "v_mfma_f32_16x16x32_bf16 v[200:203], v[248:251], v[236:239], v[200:203]\n\t" \
    "ds_read_b128 v[248:251], %[wl] offset:3072\n\t" \
    "v_mfma_f32_16x16x32_bf16 v[204:207], v[0:3], v[232:235], 0\n\t" \
    "v_mfma_f32_16x16x32_bf16 v[204:207], v[4:7], v[236:239], v[204:207]\n\t" \
    "v_mfma_f32_16x16x32_bf16 v[208:211], v[8:11], v[232:235], 0\n\t" \
    "ds_read_b128 v[232:235], v252 offset:17408\n\t" \
    "v_mfma_f32_16x16x32_bf16 v[208:211], v[12:15], v[236:239], v[208:211]\n\t" \
    "v_xor_b32 v252, 0x1000, %[xa]\n\t" \
    "ds_read_b128 v[236:239], v252 offset:16384\n\t" \
    "s_waitcnt lgkmcnt(6)\n\t" \
    "v_mfma_f32_16x16x32_bf16 v[208:211], v[32:35], v[240:243], v[208:211]\n\t" \
    "s_waitcnt lgkmcnt(5)\n\t" \
    "v_mfma_f32_16x16x32_bf16 v[208:211], v[36:39], v[224:227], v[208:211]\n\t" \
    "v_mfma_f32_16x16x32_bf16 v[204:207], v[24:27], v[240:243], v[204:207]\n\t" \
    "v_mfma_f32_16x16x32_bf16 v[204:207], v[28:31], v[224:227], v[204:207]\n\t" \
    "v_mfma_f32_16x16x32_bf16 v[200:203], v[16:19], v[240:243], v[200:203]\n\t" \
    "ds_read_b128 v[240:243], v252 offset:17408\n\t" \
    "v_mfma_f32_16x16x32_bf16 v[200:203], v[20:23], v[224:227], v[200:203]\n\t" \
    "ds_read_b128 v[224:227], v252 offset:0\n\t" \
    "s_waitcnt lgkmcnt(6)\n\t" \
    "v_mfma_f32_16x16x32_bf16 v[212:215], v[16:19], v[228:231], v[212:215]\n\t" \
    "s_waitcnt lgkmcnt(3)\n\t" \
    "v_mfma_f32_16x16x32_bf16 v[212:215], v[20:23], v[232:235], v[212:215]\n\t" \
    "v_mfma_f32_16x16x32_bf16 v[216:219], v[24:27], v[228:231], v[216:219]\n\t" \
    "v_mfma_f32_16x16x32_bf16 v[216:219], v[28:31], v[232:235], v[216:219]\n\t" \
    "v_mfma_f32_16x16x32_bf16 v[220:223], v[32:35], v[228:231], v[220:223]\n\t" \
    "ds_read_b128 v[228:231], v252 offset:1024\n\t" \
    "v_mfma_f32_16x16x32_bf16 v[220:223], v[36:39], v[232:235], v[220:223]\n\t" \
    "v_xor_b32 v252, 0x1800, %[xa]\n\t" \
    "ds_read_b128 v[232:235], v252 offset:0\n\t" \
    "s_waitcnt lgkmcnt(4)\n\t" \
    "v_mfma_f32_16x16x32_bf16 v[220:223], v[48:51], v[236:239], v[220:223]\n\t" \
    "s_waitcnt lgkmcnt(3)\n\t" \
    "v_mfma_f32_16x16x32_bf16 v[220:223], v[52:55], v[240:243], v[220:223]\n\t" \
    "v_mfma_f32_16x16x32_bf16 v[216:219], v[40:43], v[236:239], v[216:219]\n\t" \
    "v_mfma_f32_16x16x32_bf16 v[216:219], v[44:47], v[240:243], v[216:219]\n\t" \
    "v_mfma_f32_16x16x32_bf16 v[212:215], v[244:247], v[236:239], v[212:215]\n\t" \
    "ds_read_b128 v[236:239], v252 offset:1024\n\t" \
    "v_mfma_f32_16x16x32_bf16 v[212:215], v[248:251], v[240:243], v[212:215]\n\t" \
    "ds_read_b128 v[240:243], v252 offset:16384\n\t" \
    "s_waitcnt lgkmcnt(4)\n\t" \
    "v_mfma_f32_16x16x32_bf16 v[200:203], v[244:247], v[224:227], v[200:203]\n\t" \
    "ds_read_b128 v[244:247], %[wl] offset:8192\n\t" \
    "s_waitcnt lgkmcnt(4)\n\t" \
    "v_mfma_f32_16x16x32_bf16 v[200:203], v[248:251], v[228:231], v[200:203]\n\t" \
    "ds_read_b128 v[248:251], %[wl] offset:9216\n\t" \
    "v_mfma_f32_16x16x32_bf16 v[204:207], v[40:43], v[224:227], v[204:207]\n\t" \
    "v_mfma_f32_16x16x32_bf16 v[204:207], v[44:47], v[228:231], v[204:207]\n\t" \
    "v_mfma_f32_16x16x32_bf16 v[208:211], v[48:51], v[224:227], v[208:211]\n\t" \
    "ds_read_b128 v[224:227], v252 offset:17408\n\t" \
    "v_mfma_f32_16x16x32_bf16 v[208:211], v[52:55], v[228:231], v[208:211]\n\t" \
    "v_and_b32 v252, 0x3ff, %[xa]\n\t" \
    "ds_read_b128 v[228:231], v252 offset:45056\n\t" \
    "s_waitcnt lgkmcnt(6)\n\t" \
    "v_mfma_f32_16x16x32_bf16 v[208:211], v[72:75], v[232:235], v[208:211]\n\t" \
    "s_waitcnt lgkmcnt(5)\n\t" \
    "v_mfma_f32_16x16x32_bf16 v[208:211], v[76:79], v[236:239], v[208:211]\n\t" \
    "v_mfma_f32_16x16x32_bf16 v[204:207], v[64:67], v[232:235], v[204:207]\n\t" \
    "v_mfma_f32_16x16x32_bf16 v[204:207], v[68:71], v[236:239], v[204:207]\n\t" \
    "v_mfma_f32_16x16x32_bf16 v[200:203], v[56:59], v[232:235], v[200:203]\n\t" \
    "ds_read_b128 v[232:235], v252 offset:46080\n\t" \
    "v_mfma_f32_16x16x32_bf16 v[200:203], v[60:63], v[236:239], v[200:203]\n\t" \
    "ds_read_b128 v[236:239], v252 offset:40960\n\t" \
    "s_waitcnt lgkmcnt(6)\n\t" \
    "v_mfma_f32_16x16x32_bf16 v[212:215], v[56:59], v[240:243], v[212:215]\n\t" \
    "s_waitcnt lgkmcnt(3)\n\t" \
    "v_mfma_f32_16x16x32_bf16 v[212:215], v[60:63], v[224:227], v[212:215]\n\t" \
    "v_mfma_f32_16x16x32_bf16 v[216:219], v[64:67], v[240:243], v[216:219]\n\t" \
    "v_mfma_f32_16x16x32_bf16 v[216:219], v[68:71], v[224:227], v[216:219]\n\t" \
    "v_mfma_f32_16x16x32_bf16 v[220:223], v[72:75], v[240:243], v[220:223]\n\t" \
    "ds_read_b128 v[240:243], v252 offset:41984\n\t" \
    "v_mfma_f32_16x16x32_bf16 v[220:223], v[76:79], v[224:227], v[220:223]\n\t" \
    "s_waitcnt lgkmcnt(0)\n\t" \
    "s_barrier\n\t" ASM_ST \
    "v_xor_b32 v252, 0x2000, %[xa]\n\t" \
    "ds_read_b128 v[224:227], v252 offset:0\n\t" \
    "v_mfma_f32_16x16x32_bf16 v[220:223], v[168:171], v[228:231], v[220:223]\n\t" \
    "v_mfma_f32_16x16x32_bf16 v[220:223], v[172:175], v[232:235], v[220:223]\n\t" \
    "v_mfma_f32_16x16x32_bf16 v[216:219], v[160:163], v[228:231], v[216:219]\n\t" \
    "v_mfma_f32_16x16x32_bf16 v[216:219], v[164:167], v[232:235], v[216:219]\n\t" \
    "v_mfma_f32_16x16x32_bf16 v[212:215], v[244:247], v[228:231], v[212:215]\n\t" \
    "ds_read_b128 v[228:231], v252 offset:1024\n\t" \
    "v_mfma_f32_16x16x32_bf16 v[212:215], v[248:251], v[232:235], v[212:215]\n\t" \
    "ds_read_b128 v[232:235], v252 offset:16384\n\t" \
    "v_mfma_f32_16x16x32_bf16 v[200:203], v[244:247], v[236:239], v[200:203]\n\t" \
    "ds_read_b128 v[244:247], %[wl] offset:4096\n\t" \
    "v_mfma_f32_16x16x32_bf16 v[200:203], v[248:251], v[240:243], v[200:203]\n\t" \
    "ds_read_b128 v[248:251], %[wl] offset:5120\n\t" \
    "v_mfma_f32_16x16x32_bf16 v[204:207], v[160:163], v[236:239], v[204:207]\n\t" \
    "v_mfma_f32_16x16x32_bf16 v[204:207], v[164:167], v[240:243], v[204:207]\n\t" \
    "v_mfma_f32_16x16x32_bf16 v[208:211], v[168:171], v[236:239], v[208:211]\n\t" \
    "ds_read_b128 v[236:239], v252 offset:17408\n\t" \
    "v_mfma_f32_16x16x32_bf16 v[208:211], v[172:175], v[240:243], v[208:211]\n\t" \
    "v_xor_b32 v252, 0x2800, %[xa]\n\t" \
    "ds_read_b128 v[240:243], v252 offset:16384\n\t" \
    "s_waitcnt lgkmcnt(6)\n\t" \
    "v_mfma_f32_16x16x32_bf16 v[208:211], v[88:91], v[224:227], v[208:211]\n\t" \
    "s_waitcnt lgkmcnt(5)\n\t" \
    "v_mfma_f32_16x16x32_bf16 v[208:211], v[92:95], v[228:231], v[208:211]\n\t" \
    "v_mfma_f32_16x16x32_bf16 v[204:207], v[80:83], v[224:227], v[204:207]\n\t" \
    "v_mfma_f32_16x16x32_bf16 v[204:207], v[84:87], v[228:231], v[204:207]\n\t" \
    "s_waitcnt lgkmcnt(3)\n\t" \
    "v_mfma_f32_16x16x32_bf16 v[200:203], v[244:247], v[224:227], v[200:203]\n\t" \
    "ds_read_b128 v[224:227], v252 offset:17408\n\t" \
    "s_waitcnt lgkmcnt(3)\n\t" \
    "v_mfma_f32_16x16x32_bf16 v[200:203], v[248:251], v[228:231], v[200:203]\n\t" \
    "ds_read_b128 v[228:231], v252 offset:0\n\t" \
    "v_mfma_f32_16x16x32_bf16 v[212:215], v[244:247], v[232:235], v[212:215]\n\t" \
    "ds_read_b128 v[244:247], %[wl] offset:6144\n\t" \
    "s_waitcnt lgkmcnt(4)\n\t" \
    "v_mfma_f32_16x16x32_bf16 v[212:215], v[248:251], v[236:239], v[212:215]\n\t" \
    "ds_read_b128 v[248:251], %[wl] offset:7168\n\t" \
    "v_mfma_f32_16x16x32_bf16 v[216:219], v[80:83], v[232:235], v[216:219]\n\t" \
    "v_mfma_f32_16x16x32_bf16 v[216:219], v[84:87], v[236:239], v[216:219]\n\t" \
    "v_mfma_f32_16x16x32_bf16 v[220:223], v[88:91], v[232:235], v[220:223]\n\t" \
    "ds_read_b128 v[232:235], v252 offset:1024\n\t" \
    "v_mfma_f32_16x16x32_bf16 v[220:223], v[92:95], v[236:239], v[220:223]\n\t" \
    "v_xor_b32 v252, 0x3000, %[xa]\n\t" \
    "ds_read_b128 v[236:239], v252 offset:0\n\t" \
    "s_waitcnt lgkmcnt(6)\n\t" \
    "v_mfma_f32_16x16x32_bf16 v[220:223], v[112:115], v[240:243], v[220:223]\n\t" \
    "s_waitcnt lgkmcnt(5)\n\t" \
    "v_mfma_f32_16x16x32_bf16 v[220:223], v[116:119], v[224:227], v[220:223]\n\t" \
    "v_mfma_f32_16x16x32_bf16 v[216:219], v[104:107], v[240:243], v[216:219]\n\t" \
    "v_mfma_f32_16x16x32_bf16 v[216:219], v[108:111], v[224:227], v[216:219]\n\t" \
    "v_mfma_f32_16x16x32_bf16 v[212:215], v[96:99], v[240:243], v[212:215]\n\t" \
    "ds_read_b128 v[240:243], v252 offset:1024\n\t" \
    "v_mfma_f32_16x16x32_bf16 v[212:215], v[100:103], v[224:227], v[212:215]\n\t" \
    "ds_read_b128 v[224:227], v252 offset:16384\n\t" \
    "s_waitcnt lgkmcnt(6)\n\t" \
    "v_mfma_f32_16x16x32_bf16 v[200:203], v[96:99], v[228:231], v[200:203]\n\t" \
    "s_waitcnt lgkmcnt(3)\n\t" \
    "v_mfma_f32_16x16x32_bf16 v[200:203], v[100:103], v[232:235], v[200:203]\n\t" \
    "v_mfma_f32_16x16x32_bf16 v[204:207], v[104:107], v[228:231], v[204:207]\n\t" \
    "v_mfma_f32_16x16x32_bf16 v[204:207], v[108:111], v[232:235], v[204:207]\n\t" \
    "v_mfma_f32_16x16x32_bf16 v[208:211], v[112:115], v[228:231], v[208:211]\n\t" \
    "ds_read_b128 v[228:231], v252 offset:17408\n\t" \
    "v_mfma_f32_16x16x32_bf16 v[208:211], v[116:119], v[232:235], v[208:211]\n\t" \
    "v_xor_b32 v252, 0x3800, %[xa]\n\t" \
    "ds_read_b128 v[232:235], v252 offset:16384\n\t" \
    "s_waitcnt lgkmcnt(4)\n\t" \
    "v_mfma_f32_16x16x32_bf16 v[208:211], v[128:131], v[236:239], v[208:211]\n\t" \
    "s_waitcnt lgkmcnt(3)\n\t" \
    "v_mfma_f32_16x16x32_bf16 v[208:211], v[132:135], v[240:243], v[208:211]\n\t" \
    "v_mfma_f32_16x16x32_bf16 v[204:207], v[120:123], v[236:239], v[204:207]\n\t" \
    "v_mfma_f32_16x16x32_bf16 v[204:207], v[124:127], v[240:243], v[204:207]\n\t" \
    "v_mfma_f32_16x16x32_bf16 v[200:203], v[244:247], v[236:239], v[200:203]\n\t" \
    "ds_read_b128 v[236:239], v252 offset:17408\n\t" \
    "v_mfma_f32_16x16x32_bf16 v[200:203], v[248:251], v[240:243], v[200:203]\n\t" \
    "ds_read_b128 v[240:243], v252 offset:0\n\t" \
    "s_waitcnt lgkmcnt(4)\n\t" \
    "v_mfma_f32_16x16x32_bf16 v[212:215], v[244:247], v[224:227], v[212:215]\n\t" \
    "s_waitcnt lgkmcnt(3)\n\t" \
    "v_mfma_f32_16x16x32_bf16 v[212:215], v[248:251], v[228:231], v[212:215]\n\t" \
    "v_mfma_f32_16x16x32_bf16 v[216:219], v[120:123], v[224:227], v[216:219]\n\t" \
    "v_mfma_f32_16x16x32_bf16 v[216:219], v[124:127], v[228:231], v[216:219]\n\t" \
    "v_mfma_f32_16x16x32_bf16 v[220:223], v[128:131], v[224:227], v[220:223]\n\t" \
    "ds_read_b128 v[224:227], v252 offset:1024\n\t" \
    "v_mfma_f32_16x16x32_bf16 v[220:223], v[132:135], v[228:231], v[220:223]\n\t" \
    "v_and_b32 v252, 0x3ff, %[xa]\n\t" \
    "ds_read_b128 v[228:231], v252 offset:43008\n\t" \
    "s_waitcnt lgkmcnt(4)\n\t" \
    "v_mfma_f32_16x16x32_bf16 v[220:223], v[152:155], v[232:235], v[220:223]\n\t" \
    "v_mov_b64 v[244:245], 0\n\t" \
    "s_waitcnt lgkmcnt(3)\n\t" \
    "v_mfma_f32_16x16x32_bf16 v[220:223], v[156:159], v[236:239], v[220:223]\n\t" \
    "v_mov_b64 v[246:247], 0\n\t" \
    "v_mfma_f32_16x16x32_bf16 v[216:219], v[144:147], v[232:235], v[216:219]\n\t" \
    "v_mov_b64 v[248:249], 0\n\t" \
    "v_mfma_f32_16x16x32_bf16 v[216:219], v[148:151], v[236:239], v[216:219]\n\t" \
    "v_mov_b64 v[250:251], 0\n\t" \
    "v_mfma_f32_16x16x32_bf16 v[212:215], v[136:139], v[232:235], v[212:215]\n\t" \
    "ds_read_b128 v[232:235], v252 offset:44032\n\t" \
    "v_mfma_f32_16x16x32_bf16 v[212:215], v[140:143], v[236:239], v[212:215]\n\t" \
    "ds_read_b128 v[236:239], v252 offset:47104\n\t" \
    "s_waitcnt lgkmcnt(4)\n\t" \
    "v_mfma_f32_16x16x32_bf16 v[200:203], v[136:139], v[240:243], v[200:203]\n\t" \
    "s_waitcnt lgkmcnt(3)\n\t" \
    "v_mfma_f32_16x16x32_bf16 v[200:203], v[140:143], v[224:227], v[200:203]\n\t" \
    "v_mfma_f32_16x16x32_bf16 v[204:207], v[144:147], v[240:243], v[204:207]\n\t" \
    "v_mfma_f32_16x16x32_bf16 v[204:207], v[148:151], v[224:227], v[204:207]\n\t" \
    "v_mfma_f32_16x16x32_bf16 v[208:211], v[152:155], v[240:243], v[208:211]\n\t" \
    "ds_read_b128 v[240:243], v252 offset:48128\n\t" \
    "v_mfma_f32_16x16x32_bf16 v[208:211], v[156:159], v[224:227], v[208:211]\n\t" \
    "s_waitcnt lgkmcnt(3)\n\t" \
    "v_mfma_f32_16x16x32_bf16 v[208:211], v[192:195], v[228:231], v[208:211]\n\t" \
    "v_add_u32 v252, %[qs], v252\n\t" \
    "v_mov_b64 v[224:225], 0\n\t" \
    "s_waitcnt lgkmcnt(2)\n\t" \
    "v_mfma_f32_16x16x32_bf16 v[208:211], v[196:199], v[232:235], v[208:211]\n\t" \
    "v_mov_b64 v[226:227], 0\n\t" \
    "v_mfma_f32_16x16x32_bf16 v[204:207], v[184:187], v[228:231], v[204:207]\n\t" \
    "v_mfma_f32_16x16x32_bf16 v[204:207], v[188:191], v[232:235], v[204:207]\n\t" \
    "v_mfma_f32_16x16x32_bf16 v[200:203], v[176:179], v[228:231], v[200:203]\n\t" \
    "v_mfma_f32_16x16x32_bf16 v[200:203], v[180:183], v[232:235], v[200:203]\n\t" \
    "s_waitcnt lgkmcnt(1)\n\t" \
    "v_mfma_f32_16x16x32_bf16 v[212:215], v[176:179], v[236:239], v[212:215]\n\t" \
    "ds_read_b128 v[228:231], %[wl] offset:2048\n\t" \
    "ds_read_b128 v[232:235], %[wl] offset:3072\n\t" \
    "s_waitcnt lgkmcnt(2)\n\t" \
    "v_mfma_f32_16x16x32_bf16 v[212:215], v[180:183], v[240:243], v[212:215]\n\t" \
    "v_cvt_pk_bf16_f32 v208, v208, v209\n\t" \
    "v_cvt_pk_bf16_f32 v209, v210, v211\n\t" \
    "v_mfma_f32_16x16x32_bf16 v[216:219], v[184:187], v[236:239], v[216:219]\n\t" \
    "ds_write_b64 v252, v[208:209] offset:2048\n\t" \
    "v_cvt_pk_bf16_f32 v204, v204, v205\n\t" \
    "v_mfma_f32_16x16x32_bf16 v[216:219], v[188:191], v[240:243], v[216:219]\n\t" \
    "v_cvt_pk_bf16_f32 v205, v206, v207\n\t" \
    "ds_write_b64 v252, v[204:205] offset:1024\n\t" \
    "v_mfma_f32_16x16x32_bf16 v[220:223], v[192:195], v[236:239], v[220:223]\n\t" \
    "v_cvt_pk_bf16_f32 v200, v200, v201\n\t" \
    "v_cvt_pk_bf16_f32 v201, v202, v203\n\t" \
    "v_mfma_f32_16x16x32_bf16 v[220:223], v[196:199], v[240:243], v[220:223]\n\t" \
    "ds_write_b64 v252, v[200:201] offset:0\n\t" \
    "ds_read_b128 v[200:203], %[wl] offset:0\n\t" \
    "ds_read_b128 v[204:207], %[wl] offset:1024\n\t" \
    "v_cvt_pk_bf16_f32 v212, v212, v213\n\t" \
    "v_cvt_pk_bf16_f32 v213, v214, v215\n\t" \
    "ds_write_b64 v252, v[212:213] offset:6144\n\t" \
    "v_cvt_pk_bf16_f32 v216, v216, v217\n\t" \
    "v_cvt_pk_bf16_f32 v217, v218, v219\n\t" \
    "ds_write_b64 v252, v[216:217] offset:7168\n\t" \
    "v_cvt_pk_bf16_f32 v220, v220, v221\n\t" \
    "v_cvt_pk_bf16_f32 v221, v222, v223\n\t" \
    "ds_write_b64 v252, v[220:221] offset:8192\n\t" \
    "s_waitcnt lgkmcnt(0)\n\t"
#define ASM_XG_S_HI \
    "s_waitcnt lgkmcnt(0)\n\t" \
    "v_xor_b32 v252, 0x2000, %[xa]\n\t" \
    "ds_read_b128 v[224:227], v252 offset:16384\n\t" \
    "ds_read_b128 v[228:231], v252 offset:17408\n\t" \
    "ds_read_b128 v[244:247], %[wl] offset:4096\n\t" \
    "ds_read_b128 v[248:251], %[wl] offset:5120\n\t" \
    "ds_read_b128 v[232:235], v252 offset:0\n\t" \
    "ds_read_b128 v[236:239], v252 offset:1024\n\t" \
    "v_xor_b32 v252, 0x2800, %[xa]\n\t" \
    "ds_read_b128 v[240:243], v252 offset:0\n\t" \
    "s_cmp_eq_u32 %[slp], 0\n\t" \
    "s_cbranch_scc1 .Lxs%=\n\t" \
    "s_sleep 10\n\t" \
    ".Lxs%=:\n\t" \
    "s_waitcnt lgkmcnt(6)\n\t" \
    "v_mfma_f32_16x16x32_bf16 v[220:223], v[88:91], v[224:227], 0\n\t" \
    "s_waitcnt lgkmcnt(5)\n\t" \
    "v_mfma_f32_16x16x32_bf16 v[220:223], v[92:95], v[228:231], v[220:223]\n\t" \
    "v_mfma_f32_16x16x32_bf16 v[216:219], v[80:83], v[224:227], 0\n\t" \
    "v_mfma_f32_16x16x32_bf16 v[216:219], v[84:87], v[228:231], v[216:219]\n\t" \
    "s_waitcnt lgkmcnt(4)\n\t" \
    "v_mfma_f32_16x16x32_bf16 v[212:215], v[244:247], v[224:227], 0\n\t" \
    "ds_read_b128 v[224:227], v252 offset:1024\n\t" \
    "s_waitcnt lgkmcnt(4)\n\t" \
    "v_mfma_f32_16x16x32_bf16 v[212:215], v[248:251], v[228:231], v[212:215]\n\t" \
    "ds_read_b128 v[228:231], v252 offset:16384\n\t" \
    "s_waitcnt lgkmcnt(4)\n\t" \
    "v_mfma_f32_16x16x32_bf16 v[200:203], v[244:247], v[232:235], 0\n\t" \
    "ds_read_b128 v[244:247], %[wl] offset:6144\n\t" \
    "s_waitcnt lgkmcnt(4)\n\t" \
    "v_mfma_f32_16x16x32_bf16 v[200:203], v[248:251], v[236:239], v[200:203]\n\t" \
    "ds_read_b128 v[248:251], %[wl] offset:7168\n\t" \
    "v_mfma_f32_16x16x32_bf16 v[204:207], v[80:83], v[232:235], 0\n\t" \
    "v_mfma_f32_16x16x32_bf16 v[204:207], v[84:87], v[236:239], v[204:207]\n\t" \
    "v_mfma_f32_16x16x32_bf16 v[208:211], v[88:91], v[232:235], 0\n\t" \
    "ds_read_b128 v[232:235], v252 offset:17408\n\t" \
    "v_mfma_f32_16x16x32_bf16 v[208:211], v[92:95], v[236:239], v[208:211]\n\t" \
    "v_xor_b32 v252, 0x3000, %[xa]\n\t" \
    "ds_read_b128 v[236:239], v252 offset:16384\n\t" \
    "s_waitcnt lgkmcnt(6)\n\t" \
    "v_mfma_f32_16x16x32_bf16 v[208:211], v[112:115], v[240:243], v[208:211]\n\t" \
    "s_waitcnt lgkmcnt(5)\n\t" \
    "v_mfma_f32_16x16x32_bf16 v[208:211], v[116:119], v[224:227], v[208:211]\n\t" \
    "v_mfma_f32_16x16x32_bf16 v[204:207], v[104:107], v[240:243], v[204:207]\n\t" \
    "v_mfma_f32_16x16x32_bf16 v[204:207], v[108:111], v[224:227], v[204:207]\n\t" \
    "v_mfma_f32_16x16x32_bf16 v[200:203], v[96:99], v[240:243], v[200:203]\n\t" \
    "ds_read_b128 v[240:243], v252 offset:17408\n\t" \
    "v_mfma_f32_16x16x32_bf16 v[200:203], v[100:103], v[224:227], v[200:203]\n\t" \
    "ds_read_b128 v[224:227], v252 offset:0\n\t" \
    "s_waitcnt lgkmcnt(6)\n\t" \
    "v_mfma_f32_16x16x32_bf16 v[212:215], v[96:99], v[228:231], v[212:215]\n\t" \
    "s_waitcnt lgkmcnt(3)\n\t" \
    "v_mfma_f32_16x16x32_bf16 v[212:215], v[100:103], v[232:235], v[212:215]\n\t" \
    "v_mfma_f32_16x16x32_bf16 v[216:219], v[104:107], v[228:231], v[216:219]\n\t" \
    "v_mfma_f32_16x16x32_bf16 v[216:219], v[108:111], v[232:235], v[216:219]\n\t" \
    "v_mfma_f32_16x16x32_bf16 v[220:223], v[112:115], v[228:231], v[220:223]\n\t" \
    "ds_read_b128 v[228:231], v252 offset:1024\n\t" \
    "v_mfma_f32_16x16x32_bf16 v[220:223], v[116:119], v[232:235], v[220:223]\n\t" \
    "v_xor_b32 v252, 0x3800, %[xa]\n\t" \
    "ds_read_b128 v[232:235], v252 offset:0\n\t" \
    "s_waitcnt lgkmcnt(4)\n\t" \
    "v_mfma_f32_16x16x32_bf16 v[220:223], v[128:131], v[236:239], v[220:223]\n\t" \
    "s_waitcnt lgkmcnt(3)\n\t" \
    "v_mfma_f32_16x16x32_bf16 v[220:223], v[132:135], v[240:243], v[220:223]\n\t" \
    "v_mfma_f32_16x16x32_bf16 v[216:219], v[120:123], v[236:239], v[216:219]\n\t" \
    "v_mfma_f32_16x16x32_bf16 v[216:219], v[124:127], v[240:243], v[216:219]\n\t" \
    "v_mfma_f32_16x16x32_bf16 v[212:215], v[244:247], v[236:239], v[212:215]\n\t" \
    "ds_read_b128 v[236:239], v252 offset:1024\n\t" \
    "v_mfma_f32_16x16x32_bf16 v[212:215], v[248:251], v[240:243], v[212:215]\n\t" \
    "ds_read_b128 v[240:243], v252 offset:16384\n\t" \
    "s_waitcnt lgkmcnt(4)\n\t" \
    "v_mfma_f32_16x16x32_bf16 v[200:203], v[244:247], v[224:227], v[200:203]\n\t" \
    "ds_read_b128 v[244:247], %[wl] offset:8192\n\t" \
    "s_waitcnt lgkmcnt(4)\n\t" \
    "v_mfma_f32_16x16x32_bf16 v[200:203], v[248:251], v[228:231], v[200:203]\n\t" \
    "ds_read_b128 v[248:251], %[wl] offset:9216\n\t" \
    "v_mfma_f32_16x16x32_bf16 v[204:207], v[120:123], v[224:227], v[204:207]\n\t" \
    "v_mfma_f32_16x16x32_bf16 v[204:207], v[124:127], v[228:231], v[204:207]\n\t" \
    "v_mfma_f32_16x16x32_bf16 v[208:211], v[128:131], v[224:227], v[208:211]\n\t" \
    "ds_read_b128 v[224:227], v252 offset:17408\n\t" \
    "v_mfma_f32_16x16x32_bf16 v[208:211], v[132:135], v[228:231], v[208:211]\n\t" \
    "v_and_b32 v252, 0x3ff, %[xa]\n\t" \
    "ds_read_b128 v[228:231], v252 offset:45056\n\t" \
    "s_waitcnt lgkmcnt(6)\n\t" \
    "v_mfma_f32_16x16x32_bf16 v[208:211], v[152:155], v[232:235], v[208:211]\n\t" \
    "s_waitcnt lgkmcnt(5)\n\t" \
    "v_mfma_f32_16x16x32_bf16 v[208:211], v[156:159], v[236:239], v[208:211]\n\t" \
    "v_mfma_f32_16x16x32_bf16 v[204:207], v[144:147], v[232:235], v[204:207]\n\t" \
    "v_mfma_f32_16x16x32_bf16 v[204:207], v[148:151], v[236:239], v[204:207]\n\t" \
    "v_mfma_f32_16x16x32_bf16 v[200:203], v[136:139], v[232:235], v[200:203]\n\t" \
    "ds_read_b128 v[232:235], v252 offset:46080\n\t" \
    "v_mfma_f32_16x16x32_bf16 v[200:203], v[140:143], v[236:239], v[200:203]\n\t" \
    "ds_read_b128 v[236:239], v252 offset:40960\n\t" \
    "s_waitcnt lgkmcnt(6)\n\t" \
    "v_mfma_f32_16x16x32_bf16 v[212:215], v[136:139], v[240:243], v[212:215]\n\t" \
    "s_waitcnt lgkmcnt(3)\n\t" \
    "v_mfma_f32_16x16x32_bf16 v[212:215], v[140:143], v[224:227], v[212:215]\n\t" \
    "v_mfma_f32_16x16x32_bf16 v[216:219], v[144:147], v[240:243], v[216:219]\n\t" \
    "v_mfma_f32_16x16x32_bf16 v[216:219], v[148:151], v[224:227], v[216:219]\n\t" \
    "v_mfma_f32_16x16x32_bf16 v[220:223], v[152:155], v[240:243], v[220:223]\n\t" \
    "ds_read_b128 v[240:243], v252 offset:41984\n\t" \
    "v_mfma_f32_16x16x32_bf16 v[220:223], v[156:159], v[224:227], v[220:223]\n\t" \
    "s_waitcnt vmcnt(0) lgkmcnt(0)\n\t" \
    "s_barrier\n\t" ASM_ST \
    "s_mov_b32 m0, %[d0]\n\t" \
    "s_nop 0\n\t" \
    "global_load_lds_dwordx4 v252, %[g0] offset:0 sc1\n\t" \
    "global_load_lds_dwordx4 v252, %[g0] offset:1024 sc1\n\t" \
    "global_load_lds_dwordx4 v252, %[g0] offset:2048 sc1\n\t" \
    "global_load_lds_dwordx4 v252, %[g0] offset:3072 sc1\n\t" \
    "s_mov_b32 m0, %[d1]\n\t" \
    "s_nop 0\n\t" \
    "global_load_lds_dwordx4 v252, %[g1] sc1\n\t" \
    "ds_read_b128 v[224:227], %[xa] offset:0\n\t" \
    "v_mfma_f32_16x16x32_bf16 v[220:223], v[168:171], v[228:231], v[220:223]\n\t" \
    "v_mfma_f32_16x16x32_bf16 v[220:223], v[172:175], v[232:235], v[220:223]\n\t" \
    "v_mfma_f32_16x16x32_bf16 v[216:219], v[160:163], v[228:231], v[216:219]\n\t" \
    "v_mfma_f32_16x16x32_bf16 v[216:219], v[164:167], v[232:235], v[216:219]\n\t" \
    "v_mfma_f32_16x16x32_bf16 v[212:215], v[244:247], v[228:231], v[212:215]\n\t" \
    "ds_read_b128 v[228:231], %[xa] offset:1024\n\t" \
    "v_mfma_f32_16x16x32_bf16 v[212:215], v[248:251], v[232:235], v[212:215]\n\t" \
    "ds_read_b128 v[232:235], %[xa] offset:16384\n\t" \
    "v_mfma_f32_16x16x32_bf16 v[200:203], v[244:247], v[236:239], v[200:203]\n\t" \
    "ds_read_b128 v[244:247], %[wl] offset:0\n\t" \
    "v_mfma_f32_16x16x32_bf16 v[200:203], v[248:251], v[240:243], v[200:203]\n\t" \
    "ds_read_b128 v[248:251], %[wl] offset:1024\n\t" \
    "v_mfma_f32_16x16x32_bf16 v[204:207], v[160:163], v[236:239], v[204:207]\n\t" \
    "v_mfma_f32_16x16x32_bf16 v[204:207], v[164:167], v[240:243], v[204:207]\n\t" \
    "v_mfma_f32_16x16x32_bf16 v[208:211], v[168:171], v[236:239], v[208:211]\n\t" \
    "ds_read_b128 v[236:239], %[xa] offset:17408\n\t" \
    "v_mfma_f32_16x16x32_bf16 v[208:211], v[172:175], v[240:243], v[208:211]\n\t" \
    "v_xor_b32 v252, 0x800, %[xa]\n\t" \
    "ds_read_b128 v[240:243], v252 offset:16384\n\t" \
    "s_waitcnt lgkmcnt(6)\n\t" \
    "v_mfma_f32_16x16x32_bf16 v[208:211], v[8:11], v[224:227], v[208:211]\n\t" \
    "s_waitcnt lgkmcnt(5)\n\t" \
    "v_mfma_f32_16x16x32_bf16 v[208:211], v[12:15], v[228:231], v[208:211]\n\t" \
    "v_mfma_f32_16x16x32_bf16 v[204:207], v[0:3], v[224:227], v[204:207]\n\t" \
    "v_mfma_f32_16x16x32_bf16 v[204:207], v[4:7], v[228:231], v[204:207]\n\t" \
    "s_waitcnt lgkmcnt(3)\n\t" \
    "v_mfma_f32_16x16x32_bf16 v[200:203], v[244:247], v[224:227], v[200:203]\n\t" \
    "ds_read_b128 v[224:227], v252 offset:17408\n\t" \
    "s_waitcnt lgkmcnt(3)\n\t" \
    "v_mfma_f32_16x16x32_bf16 v[200:203], v[248:251], v[228:231], v[200:203]\n\t" \
    "ds_read_b128 v[228:231], v252 offset:0\n\t" \
    "v_mfma_f32_16x16x32_bf16 v[212:215], v[244:247], v[232:235], v[212:215]\n\t" \
    "ds_read_b128 v[244:247], %[wl] offset:2048\n\t" \
    "s_waitcnt lgkmcnt(4)\n\t" \
    "v_mfma_f32_16x16x32_bf16 v[212:215], v[248:251], v[236:239], v[212:215]\n\t" \
    "ds_read_b128 v[248:251], %[wl] offset:3072\n\t" \
    "v_mfma_f32_16x16x32_bf16 v[216:219], v[0:3], v[232:235], v[216:219]\n\t" \
    "v_mfma_f32_16x16x32_bf16 v[216:219], v[4:7], v[236:239], v[216:219]\n\t" \
    "v_mfma_f32_16x16x32_bf16 v[220:223], v[8:11], v[232:235], v[220:223]\n\t" \
    "ds_read_b128 v[232:235], v252 offset:1024\n\t" \
    "v_mfma_f32_16x16x32_bf16 v[220:223], v[12:15], v[236:239], v[220:223]\n\t" \
    "v_xor_b32 v252, 0x1000, %[xa]\n\t" \
    "ds_read_b128 v[236:239], v252 offset:0\n\t" \
    "s_waitcnt lgkmcnt(6)\n\t" \
    "v_mfma_f32_16x16x32_bf16 v[220:223], v[32:35], v[240:243], v[220:223]\n\t" \
    "s_waitcnt lgkmcnt(5)\n\t" \
    "v_mfma_f32_16x16x32_bf16 v[220:223], v[36:39], v[224:227], v[220:223]\n\t" \
    "v_mfma_f32_16x16x32_bf16 v[216:219], v[24:27], v[240:243], v[216:219]\n\t" \
    "v_mfma_f32_16x16x32_bf16 v[216:219], v[28:31], v[224:227], v[216:219]\n\t" \
    "v_mfma_f32_16x16x32_bf16 v[212:215], v[16:19], v[240:243], v[212:215]\n\t" \
    "ds_read_b128 v[240:243], v252 offset:1024\n\t" \
    "v_mfma_f32_16x16x32_bf16 v[212:215], v[20:23], v[224:227], v[212:215]\n\t" \
    "ds_read_b128 v[224:227], v252 offset:16384\n\t" \
    "s_waitcnt lgkmcnt(6)\n\t" \
    "v_mfma_f32_16x16x32_bf16 v[200:203], v[16:19], v[228:231], v[200:203]\n\t" \
    "s_waitcnt lgkmcnt(3)\n\t" \
    "v_mfma_f32_16x16x32_bf16 v[200:203], v[20:23], v[232:235], v[200:203]\n\t" \
    "v_mfma_f32_16x16x32_bf16 v[204:207], v[24:27], v[228:231], v[204:207]\n\t" \
    "v_mfma_f32_16x16x32_bf16 v[204:207], v[28:31], v[232:235], v[204:207]\n\t" \
    "v_mfma_f32_16x16x32_bf16 v[208:211], v[32:35], v[228:231], v[208:211]\n\t" \
    "ds_read_b128 v[228:231], v252 offset:17408\n\t" \
    "v_mfma_f32_16x16x32_bf16 v[208:211], v[36:39], v[232:235], v[208:211]\n\t" \
    "v_xor_b32 v252, 0x1800, %[xa]\n\t" \
    "ds_read_b128 v[232:235], v252 offset:16384\n\t" \
    "s_waitcnt lgkmcnt(4)\n\t" \
    "v_mfma_f32_16x16x32_bf16 v[208:211], v[48:51], v[236:239], v[208:211]\n\t" \
    "s_waitcnt lgkmcnt(3)\n\t" \
    "v_mfma_f32_16x16x32_bf16 v[208:211], v[52:55], v[240:243], v[208:211]\n\t" \
    "v_mfma_f32_16x16x32_bf16 v[204:207], v[40:43], v[236:239], v[204:207]\n\t" \
    "v_mfma_f32_16x16x32_bf16 v[204:207], v[44:47], v[240:243], v[204:207]\n\t" \
    "v_mfma_f32_16x16x32_bf16 v[200:203], v[244:247], v[236:239], v[200:203]\n\t" \
    "ds_read_b128 v[236:239], v252 offset:17408\n\t" \
    "v_mfma_f32_16x16x32_bf16 v[200:203], v[248:251], v[240:243], v[200:203]\n\t" \
    "ds_read_b128 v[240:243], v252 offset:0\n\t" \
    "s_waitcnt lgkmcnt(4)\n\t" \
    "v_mfma_f32_16x16x32_bf16 v[212:215], v[244:247], v[224:227], v[212:215]\n\t" \
    "s_waitcnt lgkmcnt(3)\n\t" \
    "v_mfma_f32_16x16x32_bf16 v[212:215], v[248:251], v[228:231], v[212:215]\n\t" \
    "v_mfma_f32_16x16x32_bf16 v[216:219], v[40:43], v[224:227], v[216:219]\n\t" \
    "v_mfma_f32_16x16x32_bf16 v[216:219], v[44:47], v[228:231], v[216:219]\n\t" \
    "v_mfma_f32_16x16x32_bf16 v[220:223], v[48:51], v[224:227], v[220:223]\n\t" \
    "ds_read_b128 v[224:227], v252 offset:1024\n\t" \
    "v_mfma_f32_16x16x32_bf16 v[220:223], v[52:55], v[228:231], v[220:223]\n\t" \
    "v_and_b32 v252, 0x3ff, %[xa]\n\t" \
    "ds_read_b128 v[228:231], v252 offset:43008\n\t" \
    "s_waitcnt lgkmcnt(4)\n\t" \
    "v_mfma_f32_16x16x32_bf16 v[220:223], v[72:75], v[232:235], v[220:223]\n\t" \
    "v_mov_b64 v[244:245], 0\n\t" \
    "s_waitcnt lgkmcnt(3)\n\t" \
    "v_mfma_f32_16x16x32_bf16 v[220:223], v[76:79], v[236:239], v[220:223]\n\t" \
    "v_mov_b64 v[246:247], 0\n\t" \
    "v_mfma_f32_16x16x32_bf16 v[216:219], v[64:67], v[232:235], v[216:219]\n\t" \
    "v_mov_b64 v[248:249], 0\n\t" \
    "v_mfma_f32_16x16x32_bf16 v[216:219], v[68:71], v[236:239], v[216:219]\n\t" \
    "v_mov_b64 v[250:251], 0\n\t" \
    "v_mfma_f32_16x16x32_bf16 v[212:215], v[56:59], v[232:235], v[212:215]\n\t" \
    "ds_read_b128 v[232:235], v252 offset:44032\n\t" \
    "v_mfma_f32_16x16x32_bf16 v[212:215], v[60:63], v[236:239], v[212:215]\n\t" \
    "ds_read_b128 v[236:239], v252 offset:47104\n\t" \
    "s_waitcnt lgkmcnt(4)\n\t" \
    "v_mfma_f32_16x16x32_bf16 v[200:203], v[56:59], v[240:243], v[200:203]\n\t" \
    "s_waitcnt lgkmcnt(3)\n\t" \
    "v_mfma_f32_16x16x32_bf16 v[200:203], v[60:63], v[224:227], v[200:203]\n\t" \
    "v_mfma_f32_16x16x32_bf16 v[204:207], v[64:67], v[240:243], v[204:207]\n\t" \
    "v_mfma_f32_16x16x32_bf16 v[204:207], v[68:71], v[224:227], v[204:207]\n\t" \
    "v_mfma_f32_16x16x32_bf16 v[208:211], v[72:75], v[240:243], v[208:211]\n\t" \
    "ds_read_b128 v[240:243], v252 offset:48128\n\t" \
    "v_mfma_f32_16x16x32_bf16 v[208:211], v[76:79], v[224:227], v[208:211]\n\t" \
    "s_waitcnt lgkmcnt(3)\n\t" \
    "v_mfma_f32_16x16x32_bf16 v[208:211], v[192:195], v[228:231], v[208:211]\n\t" \
    "v_add_u32 v252, %[qs], v252\n\t" \
    "v_mov_b64 v[224:225], 0\n\t" \
    "s_waitcnt lgkmcnt(2)\n\t" \
    "v_mfma_f32_16x16x32_bf16 v[208:211], v[196:199], v[232:235], v[208:211]\n\t" \
    "v_mov_b64 v[226:227], 0\n\t" \
    "v_mfma_f32_16x16x32_bf16 v[204:207], v[184:187], v[228:231], v[204:207]\n\t" \
    "v_mfma_f32_16x16x32_bf16 v[204:207], v[188:191], v[232:235], v[204:207]\n\t" \
    "v_mfma_f32_16x16x32_bf16 v[200:203], v[176:179], v[228:231], v[200:203]\n\t" \
    "v_mfma_f32_16x16x32_bf16 v[200:203], v[180:183], v[232:235], v[200:203]\n\t" \
    "s_waitcnt lgkmcnt(1)\n\t" \
    "v_mfma_f32_16x16x32_bf16 v[212:215], v[176:179], v[236:239], v[212:215]\n\t" \
    "ds_read_b128 v[228:231], %[wl] offset:2048\n\t" \
    "ds_read_b128 v[232:235], %[wl] offset:3072\n\t" \
    "s_waitcnt lgkmcnt(2)\n\t" \
    "v_mfma_f32_16x16x32_bf16 v[212:215], v[180:183], v[240:243], v[212:215]\n\t" \
    "v_cvt_pk_bf16_f32 v208, v208, v209\n\t" \
    "v_cvt_pk_bf16_f32 v209, v210, v211\n\t" \
    "v_mfma_f32_16x16x32_bf16 v[216:219], v[184:187], v[236:239], v[216:219]\n\t" \
    "ds_write_b64 v252, v[208:209] offset:2048\n\t" \
    "v_cvt_pk_bf16_f32 v204, v204, v205\n\t" \
    "v_mfma_f32_16x16x32_bf16 v[216:219], v[188:191], v[240:243], v[216:219]\n\t" \
    "v_cvt_pk_bf16_f32 v205, v206, v207\n\t" \
    "ds_write_b64 v252, v[204:205] offset:1024\n\t" \
    "v_mfma_f32_16x16x32_bf16 v[220:223], v[192:195], v[236:239], v[220:223]\n\t" \
    "v_cvt_pk_bf16_f32 v200, v200, v201\n\t" \
    "v_cvt_pk_bf16_f32 v201, v202, v203\n\t" \
    "v_mfma_f32_16x16x32_bf16 v[220:223], v[196:199], v[240:243], v[220:223]\n\t" \
    "ds_write_b64 v252, v[200:201] offset:0\n\t" \
    "ds_read_b128 v[200:203], %[wl] offset:0\n\t" \
    "ds_read_b128 v[204:207], %[wl] offset:1024\n\t" \
    "v_cvt_pk_bf16_f32 v212, v212, v213\n\t" \
    "v_cvt_pk_bf16_f32 v213, v214, v215\n\t" \
    "ds_write_b64 v252, v[212:213] offset:6144\n\t" \
    "v_cvt_pk_bf16_f32 v216, v216, v217\n\t" \
    "v_cvt_pk_bf16_f32 v217, v218, v219\n\t" \
    "ds_write_b64 v252, v[216:217] offset:7168\n\t" \
    "v_cvt_pk_bf16_f32 v220, v220, v221\n\t" \
    "v_cvt_pk_bf16_f32 v221, v222, v223\n\t" \
    "ds_write_b64 v252, v[220:221] offset:8192\n\t" \
    "s_waitcnt lgkmcnt(0)\n\t"
#define ASM_XG_S_LO2 \
    "s_waitcnt lgkmcnt(0)\n\t" \
    "ds_read_b128 v[224:227], %[xa] offset:16384\n\t" \
    "ds_read_b128 v[228:231], %[xa] offset:17408\n\t" \
    "v_xor_b32 v252, 0x800, %[xa]\n\t" \
    "ds_read_b128 v[232:235], v252 offset:16384\n\t" \
    "ds_read_b128 v[236:239], v252 offset:17408\n\t" \
    "v_xor_b32 v252, 0x1000, %[xa]\n\t" \
    "ds_read_b128 v[240:243], v252 offset:16384\n\t" \
    "ds_read_b128 v[212:215], v252 offset:17408\n\t" \
    "v_xor_b32 v252, 0x1800, %[xa]\n\t" \
    "ds_read_b128 v[200:203], v252 offset:16384\n\t" \
    "s_waitcnt lgkmcnt(6)\n\t" \
    "v_mfma_f32_16x16x32_bf16 v[220:223], v[8:11], v[224:227], 0\n\t" \
    "s_waitcnt lgkmcnt(5)\n\t" \
    "v_mfma_f32_16x16x32_bf16 v[220:223], v[12:15], v[228:231], v[220:223]\n\t" \
    "v_mfma_f32_16x16x32_bf16 v[216:219], v[0:3], v[224:227], 0\n\t" \
    "ds_read_b128 v[224:227], v252 offset:17408\n\t" \
    "v_mfma_f32_16x16x32_bf16 v[216:219], v[4:7], v[228:231], v[216:219]\n\t" \
    "v_and_b32 v252, 0x3ff, %[xa]\n\t" \
    "ds_read_b128 v[228:231], v252 offset:45056\n\t" \
    "s_waitcnt lgkmcnt(6)\n\t" \
    "v_mfma_f32_16x16x32_bf16 v[216:219], v[24:27], v[232:235], v[216:219]\n\t" \
    "s_waitcnt lgkmcnt(5)\n\t" \
    "v_mfma_f32_16x16x32_bf16 v[216:219], v[28:31], v[236:239], v[216:219]\n\t" \
    "v_mfma_f32_16x16x32_bf16 v[220:223], v[32:35], v[232:235], v[220:223]\n\t" \
    "ds_read_b128 v[232:235], v252 offset:46080\n\t" \
    "v_mfma_f32_16x16x32_bf16 v[220:223], v[36:39], v[236:239], v[220:223]\n\t" \
    "ds_read_b128 v[236:239], %[xa] offset:0\n\t" \
    "s_waitcnt lgkmcnt(6)\n\t" \
    "v_mfma_f32_16x16x32_bf16 v[220:223], v[48:51], v[240:243], v[220:223]\n\t" \
    "s_waitcnt lgkmcnt(5)\n\t" \
    "v_mfma_f32_16x16x32_bf16 v[220:223], v[52:55], v[212:215], v[220:223]\n\t" \
    "v_mfma_f32_16x16x32_bf16 v[216:219], v[40:43], v[240:243], v[216:219]\n\t" \
    "ds_read_b128 v[240:243], %[xa] offset:1024\n\t" \
    "v_mfma_f32_16x16x32_bf16 v[216:219], v[44:47], v[212:215], v[216:219]\n\t" \
    "v_xor_b32 v252, 0x800, %[xa]\n\t" \
    "ds_read_b128 v[212:215], v252 offset:0\n\t" \
    "s_waitcnt lgkmcnt(6)\n\t" \
    "v_mfma_f32_16x16x32_bf16 v[216:219], v[64:67], v[200:203], v[216:219]\n\t" \
    "s_waitcnt lgkmcnt(5)\n\t" \
    "v_mfma_f32_16x16x32_bf16 v[216:219], v[68:71], v[224:227], v[216:219]\n\t" \
    "v_mfma_f32_16x16x32_bf16 v[220:223], v[72:75], v[200:203], v[220:223]\n\t" \
    "ds_read_b128 v[200:203], v252 offset:1024\n\t" \
    "v_mfma_f32_16x16x32_bf16 v[220:223], v[76:79], v[224:227], v[220:223]\n\t" \
    "v_xor_b32 v252, 0x1000, %[xa]\n\t" \
    "ds_read_b128 v[224:227], v252 offset:0\n\t" \
    "s_waitcnt lgkmcnt(6)\n\t" \
    "v_mfma_f32_16x16x32_bf16 v[220:223], v[168:171], v[228:231], v[220:223]\n\t" \
    "s_waitcnt lgkmcnt(5)\n\t" \
    "v_mfma_f32_16x16x32_bf16 v[220:223], v[172:175], v[232:235], v[220:223]\n\t" \
    "v_mfma_f32_16x16x32_bf16 v[216:219], v[160:163], v[228:231], v[216:219]\n\t" \
    "ds_read_b128 v[228:231], v252 offset:1024\n\t" \
    "v_mfma_f32_16x16x32_bf16 v[216:219], v[164:167], v[232:235], v[216:219]\n\t" \
    "v_xor_b32 v252, 0x1800, %[xa]\n\t" \
    "ds_read_b128 v[232:235], v252 offset:0\n\t" \
    "s_waitcnt lgkmcnt(6)\n\t" \
    "v_mfma_f32_16x16x32_bf16 v[204:207], v[0:3], v[236:239], 0\n\t" \
    "s_waitcnt lgkmcnt(5)\n\t" \
    "v_mfma_f32_16x16x32_bf16 v[204:207], v[4:7], v[240:243], v[204:207]\n\t" \
    "v_mfma_f32_16x16x32_bf16 v[208:211], v[8:11], v[236:239], 0\n\t" \
    "ds_read_b128 v[236:239], v252 offset:1024\n\t" \
    "v_mfma_f32_16x16x32_bf16 v[208:211], v[12:15], v[240:243], v[208:211]\n\t" \
    "v_and_b32 v252, 0x3ff, %[xa]\n\t" \
    "ds_read_b128 v[240:243], v252 offset:40960\n\t" \
    "s_waitcnt lgkmcnt(6)\n\t" \
    "v_mfma_f32_16x16x32_bf16 v[208:211], v[32:35], v[212:215], v[208:211]\n\t" \
    "s_waitcnt lgkmcnt(5)\n\t" \
    "v_mfma_f32_16x16x32_bf16 v[208:211], v[36:39], v[200:203], v[208:211]\n\t" \
    "v_mfma_f32_16x16x32_bf16 v[204:207], v[24:27], v[212:215], v[204:207]\n\t" \
    "ds_read_b128 v[212:215], v252 offset:41984\n\t" \
    "v_mfma_f32_16x16x32_bf16 v[204:207], v[28:31], v[200:203], v[204:207]\n\t" \
    "s_waitcnt lgkmcnt(0)\n\t" \
    "s_barrier\n\t" ASM_ST \
    "v_xor_b32 v252, 0x2000, %[xa]\n\t" \
    "ds_read_b128 v[200:203], v252 offset:0\n\t" \
    "v_mfma_f32_16x16x32_bf16 v[204:207], v[40:43], v[224:227], v[204:207]\n\t" \
    "v_mfma_f32_16x16x32_bf16 v[204:207], v[44:47], v[228:231], v[204:207]\n\t" \
    "v_mfma_f32_16x16x32_bf16 v[208:211], v[48:51], v[224:227], v[208:211]\n\t" \
    "ds_read_b128 v[224:227], v252 offset:1024\n\t" \
    "v_mfma_f32_16x16x32_bf16 v[208:211], v[52:55], v[228:231], v[208:211]\n\t" \
    "v_xor_b32 v252, 0x2800, %[xa]\n\t" \
    "ds_read_b128 v[228:231], v252 offset:0\n\t" \
    "v_mfma_f32_16x16x32_bf16 v[208:211], v[72:75], v[232:235], v[208:211]\n\t" \
    "v_mfma_f32_16x16x32_bf16 v[208:211], v[76:79], v[236:239], v[208:211]\n\t" \
    "v_mfma_f32_16x16x32_bf16 v[204:207], v[64:67], v[232:235], v[204:207]\n\t" \
    "ds_read_b128 v[232:235], v252 offset:1024\n\t" \
    "v_mfma_f32_16x16x32_bf16 v[204:207], v[68:71], v[236:239], v[204:207]\n\t" \
    "v_xor_b32 v252, 0x3000, %[xa]\n\t" \
    "ds_read_b128 v[236:239], v252 offset:0\n\t" \
    "v_mfma_f32_16x16x32_bf16 v[204:207], v[160:163], v[240:243], v[204:207]\n\t" \
    "v_mfma_f32_16x16x32_bf16 v[204:207], v[164:167], v[212:215], v[204:207]\n\t" \
    "v_mfma_f32_16x16x32_bf16 v[208:211], v[168:171], v[240:243], v[208:211]\n\t" \
    "ds_read_b128 v[240:243], v252 offset:1024\n\t" \
    "v_mfma_f32_16x16x32_bf16 v[208:211], v[172:175], v[212:215], v[208:211]\n\t" \
    "v_xor_b32 v252, 0x3800, %[xa]\n\t" \
    "ds_read_b128 v[212:215], v252 offset:0\n\t" \
    "s_waitcnt lgkmcnt(6)\n\t" \
    "v_mfma_f32_16x16x32_bf16 v[208:211], v[88:91], v[200:203], v[208:211]\n\t" \
    "s_waitcnt lgkmcnt(5)\n\t" \
    "v_mfma_f32_16x16x32_bf16 v[208:211], v[92:95], v[224:227], v[208:211]\n\t" \
    "v_mfma_f32_16x16x32_bf16 v[204:207], v[80:83], v[200:203], v[204:207]\n\t" \
    "ds_read_b128 v[200:203], v252 offset:1024\n\t" \
    "v_mfma_f32_16x16x32_bf16 v[204:207], v[84:87], v[224:227], v[204:207]\n\t" \
    "v_and_b32 v252, 0x3ff, %[xa]\n\t" \
    "ds_read_b128 v[224:227], v252 offset:43008\n\t" \
    "s_waitcnt lgkmcnt(6)\n\t" \
    "v_mfma_f32_16x16x32_bf16 v[204:207], v[104:107], v[228:231], v[204:207]\n\t" \
    "s_waitcnt lgkmcnt(5)\n\t" \
    "v_mfma_f32_16x16x32_bf16 v[204:207], v[108:111], v[232:235], v[204:207]\n\t" \
    "v_mfma_f32_16x16x32_bf16 v[208:211], v[112:115], v[228:231], v[208:211]\n\t" \
    "ds_read_b128 v[228:231], v252 offset:44032\n\t" \
    "v_mfma_f32_16x16x32_bf16 v[208:211], v[116:119], v[232:235], v[208:211]\n\t" \
    "v_xor_b32 v252, 0x2000, %[xa]\n\t" \
    "ds_read_b128 v[232:235], v252 offset:16384\n\t" \
    "s_waitcnt lgkmcnt(6)\n\t" \
    "v_mfma_f32_16x16x32_bf16 v[208:211], v[128:131], v[236:239], v[208:211]\n\t" \
    "s_waitcnt lgkmcnt(5)\n\t" \
    "v_mfma_f32_16x16x32_bf16 v[208:211], v[132:135], v[240:243], v[208:211]\n\t" \
    "v_mfma_f32_16x16x32_bf16 v[204:207], v[120:123], v[236:239], v[204:207]\n\t" \
    "ds_read_b128 v[236:239], v252 offset:17408\n\t" \
    "v_mfma_f32_16x16x32_bf16 v[204:207], v[124:127], v[240:243], v[204:207]\n\t" \
    "v_xor_b32 v252, 0x2800, %[xa]\n\t" \
    "ds_read_b128 v[240:243], v252 offset:16384\n\t" \
    "s_waitcnt lgkmcnt(6)\n\t" \
    "v_mfma_f32_16x16x32_bf16 v[204:207], v[144:147], v[212:215], v[204:207]\n\t" \
    "s_waitcnt lgkmcnt(5)\n\t" \
    "v_mfma_f32_16x16x32_bf16 v[204:207], v[148:151], v[200:203], v[204:207]\n\t" \
    "v_mfma_f32_16x16x32_bf16 v[208:211], v[152:155], v[212:215], v[208:211]\n\t" \
    "ds_read_b128 v[212:215], v252 offset:17408\n\t" \
    "v_mfma_f32_16x16x32_bf16 v[208:211], v[156:159], v[200:203], v[208:211]\n\t" \
    "v_xor_b32 v252, 0x3000, %[xa]\n\t" \
    "ds_read_b128 v[200:203], v252 offset:16384\n\t" \
    "s_waitcnt lgkmcnt(6)\n\t" \
    "v_mfma_f32_16x16x32_bf16 v[208:211], v[192:195], v[224:227], v[208:211]\n\t" \
    "s_waitcnt lgkmcnt(5)\n\t" \
    "v_mfma_f32_16x16x32_bf16 v[208:211], v[196:199], v[228:231], v[208:211]\n\t" \
    "v_mfma_f32_16x16x32_bf16 v[204:207], v[184:187], v[224:227], v[204:207]\n\t" \
    "ds_read_b128 v[224:227], v252 offset:17408\n\t" \
    "v_mfma_f32_16x16x32_bf16 v[204:207], v[188:191], v[228:231], v[204:207]\n\t" \
    "v_xor_b32 v252, 0x3800, %[xa]\n\t" \
    "ds_read_b128 v[228:231], v252 offset:16384\n\t" \
    "s_waitcnt lgkmcnt(6)\n\t" \
    "v_mfma_f32_16x16x32_bf16 v[216:219], v[80:83], v[232:235], v[216:219]\n\t" \
    "s_waitcnt lgkmcnt(5)\n\t" \
    "v_mfma_f32_16x16x32_bf16 v[216:219], v[84:87], v[236:239], v[216:219]\n\t" \
    "v_mfma_f32_16x16x32_bf16 v[220:223], v[88:91], v[232:235], v[220:223]\n\t" \
    "ds_read_b128 v[232:235], v252 offset:17408\n\t" \
    "v_mfma_f32_16x16x32_bf16 v[220:223], v[92:95], v[236:239], v[220:223]\n\t" \
    "v_and_b32 v252, 0x3ff, %[xa]\n\t" \
    "ds_read_b128 v[236:239], v252 offset:47104\n\t" \
    "s_waitcnt lgkmcnt(6)\n\t" \
    "v_mfma_f32_16x16x32_bf16 v[220:223], v[112:115], v[240:243], v[220:223]\n\t" \
    "v_mov_b64 v[244:245], 0\n\t" \
    "s_waitcnt lgkmcnt(5)\n\t" \
    "v_mfma_f32_16x16x32_bf16 v[220:223], v[116:119], v[212:215], v[220:223]\n\t" \
    "v_mov_b64 v[246:247], 0\n\t" \
    "v_mfma_f32_16x16x32_bf16 v[216:219], v[104:107], v[240:243], v[216:219]\n\t" \
    "ds_read_b128 v[240:243], v252 offset:48128\n\t" \
    "v_mov_b64 v[248:249], 0\n\t" \
    "v_mfma_f32_16x16x32_bf16 v[216:219], v[108:111], v[212:215], v[216:219]\n\t" \
    "v_mov_b64 v[250:251], 0\n\t" \
    "s_waitcnt lgkmcnt(5)\n\t" \
    "v_mfma_f32_16x16x32_bf16 v[216:219], v[120:123], v[200:203], v[216:219]\n\t" \
    "s_waitcnt lgkmcnt(4)\n\t" \
    "v_mfma_f32_16x16x32_bf16 v[216:219], v[124:127], v[224:227], v[216:219]\n\t" \
    "v_mfma_f32_16x16x32_bf16 v[220:223], v[128:131], v[200:203], v[220:223]\n\t" \
    "v_mfma_f32_16x16x32_bf16 v[220:223], v[132:135], v[224:227], v[220:223]\n\t" \
    "s_waitcnt lgkmcnt(3)\n\t" \
    "v_mfma_f32_16x16x32_bf16 v[220:223], v[152:155], v[228:231], v[220:223]\n\t" \
    "v_add_u32 v252, %[qs], v252\n\t" \
    "v_mov_b64 v[224:225], 0\n\t" \
    "s_waitcnt lgkmcnt(2)\n\t" \
    "v_mfma_f32_16x16x32_bf16 v[220:223], v[156:159], v[232:235], v[220:223]\n\t" \
    "v_mov_b64 v[226:227], 0\n\t" \
    "v_cvt_pk_bf16_f32 v208, v208, v209\n\t" \
    "v_mfma_f32_16x16x32_bf16 v[216:219], v[144:147], v[228:231], v[216:219]\n\t" \
    "v_cvt_pk_bf16_f32 v209, v210, v211\n\t" \
    "ds_write_b64 v252, v[208:209] offset:2048\n\t" \
    "v_mfma_f32_16x16x32_bf16 v[216:219], v[148:151], v[232:235], v[216:219]\n\t" \
    "v_cvt_pk_bf16_f32 v204, v204, v205\n\t" \
    "v_cvt_pk_bf16_f32 v205, v206, v207\n\t" \
    "s_waitcnt lgkmcnt(2)\n\t" \
    "v_mfma_f32_16x16x32_bf16 v[216:219], v[184:187], v[236:239], v[216:219]\n\t" \
    "ds_write_b64 v252, v[204:205] offset:1024\n\t" \
    "ds_read_b128 v[228:231], %[wl] offset:2048\n\t" \
    "s_waitcnt lgkmcnt(3)\n\t" \
    "v_mfma_f32_16x16x32_bf16 v[216:219], v[188:191], v[240:243], v[216:219]\n\t" \
    "ds_read_b128 v[232:235], %[wl] offset:3072\n\t" \
    "v_mfma_f32_16x16x32_bf16 v[220:223], v[192:195], v[236:239], v[220:223]\n\t" \
    "v_mfma_f32_16x16x32_bf16 v[220:223], v[196:199], v[240:243], v[220:223]\n\t" \
    "ds_read_b128 v[200:203], %[wl] offset:0\n\t" \
    "ds_read_b128 v[204:207], %[wl] offset:1024\n\t" \
    "v_cvt_pk_bf16_f32 v216, v216, v217\n\t" \
    "v_cvt_pk_bf16_f32 v217, v218, v219\n\t" \
    "ds_write_b64 v252, v[216:217] offset:7168\n\t" \
    "s_nop 2\n\t" \
    "v_cvt_pk_bf16_f32 v220, v220, v221\n\t" \
    "v_cvt_pk_bf16_f32 v221, v222, v223\n\t" \
    "ds_write_b64 v252, v[220:221] offset:8192\n\t" \
    "s_waitcnt lgkmcnt(0)\n\t"
#define ASM_XG_S_LO1 \
    "s_waitcnt lgkmcnt(0)\n\t" \
    "ds_read_b128 v[224:227], %[xa] offset:16384\n\t" \
    "ds_read_b128 v[228:231], %[xa] offset:17408\n\t" \
    "v_xor_b32 v252, 0x800, %[xa]\n\t" \
    "ds_read_b128 v[232:235], v252 offset:16384\n\t" \
    "ds_read_b128 v[236:239], v252 offset:17408\n\t" \
    "v_xor_b32 v252, 0x1000, %[xa]\n\t" \
    "ds_read_b128 v[240:243], v252 offset:16384\n\t" \
    "ds_read_b128 v[212:215], v252 offset:17408\n\t" \
    "v_xor_b32 v252, 0x1800, %[xa]\n\t" \
    "ds_read_b128 v[200:203], v252 offset:16384\n\t" \
    "s_waitcnt lgkmcnt(6)\n\t" \
    "v_mfma_f32_16x16x32_bf16 v[220:223], v[8:11], v[224:227], 0\n\t" \
    "ds_read_b128 v[224:227], v252 offset:17408\n\t" \
    "s_waitcnt lgkmcnt(6)\n\t" \
    "v_mfma_f32_16x16x32_bf16 v[220:223], v[12:15], v[228:231], v[220:223]\n\t" \
    "v_and_b32 v252, 0x3ff, %[xa]\n\t" \
    "ds_read_b128 v[228:231], v252 offset:45056\n\t" \
    "s_waitcnt lgkmcnt(6)\n\t" \
    "v_mfma_f32_16x16x32_bf16 v[220:223], v[32:35], v[232:235], v[220:223]\n\t" \
    "ds_read_b128 v[232:235], v252 offset:46080\n\t" \
    "s_waitcnt lgkmcnt(6)\n\t" \
    "v_mfma_f32_16x16x32_bf16 v[220:223], v[36:39], v[236:239], v[220:223]\n\t" \
    "ds_read_b128 v[236:239], %[xa] offset:0\n\t" \
    "s_waitcnt lgkmcnt(6)\n\t" \
    "v_mfma_f32_16x16x32_bf16 v[220:223], v[48:51], v[240:243], v[220:223]\n\t" \
    "ds_read_b128 v[240:243], %[xa] offset:1024\n\t" \
    "s_waitcnt lgkmcnt(6)\n\t" \
    "v_mfma_f32_16x16x32_bf16 v[220:223], v[52:55], v[212:215], v[220:223]\n\t" \
    "v_xor_b32 v252, 0x800, %[xa]\n\t" \
    "ds_read_b128 v[212:215], v252 offset:0\n\t" \
    "s_waitcnt lgkmcnt(6)\n\t" \
    "v_mfma_f32_16x16x32_bf16 v[220:223], v[72:75], v[200:203], v[220:223]\n\t" \
    "ds_read_b128 v[200:203], v252 offset:1024\n\t" \
    "s_waitcnt lgkmcnt(6)\n\t" \
    "v_mfma_f32_16x16x32_bf16 v[220:223], v[76:79], v[224:227], v[220:223]\n\t" \
    "v_xor_b32 v252, 0x1000, %[xa]\n\t" \
    "ds_read_b128 v[224:227], v252 offset:0\n\t" \
    "s_waitcnt lgkmcnt(6)\n\t" \
    "v_mfma_f32_16x16x32_bf16 v[220:223], v[168:171], v[228:231], v[220:223]\n\t" \
    "ds_read_b128 v[228:231], v252 offset:1024\n\t" \
    "s_waitcnt lgkmcnt(6)\n\t" \
    "v_mfma_f32_16x16x32_bf16 v[220:223], v[172:175], v[232:235], v[220:223]\n\t" \
    "v_xor_b32 v252, 0x1800, %[xa]\n\t" \
    "ds_read_b128 v[232:235], v252 offset:0\n\t" \
    "s_waitcnt lgkmcnt(6)\n\t" \
    "v_mfma_f32_16x16x32_bf16 v[208:211], v[8:11], v[236:239], 0\n\t" \
    "ds_read_b128 v[236:239], v252 offset:1024\n\t" \
    "s_waitcnt lgkmcnt(6)\n\t" \
    "v_mfma_f32_16x16x32_bf16 v[208:211], v[12:15], v[240:243], v[208:211]\n\t" \
    "v_and_b32 v252, 0x3ff, %[xa]\n\t" \
    "ds_read_b128 v[240:243], v252 offset:40960\n\t" \
    "s_waitcnt lgkmcnt(6)\n\t" \
    "v_mfma_f32_16x16x32_bf16 v[208:211], v[32:35], v[212:215], v[208:211]\n\t" \
    "ds_read_b128 v[212:215], v252 offset:41984\n\t" \
    "s_waitcnt lgkmcnt(6)\n\t" \
    "v_mfma_f32_16x16x32_bf16 v[208:211], v[36:39], v[200:203], v[208:211]\n\t" \
    "s_waitcnt lgkmcnt(0)\n\t" \
    "s_barrier\n\t" ASM_ST \
    "v_xor_b32 v252, 0x2000, %[xa]\n\t" \
    "ds_read_b128 v[200:203], v252 offset:0\n\t" \
    "v_mfma_f32_16x16x32_bf16 v[208:211], v[48:51], v[224:227], v[208:211]\n\t" \
    "ds_read_b128 v[224:227], v252 offset:1024\n\t" \
    "v_mfma_f32_16x16x32_bf16 v[208:211], v[52:55], v[228:231], v[208:211]\n\t" \
    "v_xor_b32 v252, 0x2800, %[xa]\n\t" \
    "ds_read_b128 v[228:231], v252 offset:0\n\t" \
    "v_mfma_f32_16x16x32_bf16 v[208:211], v[72:75], v[232:235], v[208:211]\n\t" \
    "ds_read_b128 v[232:235], v252 offset:1024\n\t" \
    "v_mfma_f32_16x16x32_bf16 v[208:211], v[76:79], v[236:239], v[208:211]\n\t" \
    "v_xor_b32 v252, 0x3000, %[xa]\n\t" \
    "ds_read_b128 v[236:239], v252 offset:0\n\t" \
    "v_mfma_f32_16x16x32_bf16 v[208:211], v[168:171], v[240:243], v[208:211]\n\t" \
    "ds_read_b128 v[240:243], v252 offset:1024\n\t" \
    "v_mfma_f32_16x16x32_bf16 v[208:211], v[172:175], v[212:215], v[208:211]\n\t" \
    "v_xor_b32 v252, 0x3800, %[xa]\n\t" \
    "ds_read_b128 v[212:215], v252 offset:0\n\t" \
    "s_waitcnt lgkmcnt(6)\n\t" \
    "v_mfma_f32_16x16x32_bf16 v[208:211], v[88:91], v[200:203], v[208:211]\n\t" \
    "ds_read_b128 v[200:203], v252 offset:1024\n\t" \
    "s_waitcnt lgkmcnt(6)\n\t" \
    "v_mfma_f32_16x16x32_bf16 v[208:211], v[92:95], v[224:227], v[208:211]\n\t" \
    "v_and_b32 v252, 0x3ff, %[xa]\n\t" \
    "ds_read_b128 v[224:227], v252 offset:43008\n\t" \
    "s_waitcnt lgkmcnt(6)\n\t" \
    "v_mfma_f32_16x16x32_bf16 v[208:211], v[112:115], v[228:231], v[208:211]\n\t" \
    "ds_read_b128 v[228:231], v252 offset:44032\n\t" \
    "s_waitcnt lgkmcnt(6)\n\t" \
    "v_mfma_f32_16x16x32_bf16 v[208:211], v[116:119], v[232:235], v[208:211]\n\t" \
    "v_xor_b32 v252, 0x2000, %[xa]\n\t" \
    "ds_read_b128 v[232:235], v252 offset:16384\n\t" \
    "s_waitcnt lgkmcnt(6)\n\t" \
    "v_mfma_f32_16x16x32_bf16 v[208:211], v[128:131], v[236:239], v[208:211]\n\t" \
    "ds_read_b128 v[236:239], v252 offset:17408\n\t" \
    "s_waitcnt lgkmcnt(6)\n\t" \
    "v_mfma_f32_16x16x32_bf16 v[208:211], v[132:135], v[240:243], v[208:211]\n\t" \
    "v_xor_b32 v252, 0x2800, %[xa]\n\t" \
    "ds_read_b128 v[240:243], v252 offset:16384\n\t" \
    "s_waitcnt lgkmcnt(6)\n\t" \
    "v_mfma_f32_16x16x32_bf16 v[208:211], v[152:155], v[212:215], v[208:211]\n\t" \
    "ds_read_b128 v[212:215], v252 offset:17408\n\t" \
    "s_waitcnt lgkmcnt(6)\n\t" \
    "v_mfma_f32_16x16x32_bf16 v[208:211], v[156:159], v[200:203], v[208:211]\n\t" \
    "v_xor_b32 v252, 0x3000, %[xa]\n\t" \
    "ds_read_b128 v[200:203], v252 offset:16384\n\t" \
    "s_waitcnt lgkmcnt(6)\n\t" \
    "v_mfma_f32_16x16x32_bf16 v[208:211], v[192:195], v[224:227], v[208:211]\n\t" \
    "ds_read_b128 v[224:227], v252 offset:17408\n\t" \
    "s_waitcnt lgkmcnt(6)\n\t" \
    "v_mfma_f32_16x16x32_bf16 v[208:211], v[196:199], v[228:231], v[208:211]\n\t" \
    "v_xor_b32 v252, 0x3800, %[xa]\n\t" \
    "ds_read_b128 v[228:231], v252 offset:16384\n\t" \
    "s_waitcnt lgkmcnt(6)\n\t" \
    "v_mfma_f32_16x16x32_bf16 v[220:223], v[88:91], v[232:235], v[220:223]\n\t" \
    "ds_read_b128 v[232:235], v252 offset:17408\n\t" \
    "s_waitcnt lgkmcnt(6)\n\t" \
    "v_mfma_f32_16x16x32_bf16 v[220:223], v[92:95], v[236:239], v[220:223]\n\t" \
    "v_and_b32 v252, 0x3ff, %[xa]\n\t" \
    "ds_read_b128 v[236:239], v252 offset:47104\n\t" \
    "s_waitcnt lgkmcnt(6)\n\t" \
    "v_mfma_f32_16x16x32_bf16 v[220:223], v[112:115], v[240:243], v[220:223]\n\t" \
    "ds_read_b128 v[240:243], v252 offset:48128\n\t" \
    "v_mov_b64 v[244:245], 0\n\t" \
    "s_waitcnt lgkmcnt(6)\n\t" \
    "v_mfma_f32_16x16x32_bf16 v[220:223], v[116:119], v[212:215], v[220:223]\n\t" \
    "v_mov_b64 v[246:247], 0\n\t" \
    "s_waitcnt lgkmcnt(5)\n\t" \
    "v_mfma_f32_16x16x32_bf16 v[220:223], v[128:131], v[200:203], v[220:223]\n\t" \
    "v_mov_b64 v[248:249], 0\n\t" \
    "s_waitcnt lgkmcnt(4)\n\t" \
    "v_mfma_f32_16x16x32_bf16 v[220:223], v[132:135], v[224:227], v[220:223]\n\t" \
    "v_mov_b64 v[250:251], 0\n\t" \
    "s_waitcnt lgkmcnt(3)\n\t" \
    "v_mfma_f32_16x16x32_bf16 v[220:223], v[152:155], v[228:231], v[220:223]\n\t" \
    "v_add_u32 v252, %[qs], v252\n\t" \
    "v_mov_b64 v[224:225], 0\n\t" \
    "s_waitcnt lgkmcnt(2)\n\t" \
    "v_mfma_f32_16x16x32_bf16 v[220:223], v[156:159], v[232:235], v[220:223]\n\t" \
    "v_mov_b64 v[226:227], 0\n\t" \
    "v_cvt_pk_bf16_f32 v208, v208, v209\n\t" \
    "s_waitcnt lgkmcnt(1)\n\t" \
    "v_mfma_f32_16x16x32_bf16 v[220:223], v[192:195], v[236:239], v[220:223]\n\t" \
    "v_cvt_pk_bf16_f32 v209, v210, v211\n\t" \
    "ds_write_b64 v252, v[208:209] offset:2048\n\t" \
    "s_waitcnt lgkmcnt(1)\n\t" \
    "v_mfma_f32_16x16x32_bf16 v[220:223], v[196:199], v[240:243], v[220:223]\n\t" \
    "ds_read_b128 v[228:231], %[wl] offset:2048\n\t" \
    "ds_read_b128 v[232:235], %[wl] offset:3072\n\t" \
    "ds_read_b128 v[200:203], %[wl] offset:0\n\t" \
    "ds_read_b128 v[204:207], %[wl] offset:1024\n\t" \
    "s_nop 3\n\t" \
    "v_cvt_pk_bf16_f32 v220, v220, v221\n\t" \
    "v_cvt_pk_bf16_f32 v221, v222, v223\n\t" \
    "ds_write_b64 v252, v[220:221] offset:8192\n\t" \
    "s_waitcnt lgkmcnt(0)\n\t"
#define ASM_XG_V \
    "s_waitcnt lgkmcnt(0)\n\t" \
    "ds_read_b128 v[224:227], %[xa] offset:16384\n\t" \
    "ds_read_b128 v[228:231], %[xa] offset:17408\n\t" \
    "ds_read_b128 v[244:247], %[wl] offset:0\n\t" \
    "ds_read_b128 v[248:251], %[wl] offset:1024\n\t" \
    "ds_read_b128 v[232:235], %[xa] offset:0\n\t" \
    "ds_read_b128 v[236:239], %[xa] offset:1024\n\t" \
    "v_xor_b32 v252, 0x800, %[xa]\n\t" \
    "ds_read_b128 v[240:243], v252 offset:0\n\t" \
    "s_waitcnt lgkmcnt(6)\n\t" \
    "v_mfma_f32_16x16x32_bf16 v[220:223], v[8:11], v[224:227], 0\n\t" \
    "s_waitcnt lgkmcnt(5)\n\t" \
    "v_mfma_f32_16x16x32_bf16 v[220:223], v[12:15], v[228:231], v[220:223]\n\t" \
    "v_mfma_f32_16x16x32_bf16 v[216:219], v[0:3], v[224:227], 0\n\t" \
    "v_mfma_f32_16x16x32_bf16 v[216:219], v[4:7], v[228:231], v[216:219]\n\t" \
    "s_waitcnt lgkmcnt(4)\n\t" \
    "v_mfma_f32_16x16x32_bf16 v[212:215], v[244:247], v[224:227], 0\n\t" \
    "ds_read_b128 v[224:227], v252 offset:1024\n\t" \
    "s_waitcnt lgkmcnt(4)\n\t" \
    "v_mfma_f32_16x16x32_bf16 v[212:215], v[248:251], v[228:231], v[212:215]\n\t" \
    "ds_read_b128 v[228:231], v252 offset:16384\n\t" \
    "s_waitcnt lgkmcnt(4)\n\t" \
    "v_mfma_f32_16x16x32_bf16 v[200:203], v[244:247], v[232:235], 0\n\t" \
    "ds_read_b128 v[244:247], %[wl] offset:2048\n\t" \
    "s_waitcnt lgkmcnt(4)\n\t" \
    "v_mfma_f32_16x16x32_bf16 v[200:203], v[248:251], v[236:239], v[200:203]\n\t" \
    "ds_read_b128 v[248:251], %[wl] offset:3072\n\t" \
    "v_mfma_f32_16x16x32_bf16 v[204:207], v[0:3], v[232:235], 0\n\t" \
    "v_mfma_f32_16x16x32_bf16 v[204:207], v[4:7], v[236:239], v[204:207]\n\t" \
    "v_mfma_f32_16x16x32_bf16 v[208:211], v[8:11], v[232:235], 0\n\t" \
    "ds_read_b128 v[232:235], v252 offset:17408\n\t" \
    "v_mfma_f32_16x16x32_bf16 v[208:211], v[12:15], v[236:239], v[208:211]\n\t" \
    "v_xor_b32 v252, 0x1000, %[xa]\n\t" \
    "ds_read_b128 v[236:239], v252 offset:16384\n\t" \
    "s_waitcnt lgkmcnt(6)\n\t" \
    "v_mfma_f32_16x16x32_bf16 v[208:211], v[32:35], v[240:243], v[208:211]\n\t" \
    "s_waitcnt lgkmcnt(5)\n\t" \
    "v_mfma_f32_16x16x32_bf16 v[208:211], v[36:39], v[224:227], v[208:211]\n\t" \
    "v_mfma_f32_16x16x32_bf16 v[204:207], v[24:27], v[240:243], v[204:207]\n\t" \
    "v_mfma_f32_16x16x32_bf16 v[204:207], v[28:31], v[224:227], v[204:207]\n\t" \
    "v_mfma_f32_16x16x32_bf16 v[200:203], v[16:19], v[240:243], v[200:203]\n\t" \
    "ds_read_b128 v[240:243], v252 offset:17408\n\t" \
    "v_mfma_f32_16x16x32_bf16 v[200:203], v[20:23], v[224:227], v[200:203]\n\t" \
    "ds_read_b128 v[224:227], v252 offset:0\n\t" \
    "s_waitcnt lgkmcnt(6)\n\t" \
    "v_mfma_f32_16x16x32_bf16 v[212:215], v[16:19], v[228:231], v[212:215]\n\t" \
    "s_waitcnt lgkmcnt(3)\n\t" \
    "v_mfma_f32_16x16x32_bf16 v[212:215], v[20:23], v[232:235], v[212:215]\n\t" \
    "v_mfma_f32_16x16x32_bf16 v[216:219], v[24:27], v[228:231], v[216:219]\n\t" \
    "v_mfma_f32_16x16x32_bf16 v[216:219], v[28:31], v[232:235], v[216:219]\n\t" \
    "v_mfma_f32_16x16x32_bf16 v[220:223], v[32:35], v[228:231], v[220:223]\n\t" \
    "ds_read_b128 v[228:231], v252 offset:1024\n\t" \
    "v_mfma_f32_16x16x32_bf16 v[220:223], v[36:39], v[232:235], v[220:223]\n\t" \
    "v_xor_b32 v252, 0x1800, %[xa]\n\t" \
    "ds_read_b128 v[232:235], v252 offset:0\n\t" \
    "s_waitcnt lgkmcnt(4)\n\t" \
    "v_mfma_f32_16x16x32_bf16 v[220:223], v[48:51], v[236:239], v[220:223]\n\t" \
    "s_waitcnt lgkmcnt(3)\n\t" \
    "v_mfma_f32_16x16x32_bf16 v[220:223], v[52:55], v[240:243], v[220:223]\n\t" \
    "v_mfma_f32_16x16x32_bf16 v[216:219], v[40:43], v[236:239], v[216:219]\n\t" \
    "v_mfma_f32_16x16x32_bf16 v[216:219], v[44:47], v[240:243], v[216:219]\n\t" \
    "v_mfma_f32_16x16x32_bf16 v[212:215], v[244:247], v[236:239], v[212:215]\n\t" \
    "ds_read_b128 v[236:239], v252 offset:1024\n\t" \
    "v_mfma_f32_16x16x32_bf16 v[212:215], v[248:251], v[240:243], v[212:215]\n\t" \
    "ds_read_b128 v[240:243], v252 offset:16384\n\t" \
    "s_waitcnt lgkmcnt(4)\n\t" \
    "v_mfma_f32_16x16x32_bf16 v[200:203], v[244:247], v[224:227], v[200:203]\n\t" \
    "ds_read_b128 v[244:247], %[wl] offset:8192\n\t" \
    "s_waitcnt lgkmcnt(4)\n\t" \
    "v_mfma_f32_16x16x32_bf16 v[200:203], v[248:251], v[228:231], v[200:203]\n\t" \
    "ds_read_b128 v[248:251], %[wl] offset:9216\n\t" \
    "v_mfma_f32_16x16x32_bf16 v[204:207], v[40:43], v[224:227], v[204:207]\n\t" \
    "v_mfma_f32_16x16x32_bf16 v[204:207], v[44:47], v[228:231], v[204:207]\n\t" \
    "v_mfma_f32_16x16x32_bf16 v[208:211], v[48:51], v[224:227], v[208:211]\n\t" \
    "ds_read_b128 v[224:227], v252 offset:17408\n\t" \
    "v_mfma_f32_16x16x32_bf16 v[208:211], v[52:55], v[228:231], v[208:211]\n\t" \
    "v_and_b32 v252, 0x3ff, %[xa]\n\t" \
    "ds_read_b128 v[228:231], v252 offset:45056\n\t" \
    "s_waitcnt lgkmcnt(6)\n\t" \
    "v_mfma_f32_16x16x32_bf16 v[208:211], v[72:75], v[232:235], v[208:211]\n\t" \
    "s_waitcnt lgkmcnt(5)\n\t" \
    "v_mfma_f32_16x16x32_bf16 v[208:211], v[76:79], v[236:239], v[208:211]\n\t" \
    "v_mfma_f32_16x16x32_bf16 v[204:207], v[64:67], v[232:235], v[204:207]\n\t" \
    "v_mfma_f32_16x16x32_bf16 v[204:207], v[68:71], v[236:239], v[204:207]\n\t" \
    "v_mfma_f32_16x16x32_bf16 v[200:203], v[56:59], v[232:235], v[200:203]\n\t" \
    "ds_read_b128 v[232:235], v252 offset:46080\n\t" \
    "v_mfma_f32_16x16x32_bf16 v[200:203], v[60:63], v[236:239], v[200:203]\n\t" \
    "ds_read_b128 v[236:239], v252 offset:40960\n\t" \
    "s_waitcnt lgkmcnt(6)\n\t" \
    "v_mfma_f32_16x16x32_bf16 v[212:215], v[56:59], v[240:243], v[212:215]\n\t" \
    "s_waitcnt lgkmcnt(3)\n\t" \
    "v_mfma_f32_16x16x32_bf16 v[212:215], v[60:63], v[224:227], v[212:215]\n\t" \
    "v_mfma_f32_16x16x32_bf16 v[216:219], v[64:67], v[240:243], v[216:219]\n\t" \
    "v_mfma_f32_16x16x32_bf16 v[216:219], v[68:71], v[224:227], v[216:219]\n\t" \
    "v_mfma_f32_16x16x32_bf16 v[220:223], v[72:75], v[240:243], v[220:223]\n\t" \
    "ds_read_b128 v[240:243], v252 offset:41984\n\t" \
    "v_mfma_f32_16x16x32_bf16 v[220:223], v[76:79], v[224:227], v[220:223]\n\t" \
    "v_xor_b32 v252, 0x2000, %[xa]\n\t" \
    "ds_read_b128 v[224:227], v252 offset:0\n\t" \
    "s_waitcnt lgkmcnt(4)\n\t" \
    "v_mfma_f32_16x16x32_bf16 v[220:223], v[168:171], v[228:231], v[220:223]\n\t" \
    "s_waitcnt lgkmcnt(3)\n\t" \
    "v_mfma_f32_16x16x32_bf16 v[220:223], v[172:175], v[232:235], v[220:223]\n\t" \
    "v_mfma_f32_16x16x32_bf16 v[216:219], v[160:163], v[228:231], v[216:219]\n\t" \
    "v_mfma_f32_16x16x32_bf16 v[216:219], v[164:167], v[232:235], v[216:219]\n\t" \
    "v_mfma_f32_16x16x32_bf16 v[212:215], v[244:247], v[228:231], v[212:215]\n\t" \
    "ds_read_b128 v[228:231], v252 offset:1024\n\t" \
    "v_mfma_f32_16x16x32_bf16 v[212:215], v[248:251], v[232:235], v[212:215]\n\t" \
    "ds_read_b128 v[232:235], v252 offset:16384\n\t" \
    "s_waitcnt lgkmcnt(4)\n\t" \
    "v_mfma_f32_16x16x32_bf16 v[200:203], v[244:247], v[236:239], v[200:203]\n\t" \
    "ds_read_b128 v[244:247], %[wl] offset:4096\n\t" \
    "s_waitcnt lgkmcnt(4)\n\t" \
    "v_mfma_f32_16x16x32_bf16 v[200:203], v[248:251], v[240:243], v[200:203]\n\t" \
    "ds_read_b128 v[248:251], %[wl] offset:5120\n\t" \
    "v_mfma_f32_16x16x32_bf16 v[204:207], v[160:163], v[236:239], v[204:207]\n\t" \
    "v_mfma_f32_16x16x32_bf16 v[204:207], v[164:167], v[240:243], v[204:207]\n\t" \
    "v_mfma_f32_16x16x32_bf16 v[208:211], v[168:171], v[236:239], v[208:211]\n\t" \
    "ds_read_b128 v[236:239], v252 offset:17408\n\t" \
    "v_mfma_f32_16x16x32_bf16 v[208:211], v[172:175], v[240:243], v[208:211]\n\t" \
    "v_xor_b32 v252, 0x2800, %[xa]\n\t" \
    "ds_read_b128 v[240:243], v252 offset:16384\n\t" \
    "s_waitcnt lgkmcnt(6)\n\t" \
    "v_mfma_f32_16x16x32_bf16 v[208:211], v[88:91], v[224:227], v[208:211]\n\t" \
    "s_waitcnt lgkmcnt(5)\n\t" \
    "v_mfma_f32_16x16x32_bf16 v[208:211], v[92:95], v[228:231], v[208:211]\n\t" \
    "v_mfma_f32_16x16x32_bf16 v[204:207], v[80:83], v[224:227], v[204:207]\n\t" \
    "v_mfma_f32_16x16x32_bf16 v[204:207], v[84:87], v[228:231], v[204:207]\n\t" \
    "s_waitcnt lgkmcnt(3)\n\t" \
    "v_mfma_f32_16x16x32_bf16 v[200:203], v[244:247], v[224:227], v[200:203]\n\t" \
    "ds_read_b128 v[224:227], v252 offset:17408\n\t" \
    "s_waitcnt lgkmcnt(3)\n\t" \
    "v_mfma_f32_16x16x32_bf16 v[200:203], v[248:251], v[228:231], v[200:203]\n\t" \
    "ds_read_b128 v[228:231], v252 offset:0\n\t" \
    "v_mfma_f32_16x16x32_bf16 v[212:215], v[244:247], v[232:235], v[212:215]\n\t" \
    "ds_read_b128 v[244:247], %[wl] offset:6144\n\t" \
    "s_waitcnt lgkmcnt(4)\n\t" \
    "v_mfma_f32_16x16x32_bf16 v[212:215], v[248:251], v[236:239], v[212:215]\n\t" \
    "ds_read_b128 v[248:251], %[wl] offset:7168\n\t" \
    "v_mfma_f32_16x16x32_bf16 v[216:219], v[80:83], v[232:235], v[216:219]\n\t" \
    "v_mfma_f32_16x16x32_bf16 v[216:219], v[84:87], v[236:239], v[216:219]\n\t" \
    "v_mfma_f32_16x16x32_bf16 v[220:223], v[88:91], v[232:235], v[220:223]\n\t" \
    "ds_read_b128 v[232:235], v252 offset:1024\n\t" \
    "v_mfma_f32_16x16x32_bf16 v[220:223], v[92:95], v[236:239], v[220:223]\n\t" \
    "v_xor_b32 v252, 0x3000, %[xa]\n\t" \
    "ds_read_b128 v[236:239], v252 offset:0\n\t" \
    "s_waitcnt lgkmcnt(6)\n\t" \
    "v_mfma_f32_16x16x32_bf16 v[220:223], v[112:115], v[240:243], v[220:223]\n\t" \
    "s_waitcnt lgkmcnt(5)\n\t" \
    "v_mfma_f32_16x16x32_bf16 v[220:223], v[116:119], v[224:227], v[220:223]\n\t" \
    "v_mfma_f32_16x16x32_bf16 v[216:219], v[104:107], v[240:243], v[216:219]\n\t" \
    "v_mfma_f32_16x16x32_bf16 v[216:219], v[108:111], v[224:227], v[216:219]\n\t" \
    "v_mfma_f32_16x16x32_bf16 v[212:215], v[96:99], v[240:243], v[212:215]\n\t" \
    "ds_read_b128 v[240:243], v252 offset:1024\n\t" \
    "v_mfma_f32_16x16x32_bf16 v[212:215], v[100:103], v[224:227], v[212:215]\n\t" \
    "ds_read_b128 v[224:227], v252 offset:16384\n\t" \
    "s_waitcnt lgkmcnt(6)\n\t" \
    "v_mfma_f32_16x16x32_bf16 v[200:203], v[96:99], v[228:231], v[200:203]\n\t" \
    "s_waitcnt lgkmcnt(3)\n\t" \
    "v_mfma_f32_16x16x32_bf16 v[200:203], v[100:103], v[232:235], v[200:203]\n\t" \
    "v_mfma_f32_16x16x32_bf16 v[204:207], v[104:107], v[228:231], v[204:207]\n\t" \
    "v_mfma_f32_16x16x32_bf16 v[204:207], v[108:111], v[232:235], v[204:207]\n\t" \
    "v_mfma_f32_16x16x32_bf16 v[208:211], v[112:115], v[228:231], v[208:211]\n\t" \
    "ds_read_b128 v[228:231], v252 offset:17408\n\t" \
    "v_mfma_f32_16x16x32_bf16 v[208:211], v[116:119], v[232:235], v[208:211]\n\t" \
    "v_xor_b32 v252, 0x3800, %[xa]\n\t" \
    "ds_read_b128 v[232:235], v252 offset:16384\n\t" \
    "s_waitcnt lgkmcnt(4)\n\t" \
    "v_mfma_f32_16x16x32_bf16 v[208:211], v[128:131], v[236:239], v[208:211]\n\t" \
    "s_waitcnt lgkmcnt(3)\n\t" \
    "v_mfma_f32_16x16x32_bf16 v[208:211], v[132:135], v[240:243], v[208:211]\n\t" \
    "v_mfma_f32_16x16x32_bf16 v[204:207], v[120:123], v[236:239], v[204:207]\n\t" \
    "v_mfma_f32_16x16x32_bf16 v[204:207], v[124:127], v[240:243], v[204:207]\n\t" \
    "v_mfma_f32_16x16x32_bf16 v[200:203], v[244:247], v[236:239], v[200:203]\n\t" \
    "ds_read_b128 v[236:239], v252 offset:17408\n\t" \
    "v_mfma_f32_16x16x32_bf16 v[200:203], v[248:251], v[240:243], v[200:203]\n\t" \
    "ds_read_b128 v[240:243], v252 offset:0\n\t" \
    "s_waitcnt lgkmcnt(4)\n\t" \
    "v_mfma_f32_16x16x32_bf16 v[212:215], v[244:247], v[224:227], v[212:215]\n\t" \
    "s_waitcnt lgkmcnt(3)\n\t" \
    "v_mfma_f32_16x16x32_bf16 v[212:215], v[248:251], v[228:231], v[212:215]\n\t" \
    "v_mfma_f32_16x16x32_bf16 v[216:219], v[120:123], v[224:227], v[216:219]\n\t" \
    "v_mfma_f32_16x16x32_bf16 v[216:219], v[124:127], v[228:231], v[216:219]\n\t" \
    "v_mfma_f32_16x16x32_bf16 v[220:223], v[128:131], v[224:227], v[220:223]\n\t" \
    "ds_read_b128 v[224:227], v252 offset:1024\n\t" \
    "v_mfma_f32_16x16x32_bf16 v[220:223], v[132:135], v[228:231], v[220:223]\n\t" \
    "v_and_b32 v252, 0x3ff, %[xa]\n\t" \
    "ds_read_b128 v[228:231], v252 offset:43008\n\t" \
    "s_waitcnt lgkmcnt(4)\n\t" \
    "v_mfma_f32_16x16x32_bf16 v[220:223], v[152:155], v[232:235], v[220:223]\n\t" \
    "s_waitcnt lgkmcnt(3)\n\t" \
    "v_mfma_f32_16x16x32_bf16 v[220:223], v[156:159], v[236:239], v[220:223]\n\t" \
    "v_mfma_f32_16x16x32_bf16 v[216:219], v[144:147], v[232:235], v[216:219]\n\t" \
    "v_mfma_f32_16x16x32_bf16 v[216:219], v[148:151], v[236:239], v[216:219]\n\t" \
    "v_mfma_f32_16x16x32_bf16 v[212:215], v[136:139], v[232:235], v[212:215]\n\t" \
    "ds_read_b128 v[232:235], v252 offset:44032\n\t" \
    "v_mfma_f32_16x16x32_bf16 v[212:215], v[140:143], v[236:239], v[212:215]\n\t" \
    "ds_read_b128 v[236:239], v252 offset:47104\n\t" \
    "s_waitcnt lgkmcnt(4)\n\t" \
    "v_mfma_f32_16x16x32_bf16 v[200:203], v[136:139], v[240:243], v[200:203]\n\t" \
    "s_waitcnt lgkmcnt(3)\n\t" \
    "v_mfma_f32_16x16x32_bf16 v[200:203], v[140:143], v[224:227], v[200:203]\n\t" \
    "v_mfma_f32_16x16x32_bf16 v[204:207], v[144:147], v[240:243], v[204:207]\n\t" \
    "v_mfma_f32_16x16x32_bf16 v[204:207], v[148:151], v[224:227], v[204:207]\n\t" \
    "v_mfma_f32_16x16x32_bf16 v[208:211], v[152:155], v[240:243], v[208:211]\n\t" \
    "ds_read_b128 v[240:243], v252 offset:48128\n\t" \
    "v_mfma_f32_16x16x32_bf16 v[208:211], v[156:159], v[224:227], v[208:211]\n\t" \
    "s_waitcnt lgkmcnt(3)\n\t" \
    "v_mfma_f32_16x16x32_bf16 v[208:211], v[192:195], v[228:231], v[208:211]\n\t" \
    "v_add_u32 v252, %[qs], v252\n\t" \
    "s_waitcnt lgkmcnt(2)\n\t" \
    "v_mfma_f32_16x16x32_bf16 v[208:211], v[196:199], v[232:235], v[208:211]\n\t" \
    "v_mfma_f32_16x16x32_bf16 v[204:207], v[184:187], v[228:231], v[204:207]\n\t" \
    "v_mfma_f32_16x16x32_bf16 v[204:207], v[188:191], v[232:235], v[204:207]\n\t" \
    "v_mfma_f32_16x16x32_bf16 v[200:203], v[176:179], v[228:231], v[200:203]\n\t" \
    "v_mfma_f32_16x16x32_bf16 v[200:203], v[180:183], v[232:235], v[200:203]\n\t" \
    "s_waitcnt lgkmcnt(1)\n\t" \
    "v_mfma_f32_16x16x32_bf16 v[212:215], v[176:179], v[236:239], v[212:215]\n\t" \
    "v_cvt_pk_bf16_f32 v208, v208, v209\n\t" \
    "v_cvt_pk_bf16_f32 v209, v210, v211\n\t" \
    "s_waitcnt lgkmcnt(0)\n\t" \
    "v_mfma_f32_16x16x32_bf16 v[212:215], v[180:183], v[240:243], v[212:215]\n\t" \
    "ds_write_b64 v252, v[208:209] offset:2048\n\t" \
    "v_cvt_pk_bf16_f32 v204, v204, v205\n\t" \
    "v_mfma_f32_16x16x32_bf16 v[216:219], v[184:187], v[236:239], v[216:219]\n\t" \
    "v_cvt_pk_bf16_f32 v205, v206, v207\n\t" \
    "ds_write_b64 v252, v[204:205] offset:1024\n\t" \
    "v_mfma_f32_16x16x32_bf16 v[216:219], v[188:191], v[240:243], v[216:219]\n\t" \
    "v_cvt_pk_bf16_f32 v200, v200, v201\n\t" \
    "v_cvt_pk_bf16_f32 v201, v202, v203\n\t" \
    "v_mfma_f32_16x16x32_bf16 v[220:223], v[192:195], v[236:239], v[220:223]\n\t" \
    "ds_write_b64 v252, v[200:201] offset:0\n\t" \
    "v_mfma_f32_16x16x32_bf16 v[220:223], v[196:199], v[240:243], v[220:223]\n\t" \
    "v_cvt_pk_bf16_f32 v212, v212, v213\n\t" \
    "v_cvt_pk_bf16_f32 v213, v214, v215\n\t" \
    "ds_write_b64 v252, v[212:213] offset:6144\n\t" \
    "v_cvt_pk_bf16_f32 v216, v216, v217\n\t" \
    "v_cvt_pk_bf16_f32 v217, v218, v219\n\t" \
    "ds_write_b64 v252, v[216:217] offset:7168\n\t" \
    "s_nop 1\n\t" \
    "v_cvt_pk_bf16_f32 v220, v220, v221\n\t" \
    "v_cvt_pk_bf16_f32 v221, v222, v223\n\t" \
    "ds_write_b64 v252, v[220:221] offset:8192\n\t" \
    "s_waitcnt lgkmcnt(0)\n\t"
#define ASM_XG_H_LO \
    "s_waitcnt lgkmcnt(0)\n\t" \
    "ds_read_b128 v[224:227], %[xa] offset:16384\n\t" \
    "ds_read_b128 v[228:231], %[xa] offset:17408\n\t" \
    "ds_read_b128 v[244:247], %[wl] offset:0\n\t" \
    "ds_read_b128 v[248:251], %[wl] offset:1024\n\t" \
    "ds_read_b128 v[232:235], %[xa] offset:0\n\t" \
    "ds_read_b128 v[236:239], %[xa] offset:1024\n\t" \
    "v_xor_b32 v252, 0x800, %[xa]\n\t" \
    "ds_read_b128 v[240:243], v252 offset:0\n\t" \
    "s_waitcnt lgkmcnt(6)\n\t" \
    "v_mfma_f32_16x16x32_bf16 v[220:223], v[8:11], v[224:227], 0\n\t" \
    "s_waitcnt lgkmcnt(5)\n\t" \
    "v_mfma_f32_16x16x32_bf16 v[220:223], v[12:15], v[228:231], v[220:223]\n\t" \
    "v_mfma_f32_16x16x32_bf16 v[216:219], v[0:3], v[224:227], 0\n\t" \
    "v_mfma_f32_16x16x32_bf16 v[216:219], v[4:7], v[228:231], v[216:219]\n\t" \
    "s_waitcnt lgkmcnt(4)\n\t" \
    "v_mfma_f32_16x16x32_bf16 v[212:215], v[244:247], v[224:227], 0\n\t" \
    "ds_read_b128 v[224:227], v252 offset:1024\n\t" \
    "s_waitcnt lgkmcnt(4)\n\t" \
    "v_mfma_f32_16x16x32_bf16 v[212:215], v[248:251], v[228:231], v[212:215]\n\t" \
    "ds_read_b128 v[228:231], v252 offset:16384\n\t" \
    "s_waitcnt lgkmcnt(4)\n\t" \
    "v_mfma_f32_16x16x32_bf16 v[200:203], v[244:247], v[232:235], 0\n\t" \
    "ds_read_b128 v[244:247], %[wl] offset:2048\n\t" \
    "s_waitcnt lgkmcnt(4)\n\t" \
    "v_mfma_f32_16x16x32_bf16 v[200:203], v[248:251], v[236:239], v[200:203]\n\t" \
    "ds_read_b128 v[248:251], %[wl] offset:3072\n\t" \
    "v_mfma_f32_16x16x32_bf16 v[204:207], v[0:3], v[232:235], 0\n\t" \
    "v_mfma_f32_16x16x32_bf16 v[204:207], v[4:7], v[236:239], v[204:207]\n\t" \
    "v_mfma_f32_16x16x32_bf16 v[208:211], v[8:11], v[232:235], 0\n\t" \
    "ds_read_b128 v[232:235], v252 offset:17408\n\t" \
    "v_mfma_f32_16x16x32_bf16 v[208:211], v[12:15], v[236:239], v[208:211]\n\t" \
    "v_xor_b32 v252, 0x1000, %[xa]\n\t" \
    "ds_read_b128 v[236:239], v252 offset:16384\n\t" \
    "s_waitcnt lgkmcnt(6)\n\t" \
    "v_mfma_f32_16x16x32_bf16 v[208:211], v[32:35], v[240:243], v[208:211]\n\t" \
    "s_waitcnt lgkmcnt(5)\n\t" \
    "v_mfma_f32_16x16x32_bf16 v[208:211], v[36:39], v[224:227], v[208:211]\n\t" \
    "v_mfma_f32_16x16x32_bf16 v[204:207], v[24:27], v[240:243], v[204:207]\n\t" \
    "v_mfma_f32_16x16x32_bf16 v[204:207], v[28:31], v[224:227], v[204:207]\n\t" \
    "v_mfma_f32_16x16x32_bf16 v[200:203], v[16:19], v[240:243], v[200:203]\n\t" \
    "ds_read_b128 v[240:243], v252 offset:17408\n\t" \
    "v_mfma_f32_16x16x32_bf16 v[200:203], v[20:23], v[224:227], v[200:203]\n\t" \
    "ds_read_b128 v[224:227], v252 offset:0\n\t" \
    "s_waitcnt lgkmcnt(6)\n\t" \
    "v_mfma_f32_16x16x32_bf16 v[212:215], v[16:19], v[228:231], v[212:215]\n\t" \
    "s_waitcnt lgkmcnt(3)\n\t" \
    "v_mfma_f32_16x16x32_bf16 v[212:215], v[20:23], v[232:235], v[212:215]\n\t" \
    "v_mfma_f32_16x16x32_bf16 v[216:219], v[24:27], v[228:231], v[216:219]\n\t" \
    "v_mfma_f32_16x16x32_bf16 v[216:219], v[28:31], v[232:235], v[216:219]\n\t" \
    "v_mfma_f32_16x16x32_bf16 v[220:223], v[32:35], v[228:231], v[220:223]\n\t" \
    "ds_read_b128 v[228:231], v252 offset:1024\n\t" \
    "v_mfma_f32_16x16x32_bf16 v[220:223], v[36:39], v[232:235], v[220:223]\n\t" \
    "v_xor_b32 v252, 0x1800, %[xa]\n\t" \
    "ds_read_b128 v[232:235], v252 offset:0\n\t" \
    "s_waitcnt lgkmcnt(4)\n\t" \
    "v_mfma_f32_16x16x32_bf16 v[220:223], v[48:51], v[236:239], v[220:223]\n\t" \
    "s_waitcnt lgkmcnt(3)\n\t" \
    "v_mfma_f32_16x16x32_bf16 v[220:223], v[52:55], v[240:243], v[220:223]\n\t" \
    "v_mfma_f32_16x16x32_bf16 v[216:219], v[40:43], v[236:239], v[216:219]\n\t" \
    "v_mfma_f32_16x16x32_bf16 v[216:219], v[44:47], v[240:243], v[216:219]\n\t" \
    "v_mfma_f32_16x16x32_bf16 v[212:215], v[244:247], v[236:239], v[212:215]\n\t" \
    "ds_read_b128 v[236:239], v252 offset:1024\n\t" \
    "v_mfma_f32_16x16x32_bf16 v[212:215], v[248:251], v[240:243], v[212:215]\n\t" \
    "ds_read_b128 v[240:243], v252 offset:16384\n\t" \
    "s_waitcnt lgkmcnt(4)\n\t" \
    "v_mfma_f32_16x16x32_bf16 v[200:203], v[244:247], v[224:227], v[200:203]\n\t" \
    "ds_read_b128 v[244:247], %[wl] offset:8192\n\t" \
    "s_waitcnt lgkmcnt(4)\n\t" \
    "v_mfma_f32_16x16x32_bf16 v[200:203], v[248:251], v[228:231], v[200:203]\n\t" \
    "ds_read_b128 v[248:251], %[wl] offset:9216\n\t" \
    "v_mfma_f32_16x16x32_bf16 v[204:207], v[40:43], v[224:227], v[204:207]\n\t" \
    "v_mfma_f32_16x16x32_bf16 v[204:207], v[44:47], v[228:231], v[204:207]\n\t" \
    "v_mfma_f32_16x16x32_bf16 v[208:211], v[48:51], v[224:227], v[208:211]\n\t" \
    "ds_read_b128 v[224:227], v252 offset:17408\n\t" \
    "v_mfma_f32_16x16x32_bf16 v[208:211], v[52:55], v[228:231], v[208:211]\n\t" \
    "v_and_b32 v252, 0x3ff, %[xa]\n\t" \
    "ds_read_b128 v[228:231], v252 offset:45056\n\t" \
    "s_waitcnt lgkmcnt(6)\n\t" \
    "v_mfma_f32_16x16x32_bf16 v[208:211], v[72:75], v[232:235], v[208:211]\n\t" \
    "s_waitcnt lgkmcnt(5)\n\t" \
    "v_mfma_f32_16x16x32_bf16 v[208:211], v[76:79], v[236:239], v[208:211]\n\t" \
    "v_mfma_f32_16x16x32_bf16 v[204:207], v[64:67], v[232:235], v[204:207]\n\t" \
    "v_mfma_f32_16x16x32_bf16 v[204:207], v[68:71], v[236:239], v[204:207]\n\t" \
    "v_mfma_f32_16x16x32_bf16 v[200:203], v[56:59], v[232:235], v[200:203]\n\t" \
    "ds_read_b128 v[232:235], v252 offset:46080\n\t" \
    "v_mfma_f32_16x16x32_bf16 v[200:203], v[60:63], v[236:239], v[200:203]\n\t" \
    "ds_read_b128 v[236:239], v252 offset:40960\n\t" \
    "s_waitcnt lgkmcnt(6)\n\t" \
    "v_mfma_f32_16x16x32_bf16 v[212:215], v[56:59], v[240:243], v[212:215]\n\t" \
    "s_waitcnt lgkmcnt(3)\n\t" \
    "v_mfma_f32_16x16x32_bf16 v[212:215], v[60:63], v[224:227], v[212:215]\n\t" \
    "v_mfma_f32_16x16x32_bf16 v[216:219], v[64:67], v[240:243], v[216:219]\n\t" \
    "v_mfma_f32_16x16x32_bf16 v[216:219], v[68:71], v[224:227], v[216:219]\n\t" \
    "v_mfma_f32_16x16x32_bf16 v[220:223], v[72:75], v[240:243], v[220:223]\n\t" \
    "ds_read_b128 v[240:243], v252 offset:41984\n\t" \
    "v_mfma_f32_16x16x32_bf16 v[220:223], v[76:79], v[224:227], v[220:223]\n\t" \
    "s_waitcnt lgkmcnt(0)\n\t" \
    "s_barrier\n\t" ASM_ST \
    "v_xor_b32 v252, 0x2000, %[xa]\n\t" \
    "ds_read_b128 v[224:227], v252 offset:0\n\t" \
    "v_mfma_f32_16x16x32_bf16 v[220:223], v[168:171], v[228:231], v[220:223]\n\t" \
    "v_mfma_f32_16x16x32_bf16 v[220:223], v[172:175], v[232:235], v[220:223]\n\t" \
    "v_mfma_f32_16x16x32_bf16 v[216:219], v[160:163], v[228:231], v[216:219]\n\t" \
    "v_mfma_f32_16x16x32_bf16 v[216:219], v[164:167], v[232:235], v[216:219]\n\t" \
    "v_mfma_f32_16x16x32_bf16 v[212:215], v[244:247], v[228:231], v[212:215]\n\t" \
    "ds_read_b128 v[228:231], v252 offset:1024\n\t" \
    "v_mfma_f32_16x16x32_bf16 v[212:215], v[248:251], v[232:235], v[212:215]\n\t" \
    "ds_read_b128 v[232:235], v252 offset:16384\n\t" \
    "v_mfma_f32_16x16x32_bf16 v[200:203], v[244:247], v[236:239], v[200:203]\n\t" \
    "ds_read_b128 v[244:247], %[wl] offset:4096\n\t" \
    "v_mfma_f32_16x16x32_bf16 v[200:203], v[248:251], v[240:243], v[200:203]\n\t" \
    "ds_read_b128 v[248:251], %[wl] offset:5120\n\t" \
    "v_mfma_f32_16x16x32_bf16 v[204:207], v[160:163], v[236:239], v[204:207]\n\t" \
    "v_mfma_f32_16x16x32_bf16 v[204:207], v[164:167], v[240:243], v[204:207]\n\t" \
    "v_mfma_f32_16x16x32_bf16 v[208:211], v[168:171], v[236:239], v[208:211]\n\t" \
    "ds_read_b128 v[236:239], v252 offset:17408\n\t" \
    "v_mfma_f32_16x16x32_bf16 v[208:211], v[172:175], v[240:243], v[208:211]\n\t" \
    "v_xor_b32 v252, 0x2800, %[xa]\n\t" \
    "ds_read_b128 v[240:243], v252 offset:16384\n\t" \
    "s_waitcnt lgkmcnt(6)\n\t" \
    "v_mfma_f32_16x16x32_bf16 v[208:211], v[88:91], v[224:227], v[208:211]\n\t" \
    "s_waitcnt lgkmcnt(5)\n\t" \
    "v_mfma_f32_16x16x32_bf16 v[208:211], v[92:95], v[228:231], v[208:211]\n\t" \
    "v_mfma_f32_16x16x32_bf16 v[204:207], v[80:83], v[224:227], v[204:207]\n\t" \
    "v_mfma_f32_16x16x32_bf16 v[204:207], v[84:87], v[228:231], v[204:207]\n\t" \
    "s_waitcnt lgkmcnt(3)\n\t" \
    "v_mfma_f32_16x16x32_bf16 v[200:203], v[244:247], v[224:227], v[200:203]\n\t" \
    "ds_read_b128 v[224:227], v252 offset:17408\n\t" \
    "s_waitcnt lgkmcnt(3)\n\t" \
    "v_mfma_f32_16x16x32_bf16 v[200:203], v[248:251], v[228:231], v[200:203]\n\t" \
    "ds_read_b128 v[228:231], v252 offset:0\n\t" \
    "v_mfma_f32_16x16x32_bf16 v[212:215], v[244:247], v[232:235], v[212:215]\n\t" \
    "ds_read_b128 v[244:247], %[wl] offset:6144\n\t" \
    "s_waitcnt lgkmcnt(4)\n\t" \
    "v_mfma_f32_16x16x32_bf16 v[212:215], v[248:251], v[236:239], v[212:215]\n\t" \
    "ds_read_b128 v[248:251], %[wl] offset:7168\n\t" \
    "v_mfma_f32_16x16x32_bf16 v[216:219], v[80:83], v[232:235], v[216:219]\n\t" \
    "v_mfma_f32_16x16x32_bf16 v[216:219], v[84:87], v[236:239], v[216:219]\n\t" \
    "v_mfma_f32_16x16x32_bf16 v[220:223], v[88:91], v[232:235], v[220:223]\n\t" \
    "ds_read_b128 v[232:235], v252 offset:1024\n\t" \
    "v_mfma_f32_16x16x32_bf16 v[220:223], v[92:95], v[236:239], v[220:223]\n\t" \
    "v_xor_b32 v252, 0x3000, %[xa]\n\t" \
    "ds_read_b128 v[236:239], v252 offset:0\n\t" \
    "s_waitcnt lgkmcnt(6)\n\t" \
    "v_mfma_f32_16x16x32_bf16 v[220:223], v[112:115], v[240:243], v[220:223]\n\t" \
    "s_waitcnt lgkmcnt(5)\n\t" \
    "v_mfma_f32_16x16x32_bf16 v[220:223], v[116:119], v[224:227], v[220:223]\n\t" \
    "v_mfma_f32_16x16x32_bf16 v[216:219], v[104:107], v[240:243], v[216:219]\n\t" \
    "v_mfma_f32_16x16x32_bf16 v[216:219], v[108:111], v[224:227], v[216:219]\n\t" \
    "v_mfma_f32_16x16x32_bf16 v[212:215], v[96:99], v[240:243], v[212:215]\n\t" \
    "ds_read_b128 v[240:243], v252 offset:1024\n\t" \
    "v_mfma_f32_16x16x32_bf16 v[212:215], v[100:103], v[224:227], v[212:215]\n\t" \
    "ds_read_b128 v[224:227], v252 offset:16384\n\t" \
    "s_waitcnt lgkmcnt(6)\n\t" \
    "v_mfma_f32_16x16x32_bf16 v[200:203], v[96:99], v[228:231], v[200:203]\n\t" \
    "s_waitcnt lgkmcnt(3)\n\t" \
    "v_mfma_f32_16x16x32_bf16 v[200:203], v[100:103], v[232:235], v[200:203]\n\t" \
    "v_mfma_f32_16x16x32_bf16 v[204:207], v[104:107], v[228:231], v[204:207]\n\t" \
    "v_mfma_f32_16x16x32_bf16 v[204:207], v[108:111], v[232:235], v[204:207]\n\t" \
    "v_mfma_f32_16x16x32_bf16 v[208:211], v[112:115], v[228:231], v[208:211]\n\t" \
    "ds_read_b128 v[228:231], v252 offset:17408\n\t" \
    "v_mfma_f32_16x16x32_bf16 v[208:211], v[116:119], v[232:235], v[208:211]\n\t" \
    "v_xor_b32 v252, 0x3800, %[xa]\n\t" \
    "ds_read_b128 v[232:235], v252 offset:16384\n\t" \
    "s_waitcnt lgkmcnt(4)\n\t" \
    "v_mfma_f32_16x16x32_bf16 v[208:211], v[128:131], v[236:239], v[208:211]\n\t" \
    "s_waitcnt lgkmcnt(3)\n\t" \
    "v_mfma_f32_16x16x32_bf16 v[208:211], v[132:135], v[240:243], v[208:211]\n\t" \
    "v_mfma_f32_16x16x32_bf16 v[204:207], v[120:123], v[236:239], v[204:207]\n\t" \
    "v_mfma_f32_16x16x32_bf16 v[204:207], v[124:127], v[240:243], v[204:207]\n\t" \
    "v_mfma_f32_16x16x32_bf16 v[200:203], v[244:247], v[236:239], v[200:203]\n\t" \
    "ds_read_b128 v[236:239], v252 offset:17408\n\t" \
    "v_mfma_f32_16x16x32_bf16 v[200:203], v[248:251], v[240:243], v[200:203]\n\t" \
    "ds_read_b128 v[240:243], v252 offset:0\n\t" \
    "s_waitcnt lgkmcnt(4)\n\t" \
    "v_mfma_f32_16x16x32_bf16 v[212:215], v[244:247], v[224:227], v[212:215]\n\t" \
    "s_waitcnt lgkmcnt(3)\n\t" \
    "v_mfma_f32_16x16x32_bf16 v[212:215], v[248:251], v[228:231], v[212:215]\n\t" \
    "v_mfma_f32_16x16x32_bf16 v[216:219], v[120:123], v[224:227], v[216:219]\n\t" \
    "v_mfma_f32_16x16x32_bf16 v[216:219], v[124:127], v[228:231], v[216:219]\n\t" \
    "v_mfma_f32_16x16x32_bf16 v[220:223], v[128:131], v[224:227], v[220:223]\n\t" \
    "ds_read_b128 v[224:227], v252 offset:1024\n\t" \
    "v_mfma_f32_16x16x32_bf16 v[220:223], v[132:135], v[228:231], v[220:223]\n\t" \
    "v_and_b32 v252, 0x3ff, %[xa]\n\t" \
    "ds_read_b128 v[228:231], v252 offset:43008\n\t" \
    "s_waitcnt lgkmcnt(4)\n\t" \
    "v_mfma_f32_16x16x32_bf16 v[220:223], v[152:155], v[232:235], v[220:223]\n\t" \
    "s_waitcnt lgkmcnt(3)\n\t" \
    "v_mfma_f32_16x16x32_bf16 v[220:223], v[156:159], v[236:239], v[220:223]\n\t" \
    "v_mfma_f32_16x16x32_bf16 v[216:219], v[144:147], v[232:235], v[216:219]\n\t" \
    "v_mfma_f32_16x16x32_bf16 v[216:219], v[148:151], v[236:239], v[216:219]\n\t" \
    "v_mfma_f32_16x16x32_bf16 v[212:215], v[136:139], v[232:235], v[212:215]\n\t" \
    "ds_read_b128 v[232:235], v252 offset:44032\n\t" \
    "v_mfma_f32_16x16x32_bf16 v[212:215], v[140:143], v[236:239], v[212:215]\n\t" \
    "ds_read_b128 v[236:239], v252 offset:47104\n\t" \
    "s_waitcnt lgkmcnt(4)\n\t" \
    "v_mfma_f32_16x16x32_bf16 v[200:203], v[136:139], v[240:243], v[200:203]\n\t" \
    "s_waitcnt lgkmcnt(3)\n\t" \
    "v_mfma_f32_16x16x32_bf16 v[200:203], v[140:143], v[224:227], v[200:203]\n\t" \
    "v_mfma_f32_16x16x32_bf16 v[204:207], v[144:147], v[240:243], v[204:207]\n\t" \
    "v_mfma_f32_16x16x32_bf16 v[204:207], v[148:151], v[224:227], v[204:207]\n\t" \
    "v_mfma_f32_16x16x32_bf16 v[208:211], v[152:155], v[240:243], v[208:211]\n\t" \
    "ds_read_b128 v[240:243], v252 offset:48128\n\t" \
    "v_mfma_f32_16x16x32_bf16 v[208:211], v[156:159], v[224:227], v[208:211]\n\t" \
    "s_waitcnt lgkmcnt(3)\n\t" \
    "v_mfma_f32_16x16x32_bf16 v[208:211], v[192:195], v[228:231], v[208:211]\n\t" \
    "v_add_u32 v252, %[qs], v252\n\t" \
    "s_waitcnt lgkmcnt(2)\n\t" \
    "v_mfma_f32_16x16x32_bf16 v[208:211], v[196:199], v[232:235], v[208:211]\n\t" \
    "v_mfma_f32_16x16x32_bf16 v[204:207], v[184:187], v[228:231], v[204:207]\n\t" \
    "v_mfma_f32_16x16x32_bf16 v[204:207], v[188:191], v[232:235], v[204:207]\n\t" \
    "v_mfma_f32_16x16x32_bf16 v[200:203], v[176:179], v[228:231], v[200:203]\n\t" \
    "v_mfma_f32_16x16x32_bf16 v[200:203], v[180:183], v[232:235], v[200:203]\n\t" \
    "s_waitcnt lgkmcnt(1)\n\t" \
    "v_mfma_f32_16x16x32_bf16 v[212:215], v[176:179], v[236:239], v[212:215]\n\t" \
    "v_cvt_pk_bf16_f32 v208, v208, v209\n\t" \
    "v_cvt_pk_bf16_f32 v209, v210, v211\n\t" \
    "s_waitcnt lgkmcnt(0)\n\t" \
    "v_mfma_f32_16x16x32_bf16 v[212:215], v[180:183], v[240:243], v[212:215]\n\t" \
    "ds_write_b64 v252, v[208:209] offset:2048\n\t" \
    "v_cvt_pk_bf16_f32 v204, v204, v205\n\t" \
    "v_mfma_f32_16x16x32_bf16 v[216:219], v[184:187], v[236:239], v[216:219]\n\t" \
    "v_cvt_pk_bf16_f32 v205, v206, v207\n\t" \
    "ds_write_b64 v252, v[204:205] offset:1024\n\t" \
    "v_mfma_f32_16x16x32_bf16 v[216:219], v[188:191], v[240:243], v[216:219]\n\t" \
    "v_cvt_pk_bf16_f32 v200, v200, v201\n\t" \
    "v_cvt_pk_bf16_f32 v201, v202, v203\n\t" \
    "v_mfma_f32_16x16x32_bf16 v[220:223], v[192:195], v[236:239], v[220:223]\n\t" \
    "ds_write_b64 v252, v[200:201] offset:0\n\t" \
    "v_mfma_f32_16x16x32_bf16 v[220:223], v[196:199], v[240:243], v[220:223]\n\t" \
    "v_cvt_pk_bf16_f32 v212, v212, v213\n\t" \
    "v_cvt_pk_bf16_f32 v213, v214, v215\n\t" \
    "ds_write_b64 v252, v[212:213] offset:6144\n\t" \
    "v_cvt_pk_bf16_f32 v216, v216, v217\n\t" \
    "v_cvt_pk_bf16_f32 v217, v218, v219\n\t" \
    "ds_write_b64 v252, v[216:217] offset:7168\n\t" \
    "s_nop 1\n\t" \
    "v_cvt_pk_bf16_f32 v220, v220, v221\n\t" \
    "v_cvt_pk_bf16_f32 v221, v222, v223\n\t" \
    "ds_write_b64 v252, v[220:221] offset:8192\n\t" \
    "s_waitcnt lgkmcnt(0)\n\t"
#define ASM_XG_H_HI \
    "s_waitcnt lgkmcnt(0)\n\t" \
    "v_xor_b32 v252, 0x2000, %[xa]\n\t" \
    "ds_read_b128 v[224:227], v252 offset:16384\n\t" \
    "ds_read_b128 v[228:231], v252 offset:17408\n\t" \
    "ds_read_b128 v[244:247], %[wl] offset:4096\n\t" \
    "ds_read_b128 v[248:251], %[wl] offset:5120\n\t" \
    "ds_read_b128 v[232:235], v252 offset:0\n\t" \
    "ds_read_b128 v[236:239], v252 offset:1024\n\t" \
    "v_xor_b32 v252, 0x2800, %[xa]\n\t" \
    "ds_read_b128 v[240:243], v252 offset:0\n\t" \
    "s_waitcnt lgkmcnt(6)\n\t" \
    "v_mfma_f32_16x16x32_bf16 v[220:223], v[88:91], v[224:227], 0\n\t" \
    "s_waitcnt lgkmcnt(5)\n\t" \
    "v_mfma_f32_16x16x32_bf16 v[220:223], v[92:95], v[228:231], v[220:223]\n\t" \
    "v_mfma_f32_16x16x32_bf16 v[216:219], v[80:83], v[224:227], 0\n\t" \
    "v_mfma_f32_16x16x32_bf16 v[216:219], v[84:87], v[228:231], v[216:219]\n\t" \
    "s_waitcnt lgkmcnt(4)\n\t" \
    "v_mfma_f32_16x16x32_bf16 v[212:215], v[244:247], v[224:227], 0\n\t" \
    "ds_read_b128 v[224:227], v252 offset:1024\n\t" \
    "s_waitcnt lgkmcnt(4)\n\t" \
    "v_mfma_f32_16x16x32_bf16 v[212:215], v[248:251], v[228:231], v[212:215]\n\t" \
    "ds_read_b128 v[228:231], v252 offset:16384\n\t" \
    "s_waitcnt lgkmcnt(4)\n\t" \
    "v_mfma_f32_16x16x32_bf16 v[200:203], v[244:247], v[232:235], 0\n\t" \
    "ds_read_b128 v[244:247], %[wl] offset:6144\n\t" \
    "s_waitcnt lgkmcnt(4)\n\t" \
    "v_mfma_f32_16x16x32_bf16 v[200:203], v[248:251], v[236:239], v[200:203]\n\t" \
    "ds_read_b128 v[248:251], %[wl] offset:7168\n\t" \
    "v_mfma_f32_16x16x32_bf16 v[204:207], v[80:83], v[232:235], 0\n\t" \
    "v_mfma_f32_16x16x32_bf16 v[204:207], v[84:87], v[236:239], v[204:207]\n\t" \
    "v_mfma_f32_16x16x32_bf16 v[208:211], v[88:91], v[232:235], 0\n\t" \
    "ds_read_b128 v[232:235], v252 offset:17408\n\t" \
    "v_mfma_f32_16x16x32_bf16 v[208:211], v[92:95], v[236:239], v[208:211]\n\t" \
    "v_xor_b32 v252, 0x3000, %[xa]\n\t" \
    "ds_read_b128 v[236:239], v252 offset:16384\n\t" \
    "s_waitcnt lgkmcnt(6)\n\t" \
    "v_mfma_f32_16x16x32_bf16 v[208:211], v[112:115], v[240:243], v[208:211]\n\t" \
    "s_waitcnt lgkmcnt(5)\n\t" \
    "v_mfma_f32_16x16x32_bf16 v[208:211], v[116:119], v[224:227], v[208:211]\n\t" \
    "v_mfma_f32_16x16x32_bf16 v[204:207], v[104:107], v[240:243], v[204:207]\n\t" \
    "v_mfma_f32_16x16x32_bf16 v[204:207], v[108:111], v[224:227], v[204:207]\n\t" \
    "v_mfma_f32_16x16x32_bf16 v[200:203], v[96:99], v[240:243], v[200:203]\n\t" \
    "ds_read_b128 v[240:243], v252 offset:17408\n\t" \
    "v_mfma_f32_16x16x32_bf16 v[200:203], v[100:103], v[224:227], v[200:203]\n\t" \
    "s_nop 3\n\t" \
    "v_mov_b32 v224, %[tv]\n\t" \
    "s_waitcnt vmcnt(5)\n\t" \
    "buffer_store_dword v224, %[fv], %[rr], %[fp] offen sc1\n\t" \
    "ds_read_b128 v[224:227], v252 offset:0\n\t" \
    "s_waitcnt lgkmcnt(6)\n\t" \
    "v_mfma_f32_16x16x32_bf16 v[212:215], v[96:99], v[228:231], v[212:215]\n\t" \
    "s_waitcnt lgkmcnt(3)\n\t" \
    "v_mfma_f32_16x16x32_bf16 v[212:215], v[100:103], v[232:235], v[212:215]\n\t" \
    "v_mfma_f32_16x16x32_bf16 v[216:219], v[104:107], v[228:231], v[216:219]\n\t" \
    "v_mfma_f32_16x16x32_bf16 v[216:219], v[108:111], v[232:235], v[216:219]\n\t" \
    "v_mfma_f32_16x16x32_bf16 v[220:223], v[112:115], v[228:231], v[220:223]\n\t" \
    "ds_read_b128 v[228:231], v252 offset:1024\n\t" \
    "v_mfma_f32_16x16x32_bf16 v[220:223], v[116:119], v[232:235], v[220:223]\n\t" \
    "v_xor_b32 v252, 0x3800, %[xa]\n\t" \
    "ds_read_b128 v[232:235], v252 offset:0\n\t" \
    "s_waitcnt lgkmcnt(4)\n\t" \
    "v_mfma_f32_16x16x32_bf16 v[220:223], v[128:131], v[236:239], v[220:223]\n\t" \
    "s_waitcnt lgkmcnt(3)\n\t" \
    "v_mfma_f32_16x16x32_bf16 v[220:223], v[132:135], v[240:243], v[220:223]\n\t" \
    "v_mfma_f32_16x16x32_bf16 v[216:219], v[120:123], v[236:239], v[216:219]\n\t" \
    "v_mfma_f32_16x16x32_bf16 v[216:219], v[124:127], v[240:243], v[216:219]\n\t" \
    "v_mfma_f32_16x16x32_bf16 v[212:215], v[244:247], v[236:239], v[212:215]\n\t" \
    "ds_read_b128 v[236:239], v252 offset:1024\n\t" \
    "v_mfma_f32_16x16x32_bf16 v[212:215], v[248:251], v[240:243], v[212:215]\n\t" \
    "ds_read_b128 v[240:243], v252 offset:16384\n\t" \
    "s_waitcnt lgkmcnt(4)\n\t" \
    "v_mfma_f32_16x16x32_bf16 v[200:203], v[244:247], v[224:227], v[200:203]\n\t" \
    "ds_read_b128 v[244:247], %[wl] offset:8192\n\t" \
    "s_waitcnt lgkmcnt(4)\n\t" \
    "v_mfma_f32_16x16x32_bf16 v[200:203], v[248:251], v[228:231], v[200:203]\n\t" \
    "ds_read_b128 v[248:251], %[wl] offset:9216\n\t" \
    "v_mfma_f32_16x16x32_bf16 v[204:207], v[120:123], v[224:227], v[204:207]\n\t" \
    "v_mfma_f32_16x16x32_bf16 v[204:207], v[124:127], v[228:231], v[204:207]\n\t" \
    "v_mfma_f32_16x16x32_bf16 v[208:211], v[128:131], v[224:227], v[208:211]\n\t" \
    "ds_read_b128 v[224:227], v252 offset:17408\n\t" \
    "v_mfma_f32_16x16x32_bf16 v[208:211], v[132:135], v[228:231], v[208:211]\n\t" \
    "v_and_b32 v252, 0x3ff, %[xa]\n\t" \
    "ds_read_b128 v[228:231], v252 offset:45056\n\t" \
    "s_waitcnt lgkmcnt(6)\n\t" \
    "v_mfma_f32_16x16x32_bf16 v[208:211], v[152:155], v[232:235], v[208:211]\n\t" \
    "s_waitcnt lgkmcnt(5)\n\t" \
    "v_mfma_f32_16x16x32_bf16 v[208:211], v[156:159], v[236:239], v[208:211]\n\t" \
    "v_mfma_f32_16x16x32_bf16 v[204:207], v[144:147], v[232:235], v[204:207]\n\t" \
    "v_mfma_f32_16x16x32_bf16 v[204:207], v[148:151], v[236:239], v[204:207]\n\t" \
    "v_mfma_f32_16x16x32_bf16 v[200:203], v[136:139], v[232:235], v[200:203]\n\t" \
    "ds_read_b128 v[232:235], v252 offset:46080\n\t" \
    "v_mfma_f32_16x16x32_bf16 v[200:203], v[140:143], v[236:239], v[200:203]\n\t" \
    "ds_read_b128 v[236:239], v252 offset:40960\n\t" \
    "s_waitcnt lgkmcnt(6)\n\t" \
    "v_mfma_f32_16x16x32_bf16 v[212:215], v[136:139], v[240:243], v[212:215]\n\t" \
    "s_waitcnt lgkmcnt(3)\n\t" \
    "v_mfma_f32_16x16x32_bf16 v[212:215], v[140:143], v[224:227], v[212:215]\n\t" \
    "v_mfma_f32_16x16x32_bf16 v[216:219], v[144:147], v[240:243], v[216:219]\n\t" \
    "v_mfma_f32_16x16x32_bf16 v[216:219], v[148:151], v[224:227], v[216:219]\n\t" \
    "v_mfma_f32_16x16x32_bf16 v[220:223], v[152:155], v[240:243], v[220:223]\n\t" \
    "ds_read_b128 v[240:243], v252 offset:41984\n\t" \
    "v_mfma_f32_16x16x32_bf16 v[220:223], v[156:159], v[224:227], v[220:223]\n\t" \
    "s_waitcnt vmcnt(1) lgkmcnt(0)\n\t" \
    "s_barrier\n\t" ASM_ST \
    "ds_read_b128 v[224:227], %[xa] offset:0\n\t" \
    "v_mfma_f32_16x16x32_bf16 v[220:223], v[168:171], v[228:231], v[220:223]\n\t" \
    "v_mfma_f32_16x16x32_bf16 v[220:223], v[172:175], v[232:235], v[220:223]\n\t" \
    "v_mfma_f32_16x16x32_bf16 v[216:219], v[160:163], v[228:231], v[216:219]\n\t" \
    "v_mfma_f32_16x16x32_bf16 v[216:219], v[164:167], v[232:235], v[216:219]\n\t" \
    "v_mfma_f32_16x16x32_bf16 v[212:215], v[244:247], v[228:231], v[212:215]\n\t" \
    "ds_read_b128 v[228:231], %[xa] offset:1024\n\t" \
    "v_mfma_f32_16x16x32_bf16 v[212:215], v[248:251], v[232:235], v[212:215]\n\t" \
    "ds_read_b128 v[232:235], %[xa] offset:16384\n\t" \
    "v_mfma_f32_16x16x32_bf16 v[200:203], v[244:247], v[236:239], v[200:203]\n\t" \
    "ds_read_b128 v[244:247], %[wl] offset:0\n\t" \
    "v_mfma_f32_16x16x32_bf16 v[200:203], v[248:251], v[240:243], v[200:203]\n\t" \
    "ds_read_b128 v[248:251], %[wl] offset:1024\n\t" \
    "v_mfma_f32_16x16x32_bf16 v[204:207], v[160:163], v[236:239], v[204:207]\n\t" \
    "v_mfma_f32_16x16x32_bf16 v[204:207], v[164:167], v[240:243], v[204:207]\n\t" \
    "v_mfma_f32_16x16x32_bf16 v[208:211], v[168:171], v[236:239], v[208:211]\n\t" \
    "ds_read_b128 v[236:239], %[xa] offset:17408\n\t" \
    "v_mfma_f32_16x16x32_bf16 v[208:211], v[172:175], v[240:243], v[208:211]\n\t" \
    "v_xor_b32 v252, 0x800, %[xa]\n\t" \
    "ds_read_b128 v[240:243], v252 offset:16384\n\t" \
    "s_waitcnt lgkmcnt(6)\n\t" \
    "v_mfma_f32_16x16x32_bf16 v[208:211], v[8:11], v[224:227], v[208:211]\n\t" \
    "s_waitcnt lgkmcnt(5)\n\t" \
    "v_mfma_f32_16x16x32_bf16 v[208:211], v[12:15], v[228:231], v[208:211]\n\t" \
    "v_mfma_f32_16x16x32_bf16 v[204:207], v[0:3], v[224:227], v[204:207]\n\t" \
    "v_mfma_f32_16x16x32_bf16 v[204:207], v[4:7], v[228:231], v[204:207]\n\t" \
    "s_waitcnt lgkmcnt(3)\n\t" \
    "v_mfma_f32_16x16x32_bf16 v[200:203], v[244:247], v[224:227], v[200:203]\n\t" \
    "ds_read_b128 v[224:227], v252 offset:17408\n\t" \
    "s_waitcnt lgkmcnt(3)\n\t" \
    "v_mfma_f32_16x16x32_bf16 v[200:203], v[248:251], v[228:231], v[200:203]\n\t" \
    "ds_read_b128 v[228:231], v252 offset:0\n\t" \
    "v_mfma_f32_16x16x32_bf16 v[212:215], v[244:247], v[232:235], v[212:215]\n\t" \
    "ds_read_b128 v[244:247], %[wl] offset:2048\n\t" \
    "s_waitcnt lgkmcnt(4)\n\t" \
    "v_mfma_f32_16x16x32_bf16 v[212:215], v[248:251], v[236:239], v[212:215]\n\t" \
    "ds_read_b128 v[248:251], %[wl] offset:3072\n\t" \
    "v_mfma_f32_16x16x32_bf16 v[216:219], v[0:3], v[232:235], v[216:219]\n\t" \
    "v_mfma_f32_16x16x32_bf16 v[216:219], v[4:7], v[236:239], v[216:219]\n\t" \
    "v_mfma_f32_16x16x32_bf16 v[220:223], v[8:11], v[232:235], v[220:223]\n\t" \
    "ds_read_b128 v[232:235], v252 offset:1024\n\t" \
    "v_mfma_f32_16x16x32_bf16 v[220:223], v[12:15], v[236:239], v[220:223]\n\t" \
    "v_xor_b32 v252, 0x1000, %[xa]\n\t" \
    "ds_read_b128 v[236:239], v252 offset:0\n\t" \
    "s_waitcnt lgkmcnt(6)\n\t" \
    "v_mfma_f32_16x16x32_bf16 v[220:223], v[32:35], v[240:243], v[220:223]\n\t" \
    "s_waitcnt lgkmcnt(5)\n\t" \
    "v_mfma_f32_16x16x32_bf16 v[220:223], v[36:39], v[224:227], v[220:223]\n\t" \
    "v_mfma_f32_16x16x32_bf16 v[216:219], v[24:27], v[240:243], v[216:219]\n\t" \
    "v_mfma_f32_16x16x32_bf16 v[216:219], v[28:31], v[224:227], v[216:219]\n\t" \
    "v_mfma_f32_16x16x32_bf16 v[212:215], v[16:19], v[240:243], v[212:215]\n\t" \
    "ds_read_b128 v[240:243], v252 offset:1024\n\t" \
    "v_mfma_f32_16x16x32_bf16 v[212:215], v[20:23], v[224:227], v[212:215]\n\t" \
    "ds_read_b128 v[224:227], v252 offset:16384\n\t" \
    "s_waitcnt lgkmcnt(6)\n\t" \
    "v_mfma_f32_16x16x32_bf16 v[200:203], v[16:19], v[228:231], v[200:203]\n\t" \
    "s_waitcnt lgkmcnt(3)\n\t" \
    "v_mfma_f32_16x16x32_bf16 v[200:203], v[20:23], v[232:235], v[200:203]\n\t" \
    "v_mfma_f32_16x16x32_bf16 v[204:207], v[24:27], v[228:231], v[204:207]\n\t" \
    "v_mfma_f32_16x16x32_bf16 v[204:207], v[28:31], v[232:235], v[204:207]\n\t" \
    "v_mfma_f32_16x16x32_bf16 v[208:211], v[32:35], v[228:231], v[208:211]\n\t" \
    "ds_read_b128 v[228:231], v252 offset:17408\n\t" \
    "v_mfma_f32_16x16x32_bf16 v[208:211], v[36:39], v[232:235], v[208:211]\n\t" \
    "v_xor_b32 v252, 0x1800, %[xa]\n\t" \
    "ds_read_b128 v[232:235], v252 offset:16384\n\t" \
    "s_waitcnt lgkmcnt(4)\n\t" \
    "v_mfma_f32_16x16x32_bf16 v[208:211], v[48:51], v[236:239], v[208:211]\n\t" \
    "s_waitcnt lgkmcnt(3)\n\t" \
    "v_mfma_f32_16x16x32_bf16 v[208:211], v[52:55], v[240:243], v[208:211]\n\t" \
    "v_mfma_f32_16x16x32_bf16 v[204:207], v[40:43], v[236:239], v[204:207]\n\t" \
    "v_mfma_f32_16x16x32_bf16 v[204:207], v[44:47], v[240:243], v[204:207]\n\t" \
    "v_mfma_f32_16x16x32_bf16 v[200:203], v[244:247], v[236:239], v[200:203]\n\t" \
    "ds_read_b128 v[236:239], v252 offset:17408\n\t" \
    "v_mfma_f32_16x16x32_bf16 v[200:203], v[248:251], v[240:243], v[200:203]\n\t" \
    "ds_read_b128 v[240:243], v252 offset:0\n\t" \
    "s_waitcnt lgkmcnt(4)\n\t" \
    "v_mfma_f32_16x16x32_bf16 v[212:215], v[244:247], v[224:227], v[212:215]\n\t" \
    "s_waitcnt lgkmcnt(3)\n\t" \
    "v_mfma_f32_16x16x32_bf16 v[212:215], v[248:251], v[228:231], v[212:215]\n\t" \
    "v_mfma_f32_16x16x32_bf16 v[216:219], v[40:43], v[224:227], v[216:219]\n\t" \
    "v_mfma_f32_16x16x32_bf16 v[216:219], v[44:47], v[228:231], v[216:219]\n\t" \
    "v_mfma_f32_16x16x32_bf16 v[220:223], v[48:51], v[224:227], v[220:223]\n\t" \
    "ds_read_b128 v[224:227], v252 offset:1024\n\t" \
    "v_mfma_f32_16x16x32_bf16 v[220:223], v[52:55], v[228:231], v[220:223]\n\t" \
    "v_and_b32 v252, 0x3ff, %[xa]\n\t" \
    "ds_read_b128 v[228:231], v252 offset:43008\n\t" \
    "s_waitcnt lgkmcnt(4)\n\t" \
    "v_mfma_f32_16x16x32_bf16 v[220:223], v[72:75], v[232:235], v[220:223]\n\t" \
    "s_waitcnt lgkmcnt(3)\n\t" \
    "v_mfma_f32_16x16x32_bf16 v[220:223], v[76:79], v[236:239], v[220:223]\n\t" \
    "v_mfma_f32_16x16x32_bf16 v[216:219], v[64:67], v[232:235], v[216:219]\n\t" \
    "v_mfma_f32_16x16x32_bf16 v[216:219], v[68:71], v[236:239], v[216:219]\n\t" \
    "v_mfma_f32_16x16x32_bf16 v[212:215], v[56:59], v[232:235], v[212:215]\n\t" \
    "ds_read_b128 v[232:235], v252 offset:44032\n\t" \
    "v_mfma_f32_16x16x32_bf16 v[212:215], v[60:63], v[236:239], v[212:215]\n\t" \
    "ds_read_b128 v[236:239], v252 offset:47104\n\t" \
    "s_waitcnt lgkmcnt(4)\n\t" \
    "v_mfma_f32_16x16x32_bf16 v[200:203], v[56:59], v[240:243], v[200:203]\n\t" \
    "s_waitcnt lgkmcnt(3)\n\t" \
    "v_mfma_f32_16x16x32_bf16 v[200:203], v[60:63], v[224:227], v[200:203]\n\t" \
    "v_mfma_f32_16x16x32_bf16 v[204:207], v[64:67], v[240:243], v[204:207]\n\t" \
    "v_mfma_f32_16x16x32_bf16 v[204:207], v[68:71], v[224:227], v[204:207]\n\t" \
    "v_mfma_f32_16x16x32_bf16 v[208:211], v[72:75], v[240:243], v[208:211]\n\t" \
    "ds_read_b128 v[240:243], v252 offset:48128\n\t" \
    "v_mfma_f32_16x16x32_bf16 v[208:211], v[76:79], v[224:227], v[208:211]\n\t" \
    "s_waitcnt lgkmcnt(3)\n\t" \
    "v_mfma_f32_16x16x32_bf16 v[208:211], v[192:195], v[228:231], v[208:211]\n\t" \
    "v_add_u32 v252, %[qs], v252\n\t" \
    "s_waitcnt lgkmcnt(2)\n\t" \
    "v_mfma_f32_16x16x32_bf16 v[208:211], v[196:199], v[232:235], v[208:211]\n\t" \
    "v_mfma_f32_16x16x32_bf16 v[204:207], v[184:187], v[228:231], v[204:207]\n\t" \
    "v_mfma_f32_16x16x32_bf16 v[204:207], v[188:191], v[232:235], v[204:207]\n\t" \
    "v_mfma_f32_16x16x32_bf16 v[200:203], v[176:179], v[228:231], v[200:203]\n\t" \
    "v_mfma_f32_16x16x32_bf16 v[200:203], v[180:183], v[232:235], v[200:203]\n\t" \
    "s_waitcnt lgkmcnt(1)\n\t" \
    "v_mfma_f32_16x16x32_bf16 v[212:215], v[176:179], v[236:239], v[212:215]\n\t" \
    "v_cvt_pk_bf16_f32 v208, v208, v209\n\t" \
    "v_cvt_pk_bf16_f32 v209, v210, v211\n\t" \
    "s_waitcnt lgkmcnt(0)\n\t" \
    "v_mfma_f32_16x16x32_bf16 v[212:215], v[180:183], v[240:243], v[212:215]\n\t" \
    "ds_write_b64 v252, v[208:209] offset:2048\n\t" \
    "v_cvt_pk_bf16_f32 v204, v204, v205\n\t" \
    "v_mfma_f32_16x16x32_bf16 v[216:219], v[184:187], v[236:239], v[216:219]\n\t" \
    "v_cvt_pk_bf16_f32 v205, v206, v207\n\t" \
    "ds_write_b64 v252, v[204:205] offset:1024\n\t" \
    "v_mfma_f32_16x16x32_bf16 v[216:219], v[188:191], v[240:243], v[216:219]\n\t" \
    "v_cvt_pk_bf16_f32 v200, v200, v201\n\t" \
    "v_cvt_pk_bf16_f32 v201, v202, v203\n\t" \
    "v_mfma_f32_16x16x32_bf16 v[220:223], v[192:195], v[236:239], v[220:223]\n\t" \
    "ds_write_b64 v252, v[200:201] offset:0\n\t" \
    "v_mfma_f32_16x16x32_bf16 v[220:223], v[196:199], v[240:243], v[220:223]\n\t" \
    "v_cvt_pk_bf16_f32 v212, v212, v213\n\t" \
    "v_cvt_pk_bf16_f32 v213, v214, v215\n\t" \
    "ds_write_b64 v252, v[212:213] offset:6144\n\t" \
    "v_cvt_pk_bf16_f32 v216, v216, v217\n\t" \
    "v_cvt_pk_bf16_f32 v217, v218, v219\n\t" \
    "ds_write_b64 v252, v[216:217] offset:7168\n\t" \
    "s_nop 1\n\t" \
    "v_cvt_pk_bf16_f32 v220, v220, v221\n\t" \
    "v_cvt_pk_bf16_f32 v221, v222, v223\n\t" \
    "ds_write_b64 v252, v[220:221] offset:8192\n\t" \
    "s_waitcnt lgkmcnt(0)\n\t"
#define ASM_CRIT1_3 \
    "s_branch .Lc0%=\n\t" \
    ".Lcr%=:\n\t" \
    "s_cmp_eq_u32 %[n], 0\n\t" \
    "s_cbranch_scc1 .Lcb%=\n\t" \
    "s_sub_u32 %[n], %[n], 1\n\t" \
    "v_max_u32 v243, v208, v209\n\t" \
    "v_max3_u32 v243, v210, v211, v243\n\t" \
    "v_cmp_eq_u32 vcc, -1, v243\n\t" \
    "s_cbranch_vccz .Lcs0_%=\n\t" \
    "buffer_load_dwordx4 v[208:211], %[ab], %[rs], %[s0] offen sc1\n\t" \
    ".Lcs0_%=:\n\t" \
    "v_max_u32 v243, v236, v237\n\t" \
    "v_max3_u32 v243, v238, v239, v243\n\t" \
    "v_cmp_eq_u32 vcc, -1, v243\n\t" \
    "s_cbranch_vccz .Lcs1_%=\n\t" \
    "buffer_load_dwordx4 v[236:239], %[ab], %[rs], %[s1] offen sc1\n\t" \
    ".Lcs1_%=:\n\t" \
    "v_max_u32 v243, v220, v221\n\t" \
    "v_max3_u32 v243, v222, v223, v243\n\t" \
    "v_cmp_eq_u32 vcc, -1, v243\n\t" \
    "s_cbranch_vccz .Lca%=\n\t" \
    "buffer_load_dwordx4 v[220:223], %[ab], %[rs], %[s2] offen sc1\n\t" \
    "s_branch .Lca%=\n\t" \
    ".Lc0%=:\n\t" \
    "buffer_load_dwordx4 v[208:211], %[ab], %[rs], %[s0] offen sc1\n\t" \
    "buffer_load_dwordx4 v[236:239], %[ab], %[rs], %[s1] offen sc1\n\t" \
    "buffer_load_dwordx4 v[220:223], %[ab], %[rs], %[s2] offen sc1\n\t" \
    "v_bfe_i32 v241, %[ab], 7, 1\n\t" \
    "v_and_b32 v241, 0xaaaa, v241\n\t" \
    "v_or_b32 v241, 0x4444, v241\n\t" \
    "v_xor_b32 v212, 0x400, %[ab]\n\t" \
    "v_xor_b32 v216, 0x800, %[ab]\n\t" \
    "v_xor_b32 v242, 0xc00, %[ab]\n\t" \
    "v_xor_b32 v240, 0x1400, %[ab]\n\t" \
    ".Lca%=:\n\t" \
    "s_waitcnt vmcnt(0)\n\t" \
    "ds_write_b128 %[ab], v[208:211] offset:0\n\t" \
    "ds_write_b128 %[ab], v[236:239] offset:4096\n\t" \
    "ds_write_b128 %[ab], v[220:223] offset:8192\n\t" \
    "v_max_u32 v243, v208, v209\n\t" \
    "v_max3_u32 v243, v210, v211, v243\n\t" \
    "v_max3_u32 v243, v236, v237, v243\n\t" \
    "v_max3_u32 v243, v238, v239, v243\n\t" \
    "v_max3_u32 v243, v220, v221, v243\n\t" \
    "v_max3_u32 v243, v222, v223, v243\n\t" \
    "v_cmp_eq_u32 vcc, -1, v243\n\t" \
    "s_cbranch_vccnz .Lcr%=\n\t" \
    ".Lcb%=:\n\t" \
    PRIUP \
    ASM_ST2 \
    "v_smfmac_f32_16x16x64_bf16 v[224:227], v[208:211], v[200:207], v241\n\t" \
    "ds_read_b128 v[200:203], %[wl] offset:4096\n\t" \
    "ds_read_b128 v[204:207], %[wl] offset:5120\n\t" \
    "v_smfmac_f32_16x16x64_bf16 v[244:247], v[208:211], v[0:7], v241\n\t" \
    "v_smfmac_f32_16x16x64_bf16 v[248:251], v[208:211], v[8:15], v241\n\t" \
    "s_waitcnt lgkmcnt(2)\n\t" \
    "s_barrier\n\t" ASM_ST \
    "ds_read_b128 v[212:215], v212 offset:0\n\t" \
    "ds_read_b128 v[216:219], v216 offset:0\n\t" \
    "ds_read_b128 v[220:223], v242 offset:0\n\t" \
    "ds_read_b128 v[208:211], v240 offset:0\n\t" \
    "v_smfmac_f32_16x16x64_bf16 v[244:247], v[236:239], v[80:87], v241\n\t" \
    "v_xor_b32 v240, 0x1800, %[ab]\n\t" \
    "v_smfmac_f32_16x16x64_bf16 v[248:251], v[236:239], v[88:95], v241\n\t" \
    "s_waitcnt lgkmcnt(4)\n\t" \
    "v_smfmac_f32_16x16x64_bf16 v[224:227], v[236:239], v[200:207], v241\n\t" \
    "ds_read_b128 v[200:203], %[wl] offset:6144\n\t" \
    "ds_read_b128 v[204:207], %[wl] offset:7168\n\t" \
    "ds_read_b128 v[236:239], v240 offset:0\n\t" \
    "s_waitcnt lgkmcnt(6)\n\t" \
    "v_smfmac_f32_16x16x64_bf16 v[224:227], v[212:215], v[16:23], v241\n\t" \
    "v_smfmac_f32_16x16x64_bf16 v[244:247], v[212:215], v[24:31], v241\n\t" \
    "v_xor_b32 v240, 0x1c00, %[ab]\n\t" \
    "v_smfmac_f32_16x16x64_bf16 v[248:251], v[212:215], v[32:39], v241\n\t" \
    "ds_read_b128 v[212:215], v240 offset:0\n\t" \
    "s_waitcnt lgkmcnt(6)\n\t" \
    "v_smfmac_f32_16x16x64_bf16 v[224:227], v[216:219], v[228:235], v241\n\t" \
    "ds_read_b128 v[228:231], %[wl] offset:8192\n\t" \
    "ds_read_b128 v[232:235], %[wl] offset:9216\n\t" \
    "v_smfmac_f32_16x16x64_bf16 v[244:247], v[216:219], v[40:47], v241\n\t" \
    "v_and_b32 v240, 0x3ff, %[ab]\n\t" \
    "v_smfmac_f32_16x16x64_bf16 v[248:251], v[216:219], v[48:55], v241\n\t" \
    "ds_read_b128 v[216:219], v240 offset:8192\n\t" \
    "s_waitcnt lgkmcnt(8)\n\t" \
    "v_smfmac_f32_16x16x64_bf16 v[224:227], v[220:223], v[56:63], v241\n\t" \
    "v_smfmac_f32_16x16x64_bf16 v[244:247], v[220:223], v[64:71], v241\n\t" \
    "v_smfmac_f32_16x16x64_bf16 v[248:251], v[220:223], v[72:79], v241\n\t" \
    "ds_read_b128 v[220:223], v240 offset:9216\n\t" \
    "s_waitcnt lgkmcnt(8)\n\t" \
    "v_smfmac_f32_16x16x64_bf16 v[244:247], v[208:211], v[104:111], v241\n\t" \
    "s_waitcnt lgkmcnt(5)\n\t" \
    "v_smfmac_f32_16x16x64_bf16 v[244:247], v[236:239], v[120:127], v241\n\t" \
    "s_waitcnt lgkmcnt(4)\n\t" \
    "v_smfmac_f32_16x16x64_bf16 v[244:247], v[212:215], v[144:151], v241\n\t" \
    "s_waitcnt lgkmcnt(1)\n\t" \
    "v_smfmac_f32_16x16x64_bf16 v[244:247], v[216:219], v[160:167], v241\n\t" \
    "s_waitcnt lgkmcnt(0)\n\t" \
    "v_smfmac_f32_16x16x64_bf16 v[244:247], v[220:223], v[184:191], v241\n\t" \
    "v_smfmac_f32_16x16x64_bf16 v[248:251], v[208:211], v[112:119], v241\n\t" \
    "v_smfmac_f32_16x16x64_bf16 v[248:251], v[236:239], v[128:135], v241\n\t" \
    "v_smfmac_f32_16x16x64_bf16 v[248:251], v[212:215], v[152:159], v241\n\t" \
    "v_permlane32_swap_b32 v244, v246\n\t" \
    "v_permlane32_swap_b32 v245, v247\n\t" \
    "v_smfmac_f32_16x16x64_bf16 v[248:251], v[216:219], v[168:175], v241\n\t" \
    "v_add_f32 v244, v244, v246\n\t" \
    "v_add_f32 v245, v245, v247\n\t" \
    "v_smfmac_f32_16x16x64_bf16 v[248:251], v[220:223], v[192:199], v241\n\t" \
    "v_cvt_pk_bf16_f32 v244, v244, v245\n\t" \
    "ds_write_b16 %[dst], v244 offset:512\n\t" \
    "ds_write_b16_d16_hi %[dst], v244 offset:528\n\t" \
    "v_smfmac_f32_16x16x64_bf16 v[224:227], v[208:211], v[96:103], v241\n\t" \
    "v_smfmac_f32_16x16x64_bf16 v[224:227], v[236:239], v[200:207], v241\n\t" \
    "v_smfmac_f32_16x16x64_bf16 v[224:227], v[212:215], v[136:143], v241\n\t" \
    "v_permlane32_swap_b32 v248, v250\n\t" \
    "v_permlane32_swap_b32 v249, v251\n\t" \
    "v_smfmac_f32_16x16x64_bf16 v[224:227], v[216:219], v[228:235], v241\n\t" \
    "v_add_f32 v248, v248, v250\n\t" \
    "v_add_f32 v249, v249, v251\n\t" \
    "v_smfmac_f32_16x16x64_bf16 v[224:227], v[220:223], v[176:183], v241\n\t" \
    "v_cvt_pk_bf16_f32 v248, v248, v249\n\t" \
    "ds_write_b16 %[dst], v248 offset:1024\n\t" \
    "ds_write_b16_d16_hi %[dst], v248 offset:1040\n\t" \
    "s_nop 4\n\t" \
    "v_permlane32_swap_b32 v224, v226\n\t" \
    "v_permlane32_swap_b32 v225, v227\n\t" \
    "v_add_f32 v224, v224, v226\n\t" \
    "v_add_f32 v225, v225, v227\n\t" \
    "v_cvt_pk_bf16_f32 v224, v224, v225\n\t" \
    "ds_write_b16 %[dst], v224 offset:0\n\t" \
    "ds_write_b16_d16_hi %[dst], v224 offset:16\n\t"
#define ASM_CRIT1_2 \
    "s_branch .Lc0%=\n\t" \
    ".Lcr%=:\n\t" \
    "s_cmp_eq_u32 %[n], 0\n\t" \
    "s_cbranch_scc1 .Lcb%=\n\t" \
    "s_sub_u32 %[n], %[n], 1\n\t" \
    "v_max_u32 v243, v208, v209\n\t" \
    "v_max3_u32 v243, v210, v211, v243\n\t" \
    "v_cmp_eq_u32 vcc, -1, v243\n\t" \
    "s_cbranch_vccz .Lcs0_%=\n\t" \
    "buffer_load_dwordx4 v[208:211], %[ab], %[rs], %[s0] offen sc1\n\t" \
    ".Lcs0_%=:\n\t" \
    "v_max_u32 v243, v236, v237\n\t" \
    "v_max3_u32 v243, v238, v239, v243\n\t" \
    "v_cmp_eq_u32 vcc, -1, v243\n\t" \
    "s_cbranch_vccz .Lca%=\n\t" \
    "buffer_load_dwordx4 v[236:239], %[ab], %[rs], %[s1] offen sc1\n\t" \
    "s_branch .Lca%=\n\t" \
    ".Lc0%=:\n\t" \
    "buffer_load_dwordx4 v[208:211], %[ab], %[rs], %[s0] offen sc1\n\t" \
    "buffer_load_dwordx4 v[236:239], %[ab], %[rs], %[s1] offen sc1\n\t" \
    "v_bfe_i32 v241, %[ab], 7, 1\n\t" \
    "v_and_b32 v241, 0xaaaa, v241\n\t" \
    "v_or_b32 v241, 0x4444, v241\n\t" \
    "v_xor_b32 v212, 0x400, %[ab]\n\t" \
    "v_xor_b32 v216, 0x800, %[ab]\n\t" \
    "v_xor_b32 v242, 0xc00, %[ab]\n\t" \
    "v_xor_b32 v240, 0x1400, %[ab]\n\t" \
    ".Lca%=:\n\t" \
    "s_waitcnt vmcnt(0)\n\t" \
    "ds_write_b128 %[ab], v[208:211] offset:0\n\t" \
    "ds_write_b128 %[ab], v[236:239] offset:4096\n\t" \
    "v_max_u32 v243, v208, v209\n\t" \
    "v_max3_u32 v243, v210, v211, v243\n\t" \
    "v_max3_u32 v243, v236, v237, v243\n\t" \
    "v_max3_u32 v243, v238, v239, v243\n\t" \
    "v_cmp_eq_u32 vcc, -1, v243\n\t" \
    "s_cbranch_vccnz .Lcr%=\n\t" \
    ".Lcb%=:\n\t" \
    PRIUP \
    ASM_ST2 \
    "v_smfmac_f32_16x16x64_bf16 v[224:227], v[208:211], v[200:207], v241\n\t" \
    "ds_read_b128 v[200:203], %[wl] offset:4096\n\t" \
    "ds_read_b128 v[204:207], %[wl] offset:5120\n\t" \
    "v_smfmac_f32_16x16x64_bf16 v[244:247], v[208:211], v[0:7], v241\n\t" \
    "v_smfmac_f32_16x16x64_bf16 v[248:251], v[208:211], v[8:15], v241\n\t" \
    "s_waitcnt lgkmcnt(2)\n\t" \
    "s_barrier\n\t" ASM_ST \
    "ds_read_b128 v[212:215], v212 offset:0\n\t" \
    "ds_read_b128 v[216:219], v216 offset:0\n\t" \
    "ds_read_b128 v[220:223], v242 offset:0\n\t" \
    "ds_read_b128 v[208:211], v240 offset:0\n\t" \
    "v_smfmac_f32_16x16x64_bf16 v[244:247], v[236:239], v[80:87], v241\n\t" \
    "v_xor_b32 v240, 0x1800, %[ab]\n\t" \
    "v_smfmac_f32_16x16x64_bf16 v[248:251], v[236:239], v[88:95], v241\n\t" \
    "s_waitcnt lgkmcnt(4)\n\t" \
    "v_smfmac_f32_16x16x64_bf16 v[224:227], v[236:239], v[200:207], v241\n\t" \
    "ds_read_b128 v[200:203], %[wl] offset:6144\n\t" \
    "ds_read_b128 v[204:207], %[wl] offset:7168\n\t" \
    "ds_read_b128 v[236:239], v240 offset:0\n\t" \
    "s_waitcnt lgkmcnt(6)\n\t" \
    "v_smfmac_f32_16x16x64_bf16 v[224:227], v[212:215], v[16:23], v241\n\t" \
    "v_smfmac_f32_16x16x64_bf16 v[244:247], v[212:215], v[24:31], v241\n\t" \
    "v_xor_b32 v240, 0x1c00, %[ab]\n\t" \
    "v_smfmac_f32_16x16x64_bf16 v[248:251], v[212:215], v[32:39], v241\n\t" \
    "ds_read_b128 v[212:215], v240 offset:0\n\t" \
    "s_waitcnt lgkmcnt(6)\n\t" \
    "v_smfmac_f32_16x16x64_bf16 v[224:227], v[216:219], v[228:235], v241\n\t" \
    "ds_read_b128 v[228:231], %[wl] offset:8192\n\t" \
    "ds_read_b128 v[232:235], %[wl] offset:9216\n\t" \
    "v_smfmac_f32_16x16x64_bf16 v[244:247], v[216:219], v[40:47], v241\n\t" \
    "v_and_b32 v240, 0x3ff, %[ab]\n\t" \
    "v_smfmac_f32_16x16x64_bf16 v[248:251], v[216:219], v[48:55], v241\n\t" \
    "ds_read_b128 v[216:219], v240 offset:8192\n\t" \
    "s_waitcnt lgkmcnt(8)\n\t" \
    "v_smfmac_f32_16x16x64_bf16 v[224:227], v[220:223], v[56:63], v241\n\t" \
    "v_smfmac_f32_16x16x64_bf16 v[244:247], v[220:223], v[64:71], v241\n\t" \
    "v_smfmac_f32_16x16x64_bf16 v[248:251], v[220:223], v[72:79], v241\n\t" \
    "ds_read_b128 v[220:223], v240 offset:9216\n\t" \
    "s_waitcnt lgkmcnt(8)\n\t" \
    "v_smfmac_f32_16x16x64_bf16 v[244:247], v[208:211], v[104:111], v241\n\t" \
    "s_waitcnt lgkmcnt(5)\n\t" \
    "v_smfmac_f32_16x16x64_bf16 v[244:247], v[236:239], v[120:127], v241\n\t" \
    "s_waitcnt lgkmcnt(4)\n\t" \
    "v_smfmac_f32_16x16x64_bf16 v[244:247], v[212:215], v[144:151], v241\n\t" \
    "s_waitcnt lgkmcnt(1)\n\t" \
    "v_smfmac_f32_16x16x64_bf16 v[244:247], v[216:219], v[160:167], v241\n\t" \
    "s_waitcnt lgkmcnt(0)\n\t" \
    "v_smfmac_f32_16x16x64_bf16 v[244:247], v[220:223], v[184:191], v241\n\t" \
    "v_smfmac_f32_16x16x64_bf16 v[248:251], v[208:211], v[112:119], v241\n\t" \
    "v_smfmac_f32_16x16x64_bf16 v[248:251], v[236:239], v[128:135], v241\n\t" \
    "v_smfmac_f32_16x16x64_bf16 v[248:251], v[212:215], v[152:159], v241\n\t" \
    "v_permlane32_swap_b32 v244, v246\n\t" \
    "v_permlane32_swap_b32 v245, v247\n\t" \
    "v_smfmac_f32_16x16x64_bf16 v[248:251], v[216:219], v[168:175], v241\n\t" \
    "v_add_f32 v244, v244, v246\n\t" \
    "v_add_f32 v245, v245, v247\n\t" \
    "v_smfmac_f32_16x16x64_bf16 v[248:251], v[220:223], v[192:199], v241\n\t" \
    "v_cvt_pk_bf16_f32 v244, v244, v245\n\t" \
    "ds_write_b16 %[dst], v244 offset:512\n\t" \
    "ds_write_b16_d16_hi %[dst], v244 offset:528\n\t" \
    "v_smfmac_f32_16x16x64_bf16 v[224:227], v[208:211], v[96:103], v241\n\t" \
    "v_smfmac_f32_16x16x64_bf16 v[224:227], v[236:239], v[200:207], v241\n\t" \
    "v_smfmac_f32_16x16x64_bf16 v[224:227], v[212:215], v[136:143], v241\n\t" \
    "v_permlane32_swap_b32 v248, v250\n\t" \
    "v_permlane32_swap_b32 v249, v251\n\t" \
    "v_smfmac_f32_16x16x64_bf16 v[224:227], v[216:219], v[228:235], v241\n\t" \
    "v_add_f32 v248, v248, v250\n\t" \
    "v_add_f32 v249, v249, v251\n\t" \
    "v_smfmac_f32_16x16x64_bf16 v[224:227], v[220:223], v[176:183], v241\n\t" \
    "v_cvt_pk_bf16_f32 v248, v248, v249\n\t" \
    "ds_write_b16 %[dst], v248 offset:1024\n\t" \
    "ds_write_b16_d16_hi %[dst], v248 offset:1040\n\t" \
    "s_nop 4\n\t" \
    "v_permlane32_swap_b32 v224, v226\n\t" \
    "v_permlane32_swap_b32 v225, v227\n\t" \
    "v_add_f32 v224, v224, v226\n\t" \
    "v_add_f32 v225, v225, v227\n\t" \
    "v_cvt_pk_bf16_f32 v224, v224, v225\n\t" \
    "ds_write_b16 %[dst], v224 offset:0\n\t" \
    "ds_write_b16_d16_hi %[dst], v224 offset:16\n\t"
#define ASM_CRIT1_N \
    "v_bfe_i32 v241, %[ab], 7, 1\n\t" \
    "v_and_b32 v241, 0xaaaa, v241\n\t" \
    "v_or_b32 v241, 0x4444, v241\n\t" \
    "v_xor_b32 v212, 0x400, %[ab]\n\t" \
    "v_xor_b32 v216, 0x800, %[ab]\n\t" \
    "v_xor_b32 v242, 0xc00, %[ab]\n\t" \
    "v_xor_b32 v240, 0x1400, %[ab]\n\t" \
    "v_xor_b32 v236, 0x1000, %[ab]\n\t" \
    "s_waitcnt lgkmcnt(0)\n\t" \
    "s_barrier\n\t" ASM_ST \
    "ds_read_b128 v[208:211], %[ab] offset:0\n\t" \
    "ds_read_b128 v[212:215], v212 offset:0\n\t" \
    "ds_read_b128 v[216:219], v216 offset:0\n\t" \
    "ds_read_b128 v[220:223], v242 offset:0\n\t" \
    "ds_read_b128 v[236:239], v236 offset:0\n\t" \
    "s_waitcnt lgkmcnt(4)\n\t" \
    "v_smfmac_f32_16x16x64_bf16 v[224:227], v[208:211], v[200:207], v241\n\t" \
    "ds_read_b128 v[200:203], %[wl] offset:4096\n\t" \
    "ds_read_b128 v[204:207], %[wl] offset:5120\n\t" \
    "v_smfmac_f32_16x16x64_bf16 v[244:247], v[208:211], v[0:7], v241\n\t" \
    "v_smfmac_f32_16x16x64_bf16 v[248:251], v[208:211], v[8:15], v241\n\t" \
    "ds_read_b128 v[208:211], v240 offset:0\n\t" \
    "s_waitcnt lgkmcnt(6)\n\t" \
    "v_smfmac_f32_16x16x64_bf16 v[224:227], v[212:215], v[16:23], v241\n\t" \
    "v_smfmac_f32_16x16x64_bf16 v[244:247], v[212:215], v[24:31], v241\n\t" \
    "v_xor_b32 v240, 0x1800, %[ab]\n\t" \
    "v_smfmac_f32_16x16x64_bf16 v[248:251], v[212:215], v[32:39], v241\n\t" \
    "ds_read_b128 v[212:215], v240 offset:0\n\t" \
    "s_waitcnt lgkmcnt(6)\n\t" \
    "v_smfmac_f32_16x16x64_bf16 v[224:227], v[216:219], v[228:235], v241\n\t" \
    "ds_read_b128 v[228:231], %[wl] offset:6144\n\t" \
    "ds_read_b128 v[232:235], %[wl] offset:7168\n\t" \
    "v_smfmac_f32_16x16x64_bf16 v[244:247], v[216:219], v[40:47], v241\n\t" \
    "v_xor_b32 v240, 0x1c00, %[ab]\n\t" \
    "v_smfmac_f32_16x16x64_bf16 v[248:251], v[216:219], v[48:55], v241\n\t" \
    "ds_read_b128 v[216:219], v240 offset:0\n\t" \
    "s_waitcnt lgkmcnt(8)\n\t" \
    "v_smfmac_f32_16x16x64_bf16 v[224:227], v[220:223], v[56:63], v241\n\t" \
    "v_smfmac_f32_16x16x64_bf16 v[244:247], v[220:223], v[64:71], v241\n\t" \
    "v_and_b32 v240, 0x3ff, %[ab]\n\t" \
    "v_smfmac_f32_16x16x64_bf16 v[248:251], v[220:223], v[72:79], v241\n\t" \
    "ds_read_b128 v[220:223], v240 offset:8192\n\t" \
    "s_waitcnt lgkmcnt(6)\n\t" \
    "v_smfmac_f32_16x16x64_bf16 v[224:227], v[236:239], v[200:207], v241\n\t" \
    "ds_read_b128 v[200:203], %[wl] offset:8192\n\t" \
    "ds_read_b128 v[204:207], %[wl] offset:9216\n\t" \
    "v_smfmac_f32_16x16x64_bf16 v[244:247], v[236:239], v[80:87], v241\n\t" \
    "v_smfmac_f32_16x16x64_bf16 v[248:251], v[236:239], v[88:95], v241\n\t" \
    "ds_read_b128 v[236:239], v240 offset:9216\n\t" \
    "s_waitcnt lgkmcnt(8)\n\t" \
    "v_smfmac_f32_16x16x64_bf16 v[244:247], v[208:211], v[104:111], v241\n\t" \
    "s_waitcnt lgkmcnt(7)\n\t" \
    "v_smfmac_f32_16x16x64_bf16 v[244:247], v[212:215], v[120:127], v241\n\t" \
    "s_waitcnt lgkmcnt(4)\n\t" \
    "v_smfmac_f32_16x16x64_bf16 v[244:247], v[216:219], v[144:151], v241\n\t" \
    "s_waitcnt lgkmcnt(3)\n\t" \
    "v_smfmac_f32_16x16x64_bf16 v[244:247], v[220:223], v[160:167], v241\n\t" \
    "s_waitcnt lgkmcnt(0)\n\t" \
    "v_smfmac_f32_16x16x64_bf16 v[244:247], v[236:239], v[184:191], v241\n\t" \
    "v_smfmac_f32_16x16x64_bf16 v[248:251], v[208:211], v[112:119], v241\n\t" \
    "v_smfmac_f32_16x16x64_bf16 v[248:251], v[212:215], v[128:135], v241\n\t" \
    "v_smfmac_f32_16x16x64_bf16 v[248:251], v[216:219], v[152:159], v241\n\t" \
    "v_permlane32_swap_b32 v244, v246\n\t" \
    "v_permlane32_swap_b32 v245, v247\n\t" \
    "v_smfmac_f32_16x16x64_bf16 v[248:251], v[220:223], v[168:175], v241\n\t" \
    "v_add_f32 v244, v244, v246\n\t" \
    "v_add_f32 v245, v245, v247\n\t" \
    "v_smfmac_f32_16x16x64_bf16 v[248:251], v[236:239], v[192:199], v241\n\t" \
    "v_cvt_pk_bf16_f32 v244, v244, v245\n\t" \
    "ds_write_b16 %[dst], v244 offset:512\n\t" \
    "ds_write_b16_d16_hi %[dst], v244 offset:528\n\t" \
    "v_smfmac_f32_16x16x64_bf16 v[224:227], v[208:211], v[96:103], v241\n\t" \
    "v_smfmac_f32_16x16x64_bf16 v[224:227], v[212:215], v[228:235], v241\n\t" \
    "v_smfmac_f32_16x16x64_bf16 v[224:227], v[216:219], v[136:143], v241\n\t" \
    "v_permlane32_swap_b32 v248, v250\n\t" \
    "v_permlane32_swap_b32 v249, v251\n\t" \
    "v_smfmac_f32_16x16x64_bf16 v[224:227], v[220:223], v[200:207], v241\n\t" \
    "v_add_f32 v248, v248, v250\n\t" \
    "v_add_f32 v249, v249, v251\n\t" \
    "v_smfmac_f32_16x16x64_bf16 v[224:227], v[236:239], v[176:183], v241\n\t" \
    "v_cvt_pk_bf16_f32 v248, v248, v249\n\t" \
    "ds_write_b16 %[dst], v248 offset:1024\n\t" \
    "ds_write_b16_d16_hi %[dst], v248 offset:1040\n\t" \
    "s_nop 4\n\t" \
    "v_permlane32_swap_b32 v224, v226\n\t" \
    "v_permlane32_swap_b32 v225, v227\n\t" \
    "v_add_f32 v224, v224, v226\n\t" \
    "v_add_f32 v225, v225, v227\n\t" \
    "v_cvt_pk_bf16_f32 v224, v224, v225\n\t" \
    "ds_write_b16 %[dst], v224 offset:0\n\t" \
    "ds_write_b16_d16_hi %[dst], v224 offset:16\n\t"
#define ASM_CRIT1_V3 \
    "s_branch .Lc0%=\n\t" \
    ".Lcr%=:\n\t" \
    "s_cmp_eq_u32 %[n], 0\n\t" \
    "s_cbranch_scc1 .Lcb%=\n\t" \
    "s_sub_u32 %[n], %[n], 1\n\t" \
    "v_max_u32 v243, v208, v209\n\t" \
    "v_max3_u32 v243, v210, v211, v243\n\t" \
    "v_cmp_eq_u32 vcc, -1, v243\n\t" \
    "s_cbranch_vccz .Lcs0_%=\n\t" \
    "buffer_load_dwordx4 v[208:211], %[ab], %[rs], %[s0] offen sc1\n\t" \
    ".Lcs0_%=:\n\t" \
    "v_max_u32 v243, v236, v237\n\t" \
    "v_max3_u32 v243, v238, v239, v243\n\t" \
    "v_cmp_eq_u32 vcc, -1, v243\n\t" \
    "s_cbranch_vccz .Lcs1_%=\n\t" \
    "buffer_load_dwordx4 v[236:239], %[ab], %[rs], %[s1] offen sc1\n\t" \
    ".Lcs1_%=:\n\t" \
    "v_max_u32 v243, v220, v221\n\t" \
    "v_max3_u32 v243, v222, v223, v243\n\t" \
    "v_cmp_eq_u32 vcc, -1, v243\n\t" \
    "s_cbranch_vccz .Lca%=\n\t" \
    "buffer_load_dwordx4 v[220:223], %[ab], %[rs], %[s2] offen sc1\n\t" \
    "s_branch .Lca%=\n\t" \
    ".Lc0%=:\n\t" \
    "buffer_load_dwordx4 v[208:211], %[ab], %[rs], %[s0] offen sc1\n\t" \
    "buffer_load_dwordx4 v[236:239], %[ab], %[rs], %[s1] offen sc1\n\t" \
    "buffer_load_dwordx4 v[220:223], %[ab], %[rs], %[s2] offen sc1\n\t" \
    "v_bfe_i32 v241, %[ab], 7, 1\n\t" \
    "v_and_b32 v241, 0xaaaa, v241\n\t" \
    "v_or_b32 v241, 0x4444, v241\n\t" \
    "v_xor_b32 v212, 0x400, %[ab]\n\t" \
    "v_xor_b32 v216, 0x800, %[ab]\n\t" \
    "v_xor_b32 v242, 0xc00, %[ab]\n\t" \
    "v_xor_b32 v240, 0x1400, %[ab]\n\t" \
    ".Lca%=:\n\t" \
    "s_waitcnt vmcnt(0)\n\t" \
    "ds_write_b128 %[ab], v[208:211] offset:0\n\t" \
    "ds_write_b128 %[ab], v[236:239] offset:4096\n\t" \
    "ds_write_b128 %[ab], v[220:223] offset:8192\n\t" \
    "v_max_u32 v243, v208, v209\n\t" \
    "v_max3_u32 v243, v210, v211, v243\n\t" \
    "v_max3_u32 v243, v236, v237, v243\n\t" \
    "v_max3_u32 v243, v238, v239, v243\n\t" \
    "v_max3_u32 v243, v220, v221, v243\n\t" \
    "v_max3_u32 v243, v222, v223, v243\n\t" \
    "v_cmp_eq_u32 vcc, -1, v243\n\t" \
    "s_cbranch_vccnz .Lcr%=\n\t" \
    ".Lcb%=:\n\t" \
    PRIUP \
    ASM_ST2 \
    "v_smfmac_f32_16x16x64_bf16 v[224:227], v[208:211], v[200:207], v241\n\t" \
    "ds_read_b128 v[200:203], %[wl] offset:4096\n\t" \
    "ds_read_b128 v[204:207], %[wl] offset:5120\n\t" \
    "v_smfmac_f32_16x16x64_bf16 v[244:247], v[208:211], v[0:7], v241\n\t" \
    "v_smfmac_f32_16x16x64_bf16 v[248:251], v[208:211], v[8:15], v241\n\t" \
    "s_waitcnt lgkmcnt(2)\n\t" \
    "buffer_load_dword %[fv], %[fv], %[rs], %[fs] offen sc1\n\t" \
    "s_barrier\n\t" ASM_ST \
    "ds_read_b128 v[212:215], v212 offset:0\n\t" \
    "ds_read_b128 v[216:219], v216 offset:0\n\t" \
    "ds_read_b128 v[220:223], v242 offset:0\n\t" \
    "ds_read_b128 v[208:211], v240 offset:0\n\t" \
    "v_smfmac_f32_16x16x64_bf16 v[244:247], v[236:239], v[80:87], v241\n\t" \
    "v_xor_b32 v240, 0x1800, %[ab]\n\t" \
    "v_smfmac_f32_16x16x64_bf16 v[248:251], v[236:239], v[88:95], v241\n\t" \
    "s_waitcnt lgkmcnt(4)\n\t" \
    "v_smfmac_f32_16x16x64_bf16 v[224:227], v[236:239], v[200:207], v241\n\t" \
    "ds_read_b128 v[200:203], %[wl] offset:6144\n\t" \
    "ds_read_b128 v[204:207], %[wl] offset:7168\n\t" \
    "ds_read_b128 v[236:239], v240 offset:0\n\t" \
    "s_waitcnt lgkmcnt(6)\n\t" \
    "v_smfmac_f32_16x16x64_bf16 v[224:227], v[212:215], v[16:23], v241\n\t" \
    "v_smfmac_f32_16x16x64_bf16 v[244:247], v[212:215], v[24:31], v241\n\t" \
    "v_xor_b32 v240, 0x1c00, %[ab]\n\t" \
    "v_smfmac_f32_16x16x64_bf16 v[248:251], v[212:215], v[32:39], v241\n\t" \
    "ds_read_b128 v[212:215], v240 offset:0\n\t" \
    "s_waitcnt lgkmcnt(6)\n\t" \
    "v_smfmac_f32_16x16x64_bf16 v[224:227], v[216:219], v[228:235], v241\n\t" \
    "ds_read_b128 v[228:231], %[wl] offset:8192\n\t" \
    "ds_read_b128 v[232:235], %[wl] offset:9216\n\t" \
    "v_smfmac_f32_16x16x64_bf16 v[244:247], v[216:219], v[40:47], v241\n\t" \
    "v_and_b32 v240, 0x3ff, %[ab]\n\t" \
    "v_smfmac_f32_16x16x64_bf16 v[248:251], v[216:219], v[48:55], v241\n\t" \
    "ds_read_b128 v[216:219], v240 offset:8192\n\t" \
    "s_waitcnt lgkmcnt(8)\n\t" \
    "v_smfmac_f32_16x16x64_bf16 v[224:227], v[220:223], v[56:63], v241\n\t" \
    "v_smfmac_f32_16x16x64_bf16 v[244:247], v[220:223], v[64:71], v241\n\t" \
    "v_smfmac_f32_16x16x64_bf16 v[248:251], v[220:223], v[72:79], v241\n\t" \
    "ds_read_b128 v[220:223], v240 offset:9216\n\t" \
    "s_waitcnt lgkmcnt(8)\n\t" \
    "v_smfmac_f32_16x16x64_bf16 v[244:247], v[208:211], v[104:111], v241\n\t" \
    "s_waitcnt lgkmcnt(5)\n\t" \
    "v_smfmac_f32_16x16x64_bf16 v[244:247], v[236:239], v[120:127], v241\n\t" \
    "s_waitcnt lgkmcnt(4)\n\t" \
    "v_smfmac_f32_16x16x64_bf16 v[244:247], v[212:215], v[144:151], v241\n\t" \
    "s_waitcnt lgkmcnt(1)\n\t" \
    "v_smfmac_f32_16x16x64_bf16 v[244:247], v[216:219], v[160:167], v241\n\t" \
    "s_waitcnt lgkmcnt(0)\n\t" \
    "v_smfmac_f32_16x16x64_bf16 v[244:247], v[220:223], v[184:191], v241\n\t" \
    "v_smfmac_f32_16x16x64_bf16 v[248:251], v[208:211], v[112:119], v241\n\t" \
    "v_smfmac_f32_16x16x64_bf16 v[248:251], v[236:239], v[128:135], v241\n\t" \
    "v_smfmac_f32_16x16x64_bf16 v[248:251], v[212:215], v[152:159], v241\n\t" \
    "v_permlane32_swap_b32 v244, v246\n\t" \
    "v_permlane32_swap_b32 v245, v247\n\t" \
    "v_smfmac_f32_16x16x64_bf16 v[248:251], v[216:219], v[168:175], v241\n\t" \
    "v_add_f32 v244, v244, v246\n\t" \
    "v_add_f32 v245, v245, v247\n\t" \
    "v_smfmac_f32_16x16x64_bf16 v[248:251], v[220:223], v[192:199], v241\n\t" \
    "v_cvt_pk_bf16_f32 v244, v244, v245\n\t" \
    "ds_write_b16 %[dst], v244 offset:512\n\t" \
    "ds_write_b16_d16_hi %[dst], v244 offset:528\n\t" \
    "v_smfmac_f32_16x16x64_bf16 v[224:227], v[208:211], v[96:103], v241\n\t" \
    "v_smfmac_f32_16x16x64_bf16 v[224:227], v[236:239], v[200:207], v241\n\t" \
    "v_smfmac_f32_16x16x64_bf16 v[224:227], v[212:215], v[136:143], v241\n\t" \
    "v_permlane32_swap_b32 v248, v250\n\t" \
    "v_permlane32_swap_b32 v249, v251\n\t" \
    "v_smfmac_f32_16x16x64_bf16 v[224:227], v[216:219], v[228:235], v241\n\t" \
    "v_add_f32 v248, v248, v250\n\t" \
    "v_add_f32 v249, v249, v251\n\t" \
    "v_smfmac_f32_16x16x64_bf16 v[224:227], v[220:223], v[176:183], v241\n\t" \
    "v_cvt_pk_bf16_f32 v248, v248, v249\n\t" \
    "ds_write_b16 %[dst], v248 offset:1024\n\t" \
    "ds_write_b16_d16_hi %[dst], v248 offset:1040\n\t" \
    "s_nop 4\n\t" \
    "v_permlane32_swap_b32 v224, v226\n\t" \
    "v_permlane32_swap_b32 v225, v227\n\t" \
    "v_add_f32 v224, v224, v226\n\t" \
    "v_add_f32 v225, v225, v227\n\t" \
    "v_cvt_pk_bf16_f32 v224, v224, v225\n\t" \
    "ds_write_b16 %[dst], v224 offset:0\n\t" \
    "ds_write_b16_d16_hi %[dst], v224 offset:16\n\t"
#define ASM_CRIT1_V2 \
    "s_branch .Lc0%=\n\t" \
    ".Lcr%=:\n\t" \
    "s_cmp_eq_u32 %[n], 0\n\t" \
    "s_cbranch_scc1 .Lcb%=\n\t" \
    "s_sub_u32 %[n], %[n], 1\n\t" \
    "v_max_u32 v243, v208, v209\n\t" \
    "v_max3_u32 v243, v210, v211, v243\n\t" \
    "v_cmp_eq_u32 vcc, -1, v243\n\t" \
    "s_cbranch_vccz .Lcs0_%=\n\t" \
    "buffer_load_dwordx4 v[208:211], %[ab], %[rs], %[s0] offen sc1\n\t" \
    ".Lcs0_%=:\n\t" \
    "v_max_u32 v243, v236, v237\n\t" \
    "v_max3_u32 v243, v238, v239, v243\n\t" \
    "v_cmp_eq_u32 vcc, -1, v243\n\t" \
    "s_cbranch_vccz .Lca%=\n\t" \
    "buffer_load_dwordx4 v[236:239], %[ab], %[rs], %[s1] offen sc1\n\t" \
    "s_branch .Lca%=\n\t" \
    ".Lc0%=:\n\t" \
    "buffer_load_dwordx4 v[208:211], %[ab], %[rs], %[s0] offen sc1\n\t" \
    "buffer_load_dwordx4 v[236:239], %[ab], %[rs], %[s1] offen sc1\n\t" \
    "v_bfe_i32 v241, %[ab], 7, 1\n\t" \
    "v_and_b32 v241, 0xaaaa, v241\n\t" \
    "v_or_b32 v241, 0x4444, v241\n\t" \
    "v_xor_b32 v212, 0x400, %[ab]\n\t" \
    "v_xor_b32 v216, 0x800, %[ab]\n\t" \
    "v_xor_b32 v242, 0xc00, %[ab]\n\t" \
    "v_xor_b32 v240, 0x1400, %[ab]\n\t" \
    ".Lca%=:\n\t" \
    "s_waitcnt vmcnt(0)\n\t" \
    "ds_write_b128 %[ab], v[208:211] offset:0\n\t" \
    "ds_write_b128 %[ab], v[236:239] offset:4096\n\t" \
    "v_max_u32 v243, v208, v209\n\t" \
    "v_max3_u32 v243, v210, v211, v243\n\t" \
    "v_max3_u32 v243, v236, v237, v243\n\t" \
    "v_max3_u32 v243, v238, v239, v243\n\t" \
    "v_cmp_eq_u32 vcc, -1, v243\n\t" \
    "s_cbranch_vccnz .Lcr%=\n\t" \
    ".Lcb%=:\n\t" \
    PRIUP \
    ASM_ST2 \
    "v_smfmac_f32_16x16x64_bf16 v[224:227], v[208:211], v[200:207], v241\n\t" \
    "ds_read_b128 v[200:203], %[wl] offset:4096\n\t" \
    "ds_read_b128 v[204:207], %[wl] offset:5120\n\t" \
    "v_smfmac_f32_16x16x64_bf16 v[244:247], v[208:211], v[0:7], v241\n\t" \
    "v_smfmac_f32_16x16x64_bf16 v[248:251], v[208:211], v[8:15], v241\n\t" \
    "s_waitcnt lgkmcnt(2)\n\t" \
    "buffer_load_dword %[fv], %[fv], %[rs], %[fs] offen sc1\n\t" \
    "s_barrier\n\t" ASM_ST \
    "ds_read_b128 v[212:215], v212 offset:0\n\t" \
    "ds_read_b128 v[216:219], v216 offset:0\n\t" \
    "ds_read_b128 v[220:223], v242 offset:0\n\t" \
    "ds_read_b128 v[208:211], v240 offset:0\n\t" \
    "v_smfmac_f32_16x16x64_bf16 v[244:247], v[236:239], v[80:87], v241\n\t" \
    "v_xor_b32 v240, 0x1800, %[ab]\n\t" \
    "v_smfmac_f32_16x16x64_bf16 v[248:251], v[236:239], v[88:95], v241\n\t" \
    "s_waitcnt lgkmcnt(4)\n\t" \
    "v_smfmac_f32_16x16x64_bf16 v[224:227], v[236:239], v[200:207], v241\n\t" \
    "ds_read_b128 v[200:203], %[wl] offset:6144\n\t" \
    "ds_read_b128 v[204:207], %[wl] offset:7168\n\t" \
    "ds_read_b128 v[236:239], v240 offset:0\n\t" \
    "s_waitcnt lgkmcnt(6)\n\t" \
    "v_smfmac_f32_16x16x64_bf16 v[224:227], v[212:215], v[16:23], v241\n\t" \
    "v_smfmac_f32_16x16x64_bf16 v[244:247], v[212:215], v[24:31], v241\n\t" \
    "v_xor_b32 v240, 0x1c00, %[ab]\n\t" \
    "v_smfmac_f32_16x16x64_bf16 v[248:251], v[212:215], v[32:39], v241\n\t" \
    "ds_read_b128 v[212:215], v240 offset:0\n\t" \
    "s_waitcnt lgkmcnt(6)\n\t" \
    "v_smfmac_f32_16x16x64_bf16 v[224:227], v[216:219], v[228:235], v241\n\t" \
    "ds_read_b128 v[228:231], %[wl] offset:8192\n\t" \
    "ds_read_b128 v[232:235], %[wl] offset:9216\n\t" \
    "v_smfmac_f32_16x16x64_bf16 v[244:247], v[216:219], v[40:47], v241\n\t" \
    "v_and_b32 v240, 0x3ff, %[ab]\n\t" \
    "v_smfmac_f32_16x16x64_bf16 v[248:251], v[216:219], v[48:55], v241\n\t" \
    "ds_read_b128 v[216:219], v240 offset:8192\n\t" \
    "s_waitcnt lgkmcnt(8)\n\t" \
    "v_smfmac_f32_16x16x64_bf16 v[224:227], v[220:223], v[56:63], v241\n\t" \
    "v_smfmac_f32_16x16x64_bf16 v[244:247], v[220:223], v[64:71], v241\n\t" \
    "v_smfmac_f32_16x16x64_bf16 v[248:251], v[220:223], v[72:79], v241\n\t" \
    "ds_read_b128 v[220:223], v240 offset:9216\n\t" \
    "s_waitcnt lgkmcnt(8)\n\t" \
    "v_smfmac_f32_16x16x64_bf16 v[244:247], v[208:211], v[104:111], v241\n\t" \
    "s_waitcnt lgkmcnt(5)\n\t" \
    "v_smfmac_f32_16x16x64_bf16 v[244:247], v[236:239], v[120:127], v241\n\t" \
    "s_waitcnt lgkmcnt(4)\n\t" \
    "v_smfmac_f32_16x16x64_bf16 v[244:247], v[212:215], v[144:151], v241\n\t" \
    "s_waitcnt lgkmcnt(1)\n\t" \
    "v_smfmac_f32_16x16x64_bf16 v[244:247], v[216:219], v[160:167], v241\n\t" \
    "s_waitcnt lgkmcnt(0)\n\t" \
    "v_smfmac_f32_16x16x64_bf16 v[244:247], v[220:223], v[184:191], v241\n\t" \
    "v_smfmac_f32_16x16x64_bf16 v[248:251], v[208:211], v[112:119], v241\n\t" \
    "v_smfmac_f32_16x16x64_bf16 v[248:251], v[236:239], v[128:135], v241\n\t" \
    "v_smfmac_f32_16x16x64_bf16 v[248:251], v[212:215], v[152:159], v241\n\t" \
    "v_permlane32_swap_b32 v244, v246\n\t" \
    "v_permlane32_swap_b32 v245, v247\n\t" \
    "v_smfmac_f32_16x16x64_bf16 v[248:251], v[216:219], v[168:175], v241\n\t" \
    "v_add_f32 v244, v244, v246\n\t" \
    "v_add_f32 v245, v245, v247\n\t" \
    "v_smfmac_f32_16x16x64_bf16 v[248:251], v[220:223], v[192:199], v241\n\t" \
    "v_cvt_pk_bf16_f32 v244, v244, v245\n\t" \
    "ds_write_b16 %[dst], v244 offset:512\n\t" \
    "ds_write_b16_d16_hi %[dst], v244 offset:528\n\t" \
    "v_smfmac_f32_16x16x64_bf16 v[224:227], v[208:211], v[96:103], v241\n\t" \
    "v_smfmac_f32_16x16x64_bf16 v[224:227], v[236:239], v[200:207], v241\n\t" \
    "v_smfmac_f32_16x16x64_bf16 v[224:227], v[212:215], v[136:143], v241\n\t" \
    "v_permlane32_swap_b32 v248, v250\n\t" \
    "v_permlane32_swap_b32 v249, v251\n\t" \
    "v_smfmac_f32_16x16x64_bf16 v[224:227], v[216:219], v[228:235], v241\n\t" \
    "v_add_f32 v248, v248, v250\n\t" \
    "v_add_f32 v249, v249, v251\n\t" \
    "v_smfmac_f32_16x16x64_bf16 v[224:227], v[220:223], v[176:183], v241\n\t" \
    "v_cvt_pk_bf16_f32 v248, v248, v249\n\t" \
    "ds_write_b16 %[dst], v248 offset:1024\n\t" \
    "ds_write_b16_d16_hi %[dst], v248 offset:1040\n\t" \
    "s_nop 4\n\t" \
    "v_permlane32_swap_b32 v224, v226\n\t" \
    "v_permlane32_swap_b32 v225, v227\n\t" \
    "v_add_f32 v224, v224, v226\n\t" \
    "v_add_f32 v225, v225, v227\n\t" \
    "v_cvt_pk_bf16_f32 v224, v224, v225\n\t" \
    "ds_write_b16 %[dst], v224 offset:0\n\t" \
    "ds_write_b16_d16_hi %[dst], v224 offset:16\n\t"
#define ASM_VOX \
    "s_waitcnt lgkmcnt(0)\n\t" \
    "ds_read_b128 v[208:211], %[va] offset:2048\n\t" \
    "ds_read_b128 v[212:215], %[va] offset:3072\n\t" \
    "ds_read_b128 v[216:219], %[va] offset:4096\n\t" \
    "ds_read_b128 v[220:223], %[va] offset:5120\n\t" \
    "ds_read_b128 v[200:203], %[va] offset:0\n\t" \
    "ds_read_b128 v[204:207], %[va] offset:1024\n\t" \
    "ds_read_b128 v[236:239], %[wl] offset:0\n\t" \
    "ds_read_b128 v[240:243], %[wl] offset:1024\n\t" \
    "s_waitcnt lgkmcnt(7)\n\t" \
    "v_mfma_f32_16x16x32_bf16 v[244:247], v[208:211], v[0:3], 0\n\t" \
    "s_waitcnt lgkmcnt(6)\n\t" \
    "v_mfma_f32_16x16x32_bf16 v[244:247], v[212:215], v[4:7], v[244:247]\n\t" \
    "s_waitcnt lgkmcnt(5)\n\t" \
    "v_mfma_f32_16x16x32_bf16 v[244:247], v[216:219], v[8:11], v[244:247]\n\t" \
    "s_waitcnt lgkmcnt(4)\n\t" \
    "v_mfma_f32_16x16x32_bf16 v[244:247], v[220:223], v[12:15], v[244:247]\n\t" \
    "s_waitcnt lgkmcnt(1)\n\t" \
    "v_mfma_f32_16x16x32_bf16 v[244:247], v[200:203], v[236:239], v[244:247]\n\t" \
    "ds_read_b128 v[236:239], %[wl] offset:2048\n\t" \
    "s_waitcnt lgkmcnt(1)\n\t" \
    "v_mfma_f32_16x16x32_bf16 v[244:247], v[204:207], v[240:243], v[244:247]\n\t" \
    "ds_read_b128 v[240:243], %[wl] offset:3072\n\t" \
    "v_mfma_f32_16x16x32_bf16 v[248:251], v[208:211], v[40:43], 0\n\t" \
    "v_mfma_f32_16x16x32_bf16 v[248:251], v[212:215], v[44:47], v[248:251]\n\t" \
    "v_mfma_f32_16x16x32_bf16 v[248:251], v[216:219], v[48:51], v[248:251]\n\t" \
    "v_cvt_pk_bf16_f32 v224, v244, v245\n\t" \
    "v_mfma_f32_16x16x32_bf16 v[248:251], v[220:223], v[52:55], v[248:251]\n\t" \
    "v_cvt_pk_bf16_f32 v225, v246, v247\n\t" \
    "s_waitcnt lgkmcnt(1)\n\t" \
    "v_mfma_f32_16x16x32_bf16 v[248:251], v[200:203], v[236:239], v[248:251]\n\t" \
    "ds_read_b128 v[236:239], %[wl] offset:4096\n\t" \
    "s_waitcnt lgkmcnt(1)\n\t" \
    "v_mfma_f32_16x16x32_bf16 v[248:251], v[204:207], v[240:243], v[248:251]\n\t" \
    "ds_read_b128 v[240:243], %[wl] offset:5120\n\t" \
    "v_mfma_f32_16x16x32_bf16 v[244:247], v[208:211], v[80:83], 0\n\t" \
    "v_mfma_f32_16x16x32_bf16 v[244:247], v[212:215], v[84:87], v[244:247]\n\t" \
    "v_mfma_f32_16x16x32_bf16 v[244:247], v[216:219], v[88:91], v[244:247]\n\t" \
    "v_cvt_pk_bf16_f32 v226, v248, v249\n\t" \
    "v_mfma_f32_16x16x32_bf16 v[244:247], v[220:223], v[92:95], v[244:247]\n\t" \
    "v_cvt_pk_bf16_f32 v227, v250, v251\n\t" \
    "s_waitcnt lgkmcnt(1)\n\t" \
    "v_mfma_f32_16x16x32_bf16 v[244:247], v[200:203], v[236:239], v[244:247]\n\t" \
    "ds_read_b128 v[236:239], %[wl] offset:6144\n\t" \
    "s_waitcnt lgkmcnt(1)\n\t" \
    "v_mfma_f32_16x16x32_bf16 v[244:247], v[204:207], v[240:243], v[244:247]\n\t" \
    "ds_read_b128 v[240:243], %[wl] offset:7168\n\t" \
    "v_mfma_f32_16x16x32_bf16 v[248:251], v[208:211], v[120:123], 0\n\t" \
    "v_mfma_f32_16x16x32_bf16 v[248:251], v[212:215], v[124:127], v[248:251]\n\t" \
    "v_mfma_f32_16x16x32_bf16 v[248:251], v[216:219], v[128:131], v[248:251]\n\t" \
    "v_cvt_pk_bf16_f32 v228, v244, v245\n\t" \
    "v_mfma_f32_16x16x32_bf16 v[248:251], v[220:223], v[132:135], v[248:251]\n\t" \
    "v_cvt_pk_bf16_f32 v229, v246, v247\n\t" \
    "s_waitcnt lgkmcnt(1)\n\t" \
    "v_mfma_f32_16x16x32_bf16 v[248:251], v[200:203], v[236:239], v[248:251]\n\t" \
    "ds_read_b128 v[236:239], %[wl] offset:8192\n\t" \
    "s_waitcnt lgkmcnt(1)\n\t" \
    "v_mfma_f32_16x16x32_bf16 v[248:251], v[204:207], v[240:243], v[248:251]\n\t" \
    "ds_read_b128 v[240:243], %[wl] offset:9216\n\t" \
    "v_mfma_f32_16x16x32_bf16 v[244:247], v[208:211], v[160:163], 0\n\t" \
    "v_mfma_f32_16x16x32_bf16 v[244:247], v[212:215], v[164:167], v[244:247]\n\t" \
    "v_mfma_f32_16x16x32_bf16 v[244:247], v[216:219], v[168:171], v[244:247]\n\t" \
    "v_cvt_pk_bf16_f32 v230, v248, v249\n\t" \
    "v_mfma_f32_16x16x32_bf16 v[244:247], v[220:223], v[172:175], v[244:247]\n\t" \
    "v_cvt_pk_bf16_f32 v231, v250, v251\n\t" \
    "s_waitcnt lgkmcnt(1)\n\t" \
    "v_mfma_f32_16x16x32_bf16 v[244:247], v[200:203], v[236:239], v[244:247]\n\t" \
    "s_waitcnt lgkmcnt(0)\n\t" \
    "v_mfma_f32_16x16x32_bf16 v[244:247], v[204:207], v[240:243], v[244:247]\n\t" \
    "v_mfma_f32_16x16x32_bf16 v[248:251], v[208:211], v[24:27], 0\n\t" \
    "v_mfma_f32_16x16x32_bf16 v[248:251], v[212:215], v[28:31], v[248:251]\n\t" \
    "v_mfma_f32_16x16x32_bf16 v[248:251], v[216:219], v[32:35], v[248:251]\n\t" \
    "v_cvt_pk_bf16_f32 v232, v244, v245\n\t" \
    "v_mfma_f32_16x16x32_bf16 v[248:251], v[220:223], v[36:39], v[248:251]\n\t" \
    "v_cvt_pk_bf16_f32 v233, v246, v247\n\t" \
    "v_mfma_f32_16x16x32_bf16 v[248:251], v[200:203], v[16:19], v[248:251]\n\t" \
    "v_mfma_f32_16x16x32_bf16 v[248:251], v[204:207], v[20:23], v[248:251]\n\t" \
    "v_mfma_f32_16x16x32_bf16 v[244:247], v[208:211], v[64:67], 0\n\t" \
    "v_mfma_f32_16x16x32_bf16 v[244:247], v[212:215], v[68:71], v[244:247]\n\t" \
    "v_mfma_f32_16x16x32_bf16 v[244:247], v[216:219], v[72:75], v[244:247]\n\t" \
    "v_cvt_pk_bf16_f32 v236, v248, v249\n\t" \
    "v_mfma_f32_16x16x32_bf16 v[244:247], v[220:223], v[76:79], v[244:247]\n\t" \
    "v_cvt_pk_bf16_f32 v237, v250, v251\n\t" \
    "v_mfma_f32_16x16x32_bf16 v[244:247], v[200:203], v[56:59], v[244:247]\n\t" \
    "v_mfma_f32_16x16x32_bf16 v[244:247], v[204:207], v[60:63], v[244:247]\n\t" \
    "v_mfma_f32_16x16x32_bf16 v[248:251], v[208:211], v[104:107], 0\n\t" \
    "v_mfma_f32_16x16x32_bf16 v[248:251], v[212:215], v[108:111], v[248:251]\n\t" \
    "v_mfma_f32_16x16x32_bf16 v[248:251], v[216:219], v[112:115], v[248:251]\n\t" \
    "v_cvt_pk_bf16_f32 v238, v244, v245\n\t" \
    "v_mfma_f32_16x16x32_bf16 v[248:251], v[220:223], v[116:119], v[248:251]\n\t" \
    "v_cvt_pk_bf16_f32 v239, v246, v247\n\t" \
    "v_mfma_f32_16x16x32_bf16 v[248:251], v[200:203], v[96:99], v[248:251]\n\t" \
    "v_mfma_f32_16x16x32_bf16 v[248:251], v[204:207], v[100:103], v[248:251]\n\t" \
    "v_mfma_f32_16x16x32_bf16 v[244:247], v[208:211], v[144:147], 0\n\t" \
    "v_mfma_f32_16x16x32_bf16 v[244:247], v[212:215], v[148:151], v[244:247]\n\t" \
    "v_mfma_f32_16x16x32_bf16 v[244:247], v[216:219], v[152:155], v[244:247]\n\t" \
    "v_cvt_pk_bf16_f32 v240, v248, v249\n\t" \
    "v_mfma_f32_16x16x32_bf16 v[244:247], v[220:223], v[156:159], v[244:247]\n\t" \
    "v_cvt_pk_bf16_f32 v241, v250, v251\n\t" \
    "v_mfma_f32_16x16x32_bf16 v[244:247], v[200:203], v[136:139], v[244:247]\n\t" \
    "v_mfma_f32_16x16x32_bf16 v[244:247], v[204:207], v[140:143], v[244:247]\n\t" \
    "v_mfma_f32_16x16x32_bf16 v[248:251], v[208:211], v[184:187], 0\n\t" \
    "ds_read_b128 v[208:211], %[va] offset:8192\n\t" \
    "v_mfma_f32_16x16x32_bf16 v[248:251], v[212:215], v[188:191], v[248:251]\n\t" \
    "ds_read_b128 v[212:215], %[va] offset:9216\n\t" \
    "v_mfma_f32_16x16x32_bf16 v[248:251], v[216:219], v[192:195], v[248:251]\n\t" \
    "ds_read_b128 v[216:219], %[va] offset:10240\n\t" \
    "v_cvt_pk_bf16_f32 v242, v244, v245\n\t" \
    "v_mfma_f32_16x16x32_bf16 v[248:251], v[220:223], v[196:199], v[248:251]\n\t" \
    "ds_read_b128 v[220:223], %[va] offset:11264\n\t" \
    "v_cvt_pk_bf16_f32 v243, v246, v247\n\t" \
    "v_mfma_f32_16x16x32_bf16 v[248:251], v[200:203], v[176:179], v[248:251]\n\t" \
    "ds_read_b128 v[200:203], %[va] offset:6144\n\t" \
    "v_mfma_f32_16x16x32_bf16 v[248:251], v[204:207], v[180:183], v[248:251]\n\t" \
    "ds_read_b128 v[204:207], %[va] offset:7168\n\t" \
    "s_waitcnt lgkmcnt(5)\n\t" \
    "v_mfma_f32_16x16x32_bf16 v[244:247], v[208:211], v[24:27], 0\n\t" \
    "s_waitcnt lgkmcnt(4)\n\t" \
    "v_mfma_f32_16x16x32_bf16 v[244:247], v[212:215], v[28:31], v[244:247]\n\t" \
    "s_waitcnt lgkmcnt(3)\n\t" \
    "v_mfma_f32_16x16x32_bf16 v[244:247], v[216:219], v[32:35], v[244:247]\n\t" \
    "v_cvt_pk_bf16_f32 v234, v248, v249\n\t" \
    "s_waitcnt lgkmcnt(2)\n\t" \
    "v_mfma_f32_16x16x32_bf16 v[244:247], v[220:223], v[36:39], v[244:247]\n\t" \
    "v_cvt_pk_bf16_f32 v235, v250, v251\n\t" \
    "s_waitcnt lgkmcnt(1)\n\t" \
    "v_mfma_f32_16x16x32_bf16 v[244:247], v[200:203], v[16:19], v[244:247]\n\t" \
    "s_waitcnt lgkmcnt(0)\n\t" \
    "v_mfma_f32_16x16x32_bf16 v[244:247], v[204:207], v[20:23], v[244:247]\n\t" \
    "v_mfma_f32_16x16x32_bf16 v[248:251], v[208:211], v[64:67], 0\n\t" \
    "s_add_i32 %[so], %[so], 65536\n\t" \
    "v_mfma_f32_16x16x32_bf16 v[248:251], v[212:215], v[68:71], v[248:251]\n\t" \
    "s_nop 1\n\t" \
    "v_cvt_pk_bf16_f32 v247, v246, v247\n\t" \
    "v_mfma_f32_16x16x32_bf16 v[248:251], v[216:219], v[72:75], v[248:251]\n\t" \
    "v_cvt_pk_bf16_f32 v246, v244, v245\n\t" \
    "v_mfma_f32_16x16x32_bf16 v[248:251], v[220:223], v[76:79], v[248:251]\n\t" \
    "v_mov_b64 v[244:245], v[236:237]\n\t" \
    "buffer_store_dwordx4 v[244:247], %[l16], %[rd], %[so] offen sc1\n\t" \
    "v_mfma_f32_16x16x32_bf16 v[248:251], v[200:203], v[56:59], v[248:251]\n\t" \
    "v_mfma_f32_16x16x32_bf16 v[248:251], v[204:207], v[60:63], v[248:251]\n\t" \
    "v_mfma_f32_16x16x32_bf16 v[244:247], v[208:211], v[104:107], 0\n\t" \
    "s_add_i32 %[so], %[so], 131072\n\t" \
    "v_mfma_f32_16x16x32_bf16 v[244:247], v[212:215], v[108:111], v[244:247]\n\t" \
    "s_nop 1\n\t" \
    "v_cvt_pk_bf16_f32 v251, v250, v251\n\t" \
    "v_mfma_f32_16x16x32_bf16 v[244:247], v[216:219], v[112:115], v[244:247]\n\t" \
    "v_cvt_pk_bf16_f32 v250, v248, v249\n\t" \
    "v_mfma_f32_16x16x32_bf16 v[244:247], v[220:223], v[116:119], v[244:247]\n\t" \
    "v_mov_b64 v[248:249], v[238:239]\n\t" \
    "buffer_store_dwordx4 v[248:251], %[l16], %[rd], %[so] offen sc1\n\t" \
    "v_mfma_f32_16x16x32_bf16 v[244:247], v[200:203], v[96:99], v[244:247]\n\t" \
    "v_mfma_f32_16x16x32_bf16 v[244:247], v[204:207], v[100:103], v[244:247]\n\t" \
    "v_mfma_f32_16x16x32_bf16 v[248:251], v[208:211], v[144:147], 0\n\t" \
    "s_add_i32 %[so], %[so], 131072\n\t" \
    "v_mfma_f32_16x16x32_bf16 v[248:251], v[212:215], v[148:151], v[248:251]\n\t" \
    "s_nop 1\n\t" \
    "v_cvt_pk_bf16_f32 v247, v246, v247\n\t" \
    "v_mfma_f32_16x16x32_bf16 v[248:251], v[216:219], v[152:155], v[248:251]\n\t" \
    "v_cvt_pk_bf16_f32 v246, v244, v245\n\t" \
    "v_mfma_f32_16x16x32_bf16 v[248:251], v[220:223], v[156:159], v[248:251]\n\t" \
    "v_mov_b64 v[244:245], v[240:241]\n\t" \
    "buffer_store_dwordx4 v[244:247], %[l16], %[rd], %[so] offen sc1\n\t" \
    "v_mfma_f32_16x16x32_bf16 v[248:251], v[200:203], v[136:139], v[248:251]\n\t" \
    "v_mfma_f32_16x16x32_bf16 v[248:251], v[204:207], v[140:143], v[248:251]\n\t" \
    "v_mfma_f32_16x16x32_bf16 v[244:247], v[208:211], v[184:187], 0\n\t" \
    "s_add_i32 %[so], %[so], 131072\n\t" \
    "v_mfma_f32_16x16x32_bf16 v[244:247], v[212:215], v[188:191], v[244:247]\n\t" \
    "s_nop 1\n\t" \
    "v_cvt_pk_bf16_f32 v251, v250, v251\n\t" \
    "v_mfma_f32_16x16x32_bf16 v[244:247], v[216:219], v[192:195], v[244:247]\n\t" \
    "v_cvt_pk_bf16_f32 v250, v248, v249\n\t" \
    "v_mfma_f32_16x16x32_bf16 v[244:247], v[220:223], v[196:199], v[244:247]\n\t" \
    "v_mov_b64 v[248:249], v[242:243]\n\t" \
    "buffer_store_dwordx4 v[248:251], %[l16], %[rd], %[so] offen sc1\n\t" \
    "v_mfma_f32_16x16x32_bf16 v[244:247], v[200:203], v[176:179], v[244:247]\n\t" \
    "v_mfma_f32_16x16x32_bf16 v[244:247], v[204:207], v[180:183], v[244:247]\n\t" \
    "ds_read_b128 v[236:239], %[wl] offset:0\n\t" \
    "ds_read_b128 v[240:243], %[wl] offset:1024\n\t" \
    "v_mfma_f32_16x16x32_bf16 v[248:251], v[208:211], v[0:3], 0\n\t" \
    "s_add_i32 %[so], %[so], 131072\n\t" \
    "v_mfma_f32_16x16x32_bf16 v[248:251], v[212:215], v[4:7], v[248:251]\n\t" \
    "v_cvt_pk_bf16_f32 v247, v246, v247\n\t" \
    "v_mfma_f32_16x16x32_bf16 v[248:251], v[216:219], v[8:11], v[248:251]\n\t" \
    "v_cvt_pk_bf16_f32 v246, v244, v245\n\t" \
    "v_mfma_f32_16x16x32_bf16 v[248:251], v[220:223], v[12:15], v[248:251]\n\t" \
    "v_mov_b64 v[244:245], v[234:235]\n\t" \
    "buffer_store_dwordx4 v[244:247], %[l16], %[rd], %[so] offen sc1\n\t" \
    "s_waitcnt lgkmcnt(1)\n\t" \
    "v_mfma_f32_16x16x32_bf16 v[248:251], v[200:203], v[236:239], v[248:251]\n\t" \
    "ds_read_b128 v[236:239], %[wl] offset:2048\n\t" \
    "s_waitcnt lgkmcnt(1)\n\t" \
    "v_mfma_f32_16x16x32_bf16 v[248:251], v[204:207], v[240:243], v[248:251]\n\t" \
    "ds_read_b128 v[240:243], %[wl] offset:3072\n\t" \
    "v_mfma_f32_16x16x32_bf16 v[244:247], v[208:211], v[40:43], 0\n\t" \
    "s_add_i32 %[so], %[so], -589824\n\t" \
    "v_mfma_f32_16x16x32_bf16 v[244:247], v[212:215], v[44:47], v[244:247]\n\t" \
    "s_nop 0\n\t" \
    "v_cvt_pk_bf16_f32 v251, v250, v251\n\t" \
    "v_mfma_f32_16x16x32_bf16 v[244:247], v[216:219], v[48:51], v[244:247]\n\t" \
    "v_cvt_pk_bf16_f32 v250, v248, v249\n\t" \
    "v_mfma_f32_16x16x32_bf16 v[244:247], v[220:223], v[52:55], v[244:247]\n\t" \
    "v_mov_b64 v[248:249], v[224:225]\n\t" \
    "buffer_store_dwordx4 v[248:251], %[l16], %[rd], %[so] offen sc1\n\t" \
    "s_waitcnt lgkmcnt(1)\n\t" \
    "v_mfma_f32_16x16x32_bf16 v[244:247], v[200:203], v[236:239], v[244:247]\n\t" \
    "ds_read_b128 v[236:239], %[wl] offset:4096\n\t" \
    "s_waitcnt lgkmcnt(1)\n\t" \
    "v_mfma_f32_16x16x32_bf16 v[244:247], v[204:207], v[240:243], v[244:247]\n\t" \
    "ds_read_b128 v[240:243], %[wl] offset:5120\n\t" \
    "v_mfma_f32_16x16x32_bf16 v[248:251], v[208:211], v[80:83], 0\n\t" \
    "s_add_i32 %[so], %[so], 131072\n\t" \
    "v_mfma_f32_16x16x32_bf16 v[248:251], v[212:215], v[84:87], v[248:251]\n\t" \
    "s_nop 0\n\t" \
    "v_cvt_pk_bf16_f32 v247, v246, v247\n\t" \
    "v_mfma_f32_16x16x32_bf16 v[248:251], v[216:219], v[88:91], v[248:251]\n\t" \
    "v_cvt_pk_bf16_f32 v246, v244, v245\n\t" \
    "v_mfma_f32_16x16x32_bf16 v[248:251], v[220:223], v[92:95], v[248:251]\n\t" \
    "v_mov_b64 v[244:245], v[226:227]\n\t" \
    "buffer_store_dwordx4 v[244:247], %[l16], %[rd], %[so] offen sc1\n\t" \
    "s_waitcnt lgkmcnt(1)\n\t" \
    "v_mfma_f32_16x16x32_bf16 v[248:251], v[200:203], v[236:239], v[248:251]\n\t" \
    "ds_read_b128 v[236:239], %[wl] offset:6144\n\t" \
    "s_waitcnt lgkmcnt(1)\n\t" \
    "v_mfma_f32_16x16x32_bf16 v[248:251], v[204:207], v[240:243], v[248:251]\n\t" \
    "ds_read_b128 v[240:243], %[wl] offset:7168\n\t" \
    "v_mfma_f32_16x16x32_bf16 v[244:247], v[208:211], v[120:123], 0\n\t" \
    "s_add_i32 %[so], %[so], 131072\n\t" \
    "v_mfma_f32_16x16x32_bf16 v[244:247], v[212:215], v[124:127], v[244:247]\n\t" \
    "s_nop 0\n\t" \
    "v_cvt_pk_bf16_f32 v251, v250, v251\n\t" \
    "v_mfma_f32_16x16x32_bf16 v[244:247], v[216:219], v[128:131], v[244:247]\n\t" \
    "v_cvt_pk_bf16_f32 v250, v248, v249\n\t" \
    "v_mfma_f32_16x16x32_bf16 v[244:247], v[220:223], v[132:135], v[244:247]\n\t" \
    "v_mov_b64 v[248:249], v[228:229]\n\t" \
    "buffer_store_dwordx4 v[248:251], %[l16], %[rd], %[so] offen sc1\n\t" \
    "s_waitcnt lgkmcnt(1)\n\t" \
    "v_mfma_f32_16x16x32_bf16 v[244:247], v[200:203], v[236:239], v[244:247]\n\t" \
    "ds_read_b128 v[236:239], %[wl] offset:8192\n\t" \
    "s_waitcnt lgkmcnt(1)\n\t" \
    "v_mfma_f32_16x16x32_bf16 v[244:247], v[204:207], v[240:243], v[244:247]\n\t" \
    "ds_read_b128 v[240:243], %[wl] offset:9216\n\t" \
    "v_mfma_f32_16x16x32_bf16 v[248:251], v[208:211], v[160:163], 0\n\t" \
    "s_add_i32 %[so], %[so], 131072\n\t" \
    "v_mfma_f32_16x16x32_bf16 v[248:251], v[212:215], v[164:167], v[248:251]\n\t" \
    "s_nop 0\n\t" \
    "v_cvt_pk_bf16_f32 v247, v246, v247\n\t" \
    "v_mfma_f32_16x16x32_bf16 v[248:251], v[216:219], v[168:171], v[248:251]\n\t" \
    "v_cvt_pk_bf16_f32 v246, v244, v245\n\t" \
    "v_mfma_f32_16x16x32_bf16 v[248:251], v[220:223], v[172:175], v[248:251]\n\t" \
    "v_mov_b64 v[244:245], v[230:231]\n\t" \
    "buffer_store_dwordx4 v[244:247], %[l16], %[rd], %[so] offen sc1\n\t" \
    "s_waitcnt lgkmcnt(1)\n\t" \
    "v_mfma_f32_16x16x32_bf16 v[248:251], v[200:203], v[236:239], v[248:251]\n\t" \
    "s_waitcnt lgkmcnt(0)\n\t" \
    "v_mfma_f32_16x16x32_bf16 v[248:251], v[204:207], v[240:243], v[248:251]\n\t" \
    "s_add_i32 %[so], %[so], 131072\n\t" \
    "s_nop 7\n\t" \
    "v_cvt_pk_bf16_f32 v251, v250, v251\n\t" \
    "v_cvt_pk_bf16_f32 v250, v248, v249\n\t" \
    "v_mov_b64 v[248:249], v[232:233]\n\t" \
    "buffer_store_dwordx4 v[248:251], %[l16], %[rd], %[so] offen sc1\n\t" \
    "s_nop 1\n\t"
#define ASM_CRIT2 \
    "s_waitcnt lgkmcnt(0)\n\t" \
    "s_barrier\n\t" ASM_ST \
    "ds_read_b128 v[204:207], v200 offset:11264\n\t" \
    "ds_read_b128 v[208:211], v200 offset:12288\n\t" \
    "ds_read_b128 v[200:203], v200 offset:10240\n\t" \
    "s_waitcnt lgkmcnt(2)\n\t" \
    "v_smfmac_f32_16x16x64_bf16 v[224:227], v[204:207], v[24:31], %[sidx]\n\t" \
    "s_waitcnt lgkmcnt(1)\n\t" \
    "v_smfmac_f32_16x16x64_bf16 v[224:227], v[208:211], v[32:39], %[sidx]\n\t" \
    "s_waitcnt lgkmcnt(0)\n\t" \
    "v_smfmac_f32_16x16x64_bf16 v[224:227], v[200:203], v[16:23], %[sidx]\n\t" \
    "v_smfmac_f32_16x16x64_bf16 v[220:223], v[204:207], v[0:7], %[sidx]\n\t" \
    "v_smfmac_f32_16x16x64_bf16 v[220:223], v[208:211], v[8:15], %[sidx]\n\t" \
    "v_smfmac_f32_16x16x64_bf16 v[220:223], v[200:203], v[212:219], %[sidx]\n\t" \
    "ds_read_b128 v[212:215], %[wl] offset:2048\n\t" \
    "ds_read_b128 v[216:219], %[wl] offset:3072\n\t" \
    "v_smfmac_f32_16x16x64_bf16 v[232:235], v[204:207], v[64:71], %[sidx]\n\t" \
    "v_smfmac_f32_16x16x64_bf16 v[232:235], v[208:211], v[72:79], %[sidx]\n\t" \
    "v_smfmac_f32_16x16x64_bf16 v[232:235], v[200:203], v[56:63], %[sidx]\n\t" \
    "v_permlane32_swap_b32 v220, v224\n\t" \
    "v_permlane32_swap_b32 v221, v225\n\t" \
    "v_permlane32_swap_b32 v222, v226\n\t" \
    "v_smfmac_f32_16x16x64_bf16 v[228:231], v[204:207], v[40:47], %[sidx]\n\t" \
    "v_permlane32_swap_b32 v223, v227\n\t" \
    "v_add_f32 v220, v220, v224\n\t" \
    "v_add_f32 v221, v221, v225\n\t" \
    "v_smfmac_f32_16x16x64_bf16 v[228:231], v[208:211], v[48:55], %[sidx]\n\t" \
    "v_add_f32 v222, v222, v226\n\t" \
    "v_add_f32 v223, v223, v227\n\t" \
    "v_cvt_pk_bf16_f32 v226, v220, v221\n\t" \
    "s_waitcnt lgkmcnt(0)\n\t" \
    "v_smfmac_f32_16x16x64_bf16 v[228:231], v[200:203], v[212:219], %[sidx]\n\t" \
    "ds_read_b128 v[212:215], %[wl] offset:4096\n\t" \
    "ds_read_b128 v[216:219], %[wl] offset:5120\n\t" \
    "v_cvt_pk_bf16_f32 v227, v222, v223\n\t" \
    "v_min_u32 v226, -2, v226\n\t" \
    "v_min_u32 v227, -2, v227\n\t" \
    "v_smfmac_f32_16x16x64_bf16 v[240:243], v[204:207], v[104:111], %[sidx]\n\t" \
    "v_smfmac_f32_16x16x64_bf16 v[240:243], v[208:211], v[112:119], %[sidx]\n\t" \
    "v_smfmac_f32_16x16x64_bf16 v[240:243], v[200:203], v[96:103], %[sidx]\n\t" \
    "v_permlane32_swap_b32 v228, v232\n\t" \
    "v_permlane32_swap_b32 v229, v233\n\t" \
    "v_permlane32_swap_b32 v230, v234\n\t" \
    "v_smfmac_f32_16x16x64_bf16 v[236:239], v[204:207], v[80:87], %[sidx]\n\t" \
    "v_permlane32_swap_b32 v231, v235\n\t" \
    "v_add_f32 v228, v228, v232\n\t" \
    "v_add_f32 v229, v229, v233\n\t" \
    "v_smfmac_f32_16x16x64_bf16 v[236:239], v[208:211], v[88:95], %[sidx]\n\t" \
    "v_add_f32 v230, v230, v234\n\t" \
    "v_add_f32 v231, v231, v235\n\t" \
    "v_cvt_pk_bf16_f32 v228, v228, v229\n\t" \
    "s_waitcnt lgkmcnt(0)\n\t" \
    "v_smfmac_f32_16x16x64_bf16 v[236:239], v[200:203], v[212:219], %[sidx]\n\t" \
    "ds_read_b128 v[212:215], %[wl] offset:6144\n\t" \
    "ds_read_b128 v[216:219], %[wl] offset:7168\n\t" \
    "v_cvt_pk_bf16_f32 v229, v230, v231\n\t" \
    "v_min_u32 v228, -2, v228\n\t" \
    "v_min_u32 v229, -2, v229\n\t" \
    "s_nop 1\n\t" \
    "v_permlane16_swap_b32 v226, v228\n\t" \
    "v_permlane16_swap_b32 v227, v229\n\t" \
    "buffer_store_dwordx4 v[226:229], %[vo], %[rr], %[so] offen sc1\n\t" \
    "s_add_u32 %[so], %[so], 0x10000\n\t" \
    "v_smfmac_f32_16x16x64_bf16 v[248:251], v[204:207], v[144:151], %[sidx]\n\t" \
    "v_mov_b64 v[220:221], 0\n\t" \
    "v_mov_b64 v[222:223], 0\n\t" \
    "v_smfmac_f32_16x16x64_bf16 v[248:251], v[208:211], v[152:159], %[sidx]\n\t" \
    "v_mov_b64 v[224:225], 0\n\t" \
    "v_mov_b64 v[226:227], 0\n\t" \
    "v_smfmac_f32_16x16x64_bf16 v[248:251], v[200:203], v[136:143], %[sidx]\n\t" \
    "v_permlane32_swap_b32 v236, v240\n\t" \
    "v_permlane32_swap_b32 v237, v241\n\t" \
    "v_permlane32_swap_b32 v238, v242\n\t" \
    "v_smfmac_f32_16x16x64_bf16 v[244:247], v[204:207], v[120:127], %[sidx]\n\t" \
    "v_permlane32_swap_b32 v239, v243\n\t" \
    "v_add_f32 v236, v236, v240\n\t" \
    "v_add_f32 v237, v237, v241\n\t" \
    "v_smfmac_f32_16x16x64_bf16 v[244:247], v[208:211], v[128:135], %[sidx]\n\t" \
    "v_add_f32 v238, v238, v242\n\t" \
    "v_add_f32 v239, v239, v243\n\t" \
    "v_cvt_pk_bf16_f32 v242, v236, v237\n\t" \
    "s_waitcnt lgkmcnt(0)\n\t" \
    "v_smfmac_f32_16x16x64_bf16 v[244:247], v[200:203], v[212:219], %[sidx]\n\t" \
    "ds_read_b128 v[212:215], %[wl] offset:8192\n\t" \
    "ds_read_b128 v[216:219], %[wl] offset:9216\n\t" \
    "v_cvt_pk_bf16_f32 v243, v238, v239\n\t" \
    "v_min_u32 v242, -2, v242\n\t" \
    "v_min_u32 v243, -2, v243\n\t" \
    "v_smfmac_f32_16x16x64_bf16 v[224:227], v[204:207], v[184:191], %[sidx]\n\t" \
    "v_and_b32 v232, 0x3fff, %[vo]\n\t" \
    "v_bfe_u32 v233, %[vo], 14, 1\n\t" \
    "v_smfmac_f32_16x16x64_bf16 v[224:227], v[208:211], v[192:199], %[sidx]\n\t" \
    "v_lshl_add_u32 v232, v233, 3, v232\n\t" \
    "v_permlane32_swap_b32 v244, v248\n\t" \
    "v_smfmac_f32_16x16x64_bf16 v[224:227], v[200:203], v[176:183], %[sidx]\n\t" \
    "v_permlane32_swap_b32 v245, v249\n\t" \
    "v_permlane32_swap_b32 v246, v250\n\t" \
    "v_permlane32_swap_b32 v247, v251\n\t" \
    "v_smfmac_f32_16x16x64_bf16 v[220:223], v[204:207], v[160:167], %[sidx]\n\t" \
    "v_add_f32 v244, v244, v248\n\t" \
    "v_add_f32 v245, v245, v249\n\t" \
    "v_add_f32 v246, v246, v250\n\t" \
    "v_smfmac_f32_16x16x64_bf16 v[220:223], v[208:211], v[168:175], %[sidx]\n\t" \
    "v_add_f32 v247, v247, v251\n\t" \
    "v_cvt_pk_bf16_f32 v244, v244, v245\n\t" \
    "v_cvt_pk_bf16_f32 v245, v246, v247\n\t" \
    "s_waitcnt lgkmcnt(0)\n\t" \
    "v_smfmac_f32_16x16x64_bf16 v[220:223], v[200:203], v[212:219], %[sidx]\n\t" \
    "v_min_u32 v244, -2, v244\n\t" \
    "v_min_u32 v245, -2, v245\n\t" \
    "s_nop 1\n\t" \
    "v_permlane16_swap_b32 v242, v244\n\t" \
    "v_permlane16_swap_b32 v243, v245\n\t" \
    "buffer_store_dwordx4 v[242:245], %[vo], %[rr], %[so] offen sc1\n\t" \
    "s_add_u32 %[so], %[so], 0x10000\n\t" \
    "s_nop 0\n\t" \
    "v_permlane32_swap_b32 v220, v224\n\t" \
    "v_permlane32_swap_b32 v221, v225\n\t" \
    "v_permlane32_swap_b32 v222, v226\n\t" \
    "v_permlane32_swap_b32 v223, v227\n\t" \
    "v_add_f32 v220, v220, v224\n\t" \
    "v_add_f32 v221, v221, v225\n\t" \
    "v_add_f32 v222, v222, v226\n\t" \
    "v_add_f32 v223, v223, v227\n\t" \
    "v_cvt_pk_bf16_f32 v220, v220, v221\n\t" \
    "v_cvt_pk_bf16_f32 v221, v222, v223\n\t" \
    "v_min_u32 v220, -2, v220\n\t" \
    "v_min_u32 v221, -2, v221\n\t" \
    "buffer_store_dwordx2 v[220:221], v232, %[rr], %[so] offen sc1\n\t" \
    "s_nop 1\n\t"
#define ASM_SM \
    "v_mbcnt_lo_u32_b32 v252, -1, 0\n\t" \
    "v_mbcnt_hi_u32_b32 v252, -1, v252\n\t" \
    "v_lshlrev_b32 v252, 4, v252\n\t" \
    "v_lshrrev_b32 v253, 1, v252\n\t" \
    "v_xor_b32 v253, v253, v252\n\t" \
    "v_and_b32 v253, 0x20, v253\n\t" \
    "v_mul_u32_u24 v253, 3, v253\n\t" \
    "v_xor_b32 v253, v253, v252\n\t" \
    "ds_read_b128 v[200:203], v253 offset:28672\n\t" \
    "ds_read_b128 v[204:207], v253 offset:29696\n\t" \
    "ds_read_b128 v[208:211], v253 offset:34816\n\t" \
    "ds_read_b128 v[212:215], v253 offset:35840\n\t" \
    "ds_read_b128 v[216:219], v253 offset:30720\n\t" \
    "ds_read_b128 v[220:223], v253 offset:31744\n\t" \
    "ds_read_b128 v[224:227], v253 offset:36864\n\t" \
    "ds_read_b128 v[228:231], v253 offset:37888\n\t" \
    "v_lshrrev_b32 v254, 1, v252\n\t" \
    "v_and_b32 v254, 0x180, v254\n\t" \
    "v_and_or_b32 v254, v252, %[c70], v254\n\t" \
    "v_bfe_i32 v255, v252, 7, 1\n\t" \
    "v_and_b32 v255, 0xaaaa, v255\n\t" \
    "v_or_b32 v255, 0x4444, v255\n\t" \
    "v_mov_b64 v[232:233], 0\n\t" \
    "v_mov_b64 v[234:235], 0\n\t" \
    "v_mov_b64 v[236:237], 0\n\t" \
    "v_mov_b64 v[238:239], 0\n\t" \
    "s_waitcnt lgkmcnt(0)\n\t" \
    "s_barrier\n\t" ASM_ST \
    "s_setprio 3\n\t" \
    "ds_read_b128 v[240:243], v252 offset:10240\n\t" \
    "ds_read_b128 v[244:247], v252 offset:11264\n\t" \
    "ds_read_b128 v[248:251], v252 offset:12288\n\t" \
    "s_waitcnt lgkmcnt(2)\n\t" \
    "v_smfmac_f32_16x16x64_bf16 v[232:235], v[240:243], v[200:207], v255\n\t" \
    "ds_read_b128 v[200:203], v253 offset:32768\n\t" \
    "ds_read_b128 v[204:207], v253 offset:33792\n\t" \
    "v_smfmac_f32_16x16x64_bf16 v[236:239], v[240:243], v[208:215], v255\n\t" \
    "ds_read_b128 v[208:211], v253 offset:38912\n\t" \
    "ds_read_b128 v[212:215], v253 offset:39936\n\t" \
    "s_waitcnt lgkmcnt(5)\n\t" \
    "v_smfmac_f32_16x16x64_bf16 v[232:235], v[244:247], v[216:223], v255\n\t" \
    "ds_read_b128 v[216:219], v254 offset:13312\n\t" \
    "ds_read_b128 v[220:223], v254 offset:13824\n\t" \
    "v_smfmac_f32_16x16x64_bf16 v[236:239], v[244:247], v[224:231], v255\n\t" \
    "ds_read_b128 v[224:227], v254 offset:14336\n\t" \
    "ds_read_b128 v[228:231], v254 offset:14848\n\t" \
    "s_waitcnt lgkmcnt(6)\n\t" \
    "v_smfmac_f32_16x16x64_bf16 v[232:235], v[248:251], v[200:207], v255\n\t" \
    "s_waitcnt lgkmcnt(4)\n\t" \
    "v_smfmac_f32_16x16x64_bf16 v[236:239], v[248:251], v[208:215], v255\n\t" \
    "ds_read_b128 v[208:211], v254 offset:15360\n\t" \
    "ds_read_b128 v[212:215], v254 offset:15872\n\t" \
    "v_mov_b64 v[204:205], 0\n\t" \
    "v_mov_b64 v[206:207], 0\n\t" \
    "s_nop 1\n\t" \
    "s_waitcnt lgkmcnt(4)\n\t" \
    "v_smfmac_f32_16x16x64_bf16 v[204:207], v[240:243], v[216:223], v255\n\t" \
    "s_waitcnt lgkmcnt(2)\n\t" \
    "v_smfmac_f32_16x16x64_bf16 v[204:207], v[244:247], v[224:231], v255\n\t" \
    "v_bfe_u32 v253, v252, 5, 1\n\t" \
    "v_bfe_i32 v202, v252, 4, 1\n\t" \
    "v_lshlrev_b32 v200, 1, v252\n\t" \
    "v_and_b32 v200, 0x180, v200\n\t" \
    "v_permlane32_swap_b32 v232, v234\n\t" \
    "v_permlane32_swap_b32 v233, v235\n\t" \
    "v_permlane32_swap_b32 v236, v238\n\t" \
    "v_permlane32_swap_b32 v237, v239\n\t" \
    "v_lshrrev_b32 v201, 2, v252\n\t" \
    "v_lshrrev_b32 v203, 4, v252\n\t" \
    "v_and_b32 v201, 0x40, v201\n\t" \
    "v_and_or_b32 v201, v203, 32, v201\n\t" \
    "v_and_or_b32 v201, v252, 16, v201\n\t" \
    "v_pk_add_f32 v[232:233], v[232:233], v[234:235]\n\t" \
    "v_pk_add_f32 v[234:235], v[236:237], v[238:239]\n\t" \
    "v_or_b32 v200, v200, v201\n\t" \
    "v_mad_u32_u24 v200, v253, %[cs], v200\n\t" \
    "v_mad_u32_u24 v201, v253, %[cs], v201\n\t" \
    "v_lshrrev_b32 v203, 3, v252\n\t" \
    "v_bfe_u32 v253, v252, 7, 1\n\t" \
    "s_waitcnt lgkmcnt(0)\n\t" \
    "v_smfmac_f32_16x16x64_bf16 v[204:207], v[248:251], v[208:215], v255\n\t" \
    "v_and_or_b32 v201, v203, 8, v201\n\t" \
    "v_and_b32 v202, 0x06060606, v202\n\t" \
    "v_mad_u32_u24 v201, v253, %[cp], v201\n\t" \
    "v_xor_b32 v202, 0x01000504, v202\n\t" \
    "v_max_f32 v236, v232, v234\n\t" \
    "v_max_f32 v237, v233, v235\n\t" \
    "s_nop 1\n\t" \
    "v_permlane32_swap_b32 v204, v206\n\t" \
    "v_permlane32_swap_b32 v205, v207\n\t" \
    "v_add_f32 v204, v204, v206\n\t" \
    "v_add_f32 v205, v205, v207\n\t" \
    "v_max_f32 v236, v236, v204\n\t" \
    "v_max_f32 v237, v237, v205\n\t" \
    "s_nop 0\n\t" \
    "v_max_f32_dpp v236, v236, v236 quad_perm:[1,0,3,2] row_mask:0xf bank_mask:0xf\n\t" \
    "v_max_f32_dpp v237, v237, v237 quad_perm:[1,0,3,2] row_mask:0xf bank_mask:0xf\n\t" \
    "s_nop 0\n\t" \
    "v_max_f32_dpp v236, v236, v236 quad_perm:[2,3,0,1] row_mask:0xf bank_mask:0xf\n\t" \
    "v_max_f32_dpp v237, v237, v237 quad_perm:[2,3,0,1] row_mask:0xf bank_mask:0xf\n\t" \
    "s_nop 0\n\t" \
    "v_max_f32_dpp v236, v236, v236 row_half_mirror row_mask:0xf bank_mask:0xf\n\t" \
    "v_max_f32_dpp v237, v237, v237 row_half_mirror row_mask:0xf bank_mask:0xf\n\t" \
    "s_nop 0\n\t" \
    "v_max_f32_dpp v236, v236, v236 row_mirror row_mask:0xf bank_mask:0xf\n\t" \
    "v_max_f32_dpp v237, v237, v237 row_mirror row_mask:0xf bank_mask:0xf\n\t" \
    "v_mul_f32 v236, %[scl], v236\n\t" \
    "v_mul_f32 v237, %[scl], v237\n\t" \
    "v_fma_f32 v232, v232, %[scl], -v236\n\t" \
    "v_fma_f32 v233, v233, %[scl], -v237\n\t" \
    "v_fma_f32 v234, v234, %[scl], -v236\n\t" \
    "v_fma_f32 v235, v235, %[scl], -v237\n\t" \
    "v_exp_f32 v232, v232\n\t" \
    "v_exp_f32 v233, v233\n\t" \
    "v_fma_f32 v204, v204, %[scl], -v236\n\t" \
    "v_fma_f32 v205, v205, %[scl], -v237\n\t" \
    "v_exp_f32 v234, v234\n\t" \
    "v_exp_f32 v235, v235\n\t" \
    "v_exp_f32 v204, v204\n\t" \
    "v_exp_f32 v205, v205\n\t" \
    "v_add_f32 v238, v232, v234\n\t" \
    "v_add_f32 v239, v233, v235\n\t" \
    "v_fma_f32 v238, v204, 0.5, v238\n\t" \
    "v_fma_f32 v239, v205, 0.5, v239\n\t" \
    "s_nop 0\n\t" \
    "v_add_f32_dpp v238, v238, v238 quad_perm:[1,0,3,2] row_mask:0xf bank_mask:0xf\n\t" \
    "v_add_f32_dpp v239, v239, v239 quad_perm:[1,0,3,2] row_mask:0xf bank_mask:0xf\n\t" \
    "s_nop 0\n\t" \
    "v_add_f32_dpp v238, v238, v238 quad_perm:[2,3,0,1] row_mask:0xf bank_mask:0xf\n\t" \
    "v_add_f32_dpp v239, v239, v239 quad_perm:[2,3,0,1] row_mask:0xf bank_mask:0xf\n\t" \
    "s_nop 0\n\t" \
    "v_add_f32_dpp v238, v238, v238 row_half_mirror row_mask:0xf bank_mask:0xf\n\t" \
    "v_add_f32_dpp v239, v239, v239 row_half_mirror row_mask:0xf bank_mask:0xf\n\t" \
    "s_nop 0\n\t" \
    "v_add_f32_dpp v238, v238, v238 row_mirror row_mask:0xf bank_mask:0xf\n\t" \
    "v_add_f32_dpp v239, v239, v239 row_mirror row_mask:0xf bank_mask:0xf\n\t" \
    "v_rcp_f32 v238, v238\n\t" \
    "v_rcp_f32 v239, v239\n\t" \
    "s_nop 0\n\t" \
    "v_pk_mul_f32 v[232:233], v[232:233], v[238:239]\n\t" \
    "v_pk_mul_f32 v[234:235], v[234:235], v[238:239]\n\t" \
    "v_pk_mul_f32 v[204:205], v[204:205], v[238:239]\n\t" \
    "v_cvt_pk_bf16_f32 v232, |v232|, |v233|\n\t" \
    "v_cvt_pk_bf16_f32 v234, |v234|, |v235|\n\t" \
    "v_cvt_pk_bf16_f32 v204, |v204|, |v205|\n\t" \
    "v_mov_b32_dpp v233, v232 quad_perm:[1,0,3,2] row_mask:0xf bank_mask:0xf\n\t" \
    "v_mov_b32_dpp v235, v234 quad_perm:[1,0,3,2] row_mask:0xf bank_mask:0xf\n\t" \
    "v_mov_b32_dpp v205, v204 quad_perm:[1,0,3,2] row_mask:0xf bank_mask:0xf\n\t" \
    "v_perm_b32 v232, v232, v233, v202\n\t" \
    "v_perm_b32 v234, v234, v235, v202\n\t" \
    "v_perm_b32 v204, v204, v205, v202\n\t" \
    "v_mov_b32_dpp v240, v232 quad_perm:[0,1,0,1] row_mask:0xf bank_mask:0xf\n\t" \
    "v_mov_b32_dpp v241, v234 quad_perm:[0,1,0,1] row_mask:0xf bank_mask:0xf\n\t" \
    "v_mov_b32_dpp v242, v232 quad_perm:[2,3,2,3] row_mask:0xf bank_mask:0xf\n\t" \
    "v_mov_b32_dpp v243, v234 quad_perm:[2,3,2,3] row_mask:0xf bank_mask:0xf\n\t" \
    "buffer_store_dwordx4 v[240:243], v200, %[rr], %[sx] offen sc1\n\t" \
    "v_mov_b32_dpp v244, v204 quad_perm:[0,1,0,1] row_mask:0xf bank_mask:0xf\n\t" \
    "v_mov_b32_dpp v245, v204 quad_perm:[2,3,2,3] row_mask:0xf bank_mask:0xf\n\t" \
    "buffer_store_dwordx4 v[240:243], v200, %[rr], %[sy] offen sc1\n\t" \
    "buffer_store_dwordx2 v[244:245], v201, %[rr], %[sm] offen sc1\n\t" \
    "s_wakeup\n\t" \
    PRISM
#define ASM_P4_0 \
    "ds_read_b128 v[200:203], %[qa] offset:0\n\t" \
    "ds_read_b128 v[204:207], %[qa] offset:1024\n\t" \
    "ds_read_b128 v[208:211], %[qa] offset:2048\n\t" \
    "ds_read_b128 v[212:215], %[qa] offset:3072\n\t" \
    "ds_read_b128 v[216:219], %[qa] offset:4096\n\t" \
    "ds_read_b128 v[220:223], %[qa] offset:5120\n\t" \
    "ds_read_b128 v[224:227], %[qa] offset:12288\n\t" \
    "ds_read_b128 v[228:231], %[qa] offset:13312\n\t" \
    "ds_read_b128 v[232:235], %[qa] offset:14336\n\t" \
    "ds_read_b128 v[236:239], %[qa] offset:15360\n\t" \
    "s_waitcnt lgkmcnt(0)\n\t" \
    "s_barrier\n\t" ASM_ST \
    "v_mfma_f32_16x16x32_bf16 v[240:243], v[224:227], v[200:203], v[240:243]\n\t" \
    "ds_read_b128 v[224:227], %[qa] offset:16384\n\t" \
    "v_mfma_f32_16x16x32_bf16 v[240:243], v[228:231], v[204:207], v[240:243]\n\t" \
    "ds_read_b128 v[228:231], %[qa] offset:17408\n\t" \
    "v_mfma_f32_16x16x32_bf16 v[240:243], v[232:235], v[208:211], v[240:243]\n\t" \
    "ds_read_b128 v[232:235], %[km] offset:0\n\t" \
    "v_mfma_f32_16x16x32_bf16 v[240:243], v[236:239], v[212:215], v[240:243]\n\t" \
    "ds_read_b128 v[236:239], %[km] offset:512\n\t" \
    "s_waitcnt lgkmcnt(3)\n\t" \
    "v_mfma_f32_16x16x32_bf16 v[240:243], v[224:227], v[216:219], v[240:243]\n\t" \
    "ds_read_b128 v[224:227], %[km] offset:1024\n\t" \
    "s_waitcnt lgkmcnt(3)\n\t" \
    "v_mfma_f32_16x16x32_bf16 v[240:243], v[228:231], v[220:223], v[240:243]\n\t" \
    "ds_read_b128 v[228:231], %[km] offset:1536\n\t" \
    "s_waitcnt lgkmcnt(3)\n\t" \
    "v_mfma_f32_16x16x32_bf16 v[248:251], v[232:235], v[200:203], 0\n\t" \
    "ds_read_b128 v[232:235], %[km] offset:2048\n\t" \
    "s_waitcnt lgkmcnt(3)\n\t" \
    "v_mfma_f32_16x16x32_bf16 v[248:251], v[236:239], v[204:207], v[248:251]\n\t" \
    "ds_read_b128 v[236:239], %[km] offset:2560\n\t" \
    "s_waitcnt lgkmcnt(3)\n\t" \
    "v_mfma_f32_16x16x32_bf16 v[248:251], v[224:227], v[208:211], v[248:251]\n\t" \
    "s_waitcnt lgkmcnt(2)\n\t" \
    "v_mfma_f32_16x16x32_bf16 v[248:251], v[228:231], v[212:215], v[248:251]\n\t" \
    "s_waitcnt lgkmcnt(1)\n\t" \
    "v_mfma_f32_16x16x32_bf16 v[248:251], v[232:235], v[216:219], v[248:251]\n\t" \
    "s_waitcnt lgkmcnt(0)\n\t" \
    "v_mfma_f32_16x16x32_bf16 v[248:251], v[236:239], v[220:223], v[248:251]\n\t" \
    "v_max3_f32 v224, v240, v241, v242\n\t" \
    "v_mov_b32 v225, v243\n\t" \
    "v_lshlrev_b32 v201, 23, %[qa]\n\t" \
    "v_and_b32 v201, 0x80000000, v201\n\t" \
    "v_and_b32 v200, 0x3ff, %[qa]\n\t" \
    "v_or_b32 v200, v201, v200\n\t" \
    "s_nop 1\n\t" \
    "v_max3_f32 v224, v224, v248, v249\n\t" \
    "v_max3_f32 v225, v225, v250, v251\n\t" \
    "v_max_f32 v224, v224, v225\n\t" \
    "v_mov_b32 v225, v224\n\t" \
    "s_nop 1\n\t" \
    "v_permlane16_swap_b32 v224, v225\n\t" \
    "v_max_f32 v224, v224, v225\n\t" \
    "v_mov_b32 v225, v224\n\t" \
    "s_nop 1\n\t" \
    "v_permlane32_swap_b32 v224, v225\n\t" \
    "v_max_f32 v224, v224, v225\n\t" \
    "v_mul_f32 v224, %[scl], v224\n\t" \
    "v_fma_f32 v240, v240, %[scl], -v224\n\t" \
    "v_fma_f32 v241, v241, %[scl], -v224\n\t" \
    "v_exp_f32 v240, v240\n\t" \
    "v_fma_f32 v242, v242, %[scl], -v224\n\t" \
    "v_exp_f32 v241, v241\n\t" \
    "v_fma_f32 v243, v243, %[scl], -v224\n\t" \
    "v_exp_f32 v242, v242\n\t" \
    "v_fma_f32 v248, v248, %[scl], -v224\n\t" \
    "v_exp_f32 v243, v243\n\t" \
    "v_fma_f32 v249, v249, %[scl], -v224\n\t" \
    "v_exp_f32 v248, v248\n\t" \
    "v_fma_f32 v250, v250, %[scl], -v224\n\t" \
    "v_exp_f32 v249, v249\n\t" \
    "v_fma_f32 v251, v251, %[scl], -v224\n\t" \
    "v_exp_f32 v250, v250\n\t" \
    "v_exp_f32 v251, v251\n\t" \
    "v_add_f32 v224, v240, v241\n\t" \
    "v_add_f32 v225, v242, v243\n\t" \
    "v_add_f32 v232, v248, v249\n\t" \
    "v_add_f32 v233, v250, v251\n\t" \
    "v_add_f32 v224, v224, v225\n\t" \
    "v_add_f32 v232, v232, v233\n\t" \
    "v_fma_f32 v224, v232, 0.5, v224\n\t" \
    "v_mov_b32 v225, v224\n\t" \
    "s_nop 1\n\t" \
    "v_permlane16_swap_b32 v224, v225\n\t" \
    "v_add_f32 v224, v224, v225\n\t" \
    "v_mov_b32 v225, v224\n\t" \
    "s_nop 1\n\t" \
    "v_permlane32_swap_b32 v224, v225\n\t" \
    "v_add_f32 v224, v224, v225\n\t" \
    "v_rcp_f32 v224, v224\n\t" \
    "s_nop 0\n\t" \
    "v_mul_f32 v240, v240, v224\n\t" \
    "v_mul_f32 v241, v241, v224\n\t" \
    "v_mul_f32 v242, v242, v224\n\t" \
    "v_mul_f32 v243, v243, v224\n\t" \
    "v_mul_f32 v248, v248, v224\n\t" \
    "v_mul_f32 v249, v249, v224\n\t" \
    "v_mul_f32 v250, v250, v224\n\t" \
    "v_mul_f32 v251, v251, v224\n\t" \
    "v_cvt_pk_bf16_f32 v228, |v240|, |v241|\n\t" \
    "v_cvt_pk_bf16_f32 v229, |v242|, |v243|\n\t" \
    "v_cvt_pk_bf16_f32 v232, |v248|, |v249|\n\t" \
    "v_cvt_pk_bf16_f32 v233, |v250|, |v251|\n\t" \
    "v_permlane16_swap_b32 v228, v230\n\t" \
    "v_permlane16_swap_b32 v229, v231\n\t" \
    "buffer_store_dwordx4 v[228:231], v200, %[rr], %[so] offen sc1\n\t" \
    "buffer_store_dwordx2 v[232:233], %[pm], %[rr], %[so] offen sc1\n\t" \
    "s_nop 1\n\t"
#define ASM_P4_1 \
    "ds_read_b128 v[200:203], %[qa] offset:0\n\t" \
    "ds_read_b128 v[204:207], %[qa] offset:1024\n\t" \
    "ds_read_b128 v[208:211], %[qa] offset:2048\n\t" \
    "ds_read_b128 v[212:215], %[qa] offset:3072\n\t" \
    "ds_read_b128 v[216:219], %[qa] offset:4096\n\t" \
    "ds_read_b128 v[220:223], %[qa] offset:5120\n\t" \
    "ds_read_b128 v[224:227], %[qa] offset:12288\n\t" \
    "ds_read_b128 v[228:231], %[qa] offset:13312\n\t" \
    "ds_read_b128 v[232:235], %[qa] offset:14336\n\t" \
    "ds_read_b128 v[236:239], %[qa] offset:15360\n\t" \
    "s_waitcnt lgkmcnt(0)\n\t" \
    "s_barrier\n\t" ASM_ST \
    "v_mfma_f32_16x16x32_bf16 v[240:243], v[224:227], v[200:203], v[240:243]\n\t" \
    "ds_read_b128 v[224:227], %[qa] offset:16384\n\t" \
    "v_mfma_f32_16x16x32_bf16 v[240:243], v[228:231], v[204:207], v[240:243]\n\t" \
    "ds_read_b128 v[228:231], %[qa] offset:17408\n\t" \
    "v_mfma_f32_16x16x32_bf16 v[240:243], v[232:235], v[208:211], v[240:243]\n\t" \
    "ds_read_b128 v[232:235], %[qa] offset:6144\n\t" \
    "v_mfma_f32_16x16x32_bf16 v[240:243], v[236:239], v[212:215], v[240:243]\n\t" \
    "ds_read_b128 v[236:239], %[qa] offset:7168\n\t" \
    "s_waitcnt lgkmcnt(3)\n\t" \
    "v_mfma_f32_16x16x32_bf16 v[240:243], v[224:227], v[216:219], v[240:243]\n\t" \
    "ds_read_b128 v[224:227], %[qa] offset:8192\n\t" \
    "s_waitcnt lgkmcnt(3)\n\t" \
    "v_mfma_f32_16x16x32_bf16 v[240:243], v[228:231], v[220:223], v[240:243]\n\t" \
    "ds_read_b128 v[228:231], %[qa] offset:9216\n\t" \
    "s_waitcnt lgkmcnt(3)\n\t" \
    "v_mfma_f32_16x16x32_bf16 v[244:247], v[232:235], v[200:203], 0\n\t" \
    "ds_read_b128 v[232:235], %[qa] offset:10240\n\t" \
    "s_waitcnt lgkmcnt(3)\n\t" \
    "v_mfma_f32_16x16x32_bf16 v[244:247], v[236:239], v[204:207], v[244:247]\n\t" \
    "ds_read_b128 v[236:239], %[qa] offset:11264\n\t" \
    "s_waitcnt lgkmcnt(3)\n\t" \
    "v_mfma_f32_16x16x32_bf16 v[244:247], v[224:227], v[208:211], v[244:247]\n\t" \
    "ds_read_b128 v[224:227], %[km] offset:0\n\t" \
    "s_waitcnt lgkmcnt(3)\n\t" \
    "v_mfma_f32_16x16x32_bf16 v[244:247], v[228:231], v[212:215], v[244:247]\n\t" \
    "ds_read_b128 v[228:231], %[km] offset:512\n\t" \
    "s_waitcnt lgkmcnt(3)\n\t" \
    "v_mfma_f32_16x16x32_bf16 v[244:247], v[232:235], v[216:219], v[244:247]\n\t" \
    "ds_read_b128 v[232:235], %[km] offset:1024\n\t" \
    "s_waitcnt lgkmcnt(3)\n\t" \
    "v_mfma_f32_16x16x32_bf16 v[244:247], v[236:239], v[220:223], v[244:247]\n\t" \
    "ds_read_b128 v[236:239], %[km] offset:1536\n\t" \
    "s_waitcnt lgkmcnt(3)\n\t" \
    "v_mfma_f32_16x16x32_bf16 v[248:251], v[224:227], v[200:203], 0\n\t" \
    "ds_read_b128 v[224:227], %[km] offset:2048\n\t" \
    "s_waitcnt lgkmcnt(3)\n\t" \
    "v_mfma_f32_16x16x32_bf16 v[248:251], v[228:231], v[204:207], v[248:251]\n\t" \
    "ds_read_b128 v[228:231], %[km] offset:2560\n\t" \
    "s_waitcnt lgkmcnt(3)\n\t" \
    "v_mfma_f32_16x16x32_bf16 v[248:251], v[232:235], v[208:211], v[248:251]\n\t" \
    "s_waitcnt lgkmcnt(2)\n\t" \
    "v_mfma_f32_16x16x32_bf16 v[248:251], v[236:239], v[212:215], v[248:251]\n\t" \
    "s_waitcnt lgkmcnt(1)\n\t" \
    "v_mfma_f32_16x16x32_bf16 v[248:251], v[224:227], v[216:219], v[248:251]\n\t" \
    "s_waitcnt lgkmcnt(0)\n\t" \
    "v_mfma_f32_16x16x32_bf16 v[248:251], v[228:231], v[220:223], v[248:251]\n\t" \
    "v_max3_f32 v224, v240, v241, v242\n\t" \
    "v_max3_f32 v225, v244, v245, v246\n\t" \
    "v_max3_f32 v224, v224, v243, v247\n\t" \
    "v_and_b32 v200, 0x3ff, %[qa]\n\t" \
    "s_nop 3\n\t" \
    "v_max3_f32 v224, v224, v248, v249\n\t" \
    "v_max3_f32 v225, v225, v250, v251\n\t" \
    "v_max_f32 v224, v224, v225\n\t" \
    "v_mov_b32 v225, v224\n\t" \
    "s_nop 1\n\t" \
    "v_permlane16_swap_b32 v224, v225\n\t" \
    "v_max_f32 v224, v224, v225\n\t" \
    "v_mov_b32 v225, v224\n\t" \
    "s_nop 1\n\t" \
    "v_permlane32_swap_b32 v224, v225\n\t" \
    "v_max_f32 v224, v224, v225\n\t" \
    "v_mul_f32 v224, %[scl], v224\n\t" \
    "v_fma_f32 v240, v240, %[scl], -v224\n\t" \
    "v_fma_f32 v241, v241, %[scl], -v224\n\t" \
    "v_exp_f32 v240, v240\n\t" \
    "v_fma_f32 v242, v242, %[scl], -v224\n\t" \
    "v_exp_f32 v241, v241\n\t" \
    "v_fma_f32 v243, v243, %[scl], -v224\n\t" \
    "v_exp_f32 v242, v242\n\t" \
    "v_fma_f32 v244, v244, %[scl], -v224\n\t" \
    "v_exp_f32 v243, v243\n\t" \
    "v_fma_f32 v245, v245, %[scl], -v224\n\t" \
    "v_exp_f32 v244, v244\n\t" \
    "v_fma_f32 v246, v246, %[scl], -v224\n\t" \
    "v_exp_f32 v245, v245\n\t" \
    "v_fma_f32 v247, v247, %[scl], -v224\n\t" \
    "v_exp_f32 v246, v246\n\t" \
    "v_fma_f32 v248, v248, %[scl], -v224\n\t" \
    "v_exp_f32 v247, v247\n\t" \
    "v_fma_f32 v249, v249, %[scl], -v224\n\t" \
    "v_exp_f32 v248, v248\n\t" \
    "v_fma_f32 v250, v250, %[scl], -v224\n\t" \
    "v_exp_f32 v249, v249\n\t" \
    "v_fma_f32 v251, v251, %[scl], -v224\n\t" \
    "v_exp_f32 v250, v250\n\t" \
    "v_exp_f32 v251, v251\n\t" \
    "v_add_f32 v224, v240, v241\n\t" \
    "v_add_f32 v225, v242, v243\n\t" \
    "v_add_f32 v226, v244, v245\n\t" \
    "v_add_f32 v227, v246, v247\n\t" \
    "v_add_f32 v232, v248, v249\n\t" \
    "v_add_f32 v233, v250, v251\n\t" \
    "v_add_f32 v224, v224, v225\n\t" \
    "v_add_f32 v226, v226, v227\n\t" \
    "v_add_f32 v232, v232, v233\n\t" \
    "v_add_f32 v224, v224, v226\n\t" \
    "v_fma_f32 v224, v232, 0.5, v224\n\t" \
    "v_mov_b32 v225, v224\n\t" \
    "s_nop 1\n\t" \
    "v_permlane16_swap_b32 v224, v225\n\t" \
    "v_add_f32 v224, v224, v225\n\t" \
    "v_mov_b32 v225, v224\n\t" \
    "s_nop 1\n\t" \
    "v_permlane32_swap_b32 v224, v225\n\t" \
    "v_add_f32 v224, v224, v225\n\t" \
    "v_rcp_f32 v224, v224\n\t" \
    "s_nop 0\n\t" \
    "v_mul_f32 v240, v240, v224\n\t" \
    "v_mul_f32 v241, v241, v224\n\t" \
    "v_mul_f32 v242, v242, v224\n\t" \
    "v_mul_f32 v243, v243, v224\n\t" \
    "v_mul_f32 v244, v244, v224\n\t" \
    "v_mul_f32 v245, v245, v224\n\t" \
    "v_mul_f32 v246, v246, v224\n\t" \
    "v_mul_f32 v247, v247, v224\n\t" \
    "v_mul_f32 v248, v248, v224\n\t" \
    "v_mul_f32 v249, v249, v224\n\t" \
    "v_mul_f32 v250, v250, v224\n\t" \
    "v_mul_f32 v251, v251, v224\n\t" \
    "v_cvt_pk_bf16_f32 v230, |v240|, |v241|\n\t" \
    "v_cvt_pk_bf16_f32 v231, |v242|, |v243|\n\t" \
    "v_cvt_pk_bf16_f32 v228, |v244|, |v245|\n\t" \
    "v_cvt_pk_bf16_f32 v229, |v246|, |v247|\n\t" \
    "v_cvt_pk_bf16_f32 v232, |v248|, |v249|\n\t" \
    "v_cvt_pk_bf16_f32 v233, |v250|, |v251|\n\t" \
    "buffer_store_dwordx4 v[228:231], v200, %[rr], %[so] offen sc1\n\t" \
    "buffer_store_dwordx2 v[232:233], %[pm], %[rr], %[so] offen sc1\n\t" \
    "s_nop 1\n\t"
#define ASM_RCRIT \
    "v_mov_b64 v[176:177], -1\n\t" \
    "v_mov_b64 v[178:179], -1\n\t" \
    "v_mov_b64 v[180:181], -1\n\t" \
    "v_mov_b64 v[182:183], -1\n\t" \
    "v_mov_b64 v[184:185], -1\n\t" \
    "v_mov_b64 v[186:187], -1\n\t" \
    "v_mov_b64 v[188:189], -1\n\t" \
    "v_mov_b64 v[190:191], -1\n\t" \
    "v_mov_b64 v[192:193], -1\n\t" \
    "v_mov_b64 v[194:195], -1\n\t" \
    ".Lfa%=:\n\t" \
    "s_mov_b32 %[a], 0\n\t" \
    "v_max_u32 v196, v176, v177\n\t" \
    "v_max3_u32 v196, v178, v179, v196\n\t" \
    "v_cmp_eq_u32 vcc, -1, v196\n\t" \
    "s_cbranch_vccz .Lga0_%=\n\t" \
    "buffer_load_dwordx4 v[176:179], %[ov], %[rs], %[sv] offen sc1\n\t" \
    "s_mov_b32 %[a], 1\n\t" \
    ".Lga0_%=:\n\t" \
    "v_max_u32 v196, v180, v181\n\t" \
    "v_max3_u32 v196, v182, v183, v196\n\t" \
    "v_cmp_eq_u32 vcc, -1, v196\n\t" \
    "s_cbranch_vccz .Lga1_%=\n\t" \
    "buffer_load_dwordx4 v[180:183], %[ov], %[rs], %[sv] offen offset:1024 sc1\n\t" \
    "s_mov_b32 %[a], 1\n\t" \
    ".Lga1_%=:\n\t" \
    "s_cmp_eq_u32 %[a], 0\n\t" \
    "s_cbranch_scc1 .Lha%=\n\t" \
    "s_waitcnt vmcnt(12)\n\t" \
    ".Lia%=:\n\t" \
    "v_max_u32 v196, v176, v177\n\t" \
    "v_max3_u32 v196, v178, v179, v196\n\t" \
    "v_max3_u32 v196, v180, v181, v196\n\t" \
    "v_max3_u32 v196, v182, v183, v196\n\t" \
    "v_cmp_eq_u32 vcc, -1, v196\n\t" \
    "s_cbranch_vccz .Lha%=\n\t" \
    "s_nop 7\n\t" \
    "v_max_u32 v196, v176, v177\n\t" \
    "v_max3_u32 v196, v178, v179, v196\n\t" \
    "v_max3_u32 v196, v180, v181, v196\n\t" \
    "v_max3_u32 v196, v182, v183, v196\n\t" \
    "v_cmp_eq_u32 vcc, -1, v196\n\t" \
    "s_cbranch_vccz .Lha%=\n\t" \
    "s_nop 7\n\t" \
    "v_max_u32 v196, v176, v177\n\t" \
    "v_max3_u32 v196, v178, v179, v196\n\t" \
    "v_max3_u32 v196, v180, v181, v196\n\t" \
    "v_max3_u32 v196, v182, v183, v196\n\t" \
    "v_cmp_eq_u32 vcc, -1, v196\n\t" \
    "s_cbranch_vccz .Lha%=\n\t" \
    "s_nop 7\n\t" \
    "v_max_u32 v196, v176, v177\n\t" \
    "v_max3_u32 v196, v178, v179, v196\n\t" \
    "v_max3_u32 v196, v180, v181, v196\n\t" \
    "v_max3_u32 v196, v182, v183, v196\n\t" \
    "v_cmp_eq_u32 vcc, -1, v196\n\t" \
    "s_cbranch_vccz .Lha%=\n\t" \
    "s_nop 7\n\t" \
    "s_sub_u32 %[n], %[n], 1\n\t" \
    "s_cmp_gt_i32 %[n], 0\n\t" \
    "s_cbranch_scc1 .Lfa%=\n\t" \
    ".Lha%=:\n\t" \
    ASM_ST3 \
    "buffer_load_dwordx4 v[184:187], %[o0], %[rs], %[sp] offen sc1\n\t" \
    "buffer_load_dwordx4 v[188:191], %[o0], %[rs], %[sp] offen offset:1024 sc1\n\t" \
    "buffer_load_dwordx4 v[192:195], %[o2], %[rs], %[sp] offen sc1\n\t" \
    "s_waitcnt vmcnt(0)\n\t" \
    "v_max_u32 v196, v184, v185\n\t" \
    "v_max3_u32 v196, v186, v187, v196\n\t" \
    "v_max3_u32 v196, v188, v189, v196\n\t" \
    "v_max3_u32 v196, v190, v191, v196\n\t" \
    "v_max3_u32 v196, v192, v193, v196\n\t" \
    "v_max3_u32 v196, v194, v195, v196\n\t" \
    "v_cmp_eq_u32 vcc, -1, v196\n\t" \
    "s_cbranch_vccz .Lhb%=\n\t" \
    ".Lfb%=:\n\t" \
    "s_mov_b32 %[a], 0\n\t" \
    "v_max_u32 v196, v184, v185\n\t" \
    "v_max3_u32 v196, v186, v187, v196\n\t" \
    "v_cmp_eq_u32 vcc, -1, v196\n\t" \
    "s_cbranch_vccz .Lgb0_%=\n\t" \
    "buffer_load_dwordx4 v[184:187], %[o0], %[rs], %[sp] offen sc1\n\t" \
    "s_mov_b32 %[a], 1\n\t" \
    ".Lgb0_%=:\n\t" \
    "v_max_u32 v196, v188, v189\n\t" \
    "v_max3_u32 v196, v190, v191, v196\n\t" \
    "v_cmp_eq_u32 vcc, -1, v196\n\t" \
    "s_cbranch_vccz .Lgb1_%=\n\t" \
    "buffer_load_dwordx4 v[188:191], %[o0], %[rs], %[sp] offen offset:1024 sc1\n\t" \
    "s_mov_b32 %[a], 1\n\t" \
    ".Lgb1_%=:\n\t" \
    "v_max_u32 v196, v192, v193\n\t" \
    "v_max3_u32 v196, v194, v195, v196\n\t" \
    "v_cmp_eq_u32 vcc, -1, v196\n\t" \
    "s_cbranch_vccz .Lgb2_%=\n\t" \
    "buffer_load_dwordx4 v[192:195], %[o2], %[rs], %[sp] offen sc1\n\t" \
    "s_mov_b32 %[a], 1\n\t" \
    ".Lgb2_%=:\n\t" \
    "s_cmp_eq_u32 %[a], 0\n\t" \
    "s_cbranch_scc1 .Lhb%=\n\t" \
    "s_waitcnt vmcnt(12)\n\t" \
    ".Lib%=:\n\t" \
    "v_max_u32 v196, v184, v185\n\t" \
    "v_max3_u32 v196, v186, v187, v196\n\t" \
    "v_max3_u32 v196, v188, v189, v196\n\t" \
    "v_max3_u32 v196, v190, v191, v196\n\t" \
    "v_max3_u32 v196, v192, v193, v196\n\t" \
    "v_max3_u32 v196, v194, v195, v196\n\t" \
    "v_cmp_eq_u32 vcc, -1, v196\n\t" \
    "s_cbranch_vccz .Lhb%=\n\t" \
    "s_nop 7\n\t" \
    "v_max_u32 v196, v184, v185\n\t" \
    "v_max3_u32 v196, v186, v187, v196\n\t" \
    "v_max3_u32 v196, v188, v189, v196\n\t" \
    "v_max3_u32 v196, v190, v191, v196\n\t" \
    "v_max3_u32 v196, v192, v193, v196\n\t" \
    "v_max3_u32 v196, v194, v195, v196\n\t" \
    "v_cmp_eq_u32 vcc, -1, v196\n\t" \
    "s_cbranch_vccz .Lhb%=\n\t" \
    "s_nop 7\n\t" \
    "v_max_u32 v196, v184, v185\n\t" \
    "v_max3_u32 v196, v186, v187, v196\n\t" \
    "v_max3_u32 v196, v188, v189, v196\n\t" \
    "v_max3_u32 v196, v190, v191, v196\n\t" \
    "v_max3_u32 v196, v192, v193, v196\n\t" \
    "v_max3_u32 v196, v194, v195, v196\n\t" \
    "v_cmp_eq_u32 vcc, -1, v196\n\t" \
    "s_cbranch_vccz .Lhb%=\n\t" \
    "s_nop 7\n\t" \
    "v_max_u32 v196, v184, v185\n\t" \
    "v_max3_u32 v196, v186, v187, v196\n\t" \
    "v_max3_u32 v196, v188, v189, v196\n\t" \
    "v_max3_u32 v196, v190, v191, v196\n\t" \
    "v_max3_u32 v196, v192, v193, v196\n\t" \
    "v_max3_u32 v196, v194, v195, v196\n\t" \
    "v_cmp_eq_u32 vcc, -1, v196\n\t" \
    "s_cbranch_vccz .Lhb%=\n\t" \
    "s_nop 7\n\t" \
    "s_sub_u32 %[n], %[n], 1\n\t" \
    "s_cmp_gt_i32 %[n], 0\n\t" \
    "s_cbranch_scc1 .Lfb%=\n\t" \
    ".Lhb%=:\n\t" \
    ASM_ST2 \
    "v_mfma_f32_16x16x32_bf16 v[160:163], v[192:195], v[176:179], 0\n\t" \
    "v_mfma_f32_16x16x32_bf16 v[164:167], v[192:195], v[180:183], 0\n\t" \
    "v_smfmac_f32_16x16x64_bf16 v[160:163], v[184:187], v[128:135], %[sidx]\n\t" \
    "v_smfmac_f32_16x16x64_bf16 v[164:167], v[184:187], v[144:151], %[sidx]\n\t" \
    "v_smfmac_f32_16x16x64_bf16 v[160:163], v[188:191], v[136:143], %[sidx]\n\t" \
    "v_smfmac_f32_16x16x64_bf16 v[164:167], v[188:191], v[152:159], %[sidx]\n\t" \
    "s_cmp_eq_u32 %[pub], 0\n\t" \
    "s_nop 7\n\t" \
    "v_permlane32_swap_b32 v160, v164\n\t" \
    "v_permlane32_swap_b32 v161, v165\n\t" \
    "v_permlane32_swap_b32 v162, v166\n\t" \
    "v_permlane32_swap_b32 v163, v167\n\t" \
    "v_add_f32 v160, v160, v164\n\t" \
    "v_add_f32 v161, v161, v165\n\t" \
    "ds_write2_b32 %[rw], v160, v161 offset1:80\n\t" \
    "v_add_f32 v162, v162, v166\n\t" \
    "v_add_f32 v163, v163, v167\n\t" \
    "ds_write2_b32 %[rw], v162, v163 offset0:160 offset1:240\n\t" \
    "s_waitcnt lgkmcnt(0)\n\t" \
    "s_barrier\n\t" ASM_ST \
    "s_cbranch_scc1 .Lre%=\n\t" \
    "ds_read_b128 v[200:203], %[rd] offset:0\n\t" \
    "ds_read_b128 v[204:207], %[rd] offset:16\n\t" \
    "s_waitcnt lgkmcnt(1)\n\t" \
    "v_pk_add_f32 v[200:201], v[200:201], v[202:203]\n\t" \
    "s_waitcnt lgkmcnt(0)\n\t" \
    "v_pk_add_f32 v[204:205], v[204:205], v[206:207]\n\t" \
    "v_pk_add_f32 v[200:201], v[200:201], v[204:205]\n\t" \
    "s_nop 1\n\t" \
    "v_permlane32_swap_b32 v200, v201\n\t" \
    "v_add_f32 v202, v200, v201\n\t" \
    "v_add_f32 v203, v200, v201\n\t" \
    "s_nop 1\n\t" \
    "v_permlane32_swap_b32 v202, v203\n\t" \
    "v_cvt_pk_bf16_f32 v200, v202, v203\n\t" \
    "v_min_u32 v200, -2, v200\n\t" \
    "buffer_store_dword v200, %[oo], %[rs], %[so] offen sc1\n\t" \
    ".Lre%=:\n\t"
#define ASM_RX \
    "s_cmp_eq_u32 %[fin], 0\n\t" \
    "s_cbranch_scc1 .Lx0%=\n\t" \
    "buffer_load_dwordx2 v[250:251], %[xo], %[rx], %[sx] offen sc1\n\t" \
    ".Lx0%=:\n\t" \
    "buffer_load_dword v248, %[fo], %[rs], %[fs] offen sc1\n\t" \
    "buffer_load_dwordx4 v[200:203], %[l16], %[rs], %[s0] offen offset:0 sc1\n\t" \
    "buffer_load_dwordx4 v[204:207], %[l16], %[rs], %[s0] offen offset:1024 sc1\n\t" \
    "buffer_load_dwordx4 v[208:211], %[l16], %[rs], %[s0] offen offset:2048 sc1\n\t" \
    "buffer_load_dwordx4 v[212:215], %[l16], %[rs], %[s0] offen offset:3072 sc1\n\t" \
    "buffer_load_dwordx4 v[216:219], %[l16], %[rs], %[s1] offen offset:0 sc1\n\t" \
    "buffer_load_dwordx4 v[220:223], %[l16], %[rs], %[s1] offen offset:1024 sc1\n\t" \
    "buffer_load_dwordx4 v[224:227], %[l16], %[rs], %[s1] offen offset:2048 sc1\n\t" \
    "buffer_load_dwordx4 v[228:231], %[l16], %[rs], %[s1] offen offset:3072 sc1\n\t" \
    "v_mov_b64 v[96:97], v[128:129]\n\t" \
    "v_mov_b64 v[98:99], v[132:133]\n\t" \
    "v_mov_b64 v[100:101], v[136:137]\n\t" \
    "v_mov_b64 v[102:103], v[140:141]\n\t" \
    "v_mov_b64 v[104:105], v[144:145]\n\t" \
    "v_mov_b64 v[106:107], v[148:149]\n\t" \
    "v_mov_b64 v[108:109], v[152:153]\n\t" \
    "v_mov_b64 v[110:111], v[156:157]\n\t" \
    "s_movk_i32 %[n], 0x200\n\t" \
    "s_nop 1\n\t" \
    "v_permlane16_swap_b32 v96, v98\n\t" \
    "v_permlane16_swap_b32 v97, v99\n\t" \
    "v_permlane16_swap_b32 v100, v102\n\t" \
    "v_permlane16_swap_b32 v101, v103\n\t" \
    "v_permlane16_swap_b32 v104, v106\n\t" \
    "v_permlane16_swap_b32 v105, v107\n\t" \
    "v_permlane16_swap_b32 v108, v110\n\t" \
    "v_permlane16_swap_b32 v109, v111\n\t" \
    "s_waitcnt vmcnt(7)\n\t" \
    "v_max_u32 v196, v200, v201\n\t" \
    "v_max3_u32 v196, v202, v203, v196\n\t" \
    "s_waitcnt vmcnt(6)\n\t" \
    "v_max3_u32 v196, v204, v205, v196\n\t" \
    "v_max3_u32 v196, v206, v207, v196\n\t" \
    "s_waitcnt vmcnt(5)\n\t" \
    "v_max3_u32 v196, v208, v209, v196\n\t" \
    "v_max3_u32 v196, v210, v211, v196\n\t" \
    "s_waitcnt vmcnt(4)\n\t" \
    "v_max3_u32 v196, v212, v213, v196\n\t" \
    "v_max3_u32 v196, v214, v215, v196\n\t" \
    "s_waitcnt vmcnt(3)\n\t" \
    "v_max3_u32 v196, v216, v217, v196\n\t" \
    "v_max3_u32 v196, v218, v219, v196\n\t" \
    "s_waitcnt vmcnt(2)\n\t" \
    "v_max3_u32 v196, v220, v221, v196\n\t" \
    "v_max3_u32 v196, v222, v223, v196\n\t" \
    "s_waitcnt vmcnt(1)\n\t" \
    "v_max3_u32 v196, v224, v225, v196\n\t" \
    "v_max3_u32 v196, v226, v227, v196\n\t" \
    "s_waitcnt vmcnt(0)\n\t" \
    "v_max3_u32 v196, v228, v229, v196\n\t" \
    "v_max3_u32 v196, v230, v231, v196\n\t" \
    "v_cmp_eq_u32 vcc, -1, v196\n\t" \
    "s_cbranch_vccz .Lxd%=\n\t" \
    ".Lxl%=:\n\t" \
    "s_mov_b32 %[a], 0\n\t" \
    "v_max_u32 v196, v200, v201\n\t" \
    "v_max3_u32 v196, v202, v203, v196\n\t" \
    "v_cmp_eq_u32 vcc, -1, v196\n\t" \
    "s_cbranch_vccz .Lxs0_%=\n\t" \
    "buffer_load_dwordx4 v[200:203], %[l16], %[rs], %[s0] offen offset:0 sc1\n\t" \
    "s_mov_b32 %[a], 1\n\t" \
    ".Lxs0_%=:\n\t" \
    "v_max_u32 v196, v204, v205\n\t" \
    "v_max3_u32 v196, v206, v207, v196\n\t" \
    "v_cmp_eq_u32 vcc, -1, v196\n\t" \
    "s_cbranch_vccz .Lxs1_%=\n\t" \
    "buffer_load_dwordx4 v[204:207], %[l16], %[rs], %[s0] offen offset:1024 sc1\n\t" \
    "s_mov_b32 %[a], 1\n\t" \
    ".Lxs1_%=:\n\t" \
    "v_max_u32 v196, v208, v209\n\t" \
    "v_max3_u32 v196, v210, v211, v196\n\t" \
    "v_cmp_eq_u32 vcc, -1, v196\n\t" \
    "s_cbranch_vccz .Lxs2_%=\n\t" \
    "buffer_load_dwordx4 v[208:211], %[l16], %[rs], %[s0] offen offset:2048 sc1\n\t" \
    "s_mov_b32 %[a], 1\n\t" \
    ".Lxs2_%=:\n\t" \
    "v_max_u32 v196, v212, v213\n\t" \
    "v_max3_u32 v196, v214, v215, v196\n\t" \
    "v_cmp_eq_u32 vcc, -1, v196\n\t" \
    "s_cbranch_vccz .Lxs3_%=\n\t" \
    "buffer_load_dwordx4 v[212:215], %[l16], %[rs], %[s0] offen offset:3072 sc1\n\t" \
    "s_mov_b32 %[a], 1\n\t" \
    ".Lxs3_%=:\n\t" \
    "v_max_u32 v196, v216, v217\n\t" \
    "v_max3_u32 v196, v218, v219, v196\n\t" \
    "v_cmp_eq_u32 vcc, -1, v196\n\t" \
    "s_cbranch_vccz .Lxs4_%=\n\t" \
    "buffer_load_dwordx4 v[216:219], %[l16], %[rs], %[s1] offen offset:0 sc1\n\t" \
    "s_mov_b32 %[a], 1\n\t" \
    ".Lxs4_%=:\n\t" \
    "v_max_u32 v196, v220, v221\n\t" \
    "v_max3_u32 v196, v222, v223, v196\n\t" \
    "v_cmp_eq_u32 vcc, -1, v196\n\t" \
    "s_cbranch_vccz .Lxs5_%=\n\t" \
    "buffer_load_dwordx4 v[220:223], %[l16], %[rs], %[s1] offen offset:1024 sc1\n\t" \
    "s_mov_b32 %[a], 1\n\t" \
    ".Lxs5_%=:\n\t" \
    "v_max_u32 v196, v224, v225\n\t" \
    "v_max3_u32 v196, v226, v227, v196\n\t" \
    "v_cmp_eq_u32 vcc, -1, v196\n\t" \
    "s_cbranch_vccz .Lxs6_%=\n\t" \
    "buffer_load_dwordx4 v[224:227], %[l16], %[rs], %[s1] offen offset:2048 sc1\n\t" \
    "s_mov_b32 %[a], 1\n\t" \
    ".Lxs6_%=:\n\t" \
    "v_max_u32 v196, v228, v229\n\t" \
    "v_max3_u32 v196, v230, v231, v196\n\t" \
    "v_cmp_eq_u32 vcc, -1, v196\n\t" \
    "s_cbranch_vccz .Lxs7_%=\n\t" \
    "buffer_load_dwordx4 v[228:231], %[l16], %[rs], %[s1] offen offset:3072 sc1\n\t" \
    "s_mov_b32 %[a], 1\n\t" \
    ".Lxs7_%=:\n\t" \
    "s_cmp_eq_u32 %[a], 0\n\t" \
    "s_cbranch_scc1 .Lxd%=\n\t" \
    "s_waitcnt vmcnt(0)\n\t" \
    "s_sub_u32 %[n], %[n], 1\n\t" \
    "s_cmp_lg_u32 %[n], 0\n\t" \
    "s_cbranch_scc1 .Lxl%=\n\t" \
    "s_branch .Lxe%=\n\t" \
    ".Lxd%=:\n\t" \
    ASM_ST2 \
    "v_mfma_f32_16x16x32_bf16 v[120:123], v[128:131], v[200:203], 0\n\t" \
    "v_mfma_f32_16x16x32_bf16 v[124:127], v[144:147], v[200:203], 0\n\t" \
    "v_mfma_f32_16x16x32_bf16 v[120:123], v[132:135], v[204:207], v[120:123]\n\t" \
    "v_mfma_f32_16x16x32_bf16 v[124:127], v[148:151], v[204:207], v[124:127]\n\t" \
    "v_mfma_f32_16x16x32_bf16 v[120:123], v[136:139], v[208:211], v[120:123]\n\t" \
    "v_mfma_f32_16x16x32_bf16 v[124:127], v[152:155], v[208:211], v[124:127]\n\t" \
    "v_mfma_f32_16x16x32_bf16 v[120:123], v[140:143], v[212:215], v[120:123]\n\t" \
    "v_mfma_f32_16x16x32_bf16 v[124:127], v[156:159], v[212:215], v[124:127]\n\t" \
    "v_mfma_f32_16x16x32_bf16 v[112:115], v[96:99], v[220:223], 0\n\t" \
    "v_mfma_f32_16x16x32_bf16 v[116:119], v[104:107], v[220:223], 0\n\t" \
    "v_mfma_f32_16x16x32_bf16 v[120:123], v[176:179], v[216:219], v[120:123]\n\t" \
    "v_mfma_f32_16x16x32_bf16 v[124:127], v[180:183], v[216:219], v[124:127]\n\t" \
    "v_mfma_f32_16x16x32_bf16 v[112:115], v[100:103], v[224:227], v[112:115]\n\t" \
    "v_mfma_f32_16x16x32_bf16 v[116:119], v[108:111], v[224:227], v[116:119]\n\t" \
    "v_mfma_f32_16x16x32_bf16 v[112:115], v[176:179], v[228:231], v[112:115]\n\t" \
    "v_mfma_f32_16x16x32_bf16 v[116:119], v[180:183], v[228:231], v[116:119]\n\t" \
    "s_nop 1\n\t" \
    "ds_write_b128 %[pw], v[120:123] offset:16384\n\t" \
    "ds_write_b128 %[pw], v[124:127] offset:24576\n\t" \
    "s_nop 3\n\t" \
    "ds_write_b128 %[pw], v[112:115] offset:0\n\t" \
    "ds_write_b128 %[pw], v[116:119] offset:8192\n\t" \
    "v_cmp_ge_u32 vcc, %[tn], v248\n\t" \
    "s_mov_b32 %[a], 0\n\t" \
    "s_cbranch_vccnz .Lxv%=\n\t" \
    "buffer_load_dwordx4 v[128:131], %[l16], %[rp], %[sv0] offen offset:0 sc1\n\t" \
    "buffer_load_dwordx4 v[132:135], %[l16], %[rp], %[sv0] offen offset:1024 sc1\n\t" \
    "buffer_load_dwordx4 v[136:139], %[l16], %[rp], %[sv0] offen offset:2048 sc1\n\t" \
    "buffer_load_dwordx4 v[140:143], %[l16], %[rp], %[sv0] offen offset:3072 sc1\n\t" \
    "buffer_load_dwordx4 v[144:147], %[l16], %[rp], %[sv1] offen offset:0 sc1\n\t" \
    "buffer_load_dwordx4 v[148:151], %[l16], %[rp], %[sv1] offen offset:1024 sc1\n\t" \
    "buffer_load_dwordx4 v[152:155], %[l16], %[rp], %[sv1] offen offset:2048 sc1\n\t" \
    "buffer_load_dwordx4 v[156:159], %[l16], %[rp], %[sv1] offen offset:3072 sc1\n\t" \
    "s_mov_b32 %[a], 1\n\t" \
    ".Lxv%=:\n\t" \
    "s_cmp_eq_u32 %[fin], 0\n\t" \
    "s_waitcnt lgkmcnt(0)\n\t" \
    "s_barrier\n\t" ASM_ST \
    "s_cbranch_scc1 .Lxe%=\n\t" \
    "ds_read_b128 v[200:203], %[pr] offset:0\n\t" \
    "ds_read_b128 v[204:207], %[pr] offset:1024\n\t" \
    "ds_read_b128 v[208:211], %[pr] offset:2048\n\t" \
    "ds_read_b128 v[212:215], %[pr] offset:3072\n\t" \
    "ds_read_b128 v[216:219], %[pr] offset:4096\n\t" \
    "ds_read_b128 v[220:223], %[pr] offset:5120\n\t" \
    "ds_read_b128 v[224:227], %[pr] offset:6144\n\t" \
    "ds_read_b128 v[228:231], %[pr] offset:7168\n\t" \
    "v_lshlrev_b32 v240, 16, v250\n\t" \
    "v_and_b32 v241, 0xffff0000, v250\n\t" \
    "v_lshlrev_b32 v242, 16, v251\n\t" \
    "v_and_b32 v243, 0xffff0000, v251\n\t" \
    "s_waitcnt lgkmcnt(6)\n\t" \
    "v_pk_add_f32 v[200:201], v[200:201], v[204:205]\n\t" \
    "v_pk_add_f32 v[202:203], v[202:203], v[206:207]\n\t" \
    "s_waitcnt lgkmcnt(4)\n\t" \
    "v_pk_add_f32 v[208:209], v[208:209], v[212:213]\n\t" \
    "v_pk_add_f32 v[210:211], v[210:211], v[214:215]\n\t" \
    "v_pk_add_f32 v[200:201], v[200:201], v[208:209]\n\t" \
    "v_pk_add_f32 v[202:203], v[202:203], v[210:211]\n\t" \
    "s_waitcnt lgkmcnt(2)\n\t" \
    "v_pk_add_f32 v[216:217], v[216:217], v[220:221]\n\t" \
    "v_pk_add_f32 v[218:219], v[218:219], v[222:223]\n\t" \
    "s_waitcnt lgkmcnt(0)\n\t" \
    "v_pk_add_f32 v[224:225], v[224:225], v[228:229]\n\t" \
    "v_pk_add_f32 v[226:227], v[226:227], v[230:231]\n\t" \
    "v_pk_add_f32 v[216:217], v[216:217], v[224:225]\n\t" \
    "v_pk_add_f32 v[218:219], v[218:219], v[226:227]\n\t" \
    "v_pk_add_f32 v[200:201], v[200:201], v[216:217]\n\t" \
    "v_pk_add_f32 v[202:203], v[202:203], v[218:219]\n\t" \
    "v_pk_add_f32 v[200:201], v[200:201], v[240:241]\n\t" \
    "v_pk_add_f32 v[202:203], v[202:203], v[242:243]\n\t" \
    "v_cvt_pk_bf16_f32 v200, v200, v201\n\t" \
    "v_cvt_pk_bf16_f32 v201, v202, v203\n\t" \
    "v_min_u32 v200, -2, v200\n\t" \
    "v_min_u32 v201, -2, v201\n\t" \
    "buffer_store_dwordx2 v[200:201], %[xo], %[rd], %[sa] offen sc1\n\t" \
    ".Lxe%=:\n\t"
#define ASM_W4_M \
    ".Lc7_%=:\n\t" \
    "s_waitcnt vmcnt(0)\n\t" \
    "v_max_u32 v212, v188, v189\n\t" \
    "v_max3_u32 v212, v190, v191, v212\n\t" \
    "v_cmp_eq_u32 vcc, -1, v212\n\t" \
    "s_cbranch_vccnz .Ls7_%=\n\t" \
    ".Lk7_%=:\n\t" \
    ASM_ST2 \
    "buffer_load_dwordx4 v[184:187], %[l16], %[rs], %[s1] offen offset:2048 sc1\n\t" \
    "buffer_load_dwordx4 v[180:183], %[l16], %[rs], %[s1] offen offset:1024 sc1\n\t" \
    "buffer_load_dwordx4 v[176:179], %[l16], %[rs], %[s1] offen offset:0 sc1\n\t" \
    "buffer_load_dwordx4 v[172:175], %[l16], %[rs], %[s0] offen offset:3072 sc1\n\t" \
    "buffer_load_dwordx4 v[168:171], %[l16], %[rs], %[s0] offen offset:2048 sc1\n\t" \
    "buffer_load_dwordx4 v[164:167], %[l16], %[rs], %[s0] offen offset:1024 sc1\n\t" \
    "buffer_load_dwordx4 v[160:163], %[l16], %[rs], %[s0] offen offset:0 sc1\n\t" \
    "v_mfma_f32_16x16x32_bf16 v[208:211], v[156:159], v[188:191], 0\n\t" \
    "v_mfma_f32_16x16x32_bf16 v[204:207], v[124:127], v[188:191], 0\n\t" \
    "v_mfma_f32_16x16x32_bf16 v[200:203], v[92:95], v[188:191], 0\n\t" \
    "v_mfma_f32_16x16x32_bf16 v[196:199], v[60:63], v[188:191], 0\n\t" \
    "v_mfma_f32_16x16x32_bf16 v[192:195], v[28:31], v[188:191], 0\n\t" \
    ".Lc6_%=:\n\t" \
    "s_waitcnt vmcnt(6)\n\t" \
    "v_max_u32 v212, v184, v185\n\t" \
    "v_max3_u32 v212, v186, v187, v212\n\t" \
    "v_cmp_eq_u32 vcc, -1, v212\n\t" \
    "s_cbranch_vccnz .Ls6_%=\n\t" \
    ".Lk6_%=:\n\t" \
    ".Lc5_%=:\n\t" \
    "s_waitcnt vmcnt(5)\n\t" \
    "v_max_u32 v212, v180, v181\n\t" \
    "v_max3_u32 v212, v182, v183, v212\n\t" \
    "v_cmp_eq_u32 vcc, -1, v212\n\t" \
    "s_cbranch_vccnz .Ls5_%=\n\t" \
    ".Lk5_%=:\n\t" \
    "v_mfma_f32_16x16x32_bf16 v[192:195], v[24:27], v[184:187], v[192:195]\n\t" \
    "v_mfma_f32_16x16x32_bf16 v[192:195], v[20:23], v[180:183], v[192:195]\n\t" \
    "v_mfma_f32_16x16x32_bf16 v[196:199], v[56:59], v[184:187], v[196:199]\n\t" \
    "v_mfma_f32_16x16x32_bf16 v[196:199], v[52:55], v[180:183], v[196:199]\n\t" \
    "v_mfma_f32_16x16x32_bf16 v[200:203], v[88:91], v[184:187], v[200:203]\n\t" \
    "v_mfma_f32_16x16x32_bf16 v[200:203], v[84:87], v[180:183], v[200:203]\n\t" \
    "v_mfma_f32_16x16x32_bf16 v[204:207], v[120:123], v[184:187], v[204:207]\n\t" \
    "v_mfma_f32_16x16x32_bf16 v[204:207], v[116:119], v[180:183], v[204:207]\n\t" \
    "v_mfma_f32_16x16x32_bf16 v[208:211], v[152:155], v[184:187], v[208:211]\n\t" \
    "v_mfma_f32_16x16x32_bf16 v[208:211], v[148:151], v[180:183], v[208:211]\n\t" \
    ".Lc4_%=:\n\t" \
    "s_waitcnt vmcnt(4)\n\t" \
    "v_max_u32 v212, v176, v177\n\t" \
    "v_max3_u32 v212, v178, v179, v212\n\t" \
    "v_cmp_eq_u32 vcc, -1, v212\n\t" \
    "s_cbranch_vccnz .Ls4_%=\n\t" \
    ".Lk4_%=:\n\t" \
    ".Lc3_%=:\n\t" \
    "s_waitcnt vmcnt(3)\n\t" \
    "v_max_u32 v212, v172, v173\n\t" \
    "v_max3_u32 v212, v174, v175, v212\n\t" \
    "v_cmp_eq_u32 vcc, -1, v212\n\t" \
    "s_cbranch_vccnz .Ls3_%=\n\t" \
    ".Lk3_%=:\n\t" \
    "v_mfma_f32_16x16x32_bf16 v[208:211], v[144:147], v[176:179], v[208:211]\n\t" \
    "v_mfma_f32_16x16x32_bf16 v[208:211], v[140:143], v[172:175], v[208:211]\n\t" \
    "v_mfma_f32_16x16x32_bf16 v[204:207], v[112:115], v[176:179], v[204:207]\n\t" \
    "v_mfma_f32_16x16x32_bf16 v[204:207], v[108:111], v[172:175], v[204:207]\n\t" \
    "v_mfma_f32_16x16x32_bf16 v[200:203], v[80:83], v[176:179], v[200:203]\n\t" \
    "v_mfma_f32_16x16x32_bf16 v[200:203], v[76:79], v[172:175], v[200:203]\n\t" \
    "v_mfma_f32_16x16x32_bf16 v[196:199], v[48:51], v[176:179], v[196:199]\n\t" \
    "v_mfma_f32_16x16x32_bf16 v[196:199], v[44:47], v[172:175], v[196:199]\n\t" \
    "v_mfma_f32_16x16x32_bf16 v[192:195], v[16:19], v[176:179], v[192:195]\n\t" \
    "v_mfma_f32_16x16x32_bf16 v[192:195], v[12:15], v[172:175], v[192:195]\n\t" \
    ".Lc2_%=:\n\t" \
    "s_waitcnt vmcnt(2)\n\t" \
    "v_max_u32 v212, v168, v169\n\t" \
    "v_max3_u32 v212, v170, v171, v212\n\t" \
    "v_cmp_eq_u32 vcc, -1, v212\n\t" \
    "s_cbranch_vccnz .Ls2_%=\n\t" \
    ".Lk2_%=:\n\t" \
    "buffer_load_dwordx4 v[188:191], %[l16], %[rs], %[n1] offen offset:3072 sc1\n\t" \
    ".Lc1_%=:\n\t" \
    "s_waitcnt vmcnt(2)\n\t" \
    "v_max_u32 v212, v164, v165\n\t" \
    "v_max3_u32 v212, v166, v167, v212\n\t" \
    "v_cmp_eq_u32 vcc, -1, v212\n\t" \
    "s_cbranch_vccnz .Ls1_%=\n\t" \
    ".Lk1_%=:\n\t" \
    ".Lc0_%=:\n\t" \
    "s_waitcnt vmcnt(1)\n\t" \
    "v_max_u32 v212, v160, v161\n\t" \
    "v_max3_u32 v212, v162, v163, v212\n\t" \
    "v_cmp_eq_u32 vcc, -1, v212\n\t" \
    "s_cbranch_vccnz .Ls0_%=\n\t" \
    ".Lk0_%=:\n\t" \
    "v_mfma_f32_16x16x32_bf16 v[192:195], v[8:11], v[168:171], v[192:195]\n\t" \
    "v_mfma_f32_16x16x32_bf16 v[192:195], v[4:7], v[164:167], v[192:195]\n\t" \
    "v_mfma_f32_16x16x32_bf16 v[192:195], v[0:3], v[160:163], v[192:195]\n\t" \
    "v_mfma_f32_16x16x32_bf16 v[196:199], v[40:43], v[168:171], v[196:199]\n\t" \
    "v_mfma_f32_16x16x32_bf16 v[196:199], v[36:39], v[164:167], v[196:199]\n\t" \
    "v_mfma_f32_16x16x32_bf16 v[196:199], v[32:35], v[160:163], v[196:199]\n\t" \
    "v_mfma_f32_16x16x32_bf16 v[200:203], v[72:75], v[168:171], v[200:203]\n\t" \
    "v_mfma_f32_16x16x32_bf16 v[200:203], v[68:71], v[164:167], v[200:203]\n\t" \
    "v_mfma_f32_16x16x32_bf16 v[200:203], v[64:67], v[160:163], v[200:203]\n\t" \
    "v_mfma_f32_16x16x32_bf16 v[204:207], v[104:107], v[168:171], v[204:207]\n\t" \
    "v_mfma_f32_16x16x32_bf16 v[204:207], v[100:103], v[164:167], v[204:207]\n\t" \
    "v_mfma_f32_16x16x32_bf16 v[204:207], v[96:99], v[160:163], v[204:207]\n\t" \
    "v_mfma_f32_16x16x32_bf16 v[208:211], v[136:139], v[168:171], v[208:211]\n\t" \
    "v_mfma_f32_16x16x32_bf16 v[208:211], v[132:135], v[164:167], v[208:211]\n\t" \
    "v_mfma_f32_16x16x32_bf16 v[208:211], v[128:131], v[160:163], v[208:211]\n\t" \
    "v_mov_b32 v212, %[un]\n\t" \
    "ds_write_b128 %[ld], v[192:195] offset:0\n\t" \
    "ds_write_b128 %[ld], v[196:199] offset:6144\n\t" \
    "ds_write_b128 %[ld], v[200:203] offset:12288\n\t" \
    "ds_write_b128 %[ld], v[204:207] offset:18432\n\t" \
    "s_nop 2\n\t" \
    "ds_write_b128 %[ld], v[208:211] offset:24576\n\t" \
    "ds_write_b32 %[fl], v212\n\t" \
    "s_branch .Lme%=\n\t" \
    ".Ls7_%=:\n\t" \
    "s_cmp_eq_u32 %[n], 0\n\t" \
    "s_cbranch_scc1 .Lk7_%=\n\t" \
    "s_sub_u32 %[n], %[n], 1\n\t" \
    "s_sleep 1\n\t" \
    "buffer_load_dwordx4 v[188:191], %[l16], %[rs], %[s1] offen offset:3072 sc1\n\t" \
    "s_branch .Lc7_%=\n\t" \
    ".Ls6_%=:\n\t" \
    "s_cmp_eq_u32 %[n], 0\n\t" \
    "s_cbranch_scc1 .Lk6_%=\n\t" \
    "s_sub_u32 %[n], %[n], 1\n\t" \
    "buffer_load_dwordx4 v[184:187], %[l16], %[rs], %[s1] offen offset:2048 sc1\n\t" \
    "s_waitcnt vmcnt(0)\n\t" \
    "s_branch .Lc6_%=\n\t" \
    ".Ls5_%=:\n\t" \
    "s_cmp_eq_u32 %[n], 0\n\t" \
    "s_cbranch_scc1 .Lk5_%=\n\t" \
    "s_sub_u32 %[n], %[n], 1\n\t" \
    "buffer_load_dwordx4 v[180:183], %[l16], %[rs], %[s1] offen offset:1024 sc1\n\t" \
    "s_waitcnt vmcnt(0)\n\t" \
    "s_branch .Lc5_%=\n\t" \
    ".Ls4_%=:\n\t" \
    "s_cmp_eq_u32 %[n], 0\n\t" \
    "s_cbranch_scc1 .Lk4_%=\n\t" \
    "s_sub_u32 %[n], %[n], 1\n\t" \
    "buffer_load_dwordx4 v[176:179], %[l16], %[rs], %[s1] offen offset:0 sc1\n\t" \
    "s_waitcnt vmcnt(0)\n\t" \
    "s_branch .Lc4_%=\n\t" \
    ".Ls3_%=:\n\t" \
    "s_cmp_eq_u32 %[n], 0\n\t" \
    "s_cbranch_scc1 .Lk3_%=\n\t" \
    "s_sub_u32 %[n], %[n], 1\n\t" \
    "buffer_load_dwordx4 v[172:175], %[l16], %[rs], %[s0] offen offset:3072 sc1\n\t" \
    "s_waitcnt vmcnt(0)\n\t" \
    "s_branch .Lc3_%=\n\t" \
    ".Ls2_%=:\n\t" \
    "s_cmp_eq_u32 %[n], 0\n\t" \
    "s_cbranch_scc1 .Lk2_%=\n\t" \
    "s_sub_u32 %[n], %[n], 1\n\t" \
    "buffer_load_dwordx4 v[168:171], %[l16], %[rs], %[s0] offen offset:2048 sc1\n\t" \
    "s_waitcnt vmcnt(0)\n\t" \
    "s_branch .Lc2_%=\n\t" \
    ".Ls1_%=:\n\t" \
    "s_cmp_eq_u32 %[n], 0\n\t" \
    "s_cbranch_scc1 .Lk1_%=\n\t" \
    "s_sub_u32 %[n], %[n], 1\n\t" \
    "buffer_load_dwordx4 v[164:167], %[l16], %[rs], %[s0] offen offset:1024 sc1\n\t" \
    "s_waitcnt vmcnt(0)\n\t" \
    "s_branch .Lc1_%=\n\t" \
    ".Ls0_%=:\n\t" \
    "s_cmp_eq_u32 %[n], 0\n\t" \
    "s_cbranch_scc1 .Lk0_%=\n\t" \
    "s_sub_u32 %[n], %[n], 1\n\t" \
    "buffer_load_dwordx4 v[160:163], %[l16], %[rs], %[s0] offen offset:0 sc1\n\t" \
    "s_waitcnt vmcnt(0)\n\t" \
    "s_branch .Lc0_%=\n\t" \
    ".Lme%=:\n\t"
#define ASM_W4F_0 \
    "s_branch .Lf0%=\n\t" \
    ".Lfr%=:\n\t" \
    "s_cmp_eq_u32 %[n], 0\n\t" \
    "s_cbranch_scc1 .Lfb%=\n\t" \
    "s_sub_u32 %[n], %[n], 1\n\t" \
    "s_sleep 6\n\t" \
    "buffer_load_dwordx2 v[164:165], %[o0], %[rd], %[c1] offen sc1\n\t" \
    "buffer_load_dwordx2 v[166:167], %[o1], %[rd], %[c1] offen sc1\n\t" \
    "buffer_load_dwordx2 v[168:169], %[o2], %[rd], %[c1] offen sc1\n\t" \
    "buffer_load_dwordx2 v[170:171], %[o3], %[rd], %[c1] offen sc1\n\t" \
    "buffer_load_dwordx2 v[172:173], %[o4], %[rd], %[c1] offen sc1\n\t" \
    "buffer_load_dwordx2 v[174:175], %[o0], %[rd], %[ca] offen sc1\n\t" \
    "buffer_load_dwordx2 v[176:177], %[o1], %[rd], %[ca] offen sc1\n\t" \
    "buffer_load_dwordx2 v[178:179], %[o2], %[rd], %[ca] offen sc1\n\t" \
    "buffer_load_dwordx2 v[180:181], %[o3], %[rd], %[ca] offen sc1\n\t" \
    "buffer_load_dwordx2 v[182:183], %[o4], %[rd], %[ca] offen sc1\n\t" \
    "s_waitcnt vmcnt(0)\n\t" \
    "s_branch .Lfa%=\n\t" \
    ".Lfs0_%=:\n\t" \
    "s_cmp_eq_u32 %[n], 0\n\t" \
    "s_cbranch_scc1 .Lfu0_%=\n\t" \
    "s_sub_u32 %[n], %[n], 1\n\t" \
    "s_sleep 1\n\t" \
    "s_branch .Lft0_%=\n\t" \
    ".Lfs1_%=:\n\t" \
    "s_cmp_eq_u32 %[n], 0\n\t" \
    "s_cbranch_scc1 .Lfu1_%=\n\t" \
    "s_sub_u32 %[n], %[n], 1\n\t" \
    "s_sleep 1\n\t" \
    "s_branch .Lft1_%=\n\t" \
    ".Lfx%=:\n\t" \
    "s_cmp_eq_u32 %[n], 0\n\t" \
    "s_cbranch_scc1 .Lfz%=\n\t" \
    "s_sub_u32 %[n], %[n], 1\n\t" \
    "s_branch .Lfy%=\n\t" \
    ".Lf0%=:\n\t" \
    "s_waitcnt vmcnt(1)\n\t" \
    ".Lfa%=:\n\t" \
    "v_max_u32 v194, v164, v165\n\t" \
    "v_max3_u32 v194, v166, v167, v194\n\t" \
    "v_max3_u32 v194, v168, v169, v194\n\t" \
    "v_max3_u32 v194, v170, v171, v194\n\t" \
    "v_max3_u32 v194, v172, v173, v194\n\t" \
    "v_max3_u32 v194, v174, v175, v194\n\t" \
    "v_max3_u32 v194, v176, v177, v194\n\t" \
    "v_max3_u32 v194, v178, v179, v194\n\t" \
    "v_max3_u32 v194, v180, v181, v194\n\t" \
    "v_max3_u32 v194, v182, v183, v194\n\t" \
    "v_cmp_eq_u32 vcc, -1, v194\n\t" \
    "s_cbranch_vccnz .Lfr%=\n\t" \
    ".Lfb%=:\n\t" \
    "v_lshlrev_b32 v64, 16, v164\n\t" \
    "v_and_b32 v65, 0xffff0000, v164\n\t" \
    "v_lshlrev_b32 v84, 16, v174\n\t" \
    "v_and_b32 v85, 0xffff0000, v174\n\t" \
    "v_lshlrev_b32 v66, 16, v165\n\t" \
    "v_and_b32 v67, 0xffff0000, v165\n\t" \
    "v_lshlrev_b32 v86, 16, v175\n\t" \
    "v_and_b32 v87, 0xffff0000, v175\n\t" \
    "v_lshlrev_b32 v68, 16, v166\n\t" \
    "v_and_b32 v69, 0xffff0000, v166\n\t" \
    "v_lshlrev_b32 v88, 16, v176\n\t" \
    "v_and_b32 v89, 0xffff0000, v176\n\t" \
    "v_lshlrev_b32 v70, 16, v167\n\t" \
    "v_and_b32 v71, 0xffff0000, v167\n\t" \
    "v_lshlrev_b32 v90, 16, v177\n\t" \
    "v_and_b32 v91, 0xffff0000, v177\n\t" \
    "v_lshlrev_b32 v72, 16, v168\n\t" \
    "v_and_b32 v73, 0xffff0000, v168\n\t" \
    "v_lshlrev_b32 v92, 16, v178\n\t" \
    "v_and_b32 v93, 0xffff0000, v178\n\t" \
    "v_lshlrev_b32 v74, 16, v169\n\t" \
    "v_and_b32 v75, 0xffff0000, v169\n\t" \
    "v_lshlrev_b32 v94, 16, v179\n\t" \
    "v_and_b32 v95, 0xffff0000, v179\n\t" \
    "v_lshlrev_b32 v76, 16, v170\n\t" \
    "v_and_b32 v77, 0xffff0000, v170\n\t" \
    "v_lshlrev_b32 v96, 16, v180\n\t" \
    "v_and_b32 v97, 0xffff0000, v180\n\t" \
    "v_lshlrev_b32 v78, 16, v171\n\t" \
    "v_and_b32 v79, 0xffff0000, v171\n\t" \
    "v_lshlrev_b32 v98, 16, v181\n\t" \
    "v_and_b32 v99, 0xffff0000, v181\n\t" \
    "v_lshlrev_b32 v80, 16, v172\n\t" \
    "v_and_b32 v81, 0xffff0000, v172\n\t" \
    "v_lshlrev_b32 v100, 16, v182\n\t" \
    "v_and_b32 v101, 0xffff0000, v182\n\t" \
    "v_lshlrev_b32 v82, 16, v173\n\t" \
    "v_and_b32 v83, 0xffff0000, v173\n\t" \
    "v_lshlrev_b32 v102, 16, v183\n\t" \
    "v_and_b32 v103, 0xffff0000, v183\n\t" \
    "v_pk_add_f32 v[64:65], v[64:65], v[84:85]\n\t" \
    "v_pk_add_f32 v[66:67], v[66:67], v[86:87]\n\t" \
    "v_pk_add_f32 v[68:69], v[68:69], v[88:89]\n\t" \
    "v_pk_add_f32 v[70:71], v[70:71], v[90:91]\n\t" \
    "v_pk_add_f32 v[72:73], v[72:73], v[92:93]\n\t" \
    "v_pk_add_f32 v[74:75], v[74:75], v[94:95]\n\t" \
    "v_pk_add_f32 v[76:77], v[76:77], v[96:97]\n\t" \
    "v_pk_add_f32 v[78:79], v[78:79], v[98:99]\n\t" \
    "v_pk_add_f32 v[80:81], v[80:81], v[100:101]\n\t" \
    "v_pk_add_f32 v[82:83], v[82:83], v[102:103]\n\t" \
    ".Lft0_%=:\n\t" \
    "ds_read_b32 v194, %[f0]\n\t" \
    "s_waitcnt lgkmcnt(0)\n\t" \
    "v_cmp_ge_u32 vcc, %[u], v194\n\t" \
    "s_cbranch_vccnz .Lfs0_%=\n\t" \
    ".Lfu0_%=:\n\t" \
    "ds_read_b128 v[84:87], %[la] offset:0\n\t" \
    "ds_read_b128 v[88:91], %[la] offset:6144\n\t" \
    "ds_read_b128 v[92:95], %[la] offset:12288\n\t" \
    "ds_read_b128 v[96:99], %[la] offset:18432\n\t" \
    "ds_read_b128 v[100:103], %[la] offset:24576\n\t" \
    "ds_read_b128 v[104:107], %[la] offset:1024\n\t" \
    "ds_read_b128 v[108:111], %[la] offset:7168\n\t" \
    "ds_read_b128 v[112:115], %[la] offset:13312\n\t" \
    "ds_read_b128 v[116:119], %[la] offset:19456\n\t" \
    "ds_read_b128 v[120:123], %[la] offset:25600\n\t" \
    "ds_read_b128 v[124:127], %[la] offset:2048\n\t" \
    "s_waitcnt lgkmcnt(10)\n\t" \
    "v_pk_add_f32 v[64:65], v[64:65], v[84:85]\n\t" \
    "v_pk_add_f32 v[66:67], v[66:67], v[86:87]\n\t" \
    "ds_read_b128 v[128:131], %[la] offset:8192\n\t" \
    "s_waitcnt lgkmcnt(10)\n\t" \
    "v_pk_add_f32 v[68:69], v[68:69], v[88:89]\n\t" \
    "v_pk_add_f32 v[70:71], v[70:71], v[90:91]\n\t" \
    "ds_read_b128 v[132:135], %[la] offset:14336\n\t" \
    "s_waitcnt lgkmcnt(10)\n\t" \
    "v_pk_add_f32 v[72:73], v[72:73], v[92:93]\n\t" \
    "v_pk_add_f32 v[74:75], v[74:75], v[94:95]\n\t" \
    "ds_read_b128 v[136:139], %[la] offset:20480\n\t" \
    "s_waitcnt lgkmcnt(10)\n\t" \
    "v_pk_add_f32 v[76:77], v[76:77], v[96:97]\n\t" \
    "v_pk_add_f32 v[78:79], v[78:79], v[98:99]\n\t" \
    "ds_read_b128 v[140:143], %[la] offset:26624\n\t" \
    "s_waitcnt lgkmcnt(10)\n\t" \
    "v_pk_add_f32 v[80:81], v[80:81], v[100:101]\n\t" \
    "v_pk_add_f32 v[82:83], v[82:83], v[102:103]\n\t" \
    "ds_read_b128 v[144:147], %[la] offset:3072\n\t" \
    "s_waitcnt lgkmcnt(10)\n\t" \
    "v_pk_add_f32 v[64:65], v[64:65], v[104:105]\n\t" \
    "v_pk_add_f32 v[66:67], v[66:67], v[106:107]\n\t" \
    "ds_read_b128 v[148:151], %[la] offset:9216\n\t" \
    "s_waitcnt lgkmcnt(10)\n\t" \
    "v_pk_add_f32 v[68:69], v[68:69], v[108:109]\n\t" \
    "v_pk_add_f32 v[70:71], v[70:71], v[110:111]\n\t" \
    "ds_read_b128 v[152:155], %[la] offset:15360\n\t" \
    "s_waitcnt lgkmcnt(10)\n\t" \
    "v_pk_add_f32 v[72:73], v[72:73], v[112:113]\n\t" \
    "v_pk_add_f32 v[74:75], v[74:75], v[114:115]\n\t" \
    "ds_read_b128 v[156:159], %[la] offset:21504\n\t" \
    "s_waitcnt lgkmcnt(10)\n\t" \
    "v_pk_add_f32 v[76:77], v[76:77], v[116:117]\n\t" \
    "v_pk_add_f32 v[78:79], v[78:79], v[118:119]\n\t" \
    "ds_read_b128 v[160:163], %[la] offset:27648\n\t" \
    "s_waitcnt lgkmcnt(10)\n\t" \
    "v_pk_add_f32 v[80:81], v[80:81], v[120:121]\n\t" \
    "v_pk_add_f32 v[82:83], v[82:83], v[122:123]\n\t" \
    "s_waitcnt lgkmcnt(9)\n\t" \
    "v_pk_add_f32 v[64:65], v[64:65], v[124:125]\n\t" \
    "v_pk_add_f32 v[66:67], v[66:67], v[126:127]\n\t" \
    "s_waitcnt lgkmcnt(8)\n\t" \
    "v_pk_add_f32 v[68:69], v[68:69], v[128:129]\n\t" \
    "v_pk_add_f32 v[70:71], v[70:71], v[130:131]\n\t" \
    "s_waitcnt lgkmcnt(7)\n\t" \
    "v_pk_add_f32 v[72:73], v[72:73], v[132:133]\n\t" \
    "v_pk_add_f32 v[74:75], v[74:75], v[134:135]\n\t" \
    "s_waitcnt lgkmcnt(6)\n\t" \
    "v_pk_add_f32 v[76:77], v[76:77], v[136:137]\n\t" \
    "v_pk_add_f32 v[78:79], v[78:79], v[138:139]\n\t" \
    "s_waitcnt lgkmcnt(5)\n\t" \
    "v_pk_add_f32 v[80:81], v[80:81], v[140:141]\n\t" \
    "v_pk_add_f32 v[82:83], v[82:83], v[142:143]\n\t" \
    "s_waitcnt lgkmcnt(4)\n\t" \
    "v_pk_add_f32 v[64:65], v[64:65], v[144:145]\n\t" \
    "v_pk_add_f32 v[66:67], v[66:67], v[146:147]\n\t" \
    "s_waitcnt lgkmcnt(3)\n\t" \
    "v_pk_add_f32 v[68:69], v[68:69], v[148:149]\n\t" \
    "v_pk_add_f32 v[70:71], v[70:71], v[150:151]\n\t" \
    "s_waitcnt lgkmcnt(2)\n\t" \
    "v_pk_add_f32 v[72:73], v[72:73], v[152:153]\n\t" \
    "v_pk_add_f32 v[74:75], v[74:75], v[154:155]\n\t" \
    "s_waitcnt lgkmcnt(1)\n\t" \
    "v_pk_add_f32 v[76:77], v[76:77], v[156:157]\n\t" \
    "v_pk_add_f32 v[78:79], v[78:79], v[158:159]\n\t" \
    "s_waitcnt lgkmcnt(0)\n\t" \
    "v_pk_add_f32 v[80:81], v[80:81], v[160:161]\n\t" \
    "v_pk_add_f32 v[82:83], v[82:83], v[162:163]\n\t" \
    ".Lft1_%=:\n\t" \
    "ds_read_b32 v194, %[f1]\n\t" \
    "s_waitcnt lgkmcnt(0)\n\t" \
    "v_cmp_ge_u32 vcc, %[u], v194\n\t" \
    "s_cbranch_vccnz .Lfs1_%=\n\t" \
    ".Lfu1_%=:\n\t" \
    ASM_ST2 \
    "ds_read_b128 v[84:87], %[la] offset:4096\n\t" \
    "ds_read_b128 v[88:91], %[la] offset:10240\n\t" \
    "ds_read_b128 v[92:95], %[la] offset:16384\n\t" \
    "ds_read_b128 v[96:99], %[la] offset:22528\n\t" \
    "ds_read_b128 v[100:103], %[la] offset:28672\n\t" \
    "ds_read_b128 v[104:107], %[la] offset:5120\n\t" \
    "ds_read_b128 v[108:111], %[la] offset:11264\n\t" \
    "ds_read_b128 v[112:115], %[la] offset:17408\n\t" \
    "ds_read_b128 v[116:119], %[la] offset:23552\n\t" \
    "ds_read_b128 v[120:123], %[la] offset:29696\n\t" \
    "v_mov_b32 v194, %[un]\n\t" \
    "ds_write_b32 %[fd], v194\n\t" \
    "s_waitcnt lgkmcnt(10)\n\t" \
    "v_pk_add_f32 v[64:65], v[64:65], v[84:85]\n\t" \
    "v_pk_add_f32 v[66:67], v[66:67], v[86:87]\n\t" \
    "s_waitcnt lgkmcnt(9)\n\t" \
    "v_pk_add_f32 v[68:69], v[68:69], v[88:89]\n\t" \
    "v_pk_add_f32 v[70:71], v[70:71], v[90:91]\n\t" \
    "s_waitcnt lgkmcnt(8)\n\t" \
    "v_pk_add_f32 v[72:73], v[72:73], v[92:93]\n\t" \
    "v_pk_add_f32 v[74:75], v[74:75], v[94:95]\n\t" \
    "s_waitcnt lgkmcnt(7)\n\t" \
    "v_pk_add_f32 v[76:77], v[76:77], v[96:97]\n\t" \
    "v_pk_add_f32 v[78:79], v[78:79], v[98:99]\n\t" \
    "s_waitcnt lgkmcnt(6)\n\t" \
    "v_pk_add_f32 v[80:81], v[80:81], v[100:101]\n\t" \
    "v_pk_add_f32 v[82:83], v[82:83], v[102:103]\n\t" \
    "s_waitcnt lgkmcnt(5)\n\t" \
    "v_pk_add_f32 v[64:65], v[64:65], v[104:105]\n\t" \
    "v_pk_add_f32 v[66:67], v[66:67], v[106:107]\n\t" \
    "s_waitcnt lgkmcnt(4)\n\t" \
    "v_pk_add_f32 v[68:69], v[68:69], v[108:109]\n\t" \
    "v_pk_add_f32 v[70:71], v[70:71], v[110:111]\n\t" \
    "s_waitcnt lgkmcnt(3)\n\t" \
    "v_pk_add_f32 v[72:73], v[72:73], v[112:113]\n\t" \
    "v_pk_add_f32 v[74:75], v[74:75], v[114:115]\n\t" \
    "s_waitcnt lgkmcnt(2)\n\t" \
    "v_pk_add_f32 v[76:77], v[76:77], v[116:117]\n\t" \
    "v_pk_add_f32 v[78:79], v[78:79], v[118:119]\n\t" \
    "s_waitcnt lgkmcnt(1)\n\t" \
    "v_pk_add_f32 v[80:81], v[80:81], v[120:121]\n\t" \
    "v_pk_add_f32 v[82:83], v[82:83], v[122:123]\n\t" \
    "v_pk_add_f32 v[84:85], v[64:65], v[66:67]\n\t" \
    "v_pk_mul_f32 v[88:89], v[64:65], v[64:65]\n\t" \
    "v_pk_add_f32 v[86:87], v[68:69], v[70:71]\n\t" \
    "v_pk_mul_f32 v[90:91], v[66:67], v[66:67]\n\t" \
    "v_pk_fma_f32 v[88:89], v[68:69], v[68:69], v[88:89]\n\t" \
    "v_pk_fma_f32 v[90:91], v[70:71], v[70:71], v[90:91]\n\t" \
    "v_pk_add_f32 v[84:85], v[84:85], v[72:73]\n\t" \
    "v_pk_fma_f32 v[88:89], v[72:73], v[72:73], v[88:89]\n\t" \
    "v_pk_add_f32 v[86:87], v[86:87], v[74:75]\n\t" \
    "v_pk_fma_f32 v[90:91], v[74:75], v[74:75], v[90:91]\n\t" \
    "v_pk_add_f32 v[84:85], v[84:85], v[76:77]\n\t" \
    "v_pk_fma_f32 v[88:89], v[76:77], v[76:77], v[88:89]\n\t" \
    "v_pk_add_f32 v[86:87], v[86:87], v[78:79]\n\t" \
    "v_pk_fma_f32 v[90:91], v[78:79], v[78:79], v[90:91]\n\t" \
    "v_pk_add_f32 v[84:85], v[84:85], v[80:81]\n\t" \
    "v_pk_fma_f32 v[88:89], v[80:81], v[80:81], v[88:89]\n\t" \
    "v_pk_add_f32 v[86:87], v[86:87], v[82:83]\n\t" \
    "v_pk_fma_f32 v[90:91], v[82:83], v[82:83], v[90:91]\n\t" \
    "v_pk_add_f32 v[84:85], v[84:85], v[86:87]\n\t" \
    "v_pk_add_f32 v[88:89], v[88:89], v[90:91]\n\t" \
    "v_add_f32 v194, v84, v85\n\t" \
    "v_add_f32 v195, v88, v89\n\t" \
    "s_nop 1\n\t" \
    "v_permlane16_swap_b32 v194, v195\n\t" \
    "v_add_f32 v196, v194, v195\n\t" \
    "v_add_f32 v197, v194, v195\n\t" \
    "s_nop 1\n\t" \
    "v_permlane32_swap_b32 v196, v197\n\t" \
    "v_add_f32 v194, v196, v197\n\t" \
    "v_add_f32 v195, v196, v197\n\t" \
    "s_nop 1\n\t" \
    "v_permlane16_swap_b32 v194, v195\n\t" \
    "v_min_u32 v194, -2, v194\n\t" \
    "v_min_u32 v195, -2, v195\n\t" \
    "buffer_store_dwordx2 v[194:195], %[so], %[rr], %[sp] offen sc1\n\t" \
    ASM_ST3 \
    "v_cvt_pk_bf16_f32 v184, v64, v65\n\t" \
    "v_cvt_pk_bf16_f32 v185, v66, v67\n\t" \
    "v_cvt_pk_bf16_f32 v186, v68, v69\n\t" \
    "v_cvt_pk_bf16_f32 v187, v70, v71\n\t" \
    "v_cvt_pk_bf16_f32 v188, v72, v73\n\t" \
    "v_cvt_pk_bf16_f32 v189, v74, v75\n\t" \
    "v_cvt_pk_bf16_f32 v190, v76, v77\n\t" \
    "v_cvt_pk_bf16_f32 v191, v78, v79\n\t" \
    "v_cvt_pk_bf16_f32 v192, v80, v81\n\t" \
    "v_cvt_pk_bf16_f32 v193, v82, v83\n\t" \
    "v_permlane16_swap_b32 v184, v186\n\t" \
    "v_permlane16_swap_b32 v185, v187\n\t" \
    "v_permlane16_swap_b32 v188, v190\n\t" \
    "v_permlane16_swap_b32 v189, v191\n\t" \
    W4SLEEP \
    ".Lfy%=:\n\t" \
    "buffer_load_dwordx2 v[196:197], %[si], %[rr], %[sq] offen sc1\n\t" \
    "buffer_load_dwordx2 v[198:199], %[si], %[rr], %[sq] offen offset:512 sc1\n\t" \
    "s_waitcnt vmcnt(0)\n\t" \
    "v_max_u32 v200, v196, v197\n\t" \
    "v_max3_u32 v200, v198, v199, v200\n\t" \
    "v_cmp_eq_u32 vcc, -1, v200\n\t" \
    "s_cbranch_vccnz .Lfx%=\n\t" \
    ".Lfz%=:\n\t" \
    ASM_ST \
    "v_add_f32 v194, v196, v198\n\t" \
    "v_add_f32 v195, v197, v199\n\t" \
    "buffer_store_dwordx4 v[184:187], %[pa], %[rd], %[xr] offen sc1\n\t" \
    "buffer_store_dwordx4 v[188:191], %[pb], %[rd], %[xr] offen sc1\n\t" \
    "buffer_store_dwordx2 v[192:193], %[o4], %[rd], %[xr] offen sc1\n\t" \
    "s_nop 1\n\t" \
    "v_permlane16_swap_b32 v194, v195\n\t" \
    "v_add_f32 v196, v194, v195\n\t" \
    "v_add_f32 v197, v194, v195\n\t" \
    "s_nop 1\n\t" \
    "v_permlane32_swap_b32 v196, v197\n\t" \
    "v_add_f32 v194, v196, v197\n\t" \
    "v_add_f32 v195, v196, v197\n\t" \
    "s_nop 1\n\t" \
    "v_permlane16_swap_b32 v194, v195\n\t" \
    "v_mul_f32 v194, %[kd], v194\n\t" \
    "v_mul_f32 v195, %[kd], v195\n\t" \
    "v_fma_f32 v195, -v194, v194, v195\n\t" \
    "v_max_f32 v195, 0, v195\n\t" \
    "v_cvt_pk_bf16_f32 v195, v195, v195\n\t" \
    "v_lshlrev_b32 v195, 16, v195\n\t" \
    "v_add_f32 v195, %[ke], v195\n\t" \
    "v_cvt_pk_bf16_f32 v195, v195, v195\n\t" \
    "v_lshlrev_b32 v195, 16, v195\n\t" \
    "v_sqrt_f32 v195, v195\n\t" \
    "v_cvt_pk_bf16_f32 v194, v194, v194\n\t" \
    "v_lshlrev_b32 v194, 16, v194\n\t" \
    "v_cvt_pk_bf16_f32 v195, v195, v195\n\t" \
    "v_lshlrev_b32 v195, 16, v195\n\t" \
    "v_rcp_f32 v196, v195\n\t" \
    "v_rcp_f32 v197, v195\n\t" \
    "s_nop 0\n\t" \
    "v_mul_f32 v198, -v194, v196\n\t" \
    "v_mul_f32 v199, -v194, v196\n\t" \
    "v_pk_fma_f32 v[64:65], v[64:65], v[196:197], v[198:199]\n\t" \
    "v_pk_fma_f32 v[66:67], v[66:67], v[196:197], v[198:199]\n\t" \
    "v_pk_fma_f32 v[68:69], v[68:69], v[196:197], v[198:199]\n\t" \
    "v_pk_fma_f32 v[70:71], v[70:71], v[196:197], v[198:199]\n\t" \
    "v_pk_fma_f32 v[72:73], v[72:73], v[196:197], v[198:199]\n\t" \
    "v_pk_fma_f32 v[74:75], v[74:75], v[196:197], v[198:199]\n\t" \
    "v_pk_fma_f32 v[76:77], v[76:77], v[196:197], v[198:199]\n\t" \
    "v_pk_fma_f32 v[78:79], v[78:79], v[196:197], v[198:199]\n\t" \
    "v_pk_fma_f32 v[80:81], v[80:81], v[196:197], v[198:199]\n\t" \
    "v_pk_fma_f32 v[82:83], v[82:83], v[196:197], v[198:199]\n\t" \
    "v_cvt_pk_bf16_f32 v184, v64, v65\n\t" \
    "v_cvt_pk_bf16_f32 v185, v66, v67\n\t" \
    "v_cvt_pk_bf16_f32 v186, v68, v69\n\t" \
    "v_cvt_pk_bf16_f32 v187, v70, v71\n\t" \
    "v_cvt_pk_bf16_f32 v188, v72, v73\n\t" \
    "v_cvt_pk_bf16_f32 v189, v74, v75\n\t" \
    "v_cvt_pk_bf16_f32 v190, v76, v77\n\t" \
    "v_cvt_pk_bf16_f32 v191, v78, v79\n\t" \
    "v_cvt_pk_bf16_f32 v192, v80, v81\n\t" \
    "v_cvt_pk_bf16_f32 v193, v82, v83\n\t" \
    "v_permlane16_swap_b32 v184, v186\n\t" \
    "v_permlane16_swap_b32 v185, v187\n\t" \
    "v_permlane16_swap_b32 v188, v190\n\t" \
    "v_permlane16_swap_b32 v189, v191\n\t" \
    "buffer_store_dwordx4 v[184:187], %[pa], %[rd], %[xn] offen sc1\n\t" \
    "buffer_store_dwordx4 v[188:191], %[pb], %[rd], %[xn] offen sc1\n\t" \
    "buffer_store_dwordx2 v[192:193], %[o4], %[rd], %[xn] offen sc1\n\t" \
    "buffer_load_dwordx2 v[164:165], %[o0], %[rd], %[n1] offen sc1\n\t" \
    "buffer_load_dwordx2 v[166:167], %[o1], %[rd], %[n1] offen sc1\n\t" \
    "buffer_load_dwordx2 v[168:169], %[o2], %[rd], %[n1] offen sc1\n\t" \
    "buffer_load_dwordx2 v[170:171], %[o3], %[rd], %[n1] offen sc1\n\t" \
    "buffer_load_dwordx2 v[172:173], %[o4], %[rd], %[n1] offen sc1\n\t" \
    "buffer_load_dwordx2 v[174:175], %[o0], %[rd], %[na] offen sc1\n\t" \
    "buffer_load_dwordx2 v[176:177], %[o1], %[rd], %[na] offen sc1\n\t" \
    "buffer_load_dwordx2 v[178:179], %[o2], %[rd], %[na] offen sc1\n\t" \
    "buffer_load_dwordx2 v[180:181], %[o3], %[rd], %[na] offen sc1\n\t" \
    "buffer_load_dwordx2 v[182:183], %[o4], %[rd], %[na] offen sc1\n\t" \
    "v_mov_b32 v196, %[tn]\n\t" \
    "v_mov_b32 v197, %[tn]\n\t" \
    "v_mov_b32 v198, %[tn]\n\t" \
    "v_mov_b32 v199, %[tn]\n\t" \
    "s_waitcnt vmcnt(10)\n\t" \
    "buffer_store_dwordx4 v[196:199], %[fo], %[rn], %[fs] offen sc1\n\t" \
    "s_nop 1\n\t"
#define ASM_W4F_1 \
    "s_branch .Lf0%=\n\t" \
    ".Lfr%=:\n\t" \
    "s_cmp_eq_u32 %[n], 0\n\t" \
    "s_cbranch_scc1 .Lfb%=\n\t" \
    "s_sub_u32 %[n], %[n], 1\n\t" \
    "s_sleep 6\n\t" \
    "buffer_load_dwordx2 v[164:165], %[o0], %[rd], %[c1] offen sc1\n\t" \
    "buffer_load_dwordx2 v[166:167], %[o1], %[rd], %[c1] offen sc1\n\t" \
    "buffer_load_dwordx2 v[168:169], %[o2], %[rd], %[c1] offen sc1\n\t" \
    "buffer_load_dwordx2 v[170:171], %[o3], %[rd], %[c1] offen sc1\n\t" \
    "buffer_load_dwordx2 v[172:173], %[o4], %[rd], %[c1] offen sc1\n\t" \
    "buffer_load_dwordx2 v[174:175], %[o0], %[rd], %[ca] offen sc1\n\t" \
    "buffer_load_dwordx2 v[176:177], %[o1], %[rd], %[ca] offen sc1\n\t" \
    "buffer_load_dwordx2 v[178:179], %[o2], %[rd], %[ca] offen sc1\n\t" \
    "buffer_load_dwordx2 v[180:181], %[o3], %[rd], %[ca] offen sc1\n\t" \
    "buffer_load_dwordx2 v[182:183], %[o4], %[rd], %[ca] offen sc1\n\t" \
    "s_waitcnt vmcnt(0)\n\t" \
    "s_branch .Lfa%=\n\t" \
    ".Lfs0_%=:\n\t" \
    "s_cmp_eq_u32 %[n], 0\n\t" \
    "s_cbranch_scc1 .Lfu0_%=\n\t" \
    "s_sub_u32 %[n], %[n], 1\n\t" \
    "s_sleep 1\n\t" \
    "s_branch .Lft0_%=\n\t" \
    ".Lfs1_%=:\n\t" \
    "s_cmp_eq_u32 %[n], 0\n\t" \
    "s_cbranch_scc1 .Lfu1_%=\n\t" \
    "s_sub_u32 %[n], %[n], 1\n\t" \
    "s_sleep 1\n\t" \
    "s_branch .Lft1_%=\n\t" \
    ".Lf0%=:\n\t" \
    "s_waitcnt vmcnt(0)\n\t" \
    ".Lfa%=:\n\t" \
    "v_max_u32 v194, v164, v165\n\t" \
    "v_max3_u32 v194, v166, v167, v194\n\t" \
    "v_max3_u32 v194, v168, v169, v194\n\t" \
    "v_max3_u32 v194, v170, v171, v194\n\t" \
    "v_max3_u32 v194, v172, v173, v194\n\t" \
    "v_max3_u32 v194, v174, v175, v194\n\t" \
    "v_max3_u32 v194, v176, v177, v194\n\t" \
    "v_max3_u32 v194, v178, v179, v194\n\t" \
    "v_max3_u32 v194, v180, v181, v194\n\t" \
    "v_max3_u32 v194, v182, v183, v194\n\t" \
    "v_cmp_eq_u32 vcc, -1, v194\n\t" \
    "s_cbranch_vccnz .Lfr%=\n\t" \
    ".Lfb%=:\n\t" \
    "v_lshlrev_b32 v64, 16, v164\n\t" \
    "v_and_b32 v65, 0xffff0000, v164\n\t" \
    "v_lshlrev_b32 v84, 16, v174\n\t" \
    "v_and_b32 v85, 0xffff0000, v174\n\t" \
    "v_lshlrev_b32 v66, 16, v165\n\t" \
    "v_and_b32 v67, 0xffff0000, v165\n\t" \
    "v_lshlrev_b32 v86, 16, v175\n\t" \
    "v_and_b32 v87, 0xffff0000, v175\n\t" \
    "v_lshlrev_b32 v68, 16, v166\n\t" \
    "v_and_b32 v69, 0xffff0000, v166\n\t" \
    "v_lshlrev_b32 v88, 16, v176\n\t" \
    "v_and_b32 v89, 0xffff0000, v176\n\t" \
    "v_lshlrev_b32 v70, 16, v167\n\t" \
    "v_and_b32 v71, 0xffff0000, v167\n\t" \
    "v_lshlrev_b32 v90, 16, v177\n\t" \
    "v_and_b32 v91, 0xffff0000, v177\n\t" \
    "v_lshlrev_b32 v72, 16, v168\n\t" \
    "v_and_b32 v73, 0xffff0000, v168\n\t" \
    "v_lshlrev_b32 v92, 16, v178\n\t" \
    "v_and_b32 v93, 0xffff0000, v178\n\t" \
    "v_lshlrev_b32 v74, 16, v169\n\t" \
    "v_and_b32 v75, 0xffff0000, v169\n\t" \
    "v_lshlrev_b32 v94, 16, v179\n\t" \
    "v_and_b32 v95, 0xffff0000, v179\n\t" \
    "v_lshlrev_b32 v76, 16, v170\n\t" \
    "v_and_b32 v77, 0xffff0000, v170\n\t" \
    "v_lshlrev_b32 v96, 16, v180\n\t" \
    "v_and_b32 v97, 0xffff0000, v180\n\t" \
    "v_lshlrev_b32 v78, 16, v171\n\t" \
    "v_and_b32 v79, 0xffff0000, v171\n\t" \
    "v_lshlrev_b32 v98, 16, v181\n\t" \
    "v_and_b32 v99, 0xffff0000, v181\n\t" \
    "v_lshlrev_b32 v80, 16, v172\n\t" \
    "v_and_b32 v81, 0xffff0000, v172\n\t" \
    "v_lshlrev_b32 v100, 16, v182\n\t" \
    "v_and_b32 v101, 0xffff0000, v182\n\t" \
    "v_lshlrev_b32 v82, 16, v173\n\t" \
    "v_and_b32 v83, 0xffff0000, v173\n\t" \
    "v_lshlrev_b32 v102, 16, v183\n\t" \
    "v_and_b32 v103, 0xffff0000, v183\n\t" \
    "v_pk_add_f32 v[64:65], v[64:65], v[84:85]\n\t" \
    "v_pk_add_f32 v[66:67], v[66:67], v[86:87]\n\t" \
    "v_pk_add_f32 v[68:69], v[68:69], v[88:89]\n\t" \
    "v_pk_add_f32 v[70:71], v[70:71], v[90:91]\n\t" \
    "v_pk_add_f32 v[72:73], v[72:73], v[92:93]\n\t" \
    "v_pk_add_f32 v[74:75], v[74:75], v[94:95]\n\t" \
    "v_pk_add_f32 v[76:77], v[76:77], v[96:97]\n\t" \
    "v_pk_add_f32 v[78:79], v[78:79], v[98:99]\n\t" \
    "v_pk_add_f32 v[80:81], v[80:81], v[100:101]\n\t" \
    "v_pk_add_f32 v[82:83], v[82:83], v[102:103]\n\t" \
    ".Lft0_%=:\n\t" \
    "ds_read_b32 v194, %[f0]\n\t" \
    "s_waitcnt lgkmcnt(0)\n\t" \
    "v_cmp_ge_u32 vcc, %[u], v194\n\t" \
    "s_cbranch_vccnz .Lfs0_%=\n\t" \
    ".Lfu0_%=:\n\t" \
    "ds_read_b128 v[84:87], %[la] offset:0\n\t" \
    "ds_read_b128 v[88:91], %[la] offset:6144\n\t" \
    "ds_read_b128 v[92:95], %[la] offset:12288\n\t" \
    "ds_read_b128 v[96:99], %[la] offset:18432\n\t" \
    "ds_read_b128 v[100:103], %[la] offset:24576\n\t" \
    "ds_read_b128 v[104:107], %[la] offset:1024\n\t" \
    "ds_read_b128 v[108:111], %[la] offset:7168\n\t" \
    "ds_read_b128 v[112:115], %[la] offset:13312\n\t" \
    "ds_read_b128 v[116:119], %[la] offset:19456\n\t" \
    "ds_read_b128 v[120:123], %[la] offset:25600\n\t" \
    "ds_read_b128 v[124:127], %[la] offset:2048\n\t" \
    "s_waitcnt lgkmcnt(10)\n\t" \
    "v_pk_add_f32 v[64:65], v[64:65], v[84:85]\n\t" \
    "v_pk_add_f32 v[66:67], v[66:67], v[86:87]\n\t" \
    "ds_read_b128 v[128:131], %[la] offset:8192\n\t" \
    "s_waitcnt lgkmcnt(10)\n\t" \
    "v_pk_add_f32 v[68:69], v[68:69], v[88:89]\n\t" \
    "v_pk_add_f32 v[70:71], v[70:71], v[90:91]\n\t" \
    "ds_read_b128 v[132:135], %[la] offset:14336\n\t" \
    "s_waitcnt lgkmcnt(10)\n\t" \
    "v_pk_add_f32 v[72:73], v[72:73], v[92:93]\n\t" \
    "v_pk_add_f32 v[74:75], v[74:75], v[94:95]\n\t" \
    "ds_read_b128 v[136:139], %[la] offset:20480\n\t" \
    "s_waitcnt lgkmcnt(10)\n\t" \
    "v_pk_add_f32 v[76:77], v[76:77], v[96:97]\n\t" \
    "v_pk_add_f32 v[78:79], v[78:79], v[98:99]\n\t" \
    "ds_read_b128 v[140:143], %[la] offset:26624\n\t" \
    "s_waitcnt lgkmcnt(10)\n\t" \
    "v_pk_add_f32 v[80:81], v[80:81], v[100:101]\n\t" \
    "v_pk_add_f32 v[82:83], v[82:83], v[102:103]\n\t" \
    "ds_read_b128 v[144:147], %[la] offset:3072\n\t" \
    "s_waitcnt lgkmcnt(10)\n\t" \
    "v_pk_add_f32 v[64:65], v[64:65], v[104:105]\n\t" \
    "v_pk_add_f32 v[66:67], v[66:67], v[106:107]\n\t" \
    "ds_read_b128 v[148:151], %[la] offset:9216\n\t" \
    "s_waitcnt lgkmcnt(10)\n\t" \
    "v_pk_add_f32 v[68:69], v[68:69], v[108:109]\n\t" \
    "v_pk_add_f32 v[70:71], v[70:71], v[110:111]\n\t" \
    "ds_read_b128 v[152:155], %[la] offset:15360\n\t" \
    "s_waitcnt lgkmcnt(10)\n\t" \
    "v_pk_add_f32 v[72:73], v[72:73], v[112:113]\n\t" \
    "v_pk_add_f32 v[74:75], v[74:75], v[114:115]\n\t" \
    "ds_read_b128 v[156:159], %[la] offset:21504\n\t" \
    "s_waitcnt lgkmcnt(10)\n\t" \
    "v_pk_add_f32 v[76:77], v[76:77], v[116:117]\n\t" \
    "v_pk_add_f32 v[78:79], v[78:79], v[118:119]\n\t" \
    "ds_read_b128 v[160:163], %[la] offset:27648\n\t" \
    "s_waitcnt lgkmcnt(10)\n\t" \
    "v_pk_add_f32 v[80:81], v[80:81], v[120:121]\n\t" \
    "v_pk_add_f32 v[82:83], v[82:83], v[122:123]\n\t" \
    "s_waitcnt lgkmcnt(9)\n\t" \
    "v_pk_add_f32 v[64:65], v[64:65], v[124:125]\n\t" \
    "v_pk_add_f32 v[66:67], v[66:67], v[126:127]\n\t" \
    "s_waitcnt lgkmcnt(8)\n\t" \
    "v_pk_add_f32 v[68:69], v[68:69], v[128:129]\n\t" \
    "v_pk_add_f32 v[70:71], v[70:71], v[130:131]\n\t" \
    "s_waitcnt lgkmcnt(7)\n\t" \
    "v_pk_add_f32 v[72:73], v[72:73], v[132:133]\n\t" \
    "v_pk_add_f32 v[74:75], v[74:75], v[134:135]\n\t" \
    "s_waitcnt lgkmcnt(6)\n\t" \
    "v_pk_add_f32 v[76:77], v[76:77], v[136:137]\n\t" \
    "v_pk_add_f32 v[78:79], v[78:79], v[138:139]\n\t" \
    "s_waitcnt lgkmcnt(5)\n\t" \
    "v_pk_add_f32 v[80:81], v[80:81], v[140:141]\n\t" \
    "v_pk_add_f32 v[82:83], v[82:83], v[142:143]\n\t" \
    "s_waitcnt lgkmcnt(4)\n\t" \
    "v_pk_add_f32 v[64:65], v[64:65], v[144:145]\n\t" \
    "v_pk_add_f32 v[66:67], v[66:67], v[146:147]\n\t" \
    "s_waitcnt lgkmcnt(3)\n\t" \
    "v_pk_add_f32 v[68:69], v[68:69], v[148:149]\n\t" \
    "v_pk_add_f32 v[70:71], v[70:71], v[150:151]\n\t" \
    "s_waitcnt lgkmcnt(2)\n\t" \
    "v_pk_add_f32 v[72:73], v[72:73], v[152:153]\n\t" \
    "v_pk_add_f32 v[74:75], v[74:75], v[154:155]\n\t" \
    "s_waitcnt lgkmcnt(1)\n\t" \
    "v_pk_add_f32 v[76:77], v[76:77], v[156:157]\n\t" \
    "v_pk_add_f32 v[78:79], v[78:79], v[158:159]\n\t" \
    "s_waitcnt lgkmcnt(0)\n\t" \
    "v_pk_add_f32 v[80:81], v[80:81], v[160:161]\n\t" \
    "v_pk_add_f32 v[82:83], v[82:83], v[162:163]\n\t" \
    ".Lft1_%=:\n\t" \
    "ds_read_b32 v194, %[f1]\n\t" \
    "s_waitcnt lgkmcnt(0)\n\t" \
    "v_cmp_ge_u32 vcc, %[u], v194\n\t" \
    "s_cbranch_vccnz .Lfs1_%=\n\t" \
    ".Lfu1_%=:\n\t" \
    ASM_ST2 \
    "ds_read_b128 v[84:87], %[la] offset:4096\n\t" \
    "ds_read_b128 v[88:91], %[la] offset:10240\n\t" \
    "ds_read_b128 v[92:95], %[la] offset:16384\n\t" \
    "ds_read_b128 v[96:99], %[la] offset:22528\n\t" \
    "ds_read_b128 v[100:103], %[la] offset:28672\n\t" \
    "ds_read_b128 v[104:107], %[la] offset:5120\n\t" \
    "ds_read_b128 v[108:111], %[la] offset:11264\n\t" \
    "ds_read_b128 v[112:115], %[la] offset:17408\n\t" \
    "ds_read_b128 v[116:119], %[la] offset:23552\n\t" \
    "ds_read_b128 v[120:123], %[la] offset:29696\n\t" \
    "v_mov_b32 v194, %[un]\n\t" \
    "ds_write_b32 %[fd], v194\n\t" \
    "s_waitcnt lgkmcnt(10)\n\t" \
    "v_pk_add_f32 v[64:65], v[64:65], v[84:85]\n\t" \
    "v_pk_add_f32 v[66:67], v[66:67], v[86:87]\n\t" \
    "s_waitcnt lgkmcnt(9)\n\t" \
    "v_pk_add_f32 v[68:69], v[68:69], v[88:89]\n\t" \
    "v_pk_add_f32 v[70:71], v[70:71], v[90:91]\n\t" \
    "s_waitcnt lgkmcnt(8)\n\t" \
    "v_pk_add_f32 v[72:73], v[72:73], v[92:93]\n\t" \
    "v_pk_add_f32 v[74:75], v[74:75], v[94:95]\n\t" \
    "s_waitcnt lgkmcnt(7)\n\t" \
    "v_pk_add_f32 v[76:77], v[76:77], v[96:97]\n\t" \
    "v_pk_add_f32 v[78:79], v[78:79], v[98:99]\n\t" \
    "s_waitcnt lgkmcnt(6)\n\t" \
    "v_pk_add_f32 v[80:81], v[80:81], v[100:101]\n\t" \
    "v_pk_add_f32 v[82:83], v[82:83], v[102:103]\n\t" \
    "s_waitcnt lgkmcnt(5)\n\t" \
    "v_pk_add_f32 v[64:65], v[64:65], v[104:105]\n\t" \
    "v_pk_add_f32 v[66:67], v[66:67], v[106:107]\n\t" \
    "s_waitcnt lgkmcnt(4)\n\t" \
    "v_pk_add_f32 v[68:69], v[68:69], v[108:109]\n\t" \
    "v_pk_add_f32 v[70:71], v[70:71], v[110:111]\n\t" \
    "s_waitcnt lgkmcnt(3)\n\t" \
    "v_pk_add_f32 v[72:73], v[72:73], v[112:113]\n\t" \
    "v_pk_add_f32 v[74:75], v[74:75], v[114:115]\n\t" \
    "s_waitcnt lgkmcnt(2)\n\t" \
    "v_pk_add_f32 v[76:77], v[76:77], v[116:117]\n\t" \
    "v_pk_add_f32 v[78:79], v[78:79], v[118:119]\n\t" \
    "s_waitcnt lgkmcnt(1)\n\t" \
    "v_pk_add_f32 v[80:81], v[80:81], v[120:121]\n\t" \
    "v_pk_add_f32 v[82:83], v[82:83], v[122:123]\n\t" \
    "v_cvt_pk_bf16_f32 v184, v64, v65\n\t" \
    "v_cvt_pk_bf16_f32 v185, v66, v67\n\t" \
    "v_cvt_pk_bf16_f32 v186, v68, v69\n\t" \
    "v_cvt_pk_bf16_f32 v187, v70, v71\n\t" \
    "v_cvt_pk_bf16_f32 v188, v72, v73\n\t" \
    "v_cvt_pk_bf16_f32 v189, v74, v75\n\t" \
    "v_cvt_pk_bf16_f32 v190, v76, v77\n\t" \
    "v_cvt_pk_bf16_f32 v191, v78, v79\n\t" \
    "v_cvt_pk_bf16_f32 v192, v80, v81\n\t" \
    "v_cvt_pk_bf16_f32 v193, v82, v83\n\t" \
    "v_permlane16_swap_b32 v184, v186\n\t" \
    "v_permlane16_swap_b32 v185, v187\n\t" \
    "v_permlane16_swap_b32 v188, v190\n\t" \
    "v_permlane16_swap_b32 v189, v191\n\t" \
    "buffer_store_dwordx4 v[184:187], %[pa], %[ro], %[so] offen nt\n\t" \
    "buffer_store_dwordx4 v[188:191], %[pa], %[ro], %[so] offen offset:64 nt\n\t" \
    "buffer_store_dwordx2 v[192:193], %[pb], %[ro], %[so] offen offset:128 nt\n\t" \
    "buffer_load_dwordx2 v[164:165], %[o0], %[rd], %[n1] offen sc1\n\t" \
    "buffer_load_dwordx2 v[166:167], %[o1], %[rd], %[n1] offen sc1\n\t" \
    "buffer_load_dwordx2 v[168:169], %[o2], %[rd], %[n1] offen sc1\n\t" \
    "buffer_load_dwordx2 v[170:171], %[o3], %[rd], %[n1] offen sc1\n\t" \
    "buffer_load_dwordx2 v[172:173], %[o4], %[rd], %[n1] offen sc1\n\t" \
    "buffer_load_dwordx2 v[174:175], %[o0], %[rd], %[na] offen sc1\n\t" \
    "buffer_load_dwordx2 v[176:177], %[o1], %[rd], %[na] offen sc1\n\t" \
    "buffer_load_dwordx2 v[178:179], %[o2], %[rd], %[na] offen sc1\n\t" \
    "buffer_load_dwordx2 v[180:181], %[o3], %[rd], %[na] offen sc1\n\t" \
    "buffer_load_dwordx2 v[182:183], %[o4], %[rd], %[na] offen sc1\n\t"
#define ASM_WCS \
    "v_mfma_f32_16x16x32_bf16 v[196:199], v[0:3], v[216:219], 0\n\t" \
    "v_mfma_f32_16x16x32_bf16 v[200:203], v[80:83], v[216:219], 0\n\t" \
    "v_mfma_f32_16x16x32_bf16 v[196:199], v[4:7], v[216:219], v[196:199]\n\t" \
    "v_mfma_f32_16x16x32_bf16 v[200:203], v[84:87], v[216:219], v[200:203]\n\t" \
    "v_mfma_f32_16x16x32_bf16 v[196:199], v[8:11], v[216:219], v[196:199]\n\t" \
    "v_mfma_f32_16x16x32_bf16 v[200:203], v[88:91], v[216:219], v[200:203]\n\t" \
    "v_mfma_f32_16x16x32_bf16 v[196:199], v[12:15], v[216:219], v[196:199]\n\t" \
    "v_mfma_f32_16x16x32_bf16 v[200:203], v[92:95], v[216:219], v[200:203]\n\t" \
    "v_mfma_f32_16x16x32_bf16 v[196:199], v[16:19], v[216:219], v[196:199]\n\t" \
    "v_mfma_f32_16x16x32_bf16 v[200:203], v[96:99], v[216:219], v[200:203]\n\t" \
    "v_mfma_f32_16x16x32_bf16 v[196:199], v[20:23], v[216:219], v[196:199]\n\t" \
    "v_mfma_f32_16x16x32_bf16 v[200:203], v[100:103], v[216:219], v[200:203]\n\t" \
    "v_mfma_f32_16x16x32_bf16 v[196:199], v[24:27], v[216:219], v[196:199]\n\t" \
    "v_mfma_f32_16x16x32_bf16 v[200:203], v[104:107], v[216:219], v[200:203]\n\t" \
    "v_mfma_f32_16x16x32_bf16 v[196:199], v[28:31], v[216:219], v[196:199]\n\t" \
    "v_mfma_f32_16x16x32_bf16 v[200:203], v[108:111], v[216:219], v[200:203]\n\t" \
    "v_mfma_f32_16x16x32_bf16 v[196:199], v[32:35], v[216:219], v[196:199]\n\t" \
    "v_mfma_f32_16x16x32_bf16 v[200:203], v[112:115], v[216:219], v[200:203]\n\t" \
    "v_mfma_f32_16x16x32_bf16 v[196:199], v[36:39], v[216:219], v[196:199]\n\t" \
    "v_mfma_f32_16x16x32_bf16 v[200:203], v[116:119], v[216:219], v[200:203]\n\t" \
    "v_mfma_f32_16x16x32_bf16 v[196:199], v[40:43], v[216:219], v[196:199]\n\t" \
    "v_mfma_f32_16x16x32_bf16 v[200:203], v[120:123], v[216:219], v[200:203]\n\t" \
    "v_mfma_f32_16x16x32_bf16 v[196:199], v[44:47], v[216:219], v[196:199]\n\t" \
    "v_mfma_f32_16x16x32_bf16 v[200:203], v[124:127], v[216:219], v[200:203]\n\t" \
    "v_mfma_f32_16x16x32_bf16 v[196:199], v[48:51], v[216:219], v[196:199]\n\t" \
    "v_mfma_f32_16x16x32_bf16 v[200:203], v[128:131], v[216:219], v[200:203]\n\t" \
    "v_mfma_f32_16x16x32_bf16 v[196:199], v[52:55], v[216:219], v[196:199]\n\t" \
    "v_mfma_f32_16x16x32_bf16 v[200:203], v[132:135], v[216:219], v[200:203]\n\t" \
    "v_mfma_f32_16x16x32_bf16 v[196:199], v[56:59], v[216:219], v[196:199]\n\t" \
    "v_mfma_f32_16x16x32_bf16 v[200:203], v[136:139], v[216:219], v[200:203]\n\t" \
    "v_mfma_f32_16x16x32_bf16 v[196:199], v[60:63], v[216:219], v[196:199]\n\t" \
    "v_mfma_f32_16x16x32_bf16 v[200:203], v[140:143], v[216:219], v[200:203]\n\t" \
    "v_mfma_f32_16x16x32_bf16 v[196:199], v[64:67], v[216:219], v[196:199]\n\t" \
    "v_mfma_f32_16x16x32_bf16 v[200:203], v[144:147], v[216:219], v[200:203]\n\t" \
    "v_mfma_f32_16x16x32_bf16 v[196:199], v[68:71], v[216:219], v[196:199]\n\t" \
    "v_mfma_f32_16x16x32_bf16 v[200:203], v[148:151], v[216:219], v[200:203]\n\t" \
    "v_mfma_f32_16x16x32_bf16 v[196:199], v[72:75], v[216:219], v[196:199]\n\t" \
    "v_mfma_f32_16x16x32_bf16 v[200:203], v[152:155], v[216:219], v[200:203]\n\t" \
    "v_mfma_f32_16x16x32_bf16 v[196:199], v[76:79], v[216:219], v[196:199]\n\t" \
    "v_mfma_f32_16x16x32_bf16 v[200:203], v[156:159], v[216:219], v[200:203]\n\t" \
    "s_nop 7\n\t"
#define ASM_WT1_LO \
    "s_branch .Lt0%=\n\t" \
    ".Ltr%=:\n\t" \
    "s_cmp_gt_i32 %[n], 0\n\t" \
    "s_cbranch_scc0 .Ltg%=\n\t" \
    ".Lfw%=:\n\t" \
    "s_mov_b32 %[a], 0\n\t" \
    "v_max_u32 v252, v232, v233\n\t" \
    "v_max3_u32 v252, v234, v235, v252\n\t" \
    "v_cmp_eq_u32 vcc, -1, v252\n\t" \
    "s_cbranch_vccz .Lgw0_%=\n\t" \
    "buffer_load_dwordx4 v[232:235], %[l16], %[rs], %[i0] offen offset:0 sc1\n\t" \
    "s_mov_b32 %[a], 1\n\t" \
    ".Lgw0_%=:\n\t" \
    "v_max_u32 v252, v236, v237\n\t" \
    "v_max3_u32 v252, v238, v239, v252\n\t" \
    "v_cmp_eq_u32 vcc, -1, v252\n\t" \
    "s_cbranch_vccz .Lgw1_%=\n\t" \
    "buffer_load_dwordx4 v[236:239], %[l16], %[rs], %[i0] offen offset:1024 sc1\n\t" \
    "s_mov_b32 %[a], 1\n\t" \
    ".Lgw1_%=:\n\t" \
    "v_max_u32 v252, v240, v241\n\t" \
    "v_max3_u32 v252, v242, v243, v252\n\t" \
    "v_cmp_eq_u32 vcc, -1, v252\n\t" \
    "s_cbranch_vccz .Lgw2_%=\n\t" \
    "buffer_load_dwordx4 v[240:243], %[l16], %[rs], %[i0] offen offset:2048 sc1\n\t" \
    "s_mov_b32 %[a], 1\n\t" \
    ".Lgw2_%=:\n\t" \
    "v_max_u32 v252, v244, v245\n\t" \
    "v_max3_u32 v252, v246, v247, v252\n\t" \
    "v_cmp_eq_u32 vcc, -1, v252\n\t" \
    "s_cbranch_vccz .Lgw3_%=\n\t" \
    "buffer_load_dwordx4 v[244:247], %[l16], %[rs], %[i0] offen offset:3072 sc1\n\t" \
    "s_mov_b32 %[a], 1\n\t" \
    ".Lgw3_%=:\n\t" \
    "v_max_u32 v252, v248, v249\n\t" \
    "v_max3_u32 v252, v250, v251, v252\n\t" \
    "v_cmp_eq_u32 vcc, -1, v252\n\t" \
    "s_cbranch_vccz .Lgw4_%=\n\t" \
    "buffer_load_dwordx4 v[248:251], %[l16], %[rs], %[i1] offen offset:0 sc1\n\t" \
    "s_mov_b32 %[a], 1\n\t" \
    ".Lgw4_%=:\n\t" \
    "s_cmp_eq_u32 %[a], 0\n\t" \
    "s_cbranch_scc1 .Lhw%=\n\t" \
    "s_waitcnt vmcnt(20)\n\t" \
    ".Liw%=:\n\t" \
    "v_max_u32 v252, v232, v233\n\t" \
    "v_max3_u32 v252, v234, v235, v252\n\t" \
    "v_max3_u32 v252, v236, v237, v252\n\t" \
    "v_max3_u32 v252, v238, v239, v252\n\t" \
    "v_max3_u32 v252, v240, v241, v252\n\t" \
    "v_max3_u32 v252, v242, v243, v252\n\t" \
    "v_max3_u32 v252, v244, v245, v252\n\t" \
    "v_max3_u32 v252, v246, v247, v252\n\t" \
    "v_max3_u32 v252, v248, v249, v252\n\t" \
    "v_max3_u32 v252, v250, v251, v252\n\t" \
    "v_cmp_eq_u32 vcc, -1, v252\n\t" \
    "s_cbranch_vccz .Lhw%=\n\t" \
    "s_sleep 1\n\t" \
    "v_max_u32 v252, v232, v233\n\t" \
    "v_max3_u32 v252, v234, v235, v252\n\t" \
    "v_max3_u32 v252, v236, v237, v252\n\t" \
    "v_max3_u32 v252, v238, v239, v252\n\t" \
    "v_max3_u32 v252, v240, v241, v252\n\t" \
    "v_max3_u32 v252, v242, v243, v252\n\t" \
    "v_max3_u32 v252, v244, v245, v252\n\t" \
    "v_max3_u32 v252, v246, v247, v252\n\t" \
    "v_max3_u32 v252, v248, v249, v252\n\t" \
    "v_max3_u32 v252, v250, v251, v252\n\t" \
    "v_cmp_eq_u32 vcc, -1, v252\n\t" \
    "s_cbranch_vccz .Lhw%=\n\t" \
    "s_sleep 1\n\t" \
    "v_max_u32 v252, v232, v233\n\t" \
    "v_max3_u32 v252, v234, v235, v252\n\t" \
    "v_max3_u32 v252, v236, v237, v252\n\t" \
    "v_max3_u32 v252, v238, v239, v252\n\t" \
    "v_max3_u32 v252, v240, v241, v252\n\t" \
    "v_max3_u32 v252, v242, v243, v252\n\t" \
    "v_max3_u32 v252, v244, v245, v252\n\t" \
    "v_max3_u32 v252, v246, v247, v252\n\t" \
    "v_max3_u32 v252, v248, v249, v252\n\t" \
    "v_max3_u32 v252, v250, v251, v252\n\t" \
    "v_cmp_eq_u32 vcc, -1, v252\n\t" \
    "s_cbranch_vccz .Lhw%=\n\t" \
    "s_sleep 1\n\t" \
    "v_max_u32 v252, v232, v233\n\t" \
    "v_max3_u32 v252, v234, v235, v252\n\t" \
    "v_max3_u32 v252, v236, v237, v252\n\t" \
    "v_max3_u32 v252, v238, v239, v252\n\t" \
    "v_max3_u32 v252, v240, v241, v252\n\t" \
    "v_max3_u32 v252, v242, v243, v252\n\t" \
    "v_max3_u32 v252, v244, v245, v252\n\t" \
    "v_max3_u32 v252, v246, v247, v252\n\t" \
    "v_max3_u32 v252, v248, v249, v252\n\t" \
    "v_max3_u32 v252, v250, v251, v252\n\t" \
    "v_cmp_eq_u32 vcc, -1, v252\n\t" \
    "s_cbranch_vccz .Lhw%=\n\t" \
    "s_sleep 1\n\t" \
    "v_max_u32 v252, v232, v233\n\t" \
    "v_max3_u32 v252, v234, v235, v252\n\t" \
    "v_max3_u32 v252, v236, v237, v252\n\t" \
    "v_max3_u32 v252, v238, v239, v252\n\t" \
    "v_max3_u32 v252, v240, v241, v252\n\t" \
    "v_max3_u32 v252, v242, v243, v252\n\t" \
    "v_max3_u32 v252, v244, v245, v252\n\t" \
    "v_max3_u32 v252, v246, v247, v252\n\t" \
    "v_max3_u32 v252, v248, v249, v252\n\t" \
    "v_max3_u32 v252, v250, v251, v252\n\t" \
    "v_cmp_eq_u32 vcc, -1, v252\n\t" \
    "s_cbranch_vccz .Lhw%=\n\t" \
    "s_sleep 1\n\t" \
    "v_max_u32 v252, v232, v233\n\t" \
    "v_max3_u32 v252, v234, v235, v252\n\t" \
    "v_max3_u32 v252, v236, v237, v252\n\t" \
    "v_max3_u32 v252, v238, v239, v252\n\t" \
    "v_max3_u32 v252, v240, v241, v252\n\t" \
    "v_max3_u32 v252, v242, v243, v252\n\t" \
    "v_max3_u32 v252, v244, v245, v252\n\t" \
    "v_max3_u32 v252, v246, v247, v252\n\t" \
    "v_max3_u32 v252, v248, v249, v252\n\t" \
    "v_max3_u32 v252, v250, v251, v252\n\t" \
    "v_cmp_eq_u32 vcc, -1, v252\n\t" \
    "s_cbranch_vccz .Lhw%=\n\t" \
    "s_sleep 1\n\t" \
    "s_sub_u32 %[n], %[n], 1\n\t" \
    "s_cmp_gt_i32 %[n], 0\n\t" \
    "s_cbranch_scc1 .Lfw%=\n\t" \
    ".Lhw%=:\n\t" \
    "s_branch .Lta%=\n\t" \
    ".Lt0%=:\n\t" \
    "s_waitcnt vmcnt(1)\n\t" \
    ".Lta%=:\n\t" \
    "ds_write_b128 %[lw], v[232:235] offset:0\n\t" \
    "v_max_u32 v252, v232, v233\n\t" \
    "v_max3_u32 v252, v234, v235, v252\n\t" \
    "v_mfma_f32_16x16x32_bf16 v[188:191], v[232:235], v[232:235], 0\n\t" \
    "ds_write_b128 %[lw], v[236:239] offset:1024\n\t" \
    "v_max3_u32 v252, v236, v237, v252\n\t" \
    "v_max3_u32 v252, v238, v239, v252\n\t" \
    "v_mfma_f32_16x16x32_bf16 v[188:191], v[236:239], v[236:239], v[188:191]\n\t" \
    "ds_write_b128 %[lw], v[240:243] offset:2048\n\t" \
    "v_max3_u32 v252, v240, v241, v252\n\t" \
    "v_max3_u32 v252, v242, v243, v252\n\t" \
    "v_mfma_f32_16x16x32_bf16 v[188:191], v[240:243], v[240:243], v[188:191]\n\t" \
    "ds_write_b128 %[lw], v[244:247] offset:3072\n\t" \
    "v_max3_u32 v252, v244, v245, v252\n\t" \
    "v_max3_u32 v252, v246, v247, v252\n\t" \
    "v_mfma_f32_16x16x32_bf16 v[188:191], v[244:247], v[244:247], v[188:191]\n\t" \
    "ds_write_b128 %[lw], v[248:251] offset:4096\n\t" \
    "v_max3_u32 v252, v248, v249, v252\n\t" \
    "v_max3_u32 v252, v250, v251, v252\n\t" \
    "v_mfma_f32_16x16x32_bf16 v[188:191], v[248:251], v[248:251], v[188:191]\n\t" \
    "v_mfma_f32_16x16x32_bf16 v[192:195], v[216:219], v[232:235], 0\n\t" \
    "v_mfma_f32_16x16x32_bf16 v[192:195], v[216:219], v[236:239], v[192:195]\n\t" \
    "v_mfma_f32_16x16x32_bf16 v[192:195], v[216:219], v[240:243], v[192:195]\n\t" \
    "v_mfma_f32_16x16x32_bf16 v[192:195], v[216:219], v[244:247], v[192:195]\n\t" \
    "v_mfma_f32_16x16x32_bf16 v[192:195], v[216:219], v[248:251], v[192:195]\n\t" \
    "v_cmp_eq_u32 vcc, -1, v252\n\t" \
    "s_cbranch_vccnz .Ltr%=\n\t" \
    ".Ltg%=:\n\t" \
    ASM_ST2 \
    "s_nop 5\n\t" \
    "ds_write_b128 %[lw], v[188:191] offset:5120\n\t" \
    "ds_write_b128 %[lw], v[192:195] offset:6144\n\t" \
    "s_waitcnt lgkmcnt(0)\n\t" \
    "s_setprio 2\n\t" \
    "s_barrier\n\t" ASM_ST \
    "ds_read_b128 v[160:163], %[la] offset:0\n\t" \
    "ds_read_b128 v[164:167], %[la] offset:1024\n\t" \
    "ds_read_b128 v[168:171], %[la] offset:2048\n\t" \
    "ds_read2st64_b32 v[204:205], %[sr] offset0:20 offset1:24\n\t" \
    "ds_read_b128 v[172:175], %[la] offset:3072\n\t" \
    "ds_read_b128 v[176:179], %[la] offset:8192\n\t" \
    "ds_read_b128 v[180:183], %[la] offset:9216\n\t" \
    "ds_read_b128 v[184:187], %[la] offset:10240\n\t" \
    "s_waitcnt lgkmcnt(7)\n\t" \
    "v_mfma_f32_16x16x32_bf16 v[192:195], v[80:83], v[160:163], 0\n\t" \
    "s_waitcnt lgkmcnt(6)\n\t" \
    "v_mfma_f32_16x16x32_bf16 v[192:195], v[84:87], v[164:167], v[192:195]\n\t" \
    "v_mfma_f32_16x16x32_bf16 v[188:191], v[0:3], v[160:163], 0\n\t" \
    "ds_read_b128 v[160:163], %[la] offset:11264\n\t" \
    "v_mfma_f32_16x16x32_bf16 v[188:191], v[4:7], v[164:167], v[188:191]\n\t" \
    "ds_read_b128 v[164:167], %[la] offset:16384\n\t" \
    "s_waitcnt lgkmcnt(7)\n\t" \
    "v_mfma_f32_16x16x32_bf16 v[188:191], v[8:11], v[168:171], v[188:191]\n\t" \
    "s_waitcnt lgkmcnt(6)\n\t" \
    "s_waitcnt lgkmcnt(5)\n\t" \
    "v_mfma_f32_16x16x32_bf16 v[188:191], v[12:15], v[172:175], v[188:191]\n\t" \
    "v_permlane16_swap_b32 v204, v205\n\t" \
    "v_mfma_f32_16x16x32_bf16 v[192:195], v[88:91], v[168:171], v[192:195]\n\t" \
    "ds_read_b128 v[168:171], %[la] offset:17408\n\t" \
    "v_mfma_f32_16x16x32_bf16 v[192:195], v[92:95], v[172:175], v[192:195]\n\t" \
    "ds_read_b128 v[172:175], %[la] offset:18432\n\t" \
    "v_add_f32 v206, v204, v205\n\t" \
    "s_waitcnt lgkmcnt(6)\n\t" \
    "v_mfma_f32_16x16x32_bf16 v[192:195], v[96:99], v[176:179], v[192:195]\n\t" \
    "v_add_f32 v207, v204, v205\n\t" \
    "s_waitcnt lgkmcnt(5)\n\t" \
    "v_mfma_f32_16x16x32_bf16 v[192:195], v[100:103], v[180:183], v[192:195]\n\t" \
    "v_mfma_f32_16x16x32_bf16 v[188:191], v[16:19], v[176:179], v[188:191]\n\t" \
    "ds_read_b128 v[176:179], %[la] offset:19456\n\t" \
    "v_permlane32_swap_b32 v206, v207\n\t" \
    "v_mfma_f32_16x16x32_bf16 v[188:191], v[20:23], v[180:183], v[188:191]\n\t" \
    "ds_read_b128 v[180:183], %[la] offset:24576\n\t" \
    "s_waitcnt lgkmcnt(6)\n\t" \
    "v_mfma_f32_16x16x32_bf16 v[188:191], v[24:27], v[184:187], v[188:191]\n\t" \
    "v_add_f32 v204, v206, v207\n\t" \
    "s_waitcnt lgkmcnt(5)\n\t" \
    "v_mfma_f32_16x16x32_bf16 v[188:191], v[28:31], v[160:163], v[188:191]\n\t" \
    "v_add_f32 v205, v206, v207\n\t" \
    "v_mfma_f32_16x16x32_bf16 v[192:195], v[104:107], v[184:187], v[192:195]\n\t" \
    "ds_read_b128 v[184:187], %[la] offset:25600\n\t" \
    "v_mfma_f32_16x16x32_bf16 v[192:195], v[108:111], v[160:163], v[192:195]\n\t" \
    "ds_read_b128 v[160:163], %[la] offset:26624\n\t" \
    "v_permlane16_swap_b32 v204, v205\n\t" \
    "s_waitcnt lgkmcnt(6)\n\t" \
    "v_mfma_f32_16x16x32_bf16 v[192:195], v[112:115], v[164:167], v[192:195]\n\t" \
    "s_waitcnt lgkmcnt(5)\n\t" \
    "v_mfma_f32_16x16x32_bf16 v[192:195], v[116:119], v[168:171], v[192:195]\n\t" \
    "v_mul_f32 v205, %[kd], v205\n\t" \
    "v_mfma_f32_16x16x32_bf16 v[188:191], v[32:35], v[164:167], v[188:191]\n\t" \
    "ds_read_b128 v[164:167], %[la] offset:27648\n\t" \
    "v_mul_f32 v204, %[kd], v204\n\t" \
    "v_mfma_f32_16x16x32_bf16 v[188:191], v[36:39], v[168:171], v[188:191]\n\t" \
    "ds_read_b128 v[168:171], %[la] offset:4096\n\t" \
    "v_fma_f32 v204, -v205, v205, v204\n\t" \
    "s_waitcnt lgkmcnt(6)\n\t" \
    "v_mfma_f32_16x16x32_bf16 v[188:191], v[40:43], v[172:175], v[188:191]\n\t" \
    "v_max_f32 v204, 0, v204\n\t" \
    "s_waitcnt lgkmcnt(5)\n\t" \
    "v_mfma_f32_16x16x32_bf16 v[188:191], v[44:47], v[176:179], v[188:191]\n\t" \
    "v_add_f32 v206, %[ke], v204\n\t" \
    "v_mfma_f32_16x16x32_bf16 v[192:195], v[120:123], v[172:175], v[192:195]\n\t" \
    "ds_read_b128 v[172:175], %[la] offset:12288\n\t" \
    "v_rsq_f32 v206, v206\n\t" \
    "v_mfma_f32_16x16x32_bf16 v[192:195], v[124:127], v[176:179], v[192:195]\n\t" \
    "ds_read_b128 v[176:179], %[la] offset:20480\n\t" \
    "s_waitcnt lgkmcnt(6)\n\t" \
    "v_mfma_f32_16x16x32_bf16 v[192:195], v[128:131], v[180:183], v[192:195]\n\t" \
    "v_mul_f32 v207, v204, v206\n\t" \
    "s_waitcnt lgkmcnt(5)\n\t" \
    "v_mfma_f32_16x16x32_bf16 v[192:195], v[132:135], v[184:187], v[192:195]\n\t" \
    "v_fma_f32 v207, v207, v206, %[ke]\n\t" \
    "v_mfma_f32_16x16x32_bf16 v[188:191], v[48:51], v[180:183], v[188:191]\n\t" \
    "ds_read_b128 v[180:183], %[la] offset:28672\n\t" \
    "v_rsq_f32 v207, v207\n\t" \
    "v_mfma_f32_16x16x32_bf16 v[188:191], v[52:55], v[184:187], v[188:191]\n\t" \
    "v_mul_f32 v206, v206, v207\n\t" \
    "v_mul_f32 v207, -v205, v206\n\t" \
    "s_waitcnt lgkmcnt(5)\n\t" \
    "v_mfma_f32_16x16x32_bf16 v[188:191], v[56:59], v[160:163], v[188:191]\n\t" \
    "v_mul_f32 v208, v196, v207\n\t" \
    "v_mul_f32 v209, v197, v207\n\t" \
    "s_waitcnt lgkmcnt(4)\n\t" \
    "v_mfma_f32_16x16x32_bf16 v[188:191], v[60:63], v[164:167], v[188:191]\n\t" \
    "v_mul_f32 v210, v198, v207\n\t" \
    "v_mul_f32 v211, v199, v207\n\t" \
    "v_mfma_f32_16x16x32_bf16 v[192:195], v[136:139], v[160:163], v[192:195]\n\t" \
    "v_mul_f32 v212, v200, v207\n\t" \
    "v_mul_f32 v213, v201, v207\n\t" \
    "v_mfma_f32_16x16x32_bf16 v[192:195], v[140:143], v[164:167], v[192:195]\n\t" \
    "v_mul_f32 v214, v202, v207\n\t" \
    "v_mul_f32 v215, v203, v207\n\t" \
    "s_waitcnt lgkmcnt(3)\n\t" \
    "v_mfma_f32_16x16x32_bf16 v[192:195], v[144:147], v[168:171], v[192:195]\n\t" \
    "s_cmp_lg_u32 %[sw], 0\n\t" \
    "s_cbranch_scc1 .Lts%=\n\t" \
    "v_cvt_pk_bf16_f32 v217, v204, v204\n\t" \
    "v_lshlrev_b32 v217, 16, v217\n\t" \
    "v_add_f32 v217, %[ke], v217\n\t" \
    "v_cvt_pk_bf16_f32 v217, v217, v217\n\t" \
    "v_lshlrev_b32 v217, 16, v217\n\t" \
    "v_sqrt_f32 v217, v217\n\t" \
    "v_cvt_pk_bf16_f32 v216, v205, v205\n\t" \
    "v_lshlrev_b32 v216, 16, v216\n\t" \
    "v_cvt_pk_bf16_f32 v217, v217, v217\n\t" \
    "v_lshlrev_b32 v217, 16, v217\n\t" \
    "v_rcp_f32 v217, v217\n\t" \
    "v_lshrrev_b32 v218, 1, %[l16]\n\t" \
    "v_and_b32 v218, 0x78, v218\n\t" \
    "buffer_store_dwordx2 v[216:217], v218, %[rs], %[ss] offen sc1\n\t" \
    ".Lts%=:\n\t" \
    "v_mov_b64 v[216:217], -1\n\t" \
    "s_waitcnt lgkmcnt(2)\n\t" \
    "v_mfma_f32_16x16x32_bf16 v[192:195], v[148:151], v[172:175], v[192:195]\n\t" \
    "v_mov_b64 v[218:219], -1\n\t" \
    "buffer_store_dwordx4 v[216:219], %[ob], %[rs], %[sa] offen sc1\n\t" \
    "v_mfma_f32_16x16x32_bf16 v[188:191], v[64:67], v[168:171], v[188:191]\n\t" \
    "v_mfma_f32_16x16x32_bf16 v[188:191], v[68:71], v[172:175], v[188:191]\n\t" \
    "s_waitcnt lgkmcnt(1)\n\t" \
    "v_mfma_f32_16x16x32_bf16 v[188:191], v[72:75], v[176:179], v[188:191]\n\t" \
    "s_waitcnt lgkmcnt(0)\n\t" \
    "v_mfma_f32_16x16x32_bf16 v[188:191], v[76:79], v[180:183], v[188:191]\n\t" \
    "v_mfma_f32_16x16x32_bf16 v[192:195], v[152:155], v[176:179], v[192:195]\n\t" \
    "v_mfma_f32_16x16x32_bf16 v[192:195], v[156:159], v[180:183], v[192:195]\n\t" \
    "s_setprio 2\n\t" \
    "buffer_load_dwordx4 v[232:235], %[l16], %[rs], %[n0] offen offset:0 sc1\n\t" \
    "buffer_load_dwordx4 v[236:239], %[l16], %[rs], %[n0] offen offset:1024 sc1\n\t" \
    "buffer_load_dwordx4 v[240:243], %[l16], %[rs], %[n0] offen offset:2048 sc1\n\t" \
    "buffer_load_dwordx4 v[244:247], %[l16], %[rs], %[n0] offen offset:3072 sc1\n\t" \
    "buffer_load_dwordx4 v[248:251], %[l16], %[rs], %[n1] offen offset:0 sc1\n\t" \
    ASM_ST3 \
    "v_fma_f32 v188, v188, v206, v208\n\t" \
    "v_fma_f32 v189, v189, v206, v209\n\t" \
    "v_fma_f32 v190, v190, v206, v210\n\t" \
    "v_fma_f32 v191, v191, v206, v211\n\t" \
    "v_mul_f32 v160, v188, v188\n\t" \
    "v_mul_f32 v161, v189, v189\n\t" \
    "v_mul_f32 v162, v190, v190\n\t" \
    "v_mul_f32 v163, v191, v191\n\t" \
    "v_fma_f32 v160, v160, %[k1], v220\n\t" \
    "v_fma_f32 v161, v161, %[k1], v220\n\t" \
    "v_fma_f32 v162, v162, %[k1], v220\n\t" \
    "v_fma_f32 v163, v163, %[k1], v220\n\t" \
    "v_mul_f32 v160, v188, v160\n\t" \
    "v_mul_f32 v161, v189, v161\n\t" \
    "v_mul_f32 v162, v190, v162\n\t" \
    "v_mul_f32 v163, v191, v163\n\t" \
    "v_fma_f32 v192, v192, v206, v212\n\t" \
    "v_fma_f32 v193, v193, v206, v213\n\t" \
    "v_fma_f32 v194, v194, v206, v214\n\t" \
    "v_fma_f32 v195, v195, v206, v215\n\t" \
    "v_mul_f32 v164, v192, v192\n\t" \
    "v_mul_f32 v165, v193, v193\n\t" \
    "v_mul_f32 v166, v194, v194\n\t" \
    "v_mul_f32 v167, v195, v195\n\t" \
    "v_fma_f32 v164, v164, %[k1], v220\n\t" \
    "v_fma_f32 v165, v165, %[k1], v220\n\t" \
    "v_fma_f32 v166, v166, %[k1], v220\n\t" \
    "v_fma_f32 v167, v167, %[k1], v220\n\t" \
    "v_mul_f32 v164, v192, v164\n\t" \
    "v_mul_f32 v165, v193, v165\n\t" \
    "v_mul_f32 v166, v194, v166\n\t" \
    "v_mul_f32 v167, v195, v167\n\t" \
    "v_exp_f32 v160, v160\n\t" \
    "v_exp_f32 v161, v161\n\t" \
    "v_exp_f32 v162, v162\n\t" \
    "v_exp_f32 v163, v163\n\t" \
    "v_exp_f32 v164, v164\n\t" \
    "v_exp_f32 v165, v165\n\t" \
    "v_exp_f32 v166, v166\n\t" \
    "v_exp_f32 v167, v167\n\t" \
    "v_add_f32 v160, 1.0, v160\n\t" \
    "v_add_f32 v161, 1.0, v161\n\t" \
    "v_add_f32 v162, 1.0, v162\n\t" \
    "v_add_f32 v163, 1.0, v163\n\t" \
    "v_add_f32 v164, 1.0, v164\n\t" \
    "v_add_f32 v165, 1.0, v165\n\t" \
    "v_add_f32 v166, 1.0, v166\n\t" \
    "v_add_f32 v167, 1.0, v167\n\t" \
    "v_rcp_f32 v160, v160\n\t" \
    "v_rcp_f32 v161, v161\n\t" \
    "v_rcp_f32 v162, v162\n\t" \
    "v_rcp_f32 v163, v163\n\t" \
    "v_rcp_f32 v164, v164\n\t" \
    "v_rcp_f32 v165, v165\n\t" \
    "v_rcp_f32 v166, v166\n\t" \
    "v_rcp_f32 v167, v167\n\t" \
    "v_mul_f32 v188, v188, v160\n\t" \
    "v_mul_f32 v189, v189, v161\n\t" \
    "v_mul_f32 v190, v190, v162\n\t" \
    "v_mul_f32 v191, v191, v163\n\t" \
    "v_mul_f32 v192, v192, v164\n\t" \
    "v_mul_f32 v193, v193, v165\n\t" \
    "v_mul_f32 v194, v194, v166\n\t" \
    "v_mul_f32 v195, v195, v167\n\t" \
    "v_cvt_pk_bf16_f32 v188, v188, v189\n\t" \
    "v_cvt_pk_bf16_f32 v189, v190, v191\n\t" \
    "v_cvt_pk_bf16_f32 v190, v192, v193\n\t" \
    "v_cvt_pk_bf16_f32 v191, v194, v195\n\t" \
    "v_min_u32 v188, -2, v188\n\t" \
    "v_min_u32 v189, -2, v189\n\t" \
    "v_min_u32 v190, -2, v190\n\t" \
    "v_min_u32 v191, -2, v191\n\t" \
    "v_mov_b32 v216, 0x3f803f80\n\t" \
    "v_mov_b32 v217, 0x3f803f80\n\t" \
    "v_mov_b32 v218, 0x3f803f80\n\t" \
    "v_mov_b32 v219, 0x3f803f80\n\t" \
    "v_permlane16_swap_b32 v188, v190\n\t" \
    "v_permlane16_swap_b32 v189, v191\n\t" \
    "s_waitcnt vmcnt(6)\n\t" \
    "buffer_store_dwordx4 v[188:191], %[oa], %[rs], %[so] offen sc1\n\t" \
    "s_nop 1\n\t" \
    "s_setprio 0\n\t"
#define ASM_WT1_HI \
    "s_setprio 1\n\t" \
    "s_barrier\n\t" ASM_ST \
    "ds_read_b128 v[160:163], %[la] offset:0\n\t" \
    "ds_read_b128 v[164:167], %[la] offset:1024\n\t" \
    "ds_read_b128 v[168:171], %[la] offset:2048\n\t" \
    "ds_read2st64_b32 v[204:205], %[sr] offset0:20 offset1:24\n\t" \
    "ds_read_b128 v[172:175], %[la] offset:3072\n\t" \
    "ds_read_b128 v[176:179], %[la] offset:8192\n\t" \
    "ds_read_b128 v[180:183], %[la] offset:9216\n\t" \
    "ds_read_b128 v[184:187], %[la] offset:10240\n\t" \
    "s_waitcnt lgkmcnt(7)\n\t" \
    "v_mfma_f32_16x16x32_bf16 v[192:195], v[80:83], v[160:163], 0\n\t" \
    "s_waitcnt lgkmcnt(6)\n\t" \
    "v_mfma_f32_16x16x32_bf16 v[192:195], v[84:87], v[164:167], v[192:195]\n\t" \
    "v_mfma_f32_16x16x32_bf16 v[188:191], v[0:3], v[160:163], 0\n\t" \
    "ds_read_b128 v[160:163], %[la] offset:11264\n\t" \
    "v_mfma_f32_16x16x32_bf16 v[188:191], v[4:7], v[164:167], v[188:191]\n\t" \
    "ds_read_b128 v[164:167], %[la] offset:16384\n\t" \
    "s_waitcnt lgkmcnt(7)\n\t" \
    "v_mfma_f32_16x16x32_bf16 v[188:191], v[8:11], v[168:171], v[188:191]\n\t" \
    "s_waitcnt lgkmcnt(6)\n\t" \
    "s_waitcnt lgkmcnt(5)\n\t" \
    "v_mfma_f32_16x16x32_bf16 v[188:191], v[12:15], v[172:175], v[188:191]\n\t" \
    "v_permlane16_swap_b32 v204, v205\n\t" \
    "v_mfma_f32_16x16x32_bf16 v[192:195], v[88:91], v[168:171], v[192:195]\n\t" \
    "ds_read_b128 v[168:171], %[la] offset:17408\n\t" \
    "v_mfma_f32_16x16x32_bf16 v[192:195], v[92:95], v[172:175], v[192:195]\n\t" \
    "ds_read_b128 v[172:175], %[la] offset:18432\n\t" \
    "v_add_f32 v206, v204, v205\n\t" \
    "s_waitcnt lgkmcnt(6)\n\t" \
    "v_mfma_f32_16x16x32_bf16 v[192:195], v[96:99], v[176:179], v[192:195]\n\t" \
    "v_add_f32 v207, v204, v205\n\t" \
    "s_waitcnt lgkmcnt(5)\n\t" \
    "v_mfma_f32_16x16x32_bf16 v[192:195], v[100:103], v[180:183], v[192:195]\n\t" \
    "v_mfma_f32_16x16x32_bf16 v[188:191], v[16:19], v[176:179], v[188:191]\n\t" \
    "ds_read_b128 v[176:179], %[la] offset:19456\n\t" \
    "v_permlane32_swap_b32 v206, v207\n\t" \
    "v_mfma_f32_16x16x32_bf16 v[188:191], v[20:23], v[180:183], v[188:191]\n\t" \
    "ds_read_b128 v[180:183], %[la] offset:24576\n\t" \
    "s_waitcnt lgkmcnt(6)\n\t" \
    "v_mfma_f32_16x16x32_bf16 v[188:191], v[24:27], v[184:187], v[188:191]\n\t" \
    "v_add_f32 v204, v206, v207\n\t" \
    "s_waitcnt lgkmcnt(5)\n\t" \
    "v_mfma_f32_16x16x32_bf16 v[188:191], v[28:31], v[160:163], v[188:191]\n\t" \
    "v_add_f32 v205, v206, v207\n\t" \
    "v_mfma_f32_16x16x32_bf16 v[192:195], v[104:107], v[184:187], v[192:195]\n\t" \
    "ds_read_b128 v[184:187], %[la] offset:25600\n\t" \
    "v_mfma_f32_16x16x32_bf16 v[192:195], v[108:111], v[160:163], v[192:195]\n\t" \
    "ds_read_b128 v[160:163], %[la] offset:26624\n\t" \
    "v_permlane16_swap_b32 v204, v205\n\t" \
    "s_waitcnt lgkmcnt(6)\n\t" \
    "v_mfma_f32_16x16x32_bf16 v[192:195], v[112:115], v[164:167], v[192:195]\n\t" \
    "s_waitcnt lgkmcnt(5)\n\t" \
    "v_mfma_f32_16x16x32_bf16 v[192:195], v[116:119], v[168:171], v[192:195]\n\t" \
    "v_mul_f32 v205, %[kd], v205\n\t" \
    "v_mfma_f32_16x16x32_bf16 v[188:191], v[32:35], v[164:167], v[188:191]\n\t" \
    "ds_read_b128 v[164:167], %[la] offset:27648\n\t" \
    "v_mul_f32 v204, %[kd], v204\n\t" \
    "v_mfma_f32_16x16x32_bf16 v[188:191], v[36:39], v[168:171], v[188:191]\n\t" \
    "ds_read_b128 v[168:171], %[la] offset:4096\n\t" \
    "v_fma_f32 v204, -v205, v205, v204\n\t" \
    "s_waitcnt lgkmcnt(6)\n\t" \
    "v_mfma_f32_16x16x32_bf16 v[188:191], v[40:43], v[172:175], v[188:191]\n\t" \
    "v_max_f32 v204, 0, v204\n\t" \
    "s_waitcnt lgkmcnt(5)\n\t" \
    "v_mfma_f32_16x16x32_bf16 v[188:191], v[44:47], v[176:179], v[188:191]\n\t" \
    "v_add_f32 v206, %[ke], v204\n\t" \
    "v_mfma_f32_16x16x32_bf16 v[192:195], v[120:123], v[172:175], v[192:195]\n\t" \
    "ds_read_b128 v[172:175], %[la] offset:12288\n\t" \
    "v_rsq_f32 v206, v206\n\t" \
    "v_mfma_f32_16x16x32_bf16 v[192:195], v[124:127], v[176:179], v[192:195]\n\t" \
    "ds_read_b128 v[176:179], %[la] offset:20480\n\t" \
    "s_waitcnt lgkmcnt(6)\n\t" \
    "v_mfma_f32_16x16x32_bf16 v[192:195], v[128:131], v[180:183], v[192:195]\n\t" \
    "v_mul_f32 v207, v204, v206\n\t" \
    "s_waitcnt lgkmcnt(5)\n\t" \
    "v_mfma_f32_16x16x32_bf16 v[192:195], v[132:135], v[184:187], v[192:195]\n\t" \
    "v_fma_f32 v207, v207, v206, %[ke]\n\t" \
    "v_mfma_f32_16x16x32_bf16 v[188:191], v[48:51], v[180:183], v[188:191]\n\t" \
    "ds_read_b128 v[180:183], %[la] offset:28672\n\t" \
    "v_rsq_f32 v207, v207\n\t" \
    "v_mfma_f32_16x16x32_bf16 v[188:191], v[52:55], v[184:187], v[188:191]\n\t" \
    "v_mul_f32 v206, v206, v207\n\t" \
    "v_mul_f32 v207, -v205, v206\n\t" \
    "s_waitcnt lgkmcnt(5)\n\t" \
    "v_mfma_f32_16x16x32_bf16 v[188:191], v[56:59], v[160:163], v[188:191]\n\t" \
    "v_mul_f32 v208, v196, v207\n\t" \
    "v_mul_f32 v209, v197, v207\n\t" \
    "s_waitcnt lgkmcnt(4)\n\t" \
    "v_mfma_f32_16x16x32_bf16 v[188:191], v[60:63], v[164:167], v[188:191]\n\t" \
    "v_mul_f32 v210, v198, v207\n\t" \
    "v_mul_f32 v211, v199, v207\n\t" \
    "v_mfma_f32_16x16x32_bf16 v[192:195], v[136:139], v[160:163], v[192:195]\n\t" \
    "v_mul_f32 v212, v200, v207\n\t" \
    "v_mul_f32 v213, v201, v207\n\t" \
    "v_mfma_f32_16x16x32_bf16 v[192:195], v[140:143], v[164:167], v[192:195]\n\t" \
    "v_mul_f32 v214, v202, v207\n\t" \
    "v_mul_f32 v215, v203, v207\n\t" \
    "s_waitcnt lgkmcnt(3)\n\t" \
    "v_mfma_f32_16x16x32_bf16 v[192:195], v[144:147], v[168:171], v[192:195]\n\t" \
    "v_mov_b64 v[216:217], -1\n\t" \
    "v_mov_b64 v[218:219], -1\n\t" \
    "s_waitcnt lgkmcnt(2)\n\t" \
    "v_mfma_f32_16x16x32_bf16 v[192:195], v[148:151], v[172:175], v[192:195]\n\t" \
    "buffer_store_dwordx4 v[216:219], %[ob], %[rs], %[sa] offen sc1\n\t" \
    "v_mfma_f32_16x16x32_bf16 v[188:191], v[64:67], v[168:171], v[188:191]\n\t" \
    "v_mfma_f32_16x16x32_bf16 v[188:191], v[68:71], v[172:175], v[188:191]\n\t" \
    "s_waitcnt lgkmcnt(1)\n\t" \
    "v_mfma_f32_16x16x32_bf16 v[188:191], v[72:75], v[176:179], v[188:191]\n\t" \
    "s_waitcnt lgkmcnt(0)\n\t" \
    "v_mfma_f32_16x16x32_bf16 v[188:191], v[76:79], v[180:183], v[188:191]\n\t" \
    "v_mfma_f32_16x16x32_bf16 v[192:195], v[152:155], v[176:179], v[192:195]\n\t" \
    "v_mfma_f32_16x16x32_bf16 v[192:195], v[156:159], v[180:183], v[192:195]\n\t" \
    ASM_ST3 \
    "s_nop 1\n\t" \
    "v_fma_f32 v188, v188, v206, v208\n\t" \
    "v_fma_f32 v189, v189, v206, v209\n\t" \
    "v_fma_f32 v190, v190, v206, v210\n\t" \
    "v_fma_f32 v191, v191, v206, v211\n\t" \
    "v_mul_f32 v160, v188, v188\n\t" \
    "v_mul_f32 v161, v189, v189\n\t" \
    "v_mul_f32 v162, v190, v190\n\t" \
    "v_mul_f32 v163, v191, v191\n\t" \
    "v_fma_f32 v160, v160, %[k1], v220\n\t" \
    "v_fma_f32 v161, v161, %[k1], v220\n\t" \
    "v_fma_f32 v162, v162, %[k1], v220\n\t" \
    "v_fma_f32 v163, v163, %[k1], v220\n\t" \
    "v_mul_f32 v160, v188, v160\n\t" \
    "v_mul_f32 v161, v189, v161\n\t" \
    "v_mul_f32 v162, v190, v162\n\t" \
    "v_mul_f32 v163, v191, v163\n\t" \
    "v_fma_f32 v192, v192, v206, v212\n\t" \
    "v_fma_f32 v193, v193, v206, v213\n\t" \
    "v_fma_f32 v194, v194, v206, v214\n\t" \
    "v_fma_f32 v195, v195, v206, v215\n\t" \
    "v_mul_f32 v164, v192, v192\n\t" \
    "v_mul_f32 v165, v193, v193\n\t" \
    "v_mul_f32 v166, v194, v194\n\t" \
    "v_mul_f32 v167, v195, v195\n\t" \
    "v_fma_f32 v164, v164, %[k1], v220\n\t" \
    "v_fma_f32 v165, v165, %[k1], v220\n\t" \
    "v_fma_f32 v166, v166, %[k1], v220\n\t" \
    "v_fma_f32 v167, v167, %[k1], v220\n\t" \
    "v_mul_f32 v164, v192, v164\n\t" \
    "v_mul_f32 v165, v193, v165\n\t" \
    "v_mul_f32 v166, v194, v166\n\t" \
    "v_mul_f32 v167, v195, v167\n\t" \
    "v_exp_f32 v160, v160\n\t" \
    "v_exp_f32 v161, v161\n\t" \
    "v_exp_f32 v162, v162\n\t" \
    "v_exp_f32 v163, v163\n\t" \
    "v_exp_f32 v164, v164\n\t" \
    "v_exp_f32 v165, v165\n\t" \
    "v_exp_f32 v166, v166\n\t" \
    "v_exp_f32 v167, v167\n\t" \
    "v_add_f32 v160, 1.0, v160\n\t" \
    "v_add_f32 v161, 1.0, v161\n\t" \
    "v_add_f32 v162, 1.0, v162\n\t" \
    "v_add_f32 v163, 1.0, v163\n\t" \
    "v_add_f32 v164, 1.0, v164\n\t" \
    "v_add_f32 v165, 1.0, v165\n\t" \
    "v_add_f32 v166, 1.0, v166\n\t" \
    "v_add_f32 v167, 1.0, v167\n\t" \
    "v_rcp_f32 v160, v160\n\t" \
    "v_rcp_f32 v161, v161\n\t" \
    "v_rcp_f32 v162, v162\n\t" \
    "v_rcp_f32 v163, v163\n\t" \
    "v_rcp_f32 v164, v164\n\t" \
    "v_rcp_f32 v165, v165\n\t" \
    "v_rcp_f32 v166, v166\n\t" \
    "v_rcp_f32 v167, v167\n\t" \
    "v_mul_f32 v188, v188, v160\n\t" \
    "v_mul_f32 v189, v189, v161\n\t" \
    "v_mul_f32 v190, v190, v162\n\t" \
    "v_mul_f32 v191, v191, v163\n\t" \
    "v_mul_f32 v192, v192, v164\n\t" \
    "v_mul_f32 v193, v193, v165\n\t" \
    "v_mul_f32 v194, v194, v166\n\t" \
    "v_mul_f32 v195, v195, v167\n\t" \
    "v_cvt_pk_bf16_f32 v188, v188, v189\n\t" \
    "v_cvt_pk_bf16_f32 v189, v190, v191\n\t" \
    "v_cvt_pk_bf16_f32 v190, v192, v193\n\t" \
    "v_cvt_pk_bf16_f32 v191, v194, v195\n\t" \
    "v_min_u32 v188, -2, v188\n\t" \
    "v_min_u32 v189, -2, v189\n\t" \
    "v_min_u32 v190, -2, v190\n\t" \
    "v_min_u32 v191, -2, v191\n\t" \
    "s_nop 1\n\t" \
    "v_permlane16_swap_b32 v188, v190\n\t" \
    "v_permlane16_swap_b32 v189, v191\n\t" \
    "s_waitcnt vmcnt(1)\n\t" \
    "buffer_store_dwordx4 v[188:191], %[oa], %[rs], %[so] offen sc1\n\t" \
    "s_nop 1\n\t"
#define ASM_WT2_LO \
    "s_branch .Lt0%=\n\t" \
    ".Ltr%=:\n\t" \
    "s_cmp_gt_i32 %[n], 0\n\t" \
    "s_cbranch_scc0 .Ltg%=\n\t" \
    ".Lfw%=:\n\t" \
    "s_mov_b32 %[a], 0\n\t" \
    "v_max_u32 v252, v232, v233\n\t" \
    "v_max3_u32 v252, v234, v235, v252\n\t" \
    "v_cmp_eq_u32 vcc, -1, v252\n\t" \
    "s_cbranch_vccz .Lgw0_%=\n\t" \
    "buffer_load_dwordx4 v[232:235], %[l16], %[rs], %[i0] offen offset:0 sc1\n\t" \
    "s_mov_b32 %[a], 1\n\t" \
    ".Lgw0_%=:\n\t" \
    "v_max_u32 v252, v236, v237\n\t" \
    "v_max3_u32 v252, v238, v239, v252\n\t" \
    "v_cmp_eq_u32 vcc, -1, v252\n\t" \
    "s_cbranch_vccz .Lgw1_%=\n\t" \
    "buffer_load_dwordx4 v[236:239], %[l16], %[rs], %[i0] offen offset:1024 sc1\n\t" \
    "s_mov_b32 %[a], 1\n\t" \
    ".Lgw1_%=:\n\t" \
    "v_max_u32 v252, v240, v241\n\t" \
    "v_max3_u32 v252, v242, v243, v252\n\t" \
    "v_cmp_eq_u32 vcc, -1, v252\n\t" \
    "s_cbranch_vccz .Lgw2_%=\n\t" \
    "buffer_load_dwordx4 v[240:243], %[l16], %[rs], %[i0] offen offset:2048 sc1\n\t" \
    "s_mov_b32 %[a], 1\n\t" \
    ".Lgw2_%=:\n\t" \
    "v_max_u32 v252, v244, v245\n\t" \
    "v_max3_u32 v252, v246, v247, v252\n\t" \
    "v_cmp_eq_u32 vcc, -1, v252\n\t" \
    "s_cbranch_vccz .Lgw3_%=\n\t" \
    "buffer_load_dwordx4 v[244:247], %[l16], %[rs], %[i0] offen offset:3072 sc1\n\t" \
    "s_mov_b32 %[a], 1\n\t" \
    ".Lgw3_%=:\n\t" \
    "v_max_u32 v252, v248, v249\n\t" \
    "v_max3_u32 v252, v250, v251, v252\n\t" \
    "v_cmp_eq_u32 vcc, -1, v252\n\t" \
    "s_cbranch_vccz .Lgw4_%=\n\t" \
    "buffer_load_dwordx4 v[248:251], %[l16], %[rs], %[i1] offen offset:0 sc1\n\t" \
    "s_mov_b32 %[a], 1\n\t" \
    ".Lgw4_%=:\n\t" \
    "s_cmp_eq_u32 %[a], 0\n\t" \
    "s_cbranch_scc1 .Lhw%=\n\t" \
    "s_waitcnt vmcnt(20)\n\t" \
    ".Liw%=:\n\t" \
    "v_max_u32 v252, v232, v233\n\t" \
    "v_max3_u32 v252, v234, v235, v252\n\t" \
    "v_max3_u32 v252, v236, v237, v252\n\t" \
    "v_max3_u32 v252, v238, v239, v252\n\t" \
    "v_max3_u32 v252, v240, v241, v252\n\t" \
    "v_max3_u32 v252, v242, v243, v252\n\t" \
    "v_max3_u32 v252, v244, v245, v252\n\t" \
    "v_max3_u32 v252, v246, v247, v252\n\t" \
    "v_max3_u32 v252, v248, v249, v252\n\t" \
    "v_max3_u32 v252, v250, v251, v252\n\t" \
    "v_cmp_eq_u32 vcc, -1, v252\n\t" \
    "s_cbranch_vccz .Lhw%=\n\t" \
    "s_sleep 1\n\t" \
    "v_max_u32 v252, v232, v233\n\t" \
    "v_max3_u32 v252, v234, v235, v252\n\t" \
    "v_max3_u32 v252, v236, v237, v252\n\t" \
    "v_max3_u32 v252, v238, v239, v252\n\t" \
    "v_max3_u32 v252, v240, v241, v252\n\t" \
    "v_max3_u32 v252, v242, v243, v252\n\t" \
    "v_max3_u32 v252, v244, v245, v252\n\t" \
    "v_max3_u32 v252, v246, v247, v252\n\t" \
    "v_max3_u32 v252, v248, v249, v252\n\t" \
    "v_max3_u32 v252, v250, v251, v252\n\t" \
    "v_cmp_eq_u32 vcc, -1, v252\n\t" \
    "s_cbranch_vccz .Lhw%=\n\t" \
    "s_sleep 1\n\t" \
    "v_max_u32 v252, v232, v233\n\t" \
    "v_max3_u32 v252, v234, v235, v252\n\t" \
    "v_max3_u32 v252, v236, v237, v252\n\t" \
    "v_max3_u32 v252, v238, v239, v252\n\t" \
    "v_max3_u32 v252, v240, v241, v252\n\t" \
    "v_max3_u32 v252, v242, v243, v252\n\t" \
    "v_max3_u32 v252, v244, v245, v252\n\t" \
    "v_max3_u32 v252, v246, v247, v252\n\t" \
    "v_max3_u32 v252, v248, v249, v252\n\t" \
    "v_max3_u32 v252, v250, v251, v252\n\t" \
    "v_cmp_eq_u32 vcc, -1, v252\n\t" \
    "s_cbranch_vccz .Lhw%=\n\t" \
    "s_sleep 1\n\t" \
    "v_max_u32 v252, v232, v233\n\t" \
    "v_max3_u32 v252, v234, v235, v252\n\t" \
    "v_max3_u32 v252, v236, v237, v252\n\t" \
    "v_max3_u32 v252, v238, v239, v252\n\t" \
    "v_max3_u32 v252, v240, v241, v252\n\t" \
    "v_max3_u32 v252, v242, v243, v252\n\t" \
    "v_max3_u32 v252, v244, v245, v252\n\t" \
    "v_max3_u32 v252, v246, v247, v252\n\t" \
    "v_max3_u32 v252, v248, v249, v252\n\t" \
    "v_max3_u32 v252, v250, v251, v252\n\t" \
    "v_cmp_eq_u32 vcc, -1, v252\n\t" \
    "s_cbranch_vccz .Lhw%=\n\t" \
    "s_sleep 1\n\t" \
    "v_max_u32 v252, v232, v233\n\t" \
    "v_max3_u32 v252, v234, v235, v252\n\t" \
    "v_max3_u32 v252, v236, v237, v252\n\t" \
    "v_max3_u32 v252, v238, v239, v252\n\t" \
    "v_max3_u32 v252, v240, v241, v252\n\t" \
    "v_max3_u32 v252, v242, v243, v252\n\t" \
    "v_max3_u32 v252, v244, v245, v252\n\t" \
    "v_max3_u32 v252, v246, v247, v252\n\t" \
    "v_max3_u32 v252, v248, v249, v252\n\t" \
    "v_max3_u32 v252, v250, v251, v252\n\t" \
    "v_cmp_eq_u32 vcc, -1, v252\n\t" \
    "s_cbranch_vccz .Lhw%=\n\t" \
    "s_sleep 1\n\t" \
    "v_max_u32 v252, v232, v233\n\t" \
    "v_max3_u32 v252, v234, v235, v252\n\t" \
    "v_max3_u32 v252, v236, v237, v252\n\t" \
    "v_max3_u32 v252, v238, v239, v252\n\t" \
    "v_max3_u32 v252, v240, v241, v252\n\t" \
    "v_max3_u32 v252, v242, v243, v252\n\t" \
    "v_max3_u32 v252, v244, v245, v252\n\t" \
    "v_max3_u32 v252, v246, v247, v252\n\t" \
    "v_max3_u32 v252, v248, v249, v252\n\t" \
    "v_max3_u32 v252, v250, v251, v252\n\t" \
    "v_cmp_eq_u32 vcc, -1, v252\n\t" \
    "s_cbranch_vccz .Lhw%=\n\t" \
    "s_sleep 1\n\t" \
    "s_sub_u32 %[n], %[n], 1\n\t" \
    "s_cmp_gt_i32 %[n], 0\n\t" \
    "s_cbranch_scc1 .Lfw%=\n\t" \
    ".Lhw%=:\n\t" \
    "s_branch .Lta%=\n\t" \
    ".Lt0%=:\n\t" \
    "s_waitcnt vmcnt(1)\n\t" \
    ".Lta%=:\n\t" \
    "ds_write_b128 %[lw], v[232:235] offset:0\n\t" \
    "v_max_u32 v252, v232, v233\n\t" \
    "v_max3_u32 v252, v234, v235, v252\n\t" \
    "ds_write_b128 %[lw], v[236:239] offset:1024\n\t" \
    "v_max3_u32 v252, v236, v237, v252\n\t" \
    "v_max3_u32 v252, v238, v239, v252\n\t" \
    "ds_write_b128 %[lw], v[240:243] offset:2048\n\t" \
    "v_max3_u32 v252, v240, v241, v252\n\t" \
    "v_max3_u32 v252, v242, v243, v252\n\t" \
    "ds_write_b128 %[lw], v[244:247] offset:3072\n\t" \
    "v_max3_u32 v252, v244, v245, v252\n\t" \
    "v_max3_u32 v252, v246, v247, v252\n\t" \
    "ds_write_b128 %[lw], v[248:251] offset:4096\n\t" \
    "v_max3_u32 v252, v248, v249, v252\n\t" \
    "v_max3_u32 v252, v250, v251, v252\n\t" \
    "v_cmp_eq_u32 vcc, -1, v252\n\t" \
    "s_cbranch_vccnz .Ltr%=\n\t" \
    ".Ltg%=:\n\t" \
    ASM_ST2 \
    "s_waitcnt lgkmcnt(0)\n\t" \
    "s_setprio 2\n\t" \
    "s_barrier\n\t" ASM_ST \
    "ds_read_b128 v[160:163], %[la] offset:0\n\t" \
    "ds_read_b128 v[164:167], %[la] offset:1024\n\t" \
    "ds_read_b128 v[168:171], %[la] offset:2048\n\t" \
    "ds_read_b128 v[172:175], %[la] offset:3072\n\t" \
    "ds_read_b128 v[176:179], %[la] offset:8192\n\t" \
    "ds_read_b128 v[180:183], %[la] offset:9216\n\t" \
    "ds_read_b128 v[184:187], %[la] offset:10240\n\t" \
    "s_waitcnt lgkmcnt(6)\n\t" \
    "v_mfma_f32_16x16x32_bf16 v[192:195], v[80:83], v[160:163], 0\n\t" \
    "buffer_load_dwordx2 v[196:197], %[ro], %[rs], %[sx] offen sc1\n\t" \
    "s_waitcnt lgkmcnt(5)\n\t" \
    "v_mfma_f32_16x16x32_bf16 v[192:195], v[84:87], v[164:167], v[192:195]\n\t" \
    "buffer_load_dwordx2 v[198:199], %[ro], %[rs], %[sx] offen offset:512 sc1\n\t" \
    "v_mfma_f32_16x16x32_bf16 v[188:191], v[0:3], v[160:163], 0\n\t" \
    "ds_read_b128 v[160:163], %[la] offset:11264\n\t" \
    "v_lshrrev_b32 v204, 1, %[l16]\n\t" \
    "v_mfma_f32_16x16x32_bf16 v[188:191], v[4:7], v[164:167], v[188:191]\n\t" \
    "ds_read_b128 v[164:167], %[la] offset:16384\n\t" \
    "v_and_b32 v204, 0x78, v204\n\t" \
    "s_waitcnt lgkmcnt(6)\n\t" \
    "v_mfma_f32_16x16x32_bf16 v[188:191], v[8:11], v[168:171], v[188:191]\n\t" \
    "buffer_load_dwordx2 v[200:201], v204, %[rs], %[ss] offen sc1\n\t" \
    "s_waitcnt lgkmcnt(5)\n\t" \
    "v_mfma_f32_16x16x32_bf16 v[188:191], v[12:15], v[172:175], v[188:191]\n\t" \
    "v_mov_b64 v[216:217], -1\n\t" \
    "v_mfma_f32_16x16x32_bf16 v[192:195], v[88:91], v[168:171], v[192:195]\n\t" \
    "ds_read_b128 v[168:171], %[la] offset:17408\n\t" \
    "v_mov_b64 v[218:219], -1\n\t" \
    "v_mfma_f32_16x16x32_bf16 v[192:195], v[92:95], v[172:175], v[192:195]\n\t" \
    "ds_read_b128 v[172:175], %[la] offset:18432\n\t" \
    "buffer_store_dwordx4 v[216:219], %[l16], %[rs], %[sa] offen sc1\n\t" \
    "s_waitcnt lgkmcnt(6)\n\t" \
    "v_mfma_f32_16x16x32_bf16 v[192:195], v[96:99], v[176:179], v[192:195]\n\t" \
    "s_waitcnt lgkmcnt(5)\n\t" \
    "v_mfma_f32_16x16x32_bf16 v[192:195], v[100:103], v[180:183], v[192:195]\n\t" \
    "v_mfma_f32_16x16x32_bf16 v[188:191], v[16:19], v[176:179], v[188:191]\n\t" \
    "ds_read_b128 v[176:179], %[la] offset:19456\n\t" \
    "v_mfma_f32_16x16x32_bf16 v[188:191], v[20:23], v[180:183], v[188:191]\n\t" \
    "ds_read_b128 v[180:183], %[la] offset:24576\n\t" \
    "s_waitcnt lgkmcnt(6)\n\t" \
    "v_mfma_f32_16x16x32_bf16 v[188:191], v[24:27], v[184:187], v[188:191]\n\t" \
    "s_waitcnt lgkmcnt(5)\n\t" \
    "v_mfma_f32_16x16x32_bf16 v[188:191], v[28:31], v[160:163], v[188:191]\n\t" \
    "v_mfma_f32_16x16x32_bf16 v[192:195], v[104:107], v[184:187], v[192:195]\n\t" \
    "ds_read_b128 v[184:187], %[la] offset:25600\n\t" \
    "v_mfma_f32_16x16x32_bf16 v[192:195], v[108:111], v[160:163], v[192:195]\n\t" \
    "ds_read_b128 v[160:163], %[la] offset:26624\n\t" \
    "s_waitcnt lgkmcnt(6)\n\t" \
    "v_mfma_f32_16x16x32_bf16 v[192:195], v[112:115], v[164:167], v[192:195]\n\t" \
    "s_waitcnt lgkmcnt(5)\n\t" \
    "v_mfma_f32_16x16x32_bf16 v[192:195], v[116:119], v[168:171], v[192:195]\n\t" \
    "v_mfma_f32_16x16x32_bf16 v[188:191], v[32:35], v[164:167], v[188:191]\n\t" \
    "ds_read_b128 v[164:167], %[la] offset:27648\n\t" \
    "v_mfma_f32_16x16x32_bf16 v[188:191], v[36:39], v[168:171], v[188:191]\n\t" \
    "ds_read_b128 v[168:171], %[la] offset:4096\n\t" \
    "s_waitcnt lgkmcnt(6)\n\t" \
    "v_mfma_f32_16x16x32_bf16 v[188:191], v[40:43], v[172:175], v[188:191]\n\t" \
    "s_waitcnt lgkmcnt(5)\n\t" \
    "v_mfma_f32_16x16x32_bf16 v[188:191], v[44:47], v[176:179], v[188:191]\n\t" \
    "v_mfma_f32_16x16x32_bf16 v[192:195], v[120:123], v[172:175], v[192:195]\n\t" \
    "ds_read_b128 v[172:175], %[la] offset:12288\n\t" \
    "v_mfma_f32_16x16x32_bf16 v[192:195], v[124:127], v[176:179], v[192:195]\n\t" \
    "ds_read_b128 v[176:179], %[la] offset:20480\n\t" \
    "s_waitcnt lgkmcnt(6)\n\t" \
    "v_mfma_f32_16x16x32_bf16 v[192:195], v[128:131], v[180:183], v[192:195]\n\t" \
    "s_waitcnt lgkmcnt(5)\n\t" \
    "v_mfma_f32_16x16x32_bf16 v[192:195], v[132:135], v[184:187], v[192:195]\n\t" \
    "v_mfma_f32_16x16x32_bf16 v[188:191], v[48:51], v[180:183], v[188:191]\n\t" \
    "ds_read_b128 v[180:183], %[la] offset:28672\n\t" \
    "v_mfma_f32_16x16x32_bf16 v[188:191], v[52:55], v[184:187], v[188:191]\n\t" \
    "s_waitcnt lgkmcnt(5)\n\t" \
    "v_mfma_f32_16x16x32_bf16 v[188:191], v[56:59], v[160:163], v[188:191]\n\t" \
    "s_waitcnt lgkmcnt(4)\n\t" \
    "v_mfma_f32_16x16x32_bf16 v[188:191], v[60:63], v[164:167], v[188:191]\n\t" \
    "v_mfma_f32_16x16x32_bf16 v[192:195], v[136:139], v[160:163], v[192:195]\n\t" \
    "v_mfma_f32_16x16x32_bf16 v[192:195], v[140:143], v[164:167], v[192:195]\n\t" \
    "s_waitcnt lgkmcnt(3)\n\t" \
    "v_mfma_f32_16x16x32_bf16 v[192:195], v[144:147], v[168:171], v[192:195]\n\t" \
    "s_waitcnt lgkmcnt(2)\n\t" \
    "v_mfma_f32_16x16x32_bf16 v[192:195], v[148:151], v[172:175], v[192:195]\n\t" \
    "v_mfma_f32_16x16x32_bf16 v[188:191], v[64:67], v[168:171], v[188:191]\n\t" \
    "v_mfma_f32_16x16x32_bf16 v[188:191], v[68:71], v[172:175], v[188:191]\n\t" \
    "s_waitcnt lgkmcnt(1)\n\t" \
    "v_mfma_f32_16x16x32_bf16 v[188:191], v[72:75], v[176:179], v[188:191]\n\t" \
    "s_waitcnt lgkmcnt(0)\n\t" \
    "v_mfma_f32_16x16x32_bf16 v[188:191], v[76:79], v[180:183], v[188:191]\n\t" \
    "v_mfma_f32_16x16x32_bf16 v[192:195], v[152:155], v[176:179], v[192:195]\n\t" \
    "v_mfma_f32_16x16x32_bf16 v[192:195], v[156:159], v[180:183], v[192:195]\n\t" \
    "s_setprio 2\n\t" \
    "buffer_load_dwordx4 v[232:235], %[l16], %[rs], %[n0] offen offset:0 sc1\n\t" \
    "buffer_load_dwordx4 v[236:239], %[l16], %[rs], %[n0] offen offset:1024 sc1\n\t" \
    "buffer_load_dwordx4 v[240:243], %[l16], %[rs], %[n0] offen offset:2048 sc1\n\t" \
    "buffer_load_dwordx4 v[244:247], %[l16], %[rs], %[n0] offen offset:3072 sc1\n\t" \
    "buffer_load_dwordx4 v[248:251], %[l16], %[rs], %[n1] offen offset:0 sc1\n\t" \
    ASM_ST3 \
    "s_waitcnt vmcnt(6)\n\t" \
    "v_lshlrev_b32 v160, 16, v196\n\t" \
    "v_and_b32 v161, 0xffff0000, v196\n\t" \
    "v_lshlrev_b32 v162, 16, v197\n\t" \
    "v_and_b32 v163, 0xffff0000, v197\n\t" \
    "v_lshlrev_b32 v164, 16, v198\n\t" \
    "v_and_b32 v165, 0xffff0000, v198\n\t" \
    "v_lshlrev_b32 v166, 16, v199\n\t" \
    "v_and_b32 v167, 0xffff0000, v199\n\t" \
    "v_sub_f32 v160, v160, v200\n\t" \
    "v_sub_f32 v161, v161, v200\n\t" \
    "v_sub_f32 v162, v162, v200\n\t" \
    "v_sub_f32 v163, v163, v200\n\t" \
    "v_sub_f32 v164, v164, v200\n\t" \
    "v_sub_f32 v165, v165, v200\n\t" \
    "v_sub_f32 v166, v166, v200\n\t" \
    "v_sub_f32 v167, v167, v200\n\t" \
    "v_mul_f32 v160, v160, v201\n\t" \
    "v_mul_f32 v161, v161, v201\n\t" \
    "v_mul_f32 v162, v162, v201\n\t" \
    "v_mul_f32 v163, v163, v201\n\t" \
    "v_mul_f32 v164, v164, v201\n\t" \
    "v_mul_f32 v165, v165, v201\n\t" \
    "v_mul_f32 v166, v166, v201\n\t" \
    "v_mul_f32 v167, v167, v201\n\t" \
    "v_cvt_pk_bf16_f32 v160, v160, v161\n\t" \
    "v_cvt_pk_bf16_f32 v162, v162, v163\n\t" \
    "v_cvt_pk_bf16_f32 v164, v164, v165\n\t" \
    "v_cvt_pk_bf16_f32 v166, v166, v167\n\t" \
    "v_and_b32 v161, 0xffff0000, v160\n\t" \
    "v_lshlrev_b32 v160, 16, v160\n\t" \
    "v_and_b32 v163, 0xffff0000, v162\n\t" \
    "v_lshlrev_b32 v162, 16, v162\n\t" \
    "v_and_b32 v165, 0xffff0000, v164\n\t" \
    "v_lshlrev_b32 v164, 16, v164\n\t" \
    "v_and_b32 v167, 0xffff0000, v166\n\t" \
    "v_lshlrev_b32 v166, 16, v166\n\t" \
    "v_add_f32 v188, v188, v160\n\t" \
    "v_add_f32 v189, v189, v161\n\t" \
    "v_add_f32 v190, v190, v162\n\t" \
    "v_add_f32 v191, v191, v163\n\t" \
    "v_add_f32 v192, v192, v164\n\t" \
    "v_add_f32 v193, v193, v165\n\t" \
    "v_add_f32 v194, v194, v166\n\t" \
    "v_add_f32 v195, v195, v167\n\t" \
    "v_cvt_pk_bf16_f32 v188, v188, v189\n\t" \
    "v_cvt_pk_bf16_f32 v189, v190, v191\n\t" \
    "v_cvt_pk_bf16_f32 v190, v192, v193\n\t" \
    "v_cvt_pk_bf16_f32 v191, v194, v195\n\t" \
    "v_min_u32 v188, -2, v188\n\t" \
    "v_min_u32 v189, -2, v189\n\t" \
    "v_min_u32 v190, -2, v190\n\t" \
    "v_min_u32 v191, -2, v191\n\t" \
    "s_nop 1\n\t" \
    "v_permlane16_swap_b32 v188, v190\n\t" \
    "v_permlane16_swap_b32 v189, v191\n\t" \
    "s_waitcnt vmcnt(6)\n\t" \
    "buffer_store_dwordx4 v[188:191], %[oa], %[rs], %[so] offen sc1\n\t" \
    "s_nop 1\n\t" \
    "s_setprio 0\n\t"
#define ASM_WT2_HI \
    "s_setprio 1\n\t" \
    "s_barrier\n\t" ASM_ST \
    "ds_read_b128 v[160:163], %[la] offset:0\n\t" \
    "ds_read_b128 v[164:167], %[la] offset:1024\n\t" \
    "ds_read_b128 v[168:171], %[la] offset:2048\n\t" \
    "ds_read_b128 v[172:175], %[la] offset:3072\n\t" \
    "ds_read_b128 v[176:179], %[la] offset:8192\n\t" \
    "ds_read_b128 v[180:183], %[la] offset:9216\n\t" \
    "ds_read_b128 v[184:187], %[la] offset:10240\n\t" \
    "s_waitcnt lgkmcnt(6)\n\t" \
    "v_mfma_f32_16x16x32_bf16 v[192:195], v[80:83], v[160:163], 0\n\t" \
    "buffer_load_dwordx2 v[196:197], %[ro], %[rs], %[sx] offen sc1\n\t" \
    "s_waitcnt lgkmcnt(5)\n\t" \
    "v_mfma_f32_16x16x32_bf16 v[192:195], v[84:87], v[164:167], v[192:195]\n\t" \
    "buffer_load_dwordx2 v[198:199], %[ro], %[rs], %[sx] offen offset:512 sc1\n\t" \
    "v_mfma_f32_16x16x32_bf16 v[188:191], v[0:3], v[160:163], 0\n\t" \
    "ds_read_b128 v[160:163], %[la] offset:11264\n\t" \
    "v_lshrrev_b32 v204, 1, %[l16]\n\t" \
    "v_mfma_f32_16x16x32_bf16 v[188:191], v[4:7], v[164:167], v[188:191]\n\t" \
    "ds_read_b128 v[164:167], %[la] offset:16384\n\t" \
    "v_and_b32 v204, 0x78, v204\n\t" \
    "s_waitcnt lgkmcnt(6)\n\t" \
    "v_mfma_f32_16x16x32_bf16 v[188:191], v[8:11], v[168:171], v[188:191]\n\t" \
    "buffer_load_dwordx2 v[200:201], v204, %[rs], %[ss] offen sc1\n\t" \
    "s_waitcnt lgkmcnt(5)\n\t" \
    "v_mfma_f32_16x16x32_bf16 v[188:191], v[12:15], v[172:175], v[188:191]\n\t" \
    "v_mov_b64 v[216:217], -1\n\t" \
    "v_mfma_f32_16x16x32_bf16 v[192:195], v[88:91], v[168:171], v[192:195]\n\t" \
    "ds_read_b128 v[168:171], %[la] offset:17408\n\t" \
    "v_mov_b64 v[218:219], -1\n\t" \
    "v_mfma_f32_16x16x32_bf16 v[192:195], v[92:95], v[172:175], v[192:195]\n\t" \
    "ds_read_b128 v[172:175], %[la] offset:18432\n\t" \
    "buffer_store_dwordx4 v[216:219], %[l16], %[rs], %[sa] offen sc1\n\t" \
    "s_waitcnt lgkmcnt(6)\n\t" \
    "v_mfma_f32_16x16x32_bf16 v[192:195], v[96:99], v[176:179], v[192:195]\n\t" \
    "s_waitcnt lgkmcnt(5)\n\t" \
    "v_mfma_f32_16x16x32_bf16 v[192:195], v[100:103], v[180:183], v[192:195]\n\t" \
    "v_mfma_f32_16x16x32_bf16 v[188:191], v[16:19], v[176:179], v[188:191]\n\t" \
    "ds_read_b128 v[176:179], %[la] offset:19456\n\t" \
    "v_mfma_f32_16x16x32_bf16 v[188:191], v[20:23], v[180:183], v[188:191]\n\t" \
    "ds_read_b128 v[180:183], %[la] offset:24576\n\t" \
    "s_waitcnt lgkmcnt(6)\n\t" \
    "v_mfma_f32_16x16x32_bf16 v[188:191], v[24:27], v[184:187], v[188:191]\n\t" \
    "s_waitcnt lgkmcnt(5)\n\t" \
    "v_mfma_f32_16x16x32_bf16 v[188:191], v[28:31], v[160:163], v[188:191]\n\t" \
    "v_mfma_f32_16x16x32_bf16 v[192:195], v[104:107], v[184:187], v[192:195]\n\t" \
    "ds_read_b128 v[184:187], %[la] offset:25600\n\t" \
    "v_mfma_f32_16x16x32_bf16 v[192:195], v[108:111], v[160:163], v[192:195]\n\t" \
    "ds_read_b128 v[160:163], %[la] offset:26624\n\t" \
    "s_waitcnt lgkmcnt(6)\n\t" \
    "v_mfma_f32_16x16x32_bf16 v[192:195], v[112:115], v[164:167], v[192:195]\n\t" \
    "s_waitcnt lgkmcnt(5)\n\t" \
    "v_mfma_f32_16x16x32_bf16 v[192:195], v[116:119], v[168:171], v[192:195]\n\t" \
    "v_mfma_f32_16x16x32_bf16 v[188:191], v[32:35], v[164:167], v[188:191]\n\t" \
    "ds_read_b128 v[164:167], %[la] offset:27648\n\t" \
    "v_mfma_f32_16x16x32_bf16 v[188:191], v[36:39], v[168:171], v[188:191]\n\t" \
    "ds_read_b128 v[168:171], %[la] offset:4096\n\t" \
    "s_waitcnt lgkmcnt(6)\n\t" \
    "v_mfma_f32_16x16x32_bf16 v[188:191], v[40:43], v[172:175], v[188:191]\n\t" \
    "s_waitcnt lgkmcnt(5)\n\t" \
    "v_mfma_f32_16x16x32_bf16 v[188:191], v[44:47], v[176:179], v[188:191]\n\t" \
    "v_mfma_f32_16x16x32_bf16 v[192:195], v[120:123], v[172:175], v[192:195]\n\t" \
    "ds_read_b128 v[172:175], %[la] offset:12288\n\t" \
    "v_mfma_f32_16x16x32_bf16 v[192:195], v[124:127], v[176:179], v[192:195]\n\t" \
    "ds_read_b128 v[176:179], %[la] offset:20480\n\t" \
    "s_waitcnt lgkmcnt(6)\n\t" \
    "v_mfma_f32_16x16x32_bf16 v[192:195], v[128:131], v[180:183], v[192:195]\n\t" \
    "s_waitcnt lgkmcnt(5)\n\t" \
    "v_mfma_f32_16x16x32_bf16 v[192:195], v[132:135], v[184:187], v[192:195]\n\t" \
    "v_mfma_f32_16x16x32_bf16 v[188:191], v[48:51], v[180:183], v[188:191]\n\t" \
    "ds_read_b128 v[180:183], %[la] offset:28672\n\t" \
    "v_mfma_f32_16x16x32_bf16 v[188:191], v[52:55], v[184:187], v[188:191]\n\t" \
    "s_waitcnt lgkmcnt(5)\n\t" \
    "v_mfma_f32_16x16x32_bf16 v[188:191], v[56:59], v[160:163], v[188:191]\n\t" \
    "s_waitcnt lgkmcnt(4)\n\t" \
    "v_mfma_f32_16x16x32_bf16 v[188:191], v[60:63], v[164:167], v[188:191]\n\t" \
    "v_mfma_f32_16x16x32_bf16 v[192:195], v[136:139], v[160:163], v[192:195]\n\t" \
    "v_mfma_f32_16x16x32_bf16 v[192:195], v[140:143], v[164:167], v[192:195]\n\t" \
    "s_waitcnt lgkmcnt(3)\n\t" \
    "v_mfma_f32_16x16x32_bf16 v[192:195], v[144:147], v[168:171], v[192:195]\n\t" \
    "s_waitcnt lgkmcnt(2)\n\t" \
    "v_mfma_f32_16x16x32_bf16 v[192:195], v[148:151], v[172:175], v[192:195]\n\t" \
    "v_mfma_f32_16x16x32_bf16 v[188:191], v[64:67], v[168:171], v[188:191]\n\t" \
    "v_mfma_f32_16x16x32_bf16 v[188:191], v[68:71], v[172:175], v[188:191]\n\t" \
    "s_waitcnt lgkmcnt(1)\n\t" \
    "v_mfma_f32_16x16x32_bf16 v[188:191], v[72:75], v[176:179], v[188:191]\n\t" \
    "s_waitcnt lgkmcnt(0)\n\t" \
    "v_mfma_f32_16x16x32_bf16 v[188:191], v[76:79], v[180:183], v[188:191]\n\t" \
    "v_mfma_f32_16x16x32_bf16 v[192:195], v[152:155], v[176:179], v[192:195]\n\t" \
    "v_mfma_f32_16x16x32_bf16 v[192:195], v[156:159], v[180:183], v[192:195]\n\t" \
    ASM_ST3 \
    "s_waitcnt vmcnt(1)\n\t" \
    "v_lshlrev_b32 v160, 16, v196\n\t" \
    "v_and_b32 v161, 0xffff0000, v196\n\t" \
    "v_lshlrev_b32 v162, 16, v197\n\t" \
    "v_and_b32 v163, 0xffff0000, v197\n\t" \
    "v_lshlrev_b32 v164, 16, v198\n\t" \
    "v_and_b32 v165, 0xffff0000, v198\n\t" \
    "v_lshlrev_b32 v166, 16, v199\n\t" \
    "v_and_b32 v167, 0xffff0000, v199\n\t" \
    "v_sub_f32 v160, v160, v200\n\t" \
    "v_sub_f32 v161, v161, v200\n\t" \
    "v_sub_f32 v162, v162, v200\n\t" \
    "v_sub_f32 v163, v163, v200\n\t" \
    "v_sub_f32 v164, v164, v200\n\t" \
    "v_sub_f32 v165, v165, v200\n\t" \
    "v_sub_f32 v166, v166, v200\n\t" \
    "v_sub_f32 v167, v167, v200\n\t" \
    "v_mul_f32 v160, v160, v201\n\t" \
    "v_mul_f32 v161, v161, v201\n\t" \
    "v_mul_f32 v162, v162, v201\n\t" \
    "v_mul_f32 v163, v163, v201\n\t" \
    "v_mul_f32 v164, v164, v201\n\t" \
    "v_mul_f32 v165, v165, v201\n\t" \
    "v_mul_f32 v166, v166, v201\n\t" \
    "v_mul_f32 v167, v167, v201\n\t" \
    "v_cvt_pk_bf16_f32 v160, v160, v161\n\t" \
    "v_cvt_pk_bf16_f32 v162, v162, v163\n\t" \
    "v_cvt_pk_bf16_f32 v164, v164, v165\n\t" \
    "v_cvt_pk_bf16_f32 v166, v166, v167\n\t" \
    "v_and_b32 v161, 0xffff0000, v160\n\t" \
    "v_lshlrev_b32 v160, 16, v160\n\t" \
    "v_and_b32 v163, 0xffff0000, v162\n\t" \
    "v_lshlrev_b32 v162, 16, v162\n\t" \
    "v_and_b32 v165, 0xffff0000, v164\n\t" \
    "v_lshlrev_b32 v164, 16, v164\n\t" \
    "v_and_b32 v167, 0xffff0000, v166\n\t" \
    "v_lshlrev_b32 v166, 16, v166\n\t" \
    "v_add_f32 v188, v188, v160\n\t" \
    "v_add_f32 v189, v189, v161\n\t" \
    "v_add_f32 v190, v190, v162\n\t" \
    "v_add_f32 v191, v191, v163\n\t" \
    "v_add_f32 v192, v192, v164\n\t" \
    "v_add_f32 v193, v193, v165\n\t" \
    "v_add_f32 v194, v194, v166\n\t" \
    "v_add_f32 v195, v195, v167\n\t" \
    "v_cvt_pk_bf16_f32 v188, v188, v189\n\t" \
    "v_cvt_pk_bf16_f32 v189, v190, v191\n\t" \
    "v_cvt_pk_bf16_f32 v190, v192, v193\n\t" \
    "v_cvt_pk_bf16_f32 v191, v194, v195\n\t" \
    "v_min_u32 v188, -2, v188\n\t" \
    "v_min_u32 v189, -2, v189\n\t" \
    "v_min_u32 v190, -2, v190\n\t" \
    "v_min_u32 v191, -2, v191\n\t" \
    "s_nop 1\n\t" \
    "v_permlane16_swap_b32 v188, v190\n\t" \
    "v_permlane16_swap_b32 v189, v191\n\t" \
    "s_waitcnt vmcnt(1)\n\t" \
    "buffer_store_dwordx4 v[188:191], %[oa], %[rs], %[so] offen sc1\n\t" \
    "s_nop 1\n\t"
#define ASM_WT3_LO \
    "s_branch .Lt0%=\n\t" \
    ".Ltr%=:\n\t" \
    "s_cmp_gt_i32 %[n], 0\n\t" \
    "s_cbranch_scc0 .Ltg%=\n\t" \
    ".Lfw%=:\n\t" \
    "s_mov_b32 %[a], 0\n\t" \
    "v_max_u32 v252, v232, v233\n\t" \
    "v_max3_u32 v252, v234, v235, v252\n\t" \
    "v_cmp_eq_u32 vcc, -1, v252\n\t" \
    "s_cbranch_vccz .Lgw0_%=\n\t" \
    "buffer_load_dwordx4 v[232:235], %[l16], %[rs], %[i0] offen offset:0 sc1\n\t" \
    "s_mov_b32 %[a], 1\n\t" \
    ".Lgw0_%=:\n\t" \
    "v_max_u32 v252, v236, v237\n\t" \
    "v_max3_u32 v252, v238, v239, v252\n\t" \
    "v_cmp_eq_u32 vcc, -1, v252\n\t" \
    "s_cbranch_vccz .Lgw1_%=\n\t" \
    "buffer_load_dwordx4 v[236:239], %[l16], %[rs], %[i0] offen offset:1024 sc1\n\t" \
    "s_mov_b32 %[a], 1\n\t" \
    ".Lgw1_%=:\n\t" \
    "v_max_u32 v252, v240, v241\n\t" \
    "v_max3_u32 v252, v242, v243, v252\n\t" \
    "v_cmp_eq_u32 vcc, -1, v252\n\t" \
    "s_cbranch_vccz .Lgw2_%=\n\t" \
    "buffer_load_dwordx4 v[240:243], %[l16], %[rs], %[i0] offen offset:2048 sc1\n\t" \
    "s_mov_b32 %[a], 1\n\t" \
    ".Lgw2_%=:\n\t" \
    "v_max_u32 v252, v244, v245\n\t" \
    "v_max3_u32 v252, v246, v247, v252\n\t" \
    "v_cmp_eq_u32 vcc, -1, v252\n\t" \
    "s_cbranch_vccz .Lgw3_%=\n\t" \
    "buffer_load_dwordx4 v[244:247], %[l16], %[rs], %[i0] offen offset:3072 sc1\n\t" \
    "s_mov_b32 %[a], 1\n\t" \
    ".Lgw3_%=:\n\t" \
    "v_max_u32 v252, v248, v249\n\t" \
    "v_max3_u32 v252, v250, v251, v252\n\t" \
    "v_cmp_eq_u32 vcc, -1, v252\n\t" \
    "s_cbranch_vccz .Lgw4_%=\n\t" \
    "buffer_load_dwordx4 v[248:251], %[l16], %[rs], %[i1] offen offset:0 sc1\n\t" \
    "s_mov_b32 %[a], 1\n\t" \
    ".Lgw4_%=:\n\t" \
    "s_cmp_eq_u32 %[a], 0\n\t" \
    "s_cbranch_scc1 .Lhw%=\n\t" \
    "s_waitcnt vmcnt(20)\n\t" \
    ".Liw%=:\n\t" \
    "v_max_u32 v252, v232, v233\n\t" \
    "v_max3_u32 v252, v234, v235, v252\n\t" \
    "v_max3_u32 v252, v236, v237, v252\n\t" \
    "v_max3_u32 v252, v238, v239, v252\n\t" \
    "v_max3_u32 v252, v240, v241, v252\n\t" \
    "v_max3_u32 v252, v242, v243, v252\n\t" \
    "v_max3_u32 v252, v244, v245, v252\n\t" \
    "v_max3_u32 v252, v246, v247, v252\n\t" \
    "v_max3_u32 v252, v248, v249, v252\n\t" \
    "v_max3_u32 v252, v250, v251, v252\n\t" \
    "v_cmp_eq_u32 vcc, -1, v252\n\t" \
    "s_cbranch_vccz .Lhw%=\n\t" \
    "s_sleep 1\n\t" \
    "v_max_u32 v252, v232, v233\n\t" \
    "v_max3_u32 v252, v234, v235, v252\n\t" \
    "v_max3_u32 v252, v236, v237, v252\n\t" \
    "v_max3_u32 v252, v238, v239, v252\n\t" \
    "v_max3_u32 v252, v240, v241, v252\n\t" \
    "v_max3_u32 v252, v242, v243, v252\n\t" \
    "v_max3_u32 v252, v244, v245, v252\n\t" \
    "v_max3_u32 v252, v246, v247, v252\n\t" \
    "v_max3_u32 v252, v248, v249, v252\n\t" \
    "v_max3_u32 v252, v250, v251, v252\n\t" \
    "v_cmp_eq_u32 vcc, -1, v252\n\t" \
    "s_cbranch_vccz .Lhw%=\n\t" \
    "s_sleep 1\n\t" \
    "v_max_u32 v252, v232, v233\n\t" \
    "v_max3_u32 v252, v234, v235, v252\n\t" \
    "v_max3_u32 v252, v236, v237, v252\n\t" \
    "v_max3_u32 v252, v238, v239, v252\n\t" \
    "v_max3_u32 v252, v240, v241, v252\n\t" \
    "v_max3_u32 v252, v242, v243, v252\n\t" \
    "v_max3_u32 v252, v244, v245, v252\n\t" \
    "v_max3_u32 v252, v246, v247, v252\n\t" \
    "v_max3_u32 v252, v248, v249, v252\n\t" \
    "v_max3_u32 v252, v250, v251, v252\n\t" \
    "v_cmp_eq_u32 vcc, -1, v252\n\t" \
    "s_cbranch_vccz .Lhw%=\n\t" \
    "s_sleep 1\n\t" \
    "v_max_u32 v252, v232, v233\n\t" \
    "v_max3_u32 v252, v234, v235, v252\n\t" \
    "v_max3_u32 v252, v236, v237, v252\n\t" \
    "v_max3_u32 v252, v238, v239, v252\n\t" \
    "v_max3_u32 v252, v240, v241, v252\n\t" \
    "v_max3_u32 v252, v242, v243, v252\n\t" \
    "v_max3_u32 v252, v244, v245, v252\n\t" \
    "v_max3_u32 v252, v246, v247, v252\n\t" \
    "v_max3_u32 v252, v248, v249, v252\n\t" \
    "v_max3_u32 v252, v250, v251, v252\n\t" \
    "v_cmp_eq_u32 vcc, -1, v252\n\t" \
    "s_cbranch_vccz .Lhw%=\n\t" \
    "s_sleep 1\n\t" \
    "v_max_u32 v252, v232, v233\n\t" \
    "v_max3_u32 v252, v234, v235, v252\n\t" \
    "v_max3_u32 v252, v236, v237, v252\n\t" \
    "v_max3_u32 v252, v238, v239, v252\n\t" \
    "v_max3_u32 v252, v240, v241, v252\n\t" \
    "v_max3_u32 v252, v242, v243, v252\n\t" \
    "v_max3_u32 v252, v244, v245, v252\n\t" \
    "v_max3_u32 v252, v246, v247, v252\n\t" \
    "v_max3_u32 v252, v248, v249, v252\n\t" \
    "v_max3_u32 v252, v250, v251, v252\n\t" \
    "v_cmp_eq_u32 vcc, -1, v252\n\t" \
    "s_cbranch_vccz .Lhw%=\n\t" \
    "s_sleep 1\n\t" \
    "v_max_u32 v252, v232, v233\n\t" \
    "v_max3_u32 v252, v234, v235, v252\n\t" \
    "v_max3_u32 v252, v236, v237, v252\n\t" \
    "v_max3_u32 v252, v238, v239, v252\n\t" \
    "v_max3_u32 v252, v240, v241, v252\n\t" \
    "v_max3_u32 v252, v242, v243, v252\n\t" \
    "v_max3_u32 v252, v244, v245, v252\n\t" \
    "v_max3_u32 v252, v246, v247, v252\n\t" \
    "v_max3_u32 v252, v248, v249, v252\n\t" \
    "v_max3_u32 v252, v250, v251, v252\n\t" \
    "v_cmp_eq_u32 vcc, -1, v252\n\t" \
    "s_cbranch_vccz .Lhw%=\n\t" \
    "s_sleep 1\n\t" \
    "s_sub_u32 %[n], %[n], 1\n\t" \
    "s_cmp_gt_i32 %[n], 0\n\t" \
    "s_cbranch_scc1 .Lfw%=\n\t" \
    ".Lhw%=:\n\t" \
    "s_branch .Lta%=\n\t" \
    ".Lt0%=:\n\t" \
    "s_waitcnt vmcnt(1)\n\t" \
    ".Lta%=:\n\t" \
    "ds_write_b128 %[lw], v[232:235] offset:0\n\t" \
    "v_max_u32 v252, v232, v233\n\t" \
    "v_max3_u32 v252, v234, v235, v252\n\t" \
    "v_mfma_f32_16x16x32_bf16 v[188:191], v[232:235], v[232:235], 0\n\t" \
    "ds_write_b128 %[lw], v[236:239] offset:1024\n\t" \
    "v_max3_u32 v252, v236, v237, v252\n\t" \
    "v_max3_u32 v252, v238, v239, v252\n\t" \
    "v_mfma_f32_16x16x32_bf16 v[188:191], v[236:239], v[236:239], v[188:191]\n\t" \
    "ds_write_b128 %[lw], v[240:243] offset:2048\n\t" \
    "v_max3_u32 v252, v240, v241, v252\n\t" \
    "v_max3_u32 v252, v242, v243, v252\n\t" \
    "v_mfma_f32_16x16x32_bf16 v[188:191], v[240:243], v[240:243], v[188:191]\n\t" \
    "ds_write_b128 %[lw], v[244:247] offset:3072\n\t" \
    "v_max3_u32 v252, v244, v245, v252\n\t" \
    "v_max3_u32 v252, v246, v247, v252\n\t" \
    "v_mfma_f32_16x16x32_bf16 v[188:191], v[244:247], v[244:247], v[188:191]\n\t" \
    "ds_write_b128 %[lw], v[248:251] offset:4096\n\t" \
    "v_max3_u32 v252, v248, v249, v252\n\t" \
    "v_max3_u32 v252, v250, v251, v252\n\t" \
    "v_mfma_f32_16x16x32_bf16 v[188:191], v[248:251], v[248:251], v[188:191]\n\t" \
    "v_mfma_f32_16x16x32_bf16 v[192:195], v[216:219], v[232:235], 0\n\t" \
    "v_mfma_f32_16x16x32_bf16 v[192:195], v[216:219], v[236:239], v[192:195]\n\t" \
    "v_mfma_f32_16x16x32_bf16 v[192:195], v[216:219], v[240:243], v[192:195]\n\t" \
    "v_mfma_f32_16x16x32_bf16 v[192:195], v[216:219], v[244:247], v[192:195]\n\t" \
    "v_mfma_f32_16x16x32_bf16 v[192:195], v[216:219], v[248:251], v[192:195]\n\t" \
    "v_cmp_eq_u32 vcc, -1, v252\n\t" \
    "s_cbranch_vccnz .Ltr%=\n\t" \
    ".Ltg%=:\n\t" \
    ASM_ST2 \
    "s_nop 5\n\t" \
    "ds_write_b128 %[lw], v[188:191] offset:5120\n\t" \
    "ds_write_b128 %[lw], v[192:195] offset:6144\n\t" \
    "s_waitcnt lgkmcnt(0)\n\t" \
    "s_setprio 2\n\t" \
    "s_barrier\n\t" ASM_ST \
    "ds_read_b128 v[160:163], %[la] offset:0\n\t" \
    "ds_read_b128 v[164:167], %[la] offset:1024\n\t" \
    "ds_read_b128 v[168:171], %[la] offset:2048\n\t" \
    "ds_read2st64_b32 v[204:205], %[sr] offset0:20 offset1:24\n\t" \
    "ds_read_b128 v[172:175], %[la] offset:3072\n\t" \
    "ds_read_b128 v[176:179], %[la] offset:8192\n\t" \
    "ds_read_b128 v[180:183], %[la] offset:9216\n\t" \
    "ds_read_b128 v[184:187], %[la] offset:10240\n\t" \
    "s_waitcnt lgkmcnt(7)\n\t" \
    "v_mfma_f32_16x16x32_bf16 v[192:195], v[80:83], v[160:163], 0\n\t" \
    "s_waitcnt lgkmcnt(6)\n\t" \
    "v_mfma_f32_16x16x32_bf16 v[192:195], v[84:87], v[164:167], v[192:195]\n\t" \
    "v_mfma_f32_16x16x32_bf16 v[188:191], v[0:3], v[160:163], 0\n\t" \
    "ds_read_b128 v[160:163], %[la] offset:11264\n\t" \
    "v_mfma_f32_16x16x32_bf16 v[188:191], v[4:7], v[164:167], v[188:191]\n\t" \
    "ds_read_b128 v[164:167], %[la] offset:16384\n\t" \
    "s_waitcnt lgkmcnt(7)\n\t" \
    "v_mfma_f32_16x16x32_bf16 v[188:191], v[8:11], v[168:171], v[188:191]\n\t" \
    "s_waitcnt lgkmcnt(6)\n\t" \
    "s_waitcnt lgkmcnt(5)\n\t" \
    "v_mfma_f32_16x16x32_bf16 v[188:191], v[12:15], v[172:175], v[188:191]\n\t" \
    "v_permlane16_swap_b32 v204, v205\n\t" \
    "v_mfma_f32_16x16x32_bf16 v[192:195], v[88:91], v[168:171], v[192:195]\n\t" \
    "ds_read_b128 v[168:171], %[la] offset:17408\n\t" \
    "v_mfma_f32_16x16x32_bf16 v[192:195], v[92:95], v[172:175], v[192:195]\n\t" \
    "ds_read_b128 v[172:175], %[la] offset:18432\n\t" \
    "v_add_f32 v206, v204, v205\n\t" \
    "s_waitcnt lgkmcnt(6)\n\t" \
    "v_mfma_f32_16x16x32_bf16 v[192:195], v[96:99], v[176:179], v[192:195]\n\t" \
    "v_add_f32 v207, v204, v205\n\t" \
    "s_waitcnt lgkmcnt(5)\n\t" \
    "v_mfma_f32_16x16x32_bf16 v[192:195], v[100:103], v[180:183], v[192:195]\n\t" \
    "v_mfma_f32_16x16x32_bf16 v[188:191], v[16:19], v[176:179], v[188:191]\n\t" \
    "ds_read_b128 v[176:179], %[la] offset:19456\n\t" \
    "v_permlane32_swap_b32 v206, v207\n\t" \
    "v_mfma_f32_16x16x32_bf16 v[188:191], v[20:23], v[180:183], v[188:191]\n\t" \
    "ds_read_b128 v[180:183], %[la] offset:24576\n\t" \
    "s_waitcnt lgkmcnt(6)\n\t" \
    "v_mfma_f32_16x16x32_bf16 v[188:191], v[24:27], v[184:187], v[188:191]\n\t" \
    "v_add_f32 v204, v206, v207\n\t" \
    "s_waitcnt lgkmcnt(5)\n\t" \
    "v_mfma_f32_16x16x32_bf16 v[188:191], v[28:31], v[160:163], v[188:191]\n\t" \
    "v_add_f32 v205, v206, v207\n\t" \
    "v_mfma_f32_16x16x32_bf16 v[192:195], v[104:107], v[184:187], v[192:195]\n\t" \
    "ds_read_b128 v[184:187], %[la] offset:25600\n\t" \
    "v_mfma_f32_16x16x32_bf16 v[192:195], v[108:111], v[160:163], v[192:195]\n\t" \
    "ds_read_b128 v[160:163], %[la] offset:26624\n\t" \
    "v_permlane16_swap_b32 v204, v205\n\t" \
    "s_waitcnt lgkmcnt(6)\n\t" \
    "v_mfma_f32_16x16x32_bf16 v[192:195], v[112:115], v[164:167], v[192:195]\n\t" \
    "s_waitcnt lgkmcnt(5)\n\t" \
    "v_mfma_f32_16x16x32_bf16 v[192:195], v[116:119], v[168:171], v[192:195]\n\t" \
    "v_mul_f32 v205, %[kd], v205\n\t" \
    "v_mfma_f32_16x16x32_bf16 v[188:191], v[32:35], v[164:167], v[188:191]\n\t" \
    "ds_read_b128 v[164:167], %[la] offset:27648\n\t" \
    "v_mul_f32 v204, %[kd], v204\n\t" \
    "v_mfma_f32_16x16x32_bf16 v[188:191], v[36:39], v[168:171], v[188:191]\n\t" \
    "ds_read_b128 v[168:171], %[la] offset:4096\n\t" \
    "v_fma_f32 v204, -v205, v205, v204\n\t" \
    "s_waitcnt lgkmcnt(6)\n\t" \
    "v_mfma_f32_16x16x32_bf16 v[188:191], v[40:43], v[172:175], v[188:191]\n\t" \
    "v_max_f32 v204, 0, v204\n\t" \
    "s_waitcnt lgkmcnt(5)\n\t" \
    "v_mfma_f32_16x16x32_bf16 v[188:191], v[44:47], v[176:179], v[188:191]\n\t" \
    "v_add_f32 v206, %[ke], v204\n\t" \
    "v_mfma_f32_16x16x32_bf16 v[192:195], v[120:123], v[172:175], v[192:195]\n\t" \
    "ds_read_b128 v[172:175], %[la] offset:12288\n\t" \
    "v_rsq_f32 v206, v206\n\t" \
    "v_mfma_f32_16x16x32_bf16 v[192:195], v[124:127], v[176:179], v[192:195]\n\t" \
    "ds_read_b128 v[176:179], %[la] offset:20480\n\t" \
    "s_waitcnt lgkmcnt(6)\n\t" \
    "v_mfma_f32_16x16x32_bf16 v[192:195], v[128:131], v[180:183], v[192:195]\n\t" \
    "v_mul_f32 v207, -v205, v206\n\t" \
    "s_waitcnt lgkmcnt(5)\n\t" \
    "v_mfma_f32_16x16x32_bf16 v[192:195], v[132:135], v[184:187], v[192:195]\n\t" \
    "v_mul_f32 v208, v196, v207\n\t" \
    "v_mfma_f32_16x16x32_bf16 v[188:191], v[48:51], v[180:183], v[188:191]\n\t" \
    "ds_read_b128 v[180:183], %[la] offset:28672\n\t" \
    "v_mul_f32 v209, v197, v207\n\t" \
    "v_mul_f32 v210, v198, v207\n\t" \
    "v_mfma_f32_16x16x32_bf16 v[188:191], v[52:55], v[184:187], v[188:191]\n\t" \
    "v_mul_f32 v211, v199, v207\n\t" \
    "v_mul_f32 v212, v200, v207\n\t" \
    "s_waitcnt lgkmcnt(5)\n\t" \
    "v_mfma_f32_16x16x32_bf16 v[188:191], v[56:59], v[160:163], v[188:191]\n\t" \
    "v_mul_f32 v213, v201, v207\n\t" \
    "v_mul_f32 v214, v202, v207\n\t" \
    "s_waitcnt lgkmcnt(4)\n\t" \
    "v_mfma_f32_16x16x32_bf16 v[188:191], v[60:63], v[164:167], v[188:191]\n\t" \
    "v_mul_f32 v215, v203, v207\n\t" \
    "v_mov_b64 v[216:217], -1\n\t" \
    "v_mfma_f32_16x16x32_bf16 v[192:195], v[136:139], v[160:163], v[192:195]\n\t" \
    "v_mov_b64 v[218:219], -1\n\t" \
    "buffer_store_dwordx4 v[216:219], %[l16], %[rs], %[sa] offen sc1\n\t" \
    "v_mfma_f32_16x16x32_bf16 v[192:195], v[140:143], v[164:167], v[192:195]\n\t" \
    "s_waitcnt lgkmcnt(3)\n\t" \
    "v_mfma_f32_16x16x32_bf16 v[192:195], v[144:147], v[168:171], v[192:195]\n\t" \
    "s_waitcnt lgkmcnt(2)\n\t" \
    "v_mfma_f32_16x16x32_bf16 v[192:195], v[148:151], v[172:175], v[192:195]\n\t" \
    "v_mfma_f32_16x16x32_bf16 v[188:191], v[64:67], v[168:171], v[188:191]\n\t" \
    "v_mfma_f32_16x16x32_bf16 v[188:191], v[68:71], v[172:175], v[188:191]\n\t" \
    "s_waitcnt lgkmcnt(1)\n\t" \
    "v_mfma_f32_16x16x32_bf16 v[188:191], v[72:75], v[176:179], v[188:191]\n\t" \
    "s_waitcnt lgkmcnt(0)\n\t" \
    "v_mfma_f32_16x16x32_bf16 v[188:191], v[76:79], v[180:183], v[188:191]\n\t" \
    "v_mfma_f32_16x16x32_bf16 v[192:195], v[152:155], v[176:179], v[192:195]\n\t" \
    "v_mfma_f32_16x16x32_bf16 v[192:195], v[156:159], v[180:183], v[192:195]\n\t" \
    "s_setprio 2\n\t" \
    "buffer_load_dwordx4 v[232:235], %[l16], %[rs], %[n0] offen offset:0 sc1\n\t" \
    "buffer_load_dwordx4 v[236:239], %[l16], %[rs], %[n0] offen offset:1024 sc1\n\t" \
    "buffer_load_dwordx4 v[240:243], %[l16], %[rs], %[n0] offen offset:2048 sc1\n\t" \
    "buffer_load_dwordx4 v[244:247], %[l16], %[rs], %[n0] offen offset:3072 sc1\n\t" \
    "buffer_load_dwordx4 v[248:251], %[l16], %[rs], %[n1] offen offset:0 sc1\n\t" \
    ASM_ST3 \
    "v_fma_f32 v188, v188, v206, v208\n\t" \
    "v_fma_f32 v189, v189, v206, v209\n\t" \
    "v_fma_f32 v190, v190, v206, v210\n\t" \
    "v_fma_f32 v191, v191, v206, v211\n\t" \
    "v_mul_f32 v160, v188, v188\n\t" \
    "v_mul_f32 v161, v189, v189\n\t" \
    "v_mul_f32 v162, v190, v190\n\t" \
    "v_mul_f32 v163, v191, v191\n\t" \
    "v_fma_f32 v160, v160, %[k1], v220\n\t" \
    "v_fma_f32 v161, v161, %[k1], v220\n\t" \
    "v_fma_f32 v162, v162, %[k1], v220\n\t" \
    "v_fma_f32 v163, v163, %[k1], v220\n\t" \
    "v_mul_f32 v160, v188, v160\n\t" \
    "v_mul_f32 v161, v189, v161\n\t" \
    "v_mul_f32 v162, v190, v162\n\t" \
    "v_mul_f32 v163, v191, v163\n\t" \
    "v_fma_f32 v192, v192, v206, v212\n\t" \
    "v_fma_f32 v193, v193, v206, v213\n\t" \
    "v_fma_f32 v194, v194, v206, v214\n\t" \
    "v_fma_f32 v195, v195, v206, v215\n\t" \
    "v_mul_f32 v164, v192, v192\n\t" \
    "v_mul_f32 v165, v193, v193\n\t" \
    "v_mul_f32 v166, v194, v194\n\t" \
    "v_mul_f32 v167, v195, v195\n\t" \
    "v_fma_f32 v164, v164, %[k1], v220\n\t" \
    "v_fma_f32 v165, v165, %[k1], v220\n\t" \
    "v_fma_f32 v166, v166, %[k1], v220\n\t" \
    "v_fma_f32 v167, v167, %[k1], v220\n\t" \
    "v_mul_f32 v164, v192, v164\n\t" \
    "v_mul_f32 v165, v193, v165\n\t" \
    "v_mul_f32 v166, v194, v166\n\t" \
    "v_mul_f32 v167, v195, v167\n\t" \
    "v_exp_f32 v160, v160\n\t" \
    "v_exp_f32 v161, v161\n\t" \
    "v_exp_f32 v162, v162\n\t" \
    "v_exp_f32 v163, v163\n\t" \
    "v_exp_f32 v164, v164\n\t" \
    "v_exp_f32 v165, v165\n\t" \
    "v_exp_f32 v166, v166\n\t" \
    "v_exp_f32 v167, v167\n\t" \
    "v_add_f32 v160, 1.0, v160\n\t" \
    "v_add_f32 v161, 1.0, v161\n\t" \
    "v_add_f32 v162, 1.0, v162\n\t" \
    "v_add_f32 v163, 1.0, v163\n\t" \
    "v_add_f32 v164, 1.0, v164\n\t" \
    "v_add_f32 v165, 1.0, v165\n\t" \
    "v_add_f32 v166, 1.0, v166\n\t" \
    "v_add_f32 v167, 1.0, v167\n\t" \
    "v_rcp_f32 v160, v160\n\t" \
    "v_rcp_f32 v161, v161\n\t" \
    "v_rcp_f32 v162, v162\n\t" \
    "v_rcp_f32 v163, v163\n\t" \
    "v_rcp_f32 v164, v164\n\t" \
    "v_rcp_f32 v165, v165\n\t" \
    "v_rcp_f32 v166, v166\n\t" \
    "v_rcp_f32 v167, v167\n\t" \
    "v_mul_f32 v188, v188, v160\n\t" \
    "v_mul_f32 v189, v189, v161\n\t" \
    "v_mul_f32 v190, v190, v162\n\t" \
    "v_mul_f32 v191, v191, v163\n\t" \
    "v_mul_f32 v192, v192, v164\n\t" \
    "v_mul_f32 v193, v193, v165\n\t" \
    "v_mul_f32 v194, v194, v166\n\t" \
    "v_mul_f32 v195, v195, v167\n\t" \
    "v_cvt_pk_bf16_f32 v188, v188, v189\n\t" \
    "v_cvt_pk_bf16_f32 v189, v190, v191\n\t" \
    "v_cvt_pk_bf16_f32 v190, v192, v193\n\t" \
    "v_cvt_pk_bf16_f32 v191, v194, v195\n\t" \
    "v_min_u32 v188, -2, v188\n\t" \
    "v_min_u32 v189, -2, v189\n\t" \
    "v_min_u32 v190, -2, v190\n\t" \
    "v_min_u32 v191, -2, v191\n\t" \
    "v_mov_b32 v216, 0x3f803f80\n\t" \
    "v_mov_b32 v217, 0x3f803f80\n\t" \
    "v_mov_b32 v218, 0x3f803f80\n\t" \
    "v_mov_b32 v219, 0x3f803f80\n\t" \
    "v_permlane16_swap_b32 v188, v190\n\t" \
    "v_permlane16_swap_b32 v189, v191\n\t" \
    "s_waitcnt vmcnt(6)\n\t" \
    "buffer_store_dwordx4 v[188:191], %[oa], %[rs], %[so] offen sc1\n\t" \
    "s_nop 1\n\t" \
    "s_setprio 0\n\t"
#define ASM_WT3_HI \
    "s_setprio 1\n\t" \
    "s_barrier\n\t" ASM_ST \
    "ds_read_b128 v[160:163], %[la] offset:0\n\t" \
    "ds_read_b128 v[164:167], %[la] offset:1024\n\t" \
    "ds_read_b128 v[168:171], %[la] offset:2048\n\t" \
    "ds_read2st64_b32 v[204:205], %[sr] offset0:20 offset1:24\n\t" \
    "ds_read_b128 v[172:175], %[la] offset:3072\n\t" \
    "ds_read_b128 v[176:179], %[la] offset:8192\n\t" \
    "ds_read_b128 v[180:183], %[la] offset:9216\n\t" \
    "ds_read_b128 v[184:187], %[la] offset:10240\n\t" \
    "s_waitcnt lgkmcnt(7)\n\t" \
    "v_mfma_f32_16x16x32_bf16 v[192:195], v[80:83], v[160:163], 0\n\t" \
    "s_waitcnt lgkmcnt(6)\n\t" \
    "v_mfma_f32_16x16x32_bf16 v[192:195], v[84:87], v[164:167], v[192:195]\n\t" \
    "v_mfma_f32_16x16x32_bf16 v[188:191], v[0:3], v[160:163], 0\n\t" \
    "ds_read_b128 v[160:163], %[la] offset:11264\n\t" \
    "v_mfma_f32_16x16x32_bf16 v[188:191], v[4:7], v[164:167], v[188:191]\n\t" \
    "ds_read_b128 v[164:167], %[la] offset:16384\n\t" \
    "s_waitcnt lgkmcnt(7)\n\t" \
    "v_mfma_f32_16x16x32_bf16 v[188:191], v[8:11], v[168:171], v[188:191]\n\t" \
    "s_waitcnt lgkmcnt(6)\n\t" \
    "s_waitcnt lgkmcnt(5)\n\t" \
    "v_mfma_f32_16x16x32_bf16 v[188:191], v[12:15], v[172:175], v[188:191]\n\t" \
    "v_permlane16_swap_b32 v204, v205\n\t" \
    "v_mfma_f32_16x16x32_bf16 v[192:195], v[88:91], v[168:171], v[192:195]\n\t" \
    "ds_read_b128 v[168:171], %[la] offset:17408\n\t" \
    "v_mfma_f32_16x16x32_bf16 v[192:195], v[92:95], v[172:175], v[192:195]\n\t" \
    "ds_read_b128 v[172:175], %[la] offset:18432\n\t" \
    "v_add_f32 v206, v204, v205\n\t" \
    "s_waitcnt lgkmcnt(6)\n\t" \
    "v_mfma_f32_16x16x32_bf16 v[192:195], v[96:99], v[176:179], v[192:195]\n\t" \
    "v_add_f32 v207, v204, v205\n\t" \
    "s_waitcnt lgkmcnt(5)\n\t" \
    "v_mfma_f32_16x16x32_bf16 v[192:195], v[100:103], v[180:183], v[192:195]\n\t" \
    "v_mfma_f32_16x16x32_bf16 v[188:191], v[16:19], v[176:179], v[188:191]\n\t" \
    "ds_read_b128 v[176:179], %[la] offset:19456\n\t" \
    "v_permlane32_swap_b32 v206, v207\n\t" \
    "v_mfma_f32_16x16x32_bf16 v[188:191], v[20:23], v[180:183], v[188:191]\n\t" \
    "ds_read_b128 v[180:183], %[la] offset:24576\n\t" \
    "s_waitcnt lgkmcnt(6)\n\t" \
    "v_mfma_f32_16x16x32_bf16 v[188:191], v[24:27], v[184:187], v[188:191]\n\t" \
    "v_add_f32 v204, v206, v207\n\t" \
    "s_waitcnt lgkmcnt(5)\n\t" \
    "v_mfma_f32_16x16x32_bf16 v[188:191], v[28:31], v[160:163], v[188:191]\n\t" \
    "v_add_f32 v205, v206, v207\n\t" \
    "v_mfma_f32_16x16x32_bf16 v[192:195], v[104:107], v[184:187], v[192:195]\n\t" \
    "ds_read_b128 v[184:187], %[la] offset:25600\n\t" \
    "v_mfma_f32_16x16x32_bf16 v[192:195], v[108:111], v[160:163], v[192:195]\n\t" \
    "ds_read_b128 v[160:163], %[la] offset:26624\n\t" \
    "v_permlane16_swap_b32 v204, v205\n\t" \
    "s_waitcnt lgkmcnt(6)\n\t" \
    "v_mfma_f32_16x16x32_bf16 v[192:195], v[112:115], v[164:167], v[192:195]\n\t" \
    "s_waitcnt lgkmcnt(5)\n\t" \
    "v_mfma_f32_16x16x32_bf16 v[192:195], v[116:119], v[168:171], v[192:195]\n\t" \
    "v_mul_f32 v205, %[kd], v205\n\t" \
    "v_mfma_f32_16x16x32_bf16 v[188:191], v[32:35], v[164:167], v[188:191]\n\t" \
    "ds_read_b128 v[164:167], %[la] offset:27648\n\t" \
    "v_mul_f32 v204, %[kd], v204\n\t" \
    "v_mfma_f32_16x16x32_bf16 v[188:191], v[36:39], v[168:171], v[188:191]\n\t" \
    "ds_read_b128 v[168:171], %[la] offset:4096\n\t" \
    "v_fma_f32 v204, -v205, v205, v204\n\t" \
    "s_waitcnt lgkmcnt(6)\n\t" \
    "v_mfma_f32_16x16x32_bf16 v[188:191], v[40:43], v[172:175], v[188:191]\n\t" \
    "v_max_f32 v204, 0, v204\n\t" \
    "s_waitcnt lgkmcnt(5)\n\t" \
    "v_mfma_f32_16x16x32_bf16 v[188:191], v[44:47], v[176:179], v[188:191]\n\t" \
    "v_add_f32 v206, %[ke], v204\n\t" \
    "v_mfma_f32_16x16x32_bf16 v[192:195], v[120:123], v[172:175], v[192:195]\n\t" \
    "ds_read_b128 v[172:175], %[la] offset:12288\n\t" \
    "v_rsq_f32 v206, v206\n\t" \
    "v_mfma_f32_16x16x32_bf16 v[192:195], v[124:127], v[176:179], v[192:195]\n\t" \
    "ds_read_b128 v[176:179], %[la] offset:20480\n\t" \
    "s_waitcnt lgkmcnt(6)\n\t" \
    "v_mfma_f32_16x16x32_bf16 v[192:195], v[128:131], v[180:183], v[192:195]\n\t" \
    "v_mul_f32 v207, -v205, v206\n\t" \
    "s_waitcnt lgkmcnt(5)\n\t" \
    "v_mfma_f32_16x16x32_bf16 v[192:195], v[132:135], v[184:187], v[192:195]\n\t" \
    "v_mul_f32 v208, v196, v207\n\t" \
    "v_mfma_f32_16x16x32_bf16 v[188:191], v[48:51], v[180:183], v[188:191]\n\t" \
    "ds_read_b128 v[180:183], %[la] offset:28672\n\t" \
    "v_mul_f32 v209, v197, v207\n\t" \
    "v_mul_f32 v210, v198, v207\n\t" \
    "v_mfma_f32_16x16x32_bf16 v[188:191], v[52:55], v[184:187], v[188:191]\n\t" \
    "v_mul_f32 v211, v199, v207\n\t" \
    "v_mul_f32 v212, v200, v207\n\t" \
    "s_waitcnt lgkmcnt(5)\n\t" \
    "v_mfma_f32_16x16x32_bf16 v[188:191], v[56:59], v[160:163], v[188:191]\n\t" \
    "v_mul_f32 v213, v201, v207\n\t" \
    "v_mul_f32 v214, v202, v207\n\t" \
    "s_waitcnt lgkmcnt(4)\n\t" \
    "v_mfma_f32_16x16x32_bf16 v[188:191], v[60:63], v[164:167], v[188:191]\n\t" \
    "v_mul_f32 v215, v203, v207\n\t" \
    "v_mov_b64 v[216:217], -1\n\t" \
    "v_mfma_f32_16x16x32_bf16 v[192:195], v[136:139], v[160:163], v[192:195]\n\t" \
    "v_mov_b64 v[218:219], -1\n\t" \
    "buffer_store_dwordx4 v[216:219], %[l16], %[rs], %[sa] offen sc1\n\t" \
    "v_mfma_f32_16x16x32_bf16 v[192:195], v[140:143], v[164:167], v[192:195]\n\t" \
    "s_waitcnt lgkmcnt(3)\n\t" \
    "v_mfma_f32_16x16x32_bf16 v[192:195], v[144:147], v[168:171], v[192:195]\n\t" \
    "s_waitcnt lgkmcnt(2)\n\t" \
    "v_mfma_f32_16x16x32_bf16 v[192:195], v[148:151], v[172:175], v[192:195]\n\t" \
    "v_mfma_f32_16x16x32_bf16 v[188:191], v[64:67], v[168:171], v[188:191]\n\t" \
    "v_mfma_f32_16x16x32_bf16 v[188:191], v[68:71], v[172:175], v[188:191]\n\t" \
    "s_waitcnt lgkmcnt(1)\n\t" \
    "v_mfma_f32_16x16x32_bf16 v[188:191], v[72:75], v[176:179], v[188:191]\n\t" \
    "s_waitcnt lgkmcnt(0)\n\t" \
    "v_mfma_f32_16x16x32_bf16 v[188:191], v[76:79], v[180:183], v[188:191]\n\t" \
    "v_mfma_f32_16x16x32_bf16 v[192:195], v[152:155], v[176:179], v[192:195]\n\t" \
    "v_mfma_f32_16x16x32_bf16 v[192:195], v[156:159], v[180:183], v[192:195]\n\t" \
    ASM_ST3 \
    "s_nop 1\n\t" \
    "v_fma_f32 v188, v188, v206, v208\n\t" \
    "v_fma_f32 v189, v189, v206, v209\n\t" \
    "v_fma_f32 v190, v190, v206, v210\n\t" \
    "v_fma_f32 v191, v191, v206, v211\n\t" \
    "v_mul_f32 v160, v188, v188\n\t" \
    "v_mul_f32 v161, v189, v189\n\t" \
    "v_mul_f32 v162, v190, v190\n\t" \
    "v_mul_f32 v163, v191, v191\n\t" \
    "v_fma_f32 v160, v160, %[k1], v220\n\t" \
    "v_fma_f32 v161, v161, %[k1], v220\n\t" \
    "v_fma_f32 v162, v162, %[k1], v220\n\t" \
    "v_fma_f32 v163, v163, %[k1], v220\n\t" \
    "v_mul_f32 v160, v188, v160\n\t" \
    "v_mul_f32 v161, v189, v161\n\t" \
    "v_mul_f32 v162, v190, v162\n\t" \
    "v_mul_f32 v163, v191, v163\n\t" \
    "v_fma_f32 v192, v192, v206, v212\n\t" \
    "v_fma_f32 v193, v193, v206, v213\n\t" \
    "v_fma_f32 v194, v194, v206, v214\n\t" \
    "v_fma_f32 v195, v195, v206, v215\n\t" \
    "v_mul_f32 v164, v192, v192\n\t" \
    "v_mul_f32 v165, v193, v193\n\t" \
    "v_mul_f32 v166, v194, v194\n\t" \
    "v_mul_f32 v167, v195, v195\n\t" \
    "v_fma_f32 v164, v164, %[k1], v220\n\t" \
    "v_fma_f32 v165, v165, %[k1], v220\n\t" \
    "v_fma_f32 v166, v166, %[k1], v220\n\t" \
    "v_fma_f32 v167, v167, %[k1], v220\n\t" \
    "v_mul_f32 v164, v192, v164\n\t" \
    "v_mul_f32 v165, v193, v165\n\t" \
    "v_mul_f32 v166, v194, v166\n\t" \
    "v_mul_f32 v167, v195, v167\n\t" \
    "v_exp_f32 v160, v160\n\t" \
    "v_exp_f32 v161, v161\n\t" \
    "v_exp_f32 v162, v162\n\t" \
    "v_exp_f32 v163, v163\n\t" \
    "v_exp_f32 v164, v164\n\t" \
    "v_exp_f32 v165, v165\n\t" \
    "v_exp_f32 v166, v166\n\t" \
    "v_exp_f32 v167, v167\n\t" \
    "v_add_f32 v160, 1.0, v160\n\t" \
    "v_add_f32 v161, 1.0, v161\n\t" \
    "v_add_f32 v162, 1.0, v162\n\t" \
    "v_add_f32 v163, 1.0, v163\n\t" \
    "v_add_f32 v164, 1.0, v164\n\t" \
    "v_add_f32 v165, 1.0, v165\n\t" \
    "v_add_f32 v166, 1.0, v166\n\t" \
    "v_add_f32 v167, 1.0, v167\n\t" \
    "v_rcp_f32 v160, v160\n\t" \
    "v_rcp_f32 v161, v161\n\t" \
    "v_rcp_f32 v162, v162\n\t" \
    "v_rcp_f32 v163, v163\n\t" \
    "v_rcp_f32 v164, v164\n\t" \
    "v_rcp_f32 v165, v165\n\t" \
    "v_rcp_f32 v166, v166\n\t" \
    "v_rcp_f32 v167, v167\n\t" \
    "v_mul_f32 v188, v188, v160\n\t" \
    "v_mul_f32 v189, v189, v161\n\t" \
    "v_mul_f32 v190, v190, v162\n\t" \
    "v_mul_f32 v191, v191, v163\n\t" \
    "v_mul_f32 v192, v192, v164\n\t" \
    "v_mul_f32 v193, v193, v165\n\t" \
    "v_mul_f32 v194, v194, v166\n\t" \
    "v_mul_f32 v195, v195, v167\n\t" \
    "v_cvt_pk_bf16_f32 v188, v188, v189\n\t" \
    "v_cvt_pk_bf16_f32 v189, v190, v191\n\t" \
    "v_cvt_pk_bf16_f32 v190, v192, v193\n\t" \
    "v_cvt_pk_bf16_f32 v191, v194, v195\n\t" \
    "v_min_u32 v188, -2, v188\n\t" \
    "v_min_u32 v189, -2, v189\n\t" \
    "v_min_u32 v190, -2, v190\n\t" \
    "v_min_u32 v191, -2, v191\n\t" \
    "s_nop 1\n\t" \
    "v_permlane16_swap_b32 v188, v190\n\t" \
    "v_permlane16_swap_b32 v189, v191\n\t" \
    "s_waitcnt vmcnt(1)\n\t" \
    "buffer_store_dwordx4 v[188:191], %[oa], %[rs], %[so] offen sc1\n\t" \
    "s_nop 1\n\t"
#define ASM_POLL5_I \
    "buffer_load_dwordx4 v[232:235], %[l16], %[rs], %[s0] offen offset:0 sc1\n\t" \
    "buffer_load_dwordx4 v[236:239], %[l16], %[rs], %[s0] offen offset:1024 sc1\n\t" \
    "buffer_load_dwordx4 v[240:243], %[l16], %[rs], %[s0] offen offset:2048 sc1\n\t" \
    "buffer_load_dwordx4 v[244:247], %[l16], %[rs], %[s0] offen offset:3072 sc1\n\t" \
    "buffer_load_dwordx4 v[248:251], %[l16], %[rs], %[s1] offen offset:0 sc1\n\t"
using u32x8 = __attribute__((ext_vector_type(8))) unsigned;
using u32x16 = __attribute__((ext_vector_type(16))) unsigned;
using u32x32 = __attribute__((ext_vector_type(32))) unsigned;
struct WRegs { u32x32 w[6]; u32x8 t; };
#define WREGS(W) "{v[0:31]}"(W.w[0]), "{v[32:63]}"(W.w[1]), "{v[64:95]}"(W.w[2]), "{v[96:127]}"(W.w[3]), "{v[128:159]}"(W.w[4]), "{v[160:191]}"(W.w[5]), "{v[192:199]}"(W.t)
__device__ __forceinline__ u32x32 cat32(u32x8 a, u32x8 b, u32x8 c, u32x8 d) {
    const u32x16 lo = __builtin_shufflevector(a, b, 0, 1, 2, 3, 4, 5, 6, 7, 8, 9, 10, 11, 12, 13, 14, 15);
    const u32x16 hi = __builtin_shufflevector(c, d, 0, 1, 2, 3, 4, 5, 6, 7, 8, 9, 10, 11, 12, 13, 14, 15);
    return __builtin_shufflevector(lo, hi, 0, 1, 2, 3, 4, 5, 6, 7, 8, 9, 10, 11, 12, 13, 14, 15, 16, 17, 18, 19, 20, 21, 22, 23, 24, 25, 26, 27, 28, 29, 30, 31);
}
#define WOUT(W) "=&{v[0:31]}"(W.w[0]), "=&{v[32:63]}"(W.w[1]), "=&{v[64:95]}"(W.w[2]), "=&{v[96:127]}"(W.w[3]), "=&{v[128:159]}"(W.w[4]), "=&{v[160:191]}"(W.w[5]), "=&{v[192:199]}"(W.t)
#define WTMP "memory", "scc", "v200", "v201", "v202", "v203", "v204", "v205", "v206", "v207", "v208", "v209", "v210", "v211", "v212", "v213", "v214", "v215", "v216", "v217", "v218", "v219", "v220", "v221", "v222", "v223", "v224", "v225", "v226", "v227", "v228", "v229", "v230", "v231", "v232", "v233", "v234", "v235", "v236", "v237", "v238", "v239"
__device__ __forceinline__ void load_w3(WRegs& W, const bf16_t* m, int wave, unsigned c0, bool hq) {
    const int ln = lane_id();
    unsigned o[3], s[3], t;
#pragma unroll
    for (int k = 0; k < 3; ++k) {
        const int row = (k * 64 + ln) / 24, sg = (k * 64 + ln) % 24;
        o[k] = row * (HD * 2) + c0 + (hq ? ((sg >> 1) & 1) * 384 + ((0x140 >> ((sg >> 3) * 4)) & 15) * 32 + (sg & 5) * 16 : sg * 16); s[k] = wave * 13824 + row * 416 + sg * 16;
    }
    const unsigned tr = (ln >> 4) * 3456 + ((ln >> 2) & 3) * 416 + (wave & 3) * 32 + (ln & 3) * 8;
    asm volatile(ASM_WLOAD3 : WOUT(W), [t] "=&s"(t) : [rs] "s"(mkrsrc(m, DM * HD * 2)), [wx] "s"(wave), [o0] "v"(o[0]), [o1] "v"(o[1]), [o2] "v"(o[2]), [s0] "v"(s[0]), [s1] "v"(s[1]), [s2] "v"(s[2]),
                 [t0] "v"(tr + wave * 13824), [t1] "v"(tr + (wave ^ 1) * 13824), [t2] "v"(tr + (wave ^ 2) * 13824), [t3] "v"(tr + (wave ^ 3) * 13824), [t4] "v"(tr + (wave & 4) * 13824),
                 [wl] "v"(L_W + wave * 10240 + ln * 16) : WTMP);
}
__device__ __forceinline__ void load_wo(WRegs& W, const bf16_t* m, int wave, int h) {
    const int ln = lane_id();
    unsigned o[5], s[5], t;
#pragma unroll
    for (int k = 0; k < 5; ++k) {
        const int row = (k * 64 + ln) / 20, sg = (k * 64 + ln) % 20;
        o[k] = (h * DH + row) * (DM * 2) + sg * 16 + (wave & 3) * 320; s[k] = wave * 13824 + row * 352 + sg * 16;
    }
    asm volatile(ASM_WLOADO : WOUT(W), [t] "=&s"(t) : [rs] "s"(mkrsrc(m, DM * HD * 2)), [o0] "v"(o[0]), [o1] "v"(o[1]), [o2] "v"(o[2]), [o3] "v"(o[3]), [o4] "v"(o[4]),
                 [s0] "v"(s[0]), [s1] "v"(s[1]), [s2] "v"(s[2]), [s3] "v"(s[3]), [s4] "v"(s[4]), [tr] "v"(wave * 13824 + (ln >> 2) * 352 + (ln & 3) * 8), [wl] "v"(L_W + wave * 10240 + ln * 16) : WTMP);
}
using s16x4 = __attribute__((ext_vector_type(4))) short;
template <int NT, int KS, int N>
__device__ __forceinline__ void load_wn(u32x4 (&f)[NT * KS], const bf16_t* m, int rows, int wave, int c0, int k0) {
    const int ln = lane_id();
    const rsrc_t rs = mkrsrc(m, rows * N * 2);
#pragma unroll
    for (int k = 0; k < KS; ++k)
#pragma unroll
        for (int n = 0; n < NT; ++n) f[n * KS + k] = __builtin_amdgcn_raw_buffer_load_b128(rs, ((n * 64 + ln) / (2 * NT)) * (N * 2) + ((n * 64 + ln) % (2 * NT)) * 16, (k0 + k) * (N * 64) + c0 * 32, 0);
    const unsigned st = wave * 10240 + ln * 16, tr = wave * 10240 + ((ln >> 4) * 8 + ((ln >> 2) & 3)) * (NT * 32) + (ln & 3) * 8;
#pragma unroll
    for (int k = 0; k < KS; ++k) {
#pragma unroll
        for (int n = 0; n < NT; ++n) lds_st<u32x4>(st + n * 1024, f[n * KS + k]);
        asm volatile("" ::: "memory");
#pragma unroll
        for (int n = 0; n < NT; ++n) {
            const u32x2 a = __builtin_bit_cast(u32x2, __builtin_amdgcn_ds_read_tr16_b64_v4i16((LAS s16x4*)(tr + n * 32)));
            const u32x2 b = __builtin_bit_cast(u32x2, __builtin_amdgcn_ds_read_tr16_b64_v4i16((LAS s16x4*)(tr + n * 32 + NT * 128)));
            f[n * KS + k] = (u32x4){a[0], a[1], b[0], b[1]};
        }
        asm volatile("" ::: "memory");
    }
}
struct C1Regs { u32x8 lw0, lw1, z1; u32x4 z0; };
#define XG_OUT "=&{v[200:207]}"(C.lw0), "=&{v[224:227]}"(C.z0), "=&{v[228:235]}"(C.lw1), "=&{v[244:251]}"(C.z1)
#define XG_IN WREGS(W), [xa] "v"(xa), [wl] "v"(wl), [qs] "s"(qs)
#define XG_CLOB "memory", "scc", "v208", "v209", "v210", "v211", "v212", "v213", "v214", "v215", "v216", "v217", "v218", "v219", "v220", "v221", "v222", "v223", "v236", "v237", "v238", "v239", "v240", "v241", "v242", "v243", "v252"
constexpr int XS0 = 8;
__device__ __forceinline__ unsigned init_wait(const Params& P, int c, int ph, unsigned need = 0x220080u) {
    unsigned w;
    for (int spins = 0;; ++spins) {
        unsigned o;
        asm volatile("v_mov_b32 %0, 0" : "=v"(o));
        w = __builtin_amdgcn_readfirstlane(__builtin_amdgcn_raw_buffer_load_b32(mkrsrc(P.ws + FL0, FL_BYTES), o, FL_IN + (ph * 8 + (c & 7)) * 128, 16));
        if ((w & need) == need) break;
        __builtin_amdgcn_s_sleep(8);
        if (spin_fail(spins, P)) break;
    }
    asm volatile("" ::: "memory");
    return w;
}
#define PLACE() const unsigned pw_ = init_wait(P, wave, L); const int par = (L ^ (pw_ >> 16)) & 1, cpy = x ^ ((pw_ >> 20) & 1); const rsrc_t rr = mkrsrc(P.ws + par * 8192, RG_L); (void)cpy; (void)rr
__device__ __forceinline__ void xn_wait(const Params& P, rsrc_t rr, int seg, int cpy) {
    for (int spins = 0;; ++spins) {
        const unsigned f = __builtin_amdgcn_raw_buffer_load_b32(rr, (lane_id() & 15) * 128, RG_FX + (cpy & 3) * 2048, 16);
        if (__all(f > (unsigned)seg)) break;
        __builtin_amdgcn_s_sleep(4);
        if (spin_fail(spins, P)) break;
    }
    asm volatile("" ::: "memory");
}
__device__ __forceinline__ unsigned c1_dst(int wave, bool ktype) {
    const int ln = lane_id(), l15 = ln & 15, hf = (wave >> 1) & 1;
    const int rb16 = ((ln >> 2) & 4) * 16 + (ln >> 5) * 32;
    return (ktype ? L_KM + (l15 >> 2) * 128 + hf * 8 + (l15 & 3) * 2
                  : L_Q + (l15 >> 3) * 256 + ((l15 >> 1) & 1) * 128 + ((l15 >> 2) & 1) * 8 + hf * 4 + (l15 & 1) * 2) + (wave & 1) * 1536 + rb16;
}
template <int NP>
__device__ __forceinline__ void crit1(const WRegs& W, C1Regs& C, rsrc_t rr, unsigned sbase, int wave, unsigned wl, unsigned dsto, unsigned& budget, unsigned& fv, unsigned fs
) {
    unsigned abase;
    asm volatile("v_and_b32 %0, 0x3ff, %1\n\tv_or_b32 %0, %2, %0" : "=&v"(abase) : "v"(wl), "s"(wave * 1024));
    ASM_ST_DECL;
#define C1_OUT "+{v[224:227]}"(C.z0), "+{v[244:251]}"(C.z1), "+{v[200:207]}"(C.lw0), "+{v[228:235]}"(C.lw1)
#define C1_IN WREGS(W), [ab] "v"(abase), [wl] "v"(wl), [dst] "v"(dsto)
#define C1_CLOB "memory", "vcc", "scc", "v208", "v209", "v210", "v211", "v212", "v213", "v214", "v215", "v216", "v217", "v218", "v219", "v220", "v221", "v222", "v223", "v236", "v237", "v238", "v239", "v240", "v241", "v242", "v243"
#define C1_POLL [rs] "s"(rr), [s0] "s"(sbase), [s1] "s"(sbase + 4096), [s2] "s"(sbase + 2 * BLK)
    ST(1);
    if constexpr (NP == 13) asm volatile(ASM_CRIT1_V3 "s_waitcnt vmcnt(0)" : C1_OUT, [n] "+s"(budget), [fv] "+v"(fv) ASM_ST_OUT ASM_ST2_OUT : C1_IN, C1_POLL, [fs] "s"(fs) : C1_CLOB);
    else if constexpr (NP == 12) asm volatile(ASM_CRIT1_V2 "s_waitcnt vmcnt(0)" : C1_OUT, [n] "+s"(budget), [fv] "+v"(fv) ASM_ST_OUT ASM_ST2_OUT : C1_IN, C1_POLL, [fs] "s"(fs) : C1_CLOB);
    else if constexpr (NP == 3) asm volatile(ASM_CRIT1_3 : C1_OUT, [n] "+s"(budget) ASM_ST_OUT ASM_ST2_OUT : C1_IN, C1_POLL : C1_CLOB);
    else if constexpr (NP == 2) asm volatile(ASM_CRIT1_2 : C1_OUT, [n] "+s"(budget) ASM_ST_OUT ASM_ST2_OUT : C1_IN, C1_POLL : C1_CLOB);
    else asm volatile(ASM_CRIT1_N : C1_OUT ASM_ST_OUT : C1_IN : C1_CLOB);
    SB();
    ASM_ST_SAVE(3);
    if constexpr (NP != 0) { ASM_ST2_SAVE(2); }
}
#define STARG

#define SRG 5
#undef STROLE
#define STROLE 1
__device__ __forceinline__ void s_role(const Params& P, int wave, int L, int h, int x) {
    const char* xn = P.ws + PG0 + PG_XN + L * PG_LX;
    auto xdma = [&](int seg, int hb, int par) {
        const unsigned l16 = lane_id() * 16;
        const int mt = (wave >> 1) & 1;
        const char* g = xn + pgb(seg, par);
        if (hb == 0) {
            dma4(g + blin(mt * 20480 + (wave & 1) * 4096), l16, L_XA + mt * 16384 + (wave & 1) * 4096);
            dmaq<0>(g + blin(mt * 20480 + 16384) + (wave & 1) * 1024, l16, L_XB + mt * 4096 + (wave & 1) * 1024);
        } else {
            dma4(g + blin(mt * 20480 + 8192), l16, L_XA + mt * 16384 + 8192); dma4(g + blin(mt * 20480 + 12288), l16, L_XA + mt * 16384 + 12288);
            dmaq<2>(g + blin(mt * 20480 + 16384), l16, L_XB + mt * 4096); dmaq<3>(g + blin(mt * 20480 + 16384), l16, L_XB + mt * 4096);
        }
    };
    const float SCL = 0.07216878364870322f * 1.4426950408889634f;
    auto run = [&](auto Cc) {
    constexpr int CLS = decltype(Cc)::value;
    WRegs wf;
    MARK(!L && h == 0 && wave == 0, 0);
    load_w3(wf, P.w[CLS == 3], wave, h * 384, false);
    MARKX(!L && wave == 0, 1);
    PLACE();
    if (CLS == 3) {
        if (L) xn_wait(P, rr, 0, cpy);
        xdma(0, 0, par);
        if (wave == 4 || wave == 7) xdma(0, 1, par);
        wait_vm<0>();
    }
    bar();
    if (CLS == 3 && wave == 7 && L) xn_wait(P, rr, 1, cpy);
    const unsigned qs = (CLS == 3 ? L_KX : L_QX) + (wave & 1) * 3072 + ((wave >> 1) & 1) * 8;
    unsigned xa, wl, dsto;
    if (CLS == 3) { xa = L_XA + wave * 2048 + lane_id() * 16; wl = L_W + wave * 10240 + lane_id() * 16; dsto = c1_dst(wave, true); }
    __builtin_amdgcn_s_setprio(CLS == 3 ? 1 : 2);
    unsigned budget = 1u << 18, tb2 = 0;
    STDECL;
    LDECL;
#pragma unroll 1
    for (int t = 0; t < NSEG; ++t) {
        const unsigned slot = t & (NSLOT - 1);
        C1Regs C;
        {
            const WRegs& W = wf;
            ASM_ST_DECL;
            if constexpr (CLS == 3) {
                const int mt = (wave >> 1) & 1;
                const char* g = xn + pgb(t + 1 < NSEG ? t + 1 : t, par);
                asm volatile(ASM_XG_S_HI : XG_OUT ASM_ST_OUT : XG_IN, [slp] "s"(SLP4), [g0] "s"(g + blin(mt * 20480 + (wave & 1) * 4096)), [d0] "s"(L_XA + mt * 16384 + (wave & 1) * 4096),
                             [g1] "s"(g + blin(mt * 20480 + 16384) + (wave & 1) * 1024), [d1] "s"(L_XB + mt * 4096 + (wave & 1) * 1024) : XG_CLOB);
                wait_vm<0>();
                LB(t);
            } else {
                const unsigned l16 = lane_id() * 16;
                xa = L_XA + wave * 2048 + l16; wl = L_W + wave * 10240 + l16;
#define XG_Q(A) asm volatile("s_cmp_lt_u32 %[t], 2\n\ts_cbranch_scc1 .Lq3%=\n\t" A "s_branch .Lqe%=\n\t.Lq3%=:\n\t" ASM_XG_S_LO ".Lqe%=:" : XG_OUT ASM_ST_OUT : XG_IN, [t] "s"(t) : XG_CLOB)
                if constexpr (CLS == 0 || CLS == 4) XG_Q(ASM_XG_S_LO1); else XG_Q(ASM_XG_S_LO2);
                __builtin_amdgcn_s_setprio(0);
                if constexpr (CLS == 0) {
                    const int ln = lane_id();
                    const u32x4 s4 = sent4();
                    const unsigned poff = (ln * 16 + ((ln < 8) ? h * 128 : (1 + (h >> 4)) * 2 * BLK - 128 + (h & 15) * 512)) | ((ln >= 40 || t < 2) ? 0x40000000u : 0u);
#pragma unroll
                    for (int c = 0; c < (int)NCP; ++c) st128(s4, rr, poff, RG_XP + ((t - 2) & (NSLOT - 1)) * XP_SLOT + (c >> 1) * XP_PAIR + (c & 1) * BLK);
                    const unsigned pbase = RG_PX + ((t - 3) & (NSLOT - 1)) * PX_SLOT + (h >> 2) * BLK;
                    const unsigned o2 = ((ln & 16) ? 5120 + ((h >> 1) & 1) * 1024 + (h & 1) * 256 + (ln >> 5) * 512 : 4096 + (h & 3) * 256 + (ln >> 5) * 3072) + (ln & 15) * 16;
                    if (t >= 3) { st128(s4, rr, ln * 16, pbase + (h & 3) * 1024); st128(s4, rr, o2, pbase); }
                }
                if (CLS == 4 && t >= HT0) {
                    const int ln = lane_id(), u = h >> 2;
                    for (int spins = 0;; ++spins) {
                        const unsigned fl = __builtin_amdgcn_raw_buffer_load_b32(rr, (ln & 3) * 128, RG_FH + BLK + u * 512, 16);
                        if (__all(fl > (unsigned)t)) break;
                        __builtin_amdgcn_s_sleep(2);
                        if (spin_fail(spins, P)) break;
                    }
                    asm volatile("" ::: "memory");
                    const char* g = P.ws + PG0 + pgb(t, par) + PG_HV + (16 + u) * 3 * BLK;
                    unsigned t0, t1, vo;
                    static_assert(L_QX == 16384, "");
                    asm volatile("s_add_u32 %0, %4, 0\n\ts_and_b32 %1, %0, 0xe000\n\ts_add_u32 %0, %0, %1\n\tv_add_u32 %2, %0, %5\n\ts_mov_b32 m0, 16384\n\ts_nop 0\n\tglobal_load_lds_dwordx4 %2, %3 sc1\n\t"
                                 "s_add_u32 %0, %4, 1024\n\ts_and_b32 %1, %0, 0xe000\n\ts_add_u32 %0, %0, %1\n\tv_add_u32 %2, %0, %5\n\ts_mov_b32 m0, 17408\n\ts_nop 0\n\tglobal_load_lds_dwordx4 %2, %3 sc1\n\t"
                                 "s_add_u32 %0, %4, 2048\n\ts_and_b32 %1, %0, 0xe000\n\ts_add_u32 %0, %0, %1\n\tv_add_u32 %2, %0, %5\n\ts_mov_b32 m0, 19456\n\ts_nop 0\n\tglobal_load_lds_dwordx4 %2, %3 sc1\n\t"
                                 "s_add_u32 %0, %4, 6144\n\ts_and_b32 %1, %0, 0xe000\n\ts_add_u32 %0, %0, %1\n\tv_add_u32 %2, %0, %5\n\ts_mov_b32 m0, 22528\n\ts_nop 0\n\tglobal_load_lds_dwordx4 %2, %3 sc1\n\t"
                                 "s_add_u32 %0, %4, 7168\n\ts_and_b32 %1, %0, 0xe000\n\ts_add_u32 %0, %0, %1\n\tv_add_u32 %2, %0, %5\n\ts_mov_b32 m0, 23552\n\ts_nop 0\n\tglobal_load_lds_dwordx4 %2, %3 sc1\n\t"
                                 "s_add_u32 %0, %4, 8192\n\ts_and_b32 %1, %0, 0xe000\n\ts_add_u32 %0, %0, %1\n\tv_add_u32 %2, %0, %5\n\ts_mov_b32 m0, 25600\n\ts_nop 0\n\tglobal_load_lds_dwordx4 %2, %3 sc1\n\t"
                                 : "=&s"(t0), "=&s"(t1), "=&v"(vo) : "s"(g), "s"((((h >> 1) & 1) * 12 + (h & 1) * 3) * 1024), "v"(ln * 16) : "memory", "scc");
                }
                dsto = c1_dst(wave, false);
                while ((unsigned)__builtin_amdgcn_s_memtime() - tb2 < STD) __builtin_amdgcn_s_sleep(2);
            }
            ASM_ST_SAVE(7);
            ST(6);
        }
        ST(0);
        unsigned fv = 0;
        if (CLS == 2) fv = (lane_id() & 15) * 128;
        crit1<CLS == 3 ? 0 : CLS < 2 ? 3 : CLS == 2 ? 12 : 2>(wf, C, rr, RG_XM + slot * XM_SLOT + (cpy >> 1) * XM_PAIR + (cpy & 1) * BLK, wave, wl, dsto, budget, fv, RG_FX + (cpy & 3) * 2048 STARG);
        if (CLS != 3) tb2 = (unsigned)__builtin_amdgcn_s_memtime();
        if constexpr (CLS == 0) {
            const unsigned sp = RG_XP + slot * XP_SLOT, sx = sp + (1 + (h >> 4)) * 2 * BLK + (h & 15) * 512;
            ASM_ST_DECL;
            asm volatile(ASM_SM : [c0] "+s"(budget) ASM_ST_OUT : WREGS(wf), [rr] "s"(rr), [sx] "s"(sx), [sy] "s"(sx + XP_PAIR), [sm] "s"(sp + h * 128), [cs] "s"(BLK), [cp] "s"(XP_PAIR), [c70] "s"(0x70), [scl] "s"(SCL)
                         : "memory", "v200", "v201", "v202", "v203", "v204", "v205", "v206", "v207", "v208", "v209", "v210", "v211", "v212", "v213", "v214", "v215", "v216", "v217", "v218", "v219", "v220", "v221", "v222", "v223", "v224", "v225", "v226", "v227", "v228", "v229", "v230", "v231", "v232", "v233", "v234", "v235", "v236", "v237", "v238", "v239", "v240", "v241", "v242", "v243", "v244", "v245", "v246", "v247", "v248", "v249", "v250", "v251", "v252", "v253", "v254", "v255");
            ASM_ST_SAVE(4);
            ST(5);
        } else if constexpr (CLS == 1 || CLS == 4) {
            const int ln = lane_id(), l15 = ln & 15, l4 = ln >> 4;
            constexpr int mt = (CLS == 4);
            const unsigned qa = L_QX + mt * 6144 + ln * 16, kmo = L_KM + l4 * 128 + (ln & 7) * 16;
            const unsigned xo = mt ? (h & 3) * 1024 : 5120 + ((h >> 1) & 1) * 1024 + (h & 1) * 256;
            const unsigned pm = l4 < 2 ? (mt ? 4096 : 7168) + (h & 3) * 256 - xo + l15 * 16 + l4 * 8 : OOB;
            const unsigned so = RG_PX + slot * PX_SLOT + (h >> 2) * BLK + xo;
            f32x4 sd;
#pragma unroll
            for (int r = 0; r < 4; ++r) sd[r] = (l4 * 4 + r > l15) ? -3.0e38f : 0.f;
            ASM_ST_DECL;
#define P4_ARGS : "+{v[240:243]}"(sd) ASM_ST_OUT : WREGS(wf), [qa] "v"(qa), [km] "v"(kmo), [pm] "v"(pm), [rr] "s"(rr), [so] "s"(so), [scl] "s"(SCL) : "memory", "v200", "v201", "v202", "v203", "v204", "v205", "v206", "v207", "v208", "v209", "v210", "v211", "v212", "v213", "v214", "v215", "v216", "v217", "v218", "v219", "v220", "v221", "v222", "v223", "v224", "v225", "v226", "v227", "v228", "v229", "v230", "v231", "v232", "v233", "v234", "v235", "v236", "v237", "v238", "v239", "v244", "v245", "v246", "v247", "v248", "v249", "v250", "v251"
            if constexpr (mt) asm volatile(ASM_P4_1 P4_ARGS); else asm volatile(ASM_P4_0 P4_ARGS);
            ASM_ST_SAVE(4);
            ST(5);
        } else {
            bar();
            ST(4);
            if (CLS == 2 && L && t + 2 < NSEG && !__all(fv > (unsigned)t + 2)) xn_wait(P, rr, t + 2, cpy);
            if (CLS == 2) { int tt = t; asm volatile("" : "+s"(tt)); if (tt + 2 == XS0) init_wait(P, h, 1); }
            if (CLS == 3) LA();
            {
                if (CLS == 3 && (wave == 4 || wave == 7)) {
                    if (t + 1 < NSEG) xdma(t + 1, 1, par);
                }
                ST(5);
            }
        }
        STFLUSH(t);
    }
    LF();
    };
    if (wave == 0) run(std::integral_constant<int, 0>{});
    else if (wave == 1) run(std::integral_constant<int, 1>{});
    else if (wave == 2) run(std::integral_constant<int, 4>{});
    else if (wave == 3) run(std::integral_constant<int, 2>{});
    else run(std::integral_constant<int, 3>{});
}

#undef STROLE
#define STROLE 8
__device__ __forceinline__ void h_role(const Params& P, int wave, int L, int i, int x) {
    const rsrc_t rp = mkrsrc(P.ws + PG0, PG_L);
    const char* xn = P.ws + PG0 + PG_XN + L * PG_LX;
    auto run = [&](auto Cc) {
    constexpr bool HI = decltype(Cc)::value;
    const int mt = (wave >> 1) & 1;
    WRegs wf;
    load_w3(wf, i < 16 ? P.w[2] : P.w[0], wave, (i < 16 ? i * 2 + HI : (i - 16) * 4 + HI * 2) * 384, i >= 16);
    PLACE();
    auto xdma = [&](int seg, int hb) {
        const unsigned l16 = lane_id() * 16;
        const char* g = xn + pgb(seg, par);
        dma4(g + blin(mt * 20480 + hb * 8192 + (wave & 1) * 4096), l16, L_XA + mt * 16384 + hb * 8192 + (wave & 1) * 4096);
        dmaq<0>(g + blin(mt * 20480 + 16384) + hb * 2048 + (wave & 1) * 1024, l16, L_XB + mt * 4096 + hb * 2048 + (wave & 1) * 1024);
    };
    const unsigned qs = (HI ? L_KX : L_QX) + (wave & 1) * 3072 + mt * 8;
    const unsigned xa = L_XA + wave * 2048 + lane_id() * 16, wl = L_W + wave * 10240 + lane_id() * 16;
    const unsigned fp = RG_FH + (i >> 4) * BLK + ((i & 15) * 4 + (wave & 3)) * 128;
    const unsigned fb = L ? RG_FX + (cpy & 3) * 2048 : RG_FV + (i & 3) * 2048, fa = L ? 0 : HAH;
    unsigned go = 0, all = 0;
    STDECL;
    LDECL;
    for (int t = HT0; t <= NSEG; ++t) {
        ST(0);
        if constexpr (HI) {
            const unsigned l16 = lane_id() * 16;
            if (t > HT0) {
                u32x4 v[6];
                const int w4 = wave - 4;
#pragma unroll
                for (int k = 0; k < 6; ++k) v[k] = lds_ld<u32x4>(L_QX + ((k >> 1) * 8 + w4 * 2 + (k & 1)) * 1024 + l16);
                const unsigned so = pgb(t - 1, par) + PG_HV + i * 3 * BLK + w4 * 2048;
#pragma unroll
                for (int k = 0; k < 6; ++k) st128(v[k], rp, l16 + (k & 1) * 1024, so + (k >> 1) * BLK);
                if (!all) {
                    wait_vm<0>();
                    if (l16 < 512) __builtin_amdgcn_raw_buffer_store_b32(t, rr, l16 >> 2, fp, 16);
                }
            }
            ST(1);
            if (t == NSEG) break;
            if (!all) bar();
            xdma(t, 1);
        } else {
            if (t == NSEG) break;
            if (!go) {
                for (int spins = 0;; ++spins) {
                    if (__all(__builtin_amdgcn_raw_buffer_load_b32(rr, (lane_id() & 15) * 128, fb, 16) + fa > (unsigned)t)) break;
                    __builtin_amdgcn_s_sleep(HSL);
                    if (spin_fail(spins, P)) break;
                }
                asm volatile("" ::: "memory");
                xdma(t, 0); wait_vm<0>();
            }
            if (!all) bar();
        }
        ST(2);
        C1Regs C;
        const WRegs& W = wf;
        ASM_ST_DECL;
        if constexpr (HI) {
            const unsigned l4 = lane_id() * 4;
            asm volatile(ASM_XG_H_HI : XG_OUT ASM_ST_OUT : XG_IN, [rr] "s"(rr), [fp] "s"(fp), [tv] "s"(t), [fv] "v"((all && l4 < 128) ? l4 : OOB) : XG_CLOB);
        } else {
            asm volatile(ASM_XG_H_LO : XG_OUT ASM_ST_OUT : XG_IN : XG_CLOB);
            if (t + 1 == XS0) init_wait(P, wave, 1);
            const unsigned fv = __builtin_amdgcn_raw_buffer_load_b32(rr, (lane_id() & 15) * 128, fb, 16) + fa;
            go = __builtin_amdgcn_readfirstlane((t + 1 < NSEG && __all(fv > (unsigned)t + 1)) ? 1u : 0u);
            if (go) xdma(t + 1, 0);
            lds_st<unsigned>(L_MEM + wave * 4, go);
            wait_vm<0>();
        }
        ASM_ST_SAVE(3);
        ST(4);
        if (!HI) { LB(t); LA(); }
        PMARK(L && wave == 7, 6, t);
        bar();
        const u32x4 gv = lds_ld<u32x4>(L_MEM);
        all = __builtin_amdgcn_readfirstlane(gv[0] & gv[1] & gv[2] & gv[3]);
        ST(5);
        STFLUSH(t);
    }
    LF();
    };
    if (wave < 4) run(std::false_type{}); else run(std::true_type{});
}

#undef STROLE
#define STROLE 2
__device__ __forceinline__ void v_role(const Params& P, int wave, int L, int h, int x) {
    WRegs wf;
    STDECL;
    auto runv = [&](auto Cc) {
        load_w3(wf, P.w[2], wave, h * 384, false);
        MARKX(!L && wave == 0, 6);
        PLACE();
        unsigned budget = 1u << 18;
        const unsigned wl = L_W + wave * 10240 + lane_id() * 16, dsto = c1_dst(wave, false);
        auto vfetch = [&](int seg, int tries) {
            const int ln = lane_id();
            for (int spins = 0; tries != -2; ++spins) {
                if (spins == tries) return false;
                const unsigned f = __builtin_amdgcn_raw_buffer_load_b32(rr, (ln & 3) * 128, RG_FH + (h >> 1) * 512, 16);
                if (__all(f > (unsigned)seg)) break;
                __builtin_amdgcn_s_sleep(4);
                if (spin_fail(spins, P)) break;
            }
            asm volatile("" ::: "memory");
            const char* g = P.ws + PG0 + pgb(seg, par) + PG_HV + (h >> 1) * 3 * BLK;
#pragma unroll
            for (int k = 0; k < 3; ++k) {
                const int f = (h & 1) * 12 + wave * 3 + k;
                dmaq<0>(g + (f >> 3) * BLK + (f & 7) * 1024, ln * 16, L_QX + (seg & 1) * 12288 + (wave * 3 + k) * 1024);
            }
            return true;
        };
#pragma unroll 1
        for (int t = 0; t < HT0; ++t) {
            if (L) xn_wait(P, rr, t, cpy);
            const unsigned l16 = lane_id() * 16;
            const char* g = P.ws + PG0 + PG_XN + L * PG_LX + pgb(t, par);
            dma4(g + blin(wave * 4096), l16, L_XA + wave * 4096); dma4(g + blin(20480 + wave * 4096), l16, L_XA + 16384 + wave * 4096);
            dmaq<0>(g + blin(16384) + wave * 1024, l16, L_XB + wave * 1024); dmaq<0>(g + blin(20480 + 16384) + wave * 1024, l16, L_XB + 4096 + wave * 1024);
            wait_vm<0>();
            bar();
            C1Regs C;
            const WRegs& W = wf;
            const unsigned xa = L_XA + wave * 2048 + l16, qs = L_QX + (wave & 1) * 3072 + ((wave >> 1) & 1) * 8 + t * 12288;
            asm volatile(ASM_XG_V : XG_OUT : XG_IN : XG_CLOB);
            if (t == 0) bar();
        }
        bool seen = false;
        unsigned tb2 = 0;
        for (int t = 0; t < NSEG; ++t) {
            if (t + 1 - V1A >= HT0 && t + 1 - V1A < NSEG) { vfetch(t + 1 - V1A, seen ? -2 : -1); wait_vm<0>(); }
            ST(7);
            bar();
            C1Regs C;
            asm volatile("ds_read_b128 v[200:203], %4\n\tds_read_b128 v[204:207], %4 offset:1024\n\tds_read_b128 v[228:231], %4 offset:2048\n\tds_read_b128 v[232:235], %4 offset:3072\n\t"
                         "v_mov_b64 v[224:225], 0\n\tv_mov_b64 v[226:227], 0\n\tv_mov_b64 v[244:245], 0\n\tv_mov_b64 v[246:247], 0\n\tv_mov_b64 v[248:249], 0\n\tv_mov_b64 v[250:251], 0\n\ts_waitcnt lgkmcnt(0)"
                         : XG_OUT : "v"(wl) : "memory");
            ST(6);
            while ((unsigned)__builtin_amdgcn_s_memtime() - tb2 < VTD) __builtin_amdgcn_s_sleep(1);
            unsigned fv = (lane_id() & 3) * 128;
            crit1<10 + decltype(Cc)::value>(wf, C, rr, RG_XM + (t & (NSLOT - 1)) * XM_SLOT + (cpy >> 1) * XM_PAIR + (cpy & 1) * BLK, wave, wl, dsto, budget, fv, RG_FH + (h >> 1) * 512 STARG);
            __builtin_amdgcn_s_setprio(0);
            bar();
            seen = __all(fv > (unsigned)t + 2 - V1A);
            tb2 = (unsigned)__builtin_amdgcn_s_memtime();
            ST(4);
            STC(5);
            STFLUSH(t);
        }
    };
    if (wave < 2) runv(std::integral_constant<int, 3>{});
    else if (wave < 4) runv(std::integral_constant<int, 2>{});
    else {
        const int w4 = wave - 4;
        const rsrc_t rd = mkrsrc(P.ws + PG0, PG_L);
        load_wo(wf, P.w[3], wave, h);
        PLACE();
        bar();
        bar();
        for (int t = -1; t < NSEG; ++t) {
            const unsigned slot = t & (NSLOT - 1);
            bar();
            ST(7);
#pragma unroll 1
            for (int n = V1A ? t + (t <= 0) : t + (t != 1) + (t == 0); n <= (V1A && t >= 0 ? t : t + 1) && n < NSEG; ++n) {
                const unsigned l16 = lane_id() * 16;
                const unsigned wl = L_W + wave * 10240 + l16, va = L_QX + (n & 1) * 12288 + l16;
                unsigned so = ((n & 7) << 22) + pgb(64 + (w4 >> 1), par) + ((w4 & 1) * 40 + (h >> 3)) * BLK + (h & 7) * 1024;
                asm volatile(ASM_VOX : [so] "+s"(so) : WREGS(wf), [l16] "v"(l16), [wl] "v"(wl), [va] "v"(va), [rd] "s"(rd) : "memory", "scc", "v200", "v201", "v202", "v203", "v204", "v205", "v206", "v207", "v208", "v209", "v210", "v211", "v212", "v213", "v214", "v215", "v216", "v217", "v218", "v219", "v220", "v221", "v222", "v223", "v224", "v225", "v226", "v227", "v228", "v229", "v230", "v231", "v232", "v233", "v234", "v235", "v236", "v237", "v238", "v239", "v240", "v241", "v242", "v243", "v244", "v245", "v246", "v247", "v248", "v249", "v250", "v251");
                if (V1A || n < 2) {
                    wait_vm<0>();
                    const unsigned l4 = lane_id() * 4;
                    if (l4 < 128) __builtin_amdgcn_raw_buffer_store_b32(n + 1, rr, l4, RG_FV + (w4 >> 1) * BLK + ((w4 & 1) * 32 + h) * 128, 16);
                }
            }
            if (t < 0) continue;
            ST(0);
            unsigned wl = L_W + wave * 10240 + lane_id() * 16;
            bf16x16 lw = lds_frag(wl, 1024);
            u32x32 cz;
            asm volatile("v_mov_b64 v[220:221], 0\n\tv_mov_b64 v[222:223], 0\n\tv_mov_b64 v[224:225], 0\n\tv_mov_b64 v[226:227], 0\n\tv_mov_b64 v[228:229], 0\n\tv_mov_b64 v[230:231], 0\n\tv_mov_b64 v[232:233], 0\n\tv_mov_b64 v[234:235], 0\n\tv_mov_b64 v[236:237], 0\n\tv_mov_b64 v[238:239], 0\n\tv_mov_b64 v[240:241], 0\n\tv_mov_b64 v[242:243], 0\n\tv_mov_b64 v[244:245], 0\n\tv_mov_b64 v[246:247], 0\n\tv_mov_b64 v[248:249], 0\n\tv_mov_b64 v[250:251], 0" : "={v[220:251]}"(cz));
            unsigned vo = ((wl >> 8) & 1) * (1 + (w4 & 1) * 2) * BLK + ((wl >> 9) & 1) * 1024 + (wl & 255);
            asm volatile("" : "+{v[212:219]}"(lw), "+{v[220:251]}"(cz), "+v"(wl), "+v"(vo));
            unsigned l16 = wl & 1023u;
            bar();
            wait_vm<0>();
            if (!V1A && l16 < 512 && t && t + 1 < NSEG) __builtin_amdgcn_raw_buffer_store_b32(t + 2, rr, l16 >> 2, RG_FV + (w4 >> 1) * BLK + ((w4 & 1) * 32 + h) * 128, 16);
            ST(1);
            const int sidx = (wl & 128u) ? 0xEEEE : 0x4444;
            unsigned so = RG_VM + slot * VM_SLOT + (unsigned)((w4 * 10 - (w4 & 1) + (h >> 4) * 2) * BLK + ((h >> 2) & 3) * 2048 + (h & 3) * 256);
            ST(3);
            ASM_ST_DECL;
            asm volatile(ASM_CRIT2 : [so] "+s"(so), "+{v[212:219]}"(lw), "+{v200}"(l16), "+{v[220:251]}"(cz) ASM_ST_OUT
                         : WREGS(wf), [wl] "v"(wl), [vo] "v"(vo), [sidx] "v"(sidx), [rr] "s"(rr)
                         : "memory", "scc", "v201", "v202", "v203", "v204", "v205", "v206", "v207", "v208", "v209", "v210", "v211");
            ASM_ST_SAVE(4);
            ST(5);
            STFLUSH(t);
        }
    }
}

#undef STROLE
#define STROLE 4
constexpr unsigned R_RED = 0;
constexpr unsigned R_PART = 16384;
__device__ __forceinline__ void r_role(const Params& P, int wave, int L, int r5, int x) {
    PLACE();
    const int jj = (r5 * 4 + cpy) ^ (r5 * 4 + x >= 6 ? 1 : 0);
    const int lane = lane_id(), l15 = lane & 15, l4 = lane >> 4;
    const unsigned l16 = lane * 16;
    const rsrc_t rd = mkrsrc(P.ws + PG0, PG_L);
    const int sidx = (l15 < 8) ? 0x4444 : 0xEEEE;
    unsigned offp[3];
    offp[0] = RG_XP + (cpy >> 1) * XP_PAIR + (cpy & 1) * BLK + (1 + (wave >> 2)) * 2 * BLK + (wave & 3) * 2048 + (PLAY ? (l4 >> 1) * 512 + (((l15 >> 2) & 1) + (l15 & 2)) * 128 + (l4 & 1) * 64 + (l15 >> 3) * 32 + (l15 & 1) * 16 : l16);
    offp[2] = (l15 < 8) ? RG_XP + (cpy >> 1) * XP_PAIR + (cpy & 1) * BLK + wave * 512 + l4 * 128 + l15 * 16 : OOB;
    const unsigned offv = RG_VM + ((jj >> 1) * 4 + (wave >> 2) * 2 + (jj & 1)) * BLK + (wave & 3) * 2048 + l16;
    const int j = jj * 2 + ((wave >> 1) & 1), fl = (wave & 1) * 32 + (lane & 31);
    const unsigned offo = RG_XM + (wave >> 2) * XM_PAIR + (lane >> 5) * BLK + (j & 31) * 256 + (j >> 5) * 2 * BLK + fl * 4;
    MARK(!L && jj == 0 && wave == 0, 5);
    {
        const int row = (lane >> 2) & 7, col = 16 * j + 4 * (lane & 3) + 2 * (wave & 1);
        __builtin_amdgcn_raw_buffer_store_b32(nosent(*(const unsigned*)(P.mem0 + row * DM + col)), rr, offo, 0, 16);
    }
    auto red_addr = [](int fl) { return R_RED + fl * 80 + (fl >> 4) * 16; };
    const unsigned rw0 = (lane >> 5) * 5248 + red_addr((((l15 >> 1) & 1) * 8 + 4 * (l4 & 1)) * 4 + (l15 >> 2)) + (l15 & 1) * 4 + (wave ^ (l15 >> 3)) * 8;
    const unsigned rrd = ((wave >> 1) & 1) * 5248 + red_addr(fl) + (lane >> 5) * 32;
    u32x32 vx;
    const unsigned fo = (lane & 3) * 128, fs = RG_FV + (jj / 10) * BLK + (((jj / 5) & 1) * 32 + wave * 4) * 128, vxo = pgb(64 + jj / 10, par) + (unsigned)((jj % 10) * 8 + (wave >> 1)) * BLK + (wave & 1) * 4096;
    const rsrc_t rp = mkrsrc(P.ws + PG0, PG_L);
    const unsigned xo = (l4 >> 1) * 256 + l15 * 16 + (l4 & 1) * 8, pw = R_PART + wave * 1024 + l16, pr = R_PART + (wave & 3) * 8192 + l16;
    const int fin = wave >> 2;
    asm volatile("" : "={v[128:159]}"(vx));
    auto vx_fetch = [&](int t) {
        for (int spins = 0;; ++spins) {
            const unsigned f = __builtin_amdgcn_raw_buffer_load_b32(rr, fo, fs, 16);
            if (__all(f > (unsigned)t)) break;
            __builtin_amdgcn_s_sleep(2);
            if (spin_fail(spins, P)) break;
        }
        const unsigned sv = ((t & 7) << 22) + vxo;
        asm volatile("buffer_load_dwordx4 v[128:131], %1, %2, %3 offen sc1\n\tbuffer_load_dwordx4 v[132:135], %1, %2, %3 offen offset:1024 sc1\n\t"
                     "buffer_load_dwordx4 v[136:139], %1, %2, %3 offen offset:2048 sc1\n\tbuffer_load_dwordx4 v[140:143], %1, %2, %3 offen offset:3072 sc1\n\t"
                     "buffer_load_dwordx4 v[144:147], %1, %2, %4 offen sc1\n\tbuffer_load_dwordx4 v[148:151], %1, %2, %4 offen offset:1024 sc1\n\t"
                     "buffer_load_dwordx4 v[152:155], %1, %2, %4 offen offset:2048 sc1\n\tbuffer_load_dwordx4 v[156:159], %1, %2, %4 offen offset:3072 sc1"
                     : "+{v[128:159]}"(vx) : "v"(l16), "s"(rp), "s"(sv), "s"(sv + 4 * BLK) : "memory");
    };
    vx_fetch(0);
    unsigned budget = 1u << 18, tpub = 0;
    STDECL;
    LDECL;
    for (int t = 0; t < NSEG; ++t) {
        const unsigned slot = t & (NSLOT - 1);
        ST(0);
        while ((unsigned)__builtin_amdgcn_s_memtime() - tpub < RTD) __builtin_amdgcn_s_sleep(1);
        u32x8 vm, pv;
        u32x4 pm;
        {
            unsigned a;
            ASM_ST_DECL;
            asm volatile(ASM_RCRIT : "=&{v[176:183]}"(vm), "=&{v[184:191]}"(pv), "=&{v[192:195]}"(pm), [a] "=&s"(a), [n] "+s"(budget) ASM_ST_OUT ASM_ST2_OUT ASM_ST3_OUT
                         : [rs] "s"(rr), [ov] "v"(offv), [sv] "s"(slot * VM_SLOT), [o0] "v"(offp[0]), [o2] "v"(offp[2]), [sp] "s"(slot * XP_SLOT),
                           "{v[128:159]}"(vx), [sidx] "v"(sidx), [rw] "v"(rw0), [rd] "v"(rrd), [oo] "v"(offo),
                           [so] "s"(((t + 1) & (NSLOT - 1)) * XM_SLOT), [pub] "s"(NSEG - 1 - t)
                         : "memory", "vcc", "scc", "v160", "v161", "v162", "v163", "v164", "v165", "v166", "v167", "v196", "v197", "v200", "v201", "v202", "v203", "v204", "v205", "v206", "v207");
            ASM_ST_SAVE(2); ASM_ST2_SAVE(1); ASM_ST3_SAVE(6);
        }
        ST(3);
        LA();
        MARK(!L && jj == 0 && wave == 0 && t < 4, 10 + t);
        PMARK(wave == 0, L * 7, t - 1 + L);
        tpub = (unsigned)__builtin_amdgcn_s_memtime();
        __builtin_amdgcn_s_sleep(RXDEL);
        unsigned early;
#pragma unroll 1
        for (int spins = 1023;; spins += 1024) {
            unsigned n;
            const unsigned sx = pgb(t, par) + blin(((wave >> 1) & 1) * 20480 + jj * 1024) + (wave & 1) * 512;
            const unsigned pxb = RG_PX + slot * PX_SLOT + wave * BLK, sv = (((t + 1) & 7) << 22) + vxo;
            ASM_ST_DECL;
            asm volatile(ASM_RX : "+{v[128:159]}"(vx), [n] "=&s"(n), [a] "=&s"(early) ASM_ST_OUT ASM_ST2_OUT
                         : "{v[176:183]}"(vm), "{v[184:191]}"(pv), "{v[192:195]}"(pm), [l16] "v"(l16), [rs] "s"(rr), [s0] "s"(pxb), [s1] "s"(pxb + 4096), [rd] "s"(rd), [rx] "s"(rd), [xo] "v"(xo),
                           [fo] "v"(fo), [fs] "s"(fs), [tn] "s"(t + 1), [sv0] "s"(sv), [sv1] "s"(sv + 4 * BLK), [rp] "s"(rp), [sx] "s"(PG_XR + L * PG_LX + sx), [sa] "s"(PG_XA + sx),
                           [pw] "v"(pw), [pr] "v"(pr), [fin] "s"(fin)
                         : "memory", "vcc", "scc", "v96", "v97", "v98", "v99", "v100", "v101", "v102", "v103", "v104", "v105", "v106", "v107", "v108", "v109", "v110", "v111", "v112", "v113", "v114", "v115", "v116", "v117", "v118", "v119", "v120", "v121", "v122", "v123", "v124", "v125", "v126", "v127", "v196", "v200", "v201", "v202", "v203", "v204", "v205", "v206", "v207", "v208", "v209", "v210", "v211", "v212", "v213", "v214", "v215", "v216", "v217", "v218", "v219", "v220", "v221", "v222", "v223", "v224", "v225", "v226", "v227", "v228", "v229", "v230", "v231", "v240", "v241", "v242", "v243", "v248", "v250", "v251");
            ASM_ST_SAVE(4); ASM_ST2_SAVE(5);
            if (__builtin_amdgcn_readfirstlane(n)) break;
            early = 0;
            if (spin_fail(spins, P)) { bar(); break; }
        }
        if (t + 1 < NSEG && !__builtin_amdgcn_readfirstlane(early)) vx_fetch(t + 1);
        PMARK(!L && wave == 7 && PMK == 1, 1, t - 1);
        st128(sent4(), rr, offv, slot * VM_SLOT); st128(sent4(), rr, offv + 1024, slot * VM_SLOT);
        __builtin_amdgcn_raw_buffer_store_b32(SENT, rr, offo, slot * XM_SLOT, 16);
        if (wave < 2 && t + AH < NSEG) st128(sent4(), rd, l16, PG_XA + pgb(t + AH, par) + blin(wave * 20480 + jj * 1024));
        ST(7);
        LB(t);
        STFLUSH(t);
    }
    LF();
}

#undef STROLE
#define STROLE 16
template <int WHICH>
__device__ __forceinline__ void w_role(const Params& P, int wave, int L, int cu, int x) {
    constexpr int KS = 20, N = (WHICH == 3) ? FF : DM, NU = 2 * NSEG;
    constexpr unsigned D_IN = WHICH == 1 ? PG_XA : WHICH == 2 ? PG_HS : PG_X1;
    constexpr unsigned D_OUT = WHICH == 1 ? PG_HS : WHICH == 2 ? PG_X1 : PG_H2;
    if (WHICH != 3 && cu * 8 + wave >= 20) {
        for (int u = 0; u <= NU; ++u) bar();
        return;
    }
    auto run = [&](auto Cc) {
    constexpr bool LO = decltype(Cc)::value;
    const int ln = lane_id(), l15 = ln & 15, l4 = ln >> 4;
    const unsigned l16 = ln * 16;
    const rsrc_t rd = mkrsrc(P.ws + PG0, PG_L);
    const int nt0 = (cu * 8 + wave) * 2;
    u32x32 w[5];
    {
        u32x4 f[2 * KS];
        load_wn<2, KS, N>(f, P.w[3 + WHICH], DM, wave, nt0, 0);
#pragma unroll
        for (int n = 0; n < 5; ++n) {
            u32x8 q[4];
#pragma unroll
            for (int i = 0; i < 4; ++i) q[i] = __builtin_shufflevector(f[n * 8 + 2 * i], f[n * 8 + 2 * i + 1], 0, 1, 2, 3, 4, 5, 6, 7);
            w[n] = cat32(q[0], q[1], q[2], q[3]);
        }
        asm volatile("" : "+{v[0:31]}"(w[0]), "+{v[32:63]}"(w[1]), "+{v[64:95]}"(w[2]), "+{v[96:127]}"(w[3]), "+{v[128:159]}"(w[4]));
    }
    PLACE();
    bar();
    const int ocol = nt0 * 16 + (l4 & 1) * 16 + (l4 >> 1) * 8;
    const unsigned o3 = (unsigned)((l4 & 1) * 512 + (l4 >> 1) * 256 + l15 * 16), ro = (l4 >> 1) * 256 + l15 * 16 + (l4 & 1) * 8;
    const int grp = nt0 / 10, sh = ((grp & 1) ? -1 : 1) * ((grp >> 1) + 1);
    if (WHICH == 1) {
        unsigned zv;
        asm volatile("v_mov_b32 %0, 0" : "=v"(zv));
        st128(((u32x4){zv, zv, zv, zv}), rd, l15 <= (grp >> 1) ? toff((grp & 1) ? S_ - 1 - l15 : l15, ocol, par) : OOB, PG_HS);
    }
    auto in0 = [&](int u) { return D_IN + pgb(u >> 1, par) + blin((u & 1) * 20480 + wave * 4096); };
    auto in1 = [&](int u) { return D_IN + pgb(u >> 1, par) + blin((u & 1) * 20480 + 16384) + wave * 1024; };
    u32x16 va;
    u32x4 vb;
    u32x8 cs, k8;
#define WT_W "{v[0:31]}"(w[0]), "{v[32:63]}"(w[1]), "{v[64:95]}"(w[2]), "{v[96:127]}"(w[3]), "{v[128:159]}"(w[4])
    asm volatile("v_mov_b32 v216, 0x3f803f80\n\tv_mov_b32 v217, v216\n\tv_mov_b32 v218, v216\n\tv_mov_b32 v219, v216\n\tv_mov_b32 v220, %[c0]\n\tv_mov_b32 v232, -1\n\tv_mov_b32 v236, -1\n\t"
                 "v_mov_b32 v240, -1\n\tv_mov_b32 v244, -1\n\tv_mov_b32 v248, -1\n\t" ASM_WCS
                 : "=&{v[196:203]}"(cs), "=&{v[216:223]}"(k8), "=&{v[232:247]}"(va), "=&{v[248:251]}"(vb) : WT_W, [c0] "s"(-2.0f * 0.7978845608028654f * 1.4426950408889634f));
    unsigned la = l16, lw = wave * 8192 + l16, sr = l4 * 8192 + ((l15 >> 2) * 16 + l15) * 16 + (l15 & 3) * 4, budget = 1u << 18;
    wait_vm<0>();
    STDECL;
    LDECL;
#pragma unroll 1
    for (int u = 0; u < NU; ++u) {
        const int mt = u & 1;
        const unsigned pg = pgb(u >> 1, par), c3 = D_OUT + blin(mt * (N * 32) + (nt0 >> 1) * 1024);
        unsigned oa = o3, ob = 0, so = pg + c3, sa = u + 2 * AH < NU ? pgb((u >> 1) + AH, par) + c3 : pg + PG_SS + 4096, a;
        if (WHICH == 1) {
            const int orow = u * 16 + l15 + sh;
            ob = toff(orow + AH * SEG, ocol, par);
            oa = (unsigned)orow < (unsigned)S_ ? (ob ^ 8192u) - (AH << 21) : OOB;
            if (orow + AH * SEG >= S_ + (sh < 0 ? sh : 0)) ob = OOB;
            so = sa = D_OUT;
        }
        if (mt) { ST(0); LB(u >> 1); } else { LA(); }
        ASM_ST_DECL;
#define WT_A WT_W, [la] "v"(la), [l16] "v"(l16), [oa] "v"(oa), [rs] "s"(rd), [so] "s"(so), [sa] "s"(sa)
#define WT_G [sr] "v"(sr), [kd] "s"(1.0f / DM), [ke] "s"(1e-5f), [k1] "s"(-2.0f * 0.7978845608028654f * 1.4426950408889634f * 0.044715f)
#define WT_1 WT_G, [ob] "v"(ob), [ss] "s"(pg + PG_SS + mt * 128), [sw] "s"(nt0)
#define WT_2 [ro] "v"(ro), [sx] "s"(pg + PG_XA + blin(mt * 20480 + nt0 * 512)), [ss] "s"(pg + PG_SS + mt * 128)
#define WT_C "memory", "vcc", "scc", "v160", "v161", "v162", "v163", "v164", "v165", "v166", "v167", "v168", "v169", "v170", "v171", "v172", "v173", "v174", "v175", "v176", "v177", "v178", "v179", "v180", "v181", "v182", "v183", "v184", "v185", "v186", "v187", "v188", "v189", "v190", "v191", "v192", "v193", "v194", "v195", "v204", "v205", "v206", "v207", "v208", "v209", "v210", "v211", "v212", "v213", "v214", "v215", "v252"
#define WT_LO(A, ...) asm volatile(A : "+{v[196:203]}"(cs), "+{v[216:223]}"(k8), "+{v[232:247]}"(va), "+{v[248:251]}"(vb), [n] "+s"(budget), [a] "=&s"(a) ASM_ST_OUT ASM_ST2_OUT ASM_ST3_OUT \
                              : WT_A, __VA_ARGS__, [lw] "v"(lw), [i0] "s"(in0(u)), [i1] "s"(in1(u)), [n0] "s"(in0(u + 1)), [n1] "s"(in1(u + 1)) : WT_C)
#define WT_HI(A, ...) asm volatile(A : "+{v[196:203]}"(cs), "+{v[216:223]}"(k8) ASM_ST_OUT ASM_ST3_OUT : WT_A, __VA_ARGS__ : WT_C)
        if constexpr (LO) { if constexpr (WHICH == 1) WT_LO(ASM_WT1_LO, WT_1); else if constexpr (WHICH == 2) WT_LO(ASM_WT2_LO, WT_2); else WT_LO(ASM_WT3_LO, WT_G); }
        else { if constexpr (WHICH == 1) WT_HI(ASM_WT1_HI, WT_1); else if constexpr (WHICH == 2) WT_HI(ASM_WT2_HI, WT_2); else WT_HI(ASM_WT3_HI, WT_G); }
        la ^= 32768u; lw ^= 32768u; sr ^= 32768u;
        PMARK(!L && mt == (WHICH != 1) && (PMK == 1 || WHICH == 3), PMK == 1 ? 1 + WHICH : 1, (u >> 1) - (WHICH == 1));
    }
    wait_vm<0>();
    if constexpr (LO) asm volatile("" :: "{v[232:247]}"(va), "{v[248:251]}"(vb));
    LF();
    };
    if (wave < 4) run(std::true_type{}); else run(std::false_type{});
}

__device__ __forceinline__ void w4_role(const Params& P, int wave, int L, int cu, int x) {
    constexpr unsigned W_FL = 122880;
    const int ln = lane_id(), l15 = ln & 15, l4 = ln >> 4;
    const unsigned l16 = ln * 16;
    const rsrc_t rd = mkrsrc(P.ws + PG0, PG_L);
    lds_st<unsigned>(W_FL + wave * 4, 0u);
    STDECL;
    if (wave < 6) {
        u32x32 w[5];
        {
            u32x4 g[40];
            load_wn<5, 8, DM>(g, P.w[7], FF, wave, cu * 5, wave * 8);
#pragma unroll
            for (int n = 0; n < 5; ++n) {
                u32x8 f[4];
#pragma unroll
                for (int i = 0; i < 4; ++i) f[i] = __builtin_shufflevector(g[n * 8 + 2 * i], g[n * 8 + 2 * i + 1], 0, 1, 2, 3, 4, 5, 6, 7);
                w[n] = cat32(f[0], f[1], f[2], f[3]);
            }
            asm volatile("" : "+{v[0:31]}"(w[0]), "+{v[32:63]}"(w[1]), "+{v[64:95]}"(w[2]), "+{v[96:127]}"(w[3]), "+{v[128:159]}"(w[4]));
        }
        PLACE();
        bar();
        unsigned budget = 1u << 18;
        u32x32 av;
        asm volatile("v_mov_b32 v160, -1\n\tv_mov_b32 v164, -1\n\tv_mov_b32 v168, -1\n\tv_mov_b32 v172, -1\n\tv_mov_b32 v176, -1\n\tv_mov_b32 v180, -1\n\tv_mov_b32 v184, -1\n\tv_mov_b32 v188, -1" : "={v[160:191]}"(av));
        for (int u = 0; u < 2 * NSEG; ++u) {
            if (u & 1) ST(0);
            for (int spins = 0;; ++spins) {
                asm volatile("" ::: "memory");
                if ((int)__builtin_amdgcn_readfirstlane(lds_ld<unsigned>(W_FL + 24 + (u & 1) * 4)) + 3 >= u) break;
                __builtin_amdgcn_s_sleep(2);
                if (spin_fail(spins, P)) break;
            }
            const unsigned gb = PG_H2 + pgb(u >> 1, par) + ((u & 1) * 6 + wave) * BLK, gn = PG_H2 + pgb((u + 1) >> 1, par) + ((~u & 1) * 6 + wave) * BLK;
            ASM_ST_DECL;
            asm volatile(ASM_W4_M : [n] "+s"(budget), "+{v[160:191]}"(av) ASM_ST2_OUT
                         : "{v[0:31]}"(w[0]), "{v[32:63]}"(w[1]), "{v[64:95]}"(w[2]), "{v[96:127]}"(w[3]), "{v[128:159]}"(w[4]),
                           [ld] "v"(l16 + wave * 1024 + (u & 3) * 30720), [l16] "v"(l16), [rs] "s"(rd), [s0] "s"(gb), [s1] "s"(gb + 4096), [n0] "s"(gn), [n1] "s"(gn + 4096), [un] "s"(u + 1), [fl] "v"(W_FL + wave * 4)
                         : "memory", "vcc", "scc", "v192", "v193", "v194", "v195", "v196", "v197", "v198", "v199", "v200", "v201", "v202", "v203", "v204", "v205", "v206", "v207", "v208", "v209", "v210", "v211", "v212");
            PMARK(!L && (u & 1) && PMK == 2, 2, u >> 1);
        }
        asm volatile("s_waitcnt vmcnt(0)" :: "{v[160:191]}"(av) : "memory");
    } else {
        const int mt = wave - 6;
        PLACE();
        bar();
        const unsigned c0 = mt * 20480 + cu * 2560, o8 = (l4 >> 1) * 256 + l15 * 16 + (l4 & 1) * 8;
        unsigned budget = 1u << 18;
        u32x16 ra;
        u32x4 rb;
        asm volatile("v_mov_b32 v164, -1" : "={v[164:179]}"(ra), "={v[180:183]}"(rb));
        __builtin_amdgcn_s_setprio(W4P);
#define W4F_OUT "+{v[164:179]}"(ra), "+{v[180:183]}"(rb), [n] "+s"(budget)
#define W4F_IN [o0] "v"(o8 + blin(c0)), [o1] "v"(o8 + blin(c0 + 512)), [o2] "v"(o8 + blin(c0 + 1024)), [o3] "v"(o8 + blin(c0 + 1536)), [o4] "v"(o8 + blin(c0 + 2048)), [rd] "s"(rd), \
               [c1] "s"(PG_X1 + pg), [ca] "s"(PG_XA + pg), [n1] "s"(PG_X1 + pn), [na] "s"(PG_XA + pn), [f0] "v"(W_FL + (ln & 3) * 4), [f1] "v"(W_FL + 16 + (ln & 1) * 4), [fd] "v"(W_FL + wave * 4), \
               [la] "v"((u & 3) * 30720 + l16), [u] "s"(u), [un] "s"(u + 1)
#define W4F_CLOB "memory", "vcc", "scc", "v64", "v65", "v66", "v67", "v68", "v69", "v70", "v71", "v72", "v73", "v74", "v75", "v76", "v77", "v78", "v79", "v80", "v81", "v82", "v83", "v84", "v85", "v86", "v87", "v88", "v89", "v90", "v91", "v92", "v93", "v94", "v95", "v96", "v97", "v98", "v99", "v100", "v101", "v102", "v103", "v104", "v105", "v106", "v107", "v108", "v109", "v110", "v111", "v112", "v113", "v114", "v115", "v116", "v117", "v118", "v119", "v120", "v121", "v122", "v123", "v124", "v125", "v126", "v127", "v128", "v129", "v130", "v131", "v132", "v133", "v134", "v135", "v136", "v137", "v138", "v139", "v140", "v141", "v142", "v143", "v144", "v145", "v146", "v147", "v148", "v149", "v150", "v151", "v152", "v153", "v154", "v155", "v156", "v157", "v158", "v159", "v160", "v161", "v162", "v163", "v184", "v185", "v186", "v187", "v188", "v189", "v190", "v191", "v192", "v193", "v194", "v195", "v196", "v197", "v198", "v199", "v200"
        if (L) {
            const rsrc_t ro = mkrsrc(P.out, ACT);
#pragma unroll 1
            for (int t = 0; t < NSEG; ++t) {
                const unsigned u = t * 2 + mt, pg = pgb(t, par), pn = pgb(t + 1, par);
                ASM_ST_DECL;
                asm volatile(ASM_W4F_1 : W4F_OUT ASM_ST2_OUT : W4F_IN, [pa] "v"(l15 * (DM * 2) + (l4 & 1) * 32 + (l4 >> 1) * 16), [pb] "v"(l15 * (DM * 2) + l4 * 8), [ro] "s"(ro), [so] "s"(u * 20480 + cu * 160) : W4F_CLOB);
            }
        } else {
            const rsrc_t rn = mkrsrc(P.ws + (par ^ 1) * 8192, RG_L);
#pragma unroll 1
            for (int t = 0; t < NSEG; ++t) {
                const unsigned u = t * 2 + mt, pg = pgb(t, par), pn = pgb(t + 1, par), sn = (pg ^ 8192) + PG_LX, st = RG_ST + (u >> 3) * BLK + (u & 7) * 1024;
                ASM_ST_DECL;
                asm volatile(ASM_W4F_0 : W4F_OUT ASM_ST_OUT ASM_ST2_OUT ASM_ST3_OUT
                             : W4F_IN, [pa] "v"(blin(c0 + (l4 & 1) * 512) + (l4 >> 1) * 256 + l15 * 16), [pb] "v"(blin(c0 + 1024 + (l4 & 1) * 512) + (l4 >> 1) * 256 + l15 * 16),
                               [so] "v"(ln < 16 ? ln * 8 : OOB), [si] "v"(ln * 8), [rr] "s"(rr), [sp] "s"(st + cu * 128), [sq] "s"(st), [xr] "s"(PG_XR + sn), [xn] "s"(PG_XN + sn),
                               [kd] "s"(1.0f / DM), [ke] "s"(1e-5f), [tn] "s"(t + 1), [fo] "v"(ln < 32 ? (ln >> 3) * 2048 + (ln & 7) * 16 : OOB), [rn] "s"(rn), [fs] "s"(RG_FX + (cu * 2 + mt) * 128) : W4F_CLOB);
            }
        }
        asm volatile("s_waitcnt vmcnt(0)" :: "{v[164:179]}"(ra), "{v[180:183]}"(rb) : "memory");
    }
}

__device__ __forceinline__ unsigned probe(char* ws, unsigned base, unsigned bit, int n) {
    const rsrc_t r = mkrsrc(ws, RG_SLOT);
    unsigned m0 = ~0u, m1 = ~0u;
    for (int i = 0; i < n; ++i) {
        const unsigned long long t0 = __builtin_amdgcn_s_memtime();
        __builtin_amdgcn_raw_buffer_store_b32(0u, r, 96 * BLK + base + (i & 1) * bit + ((i >> 2) & 1) * 4096 + (i >> 2) * 768, 0, 16);
        asm volatile("s_waitcnt vmcnt(0)" ::: "memory");
        const unsigned d = (unsigned)(__builtin_amdgcn_s_memtime() - t0);
        if (i & 1) m1 = d < m1 ? d : m1; else m0 = d < m0 ? d : m0;
    }
    return m0 > m1 ? 1u : 0u;
}
__device__ __forceinline__ void setup(const Params& P, int wave, int mt, int par0, int ph) {
    const bool xw = wave < 4 && (mt >= 2 * XS0) == (ph != 0);
    const int tid = threadIdx.x & 255, r15 = tid & 15, slot = tid >> 4;
    const rsrc_t rw = mkrsrc(P.ws, WS_END);
    u32x4 v[5];
    float s = 0.f, q = 0.f;
    if (xw) {
#pragma unroll
        for (int c = 0; c < 5; ++c) v[c] = *(const u32x4*)(P.x + (size_t)(mt * 16 + r15) * DM + (slot * 5 + c) * 8);
#pragma unroll
        for (int c = 0; c < 5; ++c)
#pragma unroll
            for (int e = 0; e < 4; ++e) s += lo2f(v[c][e]) + hi2f(v[c][e]);
        lds_st<float>((slot * 17 + r15) * 4, s);
    } else if (wave >= 4) {
        constexpr unsigned N0 = RG_ST / BLK, N1 = 4 * N0 + 16, N2 = PG_SS / BLK;
        const u32x4 s4 = sent4();
        for (unsigned k = mt; k < 2 * (N1 + 4 * N2); k += 128) {
            const unsigned i = k >> 1;
            const unsigned o = i < N1 ? (i < 4 * N0 ? (i / N0) * RG_SLOT + (i % N0) * BLK : (i - 3 * N0) * BLK) + (par0 ^ ph) * 8192 : PG0 + pgb((i - N1) / N2, par0 ^ ph) + ((i - N1) % N2) * BLK;
            st128(s4, rw, tid * 16, o + (k & 1) * 4096);
        }
        if (ph == 0 && mt >= 108) st128(~s4, rw, tid * 16, RG_FV + (mt - 108) * 4096);
    }
    bar();
    float mean = 0.f;
    if (xw) {
        s = 0.f;
#pragma unroll
        for (int k = 0; k < 16; ++k) s += lds_ld<float>((k * 17 + r15) * 4);
        mean = s * (1.0f / DM);
#pragma unroll
        for (int c = 0; c < 5; ++c)
#pragma unroll
            for (int e = 0; e < 4; ++e) { const float a = lo2f(v[c][e]) - mean, b = hi2f(v[c][e]) - mean; q += a * a + b * b; }
        lds_st<float>(2048 + (slot * 17 + r15) * 4, q);
    }
    bar();
    if (xw) {
        q = 0.f;
#pragma unroll
        for (int k = 0; k < 16; ++k) q += lds_ld<float>(2048 + (k * 17 + r15) * 4);
        const float m1 = rbf(mean), r = 1.0f / ln_den(q * (1.0f / DM));
#pragma unroll
        for (int c = 0; c < 5; ++c) {
            const unsigned o = toff(mt * 16 + r15, (slot * 5 + c) * 8, par0);
            st128(v[c], rw, o, PG0 + PG_XR);
            u32x4 n;
#pragma unroll
            for (int e = 0; e < 4; ++e) n[e] = pack2(rbf(lo2f(v[c][e]) - m1) * r, rbf(hi2f(v[c][e]) - m1) * r);
            st128(n, rw, o, PG0 + PG_XN);
        }
    }
    wait_vm<0>();
    bar();
    if (threadIdx.x < 8) __hip_atomic_fetch_add((unsigned*)(P.ws + FL0 + FL_IN + (ph * 8 + threadIdx.x) * 128), 1u, RLX_AGENT);
}

__global__ __launch_bounds__(512, 2) void mega_kernel(Params P) {
    const int wave = __builtin_amdgcn_readfirstlane(threadIdx.x >> 6);
    u32x4 cpf;
    {
        unsigned long long pc;
        unsigned len;
        asm volatile("s_getpc_b64 %0\n\ts_mov_b32 %1, _Z8code_endv-_Z11mega_kernel6Params" : "=s"(pc), "=s"(len));
        cpf = __builtin_amdgcn_raw_buffer_load_b128(mkrsrc((const void*)(pc & ~255ull), len), (blockIdx.x >> 3) * 8192 + threadIdx.x * 16, 0, 0);
    }
    {
        const rsrc_t rf = mkrsrc(P.ws + FL0, FL_BYTES);
        if (blockIdx.x == 0) {
            unsigned h = 0;
            if (wave == 7) h = (probe(P.ws, 0, 8192, CAL1) ^ ((unsigned)__builtin_amdgcn_s_getreg((3 << 11) | 20) >> 2)) & 1u;
            if (wave == 0) {
                const unsigned l16 = lane_id() * 16;
                u32x4 z = {0u, 0u, 0u, 0u};
                asm volatile("" : "+v"(z));
#pragma unroll
                for (int k = 0; k < 4; ++k) st128(z, rf, k * 1024 + l16 < FL_HB ? k * 1024 + l16 : OOB, 0);
                wait_vm<0>();
                for (unsigned c = 1; c < 200000u; ++c) {
                    __builtin_amdgcn_raw_buffer_store_b32(c, rf, l16 < 128 ? l16 * 8 : OOB, FL_HB, 16);
                    unsigned n = __builtin_amdgcn_raw_buffer_load_b32(rf, (l16 & 127u) * 8, FL_TK, 16), sum = 0;
                    n = n < 32u ? n : 32u;
#pragma unroll
                    for (int k = 0; k < 8; ++k) sum += __builtin_amdgcn_readlane(n, k);
                    if (sum >= 255u) break;
                }
            }
            bar();
            if (wave == 7 && lane_id() < 16) __hip_atomic_fetch_add((unsigned*)(P.ws + FL0 + FL_IN + (threadIdx.x & 15) * 128), (2u + h) << 16, RLX_AGENT);
        } else if (wave == 0) {
            unsigned o;
            asm volatile("v_mov_b32 %0, 0" : "=v"(o));
            const unsigned v0 = __builtin_amdgcn_readfirstlane(__builtin_amdgcn_raw_buffer_load_b32(rf, o, FL_HB + (blockIdx.x & 7) * 128, 16));
            for (int spins = 0; spins < 400000; ++spins) {
                asm volatile("" ::: "memory");
                if (__builtin_amdgcn_readfirstlane(__builtin_amdgcn_raw_buffer_load_b32(rf, o, FL_HB + (blockIdx.x & 7) * 128, 16)) != v0) break;
                __builtin_amdgcn_s_sleep(1);
            }
            asm volatile("" ::: "memory");
        }
    }
    if (threadIdx.x == 0) {
        unsigned g = (unsigned)__builtin_amdgcn_s_getreg((3 << 11) | 20) & 7u, k;
        for (int n = 0; (k = __hip_atomic_fetch_add((unsigned*)(P.ws + FL0 + FL_TK + g * 128), 1u, RLX_AGENT)) >= 32u && n < 8; ++n) g = (g + 1) & 7u;
        lds_st<unsigned>(0, g << 5 | (k & 31u));
    }
    bar();
    const unsigned tk = __builtin_amdgcn_readfirstlane(lds_ld<unsigned>(0));
    asm volatile("" :: "v"(cpf));
    bar();
    const int g4 = (tk >> 5) & 3, k = tk & 31;
    const int L = tk >> 7, x = g4, r = k;
    if (L) {
        const int par0 = (init_wait(P, wave, 1, 0x20000u) >> 16) & 1;
        if (tk == 254 && wave == 0) {
            const unsigned xc = (unsigned)__builtin_amdgcn_s_getreg((3 << 11) | 20);
            const unsigned f = (probe(P.ws, ((par0 ^ (xc >> 2)) & 1) * 8192, BLK, CAL2) ^ 1u ^ xc) & 1u;
            if (threadIdx.x < 16) __hip_atomic_fetch_add((unsigned*)(P.ws + FL0 + FL_IN + threadIdx.x * 128), (2u + f) << 20, RLX_AGENT);
        }
#pragma unroll 1
        for (int ph = 0; ph < 2; ++ph) {
            setup(P, wave, (tk & 31) * 4 + g4, par0, ph);
            for (int k = ph * DLY_L; k < DLY_L; ++k) __builtin_amdgcn_s_sleep(32);
        }
    }
    {
        const int i = (r - 26) * 4 + x;
        const int d = (r < 8 ? DLY_S : r < 21 ? 0 : i < 4 ? DLY_H : DLY_W + DLY_X * (i < 7 ? 0 : i < 10 ? 1 : i < 16 ? 2 : 3));
        for (int k = 0; k < d; ++k) __builtin_amdgcn_s_sleep(32);
    }
    if (r < 8) s_role(P, wave, L, x * 8 + r, x);
    else if (r < 16) v_role(P, wave ^ 4, L, x * 8 + r - 8, x);
    else if (r < 21) r_role(P, wave, L, r - 16, x);
    else {
        const int i = (r - 26) * 4 + x;
        if (i < 4) h_role(P, wave, L, r < 25 ? x * 4 + r - 21 : 16 + x * 2 + r - 25, x);
        else if (i < 7) w_role<1>(P, wave, L, i - 4, x);
        else if (i < 10) w_role<2>(P, wave, L, i - 7, x);
        else if (i < 16) w_role<3>(P, wave, L, i - 10, x);
        else w4_role(P, wave, L, i - 16, x);
    }
}

__global__ void code_end() {}

extern "C" void kernel_launch(void* const* d_in, const int* in_sizes, int n_in, void* d_out, int out_size, void* d_ws,
                              size_t ws_size, hipStream_t stream) {
    char* ws = (char*)d_ws;
    Params P;
    P.ws = ws; P.x = (const bf16_t*)d_in[0]; P.mem0 = (const bf16_t*)d_in[1]; P.out = (bf16_t*)d_out;
    for (int i = 0; i < 8; ++i) P.w[i] = (const bf16_t*)d_in[i < 4 ? 2 + i : 4 + i];
    (void)hipFuncSetAttribute((const void*)mega_kernel, hipFuncAttributeMaxDynamicSharedMemorySize, MEGA_LDS);
    mega_kernel<<<256, 512, MEGA_LDS, stream>>>(P);
}
```

```cpp
#define HM_L 0
#define HM_T 4
#define HM_C 0
#define HM_H 0
#define HM_B 30000u
#include <hip/hip_runtime.h>
#include <stdint.h>
#include <type_traits>

typedef unsigned short bf16_t;
using bf16x2 = __attribute__((ext_vector_type(2))) __bf16;
using bf16x8 = __attribute__((ext_vector_type(8))) __bf16;
using bf16x16 = __attribute__((ext_vector_type(16))) __bf16;
using f32x2 = __attribute__((ext_vector_type(2))) float;
using f32x4 = __attribute__((ext_vector_type(4))) float;
using u32x2 = __attribute__((ext_vector_type(2))) unsigned;
using u32x4 = __attribute__((ext_vector_type(4))) unsigned;
typedef __amdgpu_buffer_rsrc_t rsrc_t;

__device__ __forceinline__ unsigned pack2(float a, float b) {
    bf16x2 r = __builtin_convertvector((f32x2){a, b}, bf16x2);
    return __builtin_bit_cast(unsigned, r);
}
__device__ __forceinline__ float rbf(float f) { return __uint_as_float(pack2(f, 0.f) << 16); }
__device__ __forceinline__ float lo2f(unsigned u) { return __uint_as_float(u << 16); }
__device__ __forceinline__ float hi2f(unsigned u) { return __uint_as_float(u & 0xFFFF0000u); }
__device__ __forceinline__ rsrc_t mkrsrc(const void* p, unsigned bytes) {
    return __builtin_amdgcn_make_buffer_rsrc((void*)p, 0, bytes, 0x00020000);
}

constexpr int S_ = 2048, DM = 640, NH = 32, DH = 192, HD = NH * DH  , SEG = 32, NSEG = 64;
constexpr int FF = 1536;

#define XMAP
#define NCPY 4
constexpr unsigned NSLOT = 4, NCP = NCPY;
#define FL_TK_DEF
#define DLY_L 12
#define DLY_S 0
#define DLY_W 10
#define DLY_H 3
#define EWAKE "s_nop 0"
#define LWAKE "s_wakeup\n\t"
#define SLP4 (wave == 4 ? t : 0)
#define WPF 12
#define WPRI 1
#define WPRI2 0
#define RTD 3500
#define VTD 3900
#define HAH 100
#define WEAK 0
#define BAL 0
#define HSL 8
#define OAUX 2
#define W4P 3
#define STD 3800
#define CAL1 6
#define CAL2 12
#define DLY_X 2
#define NTM 0
#define CALN 12
#define RSL 9
#define VTRY 0
#define VSL 1
#define NRR 5
constexpr unsigned BLK = 16384, RG_SLOT = 4u << 20;
constexpr unsigned XP_PAIR = 6 * BLK, XP_SLOT = RG_SLOT;
constexpr unsigned XM_PAIR = 4 * BLK, XM_SLOT = RG_SLOT;
constexpr unsigned VM_SLOT = RG_SLOT;
constexpr unsigned PX_SLOT = RG_SLOT;
constexpr unsigned RG_XP = 0, RG_XM = 12 * BLK, RG_VM = 20 * BLK, RG_PX = 60 * BLK;
constexpr unsigned RG_ST = 68 * BLK, RG_FV = 84 * BLK, RG_FH = 86 * BLK, RG_FX = 88 * BLK, RG_L = NSLOT * RG_SLOT;
constexpr unsigned FL0 = 89 * BLK, FL_TK = 512, FL_IN = FL_TK + 8 * 128, FL_HB = FL_IN + 16 * 128, FL_BYTES = FL_HB + 8 * 128;
constexpr unsigned ACT = S_ * DM * 2;
#define PXROT 5
#define W4SL 5
#define RXDEL 1
#define VXD 32
#define VXAUX 16
constexpr unsigned PG0 = RG_L, PG_L = 80u << 21;
constexpr unsigned PG_XA = 0, PG_HS = 5 * BLK, PG_X1 = 10 * BLK, PG_H2 = 15 * BLK, PG_SS = 27 * BLK, PG_XR = 28 * BLK, PG_XN = 33 * BLK, PG_LX = 10 * BLK, PG_HV = 48 * BLK;
__device__ __forceinline__ unsigned pgb(unsigned p, int par) { return p << 21 | ((p ^ par) & 1) << 13; }
__device__ __forceinline__ constexpr unsigned blin(unsigned o) { return o + (o & ~8191u); }
constexpr int HT0 = 2;
constexpr int AH = 3;
constexpr unsigned WS_END = PG0 + PG_L;

#define SB() __builtin_amdgcn_sched_barrier(0)
#define LAS __attribute__((address_space(3)))
template <class T> __device__ __forceinline__ T lds_ld(unsigned a) { return *(const LAS T*)(a); }
template <class T> __device__ __forceinline__ void lds_st(unsigned a, T v) { *(LAS T*)(a) = v; }
constexpr unsigned OOB = 0x7FFFFF00u;
template <int N> __device__ __forceinline__ void wait_vm() { asm volatile("s_waitcnt vmcnt(%0)" :: "i"(N) : "memory"); }
__device__ __forceinline__ void bar() {
    asm volatile("s_waitcnt lgkmcnt(0)" ::: "memory");
    __builtin_amdgcn_s_barrier();
    asm volatile("" ::: "memory");
}
__device__ __forceinline__ int lane_id() {
    int ln;
    asm volatile("v_mbcnt_lo_u32_b32 %0, -1, 0\n\tv_mbcnt_hi_u32_b32 %0, -1, %0" : "=v"(ln));
    return ln;
}
__device__ __forceinline__ void swap16(unsigned& a, unsigned& b) { asm volatile("s_nop 3\n\tv_permlane16_swap_b32 %0, %1\n\ts_nop 1" : "+v"(a), "+v"(b)); }
__device__ __forceinline__ void swap32(unsigned& a, unsigned& b) { asm volatile("s_nop 3\n\tv_permlane32_swap_b32 %0, %1\n\ts_nop 1" : "+v"(a), "+v"(b)); }
__device__ __forceinline__ float sum_x16(float x) { unsigned a = __float_as_uint(x), b = a; swap16(a, b); return __uint_as_float(a) + __uint_as_float(b); }
__device__ __forceinline__ float sum_x32(float x) { unsigned a = __float_as_uint(x), b = a; swap32(a, b); return __uint_as_float(a) + __uint_as_float(b); }
__device__ __forceinline__ float max_x16(float x) { unsigned a = __float_as_uint(x), b = a; swap16(a, b); return __builtin_fmaxf(__uint_as_float(a), __uint_as_float(b)); }
__device__ __forceinline__ float max_x32(float x) { unsigned a = __float_as_uint(x), b = a; swap32(a, b); return __builtin_fmaxf(__uint_as_float(a), __uint_as_float(b)); }
__device__ __forceinline__ void sum2_rows(float& s, float& q) {
    unsigned a = __float_as_uint(s), b = a, c = __float_as_uint(q), d = c;
    asm volatile("s_nop 2\n\tv_permlane16_swap_b32 %0, %1\n\tv_permlane16_swap_b32 %2, %3\n\tv_add_f32 %0, %0, %1\n\tv_add_f32 %2, %2, %3\n\tv_mov_b32 %1, %0\n\tv_mov_b32 %3, %2\n\t"
                 "s_nop 1\n\tv_permlane32_swap_b32 %0, %1\n\tv_permlane32_swap_b32 %2, %3\n\tv_add_f32 %0, %0, %1\n\tv_add_f32 %2, %2, %3" : "+v"(a), "+v"(b), "+v"(c), "+v"(d));
    s = __uint_as_float(a); q = __uint_as_float(c);
}
__device__ __forceinline__ u32x4 merge16(u32x2 a, u32x2 b) {
    unsigned a0 = a[0], a1 = a[1], b0 = b[0], b1 = b[1];
    asm volatile("s_nop 3\n\tv_permlane16_swap_b32 %0, %2\n\tv_permlane16_swap_b32 %1, %3\n\ts_nop 1" : "+v"(a0), "+v"(a1), "+v"(b0), "+v"(b1));
    return (u32x4){a0, a1, b0, b1};
}
template <int AUXV>
__device__ __forceinline__ void st128_(u32x4 v, rsrc_t r, unsigned voff, unsigned soff) {
    __builtin_amdgcn_raw_buffer_store_b128(v, r, voff, soff, AUXV);
    asm volatile("s_nop 1" :: "v"(v) : "memory");
}
#define st128(V, R, VO_, SO_) st128_<16>(V, R, VO_, SO_)
#define LD128(R, VO_, SO_) __builtin_amdgcn_raw_buffer_load_b128(R, VO_, SO_, 16)
#define LD64(R, VO_, SO_) __builtin_amdgcn_raw_buffer_load_b64(R, VO_, SO_, 16)
#define BF8(X) __builtin_bit_cast(bf16x8, X)
__device__ __forceinline__ float gelu_tanh(float x) {
    const float c0 = -2.0f * 0.7978845608028654f * 1.4426950408889634f, c1 = c0 * 0.044715f;
    const float e = __builtin_amdgcn_exp2f(x * __builtin_fmaf(x * x, c1, c0));
    return x * __builtin_amdgcn_rcpf(1.0f + e);
}
#define MFMA(A_, B_, C_) C_ = __builtin_amdgcn_mfma_f32_16x16x32_bf16(A_, B_, C_, 0, 0, 0)
#define SMF(A_, B_, C_) C_ = __builtin_amdgcn_smfmac_f32_16x16x64_bf16(A_, B_, C_, sidx, 0, 0)
__device__ __forceinline__ float ln_den(float var) { return rbf(__builtin_amdgcn_sqrtf(rbf(rbf(var) + 1e-5f))); }
__device__ __forceinline__ float rcp(float x) { return __builtin_amdgcn_rcpf(x); }

struct Params {
    char* ws;
    const bf16_t* x;
    const bf16_t* mem0;
    const bf16_t* w[8];
    bf16_t* out;
};

#define RLX_AGENT __ATOMIC_RELAXED, __HIP_MEMORY_SCOPE_AGENT
#define SENT 0xFFFFFFFFu
constexpr int SPIN_CAP = 400000;
__device__ __forceinline__ bool spin_fail(int spins, const Params& P) {
    if ((spins & 1023) == 1023) {
        const rsrc_t rt = mkrsrc(P.ws + FL0, FL_BYTES);
        unsigned o;
        asm volatile("v_mov_b32 %0, 0x100" : "=v"(o));
        if (__builtin_amdgcn_readfirstlane(__builtin_amdgcn_raw_buffer_load_b32(rt, o, 0, 16)) != 0u) return true;
        if (spins > SPIN_CAP) { __builtin_amdgcn_raw_buffer_store_b32(o, rt, o, 0, 16); return true; }
    }
    return false;
}
__device__ __forceinline__ bool ok4(u32x4 v) { return v[0] != SENT && v[1] != SENT && v[2] != SENT && v[3] != SENT; }
__device__ __forceinline__ unsigned nosent(unsigned d) { return d < 0xFFFFFFFEu ? d : 0xFFFFFFFEu; }
__device__ __forceinline__ u32x4 nosent4(u32x4 v) { return (u32x4){nosent(v[0]), nosent(v[1]), nosent(v[2]), nosent(v[3])}; }
__device__ __forceinline__ unsigned umax3(unsigned a, unsigned b, unsigned c) { const unsigned m = a > b ? a : b; return m > c ? m : c; }
__device__ __forceinline__ unsigned umax4(unsigned m, u32x4 v) { return umax3(umax3(m, v[0], v[1]), v[2], v[3]); }
__device__ __forceinline__ u32x4 sent4() { unsigned sv; asm volatile("v_mov_b32 %0, -1" : "=v"(sv)); return (u32x4){sv, sv, sv, sv}; }
template <int N>
__device__ __forceinline__ void poll(rsrc_t rs, unsigned base, const unsigned (&off)[N], u32x4 (&v)[N], const Params& P) {
    for (int spins = 0;; ++spins) {
        unsigned m = 0;
#pragma unroll
        for (int k = 0; k < N; ++k) v[k] = LD128(rs, off[k], base);
#pragma unroll
        for (int k = 0; k < N; ++k) m = umax4(m, v[k]);
        if (__all(m != SENT)) return;
        asm volatile("" ::: "memory");
        if (spin_fail(spins, P)) return;
    }
}
__device__ __forceinline__ bf16x16 cat16(bf16x8 a, bf16x8 b) {
    return __builtin_shufflevector(a, b, 0, 1, 2, 3, 4, 5, 6, 7, 8, 9, 10, 11, 12, 13, 14, 15);
}
__device__ __forceinline__ bf16x8 half8(bf16x16 v, int h) {
    return h ? __builtin_shufflevector(v, v, 8, 9, 10, 11, 12, 13, 14, 15) : __builtin_shufflevector(v, v, 0, 1, 2, 3, 4, 5, 6, 7);
}
__device__ __forceinline__ bf16x16 lds_frag(unsigned a, unsigned step) { return cat16(lds_ld<bf16x8>(a), lds_ld<bf16x8>(a + step)); }
template <int I>
__device__ __forceinline__ void dmaq(const char* g, unsigned l16, unsigned dst) {
    asm volatile("s_mov_b32 m0, %2\n\ts_nop 0\n\tglobal_load_lds_dwordx4 %0, %1 offset:%3 sc1" :: "v"(l16), "s"(g), "s"(dst), "i"(I * 1024) : "memory");
}
__device__ __forceinline__ void dma4(const char* g, unsigned l16, unsigned dst) {
    asm volatile("s_mov_b32 m0, %2\n\ts_nop 0\n\tglobal_load_lds_dwordx4 %0, %1 sc1\n\tglobal_load_lds_dwordx4 %0, %1 offset:1024 sc1\n\t"
                 "global_load_lds_dwordx4 %0, %1 offset:2048 sc1\n\tglobal_load_lds_dwordx4 %0, %1 offset:3072 sc1" :: "v"(l16), "s"(g), "s"(dst) : "memory");
}
__device__ __forceinline__ unsigned toff(int row, int col, int par) {
    return pgb(row >> 5, par) + blin((unsigned)((row >> 4) & 1) * 20480u + (unsigned)(col >> 3) * 256u) + (unsigned)(row & 15) * 16u;
}

#define ASM_ST
#define ASM_ST_DECL
#define ASM_ST_OUT
#define ASM_ST_SAVE(k)
#define ASM_ST2
#define ASM_ST3
#define ASM_ST3_OUT
#define ASM_ST3_SAVE(k)
#define ASM_ST2_OUT
#define ASM_ST2_SAVE(k)
#define STDECL
#define ST(k)
#define STC(k)
#define STFLUSH(t)

#define MARK(c, k)
#define MARKX(c, k)
#define PMARK(c, st, seg)
#define LDECL
#define LA()
#define LB(t)
#define LF()
#define STROLE 3
constexpr unsigned L_MEM = 0;
constexpr unsigned L_Q = 10240;
constexpr unsigned L_KM = 13312;
constexpr unsigned L_QX = 16384;
constexpr unsigned L_KX = 28672;
constexpr unsigned L_XB = 40960;
constexpr unsigned L_XA = 49152;
constexpr unsigned L_W = 81920;
constexpr int MEGA_LDS = 163840;
constexpr int wi(int i, int j) { return (i >> 1) * 5 + ((i & 1) ? 2 + j : j - 1); }

#define RPUB2
#define WNT ""
#define PRIUP "s_setprio 2\n\t"
#define PRISM PRIUP
#define STR2(x) #x
#define STRX(x) STR2(x)
#define W4SLEEP "s_sleep " STRX(W4SL) "\n\t"
#define PLAY 0
#define V1A 0
#define ASM_WLOAD3 \
    "s_xor_b32 %[t], %[wx], 0\n\t" \
    "s_mul_i32 %[t], %[t], 0xc0000\n\t" \
    "buffer_load_dwordx4 v[200:203], %[o0], %[rs], %[t] offen" WNT "\n\t" \
    "buffer_load_dwordx4 v[0:3], %[o1], %[rs], %[t] offen" WNT "\n\t" \
    "buffer_load_dwordx4 v[8:11], %[o2], %[rs], %[t] offen" WNT "\n\t" \
    "s_add_u32 %[t], %[t], 0x18000\n\t" \
    "buffer_load_dwordx4 v[16:19], %[o0], %[rs], %[t] offen" WNT "\n\t" \
    "buffer_load_dwordx4 v[24:27], %[o1], %[rs], %[t] offen" WNT "\n\t" \
    "buffer_load_dwordx4 v[32:35], %[o2], %[rs], %[t] offen" WNT "\n\t" \
    "s_add_u32 %[t], %[t], 0x18000\n\t" \
    "buffer_load_dwordx4 v[208:211], %[o0], %[rs], %[t] offen" WNT "\n\t" \
    "buffer_load_dwordx4 v[40:43], %[o1], %[rs], %[t] offen" WNT "\n\t" \
    "buffer_load_dwordx4 v[48:51], %[o2], %[rs], %[t] offen" WNT "\n\t" \
    "s_add_u32 %[t], %[t], 0x18000\n\t" \
    "buffer_load_dwordx4 v[56:59], %[o0], %[rs], %[t] offen" WNT "\n\t" \
    "buffer_load_dwordx4 v[64:67], %[o1], %[rs], %[t] offen" WNT "\n\t" \
    "buffer_load_dwordx4 v[72:75], %[o2], %[rs], %[t] offen" WNT "\n\t" \
    "s_add_u32 %[t], %[t], 0x18000\n\t" \
    "buffer_load_dwordx4 v[204:207], %[o0], %[rs], %[t] offen" WNT "\n\t" \
    "buffer_load_dwordx4 v[4:7], %[o1], %[rs], %[t] offen" WNT "\n\t" \
    "buffer_load_dwordx4 v[12:15], %[o2], %[rs], %[t] offen" WNT "\n\t" \
    "s_add_u32 %[t], %[t], 0x18000\n\t" \
    "buffer_load_dwordx4 v[20:23], %[o0], %[rs], %[t] offen" WNT "\n\t" \
    "buffer_load_dwordx4 v[28:31], %[o1], %[rs], %[t] offen" WNT "\n\t" \
    "buffer_load_dwordx4 v[36:39], %[o2], %[rs], %[t] offen" WNT "\n\t" \
    "s_add_u32 %[t], %[t], 0x18000\n\t" \
    "buffer_load_dwordx4 v[212:215], %[o0], %[rs], %[t] offen" WNT "\n\t" \
    "buffer_load_dwordx4 v[44:47], %[o1], %[rs], %[t] offen" WNT "\n\t" \
    "buffer_load_dwordx4 v[52:55], %[o2], %[rs], %[t] offen" WNT "\n\t" \
    "s_add_u32 %[t], %[t], 0x18000\n\t" \
    "buffer_load_dwordx4 v[60:63], %[o0], %[rs], %[t] offen" WNT "\n\t" \
    "buffer_load_dwordx4 v[68:71], %[o1], %[rs], %[t] offen" WNT "\n\t" \
    "buffer_load_dwordx4 v[76:79], %[o2], %[rs], %[t] offen" WNT "\n\t" \
    "s_add_u32 %[t], %[t], 0x18000\n\t" \
    "s_waitcnt vmcnt(21)\n\t" \
    "ds_write_b128 %[s0], v[200:203] offset:0\n\t" \
    "ds_write_b128 %[s1], v[0:3] offset:0\n\t" \
    "ds_write_b128 %[s2], v[8:11] offset:0\n\t" \
    "s_waitcnt vmcnt(18)\n\t" \
    "ds_write_b128 %[s0], v[16:19] offset:3456\n\t" \
    "ds_write_b128 %[s1], v[24:27] offset:3456\n\t" \
    "ds_write_b128 %[s2], v[32:35] offset:3456\n\t" \
    "s_waitcnt vmcnt(15)\n\t" \
    "ds_write_b128 %[s0], v[208:211] offset:6912\n\t" \
    "ds_write_b128 %[s1], v[40:43] offset:6912\n\t" \
    "ds_write_b128 %[s2], v[48:51] offset:6912\n\t" \
    "s_waitcnt vmcnt(12)\n\t" \
    "ds_write_b128 %[s0], v[56:59] offset:10368\n\t" \
    "ds_write_b128 %[s1], v[64:67] offset:10368\n\t" \
    "ds_write_b128 %[s2], v[72:75] offset:10368\n\t" \
    "s_waitcnt lgkmcnt(0)\n\t" \
    "s_barrier \n\t" \
    "ds_read_b64_tr_b16 v[200:201], %[t0] offset:0\n\t" \
    "ds_read_b64_tr_b16 v[202:203], %[t0] offset:1664\n\t" \
    "ds_read_b64_tr_b16 v[0:1], %[t0] offset:128\n\t" \
    "ds_read_b64_tr_b16 v[2:3], %[t0] offset:1792\n\t" \
    "ds_read_b64_tr_b16 v[8:9], %[t0] offset:256\n\t" \
    "ds_read_b64_tr_b16 v[10:11], %[t0] offset:1920\n\t" \
    "ds_read_b64_tr_b16 v[16:17], %[t1] offset:0\n\t" \
    "ds_read_b64_tr_b16 v[18:19], %[t1] offset:1664\n\t" \
    "ds_read_b64_tr_b16 v[24:25], %[t1] offset:128\n\t" \
    "ds_read_b64_tr_b16 v[26:27], %[t1] offset:1792\n\t" \
    "ds_read_b64_tr_b16 v[32:33], %[t1] offset:256\n\t" \
    "ds_read_b64_tr_b16 v[34:35], %[t1] offset:1920\n\t" \
    "ds_read_b64_tr_b16 v[208:209], %[t2] offset:0\n\t" \
    "ds_read_b64_tr_b16 v[210:211], %[t2] offset:1664\n\t" \
    "ds_read_b64_tr_b16 v[40:41], %[t2] offset:128\n\t" \
    "ds_read_b64_tr_b16 v[42:43], %[t2] offset:1792\n\t" \
    "ds_read_b64_tr_b16 v[48:49], %[t2] offset:256\n\t" \
    "ds_read_b64_tr_b16 v[50:51], %[t2] offset:1920\n\t" \
    "ds_read_b64_tr_b16 v[56:57], %[t3] offset:0\n\t" \
    "ds_read_b64_tr_b16 v[58:59], %[t3] offset:1664\n\t" \
    "ds_read_b64_tr_b16 v[64:65], %[t3] offset:128\n\t" \
    "ds_read_b64_tr_b16 v[66:67], %[t3] offset:1792\n\t" \
    "ds_read_b64_tr_b16 v[72:73], %[t3] offset:256\n\t" \
    "ds_read_b64_tr_b16 v[74:75], %[t3] offset:1920\n\t" \
    "s_xor_b32 %[t], %[wx], 4\n\t" \
    "s_mul_i32 %[t], %[t], 0xc0000\n\t" \
    "buffer_load_dwordx4 v[216:219], %[o0], %[rs], %[t] offen" WNT "\n\t" \
    "buffer_load_dwordx4 v[80:83], %[o1], %[rs], %[t] offen" WNT "\n\t" \
    "buffer_load_dwordx4 v[88:91], %[o2], %[rs], %[t] offen" WNT "\n\t" \
    "s_add_u32 %[t], %[t], 0x18000\n\t" \
    "buffer_load_dwordx4 v[96:99], %[o0], %[rs], %[t] offen" WNT "\n\t" \
    "buffer_load_dwordx4 v[104:107], %[o1], %[rs], %[t] offen" WNT "\n\t" \
    "buffer_load_dwordx4 v[112:115], %[o2], %[rs], %[t] offen" WNT "\n\t" \
    "s_add_u32 %[t], %[t], 0x18000\n\t" \
    "buffer_load_dwordx4 v[224:227], %[o0], %[rs], %[t] offen" WNT "\n\t" \
    "buffer_load_dwordx4 v[120:123], %[o1], %[rs], %[t] offen" WNT "\n\t" \
    "buffer_load_dwordx4 v[128:131], %[o2], %[rs], %[t] offen" WNT "\n\t" \
    "s_add_u32 %[t], %[t], 0x18000\n\t" \
    "buffer_load_dwordx4 v[136:139], %[o0], %[rs], %[t] offen" WNT "\n\t" \
    "buffer_load_dwordx4 v[144:147], %[o1], %[rs], %[t] offen" WNT "\n\t" \
    "buffer_load_dwordx4 v[152:155], %[o2], %[rs], %[t] offen" WNT "\n\t" \
    "s_add_u32 %[t], %[t], 0x18000\n\t" \
    "s_waitcnt lgkmcnt(0)\n\t" \
    "s_barrier \n\t" \
    "s_waitcnt vmcnt(21)\n\t" \
    "ds_write_b128 %[s0], v[204:207] offset:0\n\t" \
    "ds_write_b128 %[s1], v[4:7] offset:0\n\t" \
    "ds_write_b128 %[s2], v[12:15] offset:0\n\t" \
    "s_waitcnt vmcnt(18)\n\t" \
    "ds_write_b128 %[s0], v[20:23] offset:3456\n\t" \
    "ds_write_b128 %[s1], v[28:31] offset:3456\n\t" \
    "ds_write_b128 %[s2], v[36:39] offset:3456\n\t" \
    "s_waitcnt vmcnt(15)\n\t" \
    "ds_write_b128 %[s0], v[212:215] offset:6912\n\t" \
    "ds_write_b128 %[s1], v[44:47] offset:6912\n\t" \
    "ds_write_b128 %[s2], v[52:55] offset:6912\n\t" \
    "s_waitcnt vmcnt(12)\n\t" \
    "ds_write_b128 %[s0], v[60:63] offset:10368\n\t" \
    "ds_write_b128 %[s1], v[68:71] offset:10368\n\t" \
    "ds_write_b128 %[s2], v[76:79] offset:10368\n\t" \
    "s_waitcnt lgkmcnt(0)\n\t" \
    "s_barrier \n\t" \
    "ds_read_b64_tr_b16 v[204:205], %[t0] offset:0\n\t" \
    "ds_read_b64_tr_b16 v[206:207], %[t0] offset:1664\n\t" \
    "ds_read_b64_tr_b16 v[4:5], %[t0] offset:128\n\t" \
    "ds_read_b64_tr_b16 v[6:7], %[t0] offset:1792\n\t" \
    "ds_read_b64_tr_b16 v[12:13], %[t0] offset:256\n\t" \
    "ds_read_b64_tr_b16 v[14:15], %[t0] offset:1920\n\t" \
    "ds_read_b64_tr_b16 v[20:21], %[t1] offset:0\n\t" \
    "ds_read_b64_tr_b16 v[22:23], %[t1] offset:1664\n\t" \
    "ds_read_b64_tr_b16 v[28:29], %[t1] offset:128\n\t" \
    "ds_read_b64_tr_b16 v[30:31], %[t1] offset:1792\n\t" \
    "ds_read_b64_tr_b16 v[36:37], %[t1] offset:256\n\t" \
    "ds_read_b64_tr_b16 v[38:39], %[t1] offset:1920\n\t" \
    "ds_read_b64_tr_b16 v[212:213], %[t2] offset:0\n\t" \
    "ds_read_b64_tr_b16 v[214:215], %[t2] offset:1664\n\t" \
    "ds_read_b64_tr_b16 v[44:45], %[t2] offset:128\n\t" \
    "ds_read_b64_tr_b16 v[46:47], %[t2] offset:1792\n\t" \
    "ds_read_b64_tr_b16 v[52:53], %[t2] offset:256\n\t" \
    "ds_read_b64_tr_b16 v[54:55], %[t2] offset:1920\n\t" \
    "ds_read_b64_tr_b16 v[60:61], %[t3] offset:0\n\t" \
    "ds_read_b64_tr_b16 v[62:63], %[t3] offset:1664\n\t" \
    "ds_read_b64_tr_b16 v[68:69], %[t3] offset:128\n\t" \
    "ds_read_b64_tr_b16 v[70:71], %[t3] offset:1792\n\t" \
    "ds_read_b64_tr_b16 v[76:77], %[t3] offset:256\n\t" \
    "ds_read_b64_tr_b16 v[78:79], %[t3] offset:1920\n\t" \
    "buffer_load_dwordx4 v[220:223], %[o0], %[rs], %[t] offen" WNT "\n\t" \
    "buffer_load_dwordx4 v[84:87], %[o1], %[rs], %[t] offen" WNT "\n\t" \
    "buffer_load_dwordx4 v[92:95], %[o2], %[rs], %[t] offen" WNT "\n\t" \
    "s_add_u32 %[t], %[t], 0x18000\n\t" \
    "buffer_load_dwordx4 v[100:103], %[o0], %[rs], %[t] offen" WNT "\n\t" \
    "buffer_load_dwordx4 v[108:111], %[o1], %[rs], %[t] offen" WNT "\n\t" \
    "buffer_load_dwordx4 v[116:119], %[o2], %[rs], %[t] offen" WNT "\n\t" \
    "s_add_u32 %[t], %[t], 0x18000\n\t" \
    "buffer_load_dwordx4 v[228:231], %[o0], %[rs], %[t] offen" WNT "\n\t" \
    "buffer_load_dwordx4 v[124:127], %[o1], %[rs], %[t] offen" WNT "\n\t" \
    "buffer_load_dwordx4 v[132:135], %[o2], %[rs], %[t] offen" WNT "\n\t" \
    "s_add_u32 %[t], %[t], 0x18000\n\t" \
    "buffer_load_dwordx4 v[140:143], %[o0], %[rs], %[t] offen" WNT "\n\t" \
    "buffer_load_dwordx4 v[148:151], %[o1], %[rs], %[t] offen" WNT "\n\t" \
    "buffer_load_dwordx4 v[156:159], %[o2], %[rs], %[t] offen" WNT "\n\t" \
    "s_add_u32 %[t], %[t], 0x18000\n\t" \
    "s_waitcnt lgkmcnt(0)\n\t" \
    "s_barrier \n\t" \
    "s_waitcnt vmcnt(21)\n\t" \
    "ds_write_b128 %[s0], v[216:219] offset:0\n\t" \
    "ds_write_b128 %[s1], v[80:83] offset:0\n\t" \
    "ds_write_b128 %[s2], v[88:91] offset:0\n\t" \
    "s_waitcnt vmcnt(18)\n\t" \
    "ds_write_b128 %[s0], v[96:99] offset:3456\n\t" \
    "ds_write_b128 %[s1], v[104:107] offset:3456\n\t" \
    "ds_write_b128 %[s2], v[112:115] offset:3456\n\t" \
    "s_waitcnt vmcnt(15)\n\t" \
    "ds_write_b128 %[s0], v[224:227] offset:6912\n\t" \
    "ds_write_b128 %[s1], v[120:123] offset:6912\n\t" \
    "ds_write_b128 %[s2], v[128:131] offset:6912\n\t" \
    "s_waitcnt vmcnt(12)\n\t" \
    "ds_write_b128 %[s0], v[136:139] offset:10368\n\t" \
    "ds_write_b128 %[s1], v[144:147] offset:10368\n\t" \
    "ds_write_b128 %[s2], v[152:155] offset:10368\n\t" \
    "s_waitcnt lgkmcnt(0)\n\t" \
    "s_barrier \n\t" \
    "ds_read_b64_tr_b16 v[216:217], %[t0] offset:0\n\t" \
    "ds_read_b64_tr_b16 v[218:219], %[t0] offset:1664\n\t" \
    "ds_read_b64_tr_b16 v[80:81], %[t0] offset:128\n\t" \
    "ds_read_b64_tr_b16 v[82:83], %[t0] offset:1792\n\t" \
    "ds_read_b64_tr_b16 v[88:89], %[t0] offset:256\n\t" \
    "ds_read_b64_tr_b16 v[90:91], %[t0] offset:1920\n\t" \
    "ds_read_b64_tr_b16 v[96:97], %[t1] offset:0\n\t" \
    "ds_read_b64_tr_b16 v[98:99], %[t1] offset:1664\n\t" \
    "ds_read_b64_tr_b16 v[104:105], %[t1] offset:128\n\t" \
    "ds_read_b64_tr_b16 v[106:107], %[t1] offset:1792\n\t" \
    "ds_read_b64_tr_b16 v[112:113], %[t1] offset:256\n\t" \
    "ds_read_b64_tr_b16 v[114:115], %[t1] offset:1920\n\t" \
    "ds_read_b64_tr_b16 v[224:225], %[t2] offset:0\n\t" \
    "ds_read_b64_tr_b16 v[226:227], %[t2] offset:1664\n\t" \
    "ds_read_b64_tr_b16 v[120:121], %[t2] offset:128\n\t" \
    "ds_read_b64_tr_b16 v[122:123], %[t2] offset:1792\n\t" \
    "ds_read_b64_tr_b16 v[128:129], %[t2] offset:256\n\t" \
    "ds_read_b64_tr_b16 v[130:131], %[t2] offset:1920\n\t" \
    "ds_read_b64_tr_b16 v[136:137], %[t3] offset:0\n\t" \
    "ds_read_b64_tr_b16 v[138:139], %[t3] offset:1664\n\t" \
    "ds_read_b64_tr_b16 v[144:145], %[t3] offset:128\n\t" \
    "ds_read_b64_tr_b16 v[146:147], %[t3] offset:1792\n\t" \
    "ds_read_b64_tr_b16 v[152:153], %[t3] offset:256\n\t" \
    "ds_read_b64_tr_b16 v[154:155], %[t3] offset:1920\n\t" \
    "s_and_b32 %[t], %[wx], 3\n\t" \
    "s_mul_i32 %[t], %[t], 0x60000\n\t" \
    "s_add_u32 %[t], %[t], 0x600000\n\t" \
    "buffer_load_dwordx4 v[232:235], %[o0], %[rs], %[t] offen" WNT "\n\t" \
    "buffer_load_dwordx4 v[160:163], %[o1], %[rs], %[t] offen" WNT "\n\t" \
    "buffer_load_dwordx4 v[168:171], %[o2], %[rs], %[t] offen" WNT "\n\t" \
    "s_add_u32 %[t], %[t], 0x18000\n\t" \
    "buffer_load_dwordx4 v[236:239], %[o0], %[rs], %[t] offen" WNT "\n\t" \
    "buffer_load_dwordx4 v[164:167], %[o1], %[rs], %[t] offen" WNT "\n\t" \
    "buffer_load_dwordx4 v[172:175], %[o2], %[rs], %[t] offen" WNT "\n\t" \
    "s_add_u32 %[t], %[t], 0x18000\n\t" \
    "buffer_load_dwordx4 v[176:179], %[o0], %[rs], %[t] offen" WNT "\n\t" \
    "buffer_load_dwordx4 v[184:187], %[o1], %[rs], %[t] offen" WNT "\n\t" \
    "buffer_load_dwordx4 v[192:195], %[o2], %[rs], %[t] offen" WNT "\n\t" \
    "s_add_u32 %[t], %[t], 0x18000\n\t" \
    "buffer_load_dwordx4 v[180:183], %[o0], %[rs], %[t] offen" WNT "\n\t" \
    "buffer_load_dwordx4 v[188:191], %[o1], %[rs], %[t] offen" WNT "\n\t" \
    "buffer_load_dwordx4 v[196:199], %[o2], %[rs], %[t] offen" WNT "\n\t" \
    "s_add_u32 %[t], %[t], 0x18000\n\t" \
    "s_waitcnt lgkmcnt(0)\n\t" \
    "s_barrier \n\t" \
    "s_waitcnt vmcnt(21)\n\t" \
    "ds_write_b128 %[s0], v[220:223] offset:0\n\t" \
    "ds_write_b128 %[s1], v[84:87] offset:0\n\t" \
    "ds_write_b128 %[s2], v[92:95] offset:0\n\t" \
    "s_waitcnt vmcnt(18)\n\t" \
    "ds_write_b128 %[s0], v[100:103] offset:3456\n\t" \
    "ds_write_b128 %[s1], v[108:111] offset:3456\n\t" \
    "ds_write_b128 %[s2], v[116:119] offset:3456\n\t" \
    "s_waitcnt vmcnt(15)\n\t" \
    "ds_write_b128 %[s0], v[228:231] offset:6912\n\t" \
    "ds_write_b128 %[s1], v[124:127] offset:6912\n\t" \
    "ds_write_b128 %[s2], v[132:135] offset:6912\n\t" \
    "s_waitcnt vmcnt(12)\n\t" \
    "ds_write_b128 %[s0], v[140:143] offset:10368\n\t" \
    "ds_write_b128 %[s1], v[148:151] offset:10368\n\t" \
    "ds_write_b128 %[s2], v[156:159] offset:10368\n\t" \
    "s_waitcnt lgkmcnt(0)\n\t" \
    "s_barrier \n\t" \
    "ds_read_b64_tr_b16 v[220:221], %[t0] offset:0\n\t" \
    "ds_read_b64_tr_b16 v[222:223], %[t0] offset:1664\n\t" \
    "ds_read_b64_tr_b16 v[84:85], %[t0] offset:128\n\t" \
    "ds_read_b64_tr_b16 v[86:87], %[t0] offset:1792\n\t" \
    "ds_read_b64_tr_b16 v[92:93], %[t0] offset:256\n\t" \
    "ds_read_b64_tr_b16 v[94:95], %[t0] offset:1920\n\t" \
    "ds_read_b64_tr_b16 v[100:101], %[t1] offset:0\n\t" \
    "ds_read_b64_tr_b16 v[102:103], %[t1] offset:1664\n\t" \
    "ds_read_b64_tr_b16 v[108:109], %[t1] offset:128\n\t" \
    "ds_read_b64_tr_b16 v[110:111], %[t1] offset:1792\n\t" \
    "ds_read_b64_tr_b16 v[116:117], %[t1] offset:256\n\t" \
    "ds_read_b64_tr_b16 v[118:119], %[t1] offset:1920\n\t" \
    "ds_read_b64_tr_b16 v[228:229], %[t2] offset:0\n\t" \
    "ds_read_b64_tr_b16 v[230:231], %[t2] offset:1664\n\t" \
    "ds_read_b64_tr_b16 v[124:125], %[t2] offset:128\n\t" \
    "ds_read_b64_tr_b16 v[126:127], %[t2] offset:1792\n\t" \
    "ds_read_b64_tr_b16 v[132:133], %[t2] offset:256\n\t" \
    "ds_read_b64_tr_b16 v[134:135], %[t2] offset:1920\n\t" \
    "ds_read_b64_tr_b16 v[140:141], %[t3] offset:0\n\t" \
    "ds_read_b64_tr_b16 v[142:143], %[t3] offset:1664\n\t" \
    "ds_read_b64_tr_b16 v[148:149], %[t3] offset:128\n\t" \
    "ds_read_b64_tr_b16 v[150:151], %[t3] offset:1792\n\t" \
    "ds_read_b64_tr_b16 v[156:157], %[t3] offset:256\n\t" \
    "ds_read_b64_tr_b16 v[158:159], %[t3] offset:1920\n\t" \
    "s_waitcnt lgkmcnt(0)\n\t" \
    "s_barrier \n\t" \
    "s_waitcnt vmcnt(9)\n\t" \
    "ds_write_b128 %[s0], v[232:235] offset:0\n\t" \
    "ds_write_b128 %[s1], v[160:163] offset:0\n\t" \
    "ds_write_b128 %[s2], v[168:171] offset:0\n\t" \
    "s_waitcnt vmcnt(6)\n\t" \
    "ds_write_b128 %[s0], v[236:239] offset:3456\n\t" \
    "ds_write_b128 %[s1], v[164:167] offset:3456\n\t" \
    "ds_write_b128 %[s2], v[172:175] offset:3456\n\t" \
    "s_waitcnt vmcnt(3)\n\t" \
    "ds_write_b128 %[s0], v[176:179] offset:6912\n\t" \
    "ds_write_b128 %[s1], v[184:187] offset:6912\n\t" \
    "ds_write_b128 %[s2], v[192:195] offset:6912\n\t" \
    "s_waitcnt vmcnt(0)\n\t" \
    "ds_write_b128 %[s0], v[180:183] offset:10368\n\t" \
    "ds_write_b128 %[s1], v[188:191] offset:10368\n\t" \
    "ds_write_b128 %[s2], v[196:199] offset:10368\n\t" \
    "s_waitcnt lgkmcnt(0)\n\t" \
    "s_barrier \n\t" \
    "ds_read_b64_tr_b16 v[232:233], %[t4] offset:0\n\t" \
    "ds_read_b64_tr_b16 v[234:235], %[t4] offset:1664\n\t" \
    "ds_read_b64_tr_b16 v[160:161], %[t4] offset:128\n\t" \
    "ds_read_b64_tr_b16 v[162:163], %[t4] offset:1792\n\t" \
    "ds_read_b64_tr_b16 v[168:169], %[t4] offset:256\n\t" \
    "ds_read_b64_tr_b16 v[170:171], %[t4] offset:1920\n\t" \
    "ds_read_b64_tr_b16 v[236:237], %[t4] offset:13824\n\t" \
    "ds_read_b64_tr_b16 v[238:239], %[t4] offset:15488\n\t" \
    "ds_read_b64_tr_b16 v[164:165], %[t4] offset:13952\n\t" \
    "ds_read_b64_tr_b16 v[166:167], %[t4] offset:15616\n\t" \
    "ds_read_b64_tr_b16 v[172:173], %[t4] offset:14080\n\t" \
    "ds_read_b64_tr_b16 v[174:175], %[t4] offset:15744\n\t" \
    "ds_read_b64_tr_b16 v[176:177], %[t4] offset:27648\n\t" \
    "ds_read_b64_tr_b16 v[178:179], %[t4] offset:29312\n\t" \
    "ds_read_b64_tr_b16 v[184:185], %[t4] offset:27776\n\t" \
    "ds_read_b64_tr_b16 v[186:187], %[t4] offset:29440\n\t" \
    "ds_read_b64_tr_b16 v[192:193], %[t4] offset:27904\n\t" \
    "ds_read_b64_tr_b16 v[194:195], %[t4] offset:29568\n\t" \
    "ds_read_b64_tr_b16 v[180:181], %[t4] offset:41472\n\t" \
    "ds_read_b64_tr_b16 v[182:183], %[t4] offset:43136\n\t" \
    "ds_read_b64_tr_b16 v[188:189], %[t4] offset:41600\n\t" \
    "ds_read_b64_tr_b16 v[190:191], %[t4] offset:43264\n\t" \
    "ds_read_b64_tr_b16 v[196:197], %[t4] offset:41728\n\t" \
    "ds_read_b64_tr_b16 v[198:199], %[t4] offset:43392\n\t" \
    "s_waitcnt lgkmcnt(0)\n\t" \
    "s_barrier \n\t" \
    "ds_write_b128 %[wl], v[200:203] offset:0\n\t" \
    "ds_write_b128 %[wl], v[204:207] offset:1024\n\t" \
    "ds_write_b128 %[wl], v[208:211] offset:2048\n\t" \
    "ds_write_b128 %[wl], v[212:215] offset:3072\n\t" \
    "ds_write_b128 %[wl], v[216:219] offset:4096\n\t" \
    "ds_write_b128 %[wl], v[220:223] offset:5120\n\t" \
    "ds_write_b128 %[wl], v[224:227] offset:6144\n\t" \
    "ds_write_b128 %[wl], v[228:231] offset:7168\n\t" \
    "ds_write_b128 %[wl], v[232:235] offset:8192\n\t" \
    "ds_write_b128 %[wl], v[236:239] offset:9216\n\t" \
    "s_waitcnt lgkmcnt(0)\n\t"
#define ASM_WLOADO \
    "s_mov_b32 %[t], 0x0\n\t" \
    "buffer_load_dwordx4 v[200:203], %[o0], %[rs], %[t] offen" WNT "\n\t" \
    "buffer_load_dwordx4 v[16:19], %[o1], %[rs], %[t] offen" WNT "\n\t" \
    "buffer_load_dwordx4 v[208:211], %[o2], %[rs], %[t] offen" WNT "\n\t" \
    "buffer_load_dwordx4 v[56:59], %[o3], %[rs], %[t] offen" WNT "\n\t" \
    "buffer_load_dwordx4 v[216:219], %[o4], %[rs], %[t] offen" WNT "\n\t" \
    "s_mov_b32 %[t], 0xa000\n\t" \
    "buffer_load_dwordx4 v[96:99], %[o0], %[rs], %[t] offen" WNT "\n\t" \
    "buffer_load_dwordx4 v[224:227], %[o1], %[rs], %[t] offen" WNT "\n\t" \
    "buffer_load_dwordx4 v[136:139], %[o2], %[rs], %[t] offen" WNT "\n\t" \
    "buffer_load_dwordx4 v[232:235], %[o3], %[rs], %[t] offen" WNT "\n\t" \
    "buffer_load_dwordx4 v[176:179], %[o4], %[rs], %[t] offen" WNT "\n\t" \
    "s_mov_b32 %[t], 0x14000\n\t" \
    "buffer_load_dwordx4 v[204:207], %[o0], %[rs], %[t] offen" WNT "\n\t" \
    "buffer_load_dwordx4 v[20:23], %[o1], %[rs], %[t] offen" WNT "\n\t" \
    "buffer_load_dwordx4 v[212:215], %[o2], %[rs], %[t] offen" WNT "\n\t" \
    "buffer_load_dwordx4 v[60:63], %[o3], %[rs], %[t] offen" WNT "\n\t" \
    "buffer_load_dwordx4 v[220:223], %[o4], %[rs], %[t] offen" WNT "\n\t" \
    "s_mov_b32 %[t], 0x1e000\n\t" \
    "buffer_load_dwordx4 v[100:103], %[o0], %[rs], %[t] offen" WNT "\n\t" \
    "buffer_load_dwordx4 v[228:231], %[o1], %[rs], %[t] offen" WNT "\n\t" \
    "buffer_load_dwordx4 v[140:143], %[o2], %[rs], %[t] offen" WNT "\n\t" \
    "buffer_load_dwordx4 v[236:239], %[o3], %[rs], %[t] offen" WNT "\n\t" \
    "buffer_load_dwordx4 v[180:183], %[o4], %[rs], %[t] offen" WNT "\n\t" \
    "s_waitcnt vmcnt(15)\n\t" \
    "ds_write_b128 %[s0], v[200:203] offset:0\n\t" \
    "ds_write_b128 %[s1], v[16:19] offset:0\n\t" \
    "ds_write_b128 %[s2], v[208:211] offset:0\n\t" \
    "ds_write_b128 %[s3], v[56:59] offset:0\n\t" \
    "ds_write_b128 %[s4], v[216:219] offset:0\n\t" \
    "s_waitcnt vmcnt(10)\n\t" \
    "ds_write_b128 %[s0], v[96:99] offset:5632\n\t" \
    "ds_write_b128 %[s1], v[224:227] offset:5632\n\t" \
    "ds_write_b128 %[s2], v[136:139] offset:5632\n\t" \
    "ds_write_b128 %[s3], v[232:235] offset:5632\n\t" \
    "ds_write_b128 %[s4], v[176:179] offset:5632\n\t" \
    "ds_read_b64_tr_b16 v[200:201], %[tr] offset:0\n\t" \
    "ds_read_b64_tr_b16 v[202:203], %[tr] offset:5632\n\t" \
    "ds_read_b64_tr_b16 v[16:17], %[tr] offset:32\n\t" \
    "ds_read_b64_tr_b16 v[18:19], %[tr] offset:5664\n\t" \
    "ds_read_b64_tr_b16 v[208:209], %[tr] offset:64\n\t" \
    "ds_read_b64_tr_b16 v[210:211], %[tr] offset:5696\n\t" \
    "ds_read_b64_tr_b16 v[56:57], %[tr] offset:96\n\t" \
    "ds_read_b64_tr_b16 v[58:59], %[tr] offset:5728\n\t" \
    "ds_read_b64_tr_b16 v[216:217], %[tr] offset:128\n\t" \
    "ds_read_b64_tr_b16 v[218:219], %[tr] offset:5760\n\t" \
    "ds_read_b64_tr_b16 v[96:97], %[tr] offset:160\n\t" \
    "ds_read_b64_tr_b16 v[98:99], %[tr] offset:5792\n\t" \
    "ds_read_b64_tr_b16 v[224:225], %[tr] offset:192\n\t" \
    "ds_read_b64_tr_b16 v[226:227], %[tr] offset:5824\n\t" \
    "ds_read_b64_tr_b16 v[136:137], %[tr] offset:224\n\t" \
    "ds_read_b64_tr_b16 v[138:139], %[tr] offset:5856\n\t" \
    "ds_read_b64_tr_b16 v[232:233], %[tr] offset:256\n\t" \
    "ds_read_b64_tr_b16 v[234:235], %[tr] offset:5888\n\t" \
    "ds_read_b64_tr_b16 v[176:177], %[tr] offset:288\n\t" \
    "ds_read_b64_tr_b16 v[178:179], %[tr] offset:5920\n\t" \
    "s_barrier \n\t" \
    "s_mov_b32 %[t], 0x28000\n\t" \
    "buffer_load_dwordx4 v[0:3], %[o0], %[rs], %[t] offen" WNT "\n\t" \
    "buffer_load_dwordx4 v[24:27], %[o1], %[rs], %[t] offen" WNT "\n\t" \
    "buffer_load_dwordx4 v[40:43], %[o2], %[rs], %[t] offen" WNT "\n\t" \
    "buffer_load_dwordx4 v[64:67], %[o3], %[rs], %[t] offen" WNT "\n\t" \
    "buffer_load_dwordx4 v[80:83], %[o4], %[rs], %[t] offen" WNT "\n\t" \
    "s_mov_b32 %[t], 0x32000\n\t" \
    "buffer_load_dwordx4 v[104:107], %[o0], %[rs], %[t] offen" WNT "\n\t" \
    "buffer_load_dwordx4 v[120:123], %[o1], %[rs], %[t] offen" WNT "\n\t" \
    "buffer_load_dwordx4 v[144:147], %[o2], %[rs], %[t] offen" WNT "\n\t" \
    "buffer_load_dwordx4 v[160:163], %[o3], %[rs], %[t] offen" WNT "\n\t" \
    "buffer_load_dwordx4 v[184:187], %[o4], %[rs], %[t] offen" WNT "\n\t" \
    "s_waitcnt lgkmcnt(0)\n\t" \
    "s_barrier \n\t" \
    "s_waitcnt vmcnt(15)\n\t" \
    "ds_write_b128 %[s0], v[204:207] offset:0\n\t" \
    "ds_write_b128 %[s1], v[20:23] offset:0\n\t" \
    "ds_write_b128 %[s2], v[212:215] offset:0\n\t" \
    "ds_write_b128 %[s3], v[60:63] offset:0\n\t" \
    "ds_write_b128 %[s4], v[220:223] offset:0\n\t" \
    "s_waitcnt vmcnt(10)\n\t" \
    "ds_write_b128 %[s0], v[100:103] offset:5632\n\t" \
    "ds_write_b128 %[s1], v[228:231] offset:5632\n\t" \
    "ds_write_b128 %[s2], v[140:143] offset:5632\n\t" \
    "ds_write_b128 %[s3], v[236:239] offset:5632\n\t" \
    "ds_write_b128 %[s4], v[180:183] offset:5632\n\t" \
    "ds_read_b64_tr_b16 v[204:205], %[tr] offset:0\n\t" \
    "ds_read_b64_tr_b16 v[206:207], %[tr] offset:5632\n\t" \
    "ds_read_b64_tr_b16 v[20:21], %[tr] offset:32\n\t" \
    "ds_read_b64_tr_b16 v[22:23], %[tr] offset:5664\n\t" \
    "ds_read_b64_tr_b16 v[212:213], %[tr] offset:64\n\t" \
    "ds_read_b64_tr_b16 v[214:215], %[tr] offset:5696\n\t" \
    "ds_read_b64_tr_b16 v[60:61], %[tr] offset:96\n\t" \
    "ds_read_b64_tr_b16 v[62:63], %[tr] offset:5728\n\t" \
    "ds_read_b64_tr_b16 v[220:221], %[tr] offset:128\n\t" \
    "ds_read_b64_tr_b16 v[222:223], %[tr] offset:5760\n\t" \
    "ds_read_b64_tr_b16 v[100:101], %[tr] offset:160\n\t" \
    "ds_read_b64_tr_b16 v[102:103], %[tr] offset:5792\n\t" \
    "ds_read_b64_tr_b16 v[228:229], %[tr] offset:192\n\t" \
    "ds_read_b64_tr_b16 v[230:231], %[tr] offset:5824\n\t" \
    "ds_read_b64_tr_b16 v[140:141], %[tr] offset:224\n\t" \
    "ds_read_b64_tr_b16 v[142:143], %[tr] offset:5856\n\t" \
    "ds_read_b64_tr_b16 v[236:237], %[tr] offset:256\n\t" \
    "ds_read_b64_tr_b16 v[238:239], %[tr] offset:5888\n\t" \
    "ds_read_b64_tr_b16 v[180:181], %[tr] offset:288\n\t" \
    "ds_read_b64_tr_b16 v[182:183], %[tr] offset:5920\n\t" \
    "s_barrier \n\t" \
    "s_mov_b32 %[t], 0x5000\n\t" \
    "buffer_load_dwordx4 v[4:7], %[o0], %[rs], %[t] offen" WNT "\n\t" \
    "buffer_load_dwordx4 v[28:31], %[o1], %[rs], %[t] offen" WNT "\n\t" \
    "buffer_load_dwordx4 v[44:47], %[o2], %[rs], %[t] offen" WNT "\n\t" \
    "buffer_load_dwordx4 v[68:71], %[o3], %[rs], %[t] offen" WNT "\n\t" \
    "buffer_load_dwordx4 v[84:87], %[o4], %[rs], %[t] offen" WNT "\n\t" \
    "s_mov_b32 %[t], 0xf000\n\t" \
    "buffer_load_dwordx4 v[108:111], %[o0], %[rs], %[t] offen" WNT "\n\t" \
    "buffer_load_dwordx4 v[124:127], %[o1], %[rs], %[t] offen" WNT "\n\t" \
    "buffer_load_dwordx4 v[148:151], %[o2], %[rs], %[t] offen" WNT "\n\t" \
    "buffer_load_dwordx4 v[164:167], %[o3], %[rs], %[t] offen" WNT "\n\t" \
    "buffer_load_dwordx4 v[188:191], %[o4], %[rs], %[t] offen" WNT "\n\t" \
    "s_waitcnt lgkmcnt(0)\n\t" \
    "s_barrier \n\t" \
    "s_waitcnt vmcnt(15)\n\t" \
    "ds_write_b128 %[s0], v[0:3] offset:0\n\t" \
    "ds_write_b128 %[s1], v[24:27] offset:0\n\t" \
    "ds_write_b128 %[s2], v[40:43] offset:0\n\t" \
    "ds_write_b128 %[s3], v[64:67] offset:0\n\t" \
    "ds_write_b128 %[s4], v[80:83] offset:0\n\t" \
    "s_waitcnt vmcnt(10)\n\t" \
    "ds_write_b128 %[s0], v[104:107] offset:5632\n\t" \
    "ds_write_b128 %[s1], v[120:123] offset:5632\n\t" \
    "ds_write_b128 %[s2], v[144:147] offset:5632\n\t" \
    "ds_write_b128 %[s3], v[160:163] offset:5632\n\t" \
    "ds_write_b128 %[s4], v[184:187] offset:5632\n\t" \
    "ds_read_b64_tr_b16 v[0:1], %[tr] offset:0\n\t" \
    "ds_read_b64_tr_b16 v[2:3], %[tr] offset:5632\n\t" \
    "ds_read_b64_tr_b16 v[24:25], %[tr] offset:32\n\t" \
    "ds_read_b64_tr_b16 v[26:27], %[tr] offset:5664\n\t" \
    "ds_read_b64_tr_b16 v[40:41], %[tr] offset:64\n\t" \
    "ds_read_b64_tr_b16 v[42:43], %[tr] offset:5696\n\t" \
    "ds_read_b64_tr_b16 v[64:65], %[tr] offset:96\n\t" \
    "ds_read_b64_tr_b16 v[66:67], %[tr] offset:5728\n\t" \
    "ds_read_b64_tr_b16 v[80:81], %[tr] offset:128\n\t" \
    "ds_read_b64_tr_b16 v[82:83], %[tr] offset:5760\n\t" \
    "ds_read_b64_tr_b16 v[104:105], %[tr] offset:160\n\t" \
    "ds_read_b64_tr_b16 v[106:107], %[tr] offset:5792\n\t" \
    "ds_read_b64_tr_b16 v[120:121], %[tr] offset:192\n\t" \
    "ds_read_b64_tr_b16 v[122:123], %[tr] offset:5824\n\t" \
    "ds_read_b64_tr_b16 v[144:145], %[tr] offset:224\n\t" \
    "ds_read_b64_tr_b16 v[146:147], %[tr] offset:5856\n\t" \
    "ds_read_b64_tr_b16 v[160:161], %[tr] offset:256\n\t" \
    "ds_read_b64_tr_b16 v[162:163], %[tr] offset:5888\n\t" \
    "ds_read_b64_tr_b16 v[184:185], %[tr] offset:288\n\t" \
    "ds_read_b64_tr_b16 v[186:187], %[tr] offset:5920\n\t" \
    "s_barrier \n\t" \
    "s_mov_b32 %[t], 0x19000\n\t" \
    "buffer_load_dwordx4 v[8:11], %[o0], %[rs], %[t] offen" WNT "\n\t" \
    "buffer_load_dwordx4 v[32:35], %[o1], %[rs], %[t] offen" WNT "\n\t" \
    "buffer_load_dwordx4 v[48:51], %[o2], %[rs], %[t] offen" WNT "\n\t" \
    "buffer_load_dwordx4 v[72:75], %[o3], %[rs], %[t] offen" WNT "\n\t" \
    "buffer_load_dwordx4 v[88:91], %[o4], %[rs], %[t] offen" WNT "\n\t" \
    "s_mov_b32 %[t], 0x23000\n\t" \
    "buffer_load_dwordx4 v[112:115], %[o0], %[rs], %[t] offen" WNT "\n\t" \
    "buffer_load_dwordx4 v[128:131], %[o1], %[rs], %[t] offen" WNT "\n\t" \
    "buffer_load_dwordx4 v[152:155], %[o2], %[rs], %[t] offen" WNT "\n\t" \
    "buffer_load_dwordx4 v[168:171], %[o3], %[rs], %[t] offen" WNT "\n\t" \
    "buffer_load_dwordx4 v[192:195], %[o4], %[rs], %[t] offen" WNT "\n\t" \
    "s_waitcnt lgkmcnt(0)\n\t" \
    "s_barrier \n\t" \
    "s_waitcnt vmcnt(15)\n\t" \
    "ds_write_b128 %[s0], v[4:7] offset:0\n\t" \
    "ds_write_b128 %[s1], v[28:31] offset:0\n\t" \
    "ds_write_b128 %[s2], v[44:47] offset:0\n\t" \
    "ds_write_b128 %[s3], v[68:71] offset:0\n\t" \
    "ds_write_b128 %[s4], v[84:87] offset:0\n\t" \
    "s_waitcnt vmcnt(10)\n\t" \
    "ds_write_b128 %[s0], v[108:111] offset:5632\n\t" \
    "ds_write_b128 %[s1], v[124:127] offset:5632\n\t" \
    "ds_write_b128 %[s2], v[148:151] offset:5632\n\t" \
    "ds_write_b128 %[s3], v[164:167] offset:5632\n\t" \
    "ds_write_b128 %[s4], v[188:191] offset:5632\n\t" \
    "ds_read_b64_tr_b16 v[4:5], %[tr] offset:0\n\t" \
    "ds_read_b64_tr_b16 v[6:7], %[tr] offset:5632\n\t" \
    "ds_read_b64_tr_b16 v[28:29], %[tr] offset:32\n\t" \
    "ds_read_b64_tr_b16 v[30:31], %[tr] offset:5664\n\t" \
    "ds_read_b64_tr_b16 v[44:45], %[tr] offset:64\n\t" \
    "ds_read_b64_tr_b16 v[46:47], %[tr] offset:5696\n\t" \
    "ds_read_b64_tr_b16 v[68:69], %[tr] offset:96\n\t" \
    "ds_read_b64_tr_b16 v[70:71], %[tr] offset:5728\n\t" \
    "ds_read_b64_tr_b16 v[84:85], %[tr] offset:128\n\t" \
    "ds_read_b64_tr_b16 v[86:87], %[tr] offset:5760\n\t" \
    "ds_read_b64_tr_b16 v[108:109], %[tr] offset:160\n\t" \
    "ds_read_b64_tr_b16 v[110:111], %[tr] offset:5792\n\t" \
    "ds_read_b64_tr_b16 v[124:125], %[tr] offset:192\n\t" \
    "ds_read_b64_tr_b16 v[126:127], %[tr] offset:5824\n\t" \
    "ds_read_b64_tr_b16 v[148:149], %[tr] offset:224\n\t" \
    "ds_read_b64_tr_b16 v[150:151], %[tr] offset:5856\n\t" \
    "ds_read_b64_tr_b16 v[164:165], %[tr] offset:256\n\t" \
    "ds_read_b64_tr_b16 v[166:167], %[tr] offset:5888\n\t" \
    "ds_read_b64_tr_b16 v[188:189], %[tr] offset:288\n\t" \
    "ds_read_b64_tr_b16 v[190:191], %[tr] offset:5920\n\t" \
    "s_barrier \n\t" \
    "s_mov_b32 %[t], 0x2d000\n\t" \
    "buffer_load_dwordx4 v[12:15], %[o0], %[rs], %[t] offen" WNT "\n\t" \
    "buffer_load_dwordx4 v[36:39], %[o1], %[rs], %[t] offen" WNT "\n\t" \
    "buffer_load_dwordx4 v[52:55], %[o2], %[rs], %[t] offen" WNT "\n\t" \
    "buffer_load_dwordx4 v[76:79], %[o3], %[rs], %[t] offen" WNT "\n\t" \
    "buffer_load_dwordx4 v[92:95], %[o4], %[rs], %[t] offen" WNT "\n\t" \
    "s_mov_b32 %[t], 0x37000\n\t" \
    "buffer_load_dwordx4 v[116:119], %[o0], %[rs], %[t] offen" WNT "\n\t" \
    "buffer_load_dwordx4 v[132:135], %[o1], %[rs], %[t] offen" WNT "\n\t" \
    "buffer_load_dwordx4 v[156:159], %[o2], %[rs], %[t] offen" WNT "\n\t" \
    "buffer_load_dwordx4 v[172:175], %[o3], %[rs], %[t] offen" WNT "\n\t" \
    "buffer_load_dwordx4 v[196:199], %[o4], %[rs], %[t] offen" WNT "\n\t" \
    "s_waitcnt lgkmcnt(0)\n\t" \
    "s_barrier \n\t" \
    "s_waitcnt vmcnt(15)\n\t" \
    "ds_write_b128 %[s0], v[8:11] offset:0\n\t" \
    "ds_write_b128 %[s1], v[32:35] offset:0\n\t" \
    "ds_write_b128 %[s2], v[48:51] offset:0\n\t" \
    "ds_write_b128 %[s3], v[72:75] offset:0\n\t" \
    "ds_write_b128 %[s4], v[88:91] offset:0\n\t" \
    "s_waitcnt vmcnt(10)\n\t" \
    "ds_write_b128 %[s0], v[112:115] offset:5632\n\t" \
    "ds_write_b128 %[s1], v[128:131] offset:5632\n\t" \
    "ds_write_b128 %[s2], v[152:155] offset:5632\n\t" \
    "ds_write_b128 %[s3], v[168:171] offset:5632\n\t" \
    "ds_write_b128 %[s4], v[192:195] offset:5632\n\t" \
    "ds_read_b64_tr_b16 v[8:9], %[tr] offset:0\n\t" \
    "ds_read_b64_tr_b16 v[10:11], %[tr] offset:5632\n\t" \
    "ds_read_b64_tr_b16 v[32:33], %[tr] offset:32\n\t" \
    "ds_read_b64_tr_b16 v[34:35], %[tr] offset:5664\n\t" \
    "ds_read_b64_tr_b16 v[48:49], %[tr] offset:64\n\t" \
    "ds_read_b64_tr_b16 v[50:51], %[tr] offset:5696\n\t" \
    "ds_read_b64_tr_b16 v[72:73], %[tr] offset:96\n\t" \
    "ds_read_b64_tr_b16 v[74:75], %[tr] offset:5728\n\t" \
    "ds_read_b64_tr_b16 v[88:89], %[tr] offset:128\n\t" \
    "ds_read_b64_tr_b16 v[90:91], %[tr] offset:5760\n\t" \
    "ds_read_b64_tr_b16 v[112:113], %[tr] offset:160\n\t" \
    "ds_read_b64_tr_b16 v[114:115], %[tr] offset:5792\n\t" \
    "ds_read_b64_tr_b16 v[128:129], %[tr] offset:192\n\t" \
    "ds_read_b64_tr_b16 v[130:131], %[tr] offset:5824\n\t" \
    "ds_read_b64_tr_b16 v[152:153], %[tr] offset:224\n\t" \
    "ds_read_b64_tr_b16 v[154:155], %[tr] offset:5856\n\t" \
    "ds_read_b64_tr_b16 v[168:169], %[tr] offset:256\n\t" \
    "ds_read_b64_tr_b16 v[170:171], %[tr] offset:5888\n\t" \
    "ds_read_b64_tr_b16 v[192:193], %[tr] offset:288\n\t" \
    "ds_read_b64_tr_b16 v[194:195], %[tr] offset:5920\n\t" \
    "s_waitcnt lgkmcnt(0)\n\t" \
    "s_barrier \n\t" \
    "s_waitcnt vmcnt(5)\n\t" \
    "ds_write_b128 %[s0], v[12:15] offset:0\n\t" \
    "ds_write_b128 %[s1], v[36:39] offset:0\n\t" \
    "ds_write_b128 %[s2], v[52:55] offset:0\n\t" \
    "ds_write_b128 %[s3], v[76:79] offset:0\n\t" \
    "ds_write_b128 %[s4], v[92:95] offset:0\n\t" \
    "s_waitcnt vmcnt(0)\n\t" \
    "ds_write_b128 %[s0], v[116:119] offset:5632\n\t" \
    "ds_write_b128 %[s1], v[132:135] offset:5632\n\t" \
    "ds_write_b128 %[s2], v[156:159] offset:5632\n\t" \
    "ds_write_b128 %[s3], v[172:175] offset:5632\n\t" \
    "ds_write_b128 %[s4], v[196:199] offset:5632\n\t" \
    "ds_read_b64_tr_b16 v[12:13], %[tr] offset:0\n\t" \
    "ds_read_b64_tr_b16 v[14:15], %[tr] offset:5632\n\t" \
    "ds_read_b64_tr_b16 v[36:37], %[tr] offset:32\n\t" \
    "ds_read_b64_tr_b16 v[38:39], %[tr] offset:5664\n\t" \
    "ds_read_b64_tr_b16 v[52:53], %[tr] offset:64\n\t" \
    "ds_read_b64_tr_b16 v[54:55], %[tr] offset:5696\n\t" \
    "ds_read_b64_tr_b16 v[76:77], %[tr] offset:96\n\t" \
    "ds_read_b64_tr_b16 v[78:79], %[tr] offset:5728\n\t" \
    "ds_read_b64_tr_b16 v[92:93], %[tr] offset:128\n\t" \
    "ds_read_b64_tr_b16 v[94:95], %[tr] offset:5760\n\t" \
    "ds_read_b64_tr_b16 v[116:117], %[tr] offset:160\n\t" \
    "ds_read_b64_tr_b16 v[118:119], %[tr] offset:5792\n\t" \
    "ds_read_b64_tr_b16 v[132:133], %[tr] offset:192\n\t" \
    "ds_read_b64_tr_b16 v[134:135], %[tr] offset:5824\n\t" \
    "ds_read_b64_tr_b16 v[156:157], %[tr] offset:224\n\t" \
    "ds_read_b64_tr_b16 v[158:159], %[tr] offset:5856\n\t" \
    "ds_read_b64_tr_b16 v[172:173], %[tr] offset:256\n\t" \
    "ds_read_b64_tr_b16 v[174:175], %[tr] offset:5888\n\t" \
    "ds_read_b64_tr_b16 v[196:197], %[tr] offset:288\n\t" \
    "ds_read_b64_tr_b16 v[198:199], %[tr] offset:5920\n\t" \
    "s_waitcnt lgkmcnt(0)\n\t" \
    "s_barrier \n\t" \
    "ds_write_b128 %[wl], v[200:203] offset:0\n\t" \
    "ds_write_b128 %[wl], v[204:207] offset:1024\n\t" \
    "ds_write_b128 %[wl], v[208:211] offset:2048\n\t" \
    "ds_write_b128 %[wl], v[212:215] offset:3072\n\t" \
    "ds_write_b128 %[wl], v[216:219] offset:4096\n\t" \
    "ds_write_b128 %[wl], v[220:223] offset:5120\n\t" \
    "ds_write_b128 %[wl], v[224:227] offset:6144\n\t" \
    "ds_write_b128 %[wl], v[228:231] offset:7168\n\t" \
    "ds_write_b128 %[wl], v[232:235] offset:8192\n\t" \
    "ds_write_b128 %[wl], v[236:239] offset:9216\n\t" \
    "s_waitcnt lgkmcnt(0)\n\t"
#define ASM_XG_S_LO \
    "s_waitcnt lgkmcnt(0)\n\t" \
    "ds_read_b128 v[224:227], %[xa] offset:16384\n\t" \
    "ds_read_b128 v[228:231], %[xa] offset:17408\n\t" \
    "ds_read_b128 v[244:247], %[wl] offset:0\n\t" \
    "ds_read_b128 v[248:251], %[wl] offset:1024\n\t" \
    "ds_read_b128 v[232:235], %[xa] offset:0\n\t" \
    "ds_read_b128 v[236:239], %[xa] offset:1024\n\t" \
    "v_xor_b32 v252, 0x800, %[xa]\n\t" \
    "ds_read_b128 v[240:243], v252 offset:0\n\t" \
    "s_waitcnt lgkmcnt(6)\n\t" \
    "v_mfma_f32_16x16x32_bf16 v[220:223], v[8:11], v[224:227], 0\n\t" \
    "s_waitcnt lgkmcnt(5)\n\t" \
    "v_mfma_f32_16x16x32_bf16 v[220:223], v[12:15], v[228:231], v[220:223]\n\t" \
    "v_mfma_f32_16x16x32_bf16 v[216:219], v[0:3], v[224:227], 0\n\t" \
    "v_mfma_f32_16x16x32_bf16 v[216:219], v[4:7], v[228:231], v[216:219]\n\t" \
    "s_waitcnt lgkmcnt(4)\n\t" \
    "v_mfma_f32_16x16x32_bf16 v[212:215], v[244:247], v[224:227], 0\n\t" \
    "ds_read_b128 v[224:227], v252 offset:1024\n\t" \
    "s_waitcnt lgkmcnt(4)\n\t" \
    "v_mfma_f32_16x16x32_bf16 v[212:215], v[248:251], v[228:231], v[212:215]\n\t" \
    "ds_read_b128 v[228:231], v252 offset:16384\n\t" \
    "s_waitcnt lgkmcnt(4)\n\t" \
    "v_mfma_f32_16x16x32_bf16 v[200:203], v[244:247], v[232:235], 0\n\t" \
    "ds_read_b128 v[244:247], %[wl] offset:2048\n\t" \
    "s_waitcnt lgkmcnt(4)\n\t" \
    "v_mfma_f32_16x16x32_bf16 v[200:203], v[248:251], v[236:239], v[200:203]\n\t" \
    "ds_read_b128 v[248:251], %[wl] offset:3072\n\t" \
    "v_mfma_f32_16x16x32_bf16 v[204:207], v[0:3], v[232:235], 0\n\t" \
    "v_mfma_f32_16x16x32_bf16 v[204:207], v[4:7], v[236:239], v[204:207]\n\t" \
    "v_mfma_f32_16x16x32_bf16 v[208:211], v[8:11], v[232:235], 0\n\t" \
    "ds_read_b128 v[232:235], v252 offset:17408\n\t" \
    "v_mfma_f32_16x16x32_bf16 v[208:211], v[12:15], v[236:239], v[208:211]\n\t" \
    "v_xor_b32 v252, 0x1000, %[xa]\n\t" \
    "ds_read_b128 v[236:239], v252 offset:16384\n\t" \
    "s_waitcnt lgkmcnt(6)\n\t" \
    "v_mfma_f32_16x16x32_bf16 v[208:211], v[32:35], v[240:243], v[208:211]\n\t" \
    "s_waitcnt lgkmcnt(5)\n\t" \
    "v_mfma_f32_16x16x32_bf16 v[208:211], v[36:39], v[224:227], v[208:211]\n\t" \
    "v_mfma_f32_16x16x32_bf16 v[204:207], v[24:27], v[240:243], v[204:207]\n\t" \
    "v_mfma_f32_16x16x32_bf16 v[204:207], v[28:31], v[224:227], v[204:207]\n\t" \
    "v_mfma_f32_16x16x32_bf16 v[200:203], v[16:19], v[240:243], v[200:203]\n\t" \
    "ds_read_b128 v[240:243], v252 offset:17408\n\t" \
    "v_mfma_f32_16x16x32_bf16 v[200:203], v[20:23], v[224:227], v[200:203]\n\t" \
    "ds_read_b128 v[224:227], v252 offset:0\n\t" \
    "s_waitcnt lgkmcnt(6)\n\t" \
    "v_mfma_f32_16x16x32_bf16 v[212:215], v[16:19], v[228:231], v[212:215]\n\t" \
    "s_waitcnt lgkmcnt(3)\n\t" \
    "v_mfma_f32_16x16x32_bf16 v[212:215], v[20:23], v[232:235], v[212:215]\n\t" \
    "v_mfma_f32_16x16x32_bf16 v[216:219], v[24:27], v[228:231], v[216:219]\n\t" \
    "v_mfma_f32_16x16x32_bf16 v[216:219], v[28:31], v[232:235], v[216:219]\n\t" \
    "v_mfma_f32_16x16x32_bf16 v[220:223], v[32:35], v[228:231], v[220:223]\n\t" \
    "ds_read_b128 v[228:231], v252 offset:1024\n\t" \
    "v_mfma_f32_16x16x32_bf16 v[220:223], v[36:39], v[232:235], v[220:223]\n\t" \
    "v_xor_b32 v252, 0x1800, %[xa]\n\t" \
    "ds_read_b128 v[232:235], v252 offset:0\n\t" \
    "s_waitcnt lgkmcnt(4)\n\t" \
    "v_mfma_f32_16x16x32_bf16 v[220:223], v[48:51], v[236:239], v[220:223]\n\t" \
    "s_waitcnt lgkmcnt(3)\n\t" \
    "v_mfma_f32_16x16x32_bf16 v[220:223], v[52:55], v[240:243], v[220:223]\n\t" \
    "v_mfma_f32_16x16x32_bf16 v[216:219], v[40:43], v[236:239], v[216:219]\n\t" \
    "v_mfma_f32_16x16x32_bf16 v[216:219], v[44:47], v[240:243], v[216:219]\n\t" \
    "v_mfma_f32_16x16x32_bf16 v[212:215], v[244:247], v[236:239], v[212:215]\n\t" \
    "ds_read_b128 v[236:239], v252 offset:1024\n\t" \
    "v_mfma_f32_16x16x32_bf16 v[212:215], v[248:251], v[240:243], v[212:215]\n\t" \
    "ds_read_b128 v[240:243], v252 offset:16384\n\t" \
    "s_waitcnt lgkmcnt(4)\n\t" \
    "v_mfma_f32_16x16x32_bf16 v[200:203], v[244:247], v[224:227], v[200:203]\n\t" \
    "ds_read_b128 v[244:247], %[wl] offset:8192\n\t" \
    "s_waitcnt lgkmcnt(4)\n\t" \
    "v_mfma_f32_16x16x32_bf16 v[200:203], v[248:251], v[228:231], v[200:203]\n\t" \
    "ds_read_b128 v[248:251], %[wl] offset:9216\n\t" \
    "v_mfma_f32_16x16x32_bf16 v[204:207], v[40:43], v[224:227], v[204:207]\n\t" \
    "v_mfma_f32_16x16x32_bf16 v[204:207], v[44:47], v[228:231], v[204:207]\n\t" \
    "v_mfma_f32_16x16x32_bf16 v[208:211], v[48:51], v[224:227], v[208:211]\n\t" \
    "ds_read_b128 v[224:227], v252 offset:17408\n\t" \
    "v_mfma_f32_16x16x32_bf16 v[208:211], v[52:55], v[228:231], v[208:211]\n\t" \
    "v_and_b32 v252, 0x3ff, %[xa]\n\t" \
    "ds_read_b128 v[228:231], v252 offset:45056\n\t" \
    "s_waitcnt lgkmcnt(6)\n\t" \
    "v_mfma_f32_16x16x32_bf16 v[208:211], v[72:75], v[232:235], v[208:211]\n\t" \
    "s_waitcnt lgkmcnt(5)\n\t" \
    "v_mfma_f32_16x16x32_bf16 v[208:211], v[76:79], v[236:239], v[208:211]\n\t" \
    "v_mfma_f32_16x16x32_bf16 v[204:207], v[64:67], v[232:235], v[204:207]\n\t" \
    "v_mfma_f32_16x16x32_bf16 v[204:207], v[68:71], v[236:239], v[204:207]\n\t" \
    "v_mfma_f32_16x16x32_bf16 v[200:203], v[56:59], v[232:235], v[200:203]\n\t" \
    "ds_read_b128 v[232:235], v252 offset:46080\n\t" \
    "v_mfma_f32_16x16x32_bf16 v[200:203], v[60:63], v[236:239], v[200:203]\n\t" \
    "ds_read_b128 v[236:239], v252 offset:40960\n\t" \
    "s_waitcnt lgkmcnt(6)\n\t" \
    "v_mfma_f32_16x16x32_bf16 v[212:215], v[56:59], v[240:243], v[212:215]\n\t" \
    "s_waitcnt lgkmcnt(3)\n\t" \
    "v_mfma_f32_16x16x32_bf16 v[212:215], v[60:63], v[224:227], v[212:215]\n\t" \
    "v_mfma_f32_16x16x32_bf16 v[216:219], v[64:67], v[240:243], v[216:219]\n\t" \
    "v_mfma_f32_16x16x32_bf16 v[216:219], v[68:71], v[224:227], v[216:219]\n\t" \
    "v_mfma_f32_16x16x32_bf16 v[220:223], v[72:75], v[240:243], v[220:223]\n\t" \
    "ds_read_b128 v[240:243], v252 offset:41984\n\t" \
    "v_mfma_f32_16x16x32_bf16 v[220:223], v[76:79], v[224:227], v[220:223]\n\t" \
    "s_waitcnt lgkmcnt(0)\n\t" \
    "s_barrier\n\t" ASM_ST \
    "v_xor_b32 v252, 0x2000, %[xa]\n\t" \
    "ds_read_b128 v[224:227], v252 offset:0\n\t" \
    "v_mfma_f32_16x16x32_bf16 v[220:223], v[168:171], v[228:231], v[220:223]\n\t" \
    "v_mfma_f32_16x16x32_bf16 v[220:223], v[172:175], v[232:235], v[220:223]\n\t" \
    "v_mfma_f32_16x16x32_bf16 v[216:219], v[160:163], v[228:231], v[216:219]\n\t" \
    "v_mfma_f32_16x16x32_bf16 v[216:219], v[164:167], v[232:235], v[216:219]\n\t" \
    "v_mfma_f32_16x16x32_bf16 v[212:215], v[244:247], v[228:231], v[212:215]\n\t" \
    "ds_read_b128 v[228:231], v252 offset:1024\n\t" \
    "v_mfma_f32_16x16x32_bf16 v[212:215], v[248:251], v[232:235], v[212:215]\n\t" \
    "ds_read_b128 v[232:235], v252 offset:16384\n\t" \
    "v_mfma_f32_16x16x32_bf16 v[200:203], v[244:247], v[236:239], v[200:203]\n\t" \
    "ds_read_b128 v[244:247], %[wl] offset:4096\n\t" \
    "v_mfma_f32_16x16x32_bf16 v[200:203], v[248:251], v[240:243], v[200:203]\n\t" \
    "ds_read_b128 v[248:251], %[wl] offset:5120\n\t" \
    "v_mfma_f32_16x16x32_bf16 v[204:207], v[160:163], v[236:239], v[204:207]\n\t" \
    "v_mfma_f32_16x16x32_bf16 v[204:207], v[164:167], v[240:243], v[204:207]\n\t" \
    "v_mfma_f32_16x16x32_bf16 v[208:211], v[168:171], v[236:239], v[208:211]\n\t" \
    "ds_read_b128 v[236:239], v252 offset:17408\n\t" \
    "v_mfma_f32_16x16x32_bf16 v[208:211], v[172:175], v[240:243], v[208:211]\n\t" \
    "v_xor_b32 v252, 0x2800, %[xa]\n\t" \
    "ds_read_b128 v[240:243], v252 offset:16384\n\t" \
    "s_waitcnt lgkmcnt(6)\n\t" \
    "v_mfma_f32_16x16x32_bf16 v[208:211], v[88:91], v[224:227], v[208:211]\n\t" \
    "s_waitcnt lgkmcnt(5)\n\t" \
    "v_mfma_f32_16x16x32_bf16 v[208:211], v[92:95], v[228:231], v[208:211]\n\t" \
    "v_mfma_f32_16x16x32_bf16 v[204:207], v[80:83], v[224:227], v[204:207]\n\t" \
    "v_mfma_f32_16x16x32_bf16 v[204:207], v[84:87], v[228:231], v[204:207]\n\t" \
    "s_waitcnt lgkmcnt(3)\n\t" \
    "v_mfma_f32_16x16x32_bf16 v[200:203], v[244:247], v[224:227], v[200:203]\n\t" \
    "ds_read_b128 v[224:227], v252 offset:17408\n\t" \
    "s_waitcnt lgkmcnt(3)\n\t" \
    "v_mfma_f32_16x16x32_bf16 v[200:203], v[248:251], v[228:231], v[200:203]\n\t" \
    "ds_read_b128 v[228:231], v252 offset:0\n\t" \
    "v_mfma_f32_16x16x32_bf16 v[212:215], v[244:247], v[232:235], v[212:215]\n\t" \
    "ds_read_b128 v[244:247], %[wl] offset:6144\n\t" \
    "s_waitcnt lgkmcnt(4)\n\t" \
    "v_mfma_f32_16x16x32_bf16 v[212:215], v[248:251], v[236:239], v[212:215]\n\t" \
    "ds_read_b128 v[248:251], %[wl] offset:7168\n\t" \
    "v_mfma_f32_16x16x32_bf16 v[216:219], v[80:83], v[232:235], v[216:219]\n\t" \
    "v_mfma_f32_16x16x32_bf16 v[216:219], v[84:87], v[236:239], v[216:219]\n\t" \
    "v_mfma_f32_16x16x32_bf16 v[220:223], v[88:91], v[232:235], v[220:223]\n\t" \
    "ds_read_b128 v[232:235], v252 offset:1024\n\t" \
    "v_mfma_f32_16x16x32_bf16 v[220:223], v[92:95], v[236:239], v[220:223]\n\t" \
    "v_xor_b32 v252, 0x3000, %[xa]\n\t" \
    "ds_read_b128 v[236:239], v252 offset:0\n\t" \
    "s_waitcnt lgkmcnt(6)\n\t" \
    "v_mfma_f32_16x16x32_bf16 v[220:223], v[112:115], v[240:243], v[220:223]\n\t" \
    "s_waitcnt lgkmcnt(5)\n\t" \
    "v_mfma_f32_16x16x32_bf16 v[220:223], v[116:119], v[224:227], v[220:223]\n\t" \
    "v_mfma_f32_16x16x32_bf16 v[216:219], v[104:107], v[240:243], v[216:219]\n\t" \
    "v_mfma_f32_16x16x32_bf16 v[216:219], v[108:111], v[224:227], v[216:219]\n\t" \
    "v_mfma_f32_16x16x32_bf16 v[212:215], v[96:99], v[240:243], v[212:215]\n\t" \
    "ds_read_b128 v[240:243], v252 offset:1024\n\t" \
    "v_mfma_f32_16x16x32_bf16 v[212:215], v[100:103], v[224:227], v[212:215]\n\t" \
    "ds_read_b128 v[224:227], v252 offset:16384\n\t" \
    "s_waitcnt lgkmcnt(6)\n\t" \
    "v_mfma_f32_16x16x32_bf16 v[200:203], v[96:99], v[228:231], v[200:203]\n\t" \
    "s_waitcnt lgkmcnt(3)\n\t" \
    "v_mfma_f32_16x16x32_bf16 v[200:203], v[100:103], v[232:235], v[200:203]\n\t" \
    "v_mfma_f32_16x16x32_bf16 v[204:207], v[104:107], v[228:231], v[204:207]\n\t" \
    "v_mfma_f32_16x16x32_bf16 v[204:207], v[108:111], v[232:235], v[204:207]\n\t" \
    "v_mfma_f32_16x16x32_bf16 v[208:211], v[112:115], v[228:231], v[208:211]\n\t" \
    "ds_read_b128 v[228:231], v252 offset:17408\n\t" \
    "v_mfma_f32_16x16x32_bf16 v[208:211], v[116:119], v[232:235], v[208:211]\n\t" \
    "v_xor_b32 v252, 0x3800, %[xa]\n\t" \
    "ds_read_b128 v[232:235], v252 offset:16384\n\t" \
    "s_waitcnt lgkmcnt(4)\n\t" \
    "v_mfma_f32_16x16x32_bf16 v[208:211], v[128:131], v[236:239], v[208:211]\n\t" \
    "s_waitcnt lgkmcnt(3)\n\t" \
    "v_mfma_f32_16x16x32_bf16 v[208:211], v[132:135], v[240:243], v[208:211]\n\t" \
    "v_mfma_f32_16x16x32_bf16 v[204:207], v[120:123], v[236:239], v[204:207]\n\t" \
    "v_mfma_f32_16x16x32_bf16 v[204:207], v[124:127], v[240:243], v[204:207]\n\t" \
    "v_mfma_f32_16x16x32_bf16 v[200:203], v[244:247], v[236:239], v[200:203]\n\t" \
    "ds_read_b128 v[236:239], v252 offset:17408\n\t" \
    "v_mfma_f32_16x16x32_bf16 v[200:203], v[248:251], v[240:243], v[200:203]\n\t" \
    "ds_read_b128 v[240:243], v252 offset:0\n\t" \
    "s_waitcnt lgkmcnt(4)\n\t" \
    "v_mfma_f32_16x16x32_bf16 v[212:215], v[244:247], v[224:227], v[212:215]\n\t" \
    "s_waitcnt lgkmcnt(3)\n\t" \
    "v_mfma_f32_16x16x32_bf16 v[212:215], v[248:251], v[228:231], v[212:215]\n\t" \
    "v_mfma_f32_16x16x32_bf16 v[216:219], v[120:123], v[224:227], v[216:219]\n\t" \
    "v_mfma_f32_16x16x32_bf16 v[216:219], v[124:127], v[228:231], v[216:219]\n\t" \
    "v_mfma_f32_16x16x32_bf16 v[220:223], v[128:131], v[224:227], v[220:223]\n\t" \
    "ds_read_b128 v[224:227], v252 offset:1024\n\t" \
    "v_mfma_f32_16x16x32_bf16 v[220:223], v[132:135], v[228:231], v[220:223]\n\t" \
    "v_and_b32 v252, 0x3ff, %[xa]\n\t" \
    "ds_read_b128 v[228:231], v252 offset:43008\n\t" \
    "s_waitcnt lgkmcnt(4)\n\t" \
    "v_mfma_f32_16x16x32_bf16 v[220:223], v[152:155], v[232:235], v[220:223]\n\t" \
    "v_mov_b64 v[244:245], 0\n\t" \
    "s_waitcnt lgkmcnt(3)\n\t" \
    "v_mfma_f32_16x16x32_bf16 v[220:223], v[156:159], v[236:239], v[220:223]\n\t" \
    "v_mov_b64 v[246:247], 0\n\t" \
    "v_mfma_f32_16x16x32_bf16 v[216:219], v[144:147], v[232:235], v[216:219]\n\t" \
    "v_mov_b64 v[248:249], 0\n\t" \
    "v_mfma_f32_16x16x32_bf16 v[216:219], v[148:151], v[236:239], v[216:219]\n\t" \
    "v_mov_b64 v[250:251], 0\n\t" \
    "v_mfma_f32_16x16x32_bf16 v[212:215], v[136:139], v[232:235], v[212:215]\n\t" \
    "ds_read_b128 v[232:235], v252 offset:44032\n\t" \
    "v_mfma_f32_16x16x32_bf16 v[212:215], v[140:143], v[236:239], v[212:215]\n\t" \
    "ds_read_b128 v[236:239], v252 offset:47104\n\t" \
    "s_waitcnt lgkmcnt(4)\n\t" \
    "v_mfma_f32_16x16x32_bf16 v[200:203], v[136:139], v[240:243], v[200:203]\n\t" \
    "s_waitcnt lgkmcnt(3)\n\t" \
    "v_mfma_f32_16x16x32_bf16 v[200:203], v[140:143], v[224:227], v[200:203]\n\t" \
    "v_mfma_f32_16x16x32_bf16 v[204:207], v[144:147], v[240:243], v[204:207]\n\t" \
    "v_mfma_f32_16x16x32_bf16 v[204:207], v[148:151], v[224:227], v[204:207]\n\t" \
    "v_mfma_f32_16x16x32_bf16 v[208:211], v[152:155], v[240:243], v[208:211]\n\t" \
    "ds_read_b128 v[240:243], v252 offset:48128\n\t" \
    "v_mfma_f32_16x16x32_bf16 v[208:211], v[156:159], v[224:227], v[208:211]\n\t" \
    "s_waitcnt lgkmcnt(3)\n\t" \
    "v_mfma_f32_16x16x32_bf16 v[208:211], v[192:195], v[228:231], v[208:211]\n\t" \
    "v_add_u32 v252, %[qs], v252\n\t" \
    "v_mov_b64 v[224:225], 0\n\t" \
    "s_waitcnt lgkmcnt(2)\n\t" \
    "v_mfma_f32_16x16x32_bf16 v[208:211], v[196:199], v[232:235], v[208:211]\n\t" \
    "v_mov_b64 v[226:227], 0\n\t" \
    "v_mfma_f32_16x16x32_bf16 v[204:207], v[184:187], v[228:231], v[204:207]\n\t" \
    "v_mfma_f32_16x16x32_bf16 v[204:207], v[188:191], v[232:235], v[204:207]\n\t" \
    "v_mfma_f32_16x16x32_bf16 v[200:203], v[176:179], v[228:231], v[200:203]\n\t" \
    "v_mfma_f32_16x16x32_bf16 v[200:203], v[180:183], v[232:235], v[200:203]\n\t" \
    "s_waitcnt lgkmcnt(1)\n\t" \
    "v_mfma_f32_16x16x32_bf16 v[212:215], v[176:179], v[236:239], v[212:215]\n\t" \
    "ds_read_b128 v[228:231], %[wl] offset:2048\n\t" \
    "ds_read_b128 v[232:235], %[wl] offset:3072\n\t" \
    "s_waitcnt lgkmcnt(2)\n\t" \
    "v_mfma_f32_16x16x32_bf16 v[212:215], v[180:183], v[240:243], v[212:215]\n\t" \
    "v_cvt_pk_bf16_f32 v208, v208, v209\n\t" \
    "v_cvt_pk_bf16_f32 v209, v210, v211\n\t" \
    "v_mfma_f32_16x16x32_bf16 v[216:219], v[184:187], v[236:239], v[216:219]\n\t" \
    "ds_write_b64 v252, v[208:209] offset:2048\n\t" \
    "v_cvt_pk_bf16_f32 v204, v204, v205\n\t" \
    "v_mfma_f32_16x16x32_bf16 v[216:219], v[188:191], v[240:243], v[216:219]\n\t" \
    "v_cvt_pk_bf16_f32 v205, v206, v207\n\t" \
    "ds_write_b64 v252, v[204:205] offset:1024\n\t" \
    "v_mfma_f32_16x16x32_bf16 v[220:223], v[192:195], v[236:239], v[220:223]\n\t" \
    "v_cvt_pk_bf16_f32 v200, v200, v201\n\t" \
    "v_cvt_pk_bf16_f32 v201, v202, v203\n\t" \
    "v_mfma_f32_16x16x32_bf16 v[220:223], v[196:199], v[240:243], v[220:223]\n\t" \
    "ds_write_b64 v252, v[200:201] offset:0\n\t" \
    "ds_read_b128 v[200:203], %[wl] offset:0\n\t" \
    "ds_read_b128 v[204:207], %[wl] offset:1024\n\t" \
    "v_cvt_pk_bf16_f32 v212, v212, v213\n\t" \
    "v_cvt_pk_bf16_f32 v213, v214, v215\n\t" \
    "ds_write_b64 v252, v[212:213] offset:6144\n\t" \
    "v_cvt_pk_bf16_f32 v216, v216, v217\n\t" \
    "v_cvt_pk_bf16_f32 v217, v218, v219\n\t" \
    "ds_write_b64 v252, v[216:217] offset:7168\n\t" \
    "v_cvt_pk_bf16_f32 v220, v220, v221\n\t" \
    "v_cvt_pk_bf16_f32 v221, v222, v223\n\t" \
    "ds_write_b64 v252, v[220:221] offset:8192\n\t" \
    "s_waitcnt lgkmcnt(0)\n\t"
#define ASM_XG_S_HI \
    "s_waitcnt lgkmcnt(0)\n\t" \
    "v_xor_b32 v252, 0x2000, %[xa]\n\t" \
    "ds_read_b128 v[224:227], v252 offset:16384\n\t" \
    "ds_read_b128 v[228:231], v252 offset:17408\n\t" \
    "ds_read_b128 v[244:247], %[wl] offset:4096\n\t" \
    "ds_read_b128 v[248:251], %[wl] offset:5120\n\t" \
    "ds_read_b128 v[232:235], v252 offset:0\n\t" \
    "ds_read_b128 v[236:239], v252 offset:1024\n\t" \
    "v_xor_b32 v252, 0x2800, %[xa]\n\t" \
    "ds_read_b128 v[240:243], v252 offset:0\n\t" \
    "s_cmp_eq_u32 %[slp], 0\n\t" \
    "s_cbranch_scc1 .Lxs%=\n\t" \
    "s_sleep 10\n\t" \
    ".Lxs%=:\n\t" \
    "s_waitcnt lgkmcnt(6)\n\t" \
    "v_mfma_f32_16x16x32_bf16 v[220:223], v[88:91], v[224:227], 0\n\t" \
    "s_waitcnt lgkmcnt(5)\n\t" \
    "v_mfma_f32_16x16x32_bf16 v[220:223], v[92:95], v[228:231], v[220:223]\n\t" \
    "v_mfma_f32_16x16x32_bf16 v[216:219], v[80:83], v[224:227], 0\n\t" \
    "v_mfma_f32_16x16x32_bf16 v[216:219], v[84:87], v[228:231], v[216:219]\n\t" \
    "s_waitcnt lgkmcnt(4)\n\t" \
    "v_mfma_f32_16x16x32_bf16 v[212:215], v[244:247], v[224:227], 0\n\t" \
    "ds_read_b128 v[224:227], v252 offset:1024\n\t" \
    "s_waitcnt lgkmcnt(4)\n\t" \
    "v_mfma_f32_16x16x32_bf16 v[212:215], v[248:251], v[228:231], v[212:215]\n\t" \
    "ds_read_b128 v[228:231], v252 offset:16384\n\t" \
    "s_waitcnt lgkmcnt(4)\n\t" \
    "v_mfma_f32_16x16x32_bf16 v[200:203], v[244:247], v[232:235], 0\n\t" \
    "ds_read_b128 v[244:247], %[wl] offset:6144\n\t" \
    "s_waitcnt lgkmcnt(4)\n\t" \
    "v_mfma_f32_16x16x32_bf16 v[200:203], v[248:251], v[236:239], v[200:203]\n\t" \
    "ds_read_b128 v[248:251], %[wl] offset:7168\n\t" \
    "v_mfma_f32_16x16x32_bf16 v[204:207], v[80:83], v[232:235], 0\n\t" \
    "v_mfma_f32_16x16x32_bf16 v[204:207], v[84:87], v[236:239], v[204:207]\n\t" \
    "v_mfma_f32_16x16x32_bf16 v[208:211], v[88:91], v[232:235], 0\n\t" \
    "ds_read_b128 v[232:235], v252 offset:17408\n\t" \
    "v_mfma_f32_16x16x32_bf16 v[208:211], v[92:95], v[236:239], v[208:211]\n\t" \
    "v_xor_b32 v252, 0x3000, %[xa]\n\t" \
    "ds_read_b128 v[236:239], v252 offset:16384\n\t" \
    "s_waitcnt lgkmcnt(6)\n\t" \
    "v_mfma_f32_16x16x32_bf16 v[208:211], v[112:115], v[240:243], v[208:211]\n\t" \
    "s_waitcnt lgkmcnt(5)\n\t" \
    "v_mfma_f32_16x16x32_bf16 v[208:211], v[116:119], v[224:227], v[208:211]\n\t" \
    "v_mfma_f32_16x16x32_bf16 v[204:207], v[104:107], v[240:243], v[204:207]\n\t" \
    "v_mfma_f32_16x16x32_bf16 v[204:207], v[108:111], v[224:227], v[204:207]\n\t" \
    "v_mfma_f32_16x16x32_bf16 v[200:203], v[96:99], v[240:243], v[200:203]\n\t" \
    "ds_read_b128 v[240:243], v252 offset:17408\n\t" \
    "v_mfma_f32_16x16x32_bf16 v[200:203], v[100:103], v[224:227], v[200:203]\n\t" \
    "ds_read_b128 v[224:227], v252 offset:0\n\t" \
    "s_waitcnt lgkmcnt(6)\n\t" \
    "v_mfma_f32_16x16x32_bf16 v[212:215], v[96:99], v[228:231], v[212:215]\n\t" \
    "s_waitcnt lgkmcnt(3)\n\t" \
    "v_mfma_f32_16x16x32_bf16 v[212:215], v[100:103], v[232:235], v[212:215]\n\t" \
    "v_mfma_f32_16x16x32_bf16 v[216:219], v[104:107], v[228:231], v[216:219]\n\t" \
    "v_mfma_f32_16x16x32_bf16 v[216:219], v[108:111], v[232:235], v[216:219]\n\t" \
    "v_mfma_f32_16x16x32_bf16 v[220:223], v[112:115], v[228:231], v[220:223]\n\t" \
    "ds_read_b128 v[228:231], v252 offset:1024\n\t" \
    "v_mfma_f32_16x16x32_bf16 v[220:223], v[116:119], v[232:235], v[220:223]\n\t" \
    "v_xor_b32 v252, 0x3800, %[xa]\n\t" \
    "ds_read_b128 v[232:235], v252 offset:0\n\t" \
    "s_waitcnt lgkmcnt(4)\n\t" \
    "v_mfma_f32_16x16x32_bf16 v[220:223], v[128:131], v[236:239], v[220:223]\n\t" \
    "s_waitcnt lgkmcnt(3)\n\t" \
    "v_mfma_f32_16x16x32_bf16 v[220:223], v[132:135], v[240:243], v[220:223]\n\t" \
    "v_mfma_f32_16x16x32_bf16 v[216:219], v[120:123], v[236:239], v[216:219]\n\t" \
    "v_mfma_f32_16x16x32_bf16 v[216:219], v[124:127], v[240:243], v[216:219]\n\t" \
    "v_mfma_f32_16x16x32_bf16 v[212:215], v[244:247], v[236:239], v[212:215]\n\t" \
    "ds_read_b128 v[236:239], v252 offset:1024\n\t" \
    "v_mfma_f32_16x16x32_bf16 v[212:215], v[248:251], v[240:243], v[212:215]\n\t" \
    "ds_read_b128 v[240:243], v252 offset:16384\n\t" \
    "s_waitcnt lgkmcnt(4)\n\t" \
    "v_mfma_f32_16x16x32_bf16 v[200:203], v[244:247], v[224:227], v[200:203]\n\t" \
    "ds_read_b128 v[244:247], %[wl] offset:8192\n\t" \
    "s_waitcnt lgkmcnt(4)\n\t" \
    "v_mfma_f32_16x16x32_bf16 v[200:203], v[248:251], v[228:231], v[200:203]\n\t" \
    "ds_read_b128 v[248:251], %[wl] offset:9216\n\t" \
    "v_mfma_f32_16x16x32_bf16 v[204:207], v[120:123], v[224:227], v[204:207]\n\t" \
    "v_mfma_f32_16x16x32_bf16 v[204:207], v[124:127], v[228:231], v[204:207]\n\t" \
    "v_mfma_f32_16x16x32_bf16 v[208:211], v[128:131], v[224:227], v[208:211]\n\t" \
    "ds_read_b128 v[224:227], v252 offset:17408\n\t" \
    "v_mfma_f32_16x16x32_bf16 v[208:211], v[132:135], v[228:231], v[208:211]\n\t" \
    "v_and_b32 v252, 0x3ff, %[xa]\n\t" \
    "ds_read_b128 v[228:231], v252 offset:45056\n\t" \
    "s_waitcnt lgkmcnt(6)\n\t" \
    "v_mfma_f32_16x16x32_bf16 v[208:211], v[152:155], v[232:235], v[208:211]\n\t" \
    "s_waitcnt lgkmcnt(5)\n\t" \
    "v_mfma_f32_16x16x32_bf16 v[208:211], v[156:159], v[236:239], v[208:211]\n\t" \
    "v_mfma_f32_16x16x32_bf16 v[204:207], v[144:147], v[232:235], v[204:207]\n\t" \
    "v_mfma_f32_16x16x32_bf16 v[204:207], v[148:151], v[236:239], v[204:207]\n\t" \
    "v_mfma_f32_16x16x32_bf16 v[200:203], v[136:139], v[232:235], v[200:203]\n\t" \
    "ds_read_b128 v[232:235], v252 offset:46080\n\t" \
    "v_mfma_f32_16x16x32_bf16 v[200:203], v[140:143], v[236:239], v[200:203]\n\t" \
    "ds_read_b128 v[236:239], v252 offset:40960\n\t" \
    "s_waitcnt lgkmcnt(6)\n\t" \
    "v_mfma_f32_16x16x32_bf16 v[212:215], v[136:139], v[240:243], v[212:215]\n\t" \
    "s_waitcnt lgkmcnt(3)\n\t" \
    "v_mfma_f32_16x16x32_bf16 v[212:215], v[140:143], v[224:227], v[212:215]\n\t" \
    "v_mfma_f32_16x16x32_bf16 v[216:219], v[144:147], v[240:243], v[216:219]\n\t" \
    "v_mfma_f32_16x16x32_bf16 v[216:219], v[148:151], v[224:227], v[216:219]\n\t" \
    "v_mfma_f32_16x16x32_bf16 v[220:223], v[152:155], v[240:243], v[220:223]\n\t" \
    "ds_read_b128 v[240:243], v252 offset:41984\n\t" \
    "v_mfma_f32_16x16x32_bf16 v[220:223], v[156:159], v[224:227], v[220:223]\n\t" \
    "s_waitcnt vmcnt(0) lgkmcnt(0)\n\t" \
    "s_barrier\n\t" ASM_ST \
    "s_mov_b32 m0, %[d0]\n\t" \
    "s_nop 0\n\t" \
    "global_load_lds_dwordx4 v252, %[g0] offset:0 sc1\n\t" \
    "global_load_lds_dwordx4 v252, %[g0] offset:1024 sc1\n\t" \
    "global_load_lds_dwordx4 v252, %[g0] offset:2048 sc1\n\t" \
    "global_load_lds_dwordx4 v252, %[g0] offset:3072 sc1\n\t" \
    "s_mov_b32 m0, %[d1]\n\t" \
    "s_nop 0\n\t" \
    "global_load_lds_dwordx4 v252, %[g1] sc1\n\t" \
    "ds_read_b128 v[224:227], %[xa] offset:0\n\t" \
    "v_mfma_f32_16x16x32_bf16 v[220:223], v[168:171], v[228:231], v[220:223]\n\t" \
    "v_mfma_f32_16x16x32_bf16 v[220:223], v[172:175], v[232:235], v[220:223]\n\t" \
    "v_mfma_f32_16x16x32_bf16 v[216:219], v[160:163], v[228:231], v[216:219]\n\t" \
    "v_mfma_f32_16x16x32_bf16 v[216:219], v[164:167], v[232:235], v[216:219]\n\t" \
    "v_mfma_f32_16x16x32_bf16 v[212:215], v[244:247], v[228:231], v[212:215]\n\t" \
    "ds_read_b128 v[228:231], %[xa] offset:1024\n\t" \
    "v_mfma_f32_16x16x32_bf16 v[212:215], v[248:251], v[232:235], v[212:215]\n\t" \
    "ds_read_b128 v[232:235], %[xa] offset:16384\n\t" \
    "v_mfma_f32_16x16x32_bf16 v[200:203], v[244:247], v[236:239], v[200:203]\n\t" \
    "ds_read_b128 v[244:247], %[wl] offset:0\n\t" \
    "v_mfma_f32_16x16x32_bf16 v[200:203], v[248:251], v[240:243], v[200:203]\n\t" \
    "ds_read_b128 v[248:251], %[wl] offset:1024\n\t" \
    "v_mfma_f32_16x16x32_bf16 v[204:207], v[160:163], v[236:239], v[204:207]\n\t" \
    "v_mfma_f32_16x16x32_bf16 v[204:207], v[164:167], v[240:243], v[204:207]\n\t" \
    "v_mfma_f32_16x16x32_bf16 v[208:211], v[168:171], v[236:239], v[208:211]\n\t" \
    "ds_read_b128 v[236:239], %[xa] offset:17408\n\t" \
    "v_mfma_f32_16x16x32_bf16 v[208:211], v[172:175], v[240:243], v[208:211]\n\t" \
    "v_xor_b32 v252, 0x800, %[xa]\n\t" \
    "ds_read_b128 v[240:243], v252 offset:16384\n\t" \
    "s_waitcnt lgkmcnt(6)\n\t" \
    "v_mfma_f32_16x16x32_bf16 v[208:211], v[8:11], v[224:227], v[208:211]\n\t" \
    "s_waitcnt lgkmcnt(5)\n\t" \
    "v_mfma_f32_16x16x32_bf16 v[208:211], v[12:15], v[228:231], v[208:211]\n\t" \
    "v_mfma_f32_16x16x32_bf16 v[204:207], v[0:3], v[224:227], v[204:207]\n\t" \
    "v_mfma_f32_16x16x32_bf16 v[204:207], v[4:7], v[228:231], v[204:207]\n\t" \
    "s_waitcnt lgkmcnt(3)\n\t" \
    "v_mfma_f32_16x16x32_bf16 v[200:203], v[244:247], v[224:227], v[200:203]\n\t" \
    "ds_read_b128 v[224:227], v252 offset:17408\n\t" \
    "s_waitcnt lgkmcnt(3)\n\t" \
    "v_mfma_f32_16x16x32_bf16 v[200:203], v[248:251], v[228:231], v[200:203]\n\t" \
    "ds_read_b128 v[228:231], v252 offset:0\n\t" \
    "v_mfma_f32_16x16x32_bf16 v[212:215], v[244:247], v[232:235], v[212:215]\n\t" \
    "ds_read_b128 v[244:247], %[wl] offset:2048\n\t" \
    "s_waitcnt lgkmcnt(4)\n\t" \
    "v_mfma_f32_16x16x32_bf16 v[212:215], v[248:251], v[236:239], v[212:215]\n\t" \
    "ds_read_b128 v[248:251], %[wl] offset:3072\n\t" \
    "v_mfma_f32_16x16x32_bf16 v[216:219], v[0:3], v[232:235], v[216:219]\n\t" \
    "v_mfma_f32_16x16x32_bf16 v[216:219], v[4:7], v[236:239], v[216:219]\n\t" \
    "v_mfma_f32_16x16x32_bf16 v[220:223], v[8:11], v[232:235], v[220:223]\n\t" \
    "ds_read_b128 v[232:235], v252 offset:1024\n\t" \
    "v_mfma_f32_16x16x32_bf16 v[220:223], v[12:15], v[236:239], v[220:223]\n\t" \
    "v_xor_b32 v252, 0x1000, %[xa]\n\t" \
    "ds_read_b128 v[236:239], v252 offset:0\n\t" \
    "s_waitcnt lgkmcnt(6)\n\t" \
    "v_mfma_f32_16x16x32_bf16 v[220:223], v[32:35], v[240:243], v[220:223]\n\t" \
    "s_waitcnt lgkmcnt(5)\n\t" \
    "v_mfma_f32_16x16x32_bf16 v[220:223], v[36:39], v[224:227], v[220:223]\n\t" \
    "v_mfma_f32_16x16x32_bf16 v[216:219], v[24:27], v[240:243], v[216:219]\n\t" \
    "v_mfma_f32_16x16x32_bf16 v[216:219], v[28:31], v[224:227], v[216:219]\n\t" \
    "v_mfma_f32_16x16x32_bf16 v[212:215], v[16:19], v[240:243], v[212:215]\n\t" \
    "ds_read_b128 v[240:243], v252 offset:1024\n\t" \
    "v_mfma_f32_16x16x32_bf16 v[212:215], v[20:23], v[224:227], v[212:215]\n\t" \
    "ds_read_b128 v[224:227], v252 offset:16384\n\t" \
    "s_waitcnt lgkmcnt(6)\n\t" \
    "v_mfma_f32_16x16x32_bf16 v[200:203], v[16:19], v[228:231], v[200:203]\n\t" \
    "s_waitcnt lgkmcnt(3)\n\t" \
    "v_mfma_f32_16x16x32_bf16 v[200:203], v[20:23], v[232:235], v[200:203]\n\t" \
    "v_mfma_f32_16x16x32_bf16 v[204:207], v[24:27], v[228:231], v[204:207]\n\t" \
    "v_mfma_f32_16x16x32_bf16 v[204:207], v[28:31], v[232:235], v[204:207]\n\t" \
    "v_mfma_f32_16x16x32_bf16 v[208:211], v[32:35], v[228:231], v[208:211]\n\t" \
    "ds_read_b128 v[228:231], v252 offset:17408\n\t" \
    "v_mfma_f32_16x16x32_bf16 v[208:211], v[36:39], v[232:235], v[208:211]\n\t" \
    "v_xor_b32 v252, 0x1800, %[xa]\n\t" \
    "ds_read_b128 v[232:235], v252 offset:16384\n\t" \
    "s_waitcnt lgkmcnt(4)\n\t" \
    "v_mfma_f32_16x16x32_bf16 v[208:211], v[48:51], v[236:239], v[208:211]\n\t" \
    "s_waitcnt lgkmcnt(3)\n\t" \
    "v_mfma_f32_16x16x32_bf16 v[208:211], v[52:55], v[240:243], v[208:211]\n\t" \
    "v_mfma_f32_16x16x32_bf16 v[204:207], v[40:43], v[236:239], v[204:207]\n\t" \
    "v_mfma_f32_16x16x32_bf16 v[204:207], v[44:47], v[240:243], v[204:207]\n\t" \
    "v_mfma_f32_16x16x32_bf16 v[200:203], v[244:247], v[236:239], v[200:203]\n\t" \
    "ds_read_b128 v[236:239], v252 offset:17408\n\t" \
    "v_mfma_f32_16x16x32_bf16 v[200:203], v[248:251], v[240:243], v[200:203]\n\t" \
    "ds_read_b128 v[240:243], v252 offset:0\n\t" \
    "s_waitcnt lgkmcnt(4)\n\t" \
    "v_mfma_f32_16x16x32_bf16 v[212:215], v[244:247], v[224:227], v[212:215]\n\t" \
    "s_waitcnt lgkmcnt(3)\n\t" \
    "v_mfma_f32_16x16x32_bf16 v[212:215], v[248:251], v[228:231], v[212:215]\n\t" \
    "v_mfma_f32_16x16x32_bf16 v[216:219], v[40:43], v[224:227], v[216:219]\n\t" \
    "v_mfma_f32_16x16x32_bf16 v[216:219], v[44:47], v[228:231], v[216:219]\n\t" \
    "v_mfma_f32_16x16x32_bf16 v[220:223], v[48:51], v[224:227], v[220:223]\n\t" \
    "ds_read_b128 v[224:227], v252 offset:1024\n\t" \
    "v_mfma_f32_16x16x32_bf16 v[220:223], v[52:55], v[228:231], v[220:223]\n\t" \
    "v_and_b32 v252, 0x3ff, %[xa]\n\t" \
    "ds_read_b128 v[228:231], v252 offset:43008\n\t" \
    "s_waitcnt lgkmcnt(4)\n\t" \
    "v_mfma_f32_16x16x32_bf16 v[220:223], v[72:75], v[232:235], v[220:223]\n\t" \
    "v_mov_b64 v[244:245], 0\n\t" \
    "s_waitcnt lgkmcnt(3)\n\t" \
    "v_mfma_f32_16x16x32_bf16 v[220:223], v[76:79], v[236:239], v[220:223]\n\t" \
    "v_mov_b64 v[246:247], 0\n\t" \
    "v_mfma_f32_16x16x32_bf16 v[216:219], v[64:67], v[232:235], v[216:219]\n\t" \
    "v_mov_b64 v[248:249], 0\n\t" \
    "v_mfma_f32_16x16x32_bf16 v[216:219], v[68:71], v[236:239], v[216:219]\n\t" \
    "v_mov_b64 v[250:251], 0\n\t" \
    "v_mfma_f32_16x16x32_bf16 v[212:215], v[56:59], v[232:235], v[212:215]\n\t" \
    "ds_read_b128 v[232:235], v252 offset:44032\n\t" \
    "v_mfma_f32_16x16x32_bf16 v[212:215], v[60:63], v[236:239], v[212:215]\n\t" \
    "ds_read_b128 v[236:239], v252 offset:47104\n\t" \
    "s_waitcnt lgkmcnt(4)\n\t" \
    "v_mfma_f32_16x16x32_bf16 v[200:203], v[56:59], v[240:243], v[200:203]\n\t" \
    "s_waitcnt lgkmcnt(3)\n\t" \
    "v_mfma_f32_16x16x32_bf16 v[200:203], v[60:63], v[224:227], v[200:203]\n\t" \
    "v_mfma_f32_16x16x32_bf16 v[204:207], v[64:67], v[240:243], v[204:207]\n\t" \
    "v_mfma_f32_16x16x32_bf16 v[204:207], v[68:71], v[224:227], v[204:207]\n\t" \
    "v_mfma_f32_16x16x32_bf16 v[208:211], v[72:75], v[240:243], v[208:211]\n\t" \
    "ds_read_b128 v[240:243], v252 offset:48128\n\t" \
    "v_mfma_f32_16x16x32_bf16 v[208:211], v[76:79], v[224:227], v[208:211]\n\t" \
    "s_waitcnt lgkmcnt(3)\n\t" \
    "v_mfma_f32_16x16x32_bf16 v[208:211], v[192:195], v[228:231], v[208:211]\n\t" \
    "v_add_u32 v252, %[qs], v252\n\t" \
    "v_mov_b64 v[224:225], 0\n\t" \
    "s_waitcnt lgkmcnt(2)\n\t" \
    "v_mfma_f32_16x16x32_bf16 v[208:211], v[196:199], v[232:235], v[208:211]\n\t" \
    "v_mov_b64 v[226:227], 0\n\t" \
    "v_mfma_f32_16x16x32_bf16 v[204:207], v[184:187], v[228:231], v[204:207]\n\t" \
    "v_mfma_f32_16x16x32_bf16 v[204:207], v[188:191], v[232:235], v[204:207]\n\t" \
    "v_mfma_f32_16x16x32_bf16 v[200:203], v[176:179], v[228:231], v[200:203]\n\t" \
    "v_mfma_f32_16x16x32_bf16 v[200:203], v[180:183], v[232:235], v[200:203]\n\t" \
    "s_waitcnt lgkmcnt(1)\n\t" \
    "v_mfma_f32_16x16x32_bf16 v[212:215], v[176:179], v[236:239], v[212:215]\n\t" \
    "ds_read_b128 v[228:231], %[wl] offset:2048\n\t" \
    "ds_read_b128 v[232:235], %[wl] offset:3072\n\t" \
    "s_waitcnt lgkmcnt(2)\n\t" \
    "v_mfma_f32_16x16x32_bf16 v[212:215], v[180:183], v[240:243], v[212:215]\n\t" \
    "v_cvt_pk_bf16_f32 v208, v208, v209\n\t" \
    "v_cvt_pk_bf16_f32 v209, v210, v211\n\t" \
    "v_mfma_f32_16x16x32_bf16 v[216:219], v[184:187], v[236:239], v[216:219]\n\t" \
    "ds_write_b64 v252, v[208:209] offset:2048\n\t" \
    "v_cvt_pk_bf16_f32 v204, v204, v205\n\t" \
    "v_mfma_f32_16x16x32_bf16 v[216:219], v[188:191], v[240:243], v[216:219]\n\t" \
    "v_cvt_pk_bf16_f32 v205, v206, v207\n\t" \
    "ds_write_b64 v252, v[204:205] offset:1024\n\t" \
    "v_mfma_f32_16x16x32_bf16 v[220:223], v[192:195], v[236:239], v[220:223]\n\t" \
    "v_cvt_pk_bf16_f32 v200, v200, v201\n\t" \
    "v_cvt_pk_bf16_f32 v201, v202, v203\n\t" \
    "v_mfma_f32_16x16x32_bf16 v[220:223], v[196:199], v[240:243], v[220:223]\n\t" \
    "ds_write_b64 v252, v[200:201] offset:0\n\t" \
    "ds_read_b128 v[200:203], %[wl] offset:0\n\t" \
    "ds_read_b128 v[204:207], %[wl] offset:1024\n\t" \
    "v_cvt_pk_bf16_f32 v212, v212, v213\n\t" \
    "v_cvt_pk_bf16_f32 v213, v214, v215\n\t" \
    "ds_write_b64 v252, v[212:213] offset:6144\n\t" \
    "v_cvt_pk_bf16_f32 v216, v216, v217\n\t" \
    "v_cvt_pk_bf16_f32 v217, v218, v219\n\t" \
    "ds_write_b64 v252, v[216:217] offset:7168\n\t" \
    "v_cvt_pk_bf16_f32 v220, v220, v221\n\t" \
    "v_cvt_pk_bf16_f32 v221, v222, v223\n\t" \
    "ds_write_b64 v252, v[220:221] offset:8192\n\t" \
    "s_waitcnt lgkmcnt(0)\n\t"
#define ASM_XG_S_LO2 \
    "s_waitcnt lgkmcnt(0)\n\t" \
    "ds_read_b128 v[224:227], %[xa] offset:16384\n\t" \
    "ds_read_b128 v[228:231], %[xa] offset:17408\n\t" \
    "v_xor_b32 v252, 0x800, %[xa]\n\t" \
    "ds_read_b128 v[232:235], v252 offset:16384\n\t" \
    "ds_read_b128 v[236:239], v252 offset:17408\n\t" \
    "v_xor_b32 v252, 0x1000, %[xa]\n\t" \
    "ds_read_b128 v[240:243], v252 offset:16384\n\t" \
    "ds_read_b128 v[212:215], v252 offset:17408\n\t" \
    "v_xor_b32 v252, 0x1800, %[xa]\n\t" \
    "ds_read_b128 v[200:203], v252 offset:16384\n\t" \
    "s_waitcnt lgkmcnt(6)\n\t" \
    "v_mfma_f32_16x16x32_bf16 v[220:223], v[8:11], v[224:227], 0\n\t" \
    "s_waitcnt lgkmcnt(5)\n\t" \
    "v_mfma_f32_16x16x32_bf16 v[220:223], v[12:15], v[228:231], v[220:223]\n\t" \
    "v_mfma_f32_16x16x32_bf16 v[216:219], v[0:3], v[224:227], 0\n\t" \
    "ds_read_b128 v[224:227], v252 offset:17408\n\t" \
    "v_mfma_f32_16x16x32_bf16 v[216:219], v[4:7], v[228:231], v[216:219]\n\t" \
    "v_and_b32 v252, 0x3ff, %[xa]\n\t" \
    "ds_read_b128 v[228:231], v252 offset:45056\n\t" \
    "s_waitcnt lgkmcnt(6)\n\t" \
    "v_mfma_f32_16x16x32_bf16 v[216:219], v[24:27], v[232:235], v[216:219]\n\t" \
    "s_waitcnt lgkmcnt(5)\n\t" \
    "v_mfma_f32_16x16x32_bf16 v[216:219], v[28:31], v[236:239], v[216:219]\n\t" \
    "v_mfma_f32_16x16x32_bf16 v[220:223], v[32:35], v[232:235], v[220:223]\n\t" \
    "ds_read_b128 v[232:235], v252 offset:46080\n\t" \
    "v_mfma_f32_16x16x32_bf16 v[220:223], v[36:39], v[236:239], v[220:223]\n\t" \
    "ds_read_b128 v[236:239], %[xa] offset:0\n\t" \
    "s_waitcnt lgkmcnt(6)\n\t" \
    "v_mfma_f32_16x16x32_bf16 v[220:223], v[48:51], v[240:243], v[220:223]\n\t" \
    "s_waitcnt lgkmcnt(5)\n\t" \
    "v_mfma_f32_16x16x32_bf16 v[220:223], v[52:55], v[212:215], v[220:223]\n\t" \
    "v_mfma_f32_16x16x32_bf16 v[216:219], v[40:43], v[240:243], v[216:219]\n\t" \
    "ds_read_b128 v[240:243], %[xa] offset:1024\n\t" \
    "v_mfma_f32_16x16x32_bf16 v[216:219], v[44:47], v[212:215], v[216:219]\n\t" \
    "v_xor_b32 v252, 0x800, %[xa]\n\t" \
    "ds_read_b128 v[212:215], v252 offset:0\n\t" \
    "s_waitcnt lgkmcnt(6)\n\t" \
    "v_mfma_f32_16x16x32_bf16 v[216:219], v[64:67], v[200:203], v[216:219]\n\t" \
    "s_waitcnt lgkmcnt(5)\n\t" \
    "v_mfma_f32_16x16x32_bf16 v[216:219], v[68:71], v[224:227], v[216:219]\n\t" \
    "v_mfma_f32_16x16x32_bf16 v[220:223], v[72:75], v[200:203], v[220:223]\n\t" \
    "ds_read_b128 v[200:203], v252 offset:1024\n\t" \
    "v_mfma_f32_16x16x32_bf16 v[220:223], v[76:79], v[224:227], v[220:223]\n\t" \
    "v_xor_b32 v252, 0x1000, %[xa]\n\t" \
    "ds_read_b128 v[224:227], v252 offset:0\n\t" \
    "s_waitcnt lgkmcnt(6)\n\t" \
    "v_mfma_f32_16x16x32_bf16 v[220:223], v[168:171], v[228:231], v[220:223]\n\t" \
    "s_waitcnt lgkmcnt(5)\n\t" \
    "v_mfma_f32_16x16x32_bf16 v[220:223], v[172:175], v[232:235], v[220:223]\n\t" \
    "v_mfma_f32_16x16x32_bf16 v[216:219], v[160:163], v[228:231], v[216:219]\n\t" \
    "ds_read_b128 v[228:231], v252 offset:1024\n\t" \
    "v_mfma_f32_16x16x32_bf16 v[216:219], v[164:167], v[232:235], v[216:219]\n\t" \
    "v_xor_b32 v252, 0x1800, %[xa]\n\t" \
    "ds_read_b128 v[232:235], v252 offset:0\n\t" \
    "s_waitcnt lgkmcnt(6)\n\t" \
    "v_mfma_f32_16x16x32_bf16 v[204:207], v[0:3], v[236:239], 0\n\t" \
    "s_waitcnt lgkmcnt(5)\n\t" \
    "v_mfma_f32_16x16x32_bf16 v[204:207], v[4:7], v[240:243], v[204:207]\n\t" \
    "v_mfma_f32_16x16x32_bf16 v[208:211], v[8:11], v[236:239], 0\n\t" \
    "ds_read_b128 v[236:239], v252 offset:1024\n\t" \
    "v_mfma_f32_16x16x32_bf16 v[208:211], v[12:15], v[240:243], v[208:211]\n\t" \
    "v_and_b32 v252, 0x3ff, %[xa]\n\t" \
    "ds_read_b128 v[240:243], v252 offset:40960\n\t" \
    "s_waitcnt lgkmcnt(6)\n\t" \
    "v_mfma_f32_16x16x32_bf16 v[208:211], v[32:35], v[212:215], v[208:211]\n\t" \
    "s_waitcnt lgkmcnt(5)\n\t" \
    "v_mfma_f32_16x16x32_bf16 v[208:211], v[36:39], v[200:203], v[208:211]\n\t" \
    "v_mfma_f32_16x16x32_bf16 v[204:207], v[24:27], v[212:215], v[204:207]\n\t" \
    "ds_read_b128 v[212:215], v252 offset:41984\n\t" \
    "v_mfma_f32_16x16x32_bf16 v[204:207], v[28:31], v[200:203], v[204:207]\n\t" \
    "s_waitcnt lgkmcnt(0)\n\t" \
    "s_barrier\n\t" ASM_ST \
    "v_xor_b32 v252, 0x2000, %[xa]\n\t" \
    "ds_read_b128 v[200:203], v252 offset:0\n\t" \
    "v_mfma_f32_16x16x32_bf16 v[204:207], v[40:43], v[224:227], v[204:207]\n\t" \
    "v_mfma_f32_16x16x32_bf16 v[204:207], v[44:47], v[228:231], v[204:207]\n\t" \
    "v_mfma_f32_16x16x32_bf16 v[208:211], v[48:51], v[224:227], v[208:211]\n\t" \
    "ds_read_b128 v[224:227], v252 offset:1024\n\t" \
    "v_mfma_f32_16x16x32_bf16 v[208:211], v[52:55], v[228:231], v[208:211]\n\t" \
    "v_xor_b32 v252, 0x2800, %[xa]\n\t" \
    "ds_read_b128 v[228:231], v252 offset:0\n\t" \
    "v_mfma_f32_16x16x32_bf16 v[208:211], v[72:75], v[232:235], v[208:211]\n\t" \
    "v_mfma_f32_16x16x32_bf16 v[208:211], v[76:79], v[236:239], v[208:211]\n\t" \
    "v_mfma_f32_16x16x32_bf16 v[204:207], v[64:67], v[232:235], v[204:207]\n\t" \
    "ds_read_b128 v[232:235], v252 offset:1024\n\t" \
    "v_mfma_f32_16x16x32_bf16 v[204:207], v[68:71], v[236:239], v[204:207]\n\t" \
    "v_xor_b32 v252, 0x3000, %[xa]\n\t" \
    "ds_read_b128 v[236:239], v252 offset:0\n\t" \
    "v_mfma_f32_16x16x32_bf16 v[204:207], v[160:163], v[240:243], v[204:207]\n\t" \
    "v_mfma_f32_16x16x32_bf16 v[204:207], v[164:167], v[212:215], v[204:207]\n\t" \
    "v_mfma_f32_16x16x32_bf16 v[208:211], v[168:171], v[240:243], v[208:211]\n\t" \
    "ds_read_b128 v[240:243], v252 offset:1024\n\t" \
    "v_mfma_f32_16x16x32_bf16 v[208:211], v[172:175], v[212:215], v[208:211]\n\t" \
    "v_xor_b32 v252, 0x3800, %[xa]\n\t" \
    "ds_read_b128 v[212:215], v252 offset:0\n\t" \
    "s_waitcnt lgkmcnt(6)\n\t" \
    "v_mfma_f32_16x16x32_bf16 v[208:211], v[88:91], v[200:203], v[208:211]\n\t" \
    "s_waitcnt lgkmcnt(5)\n\t" \
    "v_mfma_f32_16x16x32_bf16 v[208:211], v[92:95], v[224:227], v[208:211]\n\t" \
    "v_mfma_f32_16x16x32_bf16 v[204:207], v[80:83], v[200:203], v[204:207]\n\t" \
    "ds_read_b128 v[200:203], v252 offset:1024\n\t" \
    "v_mfma_f32_16x16x32_bf16 v[204:207], v[84:87], v[224:227], v[204:207]\n\t" \
    "v_and_b32 v252, 0x3ff, %[xa]\n\t" \
    "ds_read_b128 v[224:227], v252 offset:43008\n\t" \
    "s_waitcnt lgkmcnt(6)\n\t" \
    "v_mfma_f32_16x16x32_bf16 v[204:207], v[104:107], v[228:231], v[204:207]\n\t" \
    "s_waitcnt lgkmcnt(5)\n\t" \
    "v_mfma_f32_16x16x32_bf16 v[204:207], v[108:111], v[232:235], v[204:207]\n\t" \
    "v_mfma_f32_16x16x32_bf16 v[208:211], v[112:115], v[228:231], v[208:211]\n\t" \
    "ds_read_b128 v[228:231], v252 offset:44032\n\t" \
    "v_mfma_f32_16x16x32_bf16 v[208:211], v[116:119], v[232:235], v[208:211]\n\t" \
    "v_xor_b32 v252, 0x2000, %[xa]\n\t" \
    "ds_read_b128 v[232:235], v252 offset:16384\n\t" \
    "s_waitcnt lgkmcnt(6)\n\t" \
    "v_mfma_f32_16x16x32_bf16 v[208:211], v[128:131], v[236:239], v[208:211]\n\t" \
    "s_waitcnt lgkmcnt(5)\n\t" \
    "v_mfma_f32_16x16x32_bf16 v[208:211], v[132:135], v[240:243], v[208:211]\n\t" \
    "v_mfma_f32_16x16x32_bf16 v[204:207], v[120:123], v[236:239], v[204:207]\n\t" \
    "ds_read_b128 v[236:239], v252 offset:17408\n\t" \
    "v_mfma_f32_16x16x32_bf16 v[204:207], v[124:127], v[240:243], v[204:207]\n\t" \
    "v_xor_b32 v252, 0x2800, %[xa]\n\t" \
    "ds_read_b128 v[240:243], v252 offset:16384\n\t" \
    "s_waitcnt lgkmcnt(6)\n\t" \
    "v_mfma_f32_16x16x32_bf16 v[204:207], v[144:147], v[212:215], v[204:207]\n\t" \
    "s_waitcnt lgkmcnt(5)\n\t" \
    "v_mfma_f32_16x16x32_bf16 v[204:207], v[148:151], v[200:203], v[204:207]\n\t" \
    "v_mfma_f32_16x16x32_bf16 v[208:211], v[152:155], v[212:215], v[208:211]\n\t" \
    "ds_read_b128 v[212:215], v252 offset:17408\n\t" \
    "v_mfma_f32_16x16x32_bf16 v[208:211], v[156:159], v[200:203], v[208:211]\n\t" \
    "v_xor_b32 v252, 0x3000, %[xa]\n\t" \
    "ds_read_b128 v[200:203], v252 offset:16384\n\t" \
    "s_waitcnt lgkmcnt(6)\n\t" \
    "v_mfma_f32_16x16x32_bf16 v[208:211], v[192:195], v[224:227], v[208:211]\n\t" \
    "s_waitcnt lgkmcnt(5)\n\t" \
    "v_mfma_f32_16x16x32_bf16 v[208:211], v[196:199], v[228:231], v[208:211]\n\t" \
    "v_mfma_f32_16x16x32_bf16 v[204:207], v[184:187], v[224:227], v[204:207]\n\t" \
    "ds_read_b128 v[224:227], v252 offset:17408\n\t" \
    "v_mfma_f32_16x16x32_bf16 v[204:207], v[188:191], v[228:231], v[204:207]\n\t" \
    "v_xor_b32 v252, 0x3800, %[xa]\n\t" \
    "ds_read_b128 v[228:231], v252 offset:16384\n\t" \
    "s_waitcnt lgkmcnt(6)\n\t" \
    "v_mfma_f32_16x16x32_bf16 v[216:219], v[80:83], v[232:235], v[216:219]\n\t" \
    "s_waitcnt lgkmcnt(5)\n\t" \
    "v_mfma_f32_16x16x32_bf16 v[216:219], v[84:87], v[236:239], v[216:219]\n\t" \
    "v_mfma_f32_16x16x32_bf16 v[220:223], v[88:91], v[232:235], v[220:223]\n\t" \
    "ds_read_b128 v[232:235], v252 offset:17408\n\t" \
    "v_mfma_f32_16x16x32_bf16 v[220:223], v[92:95], v[236:239], v[220:223]\n\t" \
    "v_and_b32 v252, 0x3ff, %[xa]\n\t" \
    "ds_read_b128 v[236:239], v252 offset:47104\n\t" \
    "s_waitcnt lgkmcnt(6)\n\t" \
    "v_mfma_f32_16x16x32_bf16 v[220:223], v[112:115], v[240:243], v[220:223]\n\t" \
    "v_mov_b64 v[244:245], 0\n\t" \
    "s_waitcnt lgkmcnt(5)\n\t" \
    "v_mfma_f32_16x16x32_bf16 v[220:223], v[116:119], v[212:215], v[220:223]\n\t" \
    "v_mov_b64 v[246:247], 0\n\t" \
    "v_mfma_f32_16x16x32_bf16 v[216:219], v[104:107], v[240:243], v[216:219]\n\t" \
    "ds_read_b128 v[240:243], v252 offset:48128\n\t" \
    "v_mov_b64 v[248:249], 0\n\t" \
    "v_mfma_f32_16x16x32_bf16 v[216:219], v[108:111], v[212:215], v[216:219]\n\t" \
    "v_mov_b64 v[250:251], 0\n\t" \
    "s_waitcnt lgkmcnt(5)\n\t" \
    "v_mfma_f32_16x16x32_bf16 v[216:219], v[120:123], v[200:203], v[216:219]\n\t" \
    "s_waitcnt lgkmcnt(4)\n\t" \
    "v_mfma_f32_16x16x32_bf16 v[216:219], v[124:127], v[224:227], v[216:219]\n\t" \
    "v_mfma_f32_16x16x32_bf16 v[220:223], v[128:131], v[200:203], v[220:223]\n\t" \
    "v_mfma_f32_16x16x32_bf16 v[220:223], v[132:135], v[224:227], v[220:223]\n\t" \
    "s_waitcnt lgkmcnt(3)\n\t" \
    "v_mfma_f32_16x16x32_bf16 v[220:223], v[152:155], v[228:231], v[220:223]\n\t" \
    "v_add_u32 v252, %[qs], v252\n\t" \
    "v_mov_b64 v[224:225], 0\n\t" \
    "s_waitcnt lgkmcnt(2)\n\t" \
    "v_mfma_f32_16x16x32_bf16 v[220:223], v[156:159], v[232:235], v[220:223]\n\t" \
    "v_mov_b64 v[226:227], 0\n\t" \
    "v_cvt_pk_bf16_f32 v208, v208, v209\n\t" \
    "v_mfma_f32_16x16x32_bf16 v[216:219], v[144:147], v[228:231], v[216:219]\n\t" \
    "v_cvt_pk_bf16_f32 v209, v210, v211\n\t" \
    "ds_write_b64 v252, v[208:209] offset:2048\n\t" \
    "v_mfma_f32_16x16x32_bf16 v[216:219], v[148:151], v[232:235], v[216:219]\n\t" \
    "v_cvt_pk_bf16_f32 v204, v204, v205\n\t" \
    "v_cvt_pk_bf16_f32 v205, v206, v207\n\t" \
    "s_waitcnt lgkmcnt(2)\n\t" \
    "v_mfma_f32_16x16x32_bf16 v[216:219], v[184:187], v[236:239], v[216:219]\n\t" \
    "ds_write_b64 v252, v[204:205] offset:1024\n\t" \
    "ds_read_b128 v[228:231], %[wl] offset:2048\n\t" \
    "s_waitcnt lgkmcnt(3)\n\t" \
    "v_mfma_f32_16x16x32_bf16 v[216:219], v[188:191], v[240:243], v[216:219]\n\t" \
    "ds_read_b128 v[232:235], %[wl] offset:3072\n\t" \
    "v_mfma_f32_16x16x32_bf16 v[220:223], v[192:195], v[236:239], v[220:223]\n\t" \
    "v_mfma_f32_16x16x32_bf16 v[220:223], v[196:199], v[240:243], v[220:223]\n\t" \
    "ds_read_b128 v[200:203], %[wl] offset:0\n\t" \
    "ds_read_b128 v[204:207], %[wl] offset:1024\n\t" \
    "v_cvt_pk_bf16_f32 v216, v216, v217\n\t" \
    "v_cvt_pk_bf16_f32 v217, v218, v219\n\t" \
    "ds_write_b64 v252, v[216:217] offset:7168\n\t" \
    "s_nop 2\n\t" \
    "v_cvt_pk_bf16_f32 v220, v220, v221\n\t" \
    "v_cvt_pk_bf16_f32 v221, v222, v223\n\t" \
    "ds_write_b64 v252, v[220:221] offset:8192\n\t" \
    "s_waitcnt lgkmcnt(0)\n\t"
#define ASM_XG_S_LO1 \
    "s_waitcnt lgkmcnt(0)\n\t" \
    "ds_read_b128 v[224:227], %[xa] offset:16384\n\t" \
    "ds_read_b128 v[228:231], %[xa] offset:17408\n\t" \
    "v_xor_b32 v252, 0x800, %[xa]\n\t" \
    "ds_read_b128 v[232:235], v252 offset:16384\n\t" \
    "ds_read_b128 v[236:239], v252 offset:17408\n\t" \
    "v_xor_b32 v252, 0x1000, %[xa]\n\t" \
    "ds_read_b128 v[240:243], v252 offset:16384\n\t" \
    "ds_read_b128 v[212:215], v252 offset:17408\n\t" \
    "v_xor_b32 v252, 0x1800, %[xa]\n\t" \
    "ds_read_b128 v[200:203], v252 offset:16384\n\t" \
    "s_waitcnt lgkmcnt(6)\n\t" \
    "v_mfma_f32_16x16x32_bf16 v[220:223], v[8:11], v[224:227], 0\n\t" \
    "ds_read_b128 v[224:227], v252 offset:17408\n\t" \
    "s_waitcnt lgkmcnt(6)\n\t" \
    "v_mfma_f32_16x16x32_bf16 v[220:223], v[12:15], v[228:231], v[220:223]\n\t" \
    "v_and_b32 v252, 0x3ff, %[xa]\n\t" \
    "ds_read_b128 v[228:231], v252 offset:45056\n\t" \
    "s_waitcnt lgkmcnt(6)\n\t" \
    "v_mfma_f32_16x16x32_bf16 v[220:223], v[32:35], v[232:235], v[220:223]\n\t" \
    "ds_read_b128 v[232:235], v252 offset:46080\n\t" \
    "s_waitcnt lgkmcnt(6)\n\t" \
    "v_mfma_f32_16x16x32_bf16 v[220:223], v[36:39], v[236:239], v[220:223]\n\t" \
    "ds_read_b128 v[236:239], %[xa] offset:0\n\t" \
    "s_waitcnt lgkmcnt(6)\n\t" \
    "v_mfma_f32_16x16x32_bf16 v[220:223], v[48:51], v[240:243], v[220:223]\n\t" \
    "ds_read_b128 v[240:243], %[xa] offset:1024\n\t" \
    "s_waitcnt lgkmcnt(6)\n\t" \
    "v_mfma_f32_16x16x32_bf16 v[220:223], v[52:55], v[212:215], v[220:223]\n\t" \
    "v_xor_b32 v252, 0x800, %[xa]\n\t" \
    "ds_read_b128 v[212:215], v252 offset:0\n\t" \
    "s_waitcnt lgkmcnt(6)\n\t" \
    "v_mfma_f32_16x16x32_bf16 v[220:223], v[72:75], v[200:203], v[220:223]\n\t" \
    "ds_read_b128 v[200:203], v252 offset:1024\n\t" \
    "s_waitcnt lgkmcnt(6)\n\t" \
    "v_mfma_f32_16x16x32_bf16 v[220:223], v[76:79], v[224:227], v[220:223]\n\t" \
    "v_xor_b32 v252, 0x1000, %[xa]\n\t" \
    "ds_read_b128 v[224:227], v252 offset:0\n\t" \
    "s_waitcnt lgkmcnt(6)\n\t" \
    "v_mfma_f32_16x16x32_bf16 v[220:223], v[168:171], v[228:231], v[220:223]\n\t" \
    "ds_read_b128 v[228:231], v252 offset:1024\n\t" \
    "s_waitcnt lgkmcnt(6)\n\t" \
    "v_mfma_f32_16x16x32_bf16 v[220:223], v[172:175], v[232:235], v[220:223]\n\t" \
    "v_xor_b32 v252, 0x1800, %[xa]\n\t" \
    "ds_read_b128 v[232:235], v252 offset:0\n\t" \
    "s_waitcnt lgkmcnt(6)\n\t" \
    "v_mfma_f32_16x16x32_bf16 v[208:211], v[8:11], v[236:239], 0\n\t" \
    "ds_read_b128 v[236:239], v252 offset:1024\n\t" \
    "s_waitcnt lgkmcnt(6)\n\t" \
    "v_mfma_f32_16x16x32_bf16 v[208:211], v[12:15], v[240:243], v[208:211]\n\t" \
    "v_and_b32 v252, 0x3ff, %[xa]\n\t" \
    "ds_read_b128 v[240:243], v252 offset:40960\n\t" \
    "s_waitcnt lgkmcnt(6)\n\t" \
    "v_mfma_f32_16x16x32_bf16 v[208:211], v[32:35], v[212:215], v[208:211]\n\t" \
    "ds_read_b128 v[212:215], v252 offset:41984\n\t" \
    "s_waitcnt lgkmcnt(6)\n\t" \
    "v_mfma_f32_16x16x32_bf16 v[208:211], v[36:39], v[200:203], v[208:211]\n\t" \
    "s_waitcnt lgkmcnt(0)\n\t" \
    "s_barrier\n\t" ASM_ST \
    "v_xor_b32 v252, 0x2000, %[xa]\n\t" \
    "ds_read_b128 v[200:203], v252 offset:0\n\t" \
    "v_mfma_f32_16x16x32_bf16 v[208:211], v[48:51], v[224:227], v[208:211]\n\t" \
    "ds_read_b128 v[224:227], v252 offset:1024\n\t" \
    "v_mfma_f32_16x16x32_bf16 v[208:211], v[52:55], v[228:231], v[208:211]\n\t" \
    "v_xor_b32 v252, 0x2800, %[xa]\n\t" \
    "ds_read_b128 v[228:231], v252 offset:0\n\t" \
    "v_mfma_f32_16x16x32_bf16 v[208:211], v[72:75], v[232:235], v[208:211]\n\t" \
    "ds_read_b128 v[232:235], v252 offset:1024\n\t" \
    "v_mfma_f32_16x16x32_bf16 v[208:211], v[76:79], v[236:239], v[208:211]\n\t" \
    "v_xor_b32 v252, 0x3000, %[xa]\n\t" \
    "ds_read_b128 v[236:239], v252 offset:0\n\t" \
    "v_mfma_f32_16x16x32_bf16 v[208:211], v[168:171], v[240:243], v[208:211]\n\t" \
    "ds_read_b128 v[240:243], v252 offset:1024\n\t" \
    "v_mfma_f32_16x16x32_bf16 v[208:211], v[172:175], v[212:215], v[208:211]\n\t" \
    "v_xor_b32 v252, 0x3800, %[xa]\n\t" \
    "ds_read_b128 v[212:215], v252 offset:0\n\t" \
    "s_waitcnt lgkmcnt(6)\n\t" \
    "v_mfma_f32_16x16x32_bf16 v[208:211], v[88:91], v[200:203], v[208:211]\n\t" \
    "ds_read_b128 v[200:203], v252 offset:1024\n\t" \
    "s_waitcnt lgkmcnt(6)\n\t" \
    "v_mfma_f32_16x16x32_bf16 v[208:211], v[92:95], v[224:227], v[208:211]\n\t" \
    "v_and_b32 v252, 0x3ff, %[xa]\n\t" \
    "ds_read_b128 v[224:227], v252 offset:43008\n\t" \
    "s_waitcnt lgkmcnt(6)\n\t" \
    "v_mfma_f32_16x16x32_bf16 v[208:211], v[112:115], v[228:231], v[208:211]\n\t" \
    "ds_read_b128 v[228:231], v252 offset:44032\n\t" \
    "s_waitcnt lgkmcnt(6)\n\t" \
    "v_mfma_f32_16x16x32_bf16 v[208:211], v[116:119], v[232:235], v[208:211]\n\t" \
    "v_xor_b32 v252, 0x2000, %[xa]\n\t" \
    "ds_read_b128 v[232:235], v252 offset:16384\n\t" \
    "s_waitcnt lgkmcnt(6)\n\t" \
    "v_mfma_f32_16x16x32_bf16 v[208:211], v[128:131], v[236:239], v[208:211]\n\t" \
    "ds_read_b128 v[236:239], v252 offset:17408\n\t" \
    "s_waitcnt lgkmcnt(6)\n\t" \
    "v_mfma_f32_16x16x32_bf16 v[208:211], v[132:135], v[240:243], v[208:211]\n\t" \
    "v_xor_b32 v252, 0x2800, %[xa]\n\t" \
    "ds_read_b128 v[240:243], v252 offset:16384\n\t" \
    "s_waitcnt lgkmcnt(6)\n\t" \
    "v_mfma_f32_16x16x32_bf16 v[208:211], v[152:155], v[212:215], v[208:211]\n\t" \
    "ds_read_b128 v[212:215], v252 offset:17408\n\t" \
    "s_waitcnt lgkmcnt(6)\n\t" \
    "v_mfma_f32_16x16x32_bf16 v[208:211], v[156:159], v[200:203], v[208:211]\n\t" \
    "v_xor_b32 v252, 0x3000, %[xa]\n\t" \
    "ds_read_b128 v[200:203], v252 offset:16384\n\t" \
    "s_waitcnt lgkmcnt(6)\n\t" \
    "v_mfma_f32_16x16x32_bf16 v[208:211], v[192:195], v[224:227], v[208:211]\n\t" \
    "ds_read_b128 v[224:227], v252 offset:17408\n\t" \
    "s_waitcnt lgkmcnt(6)\n\t" \
    "v_mfma_f32_16x16x32_bf16 v[208:211], v[196:199], v[228:231], v[208:211]\n\t" \
    "v_xor_b32 v252, 0x3800, %[xa]\n\t" \
    "ds_read_b128 v[228:231], v252 offset:16384\n\t" \
    "s_waitcnt lgkmcnt(6)\n\t" \
    "v_mfma_f32_16x16x32_bf16 v[220:223], v[88:91], v[232:235], v[220:223]\n\t" \
    "ds_read_b128 v[232:235], v252 offset:17408\n\t" \
    "s_waitcnt lgkmcnt(6)\n\t" \
    "v_mfma_f32_16x16x32_bf16 v[220:223], v[92:95], v[236:239], v[220:223]\n\t" \
    "v_and_b32 v252, 0x3ff, %[xa]\n\t" \
    "ds_read_b128 v[236:239], v252 offset:47104\n\t" \
    "s_waitcnt lgkmcnt(6)\n\t" \
    "v_mfma_f32_16x16x32_bf16 v[220:223], v[112:115], v[240:243], v[220:223]\n\t" \
    "ds_read_b128 v[240:243], v252 offset:48128\n\t" \
    "v_mov_b64 v[244:245], 0\n\t" \
    "s_waitcnt lgkmcnt(6)\n\t" \
    "v_mfma_f32_16x16x32_bf16 v[220:223], v[116:119], v[212:215], v[220:223]\n\t" \
    "v_mov_b64 v[246:247], 0\n\t" \
    "s_waitcnt lgkmcnt(5)\n\t" \
    "v_mfma_f32_16x16x32_bf16 v[220:223], v[128:131], v[200:203], v[220:223]\n\t" \
    "v_mov_b64 v[248:249], 0\n\t" \
    "s_waitcnt lgkmcnt(4)\n\t" \
    "v_mfma_f32_16x16x32_bf16 v[220:223], v[132:135], v[224:227], v[220:223]\n\t" \
    "v_mov_b64 v[250:251], 0\n\t" \
    "s_waitcnt lgkmcnt(3)\n\t" \
    "v_mfma_f32_16x16x32_bf16 v[220:223], v[152:155], v[228:231], v[220:223]\n\t" \
    "v_add_u32 v252, %[qs], v252\n\t" \
    "v_mov_b64 v[224:225], 0\n\t" \
    "s_waitcnt lgkmcnt(2)\n\t" \
    "v_mfma_f32_16x16x32_bf16 v[220:223], v[156:159], v[232:235], v[220:223]\n\t" \
    "v_mov_b64 v[226:227], 0\n\t" \
    "v_cvt_pk_bf16_f32 v208, v208, v209\n\t" \
    "s_waitcnt lgkmcnt(1)\n\t" \
    "v_mfma_f32_16x16x32_bf16 v[220:223], v[192:195], v[236:239], v[220:223]\n\t" \
    "v_cvt_pk_bf16_f32 v209, v210, v211\n\t" \
    "ds_write_b64 v252, v[208:209] offset:2048\n\t" \
    "s_waitcnt lgkmcnt(1)\n\t" \
    "v_mfma_f32_16x16x32_bf16 v[220:223], v[196:199], v[240:243], v[220:223]\n\t" \
    "ds_read_b128 v[228:231], %[wl] offset:2048\n\t" \
    "ds_read_b128 v[232:235], %[wl] offset:3072\n\t" \
    "ds_read_b128 v[200:203], %[wl] offset:0\n\t" \
    "ds_read_b128 v[204:207], %[wl] offset:1024\n\t" \
    "s_nop 3\n\t" \
    "v_cvt_pk_bf16_f32 v220, v220, v221\n\t" \
    "v_cvt_pk_bf16_f32 v221, v222, v223\n\t" \
    "ds_write_b64 v252, v[220:221] offset:8192\n\t" \
    "s_waitcnt lgkmcnt(0)\n\t"
#define ASM_XG_V \
    "s_waitcnt lgkmcnt(0)\n\t" \
    "ds_read_b128 v[224:227], %[xa] offset:16384\n\t" \
    "ds_read_b128 v[228:231], %[xa] offset:17408\n\t" \
    "ds_read_b128 v[244:247], %[wl] offset:0\n\t" \
    "ds_read_b128 v[248:251], %[wl] offset:1024\n\t" \
    "ds_read_b128 v[232:235], %[xa] offset:0\n\t" \
    "ds_read_b128 v[236:239], %[xa] offset:1024\n\t" \
    "v_xor_b32 v252, 0x800, %[xa]\n\t" \
    "ds_read_b128 v[240:243], v252 offset:0\n\t" \
    "s_waitcnt lgkmcnt(6)\n\t" \
    "v_mfma_f32_16x16x32_bf16 v[220:223], v[8:11], v[224:227], 0\n\t" \
    "s_waitcnt lgkmcnt(5)\n\t" \
    "v_mfma_f32_16x16x32_bf16 v[220:223], v[12:15], v[228:231], v[220:223]\n\t" \
    "v_mfma_f32_16x16x32_bf16 v[216:219], v[0:3], v[224:227], 0\n\t" \
    "v_mfma_f32_16x16x32_bf16 v[216:219], v[4:7], v[228:231], v[216:219]\n\t" \
    "s_waitcnt lgkmcnt(4)\n\t" \
    "v_mfma_f32_16x16x32_bf16 v[212:215], v[244:247], v[224:227], 0\n\t" \
    "ds_read_b128 v[224:227], v252 offset:1024\n\t" \
    "s_waitcnt lgkmcnt(4)\n\t" \
    "v_mfma_f32_16x16x32_bf16 v[212:215], v[248:251], v[228:231], v[212:215]\n\t" \
    "ds_read_b128 v[228:231], v252 offset:16384\n\t" \
    "s_waitcnt lgkmcnt(4)\n\t" \
    "v_mfma_f32_16x16x32_bf16 v[200:203], v[244:247], v[232:235], 0\n\t" \
    "ds_read_b128 v[244:247], %[wl] offset:2048\n\t" \
    "s_waitcnt lgkmcnt(4)\n\t" \
    "v_mfma_f32_16x16x32_bf16 v[200:203], v[248:251], v[236:239], v[200:203]\n\t" \
    "ds_read_b128 v[248:251], %[wl] offset:3072\n\t" \
    "v_mfma_f32_16x16x32_bf16 v[204:207], v[0:3], v[232:235], 0\n\t" \
    "v_mfma_f32_16x16x32_bf16 v[204:207], v[4:7], v[236:239], v[204:207]\n\t" \
    "v_mfma_f32_16x16x32_bf16 v[208:211], v[8:11], v[232:235], 0\n\t" \
    "ds_read_b128 v[232:235], v252 offset:17408\n\t" \
    "v_mfma_f32_16x16x32_bf16 v[208:211], v[12:15], v[236:239], v[208:211]\n\t" \
    "v_xor_b32 v252, 0x1000, %[xa]\n\t" \
    "ds_read_b128 v[236:239], v252 offset:16384\n\t" \
    "s_waitcnt lgkmcnt(6)\n\t" \
    "v_mfma_f32_16x16x32_bf16 v[208:211], v[32:35], v[240:243], v[208:211]\n\t" \
    "s_waitcnt lgkmcnt(5)\n\t" \
    "v_mfma_f32_16x16x32_bf16 v[208:211], v[36:39], v[224:227], v[208:211]\n\t" \
    "v_mfma_f32_16x16x32_bf16 v[204:207], v[24:27], v[240:243], v[204:207]\n\t" \
    "v_mfma_f32_16x16x32_bf16 v[204:207], v[28:31], v[224:227], v[204:207]\n\t" \
    "v_mfma_f32_16x16x32_bf16 v[200:203], v[16:19], v[240:243], v[200:203]\n\t" \
    "ds_read_b128 v[240:243], v252 offset:17408\n\t" \
    "v_mfma_f32_16x16x32_bf16 v[200:203], v[20:23], v[224:227], v[200:203]\n\t" \
    "ds_read_b128 v[224:227], v252 offset:0\n\t" \
    "s_waitcnt lgkmcnt(6)\n\t" \
    "v_mfma_f32_16x16x32_bf16 v[212:215], v[16:19], v[228:231], v[212:215]\n\t" \
    "s_waitcnt lgkmcnt(3)\n\t" \
    "v_mfma_f32_16x16x32_bf16 v[212:215], v[20:23], v[232:235], v[212:215]\n\t" \
    "v_mfma_f32_16x16x32_bf16 v[216:219], v[24:27], v[228:231], v[216:219]\n\t" \
    "v_mfma_f32_16x16x32_bf16 v[216:219], v[28:31], v[232:235], v[216:219]\n\t" \
    "v_mfma_f32_16x16x32_bf16 v[220:223], v[32:35], v[228:231], v[220:223]\n\t" \
    "ds_read_b128 v[228:231], v252 offset:1024\n\t" \
    "v_mfma_f32_16x16x32_bf16 v[220:223], v[36:39], v[232:235], v[220:223]\n\t" \
    "v_xor_b32 v252, 0x1800, %[xa]\n\t" \
    "ds_read_b128 v[232:235], v252 offset:0\n\t" \
    "s_waitcnt lgkmcnt(4)\n\t" \
    "v_mfma_f32_16x16x32_bf16 v[220:223], v[48:51], v[236:239], v[220:223]\n\t" \
    "s_waitcnt lgkmcnt(3)\n\t" \
    "v_mfma_f32_16x16x32_bf16 v[220:223], v[52:55], v[240:243], v[220:223]\n\t" \
    "v_mfma_f32_16x16x32_bf16 v[216:219], v[40:43], v[236:239], v[216:219]\n\t" \
    "v_mfma_f32_16x16x32_bf16 v[216:219], v[44:47], v[240:243], v[216:219]\n\t" \
    "v_mfma_f32_16x16x32_bf16 v[212:215], v[244:247], v[236:239], v[212:215]\n\t" \
    "ds_read_b128 v[236:239], v252 offset:1024\n\t" \
    "v_mfma_f32_16x16x32_bf16 v[212:215], v[248:251], v[240:243], v[212:215]\n\t" \
    "ds_read_b128 v[240:243], v252 offset:16384\n\t" \
    "s_waitcnt lgkmcnt(4)\n\t" \
    "v_mfma_f32_16x16x32_bf16 v[200:203], v[244:247], v[224:227], v[200:203]\n\t" \
    "ds_read_b128 v[244:247], %[wl] offset:8192\n\t" \
    "s_waitcnt lgkmcnt(4)\n\t" \
    "v_mfma_f32_16x16x32_bf16 v[200:203], v[248:251], v[228:231], v[200:203]\n\t" \
    "ds_read_b128 v[248:251], %[wl] offset:9216\n\t" \
    "v_mfma_f32_16x16x32_bf16 v[204:207], v[40:43], v[224:227], v[204:207]\n\t" \
    "v_mfma_f32_16x16x32_bf16 v[204:207], v[44:47], v[228:231], v[204:207]\n\t" \
    "v_mfma_f32_16x16x32_bf16 v[208:211], v[48:51], v[224:227], v[208:211]\n\t" \
    "ds_read_b128 v[224:227], v252 offset:17408\n\t" \
    "v_mfma_f32_16x16x32_bf16 v[208:211], v[52:55], v[228:231], v[208:211]\n\t" \
    "v_and_b32 v252, 0x3ff, %[xa]\n\t" \
    "ds_read_b128 v[228:231], v252 offset:45056\n\t" \
    "s_waitcnt lgkmcnt(6)\n\t" \
    "v_mfma_f32_16x16x32_bf16 v[208:211], v[72:75], v[232:235], v[208:211]\n\t" \
    "s_waitcnt lgkmcnt(5)\n\t" \
    "v_mfma_f32_16x16x32_bf16 v[208:211], v[76:79], v[236:239], v[208:211]\n\t" \
    "v_mfma_f32_16x16x32_bf16 v[204:207], v[64:67], v[232:235], v[204:207]\n\t" \
    "v_mfma_f32_16x16x32_bf16 v[204:207], v[68:71], v[236:239], v[204:207]\n\t" \
    "v_mfma_f32_16x16x32_bf16 v[200:203], v[56:59], v[232:235], v[200:203]\n\t" \
    "ds_read_b128 v[232:235], v252 offset:46080\n\t" \
    "v_mfma_f32_16x16x32_bf16 v[200:203], v[60:63], v[236:239], v[200:203]\n\t" \
    "ds_read_b128 v[236:239], v252 offset:40960\n\t" \
    "s_waitcnt lgkmcnt(6)\n\t" \
    "v_mfma_f32_16x16x32_bf16 v[212:215], v[56:59], v[240:243], v[212:215]\n\t" \
    "s_waitcnt lgkmcnt(3)\n\t" \
    "v_mfma_f32_16x16x32_bf16 v[212:215], v[60:63], v[224:227], v[212:215]\n\t" \
    "v_mfma_f32_16x16x32_bf16 v[216:219], v[64:67], v[240:243], v[216:219]\n\t" \
    "v_mfma_f32_16x16x32_bf16 v[216:219], v[68:71], v[224:227], v[216:219]\n\t" \
    "v_mfma_f32_16x16x32_bf16 v[220:223], v[72:75], v[240:243], v[220:223]\n\t" \
    "ds_read_b128 v[240:243], v252 offset:41984\n\t" \
    "v_mfma_f32_16x16x32_bf16 v[220:223], v[76:79], v[224:227], v[220:223]\n\t" \
    "v_xor_b32 v252, 0x2000, %[xa]\n\t" \
    "ds_read_b128 v[224:227], v252 offset:0\n\t" \
    "s_waitcnt lgkmcnt(4)\n\t" \
    "v_mfma_f32_16x16x32_bf16 v[220:223], v[168:171], v[228:231], v[220:223]\n\t" \
    "s_waitcnt lgkmcnt(3)\n\t" \
    "v_mfma_f32_16x16x32_bf16 v[220:223], v[172:175], v[232:235], v[220:223]\n\t" \
    "v_mfma_f32_16x16x32_bf16 v[216:219], v[160:163], v[228:231], v[216:219]\n\t" \
    "v_mfma_f32_16x16x32_bf16 v[216:219], v[164:167], v[232:235], v[216:219]\n\t" \
    "v_mfma_f32_16x16x32_bf16 v[212:215], v[244:247], v[228:231], v[212:215]\n\t" \
    "ds_read_b128 v[228:231], v252 offset:1024\n\t" \
    "v_mfma_f32_16x16x32_bf16 v[212:215], v[248:251], v[232:235], v[212:215]\n\t" \
    "ds_read_b128 v[232:235], v252 offset:16384\n\t" \
    "s_waitcnt lgkmcnt(4)\n\t" \
    "v_mfma_f32_16x16x32_bf16 v[200:203], v[244:247], v[236:239], v[200:203]\n\t" \
    "ds_read_b128 v[244:247], %[wl] offset:4096\n\t" \
    "s_waitcnt lgkmcnt(4)\n\t" \
    "v_mfma_f32_16x16x32_bf16 v[200:203], v[248:251], v[240:243], v[200:203]\n\t" \
    "ds_read_b128 v[248:251], %[wl] offset:5120\n\t" \
    "v_mfma_f32_16x16x32_bf16 v[204:207], v[160:163], v[236:239], v[204:207]\n\t" \
    "v_mfma_f32_16x16x32_bf16 v[204:207], v[164:167], v[240:243], v[204:207]\n\t" \
    "v_mfma_f32_16x16x32_bf16 v[208:211], v[168:171], v[236:239], v[208:211]\n\t" \
    "ds_read_b128 v[236:239], v252 offset:17408\n\t" \
    "v_mfma_f32_16x16x32_bf16 v[208:211], v[172:175], v[240:243], v[208:211]\n\t" \
    "v_xor_b32 v252, 0x2800, %[xa]\n\t" \
    "ds_read_b128 v[240:243], v252 offset:16384\n\t" \
    "s_waitcnt lgkmcnt(6)\n\t" \
    "v_mfma_f32_16x16x32_bf16 v[208:211], v[88:91], v[224:227], v[208:211]\n\t" \
    "s_waitcnt lgkmcnt(5)\n\t" \
    "v_mfma_f32_16x16x32_bf16 v[208:211], v[92:95], v[228:231], v[208:211]\n\t" \
    "v_mfma_f32_16x16x32_bf16 v[204:207], v[80:83], v[224:227], v[204:207]\n\t" \
    "v_mfma_f32_16x16x32_bf16 v[204:207], v[84:87], v[228:231], v[204:207]\n\t" \
    "s_waitcnt lgkmcnt(3)\n\t" \
    "v_mfma_f32_16x16x32_bf16 v[200:203], v[244:247], v[224:227], v[200:203]\n\t" \
    "ds_read_b128 v[224:227], v252 offset:17408\n\t" \
    "s_waitcnt lgkmcnt(3)\n\t" \
    "v_mfma_f32_16x16x32_bf16 v[200:203], v[248:251], v[228:231], v[200:203]\n\t" \
    "ds_read_b128 v[228:231], v252 offset:0\n\t" \
    "v_mfma_f32_16x16x32_bf16 v[212:215], v[244:247], v[232:235], v[212:215]\n\t" \
    "ds_read_b128 v[244:247], %[wl] offset:6144\n\t" \
    "s_waitcnt lgkmcnt(4)\n\t" \
    "v_mfma_f32_16x16x32_bf16 v[212:215], v[248:251], v[236:239], v[212:215]\n\t" \
    "ds_read_b128 v[248:251], %[wl] offset:7168\n\t" \
    "v_mfma_f32_16x16x32_bf16 v[216:219], v[80:83], v[232:235], v[216:219]\n\t" \
    "v_mfma_f32_16x16x32_bf16 v[216:219], v[84:87], v[236:239], v[216:219]\n\t" \
    "v_mfma_f32_16x16x32_bf16 v[220:223], v[88:91], v[232:235], v[220:223]\n\t" \
    "ds_read_b128 v[232:235], v252 offset:1024\n\t" \
    "v_mfma_f32_16x16x32_bf16 v[220:223], v[92:95], v[236:239], v[220:223]\n\t" \
    "v_xor_b32 v252, 0x3000, %[xa]\n\t" \
    "ds_read_b128 v[236:239], v252 offset:0\n\t" \
    "s_waitcnt lgkmcnt(6)\n\t" \
    "v_mfma_f32_16x16x32_bf16 v[220:223], v[112:115], v[240:243], v[220:223]\n\t" \
    "s_waitcnt lgkmcnt(5)\n\t" \
    "v_mfma_f32_16x16x32_bf16 v[220:223], v[116:119], v[224:227], v[220:223]\n\t" \
    "v_mfma_f32_16x16x32_bf16 v[216:219], v[104:107], v[240:243], v[216:219]\n\t" \
    "v_mfma_f32_16x16x32_bf16 v[216:219], v[108:111], v[224:227], v[216:219]\n\t" \
    "v_mfma_f32_16x16x32_bf16 v[212:215], v[96:99], v[240:243], v[212:215]\n\t" \
    "ds_read_b128 v[240:243], v252 offset:1024\n\t" \
    "v_mfma_f32_16x16x32_bf16 v[212:215], v[100:103], v[224:227], v[212:215]\n\t" \
    "ds_read_b128 v[224:227], v252 offset:16384\n\t" \
    "s_waitcnt lgkmcnt(6)\n\t" \
    "v_mfma_f32_16x16x32_bf16 v[200:203], v[96:99], v[228:231], v[200:203]\n\t" \
    "s_waitcnt lgkmcnt(3)\n\t" \
    "v_mfma_f32_16x16x32_bf16 v[200:203], v[100:103], v[232:235], v[200:203]\n\t" \
    "v_mfma_f32_16x16x32_bf16 v[204:207], v[104:107], v[228:231], v[204:207]\n\t" \
    "v_mfma_f32_16x16x32_bf16 v[204:207], v[108:111], v[232:235], v[204:207]\n\t" \
    "v_mfma_f32_16x16x32_bf16 v[208:211], v[112:115], v[228:231], v[208:211]\n\t" \
    "ds_read_b128 v[228:231], v252 offset:17408\n\t" \
    "v_mfma_f32_16x16x32_bf16 v[208:211], v[116:119], v[232:235], v[208:211]\n\t" \
    "v_xor_b32 v252, 0x3800, %[xa]\n\t" \
    "ds_read_b128 v[232:235], v252 offset:16384\n\t" \
    "s_waitcnt lgkmcnt(4)\n\t" \
    "v_mfma_f32_16x16x32_bf16 v[208:211], v[128:131], v[236:239], v[208:211]\n\t" \
    "s_waitcnt lgkmcnt(3)\n\t" \
    "v_mfma_f32_16x16x32_bf16 v[208:211], v[132:135], v[240:243], v[208:211]\n\t" \
    "v_mfma_f32_16x16x32_bf16 v[204:207], v[120:123], v[236:239], v[204:207]\n\t" \
    "v_mfma_f32_16x16x32_bf16 v[204:207], v[124:127], v[240:243], v[204:207]\n\t" \
    "v_mfma_f32_16x16x32_bf16 v[200:203], v[244:247], v[236:239], v[200:203]\n\t" \
    "ds_read_b128 v[236:239], v252 offset:17408\n\t" \
    "v_mfma_f32_16x16x32_bf16 v[200:203], v[248:251], v[240:243], v[200:203]\n\t" \
    "ds_read_b128 v[240:243], v252 offset:0\n\t" \
    "s_waitcnt lgkmcnt(4)\n\t" \
    "v_mfma_f32_16x16x32_bf16 v[212:215], v[244:247], v[224:227], v[212:215]\n\t" \
    "s_waitcnt lgkmcnt(3)\n\t" \
    "v_mfma_f32_16x16x32_bf16 v[212:215], v[248:251], v[228:231], v[212:215]\n\t" \
    "v_mfma_f32_16x16x32_bf16 v[216:219], v[120:123], v[224:227], v[216:219]\n\t" \
    "v_mfma_f32_16x16x32_bf16 v[216:219], v[124:127], v[228:231], v[216:219]\n\t" \
    "v_mfma_f32_16x16x32_bf16 v[220:223], v[128:131], v[224:227], v[220:223]\n\t" \
    "ds_read_b128 v[224:227], v252 offset:1024\n\t" \
    "v_mfma_f32_16x16x32_bf16 v[220:223], v[132:135], v[228:231], v[220:223]\n\t" \
    "v_and_b32 v252, 0x3ff, %[xa]\n\t" \
    "ds_read_b128 v[228:231], v252 offset:43008\n\t" \
    "s_waitcnt lgkmcnt(4)\n\t" \
    "v_mfma_f32_16x16x32_bf16 v[220:223], v[152:155], v[232:235], v[220:223]\n\t" \
    "s_waitcnt lgkmcnt(3)\n\t" \
    "v_mfma_f32_16x16x32_bf16 v[220:223], v[156:159], v[236:239], v[220:223]\n\t" \
    "v_mfma_f32_16x16x32_bf16 v[216:219], v[144:147], v[232:235], v[216:219]\n\t" \
    "v_mfma_f32_16x16x32_bf16 v[216:219], v[148:151], v[236:239], v[216:219]\n\t" \
    "v_mfma_f32_16x16x32_bf16 v[212:215], v[136:139], v[232:235], v[212:215]\n\t" \
    "ds_read_b128 v[232:235], v252 offset:44032\n\t" \
    "v_mfma_f32_16x16x32_bf16 v[212:215], v[140:143], v[236:239], v[212:215]\n\t" \
    "ds_read_b128 v[236:239], v252 offset:47104\n\t" \
    "s_waitcnt lgkmcnt(4)\n\t" \
    "v_mfma_f32_16x16x32_bf16 v[200:203], v[136:139], v[240:243], v[200:203]\n\t" \
    "s_waitcnt lgkmcnt(3)\n\t" \
    "v_mfma_f32_16x16x32_bf16 v[200:203], v[140:143], v[224:227], v[200:203]\n\t" \
    "v_mfma_f32_16x16x32_bf16 v[204:207], v[144:147], v[240:243], v[204:207]\n\t" \
    "v_mfma_f32_16x16x32_bf16 v[204:207], v[148:151], v[224:227], v[204:207]\n\t" \
    "v_mfma_f32_16x16x32_bf16 v[208:211], v[152:155], v[240:243], v[208:211]\n\t" \
    "ds_read_b128 v[240:243], v252 offset:48128\n\t" \
    "v_mfma_f32_16x16x32_bf16 v[208:211], v[156:159], v[224:227], v[208:211]\n\t" \
    "s_waitcnt lgkmcnt(3)\n\t" \
    "v_mfma_f32_16x16x32_bf16 v[208:211], v[192:195], v[228:231], v[208:211]\n\t" \
    "v_add_u32 v252, %[qs], v252\n\t" \
    "s_waitcnt lgkmcnt(2)\n\t" \
    "v_mfma_f32_16x16x32_bf16 v[208:211], v[196:199], v[232:235], v[208:211]\n\t" \
    "v_mfma_f32_16x16x32_bf16 v[204:207], v[184:187], v[228:231], v[204:207]\n\t" \
    "v_mfma_f32_16x16x32_bf16 v[204:207], v[188:191], v[232:235], v[204:207]\n\t" \
    "v_mfma_f32_16x16x32_bf16 v[200:203], v[176:179], v[228:231], v[200:203]\n\t" \
    "v_mfma_f32_16x16x32_bf16 v[200:203], v[180:183], v[232:235], v[200:203]\n\t" \
    "s_waitcnt lgkmcnt(1)\n\t" \
    "v_mfma_f32_16x16x32_bf16 v[212:215], v[176:179], v[236:239], v[212:215]\n\t" \
    "v_cvt_pk_bf16_f32 v208, v208, v209\n\t" \
    "v_cvt_pk_bf16_f32 v209, v210, v211\n\t" \
    "s_waitcnt lgkmcnt(0)\n\t" \
    "v_mfma_f32_16x16x32_bf16 v[212:215], v[180:183], v[240:243], v[212:215]\n\t" \
    "ds_write_b64 v252, v[208:209] offset:2048\n\t" \
    "v_cvt_pk_bf16_f32 v204, v204, v205\n\t" \
    "v_mfma_f32_16x16x32_bf16 v[216:219], v[184:187], v[236:239], v[216:219]\n\t" \
    "v_cvt_pk_bf16_f32 v205, v206, v207\n\t" \
    "ds_write_b64 v252, v[204:205] offset:1024\n\t" \
    "v_mfma_f32_16x16x32_bf16 v[216:219], v[188:191], v[240:243], v[216:219]\n\t" \
    "v_cvt_pk_bf16_f32 v200, v200, v201\n\t" \
    "v_cvt_pk_bf16_f32 v201, v202, v203\n\t" \
    "v_mfma_f32_16x16x32_bf16 v[220:223], v[192:195], v[236:239], v[220:223]\n\t" \
    "ds_write_b64 v252, v[200:201] offset:0\n\t" \
    "v_mfma_f32_16x16x32_bf16 v[220:223], v[196:199], v[240:243], v[220:223]\n\t" \
    "v_cvt_pk_bf16_f32 v212, v212, v213\n\t" \
    "v_cvt_pk_bf16_f32 v213, v214, v215\n\t" \
    "ds_write_b64 v252, v[212:213] offset:6144\n\t" \
    "v_cvt_pk_bf16_f32 v216, v216, v217\n\t" \
    "v_cvt_pk_bf16_f32 v217, v218, v219\n\t" \
    "ds_write_b64 v252, v[216:217] offset:7168\n\t" \
    "s_nop 1\n\t" \
    "v_cvt_pk_bf16_f32 v220, v220, v221\n\t" \
    "v_cvt_pk_bf16_f32 v221, v222, v223\n\t" \
    "ds_write_b64 v252, v[220:221] offset:8192\n\t" \
    "s_waitcnt lgkmcnt(0)\n\t"
#define ASM_XG_H_LO \
    "s_waitcnt lgkmcnt(0)\n\t" \
    "ds_read_b128 v[224:227], %[xa] offset:16384\n\t" \
    "ds_read_b128 v[228:231], %[xa] offset:17408\n\t" \
    "ds_read_b128 v[244:247], %[wl] offset:0\n\t" \
    "ds_read_b128 v[248:251], %[wl] offset:1024\n\t" \
    "ds_read_b128 v[232:235], %[xa] offset:0\n\t" \
    "ds_read_b128 v[236:239], %[xa] offset:1024\n\t" \
    "v_xor_b32 v252, 0x800, %[xa]\n\t" \
    "ds_read_b128 v[240:243], v252 offset:0\n\t" \
    "s_waitcnt lgkmcnt(6)\n\t" \
    "v_mfma_f32_16x16x32_bf16 v[220:223], v[8:11], v[224:227], 0\n\t" \
    "s_waitcnt lgkmcnt(5)\n\t" \
    "v_mfma_f32_16x16x32_bf16 v[220:223], v[12:15], v[228:231], v[220:223]\n\t" \
    "v_mfma_f32_16x16x32_bf16 v[216:219], v[0:3], v[224:227], 0\n\t" \
    "v_mfma_f32_16x16x32_bf16 v[216:219], v[4:7], v[228:231], v[216:219]\n\t" \
    "s_waitcnt lgkmcnt(4)\n\t" \
    "v_mfma_f32_16x16x32_bf16 v[212:215], v[244:247], v[224:227], 0\n\t" \
    "ds_read_b128 v[224:227], v252 offset:1024\n\t" \
    "s_waitcnt lgkmcnt(4)\n\t" \
    "v_mfma_f32_16x16x32_bf16 v[212:215], v[248:251], v[228:231], v[212:215]\n\t" \
    "ds_read_b128 v[228:231], v252 offset:16384\n\t" \
    "s_waitcnt lgkmcnt(4)\n\t" \
    "v_mfma_f32_16x16x32_bf16 v[200:203], v[244:247], v[232:235], 0\n\t" \
    "ds_read_b128 v[244:247], %[wl] offset:2048\n\t" \
    "s_waitcnt lgkmcnt(4)\n\t" \
    "v_mfma_f32_16x16x32_bf16 v[200:203], v[248:251], v[236:239], v[200:203]\n\t" \
    "ds_read_b128 v[248:251], %[wl] offset:3072\n\t" \
    "v_mfma_f32_16x16x32_bf16 v[204:207], v[0:3], v[232:235], 0\n\t" \
    "v_mfma_f32_16x16x32_bf16 v[204:207], v[4:7], v[236:239], v[204:207]\n\t" \
    "v_mfma_f32_16x16x32_bf16 v[208:211], v[8:11], v[232:235], 0\n\t" \
    "ds_read_b128 v[232:235], v252 offset:17408\n\t" \
    "v_mfma_f32_16x16x32_bf16 v[208:211], v[12:15], v[236:239], v[208:211]\n\t" \
    "v_xor_b32 v252, 0x1000, %[xa]\n\t" \
    "ds_read_b128 v[236:239], v252 offset:16384\n\t" \
    "s_waitcnt lgkmcnt(6)\n\t" \
    "v_mfma_f32_16x16x32_bf16 v[208:211], v[32:35], v[240:243], v[208:211]\n\t" \
    "s_waitcnt lgkmcnt(5)\n\t" \
    "v_mfma_f32_16x16x32_bf16 v[208:211], v[36:39], v[224:227], v[208:211]\n\t" \
    "v_mfma_f32_16x16x32_bf16 v[204:207], v[24:27], v[240:243], v[204:207]\n\t" \
    "v_mfma_f32_16x16x32_bf16 v[204:207], v[28:31], v[224:227], v[204:207]\n\t" \
    "v_mfma_f32_16x16x32_bf16 v[200:203], v[16:19], v[240:243], v[200:203]\n\t" \
    "ds_read_b128 v[240:243], v252 offset:17408\n\t" \
    "v_mfma_f32_16x16x32_bf16 v[200:203], v[20:23], v[224:227], v[200:203]\n\t" \
    "ds_read_b128 v[224:227], v252 offset:0\n\t" \
    "s_waitcnt lgkmcnt(6)\n\t" \
    "v_mfma_f32_16x16x32_bf16 v[212:215], v[16:19], v[228:231], v[212:215]\n\t" \
    "s_waitcnt lgkmcnt(3)\n\t" \
    "v_mfma_f32_16x16x32_bf16 v[212:215], v[20:23], v[232:235], v[212:215]\n\t" \
    "v_mfma_f32_16x16x32_bf16 v[216:219], v[24:27], v[228:231], v[216:219]\n\t" \
    "v_mfma_f32_16x16x32_bf16 v[216:219], v[28:31], v[232:235], v[216:219]\n\t" \
    "v_mfma_f32_16x16x32_bf16 v[220:223], v[32:35], v[228:231], v[220:223]\n\t" \
    "ds_read_b128 v[228:231], v252 offset:1024\n\t" \
    "v_mfma_f32_16x16x32_bf16 v[220:223], v[36:39], v[232:235], v[220:223]\n\t" \
    "v_xor_b32 v252, 0x1800, %[xa]\n\t" \
    "ds_read_b128 v[232:235], v252 offset:0\n\t" \
    "s_waitcnt lgkmcnt(4)\n\t" \
    "v_mfma_f32_16x16x32_bf16 v[220:223], v[48:51], v[236:239], v[220:223]\n\t" \
    "s_waitcnt lgkmcnt(3)\n\t" \
    "v_mfma_f32_16x16x32_bf16 v[220:223], v[52:55], v[240:243], v[220:223]\n\t" \
    "v_mfma_f32_16x16x32_bf16 v[216:219], v[40:43], v[236:239], v[216:219]\n\t" \
    "v_mfma_f32_16x16x32_bf16 v[216:219], v[44:47], v[240:243], v[216:219]\n\t" \
    "v_mfma_f32_16x16x32_bf16 v[212:215], v[244:247], v[236:239], v[212:215]\n\t" \
    "ds_read_b128 v[236:239], v252 offset:1024\n\t" \
    "v_mfma_f32_16x16x32_bf16 v[212:215], v[248:251], v[240:243], v[212:215]\n\t" \
    "ds_read_b128 v[240:243], v252 offset:16384\n\t" \
    "s_waitcnt lgkmcnt(4)\n\t" \
    "v_mfma_f32_16x16x32_bf16 v[200:203], v[244:247], v[224:227], v[200:203]\n\t" \
    "ds_read_b128 v[244:247], %[wl] offset:8192\n\t" \
    "s_waitcnt lgkmcnt(4)\n\t" \
    "v_mfma_f32_16x16x32_bf16 v[200:203], v[248:251], v[228:231], v[200:203]\n\t" \
    "ds_read_b128 v[248:251], %[wl] offset:9216\n\t" \
    "v_mfma_f32_16x16x32_bf16 v[204:207], v[40:43], v[224:227], v[204:207]\n\t" \
    "v_mfma_f32_16x16x32_bf16 v[204:207], v[44:47], v[228:231], v[204:207]\n\t" \
    "v_mfma_f32_16x16x32_bf16 v[208:211], v[48:51], v[224:227], v[208:211]\n\t" \
    "ds_read_b128 v[224:227], v252 offset:17408\n\t" \
    "v_mfma_f32_16x16x32_bf16 v[208:211], v[52:55], v[228:231], v[208:211]\n\t" \
    "v_and_b32 v252, 0x3ff, %[xa]\n\t" \
    "ds_read_b128 v[228:231], v252 offset:45056\n\t" \
    "s_waitcnt lgkmcnt(6)\n\t" \
    "v_mfma_f32_16x16x32_bf16 v[208:211], v[72:75], v[232:235], v[208:211]\n\t" \
    "s_waitcnt lgkmcnt(5)\n\t" \
    "v_mfma_f32_16x16x32_bf16 v[208:211], v[76:79], v[236:239], v[208:211]\n\t" \
    "v_mfma_f32_16x16x32_bf16 v[204:207], v[64:67], v[232:235], v[204:207]\n\t" \
    "v_mfma_f32_16x16x32_bf16 v[204:207], v[68:71], v[236:239], v[204:207]\n\t" \
    "v_mfma_f32_16x16x32_bf16 v[200:203], v[56:59], v[232:235], v[200:203]\n\t" \
    "ds_read_b128 v[232:235], v252 offset:46080\n\t" \
    "v_mfma_f32_16x16x32_bf16 v[200:203], v[60:63], v[236:239], v[200:203]\n\t" \
    "ds_read_b128 v[236:239], v252 offset:40960\n\t" \
    "s_waitcnt lgkmcnt(6)\n\t" \
    "v_mfma_f32_16x16x32_bf16 v[212:215], v[56:59], v[240:243], v[212:215]\n\t" \
    "s_waitcnt lgkmcnt(3)\n\t" \
    "v_mfma_f32_16x16x32_bf16 v[212:215], v[60:63], v[224:227], v[212:215]\n\t" \
    "v_mfma_f32_16x16x32_bf16 v[216:219], v[64:67], v[240:243], v[216:219]\n\t" \
    "v_mfma_f32_16x16x32_bf16 v[216:219], v[68:71], v[224:227], v[216:219]\n\t" \
    "v_mfma_f32_16x16x32_bf16 v[220:223], v[72:75], v[240:243], v[220:223]\n\t" \
    "ds_read_b128 v[240:243], v252 offset:41984\n\t" \
    "v_mfma_f32_16x16x32_bf16 v[220:223], v[76:79], v[224:227], v[220:223]\n\t" \
    "s_waitcnt lgkmcnt(0)\n\t" \
    "s_barrier\n\t" ASM_ST \
    "v_xor_b32 v252, 0x2000, %[xa]\n\t" \
    "ds_read_b128 v[224:227], v252 offset:0\n\t" \
    "v_mfma_f32_16x16x32_bf16 v[220:223], v[168:171], v[228:231], v[220:223]\n\t" \
    "v_mfma_f32_16x16x32_bf16 v[220:223], v[172:175], v[232:235], v[220:223]\n\t" \
    "v_mfma_f32_16x16x32_bf16 v[216:219], v[160:163], v[228:231], v[216:219]\n\t" \
    "v_mfma_f32_16x16x32_bf16 v[216:219], v[164:167], v[232:235], v[216:219]\n\t" \
    "v_mfma_f32_16x16x32_bf16 v[212:215], v[244:247], v[228:231], v[212:215]\n\t" \
    "ds_read_b128 v[228:231], v252 offset:1024\n\t" \
    "v_mfma_f32_16x16x32_bf16 v[212:215], v[248:251], v[232:235], v[212:215]\n\t" \
    "ds_read_b128 v[232:235], v252 offset:16384\n\t" \
    "v_mfma_f32_16x16x32_bf16 v[200:203], v[244:247], v[236:239], v[200:203]\n\t" \
    "ds_read_b128 v[244:247], %[wl] offset:4096\n\t" \
    "v_mfma_f32_16x16x32_bf16 v[200:203], v[248:251], v[240:243], v[200:203]\n\t" \
    "ds_read_b128 v[248:251], %[wl] offset:5120\n\t" \
    "v_mfma_f32_16x16x32_bf16 v[204:207], v[160:163], v[236:239], v[204:207]\n\t" \
    "v_mfma_f32_16x16x32_bf16 v[204:207], v[164:167], v[240:243], v[204:207]\n\t" \
    "v_mfma_f32_16x16x32_bf16 v[208:211], v[168:171], v[236:239], v[208:211]\n\t" \
    "ds_read_b128 v[236:239], v252 offset:17408\n\t" \
    "v_mfma_f32_16x16x32_bf16 v[208:211], v[172:175], v[240:243], v[208:211]\n\t" \
    "v_xor_b32 v252, 0x2800, %[xa]\n\t" \
    "ds_read_b128 v[240:243], v252 offset:16384\n\t" \
    "s_waitcnt lgkmcnt(6)\n\t" \
    "v_mfma_f32_16x16x32_bf16 v[208:211], v[88:91], v[224:227], v[208:211]\n\t" \
    "s_waitcnt lgkmcnt(5)\n\t" \
    "v_mfma_f32_16x16x32_bf16 v[208:211], v[92:95], v[228:231], v[208:211]\n\t" \
    "v_mfma_f32_16x16x32_bf16 v[204:207], v[80:83], v[224:227], v[204:207]\n\t" \
    "v_mfma_f32_16x16x32_bf16 v[204:207], v[84:87], v[228:231], v[204:207]\n\t" \
    "s_waitcnt lgkmcnt(3)\n\t" \
    "v_mfma_f32_16x16x32_bf16 v[200:203], v[244:247], v[224:227], v[200:203]\n\t" \
    "ds_read_b128 v[224:227], v252 offset:17408\n\t" \
    "s_waitcnt lgkmcnt(3)\n\t" \
    "v_mfma_f32_16x16x32_bf16 v[200:203], v[248:251], v[228:231], v[200:203]\n\t" \
    "ds_read_b128 v[228:231], v252 offset:0\n\t" \
    "v_mfma_f32_16x16x32_bf16 v[212:215], v[244:247], v[232:235], v[212:215]\n\t" \
    "ds_read_b128 v[244:247], %[wl] offset:6144\n\t" \
    "s_waitcnt lgkmcnt(4)\n\t" \
    "v_mfma_f32_16x16x32_bf16 v[212:215], v[248:251], v[236:239], v[212:215]\n\t" \
    "ds_read_b128 v[248:251], %[wl] offset:7168\n\t" \
    "v_mfma_f32_16x16x32_bf16 v[216:219], v[80:83], v[232:235], v[216:219]\n\t" \
    "v_mfma_f32_16x16x32_bf16 v[216:219], v[84:87], v[236:239], v[216:219]\n\t" \
    "v_mfma_f32_16x16x32_bf16 v[220:223], v[88:91], v[232:235], v[220:223]\n\t" \
    "ds_read_b128 v[232:235], v252 offset:1024\n\t" \
    "v_mfma_f32_16x16x32_bf16 v[220:223], v[92:95], v[236:239], v[220:223]\n\t" \
    "v_xor_b32 v252, 0x3000, %[xa]\n\t" \
    "ds_read_b128 v[236:239], v252 offset:0\n\t" \
    "s_waitcnt lgkmcnt(6)\n\t" \
    "v_mfma_f32_16x16x32_bf16 v[220:223], v[112:115], v[240:243], v[220:223]\n\t" \
    "s_waitcnt lgkmcnt(5)\n\t" \
    "v_mfma_f32_16x16x32_bf16 v[220:223], v[116:119], v[224:227], v[220:223]\n\t" \
    "v_mfma_f32_16x16x32_bf16 v[216:219], v[104:107], v[240:243], v[216:219]\n\t" \
    "v_mfma_f32_16x16x32_bf16 v[216:219], v[108:111], v[224:227], v[216:219]\n\t" \
    "v_mfma_f32_16x16x32_bf16 v[212:215], v[96:99], v[240:243], v[212:215]\n\t" \
    "ds_read_b128 v[240:243], v252 offset:1024\n\t" \
    "v_mfma_f32_16x16x32_bf16 v[212:215], v[100:103], v[224:227], v[212:215]\n\t" \
    "ds_read_b128 v[224:227], v252 offset:16384\n\t" \
    "s_waitcnt lgkmcnt(6)\n\t" \
    "v_mfma_f32_16x16x32_bf16 v[200:203], v[96:99], v[228:231], v[200:203]\n\t" \
    "s_waitcnt lgkmcnt(3)\n\t" \
    "v_mfma_f32_16x16x32_bf16 v[200:203], v[100:103], v[232:235], v[200:203]\n\t" \
    "v_mfma_f32_16x16x32_bf16 v[204:207], v[104:107], v[228:231], v[204:207]\n\t" \
    "v_mfma_f32_16x16x32_bf16 v[204:207], v[108:111], v[232:235], v[204:207]\n\t" \
    "v_mfma_f32_16x16x32_bf16 v[208:211], v[112:115], v[228:231], v[208:211]\n\t" \
    "ds_read_b128 v[228:231], v252 offset:17408\n\t" \
    "v_mfma_f32_16x16x32_bf16 v[208:211], v[116:119], v[232:235], v[208:211]\n\t" \
    "v_xor_b32 v252, 0x3800, %[xa]\n\t" \
    "ds_read_b128 v[232:235], v252 offset:16384\n\t" \
    "s_waitcnt lgkmcnt(4)\n\t" \
    "v_mfma_f32_16x16x32_bf16 v[208:211], v[128:131], v[236:239], v[208:211]\n\t" \
    "s_waitcnt lgkmcnt(3)\n\t" \
    "v_mfma_f32_16x16x32_bf16 v[208:211], v[132:135], v[240:243], v[208:211]\n\t" \
    "v_mfma_f32_16x16x32_bf16 v[204:207], v[120:123], v[236:239], v[204:207]\n\t" \
    "v_mfma_f32_16x16x32_bf16 v[204:207], v[124:127], v[240:243], v[204:207]\n\t" \
    "v_mfma_f32_16x16x32_bf16 v[200:203], v[244:247], v[236:239], v[200:203]\n\t" \
    "ds_read_b128 v[236:239], v252 offset:17408\n\t" \
    "v_mfma_f32_16x16x32_bf16 v[200:203], v[248:251], v[240:243], v[200:203]\n\t" \
    "ds_read_b128 v[240:243], v252 offset:0\n\t" \
    "s_waitcnt lgkmcnt(4)\n\t" \
    "v_mfma_f32_16x16x32_bf16 v[212:215], v[244:247], v[224:227], v[212:215]\n\t" \
    "s_waitcnt lgkmcnt(3)\n\t" \
    "v_mfma_f32_16x16x32_bf16 v[212:215], v[248:251], v[228:231], v[212:215]\n\t" \
    "v_mfma_f32_16x16x32_bf16 v[216:219], v[120:123], v[224:227], v[216:219]\n\t" \
    "v_mfma_f32_16x16x32_bf16 v[216:219], v[124:127], v[228:231], v[216:219]\n\t" \
    "v_mfma_f32_16x16x32_bf16 v[220:223], v[128:131], v[224:227], v[220:223]\n\t" \
    "ds_read_b128 v[224:227], v252 offset:1024\n\t" \
    "v_mfma_f32_16x16x32_bf16 v[220:223], v[132:135], v[228:231], v[220:223]\n\t" \
    "v_and_b32 v252, 0x3ff, %[xa]\n\t" \
    "ds_read_b128 v[228:231], v252 offset:43008\n\t" \
    "s_waitcnt lgkmcnt(4)\n\t" \
    "v_mfma_f32_16x16x32_bf16 v[220:223], v[152:155], v[232:235], v[220:223]\n\t" \
    "s_waitcnt lgkmcnt(3)\n\t" \
    "v_mfma_f32_16x16x32_bf16 v[220:223], v[156:159], v[236:239], v[220:223]\n\t" \
    "v_mfma_f32_16x16x32_bf16 v[216:219], v[144:147], v[232:235], v[216:219]\n\t" \
    "v_mfma_f32_16x16x32_bf16 v[216:219], v[148:151], v[236:239], v[216:219]\n\t" \
    "v_mfma_f32_16x16x32_bf16 v[212:215], v[136:139], v[232:235], v[212:215]\n\t" \
    "ds_read_b128 v[232:235], v252 offset:44032\n\t" \
    "v_mfma_f32_16x16x32_bf16 v[212:215], v[140:143], v[236:239], v[212:215]\n\t" \
    "ds_read_b128 v[236:239], v252 offset:47104\n\t" \
    "s_waitcnt lgkmcnt(4)\n\t" \
    "v_mfma_f32_16x16x32_bf16 v[200:203], v[136:139], v[240:243], v[200:203]\n\t" \
    "s_waitcnt lgkmcnt(3)\n\t" \
    "v_mfma_f32_16x16x32_bf16 v[200:203], v[140:143], v[224:227], v[200:203]\n\t" \
    "v_mfma_f32_16x16x32_bf16 v[204:207], v[144:147], v[240:243], v[204:207]\n\t" \
    "v_mfma_f32_16x16x32_bf16 v[204:207], v[148:151], v[224:227], v[204:207]\n\t" \
    "v_mfma_f32_16x16x32_bf16 v[208:211], v[152:155], v[240:243], v[208:211]\n\t" \
    "ds_read_b128 v[240:243], v252 offset:48128\n\t" \
    "v_mfma_f32_16x16x32_bf16 v[208:211], v[156:159], v[224:227], v[208:211]\n\t" \
    "s_waitcnt lgkmcnt(3)\n\t" \
    "v_mfma_f32_16x16x32_bf16 v[208:211], v[192:195], v[228:231], v[208:211]\n\t" \
    "v_add_u32 v252, %[qs], v252\n\t" \
    "s_waitcnt lgkmcnt(2)\n\t" \
    "v_mfma_f32_16x16x32_bf16 v[208:211], v[196:199], v[232:235], v[208:211]\n\t" \
    "v_mfma_f32_16x16x32_bf16 v[204:207], v[184:187], v[228:231], v[204:207]\n\t" \
    "v_mfma_f32_16x16x32_bf16 v[204:207], v[188:191], v[232:235], v[204:207]\n\t" \
    "v_mfma_f32_16x16x32_bf16 v[200:203], v[176:179], v[228:231], v[200:203]\n\t" \
    "v_mfma_f32_16x16x32_bf16 v[200:203], v[180:183], v[232:235], v[200:203]\n\t" \
    "s_waitcnt lgkmcnt(1)\n\t" \
    "v_mfma_f32_16x16x32_bf16 v[212:215], v[176:179], v[236:239], v[212:215]\n\t" \
    "v_cvt_pk_bf16_f32 v208, v208, v209\n\t" \
    "v_cvt_pk_bf16_f32 v209, v210, v211\n\t" \
    "s_waitcnt lgkmcnt(0)\n\t" \
    "v_mfma_f32_16x16x32_bf16 v[212:215], v[180:183], v[240:243], v[212:215]\n\t" \
    "ds_write_b64 v252, v[208:209] offset:2048\n\t" \
    "v_cvt_pk_bf16_f32 v204, v204, v205\n\t" \
    "v_mfma_f32_16x16x32_bf16 v[216:219], v[184:187], v[236:239], v[216:219]\n\t" \
    "v_cvt_pk_bf16_f32 v205, v206, v207\n\t" \
    "ds_write_b64 v252, v[204:205] offset:1024\n\t" \
    "v_mfma_f32_16x16x32_bf16 v[216:219], v[188:191], v[240:243], v[216:219]\n\t" \
    "v_cvt_pk_bf16_f32 v200, v200, v201\n\t" \
    "v_cvt_pk_bf16_f32 v201, v202, v203\n\t" \
    "v_mfma_f32_16x16x32_bf16 v[220:223], v[192:195], v[236:239], v[220:223]\n\t" \
    "ds_write_b64 v252, v[200:201] offset:0\n\t" \
    "v_mfma_f32_16x16x32_bf16 v[220:223], v[196:199], v[240:243], v[220:223]\n\t" \
    "v_cvt_pk_bf16_f32 v212, v212, v213\n\t" \
    "v_cvt_pk_bf16_f32 v213, v214, v215\n\t" \
    "ds_write_b64 v252, v[212:213] offset:6144\n\t" \
    "v_cvt_pk_bf16_f32 v216, v216, v217\n\t" \
    "v_cvt_pk_bf16_f32 v217, v218, v219\n\t" \
    "ds_write_b64 v252, v[216:217] offset:7168\n\t" \
    "s_nop 1\n\t" \
    "v_cvt_pk_bf16_f32 v220, v220, v221\n\t" \
    "v_cvt_pk_bf16_f32 v221, v222, v223\n\t" \
    "ds_write_b64 v252, v[220:221] offset:8192\n\t" \
    "s_waitcnt lgkmcnt(0)\n\t"
#define ASM_XG_H_HI \
    "s_waitcnt lgkmcnt(0)\n\t" \
    "v_xor_b32 v252, 0x2000, %[xa]\n\t" \
    "ds_read_b128 v[224:227], v252 offset:16384\n\t" \
    "ds_read_b128 v[228:231], v252 offset:17408\n\t" \
    "ds_read_b128 v[244:247], %[wl] offset:4096\n\t" \
    "ds_read_b128 v[248:251], %[wl] offset:5120\n\t" \
    "ds_read_b128 v[232:235], v252 offset:0\n\t" \
    "ds_read_b128 v[236:239], v252 offset:1024\n\t" \
    "v_xor_b32 v252, 0x2800, %[xa]\n\t" \
    "ds_read_b128 v[240:243], v252 offset:0\n\t" \
    "s_waitcnt lgkmcnt(6)\n\t" \
    "v_mfma_f32_16x16x32_bf16 v[220:223], v[88:91], v[224:227], 0\n\t" \
    "s_waitcnt lgkmcnt(5)\n\t" \
    "v_mfma_f32_16x16x32_bf16 v[220:223], v[92:95], v[228:231], v[220:223]\n\t" \
    "v_mfma_f32_16x16x32_bf16 v[216:219], v[80:83], v[224:227], 0\n\t" \
    "v_mfma_f32_16x16x32_bf16 v[216:219], v[84:87], v[228:231], v[216:219]\n\t" \
    "s_waitcnt lgkmcnt(4)\n\t" \
    "v_mfma_f32_16x16x32_bf16 v[212:215], v[244:247], v[224:227], 0\n\t" \
    "ds_read_b128 v[224:227], v252 offset:1024\n\t" \
    "s_waitcnt lgkmcnt(4)\n\t" \
    "v_mfma_f32_16x16x32_bf16 v[212:215], v[248:251], v[228:231], v[212:215]\n\t" \
    "ds_read_b128 v[228:231], v252 offset:16384\n\t" \
    "s_waitcnt lgkmcnt(4)\n\t" \
    "v_mfma_f32_16x16x32_bf16 v[200:203], v[244:247], v[232:235], 0\n\t" \
    "ds_read_b128 v[244:247], %[wl] offset:6144\n\t" \
    "s_waitcnt lgkmcnt(4)\n\t" \
    "v_mfma_f32_16x16x32_bf16 v[200:203], v[248:251], v[236:239], v[200:203]\n\t" \
    "ds_read_b128 v[248:251], %[wl] offset:7168\n\t" \
    "v_mfma_f32_16x16x32_bf16 v[204:207], v[80:83], v[232:235], 0\n\t" \
    "v_mfma_f32_16x16x32_bf16 v[204:207], v[84:87], v[236:239], v[204:207]\n\t" \
    "v_mfma_f32_16x16x32_bf16 v[208:211], v[88:91], v[232:235], 0\n\t" \
    "ds_read_b128 v[232:235], v252 offset:17408\n\t" \
    "v_mfma_f32_16x16x32_bf16 v[208:211], v[92:95], v[236:239], v[208:211]\n\t" \
    "v_xor_b32 v252, 0x3000, %[xa]\n\t" \
    "ds_read_b128 v[236:239], v252 offset:16384\n\t" \
    "s_waitcnt lgkmcnt(6)\n\t" \
    "v_mfma_f32_16x16x32_bf16 v[208:211], v[112:115], v[240:243], v[208:211]\n\t" \
    "s_waitcnt lgkmcnt(5)\n\t" \
    "v_mfma_f32_16x16x32_bf16 v[208:211], v[116:119], v[224:227], v[208:211]\n\t" \
    "v_mfma_f32_16x16x32_bf16 v[204:207], v[104:107], v[240:243], v[204:207]\n\t" \
    "v_mfma_f32_16x16x32_bf16 v[204:207], v[108:111], v[224:227], v[204:207]\n\t" \
    "v_mfma_f32_16x16x32_bf16 v[200:203], v[96:99], v[240:243], v[200:203]\n\t" \
    "ds_read_b128 v[240:243], v252 offset:17408\n\t" \
    "v_mfma_f32_16x16x32_bf16 v[200:203], v[100:103], v[224:227], v[200:203]\n\t" \
    "s_nop 3\n\t" \
    "v_mov_b32 v224, %[tv]\n\t" \
    "s_waitcnt vmcnt(5)\n\t" \
    "buffer_store_dword v224, %[fv], %[rr], %[fp] offen sc1\n\t" \
    "ds_read_b128 v[224:227], v252 offset:0\n\t" \
    "s_waitcnt lgkmcnt(6)\n\t" \
    "v_mfma_f32_16x16x32_bf16 v[212:215], v[96:99], v[228:231], v[212:215]\n\t" \
    "s_waitcnt lgkmcnt(3)\n\t" \
    "v_mfma_f32_16x16x32_bf16 v[212:215], v[100:103], v[232:235], v[212:215]\n\t" \
    "v_mfma_f32_16x16x32_bf16 v[216:219], v[104:107], v[228:231], v[216:219]\n\t" \
    "v_mfma_f32_16x16x32_bf16 v[216:219], v[108:111], v[232:235], v[216:219]\n\t" \
    "v_mfma_f32_16x16x32_bf16 v[220:223], v[112:115], v[228:231], v[220:223]\n\t" \
    "ds_read_b128 v[228:231], v252 offset:1024\n\t" \
    "v_mfma_f32_16x16x32_bf16 v[220:223], v[116:119], v[232:235], v[220:223]\n\t" \
    "v_xor_b32 v252, 0x3800, %[xa]\n\t" \
    "ds_read_b128 v[232:235], v252 offset:0\n\t" \
    "s_waitcnt lgkmcnt(4)\n\t" \
    "v_mfma_f32_16x16x32_bf16 v[220:223], v[128:131], v[236:239], v[220:223]\n\t" \
    "s_waitcnt lgkmcnt(3)\n\t" \
    "v_mfma_f32_16x16x32_bf16 v[220:223], v[132:135], v[240:243], v[220:223]\n\t" \
    "v_mfma_f32_16x16x32_bf16 v[216:219], v[120:123], v[236:239], v[216:219]\n\t" \
    "v_mfma_f32_16x16x32_bf16 v[216:219], v[124:127], v[240:243], v[216:219]\n\t" \
    "v_mfma_f32_16x16x32_bf16 v[212:215], v[244:247], v[236:239], v[212:215]\n\t" \
    "ds_read_b128 v[236:239], v252 offset:1024\n\t" \
    "v_mfma_f32_16x16x32_bf16 v[212:215], v[248:251], v[240:243], v[212:215]\n\t" \
    "ds_read_b128 v[240:243], v252 offset:16384\n\t" \
    "s_waitcnt lgkmcnt(4)\n\t" \
    "v_mfma_f32_16x16x32_bf16 v[200:203], v[244:247], v[224:227], v[200:203]\n\t" \
    "ds_read_b128 v[244:247], %[wl] offset:8192\n\t" \
    "s_waitcnt lgkmcnt(4)\n\t" \
    "v_mfma_f32_16x16x32_bf16 v[200:203], v[248:251], v[228:231], v[200:203]\n\t" \
    "ds_read_b128 v[248:251], %[wl] offset:9216\n\t" \
    "v_mfma_f32_16x16x32_bf16 v[204:207], v[120:123], v[224:227], v[204:207]\n\t" \
    "v_mfma_f32_16x16x32_bf16 v[204:207], v[124:127], v[228:231], v[204:207]\n\t" \
    "v_mfma_f32_16x16x32_bf16 v[208:211], v[128:131], v[224:227], v[208:211]\n\t" \
    "ds_read_b128 v[224:227], v252 offset:17408\n\t" \
    "v_mfma_f32_16x16x32_bf16 v[208:211], v[132:135], v[228:231], v[208:211]\n\t" \
    "v_and_b32 v252, 0x3ff, %[xa]\n\t" \
    "ds_read_b128 v[228:231], v252 offset:45056\n\t" \
    "s_waitcnt lgkmcnt(6)\n\t" \
    "v_mfma_f32_16x16x32_bf16 v[208:211], v[152:155], v[232:235], v[208:211]\n\t" \
    "s_waitcnt lgkmcnt(5)\n\t" \
    "v_mfma_f32_16x16x32_bf16 v[208:211], v[156:159], v[236:239], v[208:211]\n\t" \
    "v_mfma_f32_16x16x32_bf16 v[204:207], v[144:147], v[232:235], v[204:207]\n\t" \
    "v_mfma_f32_16x16x32_bf16 v[204:207], v[148:151], v[236:239], v[204:207]\n\t" \
    "v_mfma_f32_16x16x32_bf16 v[200:203], v[136:139], v[232:235], v[200:203]\n\t" \
    "ds_read_b128 v[232:235], v252 offset:46080\n\t" \
    "v_mfma_f32_16x16x32_bf16 v[200:203], v[140:143], v[236:239], v[200:203]\n\t" \
    "ds_read_b128 v[236:239], v252 offset:40960\n\t" \
    "s_waitcnt lgkmcnt(6)\n\t" \
    "v_mfma_f32_16x16x32_bf16 v[212:215], v[136:139], v[240:243], v[212:215]\n\t" \
    "s_waitcnt lgkmcnt(3)\n\t" \
    "v_mfma_f32_16x16x32_bf16 v[212:215], v[140:143], v[224:227], v[212:215]\n\t" \
    "v_mfma_f32_16x16x32_bf16 v[216:219], v[144:147], v[240:243], v[216:219]\n\t" \
    "v_mfma_f32_16x16x32_bf16 v[216:219], v[148:151], v[224:227], v[216:219]\n\t" \
    "v_mfma_f32_16x16x32_bf16 v[220:223], v[152:155], v[240:243], v[220:223]\n\t" \
    "ds_read_b128 v[240:243], v252 offset:41984\n\t" \
    "v_mfma_f32_16x16x32_bf16 v[220:223], v[156:159], v[224:227], v[220:223]\n\t" \
    "s_waitcnt vmcnt(1) lgkmcnt(0)\n\t" \
    "s_barrier\n\t" ASM_ST \
    "ds_read_b128 v[224:227], %[xa] offset:0\n\t" \
    "v_mfma_f32_16x16x32_bf16 v[220:223], v[168:171], v[228:231], v[220:223]\n\t" \
    "v_mfma_f32_16x16x32_bf16 v[220:223], v[172:175], v[232:235], v[220:223]\n\t" \
    "v_mfma_f32_16x16x32_bf16 v[216:219], v[160:163], v[228:231], v[216:219]\n\t" \
    "v_mfma_f32_16x16x32_bf16 v[216:219], v[164:167], v[232:235], v[216:219]\n\t" \
    "v_mfma_f32_16x16x32_bf16 v[212:215], v[244:247], v[228:231], v[212:215]\n\t" \
    "ds_read_b128 v[228:231], %[xa] offset:1024\n\t" \
    "v_mfma_f32_16x16x32_bf16 v[212:215], v[248:251], v[232:235], v[212:215]\n\t" \
    "ds_read_b128 v[232:235], %[xa] offset:16384\n\t" \
    "v_mfma_f32_16x16x32_bf16 v[200:203], v[244:247], v[236:239], v[200:203]\n\t" \
    "ds_read_b128 v[244:247], %[wl] offset:0\n\t" \
    "v_mfma_f32_16x16x32_bf16 v[200:203], v[248:251], v[240:243], v[200:203]\n\t" \
    "ds_read_b128 v[248:251], %[wl] offset:1024\n\t" \
    "v_mfma_f32_16x16x32_bf16 v[204:207], v[160:163], v[236:239], v[204:207]\n\t" \
    "v_mfma_f32_16x16x32_bf16 v[204:207], v[164:167], v[240:243], v[204:207]\n\t" \
    "v_mfma_f32_16x16x32_bf16 v[208:211], v[168:171], v[236:239], v[208:211]\n\t" \
    "ds_read_b128 v[236:239], %[xa] offset:17408\n\t" \
    "v_mfma_f32_16x16x32_bf16 v[208:211], v[172:175], v[240:243], v[208:211]\n\t" \
    "v_xor_b32 v252, 0x800, %[xa]\n\t" \
    "ds_read_b128 v[240:243], v252 offset:16384\n\t" \
    "s_waitcnt lgkmcnt(6)\n\t" \
    "v_mfma_f32_16x16x32_bf16 v[208:211], v[8:11], v[224:227], v[208:211]\n\t" \
    "s_waitcnt lgkmcnt(5)\n\t" \
    "v_mfma_f32_16x16x32_bf16 v[208:211], v[12:15], v[228:231], v[208:211]\n\t" \
    "v_mfma_f32_16x16x32_bf16 v[204:207], v[0:3], v[224:227], v[204:207]\n\t" \
    "v_mfma_f32_16x16x32_bf16 v[204:207], v[4:7], v[228:231], v[204:207]\n\t" \
    "s_waitcnt lgkmcnt(3)\n\t" \
    "v_mfma_f32_16x16x32_bf16 v[200:203], v[244:247], v[224:227], v[200:203]\n\t" \
    "ds_read_b128 v[224:227], v252 offset:17408\n\t" \
    "s_waitcnt lgkmcnt(3)\n\t" \
    "v_mfma_f32_16x16x32_bf16 v[200:203], v[248:251], v[228:231], v[200:203]\n\t" \
    "ds_read_b128 v[228:231], v252 offset:0\n\t" \
    "v_mfma_f32_16x16x32_bf16 v[212:215], v[244:247], v[232:235], v[212:215]\n\t" \
    "ds_read_b128 v[244:247], %[wl] offset:2048\n\t" \
    "s_waitcnt lgkmcnt(4)\n\t" \
    "v_mfma_f32_16x16x32_bf16 v[212:215], v[248:251], v[236:239], v[212:215]\n\t" \
    "ds_read_b128 v[248:251], %[wl] offset:3072\n\t" \
    "v_mfma_f32_16x16x32_bf16 v[216:219], v[0:3], v[232:235], v[216:219]\n\t" \
    "v_mfma_f32_16x16x32_bf16 v[216:219], v[4:7], v[236:239], v[216:219]\n\t" \
    "v_mfma_f32_16x16x32_bf16 v[220:223], v[8:11], v[232:235], v[220:223]\n\t" \
    "ds_read_b128 v[232:235], v252 offset:1024\n\t" \
    "v_mfma_f32_16x16x32_bf16 v[220:223], v[12:15], v[236:239], v[220:223]\n\t" \
    "v_xor_b32 v252, 0x1000, %[xa]\n\t" \
    "ds_read_b128 v[236:239], v252 offset:0\n\t" \
    "s_waitcnt lgkmcnt(6)\n\t" \
    "v_mfma_f32_16x16x32_bf16 v[220:223], v[32:35], v[240:243], v[220:223]\n\t" \
    "s_waitcnt lgkmcnt(5)\n\t" \
    "v_mfma_f32_16x16x32_bf16 v[220:223], v[36:39], v[224:227], v[220:223]\n\t" \
    "v_mfma_f32_16x16x32_bf16 v[216:219], v[24:27], v[240:243], v[216:219]\n\t" \
    "v_mfma_f32_16x16x32_bf16 v[216:219], v[28:31], v[224:227], v[216:219]\n\t" \
    "v_mfma_f32_16x16x32_bf16 v[212:215], v[16:19], v[240:243], v[212:215]\n\t" \
    "ds_read_b128 v[240:243], v252 offset:1024\n\t" \
    "v_mfma_f32_16x16x32_bf16 v[212:215], v[20:23], v[224:227], v[212:215]\n\t" \
    "ds_read_b128 v[224:227], v252 offset:16384\n\t" \
    "s_waitcnt lgkmcnt(6)\n\t" \
    "v_mfma_f32_16x16x32_bf16 v[200:203], v[16:19], v[228:231], v[200:203]\n\t" \
    "s_waitcnt lgkmcnt(3)\n\t" \
    "v_mfma_f32_16x16x32_bf16 v[200:203], v[20:23], v[232:235], v[200:203]\n\t" \
    "v_mfma_f32_16x16x32_bf16 v[204:207], v[24:27], v[228:231], v[204:207]\n\t" \
    "v_mfma_f32_16x16x32_bf16 v[204:207], v[28:31], v[232:235], v[204:207]\n\t" \
    "v_mfma_f32_16x16x32_bf16 v[208:211], v[32:35], v[228:231], v[208:211]\n\t" \
    "ds_read_b128 v[228:231], v252 offset:17408\n\t" \
    "v_mfma_f32_16x16x32_bf16 v[208:211], v[36:39], v[232:235], v[208:211]\n\t" \
    "v_xor_b32 v252, 0x1800, %[xa]\n\t" \
    "ds_read_b128 v[232:235], v252 offset:16384\n\t" \
    "s_waitcnt lgkmcnt(4)\n\t" \
    "v_mfma_f32_16x16x32_bf16 v[208:211], v[48:51], v[236:239], v[208:211]\n\t" \
    "s_waitcnt lgkmcnt(3)\n\t" \
    "v_mfma_f32_16x16x32_bf16 v[208:211], v[52:55], v[240:243], v[208:211]\n\t" \
    "v_mfma_f32_16x16x32_bf16 v[204:207], v[40:43], v[236:239], v[204:207]\n\t" \
    "v_mfma_f32_16x16x32_bf16 v[204:207], v[44:47], v[240:243], v[204:207]\n\t" \
    "v_mfma_f32_16x16x32_bf16 v[200:203], v[244:247], v[236:239], v[200:203]\n\t" \
    "ds_read_b128 v[236:239], v252 offset:17408\n\t" \
    "v_mfma_f32_16x16x32_bf16 v[200:203], v[248:251], v[240:243], v[200:203]\n\t" \
    "ds_read_b128 v[240:243], v252 offset:0\n\t" \
    "s_waitcnt lgkmcnt(4)\n\t" \
    "v_mfma_f32_16x16x32_bf16 v[212:215], v[244:247], v[224:227], v[212:215]\n\t" \
    "s_waitcnt lgkmcnt(3)\n\t" \
    "v_mfma_f32_16x16x32_bf16 v[212:215], v[248:251], v[228:231], v[212:215]\n\t" \
    "v_mfma_f32_16x16x32_bf16 v[216:219], v[40:43], v[224:227], v[216:219]\n\t" \
    "v_mfma_f32_16x16x32_bf16 v[216:219], v[44:47], v[228:231], v[216:219]\n\t" \
    "v_mfma_f32_16x16x32_bf16 v[220:223], v[48:51], v[224:227], v[220:223]\n\t" \
    "ds_read_b128 v[224:227], v252 offset:1024\n\t" \
    "v_mfma_f32_16x16x32_bf16 v[220:223], v[52:55], v[228:231], v[220:223]\n\t" \
    "v_and_b32 v252, 0x3ff, %[xa]\n\t" \
    "ds_read_b128 v[228:231], v252 offset:43008\n\t" \
    "s_waitcnt lgkmcnt(4)\n\t" \
    "v_mfma_f32_16x16x32_bf16 v[220:223], v[72:75], v[232:235], v[220:223]\n\t" \
    "s_waitcnt lgkmcnt(3)\n\t" \
    "v_mfma_f32_16x16x32_bf16 v[220:223], v[76:79], v[236:239], v[220:223]\n\t" \
    "v_mfma_f32_16x16x32_bf16 v[216:219], v[64:67], v[232:235], v[216:219]\n\t" \
    "v_mfma_f32_16x16x32_bf16 v[216:219], v[68:71], v[236:239], v[216:219]\n\t" \
    "v_mfma_f32_16x16x32_bf16 v[212:215], v[56:59], v[232:235], v[212:215]\n\t" \
    "ds_read_b128 v[232:235], v252 offset:44032\n\t" \
    "v_mfma_f32_16x16x32_bf16 v[212:215], v[60:63], v[236:239], v[212:215]\n\t" \
    "ds_read_b128 v[236:239], v252 offset:47104\n\t" \
    "s_waitcnt lgkmcnt(4)\n\t" \
    "v_mfma_f32_16x16x32_bf16 v[200:203], v[56:59], v[240:243], v[200:203]\n\t" \
    "s_waitcnt lgkmcnt(3)\n\t" \
    "v_mfma_f32_16x16x32_bf16 v[200:203], v[60:63], v[224:227], v[200:203]\n\t" \
    "v_mfma_f32_16x16x32_bf16 v[204:207], v[64:67], v[240:243], v[204:207]\n\t" \
    "v_mfma_f32_16x16x32_bf16 v[204:207], v[68:71], v[224:227], v[204:207]\n\t" \
    "v_mfma_f32_16x16x32_bf16 v[208:211], v[72:75], v[240:243], v[208:211]\n\t" \
    "ds_read_b128 v[240:243], v252 offset:48128\n\t" \
    "v_mfma_f32_16x16x32_bf16 v[208:211], v[76:79], v[224:227], v[208:211]\n\t" \
    "s_waitcnt lgkmcnt(3)\n\t" \
    "v_mfma_f32_16x16x32_bf16 v[208:211], v[192:195], v[228:231], v[208:211]\n\t" \
    "v_add_u32 v252, %[qs], v252\n\t" \
    "s_waitcnt lgkmcnt(2)\n\t" \
    "v_mfma_f32_16x16x32_bf16 v[208:211], v[196:199], v[232:235], v[208:211]\n\t" \
    "v_mfma_f32_16x16x32_bf16 v[204:207], v[184:187], v[228:231], v[204:207]\n\t" \
    "v_mfma_f32_16x16x32_bf16 v[204:207], v[188:191], v[232:235], v[204:207]\n\t" \
    "v_mfma_f32_16x16x32_bf16 v[200:203], v[176:179], v[228:231], v[200:203]\n\t" \
    "v_mfma_f32_16x16x32_bf16 v[200:203], v[180:183], v[232:235], v[200:203]\n\t" \
    "s_waitcnt lgkmcnt(1)\n\t" \
    "v_mfma_f32_16x16x32_bf16 v[212:215], v[176:179], v[236:239], v[212:215]\n\t" \
    "v_cvt_pk_bf16_f32 v208, v208, v209\n\t" \
    "v_cvt_pk_bf16_f32 v209, v210, v211\n\t" \
    "s_waitcnt lgkmcnt(0)\n\t" \
    "v_mfma_f32_16x16x32_bf16 v[212:215], v[180:183], v[240:243], v[212:215]\n\t" \
    "ds_write_b64 v252, v[208:209] offset:2048\n\t" \
    "v_cvt_pk_bf16_f32 v204, v204, v205\n\t" \
    "v_mfma_f32_16x16x32_bf16 v[216:219], v[184:187], v[236:239], v[216:219]\n\t" \
    "v_cvt_pk_bf16_f32 v205, v206, v207\n\t" \
    "ds_write_b64 v252, v[204:205] offset:1024\n\t" \
    "v_mfma_f32_16x16x32_bf16 v[216:219], v[188:191], v[240:243], v[216:219]\n\t" \
    "v_cvt_pk_bf16_f32 v200, v200, v201\n\t" \
    "v_cvt_pk_bf16_f32 v201, v202, v203\n\t" \
    "v_mfma_f32_16x16x32_bf16 v[220:223], v[192:195], v[236:239], v[220:223]\n\t" \
    "ds_write_b64 v252, v[200:201] offset:0\n\t" \
    "v_mfma_f32_16x16x32_bf16 v[220:223], v[196:199], v[240:243], v[220:223]\n\t" \
    "v_cvt_pk_bf16_f32 v212, v212, v213\n\t" \
    "v_cvt_pk_bf16_f32 v213, v214, v215\n\t" \
    "ds_write_b64 v252, v[212:213] offset:6144\n\t" \
    "v_cvt_pk_bf16_f32 v216, v216, v217\n\t" \
    "v_cvt_pk_bf16_f32 v217, v218, v219\n\t" \
    "ds_write_b64 v252, v[216:217] offset:7168\n\t" \
    "s_nop 1\n\t" \
    "v_cvt_pk_bf16_f32 v220, v220, v221\n\t" \
    "v_cvt_pk_bf16_f32 v221, v222, v223\n\t" \
    "ds_write_b64 v252, v[220:221] offset:8192\n\t" \
    "s_waitcnt lgkmcnt(0)\n\t"
#define ASM_CRIT1_3 \
    "s_branch .Lc0%=\n\t" \
    ".Lcr%=:\n\t" \
    "s_cmp_eq_u32 %[n], 0\n\t" \
    "s_cbranch_scc1 .Lcb%=\n\t" \
    "s_sub_u32 %[n], %[n], 1\n\t" \
    "v_max_u32 v243, v208, v209\n\t" \
    "v_max3_u32 v243, v210, v211, v243\n\t" \
    "v_cmp_eq_u32 vcc, -1, v243\n\t" \
    "s_cbranch_vccz .Lcs0_%=\n\t" \
    "buffer_load_dwordx4 v[208:211], %[ab], %[rs], %[s0] offen sc1\n\t" \
    ".Lcs0_%=:\n\t" \
    "v_max_u32 v243, v236, v237\n\t" \
    "v_max3_u32 v243, v238, v239, v243\n\t" \
    "v_cmp_eq_u32 vcc, -1, v243\n\t" \
    "s_cbranch_vccz .Lcs1_%=\n\t" \
    "buffer_load_dwordx4 v[236:239], %[ab], %[rs], %[s1] offen sc1\n\t" \
    ".Lcs1_%=:\n\t" \
    "v_max_u32 v243, v220, v221\n\t" \
    "v_max3_u32 v243, v222, v223, v243\n\t" \
    "v_cmp_eq_u32 vcc, -1, v243\n\t" \
    "s_cbranch_vccz .Lca%=\n\t" \
    "buffer_load_dwordx4 v[220:223], %[ab], %[rs], %[s2] offen sc1\n\t" \
    "s_branch .Lca%=\n\t" \
    ".Lc0%=:\n\t" \
    "buffer_load_dwordx4 v[208:211], %[ab], %[rs], %[s0] offen sc1\n\t" \
    "buffer_load_dwordx4 v[236:239], %[ab], %[rs], %[s1] offen sc1\n\t" \
    "buffer_load_dwordx4 v[220:223], %[ab], %[rs], %[s2] offen sc1\n\t" \
    "v_bfe_i32 v241, %[ab], 7, 1\n\t" \
    "v_and_b32 v241, 0xaaaa, v241\n\t" \
    "v_or_b32 v241, 0x4444, v241\n\t" \
    "v_xor_b32 v212, 0x400, %[ab]\n\t" \
    "v_xor_b32 v216, 0x800, %[ab]\n\t" \
    "v_xor_b32 v242, 0xc00, %[ab]\n\t" \
    "v_xor_b32 v240, 0x1400, %[ab]\n\t" \
    ".Lca%=:\n\t" \
    "s_waitcnt vmcnt(0)\n\t" \
    "ds_write_b128 %[ab], v[208:211] offset:0\n\t" \
    "ds_write_b128 %[ab], v[236:239] offset:4096\n\t" \
    "ds_write_b128 %[ab], v[220:223] offset:8192\n\t" \
    "v_max_u32 v243, v208, v209\n\t" \
    "v_max3_u32 v243, v210, v211, v243\n\t" \
    "v_max3_u32 v243, v236, v237, v243\n\t" \
    "v_max3_u32 v243, v238, v239, v243\n\t" \
    "v_max3_u32 v243, v220, v221, v243\n\t" \
    "v_max3_u32 v243, v222, v223, v243\n\t" \
    "v_cmp_eq_u32 vcc, -1, v243\n\t" \
    "s_cbranch_vccnz .Lcr%=\n\t" \
    ".Lcb%=:\n\t" \
    PRIUP \
    ASM_ST2 \
    "v_smfmac_f32_16x16x64_bf16 v[224:227], v[208:211], v[200:207], v241\n\t" \
    "ds_read_b128 v[200:203], %[wl] offset:4096\n\t" \
    "ds_read_b128 v[204:207], %[wl] offset:5120\n\t" \
    "v_smfmac_f32_16x16x64_bf16 v[244:247], v[208:211], v[0:7], v241\n\t" \
    "v_smfmac_f32_16x16x64_bf16 v[248:251], v[208:211], v[8:15], v241\n\t" \
    "s_waitcnt lgkmcnt(2)\n\t" \
    "s_barrier\n\t" ASM_ST \
    "ds_read_b128 v[212:215], v212 offset:0\n\t" \
    "ds_read_b128 v[216:219], v216 offset:0\n\t" \
    "ds_read_b128 v[220:223], v242 offset:0\n\t" \
    "ds_read_b128 v[208:211], v240 offset:0\n\t" \
    "v_smfmac_f32_16x16x64_bf16 v[244:247], v[236:239], v[80:87], v241\n\t" \
    "v_xor_b32 v240, 0x1800, %[ab]\n\t" \
    "v_smfmac_f32_16x16x64_bf16 v[248:251], v[236:239], v[88:95], v241\n\t" \
    "s_waitcnt lgkmcnt(4)\n\t" \
    "v_smfmac_f32_16x16x64_bf16 v[224:227], v[236:239], v[200:207], v241\n\t" \
    "ds_read_b128 v[200:203], %[wl] offset:6144\n\t" \
    "ds_read_b128 v[204:207], %[wl] offset:7168\n\t" \
    "ds_read_b128 v[236:239], v240 offset:0\n\t" \
    "s_waitcnt lgkmcnt(6)\n\t" \
    "v_smfmac_f32_16x16x64_bf16 v[224:227], v[212:215], v[16:23], v241\n\t" \
    "v_smfmac_f32_16x16x64_bf16 v[244:247], v[212:215], v[24:31], v241\n\t" \
    "v_xor_b32 v240, 0x1c00, %[ab]\n\t" \
    "v_smfmac_f32_16x16x64_bf16 v[248:251], v[212:215], v[32:39], v241\n\t" \
    "ds_read_b128 v[212:215], v240 offset:0\n\t" \
    "s_waitcnt lgkmcnt(6)\n\t" \
    "v_smfmac_f32_16x16x64_bf16 v[224:227], v[216:219], v[228:235], v241\n\t" \
    "ds_read_b128 v[228:231], %[wl] offset:8192\n\t" \
    "ds_read_b128 v[232:235], %[wl] offset:9216\n\t" \
    "v_smfmac_f32_16x16x64_bf16 v[244:247], v[216:219], v[40:47], v241\n\t" \
    "v_and_b32 v240, 0x3ff, %[ab]\n\t" \
    "v_smfmac_f32_16x16x64_bf16 v[248:251], v[216:219], v[48:55], v241\n\t" \
    "ds_read_b128 v[216:219], v240 offset:8192\n\t" \
    "s_waitcnt lgkmcnt(8)\n\t" \
    "v_smfmac_f32_16x16x64_bf16 v[224:227], v[220:223], v[56:63], v241\n\t" \
    "v_smfmac_f32_16x16x64_bf16 v[244:247], v[220:223], v[64:71], v241\n\t" \
    "v_smfmac_f32_16x16x64_bf16 v[248:251], v[220:223], v[72:79], v241\n\t" \
    "ds_read_b128 v[220:223], v240 offset:9216\n\t" \
    "s_waitcnt lgkmcnt(8)\n\t" \
    "v_smfmac_f32_16x16x64_bf16 v[244:247], v[208:211], v[104:111], v241\n\t" \
    "s_waitcnt lgkmcnt(5)\n\t" \
    "v_smfmac_f32_16x16x64_bf16 v[244:247], v[236:239], v[120:127], v241\n\t" \
    "s_waitcnt lgkmcnt(4)\n\t" \
    "v_smfmac_f32_16x16x64_bf16 v[244:247], v[212:215], v[144:151], v241\n\t" \
    "s_waitcnt lgkmcnt(1)\n\t" \
    "v_smfmac_f32_16x16x64_bf16 v[244:247], v[216:219], v[160:167], v241\n\t" \
    "s_waitcnt lgkmcnt(0)\n\t" \
    "v_smfmac_f32_16x16x64_bf16 v[244:247], v[220:223], v[184:191], v241\n\t" \
    "v_smfmac_f32_16x16x64_bf16 v[248:251], v[208:211], v[112:119], v241\n\t" \
    "v_smfmac_f32_16x16x64_bf16 v[248:251], v[236:239], v[128:135], v241\n\t" \
    "v_smfmac_f32_16x16x64_bf16 v[248:251], v[212:215], v[152:159], v241\n\t" \
    "v_permlane32_swap_b32 v244, v246\n\t" \
    "v_permlane32_swap_b32 v245, v247\n\t" \
    "v_smfmac_f32_16x16x64_bf16 v[248:251], v[216:219], v[168:175], v241\n\t" \
    "v_add_f32 v244, v244, v246\n\t" \
    "v_add_f32 v245, v245, v247\n\t" \
    "v_smfmac_f32_16x16x64_bf16 v[248:251], v[220:223], v[192:199], v241\n\t" \
    "v_cvt_pk_bf16_f32 v244, v244, v245\n\t" \
    "ds_write_b16 %[dst], v244 offset:512\n\t" \
    "ds_write_b16_d16_hi %[dst], v244 offset:528\n\t" \
    "v_smfmac_f32_16x16x64_bf16 v[224:227], v[208:211], v[96:103], v241\n\t" \
    "v_smfmac_f32_16x16x64_bf16 v[224:227], v[236:239], v[200:207], v241\n\t" \
    "v_smfmac_f32_16x16x64_bf16 v[224:227], v[212:215], v[136:143], v241\n\t" \
    "v_permlane32_swap_b32 v248, v250\n\t" \
    "v_permlane32_swap_b32 v249, v251\n\t" \
    "v_smfmac_f32_16x16x64_bf16 v[224:227], v[216:219], v[228:235], v241\n\t" \
    "v_add_f32 v248, v248, v250\n\t" \
    "v_add_f32 v249, v249, v251\n\t" \
    "v_smfmac_f32_16x16x64_bf16 v[224:227], v[220:223], v[176:183], v241\n\t" \
    "v_cvt_pk_bf16_f32 v248, v248, v249\n\t" \
    "ds_write_b16 %[dst], v248 offset:1024\n\t" \
    "ds_write_b16_d16_hi %[dst], v248 offset:1040\n\t" \
    "s_nop 4\n\t" \
    "v_permlane32_swap_b32 v224, v226\n\t" \
    "v_permlane32_swap_b32 v225, v227\n\t" \
    "v_add_f32 v224, v224, v226\n\t" \
    "v_add_f32 v225, v225, v227\n\t" \
    "v_cvt_pk_bf16_f32 v224, v224, v225\n\t" \
    "ds_write_b16 %[dst], v224 offset:0\n\t" \
    "ds_write_b16_d16_hi %[dst], v224 offset:16\n\t"
#define ASM_CRIT1_2 \
    "s_branch .Lc0%=\n\t" \
    ".Lcr%=:\n\t" \
    "s_cmp_eq_u32 %[n], 0\n\t" \
    "s_cbranch_scc1 .Lcb%=\n\t" \
    "s_sub_u32 %[n], %[n], 1\n\t" \
    "v_max_u32 v243, v208, v209\n\t" \
    "v_max3_u32 v243, v210, v211, v243\n\t" \
    "v_cmp_eq_u32 vcc, -1, v243\n\t" \
    "s_cbranch_vccz .Lcs0_%=\n\t" \
    "buffer_load_dwordx4 v[208:211], %[ab], %[rs], %[s0] offen sc1\n\t" \
    ".Lcs0_%=:\n\t" \
    "v_max_u32 v243, v236, v237\n\t" \
    "v_max3_u32 v243, v238, v239, v243\n\t" \
    "v_cmp_eq_u32 vcc, -1, v243\n\t" \
    "s_cbranch_vccz .Lca%=\n\t" \
    "buffer_load_dwordx4 v[236:239], %[ab], %[rs], %[s1] offen sc1\n\t" \
    "s_branch .Lca%=\n\t" \
    ".Lc0%=:\n\t" \
    "buffer_load_dwordx4 v[208:211], %[ab], %[rs], %[s0] offen sc1\n\t" \
    "buffer_load_dwordx4 v[236:239], %[ab], %[rs], %[s1] offen sc1\n\t" \
    "v_bfe_i32 v241, %[ab], 7, 1\n\t" \
    "v_and_b32 v241, 0xaaaa, v241\n\t" \
    "v_or_b32 v241, 0x4444, v241\n\t" \
    "v_xor_b32 v212, 0x400, %[ab]\n\t" \
    "v_xor_b32 v216, 0x800, %[ab]\n\t" \
    "v_xor_b32 v242, 0xc00, %[ab]\n\t" \
    "v_xor_b32 v240, 0x1400, %[ab]\n\t" \
    ".Lca%=:\n\t" \
    "s_waitcnt vmcnt(0)\n\t" \
    "ds_write_b128 %[ab], v[208:211] offset:0\n\t" \
    "ds_write_b128 %[ab], v[236:239] offset:4096\n\t" \
    "v_max_u32 v243, v208, v209\n\t" \
    "v_max3_u32 v243, v210, v211, v243\n\t" \
    "v_max3_u32 v243, v236, v237, v243\n\t" \
    "v_max3_u32 v243, v238, v239, v243\n\t" \
    "v_cmp_eq_u32 vcc, -1, v243\n\t" \
    "s_cbranch_vccnz .Lcr%=\n\t" \
    ".Lcb%=:\n\t" \
    PRIUP \
    ASM_ST2 \
    "v_smfmac_f32_16x16x64_bf16 v[224:227], v[208:211], v[200:207], v241\n\t" \
    "ds_read_b128 v[200:203], %[wl] offset:4096\n\t" \
    "ds_read_b128 v[204:207], %[wl] offset:5120\n\t" \
    "v_smfmac_f32_16x16x64_bf16 v[244:247], v[208:211], v[0:7], v241\n\t" \
    "v_smfmac_f32_16x16x64_bf16 v[248:251], v[208:211], v[8:15], v241\n\t" \
    "s_waitcnt lgkmcnt(2)\n\t" \
    "s_barrier\n\t" ASM_ST \
    "ds_read_b128 v[212:215], v212 offset:0\n\t" \
    "ds_read_b128 v[216:219], v216 offset:0\n\t" \
    "ds_read_b128 v[220:223], v242 offset:0\n\t" \
    "ds_read_b128 v[208:211], v240 offset:0\n\t" \
    "v_smfmac_f32_16x16x64_bf16 v[244:247], v[236:239], v[80:87], v241\n\t" \
    "v_xor_b32 v240, 0x1800, %[ab]\n\t" \
    "v_smfmac_f32_16x16x64_bf16 v[248:251], v[236:239], v[88:95], v241\n\t" \
    "s_waitcnt lgkmcnt(4)\n\t" \
    "v_smfmac_f32_16x16x64_bf16 v[224:227], v[236:239], v[200:207], v241\n\t" \
    "ds_read_b128 v[200:203], %[wl] offset:6144\n\t" \
    "ds_read_b128 v[204:207], %[wl] offset:7168\n\t" \
    "ds_read_b128 v[236:239], v240 offset:0\n\t" \
    "s_waitcnt lgkmcnt(6)\n\t" \
    "v_smfmac_f32_16x16x64_bf16 v[224:227], v[212:215], v[16:23], v241\n\t" \
    "v_smfmac_f32_16x16x64_bf16 v[244:247], v[212:215], v[24:31], v241\n\t" \
    "v_xor_b32 v240, 0x1c00, %[ab]\n\t" \
    "v_smfmac_f32_16x16x64_bf16 v[248:251], v[212:215], v[32:39], v241\n\t" \
    "ds_read_b128 v[212:215], v240 offset:0\n\t" \
    "s_waitcnt lgkmcnt(6)\n\t" \
    "v_smfmac_f32_16x16x64_bf16 v[224:227], v[216:219], v[228:235], v241\n\t" \
    "ds_read_b128 v[228:231], %[wl] offset:8192\n\t" \
    "ds_read_b128 v[232:235], %[wl] offset:9216\n\t" \
    "v_smfmac_f32_16x16x64_bf16 v[244:247], v[216:219], v[40:47], v241\n\t" \
    "v_and_b32 v240, 0x3ff, %[ab]\n\t" \
    "v_smfmac_f32_16x16x64_bf16 v[248:251], v[216:219], v[48:55], v241\n\t" \
    "ds_read_b128 v[216:219], v240 offset:8192\n\t" \
    "s_waitcnt lgkmcnt(8)\n\t" \
    "v_smfmac_f32_16x16x64_bf16 v[224:227], v[220:223], v[56:63], v241\n\t" \
    "v_smfmac_f32_16x16x64_bf16 v[244:247], v[220:223], v[64:71], v241\n\t" \
    "v_smfmac_f32_16x16x64_bf16 v[248:251], v[220:223], v[72:79], v241\n\t" \
    "ds_read_b128 v[220:223], v240 offset:9216\n\t" \
    "s_waitcnt lgkmcnt(8)\n\t" \
    "v_smfmac_f32_16x16x64_bf16 v[244:247], v[208:211], v[104:111], v241\n\t" \
    "s_waitcnt lgkmcnt(5)\n\t" \
    "v_smfmac_f32_16x16x64_bf16 v[244:247], v[236:239], v[120:127], v241\n\t" \
    "s_waitcnt lgkmcnt(4)\n\t" \
    "v_smfmac_f32_16x16x64_bf16 v[244:247], v[212:215], v[144:151], v241\n\t" \
    "s_waitcnt lgkmcnt(1)\n\t" \
    "v_smfmac_f32_16x16x64_bf16 v[244:247], v[216:219], v[160:167], v241\n\t" \
    "s_waitcnt lgkmcnt(0)\n\t" \
    "v_smfmac_f32_16x16x64_bf16 v[244:247], v[220:223], v[184:191], v241\n\t" \
    "v_smfmac_f32_16x16x64_bf16 v[248:251], v[208:211], v[112:119], v241\n\t" \
    "v_smfmac_f32_16x16x64_bf16 v[248:251], v[236:239], v[128:135], v241\n\t" \
    "v_smfmac_f32_16x16x64_bf16 v[248:251], v[212:215], v[152:159], v241\n\t" \
    "v_permlane32_swap_b32 v244, v246\n\t" \
    "v_permlane32_swap_b32 v245, v247\n\t" \
    "v_smfmac_f32_16x16x64_bf16 v[248:251], v[216:219], v[168:175], v241\n\t" \
    "v_add_f32 v244, v244, v246\n\t" \
    "v_add_f32 v245, v245, v247\n\t" \
    "v_smfmac_f32_16x16x64_bf16 v[248:251], v[220:223], v[192:199], v241\n\t" \
    "v_cvt_pk_bf16_f32 v244, v244, v245\n\t" \
    "ds_write_b16 %[dst], v244 offset:512\n\t" \
    "ds_write_b16_d16_hi %[dst], v244 offset:528\n\t" \
    "v_smfmac_f32_16x16x64_bf16 v[224:227], v[208:211], v[96:103], v241\n\t" \
    "v_smfmac_f32_16x16x64_bf16 v[224:227], v[236:239], v[200:207], v241\n\t" \
    "v_smfmac_f32_16x16x64_bf16 v[224:227], v[212:215], v[136:143], v241\n\t" \
    "v_permlane32_swap_b32 v248, v250\n\t" \
    "v_permlane32_swap_b32 v249, v251\n\t" \
    "v_smfmac_f32_16x16x64_bf16 v[224:227], v[216:219], v[228:235], v241\n\t" \
    "v_add_f32 v248, v248, v250\n\t" \
    "v_add_f32 v249, v249, v251\n\t" \
    "v_smfmac_f32_16x16x64_bf16 v[224:227], v[220:223], v[176:183], v241\n\t" \
    "v_cvt_pk_bf16_f32 v248, v248, v249\n\t" \
    "ds_write_b16 %[dst], v248 offset:1024\n\t" \
    "ds_write_b16_d16_hi %[dst], v248 offset:1040\n\t" \
    "s_nop 4\n\t" \
    "v_permlane32_swap_b32 v224, v226\n\t" \
    "v_permlane32_swap_b32 v225, v227\n\t" \
    "v_add_f32 v224, v224, v226\n\t" \
    "v_add_f32 v225, v225, v227\n\t" \
    "v_cvt_pk_bf16_f32 v224, v224, v225\n\t" \
    "ds_write_b16 %[dst], v224 offset:0\n\t" \
    "ds_write_b16_d16_hi %[dst], v224 offset:16\n\t"
#define ASM_CRIT1_N \
    "v_bfe_i32 v241, %[ab], 7, 1\n\t" \
    "v_and_b32 v241, 0xaaaa, v241\n\t" \
    "v_or_b32 v241, 0x4444, v241\n\t" \
    "v_xor_b32 v212, 0x400, %[ab]\n\t" \
    "v_xor_b32 v216, 0x800, %[ab]\n\t" \
    "v_xor_b32 v242, 0xc00, %[ab]\n\t" \
    "v_xor_b32 v240, 0x1400, %[ab]\n\t" \
    "v_xor_b32 v236, 0x1000, %[ab]\n\t" \
    "s_waitcnt lgkmcnt(0)\n\t" \
    "s_barrier\n\t" ASM_ST \
    "ds_read_b128 v[208:211], %[ab] offset:0\n\t" \
    "ds_read_b128 v[212:215], v212 offset:0\n\t" \
    "ds_read_b128 v[216:219], v216 offset:0\n\t" \
    "ds_read_b128 v[220:223], v242 offset:0\n\t" \
    "ds_read_b128 v[236:239], v236 offset:0\n\t" \
    "s_waitcnt lgkmcnt(4)\n\t" \
    "v_smfmac_f32_16x16x64_bf16 v[224:227], v[208:211], v[200:207], v241\n\t" \
    "ds_read_b128 v[200:203], %[wl] offset:4096\n\t" \
    "ds_read_b128 v[204:207], %[wl] offset:5120\n\t" \
    "v_smfmac_f32_16x16x64_bf16 v[244:247], v[208:211], v[0:7], v241\n\t" \
    "v_smfmac_f32_16x16x64_bf16 v[248:251], v[208:211], v[8:15], v241\n\t" \
    "ds_read_b128 v[208:211], v240 offset:0\n\t" \
    "s_waitcnt lgkmcnt(6)\n\t" \
    "v_smfmac_f32_16x16x64_bf16 v[224:227], v[212:215], v[16:23], v241\n\t" \
    "v_smfmac_f32_16x16x64_bf16 v[244:247], v[212:215], v[24:31], v241\n\t" \
    "v_xor_b32 v240, 0x1800, %[ab]\n\t" \
    "v_smfmac_f32_16x16x64_bf16 v[248:251], v[212:215], v[32:39], v241\n\t" \
    "ds_read_b128 v[212:215], v240 offset:0\n\t" \
    "s_waitcnt lgkmcnt(6)\n\t" \
    "v_smfmac_f32_16x16x64_bf16 v[224:227], v[216:219], v[228:235], v241\n\t" \
    "ds_read_b128 v[228:231], %[wl] offset:6144\n\t" \
    "ds_read_b128 v[232:235], %[wl] offset:7168\n\t" \
    "v_smfmac_f32_16x16x64_bf16 v[244:247], v[216:219], v[40:47], v241\n\t" \
    "v_xor_b32 v240, 0x1c00, %[ab]\n\t" \
    "v_smfmac_f32_16x16x64_bf16 v[248:251], v[216:219], v[48:55], v241\n\t" \
    "ds_read_b128 v[216:219], v240 offset:0\n\t" \
    "s_waitcnt lgkmcnt(8)\n\t" \
    "v_smfmac_f32_16x16x64_bf16 v[224:227], v[220:223], v[56:63], v241\n\t" \
    "v_smfmac_f32_16x16x64_bf16 v[244:247], v[220:223], v[64:71], v241\n\t" \
    "v_and_b32 v240, 0x3ff, %[ab]\n\t" \
    "v_smfmac_f32_16x16x64_bf16 v[248:251], v[220:223], v[72:79], v241\n\t" \
    "ds_read_b128 v[220:223], v240 offset:8192\n\t" \
    "s_waitcnt lgkmcnt(6)\n\t" \
    "v_smfmac_f32_16x16x64_bf16 v[224:227], v[236:239], v[200:207], v241\n\t" \
    "ds_read_b128 v[200:203], %[wl] offset:8192\n\t" \
    "ds_read_b128 v[204:207], %[wl] offset:9216\n\t" \
    "v_smfmac_f32_16x16x64_bf16 v[244:247], v[236:239], v[80:87], v241\n\t" \
    "v_smfmac_f32_16x16x64_bf16 v[248:251], v[236:239], v[88:95], v241\n\t" \
    "ds_read_b128 v[236:239], v240 offset:9216\n\t" \
    "s_waitcnt lgkmcnt(8)\n\t" \
    "v_smfmac_f32_16x16x64_bf16 v[244:247], v[208:211], v[104:111], v241\n\t" \
    "s_waitcnt lgkmcnt(7)\n\t" \
    "v_smfmac_f32_16x16x64_bf16 v[244:247], v[212:215], v[120:127], v241\n\t" \
    "s_waitcnt lgkmcnt(4)\n\t" \
    "v_smfmac_f32_16x16x64_bf16 v[244:247], v[216:219], v[144:151], v241\n\t" \
    "s_waitcnt lgkmcnt(3)\n\t" \
    "v_smfmac_f32_16x16x64_bf16 v[244:247], v[220:223], v[160:167], v241\n\t" \
    "s_waitcnt lgkmcnt(0)\n\t" \
    "v_smfmac_f32_16x16x64_bf16 v[244:247], v[236:239], v[184:191], v241\n\t" \
    "v_smfmac_f32_16x16x64_bf16 v[248:251], v[208:211], v[112:119], v241\n\t" \
    "v_smfmac_f32_16x16x64_bf16 v[248:251], v[212:215], v[128:135], v241\n\t" \
    "v_smfmac_f32_16x16x64_bf16 v[248:251], v[216:219], v[152:159], v241\n\t" \
    "v_permlane32_swap_b32 v244, v246\n\t" \
    "v_permlane32_swap_b32 v245, v247\n\t" \
    "v_smfmac_f32_16x16x64_bf16 v[248:251], v[220:223], v[168:175], v241\n\t" \
    "v_add_f32 v244, v244, v246\n\t" \
    "v_add_f32 v245, v245, v247\n\t" \
    "v_smfmac_f32_16x16x64_bf16 v[248:251], v[236:239], v[192:199], v241\n\t" \
    "v_cvt_pk_bf16_f32 v244, v244, v245\n\t" \
    "ds_write_b16 %[dst], v244 offset:512\n\t" \
    "ds_write_b16_d16_hi %[dst], v244 offset:528\n\t" \
    "v_smfmac_f32_16x16x64_bf16 v[224:227], v[208:211], v[96:103], v241\n\t" \
    "v_smfmac_f32_16x16x64_bf16 v[224:227], v[212:215], v[228:235], v241\n\t" \
    "v_smfmac_f32_16x16x64_bf16 v[224:227], v[216:219], v[136:143], v241\n\t" \
    "v_permlane32_swap_b32 v248, v250\n\t" \
    "v_permlane32_swap_b32 v249, v251\n\t" \
    "v_smfmac_f32_16x16x64_bf16 v[224:227], v[220:223], v[200:207], v241\n\t" \
    "v_add_f32 v248, v248, v250\n\t" \
    "v_add_f32 v249, v249, v251\n\t" \
    "v_smfmac_f32_16x16x64_bf16 v[224:227], v[236:239], v[176:183], v241\n\t" \
    "v_cvt_pk_bf16_f32 v248, v248, v249\n\t" \
    "ds_write_b16 %[dst], v248 offset:1024\n\t" \
    "ds_write_b16_d16_hi %[dst], v248 offset:1040\n\t" \
    "s_nop 4\n\t" \
    "v_permlane32_swap_b32 v224, v226\n\t" \
    "v_permlane32_swap_b32 v225, v227\n\t" \
    "v_add_f32 v224, v224, v226\n\t" \
    "v_add_f32 v225, v225, v227\n\t" \
    "v_cvt_pk_bf16_f32 v224, v224, v225\n\t" \
    "ds_write_b16 %[dst], v224 offset:0\n\t" \
    "ds_write_b16_d16_hi %[dst], v224 offset:16\n\t"
#define ASM_CRIT1_V3 \
    "s_branch .Lc0%=\n\t" \
    ".Lcr%=:\n\t" \
    "s_cmp_eq_u32 %[n], 0\n\t" \
    "s_cbranch_scc1 .Lcb%=\n\t" \
    "s_sub_u32 %[n], %[n], 1\n\t" \
    "v_max_u32 v243, v208, v209\n\t" \
    "v_max3_u32 v243, v210, v211, v243\n\t" \
    "v_cmp_eq_u32 vcc, -1, v243\n\t" \
    "s_cbranch_vccz .Lcs0_%=\n\t" \
    "buffer_load_dwordx4 v[208:211], %[ab], %[rs], %[s0] offen sc1\n\t" \
    ".Lcs0_%=:\n\t" \
    "v_max_u32 v243, v236, v237\n\t" \
    "v_max3_u32 v243, v238, v239, v243\n\t" \
    "v_cmp_eq_u32 vcc, -1, v243\n\t" \
    "s_cbranch_vccz .Lcs1_%=\n\t" \
    "buffer_load_dwordx4 v[236:239], %[ab], %[rs], %[s1] offen sc1\n\t" \
    ".Lcs1_%=:\n\t" \
    "v_max_u32 v243, v220, v221\n\t" \
    "v_max3_u32 v243, v222, v223, v243\n\t" \
    "v_cmp_eq_u32 vcc, -1, v243\n\t" \
    "s_cbranch_vccz .Lca%=\n\t" \
    "buffer_load_dwordx4 v[220:223], %[ab], %[rs], %[s2] offen sc1\n\t" \
    "s_branch .Lca%=\n\t" \
    ".Lc0%=:\n\t" \
    "buffer_load_dwordx4 v[208:211], %[ab], %[rs], %[s0] offen sc1\n\t" \
    "buffer_load_dwordx4 v[236:239], %[ab], %[rs], %[s1] offen sc1\n\t" \
    "buffer_load_dwordx4 v[220:223], %[ab], %[rs], %[s2] offen sc1\n\t" \
    "v_bfe_i32 v241, %[ab], 7, 1\n\t" \
    "v_and_b32 v241, 0xaaaa, v241\n\t" \
    "v_or_b32 v241, 0x4444, v241\n\t" \
    "v_xor_b32 v212, 0x400, %[ab]\n\t" \
    "v_xor_b32 v216, 0x800, %[ab]\n\t" \
    "v_xor_b32 v242, 0xc00, %[ab]\n\t" \
    "v_xor_b32 v240, 0x1400, %[ab]\n\t" \
    ".Lca%=:\n\t" \
    "s_waitcnt vmcnt(0)\n\t" \
    "ds_write_b128 %[ab], v[208:211] offset:0\n\t" \
    "ds_write_b128 %[ab], v[236:239] offset:4096\n\t" \
    "ds_write_b128 %[ab], v[220:223] offset:8192\n\t" \
    "v_max_u32 v243, v208, v209\n\t" \
    "v_max3_u32 v243, v210, v211, v243\n\t" \
    "v_max3_u32 v243, v236, v237, v243\n\t" \
    "v_max3_u32 v243, v238, v239, v243\n\t" \
    "v_max3_u32 v243, v220, v221, v243\n\t" \
    "v_max3_u32 v243, v222, v223, v243\n\t" \
    "v_cmp_eq_u32 vcc, -1, v243\n\t" \
    "s_cbranch_vccnz .Lcr%=\n\t" \
    ".Lcb%=:\n\t" \
    PRIUP \
    ASM_ST2 \
    "v_smfmac_f32_16x16x64_bf16 v[224:227], v[208:211], v[200:207], v241\n\t" \
    "ds_read_b128 v[200:203], %[wl] offset:4096\n\t" \
    "ds_read_b128 v[204:207], %[wl] offset:5120\n\t" \
    "v_smfmac_f32_16x16x64_bf16 v[244:247], v[208:211], v[0:7], v241\n\t" \
    "v_smfmac_f32_16x16x64_bf16 v[248:251], v[208:211], v[8:15], v241\n\t" \
    "s_waitcnt lgkmcnt(2)\n\t" \
    "buffer_load_dword %[fv], %[fv], %[rs], %[fs] offen sc1\n\t" \
    "s_barrier\n\t" ASM_ST \
    "ds_read_b128 v[212:215], v212 offset:0\n\t" \
    "ds_read_b128 v[216:219], v216 offset:0\n\t" \
    "ds_read_b128 v[220:223], v242 offset:0\n\t" \
    "ds_read_b128 v[208:211], v240 offset:0\n\t" \
    "v_smfmac_f32_16x16x64_bf16 v[244:247], v[236:239], v[80:87], v241\n\t" \
    "v_xor_b32 v240, 0x1800, %[ab]\n\t" \
    "v_smfmac_f32_16x16x64_bf16 v[248:251], v[236:239], v[88:95], v241\n\t" \
    "s_waitcnt lgkmcnt(4)\n\t" \
    "v_smfmac_f32_16x16x64_bf16 v[224:227], v[236:239], v[200:207], v241\n\t" \
    "ds_read_b128 v[200:203], %[wl] offset:6144\n\t" \
    "ds_read_b128 v[204:207], %[wl] offset:7168\n\t" \
    "ds_read_b128 v[236:239], v240 offset:0\n\t" \
    "s_waitcnt lgkmcnt(6)\n\t" \
    "v_smfmac_f32_16x16x64_bf16 v[224:227], v[212:215], v[16:23], v241\n\t" \
    "v_smfmac_f32_16x16x64_bf16 v[244:247], v[212:215], v[24:31], v241\n\t" \
    "v_xor_b32 v240, 0x1c00, %[ab]\n\t" \
    "v_smfmac_f32_16x16x64_bf16 v[248:251], v[212:215], v[32:39], v241\n\t" \
    "ds_read_b128 v[212:215], v240 offset:0\n\t" \
    "s_waitcnt lgkmcnt(6)\n\t" \
    "v_smfmac_f32_16x16x64_bf16 v[224:227], v[216:219], v[228:235], v241\n\t" \
    "ds_read_b128 v[228:231], %[wl] offset:8192\n\t" \
    "ds_read_b128 v[232:235], %[wl] offset:9216\n\t" \
    "v_smfmac_f32_16x16x64_bf16 v[244:247], v[216:219], v[40:47], v241\n\t" \
    "v_and_b32 v240, 0x3ff, %[ab]\n\t" \
    "v_smfmac_f32_16x16x64_bf16 v[248:251], v[216:219], v[48:55], v241\n\t" \
    "ds_read_b128 v[216:219], v240 offset:8192\n\t" \
    "s_waitcnt lgkmcnt(8)\n\t" \
    "v_smfmac_f32_16x16x64_bf16 v[224:227], v[220:223], v[56:63], v241\n\t" \
    "v_smfmac_f32_16x16x64_bf16 v[244:247], v[220:223], v[64:71], v241\n\t" \
    "v_smfmac_f32_16x16x64_bf16 v[248:251], v[220:223], v[72:79], v241\n\t" \
    "ds_read_b128 v[220:223], v240 offset:9216\n\t" \
    "s_waitcnt lgkmcnt(8)\n\t" \
    "v_smfmac_f32_16x16x64_bf16 v[244:247], v[208:211], v[104:111], v241\n\t" \
    "s_waitcnt lgkmcnt(5)\n\t" \
    "v_smfmac_f32_16x16x64_bf16 v[244:247], v[236:239], v[120:127], v241\n\t" \
    "s_waitcnt lgkmcnt(4)\n\t" \
    "v_smfmac_f32_16x16x64_bf16 v[244:247], v[212:215], v[144:151], v241\n\t" \
    "s_waitcnt lgkmcnt(1)\n\t" \
    "v_smfmac_f32_16x16x64_bf16 v[244:247], v[216:219], v[160:167], v241\n\t" \
    "s_waitcnt lgkmcnt(0)\n\t" \
    "v_smfmac_f32_16x16x64_bf16 v[244:247], v[220:223], v[184:191], v241\n\t" \
    "v_smfmac_f32_16x16x64_bf16 v[248:251], v[208:211], v[112:119], v241\n\t" \
    "v_smfmac_f32_16x16x64_bf16 v[248:251], v[236:239], v[128:135], v241\n\t" \
    "v_smfmac_f32_16x16x64_bf16 v[248:251], v[212:215], v[152:159], v241\n\t" \
    "v_permlane32_swap_b32 v244, v246\n\t" \
    "v_permlane32_swap_b32 v245, v247\n\t" \
    "v_smfmac_f32_16x16x64_bf16 v[248:251], v[216:219], v[168:175], v241\n\t" \
    "v_add_f32 v244, v244, v246\n\t" \
    "v_add_f32 v245, v245, v247\n\t" \
    "v_smfmac_f32_16x16x64_bf16 v[248:251], v[220:223], v[192:199], v241\n\t" \
    "v_cvt_pk_bf16_f32 v244, v244, v245\n\t" \
    "ds_write_b16 %[dst], v244 offset:512\n\t" \
    "ds_write_b16_d16_hi %[dst], v244 offset:528\n\t" \
    "v_smfmac_f32_16x16x64_bf16 v[224:227], v[208:211], v[96:103], v241\n\t" \
    "v_smfmac_f32_16x16x64_bf16 v[224:227], v[236:239], v[200:207], v241\n\t" \
    "v_smfmac_f32_16x16x64_bf16 v[224:227], v[212:215], v[136:143], v241\n\t" \
    "v_permlane32_swap_b32 v248, v250\n\t" \
    "v_permlane32_swap_b32 v249, v251\n\t" \
    "v_smfmac_f32_16x16x64_bf16 v[224:227], v[216:219], v[228:235], v241\n\t" \
    "v_add_f32 v248, v248, v250\n\t" \
    "v_add_f32 v249, v249, v251\n\t" \
    "v_smfmac_f32_16x16x64_bf16 v[224:227], v[220:223], v[176:183], v241\n\t" \
    "v_cvt_pk_bf16_f32 v248, v248, v249\n\t" \
    "ds_write_b16 %[dst], v248 offset:1024\n\t" \
    "ds_write_b16_d16_hi %[dst], v248 offset:1040\n\t" \
    "s_nop 4\n\t" \
    "v_permlane32_swap_b32 v224, v226\n\t" \
    "v_permlane32_swap_b32 v225, v227\n\t" \
    "v_add_f32 v224, v224, v226\n\t" \
    "v_add_f32 v225, v225, v227\n\t" \
    "v_cvt_pk_bf16_f32 v224, v224, v225\n\t" \
    "ds_write_b16 %[dst], v224 offset:0\n\t" \
    "ds_write_b16_d16_hi %[dst], v224 offset:16\n\t"
#define ASM_CRIT1_V2 \
    "s_branch .Lc0%=\n\t" \
    ".Lcr%=:\n\t" \
    "s_cmp_eq_u32 %[n], 0\n\t" \
    "s_cbranch_scc1 .Lcb%=\n\t" \
    "s_sub_u32 %[n], %[n], 1\n\t" \
    "v_max_u32 v243, v208, v209\n\t" \
    "v_max3_u32 v243, v210, v211, v243\n\t" \
    "v_cmp_eq_u32 vcc, -1, v243\n\t" \
    "s_cbranch_vccz .Lcs0_%=\n\t" \
    "buffer_load_dwordx4 v[208:211], %[ab], %[rs], %[s0] offen sc1\n\t" \
    ".Lcs0_%=:\n\t" \
    "v_max_u32 v243, v236, v237\n\t" \
    "v_max3_u32 v243, v238, v239, v243\n\t" \
    "v_cmp_eq_u32 vcc, -1, v243\n\t" \
    "s_cbranch_vccz .Lca%=\n\t" \
    "buffer_load_dwordx4 v[236:239], %[ab], %[rs], %[s1] offen sc1\n\t" \
    "s_branch .Lca%=\n\t" \
    ".Lc0%=:\n\t" \
    "buffer_load_dwordx4 v[208:211], %[ab], %[rs], %[s0] offen sc1\n\t" \
    "buffer_load_dwordx4 v[236:239], %[ab], %[rs], %[s1] offen sc1\n\t" \
    "v_bfe_i32 v241, %[ab], 7, 1\n\t" \
    "v_and_b32 v241, 0xaaaa, v241\n\t" \
    "v_or_b32 v241, 0x4444, v241\n\t" \
    "v_xor_b32 v212, 0x400, %[ab]\n\t" \
    "v_xor_b32 v216, 0x800, %[ab]\n\t" \
    "v_xor_b32 v242, 0xc00, %[ab]\n\t" \
    "v_xor_b32 v240, 0x1400, %[ab]\n\t" \
    ".Lca%=:\n\t" \
    "s_waitcnt vmcnt(0)\n\t" \
    "ds_write_b128 %[ab], v[208:211] offset:0\n\t" \
    "ds_write_b128 %[ab], v[236:239] offset:4096\n\t" \
    "v_max_u32 v243, v208, v209\n\t" \
    "v_max3_u32 v243, v210, v211, v243\n\t" \
    "v_max3_u32 v243, v236, v237, v243\n\t" \
    "v_max3_u32 v243, v238, v239, v243\n\t" \
    "v_cmp_eq_u32 vcc, -1, v243\n\t" \
    "s_cbranch_vccnz .Lcr%=\n\t" \
    ".Lcb%=:\n\t" \
    PRIUP \
    ASM_ST2 \
    "v_smfmac_f32_16x16x64_bf16 v[224:227], v[208:211], v[200:207], v241\n\t" \
    "ds_read_b128 v[200:203], %[wl] offset:4096\n\t" \
    "ds_read_b128 v[204:207], %[wl] offset:5120\n\t" \
    "v_smfmac_f32_16x16x64_bf16 v[244:247], v[208:211], v[0:7], v241\n\t" \
    "v_smfmac_f32_16x16x64_bf16 v[248:251], v[208:211], v[8:15], v241\n\t" \
    "s_waitcnt lgkmcnt(2)\n\t" \
    "buffer_load_dword %[fv], %[fv], %[rs], %[fs] offen sc1\n\t" \
    "s_barrier\n\t" ASM_ST \
    "ds_read_b128 v[212:215], v212 offset:0\n\t" \
    "ds_read_b128 v[216:219], v216 offset:0\n\t" \
    "ds_read_b128 v[220:223], v242 offset:0\n\t" \
    "ds_read_b128 v[208:211], v240 offset:0\n\t" \
    "v_smfmac_f32_16x16x64_bf16 v[244:247], v[236:239], v[80:87], v241\n\t" \
    "v_xor_b32 v240, 0x1800, %[ab]\n\t" \
    "v_smfmac_f32_16x16x64_bf16 v[248:251], v[236:239], v[88:95], v241\n\t" \
    "s_waitcnt lgkmcnt(4)\n\t" \
    "v_smfmac_f32_16x16x64_bf16 v[224:227], v[236:239], v[200:207], v241\n\t" \
    "ds_read_b128 v[200:203], %[wl] offset:6144\n\t" \
    "ds_read_b128 v[204:207], %[wl] offset:7168\n\t" \
    "ds_read_b128 v[236:239], v240 offset:0\n\t" \
    "s_waitcnt lgkmcnt(6)\n\t" \
    "v_smfmac_f32_16x16x64_bf16 v[224:227], v[212:215], v[16:23], v241\n\t" \
    "v_smfmac_f32_16x16x64_bf16 v[244:247], v[212:215], v[24:31], v241\n\t" \
    "v_xor_b32 v240, 0x1c00, %[ab]\n\t" \
    "v_smfmac_f32_16x16x64_bf16 v[248:251], v[212:215], v[32:39], v241\n\t" \
    "ds_read_b128 v[212:215], v240 offset:0\n\t" \
    "s_waitcnt lgkmcnt(6)\n\t" \
    "v_smfmac_f32_16x16x64_bf16 v[224:227], v[216:219], v[228:235], v241\n\t" \
    "ds_read_b128 v[228:231], %[wl] offset:8192\n\t" \
    "ds_read_b128 v[232:235], %[wl] offset:9216\n\t" \
    "v_smfmac_f32_16x16x64_bf16 v[244:247], v[216:219], v[40:47], v241\n\t" \
    "v_and_b32 v240, 0x3ff, %[ab]\n\t" \
    "v_smfmac_f32_16x16x64_bf16 v[248:251], v[216:219], v[48:55], v241\n\t" \
    "ds_read_b128 v[216:219], v240 offset:8192\n\t" \
    "s_waitcnt lgkmcnt(8)\n\t" \
    "v_smfmac_f32_16x16x64_bf16 v[224:227], v[220:223], v[56:63], v241\n\t" \
    "v_smfmac_f32_16x16x64_bf16 v[244:247], v[220:223], v[64:71], v241\n\t" \
    "v_smfmac_f32_16x16x64_bf16 v[248:251], v[220:223], v[72:79], v241\n\t" \
    "ds_read_b128 v[220:223], v240 offset:9216\n\t" \
    "s_waitcnt lgkmcnt(8)\n\t" \
    "v_smfmac_f32_16x16x64_bf16 v[244:247], v[208:211], v[104:111], v241\n\t" \
    "s_waitcnt lgkmcnt(5)\n\t" \
    "v_smfmac_f32_16x16x64_bf16 v[244:247], v[236:239], v[120:127], v241\n\t" \
    "s_waitcnt lgkmcnt(4)\n\t" \
    "v_smfmac_f32_16x16x64_bf16 v[244:247], v[212:215], v[144:151], v241\n\t" \
    "s_waitcnt lgkmcnt(1)\n\t" \
    "v_smfmac_f32_16x16x64_bf16 v[244:247], v[216:219], v[160:167], v241\n\t" \
    "s_waitcnt lgkmcnt(0)\n\t" \
    "v_smfmac_f32_16x16x64_bf16 v[244:247], v[220:223], v[184:191], v241\n\t" \
    "v_smfmac_f32_16x16x64_bf16 v[248:251], v[208:211], v[112:119], v241\n\t" \
    "v_smfmac_f32_16x16x64_bf16 v[248:251], v[236:239], v[128:135], v241\n\t" \
    "v_smfmac_f32_16x16x64_bf16 v[248:251], v[212:215], v[152:159], v241\n\t" \
    "v_permlane32_swap_b32 v244, v246\n\t" \
    "v_permlane32_swap_b32 v245, v247\n\t" \
    "v_smfmac_f32_16x16x64_bf16 v[248:251], v[216:219], v[168:175], v241\n\t" \
    "v_add_f32 v244, v244, v246\n\t" \
    "v_add_f32 v245, v245, v247\n\t" \
    "v_smfmac_f32_16x16x64_bf16 v[248:251], v[220:223], v[192:199], v241\n\t" \
    "v_cvt_pk_bf16_f32 v244, v244, v245\n\t" \
    "ds_write_b16 %[dst], v244 offset:512\n\t" \
    "ds_write_b16_d16_hi %[dst], v244 offset:528\n\t" \
    "v_smfmac_f32_16x16x64_bf16 v[224:227], v[208:211], v[96:103], v241\n\t" \
    "v_smfmac_f32_16x16x64_bf16 v[224:227], v[236:239], v[200:207], v241\n\t" \
    "v_smfmac_f32_16x16x64_bf16 v[224:227], v[212:215], v[136:143], v241\n\t" \
    "v_permlane32_swap_b32 v248, v250\n\t" \
    "v_permlane32_swap_b32 v249, v251\n\t" \
    "v_smfmac_f32_16x16x64_bf16 v[224:227], v[216:219], v[228:235], v241\n\t" \
    "v_add_f32 v248, v248, v250\n\t" \
    "v_add_f32 v249, v249, v251\n\t" \
    "v_smfmac_f32_16x16x64_bf16 v[224:227], v[220:223], v[176:183], v241\n\t" \
    "v_cvt_pk_bf16_f32 v248, v248, v249\n\t" \
    "ds_write_b16 %[dst], v248 offset:1024\n\t" \
    "ds_write_b16_d16_hi %[dst], v248 offset:1040\n\t" \
    "s_nop 4\n\t" \
    "v_permlane32_swap_b32 v224, v226\n\t" \
    "v_permlane32_swap_b32 v225, v227\n\t" \
    "v_add_f32 v224, v224, v226\n\t" \
    "v_add_f32 v225, v225, v227\n\t" \
    "v_cvt_pk_bf16_f32 v224, v224, v225\n\t" \
    "ds_write_b16 %[dst], v224 offset:0\n\t" \
    "ds_write_b16_d16_hi %[dst], v224 offset:16\n\t"
#define ASM_VOX \
    "s_waitcnt lgkmcnt(0)\n\t" \
    "ds_read_b128 v[208:211], %[va] offset:2048\n\t" \
    "ds_read_b128 v[212:215], %[va] offset:3072\n\t" \
    "ds_read_b128 v[216:219], %[va] offset:4096\n\t" \
    "ds_read_b128 v[220:223], %[va] offset:5120\n\t" \
    "ds_read_b128 v[200:203], %[va] offset:0\n\t" \
    "ds_read_b128 v[204:207], %[va] offset:1024\n\t" \
    "ds_read_b128 v[236:239], %[wl] offset:0\n\t" \
    "ds_read_b128 v[240:243], %[wl] offset:1024\n\t" \
    "s_waitcnt lgkmcnt(7)\n\t" \
    "v_mfma_f32_16x16x32_bf16 v[244:247], v[208:211], v[0:3], 0\n\t" \
    "s_waitcnt lgkmcnt(6)\n\t" \
    "v_mfma_f32_16x16x32_bf16 v[244:247], v[212:215], v[4:7], v[244:247]\n\t" \
    "s_waitcnt lgkmcnt(5)\n\t" \
    "v_mfma_f32_16x16x32_bf16 v[244:247], v[216:219], v[8:11], v[244:247]\n\t" \
    "s_waitcnt lgkmcnt(4)\n\t" \
    "v_mfma_f32_16x16x32_bf16 v[244:247], v[220:223], v[12:15], v[244:247]\n\t" \
    "s_waitcnt lgkmcnt(1)\n\t" \
    "v_mfma_f32_16x16x32_bf16 v[244:247], v[200:203], v[236:239], v[244:247]\n\t" \
    "ds_read_b128 v[236:239], %[wl] offset:2048\n\t" \
    "s_waitcnt lgkmcnt(1)\n\t" \
    "v_mfma_f32_16x16x32_bf16 v[244:247], v[204:207], v[240:243], v[244:247]\n\t" \
    "ds_read_b128 v[240:243], %[wl] offset:3072\n\t" \
    "v_mfma_f32_16x16x32_bf16 v[248:251], v[208:211], v[40:43], 0\n\t" \
    "v_mfma_f32_16x16x32_bf16 v[248:251], v[212:215], v[44:47], v[248:251]\n\t" \
    "v_mfma_f32_16x16x32_bf16 v[248:251], v[216:219], v[48:51], v[248:251]\n\t" \
    "v_cvt_pk_bf16_f32 v224, v244, v245\n\t" \
    "v_mfma_f32_16x16x32_bf16 v[248:251], v[220:223], v[52:55], v[248:251]\n\t" \
    "v_cvt_pk_bf16_f32 v225, v246, v247\n\t" \
    "s_waitcnt lgkmcnt(1)\n\t" \
    "v_mfma_f32_16x16x32_bf16 v[248:251], v[200:203], v[236:239], v[248:251]\n\t" \
    "ds_read_b128 v[236:239], %[wl] offset:4096\n\t" \
    "s_waitcnt lgkmcnt(1)\n\t" \
    "v_mfma_f32_16x16x32_bf16 v[248:251], v[204:207], v[240:243], v[248:251]\n\t" \
    "ds_read_b128 v[240:243], %[wl] offset:5120\n\t" \
    "v_mfma_f32_16x16x32_bf16 v[244:247], v[208:211], v[80:83], 0\n\t" \
    "v_mfma_f32_16x16x32_bf16 v[244:247], v[212:215], v[84:87], v[244:247]\n\t" \
    "v_mfma_f32_16x16x32_bf16 v[244:247], v[216:219], v[88:91], v[244:247]\n\t" \
    "v_cvt_pk_bf16_f32 v226, v248, v249\n\t" \
    "v_mfma_f32_16x16x32_bf16 v[244:247], v[220:223], v[92:95], v[244:247]\n\t" \
    "v_cvt_pk_bf16_f32 v227, v250, v251\n\t" \
    "s_waitcnt lgkmcnt(1)\n\t" \
    "v_mfma_f32_16x16x32_bf16 v[244:247], v[200:203], v[236:239], v[244:247]\n\t" \
    "ds_read_b128 v[236:239], %[wl] offset:6144\n\t" \
    "s_waitcnt lgkmcnt(1)\n\t" \
    "v_mfma_f32_16x16x32_bf16 v[244:247], v[204:207], v[240:243], v[244:247]\n\t" \
    "ds_read_b128 v[240:243], %[wl] offset:7168\n\t" \
    "v_mfma_f32_16x16x32_bf16 v[248:251], v[208:211], v[120:123], 0\n\t" \
    "v_mfma_f32_16x16x32_bf16 v[248:251], v[212:215], v[124:127], v[248:251]\n\t" \
    "v_mfma_f32_16x16x32_bf16 v[248:251], v[216:219], v[128:131], v[248:251]\n\t" \
    "v_cvt_pk_bf16_f32 v228, v244, v245\n\t" \
    "v_mfma_f32_16x16x32_bf16 v[248:251], v[220:223], v[132:135], v[248:251]\n\t" \
    "v_cvt_pk_bf16_f32 v229, v246, v247\n\t" \
    "s_waitcnt lgkmcnt(1)\n\t" \
    "v_mfma_f32_16x16x32_bf16 v[248:251], v[200:203], v[236:239], v[248:251]\n\t" \
    "ds_read_b128 v[236:239], %[wl] offset:8192\n\t" \
    "s_waitcnt lgkmcnt(1)\n\t" \
    "v_mfma_f32_16x16x32_bf16 v[248:251], v[204:207], v[240:243], v[248:251]\n\t" \
    "ds_read_b128 v[240:243], %[wl] offset:9216\n\t" \
    "v_mfma_f32_16x16x32_bf16 v[244:247], v[208:211], v[160:163], 0\n\t" \
    "v_mfma_f32_16x16x32_bf16 v[244:247], v[212:215], v[164:167], v[244:247]\n\t" \
    "v_mfma_f32_16x16x32_bf16 v[244:247], v[216:219], v[168:171], v[244:247]\n\t" \
    "v_cvt_pk_bf16_f32 v230, v248, v249\n\t" \
    "v_mfma_f32_16x16x32_bf16 v[244:247], v[220:223], v[172:175], v[244:247]\n\t" \
    "v_cvt_pk_bf16_f32 v231, v250, v251\n\t" \
    "s_waitcnt lgkmcnt(1)\n\t" \
    "v_mfma_f32_16x16x32_bf16 v[244:247], v[200:203], v[236:239], v[244:247]\n\t" \
    "s_waitcnt lgkmcnt(0)\n\t" \
    "v_mfma_f32_16x16x32_bf16 v[244:247], v[204:207], v[240:243], v[244:247]\n\t" \
    "v_mfma_f32_16x16x32_bf16 v[248:251], v[208:211], v[24:27], 0\n\t" \
    "v_mfma_f32_16x16x32_bf16 v[248:251], v[212:215], v[28:31], v[248:251]\n\t" \
    "v_mfma_f32_16x16x32_bf16 v[248:251], v[216:219], v[32:35], v[248:251]\n\t" \
    "v_cvt_pk_bf16_f32 v232, v244, v245\n\t" \
    "v_mfma_f32_16x16x32_bf16 v[248:251], v[220:223], v[36:39], v[248:251]\n\t" \
    "v_cvt_pk_bf16_f32 v233, v246, v247\n\t" \
    "v_mfma_f32_16x16x32_bf16 v[248:251], v[200:203], v[16:19], v[248:251]\n\t" \
    "v_mfma_f32_16x16x32_bf16 v[248:251], v[204:207], v[20:23], v[248:251]\n\t" \
    "v_mfma_f32_16x16x32_bf16 v[244:247], v[208:211], v[64:67], 0\n\t" \
    "v_mfma_f32_16x16x32_bf16 v[244:247], v[212:215], v[68:71], v[244:247]\n\t" \
    "v_mfma_f32_16x16x32_bf16 v[244:247], v[216:219], v[72:75], v[244:247]\n\t" \
    "v_cvt_pk_bf16_f32 v236, v248, v249\n\t" \
    "v_mfma_f32_16x16x32_bf16 v[244:247], v[220:223], v[76:79], v[244:247]\n\t" \
    "v_cvt_pk_bf16_f32 v237, v250, v251\n\t" \
    "v_mfma_f32_16x16x32_bf16 v[244:247], v[200:203], v[56:59], v[244:247]\n\t" \
    "v_mfma_f32_16x16x32_bf16 v[244:247], v[204:207], v[60:63], v[244:247]\n\t" \
    "v_mfma_f32_16x16x32_bf16 v[248:251], v[208:211], v[104:107], 0\n\t" \
    "v_mfma_f32_16x16x32_bf16 v[248:251], v[212:215], v[108:111], v[248:251]\n\t" \
    "v_mfma_f32_16x16x32_bf16 v[248:251], v[216:219], v[112:115], v[248:251]\n\t" \
    "v_cvt_pk_bf16_f32 v238, v244, v245\n\t" \
    "v_mfma_f32_16x16x32_bf16 v[248:251], v[220:223], v[116:119], v[248:251]\n\t" \
    "v_cvt_pk_bf16_f32 v239, v246, v247\n\t" \
    "v_mfma_f32_16x16x32_bf16 v[248:251], v[200:203], v[96:99], v[248:251]\n\t" \
    "v_mfma_f32_16x16x32_bf16 v[248:251], v[204:207], v[100:103], v[248:251]\n\t" \
    "v_mfma_f32_16x16x32_bf16 v[244:247], v[208:211], v[144:147], 0\n\t" \
    "v_mfma_f32_16x16x32_bf16 v[244:247], v[212:215], v[148:151], v[244:247]\n\t" \
    "v_mfma_f32_16x16x32_bf16 v[244:247], v[216:219], v[152:155], v[244:247]\n\t" \
    "v_cvt_pk_bf16_f32 v240, v248, v249\n\t" \
    "v_mfma_f32_16x16x32_bf16 v[244:247], v[220:223], v[156:159], v[244:247]\n\t" \
    "v_cvt_pk_bf16_f32 v241, v250, v251\n\t" \
    "v_mfma_f32_16x16x32_bf16 v[244:247], v[200:203], v[136:139], v[244:247]\n\t" \
    "v_mfma_f32_16x16x32_bf16 v[244:247], v[204:207], v[140:143], v[244:247]\n\t" \
    "v_mfma_f32_16x16x32_bf16 v[248:251], v[208:211], v[184:187], 0\n\t" \
    "ds_read_b128 v[208:211], %[va] offset:8192\n\t" \
    "v_mfma_f32_16x16x32_bf16 v[248:251], v[212:215], v[188:191], v[248:251]\n\t" \
    "ds_read_b128 v[212:215], %[va] offset:9216\n\t" \
    "v_mfma_f32_16x16x32_bf16 v[248:251], v[216:219], v[192:195], v[248:251]\n\t" \
    "ds_read_b128 v[216:219], %[va] offset:10240\n\t" \
    "v_cvt_pk_bf16_f32 v242, v244, v245\n\t" \
    "v_mfma_f32_16x16x32_bf16 v[248:251], v[220:223], v[196:199], v[248:251]\n\t" \
    "ds_read_b128 v[220:223], %[va] offset:11264\n\t" \
    "v_cvt_pk_bf16_f32 v243, v246, v247\n\t" \
    "v_mfma_f32_16x16x32_bf16 v[248:251], v[200:203], v[176:179], v[248:251]\n\t" \
    "ds_read_b128 v[200:203], %[va] offset:6144\n\t" \
    "v_mfma_f32_16x16x32_bf16 v[248:251], v[204:207], v[180:183], v[248:251]\n\t" \
    "ds_read_b128 v[204:207], %[va] offset:7168\n\t" \
    "s_waitcnt lgkmcnt(5)\n\t" \
    "v_mfma_f32_16x16x32_bf16 v[244:247], v[208:211], v[24:27], 0\n\t" \
    "s_waitcnt lgkmcnt(4)\n\t" \
    "v_mfma_f32_16x16x32_bf16 v[244:247], v[212:215], v[28:31], v[244:247]\n\t" \
    "s_waitcnt lgkmcnt(3)\n\t" \
    "v_mfma_f32_16x16x32_bf16 v[244:247], v[216:219], v[32:35], v[244:247]\n\t" \
    "v_cvt_pk_bf16_f32 v234, v248, v249\n\t" \
    "s_waitcnt lgkmcnt(2)\n\t" \
    "v_mfma_f32_16x16x32_bf16 v[244:247], v[220:223], v[36:39], v[244:247]\n\t" \
    "v_cvt_pk_bf16_f32 v235, v250, v251\n\t" \
    "s_waitcnt lgkmcnt(1)\n\t" \
    "v_mfma_f32_16x16x32_bf16 v[244:247], v[200:203], v[16:19], v[244:247]\n\t" \
    "s_waitcnt lgkmcnt(0)\n\t" \
    "v_mfma_f32_16x16x32_bf16 v[244:247], v[204:207], v[20:23], v[244:247]\n\t" \
    "v_mfma_f32_16x16x32_bf16 v[248:251], v[208:211], v[64:67], 0\n\t" \
    "s_add_i32 %[so], %[so], 65536\n\t" \
    "v_mfma_f32_16x16x32_bf16 v[248:251], v[212:215], v[68:71], v[248:251]\n\t" \
    "s_nop 1\n\t" \
    "v_cvt_pk_bf16_f32 v247, v246, v247\n\t" \
    "v_mfma_f32_16x16x32_bf16 v[248:251], v[216:219], v[72:75], v[248:251]\n\t" \
    "v_cvt_pk_bf16_f32 v246, v244, v245\n\t" \
    "v_mfma_f32_16x16x32_bf16 v[248:251], v[220:223], v[76:79], v[248:251]\n\t" \
    "v_mov_b64 v[244:245], v[236:237]\n\t" \
    "buffer_store_dwordx4 v[244:247], %[l16], %[rd], %[so] offen sc1\n\t" \
    "v_mfma_f32_16x16x32_bf16 v[248:251], v[200:203], v[56:59], v[248:251]\n\t" \
    "v_mfma_f32_16x16x32_bf16 v[248:251], v[204:207], v[60:63], v[248:251]\n\t" \
    "v_mfma_f32_16x16x32_bf16 v[244:247], v[208:211], v[104:107], 0\n\t" \
    "s_add_i32 %[so], %[so], 131072\n\t" \
    "v_mfma_f32_16x16x32_bf16 v[244:247], v[212:215], v[108:111], v[244:247]\n\t" \
    "s_nop 1\n\t" \
    "v_cvt_pk_bf16_f32 v251, v250, v251\n\t" \
    "v_mfma_f32_16x16x32_bf16 v[244:247], v[216:219], v[112:115], v[244:247]\n\t" \
    "v_cvt_pk_bf16_f32 v250, v248, v249\n\t" \
    "v_mfma_f32_16x16x32_bf16 v[244:247], v[220:223], v[116:119], v[244:247]\n\t" \
    "v_mov_b64 v[248:249], v[238:239]\n\t" \
    "buffer_store_dwordx4 v[248:251], %[l16], %[rd], %[so] offen sc1\n\t" \
    "v_mfma_f32_16x16x32_bf16 v[244:247], v[200:203], v[96:99], v[244:247]\n\t" \
    "v_mfma_f32_16x16x32_bf16 v[244:247], v[204:207], v[100:103], v[244:247]\n\t" \
    "v_mfma_f32_16x16x32_bf16 v[248:251], v[208:211], v[144:147], 0\n\t" \
    "s_add_i32 %[so], %[so], 131072\n\t" \
    "v_mfma_f32_16x16x32_bf16 v[248:251], v[212:215], v[148:151], v[248:251]\n\t" \
    "s_nop 1\n\t" \
    "v_cvt_pk_bf16_f32 v247, v246, v247\n\t" \
    "v_mfma_f32_16x16x32_bf16 v[248:251], v[216:219], v[152:155], v[248:251]\n\t" \
    "v_cvt_pk_bf16_f32 v246, v244, v245\n\t" \
    "v_mfma_f32_16x16x32_bf16 v[248:251], v[220:223], v[156:159], v[248:251]\n\t" \
    "v_mov_b64 v[244:245], v[240:241]\n\t" \
    "buffer_store_dwordx4 v[244:247], %[l16], %[rd], %[so] offen sc1\n\t" \
    "v_mfma_f32_16x16x32_bf16 v[248:251], v[200:203], v[136:139], v[248:251]\n\t" \
    "v_mfma_f32_16x16x32_bf16 v[248:251], v[204:207], v[140:143], v[248:251]\n\t" \
    "v_mfma_f32_16x16x32_bf16 v[244:247], v[208:211], v[184:187], 0\n\t" \
    "s_add_i32 %[so], %[so], 131072\n\t" \
    "v_mfma_f32_16x16x32_bf16 v[244:247], v[212:215], v[188:191], v[244:247]\n\t" \
    "s_nop 1\n\t" \
    "v_cvt_pk_bf16_f32 v251, v250, v251\n\t" \
    "v_mfma_f32_16x16x32_bf16 v[244:247], v[216:219], v[192:195], v[244:247]\n\t" \
    "v_cvt_pk_bf16_f32 v250, v248, v249\n\t" \
    "v_mfma_f32_16x16x32_bf16 v[244:247], v[220:223], v[196:199], v[244:247]\n\t" \
    "v_mov_b64 v[248:249], v[242:243]\n\t" \
    "buffer_store_dwordx4 v[248:251], %[l16], %[rd], %[so] offen sc1\n\t" \
    "v_mfma_f32_16x16x32_bf16 v[244:247], v[200:203], v[176:179], v[244:247]\n\t" \
    "v_mfma_f32_16x16x32_bf16 v[244:247], v[204:207], v[180:183], v[244:247]\n\t" \
    "ds_read_b128 v[236:239], %[wl] offset:0\n\t" \
    "ds_read_b128 v[240:243], %[wl] offset:1024\n\t" \
    "v_mfma_f32_16x16x32_bf16 v[248:251], v[208:211], v[0:3], 0\n\t" \
    "s_add_i32 %[so], %[so], 131072\n\t" \
    "v_mfma_f32_16x16x32_bf16 v[248:251], v[212:215], v[4:7], v[248:251]\n\t" \
    "v_cvt_pk_bf16_f32 v247, v246, v247\n\t" \
    "v_mfma_f32_16x16x32_bf16 v[248:251], v[216:219], v[8:11], v[248:251]\n\t" \
    "v_cvt_pk_bf16_f32 v246, v244, v245\n\t" \
    "v_mfma_f32_16x16x32_bf16 v[248:251], v[220:223], v[12:15], v[248:251]\n\t" \
    "v_mov_b64 v[244:245], v[234:235]\n\t" \
    "buffer_store_dwordx4 v[244:247], %[l16], %[rd], %[so] offen sc1\n\t" \
    "s_waitcnt lgkmcnt(1)\n\t" \
    "v_mfma_f32_16x16x32_bf16 v[248:251], v[200:203], v[236:239], v[248:251]\n\t" \
    "ds_read_b128 v[236:239], %[wl] offset:2048\n\t" \
    "s_waitcnt lgkmcnt(1)\n\t" \
    "v_mfma_f32_16x16x32_bf16 v[248:251], v[204:207], v[240:243], v[248:251]\n\t" \
    "ds_read_b128 v[240:243], %[wl] offset:3072\n\t" \
    "v_mfma_f32_16x16x32_bf16 v[244:247], v[208:211], v[40:43], 0\n\t" \
    "s_add_i32 %[so], %[so], -589824\n\t" \
    "v_mfma_f32_16x16x32_bf16 v[244:247], v[212:215], v[44:47], v[244:247]\n\t" \
    "s_nop 0\n\t" \
    "v_cvt_pk_bf16_f32 v251, v250, v251\n\t" \
    "v_mfma_f32_16x16x32_bf16 v[244:247], v[216:219], v[48:51], v[244:247]\n\t" \
    "v_cvt_pk_bf16_f32 v250, v248, v249\n\t" \
    "v_mfma_f32_16x16x32_bf16 v[244:247], v[220:223], v[52:55], v[244:247]\n\t" \
    "v_mov_b64 v[248:249], v[224:225]\n\t" \
    "buffer_store_dwordx4 v[248:251], %[l16], %[rd], %[so] offen sc1\n\t" \
    "s_waitcnt lgkmcnt(1)\n\t" \
    "v_mfma_f32_16x16x32_bf16 v[244:247], v[200:203], v[236:239], v[244:247]\n\t" \
    "ds_read_b128 v[236:239], %[wl] offset:4096\n\t" \
    "s_waitcnt lgkmcnt(1)\n\t" \
    "v_mfma_f32_16x16x32_bf16 v[244:247], v[204:207], v[240:243], v[244:247]\n\t" \
    "ds_read_b128 v[240:243], %[wl] offset:5120\n\t" \
    "v_mfma_f32_16x16x32_bf16 v[248:251], v[208:211], v[80:83], 0\n\t" \
    "s_add_i32 %[so], %[so], 131072\n\t" \
    "v_mfma_f32_16x16x32_bf16 v[248:251], v[212:215], v[84:87], v[248:251]\n\t" \
    "s_nop 0\n\t" \
    "v_cvt_pk_bf16_f32 v247, v246, v247\n\t" \
    "v_mfma_f32_16x16x32_bf16 v[248:251], v[216:219], v[88:91], v[248:251]\n\t" \
    "v_cvt_pk_bf16_f32 v246, v244, v245\n\t" \
    "v_mfma_f32_16x16x32_bf16 v[248:251], v[220:223], v[92:95], v[248:251]\n\t" \
    "v_mov_b64 v[244:245], v[226:227]\n\t" \
    "buffer_store_dwordx4 v[244:247], %[l16], %[rd], %[so] offen sc1\n\t" \
    "s_waitcnt lgkmcnt(1)\n\t" \
    "v_mfma_f32_16x16x32_bf16 v[248:251], v[200:203], v[236:239], v[248:251]\n\t" \
    "ds_read_b128 v[236:239], %[wl] offset:6144\n\t" \
    "s_waitcnt lgkmcnt(1)\n\t" \
    "v_mfma_f32_16x16x32_bf16 v[248:251], v[204:207], v[240:243], v[248:251]\n\t" \
    "ds_read_b128 v[240:243], %[wl] offset:7168\n\t" \
    "v_mfma_f32_16x16x32_bf16 v[244:247], v[208:211], v[120:123], 0\n\t" \
    "s_add_i32 %[so], %[so], 131072\n\t" \
    "v_mfma_f32_16x16x32_bf16 v[244:247], v[212:215], v[124:127], v[244:247]\n\t" \
    "s_nop 0\n\t" \
    "v_cvt_pk_bf16_f32 v251, v250, v251\n\t" \
    "v_mfma_f32_16x16x32_bf16 v[244:247], v[216:219], v[128:131], v[244:247]\n\t" \
    "v_cvt_pk_bf16_f32 v250, v248, v249\n\t" \
    "v_mfma_f32_16x16x32_bf16 v[244:247], v[220:223], v[132:135], v[244:247]\n\t" \
    "v_mov_b64 v[248:249], v[228:229]\n\t" \
    "buffer_store_dwordx4 v[248:251], %[l16], %[rd], %[so] offen sc1\n\t" \
    "s_waitcnt lgkmcnt(1)\n\t" \
    "v_mfma_f32_16x16x32_bf16 v[244:247], v[200:203], v[236:239], v[244:247]\n\t" \
    "ds_read_b128 v[236:239], %[wl] offset:8192\n\t" \
    "s_waitcnt lgkmcnt(1)\n\t" \
    "v_mfma_f32_16x16x32_bf16 v[244:247], v[204:207], v[240:243], v[244:247]\n\t" \
    "ds_read_b128 v[240:243], %[wl] offset:9216\n\t" \
    "v_mfma_f32_16x16x32_bf16 v[248:251], v[208:211], v[160:163], 0\n\t" \
    "s_add_i32 %[so], %[so], 131072\n\t" \
    "v_mfma_f32_16x16x32_bf16 v[248:251], v[212:215], v[164:167], v[248:251]\n\t" \
    "s_nop 0\n\t" \
    "v_cvt_pk_bf16_f32 v247, v246, v247\n\t" \
    "v_mfma_f32_16x16x32_bf16 v[248:251], v[216:219], v[168:171], v[248:251]\n\t" \
    "v_cvt_pk_bf16_f32 v246, v244, v245\n\t" \
    "v_mfma_f32_16x16x32_bf16 v[248:251], v[220:223], v[172:175], v[248:251]\n\t" \
    "v_mov_b64 v[244:245], v[230:231]\n\t" \
    "buffer_store_dwordx4 v[244:247], %[l16], %[rd], %[so] offen sc1\n\t" \
    "s_waitcnt lgkmcnt(1)\n\t" \
    "v_mfma_f32_16x16x32_bf16 v[248:251], v[200:203], v[236:239], v[248:251]\n\t" \
    "s_waitcnt lgkmcnt(0)\n\t" \
    "v_mfma_f32_16x16x32_bf16 v[248:251], v[204:207], v[240:243], v[248:251]\n\t" \
    "s_add_i32 %[so], %[so], 131072\n\t" \
    "s_nop 7\n\t" \
    "v_cvt_pk_bf16_f32 v251, v250, v251\n\t" \
    "v_cvt_pk_bf16_f32 v250, v248, v249\n\t" \
    "v_mov_b64 v[248:249], v[232:233]\n\t" \
    "buffer_store_dwordx4 v[248:251], %[l16], %[rd], %[so] offen sc1\n\t" \
    "s_nop 1\n\t"
#define ASM_CRIT2 \
    "s_waitcnt lgkmcnt(0)\n\t" \
    "s_barrier\n\t" ASM_ST \
    "ds_read_b128 v[204:207], v200 offset:11264\n\t" \
    "ds_read_b128 v[208:211], v200 offset:12288\n\t" \
    "ds_read_b128 v[200:203], v200 offset:10240\n\t" \
    "s_waitcnt lgkmcnt(2)\n\t" \
    "v_smfmac_f32_16x16x64_bf16 v[224:227], v[204:207], v[24:31], %[sidx]\n\t" \
    "s_waitcnt lgkmcnt(1)\n\t" \
    "v_smfmac_f32_16x16x64_bf16 v[224:227], v[208:211], v[32:39], %[sidx]\n\t" \
    "s_waitcnt lgkmcnt(0)\n\t" \
    "v_smfmac_f32_16x16x64_bf16 v[224:227], v[200:203], v[16:23], %[sidx]\n\t" \
    "v_smfmac_f32_16x16x64_bf16 v[220:223], v[204:207], v[0:7], %[sidx]\n\t" \
    "v_smfmac_f32_16x16x64_bf16 v[220:223], v[208:211], v[8:15], %[sidx]\n\t" \
    "v_smfmac_f32_16x16x64_bf16 v[220:223], v[200:203], v[212:219], %[sidx]\n\t" \
    "ds_read_b128 v[212:215], %[wl] offset:2048\n\t" \
    "ds_read_b128 v[216:219], %[wl] offset:3072\n\t" \
    "v_smfmac_f32_16x16x64_bf16 v[232:235], v[204:207], v[64:71], %[sidx]\n\t" \
    "v_smfmac_f32_16x16x64_bf16 v[232:235], v[208:211], v[72:79], %[sidx]\n\t" \
    "v_smfmac_f32_16x16x64_bf16 v[232:235], v[200:203], v[56:63], %[sidx]\n\t" \
    "v_permlane32_swap_b32 v220, v224\n\t" \
    "v_permlane32_swap_b32 v221, v225\n\t" \
    "v_permlane32_swap_b32 v222, v226\n\t" \
    "v_smfmac_f32_16x16x64_bf16 v[228:231], v[204:207], v[40:47], %[sidx]\n\t" \
    "v_permlane32_swap_b32 v223, v227\n\t" \
    "v_add_f32 v220, v220, v224\n\t" \
    "v_add_f32 v221, v221, v225\n\t" \
    "v_smfmac_f32_16x16x64_bf16 v[228:231], v[208:211], v[48:55], %[sidx]\n\t" \
    "v_add_f32 v222, v222, v226\n\t" \
    "v_add_f32 v223, v223, v227\n\t" \
    "v_cvt_pk_bf16_f32 v226, v220, v221\n\t" \
    "s_waitcnt lgkmcnt(0)\n\t" \
    "v_smfmac_f32_16x16x64_bf16 v[228:231], v[200:203], v[212:219], %[sidx]\n\t" \
    "ds_read_b128 v[212:215], %[wl] offset:4096\n\t" \
    "ds_read_b128 v[216:219], %[wl] offset:5120\n\t" \
    "v_cvt_pk_bf16_f32 v227, v222, v223\n\t" \
    "v_min_u32 v226, -2, v226\n\t" \
    "v_min_u32 v227, -2, v227\n\t" \
    "v_smfmac_f32_16x16x64_bf16 v[240:243], v[204:207], v[104:111], %[sidx]\n\t" \
    "v_smfmac_f32_16x16x64_bf16 v[240:243], v[208:211], v[112:119], %[sidx]\n\t" \
    "v_smfmac_f32_16x16x64_bf16 v[240:243], v[200:203], v[96:103], %[sidx]\n\t" \
    "v_permlane32_swap_b32 v228, v232\n\t" \
    "v_permlane32_swap_b32 v229, v233\n\t" \
    "v_permlane32_swap_b32 v230, v234\n\t" \
    "v_smfmac_f32_16x16x64_bf16 v[236:239], v[204:207], v[80:87], %[sidx]\n\t" \
    "v_permlane32_swap_b32 v231, v235\n\t" \
    "v_add_f32 v228, v228, v232\n\t" \
    "v_add_f32 v229, v229, v233\n\t" \
    "v_smfmac_f32_16x16x64_bf16 v[236:239], v[208:211], v[88:95], %[sidx]\n\t" \
    "v_add_f32 v230, v230, v234\n\t" \
    "v_add_f32 v231, v231, v235\n\t" \
    "v_cvt_pk_bf16_f32 v228, v228, v229\n\t" \
    "s_waitcnt lgkmcnt(0)\n\t" \
    "v_smfmac_f32_16x16x64_bf16 v[236:239], v[200:203], v[212:219], %[sidx]\n\t" \
    "ds_read_b128 v[212:215], %[wl] offset:6144\n\t" \
    "ds_read_b128 v[216:219], %[wl] offset:7168\n\t" \
    "v_cvt_pk_bf16_f32 v229, v230, v231\n\t" \
    "v_min_u32 v228, -2, v228\n\t" \
    "v_min_u32 v229, -2, v229\n\t" \
    "s_nop 1\n\t" \
    "v_permlane16_swap_b32 v226, v228\n\t" \
    "v_permlane16_swap_b32 v227, v229\n\t" \
    "buffer_store_dwordx4 v[226:229], %[vo], %[rr], %[so] offen sc1\n\t" \
    "s_add_u32 %[so], %[so], 0x10000\n\t" \
    "v_smfmac_f32_16x16x64_bf16 v[248:251], v[204:207], v[144:151], %[sidx]\n\t" \
    "v_mov_b64 v[220:221], 0\n\t" \
    "v_mov_b64 v[222:223], 0\n\t" \
    "v_smfmac_f32_16x16x64_bf16 v[248:251], v[208:211], v[152:159], %[sidx]\n\t" \
    "v_mov_b64 v[224:225], 0\n\t" \
    "v_mov_b64 v[226:227], 0\n\t" \
    "v_smfmac_f32_16x16x64_bf16 v[248:251], v[200:203], v[136:143], %[sidx]\n\t" \
    "v_permlane32_swap_b32 v236, v240\n\t" \
    "v_permlane32_swap_b32 v237, v241\n\t" \
    "v_permlane32_swap_b32 v238, v242\n\t" \
    "v_smfmac_f32_16x16x64_bf16 v[244:247], v[204:207], v[120:127], %[sidx]\n\t" \
    "v_permlane32_swap_b32 v239, v243\n\t" \
    "v_add_f32 v236, v236, v240\n\t" \
    "v_add_f32 v237, v237, v241\n\t" \
    "v_smfmac_f32_16x16x64_bf16 v[244:247], v[208:211], v[128:135], %[sidx]\n\t" \
    "v_add_f32 v238, v238, v242\n\t" \
    "v_add_f32 v239, v239, v243\n\t" \
    "v_cvt_pk_bf16_f32 v242, v236, v237\n\t" \
    "s_waitcnt lgkmcnt(0)\n\t" \
    "v_smfmac_f32_16x16x64_bf16 v[244:247], v[200:203], v[212:219], %[sidx]\n\t" \
    "ds_read_b128 v[212:215], %[wl] offset:8192\n\t" \
    "ds_read_b128 v[216:219], %[wl] offset:9216\n\t" \
    "v_cvt_pk_bf16_f32 v243, v238, v239\n\t" \
    "v_min_u32 v242, -2, v242\n\t" \
    "v_min_u32 v243, -2, v243\n\t" \
    "v_smfmac_f32_16x16x64_bf16 v[224:227], v[204:207], v[184:191], %[sidx]\n\t" \
    "v_and_b32 v232, 0x3fff, %[vo]\n\t" \
    "v_bfe_u32 v233, %[vo], 14, 1\n\t" \
    "v_smfmac_f32_16x16x64_bf16 v[224:227], v[208:211], v[192:199], %[sidx]\n\t" \
    "v_lshl_add_u32 v232, v233, 3, v232\n\t" \
    "v_permlane32_swap_b32 v244, v248\n\t" \
    "v_smfmac_f32_16x16x64_bf16 v[224:227], v[200:203], v[176:183], %[sidx]\n\t" \
    "v_permlane32_swap_b32 v245, v249\n\t" \
    "v_permlane32_swap_b32 v246, v250\n\t" \
    "v_permlane32_swap_b32 v247, v251\n\t" \
    "v_smfmac_f32_16x16x64_bf16 v[220:223], v[204:207], v[160:167], %[sidx]\n\t" \
    "v_add_f32 v244, v244, v248\n\t" \
    "v_add_f32 v245, v245, v249\n\t" \
    "v_add_f32 v246, v246, v250\n\t" \
    "v_smfmac_f32_16x16x64_bf16 v[220:223], v[208:211], v[168:175], %[sidx]\n\t" \
    "v_add_f32 v247, v247, v251\n\t" \
    "v_cvt_pk_bf16_f32 v244, v244, v245\n\t" \
    "v_cvt_pk_bf16_f32 v245, v246, v247\n\t" \
    "s_waitcnt lgkmcnt(0)\n\t" \
    "v_smfmac_f32_16x16x64_bf16 v[220:223], v[200:203], v[212:219], %[sidx]\n\t" \
    "v_min_u32 v244, -2, v244\n\t" \
    "v_min_u32 v245, -2, v245\n\t" \
    "s_nop 1\n\t" \
    "v_permlane16_swap_b32 v242, v244\n\t" \
    "v_permlane16_swap_b32 v243, v245\n\t" \
    "buffer_store_dwordx4 v[242:245], %[vo], %[rr], %[so] offen sc1\n\t" \
    "s_add_u32 %[so], %[so], 0x10000\n\t" \
    "s_nop 0\n\t" \
    "v_permlane32_swap_b32 v220, v224\n\t" \
    "v_permlane32_swap_b32 v221, v225\n\t" \
    "v_permlane32_swap_b32 v222, v226\n\t" \
    "v_permlane32_swap_b32 v223, v227\n\t" \
    "v_add_f32 v220, v220, v224\n\t" \
    "v_add_f32 v221, v221, v225\n\t" \
    "v_add_f32 v222, v222, v226\n\t" \
    "v_add_f32 v223, v223, v227\n\t" \
    "v_cvt_pk_bf16_f32 v220, v220, v221\n\t" \
    "v_cvt_pk_bf16_f32 v221, v222, v223\n\t" \
    "v_min_u32 v220, -2, v220\n\t" \
    "v_min_u32 v221, -2, v221\n\t" \
    "buffer_store_dwordx2 v[220:221], v232, %[rr], %[so] offen sc1\n\t" \
    "s_nop 1\n\t"
#define ASM_SM \
    "v_mbcnt_lo_u32_b32 v252, -1, 0\n\t" \
    "v_mbcnt_hi_u32_b32 v252, -1, v252\n\t" \
    "v_lshlrev_b32 v252, 4, v252\n\t" \
    "v_lshrrev_b32 v253, 1, v252\n\t" \
    "v_xor_b32 v253, v253, v252\n\t" \
    "v_and_b32 v253, 0x20, v253\n\t" \
    "v_mul_u32_u24 v253, 3, v253\n\t" \
    "v_xor_b32 v253, v253, v252\n\t" \
    "ds_read_b128 v[200:203], v253 offset:28672\n\t" \
    "ds_read_b128 v[204:207], v253 offset:29696\n\t" \
    "ds_read_b128 v[208:211], v253 offset:34816\n\t" \
    "ds_read_b128 v[212:215], v253 offset:35840\n\t" \
    "ds_read_b128 v[216:219], v253 offset:30720\n\t" \
    "ds_read_b128 v[220:223], v253 offset:31744\n\t" \
    "ds_read_b128 v[224:227], v253 offset:36864\n\t" \
    "ds_read_b128 v[228:231], v253 offset:37888\n\t" \
    "v_lshrrev_b32 v254, 1, v252\n\t" \
    "v_and_b32 v254, 0x180, v254\n\t" \
    "v_and_or_b32 v254, v252, %[c70], v254\n\t" \
    "v_bfe_i32 v255, v252, 7, 1\n\t" \
    "v_and_b32 v255, 0xaaaa, v255\n\t" \
    "v_or_b32 v255, 0x4444, v255\n\t" \
    "v_mov_b64 v[232:233], 0\n\t" \
    "v_mov_b64 v[234:235], 0\n\t" \
    "v_mov_b64 v[236:237], 0\n\t" \
    "v_mov_b64 v[238:239], 0\n\t" \
    "s_waitcnt lgkmcnt(0)\n\t" \
    "s_barrier\n\t" ASM_ST \
    "s_setprio 3\n\t" \
    "ds_read_b128 v[240:243], v252 offset:10240\n\t" \
    "ds_read_b128 v[244:247], v252 offset:11264\n\t" \
    "ds_read_b128 v[248:251], v252 offset:12288\n\t" \
    "s_waitcnt lgkmcnt(2)\n\t" \
    "v_smfmac_f32_16x16x64_bf16 v[232:235], v[240:243], v[200:207], v255\n\t" \
    "ds_read_b128 v[200:203], v253 offset:32768\n\t" \
    "ds_read_b128 v[204:207], v253 offset:33792\n\t" \
    "v_smfmac_f32_16x16x64_bf16 v[236:239], v[240:243], v[208:215], v255\n\t" \
    "ds_read_b128 v[208:211], v253 offset:38912\n\t" \
    "ds_read_b128 v[212:215], v253 offset:39936\n\t" \
    "s_waitcnt lgkmcnt(5)\n\t" \
    "v_smfmac_f32_16x16x64_bf16 v[232:235], v[244:247], v[216:223], v255\n\t" \
    "ds_read_b128 v[216:219], v254 offset:13312\n\t" \
    "ds_read_b128 v[220:223], v254 offset:13824\n\t" \
    "v_smfmac_f32_16x16x64_bf16 v[236:239], v[244:247], v[224:231], v255\n\t" \
    "ds_read_b128 v[224:227], v254 offset:14336\n\t" \
    "ds_read_b128 v[228:231], v254 offset:14848\n\t" \
    "s_waitcnt lgkmcnt(6)\n\t" \
    "v_smfmac_f32_16x16x64_bf16 v[232:235], v[248:251], v[200:207], v255\n\t" \
    "s_waitcnt lgkmcnt(4)\n\t" \
    "v_smfmac_f32_16x16x64_bf16 v[236:239], v[248:251], v[208:215], v255\n\t" \
    "ds_read_b128 v[208:211], v254 offset:15360\n\t" \
    "ds_read_b128 v[212:215], v254 offset:15872\n\t" \
    "v_mov_b64 v[204:205], 0\n\t" \
    "v_mov_b64 v[206:207], 0\n\t" \
    "s_nop 1\n\t" \
    "s_waitcnt lgkmcnt(4)\n\t" \
    "v_smfmac_f32_16x16x64_bf16 v[204:207], v[240:243], v[216:223], v255\n\t" \
    "s_waitcnt lgkmcnt(2)\n\t" \
    "v_smfmac_f32_16x16x64_bf16 v[204:207], v[244:247], v[224:231], v255\n\t" \
    "v_bfe_u32 v253, v252, 5, 1\n\t" \
    "v_bfe_i32 v202, v252, 4, 1\n\t" \
    "v_lshlrev_b32 v200, 1, v252\n\t" \
    "v_and_b32 v200, 0x180, v200\n\t" \
    "v_permlane32_swap_b32 v232, v234\n\t" \
    "v_permlane32_swap_b32 v233, v235\n\t" \
    "v_permlane32_swap_b32 v236, v238\n\t" \
    "v_permlane32_swap_b32 v237, v239\n\t" \
    "v_lshrrev_b32 v201, 2, v252\n\t" \
    "v_lshrrev_b32 v203, 4, v252\n\t" \
    "v_and_b32 v201, 0x40, v201\n\t" \
    "v_and_or_b32 v201, v203, 32, v201\n\t" \
    "v_and_or_b32 v201, v252, 16, v201\n\t" \
    "v_pk_add_f32 v[232:233], v[232:233], v[234:235]\n\t" \
    "v_pk_add_f32 v[234:235], v[236:237], v[238:239]\n\t" \
    "v_or_b32 v200, v200, v201\n\t" \
    "v_mad_u32_u24 v200, v253, %[cs], v200\n\t" \
    "v_mad_u32_u24 v201, v253, %[cs], v201\n\t" \
    "v_lshrrev_b32 v203, 3, v252\n\t" \
    "v_bfe_u32 v253, v252, 7, 1\n\t" \
    "s_waitcnt lgkmcnt(0)\n\t" \
    "v_smfmac_f32_16x16x64_bf16 v[204:207], v[248:251], v[208:215], v255\n\t" \
    "v_and_or_b32 v201, v203, 8, v201\n\t" \
    "v_and_b32 v202, 0x06060606, v202\n\t" \
    "v_mad_u32_u24 v201, v253, %[cp], v201\n\t" \
    "v_xor_b32 v202, 0x01000504, v202\n\t" \
    "v_max_f32 v236, v232, v234\n\t" \
    "v_max_f32 v237, v233, v235\n\t" \
    "s_nop 1\n\t" \
    "v_permlane32_swap_b32 v204, v206\n\t" \
    "v_permlane32_swap_b32 v205, v207\n\t" \
    "v_add_f32 v204, v204, v206\n\t" \
    "v_add_f32 v205, v205, v207\n\t" \
    "v_max_f32 v236, v236, v204\n\t" \
    "v_max_f32 v237, v237, v205\n\t" \
    "s_nop 0\n\t" \
    "v_max_f32_dpp v236, v236, v236 quad_perm:[1,0,3,2] row_mask:0xf bank_mask:0xf\n\t" \
    "v_max_f32_dpp v237, v237, v237 quad_perm:[1,0,3,2] row_mask:0xf bank_mask:0xf\n\t" \
    "s_nop 0\n\t" \
    "v_max_f32_dpp v236, v236, v236 quad_perm:[2,3,0,1] row_mask:0xf bank_mask:0xf\n\t" \
    "v_max_f32_dpp v237, v237, v237 quad_perm:[2,3,0,1] row_mask:0xf bank_mask:0xf\n\t" \
    "s_nop 0\n\t" \
    "v_max_f32_dpp v236, v236, v236 row_half_mirror row_mask:0xf bank_mask:0xf\n\t" \
    "v_max_f32_dpp v237, v237, v237 row_half_mirror row_mask:0xf bank_mask:0xf\n\t" \
    "s_nop 0\n\t" \
    "v_max_f32_dpp v236, v236, v236 row_mirror row_mask:0xf bank_mask:0xf\n\t" \
    "v_max_f32_dpp v237, v237, v237 row_mirror row_mask:0xf bank_mask:0xf\n\t" \
    "v_mul_f32 v236, %[scl], v236\n\t" \
    "v_mul_f32 v237, %[scl], v237\n\t" \
    "v_fma_f32 v232, v232, %[scl], -v236\n\t" \
    "v_fma_f32 v233, v233, %[scl], -v237\n\t" \
    "v_fma_f32 v234, v234, %[scl], -v236\n\t" \
    "v_fma_f32 v235, v235, %[scl], -v237\n\t" \
    "v_exp_f32 v232, v232\n\t" \
    "v_exp_f32 v233, v233\n\t" \
    "v_fma_f32 v204, v204, %[scl], -v236\n\t" \
    "v_fma_f32 v205, v205, %[scl], -v237\n\t" \
    "v_exp_f32 v234, v234\n\t" \
    "v_exp_f32 v235, v235\n\t" \
    "v_exp_f32 v204, v204\n\t" \
    "v_exp_f32 v205, v205\n\t" \
    "v_add_f32 v238, v232, v234\n\t" \
    "v_add_f32 v239, v233, v235\n\t" \
    "v_fma_f32 v238, v204, 0.5, v238\n\t" \
    "v_fma_f32 v239, v205, 0.5, v239\n\t" \
    "s_nop 0\n\t" \
    "v_add_f32_dpp v238, v238, v238 quad_perm:[1,0,3,2] row_mask:0xf bank_mask:0xf\n\t" \
    "v_add_f32_dpp v239, v239, v239 quad_perm:[1,0,3,2] row_mask:0xf bank_mask:0xf\n\t" \
    "s_nop 0\n\t" \
    "v_add_f32_dpp v238, v238, v238 quad_perm:[2,3,0,1] row_mask:0xf bank_mask:0xf\n\t" \
    "v_add_f32_dpp v239, v239, v239 quad_perm:[2,3,0,1] row_mask:0xf bank_mask:0xf\n\t" \
    "s_nop 0\n\t" \
    "v_add_f32_dpp v238, v238, v238 row_half_mirror row_mask:0xf bank_mask:0xf\n\t" \
    "v_add_f32_dpp v239, v239, v239 row_half_mirror row_mask:0xf bank_mask:0xf\n\t" \
    "s_nop 0\n\t" \
    "v_add_f32_dpp v238, v238, v238 row_mirror row_mask:0xf bank_mask:0xf\n\t" \
    "v_add_f32_dpp v239, v239, v239 row_mirror row_mask:0xf bank_mask:0xf\n\t" \
    "v_rcp_f32 v238, v238\n\t" \
    "v_rcp_f32 v239, v239\n\t" \
    "s_nop 0\n\t" \
    "v_pk_mul_f32 v[232:233], v[232:233], v[238:239]\n\t" \
    "v_pk_mul_f32 v[234:235], v[234:235], v[238:239]\n\t" \
    "v_pk_mul_f32 v[204:205], v[204:205], v[238:239]\n\t" \
    "v_cvt_pk_bf16_f32 v232, |v232|, |v233|\n\t" \
    "v_cvt_pk_bf16_f32 v234, |v234|, |v235|\n\t" \
    "v_cvt_pk_bf16_f32 v204, |v204|, |v205|\n\t" \
    "v_mov_b32_dpp v233, v232 quad_perm:[1,0,3,2] row_mask:0xf bank_mask:0xf\n\t" \
    "v_mov_b32_dpp v235, v234 quad_perm:[1,0,3,2] row_mask:0xf bank_mask:0xf\n\t" \
    "v_mov_b32_dpp v205, v204 quad_perm:[1,0,3,2] row_mask:0xf bank_mask:0xf\n\t" \
    "v_perm_b32 v232, v232, v233, v202\n\t" \
    "v_perm_b32 v234, v234, v235, v202\n\t" \
    "v_perm_b32 v204, v204, v205, v202\n\t" \
    "v_mov_b32_dpp v240, v232 quad_perm:[0,1,0,1] row_mask:0xf bank_mask:0xf\n\t" \
    "v_mov_b32_dpp v241, v234 quad_perm:[0,1,0,1] row_mask:0xf bank_mask:0xf\n\t" \
    "v_mov_b32_dpp v242, v232 quad_perm:[2,3,2,3] row_mask:0xf bank_mask:0xf\n\t" \
    "v_mov_b32_dpp v243, v234 quad_perm:[2,3,2,3] row_mask:0xf bank_mask:0xf\n\t" \
    "buffer_store_dwordx4 v[240:243], v200, %[rr], %[sx] offen sc1\n\t" \
    "v_mov_b32_dpp v244, v204 quad_perm:[0,1,0,1] row_mask:0xf bank_mask:0xf\n\t" \
    "v_mov_b32_dpp v245, v204 quad_perm:[2,3,2,3] row_mask:0xf bank_mask:0xf\n\t" \
    "buffer_store_dwordx4 v[240:243], v200, %[rr], %[sy] offen sc1\n\t" \
    "buffer_store_dwordx2 v[244:245], v201, %[rr], %[sm] offen sc1\n\t" \
    "s_wakeup\n\t" \
    PRISM
#define ASM_P4_0 \
    "ds_read_b128 v[200:203], %[qa] offset:0\n\t" \
    "ds_read_b128 v[204:207], %[qa] offset:1024\n\t" \
    "ds_read_b128 v[208:211], %[qa] offset:2048\n\t" \
    "ds_read_b128 v[212:215], %[qa] offset:3072\n\t" \
    "ds_read_b128 v[216:219], %[qa] offset:4096\n\t" \
    "ds_read_b128 v[220:223], %[qa] offset:5120\n\t" \
    "ds_read_b128 v[224:227], %[qa] offset:12288\n\t" \
    "ds_read_b128 v[228:231], %[qa] offset:13312\n\t" \
    "ds_read_b128 v[232:235], %[qa] offset:14336\n\t" \
    "ds_read_b128 v[236:239], %[qa] offset:15360\n\t" \
    "s_waitcnt lgkmcnt(0)\n\t" \
    "s_barrier\n\t" ASM_ST \
    "v_mfma_f32_16x16x32_bf16 v[240:243], v[224:227], v[200:203], v[240:243]\n\t" \
    "ds_read_b128 v[224:227], %[qa] offset:16384\n\t" \
    "v_mfma_f32_16x16x32_bf16 v[240:243], v[228:231], v[204:207], v[240:243]\n\t" \
    "ds_read_b128 v[228:231], %[qa] offset:17408\n\t" \
    "v_mfma_f32_16x16x32_bf16 v[240:243], v[232:235], v[208:211], v[240:243]\n\t" \
    "ds_read_b128 v[232:235], %[km] offset:0\n\t" \
    "v_mfma_f32_16x16x32_bf16 v[240:243], v[236:239], v[212:215], v[240:243]\n\t" \
    "ds_read_b128 v[236:239], %[km] offset:512\n\t" \
    "s_waitcnt lgkmcnt(3)\n\t" \
    "v_mfma_f32_16x16x32_bf16 v[240:243], v[224:227], v[216:219], v[240:243]\n\t" \
    "ds_read_b128 v[224:227], %[km] offset:1024\n\t" \
    "s_waitcnt lgkmcnt(3)\n\t" \
    "v_mfma_f32_16x16x32_bf16 v[240:243], v[228:231], v[220:223], v[240:243]\n\t" \
    "ds_read_b128 v[228:231], %[km] offset:1536\n\t" \
    "s_waitcnt lgkmcnt(3)\n\t" \
    "v_mfma_f32_16x16x32_bf16 v[248:251], v[232:235], v[200:203], 0\n\t" \
    "ds_read_b128 v[232:235], %[km] offset:2048\n\t" \
    "s_waitcnt lgkmcnt(3)\n\t" \
    "v_mfma_f32_16x16x32_bf16 v[248:251], v[236:239], v[204:207], v[248:251]\n\t" \
    "ds_read_b128 v[236:239], %[km] offset:2560\n\t" \
    "s_waitcnt lgkmcnt(3)\n\t" \
    "v_mfma_f32_16x16x32_bf16 v[248:251], v[224:227], v[208:211], v[248:251]\n\t" \
    "s_waitcnt lgkmcnt(2)\n\t" \
    "v_mfma_f32_16x16x32_bf16 v[248:251], v[228:231], v[212:215], v[248:251]\n\t" \
    "s_waitcnt lgkmcnt(1)\n\t" \
    "v_mfma_f32_16x16x32_bf16 v[248:251], v[232:235], v[216:219], v[248:251]\n\t" \
    "s_waitcnt lgkmcnt(0)\n\t" \
    "v_mfma_f32_16x16x32_bf16 v[248:251], v[236:239], v[220:223], v[248:251]\n\t" \
    "v_max3_f32 v224, v240, v241, v242\n\t" \
    "v_mov_b32 v225, v243\n\t" \
    "v_lshlrev_b32 v201, 23, %[qa]\n\t" \
    "v_and_b32 v201, 0x80000000, v201\n\t" \
    "v_and_b32 v200, 0x3ff, %[qa]\n\t" \
    "v_or_b32 v200, v201, v200\n\t" \
    "s_nop 1\n\t" \
    "v_max3_f32 v224, v224, v248, v249\n\t" \
    "v_max3_f32 v225, v225, v250, v251\n\t" \
    "v_max_f32 v224, v224, v225\n\t" \
    "v_mov_b32 v225, v224\n\t" \
    "s_nop 1\n\t" \
    "v_permlane16_swap_b32 v224, v225\n\t" \
    "v_max_f32 v224, v224, v225\n\t" \
    "v_mov_b32 v225, v224\n\t" \
    "s_nop 1\n\t" \
    "v_permlane32_swap_b32 v224, v225\n\t" \
    "v_max_f32 v224, v224, v225\n\t" \
    "v_mul_f32 v224, %[scl], v224\n\t" \
    "v_fma_f32 v240, v240, %[scl], -v224\n\t" \
    "v_fma_f32 v241, v241, %[scl], -v224\n\t" \
    "v_exp_f32 v240, v240\n\t" \
    "v_fma_f32 v242, v242, %[scl], -v224\n\t" \
    "v_exp_f32 v241, v241\n\t" \
    "v_fma_f32 v243, v243, %[scl], -v224\n\t" \
    "v_exp_f32 v242, v242\n\t" \
    "v_fma_f32 v248, v248, %[scl], -v224\n\t" \
    "v_exp_f32 v243, v243\n\t" \
    "v_fma_f32 v249, v249, %[scl], -v224\n\t" \
    "v_exp_f32 v248, v248\n\t" \
    "v_fma_f32 v250, v250, %[scl], -v224\n\t" \
    "v_exp_f32 v249, v249\n\t" \
    "v_fma_f32 v251, v251, %[scl], -v224\n\t" \
    "v_exp_f32 v250, v250\n\t" \
    "v_exp_f32 v251, v251\n\t" \
    "v_add_f32 v224, v240, v241\n\t" \
    "v_add_f32 v225, v242, v243\n\t" \
    "v_add_f32 v232, v248, v249\n\t" \
    "v_add_f32 v233, v250, v251\n\t" \
    "v_add_f32 v224, v224, v225\n\t" \
    "v_add_f32 v232, v232, v233\n\t" \
    "v_fma_f32 v224, v232, 0.5, v224\n\t" \
    "v_mov_b32 v225, v224\n\t" \
    "s_nop 1\n\t" \
    "v_permlane16_swap_b32 v224, v225\n\t" \
    "v_add_f32 v224, v224, v225\n\t" \
    "v_mov_b32 v225, v224\n\t" \
    "s_nop 1\n\t" \
    "v_permlane32_swap_b32 v224, v225\n\t" \
    "v_add_f32 v224, v224, v225\n\t" \
    "v_rcp_f32 v224, v224\n\t" \
    "s_nop 0\n\t" \
    "v_mul_f32 v240, v240, v224\n\t" \
    "v_mul_f32 v241, v241, v224\n\t" \
    "v_mul_f32 v242, v242, v224\n\t" \
    "v_mul_f32 v243, v243, v224\n\t" \
    "v_mul_f32 v248, v248, v224\n\t" \
    "v_mul_f32 v249, v249, v224\n\t" \
    "v_mul_f32 v250, v250, v224\n\t" \
    "v_mul_f32 v251, v251, v224\n\t" \
    "v_cvt_pk_bf16_f32 v228, |v240|, |v241|\n\t" \
    "v_cvt_pk_bf16_f32 v229, |v242|, |v243|\n\t" \
    "v_cvt_pk_bf16_f32 v232, |v248|, |v249|\n\t" \
    "v_cvt_pk_bf16_f32 v233, |v250|, |v251|\n\t" \
    "v_permlane16_swap_b32 v228, v230\n\t" \
    "v_permlane16_swap_b32 v229, v231\n\t" \
    "buffer_store_dwordx4 v[228:231], v200, %[rr], %[so] offen sc1\n\t" \
    "buffer_store_dwordx2 v[232:233], %[pm], %[rr], %[so] offen sc1\n\t" \
    "s_nop 1\n\t"
#define ASM_P4_1 \
    "ds_read_b128 v[200:203], %[qa] offset:0\n\t" \
    "ds_read_b128 v[204:207], %[qa] offset:1024\n\t" \
    "ds_read_b128 v[208:211], %[qa] offset:2048\n\t" \
    "ds_read_b128 v[212:215], %[qa] offset:3072\n\t" \
    "ds_read_b128 v[216:219], %[qa] offset:4096\n\t" \
    "ds_read_b128 v[220:223], %[qa] offset:5120\n\t" \
    "ds_read_b128 v[224:227], %[qa] offset:12288\n\t" \
    "ds_read_b128 v[228:231], %[qa] offset:13312\n\t" \
    "ds_read_b128 v[232:235], %[qa] offset:14336\n\t" \
    "ds_read_b128 v[236:239], %[qa] offset:15360\n\t" \
    "s_waitcnt lgkmcnt(0)\n\t" \
    "s_barrier\n\t" ASM_ST \
    "v_mfma_f32_16x16x32_bf16 v[240:243], v[224:227], v[200:203], v[240:243]\n\t" \
    "ds_read_b128 v[224:227], %[qa] offset:16384\n\t" \
    "v_mfma_f32_16x16x32_bf16 v[240:243], v[228:231], v[204:207], v[240:243]\n\t" \
    "ds_read_b128 v[228:231], %[qa] offset:17408\n\t" \
    "v_mfma_f32_16x16x32_bf16 v[240:243], v[232:235], v[208:211], v[240:243]\n\t" \
    "ds_read_b128 v[232:235], %[qa] offset:6144\n\t" \
    "v_mfma_f32_16x16x32_bf16 v[240:243], v[236:239], v[212:215], v[240:243]\n\t" \
    "ds_read_b128 v[236:239], %[qa] offset:7168\n\t" \
    "s_waitcnt lgkmcnt(3)\n\t" \
    "v_mfma_f32_16x16x32_bf16 v[240:243], v[224:227], v[216:219], v[240:243]\n\t" \
    "ds_read_b128 v[224:227], %[qa] offset:8192\n\t" \
    "s_waitcnt lgkmcnt(3)\n\t" \
    "v_mfma_f32_16x16x32_bf16 v[240:243], v[228:231], v[220:223], v[240:243]\n\t" \
    "ds_read_b128 v[228:231], %[qa] offset:9216\n\t" \
    "s_waitcnt lgkmcnt(3)\n\t" \
    "v_mfma_f32_16x16x32_bf16 v[244:247], v[232:235], v[200:203], 0\n\t" \
    "ds_read_b128 v[232:235], %[qa] offset:10240\n\t" \
    "s_waitcnt lgkmcnt(3)\n\t" \
    "v_mfma_f32_16x16x32_bf16 v[244:247], v[236:239], v[204:207], v[244:247]\n\t" \
    "ds_read_b128 v[236:239], %[qa] offset:11264\n\t" \
    "s_waitcnt lgkmcnt(3)\n\t" \
    "v_mfma_f32_16x16x32_bf16 v[244:247], v[224:227], v[208:211], v[244:247]\n\t" \
    "ds_read_b128 v[224:227], %[km] offset:0\n\t" \
    "s_waitcnt lgkmcnt(3)\n\t" \
    "v_mfma_f32_16x16x32_bf16 v[244:247], v[228:231], v[212:215], v[244:247]\n\t" \
    "ds_read_b128 v[228:231], %[km] offset:512\n\t" \
    "s_waitcnt lgkmcnt(3)\n\t" \
    "v_mfma_f32_16x16x32_bf16 v[244:247], v[232:235], v[216:219], v[244:247]\n\t" \
    "ds_read_b128 v[232:235], %[km] offset:1024\n\t" \
    "s_waitcnt lgkmcnt(3)\n\t" \
    "v_mfma_f32_16x16x32_bf16 v[244:247], v[236:239], v[220:223], v[244:247]\n\t" \
    "ds_read_b128 v[236:239], %[km] offset:1536\n\t" \
    "s_waitcnt lgkmcnt(3)\n\t" \
    "v_mfma_f32_16x16x32_bf16 v[248:251], v[224:227], v[200:203], 0\n\t" \
    "ds_read_b128 v[224:227], %[km] offset:2048\n\t" \
    "s_waitcnt lgkmcnt(3)\n\t" \
    "v_mfma_f32_16x16x32_bf16 v[248:251], v[228:231], v[204:207], v[248:251]\n\t" \
    "ds_read_b128 v[228:231], %[km] offset:2560\n\t" \
    "s_waitcnt lgkmcnt(3)\n\t" \
    "v_mfma_f32_16x16x32_bf16 v[248:251], v[232:235], v[208:211], v[248:251]\n\t" \
    "s_waitcnt lgkmcnt(2)\n\t" \
    "v_mfma_f32_16x16x32_bf16 v[248:251], v[236:239], v[212:215], v[248:251]\n\t" \
    "s_waitcnt lgkmcnt(1)\n\t" \
    "v_mfma_f32_16x16x32_bf16 v[248:251], v[224:227], v[216:219], v[248:251]\n\t" \
    "s_waitcnt lgkmcnt(0)\n\t" \
    "v_mfma_f32_16x16x32_bf16 v[248:251], v[228:231], v[220:223], v[248:251]\n\t" \
    "v_max3_f32 v224, v240, v241, v242\n\t" \
    "v_max3_f32 v225, v244, v245, v246\n\t" \
    "v_max3_f32 v224, v224, v243, v247\n\t" \
    "v_and_b32 v200, 0x3ff, %[qa]\n\t" \
    "s_nop 3\n\t" \
    "v_max3_f32 v224, v224, v248, v249\n\t" \
    "v_max3_f32 v225, v225, v250, v251\n\t" \
    "v_max_f32 v224, v224, v225\n\t" \
    "v_mov_b32 v225, v224\n\t" \
    "s_nop 1\n\t" \
    "v_permlane16_swap_b32 v224, v225\n\t" \
    "v_max_f32 v224, v224, v225\n\t" \
    "v_mov_b32 v225, v224\n\t" \
    "s_nop 1\n\t" \
    "v_permlane32_swap_b32 v224, v225\n\t" \
    "v_max_f32 v224, v224, v225\n\t" \
    "v_mul_f32 v224, %[scl], v224\n\t" \
    "v_fma_f32 v240, v240, %[scl], -v224\n\t" \
    "v_fma_f32 v241, v241, %[scl], -v224\n\t" \
    "v_exp_f32 v240, v240\n\t" \
    "v_fma_f32 v242, v242, %[scl], -v224\n\t" \
    "v_exp_f32 v241, v241\n\t" \
    "v_fma_f32 v243, v243, %[scl], -v224\n\t" \
    "v_exp_f32 v242, v242\n\t" \
    "v_fma_f32 v244, v244, %[scl], -v224\n\t" \
    "v_exp_f32 v243, v243\n\t" \
    "v_fma_f32 v245, v245, %[scl], -v224\n\t" \
    "v_exp_f32 v244, v244\n\t" \
    "v_fma_f32 v246, v246, %[scl], -v224\n\t" \
    "v_exp_f32 v245, v245\n\t" \
    "v_fma_f32 v247, v247, %[scl], -v224\n\t" \
    "v_exp_f32 v246, v246\n\t" \
    "v_fma_f32 v248, v248, %[scl], -v224\n\t" \
    "v_exp_f32 v247, v247\n\t" \
    "v_fma_f32 v249, v249, %[scl], -v224\n\t" \
    "v_exp_f32 v248, v248\n\t" \
    "v_fma_f32 v250, v250, %[scl], -v224\n\t" \
    "v_exp_f32 v249, v249\n\t" \
    "v_fma_f32 v251, v251, %[scl], -v224\n\t" \
    "v_exp_f32 v250, v250\n\t" \
    "v_exp_f32 v251, v251\n\t" \
    "v_add_f32 v224, v240, v241\n\t" \
    "v_add_f32 v225, v242, v243\n\t" \
    "v_add_f32 v226, v244, v245\n\t" \
    "v_add_f32 v227, v246, v247\n\t" \
    "v_add_f32 v232, v248, v249\n\t" \
    "v_add_f32 v233, v250, v251\n\t" \
    "v_add_f32 v224, v224, v225\n\t" \
    "v_add_f32 v226, v226, v227\n\t" \
    "v_add_f32 v232, v232, v233\n\t" \
    "v_add_f32 v224, v224, v226\n\t" \
    "v_fma_f32 v224, v232, 0.5, v224\n\t" \
    "v_mov_b32 v225, v224\n\t" \
    "s_nop 1\n\t" \
    "v_permlane16_swap_b32 v224, v225\n\t" \
    "v_add_f32 v224, v224, v225\n\t" \
    "v_mov_b32 v225, v224\n\t" \
    "s_nop 1\n\t" \
    "v_permlane32_swap_b32 v224, v225\n\t" \
    "v_add_f32 v224, v224, v225\n\t" \
    "v_rcp_f32 v224, v224\n\t" \
    "s_nop 0\n\t" \
    "v_mul_f32 v240, v240, v224\n\t" \
    "v_mul_f32 v241, v241, v224\n\t" \
    "v_mul_f32 v242, v242, v224\n\t" \
    "v_mul_f32 v243, v243, v224\n\t" \
    "v_mul_f32 v244, v244, v224\n\t" \
    "v_mul_f32 v245, v245, v224\n\t" \
    "v_mul_f32 v246, v246, v224\n\t" \
    "v_mul_f32 v247, v247, v224\n\t" \
    "v_mul_f32 v248, v248, v224\n\t" \
    "v_mul_f32 v249, v249, v224\n\t" \
    "v_mul_f32 v250, v250, v224\n\t" \
    "v_mul_f32 v251, v251, v224\n\t" \
    "v_cvt_pk_bf16_f32 v230, |v240|, |v241|\n\t" \
    "v_cvt_pk_bf16_f32 v231, |v242|, |v243|\n\t" \
    "v_cvt_pk_bf16_f32 v228, |v244|, |v245|\n\t" \
    "v_cvt_pk_bf16_f32 v229, |v246|, |v247|\n\t" \
    "v_cvt_pk_bf16_f32 v232, |v248|, |v249|\n\t" \
    "v_cvt_pk_bf16_f32 v233, |v250|, |v251|\n\t" \
    "buffer_store_dwordx4 v[228:231], v200, %[rr], %[so] offen sc1\n\t" \
    "buffer_store_dwordx2 v[232:233], %[pm], %[rr], %[so] offen sc1\n\t" \
    "s_nop 1\n\t"
#define ASM_RCRIT \
    "v_mov_b64 v[176:177], -1\n\t" \
    "v_mov_b64 v[178:179], -1\n\t" \
    "v_mov_b64 v[180:181], -1\n\t" \
    "v_mov_b64 v[182:183], -1\n\t" \
    "v_mov_b64 v[184:185], -1\n\t" \
    "v_mov_b64 v[186:187], -1\n\t" \
    "v_mov_b64 v[188:189], -1\n\t" \
    "v_mov_b64 v[190:191], -1\n\t" \
    "v_mov_b64 v[192:193], -1\n\t" \
    "v_mov_b64 v[194:195], -1\n\t" \
    ".Lfa%=:\n\t" \
    "s_mov_b32 %[a], 0\n\t" \
    "v_max_u32 v196, v176, v177\n\t" \
    "v_max3_u32 v196, v178, v179, v196\n\t" \
    "v_cmp_eq_u32 vcc, -1, v196\n\t" \
    "s_cbranch_vccz .Lga0_%=\n\t" \
    "buffer_load_dwordx4 v[176:179], %[ov], %[rs], %[sv] offen sc1\n\t" \
    "s_mov_b32 %[a], 1\n\t" \
    ".Lga0_%=:\n\t" \
    "v_max_u32 v196, v180, v181\n\t" \
    "v_max3_u32 v196, v182, v183, v196\n\t" \
    "v_cmp_eq_u32 vcc, -1, v196\n\t" \
    "s_cbranch_vccz .Lga1_%=\n\t" \
    "buffer_load_dwordx4 v[180:183], %[ov], %[rs], %[sv] offen offset:1024 sc1\n\t" \
    "s_mov_b32 %[a], 1\n\t" \
    ".Lga1_%=:\n\t" \
    "s_cmp_eq_u32 %[a], 0\n\t" \
    "s_cbranch_scc1 .Lha%=\n\t" \
    "s_waitcnt vmcnt(12)\n\t" \
    ".Lia%=:\n\t" \
    "v_max_u32 v196, v176, v177\n\t" \
    "v_max3_u32 v196, v178, v179, v196\n\t" \
    "v_max3_u32 v196, v180, v181, v196\n\t" \
    "v_max3_u32 v196, v182, v183, v196\n\t" \
    "v_cmp_eq_u32 vcc, -1, v196\n\t" \
    "s_cbranch_vccz .Lha%=\n\t" \
    "s_nop 7\n\t" \
    "v_max_u32 v196, v176, v177\n\t" \
    "v_max3_u32 v196, v178, v179, v196\n\t" \
    "v_max3_u32 v196, v180, v181, v196\n\t" \
    "v_max3_u32 v196, v182, v183, v196\n\t" \
    "v_cmp_eq_u32 vcc, -1, v196\n\t" \
    "s_cbranch_vccz .Lha%=\n\t" \
    "s_nop 7\n\t" \
    "v_max_u32 v196, v176, v177\n\t" \
    "v_max3_u32 v196, v178, v179, v196\n\t" \
    "v_max3_u32 v196, v180, v181, v196\n\t" \
    "v_max3_u32 v196, v182, v183, v196\n\t" \
    "v_cmp_eq_u32 vcc, -1, v196\n\t" \
    "s_cbranch_vccz .Lha%=\n\t" \
    "s_nop 7\n\t" \
    "v_max_u32 v196, v176, v177\n\t" \
    "v_max3_u32 v196, v178, v179, v196\n\t" \
    "v_max3_u32 v196, v180, v181, v196\n\t" \
    "v_max3_u32 v196, v182, v183, v196\n\t" \
    "v_cmp_eq_u32 vcc, -1, v196\n\t" \
    "s_cbranch_vccz .Lha%=\n\t" \
    "s_nop 7\n\t" \
    "s_sub_u32 %[n], %[n], 1\n\t" \
    "s_cmp_gt_i32 %[n], 0\n\t" \
    "s_cbranch_scc1 .Lfa%=\n\t" \
    ".Lha%=:\n\t" \
    ASM_ST3 \
    "buffer_load_dwordx4 v[184:187], %[o0], %[rs], %[sp] offen sc1\n\t" \
    "buffer_load_dwordx4 v[188:191], %[o0], %[rs], %[sp] offen offset:1024 sc1\n\t" \
    "buffer_load_dwordx4 v[192:195], %[o2], %[rs], %[sp] offen sc1\n\t" \
    "s_waitcnt vmcnt(0)\n\t" \
    "v_max_u32 v196, v184, v185\n\t" \
    "v_max3_u32 v196, v186, v187, v196\n\t" \
    "v_max3_u32 v196, v188, v189, v196\n\t" \
    "v_max3_u32 v196, v190, v191, v196\n\t" \
    "v_max3_u32 v196, v192, v193, v196\n\t" \
    "v_max3_u32 v196, v194, v195, v196\n\t" \
    "v_cmp_eq_u32 vcc, -1, v196\n\t" \
    "s_cbranch_vccz .Lhb%=\n\t" \
    ".Lfb%=:\n\t" \
    "s_mov_b32 %[a], 0\n\t" \
    "v_max_u32 v196, v184, v185\n\t" \
    "v_max3_u32 v196, v186, v187, v196\n\t" \
    "v_cmp_eq_u32 vcc, -1, v196\n\t" \
    "s_cbranch_vccz .Lgb0_%=\n\t" \
    "buffer_load_dwordx4 v[184:187], %[o0], %[rs], %[sp] offen sc1\n\t" \
    "s_mov_b32 %[a], 1\n\t" \
    ".Lgb0_%=:\n\t" \
    "v_max_u32 v196, v188, v189\n\t" \
    "v_max3_u32 v196, v190, v191, v196\n\t" \
    "v_cmp_eq_u32 vcc, -1, v196\n\t" \
    "s_cbranch_vccz .Lgb1_%=\n\t" \
    "buffer_load_dwordx4 v[188:191], %[o0], %[rs], %[sp] offen offset:1024 sc1\n\t" \
    "s_mov_b32 %[a], 1\n\t" \
    ".Lgb1_%=:\n\t" \
    "v_max_u32 v196, v192, v193\n\t" \
    "v_max3_u32 v196, v194, v195, v196\n\t" \
    "v_cmp_eq_u32 vcc, -1, v196\n\t" \
    "s_cbranch_vccz .Lgb2_%=\n\t" \
    "buffer_load_dwordx4 v[192:195], %[o2], %[rs], %[sp] offen sc1\n\t" \
    "s_mov_b32 %[a], 1\n\t" \
    ".Lgb2_%=:\n\t" \
    "s_cmp_eq_u32 %[a], 0\n\t" \
    "s_cbranch_scc1 .Lhb%=\n\t" \
    "s_waitcnt vmcnt(12)\n\t" \
    ".Lib%=:\n\t" \
    "v_max_u32 v196, v184, v185\n\t" \
    "v_max3_u32 v196, v186, v187, v196\n\t" \
    "v_max3_u32 v196, v188, v189, v196\n\t" \
    "v_max3_u32 v196, v190, v191, v196\n\t" \
    "v_max3_u32 v196, v192, v193, v196\n\t" \
    "v_max3_u32 v196, v194, v195, v196\n\t" \
    "v_cmp_eq_u32 vcc, -1, v196\n\t" \
    "s_cbranch_vccz .Lhb%=\n\t" \
    "s_nop 7\n\t" \
    "v_max_u32 v196, v184, v185\n\t" \
    "v_max3_u32 v196, v186, v187, v196\n\t" \
    "v_max3_u32 v196, v188, v189, v196\n\t" \
    "v_max3_u32 v196, v190, v191, v196\n\t" \
    "v_max3_u32 v196, v192, v193, v196\n\t" \
    "v_max3_u32 v196, v194, v195, v196\n\t" \
    "v_cmp_eq_u32 vcc, -1, v196\n\t" \
    "s_cbranch_vccz .Lhb%=\n\t" \
    "s_nop 7\n\t" \
    "v_max_u32 v196, v184, v185\n\t" \
    "v_max3_u32 v196, v186, v187, v196\n\t" \
    "v_max3_u32 v196, v188, v189, v196\n\t" \
    "v_max3_u32 v196, v190, v191, v196\n\t" \
    "v_max3_u32 v196, v192, v193, v196\n\t" \
    "v_max3_u32 v196, v194, v195, v196\n\t" \
    "v_cmp_eq_u32 vcc, -1, v196\n\t" \
    "s_cbranch_vccz .Lhb%=\n\t" \
    "s_nop 7\n\t" \
    "v_max_u32 v196, v184, v185\n\t" \
    "v_max3_u32 v196, v186, v187, v196\n\t" \
    "v_max3_u32 v196, v188, v189, v196\n\t" \
    "v_max3_u32 v196, v190, v191, v196\n\t" \
    "v_max3_u32 v196, v192, v193, v196\n\t" \
    "v_max3_u32 v196, v194, v195, v196\n\t" \
    "v_cmp_eq_u32 vcc, -1, v196\n\t" \
    "s_cbranch_vccz .Lhb%=\n\t" \
    "s_nop 7\n\t" \
    "s_sub_u32 %[n], %[n], 1\n\t" \
    "s_cmp_gt_i32 %[n], 0\n\t" \
    "s_cbranch_scc1 .Lfb%=\n\t" \
    ".Lhb%=:\n\t" \
    ASM_ST2 \
    "v_mfma_f32_16x16x32_bf16 v[160:163], v[192:195], v[176:179], 0\n\t" \
    "v_mfma_f32_16x16x32_bf16 v[164:167], v[192:195], v[180:183], 0\n\t" \
    "v_smfmac_f32_16x16x64_bf16 v[160:163], v[184:187], v[128:135], %[sidx]\n\t" \
    "v_smfmac_f32_16x16x64_bf16 v[164:167], v[184:187], v[144:151], %[sidx]\n\t" \
    "v_smfmac_f32_16x16x64_bf16 v[160:163], v[188:191], v[136:143], %[sidx]\n\t" \
    "v_smfmac_f32_16x16x64_bf16 v[164:167], v[188:191], v[152:159], %[sidx]\n\t" \
    "s_cmp_eq_u32 %[pub], 0\n\t" \
    "s_nop 7\n\t" \
    "v_permlane32_swap_b32 v160, v164\n\t" \
    "v_permlane32_swap_b32 v161, v165\n\t" \
    "v_permlane32_swap_b32 v162, v166\n\t" \
    "v_permlane32_swap_b32 v163, v167\n\t" \
    "v_add_f32 v160, v160, v164\n\t" \
    "v_add_f32 v161, v161, v165\n\t" \
    "ds_write2_b32 %[rw], v160, v161 offset1:80\n\t" \
    "v_add_f32 v162, v162, v166\n\t" \
    "v_add_f32 v163, v163, v167\n\t" \
    "ds_write2_b32 %[rw], v162, v163 offset0:160 offset1:240\n\t" \
    "s_waitcnt lgkmcnt(0)\n\t" \
    "s_barrier\n\t" ASM_ST \
    "s_cbranch_scc1 .Lre%=\n\t" \
    "ds_read_b128 v[200:203], %[rd] offset:0\n\t" \
    "ds_read_b128 v[204:207], %[rd] offset:16\n\t" \
    "s_waitcnt lgkmcnt(1)\n\t" \
    "v_pk_add_f32 v[200:201], v[200:201], v[202:203]\n\t" \
    "s_waitcnt lgkmcnt(0)\n\t" \
    "v_pk_add_f32 v[204:205], v[204:205], v[206:207]\n\t" \
    "v_pk_add_f32 v[200:201], v[200:201], v[204:205]\n\t" \
    "s_nop 1\n\t" \
    "v_permlane32_swap_b32 v200, v201\n\t" \
    "v_add_f32 v202, v200, v201\n\t" \
    "v_add_f32 v203, v200, v201\n\t" \
    "s_nop 1\n\t" \
    "v_permlane32_swap_b32 v202, v203\n\t" \
    "v_cvt_pk_bf16_f32 v200, v202, v203\n\t" \
    "v_min_u32 v200, -2, v200\n\t" \
    "buffer_store_dword v200, %[oo], %[rs], %[so] offen sc1\n\t" \
    ".Lre%=:\n\t"
#define ASM_RX \
    "s_cmp_eq_u32 %[fin], 0\n\t" \
    "s_cbranch_scc1 .Lx0%=\n\t" \
    "buffer_load_dwordx2 v[250:251], %[xo], %[rx], %[sx] offen sc1\n\t" \
    ".Lx0%=:\n\t" \
    "buffer_load_dword v248, %[fo], %[rs], %[fs] offen sc1\n\t" \
    "buffer_load_dwordx4 v[200:203], %[l16], %[rs], %[s0] offen offset:0 sc1\n\t" \
    "buffer_load_dwordx4 v[204:207], %[l16], %[rs], %[s0] offen offset:1024 sc1\n\t" \
    "buffer_load_dwordx4 v[208:211], %[l16], %[rs], %[s0] offen offset:2048 sc1\n\t" \
    "buffer_load_dwordx4 v[212:215], %[l16], %[rs], %[s0] offen offset:3072 sc1\n\t" \
    "buffer_load_dwordx4 v[216:219], %[l16], %[rs], %[s1] offen offset:0 sc1\n\t" \
    "buffer_load_dwordx4 v[220:223], %[l16], %[rs], %[s1] offen offset:1024 sc1\n\t" \
    "buffer_load_dwordx4 v[224:227], %[l16], %[rs], %[s1] offen offset:2048 sc1\n\t" \
    "buffer_load_dwordx4 v[228:231], %[l16], %[rs], %[s1] offen offset:3072 sc1\n\t" \
    "v_mov_b64 v[96:97], v[128:129]\n\t" \
    "v_mov_b64 v[98:99], v[132:133]\n\t" \
    "v_mov_b64 v[100:101], v[136:137]\n\t" \
    "v_mov_b64 v[102:103], v[140:141]\n\t" \
    "v_mov_b64 v[104:105], v[144:145]\n\t" \
    "v_mov_b64 v[106:107], v[148:149]\n\t" \
    "v_mov_b64 v[108:109], v[152:153]\n\t" \
    "v_mov_b64 v[110:111], v[156:157]\n\t" \
    "s_movk_i32 %[n], 0x200\n\t" \
    "s_nop 1\n\t" \
    "v_permlane16_swap_b32 v96, v98\n\t" \
    "v_permlane16_swap_b32 v97, v99\n\t" \
    "v_permlane16_swap_b32 v100, v102\n\t" \
    "v_permlane16_swap_b32 v101, v103\n\t" \
    "v_permlane16_swap_b32 v104, v106\n\t" \
    "v_permlane16_swap_b32 v105, v107\n\t" \
    "v_permlane16_swap_b32 v108, v110\n\t" \
    "v_permlane16_swap_b32 v109, v111\n\t" \
    "s_waitcnt vmcnt(7)\n\t" \
    "v_max_u32 v196, v200, v201\n\t" \
    "v_max3_u32 v196, v202, v203, v196\n\t" \
    "s_waitcnt vmcnt(6)\n\t" \
    "v_max3_u32 v196, v204, v205, v196\n\t" \
    "v_max3_u32 v196, v206, v207, v196\n\t" \
    "s_waitcnt vmcnt(5)\n\t" \
    "v_max3_u32 v196, v208, v209, v196\n\t" \
    "v_max3_u32 v196, v210, v211, v196\n\t" \
    "s_waitcnt vmcnt(4)\n\t" \
    "v_max3_u32 v196, v212, v213, v196\n\t" \
    "v_max3_u32 v196, v214, v215, v196\n\t" \
    "s_waitcnt vmcnt(3)\n\t" \
    "v_max3_u32 v196, v216, v217, v196\n\t" \
    "v_max3_u32 v196, v218, v219, v196\n\t" \
    "s_waitcnt vmcnt(2)\n\t" \
    "v_max3_u32 v196, v220, v221, v196\n\t" \
    "v_max3_u32 v196, v222, v223, v196\n\t" \
    "s_waitcnt vmcnt(1)\n\t" \
    "v_max3_u32 v196, v224, v225, v196\n\t" \
    "v_max3_u32 v196, v226, v227, v196\n\t" \
    "s_waitcnt vmcnt(0)\n\t" \
    "v_max3_u32 v196, v228, v229, v196\n\t" \
    "v_max3_u32 v196, v230, v231, v196\n\t" \
    "v_cmp_eq_u32 vcc, -1, v196\n\t" \
    "s_cbranch_vccz .Lxd%=\n\t" \
    ".Lxl%=:\n\t" \
    "s_mov_b32 %[a], 0\n\t" \
    "v_max_u32 v196, v200, v201\n\t" \
    "v_max3_u32 v196, v202, v203, v196\n\t" \
    "v_cmp_eq_u32 vcc, -1, v196\n\t" \
    "s_cbranch_vccz .Lxs0_%=\n\t" \
    "buffer_load_dwordx4 v[200:203], %[l16], %[rs], %[s0] offen offset:0 sc1\n\t" \
    "s_mov_b32 %[a], 1\n\t" \
    ".Lxs0_%=:\n\t" \
    "v_max_u32 v196, v204, v205\n\t" \
    "v_max3_u32 v196, v206, v207, v196\n\t" \
    "v_cmp_eq_u32 vcc, -1, v196\n\t" \
    "s_cbranch_vccz .Lxs1_%=\n\t" \
    "buffer_load_dwordx4 v[204:207], %[l16], %[rs], %[s0] offen offset:1024 sc1\n\t" \
    "s_mov_b32 %[a], 1\n\t" \
    ".Lxs1_%=:\n\t" \
    "v_max_u32 v196, v208, v209\n\t" \
    "v_max3_u32 v196, v210, v211, v196\n\t" \
    "v_cmp_eq_u32 vcc, -1, v196\n\t" \
    "s_cbranch_vccz .Lxs2_%=\n\t" \
    "buffer_load_dwordx4 v[208:211], %[l16], %[rs], %[s0] offen offset:2048 sc1\n\t" \
    "s_mov_b32 %[a], 1\n\t" \
    ".Lxs2_%=:\n\t" \
    "v_max_u32 v196, v212, v213\n\t" \
    "v_max3_u32 v196, v214, v215, v196\n\t" \
    "v_cmp_eq_u32 vcc, -1, v196\n\t" \
    "s_cbranch_vccz .Lxs3_%=\n\t" \
    "buffer_load_dwordx4 v[212:215], %[l16], %[rs], %[s0] offen offset:3072 sc1\n\t" \
    "s_mov_b32 %[a], 1\n\t" \
    ".Lxs3_%=:\n\t" \
    "v_max_u32 v196, v216, v217\n\t" \
    "v_max3_u32 v196, v218, v219, v196\n\t" \
    "v_cmp_eq_u32 vcc, -1, v196\n\t" \
    "s_cbranch_vccz .Lxs4_%=\n\t" \
    "buffer_load_dwordx4 v[216:219], %[l16], %[rs], %[s1] offen offset:0 sc1\n\t" \
    "s_mov_b32 %[a], 1\n\t" \
    ".Lxs4_%=:\n\t" \
    "v_max_u32 v196, v220, v221\n\t" \
    "v_max3_u32 v196, v222, v223, v196\n\t" \
    "v_cmp_eq_u32 vcc, -1, v196\n\t" \
    "s_cbranch_vccz .Lxs5_%=\n\t" \
    "buffer_load_dwordx4 v[220:223], %[l16], %[rs], %[s1] offen offset:1024 sc1\n\t" \
    "s_mov_b32 %[a], 1\n\t" \
    ".Lxs5_%=:\n\t" \
    "v_max_u32 v196, v224, v225\n\t" \
    "v_max3_u32 v196, v226, v227, v196\n\t" \
    "v_cmp_eq_u32 vcc, -1, v196\n\t" \
    "s_cbranch_vccz .Lxs6_%=\n\t" \
    "buffer_load_dwordx4 v[224:227], %[l16], %[rs], %[s1] offen offset:2048 sc1\n\t" \
    "s_mov_b32 %[a], 1\n\t" \
    ".Lxs6_%=:\n\t" \
    "v_max_u32 v196, v228, v229\n\t" \
    "v_max3_u32 v196, v230, v231, v196\n\t" \
    "v_cmp_eq_u32 vcc, -1, v196\n\t" \
    "s_cbranch_vccz .Lxs7_%=\n\t" \
    "buffer_load_dwordx4 v[228:231], %[l16], %[rs], %[s1] offen offset:3072 sc1\n\t" \
    "s_mov_b32 %[a], 1\n\t" \
    ".Lxs7_%=:\n\t" \
    "s_cmp_eq_u32 %[a], 0\n\t" \
    "s_cbranch_scc1 .Lxd%=\n\t" \
    "s_waitcnt vmcnt(0)\n\t" \
    "s_sub_u32 %[n], %[n], 1\n\t" \
    "s_cmp_lg_u32 %[n], 0\n\t" \
    "s_cbranch_scc1 .Lxl%=\n\t" \
    "s_branch .Lxe%=\n\t" \
    ".Lxd%=:\n\t" \
    ASM_ST2 \
    "v_mfma_f32_16x16x32_bf16 v[120:123], v[128:131], v[200:203], 0\n\t" \
    "v_mfma_f32_16x16x32_bf16 v[124:127], v[144:147], v[200:203], 0\n\t" \
    "v_mfma_f32_16x16x32_bf16 v[120:123], v[132:135], v[204:207], v[120:123]\n\t" \
    "v_mfma_f32_16x16x32_bf16 v[124:127], v[148:151], v[204:207], v[124:127]\n\t" \
    "v_mfma_f32_16x16x32_bf16 v[120:123], v[136:139], v[208:211], v[120:123]\n\t" \
    "v_mfma_f32_16x16x32_bf16 v[124:127], v[152:155], v[208:211], v[124:127]\n\t" \
    "v_mfma_f32_16x16x32_bf16 v[120:123], v[140:143], v[212:215], v[120:123]\n\t" \
    "v_mfma_f32_16x16x32_bf16 v[124:127], v[156:159], v[212:215], v[124:127]\n\t" \
    "v_mfma_f32_16x16x32_bf16 v[112:115], v[96:99], v[220:223], 0\n\t" \
    "v_mfma_f32_16x16x32_bf16 v[116:119], v[104:107], v[220:223], 0\n\t" \
    "v_mfma_f32_16x16x32_bf16 v[120:123], v[176:179], v[216:219], v[120:123]\n\t" \
    "v_mfma_f32_16x16x32_bf16 v[124:127], v[180:183], v[216:219], v[124:127]\n\t" \
    "v_mfma_f32_16x16x32_bf16 v[112:115], v[100:103], v[224:227], v[112:115]\n\t" \
    "v_mfma_f32_16x16x32_bf16 v[116:119], v[108:111], v[224:227], v[116:119]\n\t" \
    "v_mfma_f32_16x16x32_bf16 v[112:115], v[176:179], v[228:231], v[112:115]\n\t" \
    "v_mfma_f32_16x16x32_bf16 v[116:119], v[180:183], v[228:231], v[116:119]\n\t" \
    "s_nop 1\n\t" \
    "ds_write_b128 %[pw], v[120:123] offset:16384\n\t" \
    "ds_write_b128 %[pw], v[124:127] offset:24576\n\t" \
    "s_nop 3\n\t" \
    "ds_write_b128 %[pw], v[112:115] offset:0\n\t" \
    "ds_write_b128 %[pw], v[116:119] offset:8192\n\t" \
    "v_cmp_ge_u32 vcc, %[tn], v248\n\t" \
    "s_mov_b32 %[a], 0\n\t" \
    "s_cbranch_vccnz .Lxv%=\n\t" \
    "buffer_load_dwordx4 v[128:131], %[l16], %[rp], %[sv0] offen offset:0 sc1\n\t" \
    "buffer_load_dwordx4 v[132:135], %[l16], %[rp], %[sv0] offen offset:1024 sc1\n\t" \
    "buffer_load_dwordx4 v[136:139], %[l16], %[rp], %[sv0] offen offset:2048 sc1\n\t" \
    "buffer_load_dwordx4 v[140:143], %[l16], %[rp], %[sv0] offen offset:3072 sc1\n\t" \
    "buffer_load_dwordx4 v[144:147], %[l16], %[rp], %[sv1] offen offset:0 sc1\n\t" \
    "buffer_load_dwordx4 v[148:151], %[l16], %[rp], %[sv1] offen offset:1024 sc1\n\t" \
    "buffer_load_dwordx4 v[152:155], %[l16], %[rp], %[sv1] offen offset:2048 sc1\n\t" \
    "buffer_load_dwordx4 v[156:159], %[l16], %[rp], %[sv1] offen offset:3072 sc1\n\t" \
    "s_mov_b32 %[a], 1\n\t" \
    ".Lxv%=:\n\t" \
    "s_cmp_eq_u32 %[fin], 0\n\t" \
    "s_waitcnt lgkmcnt(0)\n\t" \
    "s_barrier\n\t" ASM_ST \
    "s_cbranch_scc1 .Lxe%=\n\t" \
    "ds_read_b128 v[200:203], %[pr] offset:0\n\t" \
    "ds_read_b128 v[204:207], %[pr] offset:1024\n\t" \
    "ds_read_b128 v[208:211], %[pr] offset:2048\n\t" \
    "ds_read_b128 v[212:215], %[pr] offset:3072\n\t" \
    "ds_read_b128 v[216:219], %[pr] offset:4096\n\t" \
    "ds_read_b128 v[220:223], %[pr] offset:5120\n\t" \
    "ds_read_b128 v[224:227], %[pr] offset:6144\n\t" \
    "ds_read_b128 v[228:231], %[pr] offset:7168\n\t" \
    "v_lshlrev_b32 v240, 16, v250\n\t" \
    "v_and_b32 v241, 0xffff0000, v250\n\t" \
    "v_lshlrev_b32 v242, 16, v251\n\t" \
    "v_and_b32 v243, 0xffff0000, v251\n\t" \
    "s_waitcnt lgkmcnt(6)\n\t" \
    "v_pk_add_f32 v[200:201], v[200:201], v[204:205]\n\t" \
    "v_pk_add_f32 v[202:203], v[202:203], v[206:207]\n\t" \
    "s_waitcnt lgkmcnt(4)\n\t" \
    "v_pk_add_f32 v[208:209], v[208:209], v[212:213]\n\t" \
    "v_pk_add_f32 v[210:211], v[210:211], v[214:215]\n\t" \
    "v_pk_add_f32 v[200:201], v[200:201], v[208:209]\n\t" \
    "v_pk_add_f32 v[202:203], v[202:203], v[210:211]\n\t" \
    "s_waitcnt lgkmcnt(2)\n\t" \
    "v_pk_add_f32 v[216:217], v[216:217], v[220:221]\n\t" \
    "v_pk_add_f32 v[218:219], v[218:219], v[222:223]\n\t" \
    "s_waitcnt lgkmcnt(0)\n\t" \
    "v_pk_add_f32 v[224:225], v[224:225], v[228:229]\n\t" \
    "v_pk_add_f32 v[226:227], v[226:227], v[230:231]\n\t" \
    "v_pk_add_f32 v[216:217], v[216:217], v[224:225]\n\t" \
    "v_pk_add_f32 v[218:219], v[218:219], v[226:227]\n\t" \
    "v_pk_add_f32 v[200:201], v[200:201], v[216:217]\n\t" \
    "v_pk_add_f32 v[202:203], v[202:203], v[218:219]\n\t" \
    "v_pk_add_f32 v[200:201], v[200:201], v[240:241]\n\t" \
    "v_pk_add_f32 v[202:203], v[202:203], v[242:243]\n\t" \
    "v_cvt_pk_bf16_f32 v200, v200, v201\n\t" \
    "v_cvt_pk_bf16_f32 v201, v202, v203\n\t" \
    "v_min_u32 v200, -2, v200\n\t" \
    "v_min_u32 v201, -2, v201\n\t" \
    "buffer_store_dwordx2 v[200:201], %[xo], %[rd], %[sa] offen sc1\n\t" \
    ".Lxe%=:\n\t"
#define ASM_W4_M \
    ".Lc7_%=:\n\t" \
    "s_waitcnt vmcnt(0)\n\t" \
    "v_max_u32 v212, v188, v189\n\t" \
    "v_max3_u32 v212, v190, v191, v212\n\t" \
    "v_cmp_eq_u32 vcc, -1, v212\n\t" \
    "s_cbranch_vccnz .Ls7_%=\n\t" \
    ".Lk7_%=:\n\t" \
    ASM_ST2 \
    "buffer_load_dwordx4 v[184:187], %[l16], %[rs], %[s1] offen offset:2048 sc1\n\t" \
    "buffer_load_dwordx4 v[180:183], %[l16], %[rs], %[s1] offen offset:1024 sc1\n\t" \
    "buffer_load_dwordx4 v[176:179], %[l16], %[rs], %[s1] offen offset:0 sc1\n\t" \
    "buffer_load_dwordx4 v[172:175], %[l16], %[rs], %[s0] offen offset:3072 sc1\n\t" \
    "buffer_load_dwordx4 v[168:171], %[l16], %[rs], %[s0] offen offset:2048 sc1\n\t" \
    "buffer_load_dwordx4 v[164:167], %[l16], %[rs], %[s0] offen offset:1024 sc1\n\t" \
    "buffer_load_dwordx4 v[160:163], %[l16], %[rs], %[s0] offen offset:0 sc1\n\t" \
    "v_mfma_f32_16x16x32_bf16 v[208:211], v[156:159], v[188:191], 0\n\t" \
    "v_mfma_f32_16x16x32_bf16 v[204:207], v[124:127], v[188:191], 0\n\t" \
    "v_mfma_f32_16x16x32_bf16 v[200:203], v[92:95], v[188:191], 0\n\t" \
    "v_mfma_f32_16x16x32_bf16 v[196:199], v[60:63], v[188:191], 0\n\t" \
    "v_mfma_f32_16x16x32_bf16 v[192:195], v[28:31], v[188:191], 0\n\t" \
    ".Lc6_%=:\n\t" \
    "s_waitcnt vmcnt(6)\n\t" \
    "v_max_u32 v212, v184, v185\n\t" \
    "v_max3_u32 v212, v186, v187, v212\n\t" \
    "v_cmp_eq_u32 vcc, -1, v212\n\t" \
    "s_cbranch_vccnz .Ls6_%=\n\t" \
    ".Lk6_%=:\n\t" \
    ".Lc5_%=:\n\t" \
    "s_waitcnt vmcnt(5)\n\t" \
    "v_max_u32 v212, v180, v181\n\t" \
    "v_max3_u32 v212, v182, v183, v212\n\t" \
    "v_cmp_eq_u32 vcc, -1, v212\n\t" \
    "s_cbranch_vccnz .Ls5_%=\n\t" \
    ".Lk5_%=:\n\t" \
    "v_mfma_f32_16x16x32_bf16 v[192:195], v[24:27], v[184:187], v[192:195]\n\t" \
    "v_mfma_f32_16x16x32_bf16 v[192:195], v[20:23], v[180:183], v[192:195]\n\t" \
    "v_mfma_f32_16x16x32_bf16 v[196:199], v[56:59], v[184:187], v[196:199]\n\t" \
    "v_mfma_f32_16x16x32_bf16 v[196:199], v[52:55], v[180:183], v[196:199]\n\t" \
    "v_mfma_f32_16x16x32_bf16 v[200:203], v[88:91], v[184:187], v[200:203]\n\t" \
    "v_mfma_f32_16x16x32_bf16 v[200:203], v[84:87], v[180:183], v[200:203]\n\t" \
    "v_mfma_f32_16x16x32_bf16 v[204:207], v[120:123], v[184:187], v[204:207]\n\t" \
    "v_mfma_f32_16x16x32_bf16 v[204:207], v[116:119], v[180:183], v[204:207]\n\t" \
    "v_mfma_f32_16x16x32_bf16 v[208:211], v[152:155], v[184:187], v[208:211]\n\t" \
    "v_mfma_f32_16x16x32_bf16 v[208:211], v[148:151], v[180:183], v[208:211]\n\t" \
    ".Lc4_%=:\n\t" \
    "s_waitcnt vmcnt(4)\n\t" \
    "v_max_u32 v212, v176, v177\n\t" \
    "v_max3_u32 v212, v178, v179, v212\n\t" \
    "v_cmp_eq_u32 vcc, -1, v212\n\t" \
    "s_cbranch_vccnz .Ls4_%=\n\t" \
    ".Lk4_%=:\n\t" \
    ".Lc3_%=:\n\t" \
    "s_waitcnt vmcnt(3)\n\t" \
    "v_max_u32 v212, v172, v173\n\t" \
    "v_max3_u32 v212, v174, v175, v212\n\t" \
    "v_cmp_eq_u32 vcc, -1, v212\n\t" \
    "s_cbranch_vccnz .Ls3_%=\n\t" \
    ".Lk3_%=:\n\t" \
    "v_mfma_f32_16x16x32_bf16 v[208:211], v[144:147], v[176:179], v[208:211]\n\t" \
    "v_mfma_f32_16x16x32_bf16 v[208:211], v[140:143], v[172:175], v[208:211]\n\t" \
    "v_mfma_f32_16x16x32_bf16 v[204:207], v[112:115], v[176:179], v[204:207]\n\t" \
    "v_mfma_f32_16x16x32_bf16 v[204:207], v[108:111], v[172:175], v[204:207]\n\t" \
    "v_mfma_f32_16x16x32_bf16 v[200:203], v[80:83], v[176:179], v[200:203]\n\t" \
    "v_mfma_f32_16x16x32_bf16 v[200:203], v[76:79], v[172:175], v[200:203]\n\t" \
    "v_mfma_f32_16x16x32_bf16 v[196:199], v[48:51], v[176:179], v[196:199]\n\t" \
    "v_mfma_f32_16x16x32_bf16 v[196:199], v[44:47], v[172:175], v[196:199]\n\t" \
    "v_mfma_f32_16x16x32_bf16 v[192:195], v[16:19], v[176:179], v[192:195]\n\t" \
    "v_mfma_f32_16x16x32_bf16 v[192:195], v[12:15], v[172:175], v[192:195]\n\t" \
    ".Lc2_%=:\n\t" \
    "s_waitcnt vmcnt(2)\n\t" \
    "v_max_u32 v212, v168, v169\n\t" \
    "v_max3_u32 v212, v170, v171, v212\n\t" \
    "v_cmp_eq_u32 vcc, -1, v212\n\t" \
    "s_cbranch_vccnz .Ls2_%=\n\t" \
    ".Lk2_%=:\n\t" \
    "buffer_load_dwordx4 v[188:191], %[l16], %[rs], %[n1] offen offset:3072 sc1\n\t" \
    ".Lc1_%=:\n\t" \
    "s_waitcnt vmcnt(2)\n\t" \
    "v_max_u32 v212, v164, v165\n\t" \
    "v_max3_u32 v212, v166, v167, v212\n\t" \
    "v_cmp_eq_u32 vcc, -1, v212\n\t" \
    "s_cbranch_vccnz .Ls1_%=\n\t" \
    ".Lk1_%=:\n\t" \
    ".Lc0_%=:\n\t" \
    "s_waitcnt vmcnt(1)\n\t" \
    "v_max_u32 v212, v160, v161\n\t" \
    "v_max3_u32 v212, v162, v163, v212\n\t" \
    "v_cmp_eq_u32 vcc, -1, v212\n\t" \
    "s_cbranch_vccnz .Ls0_%=\n\t" \
    ".Lk0_%=:\n\t" \
    "v_mfma_f32_16x16x32_bf16 v[192:195], v[8:11], v[168:171], v[192:195]\n\t" \
    "v_mfma_f32_16x16x32_bf16 v[192:195], v[4:7], v[164:167], v[192:195]\n\t" \
    "v_mfma_f32_16x16x32_bf16 v[192:195], v[0:3], v[160:163], v[192:195]\n\t" \
    "v_mfma_f32_16x16x32_bf16 v[196:199], v[40:43], v[168:171], v[196:199]\n\t" \
    "v_mfma_f32_16x16x32_bf16 v[196:199], v[36:39], v[164:167], v[196:199]\n\t" \
    "v_mfma_f32_16x16x32_bf16 v[196:199], v[32:35], v[160:163], v[196:199]\n\t" \
    "v_mfma_f32_16x16x32_bf16 v[200:203], v[72:75], v[168:171], v[200:203]\n\t" \
    "v_mfma_f32_16x16x32_bf16 v[200:203], v[68:71], v[164:167], v[200:203]\n\t" \
    "v_mfma_f32_16x16x32_bf16 v[200:203], v[64:67], v[160:163], v[200:203]\n\t" \
    "v_mfma_f32_16x16x32_bf16 v[204:207], v[104:107], v[168:171], v[204:207]\n\t" \
    "v_mfma_f32_16x16x32_bf16 v[204:207], v[100:103], v[164:167], v[204:207]\n\t" \
    "v_mfma_f32_16x16x32_bf16 v[204:207], v[96:99], v[160:163], v[204:207]\n\t" \
    "v_mfma_f32_16x16x32_bf16 v[208:211], v[136:139], v[168:171], v[208:211]\n\t" \
    "v_mfma_f32_16x16x32_bf16 v[208:211], v[132:135], v[164:167], v[208:211]\n\t" \
    "v_mfma_f32_16x16x32_bf16 v[208:211], v[128:131], v[160:163], v[208:211]\n\t" \
    "v_mov_b32 v212, %[un]\n\t" \
    "ds_write_b128 %[ld], v[192:195] offset:0\n\t" \
    "ds_write_b128 %[ld], v[196:199] offset:6144\n\t" \
    "ds_write_b128 %[ld], v[200:203] offset:12288\n\t" \
    "ds_write_b128 %[ld], v[204:207] offset:18432\n\t" \
    "s_nop 2\n\t" \
    "ds_write_b128 %[ld], v[208:211] offset:24576\n\t" \
    "ds_write_b32 %[fl], v212\n\t" \
    "s_branch .Lme%=\n\t" \
    ".Ls7_%=:\n\t" \
    "s_cmp_eq_u32 %[n], 0\n\t" \
    "s_cbranch_scc1 .Lk7_%=\n\t" \
    "s_sub_u32 %[n], %[n], 1\n\t" \
    "s_sleep 1\n\t" \
    "buffer_load_dwordx4 v[188:191], %[l16], %[rs], %[s1] offen offset:3072 sc1\n\t" \
    "s_branch .Lc7_%=\n\t" \
    ".Ls6_%=:\n\t" \
    "s_cmp_eq_u32 %[n], 0\n\t" \
    "s_cbranch_scc1 .Lk6_%=\n\t" \
    "s_sub_u32 %[n], %[n], 1\n\t" \
    "buffer_load_dwordx4 v[184:187], %[l16], %[rs], %[s1] offen offset:2048 sc1\n\t" \
    "s_waitcnt vmcnt(0)\n\t" \
    "s_branch .Lc6_%=\n\t" \
    ".Ls5_%=:\n\t" \
    "s_cmp_eq_u32 %[n], 0\n\t" \
    "s_cbranch_scc1 .Lk5_%=\n\t" \
    "s_sub_u32 %[n], %[n], 1\n\t" \
    "buffer_load_dwordx4 v[180:183], %[l16], %[rs], %[s1] offen offset:1024 sc1\n\t" \
    "s_waitcnt vmcnt(0)\n\t" \
    "s_branch .Lc5_%=\n\t" \
    ".Ls4_%=:\n\t" \
    "s_cmp_eq_u32 %[n], 0\n\t" \
    "s_cbranch_scc1 .Lk4_%=\n\t" \
    "s_sub_u32 %[n], %[n], 1\n\t" \
    "buffer_load_dwordx4 v[176:179], %[l16], %[rs], %[s1] offen offset:0 sc1\n\t" \
    "s_waitcnt vmcnt(0)\n\t" \
    "s_branch .Lc4_%=\n\t" \
    ".Ls3_%=:\n\t" \
    "s_cmp_eq_u32 %[n], 0\n\t" \
    "s_cbranch_scc1 .Lk3_%=\n\t" \
    "s_sub_u32 %[n], %[n], 1\n\t" \
    "buffer_load_dwordx4 v[172:175], %[l16], %[rs], %[s0] offen offset:3072 sc1\n\t" \
    "s_waitcnt vmcnt(0)\n\t" \
    "s_branch .Lc3_%=\n\t" \
    ".Ls2_%=:\n\t" \
    "s_cmp_eq_u32 %[n], 0\n\t" \
    "s_cbranch_scc1 .Lk2_%=\n\t" \
    "s_sub_u32 %[n], %[n], 1\n\t" \
    "buffer_load_dwordx4 v[168:171], %[l16], %[rs], %[s0] offen offset:2048 sc1\n\t" \
    "s_waitcnt vmcnt(0)\n\t" \
    "s_branch .Lc2_%=\n\t" \
    ".Ls1_%=:\n\t" \
    "s_cmp_eq_u32 %[n], 0\n\t" \
    "s_cbranch_scc1 .Lk1_%=\n\t" \
    "s_sub_u32 %[n], %[n], 1\n\t" \
    "buffer_load_dwordx4 v[164:167], %[l16], %[rs], %[s0] offen offset:1024 sc1\n\t" \
    "s_waitcnt vmcnt(0)\n\t" \
    "s_branch .Lc1_%=\n\t" \
    ".Ls0_%=:\n\t" \
    "s_cmp_eq_u32 %[n], 0\n\t" \
    "s_cbranch_scc1 .Lk0_%=\n\t" \
    "s_sub_u32 %[n], %[n], 1\n\t" \
    "buffer_load_dwordx4 v[160:163], %[l16], %[rs], %[s0] offen offset:0 sc1\n\t" \
    "s_waitcnt vmcnt(0)\n\t" \
    "s_branch .Lc0_%=\n\t" \
    ".Lme%=:\n\t"
#define ASM_W4F_0 \
    "s_branch .Lf0%=\n\t" \
    ".Lfr%=:\n\t" \
    "s_cmp_eq_u32 %[n], 0\n\t" \
    "s_cbranch_scc1 .Lfb%=\n\t" \
    "s_sub_u32 %[n], %[n], 1\n\t" \
    "s_sleep 6\n\t" \
    "buffer_load_dwordx2 v[164:165], %[o0], %[rd], %[c1] offen sc1\n\t" \
    "buffer_load_dwordx2 v[166:167], %[o1], %[rd], %[c1] offen sc1\n\t" \
    "buffer_load_dwordx2 v[168:169], %[o2], %[rd], %[c1] offen sc1\n\t" \
    "buffer_load_dwordx2 v[170:171], %[o3], %[rd], %[c1] offen sc1\n\t" \
    "buffer_load_dwordx2 v[172:173], %[o4], %[rd], %[c1] offen sc1\n\t" \
    "buffer_load_dwordx2 v[174:175], %[o0], %[rd], %[ca] offen sc1\n\t" \
    "buffer_load_dwordx2 v[176:177], %[o1], %[rd], %[ca] offen sc1\n\t" \
    "buffer_load_dwordx2 v[178:179], %[o2], %[rd], %[ca] offen sc1\n\t" \
    "buffer_load_dwordx2 v[180:181], %[o3], %[rd], %[ca] offen sc1\n\t" \
    "buffer_load_dwordx2 v[182:183], %[o4], %[rd], %[ca] offen sc1\n\t" \
    "s_waitcnt vmcnt(0)\n\t" \
    "s_branch .Lfa%=\n\t" \
    ".Lfs0_%=:\n\t" \
    "s_cmp_eq_u32 %[n], 0\n\t" \
    "s_cbranch_scc1 .Lfu0_%=\n\t" \
    "s_sub_u32 %[n], %[n], 1\n\t" \
    "s_sleep 1\n\t" \
    "s_branch .Lft0_%=\n\t" \
    ".Lfs1_%=:\n\t" \
    "s_cmp_eq_u32 %[n], 0\n\t" \
    "s_cbranch_scc1 .Lfu1_%=\n\t" \
    "s_sub_u32 %[n], %[n], 1\n\t" \
    "s_sleep 1\n\t" \
    "s_branch .Lft1_%=\n\t" \
    ".Lfx%=:\n\t" \
    "s_cmp_eq_u32 %[n], 0\n\t" \
    "s_cbranch_scc1 .Lfz%=\n\t" \
    "s_sub_u32 %[n], %[n], 1\n\t" \
    "s_branch .Lfy%=\n\t" \
    ".Lf0%=:\n\t" \
    "s_waitcnt vmcnt(1)\n\t" \
    ".Lfa%=:\n\t" \
    "v_max_u32 v194, v164, v165\n\t" \
    "v_max3_u32 v194, v166, v167, v194\n\t" \
    "v_max3_u32 v194, v168, v169, v194\n\t" \
    "v_max3_u32 v194, v170, v171, v194\n\t" \
    "v_max3_u32 v194, v172, v173, v194\n\t" \
    "v_max3_u32 v194, v174, v175, v194\n\t" \
    "v_max3_u32 v194, v176, v177, v194\n\t" \
    "v_max3_u32 v194, v178, v179, v194\n\t" \
    "v_max3_u32 v194, v180, v181, v194\n\t" \
    "v_max3_u32 v194, v182, v183, v194\n\t" \
    "v_cmp_eq_u32 vcc, -1, v194\n\t" \
    "s_cbranch_vccnz .Lfr%=\n\t" \
    ".Lfb%=:\n\t" \
    "v_lshlrev_b32 v64, 16, v164\n\t" \
    "v_and_b32 v65, 0xffff0000, v164\n\t" \
    "v_lshlrev_b32 v84, 16, v174\n\t" \
    "v_and_b32 v85, 0xffff0000, v174\n\t" \
    "v_lshlrev_b32 v66, 16, v165\n\t" \
    "v_and_b32 v67, 0xffff0000, v165\n\t" \
    "v_lshlrev_b32 v86, 16, v175\n\t" \
    "v_and_b32 v87, 0xffff0000, v175\n\t" \
    "v_lshlrev_b32 v68, 16, v166\n\t" \
    "v_and_b32 v69, 0xffff0000, v166\n\t" \
    "v_lshlrev_b32 v88, 16, v176\n\t" \
    "v_and_b32 v89, 0xffff0000, v176\n\t" \
    "v_lshlrev_b32 v70, 16, v167\n\t" \
    "v_and_b32 v71, 0xffff0000, v167\n\t" \
    "v_lshlrev_b32 v90, 16, v177\n\t" \
    "v_and_b32 v91, 0xffff0000, v177\n\t" \
    "v_lshlrev_b32 v72, 16, v168\n\t" \
    "v_and_b32 v73, 0xffff0000, v168\n\t" \
    "v_lshlrev_b32 v92, 16, v178\n\t" \
    "v_and_b32 v93, 0xffff0000, v178\n\t" \
    "v_lshlrev_b32 v74, 16, v169\n\t" \
    "v_and_b32 v75, 0xffff0000, v169\n\t" \
    "v_lshlrev_b32 v94, 16, v179\n\t" \
    "v_and_b32 v95, 0xffff0000, v179\n\t" \
    "v_lshlrev_b32 v76, 16, v170\n\t" \
    "v_and_b32 v77, 0xffff0000, v170\n\t" \
    "v_lshlrev_b32 v96, 16, v180\n\t" \
    "v_and_b32 v97, 0xffff0000, v180\n\t" \
    "v_lshlrev_b32 v78, 16, v171\n\t" \
    "v_and_b32 v79, 0xffff0000, v171\n\t" \
    "v_lshlrev_b32 v98, 16, v181\n\t" \
    "v_and_b32 v99, 0xffff0000, v181\n\t" \
    "v_lshlrev_b32 v80, 16, v172\n\t" \
    "v_and_b32 v81, 0xffff0000, v172\n\t" \
    "v_lshlrev_b32 v100, 16, v182\n\t" \
    "v_and_b32 v101, 0xffff0000, v182\n\t" \
    "v_lshlrev_b32 v82, 16, v173\n\t" \
    "v_and_b32 v83, 0xffff0000, v173\n\t" \
    "v_lshlrev_b32 v102, 16, v183\n\t" \
    "v_and_b32 v103, 0xffff0000, v183\n\t" \
    "v_pk_add_f32 v[64:65], v[64:65], v[84:85]\n\t" \
    "v_pk_add_f32 v[66:67], v[66:67], v[86:87]\n\t" \
    "v_pk_add_f32 v[68:69], v[68:69], v[88:89]\n\t" \
    "v_pk_add_f32 v[70:71], v[70:71], v[90:91]\n\t" \
    "v_pk_add_f32 v[72:73], v[72:73], v[92:93]\n\t" \
    "v_pk_add_f32 v[74:75], v[74:75], v[94:95]\n\t" \
    "v_pk_add_f32 v[76:77], v[76:77], v[96:97]\n\t" \
    "v_pk_add_f32 v[78:79], v[78:79], v[98:99]\n\t" \
    "v_pk_add_f32 v[80:81], v[80:81], v[100:101]\n\t" \
    "v_pk_add_f32 v[82:83], v[82:83], v[102:103]\n\t" \
    ".Lft0_%=:\n\t" \
    "ds_read_b32 v194, %[f0]\n\t" \
    "s_waitcnt lgkmcnt(0)\n\t" \
    "v_cmp_ge_u32 vcc, %[u], v194\n\t" \
    "s_cbranch_vccnz .Lfs0_%=\n\t" \
    ".Lfu0_%=:\n\t" \
    "ds_read_b128 v[84:87], %[la] offset:0\n\t" \
    "ds_read_b128 v[88:91], %[la] offset:6144\n\t" \
    "ds_read_b128 v[92:95], %[la] offset:12288\n\t" \
    "ds_read_b128 v[96:99], %[la] offset:18432\n\t" \
    "ds_read_b128 v[100:103], %[la] offset:24576\n\t" \
    "ds_read_b128 v[104:107], %[la] offset:1024\n\t" \
    "ds_read_b128 v[108:111], %[la] offset:7168\n\t" \
    "ds_read_b128 v[112:115], %[la] offset:13312\n\t" \
    "ds_read_b128 v[116:119], %[la] offset:19456\n\t" \
    "ds_read_b128 v[120:123], %[la] offset:25600\n\t" \
    "ds_read_b128 v[124:127], %[la] offset:2048\n\t" \
    "s_waitcnt lgkmcnt(10)\n\t" \
    "v_pk_add_f32 v[64:65], v[64:65], v[84:85]\n\t" \
    "v_pk_add_f32 v[66:67], v[66:67], v[86:87]\n\t" \
    "ds_read_b128 v[128:131], %[la] offset:8192\n\t" \
    "s_waitcnt lgkmcnt(10)\n\t" \
    "v_pk_add_f32 v[68:69], v[68:69], v[88:89]\n\t" \
    "v_pk_add_f32 v[70:71], v[70:71], v[90:91]\n\t" \
    "ds_read_b128 v[132:135], %[la] offset:14336\n\t" \
    "s_waitcnt lgkmcnt(10)\n\t" \
    "v_pk_add_f32 v[72:73], v[72:73], v[92:93]\n\t" \
    "v_pk_add_f32 v[74:75], v[74:75], v[94:95]\n\t" \
    "ds_read_b128 v[136:139], %[la] offset:20480\n\t" \
    "s_waitcnt lgkmcnt(10)\n\t" \
    "v_pk_add_f32 v[76:77], v[76:77], v[96:97]\n\t" \
    "v_pk_add_f32 v[78:79], v[78:79], v[98:99]\n\t" \
    "ds_read_b128 v[140:143], %[la] offset:26624\n\t" \
    "s_waitcnt lgkmcnt(10)\n\t" \
    "v_pk_add_f32 v[80:81], v[80:81], v[100:101]\n\t" \
    "v_pk_add_f32 v[82:83], v[82:83], v[102:103]\n\t" \
    "ds_read_b128 v[144:147], %[la] offset:3072\n\t" \
    "s_waitcnt lgkmcnt(10)\n\t" \
    "v_pk_add_f32 v[64:65], v[64:65], v[104:105]\n\t" \
    "v_pk_add_f32 v[66:67], v[66:67], v[106:107]\n\t" \
    "ds_read_b128 v[148:151], %[la] offset:9216\n\t" \
    "s_waitcnt lgkmcnt(10)\n\t" \
    "v_pk_add_f32 v[68:69], v[68:69], v[108:109]\n\t" \
    "v_pk_add_f32 v[70:71], v[70:71], v[110:111]\n\t" \
    "ds_read_b128 v[152:155], %[la] offset:15360\n\t" \
    "s_waitcnt lgkmcnt(10)\n\t" \
    "v_pk_add_f32 v[72:73], v[72:73], v[112:113]\n\t" \
    "v_pk_add_f32 v[74:75], v[74:75], v[114:115]\n\t" \
    "ds_read_b128 v[156:159], %[la] offset:21504\n\t" \
    "s_waitcnt lgkmcnt(10)\n\t" \
    "v_pk_add_f32 v[76:77], v[76:77], v[116:117]\n\t" \
    "v_pk_add_f32 v[78:79], v[78:79], v[118:119]\n\t" \
    "ds_read_b128 v[160:163], %[la] offset:27648\n\t" \
    "s_waitcnt lgkmcnt(10)\n\t" \
    "v_pk_add_f32 v[80:81], v[80:81], v[120:121]\n\t" \
    "v_pk_add_f32 v[82:83], v[82:83], v[122:123]\n\t" \
    "s_waitcnt lgkmcnt(9)\n\t" \
    "v_pk_add_f32 v[64:65], v[64:65], v[124:125]\n\t" \
    "v_pk_add_f32 v[66:67], v[66:67], v[126:127]\n\t" \
    "s_waitcnt lgkmcnt(8)\n\t" \
    "v_pk_add_f32 v[68:69], v[68:69], v[128:129]\n\t" \
    "v_pk_add_f32 v[70:71], v[70:71], v[130:131]\n\t" \
    "s_waitcnt lgkmcnt(7)\n\t" \
    "v_pk_add_f32 v[72:73], v[72:73], v[132:133]\n\t" \
    "v_pk_add_f32 v[74:75], v[74:75], v[134:135]\n\t" \
    "s_waitcnt lgkmcnt(6)\n\t" \
    "v_pk_add_f32 v[76:77], v[76:77], v[136:137]\n\t" \
    "v_pk_add_f32 v[78:79], v[78:79], v[138:139]\n\t" \
    "s_waitcnt lgkmcnt(5)\n\t" \
    "v_pk_add_f32 v[80:81], v[80:81], v[140:141]\n\t" \
    "v_pk_add_f32 v[82:83], v[82:83], v[142:143]\n\t" \
    "s_waitcnt lgkmcnt(4)\n\t" \
    "v_pk_add_f32 v[64:65], v[64:65], v[144:145]\n\t" \
    "v_pk_add_f32 v[66:67], v[66:67], v[146:147]\n\t" \
    "s_waitcnt lgkmcnt(3)\n\t" \
    "v_pk_add_f32 v[68:69], v[68:69], v[148:149]\n\t" \
    "v_pk_add_f32 v[70:71], v[70:71], v[150:151]\n\t" \
    "s_waitcnt lgkmcnt(2)\n\t" \
    "v_pk_add_f32 v[72:73], v[72:73], v[152:153]\n\t" \
    "v_pk_add_f32 v[74:75], v[74:75], v[154:155]\n\t" \
    "s_waitcnt lgkmcnt(1)\n\t" \
    "v_pk_add_f32 v[76:77], v[76:77], v[156:157]\n\t" \
    "v_pk_add_f32 v[78:79], v[78:79], v[158:159]\n\t" \
    "s_waitcnt lgkmcnt(0)\n\t" \
    "v_pk_add_f32 v[80:81], v[80:81], v[160:161]\n\t" \
    "v_pk_add_f32 v[82:83], v[82:83], v[162:163]\n\t" \
    ".Lft1_%=:\n\t" \
    "ds_read_b32 v194, %[f1]\n\t" \
    "s_waitcnt lgkmcnt(0)\n\t" \
    "v_cmp_ge_u32 vcc, %[u], v194\n\t" \
    "s_cbranch_vccnz .Lfs1_%=\n\t" \
    ".Lfu1_%=:\n\t" \
    ASM_ST2 \
    "ds_read_b128 v[84:87], %[la] offset:4096\n\t" \
    "ds_read_b128 v[88:91], %[la] offset:10240\n\t" \
    "ds_read_b128 v[92:95], %[la] offset:16384\n\t" \
    "ds_read_b128 v[96:99], %[la] offset:22528\n\t" \
    "ds_read_b128 v[100:103], %[la] offset:28672\n\t" \
    "ds_read_b128 v[104:107], %[la] offset:5120\n\t" \
    "ds_read_b128 v[108:111], %[la] offset:11264\n\t" \
    "ds_read_b128 v[112:115], %[la] offset:17408\n\t" \
    "ds_read_b128 v[116:119], %[la] offset:23552\n\t" \
    "ds_read_b128 v[120:123], %[la] offset:29696\n\t" \
    "v_mov_b32 v194, %[un]\n\t" \
    "ds_write_b32 %[fd], v194\n\t" \
    "s_waitcnt lgkmcnt(10)\n\t" \
    "v_pk_add_f32 v[64:65], v[64:65], v[84:85]\n\t" \
    "v_pk_add_f32 v[66:67], v[66:67], v[86:87]\n\t" \
    "s_waitcnt lgkmcnt(9)\n\t" \
    "v_pk_add_f32 v[68:69], v[68:69], v[88:89]\n\t" \
    "v_pk_add_f32 v[70:71], v[70:71], v[90:91]\n\t" \
    "s_waitcnt lgkmcnt(8)\n\t" \
    "v_pk_add_f32 v[72:73], v[72:73], v[92:93]\n\t" \
    "v_pk_add_f32 v[74:75], v[74:75], v[94:95]\n\t" \
    "s_waitcnt lgkmcnt(7)\n\t" \
    "v_pk_add_f32 v[76:77], v[76:77], v[96:97]\n\t" \
    "v_pk_add_f32 v[78:79], v[78:79], v[98:99]\n\t" \
    "s_waitcnt lgkmcnt(6)\n\t" \
    "v_pk_add_f32 v[80:81], v[80:81], v[100:101]\n\t" \
    "v_pk_add_f32 v[82:83], v[82:83], v[102:103]\n\t" \
    "s_waitcnt lgkmcnt(5)\n\t" \
    "v_pk_add_f32 v[64:65], v[64:65], v[104:105]\n\t" \
    "v_pk_add_f32 v[66:67], v[66:67], v[106:107]\n\t" \
    "s_waitcnt lgkmcnt(4)\n\t" \
    "v_pk_add_f32 v[68:69], v[68:69], v[108:109]\n\t" \
    "v_pk_add_f32 v[70:71], v[70:71], v[110:111]\n\t" \
    "s_waitcnt lgkmcnt(3)\n\t" \
    "v_pk_add_f32 v[72:73], v[72:73], v[112:113]\n\t" \
    "v_pk_add_f32 v[74:75], v[74:75], v[114:115]\n\t" \
    "s_waitcnt lgkmcnt(2)\n\t" \
    "v_pk_add_f32 v[76:77], v[76:77], v[116:117]\n\t" \
    "v_pk_add_f32 v[78:79], v[78:79], v[118:119]\n\t" \
    "s_waitcnt lgkmcnt(1)\n\t" \
    "v_pk_add_f32 v[80:81], v[80:81], v[120:121]\n\t" \
    "v_pk_add_f32 v[82:83], v[82:83], v[122:123]\n\t" \
    "v_pk_add_f32 v[84:85], v[64:65], v[66:67]\n\t" \
    "v_pk_mul_f32 v[88:89], v[64:65], v[64:65]\n\t" \
    "v_pk_add_f32 v[86:87], v[68:69], v[70:71]\n\t" \
    "v_pk_mul_f32 v[90:91], v[66:67], v[66:67]\n\t" \
    "v_pk_fma_f32 v[88:89], v[68:69], v[68:69], v[88:89]\n\t" \
    "v_pk_fma_f32 v[90:91], v[70:71], v[70:71], v[90:91]\n\t" \
    "v_pk_add_f32 v[84:85], v[84:85], v[72:73]\n\t" \
    "v_pk_fma_f32 v[88:89], v[72:73], v[72:73], v[88:89]\n\t" \
    "v_pk_add_f32 v[86:87], v[86:87], v[74:75]\n\t" \
    "v_pk_fma_f32 v[90:91], v[74:75], v[74:75], v[90:91]\n\t" \
    "v_pk_add_f32 v[84:85], v[84:85], v[76:77]\n\t" \
    "v_pk_fma_f32 v[88:89], v[76:77], v[76:77], v[88:89]\n\t" \
    "v_pk_add_f32 v[86:87], v[86:87], v[78:79]\n\t" \
    "v_pk_fma_f32 v[90:91], v[78:79], v[78:79], v[90:91]\n\t" \
    "v_pk_add_f32 v[84:85], v[84:85], v[80:81]\n\t" \
    "v_pk_fma_f32 v[88:89], v[80:81], v[80:81], v[88:89]\n\t" \
    "v_pk_add_f32 v[86:87], v[86:87], v[82:83]\n\t" \
    "v_pk_fma_f32 v[90:91], v[82:83], v[82:83], v[90:91]\n\t" \
    "v_pk_add_f32 v[84:85], v[84:85], v[86:87]\n\t" \
    "v_pk_add_f32 v[88:89], v[88:89], v[90:91]\n\t" \
    "v_add_f32 v194, v84, v85\n\t" \
    "v_add_f32 v195, v88, v89\n\t" \
    "s_nop 1\n\t" \
    "v_permlane16_swap_b32 v194, v195\n\t" \
    "v_add_f32 v196, v194, v195\n\t" \
    "v_add_f32 v197, v194, v195\n\t" \
    "s_nop 1\n\t" \
    "v_permlane32_swap_b32 v196, v197\n\t" \
    "v_add_f32 v194, v196, v197\n\t" \
    "v_add_f32 v195, v196, v197\n\t" \
    "s_nop 1\n\t" \
    "v_permlane16_swap_b32 v194, v195\n\t" \
    "v_min_u32 v194, -2, v194\n\t" \
    "v_min_u32 v195, -2, v195\n\t" \
    "buffer_store_dwordx2 v[194:195], %[so], %[rr], %[sp] offen sc1\n\t" \
    ASM_ST3 \
    "v_cvt_pk_bf16_f32 v184, v64, v65\n\t" \
    "v_cvt_pk_bf16_f32 v185, v66, v67\n\t" \
    "v_cvt_pk_bf16_f32 v186, v68, v69\n\t" \
    "v_cvt_pk_bf16_f32 v187, v70, v71\n\t" \
    "v_cvt_pk_bf16_f32 v188, v72, v73\n\t" \
    "v_cvt_pk_bf16_f32 v189, v74, v75\n\t" \
    "v_cvt_pk_bf16_f32 v190, v76, v77\n\t" \
    "v_cvt_pk_bf16_f32 v191, v78, v79\n\t" \
    "v_cvt_pk_bf16_f32 v192, v80, v81\n\t" \
    "v_cvt_pk_bf16_f32 v193, v82, v83\n\t" \
    "v_permlane16_swap_b32 v184, v186\n\t" \
    "v_permlane16_swap_b32 v185, v187\n\t" \
    "v_permlane16_swap_b32 v188, v190\n\t" \
    "v_permlane16_swap_b32 v189, v191\n\t" \
    W4SLEEP \
    ".Lfy%=:\n\t" \
    "buffer_load_dwordx2 v[196:197], %[si], %[rr], %[sq] offen sc1\n\t" \
    "buffer_load_dwordx2 v[198:199], %[si], %[rr], %[sq] offen offset:512 sc1\n\t" \
    "s_waitcnt vmcnt(0)\n\t" \
    "v_max_u32 v200, v196, v197\n\t" \
    "v_max3_u32 v200, v198, v199, v200\n\t" \
    "v_cmp_eq_u32 vcc, -1, v200\n\t" \
    "s_cbranch_vccnz .Lfx%=\n\t" \
    ".Lfz%=:\n\t" \
    ASM_ST \
    "v_add_f32 v194, v196, v198\n\t" \
    "v_add_f32 v195, v197, v199\n\t" \
    "buffer_store_dwordx4 v[184:187], %[pa], %[rd], %[xr] offen sc1\n\t" \
    "buffer_store_dwordx4 v[188:191], %[pb], %[rd], %[xr] offen sc1\n\t" \
    "buffer_store_dwordx2 v[192:193], %[o4], %[rd], %[xr] offen sc1\n\t" \
    "s_nop 1\n\t" \
    "v_permlane16_swap_b32 v194, v195\n\t" \
    "v_add_f32 v196, v194, v195\n\t" \
    "v_add_f32 v197, v194, v195\n\t" \
    "s_nop 1\n\t" \
    "v_permlane32_swap_b32 v196, v197\n\t" \
    "v_add_f32 v194, v196, v197\n\t" \
    "v_add_f32 v195, v196, v197\n\t" \
    "s_nop 1\n\t" \
    "v_permlane16_swap_b32 v194, v195\n\t" \
    "v_mul_f32 v194, %[kd], v194\n\t" \
    "v_mul_f32 v195, %[kd], v195\n\t" \
    "v_fma_f32 v195, -v194, v194, v195\n\t" \
    "v_max_f32 v195, 0, v195\n\t" \
    "v_cvt_pk_bf16_f32 v195, v195, v195\n\t" \
    "v_lshlrev_b32 v195, 16, v195\n\t" \
    "v_add_f32 v195, %[ke], v195\n\t" \
    "v_cvt_pk_bf16_f32 v195, v195, v195\n\t" \
    "v_lshlrev_b32 v195, 16, v195\n\t" \
    "v_sqrt_f32 v195, v195\n\t" \
    "v_cvt_pk_bf16_f32 v194, v194, v194\n\t" \
    "v_lshlrev_b32 v194, 16, v194\n\t" \
    "v_cvt_pk_bf16_f32 v195, v195, v195\n\t" \
    "v_lshlrev_b32 v195, 16, v195\n\t" \
    "v_rcp_f32 v196, v195\n\t" \
    "v_rcp_f32 v197, v195\n\t" \
    "s_nop 0\n\t" \
    "v_mul_f32 v198, -v194, v196\n\t" \
    "v_mul_f32 v199, -v194, v196\n\t" \
    "v_pk_fma_f32 v[64:65], v[64:65], v[196:197], v[198:199]\n\t" \
    "v_pk_fma_f32 v[66:67], v[66:67], v[196:197], v[198:199]\n\t" \
    "v_pk_fma_f32 v[68:69], v[68:69], v[196:197], v[198:199]\n\t" \
    "v_pk_fma_f32 v[70:71], v[70:71], v[196:197], v[198:199]\n\t" \
    "v_pk_fma_f32 v[72:73], v[72:73], v[196:197], v[198:199]\n\t" \
    "v_pk_fma_f32 v[74:75], v[74:75], v[196:197], v[198:199]\n\t" \
    "v_pk_fma_f32 v[76:77], v[76:77], v[196:197], v[198:199]\n\t" \
    "v_pk_fma_f32 v[78:79], v[78:79], v[196:197], v[198:199]\n\t" \
    "v_pk_fma_f32 v[80:81], v[80:81], v[196:197], v[198:199]\n\t" \
    "v_pk_fma_f32 v[82:83], v[82:83], v[196:197], v[198:199]\n\t" \
    "v_cvt_pk_bf16_f32 v184, v64, v65\n\t" \
    "v_cvt_pk_bf16_f32 v185, v66, v67\n\t" \
    "v_cvt_pk_bf16_f32 v186, v68, v69\n\t" \
    "v_cvt_pk_bf16_f32 v187, v70, v71\n\t" \
    "v_cvt_pk_bf16_f32 v188, v72, v73\n\t" \
    "v_cvt_pk_bf16_f32 v189, v74, v75\n\t" \
    "v_cvt_pk_bf16_f32 v190, v76, v77\n\t" \
    "v_cvt_pk_bf16_f32 v191, v78, v79\n\t" \
    "v_cvt_pk_bf16_f32 v192, v80, v81\n\t" \
    "v_cvt_pk_bf16_f32 v193, v82, v83\n\t" \
    "v_permlane16_swap_b32 v184, v186\n\t" \
    "v_permlane16_swap_b32 v185, v187\n\t" \
    "v_permlane16_swap_b32 v188, v190\n\t" \
    "v_permlane16_swap_b32 v189, v191\n\t" \
    "buffer_store_dwordx4 v[184:187], %[pa], %[rd], %[xn] offen sc1\n\t" \
    "buffer_store_dwordx4 v[188:191], %[pb], %[rd], %[xn] offen sc1\n\t" \
    "buffer_store_dwordx2 v[192:193], %[o4], %[rd], %[xn] offen sc1\n\t" \
    "buffer_load_dwordx2 v[164:165], %[o0], %[rd], %[n1] offen sc1\n\t" \
    "buffer_load_dwordx2 v[166:167], %[o1], %[rd], %[n1] offen sc1\n\t" \
    "buffer_load_dwordx2 v[168:169], %[o2], %[rd], %[n1] offen sc1\n\t" \
    "buffer_load_dwordx2 v[170:171], %[o3], %[rd], %[n1] offen sc1\n\t" \
    "buffer_load_dwordx2 v[172:173], %[o4], %[rd], %[n1] offen sc1\n\t" \
    "buffer_load_dwordx2 v[174:175], %[o0], %[rd], %[na] offen sc1\n\t" \
    "buffer_load_dwordx2 v[176:177], %[o1], %[rd], %[na] offen sc1\n\t" \
    "buffer_load_dwordx2 v[178:179], %[o2], %[rd], %[na] offen sc1\n\t" \
    "buffer_load_dwordx2 v[180:181], %[o3], %[rd], %[na] offen sc1\n\t" \
    "buffer_load_dwordx2 v[182:183], %[o4], %[rd], %[na] offen sc1\n\t" \
    "v_mov_b32 v196, %[tn]\n\t" \
    "v_mov_b32 v197, %[tn]\n\t" \
    "v_mov_b32 v198, %[tn]\n\t" \
    "v_mov_b32 v199, %[tn]\n\t" \
    "s_waitcnt vmcnt(10)\n\t" \
    "buffer_store_dwordx4 v[196:199], %[fo], %[rn], %[fs] offen sc1\n\t" \
    "s_nop 1\n\t"
#define ASM_W4F_1 \
    "s_branch .Lf0%=\n\t" \
    ".Lfr%=:\n\t" \
    "s_cmp_eq_u32 %[n], 0\n\t" \
    "s_cbranch_scc1 .Lfb%=\n\t" \
    "s_sub_u32 %[n], %[n], 1\n\t" \
    "s_sleep 6\n\t" \
    "buffer_load_dwordx2 v[164:165], %[o0], %[rd], %[c1] offen sc1\n\t" \
    "buffer_load_dwordx2 v[166:167], %[o1], %[rd], %[c1] offen sc1\n\t" \
    "buffer_load_dwordx2 v[168:169], %[o2], %[rd], %[c1] offen sc1\n\t" \
    "buffer_load_dwordx2 v[170:171], %[o3], %[rd], %[c1] offen sc1\n\t" \
    "buffer_load_dwordx2 v[172:173], %[o4], %[rd], %[c1] offen sc1\n\t" \
    "buffer_load_dwordx2 v[174:175], %[o0], %[rd], %[ca] offen sc1\n\t" \
    "buffer_load_dwordx2 v[176:177], %[o1], %[rd], %[ca] offen sc1\n\t" \
    "buffer_load_dwordx2 v[178:179], %[o2], %[rd], %[ca] offen sc1\n\t" \
    "buffer_load_dwordx2 v[180:181], %[o3], %[rd], %[ca] offen sc1\n\t" \
    "buffer_load_dwordx2 v[182:183], %[o4], %[rd], %[ca] offen sc1\n\t" \
    "s_waitcnt vmcnt(0)\n\t" \
    "s_branch .Lfa%=\n\t" \
    ".Lfs0_%=:\n\t" \
    "s_cmp_eq_u32 %[n], 0\n\t" \
    "s_cbranch_scc1 .Lfu0_%=\n\t" \
    "s_sub_u32 %[n], %[n], 1\n\t" \
    "s_sleep 1\n\t" \
    "s_branch .Lft0_%=\n\t" \
    ".Lfs1_%=:\n\t" \
    "s_cmp_eq_u32 %[n], 0\n\t" \
    "s_cbranch_scc1 .Lfu1_%=\n\t" \
    "s_sub_u32 %[n], %[n], 1\n\t" \
    "s_sleep 1\n\t" \
    "s_branch .Lft1_%=\n\t" \
    ".Lf0%=:\n\t" \
    "s_waitcnt vmcnt(0)\n\t" \
    ".Lfa%=:\n\t" \
    "v_max_u32 v194, v164, v165\n\t" \
    "v_max3_u32 v194, v166, v167, v194\n\t" \
    "v_max3_u32 v194, v168, v169, v194\n\t" \
    "v_max3_u32 v194, v170, v171, v194\n\t" \
    "v_max3_u32 v194, v172, v173, v194\n\t" \
    "v_max3_u32 v194, v174, v175, v194\n\t" \
    "v_max3_u32 v194, v176, v177, v194\n\t" \
    "v_max3_u32 v194, v178, v179, v194\n\t" \
    "v_max3_u32 v194, v180, v181, v194\n\t" \
    "v_max3_u32 v194, v182, v183, v194\n\t" \
    "v_cmp_eq_u32 vcc, -1, v194\n\t" \
    "s_cbranch_vccnz .Lfr%=\n\t" \
    ".Lfb%=:\n\t" \
    "v_lshlrev_b32 v64, 16, v164\n\t" \
    "v_and_b32 v65, 0xffff0000, v164\n\t" \
    "v_lshlrev_b32 v84, 16, v174\n\t" \
    "v_and_b32 v85, 0xffff0000, v174\n\t" \
    "v_lshlrev_b32 v66, 16, v165\n\t" \
    "v_and_b32 v67, 0xffff0000, v165\n\t" \
    "v_lshlrev_b32 v86, 16, v175\n\t" \
    "v_and_b32 v87, 0xffff0000, v175\n\t" \
    "v_lshlrev_b32 v68, 16, v166\n\t" \
    "v_and_b32 v69, 0xffff0000, v166\n\t" \
    "v_lshlrev_b32 v88, 16, v176\n\t" \
    "v_and_b32 v89, 0xffff0000, v176\n\t" \
    "v_lshlrev_b32 v70, 16, v167\n\t" \
    "v_and_b32 v71, 0xffff0000, v167\n\t" \
    "v_lshlrev_b32 v90, 16, v177\n\t" \
    "v_and_b32 v91, 0xffff0000, v177\n\t" \
    "v_lshlrev_b32 v72, 16, v168\n\t" \
    "v_and_b32 v73, 0xffff0000, v168\n\t" \
    "v_lshlrev_b32 v92, 16, v178\n\t" \
    "v_and_b32 v93, 0xffff0000, v178\n\t" \
    "v_lshlrev_b32 v74, 16, v169\n\t" \
    "v_and_b32 v75, 0xffff0000, v169\n\t" \
    "v_lshlrev_b32 v94, 16, v179\n\t" \
    "v_and_b32 v95, 0xffff0000, v179\n\t" \
    "v_lshlrev_b32 v76, 16, v170\n\t" \
    "v_and_b32 v77, 0xffff0000, v170\n\t" \
    "v_lshlrev_b32 v96, 16, v180\n\t" \
    "v_and_b32 v97, 0xffff0000, v180\n\t" \
    "v_lshlrev_b32 v78, 16, v171\n\t" \
    "v_and_b32 v79, 0xffff0000, v171\n\t" \
    "v_lshlrev_b32 v98, 16, v181\n\t" \
    "v_and_b32 v99, 0xffff0000, v181\n\t" \
    "v_lshlrev_b32 v80, 16, v172\n\t" \
    "v_and_b32 v81, 0xffff0000, v172\n\t" \
    "v_lshlrev_b32 v100, 16, v182\n\t" \
    "v_and_b32 v101, 0xffff0000, v182\n\t" \
    "v_lshlrev_b32 v82, 16, v173\n\t" \
    "v_and_b32 v83, 0xffff0000, v173\n\t" \
    "v_lshlrev_b32 v102, 16, v183\n\t" \
    "v_and_b32 v103, 0xffff0000, v183\n\t" \
    "v_pk_add_f32 v[64:65], v[64:65], v[84:85]\n\t" \
    "v_pk_add_f32 v[66:67], v[66:67], v[86:87]\n\t" \
    "v_pk_add_f32 v[68:69], v[68:69], v[88:89]\n\t" \
    "v_pk_add_f32 v[70:71], v[70:71], v[90:91]\n\t" \
    "v_pk_add_f32 v[72:73], v[72:73], v[92:93]\n\t" \
    "v_pk_add_f32 v[74:75], v[74:75], v[94:95]\n\t" \
    "v_pk_add_f32 v[76:77], v[76:77], v[96:97]\n\t" \
    "v_pk_add_f32 v[78:79], v[78:79], v[98:99]\n\t" \
    "v_pk_add_f32 v[80:81], v[80:81], v[100:101]\n\t" \
    "v_pk_add_f32 v[82:83], v[82:83], v[102:103]\n\t" \
    ".Lft0_%=:\n\t" \
    "ds_read_b32 v194, %[f0]\n\t" \
    "s_waitcnt lgkmcnt(0)\n\t" \
    "v_cmp_ge_u32 vcc, %[u], v194\n\t" \
    "s_cbranch_vccnz .Lfs0_%=\n\t" \
    ".Lfu0_%=:\n\t" \
    "ds_read_b128 v[84:87], %[la] offset:0\n\t" \
    "ds_read_b128 v[88:91], %[la] offset:6144\n\t" \
    "ds_read_b128 v[92:95], %[la] offset:12288\n\t" \
    "ds_read_b128 v[96:99], %[la] offset:18432\n\t" \
    "ds_read_b128 v[100:103], %[la] offset:24576\n\t" \
    "ds_read_b128 v[104:107], %[la] offset:1024\n\t" \
    "ds_read_b128 v[108:111], %[la] offset:7168\n\t" \
    "ds_read_b128 v[112:115], %[la] offset:13312\n\t" \
    "ds_read_b128 v[116:119], %[la] offset:19456\n\t" \
    "ds_read_b128 v[120:123], %[la] offset:25600\n\t" \
    "ds_read_b128 v[124:127], %[la] offset:2048\n\t" \
    "s_waitcnt lgkmcnt(10)\n\t" \
    "v_pk_add_f32 v[64:65], v[64:65], v[84:85]\n\t" \
    "v_pk_add_f32 v[66:67], v[66:67], v[86:87]\n\t" \
    "ds_read_b128 v[128:131], %[la] offset:8192\n\t" \
    "s_waitcnt lgkmcnt(10)\n\t" \
    "v_pk_add_f32 v[68:69], v[68:69], v[88:89]\n\t" \
    "v_pk_add_f32 v[70:71], v[70:71], v[90:91]\n\t" \
    "ds_read_b128 v[132:135], %[la] offset:14336\n\t" \
    "s_waitcnt lgkmcnt(10)\n\t" \
    "v_pk_add_f32 v[72:73], v[72:73], v[92:93]\n\t" \
    "v_pk_add_f32 v[74:75], v[74:75], v[94:95]\n\t" \
    "ds_read_b128 v[136:139], %[la] offset:20480\n\t" \
    "s_waitcnt lgkmcnt(10)\n\t" \
    "v_pk_add_f32 v[76:77], v[76:77], v[96:97]\n\t" \
    "v_pk_add_f32 v[78:79], v[78:79], v[98:99]\n\t" \
    "ds_read_b128 v[140:143], %[la] offset:26624\n\t" \
    "s_waitcnt lgkmcnt(10)\n\t" \
    "v_pk_add_f32 v[80:81], v[80:81], v[100:101]\n\t" \
    "v_pk_add_f32 v[82:83], v[82:83], v[102:103]\n\t" \
    "ds_read_b128 v[144:147], %[la] offset:3072\n\t" \
    "s_waitcnt lgkmcnt(10)\n\t" \
    "v_pk_add_f32 v[64:65], v[64:65], v[104:105]\n\t" \
    "v_pk_add_f32 v[66:67], v[66:67], v[106:107]\n\t" \
    "ds_read_b128 v[148:151], %[la] offset:9216\n\t" \
    "s_waitcnt lgkmcnt(10)\n\t" \
    "v_pk_add_f32 v[68:69], v[68:69], v[108:109]\n\t" \
    "v_pk_add_f32 v[70:71], v[70:71], v[110:111]\n\t" \
    "ds_read_b128 v[152:155], %[la] offset:15360\n\t" \
    "s_waitcnt lgkmcnt(10)\n\t" \
    "v_pk_add_f32 v[72:73], v[72:73], v[112:113]\n\t" \
    "v_pk_add_f32 v[74:75], v[74:75], v[114:115]\n\t" \
    "ds_read_b128 v[156:159], %[la] offset:21504\n\t" \
    "s_waitcnt lgkmcnt(10)\n\t" \
    "v_pk_add_f32 v[76:77], v[76:77], v[116:117]\n\t" \
    "v_pk_add_f32 v[78:79], v[78:79], v[118:119]\n\t" \
    "ds_read_b128 v[160:163], %[la] offset:27648\n\t" \
    "s_waitcnt lgkmcnt(10)\n\t" \
    "v_pk_add_f32 v[80:81], v[80:81], v[120:121]\n\t" \
    "v_pk_add_f32 v[82:83], v[82:83], v[122:123]\n\t" \
    "s_waitcnt lgkmcnt(9)\n\t" \
    "v_pk_add_f32 v[64:65], v[64:65], v[124:125]\n\t" \
    "v_pk_add_f32 v[66:67], v[66:67], v[126:127]\n\t" \
    "s_waitcnt lgkmcnt(8)\n\t" \
    "v_pk_add_f32 v[68:69], v[68:69], v[128:129]\n\t" \
    "v_pk_add_f32 v[70:71], v[70:71], v[130:131]\n\t" \
    "s_waitcnt lgkmcnt(7)\n\t" \
    "v_pk_add_f32 v[72:73], v[72:73], v[132:133]\n\t" \
    "v_pk_add_f32 v[74:75], v[74:75], v[134:135]\n\t" \
    "s_waitcnt lgkmcnt(6)\n\t" \
    "v_pk_add_f32 v[76:77], v[76:77], v[136:137]\n\t" \
    "v_pk_add_f32 v[78:79], v[78:79], v[138:139]\n\t" \
    "s_waitcnt lgkmcnt(5)\n\t" \
    "v_pk_add_f32 v[80:81], v[80:81], v[140:141]\n\t" \
    "v_pk_add_f32 v[82:83], v[82:83], v[142:143]\n\t" \
    "s_waitcnt lgkmcnt(4)\n\t" \
    "v_pk_add_f32 v[64:65], v[64:65], v[144:145]\n\t" \
    "v_pk_add_f32 v[66:67], v[66:67], v[146:147]\n\t" \
    "s_waitcnt lgkmcnt(3)\n\t" \
    "v_pk_add_f32 v[68:69], v[68:69], v[148:149]\n\t" \
    "v_pk_add_f32 v[70:71], v[70:71], v[150:151]\n\t" \
    "s_waitcnt lgkmcnt(2)\n\t" \
    "v_pk_add_f32 v[72:73], v[72:73], v[152:153]\n\t" \
    "v_pk_add_f32 v[74:75], v[74:75], v[154:155]\n\t" \
    "s_waitcnt lgkmcnt(1)\n\t" \
    "v_pk_add_f32 v[76:77], v[76:77], v[156:157]\n\t" \
    "v_pk_add_f32 v[78:79], v[78:79], v[158:159]\n\t" \
    "s_waitcnt lgkmcnt(0)\n\t" \
    "v_pk_add_f32 v[80:81], v[80:81], v[160:161]\n\t" \
    "v_pk_add_f32 v[82:83], v[82:83], v[162:163]\n\t" \
    ".Lft1_%=:\n\t" \
    "ds_read_b32 v194, %[f1]\n\t" \
    "s_waitcnt lgkmcnt(0)\n\t" \
    "v_cmp_ge_u32 vcc, %[u], v194\n\t" \
    "s_cbranch_vccnz .Lfs1_%=\n\t" \
    ".Lfu1_%=:\n\t" \
    ASM_ST2 \
    "ds_read_b128 v[84:87], %[la] offset:4096\n\t" \
    "ds_read_b128 v[88:91], %[la] offset:10240\n\t" \
    "ds_read_b128 v[92:95], %[la] offset:16384\n\t" \
    "ds_read_b128 v[96:99], %[la] offset:22528\n\t" \
    "ds_read_b128 v[100:103], %[la] offset:28672\n\t" \
    "ds_read_b128 v[104:107], %[la] offset:5120\n\t" \
    "ds_read_b128 v[108:111], %[la] offset:11264\n\t" \
    "ds_read_b128 v[112:115], %[la] offset:17408\n\t" \
    "ds_read_b128 v[116:119], %[la] offset:23552\n\t" \
    "ds_read_b128 v[120:123], %[la] offset:29696\n\t" \
    "v_mov_b32 v194, %[un]\n\t" \
    "ds_write_b32 %[fd], v194\n\t" \
    "s_waitcnt lgkmcnt(10)\n\t" \
    "v_pk_add_f32 v[64:65], v[64:65], v[84:85]\n\t" \
    "v_pk_add_f32 v[66:67], v[66:67], v[86:87]\n\t" \
    "s_waitcnt lgkmcnt(9)\n\t" \
    "v_pk_add_f32 v[68:69], v[68:69], v[88:89]\n\t" \
    "v_pk_add_f32 v[70:71], v[70:71], v[90:91]\n\t" \
    "s_waitcnt lgkmcnt(8)\n\t" \
    "v_pk_add_f32 v[72:73], v[72:73], v[92:93]\n\t" \
    "v_pk_add_f32 v[74:75], v[74:75], v[94:95]\n\t" \
    "s_waitcnt lgkmcnt(7)\n\t" \
    "v_pk_add_f32 v[76:77], v[76:77], v[96:97]\n\t" \
    "v_pk_add_f32 v[78:79], v[78:79], v[98:99]\n\t" \
    "s_waitcnt lgkmcnt(6)\n\t" \
    "v_pk_add_f32 v[80:81], v[80:81], v[100:101]\n\t" \
    "v_pk_add_f32 v[82:83], v[82:83], v[102:103]\n\t" \
    "s_waitcnt lgkmcnt(5)\n\t" \
    "v_pk_add_f32 v[64:65], v[64:65], v[104:105]\n\t" \
    "v_pk_add_f32 v[66:67], v[66:67], v[106:107]\n\t" \
    "s_waitcnt lgkmcnt(4)\n\t" \
    "v_pk_add_f32 v[68:69], v[68:69], v[108:109]\n\t" \
    "v_pk_add_f32 v[70:71], v[70:71], v[110:111]\n\t" \
    "s_waitcnt lgkmcnt(3)\n\t" \
    "v_pk_add_f32 v[72:73], v[72:73], v[112:113]\n\t" \
    "v_pk_add_f32 v[74:75], v[74:75], v[114:115]\n\t" \
    "s_waitcnt lgkmcnt(2)\n\t" \
    "v_pk_add_f32 v[76:77], v[76:77], v[116:117]\n\t" \
    "v_pk_add_f32 v[78:79], v[78:79], v[118:119]\n\t" \
    "s_waitcnt lgkmcnt(1)\n\t" \
    "v_pk_add_f32 v[80:81], v[80:81], v[120:121]\n\t" \
    "v_pk_add_f32 v[82:83], v[82:83], v[122:123]\n\t" \
    "v_cvt_pk_bf16_f32 v184, v64, v65\n\t" \
    "v_cvt_pk_bf16_f32 v185, v66, v67\n\t" \
    "v_cvt_pk_bf16_f32 v186, v68, v69\n\t" \
    "v_cvt_pk_bf16_f32 v187, v70, v71\n\t" \
    "v_cvt_pk_bf16_f32 v188, v72, v73\n\t" \
    "v_cvt_pk_bf16_f32 v189, v74, v75\n\t" \
    "v_cvt_pk_bf16_f32 v190, v76, v77\n\t" \
    "v_cvt_pk_bf16_f32 v191, v78, v79\n\t" \
    "v_cvt_pk_bf16_f32 v192, v80, v81\n\t" \
    "v_cvt_pk_bf16_f32 v193, v82, v83\n\t" \
    "v_permlane16_swap_b32 v184, v186\n\t" \
    "v_permlane16_swap_b32 v185, v187\n\t" \
    "v_permlane16_swap_b32 v188, v190\n\t" \
    "v_permlane16_swap_b32 v189, v191\n\t" \
    "buffer_store_dwordx4 v[184:187], %[pa], %[ro], %[so] offen nt\n\t" \
    "buffer_store_dwordx4 v[188:191], %[pa], %[ro], %[so] offen offset:64 nt\n\t" \
    "buffer_store_dwordx2 v[192:193], %[pb], %[ro], %[so] offen offset:128 nt\n\t" \
    "buffer_load_dwordx2 v[164:165], %[o0], %[rd], %[n1] offen sc1\n\t" \
    "buffer_load_dwordx2 v[166:167], %[o1], %[rd], %[n1] offen sc1\n\t" \
    "buffer_load_dwordx2 v[168:169], %[o2], %[rd], %[n1] offen sc1\n\t" \
    "buffer_load_dwordx2 v[170:171], %[o3], %[rd], %[n1] offen sc1\n\t" \
    "buffer_load_dwordx2 v[172:173], %[o4], %[rd], %[n1] offen sc1\n\t" \
    "buffer_load_dwordx2 v[174:175], %[o0], %[rd], %[na] offen sc1\n\t" \
    "buffer_load_dwordx2 v[176:177], %[o1], %[rd], %[na] offen sc1\n\t" \
    "buffer_load_dwordx2 v[178:179], %[o2], %[rd], %[na] offen sc1\n\t" \
    "buffer_load_dwordx2 v[180:181], %[o3], %[rd], %[na] offen sc1\n\t" \
    "buffer_load_dwordx2 v[182:183], %[o4], %[rd], %[na] offen sc1\n\t"
#define ASM_WCS \
    "v_mfma_f32_16x16x32_bf16 v[196:199], v[0:3], v[216:219], 0\n\t" \
    "v_mfma_f32_16x16x32_bf16 v[200:203], v[80:83], v[216:219], 0\n\t" \
    "v_mfma_f32_16x16x32_bf16 v[196:199], v[4:7], v[216:219], v[196:199]\n\t" \
    "v_mfma_f32_16x16x32_bf16 v[200:203], v[84:87], v[216:219], v[200:203]\n\t" \
    "v_mfma_f32_16x16x32_bf16 v[196:199], v[8:11], v[216:219], v[196:199]\n\t" \
    "v_mfma_f32_16x16x32_bf16 v[200:203], v[88:91], v[216:219], v[200:203]\n\t" \
    "v_mfma_f32_16x16x32_bf16 v[196:199], v[12:15], v[216:219], v[196:199]\n\t" \
    "v_mfma_f32_16x16x32_bf16 v[200:203], v[92:95], v[216:219], v[200:203]\n\t" \
    "v_mfma_f32_16x16x32_bf16 v[196:199], v[16:19], v[216:219], v[196:199]\n\t" \
    "v_mfma_f32_16x16x32_bf16 v[200:203], v[96:99], v[216:219], v[200:203]\n\t" \
    "v_mfma_f32_16x16x32_bf16 v[196:199], v[20:23], v[216:219], v[196:199]\n\t" \
    "v_mfma_f32_16x16x32_bf16 v[200:203], v[100:103], v[216:219], v[200:203]\n\t" \
    "v_mfma_f32_16x16x32_bf16 v[196:199], v[24:27], v[216:219], v[196:199]\n\t" \
    "v_mfma_f32_16x16x32_bf16 v[200:203], v[104:107], v[216:219], v[200:203]\n\t" \
    "v_mfma_f32_16x16x32_bf16 v[196:199], v[28:31], v[216:219], v[196:199]\n\t" \
    "v_mfma_f32_16x16x32_bf16 v[200:203], v[108:111], v[216:219], v[200:203]\n\t" \
    "v_mfma_f32_16x16x32_bf16 v[196:199], v[32:35], v[216:219], v[196:199]\n\t" \
    "v_mfma_f32_16x16x32_bf16 v[200:203], v[112:115], v[216:219], v[200:203]\n\t" \
    "v_mfma_f32_16x16x32_bf16 v[196:199], v[36:39], v[216:219], v[196:199]\n\t" \
    "v_mfma_f32_16x16x32_bf16 v[200:203], v[116:119], v[216:219], v[200:203]\n\t" \
    "v_mfma_f32_16x16x32_bf16 v[196:199], v[40:43], v[216:219], v[196:199]\n\t" \
    "v_mfma_f32_16x16x32_bf16 v[200:203], v[120:123], v[216:219], v[200:203]\n\t" \
    "v_mfma_f32_16x16x32_bf16 v[196:199], v[44:47], v[216:219], v[196:199]\n\t" \
    "v_mfma_f32_16x16x32_bf16 v[200:203], v[124:127], v[216:219], v[200:203]\n\t" \
    "v_mfma_f32_16x16x32_bf16 v[196:199], v[48:51], v[216:219], v[196:199]\n\t" \
    "v_mfma_f32_16x16x32_bf16 v[200:203], v[128:131], v[216:219], v[200:203]\n\t" \
    "v_mfma_f32_16x16x32_bf16 v[196:199], v[52:55], v[216:219], v[196:199]\n\t" \
    "v_mfma_f32_16x16x32_bf16 v[200:203], v[132:135], v[216:219], v[200:203]\n\t" \
    "v_mfma_f32_16x16x32_bf16 v[196:199], v[56:59], v[216:219], v[196:199]\n\t" \
    "v_mfma_f32_16x16x32_bf16 v[200:203], v[136:139], v[216:219], v[200:203]\n\t" \
    "v_mfma_f32_16x16x32_bf16 v[196:199], v[60:63], v[216:219], v[196:199]\n\t" \
    "v_mfma_f32_16x16x32_bf16 v[200:203], v[140:143], v[216:219], v[200:203]\n\t" \
    "v_mfma_f32_16x16x32_bf16 v[196:199], v[64:67], v[216:219], v[196:199]\n\t" \
    "v_mfma_f32_16x16x32_bf16 v[200:203], v[144:147], v[216:219], v[200:203]\n\t" \
    "v_mfma_f32_16x16x32_bf16 v[196:199], v[68:71], v[216:219], v[196:199]\n\t" \
    "v_mfma_f32_16x16x32_bf16 v[200:203], v[148:151], v[216:219], v[200:203]\n\t" \
    "v_mfma_f32_16x16x32_bf16 v[196:199], v[72:75], v[216:219], v[196:199]\n\t" \
    "v_mfma_f32_16x16x32_bf16 v[200:203], v[152:155], v[216:219], v[200:203]\n\t" \
    "v_mfma_f32_16x16x32_bf16 v[196:199], v[76:79], v[216:219], v[196:199]\n\t" \
    "v_mfma_f32_16x16x32_bf16 v[200:203], v[156:159], v[216:219], v[200:203]\n\t" \
    "s_nop 7\n\t"
#define ASM_WT1_LO \
    "s_branch .Lt0%=\n\t" \
    ".Ltr%=:\n\t" \
    "s_cmp_gt_i32 %[n], 0\n\t" \
    "s_cbranch_scc0 .Ltg%=\n\t" \
    ".Lfw%=:\n\t" \
    "s_mov_b32 %[a], 0\n\t" \
    "v_max_u32 v252, v232, v233\n\t" \
    "v_max3_u32 v252, v234, v235, v252\n\t" \
    "v_cmp_eq_u32 vcc, -1, v252\n\t" \
    "s_cbranch_vccz .Lgw0_%=\n\t" \
    "buffer_load_dwordx4 v[232:235], %[l16], %[rs], %[i0] offen offset:0 sc1\n\t" \
    "s_mov_b32 %[a], 1\n\t" \
    ".Lgw0_%=:\n\t" \
    "v_max_u32 v252, v236, v237\n\t" \
    "v_max3_u32 v252, v238, v239, v252\n\t" \
    "v_cmp_eq_u32 vcc, -1, v252\n\t" \
    "s_cbranch_vccz .Lgw1_%=\n\t" \
    "buffer_load_dwordx4 v[236:239], %[l16], %[rs], %[i0] offen offset:1024 sc1\n\t" \
    "s_mov_b32 %[a], 1\n\t" \
    ".Lgw1_%=:\n\t" \
    "v_max_u32 v252, v240, v241\n\t" \
    "v_max3_u32 v252, v242, v243, v252\n\t" \
    "v_cmp_eq_u32 vcc, -1, v252\n\t" \
    "s_cbranch_vccz .Lgw2_%=\n\t" \
    "buffer_load_dwordx4 v[240:243], %[l16], %[rs], %[i0] offen offset:2048 sc1\n\t" \
    "s_mov_b32 %[a], 1\n\t" \
    ".Lgw2_%=:\n\t" \
    "v_max_u32 v252, v244, v245\n\t" \
    "v_max3_u32 v252, v246, v247, v252\n\t" \
    "v_cmp_eq_u32 vcc, -1, v252\n\t" \
    "s_cbranch_vccz .Lgw3_%=\n\t" \
    "buffer_load_dwordx4 v[244:247], %[l16], %[rs], %[i0] offen offset:3072 sc1\n\t" \
    "s_mov_b32 %[a], 1\n\t" \
    ".Lgw3_%=:\n\t" \
    "v_max_u32 v252, v248, v249\n\t" \
    "v_max3_u32 v252, v250, v251, v252\n\t" \
    "v_cmp_eq_u32 vcc, -1, v252\n\t" \
    "s_cbranch_vccz .Lgw4_%=\n\t" \
    "buffer_load_dwordx4 v[248:251], %[l16], %[rs], %[i1] offen offset:0 sc1\n\t" \
    "s_mov_b32 %[a], 1\n\t" \
    ".Lgw4_%=:\n\t" \
    "s_cmp_eq_u32 %[a], 0\n\t" \
    "s_cbranch_scc1 .Lhw%=\n\t" \
    "s_waitcnt vmcnt(20)\n\t" \
    ".Liw%=:\n\t" \
    "v_max_u32 v252, v232, v233\n\t" \
    "v_max3_u32 v252, v234, v235, v252\n\t" \
    "v_max3_u32 v252, v236, v237, v252\n\t" \
    "v_max3_u32 v252, v238, v239, v252\n\t" \
    "v_max3_u32 v252, v240, v241, v252\n\t" \
    "v_max3_u32 v252, v242, v243, v252\n\t" \
    "v_max3_u32 v252, v244, v245, v252\n\t" \
    "v_max3_u32 v252, v246, v247, v252\n\t" \
    "v_max3_u32 v252, v248, v249, v252\n\t" \
    "v_max3_u32 v252, v250, v251, v252\n\t" \
    "v_cmp_eq_u32 vcc, -1, v252\n\t" \
    "s_cbranch_vccz .Lhw%=\n\t" \
    "s_sleep 1\n\t" \
    "v_max_u32 v252, v232, v233\n\t" \
    "v_max3_u32 v252, v234, v235, v252\n\t" \
    "v_max3_u32 v252, v236, v237, v252\n\t" \
    "v_max3_u32 v252, v238, v239, v252\n\t" \
    "v_max3_u32 v252, v240, v241, v252\n\t" \
    "v_max3_u32 v252, v242, v243, v252\n\t" \
    "v_max3_u32 v252, v244, v245, v252\n\t" \
    "v_max3_u32 v252, v246, v247, v252\n\t" \
    "v_max3_u32 v252, v248, v249, v252\n\t" \
    "v_max3_u32 v252, v250, v251, v252\n\t" \
    "v_cmp_eq_u32 vcc, -1, v252\n\t" \
    "s_cbranch_vccz .Lhw%=\n\t" \
    "s_sleep 1\n\t" \
    "v_max_u32 v252, v232, v233\n\t" \
    "v_max3_u32 v252, v234, v235, v252\n\t" \
    "v_max3_u32 v252, v236, v237, v252\n\t" \
    "v_max3_u32 v252, v238, v239, v252\n\t" \
    "v_max3_u32 v252, v240, v241, v252\n\t" \
    "v_max3_u32 v252, v242, v243, v252\n\t" \
    "v_max3_u32 v252, v244, v245, v252\n\t" \
    "v_max3_u32 v252, v246, v247, v252\n\t" \
    "v_max3_u32 v252, v248, v249, v252\n\t" \
    "v_max3_u32 v252, v250, v251, v252\n\t" \
    "v_cmp_eq_u32 vcc, -1, v252\n\t" \
    "s_cbranch_vccz .Lhw%=\n\t" \
    "s_sleep 1\n\t" \
    "v_max_u32 v252, v232, v233\n\t" \
    "v_max3_u32 v252, v234, v235, v252\n\t" \
    "v_max3_u32 v252, v236, v237, v252\n\t" \
    "v_max3_u32 v252, v238, v239, v252\n\t" \
    "v_max3_u32 v252, v240, v241, v252\n\t" \
    "v_max3_u32 v252, v242, v243, v252\n\t" \
    "v_max3_u32 v252, v244, v245, v252\n\t" \
    "v_max3_u32 v252, v246, v247, v252\n\t" \
    "v_max3_u32 v252, v248, v249, v252\n\t" \
    "v_max3_u32 v252, v250, v251, v252\n\t" \
    "v_cmp_eq_u32 vcc, -1, v252\n\t" \
    "s_cbranch_vccz .Lhw%=\n\t" \
    "s_sleep 1\n\t" \
    "v_max_u32 v252, v232, v233\n\t" \
    "v_max3_u32 v252, v234, v235, v252\n\t" \
    "v_max3_u32 v252, v236, v237, v252\n\t" \
    "v_max3_u32 v252, v238, v239, v252\n\t" \
    "v_max3_u32 v252, v240, v241, v252\n\t" \
    "v_max3_u32 v252, v242, v243, v252\n\t" \
    "v_max3_u32 v252, v244, v245, v252\n\t" \
    "v_max3_u32 v252, v246, v247, v252\n\t" \
    "v_max3_u32 v252, v248, v249, v252\n\t" \
    "v_max3_u32 v252, v250, v251, v252\n\t" \
    "v_cmp_eq_u32 vcc, -1, v252\n\t" \
    "s_cbranch_vccz .Lhw%=\n\t" \
    "s_sleep 1\n\t" \
    "v_max_u32 v252, v232, v233\n\t" \
    "v_max3_u32 v252, v234, v235, v252\n\t" \
    "v_max3_u32 v252, v236, v237, v252\n\t" \
    "v_max3_u32 v252, v238, v239, v252\n\t" \
    "v_max3_u32 v252, v240, v241, v252\n\t" \
    "v_max3_u32 v252, v242, v243, v252\n\t" \
    "v_max3_u32 v252, v244, v245, v252\n\t" \
    "v_max3_u32 v252, v246, v247, v252\n\t" \
    "v_max3_u32 v252, v248, v249, v252\n\t" \
    "v_max3_u32 v252, v250, v251, v252\n\t" \
    "v_cmp_eq_u32 vcc, -1, v252\n\t" \
    "s_cbranch_vccz .Lhw%=\n\t" \
    "s_sleep 1\n\t" \
    "s_sub_u32 %[n], %[n], 1\n\t" \
    "s_cmp_gt_i32 %[n], 0\n\t" \
    "s_cbranch_scc1 .Lfw%=\n\t" \
    ".Lhw%=:\n\t" \
    "s_branch .Lta%=\n\t" \
    ".Lt0%=:\n\t" \
    "s_waitcnt vmcnt(1)\n\t" \
    ".Lta%=:\n\t" \
    "ds_write_b128 %[lw], v[232:235] offset:0\n\t" \
    "v_max_u32 v252, v232, v233\n\t" \
    "v_max3_u32 v252, v234, v235, v252\n\t" \
    "v_mfma_f32_16x16x32_bf16 v[188:191], v[232:235], v[232:235], 0\n\t" \
    "ds_write_b128 %[lw], v[236:239] offset:1024\n\t" \
    "v_max3_u32 v252, v236, v237, v252\n\t" \
    "v_max3_u32 v252, v238, v239, v252\n\t" \
    "v_mfma_f32_16x16x32_bf16 v[188:191], v[236:239], v[236:239], v[188:191]\n\t" \
    "ds_write_b128 %[lw], v[240:243] offset:2048\n\t" \
    "v_max3_u32 v252, v240, v241, v252\n\t" \
    "v_max3_u32 v252, v242, v243, v252\n\t" \
    "v_mfma_f32_16x16x32_bf16 v[188:191], v[240:243], v[240:243], v[188:191]\n\t" \
    "ds_write_b128 %[lw], v[244:247] offset:3072\n\t" \
    "v_max3_u32 v252, v244, v245, v252\n\t" \
    "v_max3_u32 v252, v246, v247, v252\n\t" \
    "v_mfma_f32_16x16x32_bf16 v[188:191], v[244:247], v[244:247], v[188:191]\n\t" \
    "ds_write_b128 %[lw], v[248:251] offset:4096\n\t" \
    "v_max3_u32 v252, v248, v249, v252\n\t" \
    "v_max3_u32 v252, v250, v251, v252\n\t" \
    "v_mfma_f32_16x16x32_bf16 v[188:191], v[248:251], v[248:251], v[188:191]\n\t" \
    "v_mfma_f32_16x16x32_bf16 v[192:195], v[216:219], v[232:235], 0\n\t" \
    "v_mfma_f32_16x16x32_bf16 v[192:195], v[216:219], v[236:239], v[192:195]\n\t" \
    "v_mfma_f32_16x16x32_bf16 v[192:195], v[216:219], v[240:243], v[192:195]\n\t" \
    "v_mfma_f32_16x16x32_bf16 v[192:195], v[216:219], v[244:247], v[192:195]\n\t" \
    "v_mfma_f32_16x16x32_bf16 v[192:195], v[216:219], v[248:251], v[192:195]\n\t" \
    "v_cmp_eq_u32 vcc, -1, v252\n\t" \
    "s_cbranch_vccnz .Ltr%=\n\t" \
    ".Ltg%=:\n\t" \
    ASM_ST2 \
    "s_nop 5\n\t" \
    "ds_write_b128 %[lw], v[188:191] offset:5120\n\t" \
    "ds_write_b128 %[lw], v[192:195] offset:6144\n\t" \
    "s_waitcnt lgkmcnt(0)\n\t" \
    "s_setprio 2\n\t" \
    "s_barrier\n\t" ASM_ST \
    "ds_read_b128 v[160:163], %[la] offset:0\n\t" \
    "ds_read_b128 v[164:167], %[la] offset:1024\n\t" \
    "ds_read_b128 v[168:171], %[la] offset:2048\n\t" \
    "ds_read2st64_b32 v[204:205], %[sr] offset0:20 offset1:24\n\t" \
    "ds_read_b128 v[172:175], %[la] offset:3072\n\t" \
    "ds_read_b128 v[176:179], %[la] offset:8192\n\t" \
    "ds_read_b128 v[180:183], %[la] offset:9216\n\t" \
    "ds_read_b128 v[184:187], %[la] offset:10240\n\t" \
    "s_waitcnt lgkmcnt(7)\n\t" \
    "v_mfma_f32_16x16x32_bf16 v[192:195], v[80:83], v[160:163], 0\n\t" \
    "s_waitcnt lgkmcnt(6)\n\t" \
    "v_mfma_f32_16x16x32_bf16 v[192:195], v[84:87], v[164:167], v[192:195]\n\t" \
    "v_mfma_f32_16x16x32_bf16 v[188:191], v[0:3], v[160:163], 0\n\t" \
    "ds_read_b128 v[160:163], %[la] offset:11264\n\t" \
    "v_mfma_f32_16x16x32_bf16 v[188:191], v[4:7], v[164:167], v[188:191]\n\t" \
    "ds_read_b128 v[164:167], %[la] offset:16384\n\t" \
    "s_waitcnt lgkmcnt(7)\n\t" \
    "v_mfma_f32_16x16x32_bf16 v[188:191], v[8:11], v[168:171], v[188:191]\n\t" \
    "s_waitcnt lgkmcnt(6)\n\t" \
    "s_waitcnt lgkmcnt(5)\n\t" \
    "v_mfma_f32_16x16x32_bf16 v[188:191], v[12:15], v[172:175], v[188:191]\n\t" \
    "v_permlane16_swap_b32 v204, v205\n\t" \
    "v_mfma_f32_16x16x32_bf16 v[192:195], v[88:91], v[168:171], v[192:195]\n\t" \
    "ds_read_b128 v[168:171], %[la] offset:17408\n\t" \
    "v_mfma_f32_16x16x32_bf16 v[192:195], v[92:95], v[172:175], v[192:195]\n\t" \
    "ds_read_b128 v[172:175], %[la] offset:18432\n\t" \
    "v_add_f32 v206, v204, v205\n\t" \
    "s_waitcnt lgkmcnt(6)\n\t" \
    "v_mfma_f32_16x16x32_bf16 v[192:195], v[96:99], v[176:179], v[192:195]\n\t" \
    "v_add_f32 v207, v204, v205\n\t" \
    "s_waitcnt lgkmcnt(5)\n\t" \
    "v_mfma_f32_16x16x32_bf16 v[192:195], v[100:103], v[180:183], v[192:195]\n\t" \
    "v_mfma_f32_16x16x32_bf16 v[188:191], v[16:19], v[176:179], v[188:191]\n\t" \
    "ds_read_b128 v[176:179], %[la] offset:19456\n\t" \
    "v_permlane32_swap_b32 v206, v207\n\t" \
    "v_mfma_f32_16x16x32_bf16 v[188:191], v[20:23], v[180:183], v[188:191]\n\t" \
    "ds_read_b128 v[180:183], %[la] offset:24576\n\t" \
    "s_waitcnt lgkmcnt(6)\n\t" \
    "v_mfma_f32_16x16x32_bf16 v[188:191], v[24:27], v[184:187], v[188:191]\n\t" \
    "v_add_f32 v204, v206, v207\n\t" \
    "s_waitcnt lgkmcnt(5)\n\t" \
    "v_mfma_f32_16x16x32_bf16 v[188:191], v[28:31], v[160:163], v[188:191]\n\t" \
    "v_add_f32 v205, v206, v207\n\t" \
    "v_mfma_f32_16x16x32_bf16 v[192:195], v[104:107], v[184:187], v[192:195]\n\t" \
    "ds_read_b128 v[184:187], %[la] offset:25600\n\t" \
    "v_mfma_f32_16x16x32_bf16 v[192:195], v[108:111], v[160:163], v[192:195]\n\t" \
    "ds_read_b128 v[160:163], %[la] offset:26624\n\t" \
    "v_permlane16_swap_b32 v204, v205\n\t" \
    "s_waitcnt lgkmcnt(6)\n\t" \
    "v_mfma_f32_16x16x32_bf16 v[192:195], v[112:115], v[164:167], v[192:195]\n\t" \
    "s_waitcnt lgkmcnt(5)\n\t" \
    "v_mfma_f32_16x16x32_bf16 v[192:195], v[116:119], v[168:171], v[192:195]\n\t" \
    "v_mul_f32 v205, %[kd], v205\n\t" \
    "v_mfma_f32_16x16x32_bf16 v[188:191], v[32:35], v[164:167], v[188:191]\n\t" \
    "ds_read_b128 v[164:167], %[la] offset:27648\n\t" \
    "v_mul_f32 v204, %[kd], v204\n\t" \
    "v_mfma_f32_16x16x32_bf16 v[188:191], v[36:39], v[168:171], v[188:191]\n\t" \
    "ds_read_b128 v[168:171], %[la] offset:4096\n\t" \
    "v_fma_f32 v204, -v205, v205, v204\n\t" \
    "s_waitcnt lgkmcnt(6)\n\t" \
    "v_mfma_f32_16x16x32_bf16 v[188:191], v[40:43], v[172:175], v[188:191]\n\t" \
    "v_max_f32 v204, 0, v204\n\t" \
    "s_waitcnt lgkmcnt(5)\n\t" \
    "v_mfma_f32_16x16x32_bf16 v[188:191], v[44:47], v[176:179], v[188:191]\n\t" \
    "v_add_f32 v206, %[ke], v204\n\t" \
    "v_mfma_f32_16x16x32_bf16 v[192:195], v[120:123], v[172:175], v[192:195]\n\t" \
    "ds_read_b128 v[172:175], %[la] offset:12288\n\t" \
    "v_rsq_f32 v206, v206\n\t" \
    "v_mfma_f32_16x16x32_bf16 v[192:195], v[124:127], v[176:179], v[192:195]\n\t" \
    "ds_read_b128 v[176:179], %[la] offset:20480\n\t" \
    "s_waitcnt lgkmcnt(6)\n\t" \
    "v_mfma_f32_16x16x32_bf16 v[192:195], v[128:131], v[180:183], v[192:195]\n\t" \
    "v_mul_f32 v207, v204, v206\n\t" \
    "s_waitcnt lgkmcnt(5)\n\t" \
    "v_mfma_f32_16x16x32_bf16 v[192:195], v[132:135], v[184:187], v[192:195]\n\t" \
    "v_fma_f32 v207, v207, v206, %[ke]\n\t" \
    "v_mfma_f32_16x16x32_bf16 v[188:191], v[48:51], v[180:183], v[188:191]\n\t" \
    "ds_read_b128 v[180:183], %[la] offset:28672\n\t" \
    "v_rsq_f32 v207, v207\n\t" \
    "v_mfma_f32_16x16x32_bf16 v[188:191], v[52:55], v[184:187], v[188:191]\n\t" \
    "v_mul_f32 v206, v206, v207\n\t" \
    "v_mul_f32 v207, -v205, v206\n\t" \
    "s_waitcnt lgkmcnt(5)\n\t" \
    "v_mfma_f32_16x16x32_bf16 v[188:191], v[56:59], v[160:163], v[188:191]\n\t" \
    "v_mul_f32 v208, v196, v207\n\t" \
    "v_mul_f32 v209, v197, v207\n\t" \
    "s_waitcnt lgkmcnt(4)\n\t" \
    "v_mfma_f32_16x16x32_bf16 v[188:191], v[60:63], v[164:167], v[188:191]\n\t" \
    "v_mul_f32 v210, v198, v207\n\t" \
    "v_mul_f32 v211, v199, v207\n\t" \
    "v_mfma_f32_16x16x32_bf16 v[192:195], v[136:139], v[160:163], v[192:195]\n\t" \
    "v_mul_f32 v212, v200, v207\n\t" \
    "v_mul_f32 v213, v201, v207\n\t" \
    "v_mfma_f32_16x16x32_bf16 v[192:195], v[140:143], v[164:167], v[192:195]\n\t" \
    "v_mul_f32 v214, v202, v207\n\t" \
    "v_mul_f32 v215, v203, v207\n\t" \
    "s_waitcnt lgkmcnt(3)\n\t" \
    "v_mfma_f32_16x16x32_bf16 v[192:195], v[144:147], v[168:171], v[192:195]\n\t" \
    "s_cmp_lg_u32 %[sw], 0\n\t" \
    "s_cbranch_scc1 .Lts%=\n\t" \
    "v_cvt_pk_bf16_f32 v217, v204, v204\n\t" \
    "v_lshlrev_b32 v217, 16, v217\n\t" \
    "v_add_f32 v217, %[ke], v217\n\t" \
    "v_cvt_pk_bf16_f32 v217, v217, v217\n\t" \
    "v_lshlrev_b32 v217, 16, v217\n\t" \
    "v_sqrt_f32 v217, v217\n\t" \
    "v_cvt_pk_bf16_f32 v216, v205, v205\n\t" \
    "v_lshlrev_b32 v216, 16, v216\n\t" \
    "v_cvt_pk_bf16_f32 v217, v217, v217\n\t" \
    "v_lshlrev_b32 v217, 16, v217\n\t" \
    "v_rcp_f32 v217, v217\n\t" \
    "v_lshrrev_b32 v218, 1, %[l16]\n\t" \
    "v_and_b32 v218, 0x78, v218\n\t" \
    "buffer_store_dwordx2 v[216:217], v218, %[rs], %[ss] offen sc1\n\t" \
    ".Lts%=:\n\t" \
    "v_mov_b64 v[216:217], -1\n\t" \
    "s_waitcnt lgkmcnt(2)\n\t" \
    "v_mfma_f32_16x16x32_bf16 v[192:195], v[148:151], v[172:175], v[192:195]\n\t" \
    "v_mov_b64 v[218:219], -1\n\t" \
    "buffer_store_dwordx4 v[216:219], %[ob], %[rs], %[sa] offen sc1\n\t" \
    "v_mfma_f32_16x16x32_bf16 v[188:191], v[64:67], v[168:171], v[188:191]\n\t" \
    "v_mfma_f32_16x16x32_bf16 v[188:191], v[68:71], v[172:175], v[188:191]\n\t" \
    "s_waitcnt lgkmcnt(1)\n\t" \
    "v_mfma_f32_16x16x32_bf16 v[188:191], v[72:75], v[176:179], v[188:191]\n\t" \
    "s_waitcnt lgkmcnt(0)\n\t" \
    "v_mfma_f32_16x16x32_bf16 v[188:191], v[76:79], v[180:183], v[188:191]\n\t" \
    "v_mfma_f32_16x16x32_bf16 v[192:195], v[152:155], v[176:179], v[192:195]\n\t" \
    "v_mfma_f32_16x16x32_bf16 v[192:195], v[156:159], v[180:183], v[192:195]\n\t" \
    "s_setprio 2\n\t" \
    "buffer_load_dwordx4 v[232:235], %[l16], %[rs], %[n0] offen offset:0 sc1\n\t" \
    "buffer_load_dwordx4 v[236:239], %[l16], %[rs], %[n0] offen offset:1024 sc1\n\t" \
    "buffer_load_dwordx4 v[240:243], %[l16], %[rs], %[n0] offen offset:2048 sc1\n\t" \
    "buffer_load_dwordx4 v[244:247], %[l16], %[rs], %[n0] offen offset:3072 sc1\n\t" \
    "buffer_load_dwordx4 v[248:251], %[l16], %[rs], %[n1] offen offset:0 sc1\n\t" \
    ASM_ST3 \
    "v_fma_f32 v188, v188, v206, v208\n\t" \
    "v_fma_f32 v189, v189, v206, v209\n\t" \
    "v_fma_f32 v190, v190, v206, v210\n\t" \
    "v_fma_f32 v191, v191, v206, v211\n\t" \
    "v_mul_f32 v160, v188, v188\n\t" \
    "v_mul_f32 v161, v189, v189\n\t" \
    "v_mul_f32 v162, v190, v190\n\t" \
    "v_mul_f32 v163, v191, v191\n\t" \
    "v_fma_f32 v160, v160, %[k1], v220\n\t" \
    "v_fma_f32 v161, v161, %[k1], v220\n\t" \
    "v_fma_f32 v162, v162, %[k1], v220\n\t" \
    "v_fma_f32 v163, v163, %[k1], v220\n\t" \
    "v_mul_f32 v160, v188, v160\n\t" \
    "v_mul_f32 v161, v189, v161\n\t" \
    "v_mul_f32 v162, v190, v162\n\t" \
    "v_mul_f32 v163, v191, v163\n\t" \
    "v_fma_f32 v192, v192, v206, v212\n\t" \
    "v_fma_f32 v193, v193, v206, v213\n\t" \
    "v_fma_f32 v194, v194, v206, v214\n\t" \
    "v_fma_f32 v195, v195, v206, v215\n\t" \
    "v_mul_f32 v164, v192, v192\n\t" \
    "v_mul_f32 v165, v193, v193\n\t" \
    "v_mul_f32 v166, v194, v194\n\t" \
    "v_mul_f32 v167, v195, v195\n\t" \
    "v_fma_f32 v164, v164, %[k1], v220\n\t" \
    "v_fma_f32 v165, v165, %[k1], v220\n\t" \
    "v_fma_f32 v166, v166, %[k1], v220\n\t" \
    "v_fma_f32 v167, v167, %[k1], v220\n\t" \
    "v_mul_f32 v164, v192, v164\n\t" \
    "v_mul_f32 v165, v193, v165\n\t" \
    "v_mul_f32 v166, v194, v166\n\t" \
    "v_mul_f32 v167, v195, v167\n\t" \
    "v_exp_f32 v160, v160\n\t" \
    "v_exp_f32 v161, v161\n\t" \
    "v_exp_f32 v162, v162\n\t" \
    "v_exp_f32 v163, v163\n\t" \
    "v_exp_f32 v164, v164\n\t" \
    "v_exp_f32 v165, v165\n\t" \
    "v_exp_f32 v166, v166\n\t" \
    "v_exp_f32 v167, v167\n\t" \
    "v_add_f32 v160, 1.0, v160\n\t" \
    "v_add_f32 v161, 1.0, v161\n\t" \
    "v_add_f32 v162, 1.0, v162\n\t" \
    "v_add_f32 v163, 1.0, v163\n\t" \
    "v_add_f32 v164, 1.0, v164\n\t" \
    "v_add_f32 v165, 1.0, v165\n\t" \
    "v_add_f32 v166, 1.0, v166\n\t" \
    "v_add_f32 v167, 1.0, v167\n\t" \
    "v_rcp_f32 v160, v160\n\t" \
    "v_rcp_f32 v161, v161\n\t" \
    "v_rcp_f32 v162, v162\n\t" \
    "v_rcp_f32 v163, v163\n\t" \
    "v_rcp_f32 v164, v164\n\t" \
    "v_rcp_f32 v165, v165\n\t" \
    "v_rcp_f32 v166, v166\n\t" \
    "v_rcp_f32 v167, v167\n\t" \
    "v_mul_f32 v188, v188, v160\n\t" \
    "v_mul_f32 v189, v189, v161\n\t" \
    "v_mul_f32 v190, v190, v162\n\t" \
    "v_mul_f32 v191, v191, v163\n\t" \
    "v_mul_f32 v192, v192, v164\n\t" \
    "v_mul_f32 v193, v193, v165\n\t" \
    "v_mul_f32 v194, v194, v166\n\t" \
    "v_mul_f32 v195, v195, v167\n\t" \
    "v_cvt_pk_bf16_f32 v188, v188, v189\n\t" \
    "v_cvt_pk_bf16_f32 v189, v190, v191\n\t" \
    "v_cvt_pk_bf16_f32 v190, v192, v193\n\t" \
    "v_cvt_pk_bf16_f32 v191, v194, v195\n\t" \
    "v_min_u32 v188, -2, v188\n\t" \
    "v_min_u32 v189, -2, v189\n\t" \
    "v_min_u32 v190, -2, v190\n\t" \
    "v_min_u32 v191, -2, v191\n\t" \
    "v_mov_b32 v216, 0x3f803f80\n\t" \
    "v_mov_b32 v217, 0x3f803f80\n\t" \
    "v_mov_b32 v218, 0x3f803f80\n\t" \
    "v_mov_b32 v219, 0x3f803f80\n\t" \
    "v_permlane16_swap_b32 v188, v190\n\t" \
    "v_permlane16_swap_b32 v189, v191\n\t" \
    "s_waitcnt vmcnt(6)\n\t" \
    "buffer_store_dwordx4 v[188:191], %[oa], %[rs], %[so] offen sc1\n\t" \
    "s_nop 1\n\t" \
    "s_setprio 0\n\t"
#define ASM_WT1_HI \
    "s_setprio 1\n\t" \
    "s_barrier\n\t" ASM_ST \
    "ds_read_b128 v[160:163], %[la] offset:0\n\t" \
    "ds_read_b128 v[164:167], %[la] offset:1024\n\t" \
    "ds_read_b128 v[168:171], %[la] offset:2048\n\t" \
    "ds_read2st64_b32 v[204:205], %[sr] offset0:20 offset1:24\n\t" \
    "ds_read_b128 v[172:175], %[la] offset:3072\n\t" \
    "ds_read_b128 v[176:179], %[la] offset:8192\n\t" \
    "ds_read_b128 v[180:183], %[la] offset:9216\n\t" \
    "ds_read_b128 v[184:187], %[la] offset:10240\n\t" \
    "s_waitcnt lgkmcnt(7)\n\t" \
    "v_mfma_f32_16x16x32_bf16 v[192:195], v[80:83], v[160:163], 0\n\t" \
    "s_waitcnt lgkmcnt(6)\n\t" \
    "v_mfma_f32_16x16x32_bf16 v[192:195], v[84:87], v[164:167], v[192:195]\n\t" \
    "v_mfma_f32_16x16x32_bf16 v[188:191], v[0:3], v[160:163], 0\n\t" \
    "ds_read_b128 v[160:163], %[la] offset:11264\n\t" \
    "v_mfma_f32_16x16x32_bf16 v[188:191], v[4:7], v[164:167], v[188:191]\n\t" \
    "ds_read_b128 v[164:167], %[la] offset:16384\n\t" \
    "s_waitcnt lgkmcnt(7)\n\t" \
    "v_mfma_f32_16x16x32_bf16 v[188:191], v[8:11], v[168:171], v[188:191]\n\t" \
    "s_waitcnt lgkmcnt(6)\n\t" \
    "s_waitcnt lgkmcnt(5)\n\t" \
    "v_mfma_f32_16x16x32_bf16 v[188:191], v[12:15], v[172:175], v[188:191]\n\t" \
    "v_permlane16_swap_b32 v204, v205\n\t" \
    "v_mfma_f32_16x16x32_bf16 v[192:195], v[88:91], v[168:171], v[192:195]\n\t" \
    "ds_read_b128 v[168:171], %[la] offset:17408\n\t" \
    "v_mfma_f32_16x16x32_bf16 v[192:195], v[92:95], v[172:175], v[192:195]\n\t" \
    "ds_read_b128 v[172:175], %[la] offset:18432\n\t" \
    "v_add_f32 v206, v204, v205\n\t" \
    "s_waitcnt lgkmcnt(6)\n\t" \
    "v_mfma_f32_16x16x32_bf16 v[192:195], v[96:99], v[176:179], v[192:195]\n\t" \
    "v_add_f32 v207, v204, v205\n\t" \
    "s_waitcnt lgkmcnt(5)\n\t" \
    "v_mfma_f32_16x16x32_bf16 v[192:195], v[100:103], v[180:183], v[192:195]\n\t" \
    "v_mfma_f32_16x16x32_bf16 v[188:191], v[16:19], v[176:179], v[188:191]\n\t" \
    "ds_read_b128 v[176:179], %[la] offset:19456\n\t" \
    "v_permlane32_swap_b32 v206, v207\n\t" \
    "v_mfma_f32_16x16x32_bf16 v[188:191], v[20:23], v[180:183], v[188:191]\n\t" \
    "ds_read_b128 v[180:183], %[la] offset:24576\n\t" \
    "s_waitcnt lgkmcnt(6)\n\t" \
    "v_mfma_f32_16x16x32_bf16 v[188:191], v[24:27], v[184:187], v[188:191]\n\t" \
    "v_add_f32 v204, v206, v207\n\t" \
    "s_waitcnt lgkmcnt(5)\n\t" \
    "v_mfma_f32_16x16x32_bf16 v[188:191], v[28:31], v[160:163], v[188:191]\n\t" \
    "v_add_f32 v205, v206, v207\n\t" \
    "v_mfma_f32_16x16x32_bf16 v[192:195], v[104:107], v[184:187], v[192:195]\n\t" \
    "ds_read_b128 v[184:187], %[la] offset:25600\n\t" \
    "v_mfma_f32_16x16x32_bf16 v[192:195], v[108:111], v[160:163], v[192:195]\n\t" \
    "ds_read_b128 v[160:163], %[la] offset:26624\n\t" \
    "v_permlane16_swap_b32 v204, v205\n\t" \
    "s_waitcnt lgkmcnt(6)\n\t" \
    "v_mfma_f32_16x16x32_bf16 v[192:195], v[112:115], v[164:167], v[192:195]\n\t" \
    "s_waitcnt lgkmcnt(5)\n\t" \
    "v_mfma_f32_16x16x32_bf16 v[192:195], v[116:119], v[168:171], v[192:195]\n\t" \
    "v_mul_f32 v205, %[kd], v205\n\t" \
    "v_mfma_f32_16x16x32_bf16 v[188:191], v[32:35], v[164:167], v[188:191]\n\t" \
    "ds_read_b128 v[164:167], %[la] offset:27648\n\t" \
    "v_mul_f32 v204, %[kd], v204\n\t" \
    "v_mfma_f32_16x16x32_bf16 v[188:191], v[36:39], v[168:171], v[188:191]\n\t" \
    "ds_read_b128 v[168:171], %[la] offset:4096\n\t" \
    "v_fma_f32 v204, -v205, v205, v204\n\t" \
    "s_waitcnt lgkmcnt(6)\n\t" \
    "v_mfma_f32_16x16x32_bf16 v[188:191], v[40:43], v[172:175], v[188:191]\n\t" \
    "v_max_f32 v204, 0, v204\n\t" \
    "s_waitcnt lgkmcnt(5)\n\t" \
    "v_mfma_f32_16x16x32_bf16 v[188:191], v[44:47], v[176:179], v[188:191]\n\t" \
    "v_add_f32 v206, %[ke], v204\n\t" \
    "v_mfma_f32_16x16x32_bf16 v[192:195], v[120:123], v[172:175], v[192:195]\n\t" \
    "ds_read_b128 v[172:175], %[la] offset:12288\n\t" \
    "v_rsq_f32 v206, v206\n\t" \
    "v_mfma_f32_16x16x32_bf16 v[192:195], v[124:127], v[176:179], v[192:195]\n\t" \
    "ds_read_b128 v[176:179], %[la] offset:20480\n\t" \
    "s_waitcnt lgkmcnt(6)\n\t" \
    "v_mfma_f32_16x16x32_bf16 v[192:195], v[128:131], v[180:183], v[192:195]\n\t" \
    "v_mul_f32 v207, v204, v206\n\t" \
    "s_waitcnt lgkmcnt(5)\n\t" \
    "v_mfma_f32_16x16x32_bf16 v[192:195], v[132:135], v[184:187], v[192:195]\n\t" \
    "v_fma_f32 v207, v207, v206, %[ke]\n\t" \
    "v_mfma_f32_16x16x32_bf16 v[188:191], v[48:51], v[180:183], v[188:191]\n\t" \
    "ds_read_b128 v[180:183], %[la] offset:28672\n\t" \
    "v_rsq_f32 v207, v207\n\t" \
    "v_mfma_f32_16x16x32_bf16 v[188:191], v[52:55], v[184:187], v[188:191]\n\t" \
    "v_mul_f32 v206, v206, v207\n\t" \
    "v_mul_f32 v207, -v205, v206\n\t" \
    "s_waitcnt lgkmcnt(5)\n\t" \
    "v_mfma_f32_16x16x32_bf16 v[188:191], v[56:59], v[160:163], v[188:191]\n\t" \
    "v_mul_f32 v208, v196, v207\n\t" \
    "v_mul_f32 v209, v197, v207\n\t" \
    "s_waitcnt lgkmcnt(4)\n\t" \
    "v_mfma_f32_16x16x32_bf16 v[188:191], v[60:63], v[164:167], v[188:191]\n\t" \
    "v_mul_f32 v210, v198, v207\n\t" \
    "v_mul_f32 v211, v199, v207\n\t" \
    "v_mfma_f32_16x16x32_bf16 v[192:195], v[136:139], v[160:163], v[192:195]\n\t" \
    "v_mul_f32 v212, v200, v207\n\t" \
    "v_mul_f32 v213, v201, v207\n\t" \
    "v_mfma_f32_16x16x32_bf16 v[192:195], v[140:143], v[164:167], v[192:195]\n\t" \
    "v_mul_f32 v214, v202, v207\n\t" \
    "v_mul_f32 v215, v203, v207\n\t" \
    "s_waitcnt lgkmcnt(3)\n\t" \
    "v_mfma_f32_16x16x32_bf16 v[192:195], v[144:147], v[168:171], v[192:195]\n\t" \
    "v_mov_b64 v[216:217], -1\n\t" \
    "v_mov_b64 v[218:219], -1\n\t" \
    "s_waitcnt lgkmcnt(2)\n\t" \
    "v_mfma_f32_16x16x32_bf16 v[192:195], v[148:151], v[172:175], v[192:195]\n\t" \
    "buffer_store_dwordx4 v[216:219], %[ob], %[rs], %[sa] offen sc1\n\t" \
    "v_mfma_f32_16x16x32_bf16 v[188:191], v[64:67], v[168:171], v[188:191]\n\t" \
    "v_mfma_f32_16x16x32_bf16 v[188:191], v[68:71], v[172:175], v[188:191]\n\t" \
    "s_waitcnt lgkmcnt(1)\n\t" \
    "v_mfma_f32_16x16x32_bf16 v[188:191], v[72:75], v[176:179], v[188:191]\n\t" \
    "s_waitcnt lgkmcnt(0)\n\t" \
    "v_mfma_f32_16x16x32_bf16 v[188:191], v[76:79], v[180:183], v[188:191]\n\t" \
    "v_mfma_f32_16x16x32_bf16 v[192:195], v[152:155], v[176:179], v[192:195]\n\t" \
    "v_mfma_f32_16x16x32_bf16 v[192:195], v[156:159], v[180:183], v[192:195]\n\t" \
    ASM_ST3 \
    "s_nop 1\n\t" \
    "v_fma_f32 v188, v188, v206, v208\n\t" \
    "v_fma_f32 v189, v189, v206, v209\n\t" \
    "v_fma_f32 v190, v190, v206, v210\n\t" \
    "v_fma_f32 v191, v191, v206, v211\n\t" \
    "v_mul_f32 v160, v188, v188\n\t" \
    "v_mul_f32 v161, v189, v189\n\t" \
    "v_mul_f32 v162, v190, v190\n\t" \
    "v_mul_f32 v163, v191, v191\n\t" \
    "v_fma_f32 v160, v160, %[k1], v220\n\t" \
    "v_fma_f32 v161, v161, %[k1], v220\n\t" \
    "v_fma_f32 v162, v162, %[k1], v220\n\t" \
    "v_fma_f32 v163, v163, %[k1], v220\n\t" \
    "v_mul_f32 v160, v188, v160\n\t" \
    "v_mul_f32 v161, v189, v161\n\t" \
    "v_mul_f32 v162, v190, v162\n\t" \
    "v_mul_f32 v163, v191, v163\n\t" \
    "v_fma_f32 v192, v192, v206, v212\n\t" \
    "v_fma_f32 v193, v193, v206, v213\n\t" \
    "v_fma_f32 v194, v194, v206, v214\n\t" \
    "v_fma_f32 v195, v195, v206, v215\n\t" \
    "v_mul_f32 v164, v192, v192\n\t" \
    "v_mul_f32 v165, v193, v193\n\t" \
    "v_mul_f32 v166, v194, v194\n\t" \
    "v_mul_f32 v167, v195, v195\n\t" \
    "v_fma_f32 v164, v164, %[k1], v220\n\t" \
    "v_fma_f32 v165, v165, %[k1], v220\n\t" \
    "v_fma_f32 v166, v166, %[k1], v220\n\t" \
    "v_fma_f32 v167, v167, %[k1], v220\n\t" \
    "v_mul_f32 v164, v192, v164\n\t" \
    "v_mul_f32 v165, v193, v165\n\t" \
    "v_mul_f32 v166, v194, v166\n\t" \
    "v_mul_f32 v167, v195, v167\n\t" \
    "v_exp_f32 v160, v160\n\t" \
    "v_exp_f32 v161, v161\n\t" \
    "v_exp_f32 v162, v162\n\t" \
    "v_exp_f32 v163, v163\n\t" \
    "v_exp_f32 v164, v164\n\t" \
    "v_exp_f32 v165, v165\n\t" \
    "v_exp_f32 v166, v166\n\t" \
    "v_exp_f32 v167, v167\n\t" \
    "v_add_f32 v160, 1.0, v160\n\t" \
    "v_add_f32 v161, 1.0, v161\n\t" \
    "v_add_f32 v162, 1.0, v162\n\t" \
    "v_add_f32 v163, 1.0, v163\n\t" \
    "v_add_f32 v164, 1.0, v164\n\t" \
    "v_add_f32 v165, 1.0, v165\n\t" \
    "v_add_f32 v166, 1.0, v166\n\t" \
    "v_add_f32 v167, 1.0, v167\n\t" \
    "v_rcp_f32 v160, v160\n\t" \
    "v_rcp_f32 v161, v161\n\t" \
    "v_rcp_f32 v162, v162\n\t" \
    "v_rcp_f32 v163, v163\n\t" \
    "v_rcp_f32 v164, v164\n\t" \
    "v_rcp_f32 v165, v165\n\t" \
    "v_rcp_f32 v166, v166\n\t" \
    "v_rcp_f32 v167, v167\n\t" \
    "v_mul_f32 v188, v188, v160\n\t" \
    "v_mul_f32 v189, v189, v161\n\t" \
    "v_mul_f32 v190, v190, v162\n\t" \
    "v_mul_f32 v191, v191, v163\n\t" \
    "v_mul_f32 v192, v192, v164\n\t" \
    "v_mul_f32 v193, v193, v165\n\t" \
    "v_mul_f32 v194, v194, v166\n\t" \
    "v_mul_f32 v195, v195, v167\n\t" \
    "v_cvt_pk_bf16_f32 v188, v188, v189\n\t" \
    "v_cvt_pk_bf16_f32 v189, v190, v191\n\t" \
    "v_cvt_pk_bf16_f32 v190, v192, v193\n\t" \
    "v_cvt_pk_bf16_f32 v191, v194, v195\n\t" \
    "v_min_u32 v188, -2, v188\n\t" \
    "v_min_u32 v189, -2, v189\n\t" \
    "v_min_u32 v190, -2, v190\n\t" \
    "v_min_u32 v191, -2, v191\n\t" \
    "s_nop 1\n\t" \
    "v_permlane16_swap_b32 v188, v190\n\t" \
    "v_permlane16_swap_b32 v189, v191\n\t" \
    "s_waitcnt vmcnt(1)\n\t" \
    "buffer_store_dwordx4 v[188:191], %[oa], %[rs], %[so] offen sc1\n\t" \
    "s_nop 1\n\t"
#define ASM_WT2_LO \
    "s_branch .Lt0%=\n\t" \
    ".Ltr%=:\n\t" \
    "s_cmp_gt_i32 %[n], 0\n\t" \
    "s_cbranch_scc0 .Ltg%=\n\t" \
    ".Lfw%=:\n\t" \
    "s_mov_b32 %[a], 0\n\t" \
    "v_max_u32 v252, v232, v233\n\t" \
    "v_max3_u32 v252, v234, v235, v252\n\t" \
    "v_cmp_eq_u32 vcc, -1, v252\n\t" \
    "s_cbranch_vccz .Lgw0_%=\n\t" \
    "buffer_load_dwordx4 v[232:235], %[l16], %[rs], %[i0] offen offset:0 sc1\n\t" \
    "s_mov_b32 %[a], 1\n\t" \
    ".Lgw0_%=:\n\t" \
    "v_max_u32 v252, v236, v237\n\t" \
    "v_max3_u32 v252, v238, v239, v252\n\t" \
    "v_cmp_eq_u32 vcc, -1, v252\n\t" \
    "s_cbranch_vccz .Lgw1_%=\n\t" \
    "buffer_load_dwordx4 v[236:239], %[l16], %[rs], %[i0] offen offset:1024 sc1\n\t" \
    "s_mov_b32 %[a], 1\n\t" \
    ".Lgw1_%=:\n\t" \
    "v_max_u32 v252, v240, v241\n\t" \
    "v_max3_u32 v252, v242, v243, v252\n\t" \
    "v_cmp_eq_u32 vcc, -1, v252\n\t" \
    "s_cbranch_vccz .Lgw2_%=\n\t" \
    "buffer_load_dwordx4 v[240:243], %[l16], %[rs], %[i0] offen offset:2048 sc1\n\t" \
    "s_mov_b32 %[a], 1\n\t" \
    ".Lgw2_%=:\n\t" \
    "v_max_u32 v252, v244, v245\n\t" \
    "v_max3_u32 v252, v246, v247, v252\n\t" \
    "v_cmp_eq_u32 vcc, -1, v252\n\t" \
    "s_cbranch_vccz .Lgw3_%=\n\t" \
    "buffer_load_dwordx4 v[244:247], %[l16], %[rs], %[i0] offen offset:3072 sc1\n\t" \
    "s_mov_b32 %[a], 1\n\t" \
    ".Lgw3_%=:\n\t" \
    "v_max_u32 v252, v248, v249\n\t" \
    "v_max3_u32 v252, v250, v251, v252\n\t" \
    "v_cmp_eq_u32 vcc, -1, v252\n\t" \
    "s_cbranch_vccz .Lgw4_%=\n\t" \
    "buffer_load_dwordx4 v[248:251], %[l16], %[rs], %[i1] offen offset:0 sc1\n\t" \
    "s_mov_b32 %[a], 1\n\t" \
    ".Lgw4_%=:\n\t" \
    "s_cmp_eq_u32 %[a], 0\n\t" \
    "s_cbranch_scc1 .Lhw%=\n\t" \
    "s_waitcnt vmcnt(20)\n\t" \
    ".Liw%=:\n\t" \
    "v_max_u32 v252, v232, v233\n\t" \
    "v_max3_u32 v252, v234, v235, v252\n\t" \
    "v_max3_u32 v252, v236, v237, v252\n\t" \
    "v_max3_u32 v252, v238, v239, v252\n\t" \
    "v_max3_u32 v252, v240, v241, v252\n\t" \
    "v_max3_u32 v252, v242, v243, v252\n\t" \
    "v_max3_u32 v252, v244, v245, v252\n\t" \
    "v_max3_u32 v252, v246, v247, v252\n\t" \
    "v_max3_u32 v252, v248, v249, v252\n\t" \
    "v_max3_u32 v252, v250, v251, v252\n\t" \
    "v_cmp_eq_u32 vcc, -1, v252\n\t" \
    "s_cbranch_vccz .Lhw%=\n\t" \
    "s_sleep 1\n\t" \
    "v_max_u32 v252, v232, v233\n\t" \
    "v_max3_u32 v252, v234, v235, v252\n\t" \
    "v_max3_u32 v252, v236, v237, v252\n\t" \
    "v_max3_u32 v252, v238, v239, v252\n\t" \
    "v_max3_u32 v252, v240, v241, v252\n\t" \
    "v_max3_u32 v252, v242, v243, v252\n\t" \
    "v_max3_u32 v252, v244, v245, v252\n\t" \
    "v_max3_u32 v252, v246, v247, v252\n\t" \
    "v_max3_u32 v252, v248, v249, v252\n\t" \
    "v_max3_u32 v252, v250, v251, v252\n\t" \
    "v_cmp_eq_u32 vcc, -1, v252\n\t" \
    "s_cbranch_vccz .Lhw%=\n\t" \
    "s_sleep 1\n\t" \
    "v_max_u32 v252, v232, v233\n\t" \
    "v_max3_u32 v252, v234, v235, v252\n\t" \
    "v_max3_u32 v252, v236, v237, v252\n\t" \
    "v_max3_u32 v252, v238, v239, v252\n\t" \
    "v_max3_u32 v252, v240, v241, v252\n\t" \
    "v_max3_u32 v252, v242, v243, v252\n\t" \
    "v_max3_u32 v252, v244, v245, v252\n\t" \
    "v_max3_u32 v252, v246, v247, v252\n\t" \
    "v_max3_u32 v252, v248, v249, v252\n\t" \
    "v_max3_u32 v252, v250, v251, v252\n\t" \
    "v_cmp_eq_u32 vcc, -1, v252\n\t" \
    "s_cbranch_vccz .Lhw%=\n\t" \
    "s_sleep 1\n\t" \
    "v_max_u32 v252, v232, v233\n\t" \
    "v_max3_u32 v252, v234, v235, v252\n\t" \
    "v_max3_u32 v252, v236, v237, v252\n\t" \
    "v_max3_u32 v252, v238, v239, v252\n\t" \
    "v_max3_u32 v252, v240, v241, v252\n\t" \
    "v_max3_u32 v252, v242, v243, v252\n\t" \
    "v_max3_u32 v252, v244, v245, v252\n\t" \
    "v_max3_u32 v252, v246, v247, v252\n\t" \
    "v_max3_u32 v252, v248, v249, v252\n\t" \
    "v_max3_u32 v252, v250, v251, v252\n\t" \
    "v_cmp_eq_u32 vcc, -1, v252\n\t" \
    "s_cbranch_vccz .Lhw%=\n\t" \
    "s_sleep 1\n\t" \
    "v_max_u32 v252, v232, v233\n\t" \
    "v_max3_u32 v252, v234, v235, v252\n\t" \
    "v_max3_u32 v252, v236, v237, v252\n\t" \
    "v_max3_u32 v252, v238, v239, v252\n\t" \
    "v_max3_u32 v252, v240, v241, v252\n\t" \
    "v_max3_u32 v252, v242, v243, v252\n\t" \
    "v_max3_u32 v252, v244, v245, v252\n\t" \
    "v_max3_u32 v252, v246, v247, v252\n\t" \
    "v_max3_u32 v252, v248, v249, v252\n\t" \
    "v_max3_u32 v252, v250, v251, v252\n\t" \
    "v_cmp_eq_u32 vcc, -1, v252\n\t" \
    "s_cbranch_vccz .Lhw%=\n\t" \
    "s_sleep 1\n\t" \
    "v_max_u32 v252, v232, v233\n\t" \
    "v_max3_u32 v252, v234, v235, v252\n\t" \
    "v_max3_u32 v252, v236, v237, v252\n\t" \
    "v_max3_u32 v252, v238, v239, v252\n\t" \
    "v_max3_u32 v252, v240, v241, v252\n\t" \
    "v_max3_u32 v252, v242, v243, v252\n\t" \
    "v_max3_u32 v252, v244, v245, v252\n\t" \
    "v_max3_u32 v252, v246, v247, v252\n\t" \
    "v_max3_u32 v252, v248, v249, v252\n\t" \
    "v_max3_u32 v252, v250, v251, v252\n\t" \
    "v_cmp_eq_u32 vcc, -1, v252\n\t" \
    "s_cbranch_vccz .Lhw%=\n\t" \
    "s_sleep 1\n\t" \
    "s_sub_u32 %[n], %[n], 1\n\t" \
    "s_cmp_gt_i32 %[n], 0\n\t" \
    "s_cbranch_scc1 .Lfw%=\n\t" \
    ".Lhw%=:\n\t" \
    "s_branch .Lta%=\n\t" \
    ".Lt0%=:\n\t" \
    "s_waitcnt vmcnt(1)\n\t" \
    ".Lta%=:\n\t" \
    "ds_write_b128 %[lw], v[232:235] offset:0\n\t" \
    "v_max_u32 v252, v232, v233\n\t" \
    "v_max3_u32 v252, v234, v235, v252\n\t" \
    "ds_write_b128 %[lw], v[236:239] offset:1024\n\t" \
    "v_max3_u32 v252, v236, v237, v252\n\t" \
    "v_max3_u32 v252, v238, v239, v252\n\t" \
    "ds_write_b128 %[lw], v[240:243] offset:2048\n\t" \
    "v_max3_u32 v252, v240, v241, v252\n\t" \
    "v_max3_u32 v252, v242, v243, v252\n\t" \
    "ds_write_b128 %[lw], v[244:247] offset:3072\n\t" \
    "v_max3_u32 v252, v244, v245, v252\n\t" \
    "v_max3_u32 v252, v246, v247, v252\n\t" \
    "ds_write_b128 %[lw], v[248:251] offset:4096\n\t" \
    "v_max3_u32 v252, v248, v249, v252\n\t" \
    "v_max3_u32 v252, v250, v251, v252\n\t" \
    "v_cmp_eq_u32 vcc, -1, v252\n\t" \
    "s_cbranch_vccnz .Ltr%=\n\t" \
    ".Ltg%=:\n\t" \
    ASM_ST2 \
    "s_waitcnt lgkmcnt(0)\n\t" \
    "s_setprio 2\n\t" \
    "s_barrier\n\t" ASM_ST \
    "ds_read_b128 v[160:163], %[la] offset:0\n\t" \
    "ds_read_b128 v[164:167], %[la] offset:1024\n\t" \
    "ds_read_b128 v[168:171], %[la] offset:2048\n\t" \
    "ds_read_b128 v[172:175], %[la] offset:3072\n\t" \
    "ds_read_b128 v[176:179], %[la] offset:8192\n\t" \
    "ds_read_b128 v[180:183], %[la] offset:9216\n\t" \
    "ds_read_b128 v[184:187], %[la] offset:10240\n\t" \
    "s_waitcnt lgkmcnt(6)\n\t" \
    "v_mfma_f32_16x16x32_bf16 v[192:195], v[80:83], v[160:163], 0\n\t" \
    "buffer_load_dwordx2 v[196:197], %[ro], %[rs], %[sx] offen sc1\n\t" \
    "s_waitcnt lgkmcnt(5)\n\t" \
    "v_mfma_f32_16x16x32_bf16 v[192:195], v[84:87], v[164:167], v[192:195]\n\t" \
    "buffer_load_dwordx2 v[198:199], %[ro], %[rs], %[sx] offen offset:512 sc1\n\t" \
    "v_mfma_f32_16x16x32_bf16 v[188:191], v[0:3], v[160:163], 0\n\t" \
    "ds_read_b128 v[160:163], %[la] offset:11264\n\t" \
    "v_lshrrev_b32 v204, 1, %[l16]\n\t" \
    "v_mfma_f32_16x16x32_bf16 v[188:191], v[4:7], v[164:167], v[188:191]\n\t" \
    "ds_read_b128 v[164:167], %[la] offset:16384\n\t" \
    "v_and_b32 v204, 0x78, v204\n\t" \
    "s_waitcnt lgkmcnt(6)\n\t" \
    "v_mfma_f32_16x16x32_bf16 v[188:191], v[8:11], v[168:171], v[188:191]\n\t" \
    "buffer_load_dwordx2 v[200:201], v204, %[rs], %[ss] offen sc1\n\t" \
    "s_waitcnt lgkmcnt(5)\n\t" \
    "v_mfma_f32_16x16x32_bf16 v[188:191], v[12:15], v[172:175], v[188:191]\n\t" \
    "v_mov_b64 v[216:217], -1\n\t" \
    "v_mfma_f32_16x16x32_bf16 v[192:195], v[88:91], v[168:171], v[192:195]\n\t" \
    "ds_read_b128 v[168:171], %[la] offset:17408\n\t" \
    "v_mov_b64 v[218:219], -1\n\t" \
    "v_mfma_f32_16x16x32_bf16 v[192:195], v[92:95], v[172:175], v[192:195]\n\t" \
    "ds_read_b128 v[172:175], %[la] offset:18432\n\t" \
    "buffer_store_dwordx4 v[216:219], %[l16], %[rs], %[sa] offen sc1\n\t" \
    "s_waitcnt lgkmcnt(6)\n\t" \
    "v_mfma_f32_16x16x32_bf16 v[192:195], v[96:99], v[176:179], v[192:195]\n\t" \
    "s_waitcnt lgkmcnt(5)\n\t" \
    "v_mfma_f32_16x16x32_bf16 v[192:195], v[100:103], v[180:183], v[192:195]\n\t" \
    "v_mfma_f32_16x16x32_bf16 v[188:191], v[16:19], v[176:179], v[188:191]\n\t" \
    "ds_read_b128 v[176:179], %[la] offset:19456\n\t" \
    "v_mfma_f32_16x16x32_bf16 v[188:191], v[20:23], v[180:183], v[188:191]\n\t" \
    "ds_read_b128 v[180:183], %[la] offset:24576\n\t" \
    "s_waitcnt lgkmcnt(6)\n\t" \
    "v_mfma_f32_16x16x32_bf16 v[188:191], v[24:27], v[184:187], v[188:191]\n\t" \
    "s_waitcnt lgkmcnt(5)\n\t" \
    "v_mfma_f32_16x16x32_bf16 v[188:191], v[28:31], v[160:163], v[188:191]\n\t" \
    "v_mfma_f32_16x16x32_bf16 v[192:195], v[104:107], v[184:187], v[192:195]\n\t" \
    "ds_read_b128 v[184:187], %[la] offset:25600\n\t" \
    "v_mfma_f32_16x16x32_bf16 v[192:195], v[108:111], v[160:163], v[192:195]\n\t" \
    "ds_read_b128 v[160:163], %[la] offset:26624\n\t" \
    "s_waitcnt lgkmcnt(6)\n\t" \
    "v_mfma_f32_16x16x32_bf16 v[192:195], v[112:115], v[164:167], v[192:195]\n\t" \
    "s_waitcnt lgkmcnt(5)\n\t" \
    "v_mfma_f32_16x16x32_bf16 v[192:195], v[116:119], v[168:171], v[192:195]\n\t" \
    "v_mfma_f32_16x16x32_bf16 v[188:191], v[32:35], v[164:167], v[188:191]\n\t" \
    "ds_read_b128 v[164:167], %[la] offset:27648\n\t" \
    "v_mfma_f32_16x16x32_bf16 v[188:191], v[36:39], v[168:171], v[188:191]\n\t" \
    "ds_read_b128 v[168:171], %[la] offset:4096\n\t" \
    "s_waitcnt lgkmcnt(6)\n\t" \
    "v_mfma_f32_16x16x32_bf16 v[188:191], v[40:43], v[172:175], v[188:191]\n\t" \
    "s_waitcnt lgkmcnt(5)\n\t" \
    "v_mfma_f32_16x16x32_bf16 v[188:191], v[44:47], v[176:179], v[188:191]\n\t" \
    "v_mfma_f32_16x16x32_bf16 v[192:195], v[120:123], v[172:175], v[192:195]\n\t" \
    "ds_read_b128 v[172:175], %[la] offset:12288\n\t" \
    "v_mfma_f32_16x16x32_bf16 v[192:195], v[124:127], v[176:179], v[192:195]\n\t" \
    "ds_read_b128 v[176:179], %[la] offset:20480\n\t" \
    "s_waitcnt lgkmcnt(6)\n\t" \
    "v_mfma_f32_16x16x32_bf16 v[192:195], v[128:131], v[180:183], v[192:195]\n\t" \
    "s_waitcnt lgkmcnt(5)\n\t" \
    "v_mfma_f32_16x16x32_bf16 v[192:195], v[132:135], v[184:187], v[192:195]\n\t" \
    "v_mfma_f32_16x16x32_bf16 v[188:191], v[48:51], v[180:183], v[188:191]\n\t" \
    "ds_read_b128 v[180:183], %[la] offset:28672\n\t" \
    "v_mfma_f32_16x16x32_bf16 v[188:191], v[52:55], v[184:187], v[188:191]\n\t" \
    "s_waitcnt lgkmcnt(5)\n\t" \
    "v_mfma_f32_16x16x32_bf16 v[188:191], v[56:59], v[160:163], v[188:191]\n\t" \
    "s_waitcnt lgkmcnt(4)\n\t" \
    "v_mfma_f32_16x16x32_bf16 v[188:191], v[60:63], v[164:167], v[188:191]\n\t" \
    "v_mfma_f32_16x16x32_bf16 v[192:195], v[136:139], v[160:163], v[192:195]\n\t" \
    "v_mfma_f32_16x16x32_bf16 v[192:195], v[140:143], v[164:167], v[192:195]\n\t" \
    "s_waitcnt lgkmcnt(3)\n\t" \
    "v_mfma_f32_16x16x32_bf16 v[192:195], v[144:147], v[168:171], v[192:195]\n\t" \
    "s_waitcnt lgkmcnt(2)\n\t" \
    "v_mfma_f32_16x16x32_bf16 v[192:195], v[148:151], v[172:175], v[192:195]\n\t" \
    "v_mfma_f32_16x16x32_bf16 v[188:191], v[64:67], v[168:171], v[188:191]\n\t" \
    "v_mfma_f32_16x16x32_bf16 v[188:191], v[68:71], v[172:175], v[188:191]\n\t" \
    "s_waitcnt lgkmcnt(1)\n\t" \
    "v_mfma_f32_16x16x32_bf16 v[188:191], v[72:75], v[176:179], v[188:191]\n\t" \
    "s_waitcnt lgkmcnt(0)\n\t" \
    "v_mfma_f32_16x16x32_bf16 v[188:191], v[76:79], v[180:183], v[188:191]\n\t" \
    "v_mfma_f32_16x16x32_bf16 v[192:195], v[152:155], v[176:179], v[192:195]\n\t" \
    "v_mfma_f32_16x16x32_bf16 v[192:195], v[156:159], v[180:183], v[192:195]\n\t" \
    "s_setprio 2\n\t" \
    "buffer_load_dwordx4 v[232:235], %[l16], %[rs], %[n0] offen offset:0 sc1\n\t" \
    "buffer_load_dwordx4 v[236:239], %[l16], %[rs], %[n0] offen offset:1024 sc1\n\t" \
    "buffer_load_dwordx4 v[240:243], %[l16], %[rs], %[n0] offen offset:2048 sc1\n\t" \
    "buffer_load_dwordx4 v[244:247], %[l16], %[rs], %[n0] offen offset:3072 sc1\n\t" \
    "buffer_load_dwordx4 v[248:251], %[l16], %[rs], %[n1] offen offset:0 sc1\n\t" \
    ASM_ST3 \
    "s_waitcnt vmcnt(6)\n\t" \
    "v_lshlrev_b32 v160, 16, v196\n\t" \
    "v_and_b32 v161, 0xffff0000, v196\n\t" \
    "v_lshlrev_b32 v162, 16, v197\n\t" \
    "v_and_b32 v163, 0xffff0000, v197\n\t" \
    "v_lshlrev_b32 v164, 16, v198\n\t" \
    "v_and_b32 v165, 0xffff0000, v198\n\t" \
    "v_lshlrev_b32 v166, 16, v199\n\t" \
    "v_and_b32 v167, 0xffff0000, v199\n\t" \
    "v_sub_f32 v160, v160, v200\n\t" \
    "v_sub_f32 v161, v161, v200\n\t" \
    "v_sub_f32 v162, v162, v200\n\t" \
    "v_sub_f32 v163, v163, v200\n\t" \
    "v_sub_f32 v164, v164, v200\n\t" \
    "v_sub_f32 v165, v165, v200\n\t" \
    "v_sub_f32 v166, v166, v200\n\t" \
    "v_sub_f32 v167, v167, v200\n\t" \
    "v_mul_f32 v160, v160, v201\n\t" \
    "v_mul_f32 v161, v161, v201\n\t" \
    "v_mul_f32 v162, v162, v201\n\t" \
    "v_mul_f32 v163, v163, v201\n\t" \
    "v_mul_f32 v164, v164, v201\n\t" \
    "v_mul_f32 v165, v165, v201\n\t" \
    "v_mul_f32 v166, v166, v201\n\t" \
    "v_mul_f32 v167, v167, v201\n\t" \
    "v_cvt_pk_bf16_f32 v160, v160, v161\n\t" \
    "v_cvt_pk_bf16_f32 v162, v162, v163\n\t" \
    "v_cvt_pk_bf16_f32 v164, v164, v165\n\t" \
    "v_cvt_pk_bf16_f32 v166, v166, v167\n\t" \
    "v_and_b32 v161, 0xffff0000, v160\n\t" \
    "v_lshlrev_b32 v160, 16, v160\n\t" \
    "v_and_b32 v163, 0xffff0000, v162\n\t" \
    "v_lshlrev_b32 v162, 16, v162\n\t" \
    "v_and_b32 v165, 0xffff0000, v164\n\t" \
    "v_lshlrev_b32 v164, 16, v164\n\t" \
    "v_and_b32 v167, 0xffff0000, v166\n\t" \
    "v_lshlrev_b32 v166, 16, v166\n\t" \
    "v_add_f32 v188, v188, v160\n\t" \
    "v_add_f32 v189, v189, v161\n\t" \
    "v_add_f32 v190, v190, v162\n\t" \
    "v_add_f32 v191, v191, v163\n\t" \
    "v_add_f32 v192, v192, v164\n\t" \
    "v_add_f32 v193, v193, v165\n\t" \
    "v_add_f32 v194, v194, v166\n\t" \
    "v_add_f32 v195, v195, v167\n\t" \
    "v_cvt_pk_bf16_f32 v188, v188, v189\n\t" \
    "v_cvt_pk_bf16_f32 v189, v190, v191\n\t" \
    "v_cvt_pk_bf16_f32 v190, v192, v193\n\t" \
    "v_cvt_pk_bf16_f32 v191, v194, v195\n\t" \
    "v_min_u32 v188, -2, v188\n\t" \
    "v_min_u32 v189, -2, v189\n\t" \
    "v_min_u32 v190, -2, v190\n\t" \
    "v_min_u32 v191, -2, v191\n\t" \
    "s_nop 1\n\t" \
    "v_permlane16_swap_b32 v188, v190\n\t" \
    "v_permlane16_swap_b32 v189, v191\n\t" \
    "s_waitcnt vmcnt(6)\n\t" \
    "buffer_store_dwordx4 v[188:191], %[oa], %[rs], %[so] offen sc1\n\t" \
    "s_nop 1\n\t" \
    "s_setprio 0\n\t"
#define ASM_WT2_HI \
    "s_setprio 1\n\t" \
    "s_barrier\n\t" ASM_ST \
    "ds_read_b128 v[160:163], %[la] offset:0\n\t" \
    "ds_read_b128 v[164:167], %[la] offset:1024\n\t" \
    "ds_read_b128 v[168:171], %[la] offset:2048\n\t" \
    "ds_read_b128 v[172:175], %[la] offset:3072\n\t" \
    "ds_read_b128 v[176:179], %[la] offset:8192\n\t" \
    "ds_read_b128 v[180:183], %[la] offset:9216\n\t" \
    "ds_read_b128 v[184:187], %[la] offset:10240\n\t" \
    "s_waitcnt lgkmcnt(6)\n\t" \
    "v_mfma_f32_16x16x32_bf16 v[192:195], v[80:83], v[160:163], 0\n\t" \
    "buffer_load_dwordx2 v[196:197], %[ro], %[rs], %[sx] offen sc1\n\t" \
    "s_waitcnt lgkmcnt(5)\n\t" \
    "v_mfma_f32_16x16x32_bf16 v[192:195], v[84:87], v[164:167], v[192:195]\n\t" \
    "buffer_load_dwordx2 v[198:199], %[ro], %[rs], %[sx] offen offset:512 sc1\n\t" \
    "v_mfma_f32_16x16x32_bf16 v[188:191], v[0:3], v[160:163], 0\n\t" \
    "ds_read_b128 v[160:163], %[la] offset:11264\n\t" \
    "v_lshrrev_b32 v204, 1, %[l16]\n\t" \
    "v_mfma_f32_16x16x32_bf16 v[188:191], v[4:7], v[164:167], v[188:191]\n\t" \
    "ds_read_b128 v[164:167], %[la] offset:16384\n\t" \
    "v_and_b32 v204, 0x78, v204\n\t" \
    "s_waitcnt lgkmcnt(6)\n\t" \
    "v_mfma_f32_16x16x32_bf16 v[188:191], v[8:11], v[168:171], v[188:191]\n\t" \
    "buffer_load_dwordx2 v[200:201], v204, %[rs], %[ss] offen sc1\n\t" \
    "s_waitcnt lgkmcnt(5)\n\t" \
    "v_mfma_f32_16x16x32_bf16 v[188:191], v[12:15], v[172:175], v[188:191]\n\t" \
    "v_mov_b64 v[216:217], -1\n\t" \
    "v_mfma_f32_16x16x32_bf16 v[192:195], v[88:91], v[168:171], v[192:195]\n\t" \
    "ds_read_b128 v[168:171], %[la] offset:17408\n\t" \
    "v_mov_b64 v[218:219], -1\n\t" \
    "v_mfma_f32_16x16x32_bf16 v[192:195], v[92:95], v[172:175], v[192:195]\n\t" \
    "ds_read_b128 v[172:175], %[la] offset:18432\n\t" \
    "buffer_store_dwordx4 v[216:219], %[l16], %[rs], %[sa] offen sc1\n\t" \
    "s_waitcnt lgkmcnt(6)\n\t" \
    "v_mfma_f32_16x16x32_bf16 v[192:195], v[96:99], v[176:179], v[192:195]\n\t" \
    "s_waitcnt lgkmcnt(5)\n\t" \
    "v_mfma_f32_16x16x32_bf16 v[192:195], v[100:103], v[180:183], v[192:195]\n\t" \
    "v_mfma_f32_16x16x32_bf16 v[188:191], v[16:19], v[176:179], v[188:191]\n\t" \
    "ds_read_b128 v[176:179], %[la] offset:19456\n\t" \
    "v_mfma_f32_16x16x32_bf16 v[188:191], v[20:23], v[180:183], v[188:191]\n\t" \
    "ds_read_b128 v[180:183], %[la] offset:24576\n\t" \
    "s_waitcnt lgkmcnt(6)\n\t" \
    "v_mfma_f32_16x16x32_bf16 v[188:191], v[24:27], v[184:187], v[188:191]\n\t" \
    "s_waitcnt lgkmcnt(5)\n\t" \
    "v_mfma_f32_16x16x32_bf16 v[188:191], v[28:31], v[160:163], v[188:191]\n\t" \
    "v_mfma_f32_16x16x32_bf16 v[192:195], v[104:107], v[184:187], v[192:195]\n\t" \
    "ds_read_b128 v[184:187], %[la] offset:25600\n\t" \
    "v_mfma_f32_16x16x32_bf16 v[192:195], v[108:111], v[160:163], v[192:195]\n\t" \
    "ds_read_b128 v[160:163], %[la] offset:26624\n\t" \
    "s_waitcnt lgkmcnt(6)\n\t" \
    "v_mfma_f32_16x16x32_bf16 v[192:195], v[112:115], v[164:167], v[192:195]\n\t" \
    "s_waitcnt lgkmcnt(5)\n\t" \
    "v_mfma_f32_16x16x32_bf16 v[192:195], v[116:119], v[168:171], v[192:195]\n\t" \
    "v_mfma_f32_16x16x32_bf16 v[188:191], v[32:35], v[164:167], v[188:191]\n\t" \
    "ds_read_b128 v[164:167], %[la] offset:27648\n\t" \
    "v_mfma_f32_16x16x32_bf16 v[188:191], v[36:39], v[168:171], v[188:191]\n\t" \
    "ds_read_b128 v[168:171], %[la] offset:4096\n\t" \
    "s_waitcnt lgkmcnt(6)\n\t" \
    "v_mfma_f32_16x16x32_bf16 v[188:191], v[40:43], v[172:175], v[188:191]\n\t" \
    "s_waitcnt lgkmcnt(5)\n\t" \
    "v_mfma_f32_16x16x32_bf16 v[188:191], v[44:47], v[176:179], v[188:191]\n\t" \
    "v_mfma_f32_16x16x32_bf16 v[192:195], v[120:123], v[172:175], v[192:195]\n\t" \
    "ds_read_b128 v[172:175], %[la] offset:12288\n\t" \
    "v_mfma_f32_16x16x32_bf16 v[192:195], v[124:127], v[176:179], v[192:195]\n\t" \
    "ds_read_b128 v[176:179], %[la] offset:20480\n\t" \
    "s_waitcnt lgkmcnt(6)\n\t" \
    "v_mfma_f32_16x16x32_bf16 v[192:195], v[128:131], v[180:183], v[192:195]\n\t" \
    "s_waitcnt lgkmcnt(5)\n\t" \
    "v_mfma_f32_16x16x32_bf16 v[192:195], v[132:135], v[184:187], v[192:195]\n\t" \
    "v_mfma_f32_16x16x32_bf16 v[188:191], v[48:51], v[180:183], v[188:191]\n\t" \
    "ds_read_b128 v[180:183], %[la] offset:28672\n\t" \
    "v_mfma_f32_16x16x32_bf16 v[188:191], v[52:55], v[184:187], v[188:191]\n\t" \
    "s_waitcnt lgkmcnt(5)\n\t" \
    "v_mfma_f32_16x16x32_bf16 v[188:191], v[56:59], v[160:163], v[188:191]\n\t" \
    "s_waitcnt lgkmcnt(4)\n\t" \
    "v_mfma_f32_16x16x32_bf16 v[188:191], v[60:63], v[164:167], v[188:191]\n\t" \
    "v_mfma_f32_16x16x32_bf16 v[192:195], v[136:139], v[160:163], v[192:195]\n\t" \
    "v_mfma_f32_16x16x32_bf16 v[192:195], v[140:143], v[164:167], v[192:195]\n\t" \
    "s_waitcnt lgkmcnt(3)\n\t" \
    "v_mfma_f32_16x16x32_bf16 v[192:195], v[144:147], v[168:171], v[192:195]\n\t" \
    "s_waitcnt lgkmcnt(2)\n\t" \
    "v_mfma_f32_16x16x32_bf16 v[192:195], v[148:151], v[172:175], v[192:195]\n\t" \
    "v_mfma_f32_16x16x32_bf16 v[188:191], v[64:67], v[168:171], v[188:191]\n\t" \
    "v_mfma_f32_16x16x32_bf16 v[188:191], v[68:71], v[172:175], v[188:191]\n\t" \
    "s_waitcnt lgkmcnt(1)\n\t" \
    "v_mfma_f32_16x16x32_bf16 v[188:191], v[72:75], v[176:179], v[188:191]\n\t" \
    "s_waitcnt lgkmcnt(0)\n\t" \
    "v_mfma_f32_16x16x32_bf16 v[188:191], v[76:79], v[180:183], v[188:191]\n\t" \
    "v_mfma_f32_16x16x32_bf16 v[192:195], v[152:155], v[176:179], v[192:195]\n\t" \
    "v_mfma_f32_16x16x32_bf16 v[192:195], v[156:159], v[180:183], v[192:195]\n\t" \
    ASM_ST3 \
    "s_waitcnt vmcnt(1)\n\t" \
    "v_lshlrev_b32 v160, 16, v196\n\t" \
    "v_and_b32 v161, 0xffff0000, v196\n\t" \
    "v_lshlrev_b32 v162, 16, v197\n\t" \
    "v_and_b32 v163, 0xffff0000, v197\n\t" \
    "v_lshlrev_b32 v164, 16, v198\n\t" \
    "v_and_b32 v165, 0xffff0000, v198\n\t" \
    "v_lshlrev_b32 v166, 16, v199\n\t" \
    "v_and_b32 v167, 0xffff0000, v199\n\t" \
    "v_sub_f32 v160, v160, v200\n\t" \
    "v_sub_f32 v161, v161, v200\n\t" \
    "v_sub_f32 v162, v162, v200\n\t" \
    "v_sub_f32 v163, v163, v200\n\t" \
    "v_sub_f32 v164, v164, v200\n\t" \
    "v_sub_f32 v165, v165, v200\n\t" \
    "v_sub_f32 v166, v166, v200\n\t" \
    "v_sub_f32 v167, v167, v200\n\t" \
    "v_mul_f32 v160, v160, v201\n\t" \
    "v_mul_f32 v161, v161, v201\n\t" \
    "v_mul_f32 v162, v162, v201\n\t" \
    "v_mul_f32 v163, v163, v201\n\t" \
    "v_mul_f32 v164, v164, v201\n\t" \
    "v_mul_f32 v165, v165, v201\n\t" \
    "v_mul_f32 v166, v166, v201\n\t" \
    "v_mul_f32 v167, v167, v201\n\t" \
    "v_cvt_pk_bf16_f32 v160, v160, v161\n\t" \
    "v_cvt_pk_bf16_f32 v162, v162, v163\n\t" \
    "v_cvt_pk_bf16_f32 v164, v164, v165\n\t" \
    "v_cvt_pk_bf16_f32 v166, v166, v167\n\t" \
    "v_and_b32 v161, 0xffff0000, v160\n\t" \
    "v_lshlrev_b32 v160, 16, v160\n\t" \
    "v_and_b32 v163, 0xffff0000, v162\n\t" \
    "v_lshlrev_b32 v162, 16, v162\n\t" \
    "v_and_b32 v165, 0xffff0000, v164\n\t" \
    "v_lshlrev_b32 v164, 16, v164\n\t" \
    "v_and_b32 v167, 0xffff0000, v166\n\t" \
    "v_lshlrev_b32 v166, 16, v166\n\t" \
    "v_add_f32 v188, v188, v160\n\t" \
    "v_add_f32 v189, v189, v161\n\t" \
    "v_add_f32 v190, v190, v162\n\t" \
    "v_add_f32 v191, v191, v163\n\t" \
    "v_add_f32 v192, v192, v164\n\t" \
    "v_add_f32 v193, v193, v165\n\t" \
    "v_add_f32 v194, v194, v166\n\t" \
    "v_add_f32 v195, v195, v167\n\t" \
    "v_cvt_pk_bf16_f32 v188, v188, v189\n\t" \
    "v_cvt_pk_bf16_f32 v189, v190, v191\n\t" \
    "v_cvt_pk_bf16_f32 v190, v192, v193\n\t" \
    "v_cvt_pk_bf16_f32 v191, v194, v195\n\t" \
    "v_min_u32 v188, -2, v188\n\t" \
    "v_min_u32 v189, -2, v189\n\t" \
    "v_min_u32 v190, -2, v190\n\t" \
    "v_min_u32 v191, -2, v191\n\t" \
    "s_nop 1\n\t" \
    "v_permlane16_swap_b32 v188, v190\n\t" \
    "v_permlane16_swap_b32 v189, v191\n\t" \
    "s_waitcnt vmcnt(1)\n\t" \
    "buffer_store_dwordx4 v[188:191], %[oa], %[rs], %[so] offen sc1\n\t" \
    "s_nop 1\n\t"
#define ASM_WT3_LO \
    "s_branch .Lt0%=\n\t" \
    ".Ltr%=:\n\t" \
    "s_cmp_gt_i32 %[n], 0\n\t" \
    "s_cbranch_scc0 .Ltg%=\n\t" \
    ".Lfw%=:\n\t" \
    "s_mov_b32 %[a], 0\n\t" \
    "v_max_u32 v252, v232, v233\n\t" \
    "v_max3_u32 v252, v234, v235, v252\n\t" \
    "v_cmp_eq_u32 vcc, -1, v252\n\t" \
    "s_cbranch_vccz .Lgw0_%=\n\t" \
    "buffer_load_dwordx4 v[232:235], %[l16], %[rs], %[i0] offen offset:0 sc1\n\t" \
    "s_mov_b32 %[a], 1\n\t" \
    ".Lgw0_%=:\n\t" \
    "v_max_u32 v252, v236, v237\n\t" \
    "v_max3_u32 v252, v238, v239, v252\n\t" \
    "v_cmp_eq_u32 vcc, -1, v252\n\t" \
    "s_cbranch_vccz .Lgw1_%=\n\t" \
    "buffer_load_dwordx4 v[236:239], %[l16], %[rs], %[i0] offen offset:1024 sc1\n\t" \
    "s_mov_b32 %[a], 1\n\t" \
    ".Lgw1_%=:\n\t" \
    "v_max_u32 v252, v240, v241\n\t" \
    "v_max3_u32 v252, v242, v243, v252\n\t" \
    "v_cmp_eq_u32 vcc, -1, v252\n\t" \
    "s_cbranch_vccz .Lgw2_%=\n\t" \
    "buffer_load_dwordx4 v[240:243], %[l16], %[rs], %[i0] offen offset:2048 sc1\n\t" \
    "s_mov_b32 %[a], 1\n\t" \
    ".Lgw2_%=:\n\t" \
    "v_max_u32 v252, v244, v245\n\t" \
    "v_max3_u32 v252, v246, v247, v252\n\t" \
    "v_cmp_eq_u32 vcc, -1, v252\n\t" \
    "s_cbranch_vccz .Lgw3_%=\n\t" \
    "buffer_load_dwordx4 v[244:247], %[l16], %[rs], %[i0] offen offset:3072 sc1\n\t" \
    "s_mov_b32 %[a], 1\n\t" \
    ".Lgw3_%=:\n\t" \
    "v_max_u32 v252, v248, v249\n\t" \
    "v_max3_u32 v252, v250, v251, v252\n\t" \
    "v_cmp_eq_u32 vcc, -1, v252\n\t" \
    "s_cbranch_vccz .Lgw4_%=\n\t" \
    "buffer_load_dwordx4 v[248:251], %[l16], %[rs], %[i1] offen offset:0 sc1\n\t" \
    "s_mov_b32 %[a], 1\n\t" \
    ".Lgw4_%=:\n\t" \
    "s_cmp_eq_u32 %[a], 0\n\t" \
    "s_cbranch_scc1 .Lhw%=\n\t" \
    "s_waitcnt vmcnt(20)\n\t" \
    ".Liw%=:\n\t" \
    "v_max_u32 v252, v232, v233\n\t" \
    "v_max3_u32 v252, v234, v235, v252\n\t" \
    "v_max3_u32 v252, v236, v237, v252\n\t" \
    "v_max3_u32 v252, v238, v239, v252\n\t" \
    "v_max3_u32 v252, v240, v241, v252\n\t" \
    "v_max3_u32 v252, v242, v243, v252\n\t" \
    "v_max3_u32 v252, v244, v245, v252\n\t" \
    "v_max3_u32 v252, v246, v247, v252\n\t" \
    "v_max3_u32 v252, v248, v249, v252\n\t" \
    "v_max3_u32 v252, v250, v251, v252\n\t" \
    "v_cmp_eq_u32 vcc, -1, v252\n\t" \
    "s_cbranch_vccz .Lhw%=\n\t" \
    "s_sleep 1\n\t" \
    "v_max_u32 v252, v232, v233\n\t" \
    "v_max3_u32 v252, v234, v235, v252\n\t" \
    "v_max3_u32 v252, v236, v237, v252\n\t" \
    "v_max3_u32 v252, v238, v239, v252\n\t" \
    "v_max3_u32 v252, v240, v241, v252\n\t" \
    "v_max3_u32 v252, v242, v243, v252\n\t" \
    "v_max3_u32 v252, v244, v245, v252\n\t" \
    "v_max3_u32 v252, v246, v247, v252\n\t" \
    "v_max3_u32 v252, v248, v249, v252\n\t" \
    "v_max3_u32 v252, v250, v251, v252\n\t" \
    "v_cmp_eq_u32 vcc, -1, v252\n\t" \
    "s_cbranch_vccz .Lhw%=\n\t" \
    "s_sleep 1\n\t" \
    "v_max_u32 v252, v232, v233\n\t" \
    "v_max3_u32 v252, v234, v235, v252\n\t" \
    "v_max3_u32 v252, v236, v237, v252\n\t" \
    "v_max3_u32 v252, v238, v239, v252\n\t" \
    "v_max3_u32 v252, v240, v241, v252\n\t" \
    "v_max3_u32 v252, v242, v243, v252\n\t" \
    "v_max3_u32 v252, v244, v245, v252\n\t" \
    "v_max3_u32 v252, v246, v247, v252\n\t" \
    "v_max3_u32 v252, v248, v249, v252\n\t" \
    "v_max3_u32 v252, v250, v251, v252\n\t" \
    "v_cmp_eq_u32 vcc, -1, v252\n\t" \
    "s_cbranch_vccz .Lhw%=\n\t" \
    "s_sleep 1\n\t" \
    "v_max_u32 v252, v232, v233\n\t" \
    "v_max3_u32 v252, v234, v235, v252\n\t" \
    "v_max3_u32 v252, v236, v237, v252\n\t" \
    "v_max3_u32 v252, v238, v239, v252\n\t" \
    "v_max3_u32 v252, v240, v241, v252\n\t" \
    "v_max3_u32 v252, v242, v243, v252\n\t" \
    "v_max3_u32 v252, v244, v245, v252\n\t" \
    "v_max3_u32 v252, v246, v247, v252\n\t" \
    "v_max3_u32 v252, v248, v249, v252\n\t" \
    "v_max3_u32 v252, v250, v251, v252\n\t" \
    "v_cmp_eq_u32 vcc, -1, v252\n\t" \
    "s_cbranch_vccz .Lhw%=\n\t" \
    "s_sleep 1\n\t" \
    "v_max_u32 v252, v232, v233\n\t" \
    "v_max3_u32 v252, v234, v235, v252\n\t" \
    "v_max3_u32 v252, v236, v237, v252\n\t" \
    "v_max3_u32 v252, v238, v239, v252\n\t" \
    "v_max3_u32 v252, v240, v241, v252\n\t" \
    "v_max3_u32 v252, v242, v243, v252\n\t" \
    "v_max3_u32 v252, v244, v245, v252\n\t" \
    "v_max3_u32 v252, v246, v247, v252\n\t" \
    "v_max3_u32 v252, v248, v249, v252\n\t" \
    "v_max3_u32 v252, v250, v251, v252\n\t" \
    "v_cmp_eq_u32 vcc, -1, v252\n\t" \
    "s_cbranch_vccz .Lhw%=\n\t" \
    "s_sleep 1\n\t" \
    "v_max_u32 v252, v232, v233\n\t" \
    "v_max3_u32 v252, v234, v235, v252\n\t" \
    "v_max3_u32 v252, v236, v237, v252\n\t" \
    "v_max3_u32 v252, v238, v239, v252\n\t" \
    "v_max3_u32 v252, v240, v241, v252\n\t" \
    "v_max3_u32 v252, v242, v243, v252\n\t" \
    "v_max3_u32 v252, v244, v245, v252\n\t" \
    "v_max3_u32 v252, v246, v247, v252\n\t" \
    "v_max3_u32 v252, v248, v249, v252\n\t" \
    "v_max3_u32 v252, v250, v251, v252\n\t" \
    "v_cmp_eq_u32 vcc, -1, v252\n\t" \
    "s_cbranch_vccz .Lhw%=\n\t" \
    "s_sleep 1\n\t" \
    "s_sub_u32 %[n], %[n], 1\n\t" \
    "s_cmp_gt_i32 %[n], 0\n\t" \
    "s_cbranch_scc1 .Lfw%=\n\t" \
    ".Lhw%=:\n\t" \
    "s_branch .Lta%=\n\t" \
    ".Lt0%=:\n\t" \
    "s_waitcnt vmcnt(1)\n\t" \
    ".Lta%=:\n\t" \
    "ds_write_b128 %[lw], v[232:235] offset:0\n\t" \
    "v_max_u32 v252, v232, v233\n\t" \
    "v_max3_u32 v252, v234, v235, v252\n\t" \
    "v_mfma_f32_16x16x32_bf16 v[188:191], v[232:235], v[232:235], 0\n\t" \
    "ds_write_b128 %[lw], v[236:239] offset:1024\n\t" \
    "v_max3_u32 v252, v236, v237, v252\n\t" \
    "v_max3_u32 v252, v238, v239, v252\n\t" \
    "v_mfma_f32_16x16x32_bf16 v[188:191], v[236:239], v[236:239], v[188:191]\n\t" \
    "ds_write_b128 %[lw], v[240:243] offset:2048\n\t" \
    "v_max3_u32 v252, v240, v241, v252\n\t" \
    "v_max3_u32 v252, v242, v243, v252\n\t" \
    "v_mfma_f32_16x16x32_bf16 v[188:191], v[240:243], v[240:243], v[188:191]\n\t" \
    "ds_write_b128 %[lw], v[244:247] offset:3072\n\t" \
    "v_max3_u32 v252, v244, v245, v252\n\t" \
    "v_max3_u32 v252, v246, v247, v252\n\t" \
    "v_mfma_f32_16x16x32_bf16 v[188:191], v[244:247], v[244:247], v[188:191]\n\t" \
    "ds_write_b128 %[lw], v[248:251] offset:4096\n\t" \
    "v_max3_u32 v252, v248, v249, v252\n\t" \
    "v_max3_u32 v252, v250, v251, v252\n\t" \
    "v_mfma_f32_16x16x32_bf16 v[188:191], v[248:251], v[248:251], v[188:191]\n\t" \
    "v_mfma_f32_16x16x32_bf16 v[192:195], v[216:219], v[232:235], 0\n\t" \
    "v_mfma_f32_16x16x32_bf16 v[192:195], v[216:219], v[236:239], v[192:195]\n\t" \
    "v_mfma_f32_16x16x32_bf16 v[192:195], v[216:219], v[240:243], v[192:195]\n\t" \
    "v_mfma_f32_16x16x32_bf16 v[192:195], v[216:219], v[244:247], v[192:195]\n\t" \
    "v_mfma_f32_16x16x32_bf16 v[192:195], v[216:219], v[248:251], v[192:195]\n\t" \
    "v_cmp_eq_u32 vcc, -1, v252\n\t" \
    "s_cbranch_vccnz .Ltr%=\n\t" \
    ".Ltg%=:\n\t" \
    ASM_ST2 \
    "s_nop 5\n\t" \
    "ds_write_b128 %[lw], v[188:191] offset:5120\n\t" \
    "ds_write_b128 %[lw], v[192:195] offset:6144\n\t" \
    "s_waitcnt lgkmcnt(0)\n\t" \
    "s_setprio 2\n\t" \
    "s_barrier\n\t" ASM_ST \
    "ds_read_b128 v[160:163], %[la] offset:0\n\t" \
    "ds_read_b128 v[164:167], %[la] offset:1024\n\t" \
    "ds_read_b128 v[168:171], %[la] offset:2048\n\t" \
    "ds_read2st64_b32 v[204:205], %[sr] offset0:20 offset1:24\n\t" \
    "ds_read_b128 v[172:175], %[la] offset:3072\n\t" \
    "ds_read_b128 v[176:179], %[la] offset:8192\n\t" \
    "ds_read_b128 v[180:183], %[la] offset:9216\n\t" \
    "ds_read_b128 v[184:187], %[la] offset:10240\n\t" \
    "s_waitcnt lgkmcnt(7)\n\t" \
    "v_mfma_f32_16x16x32_bf16 v[192:195], v[80:83], v[160:163], 0\n\t" \
    "s_waitcnt lgkmcnt(6)\n\t" \
    "v_mfma_f32_16x16x32_bf16 v[192:195], v[84:87], v[164:167], v[192:195]\n\t" \
    "v_mfma_f32_16x16x32_bf16 v[188:191], v[0:3], v[160:163], 0\n\t" \
    "ds_read_b128 v[160:163], %[la] offset:11264\n\t" \
    "v_mfma_f32_16x16x32_bf16 v[188:191], v[4:7], v[164:167], v[188:191]\n\t" \
    "ds_read_b128 v[164:167], %[la] offset:16384\n\t" \
    "s_waitcnt lgkmcnt(7)\n\t" \
    "v_mfma_f32_16x16x32_bf16 v[188:191], v[8:11], v[168:171], v[188:191]\n\t" \
    "s_waitcnt lgkmcnt(6)\n\t" \
    "s_waitcnt lgkmcnt(5)\n\t" \
    "v_mfma_f32_16x16x32_bf16 v[188:191], v[12:15], v[172:175], v[188:191]\n\t" \
    "v_permlane16_swap_b32 v204, v205\n\t" \
    "v_mfma_f32_16x16x32_bf16 v[192:195], v[88:91], v[168:171], v[192:195]\n\t" \
    "ds_read_b128 v[168:171], %[la] offset:17408\n\t" \
    "v_mfma_f32_16x16x32_bf16 v[192:195], v[92:95], v[172:175], v[192:195]\n\t" \
    "ds_read_b128 v[172:175], %[la] offset:18432\n\t" \
    "v_add_f32 v206, v204, v205\n\t" \
    "s_waitcnt lgkmcnt(6)\n\t" \
    "v_mfma_f32_16x16x32_bf16 v[192:195], v[96:99], v[176:179], v[192:195]\n\t" \
    "v_add_f32 v207, v204, v205\n\t" \
    "s_waitcnt lgkmcnt(5)\n\t" \
    "v_mfma_f32_16x16x32_bf16 v[192:195], v[100:103], v[180:183], v[192:195]\n\t" \
    "v_mfma_f32_16x16x32_bf16 v[188:191], v[16:19], v[176:179], v[188:191]\n\t" \
    "ds_read_b128 v[176:179], %[la] offset:19456\n\t" \
    "v_permlane32_swap_b32 v206, v207\n\t" \
    "v_mfma_f32_16x16x32_bf16 v[188:191], v[20:23], v[180:183], v[188:191]\n\t" \
    "ds_read_b128 v[180:183], %[la] offset:24576\n\t" \
    "s_waitcnt lgkmcnt(6)\n\t" \
    "v_mfma_f32_16x16x32_bf16 v[188:191], v[24:27], v[184:187], v[188:191]\n\t" \
    "v_add_f32 v204, v206, v207\n\t" \
    "s_waitcnt lgkmcnt(5)\n\t" \
    "v_mfma_f32_16x16x32_bf16 v[188:191], v[28:31], v[160:163], v[188:191]\n\t" \
    "v_add_f32 v205, v206, v207\n\t" \
    "v_mfma_f32_16x16x32_bf16 v[192:195], v[104:107], v[184:187], v[192:195]\n\t" \
    "ds_read_b128 v[184:187], %[la] offset:25600\n\t" \
    "v_mfma_f32_16x16x32_bf16 v[192:195], v[108:111], v[160:163], v[192:195]\n\t" \
    "ds_read_b128 v[160:163], %[la] offset:26624\n\t" \
    "v_permlane16_swap_b32 v204, v205\n\t" \
    "s_waitcnt lgkmcnt(6)\n\t" \
    "v_mfma_f32_16x16x32_bf16 v[192:195], v[112:115], v[164:167], v[192:195]\n\t" \
    "s_waitcnt lgkmcnt(5)\n\t" \
    "v_mfma_f32_16x16x32_bf16 v[192:195], v[116:119], v[168:171], v[192:195]\n\t" \
    "v_mul_f32 v205, %[kd], v205\n\t" \
    "v_mfma_f32_16x16x32_bf16 v[188:191], v[32:35], v[164:167], v[188:191]\n\t" \
    "ds_read_b128 v[164:167], %[la] offset:27648\n\t" \
    "v_mul_f32 v204, %[kd], v204\n\t" \
    "v_mfma_f32_16x16x32_bf16 v[188:191], v[36:39], v[168:171], v[188:191]\n\t" \
    "ds_read_b128 v[168:171], %[la] offset:4096\n\t" \
    "v_fma_f32 v204, -v205, v205, v204\n\t" \
    "s_waitcnt lgkmcnt(6)\n\t" \
    "v_mfma_f32_16x16x32_bf16 v[188:191], v[40:43], v[172:175], v[188:191]\n\t" \
    "v_max_f32 v204, 0, v204\n\t" \
    "s_waitcnt lgkmcnt(5)\n\t" \
    "v_mfma_f32_16x16x32_bf16 v[188:191], v[44:47], v[176:179], v[188:191]\n\t" \
    "v_add_f32 v206, %[ke], v204\n\t" \
    "v_mfma_f32_16x16x32_bf16 v[192:195], v[120:123], v[172:175], v[192:195]\n\t" \
    "ds_read_b128 v[172:175], %[la] offset:12288\n\t" \
    "v_rsq_f32 v206, v206\n\t" \
    "v_mfma_f32_16x16x32_bf16 v[192:195], v[124:127], v[176:179], v[192:195]\n\t" \
    "ds_read_b128 v[176:179], %[la] offset:20480\n\t" \
    "s_waitcnt lgkmcnt(6)\n\t" \
    "v_mfma_f32_16x16x32_bf16 v[192:195], v[128:131], v[180:183], v[192:195]\n\t" \
    "v_mul_f32 v207, -v205, v206\n\t" \
    "s_waitcnt lgkmcnt(5)\n\t" \
    "v_mfma_f32_16x16x32_bf16 v[192:195], v[132:135], v[184:187], v[192:195]\n\t" \
    "v_mul_f32 v208, v196, v207\n\t" \
    "v_mfma_f32_16x16x32_bf16 v[188:191], v[48:51], v[180:183], v[188:191]\n\t" \
    "ds_read_b128 v[180:183], %[la] offset:28672\n\t" \
    "v_mul_f32 v209, v197, v207\n\t" \
    "v_mul_f32 v210, v198, v207\n\t" \
    "v_mfma_f32_16x16x32_bf16 v[188:191], v[52:55], v[184:187], v[188:191]\n\t" \
    "v_mul_f32 v211, v199, v207\n\t" \
    "v_mul_f32 v212, v200, v207\n\t" \
    "s_waitcnt lgkmcnt(5)\n\t" \
    "v_mfma_f32_16x16x32_bf16 v[188:191], v[56:59], v[160:163], v[188:191]\n\t" \
    "v_mul_f32 v213, v201, v207\n\t" \
    "v_mul_f32 v214, v202, v207\n\t" \
    "s_waitcnt lgkmcnt(4)\n\t" \
    "v_mfma_f32_16x16x32_bf16 v[188:191], v[60:63], v[164:167], v[188:191]\n\t" \
    "v_mul_f32 v215, v203, v207\n\t" \
    "v_mov_b64 v[216:217], -1\n\t" \
    "v_mfma_f32_16x16x32_bf16 v[192:195], v[136:139], v[160:163], v[192:195]\n\t" \
    "v_mov_b64 v[218:219], -1\n\t" \
    "buffer_store_dwordx4 v[216:219], %[l16], %[rs], %[sa] offen sc1\n\t" \
    "v_mfma_f32_16x16x32_bf16 v[192:195], v[140:143], v[164:167], v[192:195]\n\t" \
    "s_waitcnt lgkmcnt(3)\n\t" \
    "v_mfma_f32_16x16x32_bf16 v[192:195], v[144:147], v[168:171], v[192:195]\n\t" \
    "s_waitcnt lgkmcnt(2)\n\t" \
    "v_mfma_f32_16x16x32_bf16 v[192:195], v[148:151], v[172:175], v[192:195]\n\t" \
    "v_mfma_f32_16x16x32_bf16 v[188:191], v[64:67], v[168:171], v[188:191]\n\t" \
    "v_mfma_f32_16x16x32_bf16 v[188:191], v[68:71], v[172:175], v[188:191]\n\t" \
    "s_waitcnt lgkmcnt(1)\n\t" \
    "v_mfma_f32_16x16x32_bf16 v[188:191], v[72:75], v[176:179], v[188:191]\n\t" \
    "s_waitcnt lgkmcnt(0)\n\t" \
    "v_mfma_f32_16x16x32_bf16 v[188:191], v[76:79], v[180:183], v[188:191]\n\t" \
    "v_mfma_f32_16x16x32_bf16 v[192:195], v[152:155], v[176:179], v[192:195]\n\t" \
    "v_mfma_f32_16x16x32_bf16 v[192:195], v[156:159], v[180:183], v[192:195]\n\t" \
    "s_setprio 2\n\t" \
    "buffer_load_dwordx4 v[232:235], %[l16], %[rs], %[n0] offen offset:0 sc1\n\t" \
    "buffer_load_dwordx4 v[236:239], %[l16], %[rs], %[n0] offen offset:1024 sc1\n\t" \
    "buffer_load_dwordx4 v[240:243], %[l16], %[rs], %[n0] offen offset:2048 sc1\n\t" \
    "buffer_load_dwordx4 v[244:247], %[l16], %[rs], %[n0] offen offset:3072 sc1\n\t" \
    "buffer_load_dwordx4 v[248:251], %[l16], %[rs], %[n1] offen offset:0 sc1\n\t" \
    ASM_ST3 \
    "v_fma_f32 v188, v188, v206, v208\n\t" \
    "v_fma_f32 v189, v189, v206, v209\n\t" \
    "v_fma_f32 v190, v190, v206, v210\n\t" \
    "v_fma_f32 v191, v191, v206, v211\n\t" \
    "v_mul_f32 v160, v188, v188\n\t" \
    "v_mul_f32 v161, v189, v189\n\t" \
    "v_mul_f32 v162, v190, v190\n\t" \
    "v_mul_f32 v163, v191, v191\n\t" \
    "v_fma_f32 v160, v160, %[k1], v220\n\t" \
    "v_fma_f32 v161, v161, %[k1], v220\n\t" \
    "v_fma_f32 v162, v162, %[k1], v220\n\t" \
    "v_fma_f32 v163, v163, %[k1], v220\n\t" \
    "v_mul_f32 v160, v188, v160\n\t" \
    "v_mul_f32 v161, v189, v161\n\t" \
    "v_mul_f32 v162, v190, v162\n\t" \
    "v_mul_f32 v163, v191, v163\n\t" \
    "v_fma_f32 v192, v192, v206, v212\n\t" \
    "v_fma_f32 v193, v193, v206, v213\n\t" \
    "v_fma_f32 v194, v194, v206, v214\n\t" \
    "v_fma_f32 v195, v195, v206, v215\n\t" \
    "v_mul_f32 v164, v192, v192\n\t" \
    "v_mul_f32 v165, v193, v193\n\t" \
    "v_mul_f32 v166, v194, v194\n\t" \
    "v_mul_f32 v167, v195, v195\n\t" \
    "v_fma_f32 v164, v164, %[k1], v220\n\t" \
    "v_fma_f32 v165, v165, %[k1], v220\n\t" \
    "v_fma_f32 v166, v166, %[k1], v220\n\t" \
    "v_fma_f32 v167, v167, %[k1], v220\n\t" \
    "v_mul_f32 v164, v192, v164\n\t" \
    "v_mul_f32 v165, v193, v165\n\t" \
    "v_mul_f32 v166, v194, v166\n\t" \
    "v_mul_f32 v167, v195, v167\n\t" \
    "v_exp_f32 v160, v160\n\t" \
    "v_exp_f32 v161, v161\n\t" \
    "v_exp_f32 v162, v162\n\t" \
    "v_exp_f32 v163, v163\n\t" \
    "v_exp_f32 v164, v164\n\t" \
    "v_exp_f32 v165, v165\n\t" \
    "v_exp_f32 v166, v166\n\t" \
    "v_exp_f32 v167, v167\n\t" \
    "v_add_f32 v160, 1.0, v160\n\t" \
    "v_add_f32 v161, 1.0, v161\n\t" \
    "v_add_f32 v162, 1.0, v162\n\t" \
    "v_add_f32 v163, 1.0, v163\n\t" \
    "v_add_f32 v164, 1.0, v164\n\t" \
    "v_add_f32 v165, 1.0, v165\n\t" \
    "v_add_f32 v166, 1.0, v166\n\t" \
    "v_add_f32 v167, 1.0, v167\n\t" \
    "v_rcp_f32 v160, v160\n\t" \
    "v_rcp_f32 v161, v161\n\t" \
    "v_rcp_f32 v162, v162\n\t" \
    "v_rcp_f32 v163, v163\n\t" \
    "v_rcp_f32 v164, v164\n\t" \
    "v_rcp_f32 v165, v165\n\t" \
    "v_rcp_f32 v166, v166\n\t" \
    "v_rcp_f32 v167, v167\n\t" \
    "v_mul_f32 v188, v188, v160\n\t" \
    "v_mul_f32 v189, v189, v161\n\t" \
    "v_mul_f32 v190, v190, v162\n\t" \
    "v_mul_f32 v191, v191, v163\n\t" \
    "v_mul_f32 v192, v192, v164\n\t" \
    "v_mul_f32 v193, v193, v165\n\t" \
    "v_mul_f32 v194, v194, v166\n\t" \
    "v_mul_f32 v195, v195, v167\n\t" \
    "v_cvt_pk_bf16_f32 v188, v188, v189\n\t" \
    "v_cvt_pk_bf16_f32 v189, v190, v191\n\t" \
    "v_cvt_pk_bf16_f32 v190, v192, v193\n\t" \
    "v_cvt_pk_bf16_f32 v191, v194, v195\n\t" \
    "v_min_u32 v188, -2, v188\n\t" \
    "v_min_u32 v189, -2, v189\n\t" \
    "v_min_u32 v190, -2, v190\n\t" \
    "v_min_u32 v191, -2, v191\n\t" \
    "v_mov_b32 v216, 0x3f803f80\n\t" \
    "v_mov_b32 v217, 0x3f803f80\n\t" \
    "v_mov_b32 v218, 0x3f803f80\n\t" \
    "v_mov_b32 v219, 0x3f803f80\n\t" \
    "v_permlane16_swap_b32 v188, v190\n\t" \
    "v_permlane16_swap_b32 v189, v191\n\t" \
    "s_waitcnt vmcnt(6)\n\t" \
    "buffer_store_dwordx4 v[188:191], %[oa], %[rs], %[so] offen sc1\n\t" \
    "s_nop 1\n\t" \
    "s_setprio 0\n\t"
#define ASM_WT3_HI \
    "s_setprio 1\n\t" \
    "s_barrier\n\t" ASM_ST \
    "ds_read_b128 v[160:163], %[la] offset:0\n\t" \
    "ds_read_b128 v[164:167], %[la] offset:1024\n\t" \
    "ds_read_b128 v[168:171], %[la] offset:2048\n\t" \
    "ds_read2st64_b32 v[204:205], %[sr] offset0:20 offset1:24\n\t" \
    "ds_read_b128 v[172:175], %[la] offset:3072\n\t" \
    "ds_read_b128 v[176:179], %[la] offset:8192\n\t" \
    "ds_read_b128 v[180:183], %[la] offset:9216\n\t" \
    "ds_read_b128 v[184:187], %[la] offset:10240\n\t" \
    "s_waitcnt lgkmcnt(7)\n\t" \
    "v_mfma_f32_16x16x32_bf16 v[192:195], v[80:83], v[160:163], 0\n\t" \
    "s_waitcnt lgkmcnt(6)\n\t" \
    "v_mfma_f32_16x16x32_bf16 v[192:195], v[84:87], v[164:167], v[192:195]\n\t" \
    "v_mfma_f32_16x16x32_bf16 v[188:191], v[0:3], v[160:163], 0\n\t" \
    "ds_read_b128 v[160:163], %[la] offset:11264\n\t" \
    "v_mfma_f32_16x16x32_bf16 v[188:191], v[4:7], v[164:167], v[188:191]\n\t" \
    "ds_read_b128 v[164:167], %[la] offset:16384\n\t" \
    "s_waitcnt lgkmcnt(7)\n\t" \
    "v_mfma_f32_16x16x32_bf16 v[188:191], v[8:11], v[168:171], v[188:191]\n\t" \
    "s_waitcnt lgkmcnt(6)\n\t" \
    "s_waitcnt lgkmcnt(5)\n\t" \
    "v_mfma_f32_16x16x32_bf16 v[188:191], v[12:15], v[172:175], v[188:191]\n\t" \
    "v_permlane16_swap_b32 v204, v205\n\t" \
    "v_mfma_f32_16x16x32_bf16 v[192:195], v[88:91], v[168:171], v[192:195]\n\t" \
    "ds_read_b128 v[168:171], %[la] offset:17408\n\t" \
    "v_mfma_f32_16x16x32_bf16 v[192:195], v[92:95], v[172:175], v[192:195]\n\t" \
    "ds_read_b128 v[172:175], %[la] offset:18432\n\t" \
    "v_add_f32 v206, v204, v205\n\t" \
    "s_waitcnt lgkmcnt(6)\n\t" \
    "v_mfma_f32_16x16x32_bf16 v[192:195], v[96:99], v[176:179], v[192:195]\n\t" \
    "v_add_f32 v207, v204, v205\n\t" \
    "s_waitcnt lgkmcnt(5)\n\t" \
    "v_mfma_f32_16x16x32_bf16 v[192:195], v[100:103], v[180:183], v[192:195]\n\t" \
    "v_mfma_f32_16x16x32_bf16 v[188:191], v[16:19], v[176:179], v[188:191]\n\t" \
    "ds_read_b128 v[176:179], %[la] offset:19456\n\t" \
    "v_permlane32_swap_b32 v206, v207\n\t" \
    "v_mfma_f32_16x16x32_bf16 v[188:191], v[20:23], v[180:183], v[188:191]\n\t" \
    "ds_read_b128 v[180:183], %[la] offset:24576\n\t" \
    "s_waitcnt lgkmcnt(6)\n\t" \
    "v_mfma_f32_16x16x32_bf16 v[188:191], v[24:27], v[184:187], v[188:191]\n\t" \
    "v_add_f32 v204, v206, v207\n\t" \
    "s_waitcnt lgkmcnt(5)\n\t" \
    "v_mfma_f32_16x16x32_bf16 v[188:191], v[28:31], v[160:163], v[188:191]\n\t" \
    "v_add_f32 v205, v206, v207\n\t" \
    "v_mfma_f32_16x16x32_bf16 v[192:195], v[104:107], v[184:187], v[192:195]\n\t" \
    "ds_read_b128 v[184:187], %[la] offset:25600\n\t" \
    "v_mfma_f32_16x16x32_bf16 v[192:195], v[108:111], v[160:163], v[192:195]\n\t" \
    "ds_read_b128 v[160:163], %[la] offset:26624\n\t" \
    "v_permlane16_swap_b32 v204, v205\n\t" \
    "s_waitcnt lgkmcnt(6)\n\t" \
    "v_mfma_f32_16x16x32_bf16 v[192:195], v[112:115], v[164:167], v[192:195]\n\t" \
    "s_waitcnt lgkmcnt(5)\n\t" \
    "v_mfma_f32_16x16x32_bf16 v[192:195], v[116:119], v[168:171], v[192:195]\n\t" \
    "v_mul_f32 v205, %[kd], v205\n\t" \
    "v_mfma_f32_16x16x32_bf16 v[188:191], v[32:35], v[164:167], v[188:191]\n\t" \
    "ds_read_b128 v[164:167], %[la] offset:27648\n\t" \
    "v_mul_f32 v204, %[kd], v204\n\t" \
    "v_mfma_f32_16x16x32_bf16 v[188:191], v[36:39], v[168:171], v[188:191]\n\t" \
    "ds_read_b128 v[168:171], %[la] offset:4096\n\t" \
    "v_fma_f32 v204, -v205, v205, v204\n\t" \
    "s_waitcnt lgkmcnt(6)\n\t" \
    "v_mfma_f32_16x16x32_bf16 v[188:191], v[40:43], v[172:175], v[188:191]\n\t" \
    "v_max_f32 v204, 0, v204\n\t" \
    "s_waitcnt lgkmcnt(5)\n\t" \
    "v_mfma_f32_16x16x32_bf16 v[188:191], v[44:47], v[176:179], v[188:191]\n\t" \
    "v_add_f32 v206, %[ke], v204\n\t" \
    "v_mfma_f32_16x16x32_bf16 v[192:195], v[120:123], v[172:175], v[192:195]\n\t" \
    "ds_read_b128 v[172:175], %[la] offset:12288\n\t" \
    "v_rsq_f32 v206, v206\n\t" \
    "v_mfma_f32_16x16x32_bf16 v[192:195], v[124:127], v[176:179], v[192:195]\n\t" \
    "ds_read_b128 v[176:179], %[la] offset:20480\n\t" \
    "s_waitcnt lgkmcnt(6)\n\t" \
    "v_mfma_f32_16x16x32_bf16 v[192:195], v[128:131], v[180:183], v[192:195]\n\t" \
    "v_mul_f32 v207, -v205, v206\n\t" \
    "s_waitcnt lgkmcnt(5)\n\t" \
    "v_mfma_f32_16x16x32_bf16 v[192:195], v[132:135], v[184:187], v[192:195]\n\t" \
    "v_mul_f32 v208, v196, v207\n\t" \
    "v_mfma_f32_16x16x32_bf16 v[188:191], v[48:51], v[180:183], v[188:191]\n\t" \
    "ds_read_b128 v[180:183], %[la] offset:28672\n\t" \
    "v_mul_f32 v209, v197, v207\n\t" \
    "v_mul_f32 v210, v198, v207\n\t" \
    "v_mfma_f32_16x16x32_bf16 v[188:191], v[52:55], v[184:187], v[188:191]\n\t" \
    "v_mul_f32 v211, v199, v207\n\t" \
    "v_mul_f32 v212, v200, v207\n\t" \
    "s_waitcnt lgkmcnt(5)\n\t" \
    "v_mfma_f32_16x16x32_bf16 v[188:191], v[56:59], v[160:163], v[188:191]\n\t" \
    "v_mul_f32 v213, v201, v207\n\t" \
    "v_mul_f32 v214, v202, v207\n\t" \
    "s_waitcnt lgkmcnt(4)\n\t" \
    "v_mfma_f32_16x16x32_bf16 v[188:191], v[60:63], v[164:167], v[188:191]\n\t" \
    "v_mul_f32 v215, v203, v207\n\t" \
    "v_mov_b64 v[216:217], -1\n\t" \
    "v_mfma_f32_16x16x32_bf16 v[192:195], v[136:139], v[160:163], v[192:195]\n\t" \
    "v_mov_b64 v[218:219], -1\n\t" \
    "buffer_store_dwordx4 v[216:219], %[l16], %[rs], %[sa] offen sc1\n\t" \
    "v_mfma_f32_16x16x32_bf16 v[192:195], v[140:143], v[164:167], v[192:195]\n\t" \
    "s_waitcnt lgkmcnt(3)\n\t" \
    "v_mfma_f32_16x16x32_bf16 v[192:195], v[144:147], v[168:171], v[192:195]\n\t" \
    "s_waitcnt lgkmcnt(2)\n\t" \
    "v_mfma_f32_16x16x32_bf16 v[192:195], v[148:151], v[172:175], v[192:195]\n\t" \
    "v_mfma_f32_16x16x32_bf16 v[188:191], v[64:67], v[168:171], v[188:191]\n\t" \
    "v_mfma_f32_16x16x32_bf16 v[188:191], v[68:71], v[172:175], v[188:191]\n\t" \
    "s_waitcnt lgkmcnt(1)\n\t" \
    "v_mfma_f32_16x16x32_bf16 v[188:191], v[72:75], v[176:179], v[188:191]\n\t" \
    "s_waitcnt lgkmcnt(0)\n\t" \
    "v_mfma_f32_16x16x32_bf16 v[188:191], v[76:79], v[180:183], v[188:191]\n\t" \
    "v_mfma_f32_16x16x32_bf16 v[192:195], v[152:155], v[176:179], v[192:195]\n\t" \
    "v_mfma_f32_16x16x32_bf16 v[192:195], v[156:159], v[180:183], v[192:195]\n\t" \
    ASM_ST3 \
    "s_nop 1\n\t" \
    "v_fma_f32 v188, v188, v206, v208\n\t" \
    "v_fma_f32 v189, v189, v206, v209\n\t" \
    "v_fma_f32 v190, v190, v206, v210\n\t" \
    "v_fma_f32 v191, v191, v206, v211\n\t" \
    "v_mul_f32 v160, v188, v188\n\t" \
    "v_mul_f32 v161, v189, v189\n\t" \
    "v_mul_f32 v162, v190, v190\n\t" \
    "v_mul_f32 v163, v191, v191\n\t" \
    "v_fma_f32 v160, v160, %[k1], v220\n\t" \
    "v_fma_f32 v161, v161, %[k1], v220\n\t" \
    "v_fma_f32 v162, v162, %[k1], v220\n\t" \
    "v_fma_f32 v163, v163, %[k1], v220\n\t" \
    "v_mul_f32 v160, v188, v160\n\t" \
    "v_mul_f32 v161, v189, v161\n\t" \
    "v_mul_f32 v162, v190, v162\n\t" \
    "v_mul_f32 v163, v191, v163\n\t" \
    "v_fma_f32 v192, v192, v206, v212\n\t" \
    "v_fma_f32 v193, v193, v206, v213\n\t" \
    "v_fma_f32 v194, v194, v206, v214\n\t" \
    "v_fma_f32 v195, v195, v206, v215\n\t" \
    "v_mul_f32 v164, v192, v192\n\t" \
    "v_mul_f32 v165, v193, v193\n\t" \
    "v_mul_f32 v166, v194, v194\n\t" \
    "v_mul_f32 v167, v195, v195\n\t" \
    "v_fma_f32 v164, v164, %[k1], v220\n\t" \
    "v_fma_f32 v165, v165, %[k1], v220\n\t" \
    "v_fma_f32 v166, v166, %[k1], v220\n\t" \
    "v_fma_f32 v167, v167, %[k1], v220\n\t" \
    "v_mul_f32 v164, v192, v164\n\t" \
    "v_mul_f32 v165, v193, v165\n\t" \
    "v_mul_f32 v166, v194, v166\n\t" \
    "v_mul_f32 v167, v195, v167\n\t" \
    "v_exp_f32 v160, v160\n\t" \
    "v_exp_f32 v161, v161\n\t" \
    "v_exp_f32 v162, v162\n\t" \
    "v_exp_f32 v163, v163\n\t" \
    "v_exp_f32 v164, v164\n\t" \
    "v_exp_f32 v165, v165\n\t" \
    "v_exp_f32 v166, v166\n\t" \
    "v_exp_f32 v167, v167\n\t" \
    "v_add_f32 v160, 1.0, v160\n\t" \
    "v_add_f32 v161, 1.0, v161\n\t" \
    "v_add_f32 v162, 1.0, v162\n\t" \
    "v_add_f32 v163, 1.0, v163\n\t" \
    "v_add_f32 v164, 1.0, v164\n\t" \
    "v_add_f32 v165, 1.0, v165\n\t" \
    "v_add_f32 v166, 1.0, v166\n\t" \
    "v_add_f32 v167, 1.0, v167\n\t" \
    "v_rcp_f32 v160, v160\n\t" \
    "v_rcp_f32 v161, v161\n\t" \
    "v_rcp_f32 v162, v162\n\t" \
    "v_rcp_f32 v163, v163\n\t" \
    "v_rcp_f32 v164, v164\n\t" \
    "v_rcp_f32 v165, v165\n\t" \
    "v_rcp_f32 v166, v166\n\t" \
    "v_rcp_f32 v167, v167\n\t" \
    "v_mul_f32 v188, v188, v160\n\t" \
    "v_mul_f32 v189, v189, v161\n\t" \
    "v_mul_f32 v190, v190, v162\n\t" \
    "v_mul_f32 v191, v191, v163\n\t" \
    "v_mul_f32 v192, v192, v164\n\t" \
    "v_mul_f32 v193, v193, v165\n\t" \
    "v_mul_f32 v194, v194, v166\n\t" \
    "v_mul_f32 v195, v195, v167\n\t" \
    "v_cvt_pk_bf16_f32 v188, v188, v189\n\t" \
    "v_cvt_pk_bf16_f32 v189, v190, v191\n\t" \
    "v_cvt_pk_bf16_f32 v190, v192, v193\n\t" \
    "v_cvt_pk_bf16_f32 v191, v194, v195\n\t" \
    "v_min_u32 v188, -2, v188\n\t" \
    "v_min_u32 v189, -2, v189\n\t" \
    "v_min_u32 v190, -2, v190\n\t" \
    "v_min_u32 v191, -2, v191\n\t" \
    "s_nop 1\n\t" \
    "v_permlane16_swap_b32 v188, v190\n\t" \
    "v_permlane16_swap_b32 v189, v191\n\t" \
    "s_waitcnt vmcnt(1)\n\t" \
    "buffer_store_dwordx4 v[188:191], %[oa], %[rs], %[so] offen sc1\n\t" \
    "s_nop 1\n\t"
#define ASM_POLL5_I \
    "buffer_load_dwordx4 v[232:235], %[l16], %[rs], %[s0] offen offset:0 sc1\n\t" \
    "buffer_load_dwordx4 v[236:239], %[l16], %[rs], %[s0] offen offset:1024 sc1\n\t" \
    "buffer_load_dwordx4 v[240:243], %[l16], %[rs], %[s0] offen offset:2048 sc1\n\t" \
    "buffer_load_dwordx4 v[244:247], %[l16], %[rs], %[s0] offen offset:3072 sc1\n\t" \
    "buffer_load_dwordx4 v[248:251], %[l16], %[rs], %[s1] offen offset:0 sc1\n\t"
using u32x8 = __attribute__((ext_vector_type(8))) unsigned;
using u32x16 = __attribute__((ext_vector_type(16))) unsigned;
using u32x32 = __attribute__((ext_vector_type(32))) unsigned;
struct WRegs { u32x32 w[6]; u32x8 t; };
#define WREGS(W) "{v[0:31]}"(W.w[0]), "{v[32:63]}"(W.w[1]), "{v[64:95]}"(W.w[2]), "{v[96:127]}"(W.w[3]), "{v[128:159]}"(W.w[4]), "{v[160:191]}"(W.w[5]), "{v[192:199]}"(W.t)
__device__ __forceinline__ u32x32 cat32(u32x8 a, u32x8 b, u32x8 c, u32x8 d) {
    const u32x16 lo = __builtin_shufflevector(a, b, 0, 1, 2, 3, 4, 5, 6, 7, 8, 9, 10, 11, 12, 13, 14, 15);
    const u32x16 hi = __builtin_shufflevector(c, d, 0, 1, 2, 3, 4, 5, 6, 7, 8, 9, 10, 11, 12, 13, 14, 15);
    return __builtin_shufflevector(lo, hi, 0, 1, 2, 3, 4, 5, 6, 7, 8, 9, 10, 11, 12, 13, 14, 15, 16, 17, 18, 19, 20, 21, 22, 23, 24, 25, 26, 27, 28, 29, 30, 31);
}
#define WOUT(W) "=&{v[0:31]}"(W.w[0]), "=&{v[32:63]}"(W.w[1]), "=&{v[64:95]}"(W.w[2]), "=&{v[96:127]}"(W.w[3]), "=&{v[128:159]}"(W.w[4]), "=&{v[160:191]}"(W.w[5]), "=&{v[192:199]}"(W.t)
#define WTMP "memory", "scc", "v200", "v201", "v202", "v203", "v204", "v205", "v206", "v207", "v208", "v209", "v210", "v211", "v212", "v213", "v214", "v215", "v216", "v217", "v218", "v219", "v220", "v221", "v222", "v223", "v224", "v225", "v226", "v227", "v228", "v229", "v230", "v231", "v232", "v233", "v234", "v235", "v236", "v237", "v238", "v239"
template <bool NT>
__device__ __forceinline__ void load_w3(WRegs& W, const bf16_t* m, int wave, unsigned c0, bool hq) {
    const int ln = lane_id();
    unsigned o[3], s[3], t;
#pragma unroll
    for (int k = 0; k < 3; ++k) {
        const int row = (k * 64 + ln) / 24, sg = (k * 64 + ln) % 24;
        o[k] = row * (HD * 2) + c0 + (hq ? ((sg >> 1) & 1) * 384 + ((0x140 >> ((sg >> 3) * 4)) & 15) * 32 + (sg & 5) * 16 : sg * 16); s[k] = wave * 13824 + row * 416 + sg * 16;
    }
    const unsigned tr = (ln >> 4) * 3456 + ((ln >> 2) & 3) * 416 + (wave & 3) * 32 + (ln & 3) * 8;
#undef WNT
#define WNT " nt"
    if constexpr (NT) {
    asm volatile(ASM_WLOAD3 : WOUT(W), [t] "=&s"(t) : [rs] "s"(mkrsrc(m, DM * HD * 2)), [wx] "s"(wave), [o0] "v"(o[0]), [o1] "v"(o[1]), [o2] "v"(o[2]), [s0] "v"(s[0]), [s1] "v"(s[1]), [s2] "v"(s[2]),
                 [t0] "v"(tr + wave * 13824), [t1] "v"(tr + (wave ^ 1) * 13824), [t2] "v"(tr + (wave ^ 2) * 13824), [t3] "v"(tr + (wave ^ 3) * 13824), [t4] "v"(tr + (wave & 4) * 13824),
                 [wl] "v"(L_W + wave * 10240 + ln * 16) : WTMP);
    } else {
#undef WNT
#define WNT ""
    asm volatile(ASM_WLOAD3 : WOUT(W), [t] "=&s"(t) : [rs] "s"(mkrsrc(m, DM * HD * 2)), [wx] "s"(wave), [o0] "v"(o[0]), [o1] "v"(o[1]), [o2] "v"(o[2]), [s0] "v"(s[0]), [s1] "v"(s[1]), [s2] "v"(s[2]),
                 [t0] "v"(tr + wave * 13824), [t1] "v"(tr + (wave ^ 1) * 13824), [t2] "v"(tr + (wave ^ 2) * 13824), [t3] "v"(tr + (wave ^ 3) * 13824), [t4] "v"(tr + (wave & 4) * 13824),
                 [wl] "v"(L_W + wave * 10240 + ln * 16) : WTMP);
    }
}
#undef WNT
#define WNT ""
__device__ __forceinline__ void load_wo(WRegs& W, const bf16_t* m, int wave, int h) {
    const int ln = lane_id();
    unsigned o[5], s[5], t;
#pragma unroll
    for (int k = 0; k < 5; ++k) {
        const int row = (k * 64 + ln) / 20, sg = (k * 64 + ln) % 20;
        o[k] = (h * DH + row) * (DM * 2) + sg * 16 + (wave & 3) * 320; s[k] = wave * 13824 + row * 352 + sg * 16;
    }
    asm volatile(ASM_WLOADO : WOUT(W), [t] "=&s"(t) : [rs] "s"(mkrsrc(m, DM * HD * 2)), [o0] "v"(o[0]), [o1] "v"(o[1]), [o2] "v"(o[2]), [o3] "v"(o[3]), [o4] "v"(o[4]),
                 [s0] "v"(s[0]), [s1] "v"(s[1]), [s2] "v"(s[2]), [s3] "v"(s[3]), [s4] "v"(s[4]), [tr] "v"(wave * 13824 + (ln >> 2) * 352 + (ln & 3) * 8), [wl] "v"(L_W + wave * 10240 + ln * 16) : WTMP);
}
using s16x4 = __attribute__((ext_vector_type(4))) short;
template <int NT, int KS, int N>
__device__ __forceinline__ void load_wn(u32x4 (&f)[NT * KS], const bf16_t* m, int rows, int wave, int c0, int k0) {
    const int ln = lane_id();
    const rsrc_t rs = mkrsrc(m, rows * N * 2);
#pragma unroll
    for (int k = 0; k < KS; ++k)
#pragma unroll
        for (int n = 0; n < NT; ++n) f[n * KS + k] = __builtin_amdgcn_raw_buffer_load_b128(rs, ((n * 64 + ln) / (2 * NT)) * (N * 2) + ((n * 64 + ln) % (2 * NT)) * 16, (k0 + k) * (N * 64) + c0 * 32, 0);
    const unsigned st = wave * 10240 + ln * 16, tr = wave * 10240 + ((ln >> 4) * 8 + ((ln >> 2) & 3)) * (NT * 32) + (ln & 3) * 8;
#pragma unroll
    for (int k = 0; k < KS; ++k) {
#pragma unroll
        for (int n = 0; n < NT; ++n) lds_st<u32x4>(st + n * 1024, f[n * KS + k]);
        asm volatile("" ::: "memory");
#pragma unroll
        for (int n = 0; n < NT; ++n) {
            const u32x2 a = __builtin_bit_cast(u32x2, __builtin_amdgcn_ds_read_tr16_b64_v4i16((LAS s16x4*)(tr + n * 32)));
            const u32x2 b = __builtin_bit_cast(u32x2, __builtin_amdgcn_ds_read_tr16_b64_v4i16((LAS s16x4*)(tr + n * 32 + NT * 128)));
            f[n * KS + k] = (u32x4){a[0], a[1], b[0], b[1]};
        }
        asm volatile("" ::: "memory");
    }
}
struct C1Regs { u32x8 lw0, lw1, z1; u32x4 z0; };
#define XG_OUT "=&{v[200:207]}"(C.lw0), "=&{v[224:227]}"(C.z0), "=&{v[228:235]}"(C.lw1), "=&{v[244:251]}"(C.z1)
#define XG_IN WREGS(W), [xa] "v"(xa), [wl] "v"(wl), [qs] "s"(qs)
#define XG_CLOB "memory", "scc", "v208", "v209", "v210", "v211", "v212", "v213", "v214", "v215", "v216", "v217", "v218", "v219", "v220", "v221", "v222", "v223", "v236", "v237", "v238", "v239", "v240", "v241", "v242", "v243", "v252"
constexpr int XS0 = 8;
__device__ __forceinline__ unsigned init_wait(const Params& P, int c, int ph, unsigned need = 0x220080u) {
    unsigned w;
    for (int spins = 0;; ++spins) {
        unsigned o;
        asm volatile("v_mov_b32 %0, 0" : "=v"(o));
        w = __builtin_amdgcn_readfirstlane(__builtin_amdgcn_raw_buffer_load_b32(mkrsrc(P.ws + FL0, FL_BYTES), o, FL_IN + (ph * 8 + (c & 7)) * 128, 16));
        if ((w & need) == need) break;
        __builtin_amdgcn_s_sleep(8);
        if (spin_fail(spins, P)) break;
    }
    asm volatile("" ::: "memory");
    return w;
}
#define PLACE() const unsigned pw_ = init_wait(P, wave, L); const int par = (L ^ (pw_ >> 16)) & 1, cpy = x ^ ((pw_ >> 20) & 1); const rsrc_t rr = mkrsrc(P.ws + par * 8192, RG_L); (void)cpy; (void)rr
__device__ __forceinline__ void xn_wait(const Params& P, rsrc_t rr, int seg, int cpy) {
    for (int spins = 0;; ++spins) {
        const unsigned f = __builtin_amdgcn_raw_buffer_load_b32(rr, (lane_id() & 15) * 128, RG_FX + (cpy & 3) * 2048, 16);
        if (__all(f > (unsigned)seg)) break;
        __builtin_amdgcn_s_sleep(4);
        if (spin_fail(spins, P)) break;
    }
    asm volatile("" ::: "memory");
}
__device__ __forceinline__ unsigned c1_dst(int wave, bool ktype) {
    const int ln = lane_id(), l15 = ln & 15, hf = (wave >> 1) & 1;
    const int rb16 = ((ln >> 2) & 4) * 16 + (ln >> 5) * 32;
    return (ktype ? L_KM + (l15 >> 2) * 128 + hf * 8 + (l15 & 3) * 2
                  : L_Q + (l15 >> 3) * 256 + ((l15 >> 1) & 1) * 128 + ((l15 >> 2) & 1) * 8 + hf * 4 + (l15 & 1) * 2) + (wave & 1) * 1536 + rb16;
}
template <int NP>
__device__ __forceinline__ void crit1(const WRegs& W, C1Regs& C, rsrc_t rr, unsigned sbase, int wave, unsigned wl, unsigned dsto, unsigned& budget, unsigned& fv, unsigned fs
) {
    unsigned abase;
    asm volatile("v_and_b32 %0, 0x3ff, %1\n\tv_or_b32 %0, %2, %0" : "=&v"(abase) : "v"(wl), "s"(wave * 1024));
    ASM_ST_DECL;
#define C1_OUT "+{v[224:227]}"(C.z0), "+{v[244:251]}"(C.z1), "+{v[200:207]}"(C.lw0), "+{v[228:235]}"(C.lw1)
#define C1_IN WREGS(W), [ab] "v"(abase), [wl] "v"(wl), [dst] "v"(dsto)
#define C1_CLOB "memory", "vcc", "scc", "v208", "v209", "v210", "v211", "v212", "v213", "v214", "v215", "v216", "v217", "v218", "v219", "v220", "v221", "v222", "v223", "v236", "v237", "v238", "v239", "v240", "v241", "v242", "v243"
#define C1_POLL [rs] "s"(rr), [s0] "s"(sbase), [s1] "s"(sbase + 4096), [s2] "s"(sbase + 2 * BLK)
    ST(1);
    if constexpr (NP == 13) asm volatile(ASM_CRIT1_V3 "s_waitcnt vmcnt(0)" : C1_OUT, [n] "+s"(budget), [fv] "+v"(fv) ASM_ST_OUT ASM_ST2_OUT : C1_IN, C1_POLL, [fs] "s"(fs) : C1_CLOB);
    else if constexpr (NP == 12) asm volatile(ASM_CRIT1_V2 "s_waitcnt vmcnt(0)" : C1_OUT, [n] "+s"(budget), [fv] "+v"(fv) ASM_ST_OUT ASM_ST2_OUT : C1_IN, C1_POLL, [fs] "s"(fs) : C1_CLOB);
    else if constexpr (NP == 3) asm volatile(ASM_CRIT1_3 : C1_OUT, [n] "+s"(budget) ASM_ST_OUT ASM_ST2_OUT : C1_IN, C1_POLL : C1_CLOB);
    else if constexpr (NP == 2) asm volatile(ASM_CRIT1_2 : C1_OUT, [n] "+s"(budget) ASM_ST_OUT ASM_ST2_OUT : C1_IN, C1_POLL : C1_CLOB);
    else asm volatile(ASM_CRIT1_N : C1_OUT ASM_ST_OUT : C1_IN : C1_CLOB);
    SB();
    ASM_ST_SAVE(3);
    if constexpr (NP != 0) { ASM_ST2_SAVE(2); }
}
#define STARG

#define SRG 5
#undef STROLE
#define STROLE 1
__device__ __forceinline__ void s_role(const Params& P, int wave, int L, int h, int x, unsigned t0_) {
    const char* xn = P.ws + PG0 + PG_XN + L * PG_LX;
    auto xdma = [&](int seg, int hb, int par) {
        const unsigned l16 = lane_id() * 16;
        const int mt = (wave >> 1) & 1;
        const char* g = xn + pgb(seg, par);
        if (hb == 0) {
            dma4(g + blin(mt * 20480 + (wave & 1) * 4096), l16, L_XA + mt * 16384 + (wave & 1) * 4096);
            dmaq<0>(g + blin(mt * 20480 + 16384) + (wave & 1) * 1024, l16, L_XB + mt * 4096 + (wave & 1) * 1024);
        } else {
            dma4(g + blin(mt * 20480 + 8192), l16, L_XA + mt * 16384 + 8192); dma4(g + blin(mt * 20480 + 12288), l16, L_XA + mt * 16384 + 12288);
            dmaq<2>(g + blin(mt * 20480 + 16384), l16, L_XB + mt * 4096); dmaq<3>(g + blin(mt * 20480 + 16384), l16, L_XB + mt * 4096);
        }
    };
    const float SCL = 0.07216878364870322f * 1.4426950408889634f;
    auto run = [&](auto Cc) {
    constexpr int CLS = decltype(Cc)::value;
    WRegs wf;
    MARK(!L && h == 0 && wave == 0, 0);
    unsigned hm_ = 0, hc_ = 0;
    if (HM_T == -2) hm_ = (unsigned)__builtin_amdgcn_s_memrealtime() - t0_;
    load_w3<(NTM & (CLS == 3 ? 2 : 1)) != 0>(wf, P.w[CLS == 3], wave, h * 384, false);
    MARKX(!L && wave == 0, 1);
    if (HM_T == -1) hm_ = (unsigned)__builtin_amdgcn_s_memrealtime() - t0_;
    PLACE();
    if (CLS == 3) {
        if (L) xn_wait(P, rr, 0, cpy);
        xdma(0, 0, par);
        if (wave == 4 || wave == 7) xdma(0, 1, par);
        wait_vm<0>();
    }
    bar();
    if (CLS == 3 && wave == 7 && L) xn_wait(P, rr, 1, cpy);
    const unsigned qs = (CLS == 3 ? L_KX : L_QX) + (wave & 1) * 3072 + ((wave >> 1) & 1) * 8;
    unsigned xa, wl, dsto;
    if (CLS == 3) { xa = L_XA + wave * 2048 + lane_id() * 16; wl = L_W + wave * 10240 + lane_id() * 16; dsto = c1_dst(wave, true); }
    __builtin_amdgcn_s_setprio(CLS == 3 ? 1 : 2);
    unsigned budget = 1u << 18, tb2 = 0;
    STDECL;
    LDECL;
#pragma unroll 1
    for (int t = 0; t < NSEG; ++t) {
        const unsigned slot = t & (NSLOT - 1);
        C1Regs C;
        {
            const WRegs& W = wf;
            ASM_ST_DECL;
            if constexpr (CLS == 3) {
                const int mt = (wave >> 1) & 1;
                const char* g = xn + pgb(t + 1 < NSEG ? t + 1 : t, par);
                asm volatile(ASM_XG_S_HI : XG_OUT ASM_ST_OUT : XG_IN, [slp] "s"(SLP4), [g0] "s"(g + blin(mt * 20480 + (wave & 1) * 4096)), [d0] "s"(L_XA + mt * 16384 + (wave & 1) * 4096),
                             [g1] "s"(g + blin(mt * 20480 + 16384) + (wave & 1) * 1024), [d1] "s"(L_XB + mt * 4096 + (wave & 1) * 1024) : XG_CLOB);
                wait_vm<0>();
                LB(t);
            } else {
                const unsigned l16 = lane_id() * 16;
                xa = L_XA + wave * 2048 + l16; wl = L_W + wave * 10240 + l16;
#define XG_Q(A) asm volatile("s_cmp_lt_u32 %[t], 2\n\ts_cbranch_scc1 .Lq3%=\n\t" A "s_branch .Lqe%=\n\t.Lq3%=:\n\t" ASM_XG_S_LO ".Lqe%=:" : XG_OUT ASM_ST_OUT : XG_IN, [t] "s"(t) : XG_CLOB)
                if constexpr (CLS == 0 || CLS == 4) XG_Q(ASM_XG_S_LO1); else XG_Q(ASM_XG_S_LO2);
                __builtin_amdgcn_s_setprio(0);
                if constexpr (CLS == 0) {
                    const int ln = lane_id();
                    const u32x4 s4 = sent4();
                    const unsigned poff = (ln * 16 + ((ln < 8) ? h * 128 : (1 + (h >> 4)) * 2 * BLK - 128 + (h & 15) * 512)) | ((ln >= 40 || t < 2) ? 0x40000000u : 0u);
#pragma unroll
                    for (int c = 0; c < (int)NCP; ++c) st128(s4, rr, poff, RG_XP + ((t - 2) & (NSLOT - 1)) * XP_SLOT + (c >> 1) * XP_PAIR + (c & 1) * BLK);
                    const unsigned pbase = RG_PX + ((t - 3) & (NSLOT - 1)) * PX_SLOT + (h >> 2) * BLK;
                    const unsigned o2 = ((ln & 16) ? 5120 + ((h >> 1) & 1) * 1024 + (h & 1) * 256 + (ln >> 5) * 512 : 4096 + (h & 3) * 256 + (ln >> 5) * 3072) + (ln & 15) * 16;
                    if (t >= 3) { st128(s4, rr, ln * 16, pbase + (h & 3) * 1024); st128(s4, rr, o2, pbase); }
                }
                if (CLS == 4 && t >= HT0) {
                    const int ln = lane_id(), u = h >> 2;
                    for (int spins = 0;; ++spins) {
                        const unsigned fl = __builtin_amdgcn_raw_buffer_load_b32(rr, (ln & 3) * 128, RG_FH + BLK + u * 512, 16);
                        if (__all(fl > (unsigned)t)) break;
                        __builtin_amdgcn_s_sleep(2);
                        if (spin_fail(spins, P)) break;
                    }
                    asm volatile("" ::: "memory");
                    const char* g = P.ws + PG0 + pgb(t, par) + PG_HV + (16 + u) * 3 * BLK;
                    unsigned t0, t1, vo;
                    static_assert(L_QX == 16384, "");
                    asm volatile("s_add_u32 %0, %4, 0\n\ts_and_b32 %1, %0, 0xe000\n\ts_add_u32 %0, %0, %1\n\tv_add_u32 %2, %0, %5\n\ts_mov_b32 m0, 16384\n\ts_nop 0\n\tglobal_load_lds_dwordx4 %2, %3 sc1\n\t"
                                 "s_add_u32 %0, %4, 1024\n\ts_and_b32 %1, %0, 0xe000\n\ts_add_u32 %0, %0, %1\n\tv_add_u32 %2, %0, %5\n\ts_mov_b32 m0, 17408\n\ts_nop 0\n\tglobal_load_lds_dwordx4 %2, %3 sc1\n\t"
                                 "s_add_u32 %0, %4, 2048\n\ts_and_b32 %1, %0, 0xe000\n\ts_add_u32 %0, %0, %1\n\tv_add_u32 %2, %0, %5\n\ts_mov_b32 m0, 19456\n\ts_nop 0\n\tglobal_load_lds_dwordx4 %2, %3 sc1\n\t"
                                 "s_add_u32 %0, %4, 6144\n\ts_and_b32 %1, %0, 0xe000\n\ts_add_u32 %0, %0, %1\n\tv_add_u32 %2, %0, %5\n\ts_mov_b32 m0, 22528\n\ts_nop 0\n\tglobal_load_lds_dwordx4 %2, %3 sc1\n\t"
                                 "s_add_u32 %0, %4, 7168\n\ts_and_b32 %1, %0, 0xe000\n\ts_add_u32 %0, %0, %1\n\tv_add_u32 %2, %0, %5\n\ts_mov_b32 m0, 23552\n\ts_nop 0\n\tglobal_load_lds_dwordx4 %2, %3 sc1\n\t"
                                 "s_add_u32 %0, %4, 8192\n\ts_and_b32 %1, %0, 0xe000\n\ts_add_u32 %0, %0, %1\n\tv_add_u32 %2, %0, %5\n\ts_mov_b32 m0, 25600\n\ts_nop 0\n\tglobal_load_lds_dwordx4 %2, %3 sc1\n\t"
                                 : "=&s"(t0), "=&s"(t1), "=&v"(vo) : "s"(g), "s"((((h >> 1) & 1) * 12 + (h & 1) * 3) * 1024), "v"(ln * 16) : "memory", "scc");
                }
                dsto = c1_dst(wave, false);
                while ((unsigned)__builtin_amdgcn_s_memtime() - tb2 < STD) __builtin_amdgcn_s_sleep(2);
            }
            ASM_ST_SAVE(7);
            ST(6);
        }
        ST(0);
        unsigned fv = 0;
        if (CLS == 2) fv = (lane_id() & 15) * 128;
        crit1<CLS == 3 ? 0 : CLS < 2 ? 3 : CLS == 2 ? 12 : 2>(wf, C, rr, RG_XM + slot * XM_SLOT + (cpy >> 1) * XM_PAIR + (cpy & 1) * BLK, wave, wl, dsto, budget, fv, RG_FX + (cpy & 3) * 2048 STARG);
        if (CLS != 3) tb2 = (unsigned)__builtin_amdgcn_s_memtime();
        if constexpr (CLS == 0) {
            const unsigned sp = RG_XP + slot * XP_SLOT, sx = sp + (1 + (h >> 4)) * 2 * BLK + (h & 15) * 512;
            ASM_ST_DECL;
            asm volatile(ASM_SM : [c0] "+s"(budget) ASM_ST_OUT : WREGS(wf), [rr] "s"(rr), [sx] "s"(sx), [sy] "s"(sx + XP_PAIR), [sm] "s"(sp + h * 128), [cs] "s"(BLK), [cp] "s"(XP_PAIR), [c70] "s"(0x70), [scl] "s"(SCL)
                         : "memory", "v200", "v201", "v202", "v203", "v204", "v205", "v206", "v207", "v208", "v209", "v210", "v211", "v212", "v213", "v214", "v215", "v216", "v217", "v218", "v219", "v220", "v221", "v222", "v223", "v224", "v225", "v226", "v227", "v228", "v229", "v230", "v231", "v232", "v233", "v234", "v235", "v236", "v237", "v238", "v239", "v240", "v241", "v242", "v243", "v244", "v245", "v246", "v247", "v248", "v249", "v250", "v251", "v252", "v253", "v254", "v255");
            ASM_ST_SAVE(4);
            ST(5);
        } else if constexpr (CLS == 1 || CLS == 4) {
            const int ln = lane_id(), l15 = ln & 15, l4 = ln >> 4;
            constexpr int mt = (CLS == 4);
            const unsigned qa = L_QX + mt * 6144 + ln * 16, kmo = L_KM + l4 * 128 + (ln & 7) * 16;
            const unsigned xo = mt ? (h & 3) * 1024 : 5120 + ((h >> 1) & 1) * 1024 + (h & 1) * 256;
            const unsigned pm = l4 < 2 ? (mt ? 4096 : 7168) + (h & 3) * 256 - xo + l15 * 16 + l4 * 8 : OOB;
            const unsigned so = RG_PX + slot * PX_SLOT + (h >> 2) * BLK + xo;
            f32x4 sd;
#pragma unroll
            for (int r = 0; r < 4; ++r) sd[r] = (l4 * 4 + r > l15) ? -3.0e38f : 0.f;
            ASM_ST_DECL;
#define P4_ARGS : "+{v[240:243]}"(sd) ASM_ST_OUT : WREGS(wf), [qa] "v"(qa), [km] "v"(kmo), [pm] "v"(pm), [rr] "s"(rr), [so] "s"(so), [scl] "s"(SCL) : "memory", "v200", "v201", "v202", "v203", "v204", "v205", "v206", "v207", "v208", "v209", "v210", "v211", "v212", "v213", "v214", "v215", "v216", "v217", "v218", "v219", "v220", "v221", "v222", "v223", "v224", "v225", "v226", "v227", "v228", "v229", "v230", "v231", "v232", "v233", "v234", "v235", "v236", "v237", "v238", "v239", "v244", "v245", "v246", "v247", "v248", "v249", "v250", "v251"
            if constexpr (mt) asm volatile(ASM_P4_1 P4_ARGS); else asm volatile(ASM_P4_0 P4_ARGS);
            ASM_ST_SAVE(4);
            ST(5);
        } else {
            bar();
            ST(4);
            if (CLS == 2 && L && t + 2 < NSEG && !__all(fv > (unsigned)t + 2)) xn_wait(P, rr, t + 2, cpy);
            if (CLS == 2) { int tt = t; asm volatile("" : "+s"(tt)); if (tt + 2 == XS0) init_wait(P, h, 1); }
            if (CLS == 3) LA();
            if (CLS == 3 && t == HM_T) hm_ = (unsigned)__builtin_amdgcn_s_memrealtime() - t0_;
            if (CLS == 3 && HM_C && t == 8) hc_ = (unsigned)__builtin_amdgcn_s_memtime();
            if (CLS == 3 && HM_C && t == 56) hm_ = ((unsigned)__builtin_amdgcn_s_memtime() - hc_) / 20u;
            {
                if (CLS == 3 && (wave == 4 || wave == 7)) {
                    if (t + 1 < NSEG) xdma(t + 1, 1, par);
                }
                ST(5);
            }
        }
        STFLUSH(t);
    }
    LF();
    if (CLS == 3 && wave == 5 && (h == HM_H || HM_H < 0) && L == HM_L) while ((unsigned)__builtin_amdgcn_s_memrealtime() - t0_ < HM_B + hm_) __builtin_amdgcn_s_sleep(1);
    };
    if (wave == 0) run(std::integral_constant<int, 0>{});
    else if (wave == 1) run(std::integral_constant<int, 1>{});
    else if (wave == 2) run(std::integral_constant<int, 4>{});
    else if (wave == 3) run(std::integral_constant<int, 2>{});
    else run(std::integral_constant<int, 3>{});
}

#undef STROLE
#define STROLE 8
__device__ __forceinline__ void h_role(const Params& P, int wave, int L, int i, int x) {
    const rsrc_t rp = mkrsrc(P.ws + PG0, PG_L);
    const char* xn = P.ws + PG0 + PG_XN + L * PG_LX;
    auto run = [&](auto Cc) {
    constexpr bool HI = decltype(Cc)::value;
    const int mt = (wave >> 1) & 1;
    WRegs wf;
    load_w3<(NTM & 16) != 0>(wf, i < 16 ? P.w[2] : P.w[0], wave, (i < 16 ? i * 2 + HI : (i - 16) * 4 + HI * 2) * 384, i >= 16);
    PLACE();
    auto xdma = [&](int seg, int hb) {
        const unsigned l16 = lane_id() * 16;
        const char* g = xn + pgb(seg, par);
        dma4(g + blin(mt * 20480 + hb * 8192 + (wave & 1) * 4096), l16, L_XA + mt * 16384 + hb * 8192 + (wave & 1) * 4096);
        dmaq<0>(g + blin(mt * 20480 + 16384) + hb * 2048 + (wave & 1) * 1024, l16, L_XB + mt * 4096 + hb * 2048 + (wave & 1) * 1024);
    };
    const unsigned qs = (HI ? L_KX : L_QX) + (wave & 1) * 3072 + mt * 8;
    const unsigned xa = L_XA + wave * 2048 + lane_id() * 16, wl = L_W + wave * 10240 + lane_id() * 16;
    const unsigned fp = RG_FH + (i >> 4) * BLK + ((i & 15) * 4 + (wave & 3)) * 128;
    const unsigned fb = L ? RG_FX + (cpy & 3) * 2048 : RG_FV + (i & 3) * 2048, fa = L ? 0 : HAH;
    unsigned go = 0, all = 0;
    STDECL;
    LDECL;
    for (int t = HT0; t <= NSEG; ++t) {
        ST(0);
        if constexpr (HI) {
            const unsigned l16 = lane_id() * 16;
            if (t > HT0) {
                u32x4 v[6];
                const int w4 = wave - 4;
#pragma unroll
                for (int k = 0; k < 6; ++k) v[k] = lds_ld<u32x4>(L_QX + ((k >> 1) * 8 + w4 * 2 + (k & 1)) * 1024 + l16);
                const unsigned so = pgb(t - 1, par) + PG_HV + i * 3 * BLK + w4 * 2048;
#pragma unroll
                for (int k = 0; k < 6; ++k) st128(v[k], rp, l16 + (k & 1) * 1024, so + (k >> 1) * BLK);
                if (!all) {
                    wait_vm<0>();
                    if (l16 < 512) __builtin_amdgcn_raw_buffer_store_b32(t, rr, l16 >> 2, fp, 16);
                }
            }
            ST(1);
            if (t == NSEG) break;
            if (!all) bar();
            xdma(t, 1);
        } else {
            if (t == NSEG) break;
            if (!go) {
                for (int spins = 0;; ++spins) {
                    if (__all(__builtin_amdgcn_raw_buffer_load_b32(rr, (lane_id() & 15) * 128, fb, 16) + fa > (unsigned)t)) break;
                    __builtin_amdgcn_s_sleep(HSL);
                    if (spin_fail(spins, P)) break;
                }
                asm volatile("" ::: "memory");
                xdma(t, 0); wait_vm<0>();
            }
            if (!all) bar();
        }
        ST(2);
        C1Regs C;
        const WRegs& W = wf;
        ASM_ST_DECL;
        if constexpr (HI) {
            const unsigned l4 = lane_id() * 4;
            asm volatile(ASM_XG_H_HI : XG_OUT ASM_ST_OUT : XG_IN, [rr] "s"(rr), [fp] "s"(fp), [tv] "s"(t), [fv] "v"((all && l4 < 128) ? l4 : OOB) : XG_CLOB);
        } else {
            asm volatile(ASM_XG_H_LO : XG_OUT ASM_ST_OUT : XG_IN : XG_CLOB);
            if (t + 1 == XS0) init_wait(P, wave, 1);
            const unsigned fv = __builtin_amdgcn_raw_buffer_load_b32(rr, (lane_id() & 15) * 128, fb, 16) + fa;
            go = __builtin_amdgcn_readfirstlane((t + 1 < NSEG && __all(fv > (unsigned)t + 1)) ? 1u : 0u);
            if (go) xdma(t + 1, 0);
            lds_st<unsigned>(L_MEM + wave * 4, go);
            wait_vm<0>();
        }
        ASM_ST_SAVE(3);
        ST(4);
        if (!HI) { LB(t); LA(); }
        PMARK(L && wave == 7, 6, t);
        bar();
        const u32x4 gv = lds_ld<u32x4>(L_MEM);
        all = __builtin_amdgcn_readfirstlane(gv[0] & gv[1] & gv[2] & gv[3]);
        ST(5);
        STFLUSH(t);
    }
    LF();
    };
    if (wave < 4) run(std::false_type{}); else run(std::true_type{});
}

#undef STROLE
#define STROLE 2
__device__ __forceinline__ void v_role(const Params& P, int wave, int L, int h, int x) {
    WRegs wf;
    STDECL;
    auto runv = [&](auto Cc) {
        load_w3<(NTM & 4) != 0>(wf, P.w[2], wave, h * 384, false);
        MARKX(!L && wave == 0, 6);
        PLACE();
        unsigned budget = 1u << 18;
        const unsigned wl = L_W + wave * 10240 + lane_id() * 16, dsto = c1_dst(wave, false);
        auto vfetch = [&](int seg, int tries) {
            const int ln = lane_id();
            for (int spins = 0; tries != -2; ++spins) {
                if (spins == tries) return false;
                const unsigned f = __builtin_amdgcn_raw_buffer_load_b32(rr, (ln & 3) * 128, RG_FH + (h >> 1) * 512, 16);
                if (__all(f > (unsigned)seg)) break;
                __builtin_amdgcn_s_sleep(4);
                if (spin_fail(spins, P)) break;
            }
            asm volatile("" ::: "memory");
            const char* g = P.ws + PG0 + pgb(seg, par) + PG_HV + (h >> 1) * 3 * BLK;
#pragma unroll
            for (int k = 0; k < 3; ++k) {
                const int f = (h & 1) * 12 + wave * 3 + k;
                dmaq<0>(g + (f >> 3) * BLK + (f & 7) * 1024, ln * 16, L_QX + (seg & 1) * 12288 + (wave * 3 + k) * 1024);
            }
            return true;
        };
#pragma unroll 1
        for (int t = 0; t < HT0; ++t) {
            if (L) xn_wait(P, rr, t, cpy);
            const unsigned l16 = lane_id() * 16;
            const char* g = P.ws + PG0 + PG_XN + L * PG_LX + pgb(t, par);
            dma4(g + blin(wave * 4096), l16, L_XA + wave * 4096); dma4(g + blin(20480 + wave * 4096), l16, L_XA + 16384 + wave * 4096);
            dmaq<0>(g + blin(16384) + wave * 1024, l16, L_XB + wave * 1024); dmaq<0>(g + blin(20480 + 16384) + wave * 1024, l16, L_XB + 4096 + wave * 1024);
            wait_vm<0>();
            bar();
            C1Regs C;
            const WRegs& W = wf;
            const unsigned xa = L_XA + wave * 2048 + l16, qs = L_QX + (wave & 1) * 3072 + ((wave >> 1) & 1) * 8 + t * 12288;
            asm volatile(ASM_XG_V : XG_OUT : XG_IN : XG_CLOB);
            if (t == 0) bar();
        }
        bool seen = false;
        unsigned tb2 = 0;
        for (int t = 0; t < NSEG; ++t) {
            if (t + 1 - V1A >= HT0 && t + 1 - V1A < NSEG) { vfetch(t + 1 - V1A, seen ? -2 : -1); wait_vm<0>(); }
            ST(7);
            bar();
            C1Regs C;
            asm volatile("ds_read_b128 v[200:203], %4\n\tds_read_b128 v[204:207], %4 offset:1024\n\tds_read_b128 v[228:231], %4 offset:2048\n\tds_read_b128 v[232:235], %4 offset:3072\n\t"
                         "v_mov_b64 v[224:225], 0\n\tv_mov_b64 v[226:227], 0\n\tv_mov_b64 v[244:245], 0\n\tv_mov_b64 v[246:247], 0\n\tv_mov_b64 v[248:249], 0\n\tv_mov_b64 v[250:251], 0\n\ts_waitcnt lgkmcnt(0)"
                         : XG_OUT : "v"(wl) : "memory");
            ST(6);
            while ((unsigned)__builtin_amdgcn_s_memtime() - tb2 < VTD) __builtin_amdgcn_s_sleep(1);
            unsigned fv = (lane_id() & 3) * 128;
            crit1<10 + decltype(Cc)::value>(wf, C, rr, RG_XM + (t & (NSLOT - 1)) * XM_SLOT + (cpy >> 1) * XM_PAIR + (cpy & 1) * BLK, wave, wl, dsto, budget, fv, RG_FH + (h >> 1) * 512 STARG);
            __builtin_amdgcn_s_setprio(0);
            bar();
            seen = __all(fv > (unsigned)t + 2 - V1A);
            tb2 = (unsigned)__builtin_amdgcn_s_memtime();
            ST(4);
            STC(5);
            STFLUSH(t);
        }
    };
    if (wave < 2) runv(std::integral_constant<int, 3>{});
    else if (wave < 4) runv(std::integral_constant<int, 2>{});
    else {
        const int w4 = wave - 4;
        const rsrc_t rd = mkrsrc(P.ws + PG0, PG_L);
        load_wo(wf, P.w[3], wave, h);
        PLACE();
        bar();
        bar();
        for (int t = -1; t < NSEG; ++t) {
            const unsigned slot = t & (NSLOT - 1);
            bar();
            ST(7);
#pragma unroll 1
            for (int n = V1A ? t + (t <= 0) : t + (t != 1) + (t == 0); n <= (V1A && t >= 0 ? t : t + 1) && n < NSEG; ++n) {
                const unsigned l16 = lane_id() * 16;
                const unsigned wl = L_W + wave * 10240 + l16, va = L_QX + (n & 1) * 12288 + l16;
                unsigned so = ((n & 7) << 22) + pgb(64 + (w4 >> 1), par) + ((w4 & 1) * 40 + (h >> 3)) * BLK + (h & 7) * 1024;
                asm volatile(ASM_VOX : [so] "+s"(so) : WREGS(wf), [l16] "v"(l16), [wl] "v"(wl), [va] "v"(va), [rd] "s"(rd) : "memory", "scc", "v200", "v201", "v202", "v203", "v204", "v205", "v206", "v207", "v208", "v209", "v210", "v211", "v212", "v213", "v214", "v215", "v216", "v217", "v218", "v219", "v220", "v221", "v222", "v223", "v224", "v225", "v226", "v227", "v228", "v229", "v230", "v231", "v232", "v233", "v234", "v235", "v236", "v237", "v238", "v239", "v240", "v241", "v242", "v243", "v244", "v245", "v246", "v247", "v248", "v249", "v250", "v251");
                if (V1A || n < 2) {
                    wait_vm<0>();
                    const unsigned l4 = lane_id() * 4;
                    if (l4 < 128) __builtin_amdgcn_raw_buffer_store_b32(n + 1, rr, l4, RG_FV + (w4 >> 1) * BLK + ((w4 & 1) * 32 + h) * 128, 16);
                }
            }
            if (t < 0) continue;
            ST(0);
            unsigned wl = L_W + wave * 10240 + lane_id() * 16;
            bf16x16 lw = lds_frag(wl, 1024);
            u32x32 cz;
            asm volatile("v_mov_b64 v[220:221], 0\n\tv_mov_b64 v[222:223], 0\n\tv_mov_b64 v[224:225], 0\n\tv_mov_b64 v[226:227], 0\n\tv_mov_b64 v[228:229], 0\n\tv_mov_b64 v[230:231], 0\n\tv_mov_b64 v[232:233], 0\n\tv_mov_b64 v[234:235], 0\n\tv_mov_b64 v[236:237], 0\n\tv_mov_b64 v[238:239], 0\n\tv_mov_b64 v[240:241], 0\n\tv_mov_b64 v[242:243], 0\n\tv_mov_b64 v[244:245], 0\n\tv_mov_b64 v[246:247], 0\n\tv_mov_b64 v[248:249], 0\n\tv_mov_b64 v[250:251], 0" : "={v[220:251]}"(cz));
            unsigned vo = ((wl >> 8) & 1) * (1 + (w4 & 1) * 2) * BLK + ((wl >> 9) & 1) * 1024 + (wl & 255);
            asm volatile("" : "+{v[212:219]}"(lw), "+{v[220:251]}"(cz), "+v"(wl), "+v"(vo));
            unsigned l16 = wl & 1023u;
            bar();
            wait_vm<0>();
            if (!V1A && l16 < 512 && t && t + 1 < NSEG) __builtin_amdgcn_raw_buffer_store_b32(t + 2, rr, l16 >> 2, RG_FV + (w4 >> 1) * BLK + ((w4 & 1) * 32 + h) * 128, 16);
            ST(1);
            const int sidx = (wl & 128u) ? 0xEEEE : 0x4444;
            unsigned so = RG_VM + slot * VM_SLOT + (unsigned)((w4 * 10 - (w4 & 1) + (h >> 4) * 2) * BLK + ((h >> 2) & 3) * 2048 + (h & 3) * 256);
            ST(3);
            ASM_ST_DECL;
            asm volatile(ASM_CRIT2 : [so] "+s"(so), "+{v[212:219]}"(lw), "+{v200}"(l16), "+{v[220:251]}"(cz) ASM_ST_OUT
                         : WREGS(wf), [wl] "v"(wl), [vo] "v"(vo), [sidx] "v"(sidx), [rr] "s"(rr)
                         : "memory", "scc", "v201", "v202", "v203", "v204", "v205", "v206", "v207", "v208", "v209", "v210", "v211");
            ASM_ST_SAVE(4);
            ST(5);
            STFLUSH(t);
        }
    }
}

#undef STROLE
#define STROLE 4
constexpr unsigned R_RED = 0;
constexpr unsigned R_PART = 16384;
__device__ __forceinline__ void r_role(const Params& P, int wave, int L, int r5, int x) {
    PLACE();
    const int jj = (r5 * 4 + cpy) ^ (r5 * 4 + x >= 6 ? 1 : 0);
    const int lane = lane_id(), l15 = lane & 15, l4 = lane >> 4;
    const unsigned l16 = lane * 16;
    const rsrc_t rd = mkrsrc(P.ws + PG0, PG_L);
    const int sidx = (l15 < 8) ? 0x4444 : 0xEEEE;
    unsigned offp[3];
    offp[0] = RG_XP + (cpy >> 1) * XP_PAIR + (cpy & 1) * BLK + (1 + (wave >> 2)) * 2 * BLK + (wave & 3) * 2048 + (PLAY ? (l4 >> 1) * 512 + (((l15 >> 2) & 1) + (l15 & 2)) * 128 + (l4 & 1) * 64 + (l15 >> 3) * 32 + (l15 & 1) * 16 : l16);
    offp[2] = (l15 < 8) ? RG_XP + (cpy >> 1) * XP_PAIR + (cpy & 1) * BLK + wave * 512 + l4 * 128 + l15 * 16 : OOB;
    const unsigned offv = RG_VM + ((jj >> 1) * 4 + (wave >> 2) * 2 + (jj & 1)) * BLK + (wave & 3) * 2048 + l16;
    const int j = jj * 2 + ((wave >> 1) & 1), fl = (wave & 1) * 32 + (lane & 31);
    const unsigned offo = RG_XM + (wave >> 2) * XM_PAIR + (lane >> 5) * BLK + (j & 31) * 256 + (j >> 5) * 2 * BLK + fl * 4;
    MARK(!L && jj == 0 && wave == 0, 5);
    {
        const int row = (lane >> 2) & 7, col = 16 * j + 4 * (lane & 3) + 2 * (wave & 1);
        __builtin_amdgcn_raw_buffer_store_b32(nosent(*(const unsigned*)(P.mem0 + row * DM + col)), rr, offo, 0, 16);
    }
    auto red_addr = [](int fl) { return R_RED + fl * 80 + (fl >> 4) * 16; };
    const unsigned rw0 = (lane >> 5) * 5248 + red_addr((((l15 >> 1) & 1) * 8 + 4 * (l4 & 1)) * 4 + (l15 >> 2)) + (l15 & 1) * 4 + (wave ^ (l15 >> 3)) * 8;
    const unsigned rrd = ((wave >> 1) & 1) * 5248 + red_addr(fl) + (lane >> 5) * 32;
    u32x32 vx;
    const unsigned fo = (lane & 3) * 128, fs = RG_FV + (jj / 10) * BLK + (((jj / 5) & 1) * 32 + wave * 4) * 128, vxo = pgb(64 + jj / 10, par) + (unsigned)((jj % 10) * 8 + (wave >> 1)) * BLK + (wave & 1) * 4096;
    const rsrc_t rp = mkrsrc(P.ws + PG0, PG_L);
    const unsigned xo = (l4 >> 1) * 256 + l15 * 16 + (l4 & 1) * 8, pw = R_PART + wave * 1024 + l16, pr = R_PART + (wave & 3) * 8192 + l16;
    const int fin = wave >> 2;
    asm volatile("" : "={v[128:159]}"(vx));
    auto vx_fetch = [&](int t) {
        for (int spins = 0;; ++spins) {
            const unsigned f = __builtin_amdgcn_raw_buffer_load_b32(rr, fo, fs, 16);
            if (__all(f > (unsigned)t)) break;
            __builtin_amdgcn_s_sleep(2);
            if (spin_fail(spins, P)) break;
        }
        const unsigned sv = ((t & 7) << 22) + vxo;
        asm volatile("buffer_load_dwordx4 v[128:131], %1, %2, %3 offen sc1\n\tbuffer_load_dwordx4 v[132:135], %1, %2, %3 offen offset:1024 sc1\n\t"
                     "buffer_load_dwordx4 v[136:139], %1, %2, %3 offen offset:2048 sc1\n\tbuffer_load_dwordx4 v[140:143], %1, %2, %3 offen offset:3072 sc1\n\t"
                     "buffer_load_dwordx4 v[144:147], %1, %2, %4 offen sc1\n\tbuffer_load_dwordx4 v[148:151], %1, %2, %4 offen offset:1024 sc1\n\t"
                     "buffer_load_dwordx4 v[152:155], %1, %2, %4 offen offset:2048 sc1\n\tbuffer_load_dwordx4 v[156:159], %1, %2, %4 offen offset:3072 sc1"
                     : "+{v[128:159]}"(vx) : "v"(l16), "s"(rp), "s"(sv), "s"(sv + 4 * BLK) : "memory");
    };
    vx_fetch(0);
    unsigned budget = 1u << 18, tpub = 0;
    STDECL;
    LDECL;
    for (int t = 0; t < NSEG; ++t) {
        const unsigned slot = t & (NSLOT - 1);
        ST(0);
        while ((unsigned)__builtin_amdgcn_s_memtime() - tpub < RTD) __builtin_amdgcn_s_sleep(1);
        u32x8 vm, pv;
        u32x4 pm;
        {
            unsigned a;
            ASM_ST_DECL;
            asm volatile(ASM_RCRIT : "=&{v[176:183]}"(vm), "=&{v[184:191]}"(pv), "=&{v[192:195]}"(pm), [a] "=&s"(a), [n] "+s"(budget) ASM_ST_OUT ASM_ST2_OUT ASM_ST3_OUT
                         : [rs] "s"(rr), [ov] "v"(offv), [sv] "s"(slot * VM_SLOT), [o0] "v"(offp[0]), [o2] "v"(offp[2]), [sp] "s"(slot * XP_SLOT),
                           "{v[128:159]}"(vx), [sidx] "v"(sidx), [rw] "v"(rw0), [rd] "v"(rrd), [oo] "v"(offo),
                           [so] "s"(((t + 1) & (NSLOT - 1)) * XM_SLOT), [pub] "s"(NSEG - 1 - t)
                         : "memory", "vcc", "scc", "v160", "v161", "v162", "v163", "v164", "v165", "v166", "v167", "v196", "v197", "v200", "v201", "v202", "v203", "v204", "v205", "v206", "v207");
            ASM_ST_SAVE(2); ASM_ST2_SAVE(1); ASM_ST3_SAVE(6);
        }
        ST(3);
        LA();
        MARK(!L && jj == 0 && wave == 0 && t < 4, 10 + t);
        PMARK(wave == 0, L * 7, t - 1 + L);
        tpub = (unsigned)__builtin_amdgcn_s_memtime();
        __builtin_amdgcn_s_sleep(RXDEL);
        unsigned early;
#pragma unroll 1
        for (int spins = 1023;; spins += 1024) {
            unsigned n;
            const unsigned sx = pgb(t, par) + blin(((wave >> 1) & 1) * 20480 + jj * 1024) + (wave & 1) * 512;
            const unsigned pxb = RG_PX + slot * PX_SLOT + wave * BLK, sv = (((t + 1) & 7) << 22) + vxo;
            ASM_ST_DECL;
            asm volatile(ASM_RX : "+{v[128:159]}"(vx), [n] "=&s"(n), [a] "=&s"(early) ASM_ST_OUT ASM_ST2_OUT
                         : "{v[176:183]}"(vm), "{v[184:191]}"(pv), "{v[192:195]}"(pm), [l16] "v"(l16), [rs] "s"(rr), [s0] "s"(pxb), [s1] "s"(pxb + 4096), [rd] "s"(rd), [rx] "s"(rd), [xo] "v"(xo),
                           [fo] "v"(fo), [fs] "s"(fs), [tn] "s"(t + 1), [sv0] "s"(sv), [sv1] "s"(sv + 4 * BLK), [rp] "s"(rp), [sx] "s"(PG_XR + L * PG_LX + sx), [sa] "s"(PG_XA + sx),
                           [pw] "v"(pw), [pr] "v"(pr), [fin] "s"(fin)
                         : "memory", "vcc", "scc", "v96", "v97", "v98", "v99", "v100", "v101", "v102", "v103", "v104", "v105", "v106", "v107", "v108", "v109", "v110", "v111", "v112", "v113", "v114", "v115", "v116", "v117", "v118", "v119", "v120", "v121", "v122", "v123", "v124", "v125", "v126", "v127", "v196", "v200", "v201", "v202", "v203", "v204", "v205", "v206", "v207", "v208", "v209", "v210", "v211", "v212", "v213", "v214", "v215", "v216", "v217", "v218", "v219", "v220", "v221", "v222", "v223", "v224", "v225", "v226", "v227", "v228", "v229", "v230", "v231", "v240", "v241", "v242", "v243", "v248", "v250", "v251");
            ASM_ST_SAVE(4); ASM_ST2_SAVE(5);
            if (__builtin_amdgcn_readfirstlane(n)) break;
            early = 0;
            if (spin_fail(spins, P)) { bar(); break; }
        }
        if (t + 1 < NSEG && !__builtin_amdgcn_readfirstlane(early)) vx_fetch(t + 1);
        PMARK(!L && wave == 7 && PMK == 1, 1, t - 1);
        st128(sent4(), rr, offv, slot * VM_SLOT); st128(sent4(), rr, offv + 1024, slot * VM_SLOT);
        __builtin_amdgcn_raw_buffer_store_b32(SENT, rr, offo, slot * XM_SLOT, 16);
        if (wave < 2 && t + AH < NSEG) st128(sent4(), rd, l16, PG_XA + pgb(t + AH, par) + blin(wave * 20480 + jj * 1024));
        ST(7);
        LB(t);
        STFLUSH(t);
    }
    LF();
}

#undef STROLE
#define STROLE 16
template <int WHICH>
__device__ __forceinline__ void w_role(const Params& P, int wave, int L, int cu, int x) {
    constexpr int KS = 20, N = (WHICH == 3) ? FF : DM, NU = 2 * NSEG;
    constexpr unsigned D_IN = WHICH == 1 ? PG_XA : WHICH == 2 ? PG_HS : PG_X1;
    constexpr unsigned D_OUT = WHICH == 1 ? PG_HS : WHICH == 2 ? PG_X1 : PG_H2;
    if (WHICH != 3 && cu * 8 + wave >= 20) {
        for (int u = 0; u <= NU; ++u) bar();
        return;
    }
    auto run = [&](auto Cc) {
    constexpr bool LO = decltype(Cc)::value;
    const int ln = lane_id(), l15 = ln & 15, l4 = ln >> 4;
    const unsigned l16 = ln * 16;
    const rsrc_t rd = mkrsrc(P.ws + PG0, PG_L);
    const int nt0 = (cu * 8 + wave) * 2;
    u32x32 w[5];
    {
        u32x4 f[2 * KS];
        load_wn<2, KS, N>(f, P.w[3 + WHICH], DM, wave, nt0, 0);
#pragma unroll
        for (int n = 0; n < 5; ++n) {
            u32x8 q[4];
#pragma unroll
            for (int i = 0; i < 4; ++i) q[i] = __builtin_shufflevector(f[n * 8 + 2 * i], f[n * 8 + 2 * i + 1], 0, 1, 2, 3, 4, 5, 6, 7);
            w[n] = cat32(q[0], q[1], q[2], q[3]);
        }
        asm volatile("" : "+{v[0:31]}"(w[0]), "+{v[32:63]}"(w[1]), "+{v[64:95]}"(w[2]), "+{v[96:127]}"(w[3]), "+{v[128:159]}"(w[4]));
    }
    PLACE();
    bar();
    const int ocol = nt0 * 16 + (l4 & 1) * 16 + (l4 >> 1) * 8;
    const unsigned o3 = (unsigned)((l4 & 1) * 512 + (l4 >> 1) * 256 + l15 * 16), ro = (l4 >> 1) * 256 + l15 * 16 + (l4 & 1) * 8;
    const int grp = nt0 / 10, sh = ((grp & 1) ? -1 : 1) * ((grp >> 1) + 1);
    if (WHICH == 1) {
        unsigned zv;
        asm volatile("v_mov_b32 %0, 0" : "=v"(zv));
        st128(((u32x4){zv, zv, zv, zv}), rd, l15 <= (grp >> 1) ? toff((grp & 1) ? S_ - 1 - l15 : l15, ocol, par) : OOB, PG_HS);
    }
    auto in0 = [&](int u) { return D_IN + pgb(u >> 1, par) + blin((u & 1) * 20480 + wave * 4096); };
    auto in1 = [&](int u) { return D_IN + pgb(u >> 1, par) + blin((u & 1) * 20480 + 16384) + wave * 1024; };
    u32x16 va;
    u32x4 vb;
    u32x8 cs, k8;
#define WT_W "{v[0:31]}"(w[0]), "{v[32:63]}"(w[1]), "{v[64:95]}"(w[2]), "{v[96:127]}"(w[3]), "{v[128:159]}"(w[4])
    asm volatile("v_mov_b32 v216, 0x3f803f80\n\tv_mov_b32 v217, v216\n\tv_mov_b32 v218, v216\n\tv_mov_b32 v219, v216\n\tv_mov_b32 v220, %[c0]\n\tv_mov_b32 v232, -1\n\tv_mov_b32 v236, -1\n\t"
                 "v_mov_b32 v240, -1\n\tv_mov_b32 v244, -1\n\tv_mov_b32 v248, -1\n\t" ASM_WCS
                 : "=&{v[196:203]}"(cs), "=&{v[216:223]}"(k8), "=&{v[232:247]}"(va), "=&{v[248:251]}"(vb) : WT_W, [c0] "s"(-2.0f * 0.7978845608028654f * 1.4426950408889634f));
    unsigned la = l16, lw = wave * 8192 + l16, sr = l4 * 8192 + ((l15 >> 2) * 16 + l15) * 16 + (l15 & 3) * 4, budget = 1u << 18;
    wait_vm<0>();
    STDECL;
    LDECL;
#pragma unroll 1
    for (int u = 0; u < NU; ++u) {
        const int mt = u & 1;
        const unsigned pg = pgb(u >> 1, par), c3 = D_OUT + blin(mt * (N * 32) + (nt0 >> 1) * 1024);
        unsigned oa = o3, ob = 0, so = pg + c3, sa = u + 2 * AH < NU ? pgb((u >> 1) + AH, par) + c3 : pg + PG_SS + 4096, a;
        if (WHICH == 1) {
            const int orow = u * 16 + l15 + sh;
            ob = toff(orow + AH * SEG, ocol, par);
            oa = (unsigned)orow < (unsigned)S_ ? (ob ^ 8192u) - (AH << 21) : OOB;
            if (orow + AH * SEG >= S_ + (sh < 0 ? sh : 0)) ob = OOB;
            so = sa = D_OUT;
        }
        if (mt) { ST(0); LB(u >> 1); } else { LA(); }
        ASM_ST_DECL;
#define WT_A WT_W, [la] "v"(la), [l16] "v"(l16), [oa] "v"(oa), [rs] "s"(rd), [so] "s"(so), [sa] "s"(sa)
#define WT_G [sr] "v"(sr), [kd] "s"(1.0f / DM), [ke] "s"(1e-5f), [k1] "s"(-2.0f * 0.7978845608028654f * 1.4426950408889634f * 0.044715f)
#define WT_1 WT_G, [ob] "v"(ob), [ss] "s"(pg + PG_SS + mt * 128), [sw] "s"(nt0)
#define WT_2 [ro] "v"(ro), [sx] "s"(pg + PG_XA + blin(mt * 20480 + nt0 * 512)), [ss] "s"(pg + PG_SS + mt * 128)
#define WT_C "memory", "vcc", "scc", "v160", "v161", "v162", "v163", "v164", "v165", "v166", "v167", "v168", "v169", "v170", "v171", "v172", "v173", "v174", "v175", "v176", "v177", "v178", "v179", "v180", "v181", "v182", "v183", "v184", "v185", "v186", "v187", "v188", "v189", "v190", "v191", "v192", "v193", "v194", "v195", "v204", "v205", "v206", "v207", "v208", "v209", "v210", "v211", "v212", "v213", "v214", "v215", "v252"
#define WT_LO(A, ...) asm volatile(A : "+{v[196:203]}"(cs), "+{v[216:223]}"(k8), "+{v[232:247]}"(va), "+{v[248:251]}"(vb), [n] "+s"(budget), [a] "=&s"(a) ASM_ST_OUT ASM_ST2_OUT ASM_ST3_OUT \
                              : WT_A, __VA_ARGS__, [lw] "v"(lw), [i0] "s"(in0(u)), [i1] "s"(in1(u)), [n0] "s"(in0(u + 1)), [n1] "s"(in1(u + 1)) : WT_C)
#define WT_HI(A, ...) asm volatile(A : "+{v[196:203]}"(cs), "+{v[216:223]}"(k8) ASM_ST_OUT ASM_ST3_OUT : WT_A, __VA_ARGS__ : WT_C)
        if constexpr (LO) { if constexpr (WHICH == 1) WT_LO(ASM_WT1_LO, WT_1); else if constexpr (WHICH == 2) WT_LO(ASM_WT2_LO, WT_2); else WT_LO(ASM_WT3_LO, WT_G); }
        else { if constexpr (WHICH == 1) WT_HI(ASM_WT1_HI, WT_1); else if constexpr (WHICH == 2) WT_HI(ASM_WT2_HI, WT_2); else WT_HI(ASM_WT3_HI, WT_G); }
        la ^= 32768u; lw ^= 32768u; sr ^= 32768u;
        PMARK(!L && mt == (WHICH != 1) && (PMK == 1 || WHICH == 3), PMK == 1 ? 1 + WHICH : 1, (u >> 1) - (WHICH == 1));
    }
    wait_vm<0>();
    if constexpr (LO) asm volatile("" :: "{v[232:247]}"(va), "{v[248:251]}"(vb));
    LF();
    };
    if (wave < 4) run(std::true_type{}); else run(std::false_type{});
}

__device__ __forceinline__ void w4_role(const Params& P, int wave, int L, int cu, int x) {
    constexpr unsigned W_FL = 122880;
    const int ln = lane_id(), l15 = ln & 15, l4 = ln >> 4;
    const unsigned l16 = ln * 16;
    const rsrc_t rd = mkrsrc(P.ws + PG0, PG_L);
    lds_st<unsigned>(W_FL + wave * 4, 0u);
    STDECL;
    if (wave < 6) {
        u32x32 w[5];
        {
            u32x4 g[40];
            load_wn<5, 8, DM>(g, P.w[7], FF, wave, cu * 5, wave * 8);
#pragma unroll
            for (int n = 0; n < 5; ++n) {
                u32x8 f[4];
#pragma unroll
                for (int i = 0; i < 4; ++i) f[i] = __builtin_shufflevector(g[n * 8 + 2 * i], g[n * 8 + 2 * i + 1], 0, 1, 2, 3, 4, 5, 6, 7);
                w[n] = cat32(f[0], f[1], f[2], f[3]);
            }
            asm volatile("" : "+{v[0:31]}"(w[0]), "+{v[32:63]}"(w[1]), "+{v[64:95]}"(w[2]), "+{v[96:127]}"(w[3]), "+{v[128:159]}"(w[4]));
        }
        PLACE();
        bar();
        unsigned budget = 1u << 18;
        u32x32 av;
        asm volatile("v_mov_b32 v160, -1\n\tv_mov_b32 v164, -1\n\tv_mov_b32 v168, -1\n\tv_mov_b32 v172, -1\n\tv_mov_b32 v176, -1\n\tv_mov_b32 v180, -1\n\tv_mov_b32 v184, -1\n\tv_mov_b32 v188, -1" : "={v[160:191]}"(av));
        for (int u = 0; u < 2 * NSEG; ++u) {
            if (u & 1) ST(0);
            for (int spins = 0;; ++spins) {
                asm volatile("" ::: "memory");
                if ((int)__builtin_amdgcn_readfirstlane(lds_ld<unsigned>(W_FL + 24 + (u & 1) * 4)) + 3 >= u) break;
                __builtin_amdgcn_s_sleep(2);
                if (spin_fail(spins, P)) break;
            }
            const unsigned gb = PG_H2 + pgb(u >> 1, par) + ((u & 1) * 6 + wave) * BLK, gn = PG_H2 + pgb((u + 1) >> 1, par) + ((~u & 1) * 6 + wave) * BLK;
            ASM_ST_DECL;
            asm volatile(ASM_W4_M : [n] "+s"(budget), "+{v[160:191]}"(av) ASM_ST2_OUT
                         : "{v[0:31]}"(w[0]), "{v[32:63]}"(w[1]), "{v[64:95]}"(w[2]), "{v[96:127]}"(w[3]), "{v[128:159]}"(w[4]),
                           [ld] "v"(l16 + wave * 1024 + (u & 3) * 30720), [l16] "v"(l16), [rs] "s"(rd), [s0] "s"(gb), [s1] "s"(gb + 4096), [n0] "s"(gn), [n1] "s"(gn + 4096), [un] "s"(u + 1), [fl] "v"(W_FL + wave * 4)
                         : "memory", "vcc", "scc", "v192", "v193", "v194", "v195", "v196", "v197", "v198", "v199", "v200", "v201", "v202", "v203", "v204", "v205", "v206", "v207", "v208", "v209", "v210", "v211", "v212");
            PMARK(!L && (u & 1) && PMK == 2, 2, u >> 1);
        }
        asm volatile("s_waitcnt vmcnt(0)" :: "{v[160:191]}"(av) : "memory");
    } else {
        const int mt = wave - 6;
        PLACE();
        bar();
        const unsigned c0 = mt * 20480 + cu * 2560, o8 = (l4 >> 1) * 256 + l15 * 16 + (l4 & 1) * 8;
        unsigned budget = 1u << 18;
        u32x16 ra;
        u32x4 rb;
        asm volatile("v_mov_b32 v164, -1" : "={v[164:179]}"(ra), "={v[180:183]}"(rb));
        __builtin_amdgcn_s_setprio(W4P);
#define W4F_OUT "+{v[164:179]}"(ra), "+{v[180:183]}"(rb), [n] "+s"(budget)
#define W4F_IN [o0] "v"(o8 + blin(c0)), [o1] "v"(o8 + blin(c0 + 512)), [o2] "v"(o8 + blin(c0 + 1024)), [o3] "v"(o8 + blin(c0 + 1536)), [o4] "v"(o8 + blin(c0 + 2048)), [rd] "s"(rd), \
               [c1] "s"(PG_X1 + pg), [ca] "s"(PG_XA + pg), [n1] "s"(PG_X1 + pn), [na] "s"(PG_XA + pn), [f0] "v"(W_FL + (ln & 3) * 4), [f1] "v"(W_FL + 16 + (ln & 1) * 4), [fd] "v"(W_FL + wave * 4), \
               [la] "v"((u & 3) * 30720 + l16), [u] "s"(u), [un] "s"(u + 1)
#define W4F_CLOB "memory", "vcc", "scc", "v64", "v65", "v66", "v67", "v68", "v69", "v70", "v71", "v72", "v73", "v74", "v75", "v76", "v77", "v78", "v79", "v80", "v81", "v82", "v83", "v84", "v85", "v86", "v87", "v88", "v89", "v90", "v91", "v92", "v93", "v94", "v95", "v96", "v97", "v98", "v99", "v100", "v101", "v102", "v103", "v104", "v105", "v106", "v107", "v108", "v109", "v110", "v111", "v112", "v113", "v114", "v115", "v116", "v117", "v118", "v119", "v120", "v121", "v122", "v123", "v124", "v125", "v126", "v127", "v128", "v129", "v130", "v131", "v132", "v133", "v134", "v135", "v136", "v137", "v138", "v139", "v140", "v141", "v142", "v143", "v144", "v145", "v146", "v147", "v148", "v149", "v150", "v151", "v152", "v153", "v154", "v155", "v156", "v157", "v158", "v159", "v160", "v161", "v162", "v163", "v184", "v185", "v186", "v187", "v188", "v189", "v190", "v191", "v192", "v193", "v194", "v195", "v196", "v197", "v198", "v199", "v200"
        if (L) {
            const rsrc_t ro = mkrsrc(P.out, ACT);
#pragma unroll 1
            for (int t = 0; t < NSEG; ++t) {
                const unsigned u = t * 2 + mt, pg = pgb(t, par), pn = pgb(t + 1, par);
                ASM_ST_DECL;
                asm volatile(ASM_W4F_1 : W4F_OUT ASM_ST2_OUT : W4F_IN, [pa] "v"(l15 * (DM * 2) + (l4 & 1) * 32 + (l4 >> 1) * 16), [pb] "v"(l15 * (DM * 2) + l4 * 8), [ro] "s"(ro), [so] "s"(u * 20480 + cu * 160) : W4F_CLOB);
            }
        } else {
            const rsrc_t rn = mkrsrc(P.ws + (par ^ 1) * 8192, RG_L);
#pragma unroll 1
            for (int t = 0; t < NSEG; ++t) {
                const unsigned u = t * 2 + mt, pg = pgb(t, par), pn = pgb(t + 1, par), sn = (pg ^ 8192) + PG_LX, st = RG_ST + (u >> 3) * BLK + (u & 7) * 1024;
                ASM_ST_DECL;
                asm volatile(ASM_W4F_0 : W4F_OUT ASM_ST_OUT ASM_ST2_OUT ASM_ST3_OUT
                             : W4F_IN, [pa] "v"(blin(c0 + (l4 & 1) * 512) + (l4 >> 1) * 256 + l15 * 16), [pb] "v"(blin(c0 + 1024 + (l4 & 1) * 512) + (l4 >> 1) * 256 + l15 * 16),
                               [so] "v"(ln < 16 ? ln * 8 : OOB), [si] "v"(ln * 8), [rr] "s"(rr), [sp] "s"(st + cu * 128), [sq] "s"(st), [xr] "s"(PG_XR + sn), [xn] "s"(PG_XN + sn),
                               [kd] "s"(1.0f / DM), [ke] "s"(1e-5f), [tn] "s"(t + 1), [fo] "v"(ln < 32 ? (ln >> 3) * 2048 + (ln & 7) * 16 : OOB), [rn] "s"(rn), [fs] "s"(RG_FX + (cu * 2 + mt) * 128) : W4F_CLOB);
            }
        }
        asm volatile("s_waitcnt vmcnt(0)" :: "{v[164:179]}"(ra), "{v[180:183]}"(rb) : "memory");
    }
}

__device__ __forceinline__ unsigned probe(char* ws, unsigned base, unsigned bit, int n) {
    const rsrc_t r = mkrsrc(ws, RG_SLOT);
    unsigned m0 = ~0u, m1 = ~0u;
    for (int i = 0; i < n; ++i) {
        const unsigned long long t0 = __builtin_amdgcn_s_memtime();
        __builtin_amdgcn_raw_buffer_store_b32(0u, r, 96 * BLK + base + (i & 1) * bit + ((i >> 2) & 1) * 4096 + (i >> 2) * 768, 0, 16);
        asm volatile("s_waitcnt vmcnt(0)" ::: "memory");
        const unsigned d = (unsigned)(__builtin_amdgcn_s_memtime() - t0);
        if (i & 1) m1 = d < m1 ? d : m1; else m0 = d < m0 ? d : m0;
    }
    return m0 > m1 ? 1u : 0u;
}
__device__ __forceinline__ void setup(const Params& P, int wave, int mt, int par0, int ph) {
    const bool xw = wave < 4 && (mt >= 2 * XS0) == (ph != 0);
    const int tid = threadIdx.x & 255, r15 = tid & 15, slot = tid >> 4;
    const rsrc_t rw = mkrsrc(P.ws, WS_END);
    u32x4 v[5];
    float s = 0.f, q = 0.f;
    if (xw) {
#pragma unroll
        for (int c = 0; c < 5; ++c) v[c] = *(const u32x4*)(P.x + (size_t)(mt * 16 + r15) * DM + (slot * 5 + c) * 8);
#pragma unroll
        for (int c = 0; c < 5; ++c)
#pragma unroll
            for (int e = 0; e < 4; ++e) s += lo2f(v[c][e]) + hi2f(v[c][e]);
        lds_st<float>((slot * 17 + r15) * 4, s);
    } else if (wave >= 4) {
        constexpr unsigned N0 = RG_ST / BLK, N1 = 4 * N0 + 16, N2 = PG_SS / BLK;
        const u32x4 s4 = sent4();
        for (unsigned k = mt; k < 2 * (N1 + 4 * N2); k += 128) {
            const unsigned i = k >> 1;
            const unsigned o = i < N1 ? (i < 4 * N0 ? (i / N0) * RG_SLOT + (i % N0) * BLK : (i - 3 * N0) * BLK) + (par0 ^ ph) * 8192 : PG0 + pgb((i - N1) / N2, par0 ^ ph) + ((i - N1) % N2) * BLK;
            st128(s4, rw, tid * 16, o + (k & 1) * 4096);
        }
        if (ph == 0 && mt >= 108) st128(~s4, rw, tid * 16, RG_FV + (mt - 108) * 4096);
    }
    bar();
    float mean = 0.f;
    if (xw) {
        s = 0.f;
#pragma unroll
        for (int k = 0; k < 16; ++k) s += lds_ld<float>((k * 17 + r15) * 4);
        mean = s * (1.0f / DM);
#pragma unroll
        for (int c = 0; c < 5; ++c)
#pragma unroll
            for (int e = 0; e < 4; ++e) { const float a = lo2f(v[c][e]) - mean, b = hi2f(v[c][e]) - mean; q += a * a + b * b; }
        lds_st<float>(2048 + (slot * 17 + r15) * 4, q);
    }
    bar();
    if (xw) {
        q = 0.f;
#pragma unroll
        for (int k = 0; k < 16; ++k) q += lds_ld<float>(2048 + (k * 17 + r15) * 4);
        const float m1 = rbf(mean), r = 1.0f / ln_den(q * (1.0f / DM));
#pragma unroll
        for (int c = 0; c < 5; ++c) {
            const unsigned o = toff(mt * 16 + r15, (slot * 5 + c) * 8, par0);
            st128(v[c], rw, o, PG0 + PG_XR);
            u32x4 n;
#pragma unroll
            for (int e = 0; e < 4; ++e) n[e] = pack2(rbf(lo2f(v[c][e]) - m1) * r, rbf(hi2f(v[c][e]) - m1) * r);
            st128(n, rw, o, PG0 + PG_XN);
        }
    }
    wait_vm<0>();
    bar();
    if (threadIdx.x < 8) __hip_atomic_fetch_add((unsigned*)(P.ws + FL0 + FL_IN + (ph * 8 + threadIdx.x) * 128), 1u, RLX_AGENT);
}

__global__ __launch_bounds__(512, 2) void mega_kernel(Params P) {
    const unsigned t0_ = (unsigned)__builtin_amdgcn_s_memrealtime();
    const int wave = __builtin_amdgcn_readfirstlane(threadIdx.x >> 6);
    u32x4 cpf;
    {
        unsigned long long pc;
        unsigned len;
        asm volatile("s_getpc_b64 %0\n\ts_mov_b32 %1, _Z8code_endv-_Z11mega_kernel6Params" : "=s"(pc), "=s"(len));
        cpf = __builtin_amdgcn_raw_buffer_load_b128(mkrsrc((const void*)(pc & ~255ull), len), (blockIdx.x >> 3) * 8192 + threadIdx.x * 16, 0, 0);
    }
    {
        const rsrc_t rf = mkrsrc(P.ws + FL0, FL_BYTES);
        if (blockIdx.x == 0) {
            unsigned h = 0;
            if (wave == 7) h = (probe(P.ws, 0, 8192, CAL1) ^ ((unsigned)__builtin_amdgcn_s_getreg((3 << 11) | 20) >> 2)) & 1u;
            if (wave == 0) {
                const unsigned l16 = lane_id() * 16;
                u32x4 z = {0u, 0u, 0u, 0u};
                asm volatile("" : "+v"(z));
#pragma unroll
                for (int k = 0; k < 4; ++k) st128(z, rf, k * 1024 + l16 < FL_HB ? k * 1024 + l16 : OOB, 0);
                wait_vm<0>();
                for (unsigned c = 1; c < 200000u; ++c) {
                    __builtin_amdgcn_raw_buffer_store_b32(c, rf, l16 < 128 ? l16 * 8 : OOB, FL_HB, 16);
                    unsigned n = __builtin_amdgcn_raw_buffer_load_b32(rf, (l16 & 127u) * 8, FL_TK, 16), sum = 0;
                    n = n < 32u ? n : 32u;
#pragma unroll
                    for (int k = 0; k < 8; ++k) sum += __builtin_amdgcn_readlane(n, k);
                    if (sum >= 255u) break;
                }
            }
            bar();
            if (wave == 7 && lane_id() < 16) __hip_atomic_fetch_add((unsigned*)(P.ws + FL0 + FL_IN + (threadIdx.x & 15) * 128), (2u + h) << 16, RLX_AGENT);
        } else if (wave == 0) {
            unsigned o;
            asm volatile("v_mov_b32 %0, 0" : "=v"(o));
            const unsigned v0 = __builtin_amdgcn_readfirstlane(__builtin_amdgcn_raw_buffer_load_b32(rf, o, FL_HB + (blockIdx.x & 7) * 128, 16));
            for (int spins = 0; spins < 400000; ++spins) {
                asm volatile("" ::: "memory");
                if (__builtin_amdgcn_readfirstlane(__builtin_amdgcn_raw_buffer_load_b32(rf, o, FL_HB + (blockIdx.x & 7) * 128, 16)) != v0) break;
                __builtin_amdgcn_s_sleep(1);
            }
            asm volatile("" ::: "memory");
        }
    }
    if (threadIdx.x == 0) {
        unsigned g = (unsigned)__builtin_amdgcn_s_getreg((3 << 11) | 20) & 7u, k;
        for (int n = 0; (k = __hip_atomic_fetch_add((unsigned*)(P.ws + FL0 + FL_TK + g * 128), 1u, RLX_AGENT)) >= 32u && n < 8; ++n) g = (g + 1) & 7u;
        lds_st<unsigned>(0, g << 5 | (k & 31u));
    }
    bar();
    const unsigned tk = __builtin_amdgcn_readfirstlane(lds_ld<unsigned>(0));
    asm volatile("" :: "v"(cpf));
    bar();
    const int g4 = (tk >> 5) & 3, k = tk & 31;
    const int L = tk >> 7, x = g4, r = k;
    if (L) {
        const int par0 = (init_wait(P, wave, 1, 0x20000u) >> 16) & 1;
        if (tk == 254 && wave == 0) {
            const unsigned xc = (unsigned)__builtin_amdgcn_s_getreg((3 << 11) | 20);
            const unsigned f = (probe(P.ws, ((par0 ^ (xc >> 2)) & 1) * 8192, BLK, CAL2) ^ 1u ^ xc) & 1u;
            if (threadIdx.x < 16) __hip_atomic_fetch_add((unsigned*)(P.ws + FL0 + FL_IN + threadIdx.x * 128), (2u + f) << 20, RLX_AGENT);
        }
#pragma unroll 1
        for (int ph = 0; ph < 2; ++ph) {
            setup(P, wave, (tk & 31) * 4 + g4, par0, ph);
            for (int k = ph * DLY_L; k < DLY_L; ++k) __builtin_amdgcn_s_sleep(32);
        }
    }
    {
        const int i = (r - 26) * 4 + x;
        const int d = (r < 8 ? DLY_S : r < 21 ? 0 : i < 4 ? DLY_H : DLY_W + DLY_X * (i < 7 ? 0 : i < 10 ? 1 : i < 16 ? 2 : 3));
        for (int k = 0; k < d; ++k) __builtin_amdgcn_s_sleep(32);
    }
    if (r < 8) s_role(P, wave, L, x * 8 + r, x, t0_);
    else if (r < 16) v_role(P, wave ^ 4, L, x * 8 + r - 8, x);
    else if (r < 21) r_role(P, wave, L, r - 16, x);
    else {
        const int i = (r - 26) * 4 + x;
        if (i < 4) h_role(P, wave, L, r < 25 ? x * 4 + r - 21 : 16 + x * 2 + r - 25, x);
        else if (i < 7) w_role<1>(P, wave, L, i - 4, x);
        else if (i < 10) w_role<2>(P, wave, L, i - 7, x);
        else if (i < 16) w_role<3>(P, wave, L, i - 10, x);
        else w4_role(P, wave, L, i - 16, x);
    }
}

__global__ void code_end() {}

extern "C" void kernel_launch(void* const* d_in, const int* in_sizes, int n_in, void* d_out, int out_size, void* d_ws,
                              size_t ws_size, hipStream_t stream) {
    char* ws = (char*)d_ws;
    Params P;
    P.ws = ws; P.x = (const bf16_t*)d_in[0]; P.mem0 = (const bf16_t*)d_in[1]; P.out = (bf16_t*)d_out;
    for (int i = 0; i < 8; ++i) P.w[i] = (const bf16_t*)d_in[i < 4 ? 2 + i : 4 + i];
    (void)hipFuncSetAttribute((const void*)mega_kernel, hipFuncAttributeMaxDynamicSharedMemorySize, MEGA_LDS);
    mega_kernel<<<256, 512, MEGA_LDS, stream>>>(P);
}
```

```cpp
#define HM_L 0
#define HM_T 4
#define HM_C 0
#define HM_H 0
#define HM_B 30000u
#include <hip/hip_runtime.h>
#include <stdint.h>
#include <type_traits>

typedef unsigned short bf16_t;
using bf16x2 = __attribute__((ext_vector_type(2))) __bf16;
using bf16x8 = __attribute__((ext_vector_type(8))) __bf16;
using bf16x16 = __attribute__((ext_vector_type(16))) __bf16;
using f32x2 = __attribute__((ext_vector_type(2))) float;
using f32x4 = __attribute__((ext_vector_type(4))) float;
using u32x2 = __attribute__((ext_vector_type(2))) unsigned;
using u32x4 = __attribute__((ext_vector_type(4))) unsigned;
typedef __amdgpu_buffer_rsrc_t rsrc_t;

__device__ __forceinline__ unsigned pack2(float a, float b) {
    bf16x2 r = __builtin_convertvector((f32x2){a, b}, bf16x2);
    return __builtin_bit_cast(unsigned, r);
}
__device__ __forceinline__ float rbf(float f) { return __uint_as_float(pack2(f, 0.f) << 16); }
__device__ __forceinline__ float lo2f(unsigned u) { return __uint_as_float(u << 16); }
__device__ __forceinline__ float hi2f(unsigned u) { return __uint_as_float(u & 0xFFFF0000u); }
__device__ __forceinline__ rsrc_t mkrsrc(const void* p, unsigned bytes) {
    return __builtin_amdgcn_make_buffer_rsrc((void*)p, 0, bytes, 0x00020000);
}

constexpr int S_ = 2048, DM = 640, NH = 32, DH = 192, HD = NH * DH  , SEG = 32, NSEG = 64;
constexpr int FF = 1536;

#define XMAP
#define NCPY 4
constexpr unsigned NSLOT = 4, NCP = NCPY;
#define FL_TK_DEF
#define DLY_L 12
#define DLY_S 0
#define DLY_W 10
#define DLY_H 3
#define EWAKE "s_nop 0"
#define LWAKE "s_wakeup\n\t"
#define SLP4 (wave == 4 ? t : 0)
#define WPF 12
#define WPRI 1
#define WPRI2 0
#define RTD 3500
#define VTD 3900
#define HAH 100
#define WEAK 0
#define BAL 0
#define HSL 8
#define OAUX 2
#define W4P 3
#define STD 3800
#define CAL1 6
#define CAL2 12
#define DLY_X 2
#define NTM 10
#define MPF 0
#define CALN 12
#define RSL 9
#define VTRY 0
#define VSL 1
#define NRR 5
constexpr unsigned BLK = 16384, RG_SLOT = 4u << 20;
constexpr unsigned XP_PAIR = 6 * BLK, XP_SLOT = RG_SLOT;
constexpr unsigned XM_PAIR = 4 * BLK, XM_SLOT = RG_SLOT;
constexpr unsigned VM_SLOT = RG_SLOT;
constexpr unsigned PX_SLOT = RG_SLOT;
constexpr unsigned RG_XP = 0, RG_XM = 12 * BLK, RG_VM = 20 * BLK, RG_PX = 60 * BLK;
constexpr unsigned RG_ST = 68 * BLK, RG_FV = 84 * BLK, RG_FH = 86 * BLK, RG_FX = 88 * BLK, RG_L = NSLOT * RG_SLOT;
constexpr unsigned FL0 = 89 * BLK, FL_TK = 512, FL_IN = FL_TK + 8 * 128, FL_HB = FL_IN + 16 * 128, FL_BYTES = FL_HB + 8 * 128;
constexpr unsigned ACT = S_ * DM * 2;
#define PXROT 5
#define W4SL 5
#define RXDEL 1
#define VXD 32
#define VXAUX 16
constexpr unsigned PG0 = RG_L, PG_L = 80u << 21;
constexpr unsigned PG_XA = 0, PG_HS = 5 * BLK, PG_X1 = 10 * BLK, PG_H2 = 15 * BLK, PG_SS = 27 * BLK, PG_XR = 28 * BLK, PG_XN = 33 * BLK, PG_LX = 10 * BLK, PG_HV = 48 * BLK;
__device__ __forceinline__ unsigned pgb(unsigned p, int par) { return p << 21 | ((p ^ par) & 1) << 13; }
__device__ __forceinline__ constexpr unsigned blin(unsigned o) { return o + (o & ~8191u); }
constexpr int HT0 = 2;
constexpr int AH = 3;
constexpr unsigned WS_END = PG0 + PG_L;

#define SB() __builtin_amdgcn_sched_barrier(0)
#define LAS __attribute__((address_space(3)))
template <class T> __device__ __forceinline__ T lds_ld(unsigned a) { return *(const LAS T*)(a); }
template <class T> __device__ __forceinline__ void lds_st(unsigned a, T v) { *(LAS T*)(a) = v; }
constexpr unsigned OOB = 0x7FFFFF00u;
template <int N> __device__ __forceinline__ void wait_vm() { asm volatile("s_waitcnt vmcnt(%0)" :: "i"(N) : "memory"); }
__device__ __forceinline__ void bar() {
    asm volatile("s_waitcnt lgkmcnt(0)" ::: "memory");
    __builtin_amdgcn_s_barrier();
    asm volatile("" ::: "memory");
}
__device__ __forceinline__ int lane_id() {
    int ln;
    asm volatile("v_mbcnt_lo_u32_b32 %0, -1, 0\n\tv_mbcnt_hi_u32_b32 %0, -1, %0" : "=v"(ln));
    return ln;
}
__device__ __forceinline__ void swap16(unsigned& a, unsigned& b) { asm volatile("s_nop 3\n\tv_permlane16_swap_b32 %0, %1\n\ts_nop 1" : "+v"(a), "+v"(b)); }
__device__ __forceinline__ void swap32(unsigned& a, unsigned& b) { asm volatile("s_nop 3\n\tv_permlane32_swap_b32 %0, %1\n\ts_nop 1" : "+v"(a), "+v"(b)); }
__device__ __forceinline__ float sum_x16(float x) { unsigned a = __float_as_uint(x), b = a; swap16(a, b); return __uint_as_float(a) + __uint_as_float(b); }
__device__ __forceinline__ float sum_x32(float x) { unsigned a = __float_as_uint(x), b = a; swap32(a, b); return __uint_as_float(a) + __uint_as_float(b); }
__device__ __forceinline__ float max_x16(float x) { unsigned a = __float_as_uint(x), b = a; swap16(a, b); return __builtin_fmaxf(__uint_as_float(a), __uint_as_float(b)); }
__device__ __forceinline__ float max_x32(float x) { unsigned a = __float_as_uint(x), b = a; swap32(a, b); return __builtin_fmaxf(__uint_as_float(a), __uint_as_float(b)); }
__device__ __forceinline__ void sum2_rows(float& s, float& q) {
    unsigned a = __float_as_uint(s), b = a, c = __float_as_uint(q), d = c;
    asm volatile("s_nop 2\n\tv_permlane16_swap_b32 %0, %1\n\tv_permlane16_swap_b32 %2, %3\n\tv_add_f32 %0, %0, %1\n\tv_add_f32 %2, %2, %3\n\tv_mov_b32 %1, %0\n\tv_mov_b32 %3, %2\n\t"
                 "s_nop 1\n\tv_permlane32_swap_b32 %0, %1\n\tv_permlane32_swap_b32 %2, %3\n\tv_add_f32 %0, %0, %1\n\tv_add_f32 %2, %2, %3" : "+v"(a), "+v"(b), "+v"(c), "+v"(d));
    s = __uint_as_float(a); q = __uint_as_float(c);
}
__device__ __forceinline__ u32x4 merge16(u32x2 a, u32x2 b) {
    unsigned a0 = a[0], a1 = a[1], b0 = b[0], b1 = b[1];
    asm volatile("s_nop 3\n\tv_permlane16_swap_b32 %0, %2\n\tv_permlane16_swap_b32 %1, %3\n\ts_nop 1" : "+v"(a0), "+v"(a1), "+v"(b0), "+v"(b1));
    return (u32x4){a0, a1, b0, b1};
}
template <int AUXV>
__device__ __forceinline__ void st128_(u32x4 v, rsrc_t r, unsigned voff, unsigned soff) {
    __builtin_amdgcn_raw_buffer_store_b128(v, r, voff, soff, AUXV);
    asm volatile("s_nop 1" :: "v"(v) : "memory");
}
#define st128(V, R, VO_, SO_) st128_<16>(V, R, VO_, SO_)
#define LD128(R, VO_, SO_) __builtin_amdgcn_raw_buffer_load_b128(R, VO_, SO_, 16)
#define LD64(R, VO_, SO_) __builtin_amdgcn_raw_buffer_load_b64(R, VO_, SO_, 16)
#define BF8(X) __builtin_bit_cast(bf16x8, X)
__device__ __forceinline__ float gelu_tanh(float x) {
    const float c0 = -2.0f * 0.7978845608028654f * 1.4426950408889634f, c1 = c0 * 0.044715f;
    const float e = __builtin_amdgcn_exp2f(x * __builtin_fmaf(x * x, c1, c0));
    return x * __builtin_amdgcn_rcpf(1.0f + e);
}
#define MFMA(A_, B_, C_) C_ = __builtin_amdgcn_mfma_f32_16x16x32_bf16(A_, B_, C_, 0, 0, 0)
#define SMF(A_, B_, C_) C_ = __builtin_amdgcn_smfmac_f32_16x16x64_bf16(A_, B_, C_, sidx, 0, 0)
__device__ __forceinline__ float ln_den(float var) { return rbf(__builtin_amdgcn_sqrtf(rbf(rbf(var) + 1e-5f))); }
__device__ __forceinline__ float rcp(float x) { return __builtin_amdgcn_rcpf(x); }

struct Params {
    char* ws;
    const bf16_t* x;
    const bf16_t* mem0;
    const bf16_t* w[8];
    bf16_t* out;
};

#define RLX_AGENT __ATOMIC_RELAXED, __HIP_MEMORY_SCOPE_AGENT
#define SENT 0xFFFFFFFFu
constexpr int SPIN_CAP = 400000;
__device__ __forceinline__ bool spin_fail(int spins, const Params& P) {
    if ((spins & 1023) == 1023) {
        const rsrc_t rt = mkrsrc(P.ws + FL0, FL_BYTES);
        unsigned o;
        asm volatile("v_mov_b32 %0, 0x100" : "=v"(o));
        if (__builtin_amdgcn_readfirstlane(__builtin_amdgcn_raw_buffer_load_b32(rt, o, 0, 16)) != 0u) return true;
        if (spins > SPIN_CAP) { __builtin_amdgcn_raw_buffer_store_b32(o, rt, o, 0, 16); return true; }
    }
    return false;
}
__device__ __forceinline__ bool ok4(u32x4 v) { return v[0] != SENT && v[1] != SENT && v[2] != SENT && v[3] != SENT; }
__device__ __forceinline__ unsigned nosent(unsigned d) { return d < 0xFFFFFFFEu ? d : 0xFFFFFFFEu; }
__device__ __forceinline__ u32x4 nosent4(u32x4 v) { return (u32x4){nosent(v[0]), nosent(v[1]), nosent(v[2]), nosent(v[3])}; }
__device__ __forceinline__ unsigned umax3(unsigned a, unsigned b, unsigned c) { const unsigned m = a > b ? a : b; return m > c ? m : c; }
__device__ __forceinline__ unsigned umax4(unsigned m, u32x4 v) { return umax3(umax3(m, v[0], v[1]), v[2], v[3]); }
__device__ __forceinline__ u32x4 sent4() { unsigned sv; asm volatile("v_mov_b32 %0, -1" : "=v"(sv)); return (u32x4){sv, sv, sv, sv}; }
template <int N>
__device__ __forceinline__ void poll(rsrc_t rs, unsigned base, const unsigned (&off)[N], u32x4 (&v)[N], const Params& P) {
    for (int spins = 0;; ++spins) {
        unsigned m = 0;
#pragma unroll
        for (int k = 0; k < N; ++k) v[k] = LD128(rs, off[k], base);
#pragma unroll
        for (int k = 0; k < N; ++k) m = umax4(m, v[k]);
        if (__all(m != SENT)) return;
        asm volatile("" ::: "memory");
        if (spin_fail(spins, P)) return;
    }
}
__device__ __forceinline__ bf16x16 cat16(bf16x8 a, bf16x8 b) {
    return __builtin_shufflevector(a, b, 0, 1, 2, 3, 4, 5, 6, 7, 8, 9, 10, 11, 12, 13, 14, 15);
}
__device__ __forceinline__ bf16x8 half8(bf16x16 v, int h) {
    return h ? __builtin_shufflevector(v, v, 8, 9, 10, 11, 12, 13, 14, 15) : __builtin_shufflevector(v, v, 0, 1, 2, 3, 4, 5, 6, 7);
}
__device__ __forceinline__ bf16x16 lds_frag(unsigned a, unsigned step) { return cat16(lds_ld<bf16x8>(a), lds_ld<bf16x8>(a + step)); }
template <int I>
__device__ __forceinline__ void dmaq(const char* g, unsigned l16, unsigned dst) {
    asm volatile("s_mov_b32 m0, %2\n\ts_nop 0\n\tglobal_load_lds_dwordx4 %0, %1 offset:%3 sc1" :: "v"(l16), "s"(g), "s"(dst), "i"(I * 1024) : "memory");
}
__device__ __forceinline__ void dma4(const char* g, unsigned l16, unsigned dst) {
    asm volatile("s_mov_b32 m0, %2\n\ts_nop 0\n\tglobal_load_lds_dwordx4 %0, %1 sc1\n\tglobal_load_lds_dwordx4 %0, %1 offset:1024 sc1\n\t"
                 "global_load_lds_dwordx4 %0, %1 offset:2048 sc1\n\tglobal_load_lds_dwordx4 %0, %1 offset:3072 sc1" :: "v"(l16), "s"(g), "s"(dst) : "memory");
}
__device__ __forceinline__ unsigned toff(int row, int col, int par) {
    return pgb(row >> 5, par) + blin((unsigned)((row >> 4) & 1) * 20480u + (unsigned)(col >> 3) * 256u) + (unsigned)(row & 15) * 16u;
}

#define ASM_ST
#define ASM_ST_DECL
#define ASM_ST_OUT
#define ASM_ST_SAVE(k)
#define ASM_ST2
#define ASM_ST3
#define ASM_ST3_OUT
#define ASM_ST3_SAVE(k)
#define ASM_ST2_OUT
#define ASM_ST2_SAVE(k)
#define STDECL
#define ST(k)
#define STC(k)
#define STFLUSH(t)

#define MARK(c, k)
#define MARKX(c, k)
#define PMARK(c, st, seg)
#define LDECL
#define LA()
#define LB(t)
#define LF()
#define STROLE 3
constexpr unsigned L_MEM = 0;
constexpr unsigned L_Q = 10240;
constexpr unsigned L_KM = 13312;
constexpr unsigned L_QX = 16384;
constexpr unsigned L_KX = 28672;
constexpr unsigned L_XB = 40960;
constexpr unsigned L_XA = 49152;
constexpr unsigned L_W = 81920;
constexpr int MEGA_LDS = 163840;
constexpr int wi(int i, int j) { return (i >> 1) * 5 + ((i & 1) ? 2 + j : j - 1); }

#define RPUB2
#define WNT ""
#define PRIUP "s_setprio 2\n\t"
#define PRISM PRIUP
#define STR2(x) #x
#define STRX(x) STR2(x)
#define W4SLEEP "s_sleep " STRX(W4SL) "\n\t"
#define PLAY 0
#define V1A 0
#define ASM_WLOAD3 \
    "s_xor_b32 %[t], %[wx], 0\n\t" \
    "s_mul_i32 %[t], %[t], 0xc0000\n\t" \
    "buffer_load_dwordx4 v[200:203], %[o0], %[rs], %[t] offen" WNT "\n\t" \
    "buffer_load_dwordx4 v[0:3], %[o1], %[rs], %[t] offen" WNT "\n\t" \
    "buffer_load_dwordx4 v[8:11], %[o2], %[rs], %[t] offen" WNT "\n\t" \
    "s_add_u32 %[t], %[t], 0x18000\n\t" \
    "buffer_load_dwordx4 v[16:19], %[o0], %[rs], %[t] offen" WNT "\n\t" \
    "buffer_load_dwordx4 v[24:27], %[o1], %[rs], %[t] offen" WNT "\n\t" \
    "buffer_load_dwordx4 v[32:35], %[o2], %[rs], %[t] offen" WNT "\n\t" \
    "s_add_u32 %[t], %[t], 0x18000\n\t" \
    "buffer_load_dwordx4 v[208:211], %[o0], %[rs], %[t] offen" WNT "\n\t" \
    "buffer_load_dwordx4 v[40:43], %[o1], %[rs], %[t] offen" WNT "\n\t" \
    "buffer_load_dwordx4 v[48:51], %[o2], %[rs], %[t] offen" WNT "\n\t" \
    "s_add_u32 %[t], %[t], 0x18000\n\t" \
    "buffer_load_dwordx4 v[56:59], %[o0], %[rs], %[t] offen" WNT "\n\t" \
    "buffer_load_dwordx4 v[64:67], %[o1], %[rs], %[t] offen" WNT "\n\t" \
    "buffer_load_dwordx4 v[72:75], %[o2], %[rs], %[t] offen" WNT "\n\t" \
    "s_add_u32 %[t], %[t], 0x18000\n\t" \
    "buffer_load_dwordx4 v[204:207], %[o0], %[rs], %[t] offen" WNT "\n\t" \
    "buffer_load_dwordx4 v[4:7], %[o1], %[rs], %[t] offen" WNT "\n\t" \
    "buffer_load_dwordx4 v[12:15], %[o2], %[rs], %[t] offen" WNT "\n\t" \
    "s_add_u32 %[t], %[t], 0x18000\n\t" \
    "buffer_load_dwordx4 v[20:23], %[o0], %[rs], %[t] offen" WNT "\n\t" \
    "buffer_load_dwordx4 v[28:31], %[o1], %[rs], %[t] offen" WNT "\n\t" \
    "buffer_load_dwordx4 v[36:39], %[o2], %[rs], %[t] offen" WNT "\n\t" \
    "s_add_u32 %[t], %[t], 0x18000\n\t" \
    "buffer_load_dwordx4 v[212:215], %[o0], %[rs], %[t] offen" WNT "\n\t" \
    "buffer_load_dwordx4 v[44:47], %[o1], %[rs], %[t] offen" WNT "\n\t" \
    "buffer_load_dwordx4 v[52:55], %[o2], %[rs], %[t] offen" WNT "\n\t" \
    "s_add_u32 %[t], %[t], 0x18000\n\t" \
    "buffer_load_dwordx4 v[60:63], %[o0], %[rs], %[t] offen" WNT "\n\t" \
    "buffer_load_dwordx4 v[68:71], %[o1], %[rs], %[t] offen" WNT "\n\t" \
    "buffer_load_dwordx4 v[76:79], %[o2], %[rs], %[t] offen" WNT "\n\t" \
    "s_add_u32 %[t], %[t], 0x18000\n\t" \
    "s_waitcnt vmcnt(21)\n\t" \
    "ds_write_b128 %[s0], v[200:203] offset:0\n\t" \
    "ds_write_b128 %[s1], v[0:3] offset:0\n\t" \
    "ds_write_b128 %[s2], v[8:11] offset:0\n\t" \
    "s_waitcnt vmcnt(18)\n\t" \
    "ds_write_b128 %[s0], v[16:19] offset:3456\n\t" \
    "ds_write_b128 %[s1], v[24:27] offset:3456\n\t" \
    "ds_write_b128 %[s2], v[32:35] offset:3456\n\t" \
    "s_waitcnt vmcnt(15)\n\t" \
    "ds_write_b128 %[s0], v[208:211] offset:6912\n\t" \
    "ds_write_b128 %[s1], v[40:43] offset:6912\n\t" \
    "ds_write_b128 %[s2], v[48:51] offset:6912\n\t" \
    "s_waitcnt vmcnt(12)\n\t" \
    "ds_write_b128 %[s0], v[56:59] offset:10368\n\t" \
    "ds_write_b128 %[s1], v[64:67] offset:10368\n\t" \
    "ds_write_b128 %[s2], v[72:75] offset:10368\n\t" \
    "s_waitcnt lgkmcnt(0)\n\t" \
    "s_barrier \n\t" \
    "ds_read_b64_tr_b16 v[200:201], %[t0] offset:0\n\t" \
    "ds_read_b64_tr_b16 v[202:203], %[t0] offset:1664\n\t" \
    "ds_read_b64_tr_b16 v[0:1], %[t0] offset:128\n\t" \
    "ds_read_b64_tr_b16 v[2:3], %[t0] offset:1792\n\t" \
    "ds_read_b64_tr_b16 v[8:9], %[t0] offset:256\n\t" \
    "ds_read_b64_tr_b16 v[10:11], %[t0] offset:1920\n\t" \
    "ds_read_b64_tr_b16 v[16:17], %[t1] offset:0\n\t" \
    "ds_read_b64_tr_b16 v[18:19], %[t1] offset:1664\n\t" \
    "ds_read_b64_tr_b16 v[24:25], %[t1] offset:128\n\t" \
    "ds_read_b64_tr_b16 v[26:27], %[t1] offset:1792\n\t" \
    "ds_read_b64_tr_b16 v[32:33], %[t1] offset:256\n\t" \
    "ds_read_b64_tr_b16 v[34:35], %[t1] offset:1920\n\t" \
    "ds_read_b64_tr_b16 v[208:209], %[t2] offset:0\n\t" \
    "ds_read_b64_tr_b16 v[210:211], %[t2] offset:1664\n\t" \
    "ds_read_b64_tr_b16 v[40:41], %[t2] offset:128\n\t" \
    "ds_read_b64_tr_b16 v[42:43], %[t2] offset:1792\n\t" \
    "ds_read_b64_tr_b16 v[48:49], %[t2] offset:256\n\t" \
    "ds_read_b64_tr_b16 v[50:51], %[t2] offset:1920\n\t" \
    "ds_read_b64_tr_b16 v[56:57], %[t3] offset:0\n\t" \
    "ds_read_b64_tr_b16 v[58:59], %[t3] offset:1664\n\t" \
    "ds_read_b64_tr_b16 v[64:65], %[t3] offset:128\n\t" \
    "ds_read_b64_tr_b16 v[66:67], %[t3] offset:1792\n\t" \
    "ds_read_b64_tr_b16 v[72:73], %[t3] offset:256\n\t" \
    "ds_read_b64_tr_b16 v[74:75], %[t3] offset:1920\n\t" \
    "s_xor_b32 %[t], %[wx], 4\n\t" \
    "s_mul_i32 %[t], %[t], 0xc0000\n\t" \
    "buffer_load_dwordx4 v[216:219], %[o0], %[rs], %[t] offen" WNT "\n\t" \
    "buffer_load_dwordx4 v[80:83], %[o1], %[rs], %[t] offen" WNT "\n\t" \
    "buffer_load_dwordx4 v[88:91], %[o2], %[rs], %[t] offen" WNT "\n\t" \
    "s_add_u32 %[t], %[t], 0x18000\n\t" \
    "buffer_load_dwordx4 v[96:99], %[o0], %[rs], %[t] offen" WNT "\n\t" \
    "buffer_load_dwordx4 v[104:107], %[o1], %[rs], %[t] offen" WNT "\n\t" \
    "buffer_load_dwordx4 v[112:115], %[o2], %[rs], %[t] offen" WNT "\n\t" \
    "s_add_u32 %[t], %[t], 0x18000\n\t" \
    "buffer_load_dwordx4 v[224:227], %[o0], %[rs], %[t] offen" WNT "\n\t" \
    "buffer_load_dwordx4 v[120:123], %[o1], %[rs], %[t] offen" WNT "\n\t" \
    "buffer_load_dwordx4 v[128:131], %[o2], %[rs], %[t] offen" WNT "\n\t" \
    "s_add_u32 %[t], %[t], 0x18000\n\t" \
    "buffer_load_dwordx4 v[136:139], %[o0], %[rs], %[t] offen" WNT "\n\t" \
    "buffer_load_dwordx4 v[144:147], %[o1], %[rs], %[t] offen" WNT "\n\t" \
    "buffer_load_dwordx4 v[152:155], %[o2], %[rs], %[t] offen" WNT "\n\t" \
    "s_add_u32 %[t], %[t], 0x18000\n\t" \
    "s_waitcnt lgkmcnt(0)\n\t" \
    "s_barrier \n\t" \
    "s_waitcnt vmcnt(21)\n\t" \
    "ds_write_b128 %[s0], v[204:207] offset:0\n\t" \
    "ds_write_b128 %[s1], v[4:7] offset:0\n\t" \
    "ds_write_b128 %[s2], v[12:15] offset:0\n\t" \
    "s_waitcnt vmcnt(18)\n\t" \
    "ds_write_b128 %[s0], v[20:23] offset:3456\n\t" \
    "ds_write_b128 %[s1], v[28:31] offset:3456\n\t" \
    "ds_write_b128 %[s2], v[36:39] offset:3456\n\t" \
    "s_waitcnt vmcnt(15)\n\t" \
    "ds_write_b128 %[s0], v[212:215] offset:6912\n\t" \
    "ds_write_b128 %[s1], v[44:47] offset:6912\n\t" \
    "ds_write_b128 %[s2], v[52:55] offset:6912\n\t" \
    "s_waitcnt vmcnt(12)\n\t" \
    "ds_write_b128 %[s0], v[60:63] offset:10368\n\t" \
    "ds_write_b128 %[s1], v[68:71] offset:10368\n\t" \
    "ds_write_b128 %[s2], v[76:79] offset:10368\n\t" \
    "s_waitcnt lgkmcnt(0)\n\t" \
    "s_barrier \n\t" \
    "ds_read_b64_tr_b16 v[204:205], %[t0] offset:0\n\t" \
    "ds_read_b64_tr_b16 v[206:207], %[t0] offset:1664\n\t" \
    "ds_read_b64_tr_b16 v[4:5], %[t0] offset:128\n\t" \
    "ds_read_b64_tr_b16 v[6:7], %[t0] offset:1792\n\t" \
    "ds_read_b64_tr_b16 v[12:13], %[t0] offset:256\n\t" \
    "ds_read_b64_tr_b16 v[14:15], %[t0] offset:1920\n\t" \
    "ds_read_b64_tr_b16 v[20:21], %[t1] offset:0\n\t" \
    "ds_read_b64_tr_b16 v[22:23], %[t1] offset:1664\n\t" \
    "ds_read_b64_tr_b16 v[28:29], %[t1] offset:128\n\t" \
    "ds_read_b64_tr_b16 v[30:31], %[t1] offset:1792\n\t" \
    "ds_read_b64_tr_b16 v[36:37], %[t1] offset:256\n\t" \
    "ds_read_b64_tr_b16 v[38:39], %[t1] offset:1920\n\t" \
    "ds_read_b64_tr_b16 v[212:213], %[t2] offset:0\n\t" \
    "ds_read_b64_tr_b16 v[214:215], %[t2] offset:1664\n\t" \
    "ds_read_b64_tr_b16 v[44:45], %[t2] offset:128\n\t" \
    "ds_read_b64_tr_b16 v[46:47], %[t2] offset:1792\n\t" \
    "ds_read_b64_tr_b16 v[52:53], %[t2] offset:256\n\t" \
    "ds_read_b64_tr_b16 v[54:55], %[t2] offset:1920\n\t" \
    "ds_read_b64_tr_b16 v[60:61], %[t3] offset:0\n\t" \
    "ds_read_b64_tr_b16 v[62:63], %[t3] offset:1664\n\t" \
    "ds_read_b64_tr_b16 v[68:69], %[t3] offset:128\n\t" \
    "ds_read_b64_tr_b16 v[70:71], %[t3] offset:1792\n\t" \
    "ds_read_b64_tr_b16 v[76:77], %[t3] offset:256\n\t" \
    "ds_read_b64_tr_b16 v[78:79], %[t3] offset:1920\n\t" \
    "buffer_load_dwordx4 v[220:223], %[o0], %[rs], %[t] offen" WNT "\n\t" \
    "buffer_load_dwordx4 v[84:87], %[o1], %[rs], %[t] offen" WNT "\n\t" \
    "buffer_load_dwordx4 v[92:95], %[o2], %[rs], %[t] offen" WNT "\n\t" \
    "s_add_u32 %[t], %[t], 0x18000\n\t" \
    "buffer_load_dwordx4 v[100:103], %[o0], %[rs], %[t] offen" WNT "\n\t" \
    "buffer_load_dwordx4 v[108:111], %[o1], %[rs], %[t] offen" WNT "\n\t" \
    "buffer_load_dwordx4 v[116:119], %[o2], %[rs], %[t] offen" WNT "\n\t" \
    "s_add_u32 %[t], %[t], 0x18000\n\t" \
    "buffer_load_dwordx4 v[228:231], %[o0], %[rs], %[t] offen" WNT "\n\t" \
    "buffer_load_dwordx4 v[124:127], %[o1], %[rs], %[t] offen" WNT "\n\t" \
    "buffer_load_dwordx4 v[132:135], %[o2], %[rs], %[t] offen" WNT "\n\t" \
    "s_add_u32 %[t], %[t], 0x18000\n\t" \
    "buffer_load_dwordx4 v[140:143], %[o0], %[rs], %[t] offen" WNT "\n\t" \
    "buffer_load_dwordx4 v[148:151], %[o1], %[rs], %[t] offen" WNT "\n\t" \
    "buffer_load_dwordx4 v[156:159], %[o2], %[rs], %[t] offen" WNT "\n\t" \
    "s_add_u32 %[t], %[t], 0x18000\n\t" \
    "s_waitcnt lgkmcnt(0)\n\t" \
    "s_barrier \n\t" \
    "s_waitcnt vmcnt(21)\n\t" \
    "ds_write_b128 %[s0], v[216:219] offset:0\n\t" \
    "ds_write_b128 %[s1], v[80:83] offset:0\n\t" \
    "ds_write_b128 %[s2], v[88:91] offset:0\n\t" \
    "s_waitcnt vmcnt(18)\n\t" \
    "ds_write_b128 %[s0], v[96:99] offset:3456\n\t" \
    "ds_write_b128 %[s1], v[104:107] offset:3456\n\t" \
    "ds_write_b128 %[s2], v[112:115] offset:3456\n\t" \
    "s_waitcnt vmcnt(15)\n\t" \
    "ds_write_b128 %[s0], v[224:227] offset:6912\n\t" \
    "ds_write_b128 %[s1], v[120:123] offset:6912\n\t" \
    "ds_write_b128 %[s2], v[128:131] offset:6912\n\t" \
    "s_waitcnt vmcnt(12)\n\t" \
    "ds_write_b128 %[s0], v[136:139] offset:10368\n\t" \
    "ds_write_b128 %[s1], v[144:147] offset:10368\n\t" \
    "ds_write_b128 %[s2], v[152:155] offset:10368\n\t" \
    "s_waitcnt lgkmcnt(0)\n\t" \
    "s_barrier \n\t" \
    "ds_read_b64_tr_b16 v[216:217], %[t0] offset:0\n\t" \
    "ds_read_b64_tr_b16 v[218:219], %[t0] offset:1664\n\t" \
    "ds_read_b64_tr_b16 v[80:81], %[t0] offset:128\n\t" \
    "ds_read_b64_tr_b16 v[82:83], %[t0] offset:1792\n\t" \
    "ds_read_b64_tr_b16 v[88:89], %[t0] offset:256\n\t" \
    "ds_read_b64_tr_b16 v[90:91], %[t0] offset:1920\n\t" \
    "ds_read_b64_tr_b16 v[96:97], %[t1] offset:0\n\t" \
    "ds_read_b64_tr_b16 v[98:99], %[t1] offset:1664\n\t" \
    "ds_read_b64_tr_b16 v[104:105], %[t1] offset:128\n\t" \
    "ds_read_b64_tr_b16 v[106:107], %[t1] offset:1792\n\t" \
    "ds_read_b64_tr_b16 v[112:113], %[t1] offset:256\n\t" \
    "ds_read_b64_tr_b16 v[114:115], %[t1] offset:1920\n\t" \
    "ds_read_b64_tr_b16 v[224:225], %[t2] offset:0\n\t" \
    "ds_read_b64_tr_b16 v[226:227], %[t2] offset:1664\n\t" \
    "ds_read_b64_tr_b16 v[120:121], %[t2] offset:128\n\t" \
    "ds_read_b64_tr_b16 v[122:123], %[t2] offset:1792\n\t" \
    "ds_read_b64_tr_b16 v[128:129], %[t2] offset:256\n\t" \
    "ds_read_b64_tr_b16 v[130:131], %[t2] offset:1920\n\t" \
    "ds_read_b64_tr_b16 v[136:137], %[t3] offset:0\n\t" \
    "ds_read_b64_tr_b16 v[138:139], %[t3] offset:1664\n\t" \
    "ds_read_b64_tr_b16 v[144:145], %[t3] offset:128\n\t" \
    "ds_read_b64_tr_b16 v[146:147], %[t3] offset:1792\n\t" \
    "ds_read_b64_tr_b16 v[152:153], %[t3] offset:256\n\t" \
    "ds_read_b64_tr_b16 v[154:155], %[t3] offset:1920\n\t" \
    "s_and_b32 %[t], %[wx], 3\n\t" \
    "s_mul_i32 %[t], %[t], 0x60000\n\t" \
    "s_add_u32 %[t], %[t], 0x600000\n\t" \
    "buffer_load_dwordx4 v[232:235], %[o0], %[rs], %[t] offen" WNT "\n\t" \
    "buffer_load_dwordx4 v[160:163], %[o1], %[rs], %[t] offen" WNT "\n\t" \
    "buffer_load_dwordx4 v[168:171], %[o2], %[rs], %[t] offen" WNT "\n\t" \
    "s_add_u32 %[t], %[t], 0x18000\n\t" \
    "buffer_load_dwordx4 v[236:239], %[o0], %[rs], %[t] offen" WNT "\n\t" \
    "buffer_load_dwordx4 v[164:167], %[o1], %[rs], %[t] offen" WNT "\n\t" \
    "buffer_load_dwordx4 v[172:175], %[o2], %[rs], %[t] offen" WNT "\n\t" \
    "s_add_u32 %[t], %[t], 0x18000\n\t" \
    "buffer_load_dwordx4 v[176:179], %[o0], %[rs], %[t] offen" WNT "\n\t" \
    "buffer_load_dwordx4 v[184:187], %[o1], %[rs], %[t] offen" WNT "\n\t" \
    "buffer_load_dwordx4 v[192:195], %[o2], %[rs], %[t] offen" WNT "\n\t" \
    "s_add_u32 %[t], %[t], 0x18000\n\t" \
    "buffer_load_dwordx4 v[180:183], %[o0], %[rs], %[t] offen" WNT "\n\t" \
    "buffer_load_dwordx4 v[188:191], %[o1], %[rs], %[t] offen" WNT "\n\t" \
    "buffer_load_dwordx4 v[196:199], %[o2], %[rs], %[t] offen" WNT "\n\t" \
    "s_add_u32 %[t], %[t], 0x18000\n\t" \
    "s_waitcnt lgkmcnt(0)\n\t" \
    "s_barrier \n\t" \
    "s_waitcnt vmcnt(21)\n\t" \
    "ds_write_b128 %[s0], v[220:223] offset:0\n\t" \
    "ds_write_b128 %[s1], v[84:87] offset:0\n\t" \
    "ds_write_b128 %[s2], v[92:95] offset:0\n\t" \
    "s_waitcnt vmcnt(18)\n\t" \
    "ds_write_b128 %[s0], v[100:103] offset:3456\n\t" \
    "ds_write_b128 %[s1], v[108:111] offset:3456\n\t" \
    "ds_write_b128 %[s2], v[116:119] offset:3456\n\t" \
    "s_waitcnt vmcnt(15)\n\t" \
    "ds_write_b128 %[s0], v[228:231] offset:6912\n\t" \
    "ds_write_b128 %[s1], v[124:127] offset:6912\n\t" \
    "ds_write_b128 %[s2], v[132:135] offset:6912\n\t" \
    "s_waitcnt vmcnt(12)\n\t" \
    "ds_write_b128 %[s0], v[140:143] offset:10368\n\t" \
    "ds_write_b128 %[s1], v[148:151] offset:10368\n\t" \
    "ds_write_b128 %[s2], v[156:159] offset:10368\n\t" \
    "s_waitcnt lgkmcnt(0)\n\t" \
    "s_barrier \n\t" \
    "ds_read_b64_tr_b16 v[220:221], %[t0] offset:0\n\t" \
    "ds_read_b64_tr_b16 v[222:223], %[t0] offset:1664\n\t" \
    "ds_read_b64_tr_b16 v[84:85], %[t0] offset:128\n\t" \
    "ds_read_b64_tr_b16 v[86:87], %[t0] offset:1792\n\t" \
    "ds_read_b64_tr_b16 v[92:93], %[t0] offset:256\n\t" \
    "ds_read_b64_tr_b16 v[94:95], %[t0] offset:1920\n\t" \
    "ds_read_b64_tr_b16 v[100:101], %[t1] offset:0\n\t" \
    "ds_read_b64_tr_b16 v[102:103], %[t1] offset:1664\n\t" \
    "ds_read_b64_tr_b16 v[108:109], %[t1] offset:128\n\t" \
    "ds_read_b64_tr_b16 v[110:111], %[t1] offset:1792\n\t" \
    "ds_read_b64_tr_b16 v[116:117], %[t1] offset:256\n\t" \
    "ds_read_b64_tr_b16 v[118:119], %[t1] offset:1920\n\t" \
    "ds_read_b64_tr_b16 v[228:229], %[t2] offset:0\n\t" \
    "ds_read_b64_tr_b16 v[230:231], %[t2] offset:1664\n\t" \
    "ds_read_b64_tr_b16 v[124:125], %[t2] offset:128\n\t" \
    "ds_read_b64_tr_b16 v[126:127], %[t2] offset:1792\n\t" \
    "ds_read_b64_tr_b16 v[132:133], %[t2] offset:256\n\t" \
    "ds_read_b64_tr_b16 v[134:135], %[t2] offset:1920\n\t" \
    "ds_read_b64_tr_b16 v[140:141], %[t3] offset:0\n\t" \
    "ds_read_b64_tr_b16 v[142:143], %[t3] offset:1664\n\t" \
    "ds_read_b64_tr_b16 v[148:149], %[t3] offset:128\n\t" \
    "ds_read_b64_tr_b16 v[150:151], %[t3] offset:1792\n\t" \
    "ds_read_b64_tr_b16 v[156:157], %[t3] offset:256\n\t" \
    "ds_read_b64_tr_b16 v[158:159], %[t3] offset:1920\n\t" \
    "s_waitcnt lgkmcnt(0)\n\t" \
    "s_barrier \n\t" \
    "s_waitcnt vmcnt(9)\n\t" \
    "ds_write_b128 %[s0], v[232:235] offset:0\n\t" \
    "ds_write_b128 %[s1], v[160:163] offset:0\n\t" \
    "ds_write_b128 %[s2], v[168:171] offset:0\n\t" \
    "s_waitcnt vmcnt(6)\n\t" \
    "ds_write_b128 %[s0], v[236:239] offset:3456\n\t" \
    "ds_write_b128 %[s1], v[164:167] offset:3456\n\t" \
    "ds_write_b128 %[s2], v[172:175] offset:3456\n\t" \
    "s_waitcnt vmcnt(3)\n\t" \
    "ds_write_b128 %[s0], v[176:179] offset:6912\n\t" \
    "ds_write_b128 %[s1], v[184:187] offset:6912\n\t" \
    "ds_write_b128 %[s2], v[192:195] offset:6912\n\t" \
    "s_waitcnt vmcnt(0)\n\t" \
    "ds_write_b128 %[s0], v[180:183] offset:10368\n\t" \
    "ds_write_b128 %[s1], v[188:191] offset:10368\n\t" \
    "ds_write_b128 %[s2], v[196:199] offset:10368\n\t" \
    "s_waitcnt lgkmcnt(0)\n\t" \
    "s_barrier \n\t" \
    "ds_read_b64_tr_b16 v[232:233], %[t4] offset:0\n\t" \
    "ds_read_b64_tr_b16 v[234:235], %[t4] offset:1664\n\t" \
    "ds_read_b64_tr_b16 v[160:161], %[t4] offset:128\n\t" \
    "ds_read_b64_tr_b16 v[162:163], %[t4] offset:1792\n\t" \
    "ds_read_b64_tr_b16 v[168:169], %[t4] offset:256\n\t" \
    "ds_read_b64_tr_b16 v[170:171], %[t4] offset:1920\n\t" \
    "ds_read_b64_tr_b16 v[236:237], %[t4] offset:13824\n\t" \
    "ds_read_b64_tr_b16 v[238:239], %[t4] offset:15488\n\t" \
    "ds_read_b64_tr_b16 v[164:165], %[t4] offset:13952\n\t" \
    "ds_read_b64_tr_b16 v[166:167], %[t4] offset:15616\n\t" \
    "ds_read_b64_tr_b16 v[172:173], %[t4] offset:14080\n\t" \
    "ds_read_b64_tr_b16 v[174:175], %[t4] offset:15744\n\t" \
    "ds_read_b64_tr_b16 v[176:177], %[t4] offset:27648\n\t" \
    "ds_read_b64_tr_b16 v[178:179], %[t4] offset:29312\n\t" \
    "ds_read_b64_tr_b16 v[184:185], %[t4] offset:27776\n\t" \
    "ds_read_b64_tr_b16 v[186:187], %[t4] offset:29440\n\t" \
    "ds_read_b64_tr_b16 v[192:193], %[t4] offset:27904\n\t" \
    "ds_read_b64_tr_b16 v[194:195], %[t4] offset:29568\n\t" \
    "ds_read_b64_tr_b16 v[180:181], %[t4] offset:41472\n\t" \
    "ds_read_b64_tr_b16 v[182:183], %[t4] offset:43136\n\t" \
    "ds_read_b64_tr_b16 v[188:189], %[t4] offset:41600\n\t" \
    "ds_read_b64_tr_b16 v[190:191], %[t4] offset:43264\n\t" \
    "ds_read_b64_tr_b16 v[196:197], %[t4] offset:41728\n\t" \
    "ds_read_b64_tr_b16 v[198:199], %[t4] offset:43392\n\t" \
    "s_waitcnt lgkmcnt(0)\n\t" \
    "s_barrier \n\t" \
    "ds_write_b128 %[wl], v[200:203] offset:0\n\t" \
    "ds_write_b128 %[wl], v[204:207] offset:1024\n\t" \
    "ds_write_b128 %[wl], v[208:211] offset:2048\n\t" \
    "ds_write_b128 %[wl], v[212:215] offset:3072\n\t" \
    "ds_write_b128 %[wl], v[216:219] offset:4096\n\t" \
    "ds_write_b128 %[wl], v[220:223] offset:5120\n\t" \
    "ds_write_b128 %[wl], v[224:227] offset:6144\n\t" \
    "ds_write_b128 %[wl], v[228:231] offset:7168\n\t" \
    "ds_write_b128 %[wl], v[232:235] offset:8192\n\t" \
    "ds_write_b128 %[wl], v[236:239] offset:9216\n\t" \
    "s_waitcnt lgkmcnt(0)\n\t"
#define ASM_WLOADO \
    "s_mov_b32 %[t], 0x0\n\t" \
    "buffer_load_dwordx4 v[200:203], %[o0], %[rs], %[t] offen" WNT "\n\t" \
    "buffer_load_dwordx4 v[16:19], %[o1], %[rs], %[t] offen" WNT "\n\t" \
    "buffer_load_dwordx4 v[208:211], %[o2], %[rs], %[t] offen" WNT "\n\t" \
    "buffer_load_dwordx4 v[56:59], %[o3], %[rs], %[t] offen" WNT "\n\t" \
    "buffer_load_dwordx4 v[216:219], %[o4], %[rs], %[t] offen" WNT "\n\t" \
    "s_mov_b32 %[t], 0xa000\n\t" \
    "buffer_load_dwordx4 v[96:99], %[o0], %[rs], %[t] offen" WNT "\n\t" \
    "buffer_load_dwordx4 v[224:227], %[o1], %[rs], %[t] offen" WNT "\n\t" \
    "buffer_load_dwordx4 v[136:139], %[o2], %[rs], %[t] offen" WNT "\n\t" \
    "buffer_load_dwordx4 v[232:235], %[o3], %[rs], %[t] offen" WNT "\n\t" \
    "buffer_load_dwordx4 v[176:179], %[o4], %[rs], %[t] offen" WNT "\n\t" \
    "s_mov_b32 %[t], 0x14000\n\t" \
    "buffer_load_dwordx4 v[204:207], %[o0], %[rs], %[t] offen" WNT "\n\t" \
    "buffer_load_dwordx4 v[20:23], %[o1], %[rs], %[t] offen" WNT "\n\t" \
    "buffer_load_dwordx4 v[212:215], %[o2], %[rs], %[t] offen" WNT "\n\t" \
    "buffer_load_dwordx4 v[60:63], %[o3], %[rs], %[t] offen" WNT "\n\t" \
    "buffer_load_dwordx4 v[220:223], %[o4], %[rs], %[t] offen" WNT "\n\t" \
    "s_mov_b32 %[t], 0x1e000\n\t" \
    "buffer_load_dwordx4 v[100:103], %[o0], %[rs], %[t] offen" WNT "\n\t" \
    "buffer_load_dwordx4 v[228:231], %[o1], %[rs], %[t] offen" WNT "\n\t" \
    "buffer_load_dwordx4 v[140:143], %[o2], %[rs], %[t] offen" WNT "\n\t" \
    "buffer_load_dwordx4 v[236:239], %[o3], %[rs], %[t] offen" WNT "\n\t" \
    "buffer_load_dwordx4 v[180:183], %[o4], %[rs], %[t] offen" WNT "\n\t" \
    "s_waitcnt vmcnt(15)\n\t" \
    "ds_write_b128 %[s0], v[200:203] offset:0\n\t" \
    "ds_write_b128 %[s1], v[16:19] offset:0\n\t" \
    "ds_write_b128 %[s2], v[208:211] offset:0\n\t" \
    "ds_write_b128 %[s3], v[56:59] offset:0\n\t" \
    "ds_write_b128 %[s4], v[216:219] offset:0\n\t" \
    "s_waitcnt vmcnt(10)\n\t" \
    "ds_write_b128 %[s0], v[96:99] offset:5632\n\t" \
    "ds_write_b128 %[s1], v[224:227] offset:5632\n\t" \
    "ds_write_b128 %[s2], v[136:139] offset:5632\n\t" \
    "ds_write_b128 %[s3], v[232:235] offset:5632\n\t" \
    "ds_write_b128 %[s4], v[176:179] offset:5632\n\t" \
    "ds_read_b64_tr_b16 v[200:201], %[tr] offset:0\n\t" \
    "ds_read_b64_tr_b16 v[202:203], %[tr] offset:5632\n\t" \
    "ds_read_b64_tr_b16 v[16:17], %[tr] offset:32\n\t" \
    "ds_read_b64_tr_b16 v[18:19], %[tr] offset:5664\n\t" \
    "ds_read_b64_tr_b16 v[208:209], %[tr] offset:64\n\t" \
    "ds_read_b64_tr_b16 v[210:211], %[tr] offset:5696\n\t" \
    "ds_read_b64_tr_b16 v[56:57], %[tr] offset:96\n\t" \
    "ds_read_b64_tr_b16 v[58:59], %[tr] offset:5728\n\t" \
    "ds_read_b64_tr_b16 v[216:217], %[tr] offset:128\n\t" \
    "ds_read_b64_tr_b16 v[218:219], %[tr] offset:5760\n\t" \
    "ds_read_b64_tr_b16 v[96:97], %[tr] offset:160\n\t" \
    "ds_read_b64_tr_b16 v[98:99], %[tr] offset:5792\n\t" \
    "ds_read_b64_tr_b16 v[224:225], %[tr] offset:192\n\t" \
    "ds_read_b64_tr_b16 v[226:227], %[tr] offset:5824\n\t" \
    "ds_read_b64_tr_b16 v[136:137], %[tr] offset:224\n\t" \
    "ds_read_b64_tr_b16 v[138:139], %[tr] offset:5856\n\t" \
    "ds_read_b64_tr_b16 v[232:233], %[tr] offset:256\n\t" \
    "ds_read_b64_tr_b16 v[234:235], %[tr] offset:5888\n\t" \
    "ds_read_b64_tr_b16 v[176:177], %[tr] offset:288\n\t" \
    "ds_read_b64_tr_b16 v[178:179], %[tr] offset:5920\n\t" \
    "s_barrier \n\t" \
    "s_mov_b32 %[t], 0x28000\n\t" \
    "buffer_load_dwordx4 v[0:3], %[o0], %[rs], %[t] offen" WNT "\n\t" \
    "buffer_load_dwordx4 v[24:27], %[o1], %[rs], %[t] offen" WNT "\n\t" \
    "buffer_load_dwordx4 v[40:43], %[o2], %[rs], %[t] offen" WNT "\n\t" \
    "buffer_load_dwordx4 v[64:67], %[o3], %[rs], %[t] offen" WNT "\n\t" \
    "buffer_load_dwordx4 v[80:83], %[o4], %[rs], %[t] offen" WNT "\n\t" \
    "s_mov_b32 %[t], 0x32000\n\t" \
    "buffer_load_dwordx4 v[104:107], %[o0], %[rs], %[t] offen" WNT "\n\t" \
    "buffer_load_dwordx4 v[120:123], %[o1], %[rs], %[t] offen" WNT "\n\t" \
    "buffer_load_dwordx4 v[144:147], %[o2], %[rs], %[t] offen" WNT "\n\t" \
    "buffer_load_dwordx4 v[160:163], %[o3], %[rs], %[t] offen" WNT "\n\t" \
    "buffer_load_dwordx4 v[184:187], %[o4], %[rs], %[t] offen" WNT "\n\t" \
    "s_waitcnt lgkmcnt(0)\n\t" \
    "s_barrier \n\t" \
    "s_waitcnt vmcnt(15)\n\t" \
    "ds_write_b128 %[s0], v[204:207] offset:0\n\t" \
    "ds_write_b128 %[s1], v[20:23] offset:0\n\t" \
    "ds_write_b128 %[s2], v[212:215] offset:0\n\t" \
    "ds_write_b128 %[s3], v[60:63] offset:0\n\t" \
    "ds_write_b128 %[s4], v[220:223] offset:0\n\t" \
    "s_waitcnt vmcnt(10)\n\t" \
    "ds_write_b128 %[s0], v[100:103] offset:5632\n\t" \
    "ds_write_b128 %[s1], v[228:231] offset:5632\n\t" \
    "ds_write_b128 %[s2], v[140:143] offset:5632\n\t" \
    "ds_write_b128 %[s3], v[236:239] offset:5632\n\t" \
    "ds_write_b128 %[s4], v[180:183] offset:5632\n\t" \
    "ds_read_b64_tr_b16 v[204:205], %[tr] offset:0\n\t" \
    "ds_read_b64_tr_b16 v[206:207], %[tr] offset:5632\n\t" \
    "ds_read_b64_tr_b16 v[20:21], %[tr] offset:32\n\t" \
    "ds_read_b64_tr_b16 v[22:23], %[tr] offset:5664\n\t" \
    "ds_read_b64_tr_b16 v[212:213], %[tr] offset:64\n\t" \
    "ds_read_b64_tr_b16 v[214:215], %[tr] offset:5696\n\t" \
    "ds_read_b64_tr_b16 v[60:61], %[tr] offset:96\n\t" \
    "ds_read_b64_tr_b16 v[62:63], %[tr] offset:5728\n\t" \
    "ds_read_b64_tr_b16 v[220:221], %[tr] offset:128\n\t" \
    "ds_read_b64_tr_b16 v[222:223], %[tr] offset:5760\n\t" \
    "ds_read_b64_tr_b16 v[100:101], %[tr] offset:160\n\t" \
    "ds_read_b64_tr_b16 v[102:103], %[tr] offset:5792\n\t" \
    "ds_read_b64_tr_b16 v[228:229], %[tr] offset:192\n\t" \
    "ds_read_b64_tr_b16 v[230:231], %[tr] offset:5824\n\t" \
    "ds_read_b64_tr_b16 v[140:141], %[tr] offset:224\n\t" \
    "ds_read_b64_tr_b16 v[142:143], %[tr] offset:5856\n\t" \
    "ds_read_b64_tr_b16 v[236:237], %[tr] offset:256\n\t" \
    "ds_read_b64_tr_b16 v[238:239], %[tr] offset:5888\n\t" \
    "ds_read_b64_tr_b16 v[180:181], %[tr] offset:288\n\t" \
    "ds_read_b64_tr_b16 v[182:183], %[tr] offset:5920\n\t" \
    "s_barrier \n\t" \
    "s_mov_b32 %[t], 0x5000\n\t" \
    "buffer_load_dwordx4 v[4:7], %[o0], %[rs], %[t] offen" WNT "\n\t" \
    "buffer_load_dwordx4 v[28:31], %[o1], %[rs], %[t] offen" WNT "\n\t" \
    "buffer_load_dwordx4 v[44:47], %[o2], %[rs], %[t] offen" WNT "\n\t" \
    "buffer_load_dwordx4 v[68:71], %[o3], %[rs], %[t] offen" WNT "\n\t" \
    "buffer_load_dwordx4 v[84:87], %[o4], %[rs], %[t] offen" WNT "\n\t" \
    "s_mov_b32 %[t], 0xf000\n\t" \
    "buffer_load_dwordx4 v[108:111], %[o0], %[rs], %[t] offen" WNT "\n\t" \
    "buffer_load_dwordx4 v[124:127], %[o1], %[rs], %[t] offen" WNT "\n\t" \
    "buffer_load_dwordx4 v[148:151], %[o2], %[rs], %[t] offen" WNT "\n\t" \
    "buffer_load_dwordx4 v[164:167], %[o3], %[rs], %[t] offen" WNT "\n\t" \
    "buffer_load_dwordx4 v[188:191], %[o4], %[rs], %[t] offen" WNT "\n\t" \
    "s_waitcnt lgkmcnt(0)\n\t" \
    "s_barrier \n\t" \
    "s_waitcnt vmcnt(15)\n\t" \
    "ds_write_b128 %[s0], v[0:3] offset:0\n\t" \
    "ds_write_b128 %[s1], v[24:27] offset:0\n\t" \
    "ds_write_b128 %[s2], v[40:43] offset:0\n\t" \
    "ds_write_b128 %[s3], v[64:67] offset:0\n\t" \
    "ds_write_b128 %[s4], v[80:83] offset:0\n\t" \
    "s_waitcnt vmcnt(10)\n\t" \
    "ds_write_b128 %[s0], v[104:107] offset:5632\n\t" \
    "ds_write_b128 %[s1], v[120:123] offset:5632\n\t" \
    "ds_write_b128 %[s2], v[144:147] offset:5632\n\t" \
    "ds_write_b128 %[s3], v[160:163] offset:5632\n\t" \
    "ds_write_b128 %[s4], v[184:187] offset:5632\n\t" \
    "ds_read_b64_tr_b16 v[0:1], %[tr] offset:0\n\t" \
    "ds_read_b64_tr_b16 v[2:3], %[tr] offset:5632\n\t" \
    "ds_read_b64_tr_b16 v[24:25], %[tr] offset:32\n\t" \
    "ds_read_b64_tr_b16 v[26:27], %[tr] offset:5664\n\t" \
    "ds_read_b64_tr_b16 v[40:41], %[tr] offset:64\n\t" \
    "ds_read_b64_tr_b16 v[42:43], %[tr] offset:5696\n\t" \
    "ds_read_b64_tr_b16 v[64:65], %[tr] offset:96\n\t" \
    "ds_read_b64_tr_b16 v[66:67], %[tr] offset:5728\n\t" \
    "ds_read_b64_tr_b16 v[80:81], %[tr] offset:128\n\t" \
    "ds_read_b64_tr_b16 v[82:83], %[tr] offset:5760\n\t" \
    "ds_read_b64_tr_b16 v[104:105], %[tr] offset:160\n\t" \
    "ds_read_b64_tr_b16 v[106:107], %[tr] offset:5792\n\t" \
    "ds_read_b64_tr_b16 v[120:121], %[tr] offset:192\n\t" \
    "ds_read_b64_tr_b16 v[122:123], %[tr] offset:5824\n\t" \
    "ds_read_b64_tr_b16 v[144:145], %[tr] offset:224\n\t" \
    "ds_read_b64_tr_b16 v[146:147], %[tr] offset:5856\n\t" \
    "ds_read_b64_tr_b16 v[160:161], %[tr] offset:256\n\t" \
    "ds_read_b64_tr_b16 v[162:163], %[tr] offset:5888\n\t" \
    "ds_read_b64_tr_b16 v[184:185], %[tr] offset:288\n\t" \
    "ds_read_b64_tr_b16 v[186:187], %[tr] offset:5920\n\t" \
    "s_barrier \n\t" \
    "s_mov_b32 %[t], 0x19000\n\t" \
    "buffer_load_dwordx4 v[8:11], %[o0], %[rs], %[t] offen" WNT "\n\t" \
    "buffer_load_dwordx4 v[32:35], %[o1], %[rs], %[t] offen" WNT "\n\t" \
    "buffer_load_dwordx4 v[48:51], %[o2], %[rs], %[t] offen" WNT "\n\t" \
    "buffer_load_dwordx4 v[72:75], %[o3], %[rs], %[t] offen" WNT "\n\t" \
    "buffer_load_dwordx4 v[88:91], %[o4], %[rs], %[t] offen" WNT "\n\t" \
    "s_mov_b32 %[t], 0x23000\n\t" \
    "buffer_load_dwordx4 v[112:115], %[o0], %[rs], %[t] offen" WNT "\n\t" \
    "buffer_load_dwordx4 v[128:131], %[o1], %[rs], %[t] offen" WNT "\n\t" \
    "buffer_load_dwordx4 v[152:155], %[o2], %[rs], %[t] offen" WNT "\n\t" \
    "buffer_load_dwordx4 v[168:171], %[o3], %[rs], %[t] offen" WNT "\n\t" \
    "buffer_load_dwordx4 v[192:195], %[o4], %[rs], %[t] offen" WNT "\n\t" \
    "s_waitcnt lgkmcnt(0)\n\t" \
    "s_barrier \n\t" \
    "s_waitcnt vmcnt(15)\n\t" \
    "ds_write_b128 %[s0], v[4:7] offset:0\n\t" \
    "ds_write_b128 %[s1], v[28:31] offset:0\n\t" \
    "ds_write_b128 %[s2], v[44:47] offset:0\n\t" \
    "ds_write_b128 %[s3], v[68:71] offset:0\n\t" \
    "ds_write_b128 %[s4], v[84:87] offset:0\n\t" \
    "s_waitcnt vmcnt(10)\n\t" \
    "ds_write_b128 %[s0], v[108:111] offset:5632\n\t" \
    "ds_write_b128 %[s1], v[124:127] offset:5632\n\t" \
    "ds_write_b128 %[s2], v[148:151] offset:5632\n\t" \
    "ds_write_b128 %[s3], v[164:167] offset:5632\n\t" \
    "ds_write_b128 %[s4], v[188:191] offset:5632\n\t" \
    "ds_read_b64_tr_b16 v[4:5], %[tr] offset:0\n\t" \
    "ds_read_b64_tr_b16 v[6:7], %[tr] offset:5632\n\t" \
    "ds_read_b64_tr_b16 v[28:29], %[tr] offset:32\n\t" \
    "ds_read_b64_tr_b16 v[30:31], %[tr] offset:5664\n\t" \
    "ds_read_b64_tr_b16 v[44:45], %[tr] offset:64\n\t" \
    "ds_read_b64_tr_b16 v[46:47], %[tr] offset:5696\n\t" \
    "ds_read_b64_tr_b16 v[68:69], %[tr] offset:96\n\t" \
    "ds_read_b64_tr_b16 v[70:71], %[tr] offset:5728\n\t" \
    "ds_read_b64_tr_b16 v[84:85], %[tr] offset:128\n\t" \
    "ds_read_b64_tr_b16 v[86:87], %[tr] offset:5760\n\t" \
    "ds_read_b64_tr_b16 v[108:109], %[tr] offset:160\n\t" \
    "ds_read_b64_tr_b16 v[110:111], %[tr] offset:5792\n\t" \
    "ds_read_b64_tr_b16 v[124:125], %[tr] offset:192\n\t" \
    "ds_read_b64_tr_b16 v[126:127], %[tr] offset:5824\n\t" \
    "ds_read_b64_tr_b16 v[148:149], %[tr] offset:224\n\t" \
    "ds_read_b64_tr_b16 v[150:151], %[tr] offset:5856\n\t" \
    "ds_read_b64_tr_b16 v[164:165], %[tr] offset:256\n\t" \
    "ds_read_b64_tr_b16 v[166:167], %[tr] offset:5888\n\t" \
    "ds_read_b64_tr_b16 v[188:189], %[tr] offset:288\n\t" \
    "ds_read_b64_tr_b16 v[190:191], %[tr] offset:5920\n\t" \
    "s_barrier \n\t" \
    "s_mov_b32 %[t], 0x2d000\n\t" \
    "buffer_load_dwordx4 v[12:15], %[o0], %[rs], %[t] offen" WNT "\n\t" \
    "buffer_load_dwordx4 v[36:39], %[o1], %[rs], %[t] offen" WNT "\n\t" \
    "buffer_load_dwordx4 v[52:55], %[o2], %[rs], %[t] offen" WNT "\n\t" \
    "buffer_load_dwordx4 v[76:79], %[o3], %[rs], %[t] offen" WNT "\n\t" \
    "buffer_load_dwordx4 v[92:95], %[o4], %[rs], %[t] offen" WNT "\n\t" \
    "s_mov_b32 %[t], 0x37000\n\t" \
    "buffer_load_dwordx4 v[116:119], %[o0], %[rs], %[t] offen" WNT "\n\t" \
    "buffer_load_dwordx4 v[132:135], %[o1], %[rs], %[t] offen" WNT "\n\t" \
    "buffer_load_dwordx4 v[156:159], %[o2], %[rs], %[t] offen" WNT "\n\t" \
    "buffer_load_dwordx4 v[172:175], %[o3], %[rs], %[t] offen" WNT "\n\t" \
    "buffer_load_dwordx4 v[196:199], %[o4], %[rs], %[t] offen" WNT "\n\t" \
    "s_waitcnt lgkmcnt(0)\n\t" \
    "s_barrier \n\t" \
    "s_waitcnt vmcnt(15)\n\t" \
    "ds_write_b128 %[s0], v[8:11] offset:0\n\t" \
    "ds_write_b128 %[s1], v[32:35] offset:0\n\t" \
    "ds_write_b128 %[s2], v[48:51] offset:0\n\t" \
    "ds_write_b128 %[s3], v[72:75] offset:0\n\t" \
    "ds_write_b128 %[s4], v[88:91] offset:0\n\t" \
    "s_waitcnt vmcnt(10)\n\t" \
    "ds_write_b128 %[s0], v[112:115] offset:5632\n\t" \
    "ds_write_b128 %[s1], v[128:131] offset:5632\n\t" \
    "ds_write_b128 %[s2], v[152:155] offset:5632\n\t" \
    "ds_write_b128 %[s3], v[168:171] offset:5632\n\t" \
    "ds_write_b128 %[s4], v[192:195] offset:5632\n\t" \
    "ds_read_b64_tr_b16 v[8:9], %[tr] offset:0\n\t" \
    "ds_read_b64_tr_b16 v[10:11], %[tr] offset:5632\n\t" \
    "ds_read_b64_tr_b16 v[32:33], %[tr] offset:32\n\t" \
    "ds_read_b64_tr_b16 v[34:35], %[tr] offset:5664\n\t" \
    "ds_read_b64_tr_b16 v[48:49], %[tr] offset:64\n\t" \
    "ds_read_b64_tr_b16 v[50:51], %[tr] offset:5696\n\t" \
    "ds_read_b64_tr_b16 v[72:73], %[tr] offset:96\n\t" \
    "ds_read_b64_tr_b16 v[74:75], %[tr] offset:5728\n\t" \
    "ds_read_b64_tr_b16 v[88:89], %[tr] offset:128\n\t" \
    "ds_read_b64_tr_b16 v[90:91], %[tr] offset:5760\n\t" \
    "ds_read_b64_tr_b16 v[112:113], %[tr] offset:160\n\t" \
    "ds_read_b64_tr_b16 v[114:115], %[tr] offset:5792\n\t" \
    "ds_read_b64_tr_b16 v[128:129], %[tr] offset:192\n\t" \
    "ds_read_b64_tr_b16 v[130:131], %[tr] offset:5824\n\t" \
    "ds_read_b64_tr_b16 v[152:153], %[tr] offset:224\n\t" \
    "ds_read_b64_tr_b16 v[154:155], %[tr] offset:5856\n\t" \
    "ds_read_b64_tr_b16 v[168:169], %[tr] offset:256\n\t" \
    "ds_read_b64_tr_b16 v[170:171], %[tr] offset:5888\n\t" \
    "ds_read_b64_tr_b16 v[192:193], %[tr] offset:288\n\t" \
    "ds_read_b64_tr_b16 v[194:195], %[tr] offset:5920\n\t" \
    "s_waitcnt lgkmcnt(0)\n\t" \
    "s_barrier \n\t" \
    "s_waitcnt vmcnt(5)\n\t" \
    "ds_write_b128 %[s0], v[12:15] offset:0\n\t" \
    "ds_write_b128 %[s1], v[36:39] offset:0\n\t" \
    "ds_write_b128 %[s2], v[52:55] offset:0\n\t" \
    "ds_write_b128 %[s3], v[76:79] offset:0\n\t" \
    "ds_write_b128 %[s4], v[92:95] offset:0\n\t" \
    "s_waitcnt vmcnt(0)\n\t" \
    "ds_write_b128 %[s0], v[116:119] offset:5632\n\t" \
    "ds_write_b128 %[s1], v[132:135] offset:5632\n\t" \
    "ds_write_b128 %[s2], v[156:159] offset:5632\n\t" \
    "ds_write_b128 %[s3], v[172:175] offset:5632\n\t" \
    "ds_write_b128 %[s4], v[196:199] offset:5632\n\t" \
    "ds_read_b64_tr_b16 v[12:13], %[tr] offset:0\n\t" \
    "ds_read_b64_tr_b16 v[14:15], %[tr] offset:5632\n\t" \
    "ds_read_b64_tr_b16 v[36:37], %[tr] offset:32\n\t" \
    "ds_read_b64_tr_b16 v[38:39], %[tr] offset:5664\n\t" \
    "ds_read_b64_tr_b16 v[52:53], %[tr] offset:64\n\t" \
    "ds_read_b64_tr_b16 v[54:55], %[tr] offset:5696\n\t" \
    "ds_read_b64_tr_b16 v[76:77], %[tr] offset:96\n\t" \
    "ds_read_b64_tr_b16 v[78:79], %[tr] offset:5728\n\t" \
    "ds_read_b64_tr_b16 v[92:93], %[tr] offset:128\n\t" \
    "ds_read_b64_tr_b16 v[94:95], %[tr] offset:5760\n\t" \
    "ds_read_b64_tr_b16 v[116:117], %[tr] offset:160\n\t" \
    "ds_read_b64_tr_b16 v[118:119], %[tr] offset:5792\n\t" \
    "ds_read_b64_tr_b16 v[132:133], %[tr] offset:192\n\t" \
    "ds_read_b64_tr_b16 v[134:135], %[tr] offset:5824\n\t" \
    "ds_read_b64_tr_b16 v[156:157], %[tr] offset:224\n\t" \
    "ds_read_b64_tr_b16 v[158:159], %[tr] offset:5856\n\t" \
    "ds_read_b64_tr_b16 v[172:173], %[tr] offset:256\n\t" \
    "ds_read_b64_tr_b16 v[174:175], %[tr] offset:5888\n\t" \
    "ds_read_b64_tr_b16 v[196:197], %[tr] offset:288\n\t" \
    "ds_read_b64_tr_b16 v[198:199], %[tr] offset:5920\n\t" \
    "s_waitcnt lgkmcnt(0)\n\t" \
    "s_barrier \n\t" \
    "ds_write_b128 %[wl], v[200:203] offset:0\n\t" \
    "ds_write_b128 %[wl], v[204:207] offset:1024\n\t" \
    "ds_write_b128 %[wl], v[208:211] offset:2048\n\t" \
    "ds_write_b128 %[wl], v[212:215] offset:3072\n\t" \
    "ds_write_b128 %[wl], v[216:219] offset:4096\n\t" \
    "ds_write_b128 %[wl], v[220:223] offset:5120\n\t" \
    "ds_write_b128 %[wl], v[224:227] offset:6144\n\t" \
    "ds_write_b128 %[wl], v[228:231] offset:7168\n\t" \
    "ds_write_b128 %[wl], v[232:235] offset:8192\n\t" \
    "ds_write_b128 %[wl], v[236:239] offset:9216\n\t" \
    "s_waitcnt lgkmcnt(0)\n\t"
#define ASM_XG_S_LO \
    "s_waitcnt lgkmcnt(0)\n\t" \
    "ds_read_b128 v[224:227], %[xa] offset:16384\n\t" \
    "ds_read_b128 v[228:231], %[xa] offset:17408\n\t" \
    "ds_read_b128 v[244:247], %[wl] offset:0\n\t" \
    "ds_read_b128 v[248:251], %[wl] offset:1024\n\t" \
    "ds_read_b128 v[232:235], %[xa] offset:0\n\t" \
    "ds_read_b128 v[236:239], %[xa] offset:1024\n\t" \
    "v_xor_b32 v252, 0x800, %[xa]\n\t" \
    "ds_read_b128 v[240:243], v252 offset:0\n\t" \
    "s_waitcnt lgkmcnt(6)\n\t" \
    "v_mfma_f32_16x16x32_bf16 v[220:223], v[8:11], v[224:227], 0\n\t" \
    "s_waitcnt lgkmcnt(5)\n\t" \
    "v_mfma_f32_16x16x32_bf16 v[220:223], v[12:15], v[228:231], v[220:223]\n\t" \
    "v_mfma_f32_16x16x32_bf16 v[216:219], v[0:3], v[224:227], 0\n\t" \
    "v_mfma_f32_16x16x32_bf16 v[216:219], v[4:7], v[228:231], v[216:219]\n\t" \
    "s_waitcnt lgkmcnt(4)\n\t" \
    "v_mfma_f32_16x16x32_bf16 v[212:215], v[244:247], v[224:227], 0\n\t" \
    "ds_read_b128 v[224:227], v252 offset:1024\n\t" \
    "s_waitcnt lgkmcnt(4)\n\t" \
    "v_mfma_f32_16x16x32_bf16 v[212:215], v[248:251], v[228:231], v[212:215]\n\t" \
    "ds_read_b128 v[228:231], v252 offset:16384\n\t" \
    "s_waitcnt lgkmcnt(4)\n\t" \
    "v_mfma_f32_16x16x32_bf16 v[200:203], v[244:247], v[232:235], 0\n\t" \
    "ds_read_b128 v[244:247], %[wl] offset:2048\n\t" \
    "s_waitcnt lgkmcnt(4)\n\t" \
    "v_mfma_f32_16x16x32_bf16 v[200:203], v[248:251], v[236:239], v[200:203]\n\t" \
    "ds_read_b128 v[248:251], %[wl] offset:3072\n\t" \
    "v_mfma_f32_16x16x32_bf16 v[204:207], v[0:3], v[232:235], 0\n\t" \
    "v_mfma_f32_16x16x32_bf16 v[204:207], v[4:7], v[236:239], v[204:207]\n\t" \
    "v_mfma_f32_16x16x32_bf16 v[208:211], v[8:11], v[232:235], 0\n\t" \
    "ds_read_b128 v[232:235], v252 offset:17408\n\t" \
    "v_mfma_f32_16x16x32_bf16 v[208:211], v[12:15], v[236:239], v[208:211]\n\t" \
    "v_xor_b32 v252, 0x1000, %[xa]\n\t" \
    "ds_read_b128 v[236:239], v252 offset:16384\n\t" \
    "s_waitcnt lgkmcnt(6)\n\t" \
    "v_mfma_f32_16x16x32_bf16 v[208:211], v[32:35], v[240:243], v[208:211]\n\t" \
    "s_waitcnt lgkmcnt(5)\n\t" \
    "v_mfma_f32_16x16x32_bf16 v[208:211], v[36:39], v[224:227], v[208:211]\n\t" \
    "v_mfma_f32_16x16x32_bf16 v[204:207], v[24:27], v[240:243], v[204:207]\n\t" \
    "v_mfma_f32_16x16x32_bf16 v[204:207], v[28:31], v[224:227], v[204:207]\n\t" \
    "v_mfma_f32_16x16x32_bf16 v[200:203], v[16:19], v[240:243], v[200:203]\n\t" \
    "ds_read_b128 v[240:243], v252 offset:17408\n\t" \
    "v_mfma_f32_16x16x32_bf16 v[200:203], v[20:23], v[224:227], v[200:203]\n\t" \
    "ds_read_b128 v[224:227], v252 offset:0\n\t" \
    "s_waitcnt lgkmcnt(6)\n\t" \
    "v_mfma_f32_16x16x32_bf16 v[212:215], v[16:19], v[228:231], v[212:215]\n\t" \
    "s_waitcnt lgkmcnt(3)\n\t" \
    "v_mfma_f32_16x16x32_bf16 v[212:215], v[20:23], v[232:235], v[212:215]\n\t" \
    "v_mfma_f32_16x16x32_bf16 v[216:219], v[24:27], v[228:231], v[216:219]\n\t" \
    "v_mfma_f32_16x16x32_bf16 v[216:219], v[28:31], v[232:235], v[216:219]\n\t" \
    "v_mfma_f32_16x16x32_bf16 v[220:223], v[32:35], v[228:231], v[220:223]\n\t" \
    "ds_read_b128 v[228:231], v252 offset:1024\n\t" \
    "v_mfma_f32_16x16x32_bf16 v[220:223], v[36:39], v[232:235], v[220:223]\n\t" \
    "v_xor_b32 v252, 0x1800, %[xa]\n\t" \
    "ds_read_b128 v[232:235], v252 offset:0\n\t" \
    "s_waitcnt lgkmcnt(4)\n\t" \
    "v_mfma_f32_16x16x32_bf16 v[220:223], v[48:51], v[236:239], v[220:223]\n\t" \
    "s_waitcnt lgkmcnt(3)\n\t" \
    "v_mfma_f32_16x16x32_bf16 v[220:223], v[52:55], v[240:243], v[220:223]\n\t" \
    "v_mfma_f32_16x16x32_bf16 v[216:219], v[40:43], v[236:239], v[216:219]\n\t" \
    "v_mfma_f32_16x16x32_bf16 v[216:219], v[44:47], v[240:243], v[216:219]\n\t" \
    "v_mfma_f32_16x16x32_bf16 v[212:215], v[244:247], v[236:239], v[212:215]\n\t" \
    "ds_read_b128 v[236:239], v252 offset:1024\n\t" \
    "v_mfma_f32_16x16x32_bf16 v[212:215], v[248:251], v[240:243], v[212:215]\n\t" \
    "ds_read_b128 v[240:243], v252 offset:16384\n\t" \
    "s_waitcnt lgkmcnt(4)\n\t" \
    "v_mfma_f32_16x16x32_bf16 v[200:203], v[244:247], v[224:227], v[200:203]\n\t" \
    "ds_read_b128 v[244:247], %[wl] offset:8192\n\t" \
    "s_waitcnt lgkmcnt(4)\n\t" \
    "v_mfma_f32_16x16x32_bf16 v[200:203], v[248:251], v[228:231], v[200:203]\n\t" \
    "ds_read_b128 v[248:251], %[wl] offset:9216\n\t" \
    "v_mfma_f32_16x16x32_bf16 v[204:207], v[40:43], v[224:227], v[204:207]\n\t" \
    "v_mfma_f32_16x16x32_bf16 v[204:207], v[44:47], v[228:231], v[204:207]\n\t" \
    "v_mfma_f32_16x16x32_bf16 v[208:211], v[48:51], v[224:227], v[208:211]\n\t" \
    "ds_read_b128 v[224:227], v252 offset:17408\n\t" \
    "v_mfma_f32_16x16x32_bf16 v[208:211], v[52:55], v[228:231], v[208:211]\n\t" \
    "v_and_b32 v252, 0x3ff, %[xa]\n\t" \
    "ds_read_b128 v[228:231], v252 offset:45056\n\t" \
    "s_waitcnt lgkmcnt(6)\n\t" \
    "v_mfma_f32_16x16x32_bf16 v[208:211], v[72:75], v[232:235], v[208:211]\n\t" \
    "s_waitcnt lgkmcnt(5)\n\t" \
    "v_mfma_f32_16x16x32_bf16 v[208:211], v[76:79], v[236:239], v[208:211]\n\t" \
    "v_mfma_f32_16x16x32_bf16 v[204:207], v[64:67], v[232:235], v[204:207]\n\t" \
    "v_mfma_f32_16x16x32_bf16 v[204:207], v[68:71], v[236:239], v[204:207]\n\t" \
    "v_mfma_f32_16x16x32_bf16 v[200:203], v[56:59], v[232:235], v[200:203]\n\t" \
    "ds_read_b128 v[232:235], v252 offset:46080\n\t" \
    "v_mfma_f32_16x16x32_bf16 v[200:203], v[60:63], v[236:239], v[200:203]\n\t" \
    "ds_read_b128 v[236:239], v252 offset:40960\n\t" \
    "s_waitcnt lgkmcnt(6)\n\t" \
    "v_mfma_f32_16x16x32_bf16 v[212:215], v[56:59], v[240:243], v[212:215]\n\t" \
    "s_waitcnt lgkmcnt(3)\n\t" \
    "v_mfma_f32_16x16x32_bf16 v[212:215], v[60:63], v[224:227], v[212:215]\n\t" \
    "v_mfma_f32_16x16x32_bf16 v[216:219], v[64:67], v[240:243], v[216:219]\n\t" \
    "v_mfma_f32_16x16x32_bf16 v[216:219], v[68:71], v[224:227], v[216:219]\n\t" \
    "v_mfma_f32_16x16x32_bf16 v[220:223], v[72:75], v[240:243], v[220:223]\n\t" \
    "ds_read_b128 v[240:243], v252 offset:41984\n\t" \
    "v_mfma_f32_16x16x32_bf16 v[220:223], v[76:79], v[224:227], v[220:223]\n\t" \
    "s_waitcnt lgkmcnt(0)\n\t" \
    "s_barrier\n\t" ASM_ST \
    "v_xor_b32 v252, 0x2000, %[xa]\n\t" \
    "ds_read_b128 v[224:227], v252 offset:0\n\t" \
    "v_mfma_f32_16x16x32_bf16 v[220:223], v[168:171], v[228:231], v[220:223]\n\t" \
    "v_mfma_f32_16x16x32_bf16 v[220:223], v[172:175], v[232:235], v[220:223]\n\t" \
    "v_mfma_f32_16x16x32_bf16 v[216:219], v[160:163], v[228:231], v[216:219]\n\t" \
    "v_mfma_f32_16x16x32_bf16 v[216:219], v[164:167], v[232:235], v[216:219]\n\t" \
    "v_mfma_f32_16x16x32_bf16 v[212:215], v[244:247], v[228:231], v[212:215]\n\t" \
    "ds_read_b128 v[228:231], v252 offset:1024\n\t" \
    "v_mfma_f32_16x16x32_bf16 v[212:215], v[248:251], v[232:235], v[212:215]\n\t" \
    "ds_read_b128 v[232:235], v252 offset:16384\n\t" \
    "v_mfma_f32_16x16x32_bf16 v[200:203], v[244:247], v[236:239], v[200:203]\n\t" \
    "ds_read_b128 v[244:247], %[wl] offset:4096\n\t" \
    "v_mfma_f32_16x16x32_bf16 v[200:203], v[248:251], v[240:243], v[200:203]\n\t" \
    "ds_read_b128 v[248:251], %[wl] offset:5120\n\t" \
    "v_mfma_f32_16x16x32_bf16 v[204:207], v[160:163], v[236:239], v[204:207]\n\t" \
    "v_mfma_f32_16x16x32_bf16 v[204:207], v[164:167], v[240:243], v[204:207]\n\t" \
    "v_mfma_f32_16x16x32_bf16 v[208:211], v[168:171], v[236:239], v[208:211]\n\t" \
    "ds_read_b128 v[236:239], v252 offset:17408\n\t" \
    "v_mfma_f32_16x16x32_bf16 v[208:211], v[172:175], v[240:243], v[208:211]\n\t" \
    "v_xor_b32 v252, 0x2800, %[xa]\n\t" \
    "ds_read_b128 v[240:243], v252 offset:16384\n\t" \
    "s_waitcnt lgkmcnt(6)\n\t" \
    "v_mfma_f32_16x16x32_bf16 v[208:211], v[88:91], v[224:227], v[208:211]\n\t" \
    "s_waitcnt lgkmcnt(5)\n\t" \
    "v_mfma_f32_16x16x32_bf16 v[208:211], v[92:95], v[228:231], v[208:211]\n\t" \
    "v_mfma_f32_16x16x32_bf16 v[204:207], v[80:83], v[224:227], v[204:207]\n\t" \
    "v_mfma_f32_16x16x32_bf16 v[204:207], v[84:87], v[228:231], v[204:207]\n\t" \
    "s_waitcnt lgkmcnt(3)\n\t" \
    "v_mfma_f32_16x16x32_bf16 v[200:203], v[244:247], v[224:227], v[200:203]\n\t" \
    "ds_read_b128 v[224:227], v252 offset:17408\n\t" \
    "s_waitcnt lgkmcnt(3)\n\t" \
    "v_mfma_f32_16x16x32_bf16 v[200:203], v[248:251], v[228:231], v[200:203]\n\t" \
    "ds_read_b128 v[228:231], v252 offset:0\n\t" \
    "v_mfma_f32_16x16x32_bf16 v[212:215], v[244:247], v[232:235], v[212:215]\n\t" \
    "ds_read_b128 v[244:247], %[wl] offset:6144\n\t" \
    "s_waitcnt lgkmcnt(4)\n\t" \
    "v_mfma_f32_16x16x32_bf16 v[212:215], v[248:251], v[236:239], v[212:215]\n\t" \
    "ds_read_b128 v[248:251], %[wl] offset:7168\n\t" \
    "v_mfma_f32_16x16x32_bf16 v[216:219], v[80:83], v[232:235], v[216:219]\n\t" \
    "v_mfma_f32_16x16x32_bf16 v[216:219], v[84:87], v[236:239], v[216:219]\n\t" \
    "v_mfma_f32_16x16x32_bf16 v[220:223], v[88:91], v[232:235], v[220:223]\n\t" \
    "ds_read_b128 v[232:235], v252 offset:1024\n\t" \
    "v_mfma_f32_16x16x32_bf16 v[220:223], v[92:95], v[236:239], v[220:223]\n\t" \
    "v_xor_b32 v252, 0x3000, %[xa]\n\t" \
    "ds_read_b128 v[236:239], v252 offset:0\n\t" \
    "s_waitcnt lgkmcnt(6)\n\t" \
    "v_mfma_f32_16x16x32_bf16 v[220:223], v[112:115], v[240:243], v[220:223]\n\t" \
    "s_waitcnt lgkmcnt(5)\n\t" \
    "v_mfma_f32_16x16x32_bf16 v[220:223], v[116:119], v[224:227], v[220:223]\n\t" \
    "v_mfma_f32_16x16x32_bf16 v[216:219], v[104:107], v[240:243], v[216:219]\n\t" \
    "v_mfma_f32_16x16x32_bf16 v[216:219], v[108:111], v[224:227], v[216:219]\n\t" \
    "v_mfma_f32_16x16x32_bf16 v[212:215], v[96:99], v[240:243], v[212:215]\n\t" \
    "ds_read_b128 v[240:243], v252 offset:1024\n\t" \
    "v_mfma_f32_16x16x32_bf16 v[212:215], v[100:103], v[224:227], v[212:215]\n\t" \
    "ds_read_b128 v[224:227], v252 offset:16384\n\t" \
    "s_waitcnt lgkmcnt(6)\n\t" \
    "v_mfma_f32_16x16x32_bf16 v[200:203], v[96:99], v[228:231], v[200:203]\n\t" \
    "s_waitcnt lgkmcnt(3)\n\t" \
    "v_mfma_f32_16x16x32_bf16 v[200:203], v[100:103], v[232:235], v[200:203]\n\t" \
    "v_mfma_f32_16x16x32_bf16 v[204:207], v[104:107], v[228:231], v[204:207]\n\t" \
    "v_mfma_f32_16x16x32_bf16 v[204:207], v[108:111], v[232:235], v[204:207]\n\t" \
    "v_mfma_f32_16x16x32_bf16 v[208:211], v[112:115], v[228:231], v[208:211]\n\t" \
    "ds_read_b128 v[228:231], v252 offset:17408\n\t" \
    "v_mfma_f32_16x16x32_bf16 v[208:211], v[116:119], v[232:235], v[208:211]\n\t" \
    "v_xor_b32 v252, 0x3800, %[xa]\n\t" \
    "ds_read_b128 v[232:235], v252 offset:16384\n\t" \
    "s_waitcnt lgkmcnt(4)\n\t" \
    "v_mfma_f32_16x16x32_bf16 v[208:211], v[128:131], v[236:239], v[208:211]\n\t" \
    "s_waitcnt lgkmcnt(3)\n\t" \
    "v_mfma_f32_16x16x32_bf16 v[208:211], v[132:135], v[240:243], v[208:211]\n\t" \
    "v_mfma_f32_16x16x32_bf16 v[204:207], v[120:123], v[236:239], v[204:207]\n\t" \
    "v_mfma_f32_16x16x32_bf16 v[204:207], v[124:127], v[240:243], v[204:207]\n\t" \
    "v_mfma_f32_16x16x32_bf16 v[200:203], v[244:247], v[236:239], v[200:203]\n\t" \
    "ds_read_b128 v[236:239], v252 offset:17408\n\t" \
    "v_mfma_f32_16x16x32_bf16 v[200:203], v[248:251], v[240:243], v[200:203]\n\t" \
    "ds_read_b128 v[240:243], v252 offset:0\n\t" \
    "s_waitcnt lgkmcnt(4)\n\t" \
    "v_mfma_f32_16x16x32_bf16 v[212:215], v[244:247], v[224:227], v[212:215]\n\t" \
    "s_waitcnt lgkmcnt(3)\n\t" \
    "v_mfma_f32_16x16x32_bf16 v[212:215], v[248:251], v[228:231], v[212:215]\n\t" \
    "v_mfma_f32_16x16x32_bf16 v[216:219], v[120:123], v[224:227], v[216:219]\n\t" \
    "v_mfma_f32_16x16x32_bf16 v[216:219], v[124:127], v[228:231], v[216:219]\n\t" \
    "v_mfma_f32_16x16x32_bf16 v[220:223], v[128:131], v[224:227], v[220:223]\n\t" \
    "ds_read_b128 v[224:227], v252 offset:1024\n\t" \
    "v_mfma_f32_16x16x32_bf16 v[220:223], v[132:135], v[228:231], v[220:223]\n\t" \
    "v_and_b32 v252, 0x3ff, %[xa]\n\t" \
    "ds_read_b128 v[228:231], v252 offset:43008\n\t" \
    "s_waitcnt lgkmcnt(4)\n\t" \
    "v_mfma_f32_16x16x32_bf16 v[220:223], v[152:155], v[232:235], v[220:223]\n\t" \
    "v_mov_b64 v[244:245], 0\n\t" \
    "s_waitcnt lgkmcnt(3)\n\t" \
    "v_mfma_f32_16x16x32_bf16 v[220:223], v[156:159], v[236:239], v[220:223]\n\t" \
    "v_mov_b64 v[246:247], 0\n\t" \
    "v_mfma_f32_16x16x32_bf16 v[216:219], v[144:147], v[232:235], v[216:219]\n\t" \
    "v_mov_b64 v[248:249], 0\n\t" \
    "v_mfma_f32_16x16x32_bf16 v[216:219], v[148:151], v[236:239], v[216:219]\n\t" \
    "v_mov_b64 v[250:251], 0\n\t" \
    "v_mfma_f32_16x16x32_bf16 v[212:215], v[136:139], v[232:235], v[212:215]\n\t" \
    "ds_read_b128 v[232:235], v252 offset:44032\n\t" \
    "v_mfma_f32_16x16x32_bf16 v[212:215], v[140:143], v[236:239], v[212:215]\n\t" \
    "ds_read_b128 v[236:239], v252 offset:47104\n\t" \
    "s_waitcnt lgkmcnt(4)\n\t" \
    "v_mfma_f32_16x16x32_bf16 v[200:203], v[136:139], v[240:243], v[200:203]\n\t" \
    "s_waitcnt lgkmcnt(3)\n\t" \
    "v_mfma_f32_16x16x32_bf16 v[200:203], v[140:143], v[224:227], v[200:203]\n\t" \
    "v_mfma_f32_16x16x32_bf16 v[204:207], v[144:147], v[240:243], v[204:207]\n\t" \
    "v_mfma_f32_16x16x32_bf16 v[204:207], v[148:151], v[224:227], v[204:207]\n\t" \
    "v_mfma_f32_16x16x32_bf16 v[208:211], v[152:155], v[240:243], v[208:211]\n\t" \
    "ds_read_b128 v[240:243], v252 offset:48128\n\t" \
    "v_mfma_f32_16x16x32_bf16 v[208:211], v[156:159], v[224:227], v[208:211]\n\t" \
    "s_waitcnt lgkmcnt(3)\n\t" \
    "v_mfma_f32_16x16x32_bf16 v[208:211], v[192:195], v[228:231], v[208:211]\n\t" \
    "v_add_u32 v252, %[qs], v252\n\t" \
    "v_mov_b64 v[224:225], 0\n\t" \
    "s_waitcnt lgkmcnt(2)\n\t" \
    "v_mfma_f32_16x16x32_bf16 v[208:211], v[196:199], v[232:235], v[208:211]\n\t" \
    "v_mov_b64 v[226:227], 0\n\t" \
    "v_mfma_f32_16x16x32_bf16 v[204:207], v[184:187], v[228:231], v[204:207]\n\t" \
    "v_mfma_f32_16x16x32_bf16 v[204:207], v[188:191], v[232:235], v[204:207]\n\t" \
    "v_mfma_f32_16x16x32_bf16 v[200:203], v[176:179], v[228:231], v[200:203]\n\t" \
    "v_mfma_f32_16x16x32_bf16 v[200:203], v[180:183], v[232:235], v[200:203]\n\t" \
    "s_waitcnt lgkmcnt(1)\n\t" \
    "v_mfma_f32_16x16x32_bf16 v[212:215], v[176:179], v[236:239], v[212:215]\n\t" \
    "ds_read_b128 v[228:231], %[wl] offset:2048\n\t" \
    "ds_read_b128 v[232:235], %[wl] offset:3072\n\t" \
    "s_waitcnt lgkmcnt(2)\n\t" \
    "v_mfma_f32_16x16x32_bf16 v[212:215], v[180:183], v[240:243], v[212:215]\n\t" \
    "v_cvt_pk_bf16_f32 v208, v208, v209\n\t" \
    "v_cvt_pk_bf16_f32 v209, v210, v211\n\t" \
    "v_mfma_f32_16x16x32_bf16 v[216:219], v[184:187], v[236:239], v[216:219]\n\t" \
    "ds_write_b64 v252, v[208:209] offset:2048\n\t" \
    "v_cvt_pk_bf16_f32 v204, v204, v205\n\t" \
    "v_mfma_f32_16x16x32_bf16 v[216:219], v[188:191], v[240:243], v[216:219]\n\t" \
    "v_cvt_pk_bf16_f32 v205, v206, v207\n\t" \
    "ds_write_b64 v252, v[204:205] offset:1024\n\t" \
    "v_mfma_f32_16x16x32_bf16 v[220:223], v[192:195], v[236:239], v[220:223]\n\t" \
    "v_cvt_pk_bf16_f32 v200, v200, v201\n\t" \
    "v_cvt_pk_bf16_f32 v201, v202, v203\n\t" \
    "v_mfma_f32_16x16x32_bf16 v[220:223], v[196:199], v[240:243], v[220:223]\n\t" \
    "ds_write_b64 v252, v[200:201] offset:0\n\t" \
    "ds_read_b128 v[200:203], %[wl] offset:0\n\t" \
    "ds_read_b128 v[204:207], %[wl] offset:1024\n\t" \
    "v_cvt_pk_bf16_f32 v212, v212, v213\n\t" \
    "v_cvt_pk_bf16_f32 v213, v214, v215\n\t" \
    "ds_write_b64 v252, v[212:213] offset:6144\n\t" \
    "v_cvt_pk_bf16_f32 v216, v216, v217\n\t" \
    "v_cvt_pk_bf16_f32 v217, v218, v219\n\t" \
    "ds_write_b64 v252, v[216:217] offset:7168\n\t" \
    "v_cvt_pk_bf16_f32 v220, v220, v221\n\t" \
    "v_cvt_pk_bf16_f32 v221, v222, v223\n\t" \
    "ds_write_b64 v252, v[220:221] offset:8192\n\t" \
    "s_waitcnt lgkmcnt(0)\n\t"
#define ASM_XG_S_HI \
    "s_waitcnt lgkmcnt(0)\n\t" \
    "v_xor_b32 v252, 0x2000, %[xa]\n\t" \
    "ds_read_b128 v[224:227], v252 offset:16384\n\t" \
    "ds_read_b128 v[228:231], v252 offset:17408\n\t" \
    "ds_read_b128 v[244:247], %[wl] offset:4096\n\t" \
    "ds_read_b128 v[248:251], %[wl] offset:5120\n\t" \
    "ds_read_b128 v[232:235], v252 offset:0\n\t" \
    "ds_read_b128 v[236:239], v252 offset:1024\n\t" \
    "v_xor_b32 v252, 0x2800, %[xa]\n\t" \
    "ds_read_b128 v[240:243], v252 offset:0\n\t" \
    "s_cmp_eq_u32 %[slp], 0\n\t" \
    "s_cbranch_scc1 .Lxs%=\n\t" \
    "s_sleep 10\n\t" \
    ".Lxs%=:\n\t" \
    "s_waitcnt lgkmcnt(6)\n\t" \
    "v_mfma_f32_16x16x32_bf16 v[220:223], v[88:91], v[224:227], 0\n\t" \
    "s_waitcnt lgkmcnt(5)\n\t" \
    "v_mfma_f32_16x16x32_bf16 v[220:223], v[92:95], v[228:231], v[220:223]\n\t" \
    "v_mfma_f32_16x16x32_bf16 v[216:219], v[80:83], v[224:227], 0\n\t" \
    "v_mfma_f32_16x16x32_bf16 v[216:219], v[84:87], v[228:231], v[216:219]\n\t" \
    "s_waitcnt lgkmcnt(4)\n\t" \
    "v_mfma_f32_16x16x32_bf16 v[212:215], v[244:247], v[224:227], 0\n\t" \
    "ds_read_b128 v[224:227], v252 offset:1024\n\t" \
    "s_waitcnt lgkmcnt(4)\n\t" \
    "v_mfma_f32_16x16x32_bf16 v[212:215], v[248:251], v[228:231], v[212:215]\n\t" \
    "ds_read_b128 v[228:231], v252 offset:16384\n\t" \
    "s_waitcnt lgkmcnt(4)\n\t" \
    "v_mfma_f32_16x16x32_bf16 v[200:203], v[244:247], v[232:235], 0\n\t" \
    "ds_read_b128 v[244:247], %[wl] offset:6144\n\t" \
    "s_waitcnt lgkmcnt(4)\n\t" \
    "v_mfma_f32_16x16x32_bf16 v[200:203], v[248:251], v[236:239], v[200:203]\n\t" \
    "ds_read_b128 v[248:251], %[wl] offset:7168\n\t" \
    "v_mfma_f32_16x16x32_bf16 v[204:207], v[80:83], v[232:235], 0\n\t" \
    "v_mfma_f32_16x16x32_bf16 v[204:207], v[84:87], v[236:239], v[204:207]\n\t" \
    "v_mfma_f32_16x16x32_bf16 v[208:211], v[88:91], v[232:235], 0\n\t" \
    "ds_read_b128 v[232:235], v252 offset:17408\n\t" \
    "v_mfma_f32_16x16x32_bf16 v[208:211], v[92:95], v[236:239], v[208:211]\n\t" \
    "v_xor_b32 v252, 0x3000, %[xa]\n\t" \
    "ds_read_b128 v[236:239], v252 offset:16384\n\t" \
    "s_waitcnt lgkmcnt(6)\n\t" \
    "v_mfma_f32_16x16x32_bf16 v[208:211], v[112:115], v[240:243], v[208:211]\n\t" \
    "s_waitcnt lgkmcnt(5)\n\t" \
    "v_mfma_f32_16x16x32_bf16 v[208:211], v[116:119], v[224:227], v[208:211]\n\t" \
    "v_mfma_f32_16x16x32_bf16 v[204:207], v[104:107], v[240:243], v[204:207]\n\t" \
    "v_mfma_f32_16x16x32_bf16 v[204:207], v[108:111], v[224:227], v[204:207]\n\t" \
    "v_mfma_f32_16x16x32_bf16 v[200:203], v[96:99], v[240:243], v[200:203]\n\t" \
    "ds_read_b128 v[240:243], v252 offset:17408\n\t" \
    "v_mfma_f32_16x16x32_bf16 v[200:203], v[100:103], v[224:227], v[200:203]\n\t" \
    "ds_read_b128 v[224:227], v252 offset:0\n\t" \
    "s_waitcnt lgkmcnt(6)\n\t" \
    "v_mfma_f32_16x16x32_bf16 v[212:215], v[96:99], v[228:231], v[212:215]\n\t" \
    "s_waitcnt lgkmcnt(3)\n\t" \
    "v_mfma_f32_16x16x32_bf16 v[212:215], v[100:103], v[232:235], v[212:215]\n\t" \
    "v_mfma_f32_16x16x32_bf16 v[216:219], v[104:107], v[228:231], v[216:219]\n\t" \
    "v_mfma_f32_16x16x32_bf16 v[216:219], v[108:111], v[232:235], v[216:219]\n\t" \
    "v_mfma_f32_16x16x32_bf16 v[220:223], v[112:115], v[228:231], v[220:223]\n\t" \
    "ds_read_b128 v[228:231], v252 offset:1024\n\t" \
    "v_mfma_f32_16x16x32_bf16 v[220:223], v[116:119], v[232:235], v[220:223]\n\t" \
    "v_xor_b32 v252, 0x3800, %[xa]\n\t" \
    "ds_read_b128 v[232:235], v252 offset:0\n\t" \
    "s_waitcnt lgkmcnt(4)\n\t" \
    "v_mfma_f32_16x16x32_bf16 v[220:223], v[128:131], v[236:239], v[220:223]\n\t" \
    "s_waitcnt lgkmcnt(3)\n\t" \
    "v_mfma_f32_16x16x32_bf16 v[220:223], v[132:135], v[240:243], v[220:223]\n\t" \
    "v_mfma_f32_16x16x32_bf16 v[216:219], v[120:123], v[236:239], v[216:219]\n\t" \
    "v_mfma_f32_16x16x32_bf16 v[216:219], v[124:127], v[240:243], v[216:219]\n\t" \
    "v_mfma_f32_16x16x32_bf16 v[212:215], v[244:247], v[236:239], v[212:215]\n\t" \
    "ds_read_b128 v[236:239], v252 offset:1024\n\t" \
    "v_mfma_f32_16x16x32_bf16 v[212:215], v[248:251], v[240:243], v[212:215]\n\t" \
    "ds_read_b128 v[240:243], v252 offset:16384\n\t" \
    "s_waitcnt lgkmcnt(4)\n\t" \
    "v_mfma_f32_16x16x32_bf16 v[200:203], v[244:247], v[224:227], v[200:203]\n\t" \
    "ds_read_b128 v[244:247], %[wl] offset:8192\n\t" \
    "s_waitcnt lgkmcnt(4)\n\t" \
    "v_mfma_f32_16x16x32_bf16 v[200:203], v[248:251], v[228:231], v[200:203]\n\t" \
    "ds_read_b128 v[248:251], %[wl] offset:9216\n\t" \
    "v_mfma_f32_16x16x32_bf16 v[204:207], v[120:123], v[224:227], v[204:207]\n\t" \
    "v_mfma_f32_16x16x32_bf16 v[204:207], v[124:127], v[228:231], v[204:207]\n\t" \
    "v_mfma_f32_16x16x32_bf16 v[208:211], v[128:131], v[224:227], v[208:211]\n\t" \
    "ds_read_b128 v[224:227], v252 offset:17408\n\t" \
    "v_mfma_f32_16x16x32_bf16 v[208:211], v[132:135], v[228:231], v[208:211]\n\t" \
    "v_and_b32 v252, 0x3ff, %[xa]\n\t" \
    "ds_read_b128 v[228:231], v252 offset:45056\n\t" \
    "s_waitcnt lgkmcnt(6)\n\t" \
    "v_mfma_f32_16x16x32_bf16 v[208:211], v[152:155], v[232:235], v[208:211]\n\t" \
    "s_waitcnt lgkmcnt(5)\n\t" \
    "v_mfma_f32_16x16x32_bf16 v[208:211], v[156:159], v[236:239], v[208:211]\n\t" \
    "v_mfma_f32_16x16x32_bf16 v[204:207], v[144:147], v[232:235], v[204:207]\n\t" \
    "v_mfma_f32_16x16x32_bf16 v[204:207], v[148:151], v[236:239], v[204:207]\n\t" \
    "v_mfma_f32_16x16x32_bf16 v[200:203], v[136:139], v[232:235], v[200:203]\n\t" \
    "ds_read_b128 v[232:235], v252 offset:46080\n\t" \
    "v_mfma_f32_16x16x32_bf16 v[200:203], v[140:143], v[236:239], v[200:203]\n\t" \
    "ds_read_b128 v[236:239], v252 offset:40960\n\t" \
    "s_waitcnt lgkmcnt(6)\n\t" \
    "v_mfma_f32_16x16x32_bf16 v[212:215], v[136:139], v[240:243], v[212:215]\n\t" \
    "s_waitcnt lgkmcnt(3)\n\t" \
    "v_mfma_f32_16x16x32_bf16 v[212:215], v[140:143], v[224:227], v[212:215]\n\t" \
    "v_mfma_f32_16x16x32_bf16 v[216:219], v[144:147], v[240:243], v[216:219]\n\t" \
    "v_mfma_f32_16x16x32_bf16 v[216:219], v[148:151], v[224:227], v[216:219]\n\t" \
    "v_mfma_f32_16x16x32_bf16 v[220:223], v[152:155], v[240:243], v[220:223]\n\t" \
    "ds_read_b128 v[240:243], v252 offset:41984\n\t" \
    "v_mfma_f32_16x16x32_bf16 v[220:223], v[156:159], v[224:227], v[220:223]\n\t" \
    "s_waitcnt vmcnt(0) lgkmcnt(0)\n\t" \
    "s_barrier\n\t" ASM_ST \
    "s_mov_b32 m0, %[d0]\n\t" \
    "s_nop 0\n\t" \
    "global_load_lds_dwordx4 v252, %[g0] offset:0 sc1\n\t" \
    "global_load_lds_dwordx4 v252, %[g0] offset:1024 sc1\n\t" \
    "global_load_lds_dwordx4 v252, %[g0] offset:2048 sc1\n\t" \
    "global_load_lds_dwordx4 v252, %[g0] offset:3072 sc1\n\t" \
    "s_mov_b32 m0, %[d1]\n\t" \
    "s_nop 0\n\t" \
    "global_load_lds_dwordx4 v252, %[g1] sc1\n\t" \
    "ds_read_b128 v[224:227], %[xa] offset:0\n\t" \
    "v_mfma_f32_16x16x32_bf16 v[220:223], v[168:171], v[228:231], v[220:223]\n\t" \
    "v_mfma_f32_16x16x32_bf16 v[220:223], v[172:175], v[232:235], v[220:223]\n\t" \
    "v_mfma_f32_16x16x32_bf16 v[216:219], v[160:163], v[228:231], v[216:219]\n\t" \
    "v_mfma_f32_16x16x32_bf16 v[216:219], v[164:167], v[232:235], v[216:219]\n\t" \
    "v_mfma_f32_16x16x32_bf16 v[212:215], v[244:247], v[228:231], v[212:215]\n\t" \
    "ds_read_b128 v[228:231], %[xa] offset:1024\n\t" \
    "v_mfma_f32_16x16x32_bf16 v[212:215], v[248:251], v[232:235], v[212:215]\n\t" \
    "ds_read_b128 v[232:235], %[xa] offset:16384\n\t" \
    "v_mfma_f32_16x16x32_bf16 v[200:203], v[244:247], v[236:239], v[200:203]\n\t" \
    "ds_read_b128 v[244:247], %[wl] offset:0\n\t" \
    "v_mfma_f32_16x16x32_bf16 v[200:203], v[248:251], v[240:243], v[200:203]\n\t" \
    "ds_read_b128 v[248:251], %[wl] offset:1024\n\t" \
    "v_mfma_f32_16x16x32_bf16 v[204:207], v[160:163], v[236:239], v[204:207]\n\t" \
    "v_mfma_f32_16x16x32_bf16 v[204:207], v[164:167], v[240:243], v[204:207]\n\t" \
    "v_mfma_f32_16x16x32_bf16 v[208:211], v[168:171], v[236:239], v[208:211]\n\t" \
    "ds_read_b128 v[236:239], %[xa] offset:17408\n\t" \
    "v_mfma_f32_16x16x32_bf16 v[208:211], v[172:175], v[240:243], v[208:211]\n\t" \
    "v_xor_b32 v252, 0x800, %[xa]\n\t" \
    "ds_read_b128 v[240:243], v252 offset:16384\n\t" \
    "s_waitcnt lgkmcnt(6)\n\t" \
    "v_mfma_f32_16x16x32_bf16 v[208:211], v[8:11], v[224:227], v[208:211]\n\t" \
    "s_waitcnt lgkmcnt(5)\n\t" \
    "v_mfma_f32_16x16x32_bf16 v[208:211], v[12:15], v[228:231], v[208:211]\n\t" \
    "v_mfma_f32_16x16x32_bf16 v[204:207], v[0:3], v[224:227], v[204:207]\n\t" \
    "v_mfma_f32_16x16x32_bf16 v[204:207], v[4:7], v[228:231], v[204:207]\n\t" \
    "s_waitcnt lgkmcnt(3)\n\t" \
    "v_mfma_f32_16x16x32_bf16 v[200:203], v[244:247], v[224:227], v[200:203]\n\t" \
    "ds_read_b128 v[224:227], v252 offset:17408\n\t" \
    "s_waitcnt lgkmcnt(3)\n\t" \
    "v_mfma_f32_16x16x32_bf16 v[200:203], v[248:251], v[228:231], v[200:203]\n\t" \
    "ds_read_b128 v[228:231], v252 offset:0\n\t" \
    "v_mfma_f32_16x16x32_bf16 v[212:215], v[244:247], v[232:235], v[212:215]\n\t" \
    "ds_read_b128 v[244:247], %[wl] offset:2048\n\t" \
    "s_waitcnt lgkmcnt(4)\n\t" \
    "v_mfma_f32_16x16x32_bf16 v[212:215], v[248:251], v[236:239], v[212:215]\n\t" \
    "ds_read_b128 v[248:251], %[wl] offset:3072\n\t" \
    "v_mfma_f32_16x16x32_bf16 v[216:219], v[0:3], v[232:235], v[216:219]\n\t" \
    "v_mfma_f32_16x16x32_bf16 v[216:219], v[4:7], v[236:239], v[216:219]\n\t" \
    "v_mfma_f32_16x16x32_bf16 v[220:223], v[8:11], v[232:235], v[220:223]\n\t" \
    "ds_read_b128 v[232:235], v252 offset:1024\n\t" \
    "v_mfma_f32_16x16x32_bf16 v[220:223], v[12:15], v[236:239], v[220:223]\n\t" \
    "v_xor_b32 v252, 0x1000, %[xa]\n\t" \
    "ds_read_b128 v[236:239], v252 offset:0\n\t" \
    "s_waitcnt lgkmcnt(6)\n\t" \
    "v_mfma_f32_16x16x32_bf16 v[220:223], v[32:35], v[240:243], v[220:223]\n\t" \
    "s_waitcnt lgkmcnt(5)\n\t" \
    "v_mfma_f32_16x16x32_bf16 v[220:223], v[36:39], v[224:227], v[220:223]\n\t" \
    "v_mfma_f32_16x16x32_bf16 v[216:219], v[24:27], v[240:243], v[216:219]\n\t" \
    "v_mfma_f32_16x16x32_bf16 v[216:219], v[28:31], v[224:227], v[216:219]\n\t" \
    "v_mfma_f32_16x16x32_bf16 v[212:215], v[16:19], v[240:243], v[212:215]\n\t" \
    "ds_read_b128 v[240:243], v252 offset:1024\n\t" \
    "v_mfma_f32_16x16x32_bf16 v[212:215], v[20:23], v[224:227], v[212:215]\n\t" \
    "ds_read_b128 v[224:227], v252 offset:16384\n\t" \
    "s_waitcnt lgkmcnt(6)\n\t" \
    "v_mfma_f32_16x16x32_bf16 v[200:203], v[16:19], v[228:231], v[200:203]\n\t" \
    "s_waitcnt lgkmcnt(3)\n\t" \
    "v_mfma_f32_16x16x32_bf16 v[200:203], v[20:23], v[232:235], v[200:203]\n\t" \
    "v_mfma_f32_16x16x32_bf16 v[204:207], v[24:27], v[228:231], v[204:207]\n\t" \
    "v_mfma_f32_16x16x32_bf16 v[204:207], v[28:31], v[232:235], v[204:207]\n\t" \
    "v_mfma_f32_16x16x32_bf16 v[208:211], v[32:35], v[228:231], v[208:211]\n\t" \
    "ds_read_b128 v[228:231], v252 offset:17408\n\t" \
    "v_mfma_f32_16x16x32_bf16 v[208:211], v[36:39], v[232:235], v[208:211]\n\t" \
    "v_xor_b32 v252, 0x1800, %[xa]\n\t" \
    "ds_read_b128 v[232:235], v252 offset:16384\n\t" \
    "s_waitcnt lgkmcnt(4)\n\t" \
    "v_mfma_f32_16x16x32_bf16 v[208:211], v[48:51], v[236:239], v[208:211]\n\t" \
    "s_waitcnt lgkmcnt(3)\n\t" \
    "v_mfma_f32_16x16x32_bf16 v[208:211], v[52:55], v[240:243], v[208:211]\n\t" \
    "v_mfma_f32_16x16x32_bf16 v[204:207], v[40:43], v[236:239], v[204:207]\n\t" \
    "v_mfma_f32_16x16x32_bf16 v[204:207], v[44:47], v[240:243], v[204:207]\n\t" \
    "v_mfma_f32_16x16x32_bf16 v[200:203], v[244:247], v[236:239], v[200:203]\n\t" \
    "ds_read_b128 v[236:239], v252 offset:17408\n\t" \
    "v_mfma_f32_16x16x32_bf16 v[200:203], v[248:251], v[240:243], v[200:203]\n\t" \
    "ds_read_b128 v[240:243], v252 offset:0\n\t" \
    "s_waitcnt lgkmcnt(4)\n\t" \
    "v_mfma_f32_16x16x32_bf16 v[212:215], v[244:247], v[224:227], v[212:215]\n\t" \
    "s_waitcnt lgkmcnt(3)\n\t" \
    "v_mfma_f32_16x16x32_bf16 v[212:215], v[248:251], v[228:231], v[212:215]\n\t" \
    "v_mfma_f32_16x16x32_bf16 v[216:219], v[40:43], v[224:227], v[216:219]\n\t" \
    "v_mfma_f32_16x16x32_bf16 v[216:219], v[44:47], v[228:231], v[216:219]\n\t" \
    "v_mfma_f32_16x16x32_bf16 v[220:223], v[48:51], v[224:227], v[220:223]\n\t" \
    "ds_read_b128 v[224:227], v252 offset:1024\n\t" \
    "v_mfma_f32_16x16x32_bf16 v[220:223], v[52:55], v[228:231], v[220:223]\n\t" \
    "v_and_b32 v252, 0x3ff, %[xa]\n\t" \
    "ds_read_b128 v[228:231], v252 offset:43008\n\t" \
    "s_waitcnt lgkmcnt(4)\n\t" \
    "v_mfma_f32_16x16x32_bf16 v[220:223], v[72:75], v[232:235], v[220:223]\n\t" \
    "v_mov_b64 v[244:245], 0\n\t" \
    "s_waitcnt lgkmcnt(3)\n\t" \
    "v_mfma_f32_16x16x32_bf16 v[220:223], v[76:79], v[236:239], v[220:223]\n\t" \
    "v_mov_b64 v[246:247], 0\n\t" \
    "v_mfma_f32_16x16x32_bf16 v[216:219], v[64:67], v[232:235], v[216:219]\n\t" \
    "v_mov_b64 v[248:249], 0\n\t" \
    "v_mfma_f32_16x16x32_bf16 v[216:219], v[68:71], v[236:239], v[216:219]\n\t" \
    "v_mov_b64 v[250:251], 0\n\t" \
    "v_mfma_f32_16x16x32_bf16 v[212:215], v[56:59], v[232:235], v[212:215]\n\t" \
    "ds_read_b128 v[232:235], v252 offset:44032\n\t" \
    "v_mfma_f32_16x16x32_bf16 v[212:215], v[60:63], v[236:239], v[212:215]\n\t" \
    "ds_read_b128 v[236:239], v252 offset:47104\n\t" \
    "s_waitcnt lgkmcnt(4)\n\t" \
    "v_mfma_f32_16x16x32_bf16 v[200:203], v[56:59], v[240:243], v[200:203]\n\t" \
    "s_waitcnt lgkmcnt(3)\n\t" \
    "v_mfma_f32_16x16x32_bf16 v[200:203], v[60:63], v[224:227], v[200:203]\n\t" \
    "v_mfma_f32_16x16x32_bf16 v[204:207], v[64:67], v[240:243], v[204:207]\n\t" \
    "v_mfma_f32_16x16x32_bf16 v[204:207], v[68:71], v[224:227], v[204:207]\n\t" \
    "v_mfma_f32_16x16x32_bf16 v[208:211], v[72:75], v[240:243], v[208:211]\n\t" \
    "ds_read_b128 v[240:243], v252 offset:48128\n\t" \
    "v_mfma_f32_16x16x32_bf16 v[208:211], v[76:79], v[224:227], v[208:211]\n\t" \
    "s_waitcnt lgkmcnt(3)\n\t" \
    "v_mfma_f32_16x16x32_bf16 v[208:211], v[192:195], v[228:231], v[208:211]\n\t" \
    "v_add_u32 v252, %[qs], v252\n\t" \
    "v_mov_b64 v[224:225], 0\n\t" \
    "s_waitcnt lgkmcnt(2)\n\t" \
    "v_mfma_f32_16x16x32_bf16 v[208:211], v[196:199], v[232:235], v[208:211]\n\t" \
    "v_mov_b64 v[226:227], 0\n\t" \
    "v_mfma_f32_16x16x32_bf16 v[204:207], v[184:187], v[228:231], v[204:207]\n\t" \
    "v_mfma_f32_16x16x32_bf16 v[204:207], v[188:191], v[232:235], v[204:207]\n\t" \
    "v_mfma_f32_16x16x32_bf16 v[200:203], v[176:179], v[228:231], v[200:203]\n\t" \
    "v_mfma_f32_16x16x32_bf16 v[200:203], v[180:183], v[232:235], v[200:203]\n\t" \
    "s_waitcnt lgkmcnt(1)\n\t" \
    "v_mfma_f32_16x16x32_bf16 v[212:215], v[176:179], v[236:239], v[212:215]\n\t" \
    "ds_read_b128 v[228:231], %[wl] offset:2048\n\t" \
    "ds_read_b128 v[232:235], %[wl] offset:3072\n\t" \
    "s_waitcnt lgkmcnt(2)\n\t" \
    "v_mfma_f32_16x16x32_bf16 v[212:215], v[180:183], v[240:243], v[212:215]\n\t" \
    "v_cvt_pk_bf16_f32 v208, v208, v209\n\t" \
    "v_cvt_pk_bf16_f32 v209, v210, v211\n\t" \
    "v_mfma_f32_16x16x32_bf16 v[216:219], v[184:187], v[236:239], v[216:219]\n\t" \
    "ds_write_b64 v252, v[208:209] offset:2048\n\t" \
    "v_cvt_pk_bf16_f32 v204, v204, v205\n\t" \
    "v_mfma_f32_16x16x32_bf16 v[216:219], v[188:191], v[240:243], v[216:219]\n\t" \
    "v_cvt_pk_bf16_f32 v205, v206, v207\n\t" \
    "ds_write_b64 v252, v[204:205] offset:1024\n\t" \
    "v_mfma_f32_16x16x32_bf16 v[220:223], v[192:195], v[236:239], v[220:223]\n\t" \
    "v_cvt_pk_bf16_f32 v200, v200, v201\n\t" \
    "v_cvt_pk_bf16_f32 v201, v202, v203\n\t" \
    "v_mfma_f32_16x16x32_bf16 v[220:223], v[196:199], v[240:243], v[220:223]\n\t" \
    "ds_write_b64 v252, v[200:201] offset:0\n\t" \
    "ds_read_b128 v[200:203], %[wl] offset:0\n\t" \
    "ds_read_b128 v[204:207], %[wl] offset:1024\n\t" \
    "v_cvt_pk_bf16_f32 v212, v212, v213\n\t" \
    "v_cvt_pk_bf16_f32 v213, v214, v215\n\t" \
    "ds_write_b64 v252, v[212:213] offset:6144\n\t" \
    "v_cvt_pk_bf16_f32 v216, v216, v217\n\t" \
    "v_cvt_pk_bf16_f32 v217, v218, v219\n\t" \
    "ds_write_b64 v252, v[216:217] offset:7168\n\t" \
    "v_cvt_pk_bf16_f32 v220, v220, v221\n\t" \
    "v_cvt_pk_bf16_f32 v221, v222, v223\n\t" \
    "ds_write_b64 v252, v[220:221] offset:8192\n\t" \
    "s_waitcnt lgkmcnt(0)\n\t"
#define ASM_XG_S_LO2 \
    "s_waitcnt lgkmcnt(0)\n\t" \
    "ds_read_b128 v[224:227], %[xa] offset:16384\n\t" \
    "ds_read_b128 v[228:231], %[xa] offset:17408\n\t" \
    "v_xor_b32 v252, 0x800, %[xa]\n\t" \
    "ds_read_b128 v[232:235], v252 offset:16384\n\t" \
    "ds_read_b128 v[236:239], v252 offset:17408\n\t" \
    "v_xor_b32 v252, 0x1000, %[xa]\n\t" \
    "ds_read_b128 v[240:243], v252 offset:16384\n\t" \
    "ds_read_b128 v[212:215], v252 offset:17408\n\t" \
    "v_xor_b32 v252, 0x1800, %[xa]\n\t" \
    "ds_read_b128 v[200:203], v252 offset:16384\n\t" \
    "s_waitcnt lgkmcnt(6)\n\t" \
    "v_mfma_f32_16x16x32_bf16 v[220:223], v[8:11], v[224:227], 0\n\t" \
    "s_waitcnt lgkmcnt(5)\n\t" \
    "v_mfma_f32_16x16x32_bf16 v[220:223], v[12:15], v[228:231], v[220:223]\n\t" \
    "v_mfma_f32_16x16x32_bf16 v[216:219], v[0:3], v[224:227], 0\n\t" \
    "ds_read_b128 v[224:227], v252 offset:17408\n\t" \
    "v_mfma_f32_16x16x32_bf16 v[216:219], v[4:7], v[228:231], v[216:219]\n\t" \
    "v_and_b32 v252, 0x3ff, %[xa]\n\t" \
    "ds_read_b128 v[228:231], v252 offset:45056\n\t" \
    "s_waitcnt lgkmcnt(6)\n\t" \
    "v_mfma_f32_16x16x32_bf16 v[216:219], v[24:27], v[232:235], v[216:219]\n\t" \
    "s_waitcnt lgkmcnt(5)\n\t" \
    "v_mfma_f32_16x16x32_bf16 v[216:219], v[28:31], v[236:239], v[216:219]\n\t" \
    "v_mfma_f32_16x16x32_bf16 v[220:223], v[32:35], v[232:235], v[220:223]\n\t" \
    "ds_read_b128 v[232:235], v252 offset:46080\n\t" \
    "v_mfma_f32_16x16x32_bf16 v[220:223], v[36:39], v[236:239], v[220:223]\n\t" \
    "ds_read_b128 v[236:239], %[xa] offset:0\n\t" \
    "s_waitcnt lgkmcnt(6)\n\t" \
    "v_mfma_f32_16x16x32_bf16 v[220:223], v[48:51], v[240:243], v[220:223]\n\t" \
    "s_waitcnt lgkmcnt(5)\n\t" \
    "v_mfma_f32_16x16x32_bf16 v[220:223], v[52:55], v[212:215], v[220:223]\n\t" \
    "v_mfma_f32_16x16x32_bf16 v[216:219], v[40:43], v[240:243], v[216:219]\n\t" \
    "ds_read_b128 v[240:243], %[xa] offset:1024\n\t" \
    "v_mfma_f32_16x16x32_bf16 v[216:219], v[44:47], v[212:215], v[216:219]\n\t" \
    "v_xor_b32 v252, 0x800, %[xa]\n\t" \
    "ds_read_b128 v[212:215], v252 offset:0\n\t" \
    "s_waitcnt lgkmcnt(6)\n\t" \
    "v_mfma_f32_16x16x32_bf16 v[216:219], v[64:67], v[200:203], v[216:219]\n\t" \
    "s_waitcnt lgkmcnt(5)\n\t" \
    "v_mfma_f32_16x16x32_bf16 v[216:219], v[68:71], v[224:227], v[216:219]\n\t" \
    "v_mfma_f32_16x16x32_bf16 v[220:223], v[72:75], v[200:203], v[220:223]\n\t" \
    "ds_read_b128 v[200:203], v252 offset:1024\n\t" \
    "v_mfma_f32_16x16x32_bf16 v[220:223], v[76:79], v[224:227], v[220:223]\n\t" \
    "v_xor_b32 v252, 0x1000, %[xa]\n\t" \
    "ds_read_b128 v[224:227], v252 offset:0\n\t" \
    "s_waitcnt lgkmcnt(6)\n\t" \
    "v_mfma_f32_16x16x32_bf16 v[220:223], v[168:171], v[228:231], v[220:223]\n\t" \
    "s_waitcnt lgkmcnt(5)\n\t" \
    "v_mfma_f32_16x16x32_bf16 v[220:223], v[172:175], v[232:235], v[220:223]\n\t" \
    "v_mfma_f32_16x16x32_bf16 v[216:219], v[160:163], v[228:231], v[216:219]\n\t" \
    "ds_read_b128 v[228:231], v252 offset:1024\n\t" \
    "v_mfma_f32_16x16x32_bf16 v[216:219], v[164:167], v[232:235], v[216:219]\n\t" \
    "v_xor_b32 v252, 0x1800, %[xa]\n\t" \
    "ds_read_b128 v[232:235], v252 offset:0\n\t" \
    "s_waitcnt lgkmcnt(6)\n\t" \
    "v_mfma_f32_16x16x32_bf16 v[204:207], v[0:3], v[236:239], 0\n\t" \
    "s_waitcnt lgkmcnt(5)\n\t" \
    "v_mfma_f32_16x16x32_bf16 v[204:207], v[4:7], v[240:243], v[204:207]\n\t" \
    "v_mfma_f32_16x16x32_bf16 v[208:211], v[8:11], v[236:239], 0\n\t" \
    "ds_read_b128 v[236:239], v252 offset:1024\n\t" \
    "v_mfma_f32_16x16x32_bf16 v[208:211], v[12:15], v[240:243], v[208:211]\n\t" \
    "v_and_b32 v252, 0x3ff, %[xa]\n\t" \
    "ds_read_b128 v[240:243], v252 offset:40960\n\t" \
    "s_waitcnt lgkmcnt(6)\n\t" \
    "v_mfma_f32_16x16x32_bf16 v[208:211], v[32:35], v[212:215], v[208:211]\n\t" \
    "s_waitcnt lgkmcnt(5)\n\t" \
    "v_mfma_f32_16x16x32_bf16 v[208:211], v[36:39], v[200:203], v[208:211]\n\t" \
    "v_mfma_f32_16x16x32_bf16 v[204:207], v[24:27], v[212:215], v[204:207]\n\t" \
    "ds_read_b128 v[212:215], v252 offset:41984\n\t" \
    "v_mfma_f32_16x16x32_bf16 v[204:207], v[28:31], v[200:203], v[204:207]\n\t" \
    "s_waitcnt lgkmcnt(0)\n\t" \
    "s_barrier\n\t" ASM_ST \
    "v_xor_b32 v252, 0x2000, %[xa]\n\t" \
    "ds_read_b128 v[200:203], v252 offset:0\n\t" \
    "v_mfma_f32_16x16x32_bf16 v[204:207], v[40:43], v[224:227], v[204:207]\n\t" \
    "v_mfma_f32_16x16x32_bf16 v[204:207], v[44:47], v[228:231], v[204:207]\n\t" \
    "v_mfma_f32_16x16x32_bf16 v[208:211], v[48:51], v[224:227], v[208:211]\n\t" \
    "ds_read_b128 v[224:227], v252 offset:1024\n\t" \
    "v_mfma_f32_16x16x32_bf16 v[208:211], v[52:55], v[228:231], v[208:211]\n\t" \
    "v_xor_b32 v252, 0x2800, %[xa]\n\t" \
    "ds_read_b128 v[228:231], v252 offset:0\n\t" \
    "v_mfma_f32_16x16x32_bf16 v[208:211], v[72:75], v[232:235], v[208:211]\n\t" \
    "v_mfma_f32_16x16x32_bf16 v[208:211], v[76:79], v[236:239], v[208:211]\n\t" \
    "v_mfma_f32_16x16x32_bf16 v[204:207], v[64:67], v[232:235], v[204:207]\n\t" \
    "ds_read_b128 v[232:235], v252 offset:1024\n\t" \
    "v_mfma_f32_16x16x32_bf16 v[204:207], v[68:71], v[236:239], v[204:207]\n\t" \
    "v_xor_b32 v252, 0x3000, %[xa]\n\t" \
    "ds_read_b128 v[236:239], v252 offset:0\n\t" \
    "v_mfma_f32_16x16x32_bf16 v[204:207], v[160:163], v[240:243], v[204:207]\n\t" \
    "v_mfma_f32_16x16x32_bf16 v[204:207], v[164:167], v[212:215], v[204:207]\n\t" \
    "v_mfma_f32_16x16x32_bf16 v[208:211], v[168:171], v[240:243], v[208:211]\n\t" \
    "ds_read_b128 v[240:243], v252 offset:1024\n\t" \
    "v_mfma_f32_16x16x32_bf16 v[208:211], v[172:175], v[212:215], v[208:211]\n\t" \
    "v_xor_b32 v252, 0x3800, %[xa]\n\t" \
    "ds_read_b128 v[212:215], v252 offset:0\n\t" \
    "s_waitcnt lgkmcnt(6)\n\t" \
    "v_mfma_f32_16x16x32_bf16 v[208:211], v[88:91], v[200:203], v[208:211]\n\t" \
    "s_waitcnt lgkmcnt(5)\n\t" \
    "v_mfma_f32_16x16x32_bf16 v[208:211], v[92:95], v[224:227], v[208:211]\n\t" \
    "v_mfma_f32_16x16x32_bf16 v[204:207], v[80:83], v[200:203], v[204:207]\n\t" \
    "ds_read_b128 v[200:203], v252 offset:1024\n\t" \
    "v_mfma_f32_16x16x32_bf16 v[204:207], v[84:87], v[224:227], v[204:207]\n\t" \
    "v_and_b32 v252, 0x3ff, %[xa]\n\t" \
    "ds_read_b128 v[224:227], v252 offset:43008\n\t" \
    "s_waitcnt lgkmcnt(6)\n\t" \
    "v_mfma_f32_16x16x32_bf16 v[204:207], v[104:107], v[228:231], v[204:207]\n\t" \
    "s_waitcnt lgkmcnt(5)\n\t" \
    "v_mfma_f32_16x16x32_bf16 v[204:207], v[108:111], v[232:235], v[204:207]\n\t" \
    "v_mfma_f32_16x16x32_bf16 v[208:211], v[112:115], v[228:231], v[208:211]\n\t" \
    "ds_read_b128 v[228:231], v252 offset:44032\n\t" \
    "v_mfma_f32_16x16x32_bf16 v[208:211], v[116:119], v[232:235], v[208:211]\n\t" \
    "v_xor_b32 v252, 0x2000, %[xa]\n\t" \
    "ds_read_b128 v[232:235], v252 offset:16384\n\t" \
    "s_waitcnt lgkmcnt(6)\n\t" \
    "v_mfma_f32_16x16x32_bf16 v[208:211], v[128:131], v[236:239], v[208:211]\n\t" \
    "s_waitcnt lgkmcnt(5)\n\t" \
    "v_mfma_f32_16x16x32_bf16 v[208:211], v[132:135], v[240:243], v[208:211]\n\t" \
    "v_mfma_f32_16x16x32_bf16 v[204:207], v[120:123], v[236:239], v[204:207]\n\t" \
    "ds_read_b128 v[236:239], v252 offset:17408\n\t" \
    "v_mfma_f32_16x16x32_bf16 v[204:207], v[124:127], v[240:243], v[204:207]\n\t" \
    "v_xor_b32 v252, 0x2800, %[xa]\n\t" \
    "ds_read_b128 v[240:243], v252 offset:16384\n\t" \
    "s_waitcnt lgkmcnt(6)\n\t" \
    "v_mfma_f32_16x16x32_bf16 v[204:207], v[144:147], v[212:215], v[204:207]\n\t" \
    "s_waitcnt lgkmcnt(5)\n\t" \
    "v_mfma_f32_16x16x32_bf16 v[204:207], v[148:151], v[200:203], v[204:207]\n\t" \
    "v_mfma_f32_16x16x32_bf16 v[208:211], v[152:155], v[212:215], v[208:211]\n\t" \
    "ds_read_b128 v[212:215], v252 offset:17408\n\t" \
    "v_mfma_f32_16x16x32_bf16 v[208:211], v[156:159], v[200:203], v[208:211]\n\t" \
    "v_xor_b32 v252, 0x3000, %[xa]\n\t" \
    "ds_read_b128 v[200:203], v252 offset:16384\n\t" \
    "s_waitcnt lgkmcnt(6)\n\t" \
    "v_mfma_f32_16x16x32_bf16 v[208:211], v[192:195], v[224:227], v[208:211]\n\t" \
    "s_waitcnt lgkmcnt(5)\n\t" \
    "v_mfma_f32_16x16x32_bf16 v[208:211], v[196:199], v[228:231], v[208:211]\n\t" \
    "v_mfma_f32_16x16x32_bf16 v[204:207], v[184:187], v[224:227], v[204:207]\n\t" \
    "ds_read_b128 v[224:227], v252 offset:17408\n\t" \
    "v_mfma_f32_16x16x32_bf16 v[204:207], v[188:191], v[228:231], v[204:207]\n\t" \
    "v_xor_b32 v252, 0x3800, %[xa]\n\t" \
    "ds_read_b128 v[228:231], v252 offset:16384\n\t" \
    "s_waitcnt lgkmcnt(6)\n\t" \
    "v_mfma_f32_16x16x32_bf16 v[216:219], v[80:83], v[232:235], v[216:219]\n\t" \
    "s_waitcnt lgkmcnt(5)\n\t" \
    "v_mfma_f32_16x16x32_bf16 v[216:219], v[84:87], v[236:239], v[216:219]\n\t" \
    "v_mfma_f32_16x16x32_bf16 v[220:223], v[88:91], v[232:235], v[220:223]\n\t" \
    "ds_read_b128 v[232:235], v252 offset:17408\n\t" \
    "v_mfma_f32_16x16x32_bf16 v[220:223], v[92:95], v[236:239], v[220:223]\n\t" \
    "v_and_b32 v252, 0x3ff, %[xa]\n\t" \
    "ds_read_b128 v[236:239], v252 offset:47104\n\t" \
    "s_waitcnt lgkmcnt(6)\n\t" \
    "v_mfma_f32_16x16x32_bf16 v[220:223], v[112:115], v[240:243], v[220:223]\n\t" \
    "v_mov_b64 v[244:245], 0\n\t" \
    "s_waitcnt lgkmcnt(5)\n\t" \
    "v_mfma_f32_16x16x32_bf16 v[220:223], v[116:119], v[212:215], v[220:223]\n\t" \
    "v_mov_b64 v[246:247], 0\n\t" \
    "v_mfma_f32_16x16x32_bf16 v[216:219], v[104:107], v[240:243], v[216:219]\n\t" \
    "ds_read_b128 v[240:243], v252 offset:48128\n\t" \
    "v_mov_b64 v[248:249], 0\n\t" \
    "v_mfma_f32_16x16x32_bf16 v[216:219], v[108:111], v[212:215], v[216:219]\n\t" \
    "v_mov_b64 v[250:251], 0\n\t" \
    "s_waitcnt lgkmcnt(5)\n\t" \
    "v_mfma_f32_16x16x32_bf16 v[216:219], v[120:123], v[200:203], v[216:219]\n\t" \
    "s_waitcnt lgkmcnt(4)\n\t" \
    "v_mfma_f32_16x16x32_bf16 v[216:219], v[124:127], v[224:227], v[216:219]\n\t" \
    "v_mfma_f32_16x16x32_bf16 v[220:223], v[128:131], v[200:203], v[220:223]\n\t" \
    "v_mfma_f32_16x16x32_bf16 v[220:223], v[132:135], v[224:227], v[220:223]\n\t" \
    "s_waitcnt lgkmcnt(3)\n\t" \
    "v_mfma_f32_16x16x32_bf16 v[220:223], v[152:155], v[228:231], v[220:223]\n\t" \
    "v_add_u32 v252, %[qs], v252\n\t" \
    "v_mov_b64 v[224:225], 0\n\t" \
    "s_waitcnt lgkmcnt(2)\n\t" \
    "v_mfma_f32_16x16x32_bf16 v[220:223], v[156:159], v[232:235], v[220:223]\n\t" \
    "v_mov_b64 v[226:227], 0\n\t" \
    "v_cvt_pk_bf16_f32 v208, v208, v209\n\t" \
    "v_mfma_f32_16x16x32_bf16 v[216:219], v[144:147], v[228:231], v[216:219]\n\t" \
    "v_cvt_pk_bf16_f32 v209, v210, v211\n\t" \
    "ds_write_b64 v252, v[208:209] offset:2048\n\t" \
    "v_mfma_f32_16x16x32_bf16 v[216:219], v[148:151], v[232:235], v[216:219]\n\t" \
    "v_cvt_pk_bf16_f32 v204, v204, v205\n\t" \
    "v_cvt_pk_bf16_f32 v205, v206, v207\n\t" \
    "s_waitcnt lgkmcnt(2)\n\t" \
    "v_mfma_f32_16x16x32_bf16 v[216:219], v[184:187], v[236:239], v[216:219]\n\t" \
    "ds_write_b64 v252, v[204:205] offset:1024\n\t" \
    "ds_read_b128 v[228:231], %[wl] offset:2048\n\t" \
    "s_waitcnt lgkmcnt(3)\n\t" \
    "v_mfma_f32_16x16x32_bf16 v[216:219], v[188:191], v[240:243], v[216:219]\n\t" \
    "ds_read_b128 v[232:235], %[wl] offset:3072\n\t" \
    "v_mfma_f32_16x16x32_bf16 v[220:223], v[192:195], v[236:239], v[220:223]\n\t" \
    "v_mfma_f32_16x16x32_bf16 v[220:223], v[196:199], v[240:243], v[220:223]\n\t" \
    "ds_read_b128 v[200:203], %[wl] offset:0\n\t" \
    "ds_read_b128 v[204:207], %[wl] offset:1024\n\t" \
    "v_cvt_pk_bf16_f32 v216, v216, v217\n\t" \
    "v_cvt_pk_bf16_f32 v217, v218, v219\n\t" \
    "ds_write_b64 v252, v[216:217] offset:7168\n\t" \
    "s_nop 2\n\t" \
    "v_cvt_pk_bf16_f32 v220, v220, v221\n\t" \
    "v_cvt_pk_bf16_f32 v221, v222, v223\n\t" \
    "ds_write_b64 v252, v[220:221] offset:8192\n\t" \
    "s_waitcnt lgkmcnt(0)\n\t"
#define ASM_XG_S_LO1 \
    "s_waitcnt lgkmcnt(0)\n\t" \
    "ds_read_b128 v[224:227], %[xa] offset:16384\n\t" \
    "ds_read_b128 v[228:231], %[xa] offset:17408\n\t" \
    "v_xor_b32 v252, 0x800, %[xa]\n\t" \
    "ds_read_b128 v[232:235], v252 offset:16384\n\t" \
    "ds_read_b128 v[236:239], v252 offset:17408\n\t" \
    "v_xor_b32 v252, 0x1000, %[xa]\n\t" \
    "ds_read_b128 v[240:243], v252 offset:16384\n\t" \
    "ds_read_b128 v[212:215], v252 offset:17408\n\t" \
    "v_xor_b32 v252, 0x1800, %[xa]\n\t" \
    "ds_read_b128 v[200:203], v252 offset:16384\n\t" \
    "s_waitcnt lgkmcnt(6)\n\t" \
    "v_mfma_f32_16x16x32_bf16 v[220:223], v[8:11], v[224:227], 0\n\t" \
    "ds_read_b128 v[224:227], v252 offset:17408\n\t" \
    "s_waitcnt lgkmcnt(6)\n\t" \
    "v_mfma_f32_16x16x32_bf16 v[220:223], v[12:15], v[228:231], v[220:223]\n\t" \
    "v_and_b32 v252, 0x3ff, %[xa]\n\t" \
    "ds_read_b128 v[228:231], v252 offset:45056\n\t" \
    "s_waitcnt lgkmcnt(6)\n\t" \
    "v_mfma_f32_16x16x32_bf16 v[220:223], v[32:35], v[232:235], v[220:223]\n\t" \
    "ds_read_b128 v[232:235], v252 offset:46080\n\t" \
    "s_waitcnt lgkmcnt(6)\n\t" \
    "v_mfma_f32_16x16x32_bf16 v[220:223], v[36:39], v[236:239], v[220:223]\n\t" \
    "ds_read_b128 v[236:239], %[xa] offset:0\n\t" \
    "s_waitcnt lgkmcnt(6)\n\t" \
    "v_mfma_f32_16x16x32_bf16 v[220:223], v[48:51], v[240:243], v[220:223]\n\t" \
    "ds_read_b128 v[240:243], %[xa] offset:1024\n\t" \
    "s_waitcnt lgkmcnt(6)\n\t" \
    "v_mfma_f32_16x16x32_bf16 v[220:223], v[52:55], v[212:215], v[220:223]\n\t" \
    "v_xor_b32 v252, 0x800, %[xa]\n\t" \
    "ds_read_b128 v[212:215], v252 offset:0\n\t" \
    "s_waitcnt lgkmcnt(6)\n\t" \
    "v_mfma_f32_16x16x32_bf16 v[220:223], v[72:75], v[200:203], v[220:223]\n\t" \
    "ds_read_b128 v[200:203], v252 offset:1024\n\t" \
    "s_waitcnt lgkmcnt(6)\n\t" \
    "v_mfma_f32_16x16x32_bf16 v[220:223], v[76:79], v[224:227], v[220:223]\n\t" \
    "v_xor_b32 v252, 0x1000, %[xa]\n\t" \
    "ds_read_b128 v[224:227], v252 offset:0\n\t" \
    "s_waitcnt lgkmcnt(6)\n\t" \
    "v_mfma_f32_16x16x32_bf16 v[220:223], v[168:171], v[228:231], v[220:223]\n\t" \
    "ds_read_b128 v[228:231], v252 offset:1024\n\t" \
    "s_waitcnt lgkmcnt(6)\n\t" \
    "v_mfma_f32_16x16x32_bf16 v[220:223], v[172:175], v[232:235], v[220:223]\n\t" \
    "v_xor_b32 v252, 0x1800, %[xa]\n\t" \
    "ds_read_b128 v[232:235], v252 offset:0\n\t" \
    "s_waitcnt lgkmcnt(6)\n\t" \
    "v_mfma_f32_16x16x32_bf16 v[208:211], v[8:11], v[236:239], 0\n\t" \
    "ds_read_b128 v[236:239], v252 offset:1024\n\t" \
    "s_waitcnt lgkmcnt(6)\n\t" \
    "v_mfma_f32_16x16x32_bf16 v[208:211], v[12:15], v[240:243], v[208:211]\n\t" \
    "v_and_b32 v252, 0x3ff, %[xa]\n\t" \
    "ds_read_b128 v[240:243], v252 offset:40960\n\t" \
    "s_waitcnt lgkmcnt(6)\n\t" \
    "v_mfma_f32_16x16x32_bf16 v[208:211], v[32:35], v[212:215], v[208:211]\n\t" \
    "ds_read_b128 v[212:215], v252 offset:41984\n\t" \
    "s_waitcnt lgkmcnt(6)\n\t" \
    "v_mfma_f32_16x16x32_bf16 v[208:211], v[36:39], v[200:203], v[208:211]\n\t" \
    "s_waitcnt lgkmcnt(0)\n\t" \
    "s_barrier\n\t" ASM_ST \
    "v_xor_b32 v252, 0x2000, %[xa]\n\t" \
    "ds_read_b128 v[200:203], v252 offset:0\n\t" \
    "v_mfma_f32_16x16x32_bf16 v[208:211], v[48:51], v[224:227], v[208:211]\n\t" \
    "ds_read_b128 v[224:227], v252 offset:1024\n\t" \
    "v_mfma_f32_16x16x32_bf16 v[208:211], v[52:55], v[228:231], v[208:211]\n\t" \
    "v_xor_b32 v252, 0x2800, %[xa]\n\t" \
    "ds_read_b128 v[228:231], v252 offset:0\n\t" \
    "v_mfma_f32_16x16x32_bf16 v[208:211], v[72:75], v[232:235], v[208:211]\n\t" \
    "ds_read_b128 v[232:235], v252 offset:1024\n\t" \
    "v_mfma_f32_16x16x32_bf16 v[208:211], v[76:79], v[236:239], v[208:211]\n\t" \
    "v_xor_b32 v252, 0x3000, %[xa]\n\t" \
    "ds_read_b128 v[236:239], v252 offset:0\n\t" \
    "v_mfma_f32_16x16x32_bf16 v[208:211], v[168:171], v[240:243], v[208:211]\n\t" \
    "ds_read_b128 v[240:243], v252 offset:1024\n\t" \
    "v_mfma_f32_16x16x32_bf16 v[208:211], v[172:175], v[212:215], v[208:211]\n\t" \
    "v_xor_b32 v252, 0x3800, %[xa]\n\t" \
    "ds_read_b128 v[212:215], v252 offset:0\n\t" \
    "s_waitcnt lgkmcnt(6)\n\t" \
    "v_mfma_f32_16x16x32_bf16 v[208:211], v[88:91], v[200:203], v[208:211]\n\t" \
    "ds_read_b128 v[200:203], v252 offset:1024\n\t" \
    "s_waitcnt lgkmcnt(6)\n\t" \
    "v_mfma_f32_16x16x32_bf16 v[208:211], v[92:95], v[224:227], v[208:211]\n\t" \
    "v_and_b32 v252, 0x3ff, %[xa]\n\t" \
    "ds_read_b128 v[224:227], v252 offset:43008\n\t" \
    "s_waitcnt lgkmcnt(6)\n\t" \
    "v_mfma_f32_16x16x32_bf16 v[208:211], v[112:115], v[228:231], v[208:211]\n\t" \
    "ds_read_b128 v[228:231], v252 offset:44032\n\t" \
    "s_waitcnt lgkmcnt(6)\n\t" \
    "v_mfma_f32_16x16x32_bf16 v[208:211], v[116:119], v[232:235], v[208:211]\n\t" \
    "v_xor_b32 v252, 0x2000, %[xa]\n\t" \
    "ds_read_b128 v[232:235], v252 offset:16384\n\t" \
    "s_waitcnt lgkmcnt(6)\n\t" \
    "v_mfma_f32_16x16x32_bf16 v[208:211], v[128:131], v[236:239], v[208:211]\n\t" \
    "ds_read_b128 v[236:239], v252 offset:17408\n\t" \
    "s_waitcnt lgkmcnt(6)\n\t" \
    "v_mfma_f32_16x16x32_bf16 v[208:211], v[132:135], v[240:243], v[208:211]\n\t" \
    "v_xor_b32 v252, 0x2800, %[xa]\n\t" \
    "ds_read_b128 v[240:243], v252 offset:16384\n\t" \
    "s_waitcnt lgkmcnt(6)\n\t" \
    "v_mfma_f32_16x16x32_bf16 v[208:211], v[152:155], v[212:215], v[208:211]\n\t" \
    "ds_read_b128 v[212:215], v252 offset:17408\n\t" \
    "s_waitcnt lgkmcnt(6)\n\t" \
    "v_mfma_f32_16x16x32_bf16 v[208:211], v[156:159], v[200:203], v[208:211]\n\t" \
    "v_xor_b32 v252, 0x3000, %[xa]\n\t" \
    "ds_read_b128 v[200:203], v252 offset:16384\n\t" \
    "s_waitcnt lgkmcnt(6)\n\t" \
    "v_mfma_f32_16x16x32_bf16 v[208:211], v[192:195], v[224:227], v[208:211]\n\t" \
    "ds_read_b128 v[224:227], v252 offset:17408\n\t" \
    "s_waitcnt lgkmcnt(6)\n\t" \
    "v_mfma_f32_16x16x32_bf16 v[208:211], v[196:199], v[228:231], v[208:211]\n\t" \
    "v_xor_b32 v252, 0x3800, %[xa]\n\t" \
    "ds_read_b128 v[228:231], v252 offset:16384\n\t" \
    "s_waitcnt lgkmcnt(6)\n\t" \
    "v_mfma_f32_16x16x32_bf16 v[220:223], v[88:91], v[232:235], v[220:223]\n\t" \
    "ds_read_b128 v[232:235], v252 offset:17408\n\t" \
    "s_waitcnt lgkmcnt(6)\n\t" \
    "v_mfma_f32_16x16x32_bf16 v[220:223], v[92:95], v[236:239], v[220:223]\n\t" \
    "v_and_b32 v252, 0x3ff, %[xa]\n\t" \
    "ds_read_b128 v[236:239], v252 offset:47104\n\t" \
    "s_waitcnt lgkmcnt(6)\n\t" \
    "v_mfma_f32_16x16x32_bf16 v[220:223], v[112:115], v[240:243], v[220:223]\n\t" \
    "ds_read_b128 v[240:243], v252 offset:48128\n\t" \
    "v_mov_b64 v[244:245], 0\n\t" \
    "s_waitcnt lgkmcnt(6)\n\t" \
    "v_mfma_f32_16x16x32_bf16 v[220:223], v[116:119], v[212:215], v[220:223]\n\t" \
    "v_mov_b64 v[246:247], 0\n\t" \
    "s_waitcnt lgkmcnt(5)\n\t" \
    "v_mfma_f32_16x16x32_bf16 v[220:223], v[128:131], v[200:203], v[220:223]\n\t" \
    "v_mov_b64 v[248:249], 0\n\t" \
    "s_waitcnt lgkmcnt(4)\n\t" \
    "v_mfma_f32_16x16x32_bf16 v[220:223], v[132:135], v[224:227], v[220:223]\n\t" \
    "v_mov_b64 v[250:251], 0\n\t" \
    "s_waitcnt lgkmcnt(3)\n\t" \
    "v_mfma_f32_16x16x32_bf16 v[220:223], v[152:155], v[228:231], v[220:223]\n\t" \
    "v_add_u32 v252, %[qs], v252\n\t" \
    "v_mov_b64 v[224:225], 0\n\t" \
    "s_waitcnt lgkmcnt(2)\n\t" \
    "v_mfma_f32_16x16x32_bf16 v[220:223], v[156:159], v[232:235], v[220:223]\n\t" \
    "v_mov_b64 v[226:227], 0\n\t" \
    "v_cvt_pk_bf16_f32 v208, v208, v209\n\t" \
    "s_waitcnt lgkmcnt(1)\n\t" \
    "v_mfma_f32_16x16x32_bf16 v[220:223], v[192:195], v[236:239], v[220:223]\n\t" \
    "v_cvt_pk_bf16_f32 v209, v210, v211\n\t" \
    "ds_write_b64 v252, v[208:209] offset:2048\n\t" \
    "s_waitcnt lgkmcnt(1)\n\t" \
    "v_mfma_f32_16x16x32_bf16 v[220:223], v[196:199], v[240:243], v[220:223]\n\t" \
    "ds_read_b128 v[228:231], %[wl] offset:2048\n\t" \
    "ds_read_b128 v[232:235], %[wl] offset:3072\n\t" \
    "ds_read_b128 v[200:203], %[wl] offset:0\n\t" \
    "ds_read_b128 v[204:207], %[wl] offset:1024\n\t" \
    "s_nop 3\n\t" \
    "v_cvt_pk_bf16_f32 v220, v220, v221\n\t" \
    "v_cvt_pk_bf16_f32 v221, v222, v223\n\t" \
    "ds_write_b64 v252, v[220:221] offset:8192\n\t" \
    "s_waitcnt lgkmcnt(0)\n\t"
#define ASM_XG_V \
    "s_waitcnt lgkmcnt(0)\n\t" \
    "ds_read_b128 v[224:227], %[xa] offset:16384\n\t" \
    "ds_read_b128 v[228:231], %[xa] offset:17408\n\t" \
    "ds_read_b128 v[244:247], %[wl] offset:0\n\t" \
    "ds_read_b128 v[248:251], %[wl] offset:1024\n\t" \
    "ds_read_b128 v[232:235], %[xa] offset:0\n\t" \
    "ds_read_b128 v[236:239], %[xa] offset:1024\n\t" \
    "v_xor_b32 v252, 0x800, %[xa]\n\t" \
    "ds_read_b128 v[240:243], v252 offset:0\n\t" \
    "s_waitcnt lgkmcnt(6)\n\t" \
    "v_mfma_f32_16x16x32_bf16 v[220:223], v[8:11], v[224:227], 0\n\t" \
    "s_waitcnt lgkmcnt(5)\n\t" \
    "v_mfma_f32_16x16x32_bf16 v[220:223], v[12:15], v[228:231], v[220:223]\n\t" \
    "v_mfma_f32_16x16x32_bf16 v[216:219], v[0:3], v[224:227], 0\n\t" \
    "v_mfma_f32_16x16x32_bf16 v[216:219], v[4:7], v[228:231], v[216:219]\n\t" \
    "s_waitcnt lgkmcnt(4)\n\t" \
    "v_mfma_f32_16x16x32_bf16 v[212:215], v[244:247], v[224:227], 0\n\t" \
    "ds_read_b128 v[224:227], v252 offset:1024\n\t" \
    "s_waitcnt lgkmcnt(4)\n\t" \
    "v_mfma_f32_16x16x32_bf16 v[212:215], v[248:251], v[228:231], v[212:215]\n\t" \
    "ds_read_b128 v[228:231], v252 offset:16384\n\t" \
    "s_waitcnt lgkmcnt(4)\n\t" \
    "v_mfma_f32_16x16x32_bf16 v[200:203], v[244:247], v[232:235], 0\n\t" \
    "ds_read_b128 v[244:247], %[wl] offset:2048\n\t" \
    "s_waitcnt lgkmcnt(4)\n\t" \
    "v_mfma_f32_16x16x32_bf16 v[200:203], v[248:251], v[236:239], v[200:203]\n\t" \
    "ds_read_b128 v[248:251], %[wl] offset:3072\n\t" \
    "v_mfma_f32_16x16x32_bf16 v[204:207], v[0:3], v[232:235], 0\n\t" \
    "v_mfma_f32_16x16x32_bf16 v[204:207], v[4:7], v[236:239], v[204:207]\n\t" \
    "v_mfma_f32_16x16x32_bf16 v[208:211], v[8:11], v[232:235], 0\n\t" \
    "ds_read_b128 v[232:235], v252 offset:17408\n\t" \
    "v_mfma_f32_16x16x32_bf16 v[208:211], v[12:15], v[236:239], v[208:211]\n\t" \
    "v_xor_b32 v252, 0x1000, %[xa]\n\t" \
    "ds_read_b128 v[236:239], v252 offset:16384\n\t" \
    "s_waitcnt lgkmcnt(6)\n\t" \
    "v_mfma_f32_16x16x32_bf16 v[208:211], v[32:35], v[240:243], v[208:211]\n\t" \
    "s_waitcnt lgkmcnt(5)\n\t" \
    "v_mfma_f32_16x16x32_bf16 v[208:211], v[36:39], v[224:227], v[208:211]\n\t" \
    "v_mfma_f32_16x16x32_bf16 v[204:207], v[24:27], v[240:243], v[204:207]\n\t" \
    "v_mfma_f32_16x16x32_bf16 v[204:207], v[28:31], v[224:227], v[204:207]\n\t" \
    "v_mfma_f32_16x16x32_bf16 v[200:203], v[16:19], v[240:243], v[200:203]\n\t" \
    "ds_read_b128 v[240:243], v252 offset:17408\n\t" \
    "v_mfma_f32_16x16x32_bf16 v[200:203], v[20:23], v[224:227], v[200:203]\n\t" \
    "ds_read_b128 v[224:227], v252 offset:0\n\t" \
    "s_waitcnt lgkmcnt(6)\n\t" \
    "v_mfma_f32_16x16x32_bf16 v[212:215], v[16:19], v[228:231], v[212:215]\n\t" \
    "s_waitcnt lgkmcnt(3)\n\t" \
    "v_mfma_f32_16x16x32_bf16 v[212:215], v[20:23], v[232:235], v[212:215]\n\t" \
    "v_mfma_f32_16x16x32_bf16 v[216:219], v[24:27], v[228:231], v[216:219]\n\t" \
    "v_mfma_f32_16x16x32_bf16 v[216:219], v[28:31], v[232:235], v[216:219]\n\t" \
    "v_mfma_f32_16x16x32_bf16 v[220:223], v[32:35], v[228:231], v[220:223]\n\t" \
    "ds_read_b128 v[228:231], v252 offset:1024\n\t" \
    "v_mfma_f32_16x16x32_bf16 v[220:223], v[36:39], v[232:235], v[220:223]\n\t" \
    "v_xor_b32 v252, 0x1800, %[xa]\n\t" \
    "ds_read_b128 v[232:235], v252 offset:0\n\t" \
    "s_waitcnt lgkmcnt(4)\n\t" \
    "v_mfma_f32_16x16x32_bf16 v[220:223], v[48:51], v[236:239], v[220:223]\n\t" \
    "s_waitcnt lgkmcnt(3)\n\t" \
    "v_mfma_f32_16x16x32_bf16 v[220:223], v[52:55], v[240:243], v[220:223]\n\t" \
    "v_mfma_f32_16x16x32_bf16 v[216:219], v[40:43], v[236:239], v[216:219]\n\t" \
    "v_mfma_f32_16x16x32_bf16 v[216:219], v[44:47], v[240:243], v[216:219]\n\t" \
    "v_mfma_f32_16x16x32_bf16 v[212:215], v[244:247], v[236:239], v[212:215]\n\t" \
    "ds_read_b128 v[236:239], v252 offset:1024\n\t" \
    "v_mfma_f32_16x16x32_bf16 v[212:215], v[248:251], v[240:243], v[212:215]\n\t" \
    "ds_read_b128 v[240:243], v252 offset:16384\n\t" \
    "s_waitcnt lgkmcnt(4)\n\t" \
    "v_mfma_f32_16x16x32_bf16 v[200:203], v[244:247], v[224:227], v[200:203]\n\t" \
    "ds_read_b128 v[244:247], %[wl] offset:8192\n\t" \
    "s_waitcnt lgkmcnt(4)\n\t" \
    "v_mfma_f32_16x16x32_bf16 v[200:203], v[248:251], v[228:231], v[200:203]\n\t" \
    "ds_read_b128 v[248:251], %[wl] offset:9216\n\t" \
    "v_mfma_f32_16x16x32_bf16 v[204:207], v[40:43], v[224:227], v[204:207]\n\t" \
    "v_mfma_f32_16x16x32_bf16 v[204:207], v[44:47], v[228:231], v[204:207]\n\t" \
    "v_mfma_f32_16x16x32_bf16 v[208:211], v[48:51], v[224:227], v[208:211]\n\t" \
    "ds_read_b128 v[224:227], v252 offset:17408\n\t" \
    "v_mfma_f32_16x16x32_bf16 v[208:211], v[52:55], v[228:231], v[208:211]\n\t" \
    "v_and_b32 v252, 0x3ff, %[xa]\n\t" \
    "ds_read_b128 v[228:231], v252 offset:45056\n\t" \
    "s_waitcnt lgkmcnt(6)\n\t" \
    "v_mfma_f32_16x16x32_bf16 v[208:211], v[72:75], v[232:235], v[208:211]\n\t" \
    "s_waitcnt lgkmcnt(5)\n\t" \
    "v_mfma_f32_16x16x32_bf16 v[208:211], v[76:79], v[236:239], v[208:211]\n\t" \
    "v_mfma_f32_16x16x32_bf16 v[204:207], v[64:67], v[232:235], v[204:207]\n\t" \
    "v_mfma_f32_16x16x32_bf16 v[204:207], v[68:71], v[236:239], v[204:207]\n\t" \
    "v_mfma_f32_16x16x32_bf16 v[200:203], v[56:59], v[232:235], v[200:203]\n\t" \
    "ds_read_b128 v[232:235], v252 offset:46080\n\t" \
    "v_mfma_f32_16x16x32_bf16 v[200:203], v[60:63], v[236:239], v[200:203]\n\t" \
    "ds_read_b128 v[236:239], v252 offset:40960\n\t" \
    "s_waitcnt lgkmcnt(6)\n\t" \
    "v_mfma_f32_16x16x32_bf16 v[212:215], v[56:59], v[240:243], v[212:215]\n\t" \
    "s_waitcnt lgkmcnt(3)\n\t" \
    "v_mfma_f32_16x16x32_bf16 v[212:215], v[60:63], v[224:227], v[212:215]\n\t" \
    "v_mfma_f32_16x16x32_bf16 v[216:219], v[64:67], v[240:243], v[216:219]\n\t" \
    "v_mfma_f32_16x16x32_bf16 v[216:219], v[68:71], v[224:227], v[216:219]\n\t" \
    "v_mfma_f32_16x16x32_bf16 v[220:223], v[72:75], v[240:243], v[220:223]\n\t" \
    "ds_read_b128 v[240:243], v252 offset:41984\n\t" \
    "v_mfma_f32_16x16x32_bf16 v[220:223], v[76:79], v[224:227], v[220:223]\n\t" \
    "v_xor_b32 v252, 0x2000, %[xa]\n\t" \
    "ds_read_b128 v[224:227], v252 offset:0\n\t" \
    "s_waitcnt lgkmcnt(4)\n\t" \
    "v_mfma_f32_16x16x32_bf16 v[220:223], v[168:171], v[228:231], v[220:223]\n\t" \
    "s_waitcnt lgkmcnt(3)\n\t" \
    "v_mfma_f32_16x16x32_bf16 v[220:223], v[172:175], v[232:235], v[220:223]\n\t" \
    "v_mfma_f32_16x16x32_bf16 v[216:219], v[160:163], v[228:231], v[216:219]\n\t" \
    "v_mfma_f32_16x16x32_bf16 v[216:219], v[164:167], v[232:235], v[216:219]\n\t" \
    "v_mfma_f32_16x16x32_bf16 v[212:215], v[244:247], v[228:231], v[212:215]\n\t" \
    "ds_read_b128 v[228:231], v252 offset:1024\n\t" \
    "v_mfma_f32_16x16x32_bf16 v[212:215], v[248:251], v[232:235], v[212:215]\n\t" \
    "ds_read_b128 v[232:235], v252 offset:16384\n\t" \
    "s_waitcnt lgkmcnt(4)\n\t" \
    "v_mfma_f32_16x16x32_bf16 v[200:203], v[244:247], v[236:239], v[200:203]\n\t" \
    "ds_read_b128 v[244:247], %[wl] offset:4096\n\t" \
    "s_waitcnt lgkmcnt(4)\n\t" \
    "v_mfma_f32_16x16x32_bf16 v[200:203], v[248:251], v[240:243], v[200:203]\n\t" \
    "ds_read_b128 v[248:251], %[wl] offset:5120\n\t" \
    "v_mfma_f32_16x16x32_bf16 v[204:207], v[160:163], v[236:239], v[204:207]\n\t" \
    "v_mfma_f32_16x16x32_bf16 v[204:207], v[164:167], v[240:243], v[204:207]\n\t" \
    "v_mfma_f32_16x16x32_bf16 v[208:211], v[168:171], v[236:239], v[208:211]\n\t" \
    "ds_read_b128 v[236:239], v252 offset:17408\n\t" \
    "v_mfma_f32_16x16x32_bf16 v[208:211], v[172:175], v[240:243], v[208:211]\n\t" \
    "v_xor_b32 v252, 0x2800, %[xa]\n\t" \
    "ds_read_b128 v[240:243], v252 offset:16384\n\t" \
    "s_waitcnt lgkmcnt(6)\n\t" \
    "v_mfma_f32_16x16x32_bf16 v[208:211], v[88:91], v[224:227], v[208:211]\n\t" \
    "s_waitcnt lgkmcnt(5)\n\t" \
    "v_mfma_f32_16x16x32_bf16 v[208:211], v[92:95], v[228:231], v[208:211]\n\t" \
    "v_mfma_f32_16x16x32_bf16 v[204:207], v[80:83], v[224:227], v[204:207]\n\t" \
    "v_mfma_f32_16x16x32_bf16 v[204:207], v[84:87], v[228:231], v[204:207]\n\t" \
    "s_waitcnt lgkmcnt(3)\n\t" \
    "v_mfma_f32_16x16x32_bf16 v[200:203], v[244:247], v[224:227], v[200:203]\n\t" \
    "ds_read_b128 v[224:227], v252 offset:17408\n\t" \
    "s_waitcnt lgkmcnt(3)\n\t" \
    "v_mfma_f32_16x16x32_bf16 v[200:203], v[248:251], v[228:231], v[200:203]\n\t" \
    "ds_read_b128 v[228:231], v252 offset:0\n\t" \
    "v_mfma_f32_16x16x32_bf16 v[212:215], v[244:247], v[232:235], v[212:215]\n\t" \
    "ds_read_b128 v[244:247], %[wl] offset:6144\n\t" \
    "s_waitcnt lgkmcnt(4)\n\t" \
    "v_mfma_f32_16x16x32_bf16 v[212:215], v[248:251], v[236:239], v[212:215]\n\t" \
    "ds_read_b128 v[248:251], %[wl] offset:7168\n\t" \
    "v_mfma_f32_16x16x32_bf16 v[216:219], v[80:83], v[232:235], v[216:219]\n\t" \
    "v_mfma_f32_16x16x32_bf16 v[216:219], v[84:87], v[236:239], v[216:219]\n\t" \
    "v_mfma_f32_16x16x32_bf16 v[220:223], v[88:91], v[232:235], v[220:223]\n\t" \
    "ds_read_b128 v[232:235], v252 offset:1024\n\t" \
    "v_mfma_f32_16x16x32_bf16 v[220:223], v[92:95], v[236:239], v[220:223]\n\t" \
    "v_xor_b32 v252, 0x3000, %[xa]\n\t" \
    "ds_read_b128 v[236:239], v252 offset:0\n\t" \
    "s_waitcnt lgkmcnt(6)\n\t" \
    "v_mfma_f32_16x16x32_bf16 v[220:223], v[112:115], v[240:243], v[220:223]\n\t" \
    "s_waitcnt lgkmcnt(5)\n\t" \
    "v_mfma_f32_16x16x32_bf16 v[220:223], v[116:119], v[224:227], v[220:223]\n\t" \
    "v_mfma_f32_16x16x32_bf16 v[216:219], v[104:107], v[240:243], v[216:219]\n\t" \
    "v_mfma_f32_16x16x32_bf16 v[216:219], v[108:111], v[224:227], v[216:219]\n\t" \
    "v_mfma_f32_16x16x32_bf16 v[212:215], v[96:99], v[240:243], v[212:215]\n\t" \
    "ds_read_b128 v[240:243], v252 offset:1024\n\t" \
    "v_mfma_f32_16x16x32_bf16 v[212:215], v[100:103], v[224:227], v[212:215]\n\t" \
    "ds_read_b128 v[224:227], v252 offset:16384\n\t" \
    "s_waitcnt lgkmcnt(6)\n\t" \
    "v_mfma_f32_16x16x32_bf16 v[200:203], v[96:99], v[228:231], v[200:203]\n\t" \
    "s_waitcnt lgkmcnt(3)\n\t" \
    "v_mfma_f32_16x16x32_bf16 v[200:203], v[100:103], v[232:235], v[200:203]\n\t" \
    "v_mfma_f32_16x16x32_bf16 v[204:207], v[104:107], v[228:231], v[204:207]\n\t" \
    "v_mfma_f32_16x16x32_bf16 v[204:207], v[108:111], v[232:235], v[204:207]\n\t" \
    "v_mfma_f32_16x16x32_bf16 v[208:211], v[112:115], v[228:231], v[208:211]\n\t" \
    "ds_read_b128 v[228:231], v252 offset:17408\n\t" \
    "v_mfma_f32_16x16x32_bf16 v[208:211], v[116:119], v[232:235], v[208:211]\n\t" \
    "v_xor_b32 v252, 0x3800, %[xa]\n\t" \
    "ds_read_b128 v[232:235], v252 offset:16384\n\t" \
    "s_waitcnt lgkmcnt(4)\n\t" \
    "v_mfma_f32_16x16x32_bf16 v[208:211], v[128:131], v[236:239], v[208:211]\n\t" \
    "s_waitcnt lgkmcnt(3)\n\t" \
    "v_mfma_f32_16x16x32_bf16 v[208:211], v[132:135], v[240:243], v[208:211]\n\t" \
    "v_mfma_f32_16x16x32_bf16 v[204:207], v[120:123], v[236:239], v[204:207]\n\t" \
    "v_mfma_f32_16x16x32_bf16 v[204:207], v[124:127], v[240:243], v[204:207]\n\t" \
    "v_mfma_f32_16x16x32_bf16 v[200:203], v[244:247], v[236:239], v[200:203]\n\t" \
    "ds_read_b128 v[236:239], v252 offset:17408\n\t" \
    "v_mfma_f32_16x16x32_bf16 v[200:203], v[248:251], v[240:243], v[200:203]\n\t" \
    "ds_read_b128 v[240:243], v252 offset:0\n\t" \
    "s_waitcnt lgkmcnt(4)\n\t" \
    "v_mfma_f32_16x16x32_bf16 v[212:215], v[244:247], v[224:227], v[212:215]\n\t" \
    "s_waitcnt lgkmcnt(3)\n\t" \
    "v_mfma_f32_16x16x32_bf16 v[212:215], v[248:251], v[228:231], v[212:215]\n\t" \
    "v_mfma_f32_16x16x32_bf16 v[216:219], v[120:123], v[224:227], v[216:219]\n\t" \
    "v_mfma_f32_16x16x32_bf16 v[216:219], v[124:127], v[228:231], v[216:219]\n\t" \
    "v_mfma_f32_16x16x32_bf16 v[220:223], v[128:131], v[224:227], v[220:223]\n\t" \
    "ds_read_b128 v[224:227], v252 offset:1024\n\t" \
    "v_mfma_f32_16x16x32_bf16 v[220:223], v[132:135], v[228:231], v[220:223]\n\t" \
    "v_and_b32 v252, 0x3ff, %[xa]\n\t" \
    "ds_read_b128 v[228:231], v252 offset:43008\n\t" \
    "s_waitcnt lgkmcnt(4)\n\t" \
    "v_mfma_f32_16x16x32_bf16 v[220:223], v[152:155], v[232:235], v[220:223]\n\t" \
    "s_waitcnt lgkmcnt(3)\n\t" \
    "v_mfma_f32_16x16x32_bf16 v[220:223], v[156:159], v[236:239], v[220:223]\n\t" \
    "v_mfma_f32_16x16x32_bf16 v[216:219], v[144:147], v[232:235], v[216:219]\n\t" \
    "v_mfma_f32_16x16x32_bf16 v[216:219], v[148:151], v[236:239], v[216:219]\n\t" \
    "v_mfma_f32_16x16x32_bf16 v[212:215], v[136:139], v[232:235], v[212:215]\n\t" \
    "ds_read_b128 v[232:235], v252 offset:44032\n\t" \
    "v_mfma_f32_16x16x32_bf16 v[212:215], v[140:143], v[236:239], v[212:215]\n\t" \
    "ds_read_b128 v[236:239], v252 offset:47104\n\t" \
    "s_waitcnt lgkmcnt(4)\n\t" \
    "v_mfma_f32_16x16x32_bf16 v[200:203], v[136:139], v[240:243], v[200:203]\n\t" \
    "s_waitcnt lgkmcnt(3)\n\t" \
    "v_mfma_f32_16x16x32_bf16 v[200:203], v[140:143], v[224:227], v[200:203]\n\t" \
    "v_mfma_f32_16x16x32_bf16 v[204:207], v[144:147], v[240:243], v[204:207]\n\t" \
    "v_mfma_f32_16x16x32_bf16 v[204:207], v[148:151], v[224:227], v[204:207]\n\t" \
    "v_mfma_f32_16x16x32_bf16 v[208:211], v[152:155], v[240:243], v[208:211]\n\t" \
    "ds_read_b128 v[240:243], v252 offset:48128\n\t" \
    "v_mfma_f32_16x16x32_bf16 v[208:211], v[156:159], v[224:227], v[208:211]\n\t" \
    "s_waitcnt lgkmcnt(3)\n\t" \
    "v_mfma_f32_16x16x32_bf16 v[208:211], v[192:195], v[228:231], v[208:211]\n\t" \
    "v_add_u32 v252, %[qs], v252\n\t" \
    "s_waitcnt lgkmcnt(2)\n\t" \
    "v_mfma_f32_16x16x32_bf16 v[208:211], v[196:199], v[232:235], v[208:211]\n\t" \
    "v_mfma_f32_16x16x32_bf16 v[204:207], v[184:187], v[228:231], v[204:207]\n\t" \
    "v_mfma_f32_16x16x32_bf16 v[204:207], v[188:191], v[232:235], v[204:207]\n\t" \
    "v_mfma_f32_16x16x32_bf16 v[200:203], v[176:179], v[228:231], v[200:203]\n\t" \
    "v_mfma_f32_16x16x32_bf16 v[200:203], v[180:183], v[232:235], v[200:203]\n\t" \
    "s_waitcnt lgkmcnt(1)\n\t" \
    "v_mfma_f32_16x16x32_bf16 v[212:215], v[176:179], v[236:239], v[212:215]\n\t" \
    "v_cvt_pk_bf16_f32 v208, v208, v209\n\t" \
    "v_cvt_pk_bf16_f32 v209, v210, v211\n\t" \
    "s_waitcnt lgkmcnt(0)\n\t" \
    "v_mfma_f32_16x16x32_bf16 v[212:215], v[180:183], v[240:243], v[212:215]\n\t" \
    "ds_write_b64 v252, v[208:209] offset:2048\n\t" \
    "v_cvt_pk_bf16_f32 v204, v204, v205\n\t" \
    "v_mfma_f32_16x16x32_bf16 v[216:219], v[184:187], v[236:239], v[216:219]\n\t" \
    "v_cvt_pk_bf16_f32 v205, v206, v207\n\t" \
    "ds_write_b64 v252, v[204:205] offset:1024\n\t" \
    "v_mfma_f32_16x16x32_bf16 v[216:219], v[188:191], v[240:243], v[216:219]\n\t" \
    "v_cvt_pk_bf16_f32 v200, v200, v201\n\t" \
    "v_cvt_pk_bf16_f32 v201, v202, v203\n\t" \
    "v_mfma_f32_16x16x32_bf16 v[220:223], v[192:195], v[236:239], v[220:223]\n\t" \
    "ds_write_b64 v252, v[200:201] offset:0\n\t" \
    "v_mfma_f32_16x16x32_bf16 v[220:223], v[196:199], v[240:243], v[220:223]\n\t" \
    "v_cvt_pk_bf16_f32 v212, v212, v213\n\t" \
    "v_cvt_pk_bf16_f32 v213, v214, v215\n\t" \
    "ds_write_b64 v252, v[212:213] offset:6144\n\t" \
    "v_cvt_pk_bf16_f32 v216, v216, v217\n\t" \
    "v_cvt_pk_bf16_f32 v217, v218, v219\n\t" \
    "ds_write_b64 v252, v[216:217] offset:7168\n\t" \
    "s_nop 1\n\t" \
    "v_cvt_pk_bf16_f32 v220, v220, v221\n\t" \
    "v_cvt_pk_bf16_f32 v221, v222, v223\n\t" \
    "ds_write_b64 v252, v[220:221] offset:8192\n\t" \
    "s_waitcnt lgkmcnt(0)\n\t"
#define ASM_XG_H_LO \
    "s_waitcnt lgkmcnt(0)\n\t" \
    "ds_read_b128 v[224:227], %[xa] offset:16384\n\t" \
    "ds_read_b128 v[228:231], %[xa] offset:17408\n\t" \
    "ds_read_b128 v[244:247], %[wl] offset:0\n\t" \
    "ds_read_b128 v[248:251], %[wl] offset:1024\n\t" \
    "ds_read_b128 v[232:235], %[xa] offset:0\n\t" \
    "ds_read_b128 v[236:239], %[xa] offset:1024\n\t" \
    "v_xor_b32 v252, 0x800, %[xa]\n\t" \
    "ds_read_b128 v[240:243], v252 offset:0\n\t" \
    "s_waitcnt lgkmcnt(6)\n\t" \
    "v_mfma_f32_16x16x32_bf16 v[220:223], v[8:11], v[224:227], 0\n\t" \
    "s_waitcnt lgkmcnt(5)\n\t" \
    "v_mfma_f32_16x16x32_bf16 v[220:223], v[12:15], v[228:231], v[220:223]\n\t" \
    "v_mfma_f32_16x16x32_bf16 v[216:219], v[0:3], v[224:227], 0\n\t" \
    "v_mfma_f32_16x16x32_bf16 v[216:219], v[4:7], v[228:231], v[216:219]\n\t" \
    "s_waitcnt lgkmcnt(4)\n\t" \
    "v_mfma_f32_16x16x32_bf16 v[212:215], v[244:247], v[224:227], 0\n\t" \
    "ds_read_b128 v[224:227], v252 offset:1024\n\t" \
    "s_waitcnt lgkmcnt(4)\n\t" \
    "v_mfma_f32_16x16x32_bf16 v[212:215], v[248:251], v[228:231], v[212:215]\n\t" \
    "ds_read_b128 v[228:231], v252 offset:16384\n\t" \
    "s_waitcnt lgkmcnt(4)\n\t" \
    "v_mfma_f32_16x16x32_bf16 v[200:203], v[244:247], v[232:235], 0\n\t" \
    "ds_read_b128 v[244:247], %[wl] offset:2048\n\t" \
    "s_waitcnt lgkmcnt(4)\n\t" \
    "v_mfma_f32_16x16x32_bf16 v[200:203], v[248:251], v[236:239], v[200:203]\n\t" \
    "ds_read_b128 v[248:251], %[wl] offset:3072\n\t" \
    "v_mfma_f32_16x16x32_bf16 v[204:207], v[0:3], v[232:235], 0\n\t" \
    "v_mfma_f32_16x16x32_bf16 v[204:207], v[4:7], v[236:239], v[204:207]\n\t" \
    "v_mfma_f32_16x16x32_bf16 v[208:211], v[8:11], v[232:235], 0\n\t" \
    "ds_read_b128 v[232:235], v252 offset:17408\n\t" \
    "v_mfma_f32_16x16x32_bf16 v[208:211], v[12:15], v[236:239], v[208:211]\n\t" \
    "v_xor_b32 v252, 0x1000, %[xa]\n\t" \
    "ds_read_b128 v[236:239], v252 offset:16384\n\t" \
    "s_waitcnt lgkmcnt(6)\n\t" \
    "v_mfma_f32_16x16x32_bf16 v[208:211], v[32:35], v[240:243], v[208:211]\n\t" \
    "s_waitcnt lgkmcnt(5)\n\t" \
    "v_mfma_f32_16x16x32_bf16 v[208:211], v[36:39], v[224:227], v[208:211]\n\t" \
    "v_mfma_f32_16x16x32_bf16 v[204:207], v[24:27], v[240:243], v[204:207]\n\t" \
    "v_mfma_f32_16x16x32_bf16 v[204:207], v[28:31], v[224:227], v[204:207]\n\t" \
    "v_mfma_f32_16x16x32_bf16 v[200:203], v[16:19], v[240:243], v[200:203]\n\t" \
    "ds_read_b128 v[240:243], v252 offset:17408\n\t" \
    "v_mfma_f32_16x16x32_bf16 v[200:203], v[20:23], v[224:227], v[200:203]\n\t" \
    "ds_read_b128 v[224:227], v252 offset:0\n\t" \
    "s_waitcnt lgkmcnt(6)\n\t" \
    "v_mfma_f32_16x16x32_bf16 v[212:215], v[16:19], v[228:231], v[212:215]\n\t" \
    "s_waitcnt lgkmcnt(3)\n\t" \
    "v_mfma_f32_16x16x32_bf16 v[212:215], v[20:23], v[232:235], v[212:215]\n\t" \
    "v_mfma_f32_16x16x32_bf16 v[216:219], v[24:27], v[228:231], v[216:219]\n\t" \
    "v_mfma_f32_16x16x32_bf16 v[216:219], v[28:31], v[232:235], v[216:219]\n\t" \
    "v_mfma_f32_16x16x32_bf16 v[220:223], v[32:35], v[228:231], v[220:223]\n\t" \
    "ds_read_b128 v[228:231], v252 offset:1024\n\t" \
    "v_mfma_f32_16x16x32_bf16 v[220:223], v[36:39], v[232:235], v[220:223]\n\t" \
    "v_xor_b32 v252, 0x1800, %[xa]\n\t" \
    "ds_read_b128 v[232:235], v252 offset:0\n\t" \
    "s_waitcnt lgkmcnt(4)\n\t" \
    "v_mfma_f32_16x16x32_bf16 v[220:223], v[48:51], v[236:239], v[220:223]\n\t" \
    "s_waitcnt lgkmcnt(3)\n\t" \
    "v_mfma_f32_16x16x32_bf16 v[220:223], v[52:55], v[240:243], v[220:223]\n\t" \
    "v_mfma_f32_16x16x32_bf16 v[216:219], v[40:43], v[236:239], v[216:219]\n\t" \
    "v_mfma_f32_16x16x32_bf16 v[216:219], v[44:47], v[240:243], v[216:219]\n\t" \
    "v_mfma_f32_16x16x32_bf16 v[212:215], v[244:247], v[236:239], v[212:215]\n\t" \
    "ds_read_b128 v[236:239], v252 offset:1024\n\t" \
    "v_mfma_f32_16x16x32_bf16 v[212:215], v[248:251], v[240:243], v[212:215]\n\t" \
    "ds_read_b128 v[240:243], v252 offset:16384\n\t" \
    "s_waitcnt lgkmcnt(4)\n\t" \
    "v_mfma_f32_16x16x32_bf16 v[200:203], v[244:247], v[224:227], v[200:203]\n\t" \
    "ds_read_b128 v[244:247], %[wl] offset:8192\n\t" \
    "s_waitcnt lgkmcnt(4)\n\t" \
    "v_mfma_f32_16x16x32_bf16 v[200:203], v[248:251], v[228:231], v[200:203]\n\t" \
    "ds_read_b128 v[248:251], %[wl] offset:9216\n\t" \
    "v_mfma_f32_16x16x32_bf16 v[204:207], v[40:43], v[224:227], v[204:207]\n\t" \
    "v_mfma_f32_16x16x32_bf16 v[204:207], v[44:47], v[228:231], v[204:207]\n\t" \
    "v_mfma_f32_16x16x32_bf16 v[208:211], v[48:51], v[224:227], v[208:211]\n\t" \
    "ds_read_b128 v[224:227], v252 offset:17408\n\t" \
    "v_mfma_f32_16x16x32_bf16 v[208:211], v[52:55], v[228:231], v[208:211]\n\t" \
    "v_and_b32 v252, 0x3ff, %[xa]\n\t" \
    "ds_read_b128 v[228:231], v252 offset:45056\n\t" \
    "s_waitcnt lgkmcnt(6)\n\t" \
    "v_mfma_f32_16x16x32_bf16 v[208:211], v[72:75], v[232:235], v[208:211]\n\t" \
    "s_waitcnt lgkmcnt(5)\n\t" \
    "v_mfma_f32_16x16x32_bf16 v[208:211], v[76:79], v[236:239], v[208:211]\n\t" \
    "v_mfma_f32_16x16x32_bf16 v[204:207], v[64:67], v[232:235], v[204:207]\n\t" \
    "v_mfma_f32_16x16x32_bf16 v[204:207], v[68:71], v[236:239], v[204:207]\n\t" \
    "v_mfma_f32_16x16x32_bf16 v[200:203], v[56:59], v[232:235], v[200:203]\n\t" \
    "ds_read_b128 v[232:235], v252 offset:46080\n\t" \
    "v_mfma_f32_16x16x32_bf16 v[200:203], v[60:63], v[236:239], v[200:203]\n\t" \
    "ds_read_b128 v[236:239], v252 offset:40960\n\t" \
    "s_waitcnt lgkmcnt(6)\n\t" \
    "v_mfma_f32_16x16x32_bf16 v[212:215], v[56:59], v[240:243], v[212:215]\n\t" \
    "s_waitcnt lgkmcnt(3)\n\t" \
    "v_mfma_f32_16x16x32_bf16 v[212:215], v[60:63], v[224:227], v[212:215]\n\t" \
    "v_mfma_f32_16x16x32_bf16 v[216:219], v[64:67], v[240:243], v[216:219]\n\t" \
    "v_mfma_f32_16x16x32_bf16 v[216:219], v[68:71], v[224:227], v[216:219]\n\t" \
    "v_mfma_f32_16x16x32_bf16 v[220:223], v[72:75], v[240:243], v[220:223]\n\t" \
    "ds_read_b128 v[240:243], v252 offset:41984\n\t" \
    "v_mfma_f32_16x16x32_bf16 v[220:223], v[76:79], v[224:227], v[220:223]\n\t" \
    "s_waitcnt lgkmcnt(0)\n\t" \
    "s_barrier\n\t" ASM_ST \
    "v_xor_b32 v252, 0x2000, %[xa]\n\t" \
    "ds_read_b128 v[224:227], v252 offset:0\n\t" \
    "v_mfma_f32_16x16x32_bf16 v[220:223], v[168:171], v[228:231], v[220:223]\n\t" \
    "v_mfma_f32_16x16x32_bf16 v[220:223], v[172:175], v[232:235], v[220:223]\n\t" \
    "v_mfma_f32_16x16x32_bf16 v[216:219], v[160:163], v[228:231], v[216:219]\n\t" \
    "v_mfma_f32_16x16x32_bf16 v[216:219], v[164:167], v[232:235], v[216:219]\n\t" \
    "v_mfma_f32_16x16x32_bf16 v[212:215], v[244:247], v[228:231], v[212:215]\n\t" \
    "ds_read_b128 v[228:231], v252 offset:1024\n\t" \
    "v_mfma_f32_16x16x32_bf16 v[212:215], v[248:251], v[232:235], v[212:215]\n\t" \
    "ds_read_b128 v[232:235], v252 offset:16384\n\t" \
    "v_mfma_f32_16x16x32_bf16 v[200:203], v[244:247], v[236:239], v[200:203]\n\t" \
    "ds_read_b128 v[244:247], %[wl] offset:4096\n\t" \
    "v_mfma_f32_16x16x32_bf16 v[200:203], v[248:251], v[240:243], v[200:203]\n\t" \
    "ds_read_b128 v[248:251], %[wl] offset:5120\n\t" \
    "v_mfma_f32_16x16x32_bf16 v[204:207], v[160:163], v[236:239], v[204:207]\n\t" \
    "v_mfma_f32_16x16x32_bf16 v[204:207], v[164:167], v[240:243], v[204:207]\n\t" \
    "v_mfma_f32_16x16x32_bf16 v[208:211], v[168:171], v[236:239], v[208:211]\n\t" \
    "ds_read_b128 v[236:239], v252 offset:17408\n\t" \
    "v_mfma_f32_16x16x32_bf16 v[208:211], v[172:175], v[240:243], v[208:211]\n\t" \
    "v_xor_b32 v252, 0x2800, %[xa]\n\t" \
    "ds_read_b128 v[240:243], v252 offset:16384\n\t" \
    "s_waitcnt lgkmcnt(6)\n\t" \
    "v_mfma_f32_16x16x32_bf16 v[208:211], v[88:91], v[224:227], v[208:211]\n\t" \
    "s_waitcnt lgkmcnt(5)\n\t" \
    "v_mfma_f32_16x16x32_bf16 v[208:211], v[92:95], v[228:231], v[208:211]\n\t" \
    "v_mfma_f32_16x16x32_bf16 v[204:207], v[80:83], v[224:227], v[204:207]\n\t" \
    "v_mfma_f32_16x16x32_bf16 v[204:207], v[84:87], v[228:231], v[204:207]\n\t" \
    "s_waitcnt lgkmcnt(3)\n\t" \
    "v_mfma_f32_16x16x32_bf16 v[200:203], v[244:247], v[224:227], v[200:203]\n\t" \
    "ds_read_b128 v[224:227], v252 offset:17408\n\t" \
    "s_waitcnt lgkmcnt(3)\n\t" \
    "v_mfma_f32_16x16x32_bf16 v[200:203], v[248:251], v[228:231], v[200:203]\n\t" \
    "ds_read_b128 v[228:231], v252 offset:0\n\t" \
    "v_mfma_f32_16x16x32_bf16 v[212:215], v[244:247], v[232:235], v[212:215]\n\t" \
    "ds_read_b128 v[244:247], %[wl] offset:6144\n\t" \
    "s_waitcnt lgkmcnt(4)\n\t" \
    "v_mfma_f32_16x16x32_bf16 v[212:215], v[248:251], v[236:239], v[212:215]\n\t" \
    "ds_read_b128 v[248:251], %[wl] offset:7168\n\t" \
    "v_mfma_f32_16x16x32_bf16 v[216:219], v[80:83], v[232:235], v[216:219]\n\t" \
    "v_mfma_f32_16x16x32_bf16 v[216:219], v[84:87], v[236:239], v[216:219]\n\t" \
    "v_mfma_f32_16x16x32_bf16 v[220:223], v[88:91], v[232:235], v[220:223]\n\t" \
    "ds_read_b128 v[232:235], v252 offset:1024\n\t" \
    "v_mfma_f32_16x16x32_bf16 v[220:223], v[92:95], v[236:239], v[220:223]\n\t" \
    "v_xor_b32 v252, 0x3000, %[xa]\n\t" \
    "ds_read_b128 v[236:239], v252 offset:0\n\t" \
    "s_waitcnt lgkmcnt(6)\n\t" \
    "v_mfma_f32_16x16x32_bf16 v[220:223], v[112:115], v[240:243], v[220:223]\n\t" \
    "s_waitcnt lgkmcnt(5)\n\t" \
    "v_mfma_f32_16x16x32_bf16 v[220:223], v[116:119], v[224:227], v[220:223]\n\t" \
    "v_mfma_f32_16x16x32_bf16 v[216:219], v[104:107], v[240:243], v[216:219]\n\t" \
    "v_mfma_f32_16x16x32_bf16 v[216:219], v[108:111], v[224:227], v[216:219]\n\t" \
    "v_mfma_f32_16x16x32_bf16 v[212:215], v[96:99], v[240:243], v[212:215]\n\t" \
    "ds_read_b128 v[240:243], v252 offset:1024\n\t" \
    "v_mfma_f32_16x16x32_bf16 v[212:215], v[100:103], v[224:227], v[212:215]\n\t" \
    "ds_read_b128 v[224:227], v252 offset:16384\n\t" \
    "s_waitcnt lgkmcnt(6)\n\t" \
    "v_mfma_f32_16x16x32_bf16 v[200:203], v[96:99], v[228:231], v[200:203]\n\t" \
    "s_waitcnt lgkmcnt(3)\n\t" \
    "v_mfma_f32_16x16x32_bf16 v[200:203], v[100:103], v[232:235], v[200:203]\n\t" \
    "v_mfma_f32_16x16x32_bf16 v[204:207], v[104:107], v[228:231], v[204:207]\n\t" \
    "v_mfma_f32_16x16x32_bf16 v[204:207], v[108:111], v[232:235], v[204:207]\n\t" \
    "v_mfma_f32_16x16x32_bf16 v[208:211], v[112:115], v[228:231], v[208:211]\n\t" \
    "ds_read_b128 v[228:231], v252 offset:17408\n\t" \
    "v_mfma_f32_16x16x32_bf16 v[208:211], v[116:119], v[232:235], v[208:211]\n\t" \
    "v_xor_b32 v252, 0x3800, %[xa]\n\t" \
    "ds_read_b128 v[232:235], v252 offset:16384\n\t" \
    "s_waitcnt lgkmcnt(4)\n\t" \
    "v_mfma_f32_16x16x32_bf16 v[208:211], v[128:131], v[236:239], v[208:211]\n\t" \
    "s_waitcnt lgkmcnt(3)\n\t" \
    "v_mfma_f32_16x16x32_bf16 v[208:211], v[132:135], v[240:243], v[208:211]\n\t" \
    "v_mfma_f32_16x16x32_bf16 v[204:207], v[120:123], v[236:239], v[204:207]\n\t" \
    "v_mfma_f32_16x16x32_bf16 v[204:207], v[124:127], v[240:243], v[204:207]\n\t" \
    "v_mfma_f32_16x16x32_bf16 v[200:203], v[244:247], v[236:239], v[200:203]\n\t" \
    "ds_read_b128 v[236:239], v252 offset:17408\n\t" \
    "v_mfma_f32_16x16x32_bf16 v[200:203], v[248:251], v[240:243], v[200:203]\n\t" \
    "ds_read_b128 v[240:243], v252 offset:0\n\t" \
    "s_waitcnt lgkmcnt(4)\n\t" \
    "v_mfma_f32_16x16x32_bf16 v[212:215], v[244:247], v[224:227], v[212:215]\n\t" \
    "s_waitcnt lgkmcnt(3)\n\t" \
    "v_mfma_f32_16x16x32_bf16 v[212:215], v[248:251], v[228:231], v[212:215]\n\t" \
    "v_mfma_f32_16x16x32_bf16 v[216:219], v[120:123], v[224:227], v[216:219]\n\t" \
    "v_mfma_f32_16x16x32_bf16 v[216:219], v[124:127], v[228:231], v[216:219]\n\t" \
    "v_mfma_f32_16x16x32_bf16 v[220:223], v[128:131], v[224:227], v[220:223]\n\t" \
    "ds_read_b128 v[224:227], v252 offset:1024\n\t" \
    "v_mfma_f32_16x16x32_bf16 v[220:223], v[132:135], v[228:231], v[220:223]\n\t" \
    "v_and_b32 v252, 0x3ff, %[xa]\n\t" \
    "ds_read_b128 v[228:231], v252 offset:43008\n\t" \
    "s_waitcnt lgkmcnt(4)\n\t" \
    "v_mfma_f32_16x16x32_bf16 v[220:223], v[152:155], v[232:235], v[220:223]\n\t" \
    "s_waitcnt lgkmcnt(3)\n\t" \
    "v_mfma_f32_16x16x32_bf16 v[220:223], v[156:159], v[236:239], v[220:223]\n\t" \
    "v_mfma_f32_16x16x32_bf16 v[216:219], v[144:147], v[232:235], v[216:219]\n\t" \
    "v_mfma_f32_16x16x32_bf16 v[216:219], v[148:151], v[236:239], v[216:219]\n\t" \
    "v_mfma_f32_16x16x32_bf16 v[212:215], v[136:139], v[232:235], v[212:215]\n\t" \
    "ds_read_b128 v[232:235], v252 offset:44032\n\t" \
    "v_mfma_f32_16x16x32_bf16 v[212:215], v[140:143], v[236:239], v[212:215]\n\t" \
    "ds_read_b128 v[236:239], v252 offset:47104\n\t" \
    "s_waitcnt lgkmcnt(4)\n\t" \
    "v_mfma_f32_16x16x32_bf16 v[200:203], v[136:139], v[240:243], v[200:203]\n\t" \
    "s_waitcnt lgkmcnt(3)\n\t" \
    "v_mfma_f32_16x16x32_bf16 v[200:203], v[140:143], v[224:227], v[200:203]\n\t" \
    "v_mfma_f32_16x16x32_bf16 v[204:207], v[144:147], v[240:243], v[204:207]\n\t" \
    "v_mfma_f32_16x16x32_bf16 v[204:207], v[148:151], v[224:227], v[204:207]\n\t" \
    "v_mfma_f32_16x16x32_bf16 v[208:211], v[152:155], v[240:243], v[208:211]\n\t" \
    "ds_read_b128 v[240:243], v252 offset:48128\n\t" \
    "v_mfma_f32_16x16x32_bf16 v[208:211], v[156:159], v[224:227], v[208:211]\n\t" \
    "s_waitcnt lgkmcnt(3)\n\t" \
    "v_mfma_f32_16x16x32_bf16 v[208:211], v[192:195], v[228:231], v[208:211]\n\t" \
    "v_add_u32 v252, %[qs], v252\n\t" \
    "s_waitcnt lgkmcnt(2)\n\t" \
    "v_mfma_f32_16x16x32_bf16 v[208:211], v[196:199], v[232:235], v[208:211]\n\t" \
    "v_mfma_f32_16x16x32_bf16 v[204:207], v[184:187], v[228:231], v[204:207]\n\t" \
    "v_mfma_f32_16x16x32_bf16 v[204:207], v[188:191], v[232:235], v[204:207]\n\t" \
    "v_mfma_f32_16x16x32_bf16 v[200:203], v[176:179], v[228:231], v[200:203]\n\t" \
    "v_mfma_f32_16x16x32_bf16 v[200:203], v[180:183], v[232:235], v[200:203]\n\t" \
    "s_waitcnt lgkmcnt(1)\n\t" \
    "v_mfma_f32_16x16x32_bf16 v[212:215], v[176:179], v[236:239], v[212:215]\n\t" \
    "v_cvt_pk_bf16_f32 v208, v208, v209\n\t" \
    "v_cvt_pk_bf16_f32 v209, v210, v211\n\t" \
    "s_waitcnt lgkmcnt(0)\n\t" \
    "v_mfma_f32_16x16x32_bf16 v[212:215], v[180:183], v[240:243], v[212:215]\n\t" \
    "ds_write_b64 v252, v[208:209] offset:2048\n\t" \
    "v_cvt_pk_bf16_f32 v204, v204, v205\n\t" \
    "v_mfma_f32_16x16x32_bf16 v[216:219], v[184:187], v[236:239], v[216:219]\n\t" \
    "v_cvt_pk_bf16_f32 v205, v206, v207\n\t" \
    "ds_write_b64 v252, v[204:205] offset:1024\n\t" \
    "v_mfma_f32_16x16x32_bf16 v[216:219], v[188:191], v[240:243], v[216:219]\n\t" \
    "v_cvt_pk_bf16_f32 v200, v200, v201\n\t" \
    "v_cvt_pk_bf16_f32 v201, v202, v203\n\t" \
    "v_mfma_f32_16x16x32_bf16 v[220:223], v[192:195], v[236:239], v[220:223]\n\t" \
    "ds_write_b64 v252, v[200:201] offset:0\n\t" \
    "v_mfma_f32_16x16x32_bf16 v[220:223], v[196:199], v[240:243], v[220:223]\n\t" \
    "v_cvt_pk_bf16_f32 v212, v212, v213\n\t" \
    "v_cvt_pk_bf16_f32 v213, v214, v215\n\t" \
    "ds_write_b64 v252, v[212:213] offset:6144\n\t" \
    "v_cvt_pk_bf16_f32 v216, v216, v217\n\t" \
    "v_cvt_pk_bf16_f32 v217, v218, v219\n\t" \
    "ds_write_b64 v252, v[216:217] offset:7168\n\t" \
    "s_nop 1\n\t" \
    "v_cvt_pk_bf16_f32 v220, v220, v221\n\t" \
    "v_cvt_pk_bf16_f32 v221, v222, v223\n\t" \
    "ds_write_b64 v252, v[220:221] offset:8192\n\t" \
    "s_waitcnt lgkmcnt(0)\n\t"
#define ASM_XG_H_HI \
    "s_waitcnt lgkmcnt(0)\n\t" \
    "v_xor_b32 v252, 0x2000, %[xa]\n\t" \
    "ds_read_b128 v[224:227], v252 offset:16384\n\t" \
    "ds_read_b128 v[228:231], v252 offset:17408\n\t" \
    "ds_read_b128 v[244:247], %[wl] offset:4096\n\t" \
    "ds_read_b128 v[248:251], %[wl] offset:5120\n\t" \
    "ds_read_b128 v[232:235], v252 offset:0\n\t" \
    "ds_read_b128 v[236:239], v252 offset:1024\n\t" \
    "v_xor_b32 v252, 0x2800, %[xa]\n\t" \
    "ds_read_b128 v[240:243], v252 offset:0\n\t" \
    "s_waitcnt lgkmcnt(6)\n\t" \
    "v_mfma_f32_16x16x32_bf16 v[220:223], v[88:91], v[224:227], 0\n\t" \
    "s_waitcnt lgkmcnt(5)\n\t" \
    "v_mfma_f32_16x16x32_bf16 v[220:223], v[92:95], v[228:231], v[220:223]\n\t" \
    "v_mfma_f32_16x16x32_bf16 v[216:219], v[80:83], v[224:227], 0\n\t" \
    "v_mfma_f32_16x16x32_bf16 v[216:219], v[84:87], v[228:231], v[216:219]\n\t" \
    "s_waitcnt lgkmcnt(4)\n\t" \
    "v_mfma_f32_16x16x32_bf16 v[212:215], v[244:247], v[224:227], 0\n\t" \
    "ds_read_b128 v[224:227], v252 offset:1024\n\t" \
    "s_waitcnt lgkmcnt(4)\n\t" \
    "v_mfma_f32_16x16x32_bf16 v[212:215], v[248:251], v[228:231], v[212:215]\n\t" \
    "ds_read_b128 v[228:231], v252 offset:16384\n\t" \
    "s_waitcnt lgkmcnt(4)\n\t" \
    "v_mfma_f32_16x16x32_bf16 v[200:203], v[244:247], v[232:235], 0\n\t" \
    "ds_read_b128 v[244:247], %[wl] offset:6144\n\t" \
    "s_waitcnt lgkmcnt(4)\n\t" \
    "v_mfma_f32_16x16x32_bf16 v[200:203], v[248:251], v[236:239], v[200:203]\n\t" \
    "ds_read_b128 v[248:251], %[wl] offset:7168\n\t" \
    "v_mfma_f32_16x16x32_bf16 v[204:207], v[80:83], v[232:235], 0\n\t" \
    "v_mfma_f32_16x16x32_bf16 v[204:207], v[84:87], v[236:239], v[204:207]\n\t" \
    "v_mfma_f32_16x16x32_bf16 v[208:211], v[88:91], v[232:235], 0\n\t" \
    "ds_read_b128 v[232:235], v252 offset:17408\n\t" \
    "v_mfma_f32_16x16x32_bf16 v[208:211], v[92:95], v[236:239], v[208:211]\n\t" \
    "v_xor_b32 v252, 0x3000, %[xa]\n\t" \
    "ds_read_b128 v[236:239], v252 offset:16384\n\t" \
    "s_waitcnt lgkmcnt(6)\n\t" \
    "v_mfma_f32_16x16x32_bf16 v[208:211], v[112:115], v[240:243], v[208:211]\n\t" \
    "s_waitcnt lgkmcnt(5)\n\t" \
    "v_mfma_f32_16x16x32_bf16 v[208:211], v[116:119], v[224:227], v[208:211]\n\t" \
    "v_mfma_f32_16x16x32_bf16 v[204:207], v[104:107], v[240:243], v[204:207]\n\t" \
    "v_mfma_f32_16x16x32_bf16 v[204:207], v[108:111], v[224:227], v[204:207]\n\t" \
    "v_mfma_f32_16x16x32_bf16 v[200:203], v[96:99], v[240:243], v[200:203]\n\t" \
    "ds_read_b128 v[240:243], v252 offset:17408\n\t" \
    "v_mfma_f32_16x16x32_bf16 v[200:203], v[100:103], v[224:227], v[200:203]\n\t" \
    "s_nop 3\n\t" \
    "v_mov_b32 v224, %[tv]\n\t" \
    "s_waitcnt vmcnt(5)\n\t" \
    "buffer_store_dword v224, %[fv], %[rr], %[fp] offen sc1\n\t" \
    "ds_read_b128 v[224:227], v252 offset:0\n\t" \
    "s_waitcnt lgkmcnt(6)\n\t" \
    "v_mfma_f32_16x16x32_bf16 v[212:215], v[96:99], v[228:231], v[212:215]\n\t" \
    "s_waitcnt lgkmcnt(3)\n\t" \
    "v_mfma_f32_16x16x32_bf16 v[212:215], v[100:103], v[232:235], v[212:215]\n\t" \
    "v_mfma_f32_16x16x32_bf16 v[216:219], v[104:107], v[228:231], v[216:219]\n\t" \
    "v_mfma_f32_16x16x32_bf16 v[216:219], v[108:111], v[232:235], v[216:219]\n\t" \
    "v_mfma_f32_16x16x32_bf16 v[220:223], v[112:115], v[228:231], v[220:223]\n\t" \
    "ds_read_b128 v[228:231], v252 offset:1024\n\t" \
    "v_mfma_f32_16x16x32_bf16 v[220:223], v[116:119], v[232:235], v[220:223]\n\t" \
    "v_xor_b32 v252, 0x3800, %[xa]\n\t" \
    "ds_read_b128 v[232:235], v252 offset:0\n\t" \
    "s_waitcnt lgkmcnt(4)\n\t" \
    "v_mfma_f32_16x16x32_bf16 v[220:223], v[128:131], v[236:239], v[220:223]\n\t" \
    "s_waitcnt lgkmcnt(3)\n\t" \
    "v_mfma_f32_16x16x32_bf16 v[220:223], v[132:135], v[240:243], v[220:223]\n\t" \
    "v_mfma_f32_16x16x32_bf16 v[216:219], v[120:123], v[236:239], v[216:219]\n\t" \
    "v_mfma_f32_16x16x32_bf16 v[216:219], v[124:127], v[240:243], v[216:219]\n\t" \
    "v_mfma_f32_16x16x32_bf16 v[212:215], v[244:247], v[236:239], v[212:215]\n\t" \
    "ds_read_b128 v[236:239], v252 offset:1024\n\t" \
    "v_mfma_f32_16x16x32_bf16 v[212:215], v[248:251], v[240:243], v[212:215]\n\t" \
    "ds_read_b128 v[240:243], v252 offset:16384\n\t" \
    "s_waitcnt lgkmcnt(4)\n\t" \
    "v_mfma_f32_16x16x32_bf16 v[200:203], v[244:247], v[224:227], v[200:203]\n\t" \
    "ds_read_b128 v[244:247], %[wl] offset:8192\n\t" \
    "s_waitcnt lgkmcnt(4)\n\t" \
    "v_mfma_f32_16x16x32_bf16 v[200:203], v[248:251], v[228:231], v[200:203]\n\t" \
    "ds_read_b128 v[248:251], %[wl] offset:9216\n\t" \
    "v_mfma_f32_16x16x32_bf16 v[204:207], v[120:123], v[224:227], v[204:207]\n\t" \
    "v_mfma_f32_16x16x32_bf16 v[204:207], v[124:127], v[228:231], v[204:207]\n\t" \
    "v_mfma_f32_16x16x32_bf16 v[208:211], v[128:131], v[224:227], v[208:211]\n\t" \
    "ds_read_b128 v[224:227], v252 offset:17408\n\t" \
    "v_mfma_f32_16x16x32_bf16 v[208:211], v[132:135], v[228:231], v[208:211]\n\t" \
    "v_and_b32 v252, 0x3ff, %[xa]\n\t" \
    "ds_read_b128 v[228:231], v252 offset:45056\n\t" \
    "s_waitcnt lgkmcnt(6)\n\t" \
    "v_mfma_f32_16x16x32_bf16 v[208:211], v[152:155], v[232:235], v[208:211]\n\t" \
    "s_waitcnt lgkmcnt(5)\n\t" \
    "v_mfma_f32_16x16x32_bf16 v[208:211], v[156:159], v[236:239], v[208:211]\n\t" \
    "v_mfma_f32_16x16x32_bf16 v[204:207], v[144:147], v[232:235], v[204:207]\n\t" \
    "v_mfma_f32_16x16x32_bf16 v[204:207], v[148:151], v[236:239], v[204:207]\n\t" \
    "v_mfma_f32_16x16x32_bf16 v[200:203], v[136:139], v[232:235], v[200:203]\n\t" \
    "ds_read_b128 v[232:235], v252 offset:46080\n\t" \
    "v_mfma_f32_16x16x32_bf16 v[200:203], v[140:143], v[236:239], v[200:203]\n\t" \
    "ds_read_b128 v[236:239], v252 offset:40960\n\t" \
    "s_waitcnt lgkmcnt(6)\n\t" \
    "v_mfma_f32_16x16x32_bf16 v[212:215], v[136:139], v[240:243], v[212:215]\n\t" \
    "s_waitcnt lgkmcnt(3)\n\t" \
    "v_mfma_f32_16x16x32_bf16 v[212:215], v[140:143], v[224:227], v[212:215]\n\t" \
    "v_mfma_f32_16x16x32_bf16 v[216:219], v[144:147], v[240:243], v[216:219]\n\t" \
    "v_mfma_f32_16x16x32_bf16 v[216:219], v[148:151], v[224:227], v[216:219]\n\t" \
    "v_mfma_f32_16x16x32_bf16 v[220:223], v[152:155], v[240:243], v[220:223]\n\t" \
    "ds_read_b128 v[240:243], v252 offset:41984\n\t" \
    "v_mfma_f32_16x16x32_bf16 v[220:223], v[156:159], v[224:227], v[220:223]\n\t" \
    "s_waitcnt vmcnt(1) lgkmcnt(0)\n\t" \
    "s_barrier\n\t" ASM_ST \
    "ds_read_b128 v[224:227], %[xa] offset:0\n\t" \
    "v_mfma_f32_16x16x32_bf16 v[220:223], v[168:171], v[228:231], v[220:223]\n\t" \
    "v_mfma_f32_16x16x32_bf16 v[220:223], v[172:175], v[232:235], v[220:223]\n\t" \
    "v_mfma_f32_16x16x32_bf16 v[216:219], v[160:163], v[228:231], v[216:219]\n\t" \
    "v_mfma_f32_16x16x32_bf16 v[216:219], v[164:167], v[232:235], v[216:219]\n\t" \
    "v_mfma_f32_16x16x32_bf16 v[212:215], v[244:247], v[228:231], v[212:215]\n\t" \
    "ds_read_b128 v[228:231], %[xa] offset:1024\n\t" \
    "v_mfma_f32_16x16x32_bf16 v[212:215], v[248:251], v[232:235], v[212:215]\n\t" \
    "ds_read_b128 v[232:235], %[xa] offset:16384\n\t" \
    "v_mfma_f32_16x16x32_bf16 v[200:203], v[244:247], v[236:239], v[200:203]\n\t" \
    "ds_read_b128 v[244:247], %[wl] offset:0\n\t" \
    "v_mfma_f32_16x16x32_bf16 v[200:203], v[248:251], v[240:243], v[200:203]\n\t" \
    "ds_read_b128 v[248:251], %[wl] offset:1024\n\t" \
    "v_mfma_f32_16x16x32_bf16 v[204:207], v[160:163], v[236:239], v[204:207]\n\t" \
    "v_mfma_f32_16x16x32_bf16 v[204:207], v[164:167], v[240:243], v[204:207]\n\t" \
    "v_mfma_f32_16x16x32_bf16 v[208:211], v[168:171], v[236:239], v[208:211]\n\t" \
    "ds_read_b128 v[236:239], %[xa] offset:17408\n\t" \
    "v_mfma_f32_16x16x32_bf16 v[208:211], v[172:175], v[240:243], v[208:211]\n\t" \
    "v_xor_b32 v252, 0x800, %[xa]\n\t" \
    "ds_read_b128 v[240:243], v252 offset:16384\n\t" \
    "s_waitcnt lgkmcnt(6)\n\t" \
    "v_mfma_f32_16x16x32_bf16 v[208:211], v[8:11], v[224:227], v[208:211]\n\t" \
    "s_waitcnt lgkmcnt(5)\n\t" \
    "v_mfma_f32_16x16x32_bf16 v[208:211], v[12:15], v[228:231], v[208:211]\n\t" \
    "v_mfma_f32_16x16x32_bf16 v[204:207], v[0:3], v[224:227], v[204:207]\n\t" \
    "v_mfma_f32_16x16x32_bf16 v[204:207], v[4:7], v[228:231], v[204:207]\n\t" \
    "s_waitcnt lgkmcnt(3)\n\t" \
    "v_mfma_f32_16x16x32_bf16 v[200:203], v[244:247], v[224:227], v[200:203]\n\t" \
    "ds_read_b128 v[224:227], v252 offset:17408\n\t" \
    "s_waitcnt lgkmcnt(3)\n\t" \
    "v_mfma_f32_16x16x32_bf16 v[200:203], v[248:251], v[228:231], v[200:203]\n\t" \
    "ds_read_b128 v[228:231], v252 offset:0\n\t" \
    "v_mfma_f32_16x16x32_bf16 v[212:215], v[244:247], v[232:235], v[212:215]\n\t" \
    "ds_read_b128 v[244:247], %[wl] offset:2048\n\t" \
    "s_waitcnt lgkmcnt(4)\n\t" \
    "v_mfma_f32_16x16x32_bf16 v[212:215], v[248:251], v[236:239], v[212:215]\n\t" \
    "ds_read_b128 v[248:251], %[wl] offset:3072\n\t" \
    "v_mfma_f32_16x16x32_bf16 v[216:219], v[0:3], v[232:235], v[216:219]\n\t" \
    "v_mfma_f32_16x16x32_bf16 v[216:219], v[4:7], v[236:239], v[216:219]\n\t" \
    "v_mfma_f32_16x16x32_bf16 v[220:223], v[8:11], v[232:235], v[220:223]\n\t" \
    "ds_read_b128 v[232:235], v252 offset:1024\n\t" \
    "v_mfma_f32_16x16x32_bf16 v[220:223], v[12:15], v[236:239], v[220:223]\n\t" \
    "v_xor_b32 v252, 0x1000, %[xa]\n\t" \
    "ds_read_b128 v[236:239], v252 offset:0\n\t" \
    "s_waitcnt lgkmcnt(6)\n\t" \
    "v_mfma_f32_16x16x32_bf16 v[220:223], v[32:35], v[240:243], v[220:223]\n\t" \
    "s_waitcnt lgkmcnt(5)\n\t" \
    "v_mfma_f32_16x16x32_bf16 v[220:223], v[36:39], v[224:227], v[220:223]\n\t" \
    "v_mfma_f32_16x16x32_bf16 v[216:219], v[24:27], v[240:243], v[216:219]\n\t" \
    "v_mfma_f32_16x16x32_bf16 v[216:219], v[28:31], v[224:227], v[216:219]\n\t" \
    "v_mfma_f32_16x16x32_bf16 v[212:215], v[16:19], v[240:243], v[212:215]\n\t" \
    "ds_read_b128 v[240:243], v252 offset:1024\n\t" \
    "v_mfma_f32_16x16x32_bf16 v[212:215], v[20:23], v[224:227], v[212:215]\n\t" \
    "ds_read_b128 v[224:227], v252 offset:16384\n\t" \
    "s_waitcnt lgkmcnt(6)\n\t" \
    "v_mfma_f32_16x16x32_bf16 v[200:203], v[16:19], v[228:231], v[200:203]\n\t" \
    "s_waitcnt lgkmcnt(3)\n\t" \
    "v_mfma_f32_16x16x32_bf16 v[200:203], v[20:23], v[232:235], v[200:203]\n\t" \
    "v_mfma_f32_16x16x32_bf16 v[204:207], v[24:27], v[228:231], v[204:207]\n\t" \
    "v_mfma_f32_16x16x32_bf16 v[204:207], v[28:31], v[232:235], v[204:207]\n\t" \
    "v_mfma_f32_16x16x32_bf16 v[208:211], v[32:35], v[228:231], v[208:211]\n\t" \
    "ds_read_b128 v[228:231], v252 offset:17408\n\t" \
    "v_mfma_f32_16x16x32_bf16 v[208:211], v[36:39], v[232:235], v[208:211]\n\t" \
    "v_xor_b32 v252, 0x1800, %[xa]\n\t" \
    "ds_read_b128 v[232:235], v252 offset:16384\n\t" \
    "s_waitcnt lgkmcnt(4)\n\t" \
    "v_mfma_f32_16x16x32_bf16 v[208:211], v[48:51], v[236:239], v[208:211]\n\t" \
    "s_waitcnt lgkmcnt(3)\n\t" \
    "v_mfma_f32_16x16x32_bf16 v[208:211], v[52:55], v[240:243], v[208:211]\n\t" \
    "v_mfma_f32_16x16x32_bf16 v[204:207], v[40:43], v[236:239], v[204:207]\n\t" \
    "v_mfma_f32_16x16x32_bf16 v[204:207], v[44:47], v[240:243], v[204:207]\n\t" \
    "v_mfma_f32_16x16x32_bf16 v[200:203], v[244:247], v[236:239], v[200:203]\n\t" \
    "ds_read_b128 v[236:239], v252 offset:17408\n\t" \
    "v_mfma_f32_16x16x32_bf16 v[200:203], v[248:251], v[240:243], v[200:203]\n\t" \
    "ds_read_b128 v[240:243], v252 offset:0\n\t" \
    "s_waitcnt lgkmcnt(4)\n\t" \
    "v_mfma_f32_16x16x32_bf16 v[212:215], v[244:247], v[224:227], v[212:215]\n\t" \
    "s_waitcnt lgkmcnt(3)\n\t" \
    "v_mfma_f32_16x16x32_bf16 v[212:215], v[248:251], v[228:231], v[212:215]\n\t" \
    "v_mfma_f32_16x16x32_bf16 v[216:219], v[40:43], v[224:227], v[216:219]\n\t" \
    "v_mfma_f32_16x16x32_bf16 v[216:219], v[44:47], v[228:231], v[216:219]\n\t" \
    "v_mfma_f32_16x16x32_bf16 v[220:223], v[48:51], v[224:227], v[220:223]\n\t" \
    "ds_read_b128 v[224:227], v252 offset:1024\n\t" \
    "v_mfma_f32_16x16x32_bf16 v[220:223], v[52:55], v[228:231], v[220:223]\n\t" \
    "v_and_b32 v252, 0x3ff, %[xa]\n\t" \
    "ds_read_b128 v[228:231], v252 offset:43008\n\t" \
    "s_waitcnt lgkmcnt(4)\n\t" \
    "v_mfma_f32_16x16x32_bf16 v[220:223], v[72:75], v[232:235], v[220:223]\n\t" \
    "s_waitcnt lgkmcnt(3)\n\t" \
    "v_mfma_f32_16x16x32_bf16 v[220:223], v[76:79], v[236:239], v[220:223]\n\t" \
    "v_mfma_f32_16x16x32_bf16 v[216:219], v[64:67], v[232:235], v[216:219]\n\t" \
    "v_mfma_f32_16x16x32_bf16 v[216:219], v[68:71], v[236:239], v[216:219]\n\t" \
    "v_mfma_f32_16x16x32_bf16 v[212:215], v[56:59], v[232:235], v[212:215]\n\t" \
    "ds_read_b128 v[232:235], v252 offset:44032\n\t" \
    "v_mfma_f32_16x16x32_bf16 v[212:215], v[60:63], v[236:239], v[212:215]\n\t" \
    "ds_read_b128 v[236:239], v252 offset:47104\n\t" \
    "s_waitcnt lgkmcnt(4)\n\t" \
    "v_mfma_f32_16x16x32_bf16 v[200:203], v[56:59], v[240:243], v[200:203]\n\t" \
    "s_waitcnt lgkmcnt(3)\n\t" \
    "v_mfma_f32_16x16x32_bf16 v[200:203], v[60:63], v[224:227], v[200:203]\n\t" \
    "v_mfma_f32_16x16x32_bf16 v[204:207], v[64:67], v[240:243], v[204:207]\n\t" \
    "v_mfma_f32_16x16x32_bf16 v[204:207], v[68:71], v[224:227], v[204:207]\n\t" \
    "v_mfma_f32_16x16x32_bf16 v[208:211], v[72:75], v[240:243], v[208:211]\n\t" \
    "ds_read_b128 v[240:243], v252 offset:48128\n\t" \
    "v_mfma_f32_16x16x32_bf16 v[208:211], v[76:79], v[224:227], v[208:211]\n\t" \
    "s_waitcnt lgkmcnt(3)\n\t" \
    "v_mfma_f32_16x16x32_bf16 v[208:211], v[192:195], v[228:231], v[208:211]\n\t" \
    "v_add_u32 v252, %[qs], v252\n\t" \
    "s_waitcnt lgkmcnt(2)\n\t" \
    "v_mfma_f32_16x16x32_bf16 v[208:211], v[196:199], v[232:235], v[208:211]\n\t" \
    "v_mfma_f32_16x16x32_bf16 v[204:207], v[184:187], v[228:231], v[204:207]\n\t" \
    "v_mfma_f32_16x16x32_bf16 v[204:207], v[188:191], v[232:235], v[204:207]\n\t" \
    "v_mfma_f32_16x16x32_bf16 v[200:203], v[176:179], v[228:231], v[200:203]\n\t" \
    "v_mfma_f32_16x16x32_bf16 v[200:203], v[180:183], v[232:235], v[200:203]\n\t" \
    "s_waitcnt lgkmcnt(1)\n\t" \
    "v_mfma_f32_16x16x32_bf16 v[212:215], v[176:179], v[236:239], v[212:215]\n\t" \
    "v_cvt_pk_bf16_f32 v208, v208, v209\n\t" \
    "v_cvt_pk_bf16_f32 v209, v210, v211\n\t" \
    "s_waitcnt lgkmcnt(0)\n\t" \
    "v_mfma_f32_16x16x32_bf16 v[212:215], v[180:183], v[240:243], v[212:215]\n\t" \
    "ds_write_b64 v252, v[208:209] offset:2048\n\t" \
    "v_cvt_pk_bf16_f32 v204, v204, v205\n\t" \
    "v_mfma_f32_16x16x32_bf16 v[216:219], v[184:187], v[236:239], v[216:219]\n\t" \
    "v_cvt_pk_bf16_f32 v205, v206, v207\n\t" \
    "ds_write_b64 v252, v[204:205] offset:1024\n\t" \
    "v_mfma_f32_16x16x32_bf16 v[216:219], v[188:191], v[240:243], v[216:219]\n\t" \
    "v_cvt_pk_bf16_f32 v200, v200, v201\n\t" \
    "v_cvt_pk_bf16_f32 v201, v202, v203\n\t" \
    "v_mfma_f32_16x16x32_bf16 v[220:223], v[192:195], v[236:239], v[220:223]\n\t" \
    "ds_write_b64 v252, v[200:201] offset:0\n\t" \
    "v_mfma_f32_16x16x32_bf16 v[220:223], v[196:199], v[240:243], v[220:223]\n\t" \
    "v_cvt_pk_bf16_f32 v212, v212, v213\n\t" \
    "v_cvt_pk_bf16_f32 v213, v214, v215\n\t" \
    "ds_write_b64 v252, v[212:213] offset:6144\n\t" \
    "v_cvt_pk_bf16_f32 v216, v216, v217\n\t" \
    "v_cvt_pk_bf16_f32 v217, v218, v219\n\t" \
    "ds_write_b64 v252, v[216:217] offset:7168\n\t" \
    "s_nop 1\n\t" \
    "v_cvt_pk_bf16_f32 v220, v220, v221\n\t" \
    "v_cvt_pk_bf16_f32 v221, v222, v223\n\t" \
    "ds_write_b64 v252, v[220:221] offset:8192\n\t" \
    "s_waitcnt lgkmcnt(0)\n\t"
#define ASM_CRIT1_3 \
    "s_branch .Lc0%=\n\t" \
    ".Lcr%=:\n\t" \
    "s_cmp_eq_u32 %[n], 0\n\t" \
    "s_cbranch_scc1 .Lcb%=\n\t" \
    "s_sub_u32 %[n], %[n], 1\n\t" \
    "v_max_u32 v243, v208, v209\n\t" \
    "v_max3_u32 v243, v210, v211, v243\n\t" \
    "v_cmp_eq_u32 vcc, -1, v243\n\t" \
    "s_cbranch_vccz .Lcs0_%=\n\t" \
    "buffer_load_dwordx4 v[208:211], %[ab], %[rs], %[s0] offen sc1\n\t" \
    ".Lcs0_%=:\n\t" \
    "v_max_u32 v243, v236, v237\n\t" \
    "v_max3_u32 v243, v238, v239, v243\n\t" \
    "v_cmp_eq_u32 vcc, -1, v243\n\t" \
    "s_cbranch_vccz .Lcs1_%=\n\t" \
    "buffer_load_dwordx4 v[236:239], %[ab], %[rs], %[s1] offen sc1\n\t" \
    ".Lcs1_%=:\n\t" \
    "v_max_u32 v243, v220, v221\n\t" \
    "v_max3_u32 v243, v222, v223, v243\n\t" \
    "v_cmp_eq_u32 vcc, -1, v243\n\t" \
    "s_cbranch_vccz .Lca%=\n\t" \
    "buffer_load_dwordx4 v[220:223], %[ab], %[rs], %[s2] offen sc1\n\t" \
    "s_branch .Lca%=\n\t" \
    ".Lc0%=:\n\t" \
    "buffer_load_dwordx4 v[208:211], %[ab], %[rs], %[s0] offen sc1\n\t" \
    "buffer_load_dwordx4 v[236:239], %[ab], %[rs], %[s1] offen sc1\n\t" \
    "buffer_load_dwordx4 v[220:223], %[ab], %[rs], %[s2] offen sc1\n\t" \
    "v_bfe_i32 v241, %[ab], 7, 1\n\t" \
    "v_and_b32 v241, 0xaaaa, v241\n\t" \
    "v_or_b32 v241, 0x4444, v241\n\t" \
    "v_xor_b32 v212, 0x400, %[ab]\n\t" \
    "v_xor_b32 v216, 0x800, %[ab]\n\t" \
    "v_xor_b32 v242, 0xc00, %[ab]\n\t" \
    "v_xor_b32 v240, 0x1400, %[ab]\n\t" \
    ".Lca%=:\n\t" \
    "s_waitcnt vmcnt(0)\n\t" \
    "ds_write_b128 %[ab], v[208:211] offset:0\n\t" \
    "ds_write_b128 %[ab], v[236:239] offset:4096\n\t" \
    "ds_write_b128 %[ab], v[220:223] offset:8192\n\t" \
    "v_max_u32 v243, v208, v209\n\t" \
    "v_max3_u32 v243, v210, v211, v243\n\t" \
    "v_max3_u32 v243, v236, v237, v243\n\t" \
    "v_max3_u32 v243, v238, v239, v243\n\t" \
    "v_max3_u32 v243, v220, v221, v243\n\t" \
    "v_max3_u32 v243, v222, v223, v243\n\t" \
    "v_cmp_eq_u32 vcc, -1, v243\n\t" \
    "s_cbranch_vccnz .Lcr%=\n\t" \
    ".Lcb%=:\n\t" \
    PRIUP \
    ASM_ST2 \
    "v_smfmac_f32_16x16x64_bf16 v[224:227], v[208:211], v[200:207], v241\n\t" \
    "ds_read_b128 v[200:203], %[wl] offset:4096\n\t" \
    "ds_read_b128 v[204:207], %[wl] offset:5120\n\t" \
    "v_smfmac_f32_16x16x64_bf16 v[244:247], v[208:211], v[0:7], v241\n\t" \
    "v_smfmac_f32_16x16x64_bf16 v[248:251], v[208:211], v[8:15], v241\n\t" \
    "s_waitcnt lgkmcnt(2)\n\t" \
    "s_barrier\n\t" ASM_ST \
    "ds_read_b128 v[212:215], v212 offset:0\n\t" \
    "ds_read_b128 v[216:219], v216 offset:0\n\t" \
    "ds_read_b128 v[220:223], v242 offset:0\n\t" \
    "ds_read_b128 v[208:211], v240 offset:0\n\t" \
    "v_smfmac_f32_16x16x64_bf16 v[244:247], v[236:239], v[80:87], v241\n\t" \
    "v_xor_b32 v240, 0x1800, %[ab]\n\t" \
    "v_smfmac_f32_16x16x64_bf16 v[248:251], v[236:239], v[88:95], v241\n\t" \
    "s_waitcnt lgkmcnt(4)\n\t" \
    "v_smfmac_f32_16x16x64_bf16 v[224:227], v[236:239], v[200:207], v241\n\t" \
    "ds_read_b128 v[200:203], %[wl] offset:6144\n\t" \
    "ds_read_b128 v[204:207], %[wl] offset:7168\n\t" \
    "ds_read_b128 v[236:239], v240 offset:0\n\t" \
    "s_waitcnt lgkmcnt(6)\n\t" \
    "v_smfmac_f32_16x16x64_bf16 v[224:227], v[212:215], v[16:23], v241\n\t" \
    "v_smfmac_f32_16x16x64_bf16 v[244:247], v[212:215], v[24:31], v241\n\t" \
    "v_xor_b32 v240, 0x1c00, %[ab]\n\t" \
    "v_smfmac_f32_16x16x64_bf16 v[248:251], v[212:215], v[32:39], v241\n\t" \
    "ds_read_b128 v[212:215], v240 offset:0\n\t" \
    "s_waitcnt lgkmcnt(6)\n\t" \
    "v_smfmac_f32_16x16x64_bf16 v[224:227], v[216:219], v[228:235], v241\n\t" \
    "ds_read_b128 v[228:231], %[wl] offset:8192\n\t" \
    "ds_read_b128 v[232:235], %[wl] offset:9216\n\t" \
    "v_smfmac_f32_16x16x64_bf16 v[244:247], v[216:219], v[40:47], v241\n\t" \
    "v_and_b32 v240, 0x3ff, %[ab]\n\t" \
    "v_smfmac_f32_16x16x64_bf16 v[248:251], v[216:219], v[48:55], v241\n\t" \
    "ds_read_b128 v[216:219], v240 offset:8192\n\t" \
    "s_waitcnt lgkmcnt(8)\n\t" \
    "v_smfmac_f32_16x16x64_bf16 v[224:227], v[220:223], v[56:63], v241\n\t" \
    "v_smfmac_f32_16x16x64_bf16 v[244:247], v[220:223], v[64:71], v241\n\t" \
    "v_smfmac_f32_16x16x64_bf16 v[248:251], v[220:223], v[72:79], v241\n\t" \
    "ds_read_b128 v[220:223], v240 offset:9216\n\t" \
    "s_waitcnt lgkmcnt(8)\n\t" \
    "v_smfmac_f32_16x16x64_bf16 v[244:247], v[208:211], v[104:111], v241\n\t" \
    "s_waitcnt lgkmcnt(5)\n\t" \
    "v_smfmac_f32_16x16x64_bf16 v[244:247], v[236:239], v[120:127], v241\n\t" \
    "s_waitcnt lgkmcnt(4)\n\t" \
    "v_smfmac_f32_16x16x64_bf16 v[244:247], v[212:215], v[144:151], v241\n\t" \
    "s_waitcnt lgkmcnt(1)\n\t" \
    "v_smfmac_f32_16x16x64_bf16 v[244:247], v[216:219], v[160:167], v241\n\t" \
    "s_waitcnt lgkmcnt(0)\n\t" \
    "v_smfmac_f32_16x16x64_bf16 v[244:247], v[220:223], v[184:191], v241\n\t" \
    "v_smfmac_f32_16x16x64_bf16 v[248:251], v[208:211], v[112:119], v241\n\t" \
    "v_smfmac_f32_16x16x64_bf16 v[248:251], v[236:239], v[128:135], v241\n\t" \
    "v_smfmac_f32_16x16x64_bf16 v[248:251], v[212:215], v[152:159], v241\n\t" \
    "v_permlane32_swap_b32 v244, v246\n\t" \
    "v_permlane32_swap_b32 v245, v247\n\t" \
    "v_smfmac_f32_16x16x64_bf16 v[248:251], v[216:219], v[168:175], v241\n\t" \
    "v_add_f32 v244, v244, v246\n\t" \
    "v_add_f32 v245, v245, v247\n\t" \
    "v_smfmac_f32_16x16x64_bf16 v[248:251], v[220:223], v[192:199], v241\n\t" \
    "v_cvt_pk_bf16_f32 v244, v244, v245\n\t" \
    "ds_write_b16 %[dst], v244 offset:512\n\t" \
    "ds_write_b16_d16_hi %[dst], v244 offset:528\n\t" \
    "v_smfmac_f32_16x16x64_bf16 v[224:227], v[208:211], v[96:103], v241\n\t" \
    "v_smfmac_f32_16x16x64_bf16 v[224:227], v[236:239], v[200:207], v241\n\t" \
    "v_smfmac_f32_16x16x64_bf16 v[224:227], v[212:215], v[136:143], v241\n\t" \
    "v_permlane32_swap_b32 v248, v250\n\t" \
    "v_permlane32_swap_b32 v249, v251\n\t" \
    "v_smfmac_f32_16x16x64_bf16 v[224:227], v[216:219], v[228:235], v241\n\t" \
    "v_add_f32 v248, v248, v250\n\t" \
    "v_add_f32 v249, v249, v251\n\t" \
    "v_smfmac_f32_16x16x64_bf16 v[224:227], v[220:223], v[176:183], v241\n\t" \
    "v_cvt_pk_bf16_f32 v248, v248, v249\n\t" \
    "ds_write_b16 %[dst], v248 offset:1024\n\t" \
    "ds_write_b16_d16_hi %[dst], v248 offset:1040\n\t" \
    "s_nop 4\n\t" \
    "v_permlane32_swap_b32 v224, v226\n\t" \
    "v_permlane32_swap_b32 v225, v227\n\t" \
    "v_add_f32 v224, v224, v226\n\t" \
    "v_add_f32 v225, v225, v227\n\t" \
    "v_cvt_pk_bf16_f32 v224, v224, v225\n\t" \
    "ds_write_b16 %[dst], v224 offset:0\n\t" \
    "ds_write_b16_d16_hi %[dst], v224 offset:16\n\t"
#define ASM_CRIT1_2 \
    "s_branch .Lc0%=\n\t" \
    ".Lcr%=:\n\t" \
    "s_cmp_eq_u32 %[n], 0\n\t" \
    "s_cbranch_scc1 .Lcb%=\n\t" \
    "s_sub_u32 %[n], %[n], 1\n\t" \
    "v_max_u32 v243, v208, v209\n\t" \
    "v_max3_u32 v243, v210, v211, v243\n\t" \
    "v_cmp_eq_u32 vcc, -1, v243\n\t" \
    "s_cbranch_vccz .Lcs0_%=\n\t" \
    "buffer_load_dwordx4 v[208:211], %[ab], %[rs], %[s0] offen sc1\n\t" \
    ".Lcs0_%=:\n\t" \
    "v_max_u32 v243, v236, v237\n\t" \
    "v_max3_u32 v243, v238, v239, v243\n\t" \
    "v_cmp_eq_u32 vcc, -1, v243\n\t" \
    "s_cbranch_vccz .Lca%=\n\t" \
    "buffer_load_dwordx4 v[236:239], %[ab], %[rs], %[s1] offen sc1\n\t" \
    "s_branch .Lca%=\n\t" \
    ".Lc0%=:\n\t" \
    "buffer_load_dwordx4 v[208:211], %[ab], %[rs], %[s0] offen sc1\n\t" \
    "buffer_load_dwordx4 v[236:239], %[ab], %[rs], %[s1] offen sc1\n\t" \
    "v_bfe_i32 v241, %[ab], 7, 1\n\t" \
    "v_and_b32 v241, 0xaaaa, v241\n\t" \
    "v_or_b32 v241, 0x4444, v241\n\t" \
    "v_xor_b32 v212, 0x400, %[ab]\n\t" \
    "v_xor_b32 v216, 0x800, %[ab]\n\t" \
    "v_xor_b32 v242, 0xc00, %[ab]\n\t" \
    "v_xor_b32 v240, 0x1400, %[ab]\n\t" \
    ".Lca%=:\n\t" \
    "s_waitcnt vmcnt(0)\n\t" \
    "ds_write_b128 %[ab], v[208:211] offset:0\n\t" \
    "ds_write_b128 %[ab], v[236:239] offset:4096\n\t" \
    "v_max_u32 v243, v208, v209\n\t" \
    "v_max3_u32 v243, v210, v211, v243\n\t" \
    "v_max3_u32 v243, v236, v237, v243\n\t" \
    "v_max3_u32 v243, v238, v239, v243\n\t" \
    "v_cmp_eq_u32 vcc, -1, v243\n\t" \
    "s_cbranch_vccnz .Lcr%=\n\t" \
    ".Lcb%=:\n\t" \
    PRIUP \
    ASM_ST2 \
    "v_smfmac_f32_16x16x64_bf16 v[224:227], v[208:211], v[200:207], v241\n\t" \
    "ds_read_b128 v[200:203], %[wl] offset:4096\n\t" \
    "ds_read_b128 v[204:207], %[wl] offset:5120\n\t" \
    "v_smfmac_f32_16x16x64_bf16 v[244:247], v[208:211], v[0:7], v241\n\t" \
    "v_smfmac_f32_16x16x64_bf16 v[248:251], v[208:211], v[8:15], v241\n\t" \
    "s_waitcnt lgkmcnt(2)\n\t" \
    "s_barrier\n\t" ASM_ST \
    "ds_read_b128 v[212:215], v212 offset:0\n\t" \
    "ds_read_b128 v[216:219], v216 offset:0\n\t" \
    "ds_read_b128 v[220:223], v242 offset:0\n\t" \
    "ds_read_b128 v[208:211], v240 offset:0\n\t" \
    "v_smfmac_f32_16x16x64_bf16 v[244:247], v[236:239], v[80:87], v241\n\t" \
    "v_xor_b32 v240, 0x1800, %[ab]\n\t" \
    "v_smfmac_f32_16x16x64_bf16 v[248:251], v[236:239], v[88:95], v241\n\t" \
    "s_waitcnt lgkmcnt(4)\n\t" \
    "v_smfmac_f32_16x16x64_bf16 v[224:227], v[236:239], v[200:207], v241\n\t" \
    "ds_read_b128 v[200:203], %[wl] offset:6144\n\t" \
    "ds_read_b128 v[204:207], %[wl] offset:7168\n\t" \
    "ds_read_b128 v[236:239], v240 offset:0\n\t" \
    "s_waitcnt lgkmcnt(6)\n\t" \
    "v_smfmac_f32_16x16x64_bf16 v[224:227], v[212:215], v[16:23], v241\n\t" \
    "v_smfmac_f32_16x16x64_bf16 v[244:247], v[212:215], v[24:31], v241\n\t" \
    "v_xor_b32 v240, 0x1c00, %[ab]\n\t" \
    "v_smfmac_f32_16x16x64_bf16 v[248:251], v[212:215], v[32:39], v241\n\t" \
    "ds_read_b128 v[212:215], v240 offset:0\n\t" \
    "s_waitcnt lgkmcnt(6)\n\t" \
    "v_smfmac_f32_16x16x64_bf16 v[224:227], v[216:219], v[228:235], v241\n\t" \
    "ds_read_b128 v[228:231], %[wl] offset:8192\n\t" \
    "ds_read_b128 v[232:235], %[wl] offset:9216\n\t" \
    "v_smfmac_f32_16x16x64_bf16 v[244:247], v[216:219], v[40:47], v241\n\t" \
    "v_and_b32 v240, 0x3ff, %[ab]\n\t" \
    "v_smfmac_f32_16x16x64_bf16 v[248:251], v[216:219], v[48:55], v241\n\t" \
    "ds_read_b128 v[216:219], v240 offset:8192\n\t" \
    "s_waitcnt lgkmcnt(8)\n\t" \
    "v_smfmac_f32_16x16x64_bf16 v[224:227], v[220:223], v[56:63], v241\n\t" \
    "v_smfmac_f32_16x16x64_bf16 v[244:247], v[220:223], v[64:71], v241\n\t" \
    "v_smfmac_f32_16x16x64_bf16 v[248:251], v[220:223], v[72:79], v241\n\t" \
    "ds_read_b128 v[220:223], v240 offset:9216\n\t" \
    "s_waitcnt lgkmcnt(8)\n\t" \
    "v_smfmac_f32_16x16x64_bf16 v[244:247], v[208:211], v[104:111], v241\n\t" \
    "s_waitcnt lgkmcnt(5)\n\t" \
    "v_smfmac_f32_16x16x64_bf16 v[244:247], v[236:239], v[120:127], v241\n\t" \
    "s_waitcnt lgkmcnt(4)\n\t" \
    "v_smfmac_f32_16x16x64_bf16 v[244:247], v[212:215], v[144:151], v241\n\t" \
    "s_waitcnt lgkmcnt(1)\n\t" \
    "v_smfmac_f32_16x16x64_bf16 v[244:247], v[216:219], v[160:167], v241\n\t" \
    "s_waitcnt lgkmcnt(0)\n\t" \
    "v_smfmac_f32_16x16x64_bf16 v[244:247], v[220:223], v[184:191], v241\n\t" \
    "v_smfmac_f32_16x16x64_bf16 v[248:251], v[208:211], v[112:119], v241\n\t" \
    "v_smfmac_f32_16x16x64_bf16 v[248:251], v[236:239], v[128:135], v241\n\t" \
    "v_smfmac_f32_16x16x64_bf16 v[248:251], v[212:215], v[152:159], v241\n\t" \
    "v_permlane32_swap_b32 v244, v246\n\t" \
    "v_permlane32_swap_b32 v245, v247\n\t" \
    "v_smfmac_f32_16x16x64_bf16 v[248:251], v[216:219], v[168:175], v241\n\t" \
    "v_add_f32 v244, v244, v246\n\t" \
    "v_add_f32 v245, v245, v247\n\t" \
    "v_smfmac_f32_16x16x64_bf16 v[248:251], v[220:223], v[192:199], v241\n\t" \
    "v_cvt_pk_bf16_f32 v244, v244, v245\n\t" \
    "ds_write_b16 %[dst], v244 offset:512\n\t" \
    "ds_write_b16_d16_hi %[dst], v244 offset:528\n\t" \
    "v_smfmac_f32_16x16x64_bf16 v[224:227], v[208:211], v[96:103], v241\n\t" \
    "v_smfmac_f32_16x16x64_bf16 v[224:227], v[236:239], v[200:207], v241\n\t" \
    "v_smfmac_f32_16x16x64_bf16 v[224:227], v[212:215], v[136:143], v241\n\t" \
    "v_permlane32_swap_b32 v248, v250\n\t" \
    "v_permlane32_swap_b32 v249, v251\n\t" \
    "v_smfmac_f32_16x16x64_bf16 v[224:227], v[216:219], v[228:235], v241\n\t" \
    "v_add_f32 v248, v248, v250\n\t" \
    "v_add_f32 v249, v249, v251\n\t" \
    "v_smfmac_f32_16x16x64_bf16 v[224:227], v[220:223], v[176:183], v241\n\t" \
    "v_cvt_pk_bf16_f32 v248, v248, v249\n\t" \
    "ds_write_b16 %[dst], v248 offset:1024\n\t" \
    "ds_write_b16_d16_hi %[dst], v248 offset:1040\n\t" \
    "s_nop 4\n\t" \
    "v_permlane32_swap_b32 v224, v226\n\t" \
    "v_permlane32_swap_b32 v225, v227\n\t" \
    "v_add_f32 v224, v224, v226\n\t" \
    "v_add_f32 v225, v225, v227\n\t" \
    "v_cvt_pk_bf16_f32 v224, v224, v225\n\t" \
    "ds_write_b16 %[dst], v224 offset:0\n\t" \
    "ds_write_b16_d16_hi %[dst], v224 offset:16\n\t"
#define ASM_CRIT1_N \
    "v_bfe_i32 v241, %[ab], 7, 1\n\t" \
    "v_and_b32 v241, 0xaaaa, v241\n\t" \
    "v_or_b32 v241, 0x4444, v241\n\t" \
    "v_xor_b32 v212, 0x400, %[ab]\n\t" \
    "v_xor_b32 v216, 0x800, %[ab]\n\t" \
    "v_xor_b32 v242, 0xc00, %[ab]\n\t" \
    "v_xor_b32 v240, 0x1400, %[ab]\n\t" \
    "v_xor_b32 v236, 0x1000, %[ab]\n\t" \
    "s_waitcnt lgkmcnt(0)\n\t" \
    "s_barrier\n\t" ASM_ST \
    "ds_read_b128 v[208:211], %[ab] offset:0\n\t" \
    "ds_read_b128 v[212:215], v212 offset:0\n\t" \
    "ds_read_b128 v[216:219], v216 offset:0\n\t" \
    "ds_read_b128 v[220:223], v242 offset:0\n\t" \
    "ds_read_b128 v[236:239], v236 offset:0\n\t" \
    "s_waitcnt lgkmcnt(4)\n\t" \
    "v_smfmac_f32_16x16x64_bf16 v[224:227], v[208:211], v[200:207], v241\n\t" \
    "ds_read_b128 v[200:203], %[wl] offset:4096\n\t" \
    "ds_read_b128 v[204:207], %[wl] offset:5120\n\t" \
    "v_smfmac_f32_16x16x64_bf16 v[244:247], v[208:211], v[0:7], v241\n\t" \
    "v_smfmac_f32_16x16x64_bf16 v[248:251], v[208:211], v[8:15], v241\n\t" \
    "ds_read_b128 v[208:211], v240 offset:0\n\t" \
    "s_waitcnt lgkmcnt(6)\n\t" \
    "v_smfmac_f32_16x16x64_bf16 v[224:227], v[212:215], v[16:23], v241\n\t" \
    "v_smfmac_f32_16x16x64_bf16 v[244:247], v[212:215], v[24:31], v241\n\t" \
    "v_xor_b32 v240, 0x1800, %[ab]\n\t" \
    "v_smfmac_f32_16x16x64_bf16 v[248:251], v[212:215], v[32:39], v241\n\t" \
    "ds_read_b128 v[212:215], v240 offset:0\n\t" \
    "s_waitcnt lgkmcnt(6)\n\t" \
    "v_smfmac_f32_16x16x64_bf16 v[224:227], v[216:219], v[228:235], v241\n\t" \
    "ds_read_b128 v[228:231], %[wl] offset:6144\n\t" \
    "ds_read_b128 v[232:235], %[wl] offset:7168\n\t" \
    "v_smfmac_f32_16x16x64_bf16 v[244:247], v[216:219], v[40:47], v241\n\t" \
    "v_xor_b32 v240, 0x1c00, %[ab]\n\t" \
    "v_smfmac_f32_16x16x64_bf16 v[248:251], v[216:219], v[48:55], v241\n\t" \
    "ds_read_b128 v[216:219], v240 offset:0\n\t" \
    "s_waitcnt lgkmcnt(8)\n\t" \
    "v_smfmac_f32_16x16x64_bf16 v[224:227], v[220:223], v[56:63], v241\n\t" \
    "v_smfmac_f32_16x16x64_bf16 v[244:247], v[220:223], v[64:71], v241\n\t" \
    "v_and_b32 v240, 0x3ff, %[ab]\n\t" \
    "v_smfmac_f32_16x16x64_bf16 v[248:251], v[220:223], v[72:79], v241\n\t" \
    "ds_read_b128 v[220:223], v240 offset:8192\n\t" \
    "s_waitcnt lgkmcnt(6)\n\t" \
    "v_smfmac_f32_16x16x64_bf16 v[224:227], v[236:239], v[200:207], v241\n\t" \
    "ds_read_b128 v[200:203], %[wl] offset:8192\n\t" \
    "ds_read_b128 v[204:207], %[wl] offset:9216\n\t" \
    "v_smfmac_f32_16x16x64_bf16 v[244:247], v[236:239], v[80:87], v241\n\t" \
    "v_smfmac_f32_16x16x64_bf16 v[248:251], v[236:239], v[88:95], v241\n\t" \
    "ds_read_b128 v[236:239], v240 offset:9216\n\t" \
    "s_waitcnt lgkmcnt(8)\n\t" \
    "v_smfmac_f32_16x16x64_bf16 v[244:247], v[208:211], v[104:111], v241\n\t" \
    "s_waitcnt lgkmcnt(7)\n\t" \
    "v_smfmac_f32_16x16x64_bf16 v[244:247], v[212:215], v[120:127], v241\n\t" \
    "s_waitcnt lgkmcnt(4)\n\t" \
    "v_smfmac_f32_16x16x64_bf16 v[244:247], v[216:219], v[144:151], v241\n\t" \
    "s_waitcnt lgkmcnt(3)\n\t" \
    "v_smfmac_f32_16x16x64_bf16 v[244:247], v[220:223], v[160:167], v241\n\t" \
    "s_waitcnt lgkmcnt(0)\n\t" \
    "v_smfmac_f32_16x16x64_bf16 v[244:247], v[236:239], v[184:191], v241\n\t" \
    "v_smfmac_f32_16x16x64_bf16 v[248:251], v[208:211], v[112:119], v241\n\t" \
    "v_smfmac_f32_16x16x64_bf16 v[248:251], v[212:215], v[128:135], v241\n\t" \
    "v_smfmac_f32_16x16x64_bf16 v[248:251], v[216:219], v[152:159], v241\n\t" \
    "v_permlane32_swap_b32 v244, v246\n\t" \
    "v_permlane32_swap_b32 v245, v247\n\t" \
    "v_smfmac_f32_16x16x64_bf16 v[248:251], v[220:223], v[168:175], v241\n\t" \
    "v_add_f32 v244, v244, v246\n\t" \
    "v_add_f32 v245, v245, v247\n\t" \
    "v_smfmac_f32_16x16x64_bf16 v[248:251], v[236:239], v[192:199], v241\n\t" \
    "v_cvt_pk_bf16_f32 v244, v244, v245\n\t" \
    "ds_write_b16 %[dst], v244 offset:512\n\t" \
    "ds_write_b16_d16_hi %[dst], v244 offset:528\n\t" \
    "v_smfmac_f32_16x16x64_bf16 v[224:227], v[208:211], v[96:103], v241\n\t" \
    "v_smfmac_f32_16x16x64_bf16 v[224:227], v[212:215], v[228:235], v241\n\t" \
    "v_smfmac_f32_16x16x64_bf16 v[224:227], v[216:219], v[136:143], v241\n\t" \
    "v_permlane32_swap_b32 v248, v250\n\t" \
    "v_permlane32_swap_b32 v249, v251\n\t" \
    "v_smfmac_f32_16x16x64_bf16 v[224:227], v[220:223], v[200:207], v241\n\t" \
    "v_add_f32 v248, v248, v250\n\t" \
    "v_add_f32 v249, v249, v251\n\t" \
    "v_smfmac_f32_16x16x64_bf16 v[224:227], v[236:239], v[176:183], v241\n\t" \
    "v_cvt_pk_bf16_f32 v248, v248, v249\n\t" \
    "ds_write_b16 %[dst], v248 offset:1024\n\t" \
    "ds_write_b16_d16_hi %[dst], v248 offset:1040\n\t" \
    "s_nop 4\n\t" \
    "v_permlane32_swap_b32 v224, v226\n\t" \
    "v_permlane32_swap_b32 v225, v227\n\t" \
    "v_add_f32 v224, v224, v226\n\t" \
    "v_add_f32 v225, v225, v227\n\t" \
    "v_cvt_pk_bf16_f32 v224, v224, v225\n\t" \
    "ds_write_b16 %[dst], v224 offset:0\n\t" \
    "ds_write_b16_d16_hi %[dst], v224 offset:16\n\t"
#define ASM_CRIT1_V3 \
    "s_branch .Lc0%=\n\t" \
    ".Lcr%=:\n\t" \
    "s_cmp_eq_u32 %[n], 0\n\t" \
    "s_cbranch_scc1 .Lcb%=\n\t" \
    "s_sub_u32 %[n], %[n], 1\n\t" \
    "v_max_u32 v243, v208, v209\n\t" \
    "v_max3_u32 v243, v210, v211, v243\n\t" \
    "v_cmp_eq_u32 vcc, -1, v243\n\t" \
    "s_cbranch_vccz .Lcs0_%=\n\t" \
    "buffer_load_dwordx4 v[208:211], %[ab], %[rs], %[s0] offen sc1\n\t" \
    ".Lcs0_%=:\n\t" \
    "v_max_u32 v243, v236, v237\n\t" \
    "v_max3_u32 v243, v238, v239, v243\n\t" \
    "v_cmp_eq_u32 vcc, -1, v243\n\t" \
    "s_cbranch_vccz .Lcs1_%=\n\t" \
    "buffer_load_dwordx4 v[236:239], %[ab], %[rs], %[s1] offen sc1\n\t" \
    ".Lcs1_%=:\n\t" \
    "v_max_u32 v243, v220, v221\n\t" \
    "v_max3_u32 v243, v222, v223, v243\n\t" \
    "v_cmp_eq_u32 vcc, -1, v243\n\t" \
    "s_cbranch_vccz .Lca%=\n\t" \
    "buffer_load_dwordx4 v[220:223], %[ab], %[rs], %[s2] offen sc1\n\t" \
    "s_branch .Lca%=\n\t" \
    ".Lc0%=:\n\t" \
    "buffer_load_dwordx4 v[208:211], %[ab], %[rs], %[s0] offen sc1\n\t" \
    "buffer_load_dwordx4 v[236:239], %[ab], %[rs], %[s1] offen sc1\n\t" \
    "buffer_load_dwordx4 v[220:223], %[ab], %[rs], %[s2] offen sc1\n\t" \
    "v_bfe_i32 v241, %[ab], 7, 1\n\t" \
    "v_and_b32 v241, 0xaaaa, v241\n\t" \
    "v_or_b32 v241, 0x4444, v241\n\t" \
    "v_xor_b32 v212, 0x400, %[ab]\n\t" \
    "v_xor_b32 v216, 0x800, %[ab]\n\t" \
    "v_xor_b32 v242, 0xc00, %[ab]\n\t" \
    "v_xor_b32 v240, 0x1400, %[ab]\n\t" \
    ".Lca%=:\n\t" \
    "s_waitcnt vmcnt(0)\n\t" \
    "ds_write_b128 %[ab], v[208:211] offset:0\n\t" \
    "ds_write_b128 %[ab], v[236:239] offset:4096\n\t" \
    "ds_write_b128 %[ab], v[220:223] offset:8192\n\t" \
    "v_max_u32 v243, v208, v209\n\t" \
    "v_max3_u32 v243, v210, v211, v243\n\t" \
    "v_max3_u32 v243, v236, v237, v243\n\t" \
    "v_max3_u32 v243, v238, v239, v243\n\t" \
    "v_max3_u32 v243, v220, v221, v243\n\t" \
    "v_max3_u32 v243, v222, v223, v243\n\t" \
    "v_cmp_eq_u32 vcc, -1, v243\n\t" \
    "s_cbranch_vccnz .Lcr%=\n\t" \
    ".Lcb%=:\n\t" \
    PRIUP \
    ASM_ST2 \
    "v_smfmac_f32_16x16x64_bf16 v[224:227], v[208:211], v[200:207], v241\n\t" \
    "ds_read_b128 v[200:203], %[wl] offset:4096\n\t" \
    "ds_read_b128 v[204:207], %[wl] offset:5120\n\t" \
    "v_smfmac_f32_16x16x64_bf16 v[244:247], v[208:211], v[0:7], v241\n\t" \
    "v_smfmac_f32_16x16x64_bf16 v[248:251], v[208:211], v[8:15], v241\n\t" \
    "s_waitcnt lgkmcnt(2)\n\t" \
    "buffer_load_dword %[fv], %[fv], %[rs], %[fs] offen sc1\n\t" \
    "s_barrier\n\t" ASM_ST \
    "ds_read_b128 v[212:215], v212 offset:0\n\t" \
    "ds_read_b128 v[216:219], v216 offset:0\n\t" \
    "ds_read_b128 v[220:223], v242 offset:0\n\t" \
    "ds_read_b128 v[208:211], v240 offset:0\n\t" \
    "v_smfmac_f32_16x16x64_bf16 v[244:247], v[236:239], v[80:87], v241\n\t" \
    "v_xor_b32 v240, 0x1800, %[ab]\n\t" \
    "v_smfmac_f32_16x16x64_bf16 v[248:251], v[236:239], v[88:95], v241\n\t" \
    "s_waitcnt lgkmcnt(4)\n\t" \
    "v_smfmac_f32_16x16x64_bf16 v[224:227], v[236:239], v[200:207], v241\n\t" \
    "ds_read_b128 v[200:203], %[wl] offset:6144\n\t" \
    "ds_read_b128 v[204:207], %[wl] offset:7168\n\t" \
    "ds_read_b128 v[236:239], v240 offset:0\n\t" \
    "s_waitcnt lgkmcnt(6)\n\t" \
    "v_smfmac_f32_16x16x64_bf16 v[224:227], v[212:215], v[16:23], v241\n\t" \
    "v_smfmac_f32_16x16x64_bf16 v[244:247], v[212:215], v[24:31], v241\n\t" \
    "v_xor_b32 v240, 0x1c00, %[ab]\n\t" \
    "v_smfmac_f32_16x16x64_bf16 v[248:251], v[212:215], v[32:39], v241\n\t" \
    "ds_read_b128 v[212:215], v240 offset:0\n\t" \
    "s_waitcnt lgkmcnt(6)\n\t" \
    "v_smfmac_f32_16x16x64_bf16 v[224:227], v[216:219], v[228:235], v241\n\t" \
    "ds_read_b128 v[228:231], %[wl] offset:8192\n\t" \
    "ds_read_b128 v[232:235], %[wl] offset:9216\n\t" \
    "v_smfmac_f32_16x16x64_bf16 v[244:247], v[216:219], v[40:47], v241\n\t" \
    "v_and_b32 v240, 0x3ff, %[ab]\n\t" \
    "v_smfmac_f32_16x16x64_bf16 v[248:251], v[216:219], v[48:55], v241\n\t" \
    "ds_read_b128 v[216:219], v240 offset:8192\n\t" \
    "s_waitcnt lgkmcnt(8)\n\t" \
    "v_smfmac_f32_16x16x64_bf16 v[224:227], v[220:223], v[56:63], v241\n\t" \
    "v_smfmac_f32_16x16x64_bf16 v[244:247], v[220:223], v[64:71], v241\n\t" \
    "v_smfmac_f32_16x16x64_bf16 v[248:251], v[220:223], v[72:79], v241\n\t" \
    "ds_read_b128 v[220:223], v240 offset:9216\n\t" \
    "s_waitcnt lgkmcnt(8)\n\t" \
    "v_smfmac_f32_16x16x64_bf16 v[244:247], v[208:211], v[104:111], v241\n\t" \
    "s_waitcnt lgkmcnt(5)\n\t" \
    "v_smfmac_f32_16x16x64_bf16 v[244:247], v[236:239], v[120:127], v241\n\t" \
    "s_waitcnt lgkmcnt(4)\n\t" \
    "v_smfmac_f32_16x16x64_bf16 v[244:247], v[212:215], v[144:151], v241\n\t" \
    "s_waitcnt lgkmcnt(1)\n\t" \
    "v_smfmac_f32_16x16x64_bf16 v[244:247], v[216:219], v[160:167], v241\n\t" \
    "s_waitcnt lgkmcnt(0)\n\t" \
    "v_smfmac_f32_16x16x64_bf16 v[244:247], v[220:223], v[184:191], v241\n\t" \
    "v_smfmac_f32_16x16x64_bf16 v[248:251], v[208:211], v[112:119], v241\n\t" \
    "v_smfmac_f32_16x16x64_bf16 v[248:251], v[236:239], v[128:135], v241\n\t" \
    "v_smfmac_f32_16x16x64_bf16 v[248:251], v[212:215], v[152:159], v241\n\t" \
    "v_permlane32_swap_b32 v244, v246\n\t" \
    "v_permlane32_swap_b32 v245, v247\n\t" \
    "v_smfmac_f32_16x16x64_bf16 v[248:251], v[216:219], v[168:175], v241\n\t" \
    "v_add_f32 v244, v244, v246\n\t" \
    "v_add_f32 v245, v245, v247\n\t" \
    "v_smfmac_f32_16x16x64_bf16 v[248:251], v[220:223], v[192:199], v241\n\t" \
    "v_cvt_pk_bf16_f32 v244, v244, v245\n\t" \
    "ds_write_b16 %[dst], v244 offset:512\n\t" \
    "ds_write_b16_d16_hi %[dst], v244 offset:528\n\t" \
    "v_smfmac_f32_16x16x64_bf16 v[224:227], v[208:211], v[96:103], v241\n\t" \
    "v_smfmac_f32_16x16x64_bf16 v[224:227], v[236:239], v[200:207], v241\n\t" \
    "v_smfmac_f32_16x16x64_bf16 v[224:227], v[212:215], v[136:143], v241\n\t" \
    "v_permlane32_swap_b32 v248, v250\n\t" \
    "v_permlane32_swap_b32 v249, v251\n\t" \
    "v_smfmac_f32_16x16x64_bf16 v[224:227], v[216:219], v[228:235], v241\n\t" \
    "v_add_f32 v248, v248, v250\n\t" \
    "v_add_f32 v249, v249, v251\n\t" \
    "v_smfmac_f32_16x16x64_bf16 v[224:227], v[220:223], v[176:183], v241\n\t" \
    "v_cvt_pk_bf16_f32 v248, v248, v249\n\t" \
    "ds_write_b16 %[dst], v248 offset:1024\n\t" \
    "ds_write_b16_d16_hi %[dst], v248 offset:1040\n\t" \
    "s_nop 4\n\t" \
    "v_permlane32_swap_b32 v224, v226\n\t" \
    "v_permlane32_swap_b32 v225, v227\n\t" \
    "v_add_f32 v224, v224, v226\n\t" \
    "v_add_f32 v225, v225, v227\n\t" \
    "v_cvt_pk_bf16_f32 v224, v224, v225\n\t" \
    "ds_write_b16 %[dst], v224 offset:0\n\t" \
    "ds_write_b16_d16_hi %[dst], v224 offset:16\n\t"
#define ASM_CRIT1_V2 \
    "s_branch .Lc0%=\n\t" \
    ".Lcr%=:\n\t" \
    "s_cmp_eq_u32 %[n], 0\n\t" \
    "s_cbranch_scc1 .Lcb%=\n\t" \
    "s_sub_u32 %[n], %[n], 1\n\t" \
    "v_max_u32 v243, v208, v209\n\t" \
    "v_max3_u32 v243, v210, v211, v243\n\t" \
    "v_cmp_eq_u32 vcc, -1, v243\n\t" \
    "s_cbranch_vccz .Lcs0_%=\n\t" \
    "buffer_load_dwordx4 v[208:211], %[ab], %[rs], %[s0] offen sc1\n\t" \
    ".Lcs0_%=:\n\t" \
    "v_max_u32 v243, v236, v237\n\t" \
    "v_max3_u32 v243, v238, v239, v243\n\t" \
    "v_cmp_eq_u32 vcc, -1, v243\n\t" \
    "s_cbranch_vccz .Lca%=\n\t" \
    "buffer_load_dwordx4 v[236:239], %[ab], %[rs], %[s1] offen sc1\n\t" \
    "s_branch .Lca%=\n\t" \
    ".Lc0%=:\n\t" \
    "buffer_load_dwordx4 v[208:211], %[ab], %[rs], %[s0] offen sc1\n\t" \
    "buffer_load_dwordx4 v[236:239], %[ab], %[rs], %[s1] offen sc1\n\t" \
    "v_bfe_i32 v241, %[ab], 7, 1\n\t" \
    "v_and_b32 v241, 0xaaaa, v241\n\t" \
    "v_or_b32 v241, 0x4444, v241\n\t" \
    "v_xor_b32 v212, 0x400, %[ab]\n\t" \
    "v_xor_b32 v216, 0x800, %[ab]\n\t" \
    "v_xor_b32 v242, 0xc00, %[ab]\n\t" \
    "v_xor_b32 v240, 0x1400, %[ab]\n\t" \
    ".Lca%=:\n\t" \
    "s_waitcnt vmcnt(0)\n\t" \
    "ds_write_b128 %[ab], v[208:211] offset:0\n\t" \
    "ds_write_b128 %[ab], v[236:239] offset:4096\n\t" \
    "v_max_u32 v243, v208, v209\n\t" \
    "v_max3_u32 v243, v210, v211, v243\n\t" \
    "v_max3_u32 v243, v236, v237, v243\n\t" \
    "v_max3_u32 v243, v238, v239, v243\n\t" \
    "v_cmp_eq_u32 vcc, -1, v243\n\t" \
    "s_cbranch_vccnz .Lcr%=\n\t" \
    ".Lcb%=:\n\t" \
    PRIUP \
    ASM_ST2 \
    "v_smfmac_f32_16x16x64_bf16 v[224:227], v[208:211], v[200:207], v241\n\t" \
    "ds_read_b128 v[200:203], %[wl] offset:4096\n\t" \
    "ds_read_b128 v[204:207], %[wl] offset:5120\n\t" \
    "v_smfmac_f32_16x16x64_bf16 v[244:247], v[208:211], v[0:7], v241\n\t" \
    "v_smfmac_f32_16x16x64_bf16 v[248:251], v[208:211], v[8:15], v241\n\t" \
    "s_waitcnt lgkmcnt(2)\n\t" \
    "buffer_load_dword %[fv], %[fv], %[rs], %[fs] offen sc1\n\t" \
    "s_barrier\n\t" ASM_ST \
    "ds_read_b128 v[212:215], v212 offset:0\n\t" \
    "ds_read_b128 v[216:219], v216 offset:0\n\t" \
    "ds_read_b128 v[220:223], v242 offset:0\n\t" \
    "ds_read_b128 v[208:211], v240 offset:0\n\t" \
    "v_smfmac_f32_16x16x64_bf16 v[244:247], v[236:239], v[80:87], v241\n\t" \
    "v_xor_b32 v240, 0x1800, %[ab]\n\t" \
    "v_smfmac_f32_16x16x64_bf16 v[248:251], v[236:239], v[88:95], v241\n\t" \
    "s_waitcnt lgkmcnt(4)\n\t" \
    "v_smfmac_f32_16x16x64_bf16 v[224:227], v[236:239], v[200:207], v241\n\t" \
    "ds_read_b128 v[200:203], %[wl] offset:6144\n\t" \
    "ds_read_b128 v[204:207], %[wl] offset:7168\n\t" \
    "ds_read_b128 v[236:239], v240 offset:0\n\t" \
    "s_waitcnt lgkmcnt(6)\n\t" \
    "v_smfmac_f32_16x16x64_bf16 v[224:227], v[212:215], v[16:23], v241\n\t" \
    "v_smfmac_f32_16x16x64_bf16 v[244:247], v[212:215], v[24:31], v241\n\t" \
    "v_xor_b32 v240, 0x1c00, %[ab]\n\t" \
    "v_smfmac_f32_16x16x64_bf16 v[248:251], v[212:215], v[32:39], v241\n\t" \
    "ds_read_b128 v[212:215], v240 offset:0\n\t" \
    "s_waitcnt lgkmcnt(6)\n\t" \
    "v_smfmac_f32_16x16x64_bf16 v[224:227], v[216:219], v[228:235], v241\n\t" \
    "ds_read_b128 v[228:231], %[wl] offset:8192\n\t" \
    "ds_read_b128 v[232:235], %[wl] offset:9216\n\t" \
    "v_smfmac_f32_16x16x64_bf16 v[244:247], v[216:219], v[40:47], v241\n\t" \
    "v_and_b32 v240, 0x3ff, %[ab]\n\t" \
    "v_smfmac_f32_16x16x64_bf16 v[248:251], v[216:219], v[48:55], v241\n\t" \
    "ds_read_b128 v[216:219], v240 offset:8192\n\t" \
    "s_waitcnt lgkmcnt(8)\n\t" \
    "v_smfmac_f32_16x16x64_bf16 v[224:227], v[220:223], v[56:63], v241\n\t" \
    "v_smfmac_f32_16x16x64_bf16 v[244:247], v[220:223], v[64:71], v241\n\t" \
    "v_smfmac_f32_16x16x64_bf16 v[248:251], v[220:223], v[72:79], v241\n\t" \
    "ds_read_b128 v[220:223], v240 offset:9216\n\t" \
    "s_waitcnt lgkmcnt(8)\n\t" \
    "v_smfmac_f32_16x16x64_bf16 v[244:247], v[208:211], v[104:111], v241\n\t" \
    "s_waitcnt lgkmcnt(5)\n\t" \
    "v_smfmac_f32_16x16x64_bf16 v[244:247], v[236:239], v[120:127], v241\n\t" \
    "s_waitcnt lgkmcnt(4)\n\t" \
    "v_smfmac_f32_16x16x64_bf16 v[244:247], v[212:215], v[144:151], v241\n\t" \
    "s_waitcnt lgkmcnt(1)\n\t" \
    "v_smfmac_f32_16x16x64_bf16 v[244:247], v[216:219], v[160:167], v241\n\t" \
    "s_waitcnt lgkmcnt(0)\n\t" \
    "v_smfmac_f32_16x16x64_bf16 v[244:247], v[220:223], v[184:191], v241\n\t" \
    "v_smfmac_f32_16x16x64_bf16 v[248:251], v[208:211], v[112:119], v241\n\t" \
    "v_smfmac_f32_16x16x64_bf16 v[248:251], v[236:239], v[128:135], v241\n\t" \
    "v_smfmac_f32_16x16x64_bf16 v[248:251], v[212:215], v[152:159], v241\n\t" \
    "v_permlane32_swap_b32 v244, v246\n\t" \
    "v_permlane32_swap_b32 v245, v247\n\t" \
    "v_smfmac_f32_16x16x64_bf16 v[248:251], v[216:219], v[168:175], v241\n\t" \
    "v_add_f32 v244, v244, v246\n\t" \
    "v_add_f32 v245, v245, v247\n\t" \
    "v_smfmac_f32_16x16x64_bf16 v[248:251], v[220:223], v[192:199], v241\n\t" \
    "v_cvt_pk_bf16_f32 v244, v244, v245\n\t" \
    "ds_write_b16 %[dst], v244 offset:512\n\t" \
    "ds_write_b16_d16_hi %[dst], v244 offset:528\n\t" \
    "v_smfmac_f32_16x16x64_bf16 v[224:227], v[208:211], v[96:103], v241\n\t" \
    "v_smfmac_f32_16x16x64_bf16 v[224:227], v[236:239], v[200:207], v241\n\t" \
    "v_smfmac_f32_16x16x64_bf16 v[224:227], v[212:215], v[136:143], v241\n\t" \
    "v_permlane32_swap_b32 v248, v250\n\t" \
    "v_permlane32_swap_b32 v249, v251\n\t" \
    "v_smfmac_f32_16x16x64_bf16 v[224:227], v[216:219], v[228:235], v241\n\t" \
    "v_add_f32 v248, v248, v250\n\t" \
    "v_add_f32 v249, v249, v251\n\t" \
    "v_smfmac_f32_16x16x64_bf16 v[224:227], v[220:223], v[176:183], v241\n\t" \
    "v_cvt_pk_bf16_f32 v248, v248, v249\n\t" \
    "ds_write_b16 %[dst], v248 offset:1024\n\t" \
    "ds_write_b16_d16_hi %[dst], v248 offset:1040\n\t" \
    "s_nop 4\n\t" \
    "v_permlane32_swap_b32 v224, v226\n\t" \
    "v_permlane32_swap_b32 v225, v227\n\t" \
    "v_add_f32 v224, v224, v226\n\t" \
    "v_add_f32 v225, v225, v227\n\t" \
    "v_cvt_pk_bf16_f32 v224, v224, v225\n\t" \
    "ds_write_b16 %[dst], v224 offset:0\n\t" \
    "ds_write_b16_d16_hi %[dst], v224 offset:16\n\t"
#define ASM_VOX \
    "s_waitcnt lgkmcnt(0)\n\t" \
    "ds_read_b128 v[208:211], %[va] offset:2048\n\t" \
    "ds_read_b128 v[212:215], %[va] offset:3072\n\t" \
    "ds_read_b128 v[216:219], %[va] offset:4096\n\t" \
    "ds_read_b128 v[220:223], %[va] offset:5120\n\t" \
    "ds_read_b128 v[200:203], %[va] offset:0\n\t" \
    "ds_read_b128 v[204:207], %[va] offset:1024\n\t" \
    "ds_read_b128 v[236:239], %[wl] offset:0\n\t" \
    "ds_read_b128 v[240:243], %[wl] offset:1024\n\t" \
    "s_waitcnt lgkmcnt(7)\n\t" \
    "v_mfma_f32_16x16x32_bf16 v[244:247], v[208:211], v[0:3], 0\n\t" \
    "s_waitcnt lgkmcnt(6)\n\t" \
    "v_mfma_f32_16x16x32_bf16 v[244:247], v[212:215], v[4:7], v[244:247]\n\t" \
    "s_waitcnt lgkmcnt(5)\n\t" \
    "v_mfma_f32_16x16x32_bf16 v[244:247], v[216:219], v[8:11], v[244:247]\n\t" \
    "s_waitcnt lgkmcnt(4)\n\t" \
    "v_mfma_f32_16x16x32_bf16 v[244:247], v[220:223], v[12:15], v[244:247]\n\t" \
    "s_waitcnt lgkmcnt(1)\n\t" \
    "v_mfma_f32_16x16x32_bf16 v[244:247], v[200:203], v[236:239], v[244:247]\n\t" \
    "ds_read_b128 v[236:239], %[wl] offset:2048\n\t" \
    "s_waitcnt lgkmcnt(1)\n\t" \
    "v_mfma_f32_16x16x32_bf16 v[244:247], v[204:207], v[240:243], v[244:247]\n\t" \
    "ds_read_b128 v[240:243], %[wl] offset:3072\n\t" \
    "v_mfma_f32_16x16x32_bf16 v[248:251], v[208:211], v[40:43], 0\n\t" \
    "v_mfma_f32_16x16x32_bf16 v[248:251], v[212:215], v[44:47], v[248:251]\n\t" \
    "v_mfma_f32_16x16x32_bf16 v[248:251], v[216:219], v[48:51], v[248:251]\n\t" \
    "v_cvt_pk_bf16_f32 v224, v244, v245\n\t" \
    "v_mfma_f32_16x16x32_bf16 v[248:251], v[220:223], v[52:55], v[248:251]\n\t" \
    "v_cvt_pk_bf16_f32 v225, v246, v247\n\t" \
    "s_waitcnt lgkmcnt(1)\n\t" \
    "v_mfma_f32_16x16x32_bf16 v[248:251], v[200:203], v[236:239], v[248:251]\n\t" \
    "ds_read_b128 v[236:239], %[wl] offset:4096\n\t" \
    "s_waitcnt lgkmcnt(1)\n\t" \
    "v_mfma_f32_16x16x32_bf16 v[248:251], v[204:207], v[240:243], v[248:251]\n\t" \
    "ds_read_b128 v[240:243], %[wl] offset:5120\n\t" \
    "v_mfma_f32_16x16x32_bf16 v[244:247], v[208:211], v[80:83], 0\n\t" \
    "v_mfma_f32_16x16x32_bf16 v[244:247], v[212:215], v[84:87], v[244:247]\n\t" \
    "v_mfma_f32_16x16x32_bf16 v[244:247], v[216:219], v[88:91], v[244:247]\n\t" \
    "v_cvt_pk_bf16_f32 v226, v248, v249\n\t" \
    "v_mfma_f32_16x16x32_bf16 v[244:247], v[220:223], v[92:95], v[244:247]\n\t" \
    "v_cvt_pk_bf16_f32 v227, v250, v251\n\t" \
    "s_waitcnt lgkmcnt(1)\n\t" \
    "v_mfma_f32_16x16x32_bf16 v[244:247], v[200:203], v[236:239], v[244:247]\n\t" \
    "ds_read_b128 v[236:239], %[wl] offset:6144\n\t" \
    "s_waitcnt lgkmcnt(1)\n\t" \
    "v_mfma_f32_16x16x32_bf16 v[244:247], v[204:207], v[240:243], v[244:247]\n\t" \
    "ds_read_b128 v[240:243], %[wl] offset:7168\n\t" \
    "v_mfma_f32_16x16x32_bf16 v[248:251], v[208:211], v[120:123], 0\n\t" \
    "v_mfma_f32_16x16x32_bf16 v[248:251], v[212:215], v[124:127], v[248:251]\n\t" \
    "v_mfma_f32_16x16x32_bf16 v[248:251], v[216:219], v[128:131], v[248:251]\n\t" \
    "v_cvt_pk_bf16_f32 v228, v244, v245\n\t" \
    "v_mfma_f32_16x16x32_bf16 v[248:251], v[220:223], v[132:135], v[248:251]\n\t" \
    "v_cvt_pk_bf16_f32 v229, v246, v247\n\t" \
    "s_waitcnt lgkmcnt(1)\n\t" \
    "v_mfma_f32_16x16x32_bf16 v[248:251], v[200:203], v[236:239], v[248:251]\n\t" \
    "ds_read_b128 v[236:239], %[wl] offset:8192\n\t" \
    "s_waitcnt lgkmcnt(1)\n\t" \
    "v_mfma_f32_16x16x32_bf16 v[248:251], v[204:207], v[240:243], v[248:251]\n\t" \
    "ds_read_b128 v[240:243], %[wl] offset:9216\n\t" \
    "v_mfma_f32_16x16x32_bf16 v[244:247], v[208:211], v[160:163], 0\n\t" \
    "v_mfma_f32_16x16x32_bf16 v[244:247], v[212:215], v[164:167], v[244:247]\n\t" \
    "v_mfma_f32_16x16x32_bf16 v[244:247], v[216:219], v[168:171], v[244:247]\n\t" \
    "v_cvt_pk_bf16_f32 v230, v248, v249\n\t" \
    "v_mfma_f32_16x16x32_bf16 v[244:247], v[220:223], v[172:175], v[244:247]\n\t" \
    "v_cvt_pk_bf16_f32 v231, v250, v251\n\t" \
    "s_waitcnt lgkmcnt(1)\n\t" \
    "v_mfma_f32_16x16x32_bf16 v[244:247], v[200:203], v[236:239], v[244:247]\n\t" \
    "s_waitcnt lgkmcnt(0)\n\t" \
    "v_mfma_f32_16x16x32_bf16 v[244:247], v[204:207], v[240:243], v[244:247]\n\t" \
    "v_mfma_f32_16x16x32_bf16 v[248:251], v[208:211], v[24:27], 0\n\t" \
    "v_mfma_f32_16x16x32_bf16 v[248:251], v[212:215], v[28:31], v[248:251]\n\t" \
    "v_mfma_f32_16x16x32_bf16 v[248:251], v[216:219], v[32:35], v[248:251]\n\t" \
    "v_cvt_pk_bf16_f32 v232, v244, v245\n\t" \
    "v_mfma_f32_16x16x32_bf16 v[248:251], v[220:223], v[36:39], v[248:251]\n\t" \
    "v_cvt_pk_bf16_f32 v233, v246, v247\n\t" \
    "v_mfma_f32_16x16x32_bf16 v[248:251], v[200:203], v[16:19], v[248:251]\n\t" \
    "v_mfma_f32_16x16x32_bf16 v[248:251], v[204:207], v[20:23], v[248:251]\n\t" \
    "v_mfma_f32_16x16x32_bf16 v[244:247], v[208:211], v[64:67], 0\n\t" \
    "v_mfma_f32_16x16x32_bf16 v[244:247], v[212:215], v[68:71], v[244:247]\n\t" \
    "v_mfma_f32_16x16x32_bf16 v[244:247], v[216:219], v[72:75], v[244:247]\n\t" \
    "v_cvt_pk_bf16_f32 v236, v248, v249\n\t" \
    "v_mfma_f32_16x16x32_bf16 v[244:247], v[220:223], v[76:79], v[244:247]\n\t" \
    "v_cvt_pk_bf16_f32 v237, v250, v251\n\t" \
    "v_mfma_f32_16x16x32_bf16 v[244:247], v[200:203], v[56:59], v[244:247]\n\t" \
    "v_mfma_f32_16x16x32_bf16 v[244:247], v[204:207], v[60:63], v[244:247]\n\t" \
    "v_mfma_f32_16x16x32_bf16 v[248:251], v[208:211], v[104:107], 0\n\t" \
    "v_mfma_f32_16x16x32_bf16 v[248:251], v[212:215], v[108:111], v[248:251]\n\t" \
    "v_mfma_f32_16x16x32_bf16 v[248:251], v[216:219], v[112:115], v[248:251]\n\t" \
    "v_cvt_pk_bf16_f32 v238, v244, v245\n\t" \
    "v_mfma_f32_16x16x32_bf16 v[248:251], v[220:223], v[116:119], v[248:251]\n\t" \
    "v_cvt_pk_bf16_f32 v239, v246, v247\n\t" \
    "v_mfma_f32_16x16x32_bf16 v[248:251], v[200:203], v[96:99], v[248:251]\n\t" \
    "v_mfma_f32_16x16x32_bf16 v[248:251], v[204:207], v[100:103], v[248:251]\n\t" \
    "v_mfma_f32_16x16x32_bf16 v[244:247], v[208:211], v[144:147], 0\n\t" \
    "v_mfma_f32_16x16x32_bf16 v[244:247], v[212:215], v[148:151], v[244:247]\n\t" \
    "v_mfma_f32_16x16x32_bf16 v[244:247], v[216:219], v[152:155], v[244:247]\n\t" \
    "v_cvt_pk_bf16_f32 v240, v248, v249\n\t" \
    "v_mfma_f32_16x16x32_bf16 v[244:247], v[220:223], v[156:159], v[244:247]\n\t" \
    "v_cvt_pk_bf16_f32 v241, v250, v251\n\t" \
    "v_mfma_f32_16x16x32_bf16 v[244:247], v[200:203], v[136:139], v[244:247]\n\t" \
    "v_mfma_f32_16x16x32_bf16 v[244:247], v[204:207], v[140:143], v[244:247]\n\t" \
    "v_mfma_f32_16x16x32_bf16 v[248:251], v[208:211], v[184:187], 0\n\t" \
    "ds_read_b128 v[208:211], %[va] offset:8192\n\t" \
    "v_mfma_f32_16x16x32_bf16 v[248:251], v[212:215], v[188:191], v[248:251]\n\t" \
    "ds_read_b128 v[212:215], %[va] offset:9216\n\t" \
    "v_mfma_f32_16x16x32_bf16 v[248:251], v[216:219], v[192:195], v[248:251]\n\t" \
    "ds_read_b128 v[216:219], %[va] offset:10240\n\t" \
    "v_cvt_pk_bf16_f32 v242, v244, v245\n\t" \
    "v_mfma_f32_16x16x32_bf16 v[248:251], v[220:223], v[196:199], v[248:251]\n\t" \
    "ds_read_b128 v[220:223], %[va] offset:11264\n\t" \
    "v_cvt_pk_bf16_f32 v243, v246, v247\n\t" \
    "v_mfma_f32_16x16x32_bf16 v[248:251], v[200:203], v[176:179], v[248:251]\n\t" \
    "ds_read_b128 v[200:203], %[va] offset:6144\n\t" \
    "v_mfma_f32_16x16x32_bf16 v[248:251], v[204:207], v[180:183], v[248:251]\n\t" \
    "ds_read_b128 v[204:207], %[va] offset:7168\n\t" \
    "s_waitcnt lgkmcnt(5)\n\t" \
    "v_mfma_f32_16x16x32_bf16 v[244:247], v[208:211], v[24:27], 0\n\t" \
    "s_waitcnt lgkmcnt(4)\n\t" \
    "v_mfma_f32_16x16x32_bf16 v[244:247], v[212:215], v[28:31], v[244:247]\n\t" \
    "s_waitcnt lgkmcnt(3)\n\t" \
    "v_mfma_f32_16x16x32_bf16 v[244:247], v[216:219], v[32:35], v[244:247]\n\t" \
    "v_cvt_pk_bf16_f32 v234, v248, v249\n\t" \
    "s_waitcnt lgkmcnt(2)\n\t" \
    "v_mfma_f32_16x16x32_bf16 v[244:247], v[220:223], v[36:39], v[244:247]\n\t" \
    "v_cvt_pk_bf16_f32 v235, v250, v251\n\t" \
    "s_waitcnt lgkmcnt(1)\n\t" \
    "v_mfma_f32_16x16x32_bf16 v[244:247], v[200:203], v[16:19], v[244:247]\n\t" \
    "s_waitcnt lgkmcnt(0)\n\t" \
    "v_mfma_f32_16x16x32_bf16 v[244:247], v[204:207], v[20:23], v[244:247]\n\t" \
    "v_mfma_f32_16x16x32_bf16 v[248:251], v[208:211], v[64:67], 0\n\t" \
    "s_add_i32 %[so], %[so], 65536\n\t" \
    "v_mfma_f32_16x16x32_bf16 v[248:251], v[212:215], v[68:71], v[248:251]\n\t" \
    "s_nop 1\n\t" \
    "v_cvt_pk_bf16_f32 v247, v246, v247\n\t" \
    "v_mfma_f32_16x16x32_bf16 v[248:251], v[216:219], v[72:75], v[248:251]\n\t" \
    "v_cvt_pk_bf16_f32 v246, v244, v245\n\t" \
    "v_mfma_f32_16x16x32_bf16 v[248:251], v[220:223], v[76:79], v[248:251]\n\t" \
    "v_mov_b64 v[244:245], v[236:237]\n\t" \
    "buffer_store_dwordx4 v[244:247], %[l16], %[rd], %[so] offen sc1\n\t" \
    "v_mfma_f32_16x16x32_bf16 v[248:251], v[200:203], v[56:59], v[248:251]\n\t" \
    "v_mfma_f32_16x16x32_bf16 v[248:251], v[204:207], v[60:63], v[248:251]\n\t" \
    "v_mfma_f32_16x16x32_bf16 v[244:247], v[208:211], v[104:107], 0\n\t" \
    "s_add_i32 %[so], %[so], 131072\n\t" \
    "v_mfma_f32_16x16x32_bf16 v[244:247], v[212:215], v[108:111], v[244:247]\n\t" \
    "s_nop 1\n\t" \
    "v_cvt_pk_bf16_f32 v251, v250, v251\n\t" \
    "v_mfma_f32_16x16x32_bf16 v[244:247], v[216:219], v[112:115], v[244:247]\n\t" \
    "v_cvt_pk_bf16_f32 v250, v248, v249\n\t" \
    "v_mfma_f32_16x16x32_bf16 v[244:247], v[220:223], v[116:119], v[244:247]\n\t" \
    "v_mov_b64 v[248:249], v[238:239]\n\t" \
    "buffer_store_dwordx4 v[248:251], %[l16], %[rd], %[so] offen sc1\n\t" \
    "v_mfma_f32_16x16x32_bf16 v[244:247], v[200:203], v[96:99], v[244:247]\n\t" \
    "v_mfma_f32_16x16x32_bf16 v[244:247], v[204:207], v[100:103], v[244:247]\n\t" \
    "v_mfma_f32_16x16x32_bf16 v[248:251], v[208:211], v[144:147], 0\n\t" \
    "s_add_i32 %[so], %[so], 131072\n\t" \
    "v_mfma_f32_16x16x32_bf16 v[248:251], v[212:215], v[148:151], v[248:251]\n\t" \
    "s_nop 1\n\t" \
    "v_cvt_pk_bf16_f32 v247, v246, v247\n\t" \
    "v_mfma_f32_16x16x32_bf16 v[248:251], v[216:219], v[152:155], v[248:251]\n\t" \
    "v_cvt_pk_bf16_f32 v246, v244, v245\n\t" \
    "v_mfma_f32_16x16x32_bf16 v[248:251], v[220:223], v[156:159], v[248:251]\n\t" \
    "v_mov_b64 v[244:245], v[240:241]\n\t" \
    "buffer_store_dwordx4 v[244:247], %[l16], %[rd], %[so] offen sc1\n\t" \
    "v_mfma_f32_16x16x32_bf16 v[248:251], v[200:203], v[136:139], v[248:251]\n\t" \
    "v_mfma_f32_16x16x32_bf16 v[248:251], v[204:207], v[140:143], v[248:251]\n\t" \
    "v_mfma_f32_16x16x32_bf16 v[244:247], v[208:211], v[184:187], 0\n\t" \
    "s_add_i32 %[so], %[so], 131072\n\t" \
    "v_mfma_f32_16x16x32_bf16 v[244:247], v[212:215], v[188:191], v[244:247]\n\t" \
    "s_nop 1\n\t" \
    "v_cvt_pk_bf16_f32 v251, v250, v251\n\t" \
    "v_mfma_f32_16x16x32_bf16 v[244:247], v[216:219], v[192:195], v[244:247]\n\t" \
    "v_cvt_pk_bf16_f32 v250, v248, v249\n\t" \
    "v_mfma_f32_16x16x32_bf16 v[244:247], v[220:223], v[196:199], v[244:247]\n\t" \
    "v_mov_b64 v[248:249], v[242:243]\n\t" \
    "buffer_store_dwordx4 v[248:251], %[l16], %[rd], %[so] offen sc1\n\t" \
    "v_mfma_f32_16x16x32_bf16 v[244:247], v[200:203], v[176:179], v[244:247]\n\t" \
    "v_mfma_f32_16x16x32_bf16 v[244:247], v[204:207], v[180:183], v[244:247]\n\t" \
    "ds_read_b128 v[236:239], %[wl] offset:0\n\t" \
    "ds_read_b128 v[240:243], %[wl] offset:1024\n\t" \
    "v_mfma_f32_16x16x32_bf16 v[248:251], v[208:211], v[0:3], 0\n\t" \
    "s_add_i32 %[so], %[so], 131072\n\t" \
    "v_mfma_f32_16x16x32_bf16 v[248:251], v[212:215], v[4:7], v[248:251]\n\t" \
    "v_cvt_pk_bf16_f32 v247, v246, v247\n\t" \
    "v_mfma_f32_16x16x32_bf16 v[248:251], v[216:219], v[8:11], v[248:251]\n\t" \
    "v_cvt_pk_bf16_f32 v246, v244, v245\n\t" \
    "v_mfma_f32_16x16x32_bf16 v[248:251], v[220:223], v[12:15], v[248:251]\n\t" \
    "v_mov_b64 v[244:245], v[234:235]\n\t" \
    "buffer_store_dwordx4 v[244:247], %[l16], %[rd], %[so] offen sc1\n\t" \
    "s_waitcnt lgkmcnt(1)\n\t" \
    "v_mfma_f32_16x16x32_bf16 v[248:251], v[200:203], v[236:239], v[248:251]\n\t" \
    "ds_read_b128 v[236:239], %[wl] offset:2048\n\t" \
    "s_waitcnt lgkmcnt(1)\n\t" \
    "v_mfma_f32_16x16x32_bf16 v[248:251], v[204:207], v[240:243], v[248:251]\n\t" \
    "ds_read_b128 v[240:243], %[wl] offset:3072\n\t" \
    "v_mfma_f32_16x16x32_bf16 v[244:247], v[208:211], v[40:43], 0\n\t" \
    "s_add_i32 %[so], %[so], -589824\n\t" \
    "v_mfma_f32_16x16x32_bf16 v[244:247], v[212:215], v[44:47], v[244:247]\n\t" \
    "s_nop 0\n\t" \
    "v_cvt_pk_bf16_f32 v251, v250, v251\n\t" \
    "v_mfma_f32_16x16x32_bf16 v[244:247], v[216:219], v[48:51], v[244:247]\n\t" \
    "v_cvt_pk_bf16_f32 v250, v248, v249\n\t" \
    "v_mfma_f32_16x16x32_bf16 v[244:247], v[220:223], v[52:55], v[244:247]\n\t" \
    "v_mov_b64 v[248:249], v[224:225]\n\t" \
    "buffer_store_dwordx4 v[248:251], %[l16], %[rd], %[so] offen sc1\n\t" \
    "s_waitcnt lgkmcnt(1)\n\t" \
    "v_mfma_f32_16x16x32_bf16 v[244:247], v[200:203], v[236:239], v[244:247]\n\t" \
    "ds_read_b128 v[236:239], %[wl] offset:4096\n\t" \
    "s_waitcnt lgkmcnt(1)\n\t" \
    "v_mfma_f32_16x16x32_bf16 v[244:247], v[204:207], v[240:243], v[244:247]\n\t" \
    "ds_read_b128 v[240:243], %[wl] offset:5120\n\t" \
    "v_mfma_f32_16x16x32_bf16 v[248:251], v[208:211], v[80:83], 0\n\t" \
    "s_add_i32 %[so], %[so], 131072\n\t" \
    "v_mfma_f32_16x16x32_bf16 v[248:251], v[212:215], v[84:87], v[248:251]\n\t" \
    "s_nop 0\n\t" \
    "v_cvt_pk_bf16_f32 v247, v246, v247\n\t" \
    "v_mfma_f32_16x16x32_bf16 v[248:251], v[216:219], v[88:91], v[248:251]\n\t" \
    "v_cvt_pk_bf16_f32 v246, v244, v245\n\t" \
    "v_mfma_f32_16x16x32_bf16 v[248:251], v[220:223], v[92:95], v[248:251]\n\t" \
    "v_mov_b64 v[244:245], v[226:227]\n\t" \
    "buffer_store_dwordx4 v[244:247], %[l16], %[rd], %[so] offen sc1\n\t" \
    "s_waitcnt lgkmcnt(1)\n\t" \
    "v_mfma_f32_16x16x32_bf16 v[248:251], v[200:203], v[236:239], v[248:251]\n\t" \
    "ds_read_b128 v[236:239], %[wl] offset:6144\n\t" \
    "s_waitcnt lgkmcnt(1)\n\t" \
    "v_mfma_f32_16x16x32_bf16 v[248:251], v[204:207], v[240:243], v[248:251]\n\t" \
    "ds_read_b128 v[240:243], %[wl] offset:7168\n\t" \
    "v_mfma_f32_16x16x32_bf16 v[244:247], v[208:211], v[120:123], 0\n\t" \
    "s_add_i32 %[so], %[so], 131072\n\t" \
    "v_mfma_f32_16x16x32_bf16 v[244:247], v[212:215], v[124:127], v[244:247]\n\t" \
    "s_nop 0\n\t" \
    "v_cvt_pk_bf16_f32 v251, v250, v251\n\t" \
    "v_mfma_f32_16x16x32_bf16 v[244:247], v[216:219], v[128:131], v[244:247]\n\t" \
    "v_cvt_pk_bf16_f32 v250, v248, v249\n\t" \
    "v_mfma_f32_16x16x32_bf16 v[244:247], v[220:223], v[132:135], v[244:247]\n\t" \
    "v_mov_b64 v[248:249], v[228:229]\n\t" \
    "buffer_store_dwordx4 v[248:251], %[l16], %[rd], %[so] offen sc1\n\t" \
    "s_waitcnt lgkmcnt(1)\n\t" \
    "v_mfma_f32_16x16x32_bf16 v[244:247], v[200:203], v[236:239], v[244:247]\n\t" \
    "ds_read_b128 v[236:239], %[wl] offset:8192\n\t" \
    "s_waitcnt lgkmcnt(1)\n\t" \
    "v_mfma_f32_16x16x32_bf16 v[244:247], v[204:207], v[240:243], v[244:247]\n\t" \
    "ds_read_b128 v[240:243], %[wl] offset:9216\n\t" \
    "v_mfma_f32_16x16x32_bf16 v[248:251], v[208:211], v[160:163], 0\n\t" \
    "s_add_i32 %[so], %[so], 131072\n\t" \
    "v_mfma_f32_16x16x32_bf16 v[248:251], v[212:215], v[164:167], v[248:251]\n\t" \
    "s_nop 0\n\t" \
    "v_cvt_pk_bf16_f32 v247, v246, v247\n\t" \
    "v_mfma_f32_16x16x32_bf16 v[248:251], v[216:219], v[168:171], v[248:251]\n\t" \
    "v_cvt_pk_bf16_f32 v246, v244, v245\n\t" \
    "v_mfma_f32_16x16x32_bf16 v[248:251], v[220:223], v[172:175], v[248:251]\n\t" \
    "v_mov_b64 v[244:245], v[230:231]\n\t" \
    "buffer_store_dwordx4 v[244:247], %[l16], %[rd], %[so] offen sc1\n\t" \
    "s_waitcnt lgkmcnt(1)\n\t" \
    "v_mfma_f32_16x16x32_bf16 v[248:251], v[200:203], v[236:239], v[248:251]\n\t" \
    "s_waitcnt lgkmcnt(0)\n\t" \
    "v_mfma_f32_16x16x32_bf16 v[248:251], v[204:207], v[240:243], v[248:251]\n\t" \
    "s_add_i32 %[so], %[so], 131072\n\t" \
    "s_nop 7\n\t" \
    "v_cvt_pk_bf16_f32 v251, v250, v251\n\t" \
    "v_cvt_pk_bf16_f32 v250, v248, v249\n\t" \
    "v_mov_b64 v[248:249], v[232:233]\n\t" \
    "buffer_store_dwordx4 v[248:251], %[l16], %[rd], %[so] offen sc1\n\t" \
    "s_nop 1\n\t"
#define ASM_CRIT2 \
    "s_waitcnt lgkmcnt(0)\n\t" \
    "s_barrier\n\t" ASM_ST \
    "ds_read_b128 v[204:207], v200 offset:11264\n\t" \
    "ds_read_b128 v[208:211], v200 offset:12288\n\t" \
    "ds_read_b128 v[200:203], v200 offset:10240\n\t" \
    "s_waitcnt lgkmcnt(2)\n\t" \
    "v_smfmac_f32_16x16x64_bf16 v[224:227], v[204:207], v[24:31], %[sidx]\n\t" \
    "s_waitcnt lgkmcnt(1)\n\t" \
    "v_smfmac_f32_16x16x64_bf16 v[224:227], v[208:211], v[32:39], %[sidx]\n\t" \
    "s_waitcnt lgkmcnt(0)\n\t" \
    "v_smfmac_f32_16x16x64_bf16 v[224:227], v[200:203], v[16:23], %[sidx]\n\t" \
    "v_smfmac_f32_16x16x64_bf16 v[220:223], v[204:207], v[0:7], %[sidx]\n\t" \
    "v_smfmac_f32_16x16x64_bf16 v[220:223], v[208:211], v[8:15], %[sidx]\n\t" \
    "v_smfmac_f32_16x16x64_bf16 v[220:223], v[200:203], v[212:219], %[sidx]\n\t" \
    "ds_read_b128 v[212:215], %[wl] offset:2048\n\t" \
    "ds_read_b128 v[216:219], %[wl] offset:3072\n\t" \
    "v_smfmac_f32_16x16x64_bf16 v[232:235], v[204:207], v[64:71], %[sidx]\n\t" \
    "v_smfmac_f32_16x16x64_bf16 v[232:235], v[208:211], v[72:79], %[sidx]\n\t" \
    "v_smfmac_f32_16x16x64_bf16 v[232:235], v[200:203], v[56:63], %[sidx]\n\t" \
    "v_permlane32_swap_b32 v220, v224\n\t" \
    "v_permlane32_swap_b32 v221, v225\n\t" \
    "v_permlane32_swap_b32 v222, v226\n\t" \
    "v_smfmac_f32_16x16x64_bf16 v[228:231], v[204:207], v[40:47], %[sidx]\n\t" \
    "v_permlane32_swap_b32 v223, v227\n\t" \
    "v_add_f32 v220, v220, v224\n\t" \
    "v_add_f32 v221, v221, v225\n\t" \
    "v_smfmac_f32_16x16x64_bf16 v[228:231], v[208:211], v[48:55], %[sidx]\n\t" \
    "v_add_f32 v222, v222, v226\n\t" \
    "v_add_f32 v223, v223, v227\n\t" \
    "v_cvt_pk_bf16_f32 v226, v220, v221\n\t" \
    "s_waitcnt lgkmcnt(0)\n\t" \
    "v_smfmac_f32_16x16x64_bf16 v[228:231], v[200:203], v[212:219], %[sidx]\n\t" \
    "ds_read_b128 v[212:215], %[wl] offset:4096\n\t" \
    "ds_read_b128 v[216:219], %[wl] offset:5120\n\t" \
    "v_cvt_pk_bf16_f32 v227, v222, v223\n\t" \
    "v_min_u32 v226, -2, v226\n\t" \
    "v_min_u32 v227, -2, v227\n\t" \
    "v_smfmac_f32_16x16x64_bf16 v[240:243], v[204:207], v[104:111], %[sidx]\n\t" \
    "v_smfmac_f32_16x16x64_bf16 v[240:243], v[208:211], v[112:119], %[sidx]\n\t" \
    "v_smfmac_f32_16x16x64_bf16 v[240:243], v[200:203], v[96:103], %[sidx]\n\t" \
    "v_permlane32_swap_b32 v228, v232\n\t" \
    "v_permlane32_swap_b32 v229, v233\n\t" \
    "v_permlane32_swap_b32 v230, v234\n\t" \
    "v_smfmac_f32_16x16x64_bf16 v[236:239], v[204:207], v[80:87], %[sidx]\n\t" \
    "v_permlane32_swap_b32 v231, v235\n\t" \
    "v_add_f32 v228, v228, v232\n\t" \
    "v_add_f32 v229, v229, v233\n\t" \
    "v_smfmac_f32_16x16x64_bf16 v[236:239], v[208:211], v[88:95], %[sidx]\n\t" \
    "v_add_f32 v230, v230, v234\n\t" \
    "v_add_f32 v231, v231, v235\n\t" \
    "v_cvt_pk_bf16_f32 v228, v228, v229\n\t" \
    "s_waitcnt lgkmcnt(0)\n\t" \
    "v_smfmac_f32_16x16x64_bf16 v[236:239], v[200:203], v[212:219], %[sidx]\n\t" \
    "ds_read_b128 v[212:215], %[wl] offset:6144\n\t" \
    "ds_read_b128 v[216:219], %[wl] offset:7168\n\t" \
    "v_cvt_pk_bf16_f32 v229, v230, v231\n\t" \
    "v_min_u32 v228, -2, v228\n\t" \
    "v_min_u32 v229, -2, v229\n\t" \
    "s_nop 1\n\t" \
    "v_permlane16_swap_b32 v226, v228\n\t" \
    "v_permlane16_swap_b32 v227, v229\n\t" \
    "buffer_store_dwordx4 v[226:229], %[vo], %[rr], %[so] offen sc1\n\t" \
    "s_add_u32 %[so], %[so], 0x10000\n\t" \
    "v_smfmac_f32_16x16x64_bf16 v[248:251], v[204:207], v[144:151], %[sidx]\n\t" \
    "v_mov_b64 v[220:221], 0\n\t" \
    "v_mov_b64 v[222:223], 0\n\t" \
    "v_smfmac_f32_16x16x64_bf16 v[248:251], v[208:211], v[152:159], %[sidx]\n\t" \
    "v_mov_b64 v[224:225], 0\n\t" \
    "v_mov_b64 v[226:227], 0\n\t" \
    "v_smfmac_f32_16x16x64_bf16 v[248:251], v[200:203], v[136:143], %[sidx]\n\t" \
    "v_permlane32_swap_b32 v236, v240\n\t" \
    "v_permlane32_swap_b32 v237, v241\n\t" \
    "v_permlane32_swap_b32 v238, v242\n\t" \
    "v_smfmac_f32_16x16x64_bf16 v[244:247], v[204:207], v[120:127], %[sidx]\n\t" \
    "v_permlane32_swap_b32 v239, v243\n\t" \
    "v_add_f32 v236, v236, v240\n\t" \
    "v_add_f32 v237, v237, v241\n\t" \
    "v_smfmac_f32_16x16x64_bf16 v[244:247], v[208:211], v[128:135], %[sidx]\n\t" \
    "v_add_f32 v238, v238, v242\n\t" \
    "v_add_f32 v239, v239, v243\n\t" \
    "v_cvt_pk_bf16_f32 v242, v236, v237\n\t" \
    "s_waitcnt lgkmcnt(0)\n\t" \
    "v_smfmac_f32_16x16x64_bf16 v[244:247], v[200:203], v[212:219], %[sidx]\n\t" \
    "ds_read_b128 v[212:215], %[wl] offset:8192\n\t" \
    "ds_read_b128 v[216:219], %[wl] offset:9216\n\t" \
    "v_cvt_pk_bf16_f32 v243, v238, v239\n\t" \
    "v_min_u32 v242, -2, v242\n\t" \
    "v_min_u32 v243, -2, v243\n\t" \
    "v_smfmac_f32_16x16x64_bf16 v[224:227], v[204:207], v[184:191], %[sidx]\n\t" \
    "v_and_b32 v232, 0x3fff, %[vo]\n\t" \
    "v_bfe_u32 v233, %[vo], 14, 1\n\t" \
    "v_smfmac_f32_16x16x64_bf16 v[224:227], v[208:211], v[192:199], %[sidx]\n\t" \
    "v_lshl_add_u32 v232, v233, 3, v232\n\t" \
    "v_permlane32_swap_b32 v244, v248\n\t" \
    "v_smfmac_f32_16x16x64_bf16 v[224:227], v[200:203], v[176:183], %[sidx]\n\t" \
    "v_permlane32_swap_b32 v245, v249\n\t" \
    "v_permlane32_swap_b32 v246, v250\n\t" \
    "v_permlane32_swap_b32 v247, v251\n\t" \
    "v_smfmac_f32_16x16x64_bf16 v[220:223], v[204:207], v[160:167], %[sidx]\n\t" \
    "v_add_f32 v244, v244, v248\n\t" \
    "v_add_f32 v245, v245, v249\n\t" \
    "v_add_f32 v246, v246, v250\n\t" \
    "v_smfmac_f32_16x16x64_bf16 v[220:223], v[208:211], v[168:175], %[sidx]\n\t" \
    "v_add_f32 v247, v247, v251\n\t" \
    "v_cvt_pk_bf16_f32 v244, v244, v245\n\t" \
    "v_cvt_pk_bf16_f32 v245, v246, v247\n\t" \
    "s_waitcnt lgkmcnt(0)\n\t" \
    "v_smfmac_f32_16x16x64_bf16 v[220:223], v[200:203], v[212:219], %[sidx]\n\t" \
    "v_min_u32 v244, -2, v244\n\t" \
    "v_min_u32 v245, -2, v245\n\t" \
    "s_nop 1\n\t" \
    "v_permlane16_swap_b32 v242, v244\n\t" \
    "v_permlane16_swap_b32 v243, v245\n\t" \
    "buffer_store_dwordx4 v[242:245], %[vo], %[rr], %[so] offen sc1\n\t" \
    "s_add_u32 %[so], %[so], 0x10000\n\t" \
    "s_nop 0\n\t" \
    "v_permlane32_swap_b32 v220, v224\n\t" \
    "v_permlane32_swap_b32 v221, v225\n\t" \
    "v_permlane32_swap_b32 v222, v226\n\t" \
    "v_permlane32_swap_b32 v223, v227\n\t" \
    "v_add_f32 v220, v220, v224\n\t" \
    "v_add_f32 v221, v221, v225\n\t" \
    "v_add_f32 v222, v222, v226\n\t" \
    "v_add_f32 v223, v223, v227\n\t" \
    "v_cvt_pk_bf16_f32 v220, v220, v221\n\t" \
    "v_cvt_pk_bf16_f32 v221, v222, v223\n\t" \
    "v_min_u32 v220, -2, v220\n\t" \
    "v_min_u32 v221, -2, v221\n\t" \
    "buffer_store_dwordx2 v[220:221], v232, %[rr], %[so] offen sc1\n\t" \
    "s_nop 1\n\t"
#define ASM_SM \
    "v_mbcnt_lo_u32_b32 v252, -1, 0\n\t" \
    "v_mbcnt_hi_u32_b32 v252, -1, v252\n\t" \
    "v_lshlrev_b32 v252, 4, v252\n\t" \
    "v_lshrrev_b32 v253, 1, v252\n\t" \
    "v_xor_b32 v253, v253, v252\n\t" \
    "v_and_b32 v253, 0x20, v253\n\t" \
    "v_mul_u32_u24 v253, 3, v253\n\t" \
    "v_xor_b32 v253, v253, v252\n\t" \
    "ds_read_b128 v[200:203], v253 offset:28672\n\t" \
    "ds_read_b128 v[204:207], v253 offset:29696\n\t" \
    "ds_read_b128 v[208:211], v253 offset:34816\n\t" \
    "ds_read_b128 v[212:215], v253 offset:35840\n\t" \
    "ds_read_b128 v[216:219], v253 offset:30720\n\t" \
    "ds_read_b128 v[220:223], v253 offset:31744\n\t" \
    "ds_read_b128 v[224:227], v253 offset:36864\n\t" \
    "ds_read_b128 v[228:231], v253 offset:37888\n\t" \
    "v_lshrrev_b32 v254, 1, v252\n\t" \
    "v_and_b32 v254, 0x180, v254\n\t" \
    "v_and_or_b32 v254, v252, %[c70], v254\n\t" \
    "v_bfe_i32 v255, v252, 7, 1\n\t" \
    "v_and_b32 v255, 0xaaaa, v255\n\t" \
    "v_or_b32 v255, 0x4444, v255\n\t" \
    "v_mov_b64 v[232:233], 0\n\t" \
    "v_mov_b64 v[234:235], 0\n\t" \
    "v_mov_b64 v[236:237], 0\n\t" \
    "v_mov_b64 v[238:239], 0\n\t" \
    "s_waitcnt lgkmcnt(0)\n\t" \
    "s_barrier\n\t" ASM_ST \
    "s_setprio 3\n\t" \
    "ds_read_b128 v[240:243], v252 offset:10240\n\t" \
    "ds_read_b128 v[244:247], v252 offset:11264\n\t" \
    "ds_read_b128 v[248:251], v252 offset:12288\n\t" \
    "s_waitcnt lgkmcnt(2)\n\t" \
    "v_smfmac_f32_16x16x64_bf16 v[232:235], v[240:243], v[200:207], v255\n\t" \
    "ds_read_b128 v[200:203], v253 offset:32768\n\t" \
    "ds_read_b128 v[204:207], v253 offset:33792\n\t" \
    "v_smfmac_f32_16x16x64_bf16 v[236:239], v[240:243], v[208:215], v255\n\t" \
    "ds_read_b128 v[208:211], v253 offset:38912\n\t" \
    "ds_read_b128 v[212:215], v253 offset:39936\n\t" \
    "s_waitcnt lgkmcnt(5)\n\t" \
    "v_smfmac_f32_16x16x64_bf16 v[232:235], v[244:247], v[216:223], v255\n\t" \
    "ds_read_b128 v[216:219], v254 offset:13312\n\t" \
    "ds_read_b128 v[220:223], v254 offset:13824\n\t" \
    "v_smfmac_f32_16x16x64_bf16 v[236:239], v[244:247], v[224:231], v255\n\t" \
    "ds_read_b128 v[224:227], v254 offset:14336\n\t" \
    "ds_read_b128 v[228:231], v254 offset:14848\n\t" \
    "s_waitcnt lgkmcnt(6)\n\t" \
    "v_smfmac_f32_16x16x64_bf16 v[232:235], v[248:251], v[200:207], v255\n\t" \
    "s_waitcnt lgkmcnt(4)\n\t" \
    "v_smfmac_f32_16x16x64_bf16 v[236:239], v[248:251], v[208:215], v255\n\t" \
    "ds_read_b128 v[208:211], v254 offset:15360\n\t" \
    "ds_read_b128 v[212:215], v254 offset:15872\n\t" \
    "v_mov_b64 v[204:205], 0\n\t" \
    "v_mov_b64 v[206:207], 0\n\t" \
    "s_nop 1\n\t" \
    "s_waitcnt lgkmcnt(4)\n\t" \
    "v_smfmac_f32_16x16x64_bf16 v[204:207], v[240:243], v[216:223], v255\n\t" \
    "s_waitcnt lgkmcnt(2)\n\t" \
    "v_smfmac_f32_16x16x64_bf16 v[204:207], v[244:247], v[224:231], v255\n\t" \
    "v_bfe_u32 v253, v252, 5, 1\n\t" \
    "v_bfe_i32 v202, v252, 4, 1\n\t" \
    "v_lshlrev_b32 v200, 1, v252\n\t" \
    "v_and_b32 v200, 0x180, v200\n\t" \
    "v_permlane32_swap_b32 v232, v234\n\t" \
    "v_permlane32_swap_b32 v233, v235\n\t" \
    "v_permlane32_swap_b32 v236, v238\n\t" \
    "v_permlane32_swap_b32 v237, v239\n\t" \
    "v_lshrrev_b32 v201, 2, v252\n\t" \
    "v_lshrrev_b32 v203, 4, v252\n\t" \
    "v_and_b32 v201, 0x40, v201\n\t" \
    "v_and_or_b32 v201, v203, 32, v201\n\t" \
    "v_and_or_b32 v201, v252, 16, v201\n\t" \
    "v_pk_add_f32 v[232:233], v[232:233], v[234:235]\n\t" \
    "v_pk_add_f32 v[234:235], v[236:237], v[238:239]\n\t" \
    "v_or_b32 v200, v200, v201\n\t" \
    "v_mad_u32_u24 v200, v253, %[cs], v200\n\t" \
    "v_mad_u32_u24 v201, v253, %[cs], v201\n\t" \
    "v_lshrrev_b32 v203, 3, v252\n\t" \
    "v_bfe_u32 v253, v252, 7, 1\n\t" \
    "s_waitcnt lgkmcnt(0)\n\t" \
    "v_smfmac_f32_16x16x64_bf16 v[204:207], v[248:251], v[208:215], v255\n\t" \
    "v_and_or_b32 v201, v203, 8, v201\n\t" \
    "v_and_b32 v202, 0x06060606, v202\n\t" \
    "v_mad_u32_u24 v201, v253, %[cp], v201\n\t" \
    "v_xor_b32 v202, 0x01000504, v202\n\t" \
    "v_max_f32 v236, v232, v234\n\t" \
    "v_max_f32 v237, v233, v235\n\t" \
    "s_nop 1\n\t" \
    "v_permlane32_swap_b32 v204, v206\n\t" \
    "v_permlane32_swap_b32 v205, v207\n\t" \
    "v_add_f32 v204, v204, v206\n\t" \
    "v_add_f32 v205, v205, v207\n\t" \
    "v_max_f32 v236, v236, v204\n\t" \
    "v_max_f32 v237, v237, v205\n\t" \
    "s_nop 0\n\t" \
    "v_max_f32_dpp v236, v236, v236 quad_perm:[1,0,3,2] row_mask:0xf bank_mask:0xf\n\t" \
    "v_max_f32_dpp v237, v237, v237 quad_perm:[1,0,3,2] row_mask:0xf bank_mask:0xf\n\t" \
    "s_nop 0\n\t" \
    "v_max_f32_dpp v236, v236, v236 quad_perm:[2,3,0,1] row_mask:0xf bank_mask:0xf\n\t" \
    "v_max_f32_dpp v237, v237, v237 quad_perm:[2,3,0,1] row_mask:0xf bank_mask:0xf\n\t" \
    "s_nop 0\n\t" \
    "v_max_f32_dpp v236, v236, v236 row_half_mirror row_mask:0xf bank_mask:0xf\n\t" \
    "v_max_f32_dpp v237, v237, v237 row_half_mirror row_mask:0xf bank_mask:0xf\n\t" \
    "s_nop 0\n\t" \
    "v_max_f32_dpp v236, v236, v236 row_mirror row_mask:0xf bank_mask:0xf\n\t" \
    "v_max_f32_dpp v237, v237, v237 row_mirror row_mask:0xf bank_mask:0xf\n\t" \
    "v_mul_f32 v236, %[scl], v236\n\t" \
    "v_mul_f32 v237, %[scl], v237\n\t" \
    "v_fma_f32 v232, v232, %[scl], -v236\n\t" \
    "v_fma_f32 v233, v233, %[scl], -v237\n\t" \
    "v_fma_f32 v234, v234, %[scl], -v236\n\t" \
    "v_fma_f32 v235, v235, %[scl], -v237\n\t" \
    "v_exp_f32 v232, v232\n\t" \
    "v_exp_f32 v233, v233\n\t" \
    "v_fma_f32 v204, v204, %[scl], -v236\n\t" \
    "v_fma_f32 v205, v205, %[scl], -v237\n\t" \
    "v_exp_f32 v234, v234\n\t" \
    "v_exp_f32 v235, v235\n\t" \
    "v_exp_f32 v204, v204\n\t" \
    "v_exp_f32 v205, v205\n\t" \
    "v_add_f32 v238, v232, v234\n\t" \
    "v_add_f32 v239, v233, v235\n\t" \
    "v_fma_f32 v238, v204, 0.5, v238\n\t" \
    "v_fma_f32 v239, v205, 0.5, v239\n\t" \
    "s_nop 0\n\t" \
    "v_add_f32_dpp v238, v238, v238 quad_perm:[1,0,3,2] row_mask:0xf bank_mask:0xf\n\t" \
    "v_add_f32_dpp v239, v239, v239 quad_perm:[1,0,3,2] row_mask:0xf bank_mask:0xf\n\t" \
    "s_nop 0\n\t" \
    "v_add_f32_dpp v238, v238, v238 quad_perm:[2,3,0,1] row_mask:0xf bank_mask:0xf\n\t" \
    "v_add_f32_dpp v239, v239, v239 quad_perm:[2,3,0,1] row_mask:0xf bank_mask:0xf\n\t" \
    "s_nop 0\n\t" \
    "v_add_f32_dpp v238, v238, v238 row_half_mirror row_mask:0xf bank_mask:0xf\n\t" \
    "v_add_f32_dpp v239, v239, v239 row_half_mirror row_mask:0xf bank_mask:0xf\n\t" \
    "s_nop 0\n\t" \
    "v_add_f32_dpp v238, v238, v238 row_mirror row_mask:0xf bank_mask:0xf\n\t" \
    "v_add_f32_dpp v239, v239, v239 row_mirror row_mask:0xf bank_mask:0xf\n\t" \
    "v_rcp_f32 v238, v238\n\t" \
    "v_rcp_f32 v239, v239\n\t" \
    "s_nop 0\n\t" \
    "v_pk_mul_f32 v[232:233], v[232:233], v[238:239]\n\t" \
    "v_pk_mul_f32 v[234:235], v[234:235], v[238:239]\n\t" \
    "v_pk_mul_f32 v[204:205], v[204:205], v[238:239]\n\t" \
    "v_cvt_pk_bf16_f32 v232, |v232|, |v233|\n\t" \
    "v_cvt_pk_bf16_f32 v234, |v234|, |v235|\n\t" \
    "v_cvt_pk_bf16_f32 v204, |v204|, |v205|\n\t" \
    "v_mov_b32_dpp v233, v232 quad_perm:[1,0,3,2] row_mask:0xf bank_mask:0xf\n\t" \
    "v_mov_b32_dpp v235, v234 quad_perm:[1,0,3,2] row_mask:0xf bank_mask:0xf\n\t" \
    "v_mov_b32_dpp v205, v204 quad_perm:[1,0,3,2] row_mask:0xf bank_mask:0xf\n\t" \
    "v_perm_b32 v232, v232, v233, v202\n\t" \
    "v_perm_b32 v234, v234, v235, v202\n\t" \
    "v_perm_b32 v204, v204, v205, v202\n\t" \
    "v_mov_b32_dpp v240, v232 quad_perm:[0,1,0,1] row_mask:0xf bank_mask:0xf\n\t" \
    "v_mov_b32_dpp v241, v234 quad_perm:[0,1,0,1] row_mask:0xf bank_mask:0xf\n\t" \
    "v_mov_b32_dpp v242, v232 quad_perm:[2,3,2,3] row_mask:0xf bank_mask:0xf\n\t" \
    "v_mov_b32_dpp v243, v234 quad_perm:[2,3,2,3] row_mask:0xf bank_mask:0xf\n\t" \
    "buffer_store_dwordx4 v[240:243], v200, %[rr], %[sx] offen sc1\n\t" \
    "v_mov_b32_dpp v244, v204 quad_perm:[0,1,0,1] row_mask:0xf bank_mask:0xf\n\t" \
    "v_mov_b32_dpp v245, v204 quad_perm:[2,3,2,3] row_mask:0xf bank_mask:0xf\n\t" \
    "buffer_store_dwordx4 v[240:243], v200, %[rr], %[sy] offen sc1\n\t" \
    "buffer_store_dwordx2 v[244:245], v201, %[rr], %[sm] offen sc1\n\t" \
    "s_wakeup\n\t" \
    PRISM
#define ASM_P4_0 \
    "ds_read_b128 v[200:203], %[qa] offset:0\n\t" \
    "ds_read_b128 v[204:207], %[qa] offset:1024\n\t" \
    "ds_read_b128 v[208:211], %[qa] offset:2048\n\t" \
    "ds_read_b128 v[212:215], %[qa] offset:3072\n\t" \
    "ds_read_b128 v[216:219], %[qa] offset:4096\n\t" \
    "ds_read_b128 v[220:223], %[qa] offset:5120\n\t" \
    "ds_read_b128 v[224:227], %[qa] offset:12288\n\t" \
    "ds_read_b128 v[228:231], %[qa] offset:13312\n\t" \
    "ds_read_b128 v[232:235], %[qa] offset:14336\n\t" \
    "ds_read_b128 v[236:239], %[qa] offset:15360\n\t" \
    "s_waitcnt lgkmcnt(0)\n\t" \
    "s_barrier\n\t" ASM_ST \
    "v_mfma_f32_16x16x32_bf16 v[240:243], v[224:227], v[200:203], v[240:243]\n\t" \
    "ds_read_b128 v[224:227], %[qa] offset:16384\n\t" \
    "v_mfma_f32_16x16x32_bf16 v[240:243], v[228:231], v[204:207], v[240:243]\n\t" \
    "ds_read_b128 v[228:231], %[qa] offset:17408\n\t" \
    "v_mfma_f32_16x16x32_bf16 v[240:243], v[232:235], v[208:211], v[240:243]\n\t" \
    "ds_read_b128 v[232:235], %[km] offset:0\n\t" \
    "v_mfma_f32_16x16x32_bf16 v[240:243], v[236:239], v[212:215], v[240:243]\n\t" \
    "ds_read_b128 v[236:239], %[km] offset:512\n\t" \
    "s_waitcnt lgkmcnt(3)\n\t" \
    "v_mfma_f32_16x16x32_bf16 v[240:243], v[224:227], v[216:219], v[240:243]\n\t" \
    "ds_read_b128 v[224:227], %[km] offset:1024\n\t" \
    "s_waitcnt lgkmcnt(3)\n\t" \
    "v_mfma_f32_16x16x32_bf16 v[240:243], v[228:231], v[220:223], v[240:243]\n\t" \
    "ds_read_b128 v[228:231], %[km] offset:1536\n\t" \
    "s_waitcnt lgkmcnt(3)\n\t" \
    "v_mfma_f32_16x16x32_bf16 v[248:251], v[232:235], v[200:203], 0\n\t" \
    "ds_read_b128 v[232:235], %[km] offset:2048\n\t" \
    "s_waitcnt lgkmcnt(3)\n\t" \
    "v_mfma_f32_16x16x32_bf16 v[248:251], v[236:239], v[204:207], v[248:251]\n\t" \
    "ds_read_b128 v[236:239], %[km] offset:2560\n\t" \
    "s_waitcnt lgkmcnt(3)\n\t" \
    "v_mfma_f32_16x16x32_bf16 v[248:251], v[224:227], v[208:211], v[248:251]\n\t" \
    "s_waitcnt lgkmcnt(2)\n\t" \
    "v_mfma_f32_16x16x32_bf16 v[248:251], v[228:231], v[212:215], v[248:251]\n\t" \
    "s_waitcnt lgkmcnt(1)\n\t" \
    "v_mfma_f32_16x16x32_bf16 v[248:251], v[232:235], v[216:219], v[248:251]\n\t" \
    "s_waitcnt lgkmcnt(0)\n\t" \
    "v_mfma_f32_16x16x32_bf16 v[248:251], v[236:239], v[220:223], v[248:251]\n\t" \
    "v_max3_f32 v224, v240, v241, v242\n\t" \
    "v_mov_b32 v225, v243\n\t" \
    "v_lshlrev_b32 v201, 23, %[qa]\n\t" \
    "v_and_b32 v201, 0x80000000, v201\n\t" \
    "v_and_b32 v200, 0x3ff, %[qa]\n\t" \
    "v_or_b32 v200, v201, v200\n\t" \
    "s_nop 1\n\t" \
    "v_max3_f32 v224, v224, v248, v249\n\t" \
    "v_max3_f32 v225, v225, v250, v251\n\t" \
    "v_max_f32 v224, v224, v225\n\t" \
    "v_mov_b32 v225, v224\n\t" \
    "s_nop 1\n\t" \
    "v_permlane16_swap_b32 v224, v225\n\t" \
    "v_max_f32 v224, v224, v225\n\t" \
    "v_mov_b32 v225, v224\n\t" \
    "s_nop 1\n\t" \
    "v_permlane32_swap_b32 v224, v225\n\t" \
    "v_max_f32 v224, v224, v225\n\t" \
    "v_mul_f32 v224, %[scl], v224\n\t" \
    "v_fma_f32 v240, v240, %[scl], -v224\n\t" \
    "v_fma_f32 v241, v241, %[scl], -v224\n\t" \
    "v_exp_f32 v240, v240\n\t" \
    "v_fma_f32 v242, v242, %[scl], -v224\n\t" \
    "v_exp_f32 v241, v241\n\t" \
    "v_fma_f32 v243, v243, %[scl], -v224\n\t" \
    "v_exp_f32 v242, v242\n\t" \
    "v_fma_f32 v248, v248, %[scl], -v224\n\t" \
    "v_exp_f32 v243, v243\n\t" \
    "v_fma_f32 v249, v249, %[scl], -v224\n\t" \
    "v_exp_f32 v248, v248\n\t" \
    "v_fma_f32 v250, v250, %[scl], -v224\n\t" \
    "v_exp_f32 v249, v249\n\t" \
    "v_fma_f32 v251, v251, %[scl], -v224\n\t" \
    "v_exp_f32 v250, v250\n\t" \
    "v_exp_f32 v251, v251\n\t" \
    "v_add_f32 v224, v240, v241\n\t" \
    "v_add_f32 v225, v242, v243\n\t" \
    "v_add_f32 v232, v248, v249\n\t" \
    "v_add_f32 v233, v250, v251\n\t" \
    "v_add_f32 v224, v224, v225\n\t" \
    "v_add_f32 v232, v232, v233\n\t" \
    "v_fma_f32 v224, v232, 0.5, v224\n\t" \
    "v_mov_b32 v225, v224\n\t" \
    "s_nop 1\n\t" \
    "v_permlane16_swap_b32 v224, v225\n\t" \
    "v_add_f32 v224, v224, v225\n\t" \
    "v_mov_b32 v225, v224\n\t" \
    "s_nop 1\n\t" \
    "v_permlane32_swap_b32 v224, v225\n\t" \
    "v_add_f32 v224, v224, v225\n\t" \
    "v_rcp_f32 v224, v224\n\t" \
    "s_nop 0\n\t" \
    "v_mul_f32 v240, v240, v224\n\t" \
    "v_mul_f32 v241, v241, v224\n\t" \
    "v_mul_f32 v242, v242, v224\n\t" \
    "v_mul_f32 v243, v243, v224\n\t" \
    "v_mul_f32 v248, v248, v224\n\t" \
    "v_mul_f32 v249, v249, v224\n\t" \
    "v_mul_f32 v250, v250, v224\n\t" \
    "v_mul_f32 v251, v251, v224\n\t" \
    "v_cvt_pk_bf16_f32 v228, |v240|, |v241|\n\t" \
    "v_cvt_pk_bf16_f32 v229, |v242|, |v243|\n\t" \
    "v_cvt_pk_bf16_f32 v232, |v248|, |v249|\n\t" \
    "v_cvt_pk_bf16_f32 v233, |v250|, |v251|\n\t" \
    "v_permlane16_swap_b32 v228, v230\n\t" \
    "v_permlane16_swap_b32 v229, v231\n\t" \
    "buffer_store_dwordx4 v[228:231], v200, %[rr], %[so] offen sc1\n\t" \
    "buffer_store_dwordx2 v[232:233], %[pm], %[rr], %[so] offen sc1\n\t" \
    "s_nop 1\n\t"
#define ASM_P4_1 \
    "ds_read_b128 v[200:203], %[qa] offset:0\n\t" \
    "ds_read_b128 v[204:207], %[qa] offset:1024\n\t" \
    "ds_read_b128 v[208:211], %[qa] offset:2048\n\t" \
    "ds_read_b128 v[212:215], %[qa] offset:3072\n\t" \
    "ds_read_b128 v[216:219], %[qa] offset:4096\n\t" \
    "ds_read_b128 v[220:223], %[qa] offset:5120\n\t" \
    "ds_read_b128 v[224:227], %[qa] offset:12288\n\t" \
    "ds_read_b128 v[228:231], %[qa] offset:13312\n\t" \
    "ds_read_b128 v[232:235], %[qa] offset:14336\n\t" \
    "ds_read_b128 v[236:239], %[qa] offset:15360\n\t" \
    "s_waitcnt lgkmcnt(0)\n\t" \
    "s_barrier\n\t" ASM_ST \
    "v_mfma_f32_16x16x32_bf16 v[240:243], v[224:227], v[200:203], v[240:243]\n\t" \
    "ds_read_b128 v[224:227], %[qa] offset:16384\n\t" \
    "v_mfma_f32_16x16x32_bf16 v[240:243], v[228:231], v[204:207], v[240:243]\n\t" \
    "ds_read_b128 v[228:231], %[qa] offset:17408\n\t" \
    "v_mfma_f32_16x16x32_bf16 v[240:243], v[232:235], v[208:211], v[240:243]\n\t" \
    "ds_read_b128 v[232:235], %[qa] offset:6144\n\t" \
    "v_mfma_f32_16x16x32_bf16 v[240:243], v[236:239], v[212:215], v[240:243]\n\t" \
    "ds_read_b128 v[236:239], %[qa] offset:7168\n\t" \
    "s_waitcnt lgkmcnt(3)\n\t" \
    "v_mfma_f32_16x16x32_bf16 v[240:243], v[224:227], v[216:219], v[240:243]\n\t" \
    "ds_read_b128 v[224:227], %[qa] offset:8192\n\t" \
    "s_waitcnt lgkmcnt(3)\n\t" \
    "v_mfma_f32_16x16x32_bf16 v[240:243], v[228:231], v[220:223], v[240:243]\n\t" \
    "ds_read_b128 v[228:231], %[qa] offset:9216\n\t" \
    "s_waitcnt lgkmcnt(3)\n\t" \
    "v_mfma_f32_16x16x32_bf16 v[244:247], v[232:235], v[200:203], 0\n\t" \
    "ds_read_b128 v[232:235], %[qa] offset:10240\n\t" \
    "s_waitcnt lgkmcnt(3)\n\t" \
    "v_mfma_f32_16x16x32_bf16 v[244:247], v[236:239], v[204:207], v[244:247]\n\t" \
    "ds_read_b128 v[236:239], %[qa] offset:11264\n\t" \
    "s_waitcnt lgkmcnt(3)\n\t" \
    "v_mfma_f32_16x16x32_bf16 v[244:247], v[224:227], v[208:211], v[244:247]\n\t" \
    "ds_read_b128 v[224:227], %[km] offset:0\n\t" \
    "s_waitcnt lgkmcnt(3)\n\t" \
    "v_mfma_f32_16x16x32_bf16 v[244:247], v[228:231], v[212:215], v[244:247]\n\t" \
    "ds_read_b128 v[228:231], %[km] offset:512\n\t" \
    "s_waitcnt lgkmcnt(3)\n\t" \
    "v_mfma_f32_16x16x32_bf16 v[244:247], v[232:235], v[216:219], v[244:247]\n\t" \
    "ds_read_b128 v[232:235], %[km] offset:1024\n\t" \
    "s_waitcnt lgkmcnt(3)\n\t" \
    "v_mfma_f32_16x16x32_bf16 v[244:247], v[236:239], v[220:223], v[244:247]\n\t" \
    "ds_read_b128 v[236:239], %[km] offset:1536\n\t" \
    "s_waitcnt lgkmcnt(3)\n\t" \
    "v_mfma_f32_16x16x32_bf16 v[248:251], v[224:227], v[200:203], 0\n\t" \
    "ds_read_b128 v[224:227], %[km] offset:2048\n\t" \
    "s_waitcnt lgkmcnt(3)\n\t" \
    "v_mfma_f32_16x16x32_bf16 v[248:251], v[228:231], v[204:207], v[248:251]\n\t" \
    "ds_read_b128 v[228:231], %[km] offset:2560\n\t" \
    "s_waitcnt lgkmcnt(3)\n\t" \
    "v_mfma_f32_16x16x32_bf16 v[248:251], v[232:235], v[208:211], v[248:251]\n\t" \
    "s_waitcnt lgkmcnt(2)\n\t" \
    "v_mfma_f32_16x16x32_bf16 v[248:251], v[236:239], v[212:215], v[248:251]\n\t" \
    "s_waitcnt lgkmcnt(1)\n\t" \
    "v_mfma_f32_16x16x32_bf16 v[248:251], v[224:227], v[216:219], v[248:251]\n\t" \
    "s_waitcnt lgkmcnt(0)\n\t" \
    "v_mfma_f32_16x16x32_bf16 v[248:251], v[228:231], v[220:223], v[248:251]\n\t" \
    "v_max3_f32 v224, v240, v241, v242\n\t" \
    "v_max3_f32 v225, v244, v245, v246\n\t" \
    "v_max3_f32 v224, v224, v243, v247\n\t" \
    "v_and_b32 v200, 0x3ff, %[qa]\n\t" \
    "s_nop 3\n\t" \
    "v_max3_f32 v224, v224, v248, v249\n\t" \
    "v_max3_f32 v225, v225, v250, v251\n\t" \
    "v_max_f32 v224, v224, v225\n\t" \
    "v_mov_b32 v225, v224\n\t" \
    "s_nop 1\n\t" \
    "v_permlane16_swap_b32 v224, v225\n\t" \
    "v_max_f32 v224, v224, v225\n\t" \
    "v_mov_b32 v225, v224\n\t" \
    "s_nop 1\n\t" \
    "v_permlane32_swap_b32 v224, v225\n\t" \
    "v_max_f32 v224, v224, v225\n\t" \
    "v_mul_f32 v224, %[scl], v224\n\t" \
    "v_fma_f32 v240, v240, %[scl], -v224\n\t" \
    "v_fma_f32 v241, v241, %[scl], -v224\n\t" \
    "v_exp_f32 v240, v240\n\t" \
    "v_fma_f32 v242, v242, %[scl], -v224\n\t" \
    "v_exp_f32 v241, v241\n\t" \
    "v_fma_f32 v243, v243, %[scl], -v224\n\t" \
    "v_exp_f32 v242, v242\n\t" \
    "v_fma_f32 v244, v244, %[scl], -v224\n\t" \
    "v_exp_f32 v243, v243\n\t" \
    "v_fma_f32 v245, v245, %[scl], -v224\n\t" \
    "v_exp_f32 v244, v244\n\t" \
    "v_fma_f32 v246, v246, %[scl], -v224\n\t" \
    "v_exp_f32 v245, v245\n\t" \
    "v_fma_f32 v247, v247, %[scl], -v224\n\t" \
    "v_exp_f32 v246, v246\n\t" \
    "v_fma_f32 v248, v248, %[scl], -v224\n\t" \
    "v_exp_f32 v247, v247\n\t" \
    "v_fma_f32 v249, v249, %[scl], -v224\n\t" \
    "v_exp_f32 v248, v248\n\t" \
    "v_fma_f32 v250, v250, %[scl], -v224\n\t" \
    "v_exp_f32 v249, v249\n\t" \
    "v_fma_f32 v251, v251, %[scl], -v224\n\t" \
    "v_exp_f32 v250, v250\n\t" \
    "v_exp_f32 v251, v251\n\t" \
    "v_add_f32 v224, v240, v241\n\t" \
    "v_add_f32 v225, v242, v243\n\t" \
    "v_add_f32 v226, v244, v245\n\t" \
    "v_add_f32 v227, v246, v247\n\t" \
    "v_add_f32 v232, v248, v249\n\t" \
    "v_add_f32 v233, v250, v251\n\t" \
    "v_add_f32 v224, v224, v225\n\t" \
    "v_add_f32 v226, v226, v227\n\t" \
    "v_add_f32 v232, v232, v233\n\t" \
    "v_add_f32 v224, v224, v226\n\t" \
    "v_fma_f32 v224, v232, 0.5, v224\n\t" \
    "v_mov_b32 v225, v224\n\t" \
    "s_nop 1\n\t" \
    "v_permlane16_swap_b32 v224, v225\n\t" \
    "v_add_f32 v224, v224, v225\n\t" \
    "v_mov_b32 v225, v224\n\t" \
    "s_nop 1\n\t" \
    "v_permlane32_swap_b32 v224, v225\n\t" \
    "v_add_f32 v224, v224, v225\n\t" \
    "v_rcp_f32 v224, v224\n\t" \
    "s_nop 0\n\t" \
    "v_mul_f32 v240, v240, v224\n\t" \
    "v_mul_f32 v241, v241, v224\n\t" \
    "v_mul_f32 v242, v242, v224\n\t" \
    "v_mul_f32 v243, v243, v224\n\t" \
    "v_mul_f32 v244, v244, v224\n\t" \
    "v_mul_f32 v245, v245, v224\n\t" \
    "v_mul_f32 v246, v246, v224\n\t" \
    "v_mul_f32 v247, v247, v224\n\t" \
    "v_mul_f32 v248, v248, v224\n\t" \
    "v_mul_f32 v249, v249, v224\n\t" \
    "v_mul_f32 v250, v250, v224\n\t" \
    "v_mul_f32 v251, v251, v224\n\t" \
    "v_cvt_pk_bf16_f32 v230, |v240|, |v241|\n\t" \
    "v_cvt_pk_bf16_f32 v231, |v242|, |v243|\n\t" \
    "v_cvt_pk_bf16_f32 v228, |v244|, |v245|\n\t" \
    "v_cvt_pk_bf16_f32 v229, |v246|, |v247|\n\t" \
    "v_cvt_pk_bf16_f32 v232, |v248|, |v249|\n\t" \
    "v_cvt_pk_bf16_f32 v233, |v250|, |v251|\n\t" \
    "buffer_store_dwordx4 v[228:231], v200, %[rr], %[so] offen sc1\n\t" \
    "buffer_store_dwordx2 v[232:233], %[pm], %[rr], %[so] offen sc1\n\t" \
    "s_nop 1\n\t"
#define ASM_RCRIT \
    "v_mov_b64 v[176:177], -1\n\t" \
    "v_mov_b64 v[178:179], -1\n\t" \
    "v_mov_b64 v[180:181], -1\n\t" \
    "v_mov_b64 v[182:183], -1\n\t" \
    "v_mov_b64 v[184:185], -1\n\t" \
    "v_mov_b64 v[186:187], -1\n\t" \
    "v_mov_b64 v[188:189], -1\n\t" \
    "v_mov_b64 v[190:191], -1\n\t" \
    "v_mov_b64 v[192:193], -1\n\t" \
    "v_mov_b64 v[194:195], -1\n\t" \
    ".Lfa%=:\n\t" \
    "s_mov_b32 %[a], 0\n\t" \
    "v_max_u32 v196, v176, v177\n\t" \
    "v_max3_u32 v196, v178, v179, v196\n\t" \
    "v_cmp_eq_u32 vcc, -1, v196\n\t" \
    "s_cbranch_vccz .Lga0_%=\n\t" \
    "buffer_load_dwordx4 v[176:179], %[ov], %[rs], %[sv] offen sc1\n\t" \
    "s_mov_b32 %[a], 1\n\t" \
    ".Lga0_%=:\n\t" \
    "v_max_u32 v196, v180, v181\n\t" \
    "v_max3_u32 v196, v182, v183, v196\n\t" \
    "v_cmp_eq_u32 vcc, -1, v196\n\t" \
    "s_cbranch_vccz .Lga1_%=\n\t" \
    "buffer_load_dwordx4 v[180:183], %[ov], %[rs], %[sv] offen offset:1024 sc1\n\t" \
    "s_mov_b32 %[a], 1\n\t" \
    ".Lga1_%=:\n\t" \
    "s_cmp_eq_u32 %[a], 0\n\t" \
    "s_cbranch_scc1 .Lha%=\n\t" \
    "s_waitcnt vmcnt(12)\n\t" \
    ".Lia%=:\n\t" \
    "v_max_u32 v196, v176, v177\n\t" \
    "v_max3_u32 v196, v178, v179, v196\n\t" \
    "v_max3_u32 v196, v180, v181, v196\n\t" \
    "v_max3_u32 v196, v182, v183, v196\n\t" \
    "v_cmp_eq_u32 vcc, -1, v196\n\t" \
    "s_cbranch_vccz .Lha%=\n\t" \
    "s_nop 7\n\t" \
    "v_max_u32 v196, v176, v177\n\t" \
    "v_max3_u32 v196, v178, v179, v196\n\t" \
    "v_max3_u32 v196, v180, v181, v196\n\t" \
    "v_max3_u32 v196, v182, v183, v196\n\t" \
    "v_cmp_eq_u32 vcc, -1, v196\n\t" \
    "s_cbranch_vccz .Lha%=\n\t" \
    "s_nop 7\n\t" \
    "v_max_u32 v196, v176, v177\n\t" \
    "v_max3_u32 v196, v178, v179, v196\n\t" \
    "v_max3_u32 v196, v180, v181, v196\n\t" \
    "v_max3_u32 v196, v182, v183, v196\n\t" \
    "v_cmp_eq_u32 vcc, -1, v196\n\t" \
    "s_cbranch_vccz .Lha%=\n\t" \
    "s_nop 7\n\t" \
    "v_max_u32 v196, v176, v177\n\t" \
    "v_max3_u32 v196, v178, v179, v196\n\t" \
    "v_max3_u32 v196, v180, v181, v196\n\t" \
    "v_max3_u32 v196, v182, v183, v196\n\t" \
    "v_cmp_eq_u32 vcc, -1, v196\n\t" \
    "s_cbranch_vccz .Lha%=\n\t" \
    "s_nop 7\n\t" \
    "s_sub_u32 %[n], %[n], 1\n\t" \
    "s_cmp_gt_i32 %[n], 0\n\t" \
    "s_cbranch_scc1 .Lfa%=\n\t" \
    ".Lha%=:\n\t" \
    ASM_ST3 \
    "buffer_load_dwordx4 v[184:187], %[o0], %[rs], %[sp] offen sc1\n\t" \
    "buffer_load_dwordx4 v[188:191], %[o0], %[rs], %[sp] offen offset:1024 sc1\n\t" \
    "buffer_load_dwordx4 v[192:195], %[o2], %[rs], %[sp] offen sc1\n\t" \
    "s_waitcnt vmcnt(0)\n\t" \
    "v_max_u32 v196, v184, v185\n\t" \
    "v_max3_u32 v196, v186, v187, v196\n\t" \
    "v_max3_u32 v196, v188, v189, v196\n\t" \
    "v_max3_u32 v196, v190, v191, v196\n\t" \
    "v_max3_u32 v196, v192, v193, v196\n\t" \
    "v_max3_u32 v196, v194, v195, v196\n\t" \
    "v_cmp_eq_u32 vcc, -1, v196\n\t" \
    "s_cbranch_vccz .Lhb%=\n\t" \
    ".Lfb%=:\n\t" \
    "s_mov_b32 %[a], 0\n\t" \
    "v_max_u32 v196, v184, v185\n\t" \
    "v_max3_u32 v196, v186, v187, v196\n\t" \
    "v_cmp_eq_u32 vcc, -1, v196\n\t" \
    "s_cbranch_vccz .Lgb0_%=\n\t" \
    "buffer_load_dwordx4 v[184:187], %[o0], %[rs], %[sp] offen sc1\n\t" \
    "s_mov_b32 %[a], 1\n\t" \
    ".Lgb0_%=:\n\t" \
    "v_max_u32 v196, v188, v189\n\t" \
    "v_max3_u32 v196, v190, v191, v196\n\t" \
    "v_cmp_eq_u32 vcc, -1, v196\n\t" \
    "s_cbranch_vccz .Lgb1_%=\n\t" \
    "buffer_load_dwordx4 v[188:191], %[o0], %[rs], %[sp] offen offset:1024 sc1\n\t" \
    "s_mov_b32 %[a], 1\n\t" \
    ".Lgb1_%=:\n\t" \
    "v_max_u32 v196, v192, v193\n\t" \
    "v_max3_u32 v196, v194, v195, v196\n\t" \
    "v_cmp_eq_u32 vcc, -1, v196\n\t" \
    "s_cbranch_vccz .Lgb2_%=\n\t" \
    "buffer_load_dwordx4 v[192:195], %[o2], %[rs], %[sp] offen sc1\n\t" \
    "s_mov_b32 %[a], 1\n\t" \
    ".Lgb2_%=:\n\t" \
    "s_cmp_eq_u32 %[a], 0\n\t" \
    "s_cbranch_scc1 .Lhb%=\n\t" \
    "s_waitcnt vmcnt(12)\n\t" \
    ".Lib%=:\n\t" \
    "v_max_u32 v196, v184, v185\n\t" \
    "v_max3_u32 v196, v186, v187, v196\n\t" \
    "v_max3_u32 v196, v188, v189, v196\n\t" \
    "v_max3_u32 v196, v190, v191, v196\n\t" \
    "v_max3_u32 v196, v192, v193, v196\n\t" \
    "v_max3_u32 v196, v194, v195, v196\n\t" \
    "v_cmp_eq_u32 vcc, -1, v196\n\t" \
    "s_cbranch_vccz .Lhb%=\n\t" \
    "s_nop 7\n\t" \
    "v_max_u32 v196, v184, v185\n\t" \
    "v_max3_u32 v196, v186, v187, v196\n\t" \
    "v_max3_u32 v196, v188, v189, v196\n\t" \
    "v_max3_u32 v196, v190, v191, v196\n\t" \
    "v_max3_u32 v196, v192, v193, v196\n\t" \
    "v_max3_u32 v196, v194, v195, v196\n\t" \
    "v_cmp_eq_u32 vcc, -1, v196\n\t" \
    "s_cbranch_vccz .Lhb%=\n\t" \
    "s_nop 7\n\t" \
    "v_max_u32 v196, v184, v185\n\t" \
    "v_max3_u32 v196, v186, v187, v196\n\t" \
    "v_max3_u32 v196, v188, v189, v196\n\t" \
    "v_max3_u32 v196, v190, v191, v196\n\t" \
    "v_max3_u32 v196, v192, v193, v196\n\t" \
    "v_max3_u32 v196, v194, v195, v196\n\t" \
    "v_cmp_eq_u32 vcc, -1, v196\n\t" \
    "s_cbranch_vccz .Lhb%=\n\t" \
    "s_nop 7\n\t" \
    "v_max_u32 v196, v184, v185\n\t" \
    "v_max3_u32 v196, v186, v187, v196\n\t" \
    "v_max3_u32 v196, v188, v189, v196\n\t" \
    "v_max3_u32 v196, v190, v191, v196\n\t" \
    "v_max3_u32 v196, v192, v193, v196\n\t" \
    "v_max3_u32 v196, v194, v195, v196\n\t" \
    "v_cmp_eq_u32 vcc, -1, v196\n\t" \
    "s_cbranch_vccz .Lhb%=\n\t" \
    "s_nop 7\n\t" \
    "s_sub_u32 %[n], %[n], 1\n\t" \
    "s_cmp_gt_i32 %[n], 0\n\t" \
    "s_cbranch_scc1 .Lfb%=\n\t" \
    ".Lhb%=:\n\t" \
    ASM_ST2 \
    "v_mfma_f32_16x16x32_bf16 v[160:163], v[192:195], v[176:179], 0\n\t" \
    "v_mfma_f32_16x16x32_bf16 v[164:167], v[192:195], v[180:183], 0\n\t" \
    "v_smfmac_f32_16x16x64_bf16 v[160:163], v[184:187], v[128:135], %[sidx]\n\t" \
    "v_smfmac_f32_16x16x64_bf16 v[164:167], v[184:187], v[144:151], %[sidx]\n\t" \
    "v_smfmac_f32_16x16x64_bf16 v[160:163], v[188:191], v[136:143], %[sidx]\n\t" \
    "v_smfmac_f32_16x16x64_bf16 v[164:167], v[188:191], v[152:159], %[sidx]\n\t" \
    "s_cmp_eq_u32 %[pub], 0\n\t" \
    "s_nop 7\n\t" \
    "v_permlane32_swap_b32 v160, v164\n\t" \
    "v_permlane32_swap_b32 v161, v165\n\t" \
    "v_permlane32_swap_b32 v162, v166\n\t" \
    "v_permlane32_swap_b32 v163, v167\n\t" \
    "v_add_f32 v160, v160, v164\n\t" \
    "v_add_f32 v161, v161, v165\n\t" \
    "ds_write2_b32 %[rw], v160, v161 offset1:80\n\t" \
    "v_add_f32 v162, v162, v166\n\t" \
    "v_add_f32 v163, v163, v167\n\t" \
    "ds_write2_b32 %[rw], v162, v163 offset0:160 offset1:240\n\t" \
    "s_waitcnt lgkmcnt(0)\n\t" \
    "s_barrier\n\t" ASM_ST \
    "s_cbranch_scc1 .Lre%=\n\t" \
    "ds_read_b128 v[200:203], %[rd] offset:0\n\t" \
    "ds_read_b128 v[204:207], %[rd] offset:16\n\t" \
    "s_waitcnt lgkmcnt(1)\n\t" \
    "v_pk_add_f32 v[200:201], v[200:201], v[202:203]\n\t" \
    "s_waitcnt lgkmcnt(0)\n\t" \
    "v_pk_add_f32 v[204:205], v[204:205], v[206:207]\n\t" \
    "v_pk_add_f32 v[200:201], v[200:201], v[204:205]\n\t" \
    "s_nop 1\n\t" \
    "v_permlane32_swap_b32 v200, v201\n\t" \
    "v_add_f32 v202, v200, v201\n\t" \
    "v_add_f32 v203, v200, v201\n\t" \
    "s_nop 1\n\t" \
    "v_permlane32_swap_b32 v202, v203\n\t" \
    "v_cvt_pk_bf16_f32 v200, v202, v203\n\t" \
    "v_min_u32 v200, -2, v200\n\t" \
    "buffer_store_dword v200, %[oo], %[rs], %[so] offen sc1\n\t" \
    ".Lre%=:\n\t"
#define ASM_RX \
    "s_cmp_eq_u32 %[fin], 0\n\t" \
    "s_cbranch_scc1 .Lx0%=\n\t" \
    "buffer_load_dwordx2 v[250:251], %[xo], %[rx], %[sx] offen sc1\n\t" \
    ".Lx0%=:\n\t" \
    "buffer_load_dword v248, %[fo], %[rs], %[fs] offen sc1\n\t" \
    "buffer_load_dwordx4 v[200:203], %[l16], %[rs], %[s0] offen offset:0 sc1\n\t" \
    "buffer_load_dwordx4 v[204:207], %[l16], %[rs], %[s0] offen offset:1024 sc1\n\t" \
    "buffer_load_dwordx4 v[208:211], %[l16], %[rs], %[s0] offen offset:2048 sc1\n\t" \
    "buffer_load_dwordx4 v[212:215], %[l16], %[rs], %[s0] offen offset:3072 sc1\n\t" \
    "buffer_load_dwordx4 v[216:219], %[l16], %[rs], %[s1] offen offset:0 sc1\n\t" \
    "buffer_load_dwordx4 v[220:223], %[l16], %[rs], %[s1] offen offset:1024 sc1\n\t" \
    "buffer_load_dwordx4 v[224:227], %[l16], %[rs], %[s1] offen offset:2048 sc1\n\t" \
    "buffer_load_dwordx4 v[228:231], %[l16], %[rs], %[s1] offen offset:3072 sc1\n\t" \
    "v_mov_b64 v[96:97], v[128:129]\n\t" \
    "v_mov_b64 v[98:99], v[132:133]\n\t" \
    "v_mov_b64 v[100:101], v[136:137]\n\t" \
    "v_mov_b64 v[102:103], v[140:141]\n\t" \
    "v_mov_b64 v[104:105], v[144:145]\n\t" \
    "v_mov_b64 v[106:107], v[148:149]\n\t" \
    "v_mov_b64 v[108:109], v[152:153]\n\t" \
    "v_mov_b64 v[110:111], v[156:157]\n\t" \
    "s_movk_i32 %[n], 0x200\n\t" \
    "s_nop 1\n\t" \
    "v_permlane16_swap_b32 v96, v98\n\t" \
    "v_permlane16_swap_b32 v97, v99\n\t" \
    "v_permlane16_swap_b32 v100, v102\n\t" \
    "v_permlane16_swap_b32 v101, v103\n\t" \
    "v_permlane16_swap_b32 v104, v106\n\t" \
    "v_permlane16_swap_b32 v105, v107\n\t" \
    "v_permlane16_swap_b32 v108, v110\n\t" \
    "v_permlane16_swap_b32 v109, v111\n\t" \
    "s_waitcnt vmcnt(7)\n\t" \
    "v_max_u32 v196, v200, v201\n\t" \
    "v_max3_u32 v196, v202, v203, v196\n\t" \
    "s_waitcnt vmcnt(6)\n\t" \
    "v_max3_u32 v196, v204, v205, v196\n\t" \
    "v_max3_u32 v196, v206, v207, v196\n\t" \
    "s_waitcnt vmcnt(5)\n\t" \
    "v_max3_u32 v196, v208, v209, v196\n\t" \
    "v_max3_u32 v196, v210, v211, v196\n\t" \
    "s_waitcnt vmcnt(4)\n\t" \
    "v_max3_u32 v196, v212, v213, v196\n\t" \
    "v_max3_u32 v196, v214, v215, v196\n\t" \
    "s_waitcnt vmcnt(3)\n\t" \
    "v_max3_u32 v196, v216, v217, v196\n\t" \
    "v_max3_u32 v196, v218, v219, v196\n\t" \
    "s_waitcnt vmcnt(2)\n\t" \
    "v_max3_u32 v196, v220, v221, v196\n\t" \
    "v_max3_u32 v196, v222, v223, v196\n\t" \
    "s_waitcnt vmcnt(1)\n\t" \
    "v_max3_u32 v196, v224, v225, v196\n\t" \
    "v_max3_u32 v196, v226, v227, v196\n\t" \
    "s_waitcnt vmcnt(0)\n\t" \
    "v_max3_u32 v196, v228, v229, v196\n\t" \
    "v_max3_u32 v196, v230, v231, v196\n\t" \
    "v_cmp_eq_u32 vcc, -1, v196\n\t" \
    "s_cbranch_vccz .Lxd%=\n\t" \
    ".Lxl%=:\n\t" \
    "s_mov_b32 %[a], 0\n\t" \
    "v_max_u32 v196, v200, v201\n\t" \
    "v_max3_u32 v196, v202, v203, v196\n\t" \
    "v_cmp_eq_u32 vcc, -1, v196\n\t" \
    "s_cbranch_vccz .Lxs0_%=\n\t" \
    "buffer_load_dwordx4 v[200:203], %[l16], %[rs], %[s0] offen offset:0 sc1\n\t" \
    "s_mov_b32 %[a], 1\n\t" \
    ".Lxs0_%=:\n\t" \
    "v_max_u32 v196, v204, v205\n\t" \
    "v_max3_u32 v196, v206, v207, v196\n\t" \
    "v_cmp_eq_u32 vcc, -1, v196\n\t" \
    "s_cbranch_vccz .Lxs1_%=\n\t" \
    "buffer_load_dwordx4 v[204:207], %[l16], %[rs], %[s0] offen offset:1024 sc1\n\t" \
    "s_mov_b32 %[a], 1\n\t" \
    ".Lxs1_%=:\n\t" \
    "v_max_u32 v196, v208, v209\n\t" \
    "v_max3_u32 v196, v210, v211, v196\n\t" \
    "v_cmp_eq_u32 vcc, -1, v196\n\t" \
    "s_cbranch_vccz .Lxs2_%=\n\t" \
    "buffer_load_dwordx4 v[208:211], %[l16], %[rs], %[s0] offen offset:2048 sc1\n\t" \
    "s_mov_b32 %[a], 1\n\t" \
    ".Lxs2_%=:\n\t" \
    "v_max_u32 v196, v212, v213\n\t" \
    "v_max3_u32 v196, v214, v215, v196\n\t" \
    "v_cmp_eq_u32 vcc, -1, v196\n\t" \
    "s_cbranch_vccz .Lxs3_%=\n\t" \
    "buffer_load_dwordx4 v[212:215], %[l16], %[rs], %[s0] offen offset:3072 sc1\n\t" \
    "s_mov_b32 %[a], 1\n\t" \
    ".Lxs3_%=:\n\t" \
    "v_max_u32 v196, v216, v217\n\t" \
    "v_max3_u32 v196, v218, v219, v196\n\t" \
    "v_cmp_eq_u32 vcc, -1, v196\n\t" \
    "s_cbranch_vccz .Lxs4_%=\n\t" \
    "buffer_load_dwordx4 v[216:219], %[l16], %[rs], %[s1] offen offset:0 sc1\n\t" \
    "s_mov_b32 %[a], 1\n\t" \
    ".Lxs4_%=:\n\t" \
    "v_max_u32 v196, v220, v221\n\t" \
    "v_max3_u32 v196, v222, v223, v196\n\t" \
    "v_cmp_eq_u32 vcc, -1, v196\n\t" \
    "s_cbranch_vccz .Lxs5_%=\n\t" \
    "buffer_load_dwordx4 v[220:223], %[l16], %[rs], %[s1] offen offset:1024 sc1\n\t" \
    "s_mov_b32 %[a], 1\n\t" \
    ".Lxs5_%=:\n\t" \
    "v_max_u32 v196, v224, v225\n\t" \
    "v_max3_u32 v196, v226, v227, v196\n\t" \
    "v_cmp_eq_u32 vcc, -1, v196\n\t" \
    "s_cbranch_vccz .Lxs6_%=\n\t" \
    "buffer_load_dwordx4 v[224:227], %[l16], %[rs], %[s1] offen offset:2048 sc1\n\t" \
    "s_mov_b32 %[a], 1\n\t" \
    ".Lxs6_%=:\n\t" \
    "v_max_u32 v196, v228, v229\n\t" \
    "v_max3_u32 v196, v230, v231, v196\n\t" \
    "v_cmp_eq_u32 vcc, -1, v196\n\t" \
    "s_cbranch_vccz .Lxs7_%=\n\t" \
    "buffer_load_dwordx4 v[228:231], %[l16], %[rs], %[s1] offen offset:3072 sc1\n\t" \
    "s_mov_b32 %[a], 1\n\t" \
    ".Lxs7_%=:\n\t" \
    "s_cmp_eq_u32 %[a], 0\n\t" \
    "s_cbranch_scc1 .Lxd%=\n\t" \
    "s_waitcnt vmcnt(0)\n\t" \
    "s_sub_u32 %[n], %[n], 1\n\t" \
    "s_cmp_lg_u32 %[n], 0\n\t" \
    "s_cbranch_scc1 .Lxl%=\n\t" \
    "s_branch .Lxe%=\n\t" \
    ".Lxd%=:\n\t" \
    ASM_ST2 \
    "v_mfma_f32_16x16x32_bf16 v[120:123], v[128:131], v[200:203], 0\n\t" \
    "v_mfma_f32_16x16x32_bf16 v[124:127], v[144:147], v[200:203], 0\n\t" \
    "v_mfma_f32_16x16x32_bf16 v[120:123], v[132:135], v[204:207], v[120:123]\n\t" \
    "v_mfma_f32_16x16x32_bf16 v[124:127], v[148:151], v[204:207], v[124:127]\n\t" \
    "v_mfma_f32_16x16x32_bf16 v[120:123], v[136:139], v[208:211], v[120:123]\n\t" \
    "v_mfma_f32_16x16x32_bf16 v[124:127], v[152:155], v[208:211], v[124:127]\n\t" \
    "v_mfma_f32_16x16x32_bf16 v[120:123], v[140:143], v[212:215], v[120:123]\n\t" \
    "v_mfma_f32_16x16x32_bf16 v[124:127], v[156:159], v[212:215], v[124:127]\n\t" \
    "v_mfma_f32_16x16x32_bf16 v[112:115], v[96:99], v[220:223], 0\n\t" \
    "v_mfma_f32_16x16x32_bf16 v[116:119], v[104:107], v[220:223], 0\n\t" \
    "v_mfma_f32_16x16x32_bf16 v[120:123], v[176:179], v[216:219], v[120:123]\n\t" \
    "v_mfma_f32_16x16x32_bf16 v[124:127], v[180:183], v[216:219], v[124:127]\n\t" \
    "v_mfma_f32_16x16x32_bf16 v[112:115], v[100:103], v[224:227], v[112:115]\n\t" \
    "v_mfma_f32_16x16x32_bf16 v[116:119], v[108:111], v[224:227], v[116:119]\n\t" \
    "v_mfma_f32_16x16x32_bf16 v[112:115], v[176:179], v[228:231], v[112:115]\n\t" \
    "v_mfma_f32_16x16x32_bf16 v[116:119], v[180:183], v[228:231], v[116:119]\n\t" \
    "s_nop 1\n\t" \
    "ds_write_b128 %[pw], v[120:123] offset:16384\n\t" \
    "ds_write_b128 %[pw], v[124:127] offset:24576\n\t" \
    "s_nop 3\n\t" \
    "ds_write_b128 %[pw], v[112:115] offset:0\n\t" \
    "ds_write_b128 %[pw], v[116:119] offset:8192\n\t" \
    "v_cmp_ge_u32 vcc, %[tn], v248\n\t" \
    "s_mov_b32 %[a], 0\n\t" \
    "s_cbranch_vccnz .Lxv%=\n\t" \
    "buffer_load_dwordx4 v[128:131], %[l16], %[rp], %[sv0] offen offset:0 sc1\n\t" \
    "buffer_load_dwordx4 v[132:135], %[l16], %[rp], %[sv0] offen offset:1024 sc1\n\t" \
    "buffer_load_dwordx4 v[136:139], %[l16], %[rp], %[sv0] offen offset:2048 sc1\n\t" \
    "buffer_load_dwordx4 v[140:143], %[l16], %[rp], %[sv0] offen offset:3072 sc1\n\t" \
    "buffer_load_dwordx4 v[144:147], %[l16], %[rp], %[sv1] offen offset:0 sc1\n\t" \
    "buffer_load_dwordx4 v[148:151], %[l16], %[rp], %[sv1] offen offset:1024 sc1\n\t" \
    "buffer_load_dwordx4 v[152:155], %[l16], %[rp], %[sv1] offen offset:2048 sc1\n\t" \
    "buffer_load_dwordx4 v[156:159], %[l16], %[rp], %[sv1] offen offset:3072 sc1\n\t" \
    "s_mov_b32 %[a], 1\n\t" \
    ".Lxv%=:\n\t" \
    "s_cmp_eq_u32 %[fin], 0\n\t" \
    "s_waitcnt lgkmcnt(0)\n\t" \
    "s_barrier\n\t" ASM_ST \
    "s_cbranch_scc1 .Lxe%=\n\t" \
    "ds_read_b128 v[200:203], %[pr] offset:0\n\t" \
    "ds_read_b128 v[204:207], %[pr] offset:1024\n\t" \
    "ds_read_b128 v[208:211], %[pr] offset:2048\n\t" \
    "ds_read_b128 v[212:215], %[pr] offset:3072\n\t" \
    "ds_read_b128 v[216:219], %[pr] offset:4096\n\t" \
    "ds_read_b128 v[220:223], %[pr] offset:5120\n\t" \
    "ds_read_b128 v[224:227], %[pr] offset:6144\n\t" \
    "ds_read_b128 v[228:231], %[pr] offset:7168\n\t" \
    "v_lshlrev_b32 v240, 16, v250\n\t" \
    "v_and_b32 v241, 0xffff0000, v250\n\t" \
    "v_lshlrev_b32 v242, 16, v251\n\t" \
    "v_and_b32 v243, 0xffff0000, v251\n\t" \
    "s_waitcnt lgkmcnt(6)\n\t" \
    "v_pk_add_f32 v[200:201], v[200:201], v[204:205]\n\t" \
    "v_pk_add_f32 v[202:203], v[202:203], v[206:207]\n\t" \
    "s_waitcnt lgkmcnt(4)\n\t" \
    "v_pk_add_f32 v[208:209], v[208:209], v[212:213]\n\t" \
    "v_pk_add_f32 v[210:211], v[210:211], v[214:215]\n\t" \
    "v_pk_add_f32 v[200:201], v[200:201], v[208:209]\n\t" \
    "v_pk_add_f32 v[202:203], v[202:203], v[210:211]\n\t" \
    "s_waitcnt lgkmcnt(2)\n\t" \
    "v_pk_add_f32 v[216:217], v[216:217], v[220:221]\n\t" \
    "v_pk_add_f32 v[218:219], v[218:219], v[222:223]\n\t" \
    "s_waitcnt lgkmcnt(0)\n\t" \
    "v_pk_add_f32 v[224:225], v[224:225], v[228:229]\n\t" \
    "v_pk_add_f32 v[226:227], v[226:227], v[230:231]\n\t" \
    "v_pk_add_f32 v[216:217], v[216:217], v[224:225]\n\t" \
    "v_pk_add_f32 v[218:219], v[218:219], v[226:227]\n\t" \
    "v_pk_add_f32 v[200:201], v[200:201], v[216:217]\n\t" \
    "v_pk_add_f32 v[202:203], v[202:203], v[218:219]\n\t" \
    "v_pk_add_f32 v[200:201], v[200:201], v[240:241]\n\t" \
    "v_pk_add_f32 v[202:203], v[202:203], v[242:243]\n\t" \
    "v_cvt_pk_bf16_f32 v200, v200, v201\n\t" \
    "v_cvt_pk_bf16_f32 v201, v202, v203\n\t" \
    "v_min_u32 v200, -2, v200\n\t" \
    "v_min_u32 v201, -2, v201\n\t" \
    "buffer_store_dwordx2 v[200:201], %[xo], %[rd], %[sa] offen sc1\n\t" \
    ".Lxe%=:\n\t"
#define ASM_W4_M \
    ".Lc7_%=:\n\t" \
    "s_waitcnt vmcnt(0)\n\t" \
    "v_max_u32 v212, v188, v189\n\t" \
    "v_max3_u32 v212, v190, v191, v212\n\t" \
    "v_cmp_eq_u32 vcc, -1, v212\n\t" \
    "s_cbranch_vccnz .Ls7_%=\n\t" \
    ".Lk7_%=:\n\t" \
    ASM_ST2 \
    "buffer_load_dwordx4 v[184:187], %[l16], %[rs], %[s1] offen offset:2048 sc1\n\t" \
    "buffer_load_dwordx4 v[180:183], %[l16], %[rs], %[s1] offen offset:1024 sc1\n\t" \
    "buffer_load_dwordx4 v[176:179], %[l16], %[rs], %[s1] offen offset:0 sc1\n\t" \
    "buffer_load_dwordx4 v[172:175], %[l16], %[rs], %[s0] offen offset:3072 sc1\n\t" \
    "buffer_load_dwordx4 v[168:171], %[l16], %[rs], %[s0] offen offset:2048 sc1\n\t" \
    "buffer_load_dwordx4 v[164:167], %[l16], %[rs], %[s0] offen offset:1024 sc1\n\t" \
    "buffer_load_dwordx4 v[160:163], %[l16], %[rs], %[s0] offen offset:0 sc1\n\t" \
    "v_mfma_f32_16x16x32_bf16 v[208:211], v[156:159], v[188:191], 0\n\t" \
    "v_mfma_f32_16x16x32_bf16 v[204:207], v[124:127], v[188:191], 0\n\t" \
    "v_mfma_f32_16x16x32_bf16 v[200:203], v[92:95], v[188:191], 0\n\t" \
    "v_mfma_f32_16x16x32_bf16 v[196:199], v[60:63], v[188:191], 0\n\t" \
    "v_mfma_f32_16x16x32_bf16 v[192:195], v[28:31], v[188:191], 0\n\t" \
    ".Lc6_%=:\n\t" \
    "s_waitcnt vmcnt(6)\n\t" \
    "v_max_u32 v212, v184, v185\n\t" \
    "v_max3_u32 v212, v186, v187, v212\n\t" \
    "v_cmp_eq_u32 vcc, -1, v212\n\t" \
    "s_cbranch_vccnz .Ls6_%=\n\t" \
    ".Lk6_%=:\n\t" \
    ".Lc5_%=:\n\t" \
    "s_waitcnt vmcnt(5)\n\t" \
    "v_max_u32 v212, v180, v181\n\t" \
    "v_max3_u32 v212, v182, v183, v212\n\t" \
    "v_cmp_eq_u32 vcc, -1, v212\n\t" \
    "s_cbranch_vccnz .Ls5_%=\n\t" \
    ".Lk5_%=:\n\t" \
    "v_mfma_f32_16x16x32_bf16 v[192:195], v[24:27], v[184:187], v[192:195]\n\t" \
    "v_mfma_f32_16x16x32_bf16 v[192:195], v[20:23], v[180:183], v[192:195]\n\t" \
    "v_mfma_f32_16x16x32_bf16 v[196:199], v[56:59], v[184:187], v[196:199]\n\t" \
    "v_mfma_f32_16x16x32_bf16 v[196:199], v[52:55], v[180:183], v[196:199]\n\t" \
    "v_mfma_f32_16x16x32_bf16 v[200:203], v[88:91], v[184:187], v[200:203]\n\t" \
    "v_mfma_f32_16x16x32_bf16 v[200:203], v[84:87], v[180:183], v[200:203]\n\t" \
    "v_mfma_f32_16x16x32_bf16 v[204:207], v[120:123], v[184:187], v[204:207]\n\t" \
    "v_mfma_f32_16x16x32_bf16 v[204:207], v[116:119], v[180:183], v[204:207]\n\t" \
    "v_mfma_f32_16x16x32_bf16 v[208:211], v[152:155], v[184:187], v[208:211]\n\t" \
    "v_mfma_f32_16x16x32_bf16 v[208:211], v[148:151], v[180:183], v[208:211]\n\t" \
    ".Lc4_%=:\n\t" \
    "s_waitcnt vmcnt(4)\n\t" \
    "v_max_u32 v212, v176, v177\n\t" \
    "v_max3_u32 v212, v178, v179, v212\n\t" \
    "v_cmp_eq_u32 vcc, -1, v212\n\t" \
    "s_cbranch_vccnz .Ls4_%=\n\t" \
    ".Lk4_%=:\n\t" \
    ".Lc3_%=:\n\t" \
    "s_waitcnt vmcnt(3)\n\t" \
    "v_max_u32 v212, v172, v173\n\t" \
    "v_max3_u32 v212, v174, v175, v212\n\t" \
    "v_cmp_eq_u32 vcc, -1, v212\n\t" \
    "s_cbranch_vccnz .Ls3_%=\n\t" \
    ".Lk3_%=:\n\t" \
    "v_mfma_f32_16x16x32_bf16 v[208:211], v[144:147], v[176:179], v[208:211]\n\t" \
    "v_mfma_f32_16x16x32_bf16 v[208:211], v[140:143], v[172:175], v[208:211]\n\t" \
    "v_mfma_f32_16x16x32_bf16 v[204:207], v[112:115], v[176:179], v[204:207]\n\t" \
    "v_mfma_f32_16x16x32_bf16 v[204:207], v[108:111], v[172:175], v[204:207]\n\t" \
    "v_mfma_f32_16x16x32_bf16 v[200:203], v[80:83], v[176:179], v[200:203]\n\t" \
    "v_mfma_f32_16x16x32_bf16 v[200:203], v[76:79], v[172:175], v[200:203]\n\t" \
    "v_mfma_f32_16x16x32_bf16 v[196:199], v[48:51], v[176:179], v[196:199]\n\t" \
    "v_mfma_f32_16x16x32_bf16 v[196:199], v[44:47], v[172:175], v[196:199]\n\t" \
    "v_mfma_f32_16x16x32_bf16 v[192:195], v[16:19], v[176:179], v[192:195]\n\t" \
    "v_mfma_f32_16x16x32_bf16 v[192:195], v[12:15], v[172:175], v[192:195]\n\t" \
    ".Lc2_%=:\n\t" \
    "s_waitcnt vmcnt(2)\n\t" \
    "v_max_u32 v212, v168, v169\n\t" \
    "v_max3_u32 v212, v170, v171, v212\n\t" \
    "v_cmp_eq_u32 vcc, -1, v212\n\t" \
    "s_cbranch_vccnz .Ls2_%=\n\t" \
    ".Lk2_%=:\n\t" \
    "buffer_load_dwordx4 v[188:191], %[l16], %[rs], %[n1] offen offset:3072 sc1\n\t" \
    ".Lc1_%=:\n\t" \
    "s_waitcnt vmcnt(2)\n\t" \
    "v_max_u32 v212, v164, v165\n\t" \
    "v_max3_u32 v212, v166, v167, v212\n\t" \
    "v_cmp_eq_u32 vcc, -1, v212\n\t" \
    "s_cbranch_vccnz .Ls1_%=\n\t" \
    ".Lk1_%=:\n\t" \
    ".Lc0_%=:\n\t" \
    "s_waitcnt vmcnt(1)\n\t" \
    "v_max_u32 v212, v160, v161\n\t" \
    "v_max3_u32 v212, v162, v163, v212\n\t" \
    "v_cmp_eq_u32 vcc, -1, v212\n\t" \
    "s_cbranch_vccnz .Ls0_%=\n\t" \
    ".Lk0_%=:\n\t" \
    "v_mfma_f32_16x16x32_bf16 v[192:195], v[8:11], v[168:171], v[192:195]\n\t" \
    "v_mfma_f32_16x16x32_bf16 v[192:195], v[4:7], v[164:167], v[192:195]\n\t" \
    "v_mfma_f32_16x16x32_bf16 v[192:195], v[0:3], v[160:163], v[192:195]\n\t" \
    "v_mfma_f32_16x16x32_bf16 v[196:199], v[40:43], v[168:171], v[196:199]\n\t" \
    "v_mfma_f32_16x16x32_bf16 v[196:199], v[36:39], v[164:167], v[196:199]\n\t" \
    "v_mfma_f32_16x16x32_bf16 v[196:199], v[32:35], v[160:163], v[196:199]\n\t" \
    "v_mfma_f32_16x16x32_bf16 v[200:203], v[72:75], v[168:171], v[200:203]\n\t" \
    "v_mfma_f32_16x16x32_bf16 v[200:203], v[68:71], v[164:167], v[200:203]\n\t" \
    "v_mfma_f32_16x16x32_bf16 v[200:203], v[64:67], v[160:163], v[200:203]\n\t" \
    "v_mfma_f32_16x16x32_bf16 v[204:207], v[104:107], v[168:171], v[204:207]\n\t" \
    "v_mfma_f32_16x16x32_bf16 v[204:207], v[100:103], v[164:167], v[204:207]\n\t" \
    "v_mfma_f32_16x16x32_bf16 v[204:207], v[96:99], v[160:163], v[204:207]\n\t" \
    "v_mfma_f32_16x16x32_bf16 v[208:211], v[136:139], v[168:171], v[208:211]\n\t" \
    "v_mfma_f32_16x16x32_bf16 v[208:211], v[132:135], v[164:167], v[208:211]\n\t" \
    "v_mfma_f32_16x16x32_bf16 v[208:211], v[128:131], v[160:163], v[208:211]\n\t" \
    "v_mov_b32 v212, %[un]\n\t" \
    "ds_write_b128 %[ld], v[192:195] offset:0\n\t" \
    "ds_write_b128 %[ld], v[196:199] offset:6144\n\t" \
    "ds_write_b128 %[ld], v[200:203] offset:12288\n\t" \
    "ds_write_b128 %[ld], v[204:207] offset:18432\n\t" \
    "s_nop 2\n\t" \
    "ds_write_b128 %[ld], v[208:211] offset:24576\n\t" \
    "ds_write_b32 %[fl], v212\n\t" \
    "s_branch .Lme%=\n\t" \
    ".Ls7_%=:\n\t" \
    "s_cmp_eq_u32 %[n], 0\n\t" \
    "s_cbranch_scc1 .Lk7_%=\n\t" \
    "s_sub_u32 %[n], %[n], 1\n\t" \
    "s_sleep 1\n\t" \
    "buffer_load_dwordx4 v[188:191], %[l16], %[rs], %[s1] offen offset:3072 sc1\n\t" \
    "s_branch .Lc7_%=\n\t" \
    ".Ls6_%=:\n\t" \
    "s_cmp_eq_u32 %[n], 0\n\t" \
    "s_cbranch_scc1 .Lk6_%=\n\t" \
    "s_sub_u32 %[n], %[n], 1\n\t" \
    "buffer_load_dwordx4 v[184:187], %[l16], %[rs], %[s1] offen offset:2048 sc1\n\t" \
    "s_waitcnt vmcnt(0)\n\t" \
    "s_branch .Lc6_%=\n\t" \
    ".Ls5_%=:\n\t" \
    "s_cmp_eq_u32 %[n], 0\n\t" \
    "s_cbranch_scc1 .Lk5_%=\n\t" \
    "s_sub_u32 %[n], %[n], 1\n\t" \
    "buffer_load_dwordx4 v[180:183], %[l16], %[rs], %[s1] offen offset:1024 sc1\n\t" \
    "s_waitcnt vmcnt(0)\n\t" \
    "s_branch .Lc5_%=\n\t" \
    ".Ls4_%=:\n\t" \
    "s_cmp_eq_u32 %[n], 0\n\t" \
    "s_cbranch_scc1 .Lk4_%=\n\t" \
    "s_sub_u32 %[n], %[n], 1\n\t" \
    "buffer_load_dwordx4 v[176:179], %[l16], %[rs], %[s1] offen offset:0 sc1\n\t" \
    "s_waitcnt vmcnt(0)\n\t" \
    "s_branch .Lc4_%=\n\t" \
    ".Ls3_%=:\n\t" \
    "s_cmp_eq_u32 %[n], 0\n\t" \
    "s_cbranch_scc1 .Lk3_%=\n\t" \
    "s_sub_u32 %[n], %[n], 1\n\t" \
    "buffer_load_dwordx4 v[172:175], %[l16], %[rs], %[s0] offen offset:3072 sc1\n\t" \
    "s_waitcnt vmcnt(0)\n\t" \
    "s_branch .Lc3_%=\n\t" \
    ".Ls2_%=:\n\t" \
    "s_cmp_eq_u32 %[n], 0\n\t" \
    "s_cbranch_scc1 .Lk2_%=\n\t" \
    "s_sub_u32 %[n], %[n], 1\n\t" \
    "buffer_load_dwordx4 v[168:171], %[l16], %[rs], %[s0] offen offset:2048 sc1\n\t" \
    "s_waitcnt vmcnt(0)\n\t" \
    "s_branch .Lc2_%=\n\t" \
    ".Ls1_%=:\n\t" \
    "s_cmp_eq_u32 %[n], 0\n\t" \
    "s_cbranch_scc1 .Lk1_%=\n\t" \
    "s_sub_u32 %[n], %[n], 1\n\t" \
    "buffer_load_dwordx4 v[164:167], %[l16], %[rs], %[s0] offen offset:1024 sc1\n\t" \
    "s_waitcnt vmcnt(0)\n\t" \
    "s_branch .Lc1_%=\n\t" \
    ".Ls0_%=:\n\t" \
    "s_cmp_eq_u32 %[n], 0\n\t" \
    "s_cbranch_scc1 .Lk0_%=\n\t" \
    "s_sub_u32 %[n], %[n], 1\n\t" \
    "buffer_load_dwordx4 v[160:163], %[l16], %[rs], %[s0] offen offset:0 sc1\n\t" \
    "s_waitcnt vmcnt(0)\n\t" \
    "s_branch .Lc0_%=\n\t" \
    ".Lme%=:\n\t"
#define ASM_W4F_0 \
    "s_branch .Lf0%=\n\t" \
    ".Lfr%=:\n\t" \
    "s_cmp_eq_u32 %[n], 0\n\t" \
    "s_cbranch_scc1 .Lfb%=\n\t" \
    "s_sub_u32 %[n], %[n], 1\n\t" \
    "s_sleep 6\n\t" \
    "buffer_load_dwordx2 v[164:165], %[o0], %[rd], %[c1] offen sc1\n\t" \
    "buffer_load_dwordx2 v[166:167], %[o1], %[rd], %[c1] offen sc1\n\t" \
    "buffer_load_dwordx2 v[168:169], %[o2], %[rd], %[c1] offen sc1\n\t" \
    "buffer_load_dwordx2 v[170:171], %[o3], %[rd], %[c1] offen sc1\n\t" \
    "buffer_load_dwordx2 v[172:173], %[o4], %[rd], %[c1] offen sc1\n\t" \
    "buffer_load_dwordx2 v[174:175], %[o0], %[rd], %[ca] offen sc1\n\t" \
    "buffer_load_dwordx2 v[176:177], %[o1], %[rd], %[ca] offen sc1\n\t" \
    "buffer_load_dwordx2 v[178:179], %[o2], %[rd], %[ca] offen sc1\n\t" \
    "buffer_load_dwordx2 v[180:181], %[o3], %[rd], %[ca] offen sc1\n\t" \
    "buffer_load_dwordx2 v[182:183], %[o4], %[rd], %[ca] offen sc1\n\t" \
    "s_waitcnt vmcnt(0)\n\t" \
    "s_branch .Lfa%=\n\t" \
    ".Lfs0_%=:\n\t" \
    "s_cmp_eq_u32 %[n], 0\n\t" \
    "s_cbranch_scc1 .Lfu0_%=\n\t" \
    "s_sub_u32 %[n], %[n], 1\n\t" \
    "s_sleep 1\n\t" \
    "s_branch .Lft0_%=\n\t" \
    ".Lfs1_%=:\n\t" \
    "s_cmp_eq_u32 %[n], 0\n\t" \
    "s_cbranch_scc1 .Lfu1_%=\n\t" \
    "s_sub_u32 %[n], %[n], 1\n\t" \
    "s_sleep 1\n\t" \
    "s_branch .Lft1_%=\n\t" \
    ".Lfx%=:\n\t" \
    "s_cmp_eq_u32 %[n], 0\n\t" \
    "s_cbranch_scc1 .Lfz%=\n\t" \
    "s_sub_u32 %[n], %[n], 1\n\t" \
    "s_branch .Lfy%=\n\t" \
    ".Lf0%=:\n\t" \
    "s_waitcnt vmcnt(1)\n\t" \
    ".Lfa%=:\n\t" \
    "v_max_u32 v194, v164, v165\n\t" \
    "v_max3_u32 v194, v166, v167, v194\n\t" \
    "v_max3_u32 v194, v168, v169, v194\n\t" \
    "v_max3_u32 v194, v170, v171, v194\n\t" \
    "v_max3_u32 v194, v172, v173, v194\n\t" \
    "v_max3_u32 v194, v174, v175, v194\n\t" \
    "v_max3_u32 v194, v176, v177, v194\n\t" \
    "v_max3_u32 v194, v178, v179, v194\n\t" \
    "v_max3_u32 v194, v180, v181, v194\n\t" \
    "v_max3_u32 v194, v182, v183, v194\n\t" \
    "v_cmp_eq_u32 vcc, -1, v194\n\t" \
    "s_cbranch_vccnz .Lfr%=\n\t" \
    ".Lfb%=:\n\t" \
    "v_lshlrev_b32 v64, 16, v164\n\t" \
    "v_and_b32 v65, 0xffff0000, v164\n\t" \
    "v_lshlrev_b32 v84, 16, v174\n\t" \
    "v_and_b32 v85, 0xffff0000, v174\n\t" \
    "v_lshlrev_b32 v66, 16, v165\n\t" \
    "v_and_b32 v67, 0xffff0000, v165\n\t" \
    "v_lshlrev_b32 v86, 16, v175\n\t" \
    "v_and_b32 v87, 0xffff0000, v175\n\t" \
    "v_lshlrev_b32 v68, 16, v166\n\t" \
    "v_and_b32 v69, 0xffff0000, v166\n\t" \
    "v_lshlrev_b32 v88, 16, v176\n\t" \
    "v_and_b32 v89, 0xffff0000, v176\n\t" \
    "v_lshlrev_b32 v70, 16, v167\n\t" \
    "v_and_b32 v71, 0xffff0000, v167\n\t" \
    "v_lshlrev_b32 v90, 16, v177\n\t" \
    "v_and_b32 v91, 0xffff0000, v177\n\t" \
    "v_lshlrev_b32 v72, 16, v168\n\t" \
    "v_and_b32 v73, 0xffff0000, v168\n\t" \
    "v_lshlrev_b32 v92, 16, v178\n\t" \
    "v_and_b32 v93, 0xffff0000, v178\n\t" \
    "v_lshlrev_b32 v74, 16, v169\n\t" \
    "v_and_b32 v75, 0xffff0000, v169\n\t" \
    "v_lshlrev_b32 v94, 16, v179\n\t" \
    "v_and_b32 v95, 0xffff0000, v179\n\t" \
    "v_lshlrev_b32 v76, 16, v170\n\t" \
    "v_and_b32 v77, 0xffff0000, v170\n\t" \
    "v_lshlrev_b32 v96, 16, v180\n\t" \
    "v_and_b32 v97, 0xffff0000, v180\n\t" \
    "v_lshlrev_b32 v78, 16, v171\n\t" \
    "v_and_b32 v79, 0xffff0000, v171\n\t" \
    "v_lshlrev_b32 v98, 16, v181\n\t" \
    "v_and_b32 v99, 0xffff0000, v181\n\t" \
    "v_lshlrev_b32 v80, 16, v172\n\t" \
    "v_and_b32 v81, 0xffff0000, v172\n\t" \
    "v_lshlrev_b32 v100, 16, v182\n\t" \
    "v_and_b32 v101, 0xffff0000, v182\n\t" \
    "v_lshlrev_b32 v82, 16, v173\n\t" \
    "v_and_b32 v83, 0xffff0000, v173\n\t" \
    "v_lshlrev_b32 v102, 16, v183\n\t" \
    "v_and_b32 v103, 0xffff0000, v183\n\t" \
    "v_pk_add_f32 v[64:65], v[64:65], v[84:85]\n\t" \
    "v_pk_add_f32 v[66:67], v[66:67], v[86:87]\n\t" \
    "v_pk_add_f32 v[68:69], v[68:69], v[88:89]\n\t" \
    "v_pk_add_f32 v[70:71], v[70:71], v[90:91]\n\t" \
    "v_pk_add_f32 v[72:73], v[72:73], v[92:93]\n\t" \
    "v_pk_add_f32 v[74:75], v[74:75], v[94:95]\n\t" \
    "v_pk_add_f32 v[76:77], v[76:77], v[96:97]\n\t" \
    "v_pk_add_f32 v[78:79], v[78:79], v[98:99]\n\t" \
    "v_pk_add_f32 v[80:81], v[80:81], v[100:101]\n\t" \
    "v_pk_add_f32 v[82:83], v[82:83], v[102:103]\n\t" \
    ".Lft0_%=:\n\t" \
    "ds_read_b32 v194, %[f0]\n\t" \
    "s_waitcnt lgkmcnt(0)\n\t" \
    "v_cmp_ge_u32 vcc, %[u], v194\n\t" \
    "s_cbranch_vccnz .Lfs0_%=\n\t" \
    ".Lfu0_%=:\n\t" \
    "ds_read_b128 v[84:87], %[la] offset:0\n\t" \
    "ds_read_b128 v[88:91], %[la] offset:6144\n\t" \
    "ds_read_b128 v[92:95], %[la] offset:12288\n\t" \
    "ds_read_b128 v[96:99], %[la] offset:18432\n\t" \
    "ds_read_b128 v[100:103], %[la] offset:24576\n\t" \
    "ds_read_b128 v[104:107], %[la] offset:1024\n\t" \
    "ds_read_b128 v[108:111], %[la] offset:7168\n\t" \
    "ds_read_b128 v[112:115], %[la] offset:13312\n\t" \
    "ds_read_b128 v[116:119], %[la] offset:19456\n\t" \
    "ds_read_b128 v[120:123], %[la] offset:25600\n\t" \
    "ds_read_b128 v[124:127], %[la] offset:2048\n\t" \
    "s_waitcnt lgkmcnt(10)\n\t" \
    "v_pk_add_f32 v[64:65], v[64:65], v[84:85]\n\t" \
    "v_pk_add_f32 v[66:67], v[66:67], v[86:87]\n\t" \
    "ds_read_b128 v[128:131], %[la] offset:8192\n\t" \
    "s_waitcnt lgkmcnt(10)\n\t" \
    "v_pk_add_f32 v[68:69], v[68:69], v[88:89]\n\t" \
    "v_pk_add_f32 v[70:71], v[70:71], v[90:91]\n\t" \
    "ds_read_b128 v[132:135], %[la] offset:14336\n\t" \
    "s_waitcnt lgkmcnt(10)\n\t" \
    "v_pk_add_f32 v[72:73], v[72:73], v[92:93]\n\t" \
    "v_pk_add_f32 v[74:75], v[74:75], v[94:95]\n\t" \
    "ds_read_b128 v[136:139], %[la] offset:20480\n\t" \
    "s_waitcnt lgkmcnt(10)\n\t" \
    "v_pk_add_f32 v[76:77], v[76:77], v[96:97]\n\t" \
    "v_pk_add_f32 v[78:79], v[78:79], v[98:99]\n\t" \
    "ds_read_b128 v[140:143], %[la] offset:26624\n\t" \
    "s_waitcnt lgkmcnt(10)\n\t" \
    "v_pk_add_f32 v[80:81], v[80:81], v[100:101]\n\t" \
    "v_pk_add_f32 v[82:83], v[82:83], v[102:103]\n\t" \
    "ds_read_b128 v[144:147], %[la] offset:3072\n\t" \
    "s_waitcnt lgkmcnt(10)\n\t" \
    "v_pk_add_f32 v[64:65], v[64:65], v[104:105]\n\t" \
    "v_pk_add_f32 v[66:67], v[66:67], v[106:107]\n\t" \
    "ds_read_b128 v[148:151], %[la] offset:9216\n\t" \
    "s_waitcnt lgkmcnt(10)\n\t" \
    "v_pk_add_f32 v[68:69], v[68:69], v[108:109]\n\t" \
    "v_pk_add_f32 v[70:71], v[70:71], v[110:111]\n\t" \
    "ds_read_b128 v[152:155], %[la] offset:15360\n\t" \
    "s_waitcnt lgkmcnt(10)\n\t" \
    "v_pk_add_f32 v[72:73], v[72:73], v[112:113]\n\t" \
    "v_pk_add_f32 v[74:75], v[74:75], v[114:115]\n\t" \
    "ds_read_b128 v[156:159], %[la] offset:21504\n\t" \
    "s_waitcnt lgkmcnt(10)\n\t" \
    "v_pk_add_f32 v[76:77], v[76:77], v[116:117]\n\t" \
    "v_pk_add_f32 v[78:79], v[78:79], v[118:119]\n\t" \
    "ds_read_b128 v[160:163], %[la] offset:27648\n\t" \
    "s_waitcnt lgkmcnt(10)\n\t" \
    "v_pk_add_f32 v[80:81], v[80:81], v[120:121]\n\t" \
    "v_pk_add_f32 v[82:83], v[82:83], v[122:123]\n\t" \
    "s_waitcnt lgkmcnt(9)\n\t" \
    "v_pk_add_f32 v[64:65], v[64:65], v[124:125]\n\t" \
    "v_pk_add_f32 v[66:67], v[66:67], v[126:127]\n\t" \
    "s_waitcnt lgkmcnt(8)\n\t" \
    "v_pk_add_f32 v[68:69], v[68:69], v[128:129]\n\t" \
    "v_pk_add_f32 v[70:71], v[70:71], v[130:131]\n\t" \
    "s_waitcnt lgkmcnt(7)\n\t" \
    "v_pk_add_f32 v[72:73], v[72:73], v[132:133]\n\t" \
    "v_pk_add_f32 v[74:75], v[74:75], v[134:135]\n\t" \
    "s_waitcnt lgkmcnt(6)\n\t" \
    "v_pk_add_f32 v[76:77], v[76:77], v[136:137]\n\t" \
    "v_pk_add_f32 v[78:79], v[78:79], v[138:139]\n\t" \
    "s_waitcnt lgkmcnt(5)\n\t" \
    "v_pk_add_f32 v[80:81], v[80:81], v[140:141]\n\t" \
    "v_pk_add_f32 v[82:83], v[82:83], v[142:143]\n\t" \
    "s_waitcnt lgkmcnt(4)\n\t" \
    "v_pk_add_f32 v[64:65], v[64:65], v[144:145]\n\t" \
    "v_pk_add_f32 v[66:67], v[66:67], v[146:147]\n\t" \
    "s_waitcnt lgkmcnt(3)\n\t" \
    "v_pk_add_f32 v[68:69], v[68:69], v[148:149]\n\t" \
    "v_pk_add_f32 v[70:71], v[70:71], v[150:151]\n\t" \
    "s_waitcnt lgkmcnt(2)\n\t" \
    "v_pk_add_f32 v[72:73], v[72:73], v[152:153]\n\t" \
    "v_pk_add_f32 v[74:75], v[74:75], v[154:155]\n\t" \
    "s_waitcnt lgkmcnt(1)\n\t" \
    "v_pk_add_f32 v[76:77], v[76:77], v[156:157]\n\t" \
    "v_pk_add_f32 v[78:79], v[78:79], v[158:159]\n\t" \
    "s_waitcnt lgkmcnt(0)\n\t" \
    "v_pk_add_f32 v[80:81], v[80:81], v[160:161]\n\t" \
    "v_pk_add_f32 v[82:83], v[82:83], v[162:163]\n\t" \
    ".Lft1_%=:\n\t" \
    "ds_read_b32 v194, %[f1]\n\t" \
    "s_waitcnt lgkmcnt(0)\n\t" \
    "v_cmp_ge_u32 vcc, %[u], v194\n\t" \
    "s_cbranch_vccnz .Lfs1_%=\n\t" \
    ".Lfu1_%=:\n\t" \
    ASM_ST2 \
    "ds_read_b128 v[84:87], %[la] offset:4096\n\t" \
    "ds_read_b128 v[88:91], %[la] offset:10240\n\t" \
    "ds_read_b128 v[92:95], %[la] offset:16384\n\t" \
    "ds_read_b128 v[96:99], %[la] offset:22528\n\t" \
    "ds_read_b128 v[100:103], %[la] offset:28672\n\t" \
    "ds_read_b128 v[104:107], %[la] offset:5120\n\t" \
    "ds_read_b128 v[108:111], %[la] offset:11264\n\t" \
    "ds_read_b128 v[112:115], %[la] offset:17408\n\t" \
    "ds_read_b128 v[116:119], %[la] offset:23552\n\t" \
    "ds_read_b128 v[120:123], %[la] offset:29696\n\t" \
    "v_mov_b32 v194, %[un]\n\t" \
    "ds_write_b32 %[fd], v194\n\t" \
    "s_waitcnt lgkmcnt(10)\n\t" \
    "v_pk_add_f32 v[64:65], v[64:65], v[84:85]\n\t" \
    "v_pk_add_f32 v[66:67], v[66:67], v[86:87]\n\t" \
    "s_waitcnt lgkmcnt(9)\n\t" \
    "v_pk_add_f32 v[68:69], v[68:69], v[88:89]\n\t" \
    "v_pk_add_f32 v[70:71], v[70:71], v[90:91]\n\t" \
    "s_waitcnt lgkmcnt(8)\n\t" \
    "v_pk_add_f32 v[72:73], v[72:73], v[92:93]\n\t" \
    "v_pk_add_f32 v[74:75], v[74:75], v[94:95]\n\t" \
    "s_waitcnt lgkmcnt(7)\n\t" \
    "v_pk_add_f32 v[76:77], v[76:77], v[96:97]\n\t" \
    "v_pk_add_f32 v[78:79], v[78:79], v[98:99]\n\t" \
    "s_waitcnt lgkmcnt(6)\n\t" \
    "v_pk_add_f32 v[80:81], v[80:81], v[100:101]\n\t" \
    "v_pk_add_f32 v[82:83], v[82:83], v[102:103]\n\t" \
    "s_waitcnt lgkmcnt(5)\n\t" \
    "v_pk_add_f32 v[64:65], v[64:65], v[104:105]\n\t" \
    "v_pk_add_f32 v[66:67], v[66:67], v[106:107]\n\t" \
    "s_waitcnt lgkmcnt(4)\n\t" \
    "v_pk_add_f32 v[68:69], v[68:69], v[108:109]\n\t" \
    "v_pk_add_f32 v[70:71], v[70:71], v[110:111]\n\t" \
    "s_waitcnt lgkmcnt(3)\n\t" \
    "v_pk_add_f32 v[72:73], v[72:73], v[112:113]\n\t" \
    "v_pk_add_f32 v[74:75], v[74:75], v[114:115]\n\t" \
    "s_waitcnt lgkmcnt(2)\n\t" \
    "v_pk_add_f32 v[76:77], v[76:77], v[116:117]\n\t" \
    "v_pk_add_f32 v[78:79], v[78:79], v[118:119]\n\t" \
    "s_waitcnt lgkmcnt(1)\n\t" \
    "v_pk_add_f32 v[80:81], v[80:81], v[120:121]\n\t" \
    "v_pk_add_f32 v[82:83], v[82:83], v[122:123]\n\t" \
    "v_pk_add_f32 v[84:85], v[64:65], v[66:67]\n\t" \
    "v_pk_mul_f32 v[88:89], v[64:65], v[64:65]\n\t" \
    "v_pk_add_f32 v[86:87], v[68:69], v[70:71]\n\t" \
    "v_pk_mul_f32 v[90:91], v[66:67], v[66:67]\n\t" \
    "v_pk_fma_f32 v[88:89], v[68:69], v[68:69], v[88:89]\n\t" \
    "v_pk_fma_f32 v[90:91], v[70:71], v[70:71], v[90:91]\n\t" \
    "v_pk_add_f32 v[84:85], v[84:85], v[72:73]\n\t" \
    "v_pk_fma_f32 v[88:89], v[72:73], v[72:73], v[88:89]\n\t" \
    "v_pk_add_f32 v[86:87], v[86:87], v[74:75]\n\t" \
    "v_pk_fma_f32 v[90:91], v[74:75], v[74:75], v[90:91]\n\t" \
    "v_pk_add_f32 v[84:85], v[84:85], v[76:77]\n\t" \
    "v_pk_fma_f32 v[88:89], v[76:77], v[76:77], v[88:89]\n\t" \
    "v_pk_add_f32 v[86:87], v[86:87], v[78:79]\n\t" \
    "v_pk_fma_f32 v[90:91], v[78:79], v[78:79], v[90:91]\n\t" \
    "v_pk_add_f32 v[84:85], v[84:85], v[80:81]\n\t" \
    "v_pk_fma_f32 v[88:89], v[80:81], v[80:81], v[88:89]\n\t" \
    "v_pk_add_f32 v[86:87], v[86:87], v[82:83]\n\t" \
    "v_pk_fma_f32 v[90:91], v[82:83], v[82:83], v[90:91]\n\t" \
    "v_pk_add_f32 v[84:85], v[84:85], v[86:87]\n\t" \
    "v_pk_add_f32 v[88:89], v[88:89], v[90:91]\n\t" \
    "v_add_f32 v194, v84, v85\n\t" \
    "v_add_f32 v195, v88, v89\n\t" \
    "s_nop 1\n\t" \
    "v_permlane16_swap_b32 v194, v195\n\t" \
    "v_add_f32 v196, v194, v195\n\t" \
    "v_add_f32 v197, v194, v195\n\t" \
    "s_nop 1\n\t" \
    "v_permlane32_swap_b32 v196, v197\n\t" \
    "v_add_f32 v194, v196, v197\n\t" \
    "v_add_f32 v195, v196, v197\n\t" \
    "s_nop 1\n\t" \
    "v_permlane16_swap_b32 v194, v195\n\t" \
    "v_min_u32 v194, -2, v194\n\t" \
    "v_min_u32 v195, -2, v195\n\t" \
    "buffer_store_dwordx2 v[194:195], %[so], %[rr], %[sp] offen sc1\n\t" \
    ASM_ST3 \
    "v_cvt_pk_bf16_f32 v184, v64, v65\n\t" \
    "v_cvt_pk_bf16_f32 v185, v66, v67\n\t" \
    "v_cvt_pk_bf16_f32 v186, v68, v69\n\t" \
    "v_cvt_pk_bf16_f32 v187, v70, v71\n\t" \
    "v_cvt_pk_bf16_f32 v188, v72, v73\n\t" \
    "v_cvt_pk_bf16_f32 v189, v74, v75\n\t" \
    "v_cvt_pk_bf16_f32 v190, v76, v77\n\t" \
    "v_cvt_pk_bf16_f32 v191, v78, v79\n\t" \
    "v_cvt_pk_bf16_f32 v192, v80, v81\n\t" \
    "v_cvt_pk_bf16_f32 v193, v82, v83\n\t" \
    "v_permlane16_swap_b32 v184, v186\n\t" \
    "v_permlane16_swap_b32 v185, v187\n\t" \
    "v_permlane16_swap_b32 v188, v190\n\t" \
    "v_permlane16_swap_b32 v189, v191\n\t" \
    W4SLEEP \
    ".Lfy%=:\n\t" \
    "buffer_load_dwordx2 v[196:197], %[si], %[rr], %[sq] offen sc1\n\t" \
    "buffer_load_dwordx2 v[198:199], %[si], %[rr], %[sq] offen offset:512 sc1\n\t" \
    "s_waitcnt vmcnt(0)\n\t" \
    "v_max_u32 v200, v196, v197\n\t" \
    "v_max3_u32 v200, v198, v199, v200\n\t" \
    "v_cmp_eq_u32 vcc, -1, v200\n\t" \
    "s_cbranch_vccnz .Lfx%=\n\t" \
    ".Lfz%=:\n\t" \
    ASM_ST \
    "v_add_f32 v194, v196, v198\n\t" \
    "v_add_f32 v195, v197, v199\n\t" \
    "buffer_store_dwordx4 v[184:187], %[pa], %[rd], %[xr] offen sc1\n\t" \
    "buffer_store_dwordx4 v[188:191], %[pb], %[rd], %[xr] offen sc1\n\t" \
    "buffer_store_dwordx2 v[192:193], %[o4], %[rd], %[xr] offen sc1\n\t" \
    "s_nop 1\n\t" \
    "v_permlane16_swap_b32 v194, v195\n\t" \
    "v_add_f32 v196, v194, v195\n\t" \
    "v_add_f32 v197, v194, v195\n\t" \
    "s_nop 1\n\t" \
    "v_permlane32_swap_b32 v196, v197\n\t" \
    "v_add_f32 v194, v196, v197\n\t" \
    "v_add_f32 v195, v196, v197\n\t" \
    "s_nop 1\n\t" \
    "v_permlane16_swap_b32 v194, v195\n\t" \
    "v_mul_f32 v194, %[kd], v194\n\t" \
    "v_mul_f32 v195, %[kd], v195\n\t" \
    "v_fma_f32 v195, -v194, v194, v195\n\t" \
    "v_max_f32 v195, 0, v195\n\t" \
    "v_cvt_pk_bf16_f32 v195, v195, v195\n\t" \
    "v_lshlrev_b32 v195, 16, v195\n\t" \
    "v_add_f32 v195, %[ke], v195\n\t" \
    "v_cvt_pk_bf16_f32 v195, v195, v195\n\t" \
    "v_lshlrev_b32 v195, 16, v195\n\t" \
    "v_sqrt_f32 v195, v195\n\t" \
    "v_cvt_pk_bf16_f32 v194, v194, v194\n\t" \
    "v_lshlrev_b32 v194, 16, v194\n\t" \
    "v_cvt_pk_bf16_f32 v195, v195, v195\n\t" \
    "v_lshlrev_b32 v195, 16, v195\n\t" \
    "v_rcp_f32 v196, v195\n\t" \
    "v_rcp_f32 v197, v195\n\t" \
    "s_nop 0\n\t" \
    "v_mul_f32 v198, -v194, v196\n\t" \
    "v_mul_f32 v199, -v194, v196\n\t" \
    "v_pk_fma_f32 v[64:65], v[64:65], v[196:197], v[198:199]\n\t" \
    "v_pk_fma_f32 v[66:67], v[66:67], v[196:197], v[198:199]\n\t" \
    "v_pk_fma_f32 v[68:69], v[68:69], v[196:197], v[198:199]\n\t" \
    "v_pk_fma_f32 v[70:71], v[70:71], v[196:197], v[198:199]\n\t" \
    "v_pk_fma_f32 v[72:73], v[72:73], v[196:197], v[198:199]\n\t" \
    "v_pk_fma_f32 v[74:75], v[74:75], v[196:197], v[198:199]\n\t" \
    "v_pk_fma_f32 v[76:77], v[76:77], v[196:197], v[198:199]\n\t" \
    "v_pk_fma_f32 v[78:79], v[78:79], v[196:197], v[198:199]\n\t" \
    "v_pk_fma_f32 v[80:81], v[80:81], v[196:197], v[198:199]\n\t" \
    "v_pk_fma_f32 v[82:83], v[82:83], v[196:197], v[198:199]\n\t" \
    "v_cvt_pk_bf16_f32 v184, v64, v65\n\t" \
    "v_cvt_pk_bf16_f32 v185, v66, v67\n\t" \
    "v_cvt_pk_bf16_f32 v186, v68, v69\n\t" \
    "v_cvt_pk_bf16_f32 v187, v70, v71\n\t" \
    "v_cvt_pk_bf16_f32 v188, v72, v73\n\t" \
    "v_cvt_pk_bf16_f32 v189, v74, v75\n\t" \
    "v_cvt_pk_bf16_f32 v190, v76, v77\n\t" \
    "v_cvt_pk_bf16_f32 v191, v78, v79\n\t" \
    "v_cvt_pk_bf16_f32 v192, v80, v81\n\t" \
    "v_cvt_pk_bf16_f32 v193, v82, v83\n\t" \
    "v_permlane16_swap_b32 v184, v186\n\t" \
    "v_permlane16_swap_b32 v185, v187\n\t" \
    "v_permlane16_swap_b32 v188, v190\n\t" \
    "v_permlane16_swap_b32 v189, v191\n\t" \
    "buffer_store_dwordx4 v[184:187], %[pa], %[rd], %[xn] offen sc1\n\t" \
    "buffer_store_dwordx4 v[188:191], %[pb], %[rd], %[xn] offen sc1\n\t" \
    "buffer_store_dwordx2 v[192:193], %[o4], %[rd], %[xn] offen sc1\n\t" \
    "buffer_load_dwordx2 v[164:165], %[o0], %[rd], %[n1] offen sc1\n\t" \
    "buffer_load_dwordx2 v[166:167], %[o1], %[rd], %[n1] offen sc1\n\t" \
    "buffer_load_dwordx2 v[168:169], %[o2], %[rd], %[n1] offen sc1\n\t" \
    "buffer_load_dwordx2 v[170:171], %[o3], %[rd], %[n1] offen sc1\n\t" \
    "buffer_load_dwordx2 v[172:173], %[o4], %[rd], %[n1] offen sc1\n\t" \
    "buffer_load_dwordx2 v[174:175], %[o0], %[rd], %[na] offen sc1\n\t" \
    "buffer_load_dwordx2 v[176:177], %[o1], %[rd], %[na] offen sc1\n\t" \
    "buffer_load_dwordx2 v[178:179], %[o2], %[rd], %[na] offen sc1\n\t" \
    "buffer_load_dwordx2 v[180:181], %[o3], %[rd], %[na] offen sc1\n\t" \
    "buffer_load_dwordx2 v[182:183], %[o4], %[rd], %[na] offen sc1\n\t" \
    "v_mov_b32 v196, %[tn]\n\t" \
    "v_mov_b32 v197, %[tn]\n\t" \
    "v_mov_b32 v198, %[tn]\n\t" \
    "v_mov_b32 v199, %[tn]\n\t" \
    "s_waitcnt vmcnt(10)\n\t" \
    "buffer_store_dwordx4 v[196:199], %[fo], %[rn], %[fs] offen sc1\n\t" \
    "s_nop 1\n\t"
#define ASM_W4F_1 \
    "s_branch .Lf0%=\n\t" \
    ".Lfr%=:\n\t" \
    "s_cmp_eq_u32 %[n], 0\n\t" \
    "s_cbranch_scc1 .Lfb%=\n\t" \
    "s_sub_u32 %[n], %[n], 1\n\t" \
    "s_sleep 6\n\t" \
    "buffer_load_dwordx2 v[164:165], %[o0], %[rd], %[c1] offen sc1\n\t" \
    "buffer_load_dwordx2 v[166:167], %[o1], %[rd], %[c1] offen sc1\n\t" \
    "buffer_load_dwordx2 v[168:169], %[o2], %[rd], %[c1] offen sc1\n\t" \
    "buffer_load_dwordx2 v[170:171], %[o3], %[rd], %[c1] offen sc1\n\t" \
    "buffer_load_dwordx2 v[172:173], %[o4], %[rd], %[c1] offen sc1\n\t" \
    "buffer_load_dwordx2 v[174:175], %[o0], %[rd], %[ca] offen sc1\n\t" \
    "buffer_load_dwordx2 v[176:177], %[o1], %[rd], %[ca] offen sc1\n\t" \
    "buffer_load_dwordx2 v[178:179], %[o2], %[rd], %[ca] offen sc1\n\t" \
    "buffer_load_dwordx2 v[180:181], %[o3], %[rd], %[ca] offen sc1\n\t" \
    "buffer_load_dwordx2 v[182:183], %[o4], %[rd], %[ca] offen sc1\n\t" \
    "s_waitcnt vmcnt(0)\n\t" \
    "s_branch .Lfa%=\n\t" \
    ".Lfs0_%=:\n\t" \
    "s_cmp_eq_u32 %[n], 0\n\t" \
    "s_cbranch_scc1 .Lfu0_%=\n\t" \
    "s_sub_u32 %[n], %[n], 1\n\t" \
    "s_sleep 1\n\t" \
    "s_branch .Lft0_%=\n\t" \
    ".Lfs1_%=:\n\t" \
    "s_cmp_eq_u32 %[n], 0\n\t" \
    "s_cbranch_scc1 .Lfu1_%=\n\t" \
    "s_sub_u32 %[n], %[n], 1\n\t" \
    "s_sleep 1\n\t" \
    "s_branch .Lft1_%=\n\t" \
    ".Lf0%=:\n\t" \
    "s_waitcnt vmcnt(0)\n\t" \
    ".Lfa%=:\n\t" \
    "v_max_u32 v194, v164, v165\n\t" \
    "v_max3_u32 v194, v166, v167, v194\n\t" \
    "v_max3_u32 v194, v168, v169, v194\n\t" \
    "v_max3_u32 v194, v170, v171, v194\n\t" \
    "v_max3_u32 v194, v172, v173, v194\n\t" \
    "v_max3_u32 v194, v174, v175, v194\n\t" \
    "v_max3_u32 v194, v176, v177, v194\n\t" \
    "v_max3_u32 v194, v178, v179, v194\n\t" \
    "v_max3_u32 v194, v180, v181, v194\n\t" \
    "v_max3_u32 v194, v182, v183, v194\n\t" \
    "v_cmp_eq_u32 vcc, -1, v194\n\t" \
    "s_cbranch_vccnz .Lfr%=\n\t" \
    ".Lfb%=:\n\t" \
    "v_lshlrev_b32 v64, 16, v164\n\t" \
    "v_and_b32 v65, 0xffff0000, v164\n\t" \
    "v_lshlrev_b32 v84, 16, v174\n\t" \
    "v_and_b32 v85, 0xffff0000, v174\n\t" \
    "v_lshlrev_b32 v66, 16, v165\n\t" \
    "v_and_b32 v67, 0xffff0000, v165\n\t" \
    "v_lshlrev_b32 v86, 16, v175\n\t" \
    "v_and_b32 v87, 0xffff0000, v175\n\t" \
    "v_lshlrev_b32 v68, 16, v166\n\t" \
    "v_and_b32 v69, 0xffff0000, v166\n\t" \
    "v_lshlrev_b32 v88, 16, v176\n\t" \
    "v_and_b32 v89, 0xffff0000, v176\n\t" \
    "v_lshlrev_b32 v70, 16, v167\n\t" \
    "v_and_b32 v71, 0xffff0000, v167\n\t" \
    "v_lshlrev_b32 v90, 16, v177\n\t" \
    "v_and_b32 v91, 0xffff0000, v177\n\t" \
    "v_lshlrev_b32 v72, 16, v168\n\t" \
    "v_and_b32 v73, 0xffff0000, v168\n\t" \
    "v_lshlrev_b32 v92, 16, v178\n\t" \
    "v_and_b32 v93, 0xffff0000, v178\n\t" \
    "v_lshlrev_b32 v74, 16, v169\n\t" \
    "v_and_b32 v75, 0xffff0000, v169\n\t" \
    "v_lshlrev_b32 v94, 16, v179\n\t" \
    "v_and_b32 v95, 0xffff0000, v179\n\t" \
    "v_lshlrev_b32 v76, 16, v170\n\t" \
    "v_and_b32 v77, 0xffff0000, v170\n\t" \
    "v_lshlrev_b32 v96, 16, v180\n\t" \
    "v_and_b32 v97, 0xffff0000, v180\n\t" \
    "v_lshlrev_b32 v78, 16, v171\n\t" \
    "v_and_b32 v79, 0xffff0000, v171\n\t" \
    "v_lshlrev_b32 v98, 16, v181\n\t" \
    "v_and_b32 v99, 0xffff0000, v181\n\t" \
    "v_lshlrev_b32 v80, 16, v172\n\t" \
    "v_and_b32 v81, 0xffff0000, v172\n\t" \
    "v_lshlrev_b32 v100, 16, v182\n\t" \
    "v_and_b32 v101, 0xffff0000, v182\n\t" \
    "v_lshlrev_b32 v82, 16, v173\n\t" \
    "v_and_b32 v83, 0xffff0000, v173\n\t" \
    "v_lshlrev_b32 v102, 16, v183\n\t" \
    "v_and_b32 v103, 0xffff0000, v183\n\t" \
    "v_pk_add_f32 v[64:65], v[64:65], v[84:85]\n\t" \
    "v_pk_add_f32 v[66:67], v[66:67], v[86:87]\n\t" \
    "v_pk_add_f32 v[68:69], v[68:69], v[88:89]\n\t" \
    "v_pk_add_f32 v[70:71], v[70:71], v[90:91]\n\t" \
    "v_pk_add_f32 v[72:73], v[72:73], v[92:93]\n\t" \
    "v_pk_add_f32 v[74:75], v[74:75], v[94:95]\n\t" \
    "v_pk_add_f32 v[76:77], v[76:77], v[96:97]\n\t" \
    "v_pk_add_f32 v[78:79], v[78:79], v[98:99]\n\t" \
    "v_pk_add_f32 v[80:81], v[80:81], v[100:101]\n\t" \
    "v_pk_add_f32 v[82:83], v[82:83], v[102:103]\n\t" \
    ".Lft0_%=:\n\t" \
    "ds_read_b32 v194, %[f0]\n\t" \
    "s_waitcnt lgkmcnt(0)\n\t" \
    "v_cmp_ge_u32 vcc, %[u], v194\n\t" \
    "s_cbranch_vccnz .Lfs0_%=\n\t" \
    ".Lfu0_%=:\n\t" \
    "ds_read_b128 v[84:87], %[la] offset:0\n\t" \
    "ds_read_b128 v[88:91], %[la] offset:6144\n\t" \
    "ds_read_b128 v[92:95], %[la] offset:12288\n\t" \
    "ds_read_b128 v[96:99], %[la] offset:18432\n\t" \
    "ds_read_b128 v[100:103], %[la] offset:24576\n\t" \
    "ds_read_b128 v[104:107], %[la] offset:1024\n\t" \
    "ds_read_b128 v[108:111], %[la] offset:7168\n\t" \
    "ds_read_b128 v[112:115], %[la] offset:13312\n\t" \
    "ds_read_b128 v[116:119], %[la] offset:19456\n\t" \
    "ds_read_b128 v[120:123], %[la] offset:25600\n\t" \
    "ds_read_b128 v[124:127], %[la] offset:2048\n\t" \
    "s_waitcnt lgkmcnt(10)\n\t" \
    "v_pk_add_f32 v[64:65], v[64:65], v[84:85]\n\t" \
    "v_pk_add_f32 v[66:67], v[66:67], v[86:87]\n\t" \
    "ds_read_b128 v[128:131], %[la] offset:8192\n\t" \
    "s_waitcnt lgkmcnt(10)\n\t" \
    "v_pk_add_f32 v[68:69], v[68:69], v[88:89]\n\t" \
    "v_pk_add_f32 v[70:71], v[70:71], v[90:91]\n\t" \
    "ds_read_b128 v[132:135], %[la] offset:14336\n\t" \
    "s_waitcnt lgkmcnt(10)\n\t" \
    "v_pk_add_f32 v[72:73], v[72:73], v[92:93]\n\t" \
    "v_pk_add_f32 v[74:75], v[74:75], v[94:95]\n\t" \
    "ds_read_b128 v[136:139], %[la] offset:20480\n\t" \
    "s_waitcnt lgkmcnt(10)\n\t" \
    "v_pk_add_f32 v[76:77], v[76:77], v[96:97]\n\t" \
    "v_pk_add_f32 v[78:79], v[78:79], v[98:99]\n\t" \
    "ds_read_b128 v[140:143], %[la] offset:26624\n\t" \
    "s_waitcnt lgkmcnt(10)\n\t" \
    "v_pk_add_f32 v[80:81], v[80:81], v[100:101]\n\t" \
    "v_pk_add_f32 v[82:83], v[82:83], v[102:103]\n\t" \
    "ds_read_b128 v[144:147], %[la] offset:3072\n\t" \
    "s_waitcnt lgkmcnt(10)\n\t" \
    "v_pk_add_f32 v[64:65], v[64:65], v[104:105]\n\t" \
    "v_pk_add_f32 v[66:67], v[66:67], v[106:107]\n\t" \
    "ds_read_b128 v[148:151], %[la] offset:9216\n\t" \
    "s_waitcnt lgkmcnt(10)\n\t" \
    "v_pk_add_f32 v[68:69], v[68:69], v[108:109]\n\t" \
    "v_pk_add_f32 v[70:71], v[70:71], v[110:111]\n\t" \
    "ds_read_b128 v[152:155], %[la] offset:15360\n\t" \
    "s_waitcnt lgkmcnt(10)\n\t" \
    "v_pk_add_f32 v[72:73], v[72:73], v[112:113]\n\t" \
    "v_pk_add_f32 v[74:75], v[74:75], v[114:115]\n\t" \
    "ds_read_b128 v[156:159], %[la] offset:21504\n\t" \
    "s_waitcnt lgkmcnt(10)\n\t" \
    "v_pk_add_f32 v[76:77], v[76:77], v[116:117]\n\t" \
    "v_pk_add_f32 v[78:79], v[78:79], v[118:119]\n\t" \
    "ds_read_b128 v[160:163], %[la] offset:27648\n\t" \
    "s_waitcnt lgkmcnt(10)\n\t" \
    "v_pk_add_f32 v[80:81], v[80:81], v[120:121]\n\t" \
    "v_pk_add_f32 v[82:83], v[82:83], v[122:123]\n\t" \
    "s_waitcnt lgkmcnt(9)\n\t" \
    "v_pk_add_f32 v[64:65], v[64:65], v[124:125]\n\t" \
    "v_pk_add_f32 v[66:67], v[66:67], v[126:127]\n\t" \
    "s_waitcnt lgkmcnt(8)\n\t" \
    "v_pk_add_f32 v[68:69], v[68:69], v[128:129]\n\t" \
    "v_pk_add_f32 v[70:71], v[70:71], v[130:131]\n\t" \
    "s_waitcnt lgkmcnt(7)\n\t" \
    "v_pk_add_f32 v[72:73], v[72:73], v[132:133]\n\t" \
    "v_pk_add_f32 v[74:75], v[74:75], v[134:135]\n\t" \
    "s_waitcnt lgkmcnt(6)\n\t" \
    "v_pk_add_f32 v[76:77], v[76:77], v[136:137]\n\t" \
    "v_pk_add_f32 v[78:79], v[78:79], v[138:139]\n\t" \
    "s_waitcnt lgkmcnt(5)\n\t" \
    "v_pk_add_f32 v[80:81], v[80:81], v[140:141]\n\t" \
    "v_pk_add_f32 v[82:83], v[82:83], v[142:143]\n\t" \
    "s_waitcnt lgkmcnt(4)\n\t" \
    "v_pk_add_f32 v[64:65], v[64:65], v[144:145]\n\t" \
    "v_pk_add_f32 v[66:67], v[66:67], v[146:147]\n\t" \
    "s_waitcnt lgkmcnt(3)\n\t" \
    "v_pk_add_f32 v[68:69], v[68:69], v[148:149]\n\t" \
    "v_pk_add_f32 v[70:71], v[70:71], v[150:151]\n\t" \
    "s_waitcnt lgkmcnt(2)\n\t" \
    "v_pk_add_f32 v[72:73], v[72:73], v[152:153]\n\t" \
    "v_pk_add_f32 v[74:75], v[74:75], v[154:155]\n\t" \
    "s_waitcnt lgkmcnt(1)\n\t" \
    "v_pk_add_f32 v[76:77], v[76:77], v[156:157]\n\t" \
    "v_pk_add_f32 v[78:79], v[78:79], v[158:159]\n\t" \
    "s_waitcnt lgkmcnt(0)\n\t" \
    "v_pk_add_f32 v[80:81], v[80:81], v[160:161]\n\t" \
    "v_pk_add_f32 v[82:83], v[82:83], v[162:163]\n\t" \
    ".Lft1_%=:\n\t" \
    "ds_read_b32 v194, %[f1]\n\t" \
    "s_waitcnt lgkmcnt(0)\n\t" \
    "v_cmp_ge_u32 vcc, %[u], v194\n\t" \
    "s_cbranch_vccnz .Lfs1_%=\n\t" \
    ".Lfu1_%=:\n\t" \
    ASM_ST2 \
    "ds_read_b128 v[84:87], %[la] offset:4096\n\t" \
    "ds_read_b128 v[88:91], %[la] offset:10240\n\t" \
    "ds_read_b128 v[92:95], %[la] offset:16384\n\t" \
    "ds_read_b128 v[96:99], %[la] offset:22528\n\t" \
    "ds_read_b128 v[100:103], %[la] offset:28672\n\t" \
    "ds_read_b128 v[104:107], %[la] offset:5120\n\t" \
    "ds_read_b128 v[108:111], %[la] offset:11264\n\t" \
    "ds_read_b128 v[112:115], %[la] offset:17408\n\t" \
    "ds_read_b128 v[116:119], %[la] offset:23552\n\t" \
    "ds_read_b128 v[120:123], %[la] offset:29696\n\t" \
    "v_mov_b32 v194, %[un]\n\t" \
    "ds_write_b32 %[fd], v194\n\t" \
    "s_waitcnt lgkmcnt(10)\n\t" \
    "v_pk_add_f32 v[64:65], v[64:65], v[84:85]\n\t" \
    "v_pk_add_f32 v[66:67], v[66:67], v[86:87]\n\t" \
    "s_waitcnt lgkmcnt(9)\n\t" \
    "v_pk_add_f32 v[68:69], v[68:69], v[88:89]\n\t" \
    "v_pk_add_f32 v[70:71], v[70:71], v[90:91]\n\t" \
    "s_waitcnt lgkmcnt(8)\n\t" \
    "v_pk_add_f32 v[72:73], v[72:73], v[92:93]\n\t" \
    "v_pk_add_f32 v[74:75], v[74:75], v[94:95]\n\t" \
    "s_waitcnt lgkmcnt(7)\n\t" \
    "v_pk_add_f32 v[76:77], v[76:77], v[96:97]\n\t" \
    "v_pk_add_f32 v[78:79], v[78:79], v[98:99]\n\t" \
    "s_waitcnt lgkmcnt(6)\n\t" \
    "v_pk_add_f32 v[80:81], v[80:81], v[100:101]\n\t" \
    "v_pk_add_f32 v[82:83], v[82:83], v[102:103]\n\t" \
    "s_waitcnt lgkmcnt(5)\n\t" \
    "v_pk_add_f32 v[64:65], v[64:65], v[104:105]\n\t" \
    "v_pk_add_f32 v[66:67], v[66:67], v[106:107]\n\t" \
    "s_waitcnt lgkmcnt(4)\n\t" \
    "v_pk_add_f32 v[68:69], v[68:69], v[108:109]\n\t" \
    "v_pk_add_f32 v[70:71], v[70:71], v[110:111]\n\t" \
    "s_waitcnt lgkmcnt(3)\n\t" \
    "v_pk_add_f32 v[72:73], v[72:73], v[112:113]\n\t" \
    "v_pk_add_f32 v[74:75], v[74:75], v[114:115]\n\t" \
    "s_waitcnt lgkmcnt(2)\n\t" \
    "v_pk_add_f32 v[76:77], v[76:77], v[116:117]\n\t" \
    "v_pk_add_f32 v[78:79], v[78:79], v[118:119]\n\t" \
    "s_waitcnt lgkmcnt(1)\n\t" \
    "v_pk_add_f32 v[80:81], v[80:81], v[120:121]\n\t" \
    "v_pk_add_f32 v[82:83], v[82:83], v[122:123]\n\t" \
    "v_cvt_pk_bf16_f32 v184, v64, v65\n\t" \
    "v_cvt_pk_bf16_f32 v185, v66, v67\n\t" \
    "v_cvt_pk_bf16_f32 v186, v68, v69\n\t" \
    "v_cvt_pk_bf16_f32 v187, v70, v71\n\t" \
    "v_cvt_pk_bf16_f32 v188, v72, v73\n\t" \
    "v_cvt_pk_bf16_f32 v189, v74, v75\n\t" \
    "v_cvt_pk_bf16_f32 v190, v76, v77\n\t" \
    "v_cvt_pk_bf16_f32 v191, v78, v79\n\t" \
    "v_cvt_pk_bf16_f32 v192, v80, v81\n\t" \
    "v_cvt_pk_bf16_f32 v193, v82, v83\n\t" \
    "v_permlane16_swap_b32 v184, v186\n\t" \
    "v_permlane16_swap_b32 v185, v187\n\t" \
    "v_permlane16_swap_b32 v188, v190\n\t" \
    "v_permlane16_swap_b32 v189, v191\n\t" \
    "buffer_store_dwordx4 v[184:187], %[pa], %[ro], %[so] offen nt\n\t" \
    "buffer_store_dwordx4 v[188:191], %[pa], %[ro], %[so] offen offset:64 nt\n\t" \
    "buffer_store_dwordx2 v[192:193], %[pb], %[ro], %[so] offen offset:128 nt\n\t" \
    "buffer_load_dwordx2 v[164:165], %[o0], %[rd], %[n1] offen sc1\n\t" \
    "buffer_load_dwordx2 v[166:167], %[o1], %[rd], %[n1] offen sc1\n\t" \
    "buffer_load_dwordx2 v[168:169], %[o2], %[rd], %[n1] offen sc1\n\t" \
    "buffer_load_dwordx2 v[170:171], %[o3], %[rd], %[n1] offen sc1\n\t" \
    "buffer_load_dwordx2 v[172:173], %[o4], %[rd], %[n1] offen sc1\n\t" \
    "buffer_load_dwordx2 v[174:175], %[o0], %[rd], %[na] offen sc1\n\t" \
    "buffer_load_dwordx2 v[176:177], %[o1], %[rd], %[na] offen sc1\n\t" \
    "buffer_load_dwordx2 v[178:179], %[o2], %[rd], %[na] offen sc1\n\t" \
    "buffer_load_dwordx2 v[180:181], %[o3], %[rd], %[na] offen sc1\n\t" \
    "buffer_load_dwordx2 v[182:183], %[o4], %[rd], %[na] offen sc1\n\t"
#define ASM_WCS \
    "v_mfma_f32_16x16x32_bf16 v[196:199], v[0:3], v[216:219], 0\n\t" \
    "v_mfma_f32_16x16x32_bf16 v[200:203], v[80:83], v[216:219], 0\n\t" \
    "v_mfma_f32_16x16x32_bf16 v[196:199], v[4:7], v[216:219], v[196:199]\n\t" \
    "v_mfma_f32_16x16x32_bf16 v[200:203], v[84:87], v[216:219], v[200:203]\n\t" \
    "v_mfma_f32_16x16x32_bf16 v[196:199], v[8:11], v[216:219], v[196:199]\n\t" \
    "v_mfma_f32_16x16x32_bf16 v[200:203], v[88:91], v[216:219], v[200:203]\n\t" \
    "v_mfma_f32_16x16x32_bf16 v[196:199], v[12:15], v[216:219], v[196:199]\n\t" \
    "v_mfma_f32_16x16x32_bf16 v[200:203], v[92:95], v[216:219], v[200:203]\n\t" \
    "v_mfma_f32_16x16x32_bf16 v[196:199], v[16:19], v[216:219], v[196:199]\n\t" \
    "v_mfma_f32_16x16x32_bf16 v[200:203], v[96:99], v[216:219], v[200:203]\n\t" \
    "v_mfma_f32_16x16x32_bf16 v[196:199], v[20:23], v[216:219], v[196:199]\n\t" \
    "v_mfma_f32_16x16x32_bf16 v[200:203], v[100:103], v[216:219], v[200:203]\n\t" \
    "v_mfma_f32_16x16x32_bf16 v[196:199], v[24:27], v[216:219], v[196:199]\n\t" \
    "v_mfma_f32_16x16x32_bf16 v[200:203], v[104:107], v[216:219], v[200:203]\n\t" \
    "v_mfma_f32_16x16x32_bf16 v[196:199], v[28:31], v[216:219], v[196:199]\n\t" \
    "v_mfma_f32_16x16x32_bf16 v[200:203], v[108:111], v[216:219], v[200:203]\n\t" \
    "v_mfma_f32_16x16x32_bf16 v[196:199], v[32:35], v[216:219], v[196:199]\n\t" \
    "v_mfma_f32_16x16x32_bf16 v[200:203], v[112:115], v[216:219], v[200:203]\n\t" \
    "v_mfma_f32_16x16x32_bf16 v[196:199], v[36:39], v[216:219], v[196:199]\n\t" \
    "v_mfma_f32_16x16x32_bf16 v[200:203], v[116:119], v[216:219], v[200:203]\n\t" \
    "v_mfma_f32_16x16x32_bf16 v[196:199], v[40:43], v[216:219], v[196:199]\n\t" \
    "v_mfma_f32_16x16x32_bf16 v[200:203], v[120:123], v[216:219], v[200:203]\n\t" \
    "v_mfma_f32_16x16x32_bf16 v[196:199], v[44:47], v[216:219], v[196:199]\n\t" \
    "v_mfma_f32_16x16x32_bf16 v[200:203], v[124:127], v[216:219], v[200:203]\n\t" \
    "v_mfma_f32_16x16x32_bf16 v[196:199], v[48:51], v[216:219], v[196:199]\n\t" \
    "v_mfma_f32_16x16x32_bf16 v[200:203], v[128:131], v[216:219], v[200:203]\n\t" \
    "v_mfma_f32_16x16x32_bf16 v[196:199], v[52:55], v[216:219], v[196:199]\n\t" \
    "v_mfma_f32_16x16x32_bf16 v[200:203], v[132:135], v[216:219], v[200:203]\n\t" \
    "v_mfma_f32_16x16x32_bf16 v[196:199], v[56:59], v[216:219], v[196:199]\n\t" \
    "v_mfma_f32_16x16x32_bf16 v[200:203], v[136:139], v[216:219], v[200:203]\n\t" \
    "v_mfma_f32_16x16x32_bf16 v[196:199], v[60:63], v[216:219], v[196:199]\n\t" \
    "v_mfma_f32_16x16x32_bf16 v[200:203], v[140:143], v[216:219], v[200:203]\n\t" \
    "v_mfma_f32_16x16x32_bf16 v[196:199], v[64:67], v[216:219], v[196:199]\n\t" \
    "v_mfma_f32_16x16x32_bf16 v[200:203], v[144:147], v[216:219], v[200:203]\n\t" \
    "v_mfma_f32_16x16x32_bf16 v[196:199], v[68:71], v[216:219], v[196:199]\n\t" \
    "v_mfma_f32_16x16x32_bf16 v[200:203], v[148:151], v[216:219], v[200:203]\n\t" \
    "v_mfma_f32_16x16x32_bf16 v[196:199], v[72:75], v[216:219], v[196:199]\n\t" \
    "v_mfma_f32_16x16x32_bf16 v[200:203], v[152:155], v[216:219], v[200:203]\n\t" \
    "v_mfma_f32_16x16x32_bf16 v[196:199], v[76:79], v[216:219], v[196:199]\n\t" \
    "v_mfma_f32_16x16x32_bf16 v[200:203], v[156:159], v[216:219], v[200:203]\n\t" \
    "s_nop 7\n\t"
#define ASM_WT1_LO \
    "s_branch .Lt0%=\n\t" \
    ".Ltr%=:\n\t" \
    "s_cmp_gt_i32 %[n], 0\n\t" \
    "s_cbranch_scc0 .Ltg%=\n\t" \
    ".Lfw%=:\n\t" \
    "s_mov_b32 %[a], 0\n\t" \
    "v_max_u32 v252, v232, v233\n\t" \
    "v_max3_u32 v252, v234, v235, v252\n\t" \
    "v_cmp_eq_u32 vcc, -1, v252\n\t" \
    "s_cbranch_vccz .Lgw0_%=\n\t" \
    "buffer_load_dwordx4 v[232:235], %[l16], %[rs], %[i0] offen offset:0 sc1\n\t" \
    "s_mov_b32 %[a], 1\n\t" \
    ".Lgw0_%=:\n\t" \
    "v_max_u32 v252, v236, v237\n\t" \
    "v_max3_u32 v252, v238, v239, v252\n\t" \
    "v_cmp_eq_u32 vcc, -1, v252\n\t" \
    "s_cbranch_vccz .Lgw1_%=\n\t" \
    "buffer_load_dwordx4 v[236:239], %[l16], %[rs], %[i0] offen offset:1024 sc1\n\t" \
    "s_mov_b32 %[a], 1\n\t" \
    ".Lgw1_%=:\n\t" \
    "v_max_u32 v252, v240, v241\n\t" \
    "v_max3_u32 v252, v242, v243, v252\n\t" \
    "v_cmp_eq_u32 vcc, -1, v252\n\t" \
    "s_cbranch_vccz .Lgw2_%=\n\t" \
    "buffer_load_dwordx4 v[240:243], %[l16], %[rs], %[i0] offen offset:2048 sc1\n\t" \
    "s_mov_b32 %[a], 1\n\t" \
    ".Lgw2_%=:\n\t" \
    "v_max_u32 v252, v244, v245\n\t" \
    "v_max3_u32 v252, v246, v247, v252\n\t" \
    "v_cmp_eq_u32 vcc, -1, v252\n\t" \
    "s_cbranch_vccz .Lgw3_%=\n\t" \
    "buffer_load_dwordx4 v[244:247], %[l16], %[rs], %[i0] offen offset:3072 sc1\n\t" \
    "s_mov_b32 %[a], 1\n\t" \
    ".Lgw3_%=:\n\t" \
    "v_max_u32 v252, v248, v249\n\t" \
    "v_max3_u32 v252, v250, v251, v252\n\t" \
    "v_cmp_eq_u32 vcc, -1, v252\n\t" \
    "s_cbranch_vccz .Lgw4_%=\n\t" \
    "buffer_load_dwordx4 v[248:251], %[l16], %[rs], %[i1] offen offset:0 sc1\n\t" \
    "s_mov_b32 %[a], 1\n\t" \
    ".Lgw4_%=:\n\t" \
    "s_cmp_eq_u32 %[a], 0\n\t" \
    "s_cbranch_scc1 .Lhw%=\n\t" \
    "s_waitcnt vmcnt(20)\n\t" \
    ".Liw%=:\n\t" \
    "v_max_u32 v252, v232, v233\n\t" \
    "v_max3_u32 v252, v234, v235, v252\n\t" \
    "v_max3_u32 v252, v236, v237, v252\n\t" \
    "v_max3_u32 v252, v238, v239, v252\n\t" \
    "v_max3_u32 v252, v240, v241, v252\n\t" \
    "v_max3_u32 v252, v242, v243, v252\n\t" \
    "v_max3_u32 v252, v244, v245, v252\n\t" \
    "v_max3_u32 v252, v246, v247, v252\n\t" \
    "v_max3_u32 v252, v248, v249, v252\n\t" \
    "v_max3_u32 v252, v250, v251, v252\n\t" \
    "v_cmp_eq_u32 vcc, -1, v252\n\t" \
    "s_cbranch_vccz .Lhw%=\n\t" \
    "s_sleep 1\n\t" \
    "v_max_u32 v252, v232, v233\n\t" \
    "v_max3_u32 v252, v234, v235, v252\n\t" \
    "v_max3_u32 v252, v236, v237, v252\n\t" \
    "v_max3_u32 v252, v238, v239, v252\n\t" \
    "v_max3_u32 v252, v240, v241, v252\n\t" \
    "v_max3_u32 v252, v242, v243, v252\n\t" \
    "v_max3_u32 v252, v244, v245, v252\n\t" \
    "v_max3_u32 v252, v246, v247, v252\n\t" \
    "v_max3_u32 v252, v248, v249, v252\n\t" \
    "v_max3_u32 v252, v250, v251, v252\n\t" \
    "v_cmp_eq_u32 vcc, -1, v252\n\t" \
    "s_cbranch_vccz .Lhw%=\n\t" \
    "s_sleep 1\n\t" \
    "v_max_u32 v252, v232, v233\n\t" \
    "v_max3_u32 v252, v234, v235, v252\n\t" \
    "v_max3_u32 v252, v236, v237, v252\n\t" \
    "v_max3_u32 v252, v238, v239, v252\n\t" \
    "v_max3_u32 v252, v240, v241, v252\n\t" \
    "v_max3_u32 v252, v242, v243, v252\n\t" \
    "v_max3_u32 v252, v244, v245, v252\n\t" \
    "v_max3_u32 v252, v246, v247, v252\n\t" \
    "v_max3_u32 v252, v248, v249, v252\n\t" \
    "v_max3_u32 v252, v250, v251, v252\n\t" \
    "v_cmp_eq_u32 vcc, -1, v252\n\t" \
    "s_cbranch_vccz .Lhw%=\n\t" \
    "s_sleep 1\n\t" \
    "v_max_u32 v252, v232, v233\n\t" \
    "v_max3_u32 v252, v234, v235, v252\n\t" \
    "v_max3_u32 v252, v236, v237, v252\n\t" \
    "v_max3_u32 v252, v238, v239, v252\n\t" \
    "v_max3_u32 v252, v240, v241, v252\n\t" \
    "v_max3_u32 v252, v242, v243, v252\n\t" \
    "v_max3_u32 v252, v244, v245, v252\n\t" \
    "v_max3_u32 v252, v246, v247, v252\n\t" \
    "v_max3_u32 v252, v248, v249, v252\n\t" \
    "v_max3_u32 v252, v250, v251, v252\n\t" \
    "v_cmp_eq_u32 vcc, -1, v252\n\t" \
    "s_cbranch_vccz .Lhw%=\n\t" \
    "s_sleep 1\n\t" \
    "v_max_u32 v252, v232, v233\n\t" \
    "v_max3_u32 v252, v234, v235, v252\n\t" \
    "v_max3_u32 v252, v236, v237, v252\n\t" \
    "v_max3_u32 v252, v238, v239, v252\n\t" \
    "v_max3_u32 v252, v240, v241, v252\n\t" \
    "v_max3_u32 v252, v242, v243, v252\n\t" \
    "v_max3_u32 v252, v244, v245, v252\n\t" \
    "v_max3_u32 v252, v246, v247, v252\n\t" \
    "v_max3_u32 v252, v248, v249, v252\n\t" \
    "v_max3_u32 v252, v250, v251, v252\n\t" \
    "v_cmp_eq_u32 vcc, -1, v252\n\t" \
    "s_cbranch_vccz .Lhw%=\n\t" \
    "s_sleep 1\n\t" \
    "v_max_u32 v252, v232, v233\n\t" \
    "v_max3_u32 v252, v234, v235, v252\n\t" \
    "v_max3_u32 v252, v236, v237, v252\n\t" \
    "v_max3_u32 v252, v238, v239, v252\n\t" \
    "v_max3_u32 v252, v240, v241, v252\n\t" \
    "v_max3_u32 v252, v242, v243, v252\n\t" \
    "v_max3_u32 v252, v244, v245, v252\n\t" \
    "v_max3_u32 v252, v246, v247, v252\n\t" \
    "v_max3_u32 v252, v248, v249, v252\n\t" \
    "v_max3_u32 v252, v250, v251, v252\n\t" \
    "v_cmp_eq_u32 vcc, -1, v252\n\t" \
    "s_cbranch_vccz .Lhw%=\n\t" \
    "s_sleep 1\n\t" \
    "s_sub_u32 %[n], %[n], 1\n\t" \
    "s_cmp_gt_i32 %[n], 0\n\t" \
    "s_cbranch_scc1 .Lfw%=\n\t" \
    ".Lhw%=:\n\t" \
    "s_branch .Lta%=\n\t" \
    ".Lt0%=:\n\t" \
    "s_waitcnt vmcnt(1)\n\t" \
    ".Lta%=:\n\t" \
    "ds_write_b128 %[lw], v[232:235] offset:0\n\t" \
    "v_max_u32 v252, v232, v233\n\t" \
    "v_max3_u32 v252, v234, v235, v252\n\t" \
    "v_mfma_f32_16x16x32_bf16 v[188:191], v[232:235], v[232:235], 0\n\t" \
    "ds_write_b128 %[lw], v[236:239] offset:1024\n\t" \
    "v_max3_u32 v252, v236, v237, v252\n\t" \
    "v_max3_u32 v252, v238, v239, v252\n\t" \
    "v_mfma_f32_16x16x32_bf16 v[188:191], v[236:239], v[236:239], v[188:191]\n\t" \
    "ds_write_b128 %[lw], v[240:243] offset:2048\n\t" \
    "v_max3_u32 v252, v240, v241, v252\n\t" \
    "v_max3_u32 v252, v242, v243, v252\n\t" \
    "v_mfma_f32_16x16x32_bf16 v[188:191], v[240:243], v[240:243], v[188:191]\n\t" \
    "ds_write_b128 %[lw], v[244:247] offset:3072\n\t" \
    "v_max3_u32 v252, v244, v245, v252\n\t" \
    "v_max3_u32 v252, v246, v247, v252\n\t" \
    "v_mfma_f32_16x16x32_bf16 v[188:191], v[244:247], v[244:247], v[188:191]\n\t" \
    "ds_write_b128 %[lw], v[248:251] offset:4096\n\t" \
    "v_max3_u32 v252, v248, v249, v252\n\t" \
    "v_max3_u32 v252, v250, v251, v252\n\t" \
    "v_mfma_f32_16x16x32_bf16 v[188:191], v[248:251], v[248:251], v[188:191]\n\t" \
    "v_mfma_f32_16x16x32_bf16 v[192:195], v[216:219], v[232:235], 0\n\t" \
    "v_mfma_f32_16x16x32_bf16 v[192:195], v[216:219], v[236:239], v[192:195]\n\t" \
    "v_mfma_f32_16x16x32_bf16 v[192:195], v[216:219], v[240:243], v[192:195]\n\t" \
    "v_mfma_f32_16x16x32_bf16 v[192:195], v[216:219], v[244:247], v[192:195]\n\t" \
    "v_mfma_f32_16x16x32_bf16 v[192:195], v[216:219], v[248:251], v[192:195]\n\t" \
    "v_cmp_eq_u32 vcc, -1, v252\n\t" \
    "s_cbranch_vccnz .Ltr%=\n\t" \
    ".Ltg%=:\n\t" \
    ASM_ST2 \
    "s_nop 5\n\t" \
    "ds_write_b128 %[lw], v[188:191] offset:5120\n\t" \
    "ds_write_b128 %[lw], v[192:195] offset:6144\n\t" \
    "s_waitcnt lgkmcnt(0)\n\t" \
    "s_setprio 2\n\t" \
    "s_barrier\n\t" ASM_ST \
    "ds_read_b128 v[160:163], %[la] offset:0\n\t" \
    "ds_read_b128 v[164:167], %[la] offset:1024\n\t" \
    "ds_read_b128 v[168:171], %[la] offset:2048\n\t" \
    "ds_read2st64_b32 v[204:205], %[sr] offset0:20 offset1:24\n\t" \
    "ds_read_b128 v[172:175], %[la] offset:3072\n\t" \
    "ds_read_b128 v[176:179], %[la] offset:8192\n\t" \
    "ds_read_b128 v[180:183], %[la] offset:9216\n\t" \
    "ds_read_b128 v[184:187], %[la] offset:10240\n\t" \
    "s_waitcnt lgkmcnt(7)\n\t" \
    "v_mfma_f32_16x16x32_bf16 v[192:195], v[80:83], v[160:163], 0\n\t" \
    "s_waitcnt lgkmcnt(6)\n\t" \
    "v_mfma_f32_16x16x32_bf16 v[192:195], v[84:87], v[164:167], v[192:195]\n\t" \
    "v_mfma_f32_16x16x32_bf16 v[188:191], v[0:3], v[160:163], 0\n\t" \
    "ds_read_b128 v[160:163], %[la] offset:11264\n\t" \
    "v_mfma_f32_16x16x32_bf16 v[188:191], v[4:7], v[164:167], v[188:191]\n\t" \
    "ds_read_b128 v[164:167], %[la] offset:16384\n\t" \
    "s_waitcnt lgkmcnt(7)\n\t" \
    "v_mfma_f32_16x16x32_bf16 v[188:191], v[8:11], v[168:171], v[188:191]\n\t" \
    "s_waitcnt lgkmcnt(6)\n\t" \
    "s_waitcnt lgkmcnt(5)\n\t" \
    "v_mfma_f32_16x16x32_bf16 v[188:191], v[12:15], v[172:175], v[188:191]\n\t" \
    "v_permlane16_swap_b32 v204, v205\n\t" \
    "v_mfma_f32_16x16x32_bf16 v[192:195], v[88:91], v[168:171], v[192:195]\n\t" \
    "ds_read_b128 v[168:171], %[la] offset:17408\n\t" \
    "v_mfma_f32_16x16x32_bf16 v[192:195], v[92:95], v[172:175], v[192:195]\n\t" \
    "ds_read_b128 v[172:175], %[la] offset:18432\n\t" \
    "v_add_f32 v206, v204, v205\n\t" \
    "s_waitcnt lgkmcnt(6)\n\t" \
    "v_mfma_f32_16x16x32_bf16 v[192:195], v[96:99], v[176:179], v[192:195]\n\t" \
    "v_add_f32 v207, v204, v205\n\t" \
    "s_waitcnt lgkmcnt(5)\n\t" \
    "v_mfma_f32_16x16x32_bf16 v[192:195], v[100:103], v[180:183], v[192:195]\n\t" \
    "v_mfma_f32_16x16x32_bf16 v[188:191], v[16:19], v[176:179], v[188:191]\n\t" \
    "ds_read_b128 v[176:179], %[la] offset:19456\n\t" \
    "v_permlane32_swap_b32 v206, v207\n\t" \
    "v_mfma_f32_16x16x32_bf16 v[188:191], v[20:23], v[180:183], v[188:191]\n\t" \
    "ds_read_b128 v[180:183], %[la] offset:24576\n\t" \
    "s_waitcnt lgkmcnt(6)\n\t" \
    "v_mfma_f32_16x16x32_bf16 v[188:191], v[24:27], v[184:187], v[188:191]\n\t" \
    "v_add_f32 v204, v206, v207\n\t" \
    "s_waitcnt lgkmcnt(5)\n\t" \
    "v_mfma_f32_16x16x32_bf16 v[188:191], v[28:31], v[160:163], v[188:191]\n\t" \
    "v_add_f32 v205, v206, v207\n\t" \
    "v_mfma_f32_16x16x32_bf16 v[192:195], v[104:107], v[184:187], v[192:195]\n\t" \
    "ds_read_b128 v[184:187], %[la] offset:25600\n\t" \
    "v_mfma_f32_16x16x32_bf16 v[192:195], v[108:111], v[160:163], v[192:195]\n\t" \
    "ds_read_b128 v[160:163], %[la] offset:26624\n\t" \
    "v_permlane16_swap_b32 v204, v205\n\t" \
    "s_waitcnt lgkmcnt(6)\n\t" \
    "v_mfma_f32_16x16x32_bf16 v[192:195], v[112:115], v[164:167], v[192:195]\n\t" \
    "s_waitcnt lgkmcnt(5)\n\t" \
    "v_mfma_f32_16x16x32_bf16 v[192:195], v[116:119], v[168:171], v[192:195]\n\t" \
    "v_mul_f32 v205, %[kd], v205\n\t" \
    "v_mfma_f32_16x16x32_bf16 v[188:191], v[32:35], v[164:167], v[188:191]\n\t" \
    "ds_read_b128 v[164:167], %[la] offset:27648\n\t" \
    "v_mul_f32 v204, %[kd], v204\n\t" \
    "v_mfma_f32_16x16x32_bf16 v[188:191], v[36:39], v[168:171], v[188:191]\n\t" \
    "ds_read_b128 v[168:171], %[la] offset:4096\n\t" \
    "v_fma_f32 v204, -v205, v205, v204\n\t" \
    "s_waitcnt lgkmcnt(6)\n\t" \
    "v_mfma_f32_16x16x32_bf16 v[188:191], v[40:43], v[172:175], v[188:191]\n\t" \
    "v_max_f32 v204, 0, v204\n\t" \
    "s_waitcnt lgkmcnt(5)\n\t" \
    "v_mfma_f32_16x16x32_bf16 v[188:191], v[44:47], v[176:179], v[188:191]\n\t" \
    "v_add_f32 v206, %[ke], v204\n\t" \
    "v_mfma_f32_16x16x32_bf16 v[192:195], v[120:123], v[172:175], v[192:195]\n\t" \
    "ds_read_b128 v[172:175], %[la] offset:12288\n\t" \
    "v_rsq_f32 v206, v206\n\t" \
    "v_mfma_f32_16x16x32_bf16 v[192:195], v[124:127], v[176:179], v[192:195]\n\t" \
    "ds_read_b128 v[176:179], %[la] offset:20480\n\t" \
    "s_waitcnt lgkmcnt(6)\n\t" \
    "v_mfma_f32_16x16x32_bf16 v[192:195], v[128:131], v[180:183], v[192:195]\n\t" \
    "v_mul_f32 v207, v204, v206\n\t" \
    "s_waitcnt lgkmcnt(5)\n\t" \
    "v_mfma_f32_16x16x32_bf16 v[192:195], v[132:135], v[184:187], v[192:195]\n\t" \
    "v_fma_f32 v207, v207, v206, %[ke]\n\t" \
    "v_mfma_f32_16x16x32_bf16 v[188:191], v[48:51], v[180:183], v[188:191]\n\t" \
    "ds_read_b128 v[180:183], %[la] offset:28672\n\t" \
    "v_rsq_f32 v207, v207\n\t" \
    "v_mfma_f32_16x16x32_bf16 v[188:191], v[52:55], v[184:187], v[188:191]\n\t" \
    "v_mul_f32 v206, v206, v207\n\t" \
    "v_mul_f32 v207, -v205, v206\n\t" \
    "s_waitcnt lgkmcnt(5)\n\t" \
    "v_mfma_f32_16x16x32_bf16 v[188:191], v[56:59], v[160:163], v[188:191]\n\t" \
    "v_mul_f32 v208, v196, v207\n\t" \
    "v_mul_f32 v209, v197, v207\n\t" \
    "s_waitcnt lgkmcnt(4)\n\t" \
    "v_mfma_f32_16x16x32_bf16 v[188:191], v[60:63], v[164:167], v[188:191]\n\t" \
    "v_mul_f32 v210, v198, v207\n\t" \
    "v_mul_f32 v211, v199, v207\n\t" \
    "v_mfma_f32_16x16x32_bf16 v[192:195], v[136:139], v[160:163], v[192:195]\n\t" \
    "v_mul_f32 v212, v200, v207\n\t" \
    "v_mul_f32 v213, v201, v207\n\t" \
    "v_mfma_f32_16x16x32_bf16 v[192:195], v[140:143], v[164:167], v[192:195]\n\t" \
    "v_mul_f32 v214, v202, v207\n\t" \
    "v_mul_f32 v215, v203, v207\n\t" \
    "s_waitcnt lgkmcnt(3)\n\t" \
    "v_mfma_f32_16x16x32_bf16 v[192:195], v[144:147], v[168:171], v[192:195]\n\t" \
    "s_cmp_lg_u32 %[sw], 0\n\t" \
    "s_cbranch_scc1 .Lts%=\n\t" \
    "v_cvt_pk_bf16_f32 v217, v204, v204\n\t" \
    "v_lshlrev_b32 v217, 16, v217\n\t" \
    "v_add_f32 v217, %[ke], v217\n\t" \
    "v_cvt_pk_bf16_f32 v217, v217, v217\n\t" \
    "v_lshlrev_b32 v217, 16, v217\n\t" \
    "v_sqrt_f32 v217, v217\n\t" \
    "v_cvt_pk_bf16_f32 v216, v205, v205\n\t" \
    "v_lshlrev_b32 v216, 16, v216\n\t" \
    "v_cvt_pk_bf16_f32 v217, v217, v217\n\t" \
    "v_lshlrev_b32 v217, 16, v217\n\t" \
    "v_rcp_f32 v217, v217\n\t" \
    "v_lshrrev_b32 v218, 1, %[l16]\n\t" \
    "v_and_b32 v218, 0x78, v218\n\t" \
    "buffer_store_dwordx2 v[216:217], v218, %[rs], %[ss] offen sc1\n\t" \
    ".Lts%=:\n\t" \
    "v_mov_b64 v[216:217], -1\n\t" \
    "s_waitcnt lgkmcnt(2)\n\t" \
    "v_mfma_f32_16x16x32_bf16 v[192:195], v[148:151], v[172:175], v[192:195]\n\t" \
    "v_mov_b64 v[218:219], -1\n\t" \
    "buffer_store_dwordx4 v[216:219], %[ob], %[rs], %[sa] offen sc1\n\t" \
    "v_mfma_f32_16x16x32_bf16 v[188:191], v[64:67], v[168:171], v[188:191]\n\t" \
    "v_mfma_f32_16x16x32_bf16 v[188:191], v[68:71], v[172:175], v[188:191]\n\t" \
    "s_waitcnt lgkmcnt(1)\n\t" \
    "v_mfma_f32_16x16x32_bf16 v[188:191], v[72:75], v[176:179], v[188:191]\n\t" \
    "s_waitcnt lgkmcnt(0)\n\t" \
    "v_mfma_f32_16x16x32_bf16 v[188:191], v[76:79], v[180:183], v[188:191]\n\t" \
    "v_mfma_f32_16x16x32_bf16 v[192:195], v[152:155], v[176:179], v[192:195]\n\t" \
    "v_mfma_f32_16x16x32_bf16 v[192:195], v[156:159], v[180:183], v[192:195]\n\t" \
    "s_setprio 2\n\t" \
    "buffer_load_dwordx4 v[232:235], %[l16], %[rs], %[n0] offen offset:0 sc1\n\t" \
    "buffer_load_dwordx4 v[236:239], %[l16], %[rs], %[n0] offen offset:1024 sc1\n\t" \
    "buffer_load_dwordx4 v[240:243], %[l16], %[rs], %[n0] offen offset:2048 sc1\n\t" \
    "buffer_load_dwordx4 v[244:247], %[l16], %[rs], %[n0] offen offset:3072 sc1\n\t" \
    "buffer_load_dwordx4 v[248:251], %[l16], %[rs], %[n1] offen offset:0 sc1\n\t" \
    ASM_ST3 \
    "v_fma_f32 v188, v188, v206, v208\n\t" \
    "v_fma_f32 v189, v189, v206, v209\n\t" \
    "v_fma_f32 v190, v190, v206, v210\n\t" \
    "v_fma_f32 v191, v191, v206, v211\n\t" \
    "v_mul_f32 v160, v188, v188\n\t" \
    "v_mul_f32 v161, v189, v189\n\t" \
    "v_mul_f32 v162, v190, v190\n\t" \
    "v_mul_f32 v163, v191, v191\n\t" \
    "v_fma_f32 v160, v160, %[k1], v220\n\t" \
    "v_fma_f32 v161, v161, %[k1], v220\n\t" \
    "v_fma_f32 v162, v162, %[k1], v220\n\t" \
    "v_fma_f32 v163, v163, %[k1], v220\n\t" \
    "v_mul_f32 v160, v188, v160\n\t" \
    "v_mul_f32 v161, v189, v161\n\t" \
    "v_mul_f32 v162, v190, v162\n\t" \
    "v_mul_f32 v163, v191, v163\n\t" \
    "v_fma_f32 v192, v192, v206, v212\n\t" \
    "v_fma_f32 v193, v193, v206, v213\n\t" \
    "v_fma_f32 v194, v194, v206, v214\n\t" \
    "v_fma_f32 v195, v195, v206, v215\n\t" \
    "v_mul_f32 v164, v192, v192\n\t" \
    "v_mul_f32 v165, v193, v193\n\t" \
    "v_mul_f32 v166, v194, v194\n\t" \
    "v_mul_f32 v167, v195, v195\n\t" \
    "v_fma_f32 v164, v164, %[k1], v220\n\t" \
    "v_fma_f32 v165, v165, %[k1], v220\n\t" \
    "v_fma_f32 v166, v166, %[k1], v220\n\t" \
    "v_fma_f32 v167, v167, %[k1], v220\n\t" \
    "v_mul_f32 v164, v192, v164\n\t" \
    "v_mul_f32 v165, v193, v165\n\t" \
    "v_mul_f32 v166, v194, v166\n\t" \
    "v_mul_f32 v167, v195, v167\n\t" \
    "v_exp_f32 v160, v160\n\t" \
    "v_exp_f32 v161, v161\n\t" \
    "v_exp_f32 v162, v162\n\t" \
    "v_exp_f32 v163, v163\n\t" \
    "v_exp_f32 v164, v164\n\t" \
    "v_exp_f32 v165, v165\n\t" \
    "v_exp_f32 v166, v166\n\t" \
    "v_exp_f32 v167, v167\n\t" \
    "v_add_f32 v160, 1.0, v160\n\t" \
    "v_add_f32 v161, 1.0, v161\n\t" \
    "v_add_f32 v162, 1.0, v162\n\t" \
    "v_add_f32 v163, 1.0, v163\n\t" \
    "v_add_f32 v164, 1.0, v164\n\t" \
    "v_add_f32 v165, 1.0, v165\n\t" \
    "v_add_f32 v166, 1.0, v166\n\t" \
    "v_add_f32 v167, 1.0, v167\n\t" \
    "v_rcp_f32 v160, v160\n\t" \
    "v_rcp_f32 v161, v161\n\t" \
    "v_rcp_f32 v162, v162\n\t" \
    "v_rcp_f32 v163, v163\n\t" \
    "v_rcp_f32 v164, v164\n\t" \
    "v_rcp_f32 v165, v165\n\t" \
    "v_rcp_f32 v166, v166\n\t" \
    "v_rcp_f32 v167, v167\n\t" \
    "v_mul_f32 v188, v188, v160\n\t" \
    "v_mul_f32 v189, v189, v161\n\t" \
    "v_mul_f32 v190, v190, v162\n\t" \
    "v_mul_f32 v191, v191, v163\n\t" \
    "v_mul_f32 v192, v192, v164\n\t" \
    "v_mul_f32 v193, v193, v165\n\t" \
    "v_mul_f32 v194, v194, v166\n\t" \
    "v_mul_f32 v195, v195, v167\n\t" \
    "v_cvt_pk_bf16_f32 v188, v188, v189\n\t" \
    "v_cvt_pk_bf16_f32 v189, v190, v191\n\t" \
    "v_cvt_pk_bf16_f32 v190, v192, v193\n\t" \
    "v_cvt_pk_bf16_f32 v191, v194, v195\n\t" \
    "v_min_u32 v188, -2, v188\n\t" \
    "v_min_u32 v189, -2, v189\n\t" \
    "v_min_u32 v190, -2, v190\n\t" \
    "v_min_u32 v191, -2, v191\n\t" \
    "v_mov_b32 v216, 0x3f803f80\n\t" \
    "v_mov_b32 v217, 0x3f803f80\n\t" \
    "v_mov_b32 v218, 0x3f803f80\n\t" \
    "v_mov_b32 v219, 0x3f803f80\n\t" \
    "v_permlane16_swap_b32 v188, v190\n\t" \
    "v_permlane16_swap_b32 v189, v191\n\t" \
    "s_waitcnt vmcnt(6)\n\t" \
    "buffer_store_dwordx4 v[188:191], %[oa], %[rs], %[so] offen sc1\n\t" \
    "s_nop 1\n\t" \
    "s_setprio 0\n\t"
#define ASM_WT1_HI \
    "s_setprio 1\n\t" \
    "s_barrier\n\t" ASM_ST \
    "ds_read_b128 v[160:163], %[la] offset:0\n\t" \
    "ds_read_b128 v[164:167], %[la] offset:1024\n\t" \
    "ds_read_b128 v[168:171], %[la] offset:2048\n\t" \
    "ds_read2st64_b32 v[204:205], %[sr] offset0:20 offset1:24\n\t" \
    "ds_read_b128 v[172:175], %[la] offset:3072\n\t" \
    "ds_read_b128 v[176:179], %[la] offset:8192\n\t" \
    "ds_read_b128 v[180:183], %[la] offset:9216\n\t" \
    "ds_read_b128 v[184:187], %[la] offset:10240\n\t" \
    "s_waitcnt lgkmcnt(7)\n\t" \
    "v_mfma_f32_16x16x32_bf16 v[192:195], v[80:83], v[160:163], 0\n\t" \
    "s_waitcnt lgkmcnt(6)\n\t" \
    "v_mfma_f32_16x16x32_bf16 v[192:195], v[84:87], v[164:167], v[192:195]\n\t" \
    "v_mfma_f32_16x16x32_bf16 v[188:191], v[0:3], v[160:163], 0\n\t" \
    "ds_read_b128 v[160:163], %[la] offset:11264\n\t" \
    "v_mfma_f32_16x16x32_bf16 v[188:191], v[4:7], v[164:167], v[188:191]\n\t" \
    "ds_read_b128 v[164:167], %[la] offset:16384\n\t" \
    "s_waitcnt lgkmcnt(7)\n\t" \
    "v_mfma_f32_16x16x32_bf16 v[188:191], v[8:11], v[168:171], v[188:191]\n\t" \
    "s_waitcnt lgkmcnt(6)\n\t" \
    "s_waitcnt lgkmcnt(5)\n\t" \
    "v_mfma_f32_16x16x32_bf16 v[188:191], v[12:15], v[172:175], v[188:191]\n\t" \
    "v_permlane16_swap_b32 v204, v205\n\t" \
    "v_mfma_f32_16x16x32_bf16 v[192:195], v[88:91], v[168:171], v[192:195]\n\t" \
    "ds_read_b128 v[168:171], %[la] offset:17408\n\t" \
    "v_mfma_f32_16x16x32_bf16 v[192:195], v[92:95], v[172:175], v[192:195]\n\t" \
    "ds_read_b128 v[172:175], %[la] offset:18432\n\t" \
    "v_add_f32 v206, v204, v205\n\t" \
    "s_waitcnt lgkmcnt(6)\n\t" \
    "v_mfma_f32_16x16x32_bf16 v[192:195], v[96:99], v[176:179], v[192:195]\n\t" \
    "v_add_f32 v207, v204, v205\n\t" \
    "s_waitcnt lgkmcnt(5)\n\t" \
    "v_mfma_f32_16x16x32_bf16 v[192:195], v[100:103], v[180:183], v[192:195]\n\t" \
    "v_mfma_f32_16x16x32_bf16 v[188:191], v[16:19], v[176:179], v[188:191]\n\t" \
    "ds_read_b128 v[176:179], %[la] offset:19456\n\t" \
    "v_permlane32_swap_b32 v206, v207\n\t" \
    "v_mfma_f32_16x16x32_bf16 v[188:191], v[20:23], v[180:183], v[188:191]\n\t" \
    "ds_read_b128 v[180:183], %[la] offset:24576\n\t" \
    "s_waitcnt lgkmcnt(6)\n\t" \
    "v_mfma_f32_16x16x32_bf16 v[188:191], v[24:27], v[184:187], v[188:191]\n\t" \
    "v_add_f32 v204, v206, v207\n\t" \
    "s_waitcnt lgkmcnt(5)\n\t" \
    "v_mfma_f32_16x16x32_bf16 v[188:191], v[28:31], v[160:163], v[188:191]\n\t" \
    "v_add_f32 v205, v206, v207\n\t" \
    "v_mfma_f32_16x16x32_bf16 v[192:195], v[104:107], v[184:187], v[192:195]\n\t" \
    "ds_read_b128 v[184:187], %[la] offset:25600\n\t" \
    "v_mfma_f32_16x16x32_bf16 v[192:195], v[108:111], v[160:163], v[192:195]\n\t" \
    "ds_read_b128 v[160:163], %[la] offset:26624\n\t" \
    "v_permlane16_swap_b32 v204, v205\n\t" \
    "s_waitcnt lgkmcnt(6)\n\t" \
    "v_mfma_f32_16x16x32_bf16 v[192:195], v[112:115], v[164:167], v[192:195]\n\t" \
    "s_waitcnt lgkmcnt(5)\n\t" \
    "v_mfma_f32_16x16x32_bf16 v[192:195], v[116:119], v[168:171], v[192:195]\n\t" \
    "v_mul_f32 v205, %[kd], v205\n\t" \
    "v_mfma_f32_16x16x32_bf16 v[188:191], v[32:35], v[164:167], v[188:191]\n\t" \
    "ds_read_b128 v[164:167], %[la] offset:27648\n\t" \
    "v_mul_f32 v204, %[kd], v204\n\t" \
    "v_mfma_f32_16x16x32_bf16 v[188:191], v[36:39], v[168:171], v[188:191]\n\t" \
    "ds_read_b128 v[168:171], %[la] offset:4096\n\t" \
    "v_fma_f32 v204, -v205, v205, v204\n\t" \
    "s_waitcnt lgkmcnt(6)\n\t" \
    "v_mfma_f32_16x16x32_bf16 v[188:191], v[40:43], v[172:175], v[188:191]\n\t" \
    "v_max_f32 v204, 0, v204\n\t" \
    "s_waitcnt lgkmcnt(5)\n\t" \
    "v_mfma_f32_16x16x32_bf16 v[188:191], v[44:47], v[176:179], v[188:191]\n\t" \
    "v_add_f32 v206, %[ke], v204\n\t" \
    "v_mfma_f32_16x16x32_bf16 v[192:195], v[120:123], v[172:175], v[192:195]\n\t" \
    "ds_read_b128 v[172:175], %[la] offset:12288\n\t" \
    "v_rsq_f32 v206, v206\n\t" \
    "v_mfma_f32_16x16x32_bf16 v[192:195], v[124:127], v[176:179], v[192:195]\n\t" \
    "ds_read_b128 v[176:179], %[la] offset:20480\n\t" \
    "s_waitcnt lgkmcnt(6)\n\t" \
    "v_mfma_f32_16x16x32_bf16 v[192:195], v[128:131], v[180:183], v[192:195]\n\t" \
    "v_mul_f32 v207, v204, v206\n\t" \
    "s_waitcnt lgkmcnt(5)\n\t" \
    "v_mfma_f32_16x16x32_bf16 v[192:195], v[132:135], v[184:187], v[192:195]\n\t" \
    "v_fma_f32 v207, v207, v206, %[ke]\n\t" \
    "v_mfma_f32_16x16x32_bf16 v[188:191], v[48:51], v[180:183], v[188:191]\n\t" \
    "ds_read_b128 v[180:183], %[la] offset:28672\n\t" \
    "v_rsq_f32 v207, v207\n\t" \
    "v_mfma_f32_16x16x32_bf16 v[188:191], v[52:55], v[184:187], v[188:191]\n\t" \
    "v_mul_f32 v206, v206, v207\n\t" \
    "v_mul_f32 v207, -v205, v206\n\t" \
    "s_waitcnt lgkmcnt(5)\n\t" \
    "v_mfma_f32_16x16x32_bf16 v[188:191], v[56:59], v[160:163], v[188:191]\n\t" \
    "v_mul_f32 v208, v196, v207\n\t" \
    "v_mul_f32 v209, v197, v207\n\t" \
    "s_waitcnt lgkmcnt(4)\n\t" \
    "v_mfma_f32_16x16x32_bf16 v[188:191], v[60:63], v[164:167], v[188:191]\n\t" \
    "v_mul_f32 v210, v198, v207\n\t" \
    "v_mul_f32 v211, v199, v207\n\t" \
    "v_mfma_f32_16x16x32_bf16 v[192:195], v[136:139], v[160:163], v[192:195]\n\t" \
    "v_mul_f32 v212, v200, v207\n\t" \
    "v_mul_f32 v213, v201, v207\n\t" \
    "v_mfma_f32_16x16x32_bf16 v[192:195], v[140:143], v[164:167], v[192:195]\n\t" \
    "v_mul_f32 v214, v202, v207\n\t" \
    "v_mul_f32 v215, v203, v207\n\t" \
    "s_waitcnt lgkmcnt(3)\n\t" \
    "v_mfma_f32_16x16x32_bf16 v[192:195], v[144:147], v[168:171], v[192:195]\n\t" \
    "v_mov_b64 v[216:217], -1\n\t" \
    "v_mov_b64 v[218:219], -1\n\t" \
    "s_waitcnt lgkmcnt(2)\n\t" \
    "v_mfma_f32_16x16x32_bf16 v[192:195], v[148:151], v[172:175], v[192:195]\n\t" \
    "buffer_store_dwordx4 v[216:219], %[ob], %[rs], %[sa] offen sc1\n\t" \
    "v_mfma_f32_16x16x32_bf16 v[188:191], v[64:67], v[168:171], v[188:191]\n\t" \
    "v_mfma_f32_16x16x32_bf16 v[188:191], v[68:71], v[172:175], v[188:191]\n\t" \
    "s_waitcnt lgkmcnt(1)\n\t" \
    "v_mfma_f32_16x16x32_bf16 v[188:191], v[72:75], v[176:179], v[188:191]\n\t" \
    "s_waitcnt lgkmcnt(0)\n\t" \
    "v_mfma_f32_16x16x32_bf16 v[188:191], v[76:79], v[180:183], v[188:191]\n\t" \
    "v_mfma_f32_16x16x32_bf16 v[192:195], v[152:155], v[176:179], v[192:195]\n\t" \
    "v_mfma_f32_16x16x32_bf16 v[192:195], v[156:159], v[180:183], v[192:195]\n\t" \
    ASM_ST3 \
    "s_nop 1\n\t" \
    "v_fma_f32 v188, v188, v206, v208\n\t" \
    "v_fma_f32 v189, v189, v206, v209\n\t" \
    "v_fma_f32 v190, v190, v206, v210\n\t" \
    "v_fma_f32 v191, v191, v206, v211\n\t" \
    "v_mul_f32 v160, v188, v188\n\t" \
    "v_mul_f32 v161, v189, v189\n\t" \
    "v_mul_f32 v162, v190, v190\n\t" \
    "v_mul_f32 v163, v191, v191\n\t" \
    "v_fma_f32 v160, v160, %[k1], v220\n\t" \
    "v_fma_f32 v161, v161, %[k1], v220\n\t" \
    "v_fma_f32 v162, v162, %[k1], v220\n\t" \
    "v_fma_f32 v163, v163, %[k1], v220\n\t" \
    "v_mul_f32 v160, v188, v160\n\t" \
    "v_mul_f32 v161, v189, v161\n\t" \
    "v_mul_f32 v162, v190, v162\n\t" \
    "v_mul_f32 v163, v191, v163\n\t" \
    "v_fma_f32 v192, v192, v206, v212\n\t" \
    "v_fma_f32 v193, v193, v206, v213\n\t" \
    "v_fma_f32 v194, v194, v206, v214\n\t" \
    "v_fma_f32 v195, v195, v206, v215\n\t" \
    "v_mul_f32 v164, v192, v192\n\t" \
    "v_mul_f32 v165, v193, v193\n\t" \
    "v_mul_f32 v166, v194, v194\n\t" \
    "v_mul_f32 v167, v195, v195\n\t" \
    "v_fma_f32 v164, v164, %[k1], v220\n\t" \
    "v_fma_f32 v165, v165, %[k1], v220\n\t" \
    "v_fma_f32 v166, v166, %[k1], v220\n\t" \
    "v_fma_f32 v167, v167, %[k1], v220\n\t" \
    "v_mul_f32 v164, v192, v164\n\t" \
    "v_mul_f32 v165, v193, v165\n\t" \
    "v_mul_f32 v166, v194, v166\n\t" \
    "v_mul_f32 v167, v195, v167\n\t" \
    "v_exp_f32 v160, v160\n\t" \
    "v_exp_f32 v161, v161\n\t" \
    "v_exp_f32 v162, v162\n\t" \
    "v_exp_f32 v163, v163\n\t" \
    "v_exp_f32 v164, v164\n\t" \
    "v_exp_f32 v165, v165\n\t" \
    "v_exp_f32 v166, v166\n\t" \
    "v_exp_f32 v167, v167\n\t" \
    "v_add_f32 v160, 1.0, v160\n\t" \
    "v_add_f32 v161, 1.0, v161\n\t" \
    "v_add_f32 v162, 1.0, v162\n\t" \
    "v_add_f32 v163, 1.0, v163\n\t" \
    "v_add_f32 v164, 1.0, v164\n\t" \
    "v_add_f32 v165, 1.0, v165\n\t" \
    "v_add_f32 v166, 1.0, v166\n\t" \
    "v_add_f32 v167, 1.0, v167\n\t" \
    "v_rcp_f32 v160, v160\n\t" \
    "v_rcp_f32 v161, v161\n\t" \
    "v_rcp_f32 v162, v162\n\t" \
    "v_rcp_f32 v163, v163\n\t" \
    "v_rcp_f32 v164, v164\n\t" \
    "v_rcp_f32 v165, v165\n\t" \
    "v_rcp_f32 v166, v166\n\t" \
    "v_rcp_f32 v167, v167\n\t" \
    "v_mul_f32 v188, v188, v160\n\t" \
    "v_mul_f32 v189, v189, v161\n\t" \
    "v_mul_f32 v190, v190, v162\n\t" \
    "v_mul_f32 v191, v191, v163\n\t" \
    "v_mul_f32 v192, v192, v164\n\t" \
    "v_mul_f32 v193, v193, v165\n\t" \
    "v_mul_f32 v194, v194, v166\n\t" \
    "v_mul_f32 v195, v195, v167\n\t" \
    "v_cvt_pk_bf16_f32 v188, v188, v189\n\t" \
    "v_cvt_pk_bf16_f32 v189, v190, v191\n\t" \
    "v_cvt_pk_bf16_f32 v190, v192, v193\n\t" \
    "v_cvt_pk_bf16_f32 v191, v194, v195\n\t" \
    "v_min_u32 v188, -2, v188\n\t" \
    "v_min_u32 v189, -2, v189\n\t" \
    "v_min_u32 v190, -2, v190\n\t" \
    "v_min_u32 v191, -2, v191\n\t" \
    "s_nop 1\n\t" \
    "v_permlane16_swap_b32 v188, v190\n\t" \
    "v_permlane16_swap_b32 v189, v191\n\t" \
    "s_waitcnt vmcnt(1)\n\t" \
    "buffer_store_dwordx4 v[188:191], %[oa], %[rs], %[so] offen sc1\n\t" \
    "s_nop 1\n\t"
#define ASM_WT2_LO \
    "s_branch .Lt0%=\n\t" \
    ".Ltr%=:\n\t" \
    "s_cmp_gt_i32 %[n], 0\n\t" \
    "s_cbranch_scc0 .Ltg%=\n\t" \
    ".Lfw%=:\n\t" \
    "s_mov_b32 %[a], 0\n\t" \
    "v_max_u32 v252, v232, v233\n\t" \
    "v_max3_u32 v252, v234, v235, v252\n\t" \
    "v_cmp_eq_u32 vcc, -1, v252\n\t" \
    "s_cbranch_vccz .Lgw0_%=\n\t" \
    "buffer_load_dwordx4 v[232:235], %[l16], %[rs], %[i0] offen offset:0 sc1\n\t" \
    "s_mov_b32 %[a], 1\n\t" \
    ".Lgw0_%=:\n\t" \
    "v_max_u32 v252, v236, v237\n\t" \
    "v_max3_u32 v252, v238, v239, v252\n\t" \
    "v_cmp_eq_u32 vcc, -1, v252\n\t" \
    "s_cbranch_vccz .Lgw1_%=\n\t" \
    "buffer_load_dwordx4 v[236:239], %[l16], %[rs], %[i0] offen offset:1024 sc1\n\t" \
    "s_mov_b32 %[a], 1\n\t" \
    ".Lgw1_%=:\n\t" \
    "v_max_u32 v252, v240, v241\n\t" \
    "v_max3_u32 v252, v242, v243, v252\n\t" \
    "v_cmp_eq_u32 vcc, -1, v252\n\t" \
    "s_cbranch_vccz .Lgw2_%=\n\t" \
    "buffer_load_dwordx4 v[240:243], %[l16], %[rs], %[i0] offen offset:2048 sc1\n\t" \
    "s_mov_b32 %[a], 1\n\t" \
    ".Lgw2_%=:\n\t" \
    "v_max_u32 v252, v244, v245\n\t" \
    "v_max3_u32 v252, v246, v247, v252\n\t" \
    "v_cmp_eq_u32 vcc, -1, v252\n\t" \
    "s_cbranch_vccz .Lgw3_%=\n\t" \
    "buffer_load_dwordx4 v[244:247], %[l16], %[rs], %[i0] offen offset:3072 sc1\n\t" \
    "s_mov_b32 %[a], 1\n\t" \
    ".Lgw3_%=:\n\t" \
    "v_max_u32 v252, v248, v249\n\t" \
    "v_max3_u32 v252, v250, v251, v252\n\t" \
    "v_cmp_eq_u32 vcc, -1, v252\n\t" \
    "s_cbranch_vccz .Lgw4_%=\n\t" \
    "buffer_load_dwordx4 v[248:251], %[l16], %[rs], %[i1] offen offset:0 sc1\n\t" \
    "s_mov_b32 %[a], 1\n\t" \
    ".Lgw4_%=:\n\t" \
    "s_cmp_eq_u32 %[a], 0\n\t" \
    "s_cbranch_scc1 .Lhw%=\n\t" \
    "s_waitcnt vmcnt(20)\n\t" \
    ".Liw%=:\n\t" \
    "v_max_u32 v252, v232, v233\n\t" \
    "v_max3_u32 v252, v234, v235, v252\n\t" \
    "v_max3_u32 v252, v236, v237, v252\n\t" \
    "v_max3_u32 v252, v238, v239, v252\n\t" \
    "v_max3_u32 v252, v240, v241, v252\n\t" \
    "v_max3_u32 v252, v242, v243, v252\n\t" \
    "v_max3_u32 v252, v244, v245, v252\n\t" \
    "v_max3_u32 v252, v246, v247, v252\n\t" \
    "v_max3_u32 v252, v248, v249, v252\n\t" \
    "v_max3_u32 v252, v250, v251, v252\n\t" \
    "v_cmp_eq_u32 vcc, -1, v252\n\t" \
    "s_cbranch_vccz .Lhw%=\n\t" \
    "s_sleep 1\n\t" \
    "v_max_u32 v252, v232, v233\n\t" \
    "v_max3_u32 v252, v234, v235, v252\n\t" \
    "v_max3_u32 v252, v236, v237, v252\n\t" \
    "v_max3_u32 v252, v238, v239, v252\n\t" \
    "v_max3_u32 v252, v240, v241, v252\n\t" \
    "v_max3_u32 v252, v242, v243, v252\n\t" \
    "v_max3_u32 v252, v244, v245, v252\n\t" \
    "v_max3_u32 v252, v246, v247, v252\n\t" \
    "v_max3_u32 v252, v248, v249, v252\n\t" \
    "v_max3_u32 v252, v250, v251, v252\n\t" \
    "v_cmp_eq_u32 vcc, -1, v252\n\t" \
    "s_cbranch_vccz .Lhw%=\n\t" \
    "s_sleep 1\n\t" \
    "v_max_u32 v252, v232, v233\n\t" \
    "v_max3_u32 v252, v234, v235, v252\n\t" \
    "v_max3_u32 v252, v236, v237, v252\n\t" \
    "v_max3_u32 v252, v238, v239, v252\n\t" \
    "v_max3_u32 v252, v240, v241, v252\n\t" \
    "v_max3_u32 v252, v242, v243, v252\n\t" \
    "v_max3_u32 v252, v244, v245, v252\n\t" \
    "v_max3_u32 v252, v246, v247, v252\n\t" \
    "v_max3_u32 v252, v248, v249, v252\n\t" \
    "v_max3_u32 v252, v250, v251, v252\n\t" \
    "v_cmp_eq_u32 vcc, -1, v252\n\t" \
    "s_cbranch_vccz .Lhw%=\n\t" \
    "s_sleep 1\n\t" \
    "v_max_u32 v252, v232, v233\n\t" \
    "v_max3_u32 v252, v234, v235, v252\n\t" \
    "v_max3_u32 v252, v236, v237, v252\n\t" \
    "v_max3_u32 v252, v238, v239, v252\n\t" \
    "v_max3_u32 v252, v240, v241, v252\n\t" \
    "v_max3_u32 v252, v242, v243, v252\n\t" \
    "v_max3_u32 v252, v244, v245, v252\n\t" \
    "v_max3_u32 v252, v246, v247, v252\n\t" \
    "v_max3_u32 v252, v248, v249, v252\n\t" \
    "v_max3_u32 v252, v250, v251, v252\n\t" \
    "v_cmp_eq_u32 vcc, -1, v252\n\t" \
    "s_cbranch_vccz .Lhw%=\n\t" \
    "s_sleep 1\n\t" \
    "v_max_u32 v252, v232, v233\n\t" \
    "v_max3_u32 v252, v234, v235, v252\n\t" \
    "v_max3_u32 v252, v236, v237, v252\n\t" \
    "v_max3_u32 v252, v238, v239, v252\n\t" \
    "v_max3_u32 v252, v240, v241, v252\n\t" \
    "v_max3_u32 v252, v242, v243, v252\n\t" \
    "v_max3_u32 v252, v244, v245, v252\n\t" \
    "v_max3_u32 v252, v246, v247, v252\n\t" \
    "v_max3_u32 v252, v248, v249, v252\n\t" \
    "v_max3_u32 v252, v250, v251, v252\n\t" \
    "v_cmp_eq_u32 vcc, -1, v252\n\t" \
    "s_cbranch_vccz .Lhw%=\n\t" \
    "s_sleep 1\n\t" \
    "v_max_u32 v252, v232, v233\n\t" \
    "v_max3_u32 v252, v234, v235, v252\n\t" \
    "v_max3_u32 v252, v236, v237, v252\n\t" \
    "v_max3_u32 v252, v238, v239, v252\n\t" \
    "v_max3_u32 v252, v240, v241, v252\n\t" \
    "v_max3_u32 v252, v242, v243, v252\n\t" \
    "v_max3_u32 v252, v244, v245, v252\n\t" \
    "v_max3_u32 v252, v246, v247, v252\n\t" \
    "v_max3_u32 v252, v248, v249, v252\n\t" \
    "v_max3_u32 v252, v250, v251, v252\n\t" \
    "v_cmp_eq_u32 vcc, -1, v252\n\t" \
    "s_cbranch_vccz .Lhw%=\n\t" \
    "s_sleep 1\n\t" \
    "s_sub_u32 %[n], %[n], 1\n\t" \
    "s_cmp_gt_i32 %[n], 0\n\t" \
    "s_cbranch_scc1 .Lfw%=\n\t" \
    ".Lhw%=:\n\t" \
    "s_branch .Lta%=\n\t" \
    ".Lt0%=:\n\t" \
    "s_waitcnt vmcnt(1)\n\t" \
    ".Lta%=:\n\t" \
    "ds_write_b128 %[lw], v[232:235] offset:0\n\t" \
    "v_max_u32 v252, v232, v233\n\t" \
    "v_max3_u32 v252, v234, v235, v252\n\t" \
    "ds_write_b128 %[lw], v[236:239] offset:1024\n\t" \
    "v_max3_u32 v252, v236, v237, v252\n\t" \
    "v_max3_u32 v252, v238, v239, v252\n\t" \
    "ds_write_b128 %[lw], v[240:243] offset:2048\n\t" \
    "v_max3_u32 v252, v240, v241, v252\n\t" \
    "v_max3_u32 v252, v242, v243, v252\n\t" \
    "ds_write_b128 %[lw], v[244:247] offset:3072\n\t" \
    "v_max3_u32 v252, v244, v245, v252\n\t" \
    "v_max3_u32 v252, v246, v247, v252\n\t" \
    "ds_write_b128 %[lw], v[248:251] offset:4096\n\t" \
    "v_max3_u32 v252, v248, v249, v252\n\t" \
    "v_max3_u32 v252, v250, v251, v252\n\t" \
    "v_cmp_eq_u32 vcc, -1, v252\n\t" \
    "s_cbranch_vccnz .Ltr%=\n\t" \
    ".Ltg%=:\n\t" \
    ASM_ST2 \
    "s_waitcnt lgkmcnt(0)\n\t" \
    "s_setprio 2\n\t" \
    "s_barrier\n\t" ASM_ST \
    "ds_read_b128 v[160:163], %[la] offset:0\n\t" \
    "ds_read_b128 v[164:167], %[la] offset:1024\n\t" \
    "ds_read_b128 v[168:171], %[la] offset:2048\n\t" \
    "ds_read_b128 v[172:175], %[la] offset:3072\n\t" \
    "ds_read_b128 v[176:179], %[la] offset:8192\n\t" \
    "ds_read_b128 v[180:183], %[la] offset:9216\n\t" \
    "ds_read_b128 v[184:187], %[la] offset:10240\n\t" \
    "s_waitcnt lgkmcnt(6)\n\t" \
    "v_mfma_f32_16x16x32_bf16 v[192:195], v[80:83], v[160:163], 0\n\t" \
    "buffer_load_dwordx2 v[196:197], %[ro], %[rs], %[sx] offen sc1\n\t" \
    "s_waitcnt lgkmcnt(5)\n\t" \
    "v_mfma_f32_16x16x32_bf16 v[192:195], v[84:87], v[164:167], v[192:195]\n\t" \
    "buffer_load_dwordx2 v[198:199], %[ro], %[rs], %[sx] offen offset:512 sc1\n\t" \
    "v_mfma_f32_16x16x32_bf16 v[188:191], v[0:3], v[160:163], 0\n\t" \
    "ds_read_b128 v[160:163], %[la] offset:11264\n\t" \
    "v_lshrrev_b32 v204, 1, %[l16]\n\t" \
    "v_mfma_f32_16x16x32_bf16 v[188:191], v[4:7], v[164:167], v[188:191]\n\t" \
    "ds_read_b128 v[164:167], %[la] offset:16384\n\t" \
    "v_and_b32 v204, 0x78, v204\n\t" \
    "s_waitcnt lgkmcnt(6)\n\t" \
    "v_mfma_f32_16x16x32_bf16 v[188:191], v[8:11], v[168:171], v[188:191]\n\t" \
    "buffer_load_dwordx2 v[200:201], v204, %[rs], %[ss] offen sc1\n\t" \
    "s_waitcnt lgkmcnt(5)\n\t" \
    "v_mfma_f32_16x16x32_bf16 v[188:191], v[12:15], v[172:175], v[188:191]\n\t" \
    "v_mov_b64 v[216:217], -1\n\t" \
    "v_mfma_f32_16x16x32_bf16 v[192:195], v[88:91], v[168:171], v[192:195]\n\t" \
    "ds_read_b128 v[168:171], %[la] offset:17408\n\t" \
    "v_mov_b64 v[218:219], -1\n\t" \
    "v_mfma_f32_16x16x32_bf16 v[192:195], v[92:95], v[172:175], v[192:195]\n\t" \
    "ds_read_b128 v[172:175], %[la] offset:18432\n\t" \
    "buffer_store_dwordx4 v[216:219], %[l16], %[rs], %[sa] offen sc1\n\t" \
    "s_waitcnt lgkmcnt(6)\n\t" \
    "v_mfma_f32_16x16x32_bf16 v[192:195], v[96:99], v[176:179], v[192:195]\n\t" \
    "s_waitcnt lgkmcnt(5)\n\t" \
    "v_mfma_f32_16x16x32_bf16 v[192:195], v[100:103], v[180:183], v[192:195]\n\t" \
    "v_mfma_f32_16x16x32_bf16 v[188:191], v[16:19], v[176:179], v[188:191]\n\t" \
    "ds_read_b128 v[176:179], %[la] offset:19456\n\t" \
    "v_mfma_f32_16x16x32_bf16 v[188:191], v[20:23], v[180:183], v[188:191]\n\t" \
    "ds_read_b128 v[180:183], %[la] offset:24576\n\t" \
    "s_waitcnt lgkmcnt(6)\n\t" \
    "v_mfma_f32_16x16x32_bf16 v[188:191], v[24:27], v[184:187], v[188:191]\n\t" \
    "s_waitcnt lgkmcnt(5)\n\t" \
    "v_mfma_f32_16x16x32_bf16 v[188:191], v[28:31], v[160:163], v[188:191]\n\t" \
    "v_mfma_f32_16x16x32_bf16 v[192:195], v[104:107], v[184:187], v[192:195]\n\t" \
    "ds_read_b128 v[184:187], %[la] offset:25600\n\t" \
    "v_mfma_f32_16x16x32_bf16 v[192:195], v[108:111], v[160:163], v[192:195]\n\t" \
    "ds_read_b128 v[160:163], %[la] offset:26624\n\t" \
    "s_waitcnt lgkmcnt(6)\n\t" \
    "v_mfma_f32_16x16x32_bf16 v[192:195], v[112:115], v[164:167], v[192:195]\n\t" \
    "s_waitcnt lgkmcnt(5)\n\t" \
    "v_mfma_f32_16x16x32_bf16 v[192:195], v[116:119], v[168:171], v[192:195]\n\t" \
    "v_mfma_f32_16x16x32_bf16 v[188:191], v[32:35], v[164:167], v[188:191]\n\t" \
    "ds_read_b128 v[164:167], %[la] offset:27648\n\t" \
    "v_mfma_f32_16x16x32_bf16 v[188:191], v[36:39], v[168:171], v[188:191]\n\t" \
    "ds_read_b128 v[168:171], %[la] offset:4096\n\t" \
    "s_waitcnt lgkmcnt(6)\n\t" \
    "v_mfma_f32_16x16x32_bf16 v[188:191], v[40:43], v[172:175], v[188:191]\n\t" \
    "s_waitcnt lgkmcnt(5)\n\t" \
    "v_mfma_f32_16x16x32_bf16 v[188:191], v[44:47], v[176:179], v[188:191]\n\t" \
    "v_mfma_f32_16x16x32_bf16 v[192:195], v[120:123], v[172:175], v[192:195]\n\t" \
    "ds_read_b128 v[172:175], %[la] offset:12288\n\t" \
    "v_mfma_f32_16x16x32_bf16 v[192:195], v[124:127], v[176:179], v[192:195]\n\t" \
    "ds_read_b128 v[176:179], %[la] offset:20480\n\t" \
    "s_waitcnt lgkmcnt(6)\n\t" \
    "v_mfma_f32_16x16x32_bf16 v[192:195], v[128:131], v[180:183], v[192:195]\n\t" \
    "s_waitcnt lgkmcnt(5)\n\t" \
    "v_mfma_f32_16x16x32_bf16 v[192:195], v[132:135], v[184:187], v[192:195]\n\t" \
    "v_mfma_f32_16x16x32_bf16 v[188:191], v[48:51], v[180:183], v[188:191]\n\t" \
    "ds_read_b128 v[180:183], %[la] offset:28672\n\t" \
    "v_mfma_f32_16x16x32_bf16 v[188:191], v[52:55], v[184:187], v[188:191]\n\t" \
    "s_waitcnt lgkmcnt(5)\n\t" \
    "v_mfma_f32_16x16x32_bf16 v[188:191], v[56:59], v[160:163], v[188:191]\n\t" \
    "s_waitcnt lgkmcnt(4)\n\t" \
    "v_mfma_f32_16x16x32_bf16 v[188:191], v[60:63], v[164:167], v[188:191]\n\t" \
    "v_mfma_f32_16x16x32_bf16 v[192:195], v[136:139], v[160:163], v[192:195]\n\t" \
    "v_mfma_f32_16x16x32_bf16 v[192:195], v[140:143], v[164:167], v[192:195]\n\t" \
    "s_waitcnt lgkmcnt(3)\n\t" \
    "v_mfma_f32_16x16x32_bf16 v[192:195], v[144:147], v[168:171], v[192:195]\n\t" \
    "s_waitcnt lgkmcnt(2)\n\t" \
    "v_mfma_f32_16x16x32_bf16 v[192:195], v[148:151], v[172:175], v[192:195]\n\t" \
    "v_mfma_f32_16x16x32_bf16 v[188:191], v[64:67], v[168:171], v[188:191]\n\t" \
    "v_mfma_f32_16x16x32_bf16 v[188:191], v[68:71], v[172:175], v[188:191]\n\t" \
    "s_waitcnt lgkmcnt(1)\n\t" \
    "v_mfma_f32_16x16x32_bf16 v[188:191], v[72:75], v[176:179], v[188:191]\n\t" \
    "s_waitcnt lgkmcnt(0)\n\t" \
    "v_mfma_f32_16x16x32_bf16 v[188:191], v[76:79], v[180:183], v[188:191]\n\t" \
    "v_mfma_f32_16x16x32_bf16 v[192:195], v[152:155], v[176:179], v[192:195]\n\t" \
    "v_mfma_f32_16x16x32_bf16 v[192:195], v[156:159], v[180:183], v[192:195]\n\t" \
    "s_setprio 2\n\t" \
    "buffer_load_dwordx4 v[232:235], %[l16], %[rs], %[n0] offen offset:0 sc1\n\t" \
    "buffer_load_dwordx4 v[236:239], %[l16], %[rs], %[n0] offen offset:1024 sc1\n\t" \
    "buffer_load_dwordx4 v[240:243], %[l16], %[rs], %[n0] offen offset:2048 sc1\n\t" \
    "buffer_load_dwordx4 v[244:247], %[l16], %[rs], %[n0] offen offset:3072 sc1\n\t" \
    "buffer_load_dwordx4 v[248:251], %[l16], %[rs], %[n1] offen offset:0 sc1\n\t" \
    ASM_ST3 \
    "s_waitcnt vmcnt(6)\n\t" \
    "v_lshlrev_b32 v160, 16, v196\n\t" \
    "v_and_b32 v161, 0xffff0000, v196\n\t" \
    "v_lshlrev_b32 v162, 16, v197\n\t" \
    "v_and_b32 v163, 0xffff0000, v197\n\t" \
    "v_lshlrev_b32 v164, 16, v198\n\t" \
    "v_and_b32 v165, 0xffff0000, v198\n\t" \
    "v_lshlrev_b32 v166, 16, v199\n\t" \
    "v_and_b32 v167, 0xffff0000, v199\n\t" \
    "v_sub_f32 v160, v160, v200\n\t" \
    "v_sub_f32 v161, v161, v200\n\t" \
    "v_sub_f32 v162, v162, v200\n\t" \
    "v_sub_f32 v163, v163, v200\n\t" \
    "v_sub_f32 v164, v164, v200\n\t" \
    "v_sub_f32 v165, v165, v200\n\t" \
    "v_sub_f32 v166, v166, v200\n\t" \
    "v_sub_f32 v167, v167, v200\n\t" \
    "v_mul_f32 v160, v160, v201\n\t" \
    "v_mul_f32 v161, v161, v201\n\t" \
    "v_mul_f32 v162, v162, v201\n\t" \
    "v_mul_f32 v163, v163, v201\n\t" \
    "v_mul_f32 v164, v164, v201\n\t" \
    "v_mul_f32 v165, v165, v201\n\t" \
    "v_mul_f32 v166, v166, v201\n\t" \
    "v_mul_f32 v167, v167, v201\n\t" \
    "v_cvt_pk_bf16_f32 v160, v160, v161\n\t" \
    "v_cvt_pk_bf16_f32 v162, v162, v163\n\t" \
    "v_cvt_pk_bf16_f32 v164, v164, v165\n\t" \
    "v_cvt_pk_bf16_f32 v166, v166, v167\n\t" \
    "v_and_b32 v161, 0xffff0000, v160\n\t" \
    "v_lshlrev_b32 v160, 16, v160\n\t" \
    "v_and_b32 v163, 0xffff0000, v162\n\t" \
    "v_lshlrev_b32 v162, 16, v162\n\t" \
    "v_and_b32 v165, 0xffff0000, v164\n\t" \
    "v_lshlrev_b32 v164, 16, v164\n\t" \
    "v_and_b32 v167, 0xffff0000, v166\n\t" \
    "v_lshlrev_b32 v166, 16, v166\n\t" \
    "v_add_f32 v188, v188, v160\n\t" \
    "v_add_f32 v189, v189, v161\n\t" \
    "v_add_f32 v190, v190, v162\n\t" \
    "v_add_f32 v191, v191, v163\n\t" \
    "v_add_f32 v192, v192, v164\n\t" \
    "v_add_f32 v193, v193, v165\n\t" \
    "v_add_f32 v194, v194, v166\n\t" \
    "v_add_f32 v195, v195, v167\n\t" \
    "v_cvt_pk_bf16_f32 v188, v188, v189\n\t" \
    "v_cvt_pk_bf16_f32 v189, v190, v191\n\t" \
    "v_cvt_pk_bf16_f32 v190, v192, v193\n\t" \
    "v_cvt_pk_bf16_f32 v191, v194, v195\n\t" \
    "v_min_u32 v188, -2, v188\n\t" \
    "v_min_u32 v189, -2, v189\n\t" \
    "v_min_u32 v190, -2, v190\n\t" \
    "v_min_u32 v191, -2, v191\n\t" \
    "s_nop 1\n\t" \
    "v_permlane16_swap_b32 v188, v190\n\t" \
    "v_permlane16_swap_b32 v189, v191\n\t" \
    "s_waitcnt vmcnt(6)\n\t" \
    "buffer_store_dwordx4 v[188:191], %[oa], %[rs], %[so] offen sc1\n\t" \
    "s_nop 1\n\t" \
    "s_setprio 0\n\t"
#define ASM_WT2_HI \
    "s_setprio 1\n\t" \
    "s_barrier\n\t" ASM_ST \
    "ds_read_b128 v[160:163], %[la] offset:0\n\t" \
    "ds_read_b128 v[164:167], %[la] offset:1024\n\t" \
    "ds_read_b128 v[168:171], %[la] offset:2048\n\t" \
    "ds_read_b128 v[172:175], %[la] offset:3072\n\t" \
    "ds_read_b128 v[176:179], %[la] offset:8192\n\t" \
    "ds_read_b128 v[180:183], %[la] offset:9216\n\t" \
    "ds_read_b128 v[184:187], %[la] offset:10240\n\t" \
    "s_waitcnt lgkmcnt(6)\n\t" \
    "v_mfma_f32_16x16x32_bf16 v[192:195], v[80:83], v[160:163], 0\n\t" \
    "buffer_load_dwordx2 v[196:197], %[ro], %[rs], %[sx] offen sc1\n\t" \
    "s_waitcnt lgkmcnt(5)\n\t" \
    "v_mfma_f32_16x16x32_bf16 v[192:195], v[84:87], v[164:167], v[192:195]\n\t" \
    "buffer_load_dwordx2 v[198:199], %[ro], %[rs], %[sx] offen offset:512 sc1\n\t" \
    "v_mfma_f32_16x16x32_bf16 v[188:191], v[0:3], v[160:163], 0\n\t" \
    "ds_read_b128 v[160:163], %[la] offset:11264\n\t" \
    "v_lshrrev_b32 v204, 1, %[l16]\n\t" \
    "v_mfma_f32_16x16x32_bf16 v[188:191], v[4:7], v[164:167], v[188:191]\n\t" \
    "ds_read_b128 v[164:167], %[la] offset:16384\n\t" \
    "v_and_b32 v204, 0x78, v204\n\t" \
    "s_waitcnt lgkmcnt(6)\n\t" \
    "v_mfma_f32_16x16x32_bf16 v[188:191], v[8:11], v[168:171], v[188:191]\n\t" \
    "buffer_load_dwordx2 v[200:201], v204, %[rs], %[ss] offen sc1\n\t" \
    "s_waitcnt lgkmcnt(5)\n\t" \
    "v_mfma_f32_16x16x32_bf16 v[188:191], v[12:15], v[172:175], v[188:191]\n\t" \
    "v_mov_b64 v[216:217], -1\n\t" \
    "v_mfma_f32_16x16x32_bf16 v[192:195], v[88:91], v[168:171], v[192:195]\n\t" \
    "ds_read_b128 v[168:171], %[la] offset:17408\n\t" \
    "v_mov_b64 v[218:219], -1\n\t" \
    "v_mfma_f32_16x16x32_bf16 v[192:195], v[92:95], v[172:175], v[192:195]\n\t" \
    "ds_read_b128 v[172:175], %[la] offset:18432\n\t" \
    "buffer_store_dwordx4 v[216:219], %[l16], %[rs], %[sa] offen sc1\n\t" \
    "s_waitcnt lgkmcnt(6)\n\t" \
    "v_mfma_f32_16x16x32_bf16 v[192:195], v[96:99], v[176:179], v[192:195]\n\t" \
    "s_waitcnt lgkmcnt(5)\n\t" \
    "v_mfma_f32_16x16x32_bf16 v[192:195], v[100:103], v[180:183], v[192:195]\n\t" \
    "v_mfma_f32_16x16x32_bf16 v[188:191], v[16:19], v[176:179], v[188:191]\n\t" \
    "ds_read_b128 v[176:179], %[la] offset:19456\n\t" \
    "v_mfma_f32_16x16x32_bf16 v[188:191], v[20:23], v[180:183], v[188:191]\n\t" \
    "ds_read_b128 v[180:183], %[la] offset:24576\n\t" \
    "s_waitcnt lgkmcnt(6)\n\t" \
    "v_mfma_f32_16x16x32_bf16 v[188:191], v[24:27], v[184:187], v[188:191]\n\t" \
    "s_waitcnt lgkmcnt(5)\n\t" \
    "v_mfma_f32_16x16x32_bf16 v[188:191], v[28:31], v[160:163], v[188:191]\n\t" \
    "v_mfma_f32_16x16x32_bf16 v[192:195], v[104:107], v[184:187], v[192:195]\n\t" \
    "ds_read_b128 v[184:187], %[la] offset:25600\n\t" \
    "v_mfma_f32_16x16x32_bf16 v[192:195], v[108:111], v[160:163], v[192:195]\n\t" \
    "ds_read_b128 v[160:163], %[la] offset:26624\n\t" \
    "s_waitcnt lgkmcnt(6)\n\t" \
    "v_mfma_f32_16x16x32_bf16 v[192:195], v[112:115], v[164:167], v[192:195]\n\t" \
    "s_waitcnt lgkmcnt(5)\n\t" \
    "v_mfma_f32_16x16x32_bf16 v[192:195], v[116:119], v[168:171], v[192:195]\n\t" \
    "v_mfma_f32_16x16x32_bf16 v[188:191], v[32:35], v[164:167], v[188:191]\n\t" \
    "ds_read_b128 v[164:167], %[la] offset:27648\n\t" \
    "v_mfma_f32_16x16x32_bf16 v[188:191], v[36:39], v[168:171], v[188:191]\n\t" \
    "ds_read_b128 v[168:171], %[la] offset:4096\n\t" \
    "s_waitcnt lgkmcnt(6)\n\t" \
    "v_mfma_f32_16x16x32_bf16 v[188:191], v[40:43], v[172:175], v[188:191]\n\t" \
    "s_waitcnt lgkmcnt(5)\n\t" \
    "v_mfma_f32_16x16x32_bf16 v[188:191], v[44:47], v[176:179], v[188:191]\n\t" \
    "v_mfma_f32_16x16x32_bf16 v[192:195], v[120:123], v[172:175], v[192:195]\n\t" \
    "ds_read_b128 v[172:175], %[la] offset:12288\n\t" \
    "v_mfma_f32_16x16x32_bf16 v[192:195], v[124:127], v[176:179], v[192:195]\n\t" \
    "ds_read_b128 v[176:179], %[la] offset:20480\n\t" \
    "s_waitcnt lgkmcnt(6)\n\t" \
    "v_mfma_f32_16x16x32_bf16 v[192:195], v[128:131], v[180:183], v[192:195]\n\t" \
    "s_waitcnt lgkmcnt(5)\n\t" \
    "v_mfma_f32_16x16x32_bf16 v[192:195], v[132:135], v[184:187], v[192:195]\n\t" \
    "v_mfma_f32_16x16x32_bf16 v[188:191], v[48:51], v[180:183], v[188:191]\n\t" \
    "ds_read_b128 v[180:183], %[la] offset:28672\n\t" \
    "v_mfma_f32_16x16x32_bf16 v[188:191], v[52:55], v[184:187], v[188:191]\n\t" \
    "s_waitcnt lgkmcnt(5)\n\t" \
    "v_mfma_f32_16x16x32_bf16 v[188:191], v[56:59], v[160:163], v[188:191]\n\t" \
    "s_waitcnt lgkmcnt(4)\n\t" \
    "v_mfma_f32_16x16x32_bf16 v[188:191], v[60:63], v[164:167], v[188:191]\n\t" \
    "v_mfma_f32_16x16x32_bf16 v[192:195], v[136:139], v[160:163], v[192:195]\n\t" \
    "v_mfma_f32_16x16x32_bf16 v[192:195], v[140:143], v[164:167], v[192:195]\n\t" \
    "s_waitcnt lgkmcnt(3)\n\t" \
    "v_mfma_f32_16x16x32_bf16 v[192:195], v[144:147], v[168:171], v[192:195]\n\t" \
    "s_waitcnt lgkmcnt(2)\n\t" \
    "v_mfma_f32_16x16x32_bf16 v[192:195], v[148:151], v[172:175], v[192:195]\n\t" \
    "v_mfma_f32_16x16x32_bf16 v[188:191], v[64:67], v[168:171], v[188:191]\n\t" \
    "v_mfma_f32_16x16x32_bf16 v[188:191], v[68:71], v[172:175], v[188:191]\n\t" \
    "s_waitcnt lgkmcnt(1)\n\t" \
    "v_mfma_f32_16x16x32_bf16 v[188:191], v[72:75], v[176:179], v[188:191]\n\t" \
    "s_waitcnt lgkmcnt(0)\n\t" \
    "v_mfma_f32_16x16x32_bf16 v[188:191], v[76:79], v[180:183], v[188:191]\n\t" \
    "v_mfma_f32_16x16x32_bf16 v[192:195], v[152:155], v[176:179], v[192:195]\n\t" \
    "v_mfma_f32_16x16x32_bf16 v[192:195], v[156:159], v[180:183], v[192:195]\n\t" \
    ASM_ST3 \
    "s_waitcnt vmcnt(1)\n\t" \
    "v_lshlrev_b32 v160, 16, v196\n\t" \
    "v_and_b32 v161, 0xffff0000, v196\n\t" \
    "v_lshlrev_b32 v162, 16, v197\n\t" \
    "v_and_b32 v163, 0xffff0000, v197\n\t" \
    "v_lshlrev_b32 v164, 16, v198\n\t" \
    "v_and_b32 v165, 0xffff0000, v198\n\t" \
    "v_lshlrev_b32 v166, 16, v199\n\t" \
    "v_and_b32 v167, 0xffff0000, v199\n\t" \
    "v_sub_f32 v160, v160, v200\n\t" \
    "v_sub_f32 v161, v161, v200\n\t" \
    "v_sub_f32 v162, v162, v200\n\t" \
    "v_sub_f32 v163, v163, v200\n\t" \
    "v_sub_f32 v164, v164, v200\n\t" \
    "v_sub_f32 v165, v165, v200\n\t" \
    "v_sub_f32 v166, v166, v200\n\t" \
    "v_sub_f32 v167, v167, v200\n\t" \
    "v_mul_f32 v160, v160, v201\n\t" \
    "v_mul_f32 v161, v161, v201\n\t" \
    "v_mul_f32 v162, v162, v201\n\t" \
    "v_mul_f32 v163, v163, v201\n\t" \
    "v_mul_f32 v164, v164, v201\n\t" \
    "v_mul_f32 v165, v165, v201\n\t" \
    "v_mul_f32 v166, v166, v201\n\t" \
    "v_mul_f32 v167, v167, v201\n\t" \
    "v_cvt_pk_bf16_f32 v160, v160, v161\n\t" \
    "v_cvt_pk_bf16_f32 v162, v162, v163\n\t" \
    "v_cvt_pk_bf16_f32 v164, v164, v165\n\t" \
    "v_cvt_pk_bf16_f32 v166, v166, v167\n\t" \
    "v_and_b32 v161, 0xffff0000, v160\n\t" \
    "v_lshlrev_b32 v160, 16, v160\n\t" \
    "v_and_b32 v163, 0xffff0000, v162\n\t" \
    "v_lshlrev_b32 v162, 16, v162\n\t" \
    "v_and_b32 v165, 0xffff0000, v164\n\t" \
    "v_lshlrev_b32 v164, 16, v164\n\t" \
    "v_and_b32 v167, 0xffff0000, v166\n\t" \
    "v_lshlrev_b32 v166, 16, v166\n\t" \
    "v_add_f32 v188, v188, v160\n\t" \
    "v_add_f32 v189, v189, v161\n\t" \
    "v_add_f32 v190, v190, v162\n\t" \
    "v_add_f32 v191, v191, v163\n\t" \
    "v_add_f32 v192, v192, v164\n\t" \
    "v_add_f32 v193, v193, v165\n\t" \
    "v_add_f32 v194, v194, v166\n\t" \
    "v_add_f32 v195, v195, v167\n\t" \
    "v_cvt_pk_bf16_f32 v188, v188, v189\n\t" \
    "v_cvt_pk_bf16_f32 v189, v190, v191\n\t" \
    "v_cvt_pk_bf16_f32 v190, v192, v193\n\t" \
    "v_cvt_pk_bf16_f32 v191, v194, v195\n\t" \
    "v_min_u32 v188, -2, v188\n\t" \
    "v_min_u32 v189, -2, v189\n\t" \
    "v_min_u32 v190, -2, v190\n\t" \
    "v_min_u32 v191, -2, v191\n\t" \
    "s_nop 1\n\t" \
    "v_permlane16_swap_b32 v188, v190\n\t" \
    "v_permlane16_swap_b32 v189, v191\n\t" \
    "s_waitcnt vmcnt(1)\n\t" \
    "buffer_store_dwordx4 v[188:191], %[oa], %[rs], %[so] offen sc1\n\t" \
    "s_nop 1\n\t"
#define ASM_WT3_LO \
    "s_branch .Lt0%=\n\t" \
    ".Ltr%=:\n\t" \
    "s_cmp_gt_i32 %[n], 0\n\t" \
    "s_cbranch_scc0 .Ltg%=\n\t" \
    ".Lfw%=:\n\t" \
    "s_mov_b32 %[a], 0\n\t" \
    "v_max_u32 v252, v232, v233\n\t" \
    "v_max3_u32 v252, v234, v235, v252\n\t" \
    "v_cmp_eq_u32 vcc, -1, v252\n\t" \
    "s_cbranch_vccz .Lgw0_%=\n\t" \
    "buffer_load_dwordx4 v[232:235], %[l16], %[rs], %[i0] offen offset:0 sc1\n\t" \
    "s_mov_b32 %[a], 1\n\t" \
    ".Lgw0_%=:\n\t" \
    "v_max_u32 v252, v236, v237\n\t" \
    "v_max3_u32 v252, v238, v239, v252\n\t" \
    "v_cmp_eq_u32 vcc, -1, v252\n\t" \
    "s_cbranch_vccz .Lgw1_%=\n\t" \
    "buffer_load_dwordx4 v[236:239], %[l16], %[rs], %[i0] offen offset:1024 sc1\n\t" \
    "s_mov_b32 %[a], 1\n\t" \
    ".Lgw1_%=:\n\t" \
    "v_max_u32 v252, v240, v241\n\t" \
    "v_max3_u32 v252, v242, v243, v252\n\t" \
    "v_cmp_eq_u32 vcc, -1, v252\n\t" \
    "s_cbranch_vccz .Lgw2_%=\n\t" \
    "buffer_load_dwordx4 v[240:243], %[l16], %[rs], %[i0] offen offset:2048 sc1\n\t" \
    "s_mov_b32 %[a], 1\n\t" \
    ".Lgw2_%=:\n\t" \
    "v_max_u32 v252, v244, v245\n\t" \
    "v_max3_u32 v252, v246, v247, v252\n\t" \
    "v_cmp_eq_u32 vcc, -1, v252\n\t" \
    "s_cbranch_vccz .Lgw3_%=\n\t" \
    "buffer_load_dwordx4 v[244:247], %[l16], %[rs], %[i0] offen offset:3072 sc1\n\t" \
    "s_mov_b32 %[a], 1\n\t" \
    ".Lgw3_%=:\n\t" \
    "v_max_u32 v252, v248, v249\n\t" \
    "v_max3_u32 v252, v250, v251, v252\n\t" \
    "v_cmp_eq_u32 vcc, -1, v252\n\t" \
    "s_cbranch_vccz .Lgw4_%=\n\t" \
    "buffer_load_dwordx4 v[248:251], %[l16], %[rs], %[i1] offen offset:0 sc1\n\t" \
    "s_mov_b32 %[a], 1\n\t" \
    ".Lgw4_%=:\n\t" \
    "s_cmp_eq_u32 %[a], 0\n\t" \
    "s_cbranch_scc1 .Lhw%=\n\t" \
    "s_waitcnt vmcnt(20)\n\t" \
    ".Liw%=:\n\t" \
    "v_max_u32 v252, v232, v233\n\t" \
    "v_max3_u32 v252, v234, v235, v252\n\t" \
    "v_max3_u32 v252, v236, v237, v252\n\t" \
    "v_max3_u32 v252, v238, v239, v252\n\t" \
    "v_max3_u32 v252, v240, v241, v252\n\t" \
    "v_max3_u32 v252, v242, v243, v252\n\t" \
    "v_max3_u32 v252, v244, v245, v252\n\t" \
    "v_max3_u32 v252, v246, v247, v252\n\t" \
    "v_max3_u32 v252, v248, v249, v252\n\t" \
    "v_max3_u32 v252, v250, v251, v252\n\t" \
    "v_cmp_eq_u32 vcc, -1, v252\n\t" \
    "s_cbranch_vccz .Lhw%=\n\t" \
    "s_sleep 1\n\t" \
    "v_max_u32 v252, v232, v233\n\t" \
    "v_max3_u32 v252, v234, v235, v252\n\t" \
    "v_max3_u32 v252, v236, v237, v252\n\t" \
    "v_max3_u32 v252, v238, v239, v252\n\t" \
    "v_max3_u32 v252, v240, v241, v252\n\t" \
    "v_max3_u32 v252, v242, v243, v252\n\t" \
    "v_max3_u32 v252, v244, v245, v252\n\t" \
    "v_max3_u32 v252, v246, v247, v252\n\t" \
    "v_max3_u32 v252, v248, v249, v252\n\t" \
    "v_max3_u32 v252, v250, v251, v252\n\t" \
    "v_cmp_eq_u32 vcc, -1, v252\n\t" \
    "s_cbranch_vccz .Lhw%=\n\t" \
    "s_sleep 1\n\t" \
    "v_max_u32 v252, v232, v233\n\t" \
    "v_max3_u32 v252, v234, v235, v252\n\t" \
    "v_max3_u32 v252, v236, v237, v252\n\t" \
    "v_max3_u32 v252, v238, v239, v252\n\t" \
    "v_max3_u32 v252, v240, v241, v252\n\t" \
    "v_max3_u32 v252, v242, v243, v252\n\t" \
    "v_max3_u32 v252, v244, v245, v252\n\t" \
    "v_max3_u32 v252, v246, v247, v252\n\t" \
    "v_max3_u32 v252, v248, v249, v252\n\t" \
    "v_max3_u32 v252, v250, v251, v252\n\t" \
    "v_cmp_eq_u32 vcc, -1, v252\n\t" \
    "s_cbranch_vccz .Lhw%=\n\t" \
    "s_sleep 1\n\t" \
    "v_max_u32 v252, v232, v233\n\t" \
    "v_max3_u32 v252, v234, v235, v252\n\t" \
    "v_max3_u32 v252, v236, v237, v252\n\t" \
    "v_max3_u32 v252, v238, v239, v252\n\t" \
    "v_max3_u32 v252, v240, v241, v252\n\t" \
    "v_max3_u32 v252, v242, v243, v252\n\t" \
    "v_max3_u32 v252, v244, v245, v252\n\t" \
    "v_max3_u32 v252, v246, v247, v252\n\t" \
    "v_max3_u32 v252, v248, v249, v252\n\t" \
    "v_max3_u32 v252, v250, v251, v252\n\t" \
    "v_cmp_eq_u32 vcc, -1, v252\n\t" \
    "s_cbranch_vccz .Lhw%=\n\t" \
    "s_sleep 1\n\t" \
    "v_max_u32 v252, v232, v233\n\t" \
    "v_max3_u32 v252, v234, v235, v252\n\t" \
    "v_max3_u32 v252, v236, v237, v252\n\t" \
    "v_max3_u32 v252, v238, v239, v252\n\t" \
    "v_max3_u32 v252, v240, v241, v252\n\t" \
    "v_max3_u32 v252, v242, v243, v252\n\t" \
    "v_max3_u32 v252, v244, v245, v252\n\t" \
    "v_max3_u32 v252, v246, v247, v252\n\t" \
    "v_max3_u32 v252, v248, v249, v252\n\t" \
    "v_max3_u32 v252, v250, v251, v252\n\t" \
    "v_cmp_eq_u32 vcc, -1, v252\n\t" \
    "s_cbranch_vccz .Lhw%=\n\t" \
    "s_sleep 1\n\t" \
    "v_max_u32 v252, v232, v233\n\t" \
    "v_max3_u32 v252, v234, v235, v252\n\t" \
    "v_max3_u32 v252, v236, v237, v252\n\t" \
    "v_max3_u32 v252, v238, v239, v252\n\t" \
    "v_max3_u32 v252, v240, v241, v252\n\t" \
    "v_max3_u32 v252, v242, v243, v252\n\t" \
    "v_max3_u32 v252, v244, v245, v252\n\t" \
    "v_max3_u32 v252, v246, v247, v252\n\t" \
    "v_max3_u32 v252, v248, v249, v252\n\t" \
    "v_max3_u32 v252, v250, v251, v252\n\t" \
    "v_cmp_eq_u32 vcc, -1, v252\n\t" \
    "s_cbranch_vccz .Lhw%=\n\t" \
    "s_sleep 1\n\t" \
    "s_sub_u32 %[n], %[n], 1\n\t" \
    "s_cmp_gt_i32 %[n], 0\n\t" \
    "s_cbranch_scc1 .Lfw%=\n\t" \
    ".Lhw%=:\n\t" \
    "s_branch .Lta%=\n\t" \
    ".Lt0%=:\n\t" \
    "s_waitcnt vmcnt(1)\n\t" \
    ".Lta%=:\n\t" \
    "ds_write_b128 %[lw], v[232:235] offset:0\n\t" \
    "v_max_u32 v252, v232, v233\n\t" \
    "v_max3_u32 v252, v234, v235, v252\n\t" \
    "v_mfma_f32_16x16x32_bf16 v[188:191], v[232:235], v[232:235], 0\n\t" \
    "ds_write_b128 %[lw], v[236:239] offset:1024\n\t" \
    "v_max3_u32 v252, v236, v237, v252\n\t" \
    "v_max3_u32 v252, v238, v239, v252\n\t" \
    "v_mfma_f32_16x16x32_bf16 v[188:191], v[236:239], v[236:239], v[188:191]\n\t" \
    "ds_write_b128 %[lw], v[240:243] offset:2048\n\t" \
    "v_max3_u32 v252, v240, v241, v252\n\t" \
    "v_max3_u32 v252, v242, v243, v252\n\t" \
    "v_mfma_f32_16x16x32_bf16 v[188:191], v[240:243], v[240:243], v[188:191]\n\t" \
    "ds_write_b128 %[lw], v[244:247] offset:3072\n\t" \
    "v_max3_u32 v252, v244, v245, v252\n\t" \
    "v_max3_u32 v252, v246, v247, v252\n\t" \
    "v_mfma_f32_16x16x32_bf16 v[188:191], v[244:247], v[244:247], v[188:191]\n\t" \
    "ds_write_b128 %[lw], v[248:251] offset:4096\n\t" \
    "v_max3_u32 v252, v248, v249, v252\n\t" \
    "v_max3_u32 v252, v250, v251, v252\n\t" \
    "v_mfma_f32_16x16x32_bf16 v[188:191], v[248:251], v[248:251], v[188:191]\n\t" \
    "v_mfma_f32_16x16x32_bf16 v[192:195], v[216:219], v[232:235], 0\n\t" \
    "v_mfma_f32_16x16x32_bf16 v[192:195], v[216:219], v[236:239], v[192:195]\n\t" \
    "v_mfma_f32_16x16x32_bf16 v[192:195], v[216:219], v[240:243], v[192:195]\n\t" \
    "v_mfma_f32_16x16x32_bf16 v[192:195], v[216:219], v[244:247], v[192:195]\n\t" \
    "v_mfma_f32_16x16x32_bf16 v[192:195], v[216:219], v[248:251], v[192:195]\n\t" \
    "v_cmp_eq_u32 vcc, -1, v252\n\t" \
    "s_cbranch_vccnz .Ltr%=\n\t" \
    ".Ltg%=:\n\t" \
    ASM_ST2 \
    "s_nop 5\n\t" \
    "ds_write_b128 %[lw], v[188:191] offset:5120\n\t" \
    "ds_write_b128 %[lw], v[192:195] offset:6144\n\t" \
    "s_waitcnt lgkmcnt(0)\n\t" \
    "s_setprio 2\n\t" \
    "s_barrier\n\t" ASM_ST \
    "ds_read_b128 v[160:163], %[la] offset:0\n\t" \
    "ds_read_b128 v[164:167], %[la] offset:1024\n\t" \
    "ds_read_b128 v[168:171], %[la] offset:2048\n\t" \
    "ds_read2st64_b32 v[204:205], %[sr] offset0:20 offset1:24\n\t" \
    "ds_read_b128 v[172:175], %[la] offset:3072\n\t" \
    "ds_read_b128 v[176:179], %[la] offset:8192\n\t" \
    "ds_read_b128 v[180:183], %[la] offset:9216\n\t" \
    "ds_read_b128 v[184:187], %[la] offset:10240\n\t" \
    "s_waitcnt lgkmcnt(7)\n\t" \
    "v_mfma_f32_16x16x32_bf16 v[192:195], v[80:83], v[160:163], 0\n\t" \
    "s_waitcnt lgkmcnt(6)\n\t" \
    "v_mfma_f32_16x16x32_bf16 v[192:195], v[84:87], v[164:167], v[192:195]\n\t" \
    "v_mfma_f32_16x16x32_bf16 v[188:191], v[0:3], v[160:163], 0\n\t" \
    "ds_read_b128 v[160:163], %[la] offset:11264\n\t" \
    "v_mfma_f32_16x16x32_bf16 v[188:191], v[4:7], v[164:167], v[188:191]\n\t" \
    "ds_read_b128 v[164:167], %[la] offset:16384\n\t" \
    "s_waitcnt lgkmcnt(7)\n\t" \
    "v_mfma_f32_16x16x32_bf16 v[188:191], v[8:11], v[168:171], v[188:191]\n\t" \
    "s_waitcnt lgkmcnt(6)\n\t" \
    "s_waitcnt lgkmcnt(5)\n\t" \
    "v_mfma_f32_16x16x32_bf16 v[188:191], v[12:15], v[172:175], v[188:191]\n\t" \
    "v_permlane16_swap_b32 v204, v205\n\t" \
    "v_mfma_f32_16x16x32_bf16 v[192:195], v[88:91], v[168:171], v[192:195]\n\t" \
    "ds_read_b128 v[168:171], %[la] offset:17408\n\t" \
    "v_mfma_f32_16x16x32_bf16 v[192:195], v[92:95], v[172:175], v[192:195]\n\t" \
    "ds_read_b128 v[172:175], %[la] offset:18432\n\t" \
    "v_add_f32 v206, v204, v205\n\t" \
    "s_waitcnt lgkmcnt(6)\n\t" \
    "v_mfma_f32_16x16x32_bf16 v[192:195], v[96:99], v[176:179], v[192:195]\n\t" \
    "v_add_f32 v207, v204, v205\n\t" \
    "s_waitcnt lgkmcnt(5)\n\t" \
    "v_mfma_f32_16x16x32_bf16 v[192:195], v[100:103], v[180:183], v[192:195]\n\t" \
    "v_mfma_f32_16x16x32_bf16 v[188:191], v[16:19], v[176:179], v[188:191]\n\t" \
    "ds_read_b128 v[176:179], %[la] offset:19456\n\t" \
    "v_permlane32_swap_b32 v206, v207\n\t" \
    "v_mfma_f32_16x16x32_bf16 v[188:191], v[20:23], v[180:183], v[188:191]\n\t" \
    "ds_read_b128 v[180:183], %[la] offset:24576\n\t" \
    "s_waitcnt lgkmcnt(6)\n\t" \
    "v_mfma_f32_16x16x32_bf16 v[188:191], v[24:27], v[184:187], v[188:191]\n\t" \
    "v_add_f32 v204, v206, v207\n\t" \
    "s_waitcnt lgkmcnt(5)\n\t" \
    "v_mfma_f32_16x16x32_bf16 v[188:191], v[28:31], v[160:163], v[188:191]\n\t" \
    "v_add_f32 v205, v206, v207\n\t" \
    "v_mfma_f32_16x16x32_bf16 v[192:195], v[104:107], v[184:187], v[192:195]\n\t" \
    "ds_read_b128 v[184:187], %[la] offset:25600\n\t" \
    "v_mfma_f32_16x16x32_bf16 v[192:195], v[108:111], v[160:163], v[192:195]\n\t" \
    "ds_read_b128 v[160:163], %[la] offset:26624\n\t" \
    "v_permlane16_swap_b32 v204, v205\n\t" \
    "s_waitcnt lgkmcnt(6)\n\t" \
    "v_mfma_f32_16x16x32_bf16 v[192:195], v[112:115], v[164:167], v[192:195]\n\t" \
    "s_waitcnt lgkmcnt(5)\n\t" \
    "v_mfma_f32_16x16x32_bf16 v[192:195], v[116:119], v[168:171], v[192:195]\n\t" \
    "v_mul_f32 v205, %[kd], v205\n\t" \
    "v_mfma_f32_16x16x32_bf16 v[188:191], v[32:35], v[164:167], v[188:191]\n\t" \
    "ds_read_b128 v[164:167], %[la] offset:27648\n\t" \
    "v_mul_f32 v204, %[kd], v204\n\t" \
    "v_mfma_f32_16x16x32_bf16 v[188:191], v[36:39], v[168:171], v[188:191]\n\t" \
    "ds_read_b128 v[168:171], %[la] offset:4096\n\t" \
    "v_fma_f32 v204, -v205, v205, v204\n\t" \
    "s_waitcnt lgkmcnt(6)\n\t" \
    "v_mfma_f32_16x16x32_bf16 v[188:191], v[40:43], v[172:175], v[188:191]\n\t" \
    "v_max_f32 v204, 0, v204\n\t" \
    "s_waitcnt lgkmcnt(5)\n\t" \
    "v_mfma_f32_16x16x32_bf16 v[188:191], v[44:47], v[176:179], v[188:191]\n\t" \
    "v_add_f32 v206, %[ke], v204\n\t" \
    "v_mfma_f32_16x16x32_bf16 v[192:195], v[120:123], v[172:175], v[192:195]\n\t" \
    "ds_read_b128 v[172:175], %[la] offset:12288\n\t" \
    "v_rsq_f32 v206, v206\n\t" \
    "v_mfma_f32_16x16x32_bf16 v[192:195], v[124:127], v[176:179], v[192:195]\n\t" \
    "ds_read_b128 v[176:179], %[la] offset:20480\n\t" \
    "s_waitcnt lgkmcnt(6)\n\t" \
    "v_mfma_f32_16x16x32_bf16 v[192:195], v[128:131], v[180:183], v[192:195]\n\t" \
    "v_mul_f32 v207, -v205, v206\n\t" \
    "s_waitcnt lgkmcnt(5)\n\t" \
    "v_mfma_f32_16x16x32_bf16 v[192:195], v[132:135], v[184:187], v[192:195]\n\t" \
    "v_mul_f32 v208, v196, v207\n\t" \
    "v_mfma_f32_16x16x32_bf16 v[188:191], v[48:51], v[180:183], v[188:191]\n\t" \
    "ds_read_b128 v[180:183], %[la] offset:28672\n\t" \
    "v_mul_f32 v209, v197, v207\n\t" \
    "v_mul_f32 v210, v198, v207\n\t" \
    "v_mfma_f32_16x16x32_bf16 v[188:191], v[52:55], v[184:187], v[188:191]\n\t" \
    "v_mul_f32 v211, v199, v207\n\t" \
    "v_mul_f32 v212, v200, v207\n\t" \
    "s_waitcnt lgkmcnt(5)\n\t" \
    "v_mfma_f32_16x16x32_bf16 v[188:191], v[56:59], v[160:163], v[188:191]\n\t" \
    "v_mul_f32 v213, v201, v207\n\t" \
    "v_mul_f32 v214, v202, v207\n\t" \
    "s_waitcnt lgkmcnt(4)\n\t" \
    "v_mfma_f32_16x16x32_bf16 v[188:191], v[60:63], v[164:167], v[188:191]\n\t" \
    "v_mul_f32 v215, v203, v207\n\t" \
    "v_mov_b64 v[216:217], -1\n\t" \
    "v_mfma_f32_16x16x32_bf16 v[192:195], v[136:139], v[160:163], v[192:195]\n\t" \
    "v_mov_b64 v[218:219], -1\n\t" \
    "buffer_store_dwordx4 v[216:219], %[l16], %[rs], %[sa] offen sc1\n\t" \
    "v_mfma_f32_16x16x32_bf16 v[192:195], v[140:143], v[164:167], v[192:195]\n\t" \
    "s_waitcnt lgkmcnt(3)\n\t" \
    "v_mfma_f32_16x16x32_bf16 v[192:195], v[144:147], v[168:171], v[192:195]\n\t" \
    "s_waitcnt lgkmcnt(2)\n\t" \
    "v_mfma_f32_16x16x32_bf16 v[192:195], v[148:151], v[172:175], v[192:195]\n\t" \
    "v_mfma_f32_16x16x32_bf16 v[188:191], v[64:67], v[168:171], v[188:191]\n\t" \
    "v_mfma_f32_16x16x32_bf16 v[188:191], v[68:71], v[172:175], v[188:191]\n\t" \
    "s_waitcnt lgkmcnt(1)\n\t" \
    "v_mfma_f32_16x16x32_bf16 v[188:191], v[72:75], v[176:179], v[188:191]\n\t" \
    "s_waitcnt lgkmcnt(0)\n\t" \
    "v_mfma_f32_16x16x32_bf16 v[188:191], v[76:79], v[180:183], v[188:191]\n\t" \
    "v_mfma_f32_16x16x32_bf16 v[192:195], v[152:155], v[176:179], v[192:195]\n\t" \
    "v_mfma_f32_16x16x32_bf16 v[192:195], v[156:159], v[180:183], v[192:195]\n\t" \
    "s_setprio 2\n\t" \
    "buffer_load_dwordx4 v[232:235], %[l16], %[rs], %[n0] offen offset:0 sc1\n\t" \
    "buffer_load_dwordx4 v[236:239], %[l16], %[rs], %[n0] offen offset:1024 sc1\n\t" \
    "buffer_load_dwordx4 v[240:243], %[l16], %[rs], %[n0] offen offset:2048 sc1\n\t" \
    "buffer_load_dwordx4 v[244:247], %[l16], %[rs], %[n0] offen offset:3072 sc1\n\t" \
    "buffer_load_dwordx4 v[248:251], %[l16], %[rs], %[n1] offen offset:0 sc1\n\t" \
    ASM_ST3 \
    "v_fma_f32 v188, v188, v206, v208\n\t" \
    "v_fma_f32 v189, v189, v206, v209\n\t" \
    "v_fma_f32 v190, v190, v206, v210\n\t" \
    "v_fma_f32 v191, v191, v206, v211\n\t" \
    "v_mul_f32 v160, v188, v188\n\t" \
    "v_mul_f32 v161, v189, v189\n\t" \
    "v_mul_f32 v162, v190, v190\n\t" \
    "v_mul_f32 v163, v191, v191\n\t" \
    "v_fma_f32 v160, v160, %[k1], v220\n\t" \
    "v_fma_f32 v161, v161, %[k1], v220\n\t" \
    "v_fma_f32 v162, v162, %[k1], v220\n\t" \
    "v_fma_f32 v163, v163, %[k1], v220\n\t" \
    "v_mul_f32 v160, v188, v160\n\t" \
    "v_mul_f32 v161, v189, v161\n\t" \
    "v_mul_f32 v162, v190, v162\n\t" \
    "v_mul_f32 v163, v191, v163\n\t" \
    "v_fma_f32 v192, v192, v206, v212\n\t" \
    "v_fma_f32 v193, v193, v206, v213\n\t" \
    "v_fma_f32 v194, v194, v206, v214\n\t" \
    "v_fma_f32 v195, v195, v206, v215\n\t" \
    "v_mul_f32 v164, v192, v192\n\t" \
    "v_mul_f32 v165, v193, v193\n\t" \
    "v_mul_f32 v166, v194, v194\n\t" \
    "v_mul_f32 v167, v195, v195\n\t" \
    "v_fma_f32 v164, v164, %[k1], v220\n\t" \
    "v_fma_f32 v165, v165, %[k1], v220\n\t" \
    "v_fma_f32 v166, v166, %[k1], v220\n\t" \
    "v_fma_f32 v167, v167, %[k1], v220\n\t" \
    "v_mul_f32 v164, v192, v164\n\t" \
    "v_mul_f32 v165, v193, v165\n\t" \
    "v_mul_f32 v166, v194, v166\n\t" \
    "v_mul_f32 v167, v195, v167\n\t" \
    "v_exp_f32 v160, v160\n\t" \
    "v_exp_f32 v161, v161\n\t" \
    "v_exp_f32 v162, v162\n\t" \
    "v_exp_f32 v163, v163\n\t" \
    "v_exp_f32 v164, v164\n\t" \
    "v_exp_f32 v165, v165\n\t" \
    "v_exp_f32 v166, v166\n\t" \
    "v_exp_f32 v167, v167\n\t" \
    "v_add_f32 v160, 1.0, v160\n\t" \
    "v_add_f32 v161, 1.0, v161\n\t" \
    "v_add_f32 v162, 1.0, v162\n\t" \
    "v_add_f32 v163, 1.0, v163\n\t" \
    "v_add_f32 v164, 1.0, v164\n\t" \
    "v_add_f32 v165, 1.0, v165\n\t" \
    "v_add_f32 v166, 1.0, v166\n\t" \
    "v_add_f32 v167, 1.0, v167\n\t" \
    "v_rcp_f32 v160, v160\n\t" \
    "v_rcp_f32 v161, v161\n\t" \
    "v_rcp_f32 v162, v162\n\t" \
    "v_rcp_f32 v163, v163\n\t" \
    "v_rcp_f32 v164, v164\n\t" \
    "v_rcp_f32 v165, v165\n\t" \
    "v_rcp_f32 v166, v166\n\t" \
    "v_rcp_f32 v167, v167\n\t" \
    "v_mul_f32 v188, v188, v160\n\t" \
    "v_mul_f32 v189, v189, v161\n\t" \
    "v_mul_f32 v190, v190, v162\n\t" \
    "v_mul_f32 v191, v191, v163\n\t" \
    "v_mul_f32 v192, v192, v164\n\t" \
    "v_mul_f32 v193, v193, v165\n\t" \
    "v_mul_f32 v194, v194, v166\n\t" \
    "v_mul_f32 v195, v195, v167\n\t" \
    "v_cvt_pk_bf16_f32 v188, v188, v189\n\t" \
    "v_cvt_pk_bf16_f32 v189, v190, v191\n\t" \
    "v_cvt_pk_bf16_f32 v190, v192, v193\n\t" \
    "v_cvt_pk_bf16_f32 v191, v194, v195\n\t" \
    "v_min_u32 v188, -2, v188\n\t" \
    "v_min_u32 v189, -2, v189\n\t" \
    "v_min_u32 v190, -2, v190\n\t" \
    "v_min_u32 v191, -2, v191\n\t" \
    "v_mov_b32 v216, 0x3f803f80\n\t" \
    "v_mov_b32 v217, 0x3f803f80\n\t" \
    "v_mov_b32 v218, 0x3f803f80\n\t" \
    "v_mov_b32 v219, 0x3f803f80\n\t" \
    "v_permlane16_swap_b32 v188, v190\n\t" \
    "v_permlane16_swap_b32 v189, v191\n\t" \
    "s_waitcnt vmcnt(6)\n\t" \
    "buffer_store_dwordx4 v[188:191], %[oa], %[rs], %[so] offen sc1\n\t" \
    "s_nop 1\n\t" \
    "s_setprio 0\n\t"
#define ASM_WT3_HI \
    "s_setprio 1\n\t" \
    "s_barrier\n\t" ASM_ST \
    "ds_read_b128 v[160:163], %[la] offset:0\n\t" \
    "ds_read_b128 v[164:167], %[la] offset:1024\n\t" \
    "ds_read_b128 v[168:171], %[la] offset:2048\n\t" \
    "ds_read2st64_b32 v[204:205], %[sr] offset0:20 offset1:24\n\t" \
    "ds_read_b128 v[172:175], %[la] offset:3072\n\t" \
    "ds_read_b128 v[176:179], %[la] offset:8192\n\t" \
    "ds_read_b128 v[180:183], %[la] offset:9216\n\t" \
    "ds_read_b128 v[184:187], %[la] offset:10240\n\t" \
    "s_waitcnt lgkmcnt(7)\n\t" \
    "v_mfma_f32_16x16x32_bf16 v[192:195], v[80:83], v[160:163], 0\n\t" \
    "s_waitcnt lgkmcnt(6)\n\t" \
    "v_mfma_f32_16x16x32_bf16 v[192:195], v[84:87], v[164:167], v[192:195]\n\t" \
    "v_mfma_f32_16x16x32_bf16 v[188:191], v[0:3], v[160:163], 0\n\t" \
    "ds_read_b128 v[160:163], %[la] offset:11264\n\t" \
    "v_mfma_f32_16x16x32_bf16 v[188:191], v[4:7], v[164:167], v[188:191]\n\t" \
    "ds_read_b128 v[164:167], %[la] offset:16384\n\t" \
    "s_waitcnt lgkmcnt(7)\n\t" \
    "v_mfma_f32_16x16x32_bf16 v[188:191], v[8:11], v[168:171], v[188:191]\n\t" \
    "s_waitcnt lgkmcnt(6)\n\t" \
    "s_waitcnt lgkmcnt(5)\n\t" \
    "v_mfma_f32_16x16x32_bf16 v[188:191], v[12:15], v[172:175], v[188:191]\n\t" \
    "v_permlane16_swap_b32 v204, v205\n\t" \
    "v_mfma_f32_16x16x32_bf16 v[192:195], v[88:91], v[168:171], v[192:195]\n\t" \
    "ds_read_b128 v[168:171], %[la] offset:17408\n\t" \
    "v_mfma_f32_16x16x32_bf16 v[192:195], v[92:95], v[172:175], v[192:195]\n\t" \
    "ds_read_b128 v[172:175], %[la] offset:18432\n\t" \
    "v_add_f32 v206, v204, v205\n\t" \
    "s_waitcnt lgkmcnt(6)\n\t" \
    "v_mfma_f32_16x16x32_bf16 v[192:195], v[96:99], v[176:179], v[192:195]\n\t" \
    "v_add_f32 v207, v204, v205\n\t" \
    "s_waitcnt lgkmcnt(5)\n\t" \
    "v_mfma_f32_16x16x32_bf16 v[192:195], v[100:103], v[180:183], v[192:195]\n\t" \
    "v_mfma_f32_16x16x32_bf16 v[188:191], v[16:19], v[176:179], v[188:191]\n\t" \
    "ds_read_b128 v[176:179], %[la] offset:19456\n\t" \
    "v_permlane32_swap_b32 v206, v207\n\t" \
    "v_mfma_f32_16x16x32_bf16 v[188:191], v[20:23], v[180:183], v[188:191]\n\t" \
    "ds_read_b128 v[180:183], %[la] offset:24576\n\t" \
    "s_waitcnt lgkmcnt(6)\n\t" \
    "v_mfma_f32_16x16x32_bf16 v[188:191], v[24:27], v[184:187], v[188:191]\n\t" \
    "v_add_f32 v204, v206, v207\n\t" \
    "s_waitcnt lgkmcnt(5)\n\t" \
    "v_mfma_f32_16x16x32_bf16 v[188:191], v[28:31], v[160:163], v[188:191]\n\t" \
    "v_add_f32 v205, v206, v207\n\t" \
    "v_mfma_f32_16x16x32_bf16 v[192:195], v[104:107], v[184:187], v[192:195]\n\t" \
    "ds_read_b128 v[184:187], %[la] offset:25600\n\t" \
    "v_mfma_f32_16x16x32_bf16 v[192:195], v[108:111], v[160:163], v[192:195]\n\t" \
    "ds_read_b128 v[160:163], %[la] offset:26624\n\t" \
    "v_permlane16_swap_b32 v204, v205\n\t" \
    "s_waitcnt lgkmcnt(6)\n\t" \
    "v_mfma_f32_16x16x32_bf16 v[192:195], v[112:115], v[164:167], v[192:195]\n\t" \
    "s_waitcnt lgkmcnt(5)\n\t" \
    "v_mfma_f32_16x16x32_bf16 v[192:195], v[116:119], v[168:171], v[192:195]\n\t" \
    "v_mul_f32 v205, %[kd], v205\n\t" \
    "v_mfma_f32_16x16x32_bf16 v[188:191], v[32:35], v[164:167], v[188:191]\n\t" \
    "ds_read_b128 v[164:167], %[la] offset:27648\n\t" \
    "v_mul_f32 v204, %[kd], v204\n\t" \
    "v_mfma_f32_16x16x32_bf16 v[188:191], v[36:39], v[168:171], v[188:191]\n\t" \
    "ds_read_b128 v[168:171], %[la] offset:4096\n\t" \
    "v_fma_f32 v204, -v205, v205, v204\n\t" \
    "s_waitcnt lgkmcnt(6)\n\t" \
    "v_mfma_f32_16x16x32_bf16 v[188:191], v[40:43], v[172:175], v[188:191]\n\t" \
    "v_max_f32 v204, 0, v204\n\t" \
    "s_waitcnt lgkmcnt(5)\n\t" \
    "v_mfma_f32_16x16x32_bf16 v[188:191], v[44:47], v[176:179], v[188:191]\n\t" \
    "v_add_f32 v206, %[ke], v204\n\t" \
    "v_mfma_f32_16x16x32_bf16 v[192:195], v[120:123], v[172:175], v[192:195]\n\t" \
    "ds_read_b128 v[172:175], %[la] offset:12288\n\t" \
    "v_rsq_f32 v206, v206\n\t" \
    "v_mfma_f32_16x16x32_bf16 v[192:195], v[124:127], v[176:179], v[192:195]\n\t" \
    "ds_read_b128 v[176:179], %[la] offset:20480\n\t" \
    "s_waitcnt lgkmcnt(6)\n\t" \
    "v_mfma_f32_16x16x32_bf16 v[192:195], v[128:131], v[180:183], v[192:195]\n\t" \
    "v_mul_f32 v207, -v205, v206\n\t" \
    "s_waitcnt lgkmcnt(5)\n\t" \
    "v_mfma_f32_16x16x32_bf16 v[192:195], v[132:135], v[184:187], v[192:195]\n\t" \
    "v_mul_f32 v208, v196, v207\n\t" \
    "v_mfma_f32_16x16x32_bf16 v[188:191], v[48:51], v[180:183], v[188:191]\n\t" \
    "ds_read_b128 v[180:183], %[la] offset:28672\n\t" \
    "v_mul_f32 v209, v197, v207\n\t" \
    "v_mul_f32 v210, v198, v207\n\t" \
    "v_mfma_f32_16x16x32_bf16 v[188:191], v[52:55], v[184:187], v[188:191]\n\t" \
    "v_mul_f32 v211, v199, v207\n\t" \
    "v_mul_f32 v212, v200, v207\n\t" \
    "s_waitcnt lgkmcnt(5)\n\t" \
    "v_mfma_f32_16x16x32_bf16 v[188:191], v[56:59], v[160:163], v[188:191]\n\t" \
    "v_mul_f32 v213, v201, v207\n\t" \
    "v_mul_f32 v214, v202, v207\n\t" \
    "s_waitcnt lgkmcnt(4)\n\t" \
    "v_mfma_f32_16x16x32_bf16 v[188:191], v[60:63], v[164:167], v[188:191]\n\t" \
    "v_mul_f32 v215, v203, v207\n\t" \
    "v_mov_b64 v[216:217], -1\n\t" \
    "v_mfma_f32_16x16x32_bf16 v[192:195], v[136:139], v[160:163], v[192:195]\n\t" \
    "v_mov_b64 v[218:219], -1\n\t" \
    "buffer_store_dwordx4 v[216:219], %[l16], %[rs], %[sa] offen sc1\n\t" \
    "v_mfma_f32_16x16x32_bf16 v[192:195], v[140:143], v[164:167], v[192:195]\n\t" \
    "s_waitcnt lgkmcnt(3)\n\t" \
    "v_mfma_f32_16x16x32_bf16 v[192:195], v[144:147], v[168:171], v[192:195]\n\t" \
    "s_waitcnt lgkmcnt(2)\n\t" \
    "v_mfma_f32_16x16x32_bf16 v[192:195], v[148:151], v[172:175], v[192:195]\n\t" \
    "v_mfma_f32_16x16x32_bf16 v[188:191], v[64:67], v[168:171], v[188:191]\n\t" \
    "v_mfma_f32_16x16x32_bf16 v[188:191], v[68:71], v[172:175], v[188:191]\n\t" \
    "s_waitcnt lgkmcnt(1)\n\t" \
    "v_mfma_f32_16x16x32_bf16 v[188:191], v[72:75], v[176:179], v[188:191]\n\t" \
    "s_waitcnt lgkmcnt(0)\n\t" \
    "v_mfma_f32_16x16x32_bf16 v[188:191], v[76:79], v[180:183], v[188:191]\n\t" \
    "v_mfma_f32_16x16x32_bf16 v[192:195], v[152:155], v[176:179], v[192:195]\n\t" \
    "v_mfma_f32_16x16x32_bf16 v[192:195], v[156:159], v[180:183], v[192:195]\n\t" \
    ASM_ST3 \
    "s_nop 1\n\t" \
    "v_fma_f32 v188, v188, v206, v208\n\t" \
    "v_fma_f32 v189, v189, v206, v209\n\t" \
    "v_fma_f32 v190, v190, v206, v210\n\t" \
    "v_fma_f32 v191, v191, v206, v211\n\t" \
    "v_mul_f32 v160, v188, v188\n\t" \
    "v_mul_f32 v161, v189, v189\n\t" \
    "v_mul_f32 v162, v190, v190\n\t" \
    "v_mul_f32 v163, v191, v191\n\t" \
    "v_fma_f32 v160, v160, %[k1], v220\n\t" \
    "v_fma_f32 v161, v161, %[k1], v220\n\t" \
    "v_fma_f32 v162, v162, %[k1], v220\n\t" \
    "v_fma_f32 v163, v163, %[k1], v220\n\t" \
    "v_mul_f32 v160, v188, v160\n\t" \
    "v_mul_f32 v161, v189, v161\n\t" \
    "v_mul_f32 v162, v190, v162\n\t" \
    "v_mul_f32 v163, v191, v163\n\t" \
    "v_fma_f32 v192, v192, v206, v212\n\t" \
    "v_fma_f32 v193, v193, v206, v213\n\t" \
    "v_fma_f32 v194, v194, v206, v214\n\t" \
    "v_fma_f32 v195, v195, v206, v215\n\t" \
    "v_mul_f32 v164, v192, v192\n\t" \
    "v_mul_f32 v165, v193, v193\n\t" \
    "v_mul_f32 v166, v194, v194\n\t" \
    "v_mul_f32 v167, v195, v195\n\t" \
    "v_fma_f32 v164, v164, %[k1], v220\n\t" \
    "v_fma_f32 v165, v165, %[k1], v220\n\t" \
    "v_fma_f32 v166, v166, %[k1], v220\n\t" \
    "v_fma_f32 v167, v167, %[k1], v220\n\t" \
    "v_mul_f32 v164, v192, v164\n\t" \
    "v_mul_f32 v165, v193, v165\n\t" \
    "v_mul_f32 v166, v194, v166\n\t" \
    "v_mul_f32 v167, v195, v167\n\t" \
    "v_exp_f32 v160, v160\n\t" \
    "v_exp_f32 v161, v161\n\t" \
    "v_exp_f32 v162, v162\n\t" \
    "v_exp_f32 v163, v163\n\t" \
    "v_exp_f32 v164, v164\n\t" \
    "v_exp_f32 v165, v165\n\t" \
    "v_exp_f32 v166, v166\n\t" \
    "v_exp_f32 v167, v167\n\t" \
    "v_add_f32 v160, 1.0, v160\n\t" \
    "v_add_f32 v161, 1.0, v161\n\t" \
    "v_add_f32 v162, 1.0, v162\n\t" \
    "v_add_f32 v163, 1.0, v163\n\t" \
    "v_add_f32 v164, 1.0, v164\n\t" \
    "v_add_f32 v165, 1.0, v165\n\t" \
    "v_add_f32 v166, 1.0, v166\n\t" \
    "v_add_f32 v167, 1.0, v167\n\t" \
    "v_rcp_f32 v160, v160\n\t" \
    "v_rcp_f32 v161, v161\n\t" \
    "v_rcp_f32 v162, v162\n\t" \
    "v_rcp_f32 v163, v163\n\t" \
    "v_rcp_f32 v164, v164\n\t" \
    "v_rcp_f32 v165, v165\n\t" \
    "v_rcp_f32 v166, v166\n\t" \
    "v_rcp_f32 v167, v167\n\t" \
    "v_mul_f32 v188, v188, v160\n\t" \
    "v_mul_f32 v189, v189, v161\n\t" \
    "v_mul_f32 v190, v190, v162\n\t" \
    "v_mul_f32 v191, v191, v163\n\t" \
    "v_mul_f32 v192, v192, v164\n\t" \
    "v_mul_f32 v193, v193, v165\n\t" \
    "v_mul_f32 v194, v194, v166\n\t" \
    "v_mul_f32 v195, v195, v167\n\t" \
    "v_cvt_pk_bf16_f32 v188, v188, v189\n\t" \
    "v_cvt_pk_bf16_f32 v189, v190, v191\n\t" \
    "v_cvt_pk_bf16_f32 v190, v192, v193\n\t" \
    "v_cvt_pk_bf16_f32 v191, v194, v195\n\t" \
    "v_min_u32 v188, -2, v188\n\t" \
    "v_min_u32 v189, -2, v189\n\t" \
    "v_min_u32 v190, -2, v190\n\t" \
    "v_min_u32 v191, -2, v191\n\t" \
    "s_nop 1\n\t" \
    "v_permlane16_swap_b32 v188, v190\n\t" \
    "v_permlane16_swap_b32 v189, v191\n\t" \
    "s_waitcnt vmcnt(1)\n\t" \
    "buffer_store_dwordx4 v[188:191], %[oa], %[rs], %[so] offen sc1\n\t" \
    "s_nop 1\n\t"
#define ASM_POLL5_I \
    "buffer_load_dwordx4 v[232:235], %[l16], %[rs], %[s0] offen offset:0 sc1\n\t" \
    "buffer_load_dwordx4 v[236:239], %[l16], %[rs], %[s0] offen offset:1024 sc1\n\t" \
    "buffer_load_dwordx4 v[240:243], %[l16], %[rs], %[s0] offen offset:2048 sc1\n\t" \
    "buffer_load_dwordx4 v[244:247], %[l16], %[rs], %[s0] offen offset:3072 sc1\n\t" \
    "buffer_load_dwordx4 v[248:251], %[l16], %[rs], %[s1] offen offset:0 sc1\n\t"
using u32x8 = __attribute__((ext_vector_type(8))) unsigned;
using u32x16 = __attribute__((ext_vector_type(16))) unsigned;
using u32x32 = __attribute__((ext_vector_type(32))) unsigned;
struct WRegs { u32x32 w[6]; u32x8 t; };
#define WREGS(W) "{v[0:31]}"(W.w[0]), "{v[32:63]}"(W.w[1]), "{v[64:95]}"(W.w[2]), "{v[96:127]}"(W.w[3]), "{v[128:159]}"(W.w[4]), "{v[160:191]}"(W.w[5]), "{v[192:199]}"(W.t)
__device__ __forceinline__ u32x32 cat32(u32x8 a, u32x8 b, u32x8 c, u32x8 d) {
    const u32x16 lo = __builtin_shufflevector(a, b, 0, 1, 2, 3, 4, 5, 6, 7, 8, 9, 10, 11, 12, 13, 14, 15);
    const u32x16 hi = __builtin_shufflevector(c, d, 0, 1, 2, 3, 4, 5, 6, 7, 8, 9, 10, 11, 12, 13, 14, 15);
    return __builtin_shufflevector(lo, hi, 0, 1, 2, 3, 4, 5, 6, 7, 8, 9, 10, 11, 12, 13, 14, 15, 16, 17, 18, 19, 20, 21, 22, 23, 24, 25, 26, 27, 28, 29, 30, 31);
}
#define WOUT(W) "=&{v[0:31]}"(W.w[0]), "=&{v[32:63]}"(W.w[1]), "=&{v[64:95]}"(W.w[2]), "=&{v[96:127]}"(W.w[3]), "=&{v[128:159]}"(W.w[4]), "=&{v[160:191]}"(W.w[5]), "=&{v[192:199]}"(W.t)
#define WTMP "memory", "scc", "v200", "v201", "v202", "v203", "v204", "v205", "v206", "v207", "v208", "v209", "v210", "v211", "v212", "v213", "v214", "v215", "v216", "v217", "v218", "v219", "v220", "v221", "v222", "v223", "v224", "v225", "v226", "v227", "v228", "v229", "v230", "v231", "v232", "v233", "v234", "v235", "v236", "v237", "v238", "v239"
template <bool NT>
__device__ __forceinline__ void load_w3(WRegs& W, const bf16_t* m, int wave, unsigned c0, bool hq) {
    const int ln = lane_id();
    unsigned o[3], s[3], t;
#pragma unroll
    for (int k = 0; k < 3; ++k) {
        const int row = (k * 64 + ln) / 24, sg = (k * 64 + ln) % 24;
        o[k] = row * (HD * 2) + c0 + (hq ? ((sg >> 1) & 1) * 384 + ((0x140 >> ((sg >> 3) * 4)) & 15) * 32 + (sg & 5) * 16 : sg * 16); s[k] = wave * 13824 + row * 416 + sg * 16;
    }
    const unsigned tr = (ln >> 4) * 3456 + ((ln >> 2) & 3) * 416 + (wave & 3) * 32 + (ln & 3) * 8;
#undef WNT
#define WNT " nt"
    if constexpr (NT) {
    asm volatile(ASM_WLOAD3 : WOUT(W), [t] "=&s"(t) : [rs] "s"(mkrsrc(m, DM * HD * 2)), [wx] "s"(wave), [o0] "v"(o[0]), [o1] "v"(o[1]), [o2] "v"(o[2]), [s0] "v"(s[0]), [s1] "v"(s[1]), [s2] "v"(s[2]),
                 [t0] "v"(tr + wave * 13824), [t1] "v"(tr + (wave ^ 1) * 13824), [t2] "v"(tr + (wave ^ 2) * 13824), [t3] "v"(tr + (wave ^ 3) * 13824), [t4] "v"(tr + (wave & 4) * 13824),
                 [wl] "v"(L_W + wave * 10240 + ln * 16) : WTMP);
    } else {
#undef WNT
#define WNT ""
    asm volatile(ASM_WLOAD3 : WOUT(W), [t] "=&s"(t) : [rs] "s"(mkrsrc(m, DM * HD * 2)), [wx] "s"(wave), [o0] "v"(o[0]), [o1] "v"(o[1]), [o2] "v"(o[2]), [s0] "v"(s[0]), [s1] "v"(s[1]), [s2] "v"(s[2]),
                 [t0] "v"(tr + wave * 13824), [t1] "v"(tr + (wave ^ 1) * 13824), [t2] "v"(tr + (wave ^ 2) * 13824), [t3] "v"(tr + (wave ^ 3) * 13824), [t4] "v"(tr + (wave & 4) * 13824),
                 [wl] "v"(L_W + wave * 10240 + ln * 16) : WTMP);
    }
}
#undef WNT
#define WNT " nt"
__device__ __forceinline__ void load_wo(WRegs& W, const bf16_t* m, int wave, int h) {
    const int ln = lane_id();
    unsigned o[5], s[5], t;
#pragma unroll
    for (int k = 0; k < 5; ++k) {
        const int row = (k * 64 + ln) / 20, sg = (k * 64 + ln) % 20;
        o[k] = (h * DH + row) * (DM * 2) + sg * 16 + (wave & 3) * 320; s[k] = wave * 13824 + row * 352 + sg * 16;
    }
    asm volatile(ASM_WLOADO : WOUT(W), [t] "=&s"(t) : [rs] "s"(mkrsrc(m, DM * HD * 2)), [o0] "v"(o[0]), [o1] "v"(o[1]), [o2] "v"(o[2]), [o3] "v"(o[3]), [o4] "v"(o[4]),
                 [s0] "v"(s[0]), [s1] "v"(s[1]), [s2] "v"(s[2]), [s3] "v"(s[3]), [s4] "v"(s[4]), [tr] "v"(wave * 13824 + (ln >> 2) * 352 + (ln & 3) * 8), [wl] "v"(L_W + wave * 10240 + ln * 16) : WTMP);
}
using s16x4 = __attribute__((ext_vector_type(4))) short;
template <int NT, int KS, int N>
__device__ __forceinline__ void load_wn(u32x4 (&f)[NT * KS], const bf16_t* m, int rows, int wave, int c0, int k0) {
    const int ln = lane_id();
    const rsrc_t rs = mkrsrc(m, rows * N * 2);
#pragma unroll
    for (int k = 0; k < KS; ++k)
#pragma unroll
        for (int n = 0; n < NT; ++n) f[n * KS + k] = __builtin_amdgcn_raw_buffer_load_b128(rs, ((n * 64 + ln) / (2 * NT)) * (N * 2) + ((n * 64 + ln) % (2 * NT)) * 16, (k0 + k) * (N * 64) + c0 * 32, 0);
    const unsigned st = wave * 10240 + ln * 16, tr = wave * 10240 + ((ln >> 4) * 8 + ((ln >> 2) & 3)) * (NT * 32) + (ln & 3) * 8;
#pragma unroll
    for (int k = 0; k < KS; ++k) {
#pragma unroll
        for (int n = 0; n < NT; ++n) lds_st<u32x4>(st + n * 1024, f[n * KS + k]);
        asm volatile("" ::: "memory");
#pragma unroll
        for (int n = 0; n < NT; ++n) {
            const u32x2 a = __builtin_bit_cast(u32x2, __builtin_amdgcn_ds_read_tr16_b64_v4i16((LAS s16x4*)(tr + n * 32)));
            const u32x2 b = __builtin_bit_cast(u32x2, __builtin_amdgcn_ds_read_tr16_b64_v4i16((LAS s16x4*)(tr + n * 32 + NT * 128)));
            f[n * KS + k] = (u32x4){a[0], a[1], b[0], b[1]};
        }
        asm volatile("" ::: "memory");
    }
}
struct C1Regs { u32x8 lw0, lw1, z1; u32x4 z0; };
#define XG_OUT "=&{v[200:207]}"(C.lw0), "=&{v[224:227]}"(C.z0), "=&{v[228:235]}"(C.lw1), "=&{v[244:251]}"(C.z1)
#define XG_IN WREGS(W), [xa] "v"(xa), [wl] "v"(wl), [qs] "s"(qs)
#define XG_CLOB "memory", "scc", "v208", "v209", "v210", "v211", "v212", "v213", "v214", "v215", "v216", "v217", "v218", "v219", "v220", "v221", "v222", "v223", "v236", "v237", "v238", "v239", "v240", "v241", "v242", "v243", "v252"
constexpr int XS0 = 8;
__device__ __forceinline__ unsigned init_wait(const Params& P, int c, int ph, unsigned need = 0x220080u) {
    unsigned w;
    for (int spins = 0;; ++spins) {
        unsigned o;
        asm volatile("v_mov_b32 %0, 0" : "=v"(o));
        w = __builtin_amdgcn_readfirstlane(__builtin_amdgcn_raw_buffer_load_b32(mkrsrc(P.ws + FL0, FL_BYTES), o, FL_IN + (ph * 8 + (c & 7)) * 128, 16));
        if ((w & need) == need) break;
        __builtin_amdgcn_s_sleep(8);
        if (spin_fail(spins, P)) break;
    }
    asm volatile("" ::: "memory");
    return w;
}
#define PLACE() const unsigned pw_ = init_wait(P, wave, L); const int par = (L ^ (pw_ >> 16)) & 1, cpy = x ^ ((pw_ >> 20) & 1); const rsrc_t rr = mkrsrc(P.ws + par * 8192, RG_L); (void)cpy; (void)rr
__device__ __forceinline__ void xn_wait(const Params& P, rsrc_t rr, int seg, int cpy) {
    for (int spins = 0;; ++spins) {
        const unsigned f = __builtin_amdgcn_raw_buffer_load_b32(rr, (lane_id() & 15) * 128, RG_FX + (cpy & 3) * 2048, 16);
        if (__all(f > (unsigned)seg)) break;
        __builtin_amdgcn_s_sleep(4);
        if (spin_fail(spins, P)) break;
    }
    asm volatile("" ::: "memory");
}
__device__ __forceinline__ unsigned c1_dst(int wave, bool ktype) {
    const int ln = lane_id(), l15 = ln & 15, hf = (wave >> 1) & 1;
    const int rb16 = ((ln >> 2) & 4) * 16 + (ln >> 5) * 32;
    return (ktype ? L_KM + (l15 >> 2) * 128 + hf * 8 + (l15 & 3) * 2
                  : L_Q + (l15 >> 3) * 256 + ((l15 >> 1) & 1) * 128 + ((l15 >> 2) & 1) * 8 + hf * 4 + (l15 & 1) * 2) + (wave & 1) * 1536 + rb16;
}
template <int NP>
__device__ __forceinline__ void crit1(const WRegs& W, C1Regs& C, rsrc_t rr, unsigned sbase, int wave, unsigned wl, unsigned dsto, unsigned& budget, unsigned& fv, unsigned fs
) {
    unsigned abase;
    asm volatile("v_and_b32 %0, 0x3ff, %1\n\tv_or_b32 %0, %2, %0" : "=&v"(abase) : "v"(wl), "s"(wave * 1024));
    ASM_ST_DECL;
#define C1_OUT "+{v[224:227]}"(C.z0), "+{v[244:251]}"(C.z1), "+{v[200:207]}"(C.lw0), "+{v[228:235]}"(C.lw1)
#define C1_IN WREGS(W), [ab] "v"(abase), [wl] "v"(wl), [dst] "v"(dsto)
#define C1_CLOB "memory", "vcc", "scc", "v208", "v209", "v210", "v211", "v212", "v213", "v214", "v215", "v216", "v217", "v218", "v219", "v220", "v221", "v222", "v223", "v236", "v237", "v238", "v239", "v240", "v241", "v242", "v243"
#define C1_POLL [rs] "s"(rr), [s0] "s"(sbase), [s1] "s"(sbase + 4096), [s2] "s"(sbase + 2 * BLK)
    ST(1);
    if constexpr (NP == 13) asm volatile(ASM_CRIT1_V3 "s_waitcnt vmcnt(0)" : C1_OUT, [n] "+s"(budget), [fv] "+v"(fv) ASM_ST_OUT ASM_ST2_OUT : C1_IN, C1_POLL, [fs] "s"(fs) : C1_CLOB);
    else if constexpr (NP == 12) asm volatile(ASM_CRIT1_V2 "s_waitcnt vmcnt(0)" : C1_OUT, [n] "+s"(budget), [fv] "+v"(fv) ASM_ST_OUT ASM_ST2_OUT : C1_IN, C1_POLL, [fs] "s"(fs) : C1_CLOB);
    else if constexpr (NP == 3) asm volatile(ASM_CRIT1_3 : C1_OUT, [n] "+s"(budget) ASM_ST_OUT ASM_ST2_OUT : C1_IN, C1_POLL : C1_CLOB);
    else if constexpr (NP == 2) asm volatile(ASM_CRIT1_2 : C1_OUT, [n] "+s"(budget) ASM_ST_OUT ASM_ST2_OUT : C1_IN, C1_POLL : C1_CLOB);
    else asm volatile(ASM_CRIT1_N : C1_OUT ASM_ST_OUT : C1_IN : C1_CLOB);
    SB();
    ASM_ST_SAVE(3);
    if constexpr (NP != 0) { ASM_ST2_SAVE(2); }
}
#define STARG

#define SRG 5
#undef STROLE
#define STROLE 1
__device__ __forceinline__ void s_role(const Params& P, int wave, int L, int h, int x, unsigned t0_) {
    const char* xn = P.ws + PG0 + PG_XN + L * PG_LX;
    auto xdma = [&](int seg, int hb, int par) {
        const unsigned l16 = lane_id() * 16;
        const int mt = (wave >> 1) & 1;
        const char* g = xn + pgb(seg, par);
        if (hb == 0) {
            dma4(g + blin(mt * 20480 + (wave & 1) * 4096), l16, L_XA + mt * 16384 + (wave & 1) * 4096);
            dmaq<0>(g + blin(mt * 20480 + 16384) + (wave & 1) * 1024, l16, L_XB + mt * 4096 + (wave & 1) * 1024);
        } else {
            dma4(g + blin(mt * 20480 + 8192), l16, L_XA + mt * 16384 + 8192); dma4(g + blin(mt * 20480 + 12288), l16, L_XA + mt * 16384 + 12288);
            dmaq<2>(g + blin(mt * 20480 + 16384), l16, L_XB + mt * 4096); dmaq<3>(g + blin(mt * 20480 + 16384), l16, L_XB + mt * 4096);
        }
    };
    const float SCL = 0.07216878364870322f * 1.4426950408889634f;
    auto run = [&](auto Cc) {
    constexpr int CLS = decltype(Cc)::value;
    WRegs wf;
    MARK(!L && h == 0 && wave == 0, 0);
    unsigned hm_ = 0, hc_ = 0;
    if (HM_T == -2) hm_ = (unsigned)__builtin_amdgcn_s_memrealtime() - t0_;
    load_w3<(NTM & (CLS == 3 ? 2 : 1)) != 0>(wf, P.w[CLS == 3], wave, h * 384, false);
    MARKX(!L && wave == 0, 1);
    if (HM_T == -1) hm_ = (unsigned)__builtin_amdgcn_s_memrealtime() - t0_;
    PLACE();
    if (CLS == 3) {
        if (L) xn_wait(P, rr, 0, cpy);
        xdma(0, 0, par);
        if (wave == 4 || wave == 7) xdma(0, 1, par);
        wait_vm<0>();
    }
    bar();
    if (CLS == 3 && wave == 7 && L) xn_wait(P, rr, 1, cpy);
    const unsigned qs = (CLS == 3 ? L_KX : L_QX) + (wave & 1) * 3072 + ((wave >> 1) & 1) * 8;
    unsigned xa, wl, dsto;
    if (CLS == 3) { xa = L_XA + wave * 2048 + lane_id() * 16; wl = L_W + wave * 10240 + lane_id() * 16; dsto = c1_dst(wave, true); }
    __builtin_amdgcn_s_setprio(CLS == 3 ? 1 : 2);
    unsigned budget = 1u << 18, tb2 = 0;
    STDECL;
    LDECL;
#pragma unroll 1
    for (int t = 0; t < NSEG; ++t) {
        const unsigned slot = t & (NSLOT - 1);
        C1Regs C;
        {
            const WRegs& W = wf;
            ASM_ST_DECL;
            if constexpr (CLS == 3) {
                const int mt = (wave >> 1) & 1;
                const char* g = xn + pgb(t + 1 < NSEG ? t + 1 : t, par);
                asm volatile(ASM_XG_S_HI : XG_OUT ASM_ST_OUT : XG_IN, [slp] "s"(SLP4), [g0] "s"(g + blin(mt * 20480 + (wave & 1) * 4096)), [d0] "s"(L_XA + mt * 16384 + (wave & 1) * 4096),
                             [g1] "s"(g + blin(mt * 20480 + 16384) + (wave & 1) * 1024), [d1] "s"(L_XB + mt * 4096 + (wave & 1) * 1024) : XG_CLOB);
                wait_vm<0>();
                LB(t);
            } else {
                const unsigned l16 = lane_id() * 16;
                xa = L_XA + wave * 2048 + l16; wl = L_W + wave * 10240 + l16;
#define XG_Q(A) asm volatile("s_cmp_lt_u32 %[t], 2\n\ts_cbranch_scc1 .Lq3%=\n\t" A "s_branch .Lqe%=\n\t.Lq3%=:\n\t" ASM_XG_S_LO ".Lqe%=:" : XG_OUT ASM_ST_OUT : XG_IN, [t] "s"(t) : XG_CLOB)
                if constexpr (CLS == 0 || CLS == 4) XG_Q(ASM_XG_S_LO1); else XG_Q(ASM_XG_S_LO2);
                __builtin_amdgcn_s_setprio(0);
                if constexpr (CLS == 0) {
                    const int ln = lane_id();
                    const u32x4 s4 = sent4();
                    const unsigned poff = (ln * 16 + ((ln < 8) ? h * 128 : (1 + (h >> 4)) * 2 * BLK - 128 + (h & 15) * 512)) | ((ln >= 40 || t < 2) ? 0x40000000u : 0u);
#pragma unroll
                    for (int c = 0; c < (int)NCP; ++c) st128(s4, rr, poff, RG_XP + ((t - 2) & (NSLOT - 1)) * XP_SLOT + (c >> 1) * XP_PAIR + (c & 1) * BLK);
                    const unsigned pbase = RG_PX + ((t - 3) & (NSLOT - 1)) * PX_SLOT + (h >> 2) * BLK;
                    const unsigned o2 = ((ln & 16) ? 5120 + ((h >> 1) & 1) * 1024 + (h & 1) * 256 + (ln >> 5) * 512 : 4096 + (h & 3) * 256 + (ln >> 5) * 3072) + (ln & 15) * 16;
                    if (t >= 3) { st128(s4, rr, ln * 16, pbase + (h & 3) * 1024); st128(s4, rr, o2, pbase); }
                }
                if (CLS == 4 && t >= HT0) {
                    const int ln = lane_id(), u = h >> 2;
                    for (int spins = 0;; ++spins) {
                        const unsigned fl = __builtin_amdgcn_raw_buffer_load_b32(rr, (ln & 3) * 128, RG_FH + BLK + u * 512, 16);
                        if (__all(fl > (unsigned)t)) break;
                        __builtin_amdgcn_s_sleep(2);
                        if (spin_fail(spins, P)) break;
                    }
                    asm volatile("" ::: "memory");
                    const char* g = P.ws + PG0 + pgb(t, par) + PG_HV + (16 + u) * 3 * BLK;
                    unsigned t0, t1, vo;
                    static_assert(L_QX == 16384, "");
                    asm volatile("s_add_u32 %0, %4, 0\n\ts_and_b32 %1, %0, 0xe000\n\ts_add_u32 %0, %0, %1\n\tv_add_u32 %2, %0, %5\n\ts_mov_b32 m0, 16384\n\ts_nop 0\n\tglobal_load_lds_dwordx4 %2, %3 sc1\n\t"
                                 "s_add_u32 %0, %4, 1024\n\ts_and_b32 %1, %0, 0xe000\n\ts_add_u32 %0, %0, %1\n\tv_add_u32 %2, %0, %5\n\ts_mov_b32 m0, 17408\n\ts_nop 0\n\tglobal_load_lds_dwordx4 %2, %3 sc1\n\t"
                                 "s_add_u32 %0, %4, 2048\n\ts_and_b32 %1, %0, 0xe000\n\ts_add_u32 %0, %0, %1\n\tv_add_u32 %2, %0, %5\n\ts_mov_b32 m0, 19456\n\ts_nop 0\n\tglobal_load_lds_dwordx4 %2, %3 sc1\n\t"
                                 "s_add_u32 %0, %4, 6144\n\ts_and_b32 %1, %0, 0xe000\n\ts_add_u32 %0, %0, %1\n\tv_add_u32 %2, %0, %5\n\ts_mov_b32 m0, 22528\n\ts_nop 0\n\tglobal_load_lds_dwordx4 %2, %3 sc1\n\t"
                                 "s_add_u32 %0, %4, 7168\n\ts_and_b32 %1, %0, 0xe000\n\ts_add_u32 %0, %0, %1\n\tv_add_u32 %2, %0, %5\n\ts_mov_b32 m0, 23552\n\ts_nop 0\n\tglobal_load_lds_dwordx4 %2, %3 sc1\n\t"
                                 "s_add_u32 %0, %4, 8192\n\ts_and_b32 %1, %0, 0xe000\n\ts_add_u32 %0, %0, %1\n\tv_add_u32 %2, %0, %5\n\ts_mov_b32 m0, 25600\n\ts_nop 0\n\tglobal_load_lds_dwordx4 %2, %3 sc1\n\t"
                                 : "=&s"(t0), "=&s"(t1), "=&v"(vo) : "s"(g), "s"((((h >> 1) & 1) * 12 + (h & 1) * 3) * 1024), "v"(ln * 16) : "memory", "scc");
                }
                dsto = c1_dst(wave, false);
                while ((unsigned)__builtin_amdgcn_s_memtime() - tb2 < STD) __builtin_amdgcn_s_sleep(2);
            }
            ASM_ST_SAVE(7);
            ST(6);
        }
        ST(0);
        unsigned fv = 0;
        if (CLS == 2) fv = (lane_id() & 15) * 128;
        crit1<CLS == 3 ? 0 : CLS < 2 ? 3 : CLS == 2 ? 12 : 2>(wf, C, rr, RG_XM + slot * XM_SLOT + (cpy >> 1) * XM_PAIR + (cpy & 1) * BLK, wave, wl, dsto, budget, fv, RG_FX + (cpy & 3) * 2048 STARG);
        if (CLS != 3) tb2 = (unsigned)__builtin_amdgcn_s_memtime();
        if constexpr (CLS == 0) {
            const unsigned sp = RG_XP + slot * XP_SLOT, sx = sp + (1 + (h >> 4)) * 2 * BLK + (h & 15) * 512;
            ASM_ST_DECL;
            asm volatile(ASM_SM : [c0] "+s"(budget) ASM_ST_OUT : WREGS(wf), [rr] "s"(rr), [sx] "s"(sx), [sy] "s"(sx + XP_PAIR), [sm] "s"(sp + h * 128), [cs] "s"(BLK), [cp] "s"(XP_PAIR), [c70] "s"(0x70), [scl] "s"(SCL)
                         : "memory", "v200", "v201", "v202", "v203", "v204", "v205", "v206", "v207", "v208", "v209", "v210", "v211", "v212", "v213", "v214", "v215", "v216", "v217", "v218", "v219", "v220", "v221", "v222", "v223", "v224", "v225", "v226", "v227", "v228", "v229", "v230", "v231", "v232", "v233", "v234", "v235", "v236", "v237", "v238", "v239", "v240", "v241", "v242", "v243", "v244", "v245", "v246", "v247", "v248", "v249", "v250", "v251", "v252", "v253", "v254", "v255");
            ASM_ST_SAVE(4);
            ST(5);
        } else if constexpr (CLS == 1 || CLS == 4) {
            const int ln = lane_id(), l15 = ln & 15, l4 = ln >> 4;
            constexpr int mt = (CLS == 4);
            const unsigned qa = L_QX + mt * 6144 + ln * 16, kmo = L_KM + l4 * 128 + (ln & 7) * 16;
            const unsigned xo = mt ? (h & 3) * 1024 : 5120 + ((h >> 1) & 1) * 1024 + (h & 1) * 256;
            const unsigned pm = l4 < 2 ? (mt ? 4096 : 7168) + (h & 3) * 256 - xo + l15 * 16 + l4 * 8 : OOB;
            const unsigned so = RG_PX + slot * PX_SLOT + (h >> 2) * BLK + xo;
            f32x4 sd;
#pragma unroll
            for (int r = 0; r < 4; ++r) sd[r] = (l4 * 4 + r > l15) ? -3.0e38f : 0.f;
            ASM_ST_DECL;
#define P4_ARGS : "+{v[240:243]}"(sd) ASM_ST_OUT : WREGS(wf), [qa] "v"(qa), [km] "v"(kmo), [pm] "v"(pm), [rr] "s"(rr), [so] "s"(so), [scl] "s"(SCL) : "memory", "v200", "v201", "v202", "v203", "v204", "v205", "v206", "v207", "v208", "v209", "v210", "v211", "v212", "v213", "v214", "v215", "v216", "v217", "v218", "v219", "v220", "v221", "v222", "v223", "v224", "v225", "v226", "v227", "v228", "v229", "v230", "v231", "v232", "v233", "v234", "v235", "v236", "v237", "v238", "v239", "v244", "v245", "v246", "v247", "v248", "v249", "v250", "v251"
            if constexpr (mt) asm volatile(ASM_P4_1 P4_ARGS); else asm volatile(ASM_P4_0 P4_ARGS);
            ASM_ST_SAVE(4);
            ST(5);
        } else {
            bar();
            ST(4);
            if (CLS == 2 && L && t + 2 < NSEG && !__all(fv > (unsigned)t + 2)) xn_wait(P, rr, t + 2, cpy);
            if (CLS == 2) { int tt = t; asm volatile("" : "+s"(tt)); if (tt + 2 == XS0) init_wait(P, h, 1); }
            if (CLS == 3) LA();
            if (CLS == 3 && t == HM_T) hm_ = (unsigned)__builtin_amdgcn_s_memrealtime() - t0_;
            if (CLS == 3 && HM_C && t == 8) hc_ = (unsigned)__builtin_amdgcn_s_memtime();
            if (CLS == 3 && HM_C && t == 56) hm_ = ((unsigned)__builtin_amdgcn_s_memtime() - hc_) / 20u;
            {
                if (CLS == 3 && (wave == 4 || wave == 7)) {
                    if (t + 1 < NSEG) xdma(t + 1, 1, par);
                }
                ST(5);
            }
        }
        STFLUSH(t);
    }
    LF();
    if (CLS == 3 && wave == 5 && (h == HM_H || HM_H < 0) && L == HM_L) while ((unsigned)__builtin_amdgcn_s_memrealtime() - t0_ < HM_B + hm_) __builtin_amdgcn_s_sleep(1);
    };
    if (wave == 0) run(std::integral_constant<int, 0>{});
    else if (wave == 1) run(std::integral_constant<int, 1>{});
    else if (wave == 2) run(std::integral_constant<int, 4>{});
    else if (wave == 3) run(std::integral_constant<int, 2>{});
    else run(std::integral_constant<int, 3>{});
}

#undef STROLE
#define STROLE 8
__device__ __forceinline__ void h_role(const Params& P, int wave, int L, int i, int x) {
    const rsrc_t rp = mkrsrc(P.ws + PG0, PG_L);
    const char* xn = P.ws + PG0 + PG_XN + L * PG_LX;
    auto run = [&](auto Cc) {
    constexpr bool HI = decltype(Cc)::value;
    const int mt = (wave >> 1) & 1;
    WRegs wf;
    load_w3<(NTM & 16) != 0>(wf, i < 16 ? P.w[2] : P.w[0], wave, (i < 16 ? i * 2 + HI : (i - 16) * 4 + HI * 2) * 384, i >= 16);
    PLACE();
    auto xdma = [&](int seg, int hb) {
        const unsigned l16 = lane_id() * 16;
        const char* g = xn + pgb(seg, par);
        dma4(g + blin(mt * 20480 + hb * 8192 + (wave & 1) * 4096), l16, L_XA + mt * 16384 + hb * 8192 + (wave & 1) * 4096);
        dmaq<0>(g + blin(mt * 20480 + 16384) + hb * 2048 + (wave & 1) * 1024, l16, L_XB + mt * 4096 + hb * 2048 + (wave & 1) * 1024);
    };
    const unsigned qs = (HI ? L_KX : L_QX) + (wave & 1) * 3072 + mt * 8;
    const unsigned xa = L_XA + wave * 2048 + lane_id() * 16, wl = L_W + wave * 10240 + lane_id() * 16;
    const unsigned fp = RG_FH + (i >> 4) * BLK + ((i & 15) * 4 + (wave & 3)) * 128;
    const unsigned fb = L ? RG_FX + (cpy & 3) * 2048 : RG_FV + (i & 3) * 2048, fa = L ? 0 : HAH;
    unsigned go = 0, all = 0;
    STDECL;
    LDECL;
    for (int t = HT0; t <= NSEG; ++t) {
        ST(0);
        if constexpr (HI) {
            const unsigned l16 = lane_id() * 16;
            if (t > HT0) {
                u32x4 v[6];
                const int w4 = wave - 4;
#pragma unroll
                for (int k = 0; k < 6; ++k) v[k] = lds_ld<u32x4>(L_QX + ((k >> 1) * 8 + w4 * 2 + (k & 1)) * 1024 + l16);
                const unsigned so = pgb(t - 1, par) + PG_HV + i * 3 * BLK + w4 * 2048;
#pragma unroll
                for (int k = 0; k < 6; ++k) st128(v[k], rp, l16 + (k & 1) * 1024, so + (k >> 1) * BLK);
                if (!all) {
                    wait_vm<0>();
                    if (l16 < 512) __builtin_amdgcn_raw_buffer_store_b32(t, rr, l16 >> 2, fp, 16);
                }
            }
            ST(1);
            if (t == NSEG) break;
            if (!all) bar();
            xdma(t, 1);
        } else {
            if (t == NSEG) break;
            if (!go) {
                for (int spins = 0;; ++spins) {
                    if (__all(__builtin_amdgcn_raw_buffer_load_b32(rr, (lane_id() & 15) * 128, fb, 16) + fa > (unsigned)t)) break;
                    __builtin_amdgcn_s_sleep(HSL);
                    if (spin_fail(spins, P)) break;
                }
                asm volatile("" ::: "memory");
                xdma(t, 0); wait_vm<0>();
            }
            if (!all) bar();
        }
        ST(2);
        C1Regs C;
        const WRegs& W = wf;
        ASM_ST_DECL;
        if constexpr (HI) {
            const unsigned l4 = lane_id() * 4;
            asm volatile(ASM_XG_H_HI : XG_OUT ASM_ST_OUT : XG_IN, [rr] "s"(rr), [fp] "s"(fp), [tv] "s"(t), [fv] "v"((all && l4 < 128) ? l4 : OOB) : XG_CLOB);
        } else {
            asm volatile(ASM_XG_H_LO : XG_OUT ASM_ST_OUT : XG_IN : XG_CLOB);
            if (t + 1 == XS0) init_wait(P, wave, 1);
            const unsigned fv = __builtin_amdgcn_raw_buffer_load_b32(rr, (lane_id() & 15) * 128, fb, 16) + fa;
            go = __builtin_amdgcn_readfirstlane((t + 1 < NSEG && __all(fv > (unsigned)t + 1)) ? 1u : 0u);
            if (go) xdma(t + 1, 0);
            lds_st<unsigned>(L_MEM + wave * 4, go);
            wait_vm<0>();
        }
        ASM_ST_SAVE(3);
        ST(4);
        if (!HI) { LB(t); LA(); }
        PMARK(L && wave == 7, 6, t);
        bar();
        const u32x4 gv = lds_ld<u32x4>(L_MEM);
        all = __builtin_amdgcn_readfirstlane(gv[0] & gv[1] & gv[2] & gv[3]);
        ST(5);
        STFLUSH(t);
    }
    LF();
    };
    if (wave < 4) run(std::false_type{}); else run(std::true_type{});
}

#undef STROLE
#define STROLE 2
__device__ __forceinline__ void v_role(const Params& P, int wave, int L, int h, int x) {
    WRegs wf;
    STDECL;
    auto runv = [&](auto Cc) {
        load_w3<(NTM & 4) != 0>(wf, P.w[2], wave, h * 384, false);
        MARKX(!L && wave == 0, 6);
        PLACE();
        unsigned budget = 1u << 18;
        const unsigned wl = L_W + wave * 10240 + lane_id() * 16, dsto = c1_dst(wave, false);
        auto vfetch = [&](int seg, int tries) {
            const int ln = lane_id();
            for (int spins = 0; tries != -2; ++spins) {
                if (spins == tries) return false;
                const unsigned f = __builtin_amdgcn_raw_buffer_load_b32(rr, (ln & 3) * 128, RG_FH + (h >> 1) * 512, 16);
                if (__all(f > (unsigned)seg)) break;
                __builtin_amdgcn_s_sleep(4);
                if (spin_fail(spins, P)) break;
            }
            asm volatile("" ::: "memory");
            const char* g = P.ws + PG0 + pgb(seg, par) + PG_HV + (h >> 1) * 3 * BLK;
#pragma unroll
            for (int k = 0; k < 3; ++k) {
                const int f = (h & 1) * 12 + wave * 3 + k;
                dmaq<0>(g + (f >> 3) * BLK + (f & 7) * 1024, ln * 16, L_QX + (seg & 1) * 12288 + (wave * 3 + k) * 1024);
            }
            return true;
        };
#pragma unroll 1
        for (int t = 0; t < HT0; ++t) {
            if (L) xn_wait(P, rr, t, cpy);
            const unsigned l16 = lane_id() * 16;
            const char* g = P.ws + PG0 + PG_XN + L * PG_LX + pgb(t, par);
            dma4(g + blin(wave * 4096), l16, L_XA + wave * 4096); dma4(g + blin(20480 + wave * 4096), l16, L_XA + 16384 + wave * 4096);
            dmaq<0>(g + blin(16384) + wave * 1024, l16, L_XB + wave * 1024); dmaq<0>(g + blin(20480 + 16384) + wave * 1024, l16, L_XB + 4096 + wave * 1024);
            wait_vm<0>();
            bar();
            C1Regs C;
            const WRegs& W = wf;
            const unsigned xa = L_XA + wave * 2048 + l16, qs = L_QX + (wave & 1) * 3072 + ((wave >> 1) & 1) * 8 + t * 12288;
            asm volatile(ASM_XG_V : XG_OUT : XG_IN : XG_CLOB);
            if (t == 0) bar();
        }
        bool seen = false;
        unsigned tb2 = 0;
        for (int t = 0; t < NSEG; ++t) {
            if (t + 1 - V1A >= HT0 && t + 1 - V1A < NSEG) { vfetch(t + 1 - V1A, seen ? -2 : -1); wait_vm<0>(); }
            ST(7);
            bar();
            C1Regs C;
            asm volatile("ds_read_b128 v[200:203], %4\n\tds_read_b128 v[204:207], %4 offset:1024\n\tds_read_b128 v[228:231], %4 offset:2048\n\tds_read_b128 v[232:235], %4 offset:3072\n\t"
                         "v_mov_b64 v[224:225], 0\n\tv_mov_b64 v[226:227], 0\n\tv_mov_b64 v[244:245], 0\n\tv_mov_b64 v[246:247], 0\n\tv_mov_b64 v[248:249], 0\n\tv_mov_b64 v[250:251], 0\n\ts_waitcnt lgkmcnt(0)"
                         : XG_OUT : "v"(wl) : "memory");
            ST(6);
            while ((unsigned)__builtin_amdgcn_s_memtime() - tb2 < VTD) __builtin_amdgcn_s_sleep(1);
            unsigned fv = (lane_id() & 3) * 128;
            crit1<10 + decltype(Cc)::value>(wf, C, rr, RG_XM + (t & (NSLOT - 1)) * XM_SLOT + (cpy >> 1) * XM_PAIR + (cpy & 1) * BLK, wave, wl, dsto, budget, fv, RG_FH + (h >> 1) * 512 STARG);
            __builtin_amdgcn_s_setprio(0);
            bar();
            seen = __all(fv > (unsigned)t + 2 - V1A);
            tb2 = (unsigned)__builtin_amdgcn_s_memtime();
            ST(4);
            STC(5);
            STFLUSH(t);
        }
    };
    if (wave < 2) runv(std::integral_constant<int, 3>{});
    else if (wave < 4) runv(std::integral_constant<int, 2>{});
    else {
        const int w4 = wave - 4;
        const rsrc_t rd = mkrsrc(P.ws + PG0, PG_L);
        load_wo(wf, P.w[3], wave, h);
        PLACE();
        bar();
        bar();
        for (int t = -1; t < NSEG; ++t) {
            const unsigned slot = t & (NSLOT - 1);
            bar();
            ST(7);
#pragma unroll 1
            for (int n = V1A ? t + (t <= 0) : t + (t != 1) + (t == 0); n <= (V1A && t >= 0 ? t : t + 1) && n < NSEG; ++n) {
                const unsigned l16 = lane_id() * 16;
                const unsigned wl = L_W + wave * 10240 + l16, va = L_QX + (n & 1) * 12288 + l16;
                unsigned so = ((n & 7) << 22) + pgb(64 + (w4 >> 1), par) + ((w4 & 1) * 40 + (h >> 3)) * BLK + (h & 7) * 1024;
                asm volatile(ASM_VOX : [so] "+s"(so) : WREGS(wf), [l16] "v"(l16), [wl] "v"(wl), [va] "v"(va), [rd] "s"(rd) : "memory", "scc", "v200", "v201", "v202", "v203", "v204", "v205", "v206", "v207", "v208", "v209", "v210", "v211", "v212", "v213", "v214", "v215", "v216", "v217", "v218", "v219", "v220", "v221", "v222", "v223", "v224", "v225", "v226", "v227", "v228", "v229", "v230", "v231", "v232", "v233", "v234", "v235", "v236", "v237", "v238", "v239", "v240", "v241", "v242", "v243", "v244", "v245", "v246", "v247", "v248", "v249", "v250", "v251");
                if (V1A || n < 2) {
                    wait_vm<0>();
                    const unsigned l4 = lane_id() * 4;
                    if (l4 < 128) __builtin_amdgcn_raw_buffer_store_b32(n + 1, rr, l4, RG_FV + (w4 >> 1) * BLK + ((w4 & 1) * 32 + h) * 128, 16);
                }
            }
            if (t < 0) continue;
            ST(0);
            unsigned wl = L_W + wave * 10240 + lane_id() * 16;
            bf16x16 lw = lds_frag(wl, 1024);
            u32x32 cz;
            asm volatile("v_mov_b64 v[220:221], 0\n\tv_mov_b64 v[222:223], 0\n\tv_mov_b64 v[224:225], 0\n\tv_mov_b64 v[226:227], 0\n\tv_mov_b64 v[228:229], 0\n\tv_mov_b64 v[230:231], 0\n\tv_mov_b64 v[232:233], 0\n\tv_mov_b64 v[234:235], 0\n\tv_mov_b64 v[236:237], 0\n\tv_mov_b64 v[238:239], 0\n\tv_mov_b64 v[240:241], 0\n\tv_mov_b64 v[242:243], 0\n\tv_mov_b64 v[244:245], 0\n\tv_mov_b64 v[246:247], 0\n\tv_mov_b64 v[248:249], 0\n\tv_mov_b64 v[250:251], 0" : "={v[220:251]}"(cz));
            unsigned vo = ((wl >> 8) & 1) * (1 + (w4 & 1) * 2) * BLK + ((wl >> 9) & 1) * 1024 + (wl & 255);
            asm volatile("" : "+{v[212:219]}"(lw), "+{v[220:251]}"(cz), "+v"(wl), "+v"(vo));
            unsigned l16 = wl & 1023u;
            bar();
            wait_vm<0>();
            if (!V1A && l16 < 512 && t && t + 1 < NSEG) __builtin_amdgcn_raw_buffer_store_b32(t + 2, rr, l16 >> 2, RG_FV + (w4 >> 1) * BLK + ((w4 & 1) * 32 + h) * 128, 16);
            ST(1);
            const int sidx = (wl & 128u) ? 0xEEEE : 0x4444;
            unsigned so = RG_VM + slot * VM_SLOT + (unsigned)((w4 * 10 - (w4 & 1) + (h >> 4) * 2) * BLK + ((h >> 2) & 3) * 2048 + (h & 3) * 256);
            ST(3);
            ASM_ST_DECL;
            asm volatile(ASM_CRIT2 : [so] "+s"(so), "+{v[212:219]}"(lw), "+{v200}"(l16), "+{v[220:251]}"(cz) ASM_ST_OUT
                         : WREGS(wf), [wl] "v"(wl), [vo] "v"(vo), [sidx] "v"(sidx), [rr] "s"(rr)
                         : "memory", "scc", "v201", "v202", "v203", "v204", "v205", "v206", "v207", "v208", "v209", "v210", "v211");
            ASM_ST_SAVE(4);
            ST(5);
            STFLUSH(t);
        }
    }
}

#undef STROLE
#define STROLE 4
constexpr unsigned R_RED = 0;
constexpr unsigned R_PART = 16384;
__device__ __forceinline__ void r_role(const Params& P, int wave, int L, int r5, int x) {
    PLACE();
    const int jj = (r5 * 4 + cpy) ^ (r5 * 4 + x >= 6 ? 1 : 0);
    const int lane = lane_id(), l15 = lane & 15, l4 = lane >> 4;
    const unsigned l16 = lane * 16;
    const rsrc_t rd = mkrsrc(P.ws + PG0, PG_L);
    const int sidx = (l15 < 8) ? 0x4444 : 0xEEEE;
    unsigned offp[3];
    offp[0] = RG_XP + (cpy >> 1) * XP_PAIR + (cpy & 1) * BLK + (1 + (wave >> 2)) * 2 * BLK + (wave & 3) * 2048 + (PLAY ? (l4 >> 1) * 512 + (((l15 >> 2) & 1) + (l15 & 2)) * 128 + (l4 & 1) * 64 + (l15 >> 3) * 32 + (l15 & 1) * 16 : l16);
    offp[2] = (l15 < 8) ? RG_XP + (cpy >> 1) * XP_PAIR + (cpy & 1) * BLK + wave * 512 + l4 * 128 + l15 * 16 : OOB;
    const unsigned offv = RG_VM + ((jj >> 1) * 4 + (wave >> 2) * 2 + (jj & 1)) * BLK + (wave & 3) * 2048 + l16;
    const int j = jj * 2 + ((wave >> 1) & 1), fl = (wave & 1) * 32 + (lane & 31);
    const unsigned offo = RG_XM + (wave >> 2) * XM_PAIR + (lane >> 5) * BLK + (j & 31) * 256 + (j >> 5) * 2 * BLK + fl * 4;
    MARK(!L && jj == 0 && wave == 0, 5);
    {
        const int row = (lane >> 2) & 7, col = 16 * j + 4 * (lane & 3) + 2 * (wave & 1);
        __builtin_amdgcn_raw_buffer_store_b32(nosent(*(const unsigned*)(P.mem0 + row * DM + col)), rr, offo, 0, 16);
    }
    auto red_addr = [](int fl) { return R_RED + fl * 80 + (fl >> 4) * 16; };
    const unsigned rw0 = (lane >> 5) * 5248 + red_addr((((l15 >> 1) & 1) * 8 + 4 * (l4 & 1)) * 4 + (l15 >> 2)) + (l15 & 1) * 4 + (wave ^ (l15 >> 3)) * 8;
    const unsigned rrd = ((wave >> 1) & 1) * 5248 + red_addr(fl) + (lane >> 5) * 32;
    u32x32 vx;
    const unsigned fo = (lane & 3) * 128, fs = RG_FV + (jj / 10) * BLK + (((jj / 5) & 1) * 32 + wave * 4) * 128, vxo = pgb(64 + jj / 10, par) + (unsigned)((jj % 10) * 8 + (wave >> 1)) * BLK + (wave & 1) * 4096;
    const rsrc_t rp = mkrsrc(P.ws + PG0, PG_L);
    const unsigned xo = (l4 >> 1) * 256 + l15 * 16 + (l4 & 1) * 8, pw = R_PART + wave * 1024 + l16, pr = R_PART + (wave & 3) * 8192 + l16;
    const int fin = wave >> 2;
    asm volatile("" : "={v[128:159]}"(vx));
    auto vx_fetch = [&](int t) {
        for (int spins = 0;; ++spins) {
            const unsigned f = __builtin_amdgcn_raw_buffer_load_b32(rr, fo, fs, 16);
            if (__all(f > (unsigned)t)) break;
            __builtin_amdgcn_s_sleep(2);
            if (spin_fail(spins, P)) break;
        }
        const unsigned sv = ((t & 7) << 22) + vxo;
        asm volatile("buffer_load_dwordx4 v[128:131], %1, %2, %3 offen sc1\n\tbuffer_load_dwordx4 v[132:135], %1, %2, %3 offen offset:1024 sc1\n\t"
                     "buffer_load_dwordx4 v[136:139], %1, %2, %3 offen offset:2048 sc1\n\tbuffer_load_dwordx4 v[140:143], %1, %2, %3 offen offset:3072 sc1\n\t"
                     "buffer_load_dwordx4 v[144:147], %1, %2, %4 offen sc1\n\tbuffer_load_dwordx4 v[148:151], %1, %2, %4 offen offset:1024 sc1\n\t"
                     "buffer_load_dwordx4 v[152:155], %1, %2, %4 offen offset:2048 sc1\n\tbuffer_load_dwordx4 v[156:159], %1, %2, %4 offen offset:3072 sc1"
                     : "+{v[128:159]}"(vx) : "v"(l16), "s"(rp), "s"(sv), "s"(sv + 4 * BLK) : "memory");
    };
    vx_fetch(0);
    unsigned budget = 1u << 18, tpub = 0;
    STDECL;
    LDECL;
    for (int t = 0; t < NSEG; ++t) {
        const unsigned slot = t & (NSLOT - 1);
        ST(0);
        while ((unsigned)__builtin_amdgcn_s_memtime() - tpub < RTD) __builtin_amdgcn_s_sleep(1);
        u32x8 vm, pv;
        u32x4 pm;
        {
            unsigned a;
            ASM_ST_DECL;
            asm volatile(ASM_RCRIT : "=&{v[176:183]}"(vm), "=&{v[184:191]}"(pv), "=&{v[192:195]}"(pm), [a] "=&s"(a), [n] "+s"(budget) ASM_ST_OUT ASM_ST2_OUT ASM_ST3_OUT
                         : [rs] "s"(rr), [ov] "v"(offv), [sv] "s"(slot * VM_SLOT), [o0] "v"(offp[0]), [o2] "v"(offp[2]), [sp] "s"(slot * XP_SLOT),
                           "{v[128:159]}"(vx), [sidx] "v"(sidx), [rw] "v"(rw0), [rd] "v"(rrd), [oo] "v"(offo),
                           [so] "s"(((t + 1) & (NSLOT - 1)) * XM_SLOT), [pub] "s"(NSEG - 1 - t)
                         : "memory", "vcc", "scc", "v160", "v161", "v162", "v163", "v164", "v165", "v166", "v167", "v196", "v197", "v200", "v201", "v202", "v203", "v204", "v205", "v206", "v207");
            ASM_ST_SAVE(2); ASM_ST2_SAVE(1); ASM_ST3_SAVE(6);
        }
        ST(3);
        LA();
        MARK(!L && jj == 0 && wave == 0 && t < 4, 10 + t);
        PMARK(wave == 0, L * 7, t - 1 + L);
        tpub = (unsigned)__builtin_amdgcn_s_memtime();
        __builtin_amdgcn_s_sleep(RXDEL);
        unsigned early;
#pragma unroll 1
        for (int spins = 1023;; spins += 1024) {
            unsigned n;
            const unsigned sx = pgb(t, par) + blin(((wave >> 1) & 1) * 20480 + jj * 1024) + (wave & 1) * 512;
            const unsigned pxb = RG_PX + slot * PX_SLOT + wave * BLK, sv = (((t + 1) & 7) << 22) + vxo;
            ASM_ST_DECL;
            asm volatile(ASM_RX : "+{v[128:159]}"(vx), [n] "=&s"(n), [a] "=&s"(early) ASM_ST_OUT ASM_ST2_OUT
                         : "{v[176:183]}"(vm), "{v[184:191]}"(pv), "{v[192:195]}"(pm), [l16] "v"(l16), [rs] "s"(rr), [s0] "s"(pxb), [s1] "s"(pxb + 4096), [rd] "s"(rd), [rx] "s"(rd), [xo] "v"(xo),
                           [fo] "v"(fo), [fs] "s"(fs), [tn] "s"(t + 1), [sv0] "s"(sv), [sv1] "s"(sv + 4 * BLK), [rp] "s"(rp), [sx] "s"(PG_XR + L * PG_LX + sx), [sa] "s"(PG_XA + sx),
                           [pw] "v"(pw), [pr] "v"(pr), [fin] "s"(fin)
                         : "memory", "vcc", "scc", "v96", "v97", "v98", "v99", "v100", "v101", "v102", "v103", "v104", "v105", "v106", "v107", "v108", "v109", "v110", "v111", "v112", "v113", "v114", "v115", "v116", "v117", "v118", "v119", "v120", "v121", "v122", "v123", "v124", "v125", "v126", "v127", "v196", "v200", "v201", "v202", "v203", "v204", "v205", "v206", "v207", "v208", "v209", "v210", "v211", "v212", "v213", "v214", "v215", "v216", "v217", "v218", "v219", "v220", "v221", "v222", "v223", "v224", "v225", "v226", "v227", "v228", "v229", "v230", "v231", "v240", "v241", "v242", "v243", "v248", "v250", "v251");
            ASM_ST_SAVE(4); ASM_ST2_SAVE(5);
            if (__builtin_amdgcn_readfirstlane(n)) break;
            early = 0;
            if (spin_fail(spins, P)) { bar(); break; }
        }
        if (t + 1 < NSEG && !__builtin_amdgcn_readfirstlane(early)) vx_fetch(t + 1);
        PMARK(!L && wave == 7 && PMK == 1, 1, t - 1);
        st128(sent4(), rr, offv, slot * VM_SLOT); st128(sent4(), rr, offv + 1024, slot * VM_SLOT);
        __builtin_amdgcn_raw_buffer_store_b32(SENT, rr, offo, slot * XM_SLOT, 16);
        if (wave < 2 && t + AH < NSEG) st128(sent4(), rd, l16, PG_XA + pgb(t + AH, par) + blin(wave * 20480 + jj * 1024));
        ST(7);
        LB(t);
        STFLUSH(t);
    }
    LF();
}

#undef STROLE
#define STROLE 16
template <int WHICH>
__device__ __forceinline__ void w_role(const Params& P, int wave, int L, int cu, int x) {
    constexpr int KS = 20, N = (WHICH == 3) ? FF : DM, NU = 2 * NSEG;
    constexpr unsigned D_IN = WHICH == 1 ? PG_XA : WHICH == 2 ? PG_HS : PG_X1;
    constexpr unsigned D_OUT = WHICH == 1 ? PG_HS : WHICH == 2 ? PG_X1 : PG_H2;
    if (WHICH != 3 && cu * 8 + wave >= 20) {
        for (int u = 0; u <= NU; ++u) bar();
        return;
    }
    auto run = [&](auto Cc) {
    constexpr bool LO = decltype(Cc)::value;
    const int ln = lane_id(), l15 = ln & 15, l4 = ln >> 4;
    const unsigned l16 = ln * 16;
    const rsrc_t rd = mkrsrc(P.ws + PG0, PG_L);
    const int nt0 = (cu * 8 + wave) * 2;
    u32x32 w[5];
    {
        u32x4 f[2 * KS];
        load_wn<2, KS, N>(f, P.w[3 + WHICH], DM, wave, nt0, 0);
#pragma unroll
        for (int n = 0; n < 5; ++n) {
            u32x8 q[4];
#pragma unroll
            for (int i = 0; i < 4; ++i) q[i] = __builtin_shufflevector(f[n * 8 + 2 * i], f[n * 8 + 2 * i + 1], 0, 1, 2, 3, 4, 5, 6, 7);
            w[n] = cat32(q[0], q[1], q[2], q[3]);
        }
        asm volatile("" : "+{v[0:31]}"(w[0]), "+{v[32:63]}"(w[1]), "+{v[64:95]}"(w[2]), "+{v[96:127]}"(w[3]), "+{v[128:159]}"(w[4]));
    }
    PLACE();
    bar();
    const int ocol = nt0 * 16 + (l4 & 1) * 16 + (l4 >> 1) * 8;
    const unsigned o3 = (unsigned)((l4 & 1) * 512 + (l4 >> 1) * 256 + l15 * 16), ro = (l4 >> 1) * 256 + l15 * 16 + (l4 & 1) * 8;
    const int grp = nt0 / 10, sh = ((grp & 1) ? -1 : 1) * ((grp >> 1) + 1);
    if (WHICH == 1) {
        unsigned zv;
        asm volatile("v_mov_b32 %0, 0" : "=v"(zv));
        st128(((u32x4){zv, zv, zv, zv}), rd, l15 <= (grp >> 1) ? toff((grp & 1) ? S_ - 1 - l15 : l15, ocol, par) : OOB, PG_HS);
    }
    auto in0 = [&](int u) { return D_IN + pgb(u >> 1, par) + blin((u & 1) * 20480 + wave * 4096); };
    auto in1 = [&](int u) { return D_IN + pgb(u >> 1, par) + blin((u & 1) * 20480 + 16384) + wave * 1024; };
    u32x16 va;
    u32x4 vb;
    u32x8 cs, k8;
#define WT_W "{v[0:31]}"(w[0]), "{v[32:63]}"(w[1]), "{v[64:95]}"(w[2]), "{v[96:127]}"(w[3]), "{v[128:159]}"(w[4])
    asm volatile("v_mov_b32 v216, 0x3f803f80\n\tv_mov_b32 v217, v216\n\tv_mov_b32 v218, v216\n\tv_mov_b32 v219, v216\n\tv_mov_b32 v220, %[c0]\n\tv_mov_b32 v232, -1\n\tv_mov_b32 v236, -1\n\t"
                 "v_mov_b32 v240, -1\n\tv_mov_b32 v244, -1\n\tv_mov_b32 v248, -1\n\t" ASM_WCS
                 : "=&{v[196:203]}"(cs), "=&{v[216:223]}"(k8), "=&{v[232:247]}"(va), "=&{v[248:251]}"(vb) : WT_W, [c0] "s"(-2.0f * 0.7978845608028654f * 1.4426950408889634f));
    unsigned la = l16, lw = wave * 8192 + l16, sr = l4 * 8192 + ((l15 >> 2) * 16 + l15) * 16 + (l15 & 3) * 4, budget = 1u << 18;
    wait_vm<0>();
    STDECL;
    LDECL;
#pragma unroll 1
    for (int u = 0; u < NU; ++u) {
        const int mt = u & 1;
        const unsigned pg = pgb(u >> 1, par), c3 = D_OUT + blin(mt * (N * 32) + (nt0 >> 1) * 1024);
        unsigned oa = o3, ob = 0, so = pg + c3, sa = u + 2 * AH < NU ? pgb((u >> 1) + AH, par) + c3 : pg + PG_SS + 4096, a;
        if (WHICH == 1) {
            const int orow = u * 16 + l15 + sh;
            ob = toff(orow + AH * SEG, ocol, par);
            oa = (unsigned)orow < (unsigned)S_ ? (ob ^ 8192u) - (AH << 21) : OOB;
            if (orow + AH * SEG >= S_ + (sh < 0 ? sh : 0)) ob = OOB;
            so = sa = D_OUT;
        }
        if (mt) { ST(0); LB(u >> 1); } else { LA(); }
        ASM_ST_DECL;
#define WT_A WT_W, [la] "v"(la), [l16] "v"(l16), [oa] "v"(oa), [rs] "s"(rd), [so] "s"(so), [sa] "s"(sa)
#define WT_G [sr] "v"(sr), [kd] "s"(1.0f / DM), [ke] "s"(1e-5f), [k1] "s"(-2.0f * 0.7978845608028654f * 1.4426950408889634f * 0.044715f)
#define WT_1 WT_G, [ob] "v"(ob), [ss] "s"(pg + PG_SS + mt * 128), [sw] "s"(nt0)
#define WT_2 [ro] "v"(ro), [sx] "s"(pg + PG_XA + blin(mt * 20480 + nt0 * 512)), [ss] "s"(pg + PG_SS + mt * 128)
#define WT_C "memory", "vcc", "scc", "v160", "v161", "v162", "v163", "v164", "v165", "v166", "v167", "v168", "v169", "v170", "v171", "v172", "v173", "v174", "v175", "v176", "v177", "v178", "v179", "v180", "v181", "v182", "v183", "v184", "v185", "v186", "v187", "v188", "v189", "v190", "v191", "v192", "v193", "v194", "v195", "v204", "v205", "v206", "v207", "v208", "v209", "v210", "v211", "v212", "v213", "v214", "v215", "v252"
#define WT_LO(A, ...) asm volatile(A : "+{v[196:203]}"(cs), "+{v[216:223]}"(k8), "+{v[232:247]}"(va), "+{v[248:251]}"(vb), [n] "+s"(budget), [a] "=&s"(a) ASM_ST_OUT ASM_ST2_OUT ASM_ST3_OUT \
                              : WT_A, __VA_ARGS__, [lw] "v"(lw), [i0] "s"(in0(u)), [i1] "s"(in1(u)), [n0] "s"(in0(u + 1)), [n1] "s"(in1(u + 1)) : WT_C)
#define WT_HI(A, ...) asm volatile(A : "+{v[196:203]}"(cs), "+{v[216:223]}"(k8) ASM_ST_OUT ASM_ST3_OUT : WT_A, __VA_ARGS__ : WT_C)
        if constexpr (LO) { if constexpr (WHICH == 1) WT_LO(ASM_WT1_LO, WT_1); else if constexpr (WHICH == 2) WT_LO(ASM_WT2_LO, WT_2); else WT_LO(ASM_WT3_LO, WT_G); }
        else { if constexpr (WHICH == 1) WT_HI(ASM_WT1_HI, WT_1); else if constexpr (WHICH == 2) WT_HI(ASM_WT2_HI, WT_2); else WT_HI(ASM_WT3_HI, WT_G); }
        la ^= 32768u; lw ^= 32768u; sr ^= 32768u;
        PMARK(!L && mt == (WHICH != 1) && (PMK == 1 || WHICH == 3), PMK == 1 ? 1 + WHICH : 1, (u >> 1) - (WHICH == 1));
    }
    wait_vm<0>();
    if constexpr (LO) asm volatile("" :: "{v[232:247]}"(va), "{v[248:251]}"(vb));
    LF();
    };
    if (wave < 4) run(std::true_type{}); else run(std::false_type{});
}

__device__ __forceinline__ void w4_role(const Params& P, int wave, int L, int cu, int x) {
    constexpr unsigned W_FL = 122880;
    const int ln = lane_id(), l15 = ln & 15, l4 = ln >> 4;
    const unsigned l16 = ln * 16;
    const rsrc_t rd = mkrsrc(P.ws + PG0, PG_L);
    lds_st<unsigned>(W_FL + wave * 4, 0u);
    STDECL;
    if (wave < 6) {
        u32x32 w[5];
        {
            u32x4 g[40];
            load_wn<5, 8, DM>(g, P.w[7], FF, wave, cu * 5, wave * 8);
#pragma unroll
            for (int n = 0; n < 5; ++n) {
                u32x8 f[4];
#pragma unroll
                for (int i = 0; i < 4; ++i) f[i] = __builtin_shufflevector(g[n * 8 + 2 * i], g[n * 8 + 2 * i + 1], 0, 1, 2, 3, 4, 5, 6, 7);
                w[n] = cat32(f[0], f[1], f[2], f[3]);
            }
            asm volatile("" : "+{v[0:31]}"(w[0]), "+{v[32:63]}"(w[1]), "+{v[64:95]}"(w[2]), "+{v[96:127]}"(w[3]), "+{v[128:159]}"(w[4]));
        }
        PLACE();
        bar();
        unsigned budget = 1u << 18;
        u32x32 av;
        asm volatile("v_mov_b32 v160, -1\n\tv_mov_b32 v164, -1\n\tv_mov_b32 v168, -1\n\tv_mov_b32 v172, -1\n\tv_mov_b32 v176, -1\n\tv_mov_b32 v180, -1\n\tv_mov_b32 v184, -1\n\tv_mov_b32 v188, -1" : "={v[160:191]}"(av));
        for (int u = 0; u < 2 * NSEG; ++u) {
            if (u & 1) ST(0);
            for (int spins = 0;; ++spins) {
                asm volatile("" ::: "memory");
                if ((int)__builtin_amdgcn_readfirstlane(lds_ld<unsigned>(W_FL + 24 + (u & 1) * 4)) + 3 >= u) break;
                __builtin_amdgcn_s_sleep(2);
                if (spin_fail(spins, P)) break;
            }
            const unsigned gb = PG_H2 + pgb(u >> 1, par) + ((u & 1) * 6 + wave) * BLK, gn = PG_H2 + pgb((u + 1) >> 1, par) + ((~u & 1) * 6 + wave) * BLK;
            ASM_ST_DECL;
            asm volatile(ASM_W4_M : [n] "+s"(budget), "+{v[160:191]}"(av) ASM_ST2_OUT
                         : "{v[0:31]}"(w[0]), "{v[32:63]}"(w[1]), "{v[64:95]}"(w[2]), "{v[96:127]}"(w[3]), "{v[128:159]}"(w[4]),
                           [ld] "v"(l16 + wave * 1024 + (u & 3) * 30720), [l16] "v"(l16), [rs] "s"(rd), [s0] "s"(gb), [s1] "s"(gb + 4096), [n0] "s"(gn), [n1] "s"(gn + 4096), [un] "s"(u + 1), [fl] "v"(W_FL + wave * 4)
                         : "memory", "vcc", "scc", "v192", "v193", "v194", "v195", "v196", "v197", "v198", "v199", "v200", "v201", "v202", "v203", "v204", "v205", "v206", "v207", "v208", "v209", "v210", "v211", "v212");
            PMARK(!L && (u & 1) && PMK == 2, 2, u >> 1);
        }
        asm volatile("s_waitcnt vmcnt(0)" :: "{v[160:191]}"(av) : "memory");
    } else {
        const int mt = wave - 6;
        PLACE();
        bar();
        const unsigned c0 = mt * 20480 + cu * 2560, o8 = (l4 >> 1) * 256 + l15 * 16 + (l4 & 1) * 8;
        unsigned budget = 1u << 18;
        u32x16 ra;
        u32x4 rb;
        asm volatile("v_mov_b32 v164, -1" : "={v[164:179]}"(ra), "={v[180:183]}"(rb));
        __builtin_amdgcn_s_setprio(W4P);
#define W4F_OUT "+{v[164:179]}"(ra), "+{v[180:183]}"(rb), [n] "+s"(budget)
#define W4F_IN [o0] "v"(o8 + blin(c0)), [o1] "v"(o8 + blin(c0 + 512)), [o2] "v"(o8 + blin(c0 + 1024)), [o3] "v"(o8 + blin(c0 + 1536)), [o4] "v"(o8 + blin(c0 + 2048)), [rd] "s"(rd), \
               [c1] "s"(PG_X1 + pg), [ca] "s"(PG_XA + pg), [n1] "s"(PG_X1 + pn), [na] "s"(PG_XA + pn), [f0] "v"(W_FL + (ln & 3) * 4), [f1] "v"(W_FL + 16 + (ln & 1) * 4), [fd] "v"(W_FL + wave * 4), \
               [la] "v"((u & 3) * 30720 + l16), [u] "s"(u), [un] "s"(u + 1)
#define W4F_CLOB "memory", "vcc", "scc", "v64", "v65", "v66", "v67", "v68", "v69", "v70", "v71", "v72", "v73", "v74", "v75", "v76", "v77", "v78", "v79", "v80", "v81", "v82", "v83", "v84", "v85", "v86", "v87", "v88", "v89", "v90", "v91", "v92", "v93", "v94", "v95", "v96", "v97", "v98", "v99", "v100", "v101", "v102", "v103", "v104", "v105", "v106", "v107", "v108", "v109", "v110", "v111", "v112", "v113", "v114", "v115", "v116", "v117", "v118", "v119", "v120", "v121", "v122", "v123", "v124", "v125", "v126", "v127", "v128", "v129", "v130", "v131", "v132", "v133", "v134", "v135", "v136", "v137", "v138", "v139", "v140", "v141", "v142", "v143", "v144", "v145", "v146", "v147", "v148", "v149", "v150", "v151", "v152", "v153", "v154", "v155", "v156", "v157", "v158", "v159", "v160", "v161", "v162", "v163", "v184", "v185", "v186", "v187", "v188", "v189", "v190", "v191", "v192", "v193", "v194", "v195", "v196", "v197", "v198", "v199", "v200"
        if (L) {
            const rsrc_t ro = mkrsrc(P.out, ACT);
#pragma unroll 1
            for (int t = 0; t < NSEG; ++t) {
                const unsigned u = t * 2 + mt, pg = pgb(t, par), pn = pgb(t + 1, par);
                ASM_ST_DECL;
                asm volatile(ASM_W4F_1 : W4F_OUT ASM_ST2_OUT : W4F_IN, [pa] "v"(l15 * (DM * 2) + (l4 & 1) * 32 + (l4 >> 1) * 16), [pb] "v"(l15 * (DM * 2) + l4 * 8), [ro] "s"(ro), [so] "s"(u * 20480 + cu * 160) : W4F_CLOB);
            }
        } else {
            const rsrc_t rn = mkrsrc(P.ws + (par ^ 1) * 8192, RG_L);
#pragma unroll 1
            for (int t = 0; t < NSEG; ++t) {
                const unsigned u = t * 2 + mt, pg = pgb(t, par), pn = pgb(t + 1, par), sn = (pg ^ 8192) + PG_LX, st = RG_ST + (u >> 3) * BLK + (u & 7) * 1024;
                ASM_ST_DECL;
                asm volatile(ASM_W4F_0 : W4F_OUT ASM_ST_OUT ASM_ST2_OUT ASM_ST3_OUT
                             : W4F_IN, [pa] "v"(blin(c0 + (l4 & 1) * 512) + (l4 >> 1) * 256 + l15 * 16), [pb] "v"(blin(c0 + 1024 + (l4 & 1) * 512) + (l4 >> 1) * 256 + l15 * 16),
                               [so] "v"(ln < 16 ? ln * 8 : OOB), [si] "v"(ln * 8), [rr] "s"(rr), [sp] "s"(st + cu * 128), [sq] "s"(st), [xr] "s"(PG_XR + sn), [xn] "s"(PG_XN + sn),
                               [kd] "s"(1.0f / DM), [ke] "s"(1e-5f), [tn] "s"(t + 1), [fo] "v"(ln < 32 ? (ln >> 3) * 2048 + (ln & 7) * 16 : OOB), [rn] "s"(rn), [fs] "s"(RG_FX + (cu * 2 + mt) * 128) : W4F_CLOB);
            }
        }
        asm volatile("s_waitcnt vmcnt(0)" :: "{v[164:179]}"(ra), "{v[180:183]}"(rb) : "memory");
    }
}

__device__ __forceinline__ unsigned probe(char* ws, unsigned base, unsigned bit, int n) {
    const rsrc_t r = mkrsrc(ws, RG_SLOT);
    unsigned m0 = ~0u, m1 = ~0u;
    for (int i = 0; i < n; ++i) {
        const unsigned long long t0 = __builtin_amdgcn_s_memtime();
        __builtin_amdgcn_raw_buffer_store_b32(0u, r, 96 * BLK + base + (i & 1) * bit + ((i >> 2) & 1) * 4096 + (i >> 2) * 768, 0, 16);
        asm volatile("s_waitcnt vmcnt(0)" ::: "memory");
        const unsigned d = (unsigned)(__builtin_amdgcn_s_memtime() - t0);
        if (i & 1) m1 = d < m1 ? d : m1; else m0 = d < m0 ? d : m0;
    }
    return m0 > m1 ? 1u : 0u;
}
__device__ __forceinline__ void setup(const Params& P, int wave, int mt, int par0, int ph) {
    const bool xw = wave < 4 && (mt >= 2 * XS0) == (ph != 0);
    const int tid = threadIdx.x & 255, r15 = tid & 15, slot = tid >> 4;
    const rsrc_t rw = mkrsrc(P.ws, WS_END);
    u32x4 v[5];
    float s = 0.f, q = 0.f;
    if (xw) {
#pragma unroll
        for (int c = 0; c < 5; ++c) v[c] = *(const u32x4*)(P.x + (size_t)(mt * 16 + r15) * DM + (slot * 5 + c) * 8);
#pragma unroll
        for (int c = 0; c < 5; ++c)
#pragma unroll
            for (int e = 0; e < 4; ++e) s += lo2f(v[c][e]) + hi2f(v[c][e]);
        lds_st<float>((slot * 17 + r15) * 4, s);
    } else if (wave >= 4) {
        constexpr unsigned N0 = RG_ST / BLK, N1 = 4 * N0 + 16, N2 = PG_SS / BLK;
        const u32x4 s4 = sent4();
        for (unsigned k = mt; k < 2 * (N1 + 4 * N2); k += 128) {
            const unsigned i = k >> 1;
            const unsigned o = i < N1 ? (i < 4 * N0 ? (i / N0) * RG_SLOT + (i % N0) * BLK : (i - 3 * N0) * BLK) + (par0 ^ ph) * 8192 : PG0 + pgb((i - N1) / N2, par0 ^ ph) + ((i - N1) % N2) * BLK;
            st128(s4, rw, tid * 16, o + (k & 1) * 4096);
        }
        if (ph == 0 && mt >= 108) st128(~s4, rw, tid * 16, RG_FV + (mt - 108) * 4096);
    }
    bar();
    float mean = 0.f;
    if (xw) {
        s = 0.f;
#pragma unroll
        for (int k = 0; k < 16; ++k) s += lds_ld<float>((k * 17 + r15) * 4);
        mean = s * (1.0f / DM);
#pragma unroll
        for (int c = 0; c < 5; ++c)
#pragma unroll
            for (int e = 0; e < 4; ++e) { const float a = lo2f(v[c][e]) - mean, b = hi2f(v[c][e]) - mean; q += a * a + b * b; }
        lds_st<float>(2048 + (slot * 17 + r15) * 4, q);
    }
    bar();
    if (xw) {
        q = 0.f;
#pragma unroll
        for (int k = 0; k < 16; ++k) q += lds_ld<float>(2048 + (k * 17 + r15) * 4);
        const float m1 = rbf(mean), r = 1.0f / ln_den(q * (1.0f / DM));
#pragma unroll
        for (int c = 0; c < 5; ++c) {
            const unsigned o = toff(mt * 16 + r15, (slot * 5 + c) * 8, par0);
            st128(v[c], rw, o, PG0 + PG_XR);
            u32x4 n;
#pragma unroll
            for (int e = 0; e < 4; ++e) n[e] = pack2(rbf(lo2f(v[c][e]) - m1) * r, rbf(hi2f(v[c][e]) - m1) * r);
            st128(n, rw, o, PG0 + PG_XN);
        }
    }
    if (xw || wave >= 4) wait_vm<0>();
    bar();
    if (threadIdx.x < 8) __hip_atomic_fetch_add((unsigned*)(P.ws + FL0 + FL_IN + (ph * 8 + threadIdx.x) * 128), 1u, RLX_AGENT);
}

__global__ __launch_bounds__(512, 2) void mega_kernel(Params P) {
    const unsigned t0_ = (unsigned)__builtin_amdgcn_s_memrealtime();
    const int wave = __builtin_amdgcn_readfirstlane(threadIdx.x >> 6);
    u32x4 cpf;
    {
        unsigned long long pc;
        unsigned len;
        asm volatile("s_getpc_b64 %0\n\ts_mov_b32 %1, _Z8code_endv-_Z11mega_kernel6Params" : "=s"(pc), "=s"(len));
        cpf = __builtin_amdgcn_raw_buffer_load_b128(mkrsrc((const void*)(pc & ~255ull), len), (blockIdx.x >> 3) * 8192 + threadIdx.x * 16, 0, 0);
    }
    {
        const rsrc_t rf = mkrsrc(P.ws + FL0, FL_BYTES);
        if (blockIdx.x == 0) {
            unsigned h = 0;
            if (wave == 7) h = (probe(P.ws, 0, 8192, CAL1) ^ ((unsigned)__builtin_amdgcn_s_getreg((3 << 11) | 20) >> 2)) & 1u;
            if (wave == 0) {
                const unsigned l16 = lane_id() * 16;
                u32x4 z = {0u, 0u, 0u, 0u};
                asm volatile("" : "+v"(z));
#pragma unroll
                for (int k = 0; k < 4; ++k) st128(z, rf, k * 1024 + l16 < FL_HB ? k * 1024 + l16 : OOB, 0);
                wait_vm<0>();
                for (unsigned c = 1; c < 200000u; ++c) {
                    __builtin_amdgcn_raw_buffer_store_b32(c, rf, l16 < 128 ? l16 * 8 : OOB, FL_HB, 16);
                    unsigned n = __builtin_amdgcn_raw_buffer_load_b32(rf, (l16 & 127u) * 8, FL_TK, 16), sum = 0;
                    n = n < 32u ? n : 32u;
#pragma unroll
                    for (int k = 0; k < 8; ++k) sum += __builtin_amdgcn_readlane(n, k);
                    if (sum >= 255u) break;
                }
            }
            bar();
            if (wave == 7 && lane_id() < 16) __hip_atomic_fetch_add((unsigned*)(P.ws + FL0 + FL_IN + (threadIdx.x & 15) * 128), (2u + h) << 16, RLX_AGENT);
        } else if (wave == 0) {
            unsigned o;
            asm volatile("v_mov_b32 %0, 0" : "=v"(o));
            const unsigned v0 = __builtin_amdgcn_readfirstlane(__builtin_amdgcn_raw_buffer_load_b32(rf, o, FL_HB + (blockIdx.x & 7) * 128, 16));
            for (int spins = 0; spins < 400000; ++spins) {
                asm volatile("" ::: "memory");
                if (__builtin_amdgcn_readfirstlane(__builtin_amdgcn_raw_buffer_load_b32(rf, o, FL_HB + (blockIdx.x & 7) * 128, 16)) != v0) break;
                __builtin_amdgcn_s_sleep(1);
            }
            asm volatile("" ::: "memory");
        }
    }
    if (threadIdx.x == 0) {
        unsigned g = (unsigned)__builtin_amdgcn_s_getreg((3 << 11) | 20) & 7u, k;
        for (int n = 0; (k = __hip_atomic_fetch_add((unsigned*)(P.ws + FL0 + FL_TK + g * 128), 1u, RLX_AGENT)) >= 32u && n < 8; ++n) g = (g + 1) & 7u;
        lds_st<unsigned>(0, g << 5 | (k & 31u));
    }
    bar();
    const unsigned tk = __builtin_amdgcn_readfirstlane(lds_ld<unsigned>(0));
    asm volatile("" :: "v"(cpf));
    bar();
    const int g4 = (tk >> 5) & 3, k = tk & 31;
    const int L = tk >> 7, x = g4, r = k;
    if (L) {
        const int par0 = (init_wait(P, wave, 1, 0x20000u) >> 16) & 1;
        if (tk == 254 && wave == 0) {
            const unsigned xc = (unsigned)__builtin_amdgcn_s_getreg((3 << 11) | 20);
            const unsigned f = (probe(P.ws, ((par0 ^ (xc >> 2)) & 1) * 8192, BLK, CAL2) ^ 1u ^ xc) & 1u;
            if (threadIdx.x < 16) __hip_atomic_fetch_add((unsigned*)(P.ws + FL0 + FL_IN + threadIdx.x * 128), (2u + f) << 20, RLX_AGENT);
        }
        if (MPF && wave < 4 && k >= 4 && tk != 254) {
            const int wid = (k - 4) * 4 + g4;
            const rsrc_t rs = mkrsrc(wave == 0 ? P.w[0] : wave == 1 ? P.w[1] : wave == 2 ? P.w[2] : P.w[3], DM * HD * 2);
            const unsigned l16 = lane_id() * 16;
#pragma unroll 1
            for (int q = 0; q < MPF; ++q) {
                unsigned u = 7679 - (q * 112 + wid);
                if (wave == 1 && u < 6144) u = u < 3072 ? u + 3072 : u - 3072;
                asm volatile("buffer_load_dwordx4 v[252:255], %0, %1, %2 offen" :: "v"(l16), "s"(rs), "s"(u * 1024) : "memory", "v252", "v253", "v254", "v255");
            }
        }
#pragma unroll 1
        for (int ph = 0; ph < 2; ++ph) {
            setup(P, wave, (tk & 31) * 4 + g4, par0, ph);
            for (int k = ph * DLY_L; k < DLY_L; ++k) __builtin_amdgcn_s_sleep(32);
            wait_vm<0>();
        }
    }
    {
        const int i = (r - 26) * 4 + x;
        const int d = (r < 8 ? DLY_S : r < 21 ? 0 : i < 4 ? DLY_H : DLY_W + DLY_X * (i < 7 ? 0 : i < 10 ? 1 : i < 16 ? 2 : 3));
        for (int k = 0; k < d; ++k) __builtin_amdgcn_s_sleep(32);
    }
    if (r < 8) s_role(P, wave, L, x * 8 + r, x, t0_);
    else if (r < 16) v_role(P, wave ^ 4, L, x * 8 + r - 8, x);
    else if (r < 21) r_role(P, wave, L, r - 16, x);
    else {
        const int i = (r - 26) * 4 + x;
        if (i < 4) h_role(P, wave, L, r < 25 ? x * 4 + r - 21 : 16 + x * 2 + r - 25, x);
        else if (i < 7) w_role<1>(P, wave, L, i - 4, x);
        else if (i < 10) w_role<2>(P, wave, L, i - 7, x);
        else if (i < 16) w_role<3>(P, wave, L, i - 10, x);
        else w4_role(P, wave, L, i - 16, x);
    }
}

__global__ void code_end() {}

extern "C" void kernel_launch(void* const* d_in, const int* in_sizes, int n_in, void* d_out, int out_size, void* d_ws,
                              size_t ws_size, hipStream_t stream) {
    char* ws = (char*)d_ws;
    Params P;
    P.ws = ws; P.x = (const bf16_t*)d_in[0]; P.mem0 = (const bf16_t*)d_in[1]; P.out = (bf16_t*)d_out;
    for (int i = 0; i < 8; ++i) P.w[i] = (const bf16_t*)d_in[i < 4 ? 2 + i : 4 + i];
    (void)hipFuncSetAttribute((const void*)mega_kernel, hipFuncAttributeMaxDynamicSharedMemorySize, MEGA_LDS);
    mega_kernel<<<256, 512, MEGA_LDS, stream>>>(P);
}
```

```cpp
#define HM_L 1
#define HM_T 8
#define HM_C 0
#define HM_H 0
#define HM_B 30000u
#include <hip/hip_runtime.h>
#include <stdint.h>
#include <type_traits>

typedef unsigned short bf16_t;
using bf16x2 = __attribute__((ext_vector_type(2))) __bf16;
using bf16x8 = __attribute__((ext_vector_type(8))) __bf16;
using bf16x16 = __attribute__((ext_vector_type(16))) __bf16;
using f32x2 = __attribute__((ext_vector_type(2))) float;
using f32x4 = __attribute__((ext_vector_type(4))) float;
using u32x2 = __attribute__((ext_vector_type(2))) unsigned;
using u32x4 = __attribute__((ext_vector_type(4))) unsigned;
typedef __amdgpu_buffer_rsrc_t rsrc_t;

__device__ __forceinline__ unsigned pack2(float a, float b) {
    bf16x2 r = __builtin_convertvector((f32x2){a, b}, bf16x2);
    return __builtin_bit_cast(unsigned, r);
}
__device__ __forceinline__ float rbf(float f) { return __uint_as_float(pack2(f, 0.f) << 16); }
__device__ __forceinline__ float lo2f(unsigned u) { return __uint_as_float(u << 16); }
__device__ __forceinline__ float hi2f(unsigned u) { return __uint_as_float(u & 0xFFFF0000u); }
__device__ __forceinline__ rsrc_t mkrsrc(const void* p, unsigned bytes) {
    return __builtin_amdgcn_make_buffer_rsrc((void*)p, 0, bytes, 0x00020000);
}

constexpr int S_ = 2048, DM = 640, NH = 32, DH = 192, HD = NH * DH  , SEG = 32, NSEG = 64;
constexpr int FF = 1536;

#define XMAP
#define NCPY 4
constexpr unsigned NSLOT = 4, NCP = NCPY;
#define FL_TK_DEF
#define DLY_L 12
#define DLY_S 0
#define DLY_W 10
#define DLY_H 3
#define EWAKE "s_nop 0"
#define LWAKE "s_wakeup\n\t"
#define SLP4 (wave == 4 ? t : 0)
#define WPF 12
#define WPRI 1
#define WPRI2 0
#define RTD 3500
#define VTD 3900
#define HAH 100
#define WEAK 0
#define BAL 0
#define HSL 8
#define OAUX 2
#define W4P 3
#define STD 3800
#define CAL1 6
#define CAL2 12
#define DLY_X 2
#define NTM 10
#define MPF 0
#define MPW 0
#define CALN 12
#define RSL 9
#define VTRY 0
#define VSL 1
#define NRR 5
constexpr unsigned BLK = 16384, RG_SLOT = 4u << 20;
constexpr unsigned XP_PAIR = 6 * BLK, XP_SLOT = RG_SLOT;
constexpr unsigned XM_PAIR = 4 * BLK, XM_SLOT = RG_SLOT;
constexpr unsigned VM_SLOT = RG_SLOT;
constexpr unsigned PX_SLOT = RG_SLOT;
constexpr unsigned RG_XP = 0, RG_XM = 12 * BLK, RG_VM = 20 * BLK, RG_PX = 60 * BLK;
constexpr unsigned RG_ST = 68 * BLK, RG_FV = 84 * BLK, RG_FH = 86 * BLK, RG_FX = 88 * BLK, RG_L = NSLOT * RG_SLOT;
constexpr unsigned FL0 = 89 * BLK, FL_TK = 512, FL_IN = FL_TK + 8 * 128, FL_HB = FL_IN + 16 * 128, FL_BYTES = FL_HB + 8 * 128;
constexpr unsigned ACT = S_ * DM * 2;
#define PXROT 5
#define W4SL 5
#define RXDEL 1
#define VXD 32
#define VXAUX 16
constexpr unsigned PG0 = RG_L, PG_L = 80u << 21;
constexpr unsigned PG_XA = 0, PG_HS = 5 * BLK, PG_X1 = 10 * BLK, PG_H2 = 15 * BLK, PG_SS = 27 * BLK, PG_XR = 28 * BLK, PG_XN = 33 * BLK, PG_LX = 10 * BLK, PG_HV = 48 * BLK;
__device__ __forceinline__ unsigned pgb(unsigned p, int par) { return p << 21 | ((p ^ par) & 1) << 13; }
__device__ __forceinline__ constexpr unsigned blin(unsigned o) { return o + (o & ~8191u); }
constexpr int HT0 = 2;
constexpr int AH = 3;
constexpr unsigned WS_END = PG0 + PG_L;

#define SB() __builtin_amdgcn_sched_barrier(0)
#define LAS __attribute__((address_space(3)))
template <class T> __device__ __forceinline__ T lds_ld(unsigned a) { return *(const LAS T*)(a); }
template <class T> __device__ __forceinline__ void lds_st(unsigned a, T v) { *(LAS T*)(a) = v; }
constexpr unsigned OOB = 0x7FFFFF00u;
template <int N> __device__ __forceinline__ void wait_vm() { asm volatile("s_waitcnt vmcnt(%0)" :: "i"(N) : "memory"); }
__device__ __forceinline__ void bar() {
    asm volatile("s_waitcnt lgkmcnt(0)" ::: "memory");
    __builtin_amdgcn_s_barrier();
    asm volatile("" ::: "memory");
}
__device__ __forceinline__ int lane_id() {
    int ln;
    asm volatile("v_mbcnt_lo_u32_b32 %0, -1, 0\n\tv_mbcnt_hi_u32_b32 %0, -1, %0" : "=v"(ln));
    return ln;
}
__device__ __forceinline__ void swap16(unsigned& a, unsigned& b) { asm volatile("s_nop 3\n\tv_permlane16_swap_b32 %0, %1\n\ts_nop 1" : "+v"(a), "+v"(b)); }
__device__ __forceinline__ void swap32(unsigned& a, unsigned& b) { asm volatile("s_nop 3\n\tv_permlane32_swap_b32 %0, %1\n\ts_nop 1" : "+v"(a), "+v"(b)); }
__device__ __forceinline__ float sum_x16(float x) { unsigned a = __float_as_uint(x), b = a; swap16(a, b); return __uint_as_float(a) + __uint_as_float(b); }
__device__ __forceinline__ float sum_x32(float x) { unsigned a = __float_as_uint(x), b = a; swap32(a, b); return __uint_as_float(a) + __uint_as_float(b); }
__device__ __forceinline__ float max_x16(float x) { unsigned a = __float_as_uint(x), b = a; swap16(a, b); return __builtin_fmaxf(__uint_as_float(a), __uint_as_float(b)); }
__device__ __forceinline__ float max_x32(float x) { unsigned a = __float_as_uint(x), b = a; swap32(a, b); return __builtin_fmaxf(__uint_as_float(a), __uint_as_float(b)); }
__device__ __forceinline__ void sum2_rows(float& s, float& q) {
    unsigned a = __float_as_uint(s), b = a, c = __float_as_uint(q), d = c;
    asm volatile("s_nop 2\n\tv_permlane16_swap_b32 %0, %1\n\tv_permlane16_swap_b32 %2, %3\n\tv_add_f32 %0, %0, %1\n\tv_add_f32 %2, %2, %3\n\tv_mov_b32 %1, %0\n\tv_mov_b32 %3, %2\n\t"
                 "s_nop 1\n\tv_permlane32_swap_b32 %0, %1\n\tv_permlane32_swap_b32 %2, %3\n\tv_add_f32 %0, %0, %1\n\tv_add_f32 %2, %2, %3" : "+v"(a), "+v"(b), "+v"(c), "+v"(d));
    s = __uint_as_float(a); q = __uint_as_float(c);
}
__device__ __forceinline__ u32x4 merge16(u32x2 a, u32x2 b) {
    unsigned a0 = a[0], a1 = a[1], b0 = b[0], b1 = b[1];
    asm volatile("s_nop 3\n\tv_permlane16_swap_b32 %0, %2\n\tv_permlane16_swap_b32 %1, %3\n\ts_nop 1" : "+v"(a0), "+v"(a1), "+v"(b0), "+v"(b1));
    return (u32x4){a0, a1, b0, b1};
}
template <int AUXV>
__device__ __forceinline__ void st128_(u32x4 v, rsrc_t r, unsigned voff, unsigned soff) {
    __builtin_amdgcn_raw_buffer_store_b128(v, r, voff, soff, AUXV);
    asm volatile("s_nop 1" :: "v"(v) : "memory");
}
#define st128(V, R, VO_, SO_) st128_<16>(V, R, VO_, SO_)
#define LD128(R, VO_, SO_) __builtin_amdgcn_raw_buffer_load_b128(R, VO_, SO_, 16)
#define LD64(R, VO_, SO_) __builtin_amdgcn_raw_buffer_load_b64(R, VO_, SO_, 16)
#define BF8(X) __builtin_bit_cast(bf16x8, X)
__device__ __forceinline__ float gelu_tanh(float x) {
    const float c0 = -2.0f * 0.7978845608028654f * 1.4426950408889634f, c1 = c0 * 0.044715f;
    const float e = __builtin_amdgcn_exp2f(x * __builtin_fmaf(x * x, c1, c0));
    return x * __builtin_amdgcn_rcpf(1.0f + e);
}
#define MFMA(A_, B_, C_) C_ = __builtin_amdgcn_mfma_f32_16x16x32_bf16(A_, B_, C_, 0, 0, 0)
#define SMF(A_, B_, C_) C_ = __builtin_amdgcn_smfmac_f32_16x16x64_bf16(A_, B_, C_, sidx, 0, 0)
__device__ __forceinline__ float ln_den(float var) { return rbf(__builtin_amdgcn_sqrtf(rbf(rbf(var) + 1e-5f))); }
__device__ __forceinline__ float rcp(float x) { return __builtin_amdgcn_rcpf(x); }

struct Params {
    char* ws;
    const bf16_t* x;
    const bf16_t* mem0;
    const bf16_t* w[8];
    bf16_t* out;
};

#define RLX_AGENT __ATOMIC_RELAXED, __HIP_MEMORY_SCOPE_AGENT
#define SENT 0xFFFFFFFFu
constexpr int SPIN_CAP = 400000;
__device__ __forceinline__ bool spin_fail(int spins, const Params& P) {
    if ((spins & 1023) == 1023) {
        const rsrc_t rt = mkrsrc(P.ws + FL0, FL_BYTES);
        unsigned o;
        asm volatile("v_mov_b32 %0, 0x100" : "=v"(o));
        if (__builtin_amdgcn_readfirstlane(__builtin_amdgcn_raw_buffer_load_b32(rt, o, 0, 16)) != 0u) return true;
        if (spins > SPIN_CAP) { __builtin_amdgcn_raw_buffer_store_b32(o, rt, o, 0, 16); return true; }
    }
    return false;
}
__device__ __forceinline__ bool ok4(u32x4 v) { return v[0] != SENT && v[1] != SENT && v[2] != SENT && v[3] != SENT; }
__device__ __forceinline__ unsigned nosent(unsigned d) { return d < 0xFFFFFFFEu ? d : 0xFFFFFFFEu; }
__device__ __forceinline__ u32x4 nosent4(u32x4 v) { return (u32x4){nosent(v[0]), nosent(v[1]), nosent(v[2]), nosent(v[3])}; }
__device__ __forceinline__ unsigned umax3(unsigned a, unsigned b, unsigned c) { const unsigned m = a > b ? a : b; return m > c ? m : c; }
__device__ __forceinline__ unsigned umax4(unsigned m, u32x4 v) { return umax3(umax3(m, v[0], v[1]), v[2], v[3]); }
__device__ __forceinline__ u32x4 sent4() { unsigned sv; asm volatile("v_mov_b32 %0, -1" : "=v"(sv)); return (u32x4){sv, sv, sv, sv}; }
template <int N>
__device__ __forceinline__ void poll(rsrc_t rs, unsigned base, const unsigned (&off)[N], u32x4 (&v)[N], const Params& P) {
    for (int spins = 0;; ++spins) {
        unsigned m = 0;
#pragma unroll
        for (int k = 0; k < N; ++k) v[k] = LD128(rs, off[k], base);
#pragma unroll
        for (int k = 0; k < N; ++k) m = umax4(m, v[k]);
        if (__all(m != SENT)) return;
        asm volatile("" ::: "memory");
        if (spin_fail(spins, P)) return;
    }
}
__device__ __forceinline__ bf16x16 cat16(bf16x8 a, bf16x8 b) {
    return __builtin_shufflevector(a, b, 0, 1, 2, 3, 4, 5, 6, 7, 8, 9, 10, 11, 12, 13, 14, 15);
}
__device__ __forceinline__ bf16x8 half8(bf16x16 v, int h) {
    return h ? __builtin_shufflevector(v, v, 8, 9, 10, 11, 12, 13, 14, 15) : __builtin_shufflevector(v, v, 0, 1, 2, 3, 4, 5, 6, 7);
}
__device__ __forceinline__ bf16x16 lds_frag(unsigned a, unsigned step) { return cat16(lds_ld<bf16x8>(a), lds_ld<bf16x8>(a + step)); }
template <int I>
__device__ __forceinline__ void dmaq(const char* g, unsigned l16, unsigned dst) {
    asm volatile("s_mov_b32 m0, %2\n\ts_nop 0\n\tglobal_load_lds_dwordx4 %0, %1 offset:%3 sc1" :: "v"(l16), "s"(g), "s"(dst), "i"(I * 1024) : "memory");
}
__device__ __forceinline__ void dma4(const char* g, unsigned l16, unsigned dst) {
    asm volatile("s_mov_b32 m0, %2\n\ts_nop 0\n\tglobal_load_lds_dwordx4 %0, %1 sc1\n\tglobal_load_lds_dwordx4 %0, %1 offset:1024 sc1\n\t"
                 "global_load_lds_dwordx4 %0, %1 offset:2048 sc1\n\tglobal_load_lds_dwordx4 %0, %1 offset:3072 sc1" :: "v"(l16), "s"(g), "s"(dst) : "memory");
}
__device__ __forceinline__ unsigned toff(int row, int col, int par) {
    return pgb(row >> 5, par) + blin((unsigned)((row >> 4) & 1) * 20480u + (unsigned)(col >> 3) * 256u) + (unsigned)(row & 15) * 16u;
}

#define ASM_ST
#define ASM_ST_DECL
#define ASM_ST_OUT
#define ASM_ST_SAVE(k)
#define ASM_ST2
#define ASM_ST3
#define ASM_ST3_OUT
#define ASM_ST3_SAVE(k)
#define ASM_ST2_OUT
#define ASM_ST2_SAVE(k)
#define STDECL
#define ST(k)
#define STC(k)
#define STFLUSH(t)

#define MARK(c, k)
#define MARKX(c, k)
#define PMARK(c, st, seg)
#define LDECL
#define LA()
#define LB(t)
#define LF()
#define STROLE 3
constexpr unsigned L_MEM = 0;
constexpr unsigned L_Q = 10240;
constexpr unsigned L_KM = 13312;
constexpr unsigned L_QX = 16384;
constexpr unsigned L_KX = 28672;
constexpr unsigned L_XB = 40960;
constexpr unsigned L_XA = 49152;
constexpr unsigned L_W = 81920;
constexpr int MEGA_LDS = 163840;
constexpr int wi(int i, int j) { return (i >> 1) * 5 + ((i & 1) ? 2 + j : j - 1); }

#define RPUB2
#define WNT ""
#define PRIUP "s_setprio 2\n\t"
#define PRISM PRIUP
#define STR2(x) #x
#define STRX(x) STR2(x)
#define W4SLEEP "s_sleep " STRX(W4SL) "\n\t"
#define PLAY 0
#define V1A 0
#define ASM_WLOAD3 \
    "s_xor_b32 %[t], %[wx], 0\n\t" \
    "s_mul_i32 %[t], %[t], 0xc0000\n\t" \
    "buffer_load_dwordx4 v[200:203], %[o0], %[rs], %[t] offen" WNT "\n\t" \
    "buffer_load_dwordx4 v[0:3], %[o1], %[rs], %[t] offen" WNT "\n\t" \
    "buffer_load_dwordx4 v[8:11], %[o2], %[rs], %[t] offen" WNT "\n\t" \
    "s_add_u32 %[t], %[t], 0x18000\n\t" \
    "buffer_load_dwordx4 v[16:19], %[o0], %[rs], %[t] offen" WNT "\n\t" \
    "buffer_load_dwordx4 v[24:27], %[o1], %[rs], %[t] offen" WNT "\n\t" \
    "buffer_load_dwordx4 v[32:35], %[o2], %[rs], %[t] offen" WNT "\n\t" \
    "s_add_u32 %[t], %[t], 0x18000\n\t" \
    "buffer_load_dwordx4 v[208:211], %[o0], %[rs], %[t] offen" WNT "\n\t" \
    "buffer_load_dwordx4 v[40:43], %[o1], %[rs], %[t] offen" WNT "\n\t" \
    "buffer_load_dwordx4 v[48:51], %[o2], %[rs], %[t] offen" WNT "\n\t" \
    "s_add_u32 %[t], %[t], 0x18000\n\t" \
    "buffer_load_dwordx4 v[56:59], %[o0], %[rs], %[t] offen" WNT "\n\t" \
    "buffer_load_dwordx4 v[64:67], %[o1], %[rs], %[t] offen" WNT "\n\t" \
    "buffer_load_dwordx4 v[72:75], %[o2], %[rs], %[t] offen" WNT "\n\t" \
    "s_add_u32 %[t], %[t], 0x18000\n\t" \
    "buffer_load_dwordx4 v[204:207], %[o0], %[rs], %[t] offen" WNT "\n\t" \
    "buffer_load_dwordx4 v[4:7], %[o1], %[rs], %[t] offen" WNT "\n\t" \
    "buffer_load_dwordx4 v[12:15], %[o2], %[rs], %[t] offen" WNT "\n\t" \
    "s_add_u32 %[t], %[t], 0x18000\n\t" \
    "buffer_load_dwordx4 v[20:23], %[o0], %[rs], %[t] offen" WNT "\n\t" \
    "buffer_load_dwordx4 v[28:31], %[o1], %[rs], %[t] offen" WNT "\n\t" \
    "buffer_load_dwordx4 v[36:39], %[o2], %[rs], %[t] offen" WNT "\n\t" \
    "s_add_u32 %[t], %[t], 0x18000\n\t" \
    "buffer_load_dwordx4 v[212:215], %[o0], %[rs], %[t] offen" WNT "\n\t" \
    "buffer_load_dwordx4 v[44:47], %[o1], %[rs], %[t] offen" WNT "\n\t" \
    "buffer_load_dwordx4 v[52:55], %[o2], %[rs], %[t] offen" WNT "\n\t" \
    "s_add_u32 %[t], %[t], 0x18000\n\t" \
    "buffer_load_dwordx4 v[60:63], %[o0], %[rs], %[t] offen" WNT "\n\t" \
    "buffer_load_dwordx4 v[68:71], %[o1], %[rs], %[t] offen" WNT "\n\t" \
    "buffer_load_dwordx4 v[76:79], %[o2], %[rs], %[t] offen" WNT "\n\t" \
    "s_add_u32 %[t], %[t], 0x18000\n\t" \
    "s_waitcnt vmcnt(21)\n\t" \
    "ds_write_b128 %[s0], v[200:203] offset:0\n\t" \
    "ds_write_b128 %[s1], v[0:3] offset:0\n\t" \
    "ds_write_b128 %[s2], v[8:11] offset:0\n\t" \
    "s_waitcnt vmcnt(18)\n\t" \
    "ds_write_b128 %[s0], v[16:19] offset:3456\n\t" \
    "ds_write_b128 %[s1], v[24:27] offset:3456\n\t" \
    "ds_write_b128 %[s2], v[32:35] offset:3456\n\t" \
    "s_waitcnt vmcnt(15)\n\t" \
    "ds_write_b128 %[s0], v[208:211] offset:6912\n\t" \
    "ds_write_b128 %[s1], v[40:43] offset:6912\n\t" \
    "ds_write_b128 %[s2], v[48:51] offset:6912\n\t" \
    "s_waitcnt vmcnt(12)\n\t" \
    "ds_write_b128 %[s0], v[56:59] offset:10368\n\t" \
    "ds_write_b128 %[s1], v[64:67] offset:10368\n\t" \
    "ds_write_b128 %[s2], v[72:75] offset:10368\n\t" \
    "s_waitcnt lgkmcnt(0)\n\t" \
    "s_barrier \n\t" \
    "ds_read_b64_tr_b16 v[200:201], %[t0] offset:0\n\t" \
    "ds_read_b64_tr_b16 v[202:203], %[t0] offset:1664\n\t" \
    "ds_read_b64_tr_b16 v[0:1], %[t0] offset:128\n\t" \
    "ds_read_b64_tr_b16 v[2:3], %[t0] offset:1792\n\t" \
    "ds_read_b64_tr_b16 v[8:9], %[t0] offset:256\n\t" \
    "ds_read_b64_tr_b16 v[10:11], %[t0] offset:1920\n\t" \
    "ds_read_b64_tr_b16 v[16:17], %[t1] offset:0\n\t" \
    "ds_read_b64_tr_b16 v[18:19], %[t1] offset:1664\n\t" \
    "ds_read_b64_tr_b16 v[24:25], %[t1] offset:128\n\t" \
    "ds_read_b64_tr_b16 v[26:27], %[t1] offset:1792\n\t" \
    "ds_read_b64_tr_b16 v[32:33], %[t1] offset:256\n\t" \
    "ds_read_b64_tr_b16 v[34:35], %[t1] offset:1920\n\t" \
    "ds_read_b64_tr_b16 v[208:209], %[t2] offset:0\n\t" \
    "ds_read_b64_tr_b16 v[210:211], %[t2] offset:1664\n\t" \
    "ds_read_b64_tr_b16 v[40:41], %[t2] offset:128\n\t" \
    "ds_read_b64_tr_b16 v[42:43], %[t2] offset:1792\n\t" \
    "ds_read_b64_tr_b16 v[48:49], %[t2] offset:256\n\t" \
    "ds_read_b64_tr_b16 v[50:51], %[t2] offset:1920\n\t" \
    "ds_read_b64_tr_b16 v[56:57], %[t3] offset:0\n\t" \
    "ds_read_b64_tr_b16 v[58:59], %[t3] offset:1664\n\t" \
    "ds_read_b64_tr_b16 v[64:65], %[t3] offset:128\n\t" \
    "ds_read_b64_tr_b16 v[66:67], %[t3] offset:1792\n\t" \
    "ds_read_b64_tr_b16 v[72:73], %[t3] offset:256\n\t" \
    "ds_read_b64_tr_b16 v[74:75], %[t3] offset:1920\n\t" \
    "s_xor_b32 %[t], %[wx], 4\n\t" \
    "s_mul_i32 %[t], %[t], 0xc0000\n\t" \
    "buffer_load_dwordx4 v[216:219], %[o0], %[rs], %[t] offen" WNT "\n\t" \
    "buffer_load_dwordx4 v[80:83], %[o1], %[rs], %[t] offen" WNT "\n\t" \
    "buffer_load_dwordx4 v[88:91], %[o2], %[rs], %[t] offen" WNT "\n\t" \
    "s_add_u32 %[t], %[t], 0x18000\n\t" \
    "buffer_load_dwordx4 v[96:99], %[o0], %[rs], %[t] offen" WNT "\n\t" \
    "buffer_load_dwordx4 v[104:107], %[o1], %[rs], %[t] offen" WNT "\n\t" \
    "buffer_load_dwordx4 v[112:115], %[o2], %[rs], %[t] offen" WNT "\n\t" \
    "s_add_u32 %[t], %[t], 0x18000\n\t" \
    "buffer_load_dwordx4 v[224:227], %[o0], %[rs], %[t] offen" WNT "\n\t" \
    "buffer_load_dwordx4 v[120:123], %[o1], %[rs], %[t] offen" WNT "\n\t" \
    "buffer_load_dwordx4 v[128:131], %[o2], %[rs], %[t] offen" WNT "\n\t" \
    "s_add_u32 %[t], %[t], 0x18000\n\t" \
    "buffer_load_dwordx4 v[136:139], %[o0], %[rs], %[t] offen" WNT "\n\t" \
    "buffer_load_dwordx4 v[144:147], %[o1], %[rs], %[t] offen" WNT "\n\t" \
    "buffer_load_dwordx4 v[152:155], %[o2], %[rs], %[t] offen" WNT "\n\t" \
    "s_add_u32 %[t], %[t], 0x18000\n\t" \
    "s_waitcnt lgkmcnt(0)\n\t" \
    "s_barrier \n\t" \
    "s_waitcnt vmcnt(21)\n\t" \
    "ds_write_b128 %[s0], v[204:207] offset:0\n\t" \
    "ds_write_b128 %[s1], v[4:7] offset:0\n\t" \
    "ds_write_b128 %[s2], v[12:15] offset:0\n\t" \
    "s_waitcnt vmcnt(18)\n\t" \
    "ds_write_b128 %[s0], v[20:23] offset:3456\n\t" \
    "ds_write_b128 %[s1], v[28:31] offset:3456\n\t" \
    "ds_write_b128 %[s2], v[36:39] offset:3456\n\t" \
    "s_waitcnt vmcnt(15)\n\t" \
    "ds_write_b128 %[s0], v[212:215] offset:6912\n\t" \
    "ds_write_b128 %[s1], v[44:47] offset:6912\n\t" \
    "ds_write_b128 %[s2], v[52:55] offset:6912\n\t" \
    "s_waitcnt vmcnt(12)\n\t" \
    "ds_write_b128 %[s0], v[60:63] offset:10368\n\t" \
    "ds_write_b128 %[s1], v[68:71] offset:10368\n\t" \
    "ds_write_b128 %[s2], v[76:79] offset:10368\n\t" \
    "s_waitcnt lgkmcnt(0)\n\t" \
    "s_barrier \n\t" \
    "ds_read_b64_tr_b16 v[204:205], %[t0] offset:0\n\t" \
    "ds_read_b64_tr_b16 v[206:207], %[t0] offset:1664\n\t" \
    "ds_read_b64_tr_b16 v[4:5], %[t0] offset:128\n\t" \
    "ds_read_b64_tr_b16 v[6:7], %[t0] offset:1792\n\t" \
    "ds_read_b64_tr_b16 v[12:13], %[t0] offset:256\n\t" \
    "ds_read_b64_tr_b16 v[14:15], %[t0] offset:1920\n\t" \
    "ds_read_b64_tr_b16 v[20:21], %[t1] offset:0\n\t" \
    "ds_read_b64_tr_b16 v[22:23], %[t1] offset:1664\n\t" \
    "ds_read_b64_tr_b16 v[28:29], %[t1] offset:128\n\t" \
    "ds_read_b64_tr_b16 v[30:31], %[t1] offset:1792\n\t" \
    "ds_read_b64_tr_b16 v[36:37], %[t1] offset:256\n\t" \
    "ds_read_b64_tr_b16 v[38:39], %[t1] offset:1920\n\t" \
    "ds_read_b64_tr_b16 v[212:213], %[t2] offset:0\n\t" \
    "ds_read_b64_tr_b16 v[214:215], %[t2] offset:1664\n\t" \
    "ds_read_b64_tr_b16 v[44:45], %[t2] offset:128\n\t" \
    "ds_read_b64_tr_b16 v[46:47], %[t2] offset:1792\n\t" \
    "ds_read_b64_tr_b16 v[52:53], %[t2] offset:256\n\t" \
    "ds_read_b64_tr_b16 v[54:55], %[t2] offset:1920\n\t" \
    "ds_read_b64_tr_b16 v[60:61], %[t3] offset:0\n\t" \
    "ds_read_b64_tr_b16 v[62:63], %[t3] offset:1664\n\t" \
    "ds_read_b64_tr_b16 v[68:69], %[t3] offset:128\n\t" \
    "ds_read_b64_tr_b16 v[70:71], %[t3] offset:1792\n\t" \
    "ds_read_b64_tr_b16 v[76:77], %[t3] offset:256\n\t" \
    "ds_read_b64_tr_b16 v[78:79], %[t3] offset:1920\n\t" \
    "buffer_load_dwordx4 v[220:223], %[o0], %[rs], %[t] offen" WNT "\n\t" \
    "buffer_load_dwordx4 v[84:87], %[o1], %[rs], %[t] offen" WNT "\n\t" \
    "buffer_load_dwordx4 v[92:95], %[o2], %[rs], %[t] offen" WNT "\n\t" \
    "s_add_u32 %[t], %[t], 0x18000\n\t" \
    "buffer_load_dwordx4 v[100:103], %[o0], %[rs], %[t] offen" WNT "\n\t" \
    "buffer_load_dwordx4 v[108:111], %[o1], %[rs], %[t] offen" WNT "\n\t" \
    "buffer_load_dwordx4 v[116:119], %[o2], %[rs], %[t] offen" WNT "\n\t" \
    "s_add_u32 %[t], %[t], 0x18000\n\t" \
    "buffer_load_dwordx4 v[228:231], %[o0], %[rs], %[t] offen" WNT "\n\t" \
    "buffer_load_dwordx4 v[124:127], %[o1], %[rs], %[t] offen" WNT "\n\t" \
    "buffer_load_dwordx4 v[132:135], %[o2], %[rs], %[t] offen" WNT "\n\t" \
    "s_add_u32 %[t], %[t], 0x18000\n\t" \
    "buffer_load_dwordx4 v[140:143], %[o0], %[rs], %[t] offen" WNT "\n\t" \
    "buffer_load_dwordx4 v[148:151], %[o1], %[rs], %[t] offen" WNT "\n\t" \
    "buffer_load_dwordx4 v[156:159], %[o2], %[rs], %[t] offen" WNT "\n\t" \
    "s_add_u32 %[t], %[t], 0x18000\n\t" \
    "s_waitcnt lgkmcnt(0)\n\t" \
    "s_barrier \n\t" \
    "s_waitcnt vmcnt(21)\n\t" \
    "ds_write_b128 %[s0], v[216:219] offset:0\n\t" \
    "ds_write_b128 %[s1], v[80:83] offset:0\n\t" \
    "ds_write_b128 %[s2], v[88:91] offset:0\n\t" \
    "s_waitcnt vmcnt(18)\n\t" \
    "ds_write_b128 %[s0], v[96:99] offset:3456\n\t" \
    "ds_write_b128 %[s1], v[104:107] offset:3456\n\t" \
    "ds_write_b128 %[s2], v[112:115] offset:3456\n\t" \
    "s_waitcnt vmcnt(15)\n\t" \
    "ds_write_b128 %[s0], v[224:227] offset:6912\n\t" \
    "ds_write_b128 %[s1], v[120:123] offset:6912\n\t" \
    "ds_write_b128 %[s2], v[128:131] offset:6912\n\t" \
    "s_waitcnt vmcnt(12)\n\t" \
    "ds_write_b128 %[s0], v[136:139] offset:10368\n\t" \
    "ds_write_b128 %[s1], v[144:147] offset:10368\n\t" \
    "ds_write_b128 %[s2], v[152:155] offset:10368\n\t" \
    "s_waitcnt lgkmcnt(0)\n\t" \
    "s_barrier \n\t" \
    "ds_read_b64_tr_b16 v[216:217], %[t0] offset:0\n\t" \
    "ds_read_b64_tr_b16 v[218:219], %[t0] offset:1664\n\t" \
    "ds_read_b64_tr_b16 v[80:81], %[t0] offset:128\n\t" \
    "ds_read_b64_tr_b16 v[82:83], %[t0] offset:1792\n\t" \
    "ds_read_b64_tr_b16 v[88:89], %[t0] offset:256\n\t" \
    "ds_read_b64_tr_b16 v[90:91], %[t0] offset:1920\n\t" \
    "ds_read_b64_tr_b16 v[96:97], %[t1] offset:0\n\t" \
    "ds_read_b64_tr_b16 v[98:99], %[t1] offset:1664\n\t" \
    "ds_read_b64_tr_b16 v[104:105], %[t1] offset:128\n\t" \
    "ds_read_b64_tr_b16 v[106:107], %[t1] offset:1792\n\t" \
    "ds_read_b64_tr_b16 v[112:113], %[t1] offset:256\n\t" \
    "ds_read_b64_tr_b16 v[114:115], %[t1] offset:1920\n\t" \
    "ds_read_b64_tr_b16 v[224:225], %[t2] offset:0\n\t" \
    "ds_read_b64_tr_b16 v[226:227], %[t2] offset:1664\n\t" \
    "ds_read_b64_tr_b16 v[120:121], %[t2] offset:128\n\t" \
    "ds_read_b64_tr_b16 v[122:123], %[t2] offset:1792\n\t" \
    "ds_read_b64_tr_b16 v[128:129], %[t2] offset:256\n\t" \
    "ds_read_b64_tr_b16 v[130:131], %[t2] offset:1920\n\t" \
    "ds_read_b64_tr_b16 v[136:137], %[t3] offset:0\n\t" \
    "ds_read_b64_tr_b16 v[138:139], %[t3] offset:1664\n\t" \
    "ds_read_b64_tr_b16 v[144:145], %[t3] offset:128\n\t" \
    "ds_read_b64_tr_b16 v[146:147], %[t3] offset:1792\n\t" \
    "ds_read_b64_tr_b16 v[152:153], %[t3] offset:256\n\t" \
    "ds_read_b64_tr_b16 v[154:155], %[t3] offset:1920\n\t" \
    "s_and_b32 %[t], %[wx], 3\n\t" \
    "s_mul_i32 %[t], %[t], 0x60000\n\t" \
    "s_add_u32 %[t], %[t], 0x600000\n\t" \
    "buffer_load_dwordx4 v[232:235], %[o0], %[rs], %[t] offen" WNT "\n\t" \
    "buffer_load_dwordx4 v[160:163], %[o1], %[rs], %[t] offen" WNT "\n\t" \
    "buffer_load_dwordx4 v[168:171], %[o2], %[rs], %[t] offen" WNT "\n\t" \
    "s_add_u32 %[t], %[t], 0x18000\n\t" \
    "buffer_load_dwordx4 v[236:239], %[o0], %[rs], %[t] offen" WNT "\n\t" \
    "buffer_load_dwordx4 v[164:167], %[o1], %[rs], %[t] offen" WNT "\n\t" \
    "buffer_load_dwordx4 v[172:175], %[o2], %[rs], %[t] offen" WNT "\n\t" \
    "s_add_u32 %[t], %[t], 0x18000\n\t" \
    "buffer_load_dwordx4 v[176:179], %[o0], %[rs], %[t] offen" WNT "\n\t" \
    "buffer_load_dwordx4 v[184:187], %[o1], %[rs], %[t] offen" WNT "\n\t" \
    "buffer_load_dwordx4 v[192:195], %[o2], %[rs], %[t] offen" WNT "\n\t" \
    "s_add_u32 %[t], %[t], 0x18000\n\t" \
    "buffer_load_dwordx4 v[180:183], %[o0], %[rs], %[t] offen" WNT "\n\t" \
    "buffer_load_dwordx4 v[188:191], %[o1], %[rs], %[t] offen" WNT "\n\t" \
    "buffer_load_dwordx4 v[196:199], %[o2], %[rs], %[t] offen" WNT "\n\t" \
    "s_add_u32 %[t], %[t], 0x18000\n\t" \
    "s_waitcnt lgkmcnt(0)\n\t" \
    "s_barrier \n\t" \
    "s_waitcnt vmcnt(21)\n\t" \
    "ds_write_b128 %[s0], v[220:223] offset:0\n\t" \
    "ds_write_b128 %[s1], v[84:87] offset:0\n\t" \
    "ds_write_b128 %[s2], v[92:95] offset:0\n\t" \
    "s_waitcnt vmcnt(18)\n\t" \
    "ds_write_b128 %[s0], v[100:103] offset:3456\n\t" \
    "ds_write_b128 %[s1], v[108:111] offset:3456\n\t" \
    "ds_write_b128 %[s2], v[116:119] offset:3456\n\t" \
    "s_waitcnt vmcnt(15)\n\t" \
    "ds_write_b128 %[s0], v[228:231] offset:6912\n\t" \
    "ds_write_b128 %[s1], v[124:127] offset:6912\n\t" \
    "ds_write_b128 %[s2], v[132:135] offset:6912\n\t" \
    "s_waitcnt vmcnt(12)\n\t" \
    "ds_write_b128 %[s0], v[140:143] offset:10368\n\t" \
    "ds_write_b128 %[s1], v[148:151] offset:10368\n\t" \
    "ds_write_b128 %[s2], v[156:159] offset:10368\n\t" \
    "s_waitcnt lgkmcnt(0)\n\t" \
    "s_barrier \n\t" \
    "ds_read_b64_tr_b16 v[220:221], %[t0] offset:0\n\t" \
    "ds_read_b64_tr_b16 v[222:223], %[t0] offset:1664\n\t" \
    "ds_read_b64_tr_b16 v[84:85], %[t0] offset:128\n\t" \
    "ds_read_b64_tr_b16 v[86:87], %[t0] offset:1792\n\t" \
    "ds_read_b64_tr_b16 v[92:93], %[t0] offset:256\n\t" \
    "ds_read_b64_tr_b16 v[94:95], %[t0] offset:1920\n\t" \
    "ds_read_b64_tr_b16 v[100:101], %[t1] offset:0\n\t" \
    "ds_read_b64_tr_b16 v[102:103], %[t1] offset:1664\n\t" \
    "ds_read_b64_tr_b16 v[108:109], %[t1] offset:128\n\t" \
    "ds_read_b64_tr_b16 v[110:111], %[t1] offset:1792\n\t" \
    "ds_read_b64_tr_b16 v[116:117], %[t1] offset:256\n\t" \
    "ds_read_b64_tr_b16 v[118:119], %[t1] offset:1920\n\t" \
    "ds_read_b64_tr_b16 v[228:229], %[t2] offset:0\n\t" \
    "ds_read_b64_tr_b16 v[230:231], %[t2] offset:1664\n\t" \
    "ds_read_b64_tr_b16 v[124:125], %[t2] offset:128\n\t" \
    "ds_read_b64_tr_b16 v[126:127], %[t2] offset:1792\n\t" \
    "ds_read_b64_tr_b16 v[132:133], %[t2] offset:256\n\t" \
    "ds_read_b64_tr_b16 v[134:135], %[t2] offset:1920\n\t" \
    "ds_read_b64_tr_b16 v[140:141], %[t3] offset:0\n\t" \
    "ds_read_b64_tr_b16 v[142:143], %[t3] offset:1664\n\t" \
    "ds_read_b64_tr_b16 v[148:149], %[t3] offset:128\n\t" \
    "ds_read_b64_tr_b16 v[150:151], %[t3] offset:1792\n\t" \
    "ds_read_b64_tr_b16 v[156:157], %[t3] offset:256\n\t" \
    "ds_read_b64_tr_b16 v[158:159], %[t3] offset:1920\n\t" \
    "s_waitcnt lgkmcnt(0)\n\t" \
    "s_barrier \n\t" \
    "s_waitcnt vmcnt(9)\n\t" \
    "ds_write_b128 %[s0], v[232:235] offset:0\n\t" \
    "ds_write_b128 %[s1], v[160:163] offset:0\n\t" \
    "ds_write_b128 %[s2], v[168:171] offset:0\n\t" \
    "s_waitcnt vmcnt(6)\n\t" \
    "ds_write_b128 %[s0], v[236:239] offset:3456\n\t" \
    "ds_write_b128 %[s1], v[164:167] offset:3456\n\t" \
    "ds_write_b128 %[s2], v[172:175] offset:3456\n\t" \
    "s_waitcnt vmcnt(3)\n\t" \
    "ds_write_b128 %[s0], v[176:179] offset:6912\n\t" \
    "ds_write_b128 %[s1], v[184:187] offset:6912\n\t" \
    "ds_write_b128 %[s2], v[192:195] offset:6912\n\t" \
    "s_waitcnt vmcnt(0)\n\t" \
    "ds_write_b128 %[s0], v[180:183] offset:10368\n\t" \
    "ds_write_b128 %[s1], v[188:191] offset:10368\n\t" \
    "ds_write_b128 %[s2], v[196:199] offset:10368\n\t" \
    "s_waitcnt lgkmcnt(0)\n\t" \
    "s_barrier \n\t" \
    "ds_read_b64_tr_b16 v[232:233], %[t4] offset:0\n\t" \
    "ds_read_b64_tr_b16 v[234:235], %[t4] offset:1664\n\t" \
    "ds_read_b64_tr_b16 v[160:161], %[t4] offset:128\n\t" \
    "ds_read_b64_tr_b16 v[162:163], %[t4] offset:1792\n\t" \
    "ds_read_b64_tr_b16 v[168:169], %[t4] offset:256\n\t" \
    "ds_read_b64_tr_b16 v[170:171], %[t4] offset:1920\n\t" \
    "ds_read_b64_tr_b16 v[236:237], %[t4] offset:13824\n\t" \
    "ds_read_b64_tr_b16 v[238:239], %[t4] offset:15488\n\t" \
    "ds_read_b64_tr_b16 v[164:165], %[t4] offset:13952\n\t" \
    "ds_read_b64_tr_b16 v[166:167], %[t4] offset:15616\n\t" \
    "ds_read_b64_tr_b16 v[172:173], %[t4] offset:14080\n\t" \
    "ds_read_b64_tr_b16 v[174:175], %[t4] offset:15744\n\t" \
    "ds_read_b64_tr_b16 v[176:177], %[t4] offset:27648\n\t" \
    "ds_read_b64_tr_b16 v[178:179], %[t4] offset:29312\n\t" \
    "ds_read_b64_tr_b16 v[184:185], %[t4] offset:27776\n\t" \
    "ds_read_b64_tr_b16 v[186:187], %[t4] offset:29440\n\t" \
    "ds_read_b64_tr_b16 v[192:193], %[t4] offset:27904\n\t" \
    "ds_read_b64_tr_b16 v[194:195], %[t4] offset:29568\n\t" \
    "ds_read_b64_tr_b16 v[180:181], %[t4] offset:41472\n\t" \
    "ds_read_b64_tr_b16 v[182:183], %[t4] offset:43136\n\t" \
    "ds_read_b64_tr_b16 v[188:189], %[t4] offset:41600\n\t" \
    "ds_read_b64_tr_b16 v[190:191], %[t4] offset:43264\n\t" \
    "ds_read_b64_tr_b16 v[196:197], %[t4] offset:41728\n\t" \
    "ds_read_b64_tr_b16 v[198:199], %[t4] offset:43392\n\t" \
    "s_waitcnt lgkmcnt(0)\n\t" \
    "s_barrier \n\t" \
    "ds_write_b128 %[wl], v[200:203] offset:0\n\t" \
    "ds_write_b128 %[wl], v[204:207] offset:1024\n\t" \
    "ds_write_b128 %[wl], v[208:211] offset:2048\n\t" \
    "ds_write_b128 %[wl], v[212:215] offset:3072\n\t" \
    "ds_write_b128 %[wl], v[216:219] offset:4096\n\t" \
    "ds_write_b128 %[wl], v[220:223] offset:5120\n\t" \
    "ds_write_b128 %[wl], v[224:227] offset:6144\n\t" \
    "ds_write_b128 %[wl], v[228:231] offset:7168\n\t" \
    "ds_write_b128 %[wl], v[232:235] offset:8192\n\t" \
    "ds_write_b128 %[wl], v[236:239] offset:9216\n\t" \
    "s_waitcnt lgkmcnt(0)\n\t"
#define ASM_WLOADO \
    "s_mov_b32 %[t], 0x0\n\t" \
    "buffer_load_dwordx4 v[200:203], %[o0], %[rs], %[t] offen" WNT "\n\t" \
    "buffer_load_dwordx4 v[16:19], %[o1], %[rs], %[t] offen" WNT "\n\t" \
    "buffer_load_dwordx4 v[208:211], %[o2], %[rs], %[t] offen" WNT "\n\t" \
    "buffer_load_dwordx4 v[56:59], %[o3], %[rs], %[t] offen" WNT "\n\t" \
    "buffer_load_dwordx4 v[216:219], %[o4], %[rs], %[t] offen" WNT "\n\t" \
    "s_mov_b32 %[t], 0xa000\n\t" \
    "buffer_load_dwordx4 v[96:99], %[o0], %[rs], %[t] offen" WNT "\n\t" \
    "buffer_load_dwordx4 v[224:227], %[o1], %[rs], %[t] offen" WNT "\n\t" \
    "buffer_load_dwordx4 v[136:139], %[o2], %[rs], %[t] offen" WNT "\n\t" \
    "buffer_load_dwordx4 v[232:235], %[o3], %[rs], %[t] offen" WNT "\n\t" \
    "buffer_load_dwordx4 v[176:179], %[o4], %[rs], %[t] offen" WNT "\n\t" \
    "s_mov_b32 %[t], 0x14000\n\t" \
    "buffer_load_dwordx4 v[204:207], %[o0], %[rs], %[t] offen" WNT "\n\t" \
    "buffer_load_dwordx4 v[20:23], %[o1], %[rs], %[t] offen" WNT "\n\t" \
    "buffer_load_dwordx4 v[212:215], %[o2], %[rs], %[t] offen" WNT "\n\t" \
    "buffer_load_dwordx4 v[60:63], %[o3], %[rs], %[t] offen" WNT "\n\t" \
    "buffer_load_dwordx4 v[220:223], %[o4], %[rs], %[t] offen" WNT "\n\t" \
    "s_mov_b32 %[t], 0x1e000\n\t" \
    "buffer_load_dwordx4 v[100:103], %[o0], %[rs], %[t] offen" WNT "\n\t" \
    "buffer_load_dwordx4 v[228:231], %[o1], %[rs], %[t] offen" WNT "\n\t" \
    "buffer_load_dwordx4 v[140:143], %[o2], %[rs], %[t] offen" WNT "\n\t" \
    "buffer_load_dwordx4 v[236:239], %[o3], %[rs], %[t] offen" WNT "\n\t" \
    "buffer_load_dwordx4 v[180:183], %[o4], %[rs], %[t] offen" WNT "\n\t" \
    "s_waitcnt vmcnt(15)\n\t" \
    "ds_write_b128 %[s0], v[200:203] offset:0\n\t" \
    "ds_write_b128 %[s1], v[16:19] offset:0\n\t" \
    "ds_write_b128 %[s2], v[208:211] offset:0\n\t" \
    "ds_write_b128 %[s3], v[56:59] offset:0\n\t" \
    "ds_write_b128 %[s4], v[216:219] offset:0\n\t" \
    "s_waitcnt vmcnt(10)\n\t" \
    "ds_write_b128 %[s0], v[96:99] offset:5632\n\t" \
    "ds_write_b128 %[s1], v[224:227] offset:5632\n\t" \
    "ds_write_b128 %[s2], v[136:139] offset:5632\n\t" \
    "ds_write_b128 %[s3], v[232:235] offset:5632\n\t" \
    "ds_write_b128 %[s4], v[176:179] offset:5632\n\t" \
    "ds_read_b64_tr_b16 v[200:201], %[tr] offset:0\n\t" \
    "ds_read_b64_tr_b16 v[202:203], %[tr] offset:5632\n\t" \
    "ds_read_b64_tr_b16 v[16:17], %[tr] offset:32\n\t" \
    "ds_read_b64_tr_b16 v[18:19], %[tr] offset:5664\n\t" \
    "ds_read_b64_tr_b16 v[208:209], %[tr] offset:64\n\t" \
    "ds_read_b64_tr_b16 v[210:211], %[tr] offset:5696\n\t" \
    "ds_read_b64_tr_b16 v[56:57], %[tr] offset:96\n\t" \
    "ds_read_b64_tr_b16 v[58:59], %[tr] offset:5728\n\t" \
    "ds_read_b64_tr_b16 v[216:217], %[tr] offset:128\n\t" \
    "ds_read_b64_tr_b16 v[218:219], %[tr] offset:5760\n\t" \
    "ds_read_b64_tr_b16 v[96:97], %[tr] offset:160\n\t" \
    "ds_read_b64_tr_b16 v[98:99], %[tr] offset:5792\n\t" \
    "ds_read_b64_tr_b16 v[224:225], %[tr] offset:192\n\t" \
    "ds_read_b64_tr_b16 v[226:227], %[tr] offset:5824\n\t" \
    "ds_read_b64_tr_b16 v[136:137], %[tr] offset:224\n\t" \
    "ds_read_b64_tr_b16 v[138:139], %[tr] offset:5856\n\t" \
    "ds_read_b64_tr_b16 v[232:233], %[tr] offset:256\n\t" \
    "ds_read_b64_tr_b16 v[234:235], %[tr] offset:5888\n\t" \
    "ds_read_b64_tr_b16 v[176:177], %[tr] offset:288\n\t" \
    "ds_read_b64_tr_b16 v[178:179], %[tr] offset:5920\n\t" \
    "s_barrier \n\t" \
    "s_mov_b32 %[t], 0x28000\n\t" \
    "buffer_load_dwordx4 v[0:3], %[o0], %[rs], %[t] offen" WNT "\n\t" \
    "buffer_load_dwordx4 v[24:27], %[o1], %[rs], %[t] offen" WNT "\n\t" \
    "buffer_load_dwordx4 v[40:43], %[o2], %[rs], %[t] offen" WNT "\n\t" \
    "buffer_load_dwordx4 v[64:67], %[o3], %[rs], %[t] offen" WNT "\n\t" \
    "buffer_load_dwordx4 v[80:83], %[o4], %[rs], %[t] offen" WNT "\n\t" \
    "s_mov_b32 %[t], 0x32000\n\t" \
    "buffer_load_dwordx4 v[104:107], %[o0], %[rs], %[t] offen" WNT "\n\t" \
    "buffer_load_dwordx4 v[120:123], %[o1], %[rs], %[t] offen" WNT "\n\t" \
    "buffer_load_dwordx4 v[144:147], %[o2], %[rs], %[t] offen" WNT "\n\t" \
    "buffer_load_dwordx4 v[160:163], %[o3], %[rs], %[t] offen" WNT "\n\t" \
    "buffer_load_dwordx4 v[184:187], %[o4], %[rs], %[t] offen" WNT "\n\t" \
    "s_waitcnt lgkmcnt(0)\n\t" \
    "s_barrier \n\t" \
    "s_waitcnt vmcnt(15)\n\t" \
    "ds_write_b128 %[s0], v[204:207] offset:0\n\t" \
    "ds_write_b128 %[s1], v[20:23] offset:0\n\t" \
    "ds_write_b128 %[s2], v[212:215] offset:0\n\t" \
    "ds_write_b128 %[s3], v[60:63] offset:0\n\t" \
    "ds_write_b128 %[s4], v[220:223] offset:0\n\t" \
    "s_waitcnt vmcnt(10)\n\t" \
    "ds_write_b128 %[s0], v[100:103] offset:5632\n\t" \
    "ds_write_b128 %[s1], v[228:231] offset:5632\n\t" \
    "ds_write_b128 %[s2], v[140:143] offset:5632\n\t" \
    "ds_write_b128 %[s3], v[236:239] offset:5632\n\t" \
    "ds_write_b128 %[s4], v[180:183] offset:5632\n\t" \
    "ds_read_b64_tr_b16 v[204:205], %[tr] offset:0\n\t" \
    "ds_read_b64_tr_b16 v[206:207], %[tr] offset:5632\n\t" \
    "ds_read_b64_tr_b16 v[20:21], %[tr] offset:32\n\t" \
    "ds_read_b64_tr_b16 v[22:23], %[tr] offset:5664\n\t" \
    "ds_read_b64_tr_b16 v[212:213], %[tr] offset:64\n\t" \
    "ds_read_b64_tr_b16 v[214:215], %[tr] offset:5696\n\t" \
    "ds_read_b64_tr_b16 v[60:61], %[tr] offset:96\n\t" \
    "ds_read_b64_tr_b16 v[62:63], %[tr] offset:5728\n\t" \
    "ds_read_b64_tr_b16 v[220:221], %[tr] offset:128\n\t" \
    "ds_read_b64_tr_b16 v[222:223], %[tr] offset:5760\n\t" \
    "ds_read_b64_tr_b16 v[100:101], %[tr] offset:160\n\t" \
    "ds_read_b64_tr_b16 v[102:103], %[tr] offset:5792\n\t" \
    "ds_read_b64_tr_b16 v[228:229], %[tr] offset:192\n\t" \
    "ds_read_b64_tr_b16 v[230:231], %[tr] offset:5824\n\t" \
    "ds_read_b64_tr_b16 v[140:141], %[tr] offset:224\n\t" \
    "ds_read_b64_tr_b16 v[142:143], %[tr] offset:5856\n\t" \
    "ds_read_b64_tr_b16 v[236:237], %[tr] offset:256\n\t" \
    "ds_read_b64_tr_b16 v[238:239], %[tr] offset:5888\n\t" \
    "ds_read_b64_tr_b16 v[180:181], %[tr] offset:288\n\t" \
    "ds_read_b64_tr_b16 v[182:183], %[tr] offset:5920\n\t" \
    "s_barrier \n\t" \
    "s_mov_b32 %[t], 0x5000\n\t" \
    "buffer_load_dwordx4 v[4:7], %[o0], %[rs], %[t] offen" WNT "\n\t" \
    "buffer_load_dwordx4 v[28:31], %[o1], %[rs], %[t] offen" WNT "\n\t" \
    "buffer_load_dwordx4 v[44:47], %[o2], %[rs], %[t] offen" WNT "\n\t" \
    "buffer_load_dwordx4 v[68:71], %[o3], %[rs], %[t] offen" WNT "\n\t" \
    "buffer_load_dwordx4 v[84:87], %[o4], %[rs], %[t] offen" WNT "\n\t" \
    "s_mov_b32 %[t], 0xf000\n\t" \
    "buffer_load_dwordx4 v[108:111], %[o0], %[rs], %[t] offen" WNT "\n\t" \
    "buffer_load_dwordx4 v[124:127], %[o1], %[rs], %[t] offen" WNT "\n\t" \
    "buffer_load_dwordx4 v[148:151], %[o2], %[rs], %[t] offen" WNT "\n\t" \
    "buffer_load_dwordx4 v[164:167], %[o3], %[rs], %[t] offen" WNT "\n\t" \
    "buffer_load_dwordx4 v[188:191], %[o4], %[rs], %[t] offen" WNT "\n\t" \
    "s_waitcnt lgkmcnt(0)\n\t" \
    "s_barrier \n\t" \
    "s_waitcnt vmcnt(15)\n\t" \
    "ds_write_b128 %[s0], v[0:3] offset:0\n\t" \
    "ds_write_b128 %[s1], v[24:27] offset:0\n\t" \
    "ds_write_b128 %[s2], v[40:43] offset:0\n\t" \
    "ds_write_b128 %[s3], v[64:67] offset:0\n\t" \
    "ds_write_b128 %[s4], v[80:83] offset:0\n\t" \
    "s_waitcnt vmcnt(10)\n\t" \
    "ds_write_b128 %[s0], v[104:107] offset:5632\n\t" \
    "ds_write_b128 %[s1], v[120:123] offset:5632\n\t" \
    "ds_write_b128 %[s2], v[144:147] offset:5632\n\t" \
    "ds_write_b128 %[s3], v[160:163] offset:5632\n\t" \
    "ds_write_b128 %[s4], v[184:187] offset:5632\n\t" \
    "ds_read_b64_tr_b16 v[0:1], %[tr] offset:0\n\t" \
    "ds_read_b64_tr_b16 v[2:3], %[tr] offset:5632\n\t" \
    "ds_read_b64_tr_b16 v[24:25], %[tr] offset:32\n\t" \
    "ds_read_b64_tr_b16 v[26:27], %[tr] offset:5664\n\t" \
    "ds_read_b64_tr_b16 v[40:41], %[tr] offset:64\n\t" \
    "ds_read_b64_tr_b16 v[42:43], %[tr] offset:5696\n\t" \
    "ds_read_b64_tr_b16 v[64:65], %[tr] offset:96\n\t" \
    "ds_read_b64_tr_b16 v[66:67], %[tr] offset:5728\n\t" \
    "ds_read_b64_tr_b16 v[80:81], %[tr] offset:128\n\t" \
    "ds_read_b64_tr_b16 v[82:83], %[tr] offset:5760\n\t" \
    "ds_read_b64_tr_b16 v[104:105], %[tr] offset:160\n\t" \
    "ds_read_b64_tr_b16 v[106:107], %[tr] offset:5792\n\t" \
    "ds_read_b64_tr_b16 v[120:121], %[tr] offset:192\n\t" \
    "ds_read_b64_tr_b16 v[122:123], %[tr] offset:5824\n\t" \
    "ds_read_b64_tr_b16 v[144:145], %[tr] offset:224\n\t" \
    "ds_read_b64_tr_b16 v[146:147], %[tr] offset:5856\n\t" \
    "ds_read_b64_tr_b16 v[160:161], %[tr] offset:256\n\t" \
    "ds_read_b64_tr_b16 v[162:163], %[tr] offset:5888\n\t" \
    "ds_read_b64_tr_b16 v[184:185], %[tr] offset:288\n\t" \
    "ds_read_b64_tr_b16 v[186:187], %[tr] offset:5920\n\t" \
    "s_barrier \n\t" \
    "s_mov_b32 %[t], 0x19000\n\t" \
    "buffer_load_dwordx4 v[8:11], %[o0], %[rs], %[t] offen" WNT "\n\t" \
    "buffer_load_dwordx4 v[32:35], %[o1], %[rs], %[t] offen" WNT "\n\t" \
    "buffer_load_dwordx4 v[48:51], %[o2], %[rs], %[t] offen" WNT "\n\t" \
    "buffer_load_dwordx4 v[72:75], %[o3], %[rs], %[t] offen" WNT "\n\t" \
    "buffer_load_dwordx4 v[88:91], %[o4], %[rs], %[t] offen" WNT "\n\t" \
    "s_mov_b32 %[t], 0x23000\n\t" \
    "buffer_load_dwordx4 v[112:115], %[o0], %[rs], %[t] offen" WNT "\n\t" \
    "buffer_load_dwordx4 v[128:131], %[o1], %[rs], %[t] offen" WNT "\n\t" \
    "buffer_load_dwordx4 v[152:155], %[o2], %[rs], %[t] offen" WNT "\n\t" \
    "buffer_load_dwordx4 v[168:171], %[o3], %[rs], %[t] offen" WNT "\n\t" \
    "buffer_load_dwordx4 v[192:195], %[o4], %[rs], %[t] offen" WNT "\n\t" \
    "s_waitcnt lgkmcnt(0)\n\t" \
    "s_barrier \n\t" \
    "s_waitcnt vmcnt(15)\n\t" \
    "ds_write_b128 %[s0], v[4:7] offset:0\n\t" \
    "ds_write_b128 %[s1], v[28:31] offset:0\n\t" \
    "ds_write_b128 %[s2], v[44:47] offset:0\n\t" \
    "ds_write_b128 %[s3], v[68:71] offset:0\n\t" \
    "ds_write_b128 %[s4], v[84:87] offset:0\n\t" \
    "s_waitcnt vmcnt(10)\n\t" \
    "ds_write_b128 %[s0], v[108:111] offset:5632\n\t" \
    "ds_write_b128 %[s1], v[124:127] offset:5632\n\t" \
    "ds_write_b128 %[s2], v[148:151] offset:5632\n\t" \
    "ds_write_b128 %[s3], v[164:167] offset:5632\n\t" \
    "ds_write_b128 %[s4], v[188:191] offset:5632\n\t" \
    "ds_read_b64_tr_b16 v[4:5], %[tr] offset:0\n\t" \
    "ds_read_b64_tr_b16 v[6:7], %[tr] offset:5632\n\t" \
    "ds_read_b64_tr_b16 v[28:29], %[tr] offset:32\n\t" \
    "ds_read_b64_tr_b16 v[30:31], %[tr] offset:5664\n\t" \
    "ds_read_b64_tr_b16 v[44:45], %[tr] offset:64\n\t" \
    "ds_read_b64_tr_b16 v[46:47], %[tr] offset:5696\n\t" \
    "ds_read_b64_tr_b16 v[68:69], %[tr] offset:96\n\t" \
    "ds_read_b64_tr_b16 v[70:71], %[tr] offset:5728\n\t" \
    "ds_read_b64_tr_b16 v[84:85], %[tr] offset:128\n\t" \
    "ds_read_b64_tr_b16 v[86:87], %[tr] offset:5760\n\t" \
    "ds_read_b64_tr_b16 v[108:109], %[tr] offset:160\n\t" \
    "ds_read_b64_tr_b16 v[110:111], %[tr] offset:5792\n\t" \
    "ds_read_b64_tr_b16 v[124:125], %[tr] offset:192\n\t" \
    "ds_read_b64_tr_b16 v[126:127], %[tr] offset:5824\n\t" \
    "ds_read_b64_tr_b16 v[148:149], %[tr] offset:224\n\t" \
    "ds_read_b64_tr_b16 v[150:151], %[tr] offset:5856\n\t" \
    "ds_read_b64_tr_b16 v[164:165], %[tr] offset:256\n\t" \
    "ds_read_b64_tr_b16 v[166:167], %[tr] offset:5888\n\t" \
    "ds_read_b64_tr_b16 v[188:189], %[tr] offset:288\n\t" \
    "ds_read_b64_tr_b16 v[190:191], %[tr] offset:5920\n\t" \
    "s_barrier \n\t" \
    "s_mov_b32 %[t], 0x2d000\n\t" \
    "buffer_load_dwordx4 v[12:15], %[o0], %[rs], %[t] offen" WNT "\n\t" \
    "buffer_load_dwordx4 v[36:39], %[o1], %[rs], %[t] offen" WNT "\n\t" \
    "buffer_load_dwordx4 v[52:55], %[o2], %[rs], %[t] offen" WNT "\n\t" \
    "buffer_load_dwordx4 v[76:79], %[o3], %[rs], %[t] offen" WNT "\n\t" \
    "buffer_load_dwordx4 v[92:95], %[o4], %[rs], %[t] offen" WNT "\n\t" \
    "s_mov_b32 %[t], 0x37000\n\t" \
    "buffer_load_dwordx4 v[116:119], %[o0], %[rs], %[t] offen" WNT "\n\t" \
    "buffer_load_dwordx4 v[132:135], %[o1], %[rs], %[t] offen" WNT "\n\t" \
    "buffer_load_dwordx4 v[156:159], %[o2], %[rs], %[t] offen" WNT "\n\t" \
    "buffer_load_dwordx4 v[172:175], %[o3], %[rs], %[t] offen" WNT "\n\t" \
    "buffer_load_dwordx4 v[196:199], %[o4], %[rs], %[t] offen" WNT "\n\t" \
    "s_waitcnt lgkmcnt(0)\n\t" \
    "s_barrier \n\t" \
    "s_waitcnt vmcnt(15)\n\t" \
    "ds_write_b128 %[s0], v[8:11] offset:0\n\t" \
    "ds_write_b128 %[s1], v[32:35] offset:0\n\t" \
    "ds_write_b128 %[s2], v[48:51] offset:0\n\t" \
    "ds_write_b128 %[s3], v[72:75] offset:0\n\t" \
    "ds_write_b128 %[s4], v[88:91] offset:0\n\t" \
    "s_waitcnt vmcnt(10)\n\t" \
    "ds_write_b128 %[s0], v[112:115] offset:5632\n\t" \
    "ds_write_b128 %[s1], v[128:131] offset:5632\n\t" \
    "ds_write_b128 %[s2], v[152:155] offset:5632\n\t" \
    "ds_write_b128 %[s3], v[168:171] offset:5632\n\t" \
    "ds_write_b128 %[s4], v[192:195] offset:5632\n\t" \
    "ds_read_b64_tr_b16 v[8:9], %[tr] offset:0\n\t" \
    "ds_read_b64_tr_b16 v[10:11], %[tr] offset:5632\n\t" \
    "ds_read_b64_tr_b16 v[32:33], %[tr] offset:32\n\t" \
    "ds_read_b64_tr_b16 v[34:35], %[tr] offset:5664\n\t" \
    "ds_read_b64_tr_b16 v[48:49], %[tr] offset:64\n\t" \
    "ds_read_b64_tr_b16 v[50:51], %[tr] offset:5696\n\t" \
    "ds_read_b64_tr_b16 v[72:73], %[tr] offset:96\n\t" \
    "ds_read_b64_tr_b16 v[74:75], %[tr] offset:5728\n\t" \
    "ds_read_b64_tr_b16 v[88:89], %[tr] offset:128\n\t" \
    "ds_read_b64_tr_b16 v[90:91], %[tr] offset:5760\n\t" \
    "ds_read_b64_tr_b16 v[112:113], %[tr] offset:160\n\t" \
    "ds_read_b64_tr_b16 v[114:115], %[tr] offset:5792\n\t" \
    "ds_read_b64_tr_b16 v[128:129], %[tr] offset:192\n\t" \
    "ds_read_b64_tr_b16 v[130:131], %[tr] offset:5824\n\t" \
    "ds_read_b64_tr_b16 v[152:153], %[tr] offset:224\n\t" \
    "ds_read_b64_tr_b16 v[154:155], %[tr] offset:5856\n\t" \
    "ds_read_b64_tr_b16 v[168:169], %[tr] offset:256\n\t" \
    "ds_read_b64_tr_b16 v[170:171], %[tr] offset:5888\n\t" \
    "ds_read_b64_tr_b16 v[192:193], %[tr] offset:288\n\t" \
    "ds_read_b64_tr_b16 v[194:195], %[tr] offset:5920\n\t" \
    "s_waitcnt lgkmcnt(0)\n\t" \
    "s_barrier \n\t" \
    "s_waitcnt vmcnt(5)\n\t" \
    "ds_write_b128 %[s0], v[12:15] offset:0\n\t" \
    "ds_write_b128 %[s1], v[36:39] offset:0\n\t" \
    "ds_write_b128 %[s2], v[52:55] offset:0\n\t" \
    "ds_write_b128 %[s3], v[76:79] offset:0\n\t" \
    "ds_write_b128 %[s4], v[92:95] offset:0\n\t" \
    "s_waitcnt vmcnt(0)\n\t" \
    "ds_write_b128 %[s0], v[116:119] offset:5632\n\t" \
    "ds_write_b128 %[s1], v[132:135] offset:5632\n\t" \
    "ds_write_b128 %[s2], v[156:159] offset:5632\n\t" \
    "ds_write_b128 %[s3], v[172:175] offset:5632\n\t" \
    "ds_write_b128 %[s4], v[196:199] offset:5632\n\t" \
    "ds_read_b64_tr_b16 v[12:13], %[tr] offset:0\n\t" \
    "ds_read_b64_tr_b16 v[14:15], %[tr] offset:5632\n\t" \
    "ds_read_b64_tr_b16 v[36:37], %[tr] offset:32\n\t" \
    "ds_read_b64_tr_b16 v[38:39], %[tr] offset:5664\n\t" \
    "ds_read_b64_tr_b16 v[52:53], %[tr] offset:64\n\t" \
    "ds_read_b64_tr_b16 v[54:55], %[tr] offset:5696\n\t" \
    "ds_read_b64_tr_b16 v[76:77], %[tr] offset:96\n\t" \
    "ds_read_b64_tr_b16 v[78:79], %[tr] offset:5728\n\t" \
    "ds_read_b64_tr_b16 v[92:93], %[tr] offset:128\n\t" \
    "ds_read_b64_tr_b16 v[94:95], %[tr] offset:5760\n\t" \
    "ds_read_b64_tr_b16 v[116:117], %[tr] offset:160\n\t" \
    "ds_read_b64_tr_b16 v[118:119], %[tr] offset:5792\n\t" \
    "ds_read_b64_tr_b16 v[132:133], %[tr] offset:192\n\t" \
    "ds_read_b64_tr_b16 v[134:135], %[tr] offset:5824\n\t" \
    "ds_read_b64_tr_b16 v[156:157], %[tr] offset:224\n\t" \
    "ds_read_b64_tr_b16 v[158:159], %[tr] offset:5856\n\t" \
    "ds_read_b64_tr_b16 v[172:173], %[tr] offset:256\n\t" \
    "ds_read_b64_tr_b16 v[174:175], %[tr] offset:5888\n\t" \
    "ds_read_b64_tr_b16 v[196:197], %[tr] offset:288\n\t" \
    "ds_read_b64_tr_b16 v[198:199], %[tr] offset:5920\n\t" \
    "s_waitcnt lgkmcnt(0)\n\t" \
    "s_barrier \n\t" \
    "ds_write_b128 %[wl], v[200:203] offset:0\n\t" \
    "ds_write_b128 %[wl], v[204:207] offset:1024\n\t" \
    "ds_write_b128 %[wl], v[208:211] offset:2048\n\t" \
    "ds_write_b128 %[wl], v[212:215] offset:3072\n\t" \
    "ds_write_b128 %[wl], v[216:219] offset:4096\n\t" \
    "ds_write_b128 %[wl], v[220:223] offset:5120\n\t" \
    "ds_write_b128 %[wl], v[224:227] offset:6144\n\t" \
    "ds_write_b128 %[wl], v[228:231] offset:7168\n\t" \
    "ds_write_b128 %[wl], v[232:235] offset:8192\n\t" \
    "ds_write_b128 %[wl], v[236:239] offset:9216\n\t" \
    "s_waitcnt lgkmcnt(0)\n\t"
#define ASM_XG_S_LO \
    "s_waitcnt lgkmcnt(0)\n\t" \
    "ds_read_b128 v[224:227], %[xa] offset:16384\n\t" \
    "ds_read_b128 v[228:231], %[xa] offset:17408\n\t" \
    "ds_read_b128 v[244:247], %[wl] offset:0\n\t" \
    "ds_read_b128 v[248:251], %[wl] offset:1024\n\t" \
    "ds_read_b128 v[232:235], %[xa] offset:0\n\t" \
    "ds_read_b128 v[236:239], %[xa] offset:1024\n\t" \
    "v_xor_b32 v252, 0x800, %[xa]\n\t" \
    "ds_read_b128 v[240:243], v252 offset:0\n\t" \
    "s_waitcnt lgkmcnt(6)\n\t" \
    "v_mfma_f32_16x16x32_bf16 v[220:223], v[8:11], v[224:227], 0\n\t" \
    "s_waitcnt lgkmcnt(5)\n\t" \
    "v_mfma_f32_16x16x32_bf16 v[220:223], v[12:15], v[228:231], v[220:223]\n\t" \
    "v_mfma_f32_16x16x32_bf16 v[216:219], v[0:3], v[224:227], 0\n\t" \
    "v_mfma_f32_16x16x32_bf16 v[216:219], v[4:7], v[228:231], v[216:219]\n\t" \
    "s_waitcnt lgkmcnt(4)\n\t" \
    "v_mfma_f32_16x16x32_bf16 v[212:215], v[244:247], v[224:227], 0\n\t" \
    "ds_read_b128 v[224:227], v252 offset:1024\n\t" \
    "s_waitcnt lgkmcnt(4)\n\t" \
    "v_mfma_f32_16x16x32_bf16 v[212:215], v[248:251], v[228:231], v[212:215]\n\t" \
    "ds_read_b128 v[228:231], v252 offset:16384\n\t" \
    "s_waitcnt lgkmcnt(4)\n\t" \
    "v_mfma_f32_16x16x32_bf16 v[200:203], v[244:247], v[232:235], 0\n\t" \
    "ds_read_b128 v[244:247], %[wl] offset:2048\n\t" \
    "s_waitcnt lgkmcnt(4)\n\t" \
    "v_mfma_f32_16x16x32_bf16 v[200:203], v[248:251], v[236:239], v[200:203]\n\t" \
    "ds_read_b128 v[248:251], %[wl] offset:3072\n\t" \
    "v_mfma_f32_16x16x32_bf16 v[204:207], v[0:3], v[232:235], 0\n\t" \
    "v_mfma_f32_16x16x32_bf16 v[204:207], v[4:7], v[236:239], v[204:207]\n\t" \
    "v_mfma_f32_16x16x32_bf16 v[208:211], v[8:11], v[232:235], 0\n\t" \
    "ds_read_b128 v[232:235], v252 offset:17408\n\t" \
    "v_mfma_f32_16x16x32_bf16 v[208:211], v[12:15], v[236:239], v[208:211]\n\t" \
    "v_xor_b32 v252, 0x1000, %[xa]\n\t" \
    "ds_read_b128 v[236:239], v252 offset:16384\n\t" \
    "s_waitcnt lgkmcnt(6)\n\t" \
    "v_mfma_f32_16x16x32_bf16 v[208:211], v[32:35], v[240:243], v[208:211]\n\t" \
    "s_waitcnt lgkmcnt(5)\n\t" \
    "v_mfma_f32_16x16x32_bf16 v[208:211], v[36:39], v[224:227], v[208:211]\n\t" \
    "v_mfma_f32_16x16x32_bf16 v[204:207], v[24:27], v[240:243], v[204:207]\n\t" \
    "v_mfma_f32_16x16x32_bf16 v[204:207], v[28:31], v[224:227], v[204:207]\n\t" \
    "v_mfma_f32_16x16x32_bf16 v[200:203], v[16:19], v[240:243], v[200:203]\n\t" \
    "ds_read_b128 v[240:243], v252 offset:17408\n\t" \
    "v_mfma_f32_16x16x32_bf16 v[200:203], v[20:23], v[224:227], v[200:203]\n\t" \
    "ds_read_b128 v[224:227], v252 offset:0\n\t" \
    "s_waitcnt lgkmcnt(6)\n\t" \
    "v_mfma_f32_16x16x32_bf16 v[212:215], v[16:19], v[228:231], v[212:215]\n\t" \
    "s_waitcnt lgkmcnt(3)\n\t" \
    "v_mfma_f32_16x16x32_bf16 v[212:215], v[20:23], v[232:235], v[212:215]\n\t" \
    "v_mfma_f32_16x16x32_bf16 v[216:219], v[24:27], v[228:231], v[216:219]\n\t" \
    "v_mfma_f32_16x16x32_bf16 v[216:219], v[28:31], v[232:235], v[216:219]\n\t" \
    "v_mfma_f32_16x16x32_bf16 v[220:223], v[32:35], v[228:231], v[220:223]\n\t" \
    "ds_read_b128 v[228:231], v252 offset:1024\n\t" \
    "v_mfma_f32_16x16x32_bf16 v[220:223], v[36:39], v[232:235], v[220:223]\n\t" \
    "v_xor_b32 v252, 0x1800, %[xa]\n\t" \
    "ds_read_b128 v[232:235], v252 offset:0\n\t" \
    "s_waitcnt lgkmcnt(4)\n\t" \
    "v_mfma_f32_16x16x32_bf16 v[220:223], v[48:51], v[236:239], v[220:223]\n\t" \
    "s_waitcnt lgkmcnt(3)\n\t" \
    "v_mfma_f32_16x16x32_bf16 v[220:223], v[52:55], v[240:243], v[220:223]\n\t" \
    "v_mfma_f32_16x16x32_bf16 v[216:219], v[40:43], v[236:239], v[216:219]\n\t" \
    "v_mfma_f32_16x16x32_bf16 v[216:219], v[44:47], v[240:243], v[216:219]\n\t" \
    "v_mfma_f32_16x16x32_bf16 v[212:215], v[244:247], v[236:239], v[212:215]\n\t" \
    "ds_read_b128 v[236:239], v252 offset:1024\n\t" \
    "v_mfma_f32_16x16x32_bf16 v[212:215], v[248:251], v[240:243], v[212:215]\n\t" \
    "ds_read_b128 v[240:243], v252 offset:16384\n\t" \
    "s_waitcnt lgkmcnt(4)\n\t" \
    "v_mfma_f32_16x16x32_bf16 v[200:203], v[244:247], v[224:227], v[200:203]\n\t" \
    "ds_read_b128 v[244:247], %[wl] offset:8192\n\t" \
    "s_waitcnt lgkmcnt(4)\n\t" \
    "v_mfma_f32_16x16x32_bf16 v[200:203], v[248:251], v[228:231], v[200:203]\n\t" \
    "ds_read_b128 v[248:251], %[wl] offset:9216\n\t" \
    "v_mfma_f32_16x16x32_bf16 v[204:207], v[40:43], v[224:227], v[204:207]\n\t" \
    "v_mfma_f32_16x16x32_bf16 v[204:207], v[44:47], v[228:231], v[204:207]\n\t" \
    "v_mfma_f32_16x16x32_bf16 v[208:211], v[48:51], v[224:227], v[208:211]\n\t" \
    "ds_read_b128 v[224:227], v252 offset:17408\n\t" \
    "v_mfma_f32_16x16x32_bf16 v[208:211], v[52:55], v[228:231], v[208:211]\n\t" \
    "v_and_b32 v252, 0x3ff, %[xa]\n\t" \
    "ds_read_b128 v[228:231], v252 offset:45056\n\t" \
    "s_waitcnt lgkmcnt(6)\n\t" \
    "v_mfma_f32_16x16x32_bf16 v[208:211], v[72:75], v[232:235], v[208:211]\n\t" \
    "s_waitcnt lgkmcnt(5)\n\t" \
    "v_mfma_f32_16x16x32_bf16 v[208:211], v[76:79], v[236:239], v[208:211]\n\t" \
    "v_mfma_f32_16x16x32_bf16 v[204:207], v[64:67], v[232:235], v[204:207]\n\t" \
    "v_mfma_f32_16x16x32_bf16 v[204:207], v[68:71], v[236:239], v[204:207]\n\t" \
    "v_mfma_f32_16x16x32_bf16 v[200:203], v[56:59], v[232:235], v[200:203]\n\t" \
    "ds_read_b128 v[232:235], v252 offset:46080\n\t" \
    "v_mfma_f32_16x16x32_bf16 v[200:203], v[60:63], v[236:239], v[200:203]\n\t" \
    "ds_read_b128 v[236:239], v252 offset:40960\n\t" \
    "s_waitcnt lgkmcnt(6)\n\t" \
    "v_mfma_f32_16x16x32_bf16 v[212:215], v[56:59], v[240:243], v[212:215]\n\t" \
    "s_waitcnt lgkmcnt(3)\n\t" \
    "v_mfma_f32_16x16x32_bf16 v[212:215], v[60:63], v[224:227], v[212:215]\n\t" \
    "v_mfma_f32_16x16x32_bf16 v[216:219], v[64:67], v[240:243], v[216:219]\n\t" \
    "v_mfma_f32_16x16x32_bf16 v[216:219], v[68:71], v[224:227], v[216:219]\n\t" \
    "v_mfma_f32_16x16x32_bf16 v[220:223], v[72:75], v[240:243], v[220:223]\n\t" \
    "ds_read_b128 v[240:243], v252 offset:41984\n\t" \
    "v_mfma_f32_16x16x32_bf16 v[220:223], v[76:79], v[224:227], v[220:223]\n\t" \
    "s_waitcnt lgkmcnt(0)\n\t" \
    "s_barrier\n\t" ASM_ST \
    "v_xor_b32 v252, 0x2000, %[xa]\n\t" \
    "ds_read_b128 v[224:227], v252 offset:0\n\t" \
    "v_mfma_f32_16x16x32_bf16 v[220:223], v[168:171], v[228:231], v[220:223]\n\t" \
    "v_mfma_f32_16x16x32_bf16 v[220:223], v[172:175], v[232:235], v[220:223]\n\t" \
    "v_mfma_f32_16x16x32_bf16 v[216:219], v[160:163], v[228:231], v[216:219]\n\t" \
    "v_mfma_f32_16x16x32_bf16 v[216:219], v[164:167], v[232:235], v[216:219]\n\t" \
    "v_mfma_f32_16x16x32_bf16 v[212:215], v[244:247], v[228:231], v[212:215]\n\t" \
    "ds_read_b128 v[228:231], v252 offset:1024\n\t" \
    "v_mfma_f32_16x16x32_bf16 v[212:215], v[248:251], v[232:235], v[212:215]\n\t" \
    "ds_read_b128 v[232:235], v252 offset:16384\n\t" \
    "v_mfma_f32_16x16x32_bf16 v[200:203], v[244:247], v[236:239], v[200:203]\n\t" \
    "ds_read_b128 v[244:247], %[wl] offset:4096\n\t" \
    "v_mfma_f32_16x16x32_bf16 v[200:203], v[248:251], v[240:243], v[200:203]\n\t" \
    "ds_read_b128 v[248:251], %[wl] offset:5120\n\t" \
    "v_mfma_f32_16x16x32_bf16 v[204:207], v[160:163], v[236:239], v[204:207]\n\t" \
    "v_mfma_f32_16x16x32_bf16 v[204:207], v[164:167], v[240:243], v[204:207]\n\t" \
    "v_mfma_f32_16x16x32_bf16 v[208:211], v[168:171], v[236:239], v[208:211]\n\t" \
    "ds_read_b128 v[236:239], v252 offset:17408\n\t" \
    "v_mfma_f32_16x16x32_bf16 v[208:211], v[172:175], v[240:243], v[208:211]\n\t" \
    "v_xor_b32 v252, 0x2800, %[xa]\n\t" \
    "ds_read_b128 v[240:243], v252 offset:16384\n\t" \
    "s_waitcnt lgkmcnt(6)\n\t" \
    "v_mfma_f32_16x16x32_bf16 v[208:211], v[88:91], v[224:227], v[208:211]\n\t" \
    "s_waitcnt lgkmcnt(5)\n\t" \
    "v_mfma_f32_16x16x32_bf16 v[208:211], v[92:95], v[228:231], v[208:211]\n\t" \
    "v_mfma_f32_16x16x32_bf16 v[204:207], v[80:83], v[224:227], v[204:207]\n\t" \
    "v_mfma_f32_16x16x32_bf16 v[204:207], v[84:87], v[228:231], v[204:207]\n\t" \
    "s_waitcnt lgkmcnt(3)\n\t" \
    "v_mfma_f32_16x16x32_bf16 v[200:203], v[244:247], v[224:227], v[200:203]\n\t" \
    "ds_read_b128 v[224:227], v252 offset:17408\n\t" \
    "s_waitcnt lgkmcnt(3)\n\t" \
    "v_mfma_f32_16x16x32_bf16 v[200:203], v[248:251], v[228:231], v[200:203]\n\t" \
    "ds_read_b128 v[228:231], v252 offset:0\n\t" \
    "v_mfma_f32_16x16x32_bf16 v[212:215], v[244:247], v[232:235], v[212:215]\n\t" \
    "ds_read_b128 v[244:247], %[wl] offset:6144\n\t" \
    "s_waitcnt lgkmcnt(4)\n\t" \
    "v_mfma_f32_16x16x32_bf16 v[212:215], v[248:251], v[236:239], v[212:215]\n\t" \
    "ds_read_b128 v[248:251], %[wl] offset:7168\n\t" \
    "v_mfma_f32_16x16x32_bf16 v[216:219], v[80:83], v[232:235], v[216:219]\n\t" \
    "v_mfma_f32_16x16x32_bf16 v[216:219], v[84:87], v[236:239], v[216:219]\n\t" \
    "v_mfma_f32_16x16x32_bf16 v[220:223], v[88:91], v[232:235], v[220:223]\n\t" \
    "ds_read_b128 v[232:235], v252 offset:1024\n\t" \
    "v_mfma_f32_16x16x32_bf16 v[220:223], v[92:95], v[236:239], v[220:223]\n\t" \
    "v_xor_b32 v252, 0x3000, %[xa]\n\t" \
    "ds_read_b128 v[236:239], v252 offset:0\n\t" \
    "s_waitcnt lgkmcnt(6)\n\t" \
    "v_mfma_f32_16x16x32_bf16 v[220:223], v[112:115], v[240:243], v[220:223]\n\t" \
    "s_waitcnt lgkmcnt(5)\n\t" \
    "v_mfma_f32_16x16x32_bf16 v[220:223], v[116:119], v[224:227], v[220:223]\n\t" \
    "v_mfma_f32_16x16x32_bf16 v[216:219], v[104:107], v[240:243], v[216:219]\n\t" \
    "v_mfma_f32_16x16x32_bf16 v[216:219], v[108:111], v[224:227], v[216:219]\n\t" \
    "v_mfma_f32_16x16x32_bf16 v[212:215], v[96:99], v[240:243], v[212:215]\n\t" \
    "ds_read_b128 v[240:243], v252 offset:1024\n\t" \
    "v_mfma_f32_16x16x32_bf16 v[212:215], v[100:103], v[224:227], v[212:215]\n\t" \
    "ds_read_b128 v[224:227], v252 offset:16384\n\t" \
    "s_waitcnt lgkmcnt(6)\n\t" \
    "v_mfma_f32_16x16x32_bf16 v[200:203], v[96:99], v[228:231], v[200:203]\n\t" \
    "s_waitcnt lgkmcnt(3)\n\t" \
    "v_mfma_f32_16x16x32_bf16 v[200:203], v[100:103], v[232:235], v[200:203]\n\t" \
    "v_mfma_f32_16x16x32_bf16 v[204:207], v[104:107], v[228:231], v[204:207]\n\t" \
    "v_mfma_f32_16x16x32_bf16 v[204:207], v[108:111], v[232:235], v[204:207]\n\t" \
    "v_mfma_f32_16x16x32_bf16 v[208:211], v[112:115], v[228:231], v[208:211]\n\t" \
    "ds_read_b128 v[228:231], v252 offset:17408\n\t" \
    "v_mfma_f32_16x16x32_bf16 v[208:211], v[116:119], v[232:235], v[208:211]\n\t" \
    "v_xor_b32 v252, 0x3800, %[xa]\n\t" \
    "ds_read_b128 v[232:235], v252 offset:16384\n\t" \
    "s_waitcnt lgkmcnt(4)\n\t" \
    "v_mfma_f32_16x16x32_bf16 v[208:211], v[128:131], v[236:239], v[208:211]\n\t" \
    "s_waitcnt lgkmcnt(3)\n\t" \
    "v_mfma_f32_16x16x32_bf16 v[208:211], v[132:135], v[240:243], v[208:211]\n\t" \
    "v_mfma_f32_16x16x32_bf16 v[204:207], v[120:123], v[236:239], v[204:207]\n\t" \
    "v_mfma_f32_16x16x32_bf16 v[204:207], v[124:127], v[240:243], v[204:207]\n\t" \
    "v_mfma_f32_16x16x32_bf16 v[200:203], v[244:247], v[236:239], v[200:203]\n\t" \
    "ds_read_b128 v[236:239], v252 offset:17408\n\t" \
    "v_mfma_f32_16x16x32_bf16 v[200:203], v[248:251], v[240:243], v[200:203]\n\t" \
    "ds_read_b128 v[240:243], v252 offset:0\n\t" \
    "s_waitcnt lgkmcnt(4)\n\t" \
    "v_mfma_f32_16x16x32_bf16 v[212:215], v[244:247], v[224:227], v[212:215]\n\t" \
    "s_waitcnt lgkmcnt(3)\n\t" \
    "v_mfma_f32_16x16x32_bf16 v[212:215], v[248:251], v[228:231], v[212:215]\n\t" \
    "v_mfma_f32_16x16x32_bf16 v[216:219], v[120:123], v[224:227], v[216:219]\n\t" \
    "v_mfma_f32_16x16x32_bf16 v[216:219], v[124:127], v[228:231], v[216:219]\n\t" \
    "v_mfma_f32_16x16x32_bf16 v[220:223], v[128:131], v[224:227], v[220:223]\n\t" \
    "ds_read_b128 v[224:227], v252 offset:1024\n\t" \
    "v_mfma_f32_16x16x32_bf16 v[220:223], v[132:135], v[228:231], v[220:223]\n\t" \
    "v_and_b32 v252, 0x3ff, %[xa]\n\t" \
    "ds_read_b128 v[228:231], v252 offset:43008\n\t" \
    "s_waitcnt lgkmcnt(4)\n\t" \
    "v_mfma_f32_16x16x32_bf16 v[220:223], v[152:155], v[232:235], v[220:223]\n\t" \
    "v_mov_b64 v[244:245], 0\n\t" \
    "s_waitcnt lgkmcnt(3)\n\t" \
    "v_mfma_f32_16x16x32_bf16 v[220:223], v[156:159], v[236:239], v[220:223]\n\t" \
    "v_mov_b64 v[246:247], 0\n\t" \
    "v_mfma_f32_16x16x32_bf16 v[216:219], v[144:147], v[232:235], v[216:219]\n\t" \
    "v_mov_b64 v[248:249], 0\n\t" \
    "v_mfma_f32_16x16x32_bf16 v[216:219], v[148:151], v[236:239], v[216:219]\n\t" \
    "v_mov_b64 v[250:251], 0\n\t" \
    "v_mfma_f32_16x16x32_bf16 v[212:215], v[136:139], v[232:235], v[212:215]\n\t" \
    "ds_read_b128 v[232:235], v252 offset:44032\n\t" \
    "v_mfma_f32_16x16x32_bf16 v[212:215], v[140:143], v[236:239], v[212:215]\n\t" \
    "ds_read_b128 v[236:239], v252 offset:47104\n\t" \
    "s_waitcnt lgkmcnt(4)\n\t" \
    "v_mfma_f32_16x16x32_bf16 v[200:203], v[136:139], v[240:243], v[200:203]\n\t" \
    "s_waitcnt lgkmcnt(3)\n\t" \
    "v_mfma_f32_16x16x32_bf16 v[200:203], v[140:143], v[224:227], v[200:203]\n\t" \
    "v_mfma_f32_16x16x32_bf16 v[204:207], v[144:147], v[240:243], v[204:207]\n\t" \
    "v_mfma_f32_16x16x32_bf16 v[204:207], v[148:151], v[224:227], v[204:207]\n\t" \
    "v_mfma_f32_16x16x32_bf16 v[208:211], v[152:155], v[240:243], v[208:211]\n\t" \
    "ds_read_b128 v[240:243], v252 offset:48128\n\t" \
    "v_mfma_f32_16x16x32_bf16 v[208:211], v[156:159], v[224:227], v[208:211]\n\t" \
    "s_waitcnt lgkmcnt(3)\n\t" \
    "v_mfma_f32_16x16x32_bf16 v[208:211], v[192:195], v[228:231], v[208:211]\n\t" \
    "v_add_u32 v252, %[qs], v252\n\t" \
    "v_mov_b64 v[224:225], 0\n\t" \
    "s_waitcnt lgkmcnt(2)\n\t" \
    "v_mfma_f32_16x16x32_bf16 v[208:211], v[196:199], v[232:235], v[208:211]\n\t" \
    "v_mov_b64 v[226:227], 0\n\t" \
    "v_mfma_f32_16x16x32_bf16 v[204:207], v[184:187], v[228:231], v[204:207]\n\t" \
    "v_mfma_f32_16x16x32_bf16 v[204:207], v[188:191], v[232:235], v[204:207]\n\t" \
    "v_mfma_f32_16x16x32_bf16 v[200:203], v[176:179], v[228:231], v[200:203]\n\t" \
    "v_mfma_f32_16x16x32_bf16 v[200:203], v[180:183], v[232:235], v[200:203]\n\t" \
    "s_waitcnt lgkmcnt(1)\n\t" \
    "v_mfma_f32_16x16x32_bf16 v[212:215], v[176:179], v[236:239], v[212:215]\n\t" \
    "ds_read_b128 v[228:231], %[wl] offset:2048\n\t" \
    "ds_read_b128 v[232:235], %[wl] offset:3072\n\t" \
    "s_waitcnt lgkmcnt(2)\n\t" \
    "v_mfma_f32_16x16x32_bf16 v[212:215], v[180:183], v[240:243], v[212:215]\n\t" \
    "v_cvt_pk_bf16_f32 v208, v208, v209\n\t" \
    "v_cvt_pk_bf16_f32 v209, v210, v211\n\t" \
    "v_mfma_f32_16x16x32_bf16 v[216:219], v[184:187], v[236:239], v[216:219]\n\t" \
    "ds_write_b64 v252, v[208:209] offset:2048\n\t" \
    "v_cvt_pk_bf16_f32 v204, v204, v205\n\t" \
    "v_mfma_f32_16x16x32_bf16 v[216:219], v[188:191], v[240:243], v[216:219]\n\t" \
    "v_cvt_pk_bf16_f32 v205, v206, v207\n\t" \
    "ds_write_b64 v252, v[204:205] offset:1024\n\t" \
    "v_mfma_f32_16x16x32_bf16 v[220:223], v[192:195], v[236:239], v[220:223]\n\t" \
    "v_cvt_pk_bf16_f32 v200, v200, v201\n\t" \
    "v_cvt_pk_bf16_f32 v201, v202, v203\n\t" \
    "v_mfma_f32_16x16x32_bf16 v[220:223], v[196:199], v[240:243], v[220:223]\n\t" \
    "ds_write_b64 v252, v[200:201] offset:0\n\t" \
    "ds_read_b128 v[200:203], %[wl] offset:0\n\t" \
    "ds_read_b128 v[204:207], %[wl] offset:1024\n\t" \
    "v_cvt_pk_bf16_f32 v212, v212, v213\n\t" \
    "v_cvt_pk_bf16_f32 v213, v214, v215\n\t" \
    "ds_write_b64 v252, v[212:213] offset:6144\n\t" \
    "v_cvt_pk_bf16_f32 v216, v216, v217\n\t" \
    "v_cvt_pk_bf16_f32 v217, v218, v219\n\t" \
    "ds_write_b64 v252, v[216:217] offset:7168\n\t" \
    "v_cvt_pk_bf16_f32 v220, v220, v221\n\t" \
    "v_cvt_pk_bf16_f32 v221, v222, v223\n\t" \
    "ds_write_b64 v252, v[220:221] offset:8192\n\t" \
    "s_waitcnt lgkmcnt(0)\n\t"
#define ASM_XG_S_HI \
    "s_waitcnt lgkmcnt(0)\n\t" \
    "v_xor_b32 v252, 0x2000, %[xa]\n\t" \
    "ds_read_b128 v[224:227], v252 offset:16384\n\t" \
    "ds_read_b128 v[228:231], v252 offset:17408\n\t" \
    "ds_read_b128 v[244:247], %[wl] offset:4096\n\t" \
    "ds_read_b128 v[248:251], %[wl] offset:5120\n\t" \
    "ds_read_b128 v[232:235], v252 offset:0\n\t" \
    "ds_read_b128 v[236:239], v252 offset:1024\n\t" \
    "v_xor_b32 v252, 0x2800, %[xa]\n\t" \
    "ds_read_b128 v[240:243], v252 offset:0\n\t" \
    "s_cmp_eq_u32 %[slp], 0\n\t" \
    "s_cbranch_scc1 .Lxs%=\n\t" \
    "s_sleep 10\n\t" \
    ".Lxs%=:\n\t" \
    "s_waitcnt lgkmcnt(6)\n\t" \
    "v_mfma_f32_16x16x32_bf16 v[220:223], v[88:91], v[224:227], 0\n\t" \
    "s_waitcnt lgkmcnt(5)\n\t" \
    "v_mfma_f32_16x16x32_bf16 v[220:223], v[92:95], v[228:231], v[220:223]\n\t" \
    "v_mfma_f32_16x16x32_bf16 v[216:219], v[80:83], v[224:227], 0\n\t" \
    "v_mfma_f32_16x16x32_bf16 v[216:219], v[84:87], v[228:231], v[216:219]\n\t" \
    "s_waitcnt lgkmcnt(4)\n\t" \
    "v_mfma_f32_16x16x32_bf16 v[212:215], v[244:247], v[224:227], 0\n\t" \
    "ds_read_b128 v[224:227], v252 offset:1024\n\t" \
    "s_waitcnt lgkmcnt(4)\n\t" \
    "v_mfma_f32_16x16x32_bf16 v[212:215], v[248:251], v[228:231], v[212:215]\n\t" \
    "ds_read_b128 v[228:231], v252 offset:16384\n\t" \
    "s_waitcnt lgkmcnt(4)\n\t" \
    "v_mfma_f32_16x16x32_bf16 v[200:203], v[244:247], v[232:235], 0\n\t" \
    "ds_read_b128 v[244:247], %[wl] offset:6144\n\t" \
    "s_waitcnt lgkmcnt(4)\n\t" \
    "v_mfma_f32_16x16x32_bf16 v[200:203], v[248:251], v[236:239], v[200:203]\n\t" \
    "ds_read_b128 v[248:251], %[wl] offset:7168\n\t" \
    "v_mfma_f32_16x16x32_bf16 v[204:207], v[80:83], v[232:235], 0\n\t" \
    "v_mfma_f32_16x16x32_bf16 v[204:207], v[84:87], v[236:239], v[204:207]\n\t" \
    "v_mfma_f32_16x16x32_bf16 v[208:211], v[88:91], v[232:235], 0\n\t" \
    "ds_read_b128 v[232:235], v252 offset:17408\n\t" \
    "v_mfma_f32_16x16x32_bf16 v[208:211], v[92:95], v[236:239], v[208:211]\n\t" \
    "v_xor_b32 v252, 0x3000, %[xa]\n\t" \
    "ds_read_b128 v[236:239], v252 offset:16384\n\t" \
    "s_waitcnt lgkmcnt(6)\n\t" \
    "v_mfma_f32_16x16x32_bf16 v[208:211], v[112:115], v[240:243], v[208:211]\n\t" \
    "s_waitcnt lgkmcnt(5)\n\t" \
    "v_mfma_f32_16x16x32_bf16 v[208:211], v[116:119], v[224:227], v[208:211]\n\t" \
    "v_mfma_f32_16x16x32_bf16 v[204:207], v[104:107], v[240:243], v[204:207]\n\t" \
    "v_mfma_f32_16x16x32_bf16 v[204:207], v[108:111], v[224:227], v[204:207]\n\t" \
    "v_mfma_f32_16x16x32_bf16 v[200:203], v[96:99], v[240:243], v[200:203]\n\t" \
    "ds_read_b128 v[240:243], v252 offset:17408\n\t" \
    "v_mfma_f32_16x16x32_bf16 v[200:203], v[100:103], v[224:227], v[200:203]\n\t" \
    "ds_read_b128 v[224:227], v252 offset:0\n\t" \
    "s_waitcnt lgkmcnt(6)\n\t" \
    "v_mfma_f32_16x16x32_bf16 v[212:215], v[96:99], v[228:231], v[212:215]\n\t" \
    "s_waitcnt lgkmcnt(3)\n\t" \
    "v_mfma_f32_16x16x32_bf16 v[212:215], v[100:103], v[232:235], v[212:215]\n\t" \
    "v_mfma_f32_16x16x32_bf16 v[216:219], v[104:107], v[228:231], v[216:219]\n\t" \
    "v_mfma_f32_16x16x32_bf16 v[216:219], v[108:111], v[232:235], v[216:219]\n\t" \
    "v_mfma_f32_16x16x32_bf16 v[220:223], v[112:115], v[228:231], v[220:223]\n\t" \
    "ds_read_b128 v[228:231], v252 offset:1024\n\t" \
    "v_mfma_f32_16x16x32_bf16 v[220:223], v[116:119], v[232:235], v[220:223]\n\t" \
    "v_xor_b32 v252, 0x3800, %[xa]\n\t" \
    "ds_read_b128 v[232:235], v252 offset:0\n\t" \
    "s_waitcnt lgkmcnt(4)\n\t" \
    "v_mfma_f32_16x16x32_bf16 v[220:223], v[128:131], v[236:239], v[220:223]\n\t" \
    "s_waitcnt lgkmcnt(3)\n\t" \
    "v_mfma_f32_16x16x32_bf16 v[220:223], v[132:135], v[240:243], v[220:223]\n\t" \
    "v_mfma_f32_16x16x32_bf16 v[216:219], v[120:123], v[236:239], v[216:219]\n\t" \
    "v_mfma_f32_16x16x32_bf16 v[216:219], v[124:127], v[240:243], v[216:219]\n\t" \
    "v_mfma_f32_16x16x32_bf16 v[212:215], v[244:247], v[236:239], v[212:215]\n\t" \
    "ds_read_b128 v[236:239], v252 offset:1024\n\t" \
    "v_mfma_f32_16x16x32_bf16 v[212:215], v[248:251], v[240:243], v[212:215]\n\t" \
    "ds_read_b128 v[240:243], v252 offset:16384\n\t" \
    "s_waitcnt lgkmcnt(4)\n\t" \
    "v_mfma_f32_16x16x32_bf16 v[200:203], v[244:247], v[224:227], v[200:203]\n\t" \
    "ds_read_b128 v[244:247], %[wl] offset:8192\n\t" \
    "s_waitcnt lgkmcnt(4)\n\t" \
    "v_mfma_f32_16x16x32_bf16 v[200:203], v[248:251], v[228:231], v[200:203]\n\t" \
    "ds_read_b128 v[248:251], %[wl] offset:9216\n\t" \
    "v_mfma_f32_16x16x32_bf16 v[204:207], v[120:123], v[224:227], v[204:207]\n\t" \
    "v_mfma_f32_16x16x32_bf16 v[204:207], v[124:127], v[228:231], v[204:207]\n\t" \
    "v_mfma_f32_16x16x32_bf16 v[208:211], v[128:131], v[224:227], v[208:211]\n\t" \
    "ds_read_b128 v[224:227], v252 offset:17408\n\t" \
    "v_mfma_f32_16x16x32_bf16 v[208:211], v[132:135], v[228:231], v[208:211]\n\t" \
    "v_and_b32 v252, 0x3ff, %[xa]\n\t" \
    "ds_read_b128 v[228:231], v252 offset:45056\n\t" \
    "s_waitcnt lgkmcnt(6)\n\t" \
    "v_mfma_f32_16x16x32_bf16 v[208:211], v[152:155], v[232:235], v[208:211]\n\t" \
    "s_waitcnt lgkmcnt(5)\n\t" \
    "v_mfma_f32_16x16x32_bf16 v[208:211], v[156:159], v[236:239], v[208:211]\n\t" \
    "v_mfma_f32_16x16x32_bf16 v[204:207], v[144:147], v[232:235], v[204:207]\n\t" \
    "v_mfma_f32_16x16x32_bf16 v[204:207], v[148:151], v[236:239], v[204:207]\n\t" \
    "v_mfma_f32_16x16x32_bf16 v[200:203], v[136:139], v[232:235], v[200:203]\n\t" \
    "ds_read_b128 v[232:235], v252 offset:46080\n\t" \
    "v_mfma_f32_16x16x32_bf16 v[200:203], v[140:143], v[236:239], v[200:203]\n\t" \
    "ds_read_b128 v[236:239], v252 offset:40960\n\t" \
    "s_waitcnt lgkmcnt(6)\n\t" \
    "v_mfma_f32_16x16x32_bf16 v[212:215], v[136:139], v[240:243], v[212:215]\n\t" \
    "s_waitcnt lgkmcnt(3)\n\t" \
    "v_mfma_f32_16x16x32_bf16 v[212:215], v[140:143], v[224:227], v[212:215]\n\t" \
    "v_mfma_f32_16x16x32_bf16 v[216:219], v[144:147], v[240:243], v[216:219]\n\t" \
    "v_mfma_f32_16x16x32_bf16 v[216:219], v[148:151], v[224:227], v[216:219]\n\t" \
    "v_mfma_f32_16x16x32_bf16 v[220:223], v[152:155], v[240:243], v[220:223]\n\t" \
    "ds_read_b128 v[240:243], v252 offset:41984\n\t" \
    "v_mfma_f32_16x16x32_bf16 v[220:223], v[156:159], v[224:227], v[220:223]\n\t" \
    "s_waitcnt vmcnt(0) lgkmcnt(0)\n\t" \
    "s_barrier\n\t" ASM_ST \
    "s_mov_b32 m0, %[d0]\n\t" \
    "s_nop 0\n\t" \
    "global_load_lds_dwordx4 v252, %[g0] offset:0 sc1\n\t" \
    "global_load_lds_dwordx4 v252, %[g0] offset:1024 sc1\n\t" \
    "global_load_lds_dwordx4 v252, %[g0] offset:2048 sc1\n\t" \
    "global_load_lds_dwordx4 v252, %[g0] offset:3072 sc1\n\t" \
    "s_mov_b32 m0, %[d1]\n\t" \
    "s_nop 0\n\t" \
    "global_load_lds_dwordx4 v252, %[g1] sc1\n\t" \
    "ds_read_b128 v[224:227], %[xa] offset:0\n\t" \
    "v_mfma_f32_16x16x32_bf16 v[220:223], v[168:171], v[228:231], v[220:223]\n\t" \
    "v_mfma_f32_16x16x32_bf16 v[220:223], v[172:175], v[232:235], v[220:223]\n\t" \
    "v_mfma_f32_16x16x32_bf16 v[216:219], v[160:163], v[228:231], v[216:219]\n\t" \
    "v_mfma_f32_16x16x32_bf16 v[216:219], v[164:167], v[232:235], v[216:219]\n\t" \
    "v_mfma_f32_16x16x32_bf16 v[212:215], v[244:247], v[228:231], v[212:215]\n\t" \
    "ds_read_b128 v[228:231], %[xa] offset:1024\n\t" \
    "v_mfma_f32_16x16x32_bf16 v[212:215], v[248:251], v[232:235], v[212:215]\n\t" \
    "ds_read_b128 v[232:235], %[xa] offset:16384\n\t" \
    "v_mfma_f32_16x16x32_bf16 v[200:203], v[244:247], v[236:239], v[200:203]\n\t" \
    "ds_read_b128 v[244:247], %[wl] offset:0\n\t" \
    "v_mfma_f32_16x16x32_bf16 v[200:203], v[248:251], v[240:243], v[200:203]\n\t" \
    "ds_read_b128 v[248:251], %[wl] offset:1024\n\t" \
    "v_mfma_f32_16x16x32_bf16 v[204:207], v[160:163], v[236:239], v[204:207]\n\t" \
    "v_mfma_f32_16x16x32_bf16 v[204:207], v[164:167], v[240:243], v[204:207]\n\t" \
    "v_mfma_f32_16x16x32_bf16 v[208:211], v[168:171], v[236:239], v[208:211]\n\t" \
    "ds_read_b128 v[236:239], %[xa] offset:17408\n\t" \
    "v_mfma_f32_16x16x32_bf16 v[208:211], v[172:175], v[240:243], v[208:211]\n\t" \
    "v_xor_b32 v252, 0x800, %[xa]\n\t" \
    "ds_read_b128 v[240:243], v252 offset:16384\n\t" \
    "s_waitcnt lgkmcnt(6)\n\t" \
    "v_mfma_f32_16x16x32_bf16 v[208:211], v[8:11], v[224:227], v[208:211]\n\t" \
    "s_waitcnt lgkmcnt(5)\n\t" \
    "v_mfma_f32_16x16x32_bf16 v[208:211], v[12:15], v[228:231], v[208:211]\n\t" \
    "v_mfma_f32_16x16x32_bf16 v[204:207], v[0:3], v[224:227], v[204:207]\n\t" \
    "v_mfma_f32_16x16x32_bf16 v[204:207], v[4:7], v[228:231], v[204:207]\n\t" \
    "s_waitcnt lgkmcnt(3)\n\t" \
    "v_mfma_f32_16x16x32_bf16 v[200:203], v[244:247], v[224:227], v[200:203]\n\t" \
    "ds_read_b128 v[224:227], v252 offset:17408\n\t" \
    "s_waitcnt lgkmcnt(3)\n\t" \
    "v_mfma_f32_16x16x32_bf16 v[200:203], v[248:251], v[228:231], v[200:203]\n\t" \
    "ds_read_b128 v[228:231], v252 offset:0\n\t" \
    "v_mfma_f32_16x16x32_bf16 v[212:215], v[244:247], v[232:235], v[212:215]\n\t" \
    "ds_read_b128 v[244:247], %[wl] offset:2048\n\t" \
    "s_waitcnt lgkmcnt(4)\n\t" \
    "v_mfma_f32_16x16x32_bf16 v[212:215], v[248:251], v[236:239], v[212:215]\n\t" \
    "ds_read_b128 v[248:251], %[wl] offset:3072\n\t" \
    "v_mfma_f32_16x16x32_bf16 v[216:219], v[0:3], v[232:235], v[216:219]\n\t" \
    "v_mfma_f32_16x16x32_bf16 v[216:219], v[4:7], v[236:239], v[216:219]\n\t" \
    "v_mfma_f32_16x16x32_bf16 v[220:223], v[8:11], v[232:235], v[220:223]\n\t" \
    "ds_read_b128 v[232:235], v252 offset:1024\n\t" \
    "v_mfma_f32_16x16x32_bf16 v[220:223], v[12:15], v[236:239], v[220:223]\n\t" \
    "v_xor_b32 v252, 0x1000, %[xa]\n\t" \
    "ds_read_b128 v[236:239], v252 offset:0\n\t" \
    "s_waitcnt lgkmcnt(6)\n\t" \
    "v_mfma_f32_16x16x32_bf16 v[220:223], v[32:35], v[240:243], v[220:223]\n\t" \
    "s_waitcnt lgkmcnt(5)\n\t" \
    "v_mfma_f32_16x16x32_bf16 v[220:223], v[36:39], v[224:227], v[220:223]\n\t" \
    "v_mfma_f32_16x16x32_bf16 v[216:219], v[24:27], v[240:243], v[216:219]\n\t" \
    "v_mfma_f32_16x16x32_bf16 v[216:219], v[28:31], v[224:227], v[216:219]\n\t" \
    "v_mfma_f32_16x16x32_bf16 v[212:215], v[16:19], v[240:243], v[212:215]\n\t" \
    "ds_read_b128 v[240:243], v252 offset:1024\n\t" \
    "v_mfma_f32_16x16x32_bf16 v[212:215], v[20:23], v[224:227], v[212:215]\n\t" \
    "ds_read_b128 v[224:227], v252 offset:16384\n\t" \
    "s_waitcnt lgkmcnt(6)\n\t" \
    "v_mfma_f32_16x16x32_bf16 v[200:203], v[16:19], v[228:231], v[200:203]\n\t" \
    "s_waitcnt lgkmcnt(3)\n\t" \
    "v_mfma_f32_16x16x32_bf16 v[200:203], v[20:23], v[232:235], v[200:203]\n\t" \
    "v_mfma_f32_16x16x32_bf16 v[204:207], v[24:27], v[228:231], v[204:207]\n\t" \
    "v_mfma_f32_16x16x32_bf16 v[204:207], v[28:31], v[232:235], v[204:207]\n\t" \
    "v_mfma_f32_16x16x32_bf16 v[208:211], v[32:35], v[228:231], v[208:211]\n\t" \
    "ds_read_b128 v[228:231], v252 offset:17408\n\t" \
    "v_mfma_f32_16x16x32_bf16 v[208:211], v[36:39], v[232:235], v[208:211]\n\t" \
    "v_xor_b32 v252, 0x1800, %[xa]\n\t" \
    "ds_read_b128 v[232:235], v252 offset:16384\n\t" \
    "s_waitcnt lgkmcnt(4)\n\t" \
    "v_mfma_f32_16x16x32_bf16 v[208:211], v[48:51], v[236:239], v[208:211]\n\t" \
    "s_waitcnt lgkmcnt(3)\n\t" \
    "v_mfma_f32_16x16x32_bf16 v[208:211], v[52:55], v[240:243], v[208:211]\n\t" \
    "v_mfma_f32_16x16x32_bf16 v[204:207], v[40:43], v[236:239], v[204:207]\n\t" \
    "v_mfma_f32_16x16x32_bf16 v[204:207], v[44:47], v[240:243], v[204:207]\n\t" \
    "v_mfma_f32_16x16x32_bf16 v[200:203], v[244:247], v[236:239], v[200:203]\n\t" \
    "ds_read_b128 v[236:239], v252 offset:17408\n\t" \
    "v_mfma_f32_16x16x32_bf16 v[200:203], v[248:251], v[240:243], v[200:203]\n\t" \
    "ds_read_b128 v[240:243], v252 offset:0\n\t" \
    "s_waitcnt lgkmcnt(4)\n\t" \
    "v_mfma_f32_16x16x32_bf16 v[212:215], v[244:247], v[224:227], v[212:215]\n\t" \
    "s_waitcnt lgkmcnt(3)\n\t" \
    "v_mfma_f32_16x16x32_bf16 v[212:215], v[248:251], v[228:231], v[212:215]\n\t" \
    "v_mfma_f32_16x16x32_bf16 v[216:219], v[40:43], v[224:227], v[216:219]\n\t" \
    "v_mfma_f32_16x16x32_bf16 v[216:219], v[44:47], v[228:231], v[216:219]\n\t" \
    "v_mfma_f32_16x16x32_bf16 v[220:223], v[48:51], v[224:227], v[220:223]\n\t" \
    "ds_read_b128 v[224:227], v252 offset:1024\n\t" \
    "v_mfma_f32_16x16x32_bf16 v[220:223], v[52:55], v[228:231], v[220:223]\n\t" \
    "v_and_b32 v252, 0x3ff, %[xa]\n\t" \
    "ds_read_b128 v[228:231], v252 offset:43008\n\t" \
    "s_waitcnt lgkmcnt(4)\n\t" \
    "v_mfma_f32_16x16x32_bf16 v[220:223], v[72:75], v[232:235], v[220:223]\n\t" \
    "v_mov_b64 v[244:245], 0\n\t" \
    "s_waitcnt lgkmcnt(3)\n\t" \
    "v_mfma_f32_16x16x32_bf16 v[220:223], v[76:79], v[236:239], v[220:223]\n\t" \
    "v_mov_b64 v[246:247], 0\n\t" \
    "v_mfma_f32_16x16x32_bf16 v[216:219], v[64:67], v[232:235], v[216:219]\n\t" \
    "v_mov_b64 v[248:249], 0\n\t" \
    "v_mfma_f32_16x16x32_bf16 v[216:219], v[68:71], v[236:239], v[216:219]\n\t" \
    "v_mov_b64 v[250:251], 0\n\t" \
    "v_mfma_f32_16x16x32_bf16 v[212:215], v[56:59], v[232:235], v[212:215]\n\t" \
    "ds_read_b128 v[232:235], v252 offset:44032\n\t" \
    "v_mfma_f32_16x16x32_bf16 v[212:215], v[60:63], v[236:239], v[212:215]\n\t" \
    "ds_read_b128 v[236:239], v252 offset:47104\n\t" \
    "s_waitcnt lgkmcnt(4)\n\t" \
    "v_mfma_f32_16x16x32_bf16 v[200:203], v[56:59], v[240:243], v[200:203]\n\t" \
    "s_waitcnt lgkmcnt(3)\n\t" \
    "v_mfma_f32_16x16x32_bf16 v[200:203], v[60:63], v[224:227], v[200:203]\n\t" \
    "v_mfma_f32_16x16x32_bf16 v[204:207], v[64:67], v[240:243], v[204:207]\n\t" \
    "v_mfma_f32_16x16x32_bf16 v[204:207], v[68:71], v[224:227], v[204:207]\n\t" \
    "v_mfma_f32_16x16x32_bf16 v[208:211], v[72:75], v[240:243], v[208:211]\n\t" \
    "ds_read_b128 v[240:243], v252 offset:48128\n\t" \
    "v_mfma_f32_16x16x32_bf16 v[208:211], v[76:79], v[224:227], v[208:211]\n\t" \
    "s_waitcnt lgkmcnt(3)\n\t" \
    "v_mfma_f32_16x16x32_bf16 v[208:211], v[192:195], v[228:231], v[208:211]\n\t" \
    "v_add_u32 v252, %[qs], v252\n\t" \
    "v_mov_b64 v[224:225], 0\n\t" \
    "s_waitcnt lgkmcnt(2)\n\t" \
    "v_mfma_f32_16x16x32_bf16 v[208:211], v[196:199], v[232:235], v[208:211]\n\t" \
    "v_mov_b64 v[226:227], 0\n\t" \
    "v_mfma_f32_16x16x32_bf16 v[204:207], v[184:187], v[228:231], v[204:207]\n\t" \
    "v_mfma_f32_16x16x32_bf16 v[204:207], v[188:191], v[232:235], v[204:207]\n\t" \
    "v_mfma_f32_16x16x32_bf16 v[200:203], v[176:179], v[228:231], v[200:203]\n\t" \
    "v_mfma_f32_16x16x32_bf16 v[200:203], v[180:183], v[232:235], v[200:203]\n\t" \
    "s_waitcnt lgkmcnt(1)\n\t" \
    "v_mfma_f32_16x16x32_bf16 v[212:215], v[176:179], v[236:239], v[212:215]\n\t" \
    "ds_read_b128 v[228:231], %[wl] offset:2048\n\t" \
    "ds_read_b128 v[232:235], %[wl] offset:3072\n\t" \
    "s_waitcnt lgkmcnt(2)\n\t" \
    "v_mfma_f32_16x16x32_bf16 v[212:215], v[180:183], v[240:243], v[212:215]\n\t" \
    "v_cvt_pk_bf16_f32 v208, v208, v209\n\t" \
    "v_cvt_pk_bf16_f32 v209, v210, v211\n\t" \
    "v_mfma_f32_16x16x32_bf16 v[216:219], v[184:187], v[236:239], v[216:219]\n\t" \
    "ds_write_b64 v252, v[208:209] offset:2048\n\t" \
    "v_cvt_pk_bf16_f32 v204, v204, v205\n\t" \
    "v_mfma_f32_16x16x32_bf16 v[216:219], v[188:191], v[240:243], v[216:219]\n\t" \
    "v_cvt_pk_bf16_f32 v205, v206, v207\n\t" \
    "ds_write_b64 v252, v[204:205] offset:1024\n\t" \
    "v_mfma_f32_16x16x32_bf16 v[220:223], v[192:195], v[236:239], v[220:223]\n\t" \
    "v_cvt_pk_bf16_f32 v200, v200, v201\n\t" \
    "v_cvt_pk_bf16_f32 v201, v202, v203\n\t" \
    "v_mfma_f32_16x16x32_bf16 v[220:223], v[196:199], v[240:243], v[220:223]\n\t" \
    "ds_write_b64 v252, v[200:201] offset:0\n\t" \
    "ds_read_b128 v[200:203], %[wl] offset:0\n\t" \
    "ds_read_b128 v[204:207], %[wl] offset:1024\n\t" \
    "v_cvt_pk_bf16_f32 v212, v212, v213\n\t" \
    "v_cvt_pk_bf16_f32 v213, v214, v215\n\t" \
    "ds_write_b64 v252, v[212:213] offset:6144\n\t" \
    "v_cvt_pk_bf16_f32 v216, v216, v217\n\t" \
    "v_cvt_pk_bf16_f32 v217, v218, v219\n\t" \
    "ds_write_b64 v252, v[216:217] offset:7168\n\t" \
    "v_cvt_pk_bf16_f32 v220, v220, v221\n\t" \
    "v_cvt_pk_bf16_f32 v221, v222, v223\n\t" \
    "ds_write_b64 v252, v[220:221] offset:8192\n\t" \
    "s_waitcnt lgkmcnt(0)\n\t"
#define ASM_XG_S_LO2 \
    "s_waitcnt lgkmcnt(0)\n\t" \
    "ds_read_b128 v[224:227], %[xa] offset:16384\n\t" \
    "ds_read_b128 v[228:231], %[xa] offset:17408\n\t" \
    "v_xor_b32 v252, 0x800, %[xa]\n\t" \
    "ds_read_b128 v[232:235], v252 offset:16384\n\t" \
    "ds_read_b128 v[236:239], v252 offset:17408\n\t" \
    "v_xor_b32 v252, 0x1000, %[xa]\n\t" \
    "ds_read_b128 v[240:243], v252 offset:16384\n\t" \
    "ds_read_b128 v[212:215], v252 offset:17408\n\t" \
    "v_xor_b32 v252, 0x1800, %[xa]\n\t" \
    "ds_read_b128 v[200:203], v252 offset:16384\n\t" \
    "s_waitcnt lgkmcnt(6)\n\t" \
    "v_mfma_f32_16x16x32_bf16 v[220:223], v[8:11], v[224:227], 0\n\t" \
    "s_waitcnt lgkmcnt(5)\n\t" \
    "v_mfma_f32_16x16x32_bf16 v[220:223], v[12:15], v[228:231], v[220:223]\n\t" \
    "v_mfma_f32_16x16x32_bf16 v[216:219], v[0:3], v[224:227], 0\n\t" \
    "ds_read_b128 v[224:227], v252 offset:17408\n\t" \
    "v_mfma_f32_16x16x32_bf16 v[216:219], v[4:7], v[228:231], v[216:219]\n\t" \
    "v_and_b32 v252, 0x3ff, %[xa]\n\t" \
    "ds_read_b128 v[228:231], v252 offset:45056\n\t" \
    "s_waitcnt lgkmcnt(6)\n\t" \
    "v_mfma_f32_16x16x32_bf16 v[216:219], v[24:27], v[232:235], v[216:219]\n\t" \
    "s_waitcnt lgkmcnt(5)\n\t" \
    "v_mfma_f32_16x16x32_bf16 v[216:219], v[28:31], v[236:239], v[216:219]\n\t" \
    "v_mfma_f32_16x16x32_bf16 v[220:223], v[32:35], v[232:235], v[220:223]\n\t" \
    "ds_read_b128 v[232:235], v252 offset:46080\n\t" \
    "v_mfma_f32_16x16x32_bf16 v[220:223], v[36:39], v[236:239], v[220:223]\n\t" \
    "ds_read_b128 v[236:239], %[xa] offset:0\n\t" \
    "s_waitcnt lgkmcnt(6)\n\t" \
    "v_mfma_f32_16x16x32_bf16 v[220:223], v[48:51], v[240:243], v[220:223]\n\t" \
    "s_waitcnt lgkmcnt(5)\n\t" \
    "v_mfma_f32_16x16x32_bf16 v[220:223], v[52:55], v[212:215], v[220:223]\n\t" \
    "v_mfma_f32_16x16x32_bf16 v[216:219], v[40:43], v[240:243], v[216:219]\n\t" \
    "ds_read_b128 v[240:243], %[xa] offset:1024\n\t" \
    "v_mfma_f32_16x16x32_bf16 v[216:219], v[44:47], v[212:215], v[216:219]\n\t" \
    "v_xor_b32 v252, 0x800, %[xa]\n\t" \
    "ds_read_b128 v[212:215], v252 offset:0\n\t" \
    "s_waitcnt lgkmcnt(6)\n\t" \
    "v_mfma_f32_16x16x32_bf16 v[216:219], v[64:67], v[200:203], v[216:219]\n\t" \
    "s_waitcnt lgkmcnt(5)\n\t" \
    "v_mfma_f32_16x16x32_bf16 v[216:219], v[68:71], v[224:227], v[216:219]\n\t" \
    "v_mfma_f32_16x16x32_bf16 v[220:223], v[72:75], v[200:203], v[220:223]\n\t" \
    "ds_read_b128 v[200:203], v252 offset:1024\n\t" \
    "v_mfma_f32_16x16x32_bf16 v[220:223], v[76:79], v[224:227], v[220:223]\n\t" \
    "v_xor_b32 v252, 0x1000, %[xa]\n\t" \
    "ds_read_b128 v[224:227], v252 offset:0\n\t" \
    "s_waitcnt lgkmcnt(6)\n\t" \
    "v_mfma_f32_16x16x32_bf16 v[220:223], v[168:171], v[228:231], v[220:223]\n\t" \
    "s_waitcnt lgkmcnt(5)\n\t" \
    "v_mfma_f32_16x16x32_bf16 v[220:223], v[172:175], v[232:235], v[220:223]\n\t" \
    "v_mfma_f32_16x16x32_bf16 v[216:219], v[160:163], v[228:231], v[216:219]\n\t" \
    "ds_read_b128 v[228:231], v252 offset:1024\n\t" \
    "v_mfma_f32_16x16x32_bf16 v[216:219], v[164:167], v[232:235], v[216:219]\n\t" \
    "v_xor_b32 v252, 0x1800, %[xa]\n\t" \
    "ds_read_b128 v[232:235], v252 offset:0\n\t" \
    "s_waitcnt lgkmcnt(6)\n\t" \
    "v_mfma_f32_16x16x32_bf16 v[204:207], v[0:3], v[236:239], 0\n\t" \
    "s_waitcnt lgkmcnt(5)\n\t" \
    "v_mfma_f32_16x16x32_bf16 v[204:207], v[4:7], v[240:243], v[204:207]\n\t" \
    "v_mfma_f32_16x16x32_bf16 v[208:211], v[8:11], v[236:239], 0\n\t" \
    "ds_read_b128 v[236:239], v252 offset:1024\n\t" \
    "v_mfma_f32_16x16x32_bf16 v[208:211], v[12:15], v[240:243], v[208:211]\n\t" \
    "v_and_b32 v252, 0x3ff, %[xa]\n\t" \
    "ds_read_b128 v[240:243], v252 offset:40960\n\t" \
    "s_waitcnt lgkmcnt(6)\n\t" \
    "v_mfma_f32_16x16x32_bf16 v[208:211], v[32:35], v[212:215], v[208:211]\n\t" \
    "s_waitcnt lgkmcnt(5)\n\t" \
    "v_mfma_f32_16x16x32_bf16 v[208:211], v[36:39], v[200:203], v[208:211]\n\t" \
    "v_mfma_f32_16x16x32_bf16 v[204:207], v[24:27], v[212:215], v[204:207]\n\t" \
    "ds_read_b128 v[212:215], v252 offset:41984\n\t" \
    "v_mfma_f32_16x16x32_bf16 v[204:207], v[28:31], v[200:203], v[204:207]\n\t" \
    "s_waitcnt lgkmcnt(0)\n\t" \
    "s_barrier\n\t" ASM_ST \
    "v_xor_b32 v252, 0x2000, %[xa]\n\t" \
    "ds_read_b128 v[200:203], v252 offset:0\n\t" \
    "v_mfma_f32_16x16x32_bf16 v[204:207], v[40:43], v[224:227], v[204:207]\n\t" \
    "v_mfma_f32_16x16x32_bf16 v[204:207], v[44:47], v[228:231], v[204:207]\n\t" \
    "v_mfma_f32_16x16x32_bf16 v[208:211], v[48:51], v[224:227], v[208:211]\n\t" \
    "ds_read_b128 v[224:227], v252 offset:1024\n\t" \
    "v_mfma_f32_16x16x32_bf16 v[208:211], v[52:55], v[228:231], v[208:211]\n\t" \
    "v_xor_b32 v252, 0x2800, %[xa]\n\t" \
    "ds_read_b128 v[228:231], v252 offset:0\n\t" \
    "v_mfma_f32_16x16x32_bf16 v[208:211], v[72:75], v[232:235], v[208:211]\n\t" \
    "v_mfma_f32_16x16x32_bf16 v[208:211], v[76:79], v[236:239], v[208:211]\n\t" \
    "v_mfma_f32_16x16x32_bf16 v[204:207], v[64:67], v[232:235], v[204:207]\n\t" \
    "ds_read_b128 v[232:235], v252 offset:1024\n\t" \
    "v_mfma_f32_16x16x32_bf16 v[204:207], v[68:71], v[236:239], v[204:207]\n\t" \
    "v_xor_b32 v252, 0x3000, %[xa]\n\t" \
    "ds_read_b128 v[236:239], v252 offset:0\n\t" \
    "v_mfma_f32_16x16x32_bf16 v[204:207], v[160:163], v[240:243], v[204:207]\n\t" \
    "v_mfma_f32_16x16x32_bf16 v[204:207], v[164:167], v[212:215], v[204:207]\n\t" \
    "v_mfma_f32_16x16x32_bf16 v[208:211], v[168:171], v[240:243], v[208:211]\n\t" \
    "ds_read_b128 v[240:243], v252 offset:1024\n\t" \
    "v_mfma_f32_16x16x32_bf16 v[208:211], v[172:175], v[212:215], v[208:211]\n\t" \
    "v_xor_b32 v252, 0x3800, %[xa]\n\t" \
    "ds_read_b128 v[212:215], v252 offset:0\n\t" \
    "s_waitcnt lgkmcnt(6)\n\t" \
    "v_mfma_f32_16x16x32_bf16 v[208:211], v[88:91], v[200:203], v[208:211]\n\t" \
    "s_waitcnt lgkmcnt(5)\n\t" \
    "v_mfma_f32_16x16x32_bf16 v[208:211], v[92:95], v[224:227], v[208:211]\n\t" \
    "v_mfma_f32_16x16x32_bf16 v[204:207], v[80:83], v[200:203], v[204:207]\n\t" \
    "ds_read_b128 v[200:203], v252 offset:1024\n\t" \
    "v_mfma_f32_16x16x32_bf16 v[204:207], v[84:87], v[224:227], v[204:207]\n\t" \
    "v_and_b32 v252, 0x3ff, %[xa]\n\t" \
    "ds_read_b128 v[224:227], v252 offset:43008\n\t" \
    "s_waitcnt lgkmcnt(6)\n\t" \
    "v_mfma_f32_16x16x32_bf16 v[204:207], v[104:107], v[228:231], v[204:207]\n\t" \
    "s_waitcnt lgkmcnt(5)\n\t" \
    "v_mfma_f32_16x16x32_bf16 v[204:207], v[108:111], v[232:235], v[204:207]\n\t" \
    "v_mfma_f32_16x16x32_bf16 v[208:211], v[112:115], v[228:231], v[208:211]\n\t" \
    "ds_read_b128 v[228:231], v252 offset:44032\n\t" \
    "v_mfma_f32_16x16x32_bf16 v[208:211], v[116:119], v[232:235], v[208:211]\n\t" \
    "v_xor_b32 v252, 0x2000, %[xa]\n\t" \
    "ds_read_b128 v[232:235], v252 offset:16384\n\t" \
    "s_waitcnt lgkmcnt(6)\n\t" \
    "v_mfma_f32_16x16x32_bf16 v[208:211], v[128:131], v[236:239], v[208:211]\n\t" \
    "s_waitcnt lgkmcnt(5)\n\t" \
    "v_mfma_f32_16x16x32_bf16 v[208:211], v[132:135], v[240:243], v[208:211]\n\t" \
    "v_mfma_f32_16x16x32_bf16 v[204:207], v[120:123], v[236:239], v[204:207]\n\t" \
    "ds_read_b128 v[236:239], v252 offset:17408\n\t" \
    "v_mfma_f32_16x16x32_bf16 v[204:207], v[124:127], v[240:243], v[204:207]\n\t" \
    "v_xor_b32 v252, 0x2800, %[xa]\n\t" \
    "ds_read_b128 v[240:243], v252 offset:16384\n\t" \
    "s_waitcnt lgkmcnt(6)\n\t" \
    "v_mfma_f32_16x16x32_bf16 v[204:207], v[144:147], v[212:215], v[204:207]\n\t" \
    "s_waitcnt lgkmcnt(5)\n\t" \
    "v_mfma_f32_16x16x32_bf16 v[204:207], v[148:151], v[200:203], v[204:207]\n\t" \
    "v_mfma_f32_16x16x32_bf16 v[208:211], v[152:155], v[212:215], v[208:211]\n\t" \
    "ds_read_b128 v[212:215], v252 offset:17408\n\t" \
    "v_mfma_f32_16x16x32_bf16 v[208:211], v[156:159], v[200:203], v[208:211]\n\t" \
    "v_xor_b32 v252, 0x3000, %[xa]\n\t" \
    "ds_read_b128 v[200:203], v252 offset:16384\n\t" \
    "s_waitcnt lgkmcnt(6)\n\t" \
    "v_mfma_f32_16x16x32_bf16 v[208:211], v[192:195], v[224:227], v[208:211]\n\t" \
    "s_waitcnt lgkmcnt(5)\n\t" \
    "v_mfma_f32_16x16x32_bf16 v[208:211], v[196:199], v[228:231], v[208:211]\n\t" \
    "v_mfma_f32_16x16x32_bf16 v[204:207], v[184:187], v[224:227], v[204:207]\n\t" \
    "ds_read_b128 v[224:227], v252 offset:17408\n\t" \
    "v_mfma_f32_16x16x32_bf16 v[204:207], v[188:191], v[228:231], v[204:207]\n\t" \
    "v_xor_b32 v252, 0x3800, %[xa]\n\t" \
    "ds_read_b128 v[228:231], v252 offset:16384\n\t" \
    "s_waitcnt lgkmcnt(6)\n\t" \
    "v_mfma_f32_16x16x32_bf16 v[216:219], v[80:83], v[232:235], v[216:219]\n\t" \
    "s_waitcnt lgkmcnt(5)\n\t" \
    "v_mfma_f32_16x16x32_bf16 v[216:219], v[84:87], v[236:239], v[216:219]\n\t" \
    "v_mfma_f32_16x16x32_bf16 v[220:223], v[88:91], v[232:235], v[220:223]\n\t" \
    "ds_read_b128 v[232:235], v252 offset:17408\n\t" \
    "v_mfma_f32_16x16x32_bf16 v[220:223], v[92:95], v[236:239], v[220:223]\n\t" \
    "v_and_b32 v252, 0x3ff, %[xa]\n\t" \
    "ds_read_b128 v[236:239], v252 offset:47104\n\t" \
    "s_waitcnt lgkmcnt(6)\n\t" \
    "v_mfma_f32_16x16x32_bf16 v[220:223], v[112:115], v[240:243], v[220:223]\n\t" \
    "v_mov_b64 v[244:245], 0\n\t" \
    "s_waitcnt lgkmcnt(5)\n\t" \
    "v_mfma_f32_16x16x32_bf16 v[220:223], v[116:119], v[212:215], v[220:223]\n\t" \
    "v_mov_b64 v[246:247], 0\n\t" \
    "v_mfma_f32_16x16x32_bf16 v[216:219], v[104:107], v[240:243], v[216:219]\n\t" \
    "ds_read_b128 v[240:243], v252 offset:48128\n\t" \
    "v_mov_b64 v[248:249], 0\n\t" \
    "v_mfma_f32_16x16x32_bf16 v[216:219], v[108:111], v[212:215], v[216:219]\n\t" \
    "v_mov_b64 v[250:251], 0\n\t" \
    "s_waitcnt lgkmcnt(5)\n\t" \
    "v_mfma_f32_16x16x32_bf16 v[216:219], v[120:123], v[200:203], v[216:219]\n\t" \
    "s_waitcnt lgkmcnt(4)\n\t" \
    "v_mfma_f32_16x16x32_bf16 v[216:219], v[124:127], v[224:227], v[216:219]\n\t" \
    "v_mfma_f32_16x16x32_bf16 v[220:223], v[128:131], v[200:203], v[220:223]\n\t" \
    "v_mfma_f32_16x16x32_bf16 v[220:223], v[132:135], v[224:227], v[220:223]\n\t" \
    "s_waitcnt lgkmcnt(3)\n\t" \
    "v_mfma_f32_16x16x32_bf16 v[220:223], v[152:155], v[228:231], v[220:223]\n\t" \
    "v_add_u32 v252, %[qs], v252\n\t" \
    "v_mov_b64 v[224:225], 0\n\t" \
    "s_waitcnt lgkmcnt(2)\n\t" \
    "v_mfma_f32_16x16x32_bf16 v[220:223], v[156:159], v[232:235], v[220:223]\n\t" \
    "v_mov_b64 v[226:227], 0\n\t" \
    "v_cvt_pk_bf16_f32 v208, v208, v209\n\t" \
    "v_mfma_f32_16x16x32_bf16 v[216:219], v[144:147], v[228:231], v[216:219]\n\t" \
    "v_cvt_pk_bf16_f32 v209, v210, v211\n\t" \
    "ds_write_b64 v252, v[208:209] offset:2048\n\t" \
    "v_mfma_f32_16x16x32_bf16 v[216:219], v[148:151], v[232:235], v[216:219]\n\t" \
    "v_cvt_pk_bf16_f32 v204, v204, v205\n\t" \
    "v_cvt_pk_bf16_f32 v205, v206, v207\n\t" \
    "s_waitcnt lgkmcnt(2)\n\t" \
    "v_mfma_f32_16x16x32_bf16 v[216:219], v[184:187], v[236:239], v[216:219]\n\t" \
    "ds_write_b64 v252, v[204:205] offset:1024\n\t" \
    "ds_read_b128 v[228:231], %[wl] offset:2048\n\t" \
    "s_waitcnt lgkmcnt(3)\n\t" \
    "v_mfma_f32_16x16x32_bf16 v[216:219], v[188:191], v[240:243], v[216:219]\n\t" \
    "ds_read_b128 v[232:235], %[wl] offset:3072\n\t" \
    "v_mfma_f32_16x16x32_bf16 v[220:223], v[192:195], v[236:239], v[220:223]\n\t" \
    "v_mfma_f32_16x16x32_bf16 v[220:223], v[196:199], v[240:243], v[220:223]\n\t" \
    "ds_read_b128 v[200:203], %[wl] offset:0\n\t" \
    "ds_read_b128 v[204:207], %[wl] offset:1024\n\t" \
    "v_cvt_pk_bf16_f32 v216, v216, v217\n\t" \
    "v_cvt_pk_bf16_f32 v217, v218, v219\n\t" \
    "ds_write_b64 v252, v[216:217] offset:7168\n\t" \
    "s_nop 2\n\t" \
    "v_cvt_pk_bf16_f32 v220, v220, v221\n\t" \
    "v_cvt_pk_bf16_f32 v221, v222, v223\n\t" \
    "ds_write_b64 v252, v[220:221] offset:8192\n\t" \
    "s_waitcnt lgkmcnt(0)\n\t"
#define ASM_XG_S_LO1 \
    "s_waitcnt lgkmcnt(0)\n\t" \
    "ds_read_b128 v[224:227], %[xa] offset:16384\n\t" \
    "ds_read_b128 v[228:231], %[xa] offset:17408\n\t" \
    "v_xor_b32 v252, 0x800, %[xa]\n\t" \
    "ds_read_b128 v[232:235], v252 offset:16384\n\t" \
    "ds_read_b128 v[236:239], v252 offset:17408\n\t" \
    "v_xor_b32 v252, 0x1000, %[xa]\n\t" \
    "ds_read_b128 v[240:243], v252 offset:16384\n\t" \
    "ds_read_b128 v[212:215], v252 offset:17408\n\t" \
    "v_xor_b32 v252, 0x1800, %[xa]\n\t" \
    "ds_read_b128 v[200:203], v252 offset:16384\n\t" \
    "s_waitcnt lgkmcnt(6)\n\t" \
    "v_mfma_f32_16x16x32_bf16 v[220:223], v[8:11], v[224:227], 0\n\t" \
    "ds_read_b128 v[224:227], v252 offset:17408\n\t" \
    "s_waitcnt lgkmcnt(6)\n\t" \
    "v_mfma_f32_16x16x32_bf16 v[220:223], v[12:15], v[228:231], v[220:223]\n\t" \
    "v_and_b32 v252, 0x3ff, %[xa]\n\t" \
    "ds_read_b128 v[228:231], v252 offset:45056\n\t" \
    "s_waitcnt lgkmcnt(6)\n\t" \
    "v_mfma_f32_16x16x32_bf16 v[220:223], v[32:35], v[232:235], v[220:223]\n\t" \
    "ds_read_b128 v[232:235], v252 offset:46080\n\t" \
    "s_waitcnt lgkmcnt(6)\n\t" \
    "v_mfma_f32_16x16x32_bf16 v[220:223], v[36:39], v[236:239], v[220:223]\n\t" \
    "ds_read_b128 v[236:239], %[xa] offset:0\n\t" \
    "s_waitcnt lgkmcnt(6)\n\t" \
    "v_mfma_f32_16x16x32_bf16 v[220:223], v[48:51], v[240:243], v[220:223]\n\t" \
    "ds_read_b128 v[240:243], %[xa] offset:1024\n\t" \
    "s_waitcnt lgkmcnt(6)\n\t" \
    "v_mfma_f32_16x16x32_bf16 v[220:223], v[52:55], v[212:215], v[220:223]\n\t" \
    "v_xor_b32 v252, 0x800, %[xa]\n\t" \
    "ds_read_b128 v[212:215], v252 offset:0\n\t" \
    "s_waitcnt lgkmcnt(6)\n\t" \
    "v_mfma_f32_16x16x32_bf16 v[220:223], v[72:75], v[200:203], v[220:223]\n\t" \
    "ds_read_b128 v[200:203], v252 offset:1024\n\t" \
    "s_waitcnt lgkmcnt(6)\n\t" \
    "v_mfma_f32_16x16x32_bf16 v[220:223], v[76:79], v[224:227], v[220:223]\n\t" \
    "v_xor_b32 v252, 0x1000, %[xa]\n\t" \
    "ds_read_b128 v[224:227], v252 offset:0\n\t" \
    "s_waitcnt lgkmcnt(6)\n\t" \
    "v_mfma_f32_16x16x32_bf16 v[220:223], v[168:171], v[228:231], v[220:223]\n\t" \
    "ds_read_b128 v[228:231], v252 offset:1024\n\t" \
    "s_waitcnt lgkmcnt(6)\n\t" \
    "v_mfma_f32_16x16x32_bf16 v[220:223], v[172:175], v[232:235], v[220:223]\n\t" \
    "v_xor_b32 v252, 0x1800, %[xa]\n\t" \
    "ds_read_b128 v[232:235], v252 offset:0\n\t" \
    "s_waitcnt lgkmcnt(6)\n\t" \
    "v_mfma_f32_16x16x32_bf16 v[208:211], v[8:11], v[236:239], 0\n\t" \
    "ds_read_b128 v[236:239], v252 offset:1024\n\t" \
    "s_waitcnt lgkmcnt(6)\n\t" \
    "v_mfma_f32_16x16x32_bf16 v[208:211], v[12:15], v[240:243], v[208:211]\n\t" \
    "v_and_b32 v252, 0x3ff, %[xa]\n\t" \
    "ds_read_b128 v[240:243], v252 offset:40960\n\t" \
    "s_waitcnt lgkmcnt(6)\n\t" \
    "v_mfma_f32_16x16x32_bf16 v[208:211], v[32:35], v[212:215], v[208:211]\n\t" \
    "ds_read_b128 v[212:215], v252 offset:41984\n\t" \
    "s_waitcnt lgkmcnt(6)\n\t" \
    "v_mfma_f32_16x16x32_bf16 v[208:211], v[36:39], v[200:203], v[208:211]\n\t" \
    "s_waitcnt lgkmcnt(0)\n\t" \
    "s_barrier\n\t" ASM_ST \
    "v_xor_b32 v252, 0x2000, %[xa]\n\t" \
    "ds_read_b128 v[200:203], v252 offset:0\n\t" \
    "v_mfma_f32_16x16x32_bf16 v[208:211], v[48:51], v[224:227], v[208:211]\n\t" \
    "ds_read_b128 v[224:227], v252 offset:1024\n\t" \
    "v_mfma_f32_16x16x32_bf16 v[208:211], v[52:55], v[228:231], v[208:211]\n\t" \
    "v_xor_b32 v252, 0x2800, %[xa]\n\t" \
    "ds_read_b128 v[228:231], v252 offset:0\n\t" \
    "v_mfma_f32_16x16x32_bf16 v[208:211], v[72:75], v[232:235], v[208:211]\n\t" \
    "ds_read_b128 v[232:235], v252 offset:1024\n\t" \
    "v_mfma_f32_16x16x32_bf16 v[208:211], v[76:79], v[236:239], v[208:211]\n\t" \
    "v_xor_b32 v252, 0x3000, %[xa]\n\t" \
    "ds_read_b128 v[236:239], v252 offset:0\n\t" \
    "v_mfma_f32_16x16x32_bf16 v[208:211], v[168:171], v[240:243], v[208:211]\n\t" \
    "ds_read_b128 v[240:243], v252 offset:1024\n\t" \
    "v_mfma_f32_16x16x32_bf16 v[208:211], v[172:175], v[212:215], v[208:211]\n\t" \
    "v_xor_b32 v252, 0x3800, %[xa]\n\t" \
    "ds_read_b128 v[212:215], v252 offset:0\n\t" \
    "s_waitcnt lgkmcnt(6)\n\t" \
    "v_mfma_f32_16x16x32_bf16 v[208:211], v[88:91], v[200:203], v[208:211]\n\t" \
    "ds_read_b128 v[200:203], v252 offset:1024\n\t" \
    "s_waitcnt lgkmcnt(6)\n\t" \
    "v_mfma_f32_16x16x32_bf16 v[208:211], v[92:95], v[224:227], v[208:211]\n\t" \
    "v_and_b32 v252, 0x3ff, %[xa]\n\t" \
    "ds_read_b128 v[224:227], v252 offset:43008\n\t" \
    "s_waitcnt lgkmcnt(6)\n\t" \
    "v_mfma_f32_16x16x32_bf16 v[208:211], v[112:115], v[228:231], v[208:211]\n\t" \
    "ds_read_b128 v[228:231], v252 offset:44032\n\t" \
    "s_waitcnt lgkmcnt(6)\n\t" \
    "v_mfma_f32_16x16x32_bf16 v[208:211], v[116:119], v[232:235], v[208:211]\n\t" \
    "v_xor_b32 v252, 0x2000, %[xa]\n\t" \
    "ds_read_b128 v[232:235], v252 offset:16384\n\t" \
    "s_waitcnt lgkmcnt(6)\n\t" \
    "v_mfma_f32_16x16x32_bf16 v[208:211], v[128:131], v[236:239], v[208:211]\n\t" \
    "ds_read_b128 v[236:239], v252 offset:17408\n\t" \
    "s_waitcnt lgkmcnt(6)\n\t" \
    "v_mfma_f32_16x16x32_bf16 v[208:211], v[132:135], v[240:243], v[208:211]\n\t" \
    "v_xor_b32 v252, 0x2800, %[xa]\n\t" \
    "ds_read_b128 v[240:243], v252 offset:16384\n\t" \
    "s_waitcnt lgkmcnt(6)\n\t" \
    "v_mfma_f32_16x16x32_bf16 v[208:211], v[152:155], v[212:215], v[208:211]\n\t" \
    "ds_read_b128 v[212:215], v252 offset:17408\n\t" \
    "s_waitcnt lgkmcnt(6)\n\t" \
    "v_mfma_f32_16x16x32_bf16 v[208:211], v[156:159], v[200:203], v[208:211]\n\t" \
    "v_xor_b32 v252, 0x3000, %[xa]\n\t" \
    "ds_read_b128 v[200:203], v252 offset:16384\n\t" \
    "s_waitcnt lgkmcnt(6)\n\t" \
    "v_mfma_f32_16x16x32_bf16 v[208:211], v[192:195], v[224:227], v[208:211]\n\t" \
    "ds_read_b128 v[224:227], v252 offset:17408\n\t" \
    "s_waitcnt lgkmcnt(6)\n\t" \
    "v_mfma_f32_16x16x32_bf16 v[208:211], v[196:199], v[228:231], v[208:211]\n\t" \
    "v_xor_b32 v252, 0x3800, %[xa]\n\t" \
    "ds_read_b128 v[228:231], v252 offset:16384\n\t" \
    "s_waitcnt lgkmcnt(6)\n\t" \
    "v_mfma_f32_16x16x32_bf16 v[220:223], v[88:91], v[232:235], v[220:223]\n\t" \
    "ds_read_b128 v[232:235], v252 offset:17408\n\t" \
    "s_waitcnt lgkmcnt(6)\n\t" \
    "v_mfma_f32_16x16x32_bf16 v[220:223], v[92:95], v[236:239], v[220:223]\n\t" \
    "v_and_b32 v252, 0x3ff, %[xa]\n\t" \
    "ds_read_b128 v[236:239], v252 offset:47104\n\t" \
    "s_waitcnt lgkmcnt(6)\n\t" \
    "v_mfma_f32_16x16x32_bf16 v[220:223], v[112:115], v[240:243], v[220:223]\n\t" \
    "ds_read_b128 v[240:243], v252 offset:48128\n\t" \
    "v_mov_b64 v[244:245], 0\n\t" \
    "s_waitcnt lgkmcnt(6)\n\t" \
    "v_mfma_f32_16x16x32_bf16 v[220:223], v[116:119], v[212:215], v[220:223]\n\t" \
    "v_mov_b64 v[246:247], 0\n\t" \
    "s_waitcnt lgkmcnt(5)\n\t" \
    "v_mfma_f32_16x16x32_bf16 v[220:223], v[128:131], v[200:203], v[220:223]\n\t" \
    "v_mov_b64 v[248:249], 0\n\t" \
    "s_waitcnt lgkmcnt(4)\n\t" \
    "v_mfma_f32_16x16x32_bf16 v[220:223], v[132:135], v[224:227], v[220:223]\n\t" \
    "v_mov_b64 v[250:251], 0\n\t" \
    "s_waitcnt lgkmcnt(3)\n\t" \
    "v_mfma_f32_16x16x32_bf16 v[220:223], v[152:155], v[228:231], v[220:223]\n\t" \
    "v_add_u32 v252, %[qs], v252\n\t" \
    "v_mov_b64 v[224:225], 0\n\t" \
    "s_waitcnt lgkmcnt(2)\n\t" \
    "v_mfma_f32_16x16x32_bf16 v[220:223], v[156:159], v[232:235], v[220:223]\n\t" \
    "v_mov_b64 v[226:227], 0\n\t" \
    "v_cvt_pk_bf16_f32 v208, v208, v209\n\t" \
    "s_waitcnt lgkmcnt(1)\n\t" \
    "v_mfma_f32_16x16x32_bf16 v[220:223], v[192:195], v[236:239], v[220:223]\n\t" \
    "v_cvt_pk_bf16_f32 v209, v210, v211\n\t" \
    "ds_write_b64 v252, v[208:209] offset:2048\n\t" \
    "s_waitcnt lgkmcnt(1)\n\t" \
    "v_mfma_f32_16x16x32_bf16 v[220:223], v[196:199], v[240:243], v[220:223]\n\t" \
    "ds_read_b128 v[228:231], %[wl] offset:2048\n\t" \
    "ds_read_b128 v[232:235], %[wl] offset:3072\n\t" \
    "ds_read_b128 v[200:203], %[wl] offset:0\n\t" \
    "ds_read_b128 v[204:207], %[wl] offset:1024\n\t" \
    "s_nop 3\n\t" \
    "v_cvt_pk_bf16_f32 v220, v220, v221\n\t" \
    "v_cvt_pk_bf16_f32 v221, v222, v223\n\t" \
    "ds_write_b64 v252, v[220:221] offset:8192\n\t" \
    "s_waitcnt lgkmcnt(0)\n\t"
#define ASM_XG_V \
    "s_waitcnt lgkmcnt(0)\n\t" \
    "ds_read_b128 v[224:227], %[xa] offset:16384\n\t" \
    "ds_read_b128 v[228:231], %[xa] offset:17408\n\t" \
    "ds_read_b128 v[244:247], %[wl] offset:0\n\t" \
    "ds_read_b128 v[248:251], %[wl] offset:1024\n\t" \
    "ds_read_b128 v[232:235], %[xa] offset:0\n\t" \
    "ds_read_b128 v[236:239], %[xa] offset:1024\n\t" \
    "v_xor_b32 v252, 0x800, %[xa]\n\t" \
    "ds_read_b128 v[240:243], v252 offset:0\n\t" \
    "s_waitcnt lgkmcnt(6)\n\t" \
    "v_mfma_f32_16x16x32_bf16 v[220:223], v[8:11], v[224:227], 0\n\t" \
    "s_waitcnt lgkmcnt(5)\n\t" \
    "v_mfma_f32_16x16x32_bf16 v[220:223], v[12:15], v[228:231], v[220:223]\n\t" \
    "v_mfma_f32_16x16x32_bf16 v[216:219], v[0:3], v[224:227], 0\n\t" \
    "v_mfma_f32_16x16x32_bf16 v[216:219], v[4:7], v[228:231], v[216:219]\n\t" \
    "s_waitcnt lgkmcnt(4)\n\t" \
    "v_mfma_f32_16x16x32_bf16 v[212:215], v[244:247], v[224:227], 0\n\t" \
    "ds_read_b128 v[224:227], v252 offset:1024\n\t" \
    "s_waitcnt lgkmcnt(4)\n\t" \
    "v_mfma_f32_16x16x32_bf16 v[212:215], v[248:251], v[228:231], v[212:215]\n\t" \
    "ds_read_b128 v[228:231], v252 offset:16384\n\t" \
    "s_waitcnt lgkmcnt(4)\n\t" \
    "v_mfma_f32_16x16x32_bf16 v[200:203], v[244:247], v[232:235], 0\n\t" \
    "ds_read_b128 v[244:247], %[wl] offset:2048\n\t" \
    "s_waitcnt lgkmcnt(4)\n\t" \
    "v_mfma_f32_16x16x32_bf16 v[200:203], v[248:251], v[236:239], v[200:203]\n\t" \
    "ds_read_b128 v[248:251], %[wl] offset:3072\n\t" \
    "v_mfma_f32_16x16x32_bf16 v[204:207], v[0:3], v[232:235], 0\n\t" \
    "v_mfma_f32_16x16x32_bf16 v[204:207], v[4:7], v[236:239], v[204:207]\n\t" \
    "v_mfma_f32_16x16x32_bf16 v[208:211], v[8:11], v[232:235], 0\n\t" \
    "ds_read_b128 v[232:235], v252 offset:17408\n\t" \
    "v_mfma_f32_16x16x32_bf16 v[208:211], v[12:15], v[236:239], v[208:211]\n\t" \
    "v_xor_b32 v252, 0x1000, %[xa]\n\t" \
    "ds_read_b128 v[236:239], v252 offset:16384\n\t" \
    "s_waitcnt lgkmcnt(6)\n\t" \
    "v_mfma_f32_16x16x32_bf16 v[208:211], v[32:35], v[240:243], v[208:211]\n\t" \
    "s_waitcnt lgkmcnt(5)\n\t" \
    "v_mfma_f32_16x16x32_bf16 v[208:211], v[36:39], v[224:227], v[208:211]\n\t" \
    "v_mfma_f32_16x16x32_bf16 v[204:207], v[24:27], v[240:243], v[204:207]\n\t" \
    "v_mfma_f32_16x16x32_bf16 v[204:207], v[28:31], v[224:227], v[204:207]\n\t" \
    "v_mfma_f32_16x16x32_bf16 v[200:203], v[16:19], v[240:243], v[200:203]\n\t" \
    "ds_read_b128 v[240:243], v252 offset:17408\n\t" \
    "v_mfma_f32_16x16x32_bf16 v[200:203], v[20:23], v[224:227], v[200:203]\n\t" \
    "ds_read_b128 v[224:227], v252 offset:0\n\t" \
    "s_waitcnt lgkmcnt(6)\n\t" \
    "v_mfma_f32_16x16x32_bf16 v[212:215], v[16:19], v[228:231], v[212:215]\n\t" \
    "s_waitcnt lgkmcnt(3)\n\t" \
    "v_mfma_f32_16x16x32_bf16 v[212:215], v[20:23], v[232:235], v[212:215]\n\t" \
    "v_mfma_f32_16x16x32_bf16 v[216:219], v[24:27], v[228:231], v[216:219]\n\t" \
    "v_mfma_f32_16x16x32_bf16 v[216:219], v[28:31], v[232:235], v[216:219]\n\t" \
    "v_mfma_f32_16x16x32_bf16 v[220:223], v[32:35], v[228:231], v[220:223]\n\t" \
    "ds_read_b128 v[228:231], v252 offset:1024\n\t" \
    "v_mfma_f32_16x16x32_bf16 v[220:223], v[36:39], v[232:235], v[220:223]\n\t" \
    "v_xor_b32 v252, 0x1800, %[xa]\n\t" \
    "ds_read_b128 v[232:235], v252 offset:0\n\t" \
    "s_waitcnt lgkmcnt(4)\n\t" \
    "v_mfma_f32_16x16x32_bf16 v[220:223], v[48:51], v[236:239], v[220:223]\n\t" \
    "s_waitcnt lgkmcnt(3)\n\t" \
    "v_mfma_f32_16x16x32_bf16 v[220:223], v[52:55], v[240:243], v[220:223]\n\t" \
    "v_mfma_f32_16x16x32_bf16 v[216:219], v[40:43], v[236:239], v[216:219]\n\t" \
    "v_mfma_f32_16x16x32_bf16 v[216:219], v[44:47], v[240:243], v[216:219]\n\t" \
    "v_mfma_f32_16x16x32_bf16 v[212:215], v[244:247], v[236:239], v[212:215]\n\t" \
    "ds_read_b128 v[236:239], v252 offset:1024\n\t" \
    "v_mfma_f32_16x16x32_bf16 v[212:215], v[248:251], v[240:243], v[212:215]\n\t" \
    "ds_read_b128 v[240:243], v252 offset:16384\n\t" \
    "s_waitcnt lgkmcnt(4)\n\t" \
    "v_mfma_f32_16x16x32_bf16 v[200:203], v[244:247], v[224:227], v[200:203]\n\t" \
    "ds_read_b128 v[244:247], %[wl] offset:8192\n\t" \
    "s_waitcnt lgkmcnt(4)\n\t" \
    "v_mfma_f32_16x16x32_bf16 v[200:203], v[248:251], v[228:231], v[200:203]\n\t" \
    "ds_read_b128 v[248:251], %[wl] offset:9216\n\t" \
    "v_mfma_f32_16x16x32_bf16 v[204:207], v[40:43], v[224:227], v[204:207]\n\t" \
    "v_mfma_f32_16x16x32_bf16 v[204:207], v[44:47], v[228:231], v[204:207]\n\t" \
    "v_mfma_f32_16x16x32_bf16 v[208:211], v[48:51], v[224:227], v[208:211]\n\t" \
    "ds_read_b128 v[224:227], v252 offset:17408\n\t" \
    "v_mfma_f32_16x16x32_bf16 v[208:211], v[52:55], v[228:231], v[208:211]\n\t" \
    "v_and_b32 v252, 0x3ff, %[xa]\n\t" \
    "ds_read_b128 v[228:231], v252 offset:45056\n\t" \
    "s_waitcnt lgkmcnt(6)\n\t" \
    "v_mfma_f32_16x16x32_bf16 v[208:211], v[72:75], v[232:235], v[208:211]\n\t" \
    "s_waitcnt lgkmcnt(5)\n\t" \
    "v_mfma_f32_16x16x32_bf16 v[208:211], v[76:79], v[236:239], v[208:211]\n\t" \
    "v_mfma_f32_16x16x32_bf16 v[204:207], v[64:67], v[232:235], v[204:207]\n\t" \
    "v_mfma_f32_16x16x32_bf16 v[204:207], v[68:71], v[236:239], v[204:207]\n\t" \
    "v_mfma_f32_16x16x32_bf16 v[200:203], v[56:59], v[232:235], v[200:203]\n\t" \
    "ds_read_b128 v[232:235], v252 offset:46080\n\t" \
    "v_mfma_f32_16x16x32_bf16 v[200:203], v[60:63], v[236:239], v[200:203]\n\t" \
    "ds_read_b128 v[236:239], v252 offset:40960\n\t" \
    "s_waitcnt lgkmcnt(6)\n\t" \
    "v_mfma_f32_16x16x32_bf16 v[212:215], v[56:59], v[240:243], v[212:215]\n\t" \
    "s_waitcnt lgkmcnt(3)\n\t" \
    "v_mfma_f32_16x16x32_bf16 v[212:215], v[60:63], v[224:227], v[212:215]\n\t" \
    "v_mfma_f32_16x16x32_bf16 v[216:219], v[64:67], v[240:243], v[216:219]\n\t" \
    "v_mfma_f32_16x16x32_bf16 v[216:219], v[68:71], v[224:227], v[216:219]\n\t" \
    "v_mfma_f32_16x16x32_bf16 v[220:223], v[72:75], v[240:243], v[220:223]\n\t" \
    "ds_read_b128 v[240:243], v252 offset:41984\n\t" \
    "v_mfma_f32_16x16x32_bf16 v[220:223], v[76:79], v[224:227], v[220:223]\n\t" \
    "v_xor_b32 v252, 0x2000, %[xa]\n\t" \
    "ds_read_b128 v[224:227], v252 offset:0\n\t" \
    "s_waitcnt lgkmcnt(4)\n\t" \
    "v_mfma_f32_16x16x32_bf16 v[220:223], v[168:171], v[228:231], v[220:223]\n\t" \
    "s_waitcnt lgkmcnt(3)\n\t" \
    "v_mfma_f32_16x16x32_bf16 v[220:223], v[172:175], v[232:235], v[220:223]\n\t" \
    "v_mfma_f32_16x16x32_bf16 v[216:219], v[160:163], v[228:231], v[216:219]\n\t" \
    "v_mfma_f32_16x16x32_bf16 v[216:219], v[164:167], v[232:235], v[216:219]\n\t" \
    "v_mfma_f32_16x16x32_bf16 v[212:215], v[244:247], v[228:231], v[212:215]\n\t" \
    "ds_read_b128 v[228:231], v252 offset:1024\n\t" \
    "v_mfma_f32_16x16x32_bf16 v[212:215], v[248:251], v[232:235], v[212:215]\n\t" \
    "ds_read_b128 v[232:235], v252 offset:16384\n\t" \
    "s_waitcnt lgkmcnt(4)\n\t" \
    "v_mfma_f32_16x16x32_bf16 v[200:203], v[244:247], v[236:239], v[200:203]\n\t" \
    "ds_read_b128 v[244:247], %[wl] offset:4096\n\t" \
    "s_waitcnt lgkmcnt(4)\n\t" \
    "v_mfma_f32_16x16x32_bf16 v[200:203], v[248:251], v[240:243], v[200:203]\n\t" \
    "ds_read_b128 v[248:251], %[wl] offset:5120\n\t" \
    "v_mfma_f32_16x16x32_bf16 v[204:207], v[160:163], v[236:239], v[204:207]\n\t" \
    "v_mfma_f32_16x16x32_bf16 v[204:207], v[164:167], v[240:243], v[204:207]\n\t" \
    "v_mfma_f32_16x16x32_bf16 v[208:211], v[168:171], v[236:239], v[208:211]\n\t" \
    "ds_read_b128 v[236:239], v252 offset:17408\n\t" \
    "v_mfma_f32_16x16x32_bf16 v[208:211], v[172:175], v[240:243], v[208:211]\n\t" \
    "v_xor_b32 v252, 0x2800, %[xa]\n\t" \
    "ds_read_b128 v[240:243], v252 offset:16384\n\t" \
    "s_waitcnt lgkmcnt(6)\n\t" \
    "v_mfma_f32_16x16x32_bf16 v[208:211], v[88:91], v[224:227], v[208:211]\n\t" \
    "s_waitcnt lgkmcnt(5)\n\t" \
    "v_mfma_f32_16x16x32_bf16 v[208:211], v[92:95], v[228:231], v[208:211]\n\t" \
    "v_mfma_f32_16x16x32_bf16 v[204:207], v[80:83], v[224:227], v[204:207]\n\t" \
    "v_mfma_f32_16x16x32_bf16 v[204:207], v[84:87], v[228:231], v[204:207]\n\t" \
    "s_waitcnt lgkmcnt(3)\n\t" \
    "v_mfma_f32_16x16x32_bf16 v[200:203], v[244:247], v[224:227], v[200:203]\n\t" \
    "ds_read_b128 v[224:227], v252 offset:17408\n\t" \
    "s_waitcnt lgkmcnt(3)\n\t" \
    "v_mfma_f32_16x16x32_bf16 v[200:203], v[248:251], v[228:231], v[200:203]\n\t" \
    "ds_read_b128 v[228:231], v252 offset:0\n\t" \
    "v_mfma_f32_16x16x32_bf16 v[212:215], v[244:247], v[232:235], v[212:215]\n\t" \
    "ds_read_b128 v[244:247], %[wl] offset:6144\n\t" \
    "s_waitcnt lgkmcnt(4)\n\t" \
    "v_mfma_f32_16x16x32_bf16 v[212:215], v[248:251], v[236:239], v[212:215]\n\t" \
    "ds_read_b128 v[248:251], %[wl] offset:7168\n\t" \
    "v_mfma_f32_16x16x32_bf16 v[216:219], v[80:83], v[232:235], v[216:219]\n\t" \
    "v_mfma_f32_16x16x32_bf16 v[216:219], v[84:87], v[236:239], v[216:219]\n\t" \
    "v_mfma_f32_16x16x32_bf16 v[220:223], v[88:91], v[232:235], v[220:223]\n\t" \
    "ds_read_b128 v[232:235], v252 offset:1024\n\t" \
    "v_mfma_f32_16x16x32_bf16 v[220:223], v[92:95], v[236:239], v[220:223]\n\t" \
    "v_xor_b32 v252, 0x3000, %[xa]\n\t" \
    "ds_read_b128 v[236:239], v252 offset:0\n\t" \
    "s_waitcnt lgkmcnt(6)\n\t" \
    "v_mfma_f32_16x16x32_bf16 v[220:223], v[112:115], v[240:243], v[220:223]\n\t" \
    "s_waitcnt lgkmcnt(5)\n\t" \
    "v_mfma_f32_16x16x32_bf16 v[220:223], v[116:119], v[224:227], v[220:223]\n\t" \
    "v_mfma_f32_16x16x32_bf16 v[216:219], v[104:107], v[240:243], v[216:219]\n\t" \
    "v_mfma_f32_16x16x32_bf16 v[216:219], v[108:111], v[224:227], v[216:219]\n\t" \
    "v_mfma_f32_16x16x32_bf16 v[212:215], v[96:99], v[240:243], v[212:215]\n\t" \
    "ds_read_b128 v[240:243], v252 offset:1024\n\t" \
    "v_mfma_f32_16x16x32_bf16 v[212:215], v[100:103], v[224:227], v[212:215]\n\t" \
    "ds_read_b128 v[224:227], v252 offset:16384\n\t" \
    "s_waitcnt lgkmcnt(6)\n\t" \
    "v_mfma_f32_16x16x32_bf16 v[200:203], v[96:99], v[228:231], v[200:203]\n\t" \
    "s_waitcnt lgkmcnt(3)\n\t" \
    "v_mfma_f32_16x16x32_bf16 v[200:203], v[100:103], v[232:235], v[200:203]\n\t" \
    "v_mfma_f32_16x16x32_bf16 v[204:207], v[104:107], v[228:231], v[204:207]\n\t" \
    "v_mfma_f32_16x16x32_bf16 v[204:207], v[108:111], v[232:235], v[204:207]\n\t" \
    "v_mfma_f32_16x16x32_bf16 v[208:211], v[112:115], v[228:231], v[208:211]\n\t" \
    "ds_read_b128 v[228:231], v252 offset:17408\n\t" \
    "v_mfma_f32_16x16x32_bf16 v[208:211], v[116:119], v[232:235], v[208:211]\n\t" \
    "v_xor_b32 v252, 0x3800, %[xa]\n\t" \
    "ds_read_b128 v[232:235], v252 offset:16384\n\t" \
    "s_waitcnt lgkmcnt(4)\n\t" \
    "v_mfma_f32_16x16x32_bf16 v[208:211], v[128:131], v[236:239], v[208:211]\n\t" \
    "s_waitcnt lgkmcnt(3)\n\t" \
    "v_mfma_f32_16x16x32_bf16 v[208:211], v[132:135], v[240:243], v[208:211]\n\t" \
    "v_mfma_f32_16x16x32_bf16 v[204:207], v[120:123], v[236:239], v[204:207]\n\t" \
    "v_mfma_f32_16x16x32_bf16 v[204:207], v[124:127], v[240:243], v[204:207]\n\t" \
    "v_mfma_f32_16x16x32_bf16 v[200:203], v[244:247], v[236:239], v[200:203]\n\t" \
    "ds_read_b128 v[236:239], v252 offset:17408\n\t" \
    "v_mfma_f32_16x16x32_bf16 v[200:203], v[248:251], v[240:243], v[200:203]\n\t" \
    "ds_read_b128 v[240:243], v252 offset:0\n\t" \
    "s_waitcnt lgkmcnt(4)\n\t" \
    "v_mfma_f32_16x16x32_bf16 v[212:215], v[244:247], v[224:227], v[212:215]\n\t" \
    "s_waitcnt lgkmcnt(3)\n\t" \
    "v_mfma_f32_16x16x32_bf16 v[212:215], v[248:251], v[228:231], v[212:215]\n\t" \
    "v_mfma_f32_16x16x32_bf16 v[216:219], v[120:123], v[224:227], v[216:219]\n\t" \
    "v_mfma_f32_16x16x32_bf16 v[216:219], v[124:127], v[228:231], v[216:219]\n\t" \
    "v_mfma_f32_16x16x32_bf16 v[220:223], v[128:131], v[224:227], v[220:223]\n\t" \
    "ds_read_b128 v[224:227], v252 offset:1024\n\t" \
    "v_mfma_f32_16x16x32_bf16 v[220:223], v[132:135], v[228:231], v[220:223]\n\t" \
    "v_and_b32 v252, 0x3ff, %[xa]\n\t" \
    "ds_read_b128 v[228:231], v252 offset:43008\n\t" \
    "s_waitcnt lgkmcnt(4)\n\t" \
    "v_mfma_f32_16x16x32_bf16 v[220:223], v[152:155], v[232:235], v[220:223]\n\t" \
    "s_waitcnt lgkmcnt(3)\n\t" \
    "v_mfma_f32_16x16x32_bf16 v[220:223], v[156:159], v[236:239], v[220:223]\n\t" \
    "v_mfma_f32_16x16x32_bf16 v[216:219], v[144:147], v[232:235], v[216:219]\n\t" \
    "v_mfma_f32_16x16x32_bf16 v[216:219], v[148:151], v[236:239], v[216:219]\n\t" \
    "v_mfma_f32_16x16x32_bf16 v[212:215], v[136:139], v[232:235], v[212:215]\n\t" \
    "ds_read_b128 v[232:235], v252 offset:44032\n\t" \
    "v_mfma_f32_16x16x32_bf16 v[212:215], v[140:143], v[236:239], v[212:215]\n\t" \
    "ds_read_b128 v[236:239], v252 offset:47104\n\t" \
    "s_waitcnt lgkmcnt(4)\n\t" \
    "v_mfma_f32_16x16x32_bf16 v[200:203], v[136:139], v[240:243], v[200:203]\n\t" \
    "s_waitcnt lgkmcnt(3)\n\t" \
    "v_mfma_f32_16x16x32_bf16 v[200:203], v[140:143], v[224:227], v[200:203]\n\t" \
    "v_mfma_f32_16x16x32_bf16 v[204:207], v[144:147], v[240:243], v[204:207]\n\t" \
    "v_mfma_f32_16x16x32_bf16 v[204:207], v[148:151], v[224:227], v[204:207]\n\t" \
    "v_mfma_f32_16x16x32_bf16 v[208:211], v[152:155], v[240:243], v[208:211]\n\t" \
    "ds_read_b128 v[240:243], v252 offset:48128\n\t" \
    "v_mfma_f32_16x16x32_bf16 v[208:211], v[156:159], v[224:227], v[208:211]\n\t" \
    "s_waitcnt lgkmcnt(3)\n\t" \
    "v_mfma_f32_16x16x32_bf16 v[208:211], v[192:195], v[228:231], v[208:211]\n\t" \
    "v_add_u32 v252, %[qs], v252\n\t" \
    "s_waitcnt lgkmcnt(2)\n\t" \
    "v_mfma_f32_16x16x32_bf16 v[208:211], v[196:199], v[232:235], v[208:211]\n\t" \
    "v_mfma_f32_16x16x32_bf16 v[204:207], v[184:187], v[228:231], v[204:207]\n\t" \
    "v_mfma_f32_16x16x32_bf16 v[204:207], v[188:191], v[232:235], v[204:207]\n\t" \
    "v_mfma_f32_16x16x32_bf16 v[200:203], v[176:179], v[228:231], v[200:203]\n\t" \
    "v_mfma_f32_16x16x32_bf16 v[200:203], v[180:183], v[232:235], v[200:203]\n\t" \
    "s_waitcnt lgkmcnt(1)\n\t" \
    "v_mfma_f32_16x16x32_bf16 v[212:215], v[176:179], v[236:239], v[212:215]\n\t" \
    "v_cvt_pk_bf16_f32 v208, v208, v209\n\t" \
    "v_cvt_pk_bf16_f32 v209, v210, v211\n\t" \
    "s_waitcnt lgkmcnt(0)\n\t" \
    "v_mfma_f32_16x16x32_bf16 v[212:215], v[180:183], v[240:243], v[212:215]\n\t" \
    "ds_write_b64 v252, v[208:209] offset:2048\n\t" \
    "v_cvt_pk_bf16_f32 v204, v204, v205\n\t" \
    "v_mfma_f32_16x16x32_bf16 v[216:219], v[184:187], v[236:239], v[216:219]\n\t" \
    "v_cvt_pk_bf16_f32 v205, v206, v207\n\t" \
    "ds_write_b64 v252, v[204:205] offset:1024\n\t" \
    "v_mfma_f32_16x16x32_bf16 v[216:219], v[188:191], v[240:243], v[216:219]\n\t" \
    "v_cvt_pk_bf16_f32 v200, v200, v201\n\t" \
    "v_cvt_pk_bf16_f32 v201, v202, v203\n\t" \
    "v_mfma_f32_16x16x32_bf16 v[220:223], v[192:195], v[236:239], v[220:223]\n\t" \
    "ds_write_b64 v252, v[200:201] offset:0\n\t" \
    "v_mfma_f32_16x16x32_bf16 v[220:223], v[196:199], v[240:243], v[220:223]\n\t" \
    "v_cvt_pk_bf16_f32 v212, v212, v213\n\t" \
    "v_cvt_pk_bf16_f32 v213, v214, v215\n\t" \
    "ds_write_b64 v252, v[212:213] offset:6144\n\t" \
    "v_cvt_pk_bf16_f32 v216, v216, v217\n\t" \
    "v_cvt_pk_bf16_f32 v217, v218, v219\n\t" \
    "ds_write_b64 v252, v[216:217] offset:7168\n\t" \
    "s_nop 1\n\t" \
    "v_cvt_pk_bf16_f32 v220, v220, v221\n\t" \
    "v_cvt_pk_bf16_f32 v221, v222, v223\n\t" \
    "ds_write_b64 v252, v[220:221] offset:8192\n\t" \
    "s_waitcnt lgkmcnt(0)\n\t"
#define ASM_XG_H_LO \
    "s_waitcnt lgkmcnt(0)\n\t" \
    "ds_read_b128 v[224:227], %[xa] offset:16384\n\t" \
    "ds_read_b128 v[228:231], %[xa] offset:17408\n\t" \
    "ds_read_b128 v[244:247], %[wl] offset:0\n\t" \
    "ds_read_b128 v[248:251], %[wl] offset:1024\n\t" \
    "ds_read_b128 v[232:235], %[xa] offset:0\n\t" \
    "ds_read_b128 v[236:239], %[xa] offset:1024\n\t" \
    "v_xor_b32 v252, 0x800, %[xa]\n\t" \
    "ds_read_b128 v[240:243], v252 offset:0\n\t" \
    "s_waitcnt lgkmcnt(6)\n\t" \
    "v_mfma_f32_16x16x32_bf16 v[220:223], v[8:11], v[224:227], 0\n\t" \
    "s_waitcnt lgkmcnt(5)\n\t" \
    "v_mfma_f32_16x16x32_bf16 v[220:223], v[12:15], v[228:231], v[220:223]\n\t" \
    "v_mfma_f32_16x16x32_bf16 v[216:219], v[0:3], v[224:227], 0\n\t" \
    "v_mfma_f32_16x16x32_bf16 v[216:219], v[4:7], v[228:231], v[216:219]\n\t" \
    "s_waitcnt lgkmcnt(4)\n\t" \
    "v_mfma_f32_16x16x32_bf16 v[212:215], v[244:247], v[224:227], 0\n\t" \
    "ds_read_b128 v[224:227], v252 offset:1024\n\t" \
    "s_waitcnt lgkmcnt(4)\n\t" \
    "v_mfma_f32_16x16x32_bf16 v[212:215], v[248:251], v[228:231], v[212:215]\n\t" \
    "ds_read_b128 v[228:231], v252 offset:16384\n\t" \
    "s_waitcnt lgkmcnt(4)\n\t" \
    "v_mfma_f32_16x16x32_bf16 v[200:203], v[244:247], v[232:235], 0\n\t" \
    "ds_read_b128 v[244:247], %[wl] offset:2048\n\t" \
    "s_waitcnt lgkmcnt(4)\n\t" \
    "v_mfma_f32_16x16x32_bf16 v[200:203], v[248:251], v[236:239], v[200:203]\n\t" \
    "ds_read_b128 v[248:251], %[wl] offset:3072\n\t" \
    "v_mfma_f32_16x16x32_bf16 v[204:207], v[0:3], v[232:235], 0\n\t" \
    "v_mfma_f32_16x16x32_bf16 v[204:207], v[4:7], v[236:239], v[204:207]\n\t" \
    "v_mfma_f32_16x16x32_bf16 v[208:211], v[8:11], v[232:235], 0\n\t" \
    "ds_read_b128 v[232:235], v252 offset:17408\n\t" \
    "v_mfma_f32_16x16x32_bf16 v[208:211], v[12:15], v[236:239], v[208:211]\n\t" \
    "v_xor_b32 v252, 0x1000, %[xa]\n\t" \
    "ds_read_b128 v[236:239], v252 offset:16384\n\t" \
    "s_waitcnt lgkmcnt(6)\n\t" \
    "v_mfma_f32_16x16x32_bf16 v[208:211], v[32:35], v[240:243], v[208:211]\n\t" \
    "s_waitcnt lgkmcnt(5)\n\t" \
    "v_mfma_f32_16x16x32_bf16 v[208:211], v[36:39], v[224:227], v[208:211]\n\t" \
    "v_mfma_f32_16x16x32_bf16 v[204:207], v[24:27], v[240:243], v[204:207]\n\t" \
    "v_mfma_f32_16x16x32_bf16 v[204:207], v[28:31], v[224:227], v[204:207]\n\t" \
    "v_mfma_f32_16x16x32_bf16 v[200:203], v[16:19], v[240:243], v[200:203]\n\t" \
    "ds_read_b128 v[240:243], v252 offset:17408\n\t" \
    "v_mfma_f32_16x16x32_bf16 v[200:203], v[20:23], v[224:227], v[200:203]\n\t" \
    "ds_read_b128 v[224:227], v252 offset:0\n\t" \
    "s_waitcnt lgkmcnt(6)\n\t" \
    "v_mfma_f32_16x16x32_bf16 v[212:215], v[16:19], v[228:231], v[212:215]\n\t" \
    "s_waitcnt lgkmcnt(3)\n\t" \
    "v_mfma_f32_16x16x32_bf16 v[212:215], v[20:23], v[232:235], v[212:215]\n\t" \
    "v_mfma_f32_16x16x32_bf16 v[216:219], v[24:27], v[228:231], v[216:219]\n\t" \
    "v_mfma_f32_16x16x32_bf16 v[216:219], v[28:31], v[232:235], v[216:219]\n\t" \
    "v_mfma_f32_16x16x32_bf16 v[220:223], v[32:35], v[228:231], v[220:223]\n\t" \
    "ds_read_b128 v[228:231], v252 offset:1024\n\t" \
    "v_mfma_f32_16x16x32_bf16 v[220:223], v[36:39], v[232:235], v[220:223]\n\t" \
    "v_xor_b32 v252, 0x1800, %[xa]\n\t" \
    "ds_read_b128 v[232:235], v252 offset:0\n\t" \
    "s_waitcnt lgkmcnt(4)\n\t" \
    "v_mfma_f32_16x16x32_bf16 v[220:223], v[48:51], v[236:239], v[220:223]\n\t" \
    "s_waitcnt lgkmcnt(3)\n\t" \
    "v_mfma_f32_16x16x32_bf16 v[220:223], v[52:55], v[240:243], v[220:223]\n\t" \
    "v_mfma_f32_16x16x32_bf16 v[216:219], v[40:43], v[236:239], v[216:219]\n\t" \
    "v_mfma_f32_16x16x32_bf16 v[216:219], v[44:47], v[240:243], v[216:219]\n\t" \
    "v_mfma_f32_16x16x32_bf16 v[212:215], v[244:247], v[236:239], v[212:215]\n\t" \
    "ds_read_b128 v[236:239], v252 offset:1024\n\t" \
    "v_mfma_f32_16x16x32_bf16 v[212:215], v[248:251], v[240:243], v[212:215]\n\t" \
    "ds_read_b128 v[240:243], v252 offset:16384\n\t" \
    "s_waitcnt lgkmcnt(4)\n\t" \
    "v_mfma_f32_16x16x32_bf16 v[200:203], v[244:247], v[224:227], v[200:203]\n\t" \
    "ds_read_b128 v[244:247], %[wl] offset:8192\n\t" \
    "s_waitcnt lgkmcnt(4)\n\t" \
    "v_mfma_f32_16x16x32_bf16 v[200:203], v[248:251], v[228:231], v[200:203]\n\t" \
    "ds_read_b128 v[248:251], %[wl] offset:9216\n\t" \
    "v_mfma_f32_16x16x32_bf16 v[204:207], v[40:43], v[224:227], v[204:207]\n\t" \
    "v_mfma_f32_16x16x32_bf16 v[204:207], v[44:47], v[228:231], v[204:207]\n\t" \
    "v_mfma_f32_16x16x32_bf16 v[208:211], v[48:51], v[224:227], v[208:211]\n\t" \
    "ds_read_b128 v[224:227], v252 offset:17408\n\t" \
    "v_mfma_f32_16x16x32_bf16 v[208:211], v[52:55], v[228:231], v[208:211]\n\t" \
    "v_and_b32 v252, 0x3ff, %[xa]\n\t" \
    "ds_read_b128 v[228:231], v252 offset:45056\n\t" \
    "s_waitcnt lgkmcnt(6)\n\t" \
    "v_mfma_f32_16x16x32_bf16 v[208:211], v[72:75], v[232:235], v[208:211]\n\t" \
    "s_waitcnt lgkmcnt(5)\n\t" \
    "v_mfma_f32_16x16x32_bf16 v[208:211], v[76:79], v[236:239], v[208:211]\n\t" \
    "v_mfma_f32_16x16x32_bf16 v[204:207], v[64:67], v[232:235], v[204:207]\n\t" \
    "v_mfma_f32_16x16x32_bf16 v[204:207], v[68:71], v[236:239], v[204:207]\n\t" \
    "v_mfma_f32_16x16x32_bf16 v[200:203], v[56:59], v[232:235], v[200:203]\n\t" \
    "ds_read_b128 v[232:235], v252 offset:46080\n\t" \
    "v_mfma_f32_16x16x32_bf16 v[200:203], v[60:63], v[236:239], v[200:203]\n\t" \
    "ds_read_b128 v[236:239], v252 offset:40960\n\t" \
    "s_waitcnt lgkmcnt(6)\n\t" \
    "v_mfma_f32_16x16x32_bf16 v[212:215], v[56:59], v[240:243], v[212:215]\n\t" \
    "s_waitcnt lgkmcnt(3)\n\t" \
    "v_mfma_f32_16x16x32_bf16 v[212:215], v[60:63], v[224:227], v[212:215]\n\t" \
    "v_mfma_f32_16x16x32_bf16 v[216:219], v[64:67], v[240:243], v[216:219]\n\t" \
    "v_mfma_f32_16x16x32_bf16 v[216:219], v[68:71], v[224:227], v[216:219]\n\t" \
    "v_mfma_f32_16x16x32_bf16 v[220:223], v[72:75], v[240:243], v[220:223]\n\t" \
    "ds_read_b128 v[240:243], v252 offset:41984\n\t" \
    "v_mfma_f32_16x16x32_bf16 v[220:223], v[76:79], v[224:227], v[220:223]\n\t" \
    "s_waitcnt lgkmcnt(0)\n\t" \
    "s_barrier\n\t" ASM_ST \
    "v_xor_b32 v252, 0x2000, %[xa]\n\t" \
    "ds_read_b128 v[224:227], v252 offset:0\n\t" \
    "v_mfma_f32_16x16x32_bf16 v[220:223], v[168:171], v[228:231], v[220:223]\n\t" \
    "v_mfma_f32_16x16x32_bf16 v[220:223], v[172:175], v[232:235], v[220:223]\n\t" \
    "v_mfma_f32_16x16x32_bf16 v[216:219], v[160:163], v[228:231], v[216:219]\n\t" \
    "v_mfma_f32_16x16x32_bf16 v[216:219], v[164:167], v[232:235], v[216:219]\n\t" \
    "v_mfma_f32_16x16x32_bf16 v[212:215], v[244:247], v[228:231], v[212:215]\n\t" \
    "ds_read_b128 v[228:231], v252 offset:1024\n\t" \
    "v_mfma_f32_16x16x32_bf16 v[212:215], v[248:251], v[232:235], v[212:215]\n\t" \
    "ds_read_b128 v[232:235], v252 offset:16384\n\t" \
    "v_mfma_f32_16x16x32_bf16 v[200:203], v[244:247], v[236:239], v[200:203]\n\t" \
    "ds_read_b128 v[244:247], %[wl] offset:4096\n\t" \
    "v_mfma_f32_16x16x32_bf16 v[200:203], v[248:251], v[240:243], v[200:203]\n\t" \
    "ds_read_b128 v[248:251], %[wl] offset:5120\n\t" \
    "v_mfma_f32_16x16x32_bf16 v[204:207], v[160:163], v[236:239], v[204:207]\n\t" \
    "v_mfma_f32_16x16x32_bf16 v[204:207], v[164:167], v[240:243], v[204:207]\n\t" \
    "v_mfma_f32_16x16x32_bf16 v[208:211], v[168:171], v[236:239], v[208:211]\n\t" \
    "ds_read_b128 v[236:239], v252 offset:17408\n\t" \
    "v_mfma_f32_16x16x32_bf16 v[208:211], v[172:175], v[240:243], v[208:211]\n\t" \
    "v_xor_b32 v252, 0x2800, %[xa]\n\t" \
    "ds_read_b128 v[240:243], v252 offset:16384\n\t" \
    "s_waitcnt lgkmcnt(6)\n\t" \
    "v_mfma_f32_16x16x32_bf16 v[208:211], v[88:91], v[224:227], v[208:211]\n\t" \
    "s_waitcnt lgkmcnt(5)\n\t" \
    "v_mfma_f32_16x16x32_bf16 v[208:211], v[92:95], v[228:231], v[208:211]\n\t" \
    "v_mfma_f32_16x16x32_bf16 v[204:207], v[80:83], v[224:227], v[204:207]\n\t" \
    "v_mfma_f32_16x16x32_bf16 v[204:207], v[84:87], v[228:231], v[204:207]\n\t" \
    "s_waitcnt lgkmcnt(3)\n\t" \
    "v_mfma_f32_16x16x32_bf16 v[200:203], v[244:247], v[224:227], v[200:203]\n\t" \
    "ds_read_b128 v[224:227], v252 offset:17408\n\t" \
    "s_waitcnt lgkmcnt(3)\n\t" \
    "v_mfma_f32_16x16x32_bf16 v[200:203], v[248:251], v[228:231], v[200:203]\n\t" \
    "ds_read_b128 v[228:231], v252 offset:0\n\t" \
    "v_mfma_f32_16x16x32_bf16 v[212:215], v[244:247], v[232:235], v[212:215]\n\t" \
    "ds_read_b128 v[244:247], %[wl] offset:6144\n\t" \
    "s_waitcnt lgkmcnt(4)\n\t" \
    "v_mfma_f32_16x16x32_bf16 v[212:215], v[248:251], v[236:239], v[212:215]\n\t" \
    "ds_read_b128 v[248:251], %[wl] offset:7168\n\t" \
    "v_mfma_f32_16x16x32_bf16 v[216:219], v[80:83], v[232:235], v[216:219]\n\t" \
    "v_mfma_f32_16x16x32_bf16 v[216:219], v[84:87], v[236:239], v[216:219]\n\t" \
    "v_mfma_f32_16x16x32_bf16 v[220:223], v[88:91], v[232:235], v[220:223]\n\t" \
    "ds_read_b128 v[232:235], v252 offset:1024\n\t" \
    "v_mfma_f32_16x16x32_bf16 v[220:223], v[92:95], v[236:239], v[220:223]\n\t" \
    "v_xor_b32 v252, 0x3000, %[xa]\n\t" \
    "ds_read_b128 v[236:239], v252 offset:0\n\t" \
    "s_waitcnt lgkmcnt(6)\n\t" \
    "v_mfma_f32_16x16x32_bf16 v[220:223], v[112:115], v[240:243], v[220:223]\n\t" \
    "s_waitcnt lgkmcnt(5)\n\t" \
    "v_mfma_f32_16x16x32_bf16 v[220:223], v[116:119], v[224:227], v[220:223]\n\t" \
    "v_mfma_f32_16x16x32_bf16 v[216:219], v[104:107], v[240:243], v[216:219]\n\t" \
    "v_mfma_f32_16x16x32_bf16 v[216:219], v[108:111], v[224:227], v[216:219]\n\t" \
    "v_mfma_f32_16x16x32_bf16 v[212:215], v[96:99], v[240:243], v[212:215]\n\t" \
    "ds_read_b128 v[240:243], v252 offset:1024\n\t" \
    "v_mfma_f32_16x16x32_bf16 v[212:215], v[100:103], v[224:227], v[212:215]\n\t" \
    "ds_read_b128 v[224:227], v252 offset:16384\n\t" \
    "s_waitcnt lgkmcnt(6)\n\t" \
    "v_mfma_f32_16x16x32_bf16 v[200:203], v[96:99], v[228:231], v[200:203]\n\t" \
    "s_waitcnt lgkmcnt(3)\n\t" \
    "v_mfma_f32_16x16x32_bf16 v[200:203], v[100:103], v[232:235], v[200:203]\n\t" \
    "v_mfma_f32_16x16x32_bf16 v[204:207], v[104:107], v[228:231], v[204:207]\n\t" \
    "v_mfma_f32_16x16x32_bf16 v[204:207], v[108:111], v[232:235], v[204:207]\n\t" \
    "v_mfma_f32_16x16x32_bf16 v[208:211], v[112:115], v[228:231], v[208:211]\n\t" \
    "ds_read_b128 v[228:231], v252 offset:17408\n\t" \
    "v_mfma_f32_16x16x32_bf16 v[208:211], v[116:119], v[232:235], v[208:211]\n\t" \
    "v_xor_b32 v252, 0x3800, %[xa]\n\t" \
    "ds_read_b128 v[232:235], v252 offset:16384\n\t" \
    "s_waitcnt lgkmcnt(4)\n\t" \
    "v_mfma_f32_16x16x32_bf16 v[208:211], v[128:131], v[236:239], v[208:211]\n\t" \
    "s_waitcnt lgkmcnt(3)\n\t" \
    "v_mfma_f32_16x16x32_bf16 v[208:211], v[132:135], v[240:243], v[208:211]\n\t" \
    "v_mfma_f32_16x16x32_bf16 v[204:207], v[120:123], v[236:239], v[204:207]\n\t" \
    "v_mfma_f32_16x16x32_bf16 v[204:207], v[124:127], v[240:243], v[204:207]\n\t" \
    "v_mfma_f32_16x16x32_bf16 v[200:203], v[244:247], v[236:239], v[200:203]\n\t" \
    "ds_read_b128 v[236:239], v252 offset:17408\n\t" \
    "v_mfma_f32_16x16x32_bf16 v[200:203], v[248:251], v[240:243], v[200:203]\n\t" \
    "ds_read_b128 v[240:243], v252 offset:0\n\t" \
    "s_waitcnt lgkmcnt(4)\n\t" \
    "v_mfma_f32_16x16x32_bf16 v[212:215], v[244:247], v[224:227], v[212:215]\n\t" \
    "s_waitcnt lgkmcnt(3)\n\t" \
    "v_mfma_f32_16x16x32_bf16 v[212:215], v[248:251], v[228:231], v[212:215]\n\t" \
    "v_mfma_f32_16x16x32_bf16 v[216:219], v[120:123], v[224:227], v[216:219]\n\t" \
    "v_mfma_f32_16x16x32_bf16 v[216:219], v[124:127], v[228:231], v[216:219]\n\t" \
    "v_mfma_f32_16x16x32_bf16 v[220:223], v[128:131], v[224:227], v[220:223]\n\t" \
    "ds_read_b128 v[224:227], v252 offset:1024\n\t" \
    "v_mfma_f32_16x16x32_bf16 v[220:223], v[132:135], v[228:231], v[220:223]\n\t" \
    "v_and_b32 v252, 0x3ff, %[xa]\n\t" \
    "ds_read_b128 v[228:231], v252 offset:43008\n\t" \
    "s_waitcnt lgkmcnt(4)\n\t" \
    "v_mfma_f32_16x16x32_bf16 v[220:223], v[152:155], v[232:235], v[220:223]\n\t" \
    "s_waitcnt lgkmcnt(3)\n\t" \
    "v_mfma_f32_16x16x32_bf16 v[220:223], v[156:159], v[236:239], v[220:223]\n\t" \
    "v_mfma_f32_16x16x32_bf16 v[216:219], v[144:147], v[232:235], v[216:219]\n\t" \
    "v_mfma_f32_16x16x32_bf16 v[216:219], v[148:151], v[236:239], v[216:219]\n\t" \
    "v_mfma_f32_16x16x32_bf16 v[212:215], v[136:139], v[232:235], v[212:215]\n\t" \
    "ds_read_b128 v[232:235], v252 offset:44032\n\t" \
    "v_mfma_f32_16x16x32_bf16 v[212:215], v[140:143], v[236:239], v[212:215]\n\t" \
    "ds_read_b128 v[236:239], v252 offset:47104\n\t" \
    "s_waitcnt lgkmcnt(4)\n\t" \
    "v_mfma_f32_16x16x32_bf16 v[200:203], v[136:139], v[240:243], v[200:203]\n\t" \
    "s_waitcnt lgkmcnt(3)\n\t" \
    "v_mfma_f32_16x16x32_bf16 v[200:203], v[140:143], v[224:227], v[200:203]\n\t" \
    "v_mfma_f32_16x16x32_bf16 v[204:207], v[144:147], v[240:243], v[204:207]\n\t" \
    "v_mfma_f32_16x16x32_bf16 v[204:207], v[148:151], v[224:227], v[204:207]\n\t" \
    "v_mfma_f32_16x16x32_bf16 v[208:211], v[152:155], v[240:243], v[208:211]\n\t" \
    "ds_read_b128 v[240:243], v252 offset:48128\n\t" \
    "v_mfma_f32_16x16x32_bf16 v[208:211], v[156:159], v[224:227], v[208:211]\n\t" \
    "s_waitcnt lgkmcnt(3)\n\t" \
    "v_mfma_f32_16x16x32_bf16 v[208:211], v[192:195], v[228:231], v[208:211]\n\t" \
    "v_add_u32 v252, %[qs], v252\n\t" \
    "s_waitcnt lgkmcnt(2)\n\t" \
    "v_mfma_f32_16x16x32_bf16 v[208:211], v[196:199], v[232:235], v[208:211]\n\t" \
    "v_mfma_f32_16x16x32_bf16 v[204:207], v[184:187], v[228:231], v[204:207]\n\t" \
    "v_mfma_f32_16x16x32_bf16 v[204:207], v[188:191], v[232:235], v[204:207]\n\t" \
    "v_mfma_f32_16x16x32_bf16 v[200:203], v[176:179], v[228:231], v[200:203]\n\t" \
    "v_mfma_f32_16x16x32_bf16 v[200:203], v[180:183], v[232:235], v[200:203]\n\t" \
    "s_waitcnt lgkmcnt(1)\n\t" \
    "v_mfma_f32_16x16x32_bf16 v[212:215], v[176:179], v[236:239], v[212:215]\n\t" \
    "v_cvt_pk_bf16_f32 v208, v208, v209\n\t" \
    "v_cvt_pk_bf16_f32 v209, v210, v211\n\t" \
    "s_waitcnt lgkmcnt(0)\n\t" \
    "v_mfma_f32_16x16x32_bf16 v[212:215], v[180:183], v[240:243], v[212:215]\n\t" \
    "ds_write_b64 v252, v[208:209] offset:2048\n\t" \
    "v_cvt_pk_bf16_f32 v204, v204, v205\n\t" \
    "v_mfma_f32_16x16x32_bf16 v[216:219], v[184:187], v[236:239], v[216:219]\n\t" \
    "v_cvt_pk_bf16_f32 v205, v206, v207\n\t" \
    "ds_write_b64 v252, v[204:205] offset:1024\n\t" \
    "v_mfma_f32_16x16x32_bf16 v[216:219], v[188:191], v[240:243], v[216:219]\n\t" \
    "v_cvt_pk_bf16_f32 v200, v200, v201\n\t" \
    "v_cvt_pk_bf16_f32 v201, v202, v203\n\t" \
    "v_mfma_f32_16x16x32_bf16 v[220:223], v[192:195], v[236:239], v[220:223]\n\t" \
    "ds_write_b64 v252, v[200:201] offset:0\n\t" \
    "v_mfma_f32_16x16x32_bf16 v[220:223], v[196:199], v[240:243], v[220:223]\n\t" \
    "v_cvt_pk_bf16_f32 v212, v212, v213\n\t" \
    "v_cvt_pk_bf16_f32 v213, v214, v215\n\t" \
    "ds_write_b64 v252, v[212:213] offset:6144\n\t" \
    "v_cvt_pk_bf16_f32 v216, v216, v217\n\t" \
    "v_cvt_pk_bf16_f32 v217, v218, v219\n\t" \
    "ds_write_b64 v252, v[216:217] offset:7168\n\t" \
    "s_nop 1\n\t" \
    "v_cvt_pk_bf16_f32 v220, v220, v221\n\t" \
    "v_cvt_pk_bf16_f32 v221, v222, v223\n\t" \
    "ds_write_b64 v252, v[220:221] offset:8192\n\t" \
    "s_waitcnt lgkmcnt(0)\n\t"
#define ASM_XG_H_HI \
    "s_waitcnt lgkmcnt(0)\n\t" \
    "v_xor_b32 v252, 0x2000, %[xa]\n\t" \
    "ds_read_b128 v[224:227], v252 offset:16384\n\t" \
    "ds_read_b128 v[228:231], v252 offset:17408\n\t" \
    "ds_read_b128 v[244:247], %[wl] offset:4096\n\t" \
    "ds_read_b128 v[248:251], %[wl] offset:5120\n\t" \
    "ds_read_b128 v[232:235], v252 offset:0\n\t" \
    "ds_read_b128 v[236:239], v252 offset:1024\n\t" \
    "v_xor_b32 v252, 0x2800, %[xa]\n\t" \
    "ds_read_b128 v[240:243], v252 offset:0\n\t" \
    "s_waitcnt lgkmcnt(6)\n\t" \
    "v_mfma_f32_16x16x32_bf16 v[220:223], v[88:91], v[224:227], 0\n\t" \
    "s_waitcnt lgkmcnt(5)\n\t" \
    "v_mfma_f32_16x16x32_bf16 v[220:223], v[92:95], v[228:231], v[220:223]\n\t" \
    "v_mfma_f32_16x16x32_bf16 v[216:219], v[80:83], v[224:227], 0\n\t" \
    "v_mfma_f32_16x16x32_bf16 v[216:219], v[84:87], v[228:231], v[216:219]\n\t" \
    "s_waitcnt lgkmcnt(4)\n\t" \
    "v_mfma_f32_16x16x32_bf16 v[212:215], v[244:247], v[224:227], 0\n\t" \
    "ds_read_b128 v[224:227], v252 offset:1024\n\t" \
    "s_waitcnt lgkmcnt(4)\n\t" \
    "v_mfma_f32_16x16x32_bf16 v[212:215], v[248:251], v[228:231], v[212:215]\n\t" \
    "ds_read_b128 v[228:231], v252 offset:16384\n\t" \
    "s_waitcnt lgkmcnt(4)\n\t" \
    "v_mfma_f32_16x16x32_bf16 v[200:203], v[244:247], v[232:235], 0\n\t" \
    "ds_read_b128 v[244:247], %[wl] offset:6144\n\t" \
    "s_waitcnt lgkmcnt(4)\n\t" \
    "v_mfma_f32_16x16x32_bf16 v[200:203], v[248:251], v[236:239], v[200:203]\n\t" \
    "ds_read_b128 v[248:251], %[wl] offset:7168\n\t" \
    "v_mfma_f32_16x16x32_bf16 v[204:207], v[80:83], v[232:235], 0\n\t" \
    "v_mfma_f32_16x16x32_bf16 v[204:207], v[84:87], v[236:239], v[204:207]\n\t" \
    "v_mfma_f32_16x16x32_bf16 v[208:211], v[88:91], v[232:235], 0\n\t" \
    "ds_read_b128 v[232:235], v252 offset:17408\n\t" \
    "v_mfma_f32_16x16x32_bf16 v[208:211], v[92:95], v[236:239], v[208:211]\n\t" \
    "v_xor_b32 v252, 0x3000, %[xa]\n\t" \
    "ds_read_b128 v[236:239], v252 offset:16384\n\t" \
    "s_waitcnt lgkmcnt(6)\n\t" \
    "v_mfma_f32_16x16x32_bf16 v[208:211], v[112:115], v[240:243], v[208:211]\n\t" \
    "s_waitcnt lgkmcnt(5)\n\t" \
    "v_mfma_f32_16x16x32_bf16 v[208:211], v[116:119], v[224:227], v[208:211]\n\t" \
    "v_mfma_f32_16x16x32_bf16 v[204:207], v[104:107], v[240:243], v[204:207]\n\t" \
    "v_mfma_f32_16x16x32_bf16 v[204:207], v[108:111], v[224:227], v[204:207]\n\t" \
    "v_mfma_f32_16x16x32_bf16 v[200:203], v[96:99], v[240:243], v[200:203]\n\t" \
    "ds_read_b128 v[240:243], v252 offset:17408\n\t" \
    "v_mfma_f32_16x16x32_bf16 v[200:203], v[100:103], v[224:227], v[200:203]\n\t" \
    "s_nop 3\n\t" \
    "v_mov_b32 v224, %[tv]\n\t" \
    "s_waitcnt vmcnt(5)\n\t" \
    "buffer_store_dword v224, %[fv], %[rr], %[fp] offen sc1\n\t" \
    "ds_read_b128 v[224:227], v252 offset:0\n\t" \
    "s_waitcnt lgkmcnt(6)\n\t" \
    "v_mfma_f32_16x16x32_bf16 v[212:215], v[96:99], v[228:231], v[212:215]\n\t" \
    "s_waitcnt lgkmcnt(3)\n\t" \
    "v_mfma_f32_16x16x32_bf16 v[212:215], v[100:103], v[232:235], v[212:215]\n\t" \
    "v_mfma_f32_16x16x32_bf16 v[216:219], v[104:107], v[228:231], v[216:219]\n\t" \
    "v_mfma_f32_16x16x32_bf16 v[216:219], v[108:111], v[232:235], v[216:219]\n\t" \
    "v_mfma_f32_16x16x32_bf16 v[220:223], v[112:115], v[228:231], v[220:223]\n\t" \
    "ds_read_b128 v[228:231], v252 offset:1024\n\t" \
    "v_mfma_f32_16x16x32_bf16 v[220:223], v[116:119], v[232:235], v[220:223]\n\t" \
    "v_xor_b32 v252, 0x3800, %[xa]\n\t" \
    "ds_read_b128 v[232:235], v252 offset:0\n\t" \
    "s_waitcnt lgkmcnt(4)\n\t" \
    "v_mfma_f32_16x16x32_bf16 v[220:223], v[128:131], v[236:239], v[220:223]\n\t" \
    "s_waitcnt lgkmcnt(3)\n\t" \
    "v_mfma_f32_16x16x32_bf16 v[220:223], v[132:135], v[240:243], v[220:223]\n\t" \
    "v_mfma_f32_16x16x32_bf16 v[216:219], v[120:123], v[236:239], v[216:219]\n\t" \
    "v_mfma_f32_16x16x32_bf16 v[216:219], v[124:127], v[240:243], v[216:219]\n\t" \
    "v_mfma_f32_16x16x32_bf16 v[212:215], v[244:247], v[236:239], v[212:215]\n\t" \
    "ds_read_b128 v[236:239], v252 offset:1024\n\t" \
    "v_mfma_f32_16x16x32_bf16 v[212:215], v[248:251], v[240:243], v[212:215]\n\t" \
    "ds_read_b128 v[240:243], v252 offset:16384\n\t" \
    "s_waitcnt lgkmcnt(4)\n\t" \
    "v_mfma_f32_16x16x32_bf16 v[200:203], v[244:247], v[224:227], v[200:203]\n\t" \
    "ds_read_b128 v[244:247], %[wl] offset:8192\n\t" \
    "s_waitcnt lgkmcnt(4)\n\t" \
    "v_mfma_f32_16x16x32_bf16 v[200:203], v[248:251], v[228:231], v[200:203]\n\t" \
    "ds_read_b128 v[248:251], %[wl] offset:9216\n\t" \
    "v_mfma_f32_16x16x32_bf16 v[204:207], v[120:123], v[224:227], v[204:207]\n\t" \
    "v_mfma_f32_16x16x32_bf16 v[204:207], v[124:127], v[228:231], v[204:207]\n\t" \
    "v_mfma_f32_16x16x32_bf16 v[208:211], v[128:131], v[224:227], v[208:211]\n\t" \
    "ds_read_b128 v[224:227], v252 offset:17408\n\t" \
    "v_mfma_f32_16x16x32_bf16 v[208:211], v[132:135], v[228:231], v[208:211]\n\t" \
    "v_and_b32 v252, 0x3ff, %[xa]\n\t" \
    "ds_read_b128 v[228:231], v252 offset:45056\n\t" \
    "s_waitcnt lgkmcnt(6)\n\t" \
    "v_mfma_f32_16x16x32_bf16 v[208:211], v[152:155], v[232:235], v[208:211]\n\t" \
    "s_waitcnt lgkmcnt(5)\n\t" \
    "v_mfma_f32_16x16x32_bf16 v[208:211], v[156:159], v[236:239], v[208:211]\n\t" \
    "v_mfma_f32_16x16x32_bf16 v[204:207], v[144:147], v[232:235], v[204:207]\n\t" \
    "v_mfma_f32_16x16x32_bf16 v[204:207], v[148:151], v[236:239], v[204:207]\n\t" \
    "v_mfma_f32_16x16x32_bf16 v[200:203], v[136:139], v[232:235], v[200:203]\n\t" \
    "ds_read_b128 v[232:235], v252 offset:46080\n\t" \
    "v_mfma_f32_16x16x32_bf16 v[200:203], v[140:143], v[236:239], v[200:203]\n\t" \
    "ds_read_b128 v[236:239], v252 offset:40960\n\t" \
    "s_waitcnt lgkmcnt(6)\n\t" \
    "v_mfma_f32_16x16x32_bf16 v[212:215], v[136:139], v[240:243], v[212:215]\n\t" \
    "s_waitcnt lgkmcnt(3)\n\t" \
    "v_mfma_f32_16x16x32_bf16 v[212:215], v[140:143], v[224:227], v[212:215]\n\t" \
    "v_mfma_f32_16x16x32_bf16 v[216:219], v[144:147], v[240:243], v[216:219]\n\t" \
    "v_mfma_f32_16x16x32_bf16 v[216:219], v[148:151], v[224:227], v[216:219]\n\t" \
    "v_mfma_f32_16x16x32_bf16 v[220:223], v[152:155], v[240:243], v[220:223]\n\t" \
    "ds_read_b128 v[240:243], v252 offset:41984\n\t" \
    "v_mfma_f32_16x16x32_bf16 v[220:223], v[156:159], v[224:227], v[220:223]\n\t" \
    "s_waitcnt vmcnt(1) lgkmcnt(0)\n\t" \
    "s_barrier\n\t" ASM_ST \
    "ds_read_b128 v[224:227], %[xa] offset:0\n\t" \
    "v_mfma_f32_16x16x32_bf16 v[220:223], v[168:171], v[228:231], v[220:223]\n\t" \
    "v_mfma_f32_16x16x32_bf16 v[220:223], v[172:175], v[232:235], v[220:223]\n\t" \
    "v_mfma_f32_16x16x32_bf16 v[216:219], v[160:163], v[228:231], v[216:219]\n\t" \
    "v_mfma_f32_16x16x32_bf16 v[216:219], v[164:167], v[232:235], v[216:219]\n\t" \
    "v_mfma_f32_16x16x32_bf16 v[212:215], v[244:247], v[228:231], v[212:215]\n\t" \
    "ds_read_b128 v[228:231], %[xa] offset:1024\n\t" \
    "v_mfma_f32_16x16x32_bf16 v[212:215], v[248:251], v[232:235], v[212:215]\n\t" \
    "ds_read_b128 v[232:235], %[xa] offset:16384\n\t" \
    "v_mfma_f32_16x16x32_bf16 v[200:203], v[244:247], v[236:239], v[200:203]\n\t" \
    "ds_read_b128 v[244:247], %[wl] offset:0\n\t" \
    "v_mfma_f32_16x16x32_bf16 v[200:203], v[248:251], v[240:243], v[200:203]\n\t" \
    "ds_read_b128 v[248:251], %[wl] offset:1024\n\t" \
    "v_mfma_f32_16x16x32_bf16 v[204:207], v[160:163], v[236:239], v[204:207]\n\t" \
    "v_mfma_f32_16x16x32_bf16 v[204:207], v[164:167], v[240:243], v[204:207]\n\t" \
    "v_mfma_f32_16x16x32_bf16 v[208:211], v[168:171], v[236:239], v[208:211]\n\t" \
    "ds_read_b128 v[236:239], %[xa] offset:17408\n\t" \
    "v_mfma_f32_16x16x32_bf16 v[208:211], v[172:175], v[240:243], v[208:211]\n\t" \
    "v_xor_b32 v252, 0x800, %[xa]\n\t" \
    "ds_read_b128 v[240:243], v252 offset:16384\n\t" \
    "s_waitcnt lgkmcnt(6)\n\t" \
    "v_mfma_f32_16x16x32_bf16 v[208:211], v[8:11], v[224:227], v[208:211]\n\t" \
    "s_waitcnt lgkmcnt(5)\n\t" \
    "v_mfma_f32_16x16x32_bf16 v[208:211], v[12:15], v[228:231], v[208:211]\n\t" \
    "v_mfma_f32_16x16x32_bf16 v[204:207], v[0:3], v[224:227], v[204:207]\n\t" \
    "v_mfma_f32_16x16x32_bf16 v[204:207], v[4:7], v[228:231], v[204:207]\n\t" \
    "s_waitcnt lgkmcnt(3)\n\t" \
    "v_mfma_f32_16x16x32_bf16 v[200:203], v[244:247], v[224:227], v[200:203]\n\t" \
    "ds_read_b128 v[224:227], v252 offset:17408\n\t" \
    "s_waitcnt lgkmcnt(3)\n\t" \
    "v_mfma_f32_16x16x32_bf16 v[200:203], v[248:251], v[228:231], v[200:203]\n\t" \
    "ds_read_b128 v[228:231], v252 offset:0\n\t" \
    "v_mfma_f32_16x16x32_bf16 v[212:215], v[244:247], v[232:235], v[212:215]\n\t" \
    "ds_read_b128 v[244:247], %[wl] offset:2048\n\t" \
    "s_waitcnt lgkmcnt(4)\n\t" \
    "v_mfma_f32_16x16x32_bf16 v[212:215], v[248:251], v[236:239], v[212:215]\n\t" \
    "ds_read_b128 v[248:251], %[wl] offset:3072\n\t" \
    "v_mfma_f32_16x16x32_bf16 v[216:219], v[0:3], v[232:235], v[216:219]\n\t" \
    "v_mfma_f32_16x16x32_bf16 v[216:219], v[4:7], v[236:239], v[216:219]\n\t" \
    "v_mfma_f32_16x16x32_bf16 v[220:223], v[8:11], v[232:235], v[220:223]\n\t" \
    "ds_read_b128 v[232:235], v252 offset:1024\n\t" \
    "v_mfma_f32_16x16x32_bf16 v[220:223], v[12:15], v[236:239], v[220:223]\n\t" \
    "v_xor_b32 v252, 0x1000, %[xa]\n\t" \
    "ds_read_b128 v[236:239], v252 offset:0\n\t" \
    "s_waitcnt lgkmcnt(6)\n\t" \
    "v_mfma_f32_16x16x32_bf16 v[220:223], v[32:35], v[240:243], v[220:223]\n\t" \
    "s_waitcnt lgkmcnt(5)\n\t" \
    "v_mfma_f32_16x16x32_bf16 v[220:223], v[36:39], v[224:227], v[220:223]\n\t" \
    "v_mfma_f32_16x16x32_bf16 v[216:219], v[24:27], v[240:243], v[216:219]\n\t" \
    "v_mfma_f32_16x16x32_bf16 v[216:219], v[28:31], v[224:227], v[216:219]\n\t" \
    "v_mfma_f32_16x16x32_bf16 v[212:215], v[16:19], v[240:243], v[212:215]\n\t" \
    "ds_read_b128 v[240:243], v252 offset:1024\n\t" \
    "v_mfma_f32_16x16x32_bf16 v[212:215], v[20:23], v[224:227], v[212:215]\n\t" \
    "ds_read_b128 v[224:227], v252 offset:16384\n\t" \
    "s_waitcnt lgkmcnt(6)\n\t" \
    "v_mfma_f32_16x16x32_bf16 v[200:203], v[16:19], v[228:231], v[200:203]\n\t" \
    "s_waitcnt lgkmcnt(3)\n\t" \
    "v_mfma_f32_16x16x32_bf16 v[200:203], v[20:23], v[232:235], v[200:203]\n\t" \
    "v_mfma_f32_16x16x32_bf16 v[204:207], v[24:27], v[228:231], v[204:207]\n\t" \
    "v_mfma_f32_16x16x32_bf16 v[204:207], v[28:31], v[232:235], v[204:207]\n\t" \
    "v_mfma_f32_16x16x32_bf16 v[208:211], v[32:35], v[228:231], v[208:211]\n\t" \
    "ds_read_b128 v[228:231], v252 offset:17408\n\t" \
    "v_mfma_f32_16x16x32_bf16 v[208:211], v[36:39], v[232:235], v[208:211]\n\t" \
    "v_xor_b32 v252, 0x1800, %[xa]\n\t" \
    "ds_read_b128 v[232:235], v252 offset:16384\n\t" \
    "s_waitcnt lgkmcnt(4)\n\t" \
    "v_mfma_f32_16x16x32_bf16 v[208:211], v[48:51], v[236:239], v[208:211]\n\t" \
    "s_waitcnt lgkmcnt(3)\n\t" \
    "v_mfma_f32_16x16x32_bf16 v[208:211], v[52:55], v[240:243], v[208:211]\n\t" \
    "v_mfma_f32_16x16x32_bf16 v[204:207], v[40:43], v[236:239], v[204:207]\n\t" \
    "v_mfma_f32_16x16x32_bf16 v[204:207], v[44:47], v[240:243], v[204:207]\n\t" \
    "v_mfma_f32_16x16x32_bf16 v[200:203], v[244:247], v[236:239], v[200:203]\n\t" \
    "ds_read_b128 v[236:239], v252 offset:17408\n\t" \
    "v_mfma_f32_16x16x32_bf16 v[200:203], v[248:251], v[240:243], v[200:203]\n\t" \
    "ds_read_b128 v[240:243], v252 offset:0\n\t" \
    "s_waitcnt lgkmcnt(4)\n\t" \
    "v_mfma_f32_16x16x32_bf16 v[212:215], v[244:247], v[224:227], v[212:215]\n\t" \
    "s_waitcnt lgkmcnt(3)\n\t" \
    "v_mfma_f32_16x16x32_bf16 v[212:215], v[248:251], v[228:231], v[212:215]\n\t" \
    "v_mfma_f32_16x16x32_bf16 v[216:219], v[40:43], v[224:227], v[216:219]\n\t" \
    "v_mfma_f32_16x16x32_bf16 v[216:219], v[44:47], v[228:231], v[216:219]\n\t" \
    "v_mfma_f32_16x16x32_bf16 v[220:223], v[48:51], v[224:227], v[220:223]\n\t" \
    "ds_read_b128 v[224:227], v252 offset:1024\n\t" \
    "v_mfma_f32_16x16x32_bf16 v[220:223], v[52:55], v[228:231], v[220:223]\n\t" \
    "v_and_b32 v252, 0x3ff, %[xa]\n\t" \
    "ds_read_b128 v[228:231], v252 offset:43008\n\t" \
    "s_waitcnt lgkmcnt(4)\n\t" \
    "v_mfma_f32_16x16x32_bf16 v[220:223], v[72:75], v[232:235], v[220:223]\n\t" \
    "s_waitcnt lgkmcnt(3)\n\t" \
    "v_mfma_f32_16x16x32_bf16 v[220:223], v[76:79], v[236:239], v[220:223]\n\t" \
    "v_mfma_f32_16x16x32_bf16 v[216:219], v[64:67], v[232:235], v[216:219]\n\t" \
    "v_mfma_f32_16x16x32_bf16 v[216:219], v[68:71], v[236:239], v[216:219]\n\t" \
    "v_mfma_f32_16x16x32_bf16 v[212:215], v[56:59], v[232:235], v[212:215]\n\t" \
    "ds_read_b128 v[232:235], v252 offset:44032\n\t" \
    "v_mfma_f32_16x16x32_bf16 v[212:215], v[60:63], v[236:239], v[212:215]\n\t" \
    "ds_read_b128 v[236:239], v252 offset:47104\n\t" \
    "s_waitcnt lgkmcnt(4)\n\t" \
    "v_mfma_f32_16x16x32_bf16 v[200:203], v[56:59], v[240:243], v[200:203]\n\t" \
    "s_waitcnt lgkmcnt(3)\n\t" \
    "v_mfma_f32_16x16x32_bf16 v[200:203], v[60:63], v[224:227], v[200:203]\n\t" \
    "v_mfma_f32_16x16x32_bf16 v[204:207], v[64:67], v[240:243], v[204:207]\n\t" \
    "v_mfma_f32_16x16x32_bf16 v[204:207], v[68:71], v[224:227], v[204:207]\n\t" \
    "v_mfma_f32_16x16x32_bf16 v[208:211], v[72:75], v[240:243], v[208:211]\n\t" \
    "ds_read_b128 v[240:243], v252 offset:48128\n\t" \
    "v_mfma_f32_16x16x32_bf16 v[208:211], v[76:79], v[224:227], v[208:211]\n\t" \
    "s_waitcnt lgkmcnt(3)\n\t" \
    "v_mfma_f32_16x16x32_bf16 v[208:211], v[192:195], v[228:231], v[208:211]\n\t" \
    "v_add_u32 v252, %[qs], v252\n\t" \
    "s_waitcnt lgkmcnt(2)\n\t" \
    "v_mfma_f32_16x16x32_bf16 v[208:211], v[196:199], v[232:235], v[208:211]\n\t" \
    "v_mfma_f32_16x16x32_bf16 v[204:207], v[184:187], v[228:231], v[204:207]\n\t" \
    "v_mfma_f32_16x16x32_bf16 v[204:207], v[188:191], v[232:235], v[204:207]\n\t" \
    "v_mfma_f32_16x16x32_bf16 v[200:203], v[176:179], v[228:231], v[200:203]\n\t" \
    "v_mfma_f32_16x16x32_bf16 v[200:203], v[180:183], v[232:235], v[200:203]\n\t" \
    "s_waitcnt lgkmcnt(1)\n\t" \
    "v_mfma_f32_16x16x32_bf16 v[212:215], v[176:179], v[236:239], v[212:215]\n\t" \
    "v_cvt_pk_bf16_f32 v208, v208, v209\n\t" \
    "v_cvt_pk_bf16_f32 v209, v210, v211\n\t" \
    "s_waitcnt lgkmcnt(0)\n\t" \
    "v_mfma_f32_16x16x32_bf16 v[212:215], v[180:183], v[240:243], v[212:215]\n\t" \
    "ds_write_b64 v252, v[208:209] offset:2048\n\t" \
    "v_cvt_pk_bf16_f32 v204, v204, v205\n\t" \
    "v_mfma_f32_16x16x32_bf16 v[216:219], v[184:187], v[236:239], v[216:219]\n\t" \
    "v_cvt_pk_bf16_f32 v205, v206, v207\n\t" \
    "ds_write_b64 v252, v[204:205] offset:1024\n\t" \
    "v_mfma_f32_16x16x32_bf16 v[216:219], v[188:191], v[240:243], v[216:219]\n\t" \
    "v_cvt_pk_bf16_f32 v200, v200, v201\n\t" \
    "v_cvt_pk_bf16_f32 v201, v202, v203\n\t" \
    "v_mfma_f32_16x16x32_bf16 v[220:223], v[192:195], v[236:239], v[220:223]\n\t" \
    "ds_write_b64 v252, v[200:201] offset:0\n\t" \
    "v_mfma_f32_16x16x32_bf16 v[220:223], v[196:199], v[240:243], v[220:223]\n\t" \
    "v_cvt_pk_bf16_f32 v212, v212, v213\n\t" \
    "v_cvt_pk_bf16_f32 v213, v214, v215\n\t" \
    "ds_write_b64 v252, v[212:213] offset:6144\n\t" \
    "v_cvt_pk_bf16_f32 v216, v216, v217\n\t" \
    "v_cvt_pk_bf16_f32 v217, v218, v219\n\t" \
    "ds_write_b64 v252, v[216:217] offset:7168\n\t" \
    "s_nop 1\n\t" \
    "v_cvt_pk_bf16_f32 v220, v220, v221\n\t" \
    "v_cvt_pk_bf16_f32 v221, v222, v223\n\t" \
    "ds_write_b64 v252, v[220:221] offset:8192\n\t" \
    "s_waitcnt lgkmcnt(0)\n\t"
#define ASM_CRIT1_3 \
    "s_branch .Lc0%=\n\t" \
    ".Lcr%=:\n\t" \
    "s_cmp_eq_u32 %[n], 0\n\t" \
    "s_cbranch_scc1 .Lcb%=\n\t" \
    "s_sub_u32 %[n], %[n], 1\n\t" \
    "v_max_u32 v243, v208, v209\n\t" \
    "v_max3_u32 v243, v210, v211, v243\n\t" \
    "v_cmp_eq_u32 vcc, -1, v243\n\t" \
    "s_cbranch_vccz .Lcs0_%=\n\t" \
    "buffer_load_dwordx4 v[208:211], %[ab], %[rs], %[s0] offen sc1\n\t" \
    ".Lcs0_%=:\n\t" \
    "v_max_u32 v243, v236, v237\n\t" \
    "v_max3_u32 v243, v238, v239, v243\n\t" \
    "v_cmp_eq_u32 vcc, -1, v243\n\t" \
    "s_cbranch_vccz .Lcs1_%=\n\t" \
    "buffer_load_dwordx4 v[236:239], %[ab], %[rs], %[s1] offen sc1\n\t" \
    ".Lcs1_%=:\n\t" \
    "v_max_u32 v243, v220, v221\n\t" \
    "v_max3_u32 v243, v222, v223, v243\n\t" \
    "v_cmp_eq_u32 vcc, -1, v243\n\t" \
    "s_cbranch_vccz .Lca%=\n\t" \
    "buffer_load_dwordx4 v[220:223], %[ab], %[rs], %[s2] offen sc1\n\t" \
    "s_branch .Lca%=\n\t" \
    ".Lc0%=:\n\t" \
    "buffer_load_dwordx4 v[208:211], %[ab], %[rs], %[s0] offen sc1\n\t" \
    "buffer_load_dwordx4 v[236:239], %[ab], %[rs], %[s1] offen sc1\n\t" \
    "buffer_load_dwordx4 v[220:223], %[ab], %[rs], %[s2] offen sc1\n\t" \
    "v_bfe_i32 v241, %[ab], 7, 1\n\t" \
    "v_and_b32 v241, 0xaaaa, v241\n\t" \
    "v_or_b32 v241, 0x4444, v241\n\t" \
    "v_xor_b32 v212, 0x400, %[ab]\n\t" \
    "v_xor_b32 v216, 0x800, %[ab]\n\t" \
    "v_xor_b32 v242, 0xc00, %[ab]\n\t" \
    "v_xor_b32 v240, 0x1400, %[ab]\n\t" \
    ".Lca%=:\n\t" \
    "s_waitcnt vmcnt(0)\n\t" \
    "ds_write_b128 %[ab], v[208:211] offset:0\n\t" \
    "ds_write_b128 %[ab], v[236:239] offset:4096\n\t" \
    "ds_write_b128 %[ab], v[220:223] offset:8192\n\t" \
    "v_max_u32 v243, v208, v209\n\t" \
    "v_max3_u32 v243, v210, v211, v243\n\t" \
    "v_max3_u32 v243, v236, v237, v243\n\t" \
    "v_max3_u32 v243, v238, v239, v243\n\t" \
    "v_max3_u32 v243, v220, v221, v243\n\t" \
    "v_max3_u32 v243, v222, v223, v243\n\t" \
    "v_cmp_eq_u32 vcc, -1, v243\n\t" \
    "s_cbranch_vccnz .Lcr%=\n\t" \
    ".Lcb%=:\n\t" \
    PRIUP \
    ASM_ST2 \
    "v_smfmac_f32_16x16x64_bf16 v[224:227], v[208:211], v[200:207], v241\n\t" \
    "ds_read_b128 v[200:203], %[wl] offset:4096\n\t" \
    "ds_read_b128 v[204:207], %[wl] offset:5120\n\t" \
    "v_smfmac_f32_16x16x64_bf16 v[244:247], v[208:211], v[0:7], v241\n\t" \
    "v_smfmac_f32_16x16x64_bf16 v[248:251], v[208:211], v[8:15], v241\n\t" \
    "s_waitcnt lgkmcnt(2)\n\t" \
    "s_barrier\n\t" ASM_ST \
    "ds_read_b128 v[212:215], v212 offset:0\n\t" \
    "ds_read_b128 v[216:219], v216 offset:0\n\t" \
    "ds_read_b128 v[220:223], v242 offset:0\n\t" \
    "ds_read_b128 v[208:211], v240 offset:0\n\t" \
    "v_smfmac_f32_16x16x64_bf16 v[244:247], v[236:239], v[80:87], v241\n\t" \
    "v_xor_b32 v240, 0x1800, %[ab]\n\t" \
    "v_smfmac_f32_16x16x64_bf16 v[248:251], v[236:239], v[88:95], v241\n\t" \
    "s_waitcnt lgkmcnt(4)\n\t" \
    "v_smfmac_f32_16x16x64_bf16 v[224:227], v[236:239], v[200:207], v241\n\t" \
    "ds_read_b128 v[200:203], %[wl] offset:6144\n\t" \
    "ds_read_b128 v[204:207], %[wl] offset:7168\n\t" \
    "ds_read_b128 v[236:239], v240 offset:0\n\t" \
    "s_waitcnt lgkmcnt(6)\n\t" \
    "v_smfmac_f32_16x16x64_bf16 v[224:227], v[212:215], v[16:23], v241\n\t" \
    "v_smfmac_f32_16x16x64_bf16 v[244:247], v[212:215], v[24:31], v241\n\t" \
    "v_xor_b32 v240, 0x1c00, %[ab]\n\t" \
    "v_smfmac_f32_16x16x64_bf16 v[248:251], v[212:215], v[32:39], v241\n\t" \
    "ds_read_b128 v[212:215], v240 offset:0\n\t" \
    "s_waitcnt lgkmcnt(6)\n\t" \
    "v_smfmac_f32_16x16x64_bf16 v[224:227], v[216:219], v[228:235], v241\n\t" \
    "ds_read_b128 v[228:231], %[wl] offset:8192\n\t" \
    "ds_read_b128 v[232:235], %[wl] offset:9216\n\t" \
    "v_smfmac_f32_16x16x64_bf16 v[244:247], v[216:219], v[40:47], v241\n\t" \
    "v_and_b32 v240, 0x3ff, %[ab]\n\t" \
    "v_smfmac_f32_16x16x64_bf16 v[248:251], v[216:219], v[48:55], v241\n\t" \
    "ds_read_b128 v[216:219], v240 offset:8192\n\t" \
    "s_waitcnt lgkmcnt(8)\n\t" \
    "v_smfmac_f32_16x16x64_bf16 v[224:227], v[220:223], v[56:63], v241\n\t" \
    "v_smfmac_f32_16x16x64_bf16 v[244:247], v[220:223], v[64:71], v241\n\t" \
    "v_smfmac_f32_16x16x64_bf16 v[248:251], v[220:223], v[72:79], v241\n\t" \
    "ds_read_b128 v[220:223], v240 offset:9216\n\t" \
    "s_waitcnt lgkmcnt(8)\n\t" \
    "v_smfmac_f32_16x16x64_bf16 v[244:247], v[208:211], v[104:111], v241\n\t" \
    "s_waitcnt lgkmcnt(5)\n\t" \
    "v_smfmac_f32_16x16x64_bf16 v[244:247], v[236:239], v[120:127], v241\n\t" \
    "s_waitcnt lgkmcnt(4)\n\t" \
    "v_smfmac_f32_16x16x64_bf16 v[244:247], v[212:215], v[144:151], v241\n\t" \
    "s_waitcnt lgkmcnt(1)\n\t" \
    "v_smfmac_f32_16x16x64_bf16 v[244:247], v[216:219], v[160:167], v241\n\t" \
    "s_waitcnt lgkmcnt(0)\n\t" \
    "v_smfmac_f32_16x16x64_bf16 v[244:247], v[220:223], v[184:191], v241\n\t" \
    "v_smfmac_f32_16x16x64_bf16 v[248:251], v[208:211], v[112:119], v241\n\t" \
    "v_smfmac_f32_16x16x64_bf16 v[248:251], v[236:239], v[128:135], v241\n\t" \
    "v_smfmac_f32_16x16x64_bf16 v[248:251], v[212:215], v[152:159], v241\n\t" \
    "v_permlane32_swap_b32 v244, v246\n\t" \
    "v_permlane32_swap_b32 v245, v247\n\t" \
    "v_smfmac_f32_16x16x64_bf16 v[248:251], v[216:219], v[168:175], v241\n\t" \
    "v_add_f32 v244, v244, v246\n\t" \
    "v_add_f32 v245, v245, v247\n\t" \
    "v_smfmac_f32_16x16x64_bf16 v[248:251], v[220:223], v[192:199], v241\n\t" \
    "v_cvt_pk_bf16_f32 v244, v244, v245\n\t" \
    "ds_write_b16 %[dst], v244 offset:512\n\t" \
    "ds_write_b16_d16_hi %[dst], v244 offset:528\n\t" \
    "v_smfmac_f32_16x16x64_bf16 v[224:227], v[208:211], v[96:103], v241\n\t" \
    "v_smfmac_f32_16x16x64_bf16 v[224:227], v[236:239], v[200:207], v241\n\t" \
    "v_smfmac_f32_16x16x64_bf16 v[224:227], v[212:215], v[136:143], v241\n\t" \
    "v_permlane32_swap_b32 v248, v250\n\t" \
    "v_permlane32_swap_b32 v249, v251\n\t" \
    "v_smfmac_f32_16x16x64_bf16 v[224:227], v[216:219], v[228:235], v241\n\t" \
    "v_add_f32 v248, v248, v250\n\t" \
    "v_add_f32 v249, v249, v251\n\t" \
    "v_smfmac_f32_16x16x64_bf16 v[224:227], v[220:223], v[176:183], v241\n\t" \
    "v_cvt_pk_bf16_f32 v248, v248, v249\n\t" \
    "ds_write_b16 %[dst], v248 offset:1024\n\t" \
    "ds_write_b16_d16_hi %[dst], v248 offset:1040\n\t" \
    "s_nop 4\n\t" \
    "v_permlane32_swap_b32 v224, v226\n\t" \
    "v_permlane32_swap_b32 v225, v227\n\t" \
    "v_add_f32 v224, v224, v226\n\t" \
    "v_add_f32 v225, v225, v227\n\t" \
    "v_cvt_pk_bf16_f32 v224, v224, v225\n\t" \
    "ds_write_b16 %[dst], v224 offset:0\n\t" \
    "ds_write_b16_d16_hi %[dst], v224 offset:16\n\t"
#define ASM_CRIT1_2 \
    "s_branch .Lc0%=\n\t" \
    ".Lcr%=:\n\t" \
    "s_cmp_eq_u32 %[n], 0\n\t" \
    "s_cbranch_scc1 .Lcb%=\n\t" \
    "s_sub_u32 %[n], %[n], 1\n\t" \
    "v_max_u32 v243, v208, v209\n\t" \
    "v_max3_u32 v243, v210, v211, v243\n\t" \
    "v_cmp_eq_u32 vcc, -1, v243\n\t" \
    "s_cbranch_vccz .Lcs0_%=\n\t" \
    "buffer_load_dwordx4 v[208:211], %[ab], %[rs], %[s0] offen sc1\n\t" \
    ".Lcs0_%=:\n\t" \
    "v_max_u32 v243, v236, v237\n\t" \
    "v_max3_u32 v243, v238, v239, v243\n\t" \
    "v_cmp_eq_u32 vcc, -1, v243\n\t" \
    "s_cbranch_vccz .Lca%=\n\t" \
    "buffer_load_dwordx4 v[236:239], %[ab], %[rs], %[s1] offen sc1\n\t" \
    "s_branch .Lca%=\n\t" \
    ".Lc0%=:\n\t" \
    "buffer_load_dwordx4 v[208:211], %[ab], %[rs], %[s0] offen sc1\n\t" \
    "buffer_load_dwordx4 v[236:239], %[ab], %[rs], %[s1] offen sc1\n\t" \
    "v_bfe_i32 v241, %[ab], 7, 1\n\t" \
    "v_and_b32 v241, 0xaaaa, v241\n\t" \
    "v_or_b32 v241, 0x4444, v241\n\t" \
    "v_xor_b32 v212, 0x400, %[ab]\n\t" \
    "v_xor_b32 v216, 0x800, %[ab]\n\t" \
    "v_xor_b32 v242, 0xc00, %[ab]\n\t" \
    "v_xor_b32 v240, 0x1400, %[ab]\n\t" \
    ".Lca%=:\n\t" \
    "s_waitcnt vmcnt(0)\n\t" \
    "ds_write_b128 %[ab], v[208:211] offset:0\n\t" \
    "ds_write_b128 %[ab], v[236:239] offset:4096\n\t" \
    "v_max_u32 v243, v208, v209\n\t" \
    "v_max3_u32 v243, v210, v211, v243\n\t" \
    "v_max3_u32 v243, v236, v237, v243\n\t" \
    "v_max3_u32 v243, v238, v239, v243\n\t" \
    "v_cmp_eq_u32 vcc, -1, v243\n\t" \
    "s_cbranch_vccnz .Lcr%=\n\t" \
    ".Lcb%=:\n\t" \
    PRIUP \
    ASM_ST2 \
    "v_smfmac_f32_16x16x64_bf16 v[224:227], v[208:211], v[200:207], v241\n\t" \
    "ds_read_b128 v[200:203], %[wl] offset:4096\n\t" \
    "ds_read_b128 v[204:207], %[wl] offset:5120\n\t" \
    "v_smfmac_f32_16x16x64_bf16 v[244:247], v[208:211], v[0:7], v241\n\t" \
    "v_smfmac_f32_16x16x64_bf16 v[248:251], v[208:211], v[8:15], v241\n\t" \
    "s_waitcnt lgkmcnt(2)\n\t" \
    "s_barrier\n\t" ASM_ST \
    "ds_read_b128 v[212:215], v212 offset:0\n\t" \
    "ds_read_b128 v[216:219], v216 offset:0\n\t" \
    "ds_read_b128 v[220:223], v242 offset:0\n\t" \
    "ds_read_b128 v[208:211], v240 offset:0\n\t" \
    "v_smfmac_f32_16x16x64_bf16 v[244:247], v[236:239], v[80:87], v241\n\t" \
    "v_xor_b32 v240, 0x1800, %[ab]\n\t" \
    "v_smfmac_f32_16x16x64_bf16 v[248:251], v[236:239], v[88:95], v241\n\t" \
    "s_waitcnt lgkmcnt(4)\n\t" \
    "v_smfmac_f32_16x16x64_bf16 v[224:227], v[236:239], v[200:207], v241\n\t" \
    "ds_read_b128 v[200:203], %[wl] offset:6144\n\t" \
    "ds_read_b128 v[204:207], %[wl] offset:7168\n\t" \
    "ds_read_b128 v[236:239], v240 offset:0\n\t" \
    "s_waitcnt lgkmcnt(6)\n\t" \
    "v_smfmac_f32_16x16x64_bf16 v[224:227], v[212:215], v[16:23], v241\n\t" \
    "v_smfmac_f32_16x16x64_bf16 v[244:247], v[212:215], v[24:31], v241\n\t" \
    "v_xor_b32 v240, 0x1c00, %[ab]\n\t" \
    "v_smfmac_f32_16x16x64_bf16 v[248:251], v[212:215], v[32:39], v241\n\t" \
    "ds_read_b128 v[212:215], v240 offset:0\n\t" \
    "s_waitcnt lgkmcnt(6)\n\t" \
    "v_smfmac_f32_16x16x64_bf16 v[224:227], v[216:219], v[228:235], v241\n\t" \
    "ds_read_b128 v[228:231], %[wl] offset:8192\n\t" \
    "ds_read_b128 v[232:235], %[wl] offset:9216\n\t" \
    "v_smfmac_f32_16x16x64_bf16 v[244:247], v[216:219], v[40:47], v241\n\t" \
    "v_and_b32 v240, 0x3ff, %[ab]\n\t" \
    "v_smfmac_f32_16x16x64_bf16 v[248:251], v[216:219], v[48:55], v241\n\t" \
    "ds_read_b128 v[216:219], v240 offset:8192\n\t" \
    "s_waitcnt lgkmcnt(8)\n\t" \
    "v_smfmac_f32_16x16x64_bf16 v[224:227], v[220:223], v[56:63], v241\n\t" \
    "v_smfmac_f32_16x16x64_bf16 v[244:247], v[220:223], v[64:71], v241\n\t" \
    "v_smfmac_f32_16x16x64_bf16 v[248:251], v[220:223], v[72:79], v241\n\t" \
    "ds_read_b128 v[220:223], v240 offset:9216\n\t" \
    "s_waitcnt lgkmcnt(8)\n\t" \
    "v_smfmac_f32_16x16x64_bf16 v[244:247], v[208:211], v[104:111], v241\n\t" \
    "s_waitcnt lgkmcnt(5)\n\t" \
    "v_smfmac_f32_16x16x64_bf16 v[244:247], v[236:239], v[120:127], v241\n\t" \
    "s_waitcnt lgkmcnt(4)\n\t" \
    "v_smfmac_f32_16x16x64_bf16 v[244:247], v[212:215], v[144:151], v241\n\t" \
    "s_waitcnt lgkmcnt(1)\n\t" \
    "v_smfmac_f32_16x16x64_bf16 v[244:247], v[216:219], v[160:167], v241\n\t" \
    "s_waitcnt lgkmcnt(0)\n\t" \
    "v_smfmac_f32_16x16x64_bf16 v[244:247], v[220:223], v[184:191], v241\n\t" \
    "v_smfmac_f32_16x16x64_bf16 v[248:251], v[208:211], v[112:119], v241\n\t" \
    "v_smfmac_f32_16x16x64_bf16 v[248:251], v[236:239], v[128:135], v241\n\t" \
    "v_smfmac_f32_16x16x64_bf16 v[248:251], v[212:215], v[152:159], v241\n\t" \
    "v_permlane32_swap_b32 v244, v246\n\t" \
    "v_permlane32_swap_b32 v245, v247\n\t" \
    "v_smfmac_f32_16x16x64_bf16 v[248:251], v[216:219], v[168:175], v241\n\t" \
    "v_add_f32 v244, v244, v246\n\t" \
    "v_add_f32 v245, v245, v247\n\t" \
    "v_smfmac_f32_16x16x64_bf16 v[248:251], v[220:223], v[192:199], v241\n\t" \
    "v_cvt_pk_bf16_f32 v244, v244, v245\n\t" \
    "ds_write_b16 %[dst], v244 offset:512\n\t" \
    "ds_write_b16_d16_hi %[dst], v244 offset:528\n\t" \
    "v_smfmac_f32_16x16x64_bf16 v[224:227], v[208:211], v[96:103], v241\n\t" \
    "v_smfmac_f32_16x16x64_bf16 v[224:227], v[236:239], v[200:207], v241\n\t" \
    "v_smfmac_f32_16x16x64_bf16 v[224:227], v[212:215], v[136:143], v241\n\t" \
    "v_permlane32_swap_b32 v248, v250\n\t" \
    "v_permlane32_swap_b32 v249, v251\n\t" \
    "v_smfmac_f32_16x16x64_bf16 v[224:227], v[216:219], v[228:235], v241\n\t" \
    "v_add_f32 v248, v248, v250\n\t" \
    "v_add_f32 v249, v249, v251\n\t" \
    "v_smfmac_f32_16x16x64_bf16 v[224:227], v[220:223], v[176:183], v241\n\t" \
    "v_cvt_pk_bf16_f32 v248, v248, v249\n\t" \
    "ds_write_b16 %[dst], v248 offset:1024\n\t" \
    "ds_write_b16_d16_hi %[dst], v248 offset:1040\n\t" \
    "s_nop 4\n\t" \
    "v_permlane32_swap_b32 v224, v226\n\t" \
    "v_permlane32_swap_b32 v225, v227\n\t" \
    "v_add_f32 v224, v224, v226\n\t" \
    "v_add_f32 v225, v225, v227\n\t" \
    "v_cvt_pk_bf16_f32 v224, v224, v225\n\t" \
    "ds_write_b16 %[dst], v224 offset:0\n\t" \
    "ds_write_b16_d16_hi %[dst], v224 offset:16\n\t"
#define ASM_CRIT1_N \
    "v_bfe_i32 v241, %[ab], 7, 1\n\t" \
    "v_and_b32 v241, 0xaaaa, v241\n\t" \
    "v_or_b32 v241, 0x4444, v241\n\t" \
    "v_xor_b32 v212, 0x400, %[ab]\n\t" \
    "v_xor_b32 v216, 0x800, %[ab]\n\t" \
    "v_xor_b32 v242, 0xc00, %[ab]\n\t" \
    "v_xor_b32 v240, 0x1400, %[ab]\n\t" \
    "v_xor_b32 v236, 0x1000, %[ab]\n\t" \
    "s_waitcnt lgkmcnt(0)\n\t" \
    "s_barrier\n\t" ASM_ST \
    "ds_read_b128 v[208:211], %[ab] offset:0\n\t" \
    "ds_read_b128 v[212:215], v212 offset:0\n\t" \
    "ds_read_b128 v[216:219], v216 offset:0\n\t" \
    "ds_read_b128 v[220:223], v242 offset:0\n\t" \
    "ds_read_b128 v[236:239], v236 offset:0\n\t" \
    "s_waitcnt lgkmcnt(4)\n\t" \
    "v_smfmac_f32_16x16x64_bf16 v[224:227], v[208:211], v[200:207], v241\n\t" \
    "ds_read_b128 v[200:203], %[wl] offset:4096\n\t" \
    "ds_read_b128 v[204:207], %[wl] offset:5120\n\t" \
    "v_smfmac_f32_16x16x64_bf16 v[244:247], v[208:211], v[0:7], v241\n\t" \
    "v_smfmac_f32_16x16x64_bf16 v[248:251], v[208:211], v[8:15], v241\n\t" \
    "ds_read_b128 v[208:211], v240 offset:0\n\t" \
    "s_waitcnt lgkmcnt(6)\n\t" \
    "v_smfmac_f32_16x16x64_bf16 v[224:227], v[212:215], v[16:23], v241\n\t" \
    "v_smfmac_f32_16x16x64_bf16 v[244:247], v[212:215], v[24:31], v241\n\t" \
    "v_xor_b32 v240, 0x1800, %[ab]\n\t" \
    "v_smfmac_f32_16x16x64_bf16 v[248:251], v[212:215], v[32:39], v241\n\t" \
    "ds_read_b128 v[212:215], v240 offset:0\n\t" \
    "s_waitcnt lgkmcnt(6)\n\t" \
    "v_smfmac_f32_16x16x64_bf16 v[224:227], v[216:219], v[228:235], v241\n\t" \
    "ds_read_b128 v[228:231], %[wl] offset:6144\n\t" \
    "ds_read_b128 v[232:235], %[wl] offset:7168\n\t" \
    "v_smfmac_f32_16x16x64_bf16 v[244:247], v[216:219], v[40:47], v241\n\t" \
    "v_xor_b32 v240, 0x1c00, %[ab]\n\t" \
    "v_smfmac_f32_16x16x64_bf16 v[248:251], v[216:219], v[48:55], v241\n\t" \
    "ds_read_b128 v[216:219], v240 offset:0\n\t" \
    "s_waitcnt lgkmcnt(8)\n\t" \
    "v_smfmac_f32_16x16x64_bf16 v[224:227], v[220:223], v[56:63], v241\n\t" \
    "v_smfmac_f32_16x16x64_bf16 v[244:247], v[220:223], v[64:71], v241\n\t" \
    "v_and_b32 v240, 0x3ff, %[ab]\n\t" \
    "v_smfmac_f32_16x16x64_bf16 v[248:251], v[220:223], v[72:79], v241\n\t" \
    "ds_read_b128 v[220:223], v240 offset:8192\n\t" \
    "s_waitcnt lgkmcnt(6)\n\t" \
    "v_smfmac_f32_16x16x64_bf16 v[224:227], v[236:239], v[200:207], v241\n\t" \
    "ds_read_b128 v[200:203], %[wl] offset:8192\n\t" \
    "ds_read_b128 v[204:207], %[wl] offset:9216\n\t" \
    "v_smfmac_f32_16x16x64_bf16 v[244:247], v[236:239], v[80:87], v241\n\t" \
    "v_smfmac_f32_16x16x64_bf16 v[248:251], v[236:239], v[88:95], v241\n\t" \
    "ds_read_b128 v[236:239], v240 offset:9216\n\t" \
    "s_waitcnt lgkmcnt(8)\n\t" \
    "v_smfmac_f32_16x16x64_bf16 v[244:247], v[208:211], v[104:111], v241\n\t" \
    "s_waitcnt lgkmcnt(7)\n\t" \
    "v_smfmac_f32_16x16x64_bf16 v[244:247], v[212:215], v[120:127], v241\n\t" \
    "s_waitcnt lgkmcnt(4)\n\t" \
    "v_smfmac_f32_16x16x64_bf16 v[244:247], v[216:219], v[144:151], v241\n\t" \
    "s_waitcnt lgkmcnt(3)\n\t" \
    "v_smfmac_f32_16x16x64_bf16 v[244:247], v[220:223], v[160:167], v241\n\t" \
    "s_waitcnt lgkmcnt(0)\n\t" \
    "v_smfmac_f32_16x16x64_bf16 v[244:247], v[236:239], v[184:191], v241\n\t" \
    "v_smfmac_f32_16x16x64_bf16 v[248:251], v[208:211], v[112:119], v241\n\t" \
    "v_smfmac_f32_16x16x64_bf16 v[248:251], v[212:215], v[128:135], v241\n\t" \
    "v_smfmac_f32_16x16x64_bf16 v[248:251], v[216:219], v[152:159], v241\n\t" \
    "v_permlane32_swap_b32 v244, v246\n\t" \
    "v_permlane32_swap_b32 v245, v247\n\t" \
    "v_smfmac_f32_16x16x64_bf16 v[248:251], v[220:223], v[168:175], v241\n\t" \
    "v_add_f32 v244, v244, v246\n\t" \
    "v_add_f32 v245, v245, v247\n\t" \
    "v_smfmac_f32_16x16x64_bf16 v[248:251], v[236:239], v[192:199], v241\n\t" \
    "v_cvt_pk_bf16_f32 v244, v244, v245\n\t" \
    "ds_write_b16 %[dst], v244 offset:512\n\t" \
    "ds_write_b16_d16_hi %[dst], v244 offset:528\n\t" \
    "v_smfmac_f32_16x16x64_bf16 v[224:227], v[208:211], v[96:103], v241\n\t" \
    "v_smfmac_f32_16x16x64_bf16 v[224:227], v[212:215], v[228:235], v241\n\t" \
    "v_smfmac_f32_16x16x64_bf16 v[224:227], v[216:219], v[136:143], v241\n\t" \
    "v_permlane32_swap_b32 v248, v250\n\t" \
    "v_permlane32_swap_b32 v249, v251\n\t" \
    "v_smfmac_f32_16x16x64_bf16 v[224:227], v[220:223], v[200:207], v241\n\t" \
    "v_add_f32 v248, v248, v250\n\t" \
    "v_add_f32 v249, v249, v251\n\t" \
    "v_smfmac_f32_16x16x64_bf16 v[224:227], v[236:239], v[176:183], v241\n\t" \
    "v_cvt_pk_bf16_f32 v248, v248, v249\n\t" \
    "ds_write_b16 %[dst], v248 offset:1024\n\t" \
    "ds_write_b16_d16_hi %[dst], v248 offset:1040\n\t" \
    "s_nop 4\n\t" \
    "v_permlane32_swap_b32 v224, v226\n\t" \
    "v_permlane32_swap_b32 v225, v227\n\t" \
    "v_add_f32 v224, v224, v226\n\t" \
    "v_add_f32 v225, v225, v227\n\t" \
    "v_cvt_pk_bf16_f32 v224, v224, v225\n\t" \
    "ds_write_b16 %[dst], v224 offset:0\n\t" \
    "ds_write_b16_d16_hi %[dst], v224 offset:16\n\t"
#define ASM_CRIT1_V3 \
    "s_branch .Lc0%=\n\t" \
    ".Lcr%=:\n\t" \
    "s_cmp_eq_u32 %[n], 0\n\t" \
    "s_cbranch_scc1 .Lcb%=\n\t" \
    "s_sub_u32 %[n], %[n], 1\n\t" \
    "v_max_u32 v243, v208, v209\n\t" \
    "v_max3_u32 v243, v210, v211, v243\n\t" \
    "v_cmp_eq_u32 vcc, -1, v243\n\t" \
    "s_cbranch_vccz .Lcs0_%=\n\t" \
    "buffer_load_dwordx4 v[208:211], %[ab], %[rs], %[s0] offen sc1\n\t" \
    ".Lcs0_%=:\n\t" \
    "v_max_u32 v243, v236, v237\n\t" \
    "v_max3_u32 v243, v238, v239, v243\n\t" \
    "v_cmp_eq_u32 vcc, -1, v243\n\t" \
    "s_cbranch_vccz .Lcs1_%=\n\t" \
    "buffer_load_dwordx4 v[236:239], %[ab], %[rs], %[s1] offen sc1\n\t" \
    ".Lcs1_%=:\n\t" \
    "v_max_u32 v243, v220, v221\n\t" \
    "v_max3_u32 v243, v222, v223, v243\n\t" \
    "v_cmp_eq_u32 vcc, -1, v243\n\t" \
    "s_cbranch_vccz .Lca%=\n\t" \
    "buffer_load_dwordx4 v[220:223], %[ab], %[rs], %[s2] offen sc1\n\t" \
    "s_branch .Lca%=\n\t" \
    ".Lc0%=:\n\t" \
    "buffer_load_dwordx4 v[208:211], %[ab], %[rs], %[s0] offen sc1\n\t" \
    "buffer_load_dwordx4 v[236:239], %[ab], %[rs], %[s1] offen sc1\n\t" \
    "buffer_load_dwordx4 v[220:223], %[ab], %[rs], %[s2] offen sc1\n\t" \
    "v_bfe_i32 v241, %[ab], 7, 1\n\t" \
    "v_and_b32 v241, 0xaaaa, v241\n\t" \
    "v_or_b32 v241, 0x4444, v241\n\t" \
    "v_xor_b32 v212, 0x400, %[ab]\n\t" \
    "v_xor_b32 v216, 0x800, %[ab]\n\t" \
    "v_xor_b32 v242, 0xc00, %[ab]\n\t" \
    "v_xor_b32 v240, 0x1400, %[ab]\n\t" \
    ".Lca%=:\n\t" \
    "s_waitcnt vmcnt(0)\n\t" \
    "ds_write_b128 %[ab], v[208:211] offset:0\n\t" \
    "ds_write_b128 %[ab], v[236:239] offset:4096\n\t" \
    "ds_write_b128 %[ab], v[220:223] offset:8192\n\t" \
    "v_max_u32 v243, v208, v209\n\t" \
    "v_max3_u32 v243, v210, v211, v243\n\t" \
    "v_max3_u32 v243, v236, v237, v243\n\t" \
    "v_max3_u32 v243, v238, v239, v243\n\t" \
    "v_max3_u32 v243, v220, v221, v243\n\t" \
    "v_max3_u32 v243, v222, v223, v243\n\t" \
    "v_cmp_eq_u32 vcc, -1, v243\n\t" \
    "s_cbranch_vccnz .Lcr%=\n\t" \
    ".Lcb%=:\n\t" \
    PRIUP \
    ASM_ST2 \
    "v_smfmac_f32_16x16x64_bf16 v[224:227], v[208:211], v[200:207], v241\n\t" \
    "ds_read_b128 v[200:203], %[wl] offset:4096\n\t" \
    "ds_read_b128 v[204:207], %[wl] offset:5120\n\t" \
    "v_smfmac_f32_16x16x64_bf16 v[244:247], v[208:211], v[0:7], v241\n\t" \
    "v_smfmac_f32_16x16x64_bf16 v[248:251], v[208:211], v[8:15], v241\n\t" \
    "s_waitcnt lgkmcnt(2)\n\t" \
    "buffer_load_dword %[fv], %[fv], %[rs], %[fs] offen sc1\n\t" \
    "s_barrier\n\t" ASM_ST \
    "ds_read_b128 v[212:215], v212 offset:0\n\t" \
    "ds_read_b128 v[216:219], v216 offset:0\n\t" \
    "ds_read_b128 v[220:223], v242 offset:0\n\t" \
    "ds_read_b128 v[208:211], v240 offset:0\n\t" \
    "v_smfmac_f32_16x16x64_bf16 v[244:247], v[236:239], v[80:87], v241\n\t" \
    "v_xor_b32 v240, 0x1800, %[ab]\n\t" \
    "v_smfmac_f32_16x16x64_bf16 v[248:251], v[236:239], v[88:95], v241\n\t" \
    "s_waitcnt lgkmcnt(4)\n\t" \
    "v_smfmac_f32_16x16x64_bf16 v[224:227], v[236:239], v[200:207], v241\n\t" \
    "ds_read_b128 v[200:203], %[wl] offset:6144\n\t" \
    "ds_read_b128 v[204:207], %[wl] offset:7168\n\t" \
    "ds_read_b128 v[236:239], v240 offset:0\n\t" \
    "s_waitcnt lgkmcnt(6)\n\t" \
    "v_smfmac_f32_16x16x64_bf16 v[224:227], v[212:215], v[16:23], v241\n\t" \
    "v_smfmac_f32_16x16x64_bf16 v[244:247], v[212:215], v[24:31], v241\n\t" \
    "v_xor_b32 v240, 0x1c00, %[ab]\n\t" \
    "v_smfmac_f32_16x16x64_bf16 v[248:251], v[212:215], v[32:39], v241\n\t" \
    "ds_read_b128 v[212:215], v240 offset:0\n\t" \
    "s_waitcnt lgkmcnt(6)\n\t" \
    "v_smfmac_f32_16x16x64_bf16 v[224:227], v[216:219], v[228:235], v241\n\t" \
    "ds_read_b128 v[228:231], %[wl] offset:8192\n\t" \
    "ds_read_b128 v[232:235], %[wl] offset:9216\n\t" \
    "v_smfmac_f32_16x16x64_bf16 v[244:247], v[216:219], v[40:47], v241\n\t" \
    "v_and_b32 v240, 0x3ff, %[ab]\n\t" \
    "v_smfmac_f32_16x16x64_bf16 v[248:251], v[216:219], v[48:55], v241\n\t" \
    "ds_read_b128 v[216:219], v240 offset:8192\n\t" \
    "s_waitcnt lgkmcnt(8)\n\t" \
    "v_smfmac_f32_16x16x64_bf16 v[224:227], v[220:223], v[56:63], v241\n\t" \
    "v_smfmac_f32_16x16x64_bf16 v[244:247], v[220:223], v[64:71], v241\n\t" \
    "v_smfmac_f32_16x16x64_bf16 v[248:251], v[220:223], v[72:79], v241\n\t" \
    "ds_read_b128 v[220:223], v240 offset:9216\n\t" \
    "s_waitcnt lgkmcnt(8)\n\t" \
    "v_smfmac_f32_16x16x64_bf16 v[244:247], v[208:211], v[104:111], v241\n\t" \
    "s_waitcnt lgkmcnt(5)\n\t" \
    "v_smfmac_f32_16x16x64_bf16 v[244:247], v[236:239], v[120:127], v241\n\t" \
    "s_waitcnt lgkmcnt(4)\n\t" \
    "v_smfmac_f32_16x16x64_bf16 v[244:247], v[212:215], v[144:151], v241\n\t" \
    "s_waitcnt lgkmcnt(1)\n\t" \
    "v_smfmac_f32_16x16x64_bf16 v[244:247], v[216:219], v[160:167], v241\n\t" \
    "s_waitcnt lgkmcnt(0)\n\t" \
    "v_smfmac_f32_16x16x64_bf16 v[244:247], v[220:223], v[184:191], v241\n\t" \
    "v_smfmac_f32_16x16x64_bf16 v[248:251], v[208:211], v[112:119], v241\n\t" \
    "v_smfmac_f32_16x16x64_bf16 v[248:251], v[236:239], v[128:135], v241\n\t" \
    "v_smfmac_f32_16x16x64_bf16 v[248:251], v[212:215], v[152:159], v241\n\t" \
    "v_permlane32_swap_b32 v244, v246\n\t" \
    "v_permlane32_swap_b32 v245, v247\n\t" \
    "v_smfmac_f32_16x16x64_bf16 v[248:251], v[216:219], v[168:175], v241\n\t" \
    "v_add_f32 v244, v244, v246\n\t" \
    "v_add_f32 v245, v245, v247\n\t" \
    "v_smfmac_f32_16x16x64_bf16 v[248:251], v[220:223], v[192:199], v241\n\t" \
    "v_cvt_pk_bf16_f32 v244, v244, v245\n\t" \
    "ds_write_b16 %[dst], v244 offset:512\n\t" \
    "ds_write_b16_d16_hi %[dst], v244 offset:528\n\t" \
    "v_smfmac_f32_16x16x64_bf16 v[224:227], v[208:211], v[96:103], v241\n\t" \
    "v_smfmac_f32_16x16x64_bf16 v[224:227], v[236:239], v[200:207], v241\n\t" \
    "v_smfmac_f32_16x16x64_bf16 v[224:227], v[212:215], v[136:143], v241\n\t" \
    "v_permlane32_swap_b32 v248, v250\n\t" \
    "v_permlane32_swap_b32 v249, v251\n\t" \
    "v_smfmac_f32_16x16x64_bf16 v[224:227], v[216:219], v[228:235], v241\n\t" \
    "v_add_f32 v248, v248, v250\n\t" \
    "v_add_f32 v249, v249, v251\n\t" \
    "v_smfmac_f32_16x16x64_bf16 v[224:227], v[220:223], v[176:183], v241\n\t" \
    "v_cvt_pk_bf16_f32 v248, v248, v249\n\t" \
    "ds_write_b16 %[dst], v248 offset:1024\n\t" \
    "ds_write_b16_d16_hi %[dst], v248 offset:1040\n\t" \
    "s_nop 4\n\t" \
    "v_permlane32_swap_b32 v224, v226\n\t" \
    "v_permlane32_swap_b32 v225, v227\n\t" \
    "v_add_f32 v224, v224, v226\n\t" \
    "v_add_f32 v225, v225, v227\n\t" \
    "v_cvt_pk_bf16_f32 v224, v224, v225\n\t" \
    "ds_write_b16 %[dst], v224 offset:0\n\t" \
    "ds_write_b16_d16_hi %[dst], v224 offset:16\n\t"
#define ASM_CRIT1_V2 \
    "s_branch .Lc0%=\n\t" \
    ".Lcr%=:\n\t" \
    "s_cmp_eq_u32 %[n], 0\n\t" \
    "s_cbranch_scc1 .Lcb%=\n\t" \
    "s_sub_u32 %[n], %[n], 1\n\t" \
    "v_max_u32 v243, v208, v209\n\t" \
    "v_max3_u32 v243, v210, v211, v243\n\t" \
    "v_cmp_eq_u32 vcc, -1, v243\n\t" \
    "s_cbranch_vccz .Lcs0_%=\n\t" \
    "buffer_load_dwordx4 v[208:211], %[ab], %[rs], %[s0] offen sc1\n\t" \
    ".Lcs0_%=:\n\t" \
    "v_max_u32 v243, v236, v237\n\t" \
    "v_max3_u32 v243, v238, v239, v243\n\t" \
    "v_cmp_eq_u32 vcc, -1, v243\n\t" \
    "s_cbranch_vccz .Lca%=\n\t" \
    "buffer_load_dwordx4 v[236:239], %[ab], %[rs], %[s1] offen sc1\n\t" \
    "s_branch .Lca%=\n\t" \
    ".Lc0%=:\n\t" \
    "buffer_load_dwordx4 v[208:211], %[ab], %[rs], %[s0] offen sc1\n\t" \
    "buffer_load_dwordx4 v[236:239], %[ab], %[rs], %[s1] offen sc1\n\t" \
    "v_bfe_i32 v241, %[ab], 7, 1\n\t" \
    "v_and_b32 v241, 0xaaaa, v241\n\t" \
    "v_or_b32 v241, 0x4444, v241\n\t" \
    "v_xor_b32 v212, 0x400, %[ab]\n\t" \
    "v_xor_b32 v216, 0x800, %[ab]\n\t" \
    "v_xor_b32 v242, 0xc00, %[ab]\n\t" \
    "v_xor_b32 v240, 0x1400, %[ab]\n\t" \
    ".Lca%=:\n\t" \
    "s_waitcnt vmcnt(0)\n\t" \
    "ds_write_b128 %[ab], v[208:211] offset:0\n\t" \
    "ds_write_b128 %[ab], v[236:239] offset:4096\n\t" \
    "v_max_u32 v243, v208, v209\n\t" \
    "v_max3_u32 v243, v210, v211, v243\n\t" \
    "v_max3_u32 v243, v236, v237, v243\n\t" \
    "v_max3_u32 v243, v238, v239, v243\n\t" \
    "v_cmp_eq_u32 vcc, -1, v243\n\t" \
    "s_cbranch_vccnz .Lcr%=\n\t" \
    ".Lcb%=:\n\t" \
    PRIUP \
    ASM_ST2 \
    "v_smfmac_f32_16x16x64_bf16 v[224:227], v[208:211], v[200:207], v241\n\t" \
    "ds_read_b128 v[200:203], %[wl] offset:4096\n\t" \
    "ds_read_b128 v[204:207], %[wl] offset:5120\n\t" \
    "v_smfmac_f32_16x16x64_bf16 v[244:247], v[208:211], v[0:7], v241\n\t" \
    "v_smfmac_f32_16x16x64_bf16 v[248:251], v[208:211], v[8:15], v241\n\t" \
    "s_waitcnt lgkmcnt(2)\n\t" \
    "buffer_load_dword %[fv], %[fv], %[rs], %[fs] offen sc1\n\t" \
    "s_barrier\n\t" ASM_ST \
    "ds_read_b128 v[212:215], v212 offset:0\n\t" \
    "ds_read_b128 v[216:219], v216 offset:0\n\t" \
    "ds_read_b128 v[220:223], v242 offset:0\n\t" \
    "ds_read_b128 v[208:211], v240 offset:0\n\t" \
    "v_smfmac_f32_16x16x64_bf16 v[244:247], v[236:239], v[80:87], v241\n\t" \
    "v_xor_b32 v240, 0x1800, %[ab]\n\t" \
    "v_smfmac_f32_16x16x64_bf16 v[248:251], v[236:239], v[88:95], v241\n\t" \
    "s_waitcnt lgkmcnt(4)\n\t" \
    "v_smfmac_f32_16x16x64_bf16 v[224:227], v[236:239], v[200:207], v241\n\t" \
    "ds_read_b128 v[200:203], %[wl] offset:6144\n\t" \
    "ds_read_b128 v[204:207], %[wl] offset:7168\n\t" \
    "ds_read_b128 v[236:239], v240 offset:0\n\t" \
    "s_waitcnt lgkmcnt(6)\n\t" \
    "v_smfmac_f32_16x16x64_bf16 v[224:227], v[212:215], v[16:23], v241\n\t" \
    "v_smfmac_f32_16x16x64_bf16 v[244:247], v[212:215], v[24:31], v241\n\t" \
    "v_xor_b32 v240, 0x1c00, %[ab]\n\t" \
    "v_smfmac_f32_16x16x64_bf16 v[248:251], v[212:215], v[32:39], v241\n\t" \
    "ds_read_b128 v[212:215], v240 offset:0\n\t" \
    "s_waitcnt lgkmcnt(6)\n\t" \
    "v_smfmac_f32_16x16x64_bf16 v[224:227], v[216:219], v[228:235], v241\n\t" \
    "ds_read_b128 v[228:231], %[wl] offset:8192\n\t" \
    "ds_read_b128 v[232:235], %[wl] offset:9216\n\t" \
    "v_smfmac_f32_16x16x64_bf16 v[244:247], v[216:219], v[40:47], v241\n\t" \
    "v_and_b32 v240, 0x3ff, %[ab]\n\t" \
    "v_smfmac_f32_16x16x64_bf16 v[248:251], v[216:219], v[48:55], v241\n\t" \
    "ds_read_b128 v[216:219], v240 offset:8192\n\t" \
    "s_waitcnt lgkmcnt(8)\n\t" \
    "v_smfmac_f32_16x16x64_bf16 v[224:227], v[220:223], v[56:63], v241\n\t" \
    "v_smfmac_f32_16x16x64_bf16 v[244:247], v[220:223], v[64:71], v241\n\t" \
    "v_smfmac_f32_16x16x64_bf16 v[248:251], v[220:223], v[72:79], v241\n\t" \
    "ds_read_b128 v[220:223], v240 offset:9216\n\t" \
    "s_waitcnt lgkmcnt(8)\n\t" \
    "v_smfmac_f32_16x16x64_bf16 v[244:247], v[208:211], v[104:111], v241\n\t" \
    "s_waitcnt lgkmcnt(5)\n\t" \
    "v_smfmac_f32_16x16x64_bf16 v[244:247], v[236:239], v[120:127], v241\n\t" \
    "s_waitcnt lgkmcnt(4)\n\t" \
    "v_smfmac_f32_16x16x64_bf16 v[244:247], v[212:215], v[144:151], v241\n\t" \
    "s_waitcnt lgkmcnt(1)\n\t" \
    "v_smfmac_f32_16x16x64_bf16 v[244:247], v[216:219], v[160:167], v241\n\t" \
    "s_waitcnt lgkmcnt(0)\n\t" \
    "v_smfmac_f32_16x16x64_bf16 v[244:247], v[220:223], v[184:191], v241\n\t" \
    "v_smfmac_f32_16x16x64_bf16 v[248:251], v[208:211], v[112:119], v241\n\t" \
    "v_smfmac_f32_16x16x64_bf16 v[248:251], v[236:239], v[128:135], v241\n\t" \
    "v_smfmac_f32_16x16x64_bf16 v[248:251], v[212:215], v[152:159], v241\n\t" \
    "v_permlane32_swap_b32 v244, v246\n\t" \
    "v_permlane32_swap_b32 v245, v247\n\t" \
    "v_smfmac_f32_16x16x64_bf16 v[248:251], v[216:219], v[168:175], v241\n\t" \
    "v_add_f32 v244, v244, v246\n\t" \
    "v_add_f32 v245, v245, v247\n\t" \
    "v_smfmac_f32_16x16x64_bf16 v[248:251], v[220:223], v[192:199], v241\n\t" \
    "v_cvt_pk_bf16_f32 v244, v244, v245\n\t" \
    "ds_write_b16 %[dst], v244 offset:512\n\t" \
    "ds_write_b16_d16_hi %[dst], v244 offset:528\n\t" \
    "v_smfmac_f32_16x16x64_bf16 v[224:227], v[208:211], v[96:103], v241\n\t" \
    "v_smfmac_f32_16x16x64_bf16 v[224:227], v[236:239], v[200:207], v241\n\t" \
    "v_smfmac_f32_16x16x64_bf16 v[224:227], v[212:215], v[136:143], v241\n\t" \
    "v_permlane32_swap_b32 v248, v250\n\t" \
    "v_permlane32_swap_b32 v249, v251\n\t" \
    "v_smfmac_f32_16x16x64_bf16 v[224:227], v[216:219], v[228:235], v241\n\t" \
    "v_add_f32 v248, v248, v250\n\t" \
    "v_add_f32 v249, v249, v251\n\t" \
    "v_smfmac_f32_16x16x64_bf16 v[224:227], v[220:223], v[176:183], v241\n\t" \
    "v_cvt_pk_bf16_f32 v248, v248, v249\n\t" \
    "ds_write_b16 %[dst], v248 offset:1024\n\t" \
    "ds_write_b16_d16_hi %[dst], v248 offset:1040\n\t" \
    "s_nop 4\n\t" \
    "v_permlane32_swap_b32 v224, v226\n\t" \
    "v_permlane32_swap_b32 v225, v227\n\t" \
    "v_add_f32 v224, v224, v226\n\t" \
    "v_add_f32 v225, v225, v227\n\t" \
    "v_cvt_pk_bf16_f32 v224, v224, v225\n\t" \
    "ds_write_b16 %[dst], v224 offset:0\n\t" \
    "ds_write_b16_d16_hi %[dst], v224 offset:16\n\t"
#define ASM_VOX \
    "s_waitcnt lgkmcnt(0)\n\t" \
    "ds_read_b128 v[208:211], %[va] offset:2048\n\t" \
    "ds_read_b128 v[212:215], %[va] offset:3072\n\t" \
    "ds_read_b128 v[216:219], %[va] offset:4096\n\t" \
    "ds_read_b128 v[220:223], %[va] offset:5120\n\t" \
    "ds_read_b128 v[200:203], %[va] offset:0\n\t" \
    "ds_read_b128 v[204:207], %[va] offset:1024\n\t" \
    "ds_read_b128 v[236:239], %[wl] offset:0\n\t" \
    "ds_read_b128 v[240:243], %[wl] offset:1024\n\t" \
    "s_waitcnt lgkmcnt(7)\n\t" \
    "v_mfma_f32_16x16x32_bf16 v[244:247], v[208:211], v[0:3], 0\n\t" \
    "s_waitcnt lgkmcnt(6)\n\t" \
    "v_mfma_f32_16x16x32_bf16 v[244:247], v[212:215], v[4:7], v[244:247]\n\t" \
    "s_waitcnt lgkmcnt(5)\n\t" \
    "v_mfma_f32_16x16x32_bf16 v[244:247], v[216:219], v[8:11], v[244:247]\n\t" \
    "s_waitcnt lgkmcnt(4)\n\t" \
    "v_mfma_f32_16x16x32_bf16 v[244:247], v[220:223], v[12:15], v[244:247]\n\t" \
    "s_waitcnt lgkmcnt(1)\n\t" \
    "v_mfma_f32_16x16x32_bf16 v[244:247], v[200:203], v[236:239], v[244:247]\n\t" \
    "ds_read_b128 v[236:239], %[wl] offset:2048\n\t" \
    "s_waitcnt lgkmcnt(1)\n\t" \
    "v_mfma_f32_16x16x32_bf16 v[244:247], v[204:207], v[240:243], v[244:247]\n\t" \
    "ds_read_b128 v[240:243], %[wl] offset:3072\n\t" \
    "v_mfma_f32_16x16x32_bf16 v[248:251], v[208:211], v[40:43], 0\n\t" \
    "v_mfma_f32_16x16x32_bf16 v[248:251], v[212:215], v[44:47], v[248:251]\n\t" \
    "v_mfma_f32_16x16x32_bf16 v[248:251], v[216:219], v[48:51], v[248:251]\n\t" \
    "v_cvt_pk_bf16_f32 v224, v244, v245\n\t" \
    "v_mfma_f32_16x16x32_bf16 v[248:251], v[220:223], v[52:55], v[248:251]\n\t" \
    "v_cvt_pk_bf16_f32 v225, v246, v247\n\t" \
    "s_waitcnt lgkmcnt(1)\n\t" \
    "v_mfma_f32_16x16x32_bf16 v[248:251], v[200:203], v[236:239], v[248:251]\n\t" \
    "ds_read_b128 v[236:239], %[wl] offset:4096\n\t" \
    "s_waitcnt lgkmcnt(1)\n\t" \
    "v_mfma_f32_16x16x32_bf16 v[248:251], v[204:207], v[240:243], v[248:251]\n\t" \
    "ds_read_b128 v[240:243], %[wl] offset:5120\n\t" \
    "v_mfma_f32_16x16x32_bf16 v[244:247], v[208:211], v[80:83], 0\n\t" \
    "v_mfma_f32_16x16x32_bf16 v[244:247], v[212:215], v[84:87], v[244:247]\n\t" \
    "v_mfma_f32_16x16x32_bf16 v[244:247], v[216:219], v[88:91], v[244:247]\n\t" \
    "v_cvt_pk_bf16_f32 v226, v248, v249\n\t" \
    "v_mfma_f32_16x16x32_bf16 v[244:247], v[220:223], v[92:95], v[244:247]\n\t" \
    "v_cvt_pk_bf16_f32 v227, v250, v251\n\t" \
    "s_waitcnt lgkmcnt(1)\n\t" \
    "v_mfma_f32_16x16x32_bf16 v[244:247], v[200:203], v[236:239], v[244:247]\n\t" \
    "ds_read_b128 v[236:239], %[wl] offset:6144\n\t" \
    "s_waitcnt lgkmcnt(1)\n\t" \
    "v_mfma_f32_16x16x32_bf16 v[244:247], v[204:207], v[240:243], v[244:247]\n\t" \
    "ds_read_b128 v[240:243], %[wl] offset:7168\n\t" \
    "v_mfma_f32_16x16x32_bf16 v[248:251], v[208:211], v[120:123], 0\n\t" \
    "v_mfma_f32_16x16x32_bf16 v[248:251], v[212:215], v[124:127], v[248:251]\n\t" \
    "v_mfma_f32_16x16x32_bf16 v[248:251], v[216:219], v[128:131], v[248:251]\n\t" \
    "v_cvt_pk_bf16_f32 v228, v244, v245\n\t" \
    "v_mfma_f32_16x16x32_bf16 v[248:251], v[220:223], v[132:135], v[248:251]\n\t" \
    "v_cvt_pk_bf16_f32 v229, v246, v247\n\t" \
    "s_waitcnt lgkmcnt(1)\n\t" \
    "v_mfma_f32_16x16x32_bf16 v[248:251], v[200:203], v[236:239], v[248:251]\n\t" \
    "ds_read_b128 v[236:239], %[wl] offset:8192\n\t" \
    "s_waitcnt lgkmcnt(1)\n\t" \
    "v_mfma_f32_16x16x32_bf16 v[248:251], v[204:207], v[240:243], v[248:251]\n\t" \
    "ds_read_b128 v[240:243], %[wl] offset:9216\n\t" \
    "v_mfma_f32_16x16x32_bf16 v[244:247], v[208:211], v[160:163], 0\n\t" \
    "v_mfma_f32_16x16x32_bf16 v[244:247], v[212:215], v[164:167], v[244:247]\n\t" \
    "v_mfma_f32_16x16x32_bf16 v[244:247], v[216:219], v[168:171], v[244:247]\n\t" \
    "v_cvt_pk_bf16_f32 v230, v248, v249\n\t" \
    "v_mfma_f32_16x16x32_bf16 v[244:247], v[220:223], v[172:175], v[244:247]\n\t" \
    "v_cvt_pk_bf16_f32 v231, v250, v251\n\t" \
    "s_waitcnt lgkmcnt(1)\n\t" \
    "v_mfma_f32_16x16x32_bf16 v[244:247], v[200:203], v[236:239], v[244:247]\n\t" \
    "s_waitcnt lgkmcnt(0)\n\t" \
    "v_mfma_f32_16x16x32_bf16 v[244:247], v[204:207], v[240:243], v[244:247]\n\t" \
    "v_mfma_f32_16x16x32_bf16 v[248:251], v[208:211], v[24:27], 0\n\t" \
    "v_mfma_f32_16x16x32_bf16 v[248:251], v[212:215], v[28:31], v[248:251]\n\t" \
    "v_mfma_f32_16x16x32_bf16 v[248:251], v[216:219], v[32:35], v[248:251]\n\t" \
    "v_cvt_pk_bf16_f32 v232, v244, v245\n\t" \
    "v_mfma_f32_16x16x32_bf16 v[248:251], v[220:223], v[36:39], v[248:251]\n\t" \
    "v_cvt_pk_bf16_f32 v233, v246, v247\n\t" \
    "v_mfma_f32_16x16x32_bf16 v[248:251], v[200:203], v[16:19], v[248:251]\n\t" \
    "v_mfma_f32_16x16x32_bf16 v[248:251], v[204:207], v[20:23], v[248:251]\n\t" \
    "v_mfma_f32_16x16x32_bf16 v[244:247], v[208:211], v[64:67], 0\n\t" \
    "v_mfma_f32_16x16x32_bf16 v[244:247], v[212:215], v[68:71], v[244:247]\n\t" \
    "v_mfma_f32_16x16x32_bf16 v[244:247], v[216:219], v[72:75], v[244:247]\n\t" \
    "v_cvt_pk_bf16_f32 v236, v248, v249\n\t" \
    "v_mfma_f32_16x16x32_bf16 v[244:247], v[220:223], v[76:79], v[244:247]\n\t" \
    "v_cvt_pk_bf16_f32 v237, v250, v251\n\t" \
    "v_mfma_f32_16x16x32_bf16 v[244:247], v[200:203], v[56:59], v[244:247]\n\t" \
    "v_mfma_f32_16x16x32_bf16 v[244:247], v[204:207], v[60:63], v[244:247]\n\t" \
    "v_mfma_f32_16x16x32_bf16 v[248:251], v[208:211], v[104:107], 0\n\t" \
    "v_mfma_f32_16x16x32_bf16 v[248:251], v[212:215], v[108:111], v[248:251]\n\t" \
    "v_mfma_f32_16x16x32_bf16 v[248:251], v[216:219], v[112:115], v[248:251]\n\t" \
    "v_cvt_pk_bf16_f32 v238, v244, v245\n\t" \
    "v_mfma_f32_16x16x32_bf16 v[248:251], v[220:223], v[116:119], v[248:251]\n\t" \
    "v_cvt_pk_bf16_f32 v239, v246, v247\n\t" \
    "v_mfma_f32_16x16x32_bf16 v[248:251], v[200:203], v[96:99], v[248:251]\n\t" \
    "v_mfma_f32_16x16x32_bf16 v[248:251], v[204:207], v[100:103], v[248:251]\n\t" \
    "v_mfma_f32_16x16x32_bf16 v[244:247], v[208:211], v[144:147], 0\n\t" \
    "v_mfma_f32_16x16x32_bf16 v[244:247], v[212:215], v[148:151], v[244:247]\n\t" \
    "v_mfma_f32_16x16x32_bf16 v[244:247], v[216:219], v[152:155], v[244:247]\n\t" \
    "v_cvt_pk_bf16_f32 v240, v248, v249\n\t" \
    "v_mfma_f32_16x16x32_bf16 v[244:247], v[220:223], v[156:159], v[244:247]\n\t" \
    "v_cvt_pk_bf16_f32 v241, v250, v251\n\t" \
    "v_mfma_f32_16x16x32_bf16 v[244:247], v[200:203], v[136:139], v[244:247]\n\t" \
    "v_mfma_f32_16x16x32_bf16 v[244:247], v[204:207], v[140:143], v[244:247]\n\t" \
    "v_mfma_f32_16x16x32_bf16 v[248:251], v[208:211], v[184:187], 0\n\t" \
    "ds_read_b128 v[208:211], %[va] offset:8192\n\t" \
    "v_mfma_f32_16x16x32_bf16 v[248:251], v[212:215], v[188:191], v[248:251]\n\t" \
    "ds_read_b128 v[212:215], %[va] offset:9216\n\t" \
    "v_mfma_f32_16x16x32_bf16 v[248:251], v[216:219], v[192:195], v[248:251]\n\t" \
    "ds_read_b128 v[216:219], %[va] offset:10240\n\t" \
    "v_cvt_pk_bf16_f32 v242, v244, v245\n\t" \
    "v_mfma_f32_16x16x32_bf16 v[248:251], v[220:223], v[196:199], v[248:251]\n\t" \
    "ds_read_b128 v[220:223], %[va] offset:11264\n\t" \
    "v_cvt_pk_bf16_f32 v243, v246, v247\n\t" \
    "v_mfma_f32_16x16x32_bf16 v[248:251], v[200:203], v[176:179], v[248:251]\n\t" \
    "ds_read_b128 v[200:203], %[va] offset:6144\n\t" \
    "v_mfma_f32_16x16x32_bf16 v[248:251], v[204:207], v[180:183], v[248:251]\n\t" \
    "ds_read_b128 v[204:207], %[va] offset:7168\n\t" \
    "s_waitcnt lgkmcnt(5)\n\t" \
    "v_mfma_f32_16x16x32_bf16 v[244:247], v[208:211], v[24:27], 0\n\t" \
    "s_waitcnt lgkmcnt(4)\n\t" \
    "v_mfma_f32_16x16x32_bf16 v[244:247], v[212:215], v[28:31], v[244:247]\n\t" \
    "s_waitcnt lgkmcnt(3)\n\t" \
    "v_mfma_f32_16x16x32_bf16 v[244:247], v[216:219], v[32:35], v[244:247]\n\t" \
    "v_cvt_pk_bf16_f32 v234, v248, v249\n\t" \
    "s_waitcnt lgkmcnt(2)\n\t" \
    "v_mfma_f32_16x16x32_bf16 v[244:247], v[220:223], v[36:39], v[244:247]\n\t" \
    "v_cvt_pk_bf16_f32 v235, v250, v251\n\t" \
    "s_waitcnt lgkmcnt(1)\n\t" \
    "v_mfma_f32_16x16x32_bf16 v[244:247], v[200:203], v[16:19], v[244:247]\n\t" \
    "s_waitcnt lgkmcnt(0)\n\t" \
    "v_mfma_f32_16x16x32_bf16 v[244:247], v[204:207], v[20:23], v[244:247]\n\t" \
    "v_mfma_f32_16x16x32_bf16 v[248:251], v[208:211], v[64:67], 0\n\t" \
    "s_add_i32 %[so], %[so], 65536\n\t" \
    "v_mfma_f32_16x16x32_bf16 v[248:251], v[212:215], v[68:71], v[248:251]\n\t" \
    "s_nop 1\n\t" \
    "v_cvt_pk_bf16_f32 v247, v246, v247\n\t" \
    "v_mfma_f32_16x16x32_bf16 v[248:251], v[216:219], v[72:75], v[248:251]\n\t" \
    "v_cvt_pk_bf16_f32 v246, v244, v245\n\t" \
    "v_mfma_f32_16x16x32_bf16 v[248:251], v[220:223], v[76:79], v[248:251]\n\t" \
    "v_mov_b64 v[244:245], v[236:237]\n\t" \
    "buffer_store_dwordx4 v[244:247], %[l16], %[rd], %[so] offen sc1\n\t" \
    "v_mfma_f32_16x16x32_bf16 v[248:251], v[200:203], v[56:59], v[248:251]\n\t" \
    "v_mfma_f32_16x16x32_bf16 v[248:251], v[204:207], v[60:63], v[248:251]\n\t" \
    "v_mfma_f32_16x16x32_bf16 v[244:247], v[208:211], v[104:107], 0\n\t" \
    "s_add_i32 %[so], %[so], 131072\n\t" \
    "v_mfma_f32_16x16x32_bf16 v[244:247], v[212:215], v[108:111], v[244:247]\n\t" \
    "s_nop 1\n\t" \
    "v_cvt_pk_bf16_f32 v251, v250, v251\n\t" \
    "v_mfma_f32_16x16x32_bf16 v[244:247], v[216:219], v[112:115], v[244:247]\n\t" \
    "v_cvt_pk_bf16_f32 v250, v248, v249\n\t" \
    "v_mfma_f32_16x16x32_bf16 v[244:247], v[220:223], v[116:119], v[244:247]\n\t" \
    "v_mov_b64 v[248:249], v[238:239]\n\t" \
    "buffer_store_dwordx4 v[248:251], %[l16], %[rd], %[so] offen sc1\n\t" \
    "v_mfma_f32_16x16x32_bf16 v[244:247], v[200:203], v[96:99], v[244:247]\n\t" \
    "v_mfma_f32_16x16x32_bf16 v[244:247], v[204:207], v[100:103], v[244:247]\n\t" \
    "v_mfma_f32_16x16x32_bf16 v[248:251], v[208:211], v[144:147], 0\n\t" \
    "s_add_i32 %[so], %[so], 131072\n\t" \
    "v_mfma_f32_16x16x32_bf16 v[248:251], v[212:215], v[148:151], v[248:251]\n\t" \
    "s_nop 1\n\t" \
    "v_cvt_pk_bf16_f32 v247, v246, v247\n\t" \
    "v_mfma_f32_16x16x32_bf16 v[248:251], v[216:219], v[152:155], v[248:251]\n\t" \
    "v_cvt_pk_bf16_f32 v246, v244, v245\n\t" \
    "v_mfma_f32_16x16x32_bf16 v[248:251], v[220:223], v[156:159], v[248:251]\n\t" \
    "v_mov_b64 v[244:245], v[240:241]\n\t" \
    "buffer_store_dwordx4 v[244:247], %[l16], %[rd], %[so] offen sc1\n\t" \
    "v_mfma_f32_16x16x32_bf16 v[248:251], v[200:203], v[136:139], v[248:251]\n\t" \
    "v_mfma_f32_16x16x32_bf16 v[248:251], v[204:207], v[140:143], v[248:251]\n\t" \
    "v_mfma_f32_16x16x32_bf16 v[244:247], v[208:211], v[184:187], 0\n\t" \
    "s_add_i32 %[so], %[so], 131072\n\t" \
    "v_mfma_f32_16x16x32_bf16 v[244:247], v[212:215], v[188:191], v[244:247]\n\t" \
    "s_nop 1\n\t" \
    "v_cvt_pk_bf16_f32 v251, v250, v251\n\t" \
    "v_mfma_f32_16x16x32_bf16 v[244:247], v[216:219], v[192:195], v[244:247]\n\t" \
    "v_cvt_pk_bf16_f32 v250, v248, v249\n\t" \
    "v_mfma_f32_16x16x32_bf16 v[244:247], v[220:223], v[196:199], v[244:247]\n\t" \
    "v_mov_b64 v[248:249], v[242:243]\n\t" \
    "buffer_store_dwordx4 v[248:251], %[l16], %[rd], %[so] offen sc1\n\t" \
    "v_mfma_f32_16x16x32_bf16 v[244:247], v[200:203], v[176:179], v[244:247]\n\t" \
    "v_mfma_f32_16x16x32_bf16 v[244:247], v[204:207], v[180:183], v[244:247]\n\t" \
    "ds_read_b128 v[236:239], %[wl] offset:0\n\t" \
    "ds_read_b128 v[240:243], %[wl] offset:1024\n\t" \
    "v_mfma_f32_16x16x32_bf16 v[248:251], v[208:211], v[0:3], 0\n\t" \
    "s_add_i32 %[so], %[so], 131072\n\t" \
    "v_mfma_f32_16x16x32_bf16 v[248:251], v[212:215], v[4:7], v[248:251]\n\t" \
    "v_cvt_pk_bf16_f32 v247, v246, v247\n\t" \
    "v_mfma_f32_16x16x32_bf16 v[248:251], v[216:219], v[8:11], v[248:251]\n\t" \
    "v_cvt_pk_bf16_f32 v246, v244, v245\n\t" \
    "v_mfma_f32_16x16x32_bf16 v[248:251], v[220:223], v[12:15], v[248:251]\n\t" \
    "v_mov_b64 v[244:245], v[234:235]\n\t" \
    "buffer_store_dwordx4 v[244:247], %[l16], %[rd], %[so] offen sc1\n\t" \
    "s_waitcnt lgkmcnt(1)\n\t" \
    "v_mfma_f32_16x16x32_bf16 v[248:251], v[200:203], v[236:239], v[248:251]\n\t" \
    "ds_read_b128 v[236:239], %[wl] offset:2048\n\t" \
    "s_waitcnt lgkmcnt(1)\n\t" \
    "v_mfma_f32_16x16x32_bf16 v[248:251], v[204:207], v[240:243], v[248:251]\n\t" \
    "ds_read_b128 v[240:243], %[wl] offset:3072\n\t" \
    "v_mfma_f32_16x16x32_bf16 v[244:247], v[208:211], v[40:43], 0\n\t" \
    "s_add_i32 %[so], %[so], -589824\n\t" \
    "v_mfma_f32_16x16x32_bf16 v[244:247], v[212:215], v[44:47], v[244:247]\n\t" \
    "s_nop 0\n\t" \
    "v_cvt_pk_bf16_f32 v251, v250, v251\n\t" \
    "v_mfma_f32_16x16x32_bf16 v[244:247], v[216:219], v[48:51], v[244:247]\n\t" \
    "v_cvt_pk_bf16_f32 v250, v248, v249\n\t" \
    "v_mfma_f32_16x16x32_bf16 v[244:247], v[220:223], v[52:55], v[244:247]\n\t" \
    "v_mov_b64 v[248:249], v[224:225]\n\t" \
    "buffer_store_dwordx4 v[248:251], %[l16], %[rd], %[so] offen sc1\n\t" \
    "s_waitcnt lgkmcnt(1)\n\t" \
    "v_mfma_f32_16x16x32_bf16 v[244:247], v[200:203], v[236:239], v[244:247]\n\t" \
    "ds_read_b128 v[236:239], %[wl] offset:4096\n\t" \
    "s_waitcnt lgkmcnt(1)\n\t" \
    "v_mfma_f32_16x16x32_bf16 v[244:247], v[204:207], v[240:243], v[244:247]\n\t" \
    "ds_read_b128 v[240:243], %[wl] offset:5120\n\t" \
    "v_mfma_f32_16x16x32_bf16 v[248:251], v[208:211], v[80:83], 0\n\t" \
    "s_add_i32 %[so], %[so], 131072\n\t" \
    "v_mfma_f32_16x16x32_bf16 v[248:251], v[212:215], v[84:87], v[248:251]\n\t" \
    "s_nop 0\n\t" \
    "v_cvt_pk_bf16_f32 v247, v246, v247\n\t" \
    "v_mfma_f32_16x16x32_bf16 v[248:251], v[216:219], v[88:91], v[248:251]\n\t" \
    "v_cvt_pk_bf16_f32 v246, v244, v245\n\t" \
    "v_mfma_f32_16x16x32_bf16 v[248:251], v[220:223], v[92:95], v[248:251]\n\t" \
    "v_mov_b64 v[244:245], v[226:227]\n\t" \
    "buffer_store_dwordx4 v[244:247], %[l16], %[rd], %[so] offen sc1\n\t" \
    "s_waitcnt lgkmcnt(1)\n\t" \
    "v_mfma_f32_16x16x32_bf16 v[248:251], v[200:203], v[236:239], v[248:251]\n\t" \
    "ds_read_b128 v[236:239], %[wl] offset:6144\n\t" \
    "s_waitcnt lgkmcnt(1)\n\t" \
    "v_mfma_f32_16x16x32_bf16 v[248:251], v[204:207], v[240:243], v[248:251]\n\t" \
    "ds_read_b128 v[240:243], %[wl] offset:7168\n\t" \
    "v_mfma_f32_16x16x32_bf16 v[244:247], v[208:211], v[120:123], 0\n\t" \
    "s_add_i32 %[so], %[so], 131072\n\t" \
    "v_mfma_f32_16x16x32_bf16 v[244:247], v[212:215], v[124:127], v[244:247]\n\t" \
    "s_nop 0\n\t" \
    "v_cvt_pk_bf16_f32 v251, v250, v251\n\t" \
    "v_mfma_f32_16x16x32_bf16 v[244:247], v[216:219], v[128:131], v[244:247]\n\t" \
    "v_cvt_pk_bf16_f32 v250, v248, v249\n\t" \
    "v_mfma_f32_16x16x32_bf16 v[244:247], v[220:223], v[132:135], v[244:247]\n\t" \
    "v_mov_b64 v[248:249], v[228:229]\n\t" \
    "buffer_store_dwordx4 v[248:251], %[l16], %[rd], %[so] offen sc1\n\t" \
    "s_waitcnt lgkmcnt(1)\n\t" \
    "v_mfma_f32_16x16x32_bf16 v[244:247], v[200:203], v[236:239], v[244:247]\n\t" \
    "ds_read_b128 v[236:239], %[wl] offset:8192\n\t" \
    "s_waitcnt lgkmcnt(1)\n\t" \
    "v_mfma_f32_16x16x32_bf16 v[244:247], v[204:207], v[240:243], v[244:247]\n\t" \
    "ds_read_b128 v[240:243], %[wl] offset:9216\n\t" \
    "v_mfma_f32_16x16x32_bf16 v[248:251], v[208:211], v[160:163], 0\n\t" \
    "s_add_i32 %[so], %[so], 131072\n\t" \
    "v_mfma_f32_16x16x32_bf16 v[248:251], v[212:215], v[164:167], v[248:251]\n\t" \
    "s_nop 0\n\t" \
    "v_cvt_pk_bf16_f32 v247, v246, v247\n\t" \
    "v_mfma_f32_16x16x32_bf16 v[248:251], v[216:219], v[168:171], v[248:251]\n\t" \
    "v_cvt_pk_bf16_f32 v246, v244, v245\n\t" \
    "v_mfma_f32_16x16x32_bf16 v[248:251], v[220:223], v[172:175], v[248:251]\n\t" \
    "v_mov_b64 v[244:245], v[230:231]\n\t" \
    "buffer_store_dwordx4 v[244:247], %[l16], %[rd], %[so] offen sc1\n\t" \
    "s_waitcnt lgkmcnt(1)\n\t" \
    "v_mfma_f32_16x16x32_bf16 v[248:251], v[200:203], v[236:239], v[248:251]\n\t" \
    "s_waitcnt lgkmcnt(0)\n\t" \
    "v_mfma_f32_16x16x32_bf16 v[248:251], v[204:207], v[240:243], v[248:251]\n\t" \
    "s_add_i32 %[so], %[so], 131072\n\t" \
    "s_nop 7\n\t" \
    "v_cvt_pk_bf16_f32 v251, v250, v251\n\t" \
    "v_cvt_pk_bf16_f32 v250, v248, v249\n\t" \
    "v_mov_b64 v[248:249], v[232:233]\n\t" \
    "buffer_store_dwordx4 v[248:251], %[l16], %[rd], %[so] offen sc1\n\t" \
    "s_nop 1\n\t"
#define ASM_CRIT2 \
    "s_waitcnt lgkmcnt(0)\n\t" \
    "s_barrier\n\t" ASM_ST \
    "ds_read_b128 v[204:207], v200 offset:11264\n\t" \
    "ds_read_b128 v[208:211], v200 offset:12288\n\t" \
    "ds_read_b128 v[200:203], v200 offset:10240\n\t" \
    "s_waitcnt lgkmcnt(2)\n\t" \
    "v_smfmac_f32_16x16x64_bf16 v[224:227], v[204:207], v[24:31], %[sidx]\n\t" \
    "s_waitcnt lgkmcnt(1)\n\t" \
    "v_smfmac_f32_16x16x64_bf16 v[224:227], v[208:211], v[32:39], %[sidx]\n\t" \
    "s_waitcnt lgkmcnt(0)\n\t" \
    "v_smfmac_f32_16x16x64_bf16 v[224:227], v[200:203], v[16:23], %[sidx]\n\t" \
    "v_smfmac_f32_16x16x64_bf16 v[220:223], v[204:207], v[0:7], %[sidx]\n\t" \
    "v_smfmac_f32_16x16x64_bf16 v[220:223], v[208:211], v[8:15], %[sidx]\n\t" \
    "v_smfmac_f32_16x16x64_bf16 v[220:223], v[200:203], v[212:219], %[sidx]\n\t" \
    "ds_read_b128 v[212:215], %[wl] offset:2048\n\t" \
    "ds_read_b128 v[216:219], %[wl] offset:3072\n\t" \
    "v_smfmac_f32_16x16x64_bf16 v[232:235], v[204:207], v[64:71], %[sidx]\n\t" \
    "v_smfmac_f32_16x16x64_bf16 v[232:235], v[208:211], v[72:79], %[sidx]\n\t" \
    "v_smfmac_f32_16x16x64_bf16 v[232:235], v[200:203], v[56:63], %[sidx]\n\t" \
    "v_permlane32_swap_b32 v220, v224\n\t" \
    "v_permlane32_swap_b32 v221, v225\n\t" \
    "v_permlane32_swap_b32 v222, v226\n\t" \
    "v_smfmac_f32_16x16x64_bf16 v[228:231], v[204:207], v[40:47], %[sidx]\n\t" \
    "v_permlane32_swap_b32 v223, v227\n\t" \
    "v_add_f32 v220, v220, v224\n\t" \
    "v_add_f32 v221, v221, v225\n\t" \
    "v_smfmac_f32_16x16x64_bf16 v[228:231], v[208:211], v[48:55], %[sidx]\n\t" \
    "v_add_f32 v222, v222, v226\n\t" \
    "v_add_f32 v223, v223, v227\n\t" \
    "v_cvt_pk_bf16_f32 v226, v220, v221\n\t" \
    "s_waitcnt lgkmcnt(0)\n\t" \
    "v_smfmac_f32_16x16x64_bf16 v[228:231], v[200:203], v[212:219], %[sidx]\n\t" \
    "ds_read_b128 v[212:215], %[wl] offset:4096\n\t" \
    "ds_read_b128 v[216:219], %[wl] offset:5120\n\t" \
    "v_cvt_pk_bf16_f32 v227, v222, v223\n\t" \
    "v_min_u32 v226, -2, v226\n\t" \
    "v_min_u32 v227, -2, v227\n\t" \
    "v_smfmac_f32_16x16x64_bf16 v[240:243], v[204:207], v[104:111], %[sidx]\n\t" \
    "v_smfmac_f32_16x16x64_bf16 v[240:243], v[208:211], v[112:119], %[sidx]\n\t" \
    "v_smfmac_f32_16x16x64_bf16 v[240:243], v[200:203], v[96:103], %[sidx]\n\t" \
    "v_permlane32_swap_b32 v228, v232\n\t" \
    "v_permlane32_swap_b32 v229, v233\n\t" \
    "v_permlane32_swap_b32 v230, v234\n\t" \
    "v_smfmac_f32_16x16x64_bf16 v[236:239], v[204:207], v[80:87], %[sidx]\n\t" \
    "v_permlane32_swap_b32 v231, v235\n\t" \
    "v_add_f32 v228, v228, v232\n\t" \
    "v_add_f32 v229, v229, v233\n\t" \
    "v_smfmac_f32_16x16x64_bf16 v[236:239], v[208:211], v[88:95], %[sidx]\n\t" \
    "v_add_f32 v230, v230, v234\n\t" \
    "v_add_f32 v231, v231, v235\n\t" \
    "v_cvt_pk_bf16_f32 v228, v228, v229\n\t" \
    "s_waitcnt lgkmcnt(0)\n\t" \
    "v_smfmac_f32_16x16x64_bf16 v[236:239], v[200:203], v[212:219], %[sidx]\n\t" \
    "ds_read_b128 v[212:215], %[wl] offset:6144\n\t" \
    "ds_read_b128 v[216:219], %[wl] offset:7168\n\t" \
    "v_cvt_pk_bf16_f32 v229, v230, v231\n\t" \
    "v_min_u32 v228, -2, v228\n\t" \
    "v_min_u32 v229, -2, v229\n\t" \
    "s_nop 1\n\t" \
    "v_permlane16_swap_b32 v226, v228\n\t" \
    "v_permlane16_swap_b32 v227, v229\n\t" \
    "buffer_store_dwordx4 v[226:229], %[vo], %[rr], %[so] offen sc1\n\t" \
    "s_add_u32 %[so], %[so], 0x10000\n\t" \
    "v_smfmac_f32_16x16x64_bf16 v[248:251], v[204:207], v[144:151], %[sidx]\n\t" \
    "v_mov_b64 v[220:221], 0\n\t" \
    "v_mov_b64 v[222:223], 0\n\t" \
    "v_smfmac_f32_16x16x64_bf16 v[248:251], v[208:211], v[152:159], %[sidx]\n\t" \
    "v_mov_b64 v[224:225], 0\n\t" \
    "v_mov_b64 v[226:227], 0\n\t" \
    "v_smfmac_f32_16x16x64_bf16 v[248:251], v[200:203], v[136:143], %[sidx]\n\t" \
    "v_permlane32_swap_b32 v236, v240\n\t" \
    "v_permlane32_swap_b32 v237, v241\n\t" \
    "v_permlane32_swap_b32 v238, v242\n\t" \
    "v_smfmac_f32_16x16x64_bf16 v[244:247], v[204:207], v[120:127], %[sidx]\n\t" \
    "v_permlane32_swap_b32 v239, v243\n\t" \
    "v_add_f32 v236, v236, v240\n\t" \
    "v_add_f32 v237, v237, v241\n\t" \
    "v_smfmac_f32_16x16x64_bf16 v[244:247], v[208:211], v[128:135], %[sidx]\n\t" \
    "v_add_f32 v238, v238, v242\n\t" \
    "v_add_f32 v239, v239, v243\n\t" \
    "v_cvt_pk_bf16_f32 v242, v236, v237\n\t" \
    "s_waitcnt lgkmcnt(0)\n\t" \
    "v_smfmac_f32_16x16x64_bf16 v[244:247], v[200:203], v[212:219], %[sidx]\n\t" \
    "ds_read_b128 v[212:215], %[wl] offset:8192\n\t" \
    "ds_read_b128 v[216:219], %[wl] offset:9216\n\t" \
    "v_cvt_pk_bf16_f32 v243, v238, v239\n\t" \
    "v_min_u32 v242, -2, v242\n\t" \
    "v_min_u32 v243, -2, v243\n\t" \
    "v_smfmac_f32_16x16x64_bf16 v[224:227], v[204:207], v[184:191], %[sidx]\n\t" \
    "v_and_b32 v232, 0x3fff, %[vo]\n\t" \
    "v_bfe_u32 v233, %[vo], 14, 1\n\t" \
    "v_smfmac_f32_16x16x64_bf16 v[224:227], v[208:211], v[192:199], %[sidx]\n\t" \
    "v_lshl_add_u32 v232, v233, 3, v232\n\t" \
    "v_permlane32_swap_b32 v244, v248\n\t" \
    "v_smfmac_f32_16x16x64_bf16 v[224:227], v[200:203], v[176:183], %[sidx]\n\t" \
    "v_permlane32_swap_b32 v245, v249\n\t" \
    "v_permlane32_swap_b32 v246, v250\n\t" \
    "v_permlane32_swap_b32 v247, v251\n\t" \
    "v_smfmac_f32_16x16x64_bf16 v[220:223], v[204:207], v[160:167], %[sidx]\n\t" \
    "v_add_f32 v244, v244, v248\n\t" \
    "v_add_f32 v245, v245, v249\n\t" \
    "v_add_f32 v246, v246, v250\n\t" \
    "v_smfmac_f32_16x16x64_bf16 v[220:223], v[208:211], v[168:175], %[sidx]\n\t" \
    "v_add_f32 v247, v247, v251\n\t" \
    "v_cvt_pk_bf16_f32 v244, v244, v245\n\t" \
    "v_cvt_pk_bf16_f32 v245, v246, v247\n\t" \
    "s_waitcnt lgkmcnt(0)\n\t" \
    "v_smfmac_f32_16x16x64_bf16 v[220:223], v[200:203], v[212:219], %[sidx]\n\t" \
    "v_min_u32 v244, -2, v244\n\t" \
    "v_min_u32 v245, -2, v245\n\t" \
    "s_nop 1\n\t" \
    "v_permlane16_swap_b32 v242, v244\n\t" \
    "v_permlane16_swap_b32 v243, v245\n\t" \
    "buffer_store_dwordx4 v[242:245], %[vo], %[rr], %[so] offen sc1\n\t" \
    "s_add_u32 %[so], %[so], 0x10000\n\t" \
    "s_nop 0\n\t" \
    "v_permlane32_swap_b32 v220, v224\n\t" \
    "v_permlane32_swap_b32 v221, v225\n\t" \
    "v_permlane32_swap_b32 v222, v226\n\t" \
    "v_permlane32_swap_b32 v223, v227\n\t" \
    "v_add_f32 v220, v220, v224\n\t" \
    "v_add_f32 v221, v221, v225\n\t" \
    "v_add_f32 v222, v222, v226\n\t" \
    "v_add_f32 v223, v223, v227\n\t" \
    "v_cvt_pk_bf16_f32 v220, v220, v221\n\t" \
    "v_cvt_pk_bf16_f32 v221, v222, v223\n\t" \
    "v_min_u32 v220, -2, v220\n\t" \
    "v_min_u32 v221, -2, v221\n\t" \
    "buffer_store_dwordx2 v[220:221], v232, %[rr], %[so] offen sc1\n\t" \
    "s_nop 1\n\t"
#define ASM_SM \
    "v_mbcnt_lo_u32_b32 v252, -1, 0\n\t" \
    "v_mbcnt_hi_u32_b32 v252, -1, v252\n\t" \
    "v_lshlrev_b32 v252, 4, v252\n\t" \
    "v_lshrrev_b32 v253, 1, v252\n\t" \
    "v_xor_b32 v253, v253, v252\n\t" \
    "v_and_b32 v253, 0x20, v253\n\t" \
    "v_mul_u32_u24 v253, 3, v253\n\t" \
    "v_xor_b32 v253, v253, v252\n\t" \
    "ds_read_b128 v[200:203], v253 offset:28672\n\t" \
    "ds_read_b128 v[204:207], v253 offset:29696\n\t" \
    "ds_read_b128 v[208:211], v253 offset:34816\n\t" \
    "ds_read_b128 v[212:215], v253 offset:35840\n\t" \
    "ds_read_b128 v[216:219], v253 offset:30720\n\t" \
    "ds_read_b128 v[220:223], v253 offset:31744\n\t" \
    "ds_read_b128 v[224:227], v253 offset:36864\n\t" \
    "ds_read_b128 v[228:231], v253 offset:37888\n\t" \
    "v_lshrrev_b32 v254, 1, v252\n\t" \
    "v_and_b32 v254, 0x180, v254\n\t" \
    "v_and_or_b32 v254, v252, %[c70], v254\n\t" \
    "v_bfe_i32 v255, v252, 7, 1\n\t" \
    "v_and_b32 v255, 0xaaaa, v255\n\t" \
    "v_or_b32 v255, 0x4444, v255\n\t" \
    "v_mov_b64 v[232:233], 0\n\t" \
    "v_mov_b64 v[234:235], 0\n\t" \
    "v_mov_b64 v[236:237], 0\n\t" \
    "v_mov_b64 v[238:239], 0\n\t" \
    "s_waitcnt lgkmcnt(0)\n\t" \
    "s_barrier\n\t" ASM_ST \
    "s_setprio 3\n\t" \
    "ds_read_b128 v[240:243], v252 offset:10240\n\t" \
    "ds_read_b128 v[244:247], v252 offset:11264\n\t" \
    "ds_read_b128 v[248:251], v252 offset:12288\n\t" \
    "s_waitcnt lgkmcnt(2)\n\t" \
    "v_smfmac_f32_16x16x64_bf16 v[232:235], v[240:243], v[200:207], v255\n\t" \
    "ds_read_b128 v[200:203], v253 offset:32768\n\t" \
    "ds_read_b128 v[204:207], v253 offset:33792\n\t" \
    "v_smfmac_f32_16x16x64_bf16 v[236:239], v[240:243], v[208:215], v255\n\t" \
    "ds_read_b128 v[208:211], v253 offset:38912\n\t" \
    "ds_read_b128 v[212:215], v253 offset:39936\n\t" \
    "s_waitcnt lgkmcnt(5)\n\t" \
    "v_smfmac_f32_16x16x64_bf16 v[232:235], v[244:247], v[216:223], v255\n\t" \
    "ds_read_b128 v[216:219], v254 offset:13312\n\t" \
    "ds_read_b128 v[220:223], v254 offset:13824\n\t" \
    "v_smfmac_f32_16x16x64_bf16 v[236:239], v[244:247], v[224:231], v255\n\t" \
    "ds_read_b128 v[224:227], v254 offset:14336\n\t" \
    "ds_read_b128 v[228:231], v254 offset:14848\n\t" \
    "s_waitcnt lgkmcnt(6)\n\t" \
    "v_smfmac_f32_16x16x64_bf16 v[232:235], v[248:251], v[200:207], v255\n\t" \
    "s_waitcnt lgkmcnt(4)\n\t" \
    "v_smfmac_f32_16x16x64_bf16 v[236:239], v[248:251], v[208:215], v255\n\t" \
    "ds_read_b128 v[208:211], v254 offset:15360\n\t" \
    "ds_read_b128 v[212:215], v254 offset:15872\n\t" \
    "v_mov_b64 v[204:205], 0\n\t" \
    "v_mov_b64 v[206:207], 0\n\t" \
    "s_nop 1\n\t" \
    "s_waitcnt lgkmcnt(4)\n\t" \
    "v_smfmac_f32_16x16x64_bf16 v[204:207], v[240:243], v[216:223], v255\n\t" \
    "s_waitcnt lgkmcnt(2)\n\t" \
    "v_smfmac_f32_16x16x64_bf16 v[204:207], v[244:247], v[224:231], v255\n\t" \
    "v_bfe_u32 v253, v252, 5, 1\n\t" \
    "v_bfe_i32 v202, v252, 4, 1\n\t" \
    "v_lshlrev_b32 v200, 1, v252\n\t" \
    "v_and_b32 v200, 0x180, v200\n\t" \
    "v_permlane32_swap_b32 v232, v234\n\t" \
    "v_permlane32_swap_b32 v233, v235\n\t" \
    "v_permlane32_swap_b32 v236, v238\n\t" \
    "v_permlane32_swap_b32 v237, v239\n\t" \
    "v_lshrrev_b32 v201, 2, v252\n\t" \
    "v_lshrrev_b32 v203, 4, v252\n\t" \
    "v_and_b32 v201, 0x40, v201\n\t" \
    "v_and_or_b32 v201, v203, 32, v201\n\t" \
    "v_and_or_b32 v201, v252, 16, v201\n\t" \
    "v_pk_add_f32 v[232:233], v[232:233], v[234:235]\n\t" \
    "v_pk_add_f32 v[234:235], v[236:237], v[238:239]\n\t" \
    "v_or_b32 v200, v200, v201\n\t" \
    "v_mad_u32_u24 v200, v253, %[cs], v200\n\t" \
    "v_mad_u32_u24 v201, v253, %[cs], v201\n\t" \
    "v_lshrrev_b32 v203, 3, v252\n\t" \
    "v_bfe_u32 v253, v252, 7, 1\n\t" \
    "s_waitcnt lgkmcnt(0)\n\t" \
    "v_smfmac_f32_16x16x64_bf16 v[204:207], v[248:251], v[208:215], v255\n\t" \
    "v_and_or_b32 v201, v203, 8, v201\n\t" \
    "v_and_b32 v202, 0x06060606, v202\n\t" \
    "v_mad_u32_u24 v201, v253, %[cp], v201\n\t" \
    "v_xor_b32 v202, 0x01000504, v202\n\t" \
    "v_max_f32 v236, v232, v234\n\t" \
    "v_max_f32 v237, v233, v235\n\t" \
    "s_nop 1\n\t" \
    "v_permlane32_swap_b32 v204, v206\n\t" \
    "v_permlane32_swap_b32 v205, v207\n\t" \
    "v_add_f32 v204, v204, v206\n\t" \
    "v_add_f32 v205, v205, v207\n\t" \
    "v_max_f32 v236, v236, v204\n\t" \
    "v_max_f32 v237, v237, v205\n\t" \
    "s_nop 0\n\t" \
    "v_max_f32_dpp v236, v236, v236 quad_perm:[1,0,3,2] row_mask:0xf bank_mask:0xf\n\t" \
    "v_max_f32_dpp v237, v237, v237 quad_perm:[1,0,3,2] row_mask:0xf bank_mask:0xf\n\t" \
    "s_nop 0\n\t" \
    "v_max_f32_dpp v236, v236, v236 quad_perm:[2,3,0,1] row_mask:0xf bank_mask:0xf\n\t" \
    "v_max_f32_dpp v237, v237, v237 quad_perm:[2,3,0,1] row_mask:0xf bank_mask:0xf\n\t" \
    "s_nop 0\n\t" \
    "v_max_f32_dpp v236, v236, v236 row_half_mirror row_mask:0xf bank_mask:0xf\n\t" \
    "v_max_f32_dpp v237, v237, v237 row_half_mirror row_mask:0xf bank_mask:0xf\n\t" \
    "s_nop 0\n\t" \
    "v_max_f32_dpp v236, v236, v236 row_mirror row_mask:0xf bank_mask:0xf\n\t" \
    "v_max_f32_dpp v237, v237, v237 row_mirror row_mask:0xf bank_mask:0xf\n\t" \
    "v_mul_f32 v236, %[scl], v236\n\t" \
    "v_mul_f32 v237, %[scl], v237\n\t" \
    "v_fma_f32 v232, v232, %[scl], -v236\n\t" \
    "v_fma_f32 v233, v233, %[scl], -v237\n\t" \
    "v_fma_f32 v234, v234, %[scl], -v236\n\t" \
    "v_fma_f32 v235, v235, %[scl], -v237\n\t" \
    "v_exp_f32 v232, v232\n\t" \
    "v_exp_f32 v233, v233\n\t" \
    "v_fma_f32 v204, v204, %[scl], -v236\n\t" \
    "v_fma_f32 v205, v205, %[scl], -v237\n\t" \
    "v_exp_f32 v234, v234\n\t" \
    "v_exp_f32 v235, v235\n\t" \
    "v_exp_f32 v204, v204\n\t" \
    "v_exp_f32 v205, v205\n\t" \
    "v_add_f32 v238, v232, v234\n\t" \
    "v_add_f32 v239, v233, v235\n\t" \
    "v_fma_f32 v238, v204, 0.5, v238\n\t" \
    "v_fma_f32 v239, v205, 0.5, v239\n\t" \
    "s_nop 0\n\t" \
    "v_add_f32_dpp v238, v238, v238 quad_perm:[1,0,3,2] row_mask:0xf bank_mask:0xf\n\t" \
    "v_add_f32_dpp v239, v239, v239 quad_perm:[1,0,3,2] row_mask:0xf bank_mask:0xf\n\t" \
    "s_nop 0\n\t" \
    "v_add_f32_dpp v238, v238, v238 quad_perm:[2,3,0,1] row_mask:0xf bank_mask:0xf\n\t" \
    "v_add_f32_dpp v239, v239, v239 quad_perm:[2,3,0,1] row_mask:0xf bank_mask:0xf\n\t" \
    "s_nop 0\n\t" \
    "v_add_f32_dpp v238, v238, v238 row_half_mirror row_mask:0xf bank_mask:0xf\n\t" \
    "v_add_f32_dpp v239, v239, v239 row_half_mirror row_mask:0xf bank_mask:0xf\n\t" \
    "s_nop 0\n\t" \
    "v_add_f32_dpp v238, v238, v238 row_mirror row_mask:0xf bank_mask:0xf\n\t" \
    "v_add_f32_dpp v239, v239, v239 row_mirror row_mask:0xf bank_mask:0xf\n\t" \
    "v_rcp_f32 v238, v238\n\t" \
    "v_rcp_f32 v239, v239\n\t" \
    "s_nop 0\n\t" \
    "v_pk_mul_f32 v[232:233], v[232:233], v[238:239]\n\t" \
    "v_pk_mul_f32 v[234:235], v[234:235], v[238:239]\n\t" \
    "v_pk_mul_f32 v[204:205], v[204:205], v[238:239]\n\t" \
    "v_cvt_pk_bf16_f32 v232, |v232|, |v233|\n\t" \
    "v_cvt_pk_bf16_f32 v234, |v234|, |v235|\n\t" \
    "v_cvt_pk_bf16_f32 v204, |v204|, |v205|\n\t" \
    "v_mov_b32_dpp v233, v232 quad_perm:[1,0,3,2] row_mask:0xf bank_mask:0xf\n\t" \
    "v_mov_b32_dpp v235, v234 quad_perm:[1,0,3,2] row_mask:0xf bank_mask:0xf\n\t" \
    "v_mov_b32_dpp v205, v204 quad_perm:[1,0,3,2] row_mask:0xf bank_mask:0xf\n\t" \
    "v_perm_b32 v232, v232, v233, v202\n\t" \
    "v_perm_b32 v234, v234, v235, v202\n\t" \
    "v_perm_b32 v204, v204, v205, v202\n\t" \
    "v_mov_b32_dpp v240, v232 quad_perm:[0,1,0,1] row_mask:0xf bank_mask:0xf\n\t" \
    "v_mov_b32_dpp v241, v234 quad_perm:[0,1,0,1] row_mask:0xf bank_mask:0xf\n\t" \
    "v_mov_b32_dpp v242, v232 quad_perm:[2,3,2,3] row_mask:0xf bank_mask:0xf\n\t" \
    "v_mov_b32_dpp v243, v234 quad_perm:[2,3,2,3] row_mask:0xf bank_mask:0xf\n\t" \
    "buffer_store_dwordx4 v[240:243], v200, %[rr], %[sx] offen sc1\n\t" \
    "v_mov_b32_dpp v244, v204 quad_perm:[0,1,0,1] row_mask:0xf bank_mask:0xf\n\t" \
    "v_mov_b32_dpp v245, v204 quad_perm:[2,3,2,3] row_mask:0xf bank_mask:0xf\n\t" \
    "buffer_store_dwordx4 v[240:243], v200, %[rr], %[sy] offen sc1\n\t" \
    "buffer_store_dwordx2 v[244:245], v201, %[rr], %[sm] offen sc1\n\t" \
    "s_wakeup\n\t" \
    PRISM
#define ASM_P4_0 \
    "ds_read_b128 v[200:203], %[qa] offset:0\n\t" \
    "ds_read_b128 v[204:207], %[qa] offset:1024\n\t" \
    "ds_read_b128 v[208:211], %[qa] offset:2048\n\t" \
    "ds_read_b128 v[212:215], %[qa] offset:3072\n\t" \
    "ds_read_b128 v[216:219], %[qa] offset:4096\n\t" \
    "ds_read_b128 v[220:223], %[qa] offset:5120\n\t" \
    "ds_read_b128 v[224:227], %[qa] offset:12288\n\t" \
    "ds_read_b128 v[228:231], %[qa] offset:13312\n\t" \
    "ds_read_b128 v[232:235], %[qa] offset:14336\n\t" \
    "ds_read_b128 v[236:239], %[qa] offset:15360\n\t" \
    "s_waitcnt lgkmcnt(0)\n\t" \
    "s_barrier\n\t" ASM_ST \
    "v_mfma_f32_16x16x32_bf16 v[240:243], v[224:227], v[200:203], v[240:243]\n\t" \
    "ds_read_b128 v[224:227], %[qa] offset:16384\n\t" \
    "v_mfma_f32_16x16x32_bf16 v[240:243], v[228:231], v[204:207], v[240:243]\n\t" \
    "ds_read_b128 v[228:231], %[qa] offset:17408\n\t" \
    "v_mfma_f32_16x16x32_bf16 v[240:243], v[232:235], v[208:211], v[240:243]\n\t" \
    "ds_read_b128 v[232:235], %[km] offset:0\n\t" \
    "v_mfma_f32_16x16x32_bf16 v[240:243], v[236:239], v[212:215], v[240:243]\n\t" \
    "ds_read_b128 v[236:239], %[km] offset:512\n\t" \
    "s_waitcnt lgkmcnt(3)\n\t" \
    "v_mfma_f32_16x16x32_bf16 v[240:243], v[224:227], v[216:219], v[240:243]\n\t" \
    "ds_read_b128 v[224:227], %[km] offset:1024\n\t" \
    "s_waitcnt lgkmcnt(3)\n\t" \
    "v_mfma_f32_16x16x32_bf16 v[240:243], v[228:231], v[220:223], v[240:243]\n\t" \
    "ds_read_b128 v[228:231], %[km] offset:1536\n\t" \
    "s_waitcnt lgkmcnt(3)\n\t" \
    "v_mfma_f32_16x16x32_bf16 v[248:251], v[232:235], v[200:203], 0\n\t" \
    "ds_read_b128 v[232:235], %[km] offset:2048\n\t" \
    "s_waitcnt lgkmcnt(3)\n\t" \
    "v_mfma_f32_16x16x32_bf16 v[248:251], v[236:239], v[204:207], v[248:251]\n\t" \
    "ds_read_b128 v[236:239], %[km] offset:2560\n\t" \
    "s_waitcnt lgkmcnt(3)\n\t" \
    "v_mfma_f32_16x16x32_bf16 v[248:251], v[224:227], v[208:211], v[248:251]\n\t" \
    "s_waitcnt lgkmcnt(2)\n\t" \
    "v_mfma_f32_16x16x32_bf16 v[248:251], v[228:231], v[212:215], v[248:251]\n\t" \
    "s_waitcnt lgkmcnt(1)\n\t" \
    "v_mfma_f32_16x16x32_bf16 v[248:251], v[232:235], v[216:219], v[248:251]\n\t" \
    "s_waitcnt lgkmcnt(0)\n\t" \
    "v_mfma_f32_16x16x32_bf16 v[248:251], v[236:239], v[220:223], v[248:251]\n\t" \
    "v_max3_f32 v224, v240, v241, v242\n\t" \
    "v_mov_b32 v225, v243\n\t" \
    "v_lshlrev_b32 v201, 23, %[qa]\n\t" \
    "v_and_b32 v201, 0x80000000, v201\n\t" \
    "v_and_b32 v200, 0x3ff, %[qa]\n\t" \
    "v_or_b32 v200, v201, v200\n\t" \
    "s_nop 1\n\t" \
    "v_max3_f32 v224, v224, v248, v249\n\t" \
    "v_max3_f32 v225, v225, v250, v251\n\t" \
    "v_max_f32 v224, v224, v225\n\t" \
    "v_mov_b32 v225, v224\n\t" \
    "s_nop 1\n\t" \
    "v_permlane16_swap_b32 v224, v225\n\t" \
    "v_max_f32 v224, v224, v225\n\t" \
    "v_mov_b32 v225, v224\n\t" \
    "s_nop 1\n\t" \
    "v_permlane32_swap_b32 v224, v225\n\t" \
    "v_max_f32 v224, v224, v225\n\t" \
    "v_mul_f32 v224, %[scl], v224\n\t" \
    "v_fma_f32 v240, v240, %[scl], -v224\n\t" \
    "v_fma_f32 v241, v241, %[scl], -v224\n\t" \
    "v_exp_f32 v240, v240\n\t" \
    "v_fma_f32 v242, v242, %[scl], -v224\n\t" \
    "v_exp_f32 v241, v241\n\t" \
    "v_fma_f32 v243, v243, %[scl], -v224\n\t" \
    "v_exp_f32 v242, v242\n\t" \
    "v_fma_f32 v248, v248, %[scl], -v224\n\t" \
    "v_exp_f32 v243, v243\n\t" \
    "v_fma_f32 v249, v249, %[scl], -v224\n\t" \
    "v_exp_f32 v248, v248\n\t" \
    "v_fma_f32 v250, v250, %[scl], -v224\n\t" \
    "v_exp_f32 v249, v249\n\t" \
    "v_fma_f32 v251, v251, %[scl], -v224\n\t" \
    "v_exp_f32 v250, v250\n\t" \
    "v_exp_f32 v251, v251\n\t" \
    "v_add_f32 v224, v240, v241\n\t" \
    "v_add_f32 v225, v242, v243\n\t" \
    "v_add_f32 v232, v248, v249\n\t" \
    "v_add_f32 v233, v250, v251\n\t" \
    "v_add_f32 v224, v224, v225\n\t" \
    "v_add_f32 v232, v232, v233\n\t" \
    "v_fma_f32 v224, v232, 0.5, v224\n\t" \
    "v_mov_b32 v225, v224\n\t" \
    "s_nop 1\n\t" \
    "v_permlane16_swap_b32 v224, v225\n\t" \
    "v_add_f32 v224, v224, v225\n\t" \
    "v_mov_b32 v225, v224\n\t" \
    "s_nop 1\n\t" \
    "v_permlane32_swap_b32 v224, v225\n\t" \
    "v_add_f32 v224, v224, v225\n\t" \
    "v_rcp_f32 v224, v224\n\t" \
    "s_nop 0\n\t" \
    "v_mul_f32 v240, v240, v224\n\t" \
    "v_mul_f32 v241, v241, v224\n\t" \
    "v_mul_f32 v242, v242, v224\n\t" \
    "v_mul_f32 v243, v243, v224\n\t" \
    "v_mul_f32 v248, v248, v224\n\t" \
    "v_mul_f32 v249, v249, v224\n\t" \
    "v_mul_f32 v250, v250, v224\n\t" \
    "v_mul_f32 v251, v251, v224\n\t" \
    "v_cvt_pk_bf16_f32 v228, |v240|, |v241|\n\t" \
    "v_cvt_pk_bf16_f32 v229, |v242|, |v243|\n\t" \
    "v_cvt_pk_bf16_f32 v232, |v248|, |v249|\n\t" \
    "v_cvt_pk_bf16_f32 v233, |v250|, |v251|\n\t" \
    "v_permlane16_swap_b32 v228, v230\n\t" \
    "v_permlane16_swap_b32 v229, v231\n\t" \
    "buffer_store_dwordx4 v[228:231], v200, %[rr], %[so] offen sc1\n\t" \
    "buffer_store_dwordx2 v[232:233], %[pm], %[rr], %[so] offen sc1\n\t" \
    "s_nop 1\n\t"
#define ASM_P4_1 \
    "ds_read_b128 v[200:203], %[qa] offset:0\n\t" \
    "ds_read_b128 v[204:207], %[qa] offset:1024\n\t" \
    "ds_read_b128 v[208:211], %[qa] offset:2048\n\t" \
    "ds_read_b128 v[212:215], %[qa] offset:3072\n\t" \
    "ds_read_b128 v[216:219], %[qa] offset:4096\n\t" \
    "ds_read_b128 v[220:223], %[qa] offset:5120\n\t" \
    "ds_read_b128 v[224:227], %[qa] offset:12288\n\t" \
    "ds_read_b128 v[228:231], %[qa] offset:13312\n\t" \
    "ds_read_b128 v[232:235], %[qa] offset:14336\n\t" \
    "ds_read_b128 v[236:239], %[qa] offset:15360\n\t" \
    "s_waitcnt lgkmcnt(0)\n\t" \
    "s_barrier\n\t" ASM_ST \
    "v_mfma_f32_16x16x32_bf16 v[240:243], v[224:227], v[200:203], v[240:243]\n\t" \
    "ds_read_b128 v[224:227], %[qa] offset:16384\n\t" \
    "v_mfma_f32_16x16x32_bf16 v[240:243], v[228:231], v[204:207], v[240:243]\n\t" \
    "ds_read_b128 v[228:231], %[qa] offset:17408\n\t" \
    "v_mfma_f32_16x16x32_bf16 v[240:243], v[232:235], v[208:211], v[240:243]\n\t" \
    "ds_read_b128 v[232:235], %[qa] offset:6144\n\t" \
    "v_mfma_f32_16x16x32_bf16 v[240:243], v[236:239], v[212:215], v[240:243]\n\t" \
    "ds_read_b128 v[236:239], %[qa] offset:7168\n\t" \
    "s_waitcnt lgkmcnt(3)\n\t" \
    "v_mfma_f32_16x16x32_bf16 v[240:243], v[224:227], v[216:219], v[240:243]\n\t" \
    "ds_read_b128 v[224:227], %[qa] offset:8192\n\t" \
    "s_waitcnt lgkmcnt(3)\n\t" \
    "v_mfma_f32_16x16x32_bf16 v[240:243], v[228:231], v[220:223], v[240:243]\n\t" \
    "ds_read_b128 v[228:231], %[qa] offset:9216\n\t" \
    "s_waitcnt lgkmcnt(3)\n\t" \
    "v_mfma_f32_16x16x32_bf16 v[244:247], v[232:235], v[200:203], 0\n\t" \
    "ds_read_b128 v[232:235], %[qa] offset:10240\n\t" \
    "s_waitcnt lgkmcnt(3)\n\t" \
    "v_mfma_f32_16x16x32_bf16 v[244:247], v[236:239], v[204:207], v[244:247]\n\t" \
    "ds_read_b128 v[236:239], %[qa] offset:11264\n\t" \
    "s_waitcnt lgkmcnt(3)\n\t" \
    "v_mfma_f32_16x16x32_bf16 v[244:247], v[224:227], v[208:211], v[244:247]\n\t" \
    "ds_read_b128 v[224:227], %[km] offset:0\n\t" \
    "s_waitcnt lgkmcnt(3)\n\t" \
    "v_mfma_f32_16x16x32_bf16 v[244:247], v[228:231], v[212:215], v[244:247]\n\t" \
    "ds_read_b128 v[228:231], %[km] offset:512\n\t" \
    "s_waitcnt lgkmcnt(3)\n\t" \
    "v_mfma_f32_16x16x32_bf16 v[244:247], v[232:235], v[216:219], v[244:247]\n\t" \
    "ds_read_b128 v[232:235], %[km] offset:1024\n\t" \
    "s_waitcnt lgkmcnt(3)\n\t" \
    "v_mfma_f32_16x16x32_bf16 v[244:247], v[236:239], v[220:223], v[244:247]\n\t" \
    "ds_read_b128 v[236:239], %[km] offset:1536\n\t" \
    "s_waitcnt lgkmcnt(3)\n\t" \
    "v_mfma_f32_16x16x32_bf16 v[248:251], v[224:227], v[200:203], 0\n\t" \
    "ds_read_b128 v[224:227], %[km] offset:2048\n\t" \
    "s_waitcnt lgkmcnt(3)\n\t" \
    "v_mfma_f32_16x16x32_bf16 v[248:251], v[228:231], v[204:207], v[248:251]\n\t" \
    "ds_read_b128 v[228:231], %[km] offset:2560\n\t" \
    "s_waitcnt lgkmcnt(3)\n\t" \
    "v_mfma_f32_16x16x32_bf16 v[248:251], v[232:235], v[208:211], v[248:251]\n\t" \
    "s_waitcnt lgkmcnt(2)\n\t" \
    "v_mfma_f32_16x16x32_bf16 v[248:251], v[236:239], v[212:215], v[248:251]\n\t" \
    "s_waitcnt lgkmcnt(1)\n\t" \
    "v_mfma_f32_16x16x32_bf16 v[248:251], v[224:227], v[216:219], v[248:251]\n\t" \
    "s_waitcnt lgkmcnt(0)\n\t" \
    "v_mfma_f32_16x16x32_bf16 v[248:251], v[228:231], v[220:223], v[248:251]\n\t" \
    "v_max3_f32 v224, v240, v241, v242\n\t" \
    "v_max3_f32 v225, v244, v245, v246\n\t" \
    "v_max3_f32 v224, v224, v243, v247\n\t" \
    "v_and_b32 v200, 0x3ff, %[qa]\n\t" \
    "s_nop 3\n\t" \
    "v_max3_f32 v224, v224, v248, v249\n\t" \
    "v_max3_f32 v225, v225, v250, v251\n\t" \
    "v_max_f32 v224, v224, v225\n\t" \
    "v_mov_b32 v225, v224\n\t" \
    "s_nop 1\n\t" \
    "v_permlane16_swap_b32 v224, v225\n\t" \
    "v_max_f32 v224, v224, v225\n\t" \
    "v_mov_b32 v225, v224\n\t" \
    "s_nop 1\n\t" \
    "v_permlane32_swap_b32 v224, v225\n\t" \
    "v_max_f32 v224, v224, v225\n\t" \
    "v_mul_f32 v224, %[scl], v224\n\t" \
    "v_fma_f32 v240, v240, %[scl], -v224\n\t" \
    "v_fma_f32 v241, v241, %[scl], -v224\n\t" \
    "v_exp_f32 v240, v240\n\t" \
    "v_fma_f32 v242, v242, %[scl], -v224\n\t" \
    "v_exp_f32 v241, v241\n\t" \
    "v_fma_f32 v243, v243, %[scl], -v224\n\t" \
    "v_exp_f32 v242, v242\n\t" \
    "v_fma_f32 v244, v244, %[scl], -v224\n\t" \
    "v_exp_f32 v243, v243\n\t" \
    "v_fma_f32 v245, v245, %[scl], -v224\n\t" \
    "v_exp_f32 v244, v244\n\t" \
    "v_fma_f32 v246, v246, %[scl], -v224\n\t" \
    "v_exp_f32 v245, v245\n\t" \
    "v_fma_f32 v247, v247, %[scl], -v224\n\t" \
    "v_exp_f32 v246, v246\n\t" \
    "v_fma_f32 v248, v248, %[scl], -v224\n\t" \
    "v_exp_f32 v247, v247\n\t" \
    "v_fma_f32 v249, v249, %[scl], -v224\n\t" \
    "v_exp_f32 v248, v248\n\t" \
    "v_fma_f32 v250, v250, %[scl], -v224\n\t" \
    "v_exp_f32 v249, v249\n\t" \
    "v_fma_f32 v251, v251, %[scl], -v224\n\t" \
    "v_exp_f32 v250, v250\n\t" \
    "v_exp_f32 v251, v251\n\t" \
    "v_add_f32 v224, v240, v241\n\t" \
    "v_add_f32 v225, v242, v243\n\t" \
    "v_add_f32 v226, v244, v245\n\t" \
    "v_add_f32 v227, v246, v247\n\t" \
    "v_add_f32 v232, v248, v249\n\t" \
    "v_add_f32 v233, v250, v251\n\t" \
    "v_add_f32 v224, v224, v225\n\t" \
    "v_add_f32 v226, v226, v227\n\t" \
    "v_add_f32 v232, v232, v233\n\t" \
    "v_add_f32 v224, v224, v226\n\t" \
    "v_fma_f32 v224, v232, 0.5, v224\n\t" \
    "v_mov_b32 v225, v224\n\t" \
    "s_nop 1\n\t" \
    "v_permlane16_swap_b32 v224, v225\n\t" \
    "v_add_f32 v224, v224, v225\n\t" \
    "v_mov_b32 v225, v224\n\t" \
    "s_nop 1\n\t" \
    "v_permlane32_swap_b32 v224, v225\n\t" \
    "v_add_f32 v224, v224, v225\n\t" \
    "v_rcp_f32 v224, v224\n\t" \
    "s_nop 0\n\t" \
    "v_mul_f32 v240, v240, v224\n\t" \
    "v_mul_f32 v241, v241, v224\n\t" \
    "v_mul_f32 v242, v242, v224\n\t" \
    "v_mul_f32 v243, v243, v224\n\t" \
    "v_mul_f32 v244, v244, v224\n\t" \
    "v_mul_f32 v245, v245, v224\n\t" \
    "v_mul_f32 v246, v246, v224\n\t" \
    "v_mul_f32 v247, v247, v224\n\t" \
    "v_mul_f32 v248, v248, v224\n\t" \
    "v_mul_f32 v249, v249, v224\n\t" \
    "v_mul_f32 v250, v250, v224\n\t" \
    "v_mul_f32 v251, v251, v224\n\t" \
    "v_cvt_pk_bf16_f32 v230, |v240|, |v241|\n\t" \
    "v_cvt_pk_bf16_f32 v231, |v242|, |v243|\n\t" \
    "v_cvt_pk_bf16_f32 v228, |v244|, |v245|\n\t" \
    "v_cvt_pk_bf16_f32 v229, |v246|, |v247|\n\t" \
    "v_cvt_pk_bf16_f32 v232, |v248|, |v249|\n\t" \
    "v_cvt_pk_bf16_f32 v233, |v250|, |v251|\n\t" \
    "buffer_store_dwordx4 v[228:231], v200, %[rr], %[so] offen sc1\n\t" \
    "buffer_store_dwordx2 v[232:233], %[pm], %[rr], %[so] offen sc1\n\t" \
    "s_nop 1\n\t"
#define ASM_RCRIT \
    "v_mov_b64 v[176:177], -1\n\t" \
    "v_mov_b64 v[178:179], -1\n\t" \
    "v_mov_b64 v[180:181], -1\n\t" \
    "v_mov_b64 v[182:183], -1\n\t" \
    "v_mov_b64 v[184:185], -1\n\t" \
    "v_mov_b64 v[186:187], -1\n\t" \
    "v_mov_b64 v[188:189], -1\n\t" \
    "v_mov_b64 v[190:191], -1\n\t" \
    "v_mov_b64 v[192:193], -1\n\t" \
    "v_mov_b64 v[194:195], -1\n\t" \
    ".Lfa%=:\n\t" \
    "s_mov_b32 %[a], 0\n\t" \
    "v_max_u32 v196, v176, v177\n\t" \
    "v_max3_u32 v196, v178, v179, v196\n\t" \
    "v_cmp_eq_u32 vcc, -1, v196\n\t" \
    "s_cbranch_vccz .Lga0_%=\n\t" \
    "buffer_load_dwordx4 v[176:179], %[ov], %[rs], %[sv] offen sc1\n\t" \
    "s_mov_b32 %[a], 1\n\t" \
    ".Lga0_%=:\n\t" \
    "v_max_u32 v196, v180, v181\n\t" \
    "v_max3_u32 v196, v182, v183, v196\n\t" \
    "v_cmp_eq_u32 vcc, -1, v196\n\t" \
    "s_cbranch_vccz .Lga1_%=\n\t" \
    "buffer_load_dwordx4 v[180:183], %[ov], %[rs], %[sv] offen offset:1024 sc1\n\t" \
    "s_mov_b32 %[a], 1\n\t" \
    ".Lga1_%=:\n\t" \
    "s_cmp_eq_u32 %[a], 0\n\t" \
    "s_cbranch_scc1 .Lha%=\n\t" \
    "s_waitcnt vmcnt(12)\n\t" \
    ".Lia%=:\n\t" \
    "v_max_u32 v196, v176, v177\n\t" \
    "v_max3_u32 v196, v178, v179, v196\n\t" \
    "v_max3_u32 v196, v180, v181, v196\n\t" \
    "v_max3_u32 v196, v182, v183, v196\n\t" \
    "v_cmp_eq_u32 vcc, -1, v196\n\t" \
    "s_cbranch_vccz .Lha%=\n\t" \
    "s_nop 7\n\t" \
    "v_max_u32 v196, v176, v177\n\t" \
    "v_max3_u32 v196, v178, v179, v196\n\t" \
    "v_max3_u32 v196, v180, v181, v196\n\t" \
    "v_max3_u32 v196, v182, v183, v196\n\t" \
    "v_cmp_eq_u32 vcc, -1, v196\n\t" \
    "s_cbranch_vccz .Lha%=\n\t" \
    "s_nop 7\n\t" \
    "v_max_u32 v196, v176, v177\n\t" \
    "v_max3_u32 v196, v178, v179, v196\n\t" \
    "v_max3_u32 v196, v180, v181, v196\n\t" \
    "v_max3_u32 v196, v182, v183, v196\n\t" \
    "v_cmp_eq_u32 vcc, -1, v196\n\t" \
    "s_cbranch_vccz .Lha%=\n\t" \
    "s_nop 7\n\t" \
    "v_max_u32 v196, v176, v177\n\t" \
    "v_max3_u32 v196, v178, v179, v196\n\t" \
    "v_max3_u32 v196, v180, v181, v196\n\t" \
    "v_max3_u32 v196, v182, v183, v196\n\t" \
    "v_cmp_eq_u32 vcc, -1, v196\n\t" \
    "s_cbranch_vccz .Lha%=\n\t" \
    "s_nop 7\n\t" \
    "s_sub_u32 %[n], %[n], 1\n\t" \
    "s_cmp_gt_i32 %[n], 0\n\t" \
    "s_cbranch_scc1 .Lfa%=\n\t" \
    ".Lha%=:\n\t" \
    ASM_ST3 \
    "buffer_load_dwordx4 v[184:187], %[o0], %[rs], %[sp] offen sc1\n\t" \
    "buffer_load_dwordx4 v[188:191], %[o0], %[rs], %[sp] offen offset:1024 sc1\n\t" \
    "buffer_load_dwordx4 v[192:195], %[o2], %[rs], %[sp] offen sc1\n\t" \
    "s_waitcnt vmcnt(0)\n\t" \
    "v_max_u32 v196, v184, v185\n\t" \
    "v_max3_u32 v196, v186, v187, v196\n\t" \
    "v_max3_u32 v196, v188, v189, v196\n\t" \
    "v_max3_u32 v196, v190, v191, v196\n\t" \
    "v_max3_u32 v196, v192, v193, v196\n\t" \
    "v_max3_u32 v196, v194, v195, v196\n\t" \
    "v_cmp_eq_u32 vcc, -1, v196\n\t" \
    "s_cbranch_vccz .Lhb%=\n\t" \
    ".Lfb%=:\n\t" \
    "s_mov_b32 %[a], 0\n\t" \
    "v_max_u32 v196, v184, v185\n\t" \
    "v_max3_u32 v196, v186, v187, v196\n\t" \
    "v_cmp_eq_u32 vcc, -1, v196\n\t" \
    "s_cbranch_vccz .Lgb0_%=\n\t" \
    "buffer_load_dwordx4 v[184:187], %[o0], %[rs], %[sp] offen sc1\n\t" \
    "s_mov_b32 %[a], 1\n\t" \
    ".Lgb0_%=:\n\t" \
    "v_max_u32 v196, v188, v189\n\t" \
    "v_max3_u32 v196, v190, v191, v196\n\t" \
    "v_cmp_eq_u32 vcc, -1, v196\n\t" \
    "s_cbranch_vccz .Lgb1_%=\n\t" \
    "buffer_load_dwordx4 v[188:191], %[o0], %[rs], %[sp] offen offset:1024 sc1\n\t" \
    "s_mov_b32 %[a], 1\n\t" \
    ".Lgb1_%=:\n\t" \
    "v_max_u32 v196, v192, v193\n\t" \
    "v_max3_u32 v196, v194, v195, v196\n\t" \
    "v_cmp_eq_u32 vcc, -1, v196\n\t" \
    "s_cbranch_vccz .Lgb2_%=\n\t" \
    "buffer_load_dwordx4 v[192:195], %[o2], %[rs], %[sp] offen sc1\n\t" \
    "s_mov_b32 %[a], 1\n\t" \
    ".Lgb2_%=:\n\t" \
    "s_cmp_eq_u32 %[a], 0\n\t" \
    "s_cbranch_scc1 .Lhb%=\n\t" \
    "s_waitcnt vmcnt(12)\n\t" \
    ".Lib%=:\n\t" \
    "v_max_u32 v196, v184, v185\n\t" \
    "v_max3_u32 v196, v186, v187, v196\n\t" \
    "v_max3_u32 v196, v188, v189, v196\n\t" \
    "v_max3_u32 v196, v190, v191, v196\n\t" \
    "v_max3_u32 v196, v192, v193, v196\n\t" \
    "v_max3_u32 v196, v194, v195, v196\n\t" \
    "v_cmp_eq_u32 vcc, -1, v196\n\t" \
    "s_cbranch_vccz .Lhb%=\n\t" \
    "s_nop 7\n\t" \
    "v_max_u32 v196, v184, v185\n\t" \
    "v_max3_u32 v196, v186, v187, v196\n\t" \
    "v_max3_u32 v196, v188, v189, v196\n\t" \
    "v_max3_u32 v196, v190, v191, v196\n\t" \
    "v_max3_u32 v196, v192, v193, v196\n\t" \
    "v_max3_u32 v196, v194, v195, v196\n\t" \
    "v_cmp_eq_u32 vcc, -1, v196\n\t" \
    "s_cbranch_vccz .Lhb%=\n\t" \
    "s_nop 7\n\t" \
    "v_max_u32 v196, v184, v185\n\t" \
    "v_max3_u32 v196, v186, v187, v196\n\t" \
    "v_max3_u32 v196, v188, v189, v196\n\t" \
    "v_max3_u32 v196, v190, v191, v196\n\t" \
    "v_max3_u32 v196, v192, v193, v196\n\t" \
    "v_max3_u32 v196, v194, v195, v196\n\t" \
    "v_cmp_eq_u32 vcc, -1, v196\n\t" \
    "s_cbranch_vccz .Lhb%=\n\t" \
    "s_nop 7\n\t" \
    "v_max_u32 v196, v184, v185\n\t" \
    "v_max3_u32 v196, v186, v187, v196\n\t" \
    "v_max3_u32 v196, v188, v189, v196\n\t" \
    "v_max3_u32 v196, v190, v191, v196\n\t" \
    "v_max3_u32 v196, v192, v193, v196\n\t" \
    "v_max3_u32 v196, v194, v195, v196\n\t" \
    "v_cmp_eq_u32 vcc, -1, v196\n\t" \
    "s_cbranch_vccz .Lhb%=\n\t" \
    "s_nop 7\n\t" \
    "s_sub_u32 %[n], %[n], 1\n\t" \
    "s_cmp_gt_i32 %[n], 0\n\t" \
    "s_cbranch_scc1 .Lfb%=\n\t" \
    ".Lhb%=:\n\t" \
    ASM_ST2 \
    "v_mfma_f32_16x16x32_bf16 v[160:163], v[192:195], v[176:179], 0\n\t" \
    "v_mfma_f32_16x16x32_bf16 v[164:167], v[192:195], v[180:183], 0\n\t" \
    "v_smfmac_f32_16x16x64_bf16 v[160:163], v[184:187], v[128:135], %[sidx]\n\t" \
    "v_smfmac_f32_16x16x64_bf16 v[164:167], v[184:187], v[144:151], %[sidx]\n\t" \
    "v_smfmac_f32_16x16x64_bf16 v[160:163], v[188:191], v[136:143], %[sidx]\n\t" \
    "v_smfmac_f32_16x16x64_bf16 v[164:167], v[188:191], v[152:159], %[sidx]\n\t" \
    "s_cmp_eq_u32 %[pub], 0\n\t" \
    "s_nop 7\n\t" \
    "v_permlane32_swap_b32 v160, v164\n\t" \
    "v_permlane32_swap_b32 v161, v165\n\t" \
    "v_permlane32_swap_b32 v162, v166\n\t" \
    "v_permlane32_swap_b32 v163, v167\n\t" \
    "v_add_f32 v160, v160, v164\n\t" \
    "v_add_f32 v161, v161, v165\n\t" \
    "ds_write2_b32 %[rw], v160, v161 offset1:80\n\t" \
    "v_add_f32 v162, v162, v166\n\t" \
    "v_add_f32 v163, v163, v167\n\t" \
    "ds_write2_b32 %[rw], v162, v163 offset0:160 offset1:240\n\t" \
    "s_waitcnt lgkmcnt(0)\n\t" \
    "s_barrier\n\t" ASM_ST \
    "s_cbranch_scc1 .Lre%=\n\t" \
    "ds_read_b128 v[200:203], %[rd] offset:0\n\t" \
    "ds_read_b128 v[204:207], %[rd] offset:16\n\t" \
    "s_waitcnt lgkmcnt(1)\n\t" \
    "v_pk_add_f32 v[200:201], v[200:201], v[202:203]\n\t" \
    "s_waitcnt lgkmcnt(0)\n\t" \
    "v_pk_add_f32 v[204:205], v[204:205], v[206:207]\n\t" \
    "v_pk_add_f32 v[200:201], v[200:201], v[204:205]\n\t" \
    "s_nop 1\n\t" \
    "v_permlane32_swap_b32 v200, v201\n\t" \
    "v_add_f32 v202, v200, v201\n\t" \
    "v_add_f32 v203, v200, v201\n\t" \
    "s_nop 1\n\t" \
    "v_permlane32_swap_b32 v202, v203\n\t" \
    "v_cvt_pk_bf16_f32 v200, v202, v203\n\t" \
    "v_min_u32 v200, -2, v200\n\t" \
    "buffer_store_dword v200, %[oo], %[rs], %[so] offen sc1\n\t" \
    ".Lre%=:\n\t"
#define ASM_RX \
    "s_cmp_eq_u32 %[fin], 0\n\t" \
    "s_cbranch_scc1 .Lx0%=\n\t" \
    "buffer_load_dwordx2 v[250:251], %[xo], %[rx], %[sx] offen sc1\n\t" \
    ".Lx0%=:\n\t" \
    "buffer_load_dword v248, %[fo], %[rs], %[fs] offen sc1\n\t" \
    "buffer_load_dwordx4 v[200:203], %[l16], %[rs], %[s0] offen offset:0 sc1\n\t" \
    "buffer_load_dwordx4 v[204:207], %[l16], %[rs], %[s0] offen offset:1024 sc1\n\t" \
    "buffer_load_dwordx4 v[208:211], %[l16], %[rs], %[s0] offen offset:2048 sc1\n\t" \
    "buffer_load_dwordx4 v[212:215], %[l16], %[rs], %[s0] offen offset:3072 sc1\n\t" \
    "buffer_load_dwordx4 v[216:219], %[l16], %[rs], %[s1] offen offset:0 sc1\n\t" \
    "buffer_load_dwordx4 v[220:223], %[l16], %[rs], %[s1] offen offset:1024 sc1\n\t" \
    "buffer_load_dwordx4 v[224:227], %[l16], %[rs], %[s1] offen offset:2048 sc1\n\t" \
    "buffer_load_dwordx4 v[228:231], %[l16], %[rs], %[s1] offen offset:3072 sc1\n\t" \
    "v_mov_b64 v[96:97], v[128:129]\n\t" \
    "v_mov_b64 v[98:99], v[132:133]\n\t" \
    "v_mov_b64 v[100:101], v[136:137]\n\t" \
    "v_mov_b64 v[102:103], v[140:141]\n\t" \
    "v_mov_b64 v[104:105], v[144:145]\n\t" \
    "v_mov_b64 v[106:107], v[148:149]\n\t" \
    "v_mov_b64 v[108:109], v[152:153]\n\t" \
    "v_mov_b64 v[110:111], v[156:157]\n\t" \
    "s_movk_i32 %[n], 0x200\n\t" \
    "s_nop 1\n\t" \
    "v_permlane16_swap_b32 v96, v98\n\t" \
    "v_permlane16_swap_b32 v97, v99\n\t" \
    "v_permlane16_swap_b32 v100, v102\n\t" \
    "v_permlane16_swap_b32 v101, v103\n\t" \
    "v_permlane16_swap_b32 v104, v106\n\t" \
    "v_permlane16_swap_b32 v105, v107\n\t" \
    "v_permlane16_swap_b32 v108, v110\n\t" \
    "v_permlane16_swap_b32 v109, v111\n\t" \
    "s_waitcnt vmcnt(7)\n\t" \
    "v_max_u32 v196, v200, v201\n\t" \
    "v_max3_u32 v196, v202, v203, v196\n\t" \
    "s_waitcnt vmcnt(6)\n\t" \
    "v_max3_u32 v196, v204, v205, v196\n\t" \
    "v_max3_u32 v196, v206, v207, v196\n\t" \
    "s_waitcnt vmcnt(5)\n\t" \
    "v_max3_u32 v196, v208, v209, v196\n\t" \
    "v_max3_u32 v196, v210, v211, v196\n\t" \
    "s_waitcnt vmcnt(4)\n\t" \
    "v_max3_u32 v196, v212, v213, v196\n\t" \
    "v_max3_u32 v196, v214, v215, v196\n\t" \
    "s_waitcnt vmcnt(3)\n\t" \
    "v_max3_u32 v196, v216, v217, v196\n\t" \
    "v_max3_u32 v196, v218, v219, v196\n\t" \
    "s_waitcnt vmcnt(2)\n\t" \
    "v_max3_u32 v196, v220, v221, v196\n\t" \
    "v_max3_u32 v196, v222, v223, v196\n\t" \
    "s_waitcnt vmcnt(1)\n\t" \
    "v_max3_u32 v196, v224, v225, v196\n\t" \
    "v_max3_u32 v196, v226, v227, v196\n\t" \
    "s_waitcnt vmcnt(0)\n\t" \
    "v_max3_u32 v196, v228, v229, v196\n\t" \
    "v_max3_u32 v196, v230, v231, v196\n\t" \
    "v_cmp_eq_u32 vcc, -1, v196\n\t" \
    "s_cbranch_vccz .Lxd%=\n\t" \
    ".Lxl%=:\n\t" \
    "s_mov_b32 %[a], 0\n\t" \
    "v_max_u32 v196, v200, v201\n\t" \
    "v_max3_u32 v196, v202, v203, v196\n\t" \
    "v_cmp_eq_u32 vcc, -1, v196\n\t" \
    "s_cbranch_vccz .Lxs0_%=\n\t" \
    "buffer_load_dwordx4 v[200:203], %[l16], %[rs], %[s0] offen offset:0 sc1\n\t" \
    "s_mov_b32 %[a], 1\n\t" \
    ".Lxs0_%=:\n\t" \
    "v_max_u32 v196, v204, v205\n\t" \
    "v_max3_u32 v196, v206, v207, v196\n\t" \
    "v_cmp_eq_u32 vcc, -1, v196\n\t" \
    "s_cbranch_vccz .Lxs1_%=\n\t" \
    "buffer_load_dwordx4 v[204:207], %[l16], %[rs], %[s0] offen offset:1024 sc1\n\t" \
    "s_mov_b32 %[a], 1\n\t" \
    ".Lxs1_%=:\n\t" \
    "v_max_u32 v196, v208, v209\n\t" \
    "v_max3_u32 v196, v210, v211, v196\n\t" \
    "v_cmp_eq_u32 vcc, -1, v196\n\t" \
    "s_cbranch_vccz .Lxs2_%=\n\t" \
    "buffer_load_dwordx4 v[208:211], %[l16], %[rs], %[s0] offen offset:2048 sc1\n\t" \
    "s_mov_b32 %[a], 1\n\t" \
    ".Lxs2_%=:\n\t" \
    "v_max_u32 v196, v212, v213\n\t" \
    "v_max3_u32 v196, v214, v215, v196\n\t" \
    "v_cmp_eq_u32 vcc, -1, v196\n\t" \
    "s_cbranch_vccz .Lxs3_%=\n\t" \
    "buffer_load_dwordx4 v[212:215], %[l16], %[rs], %[s0] offen offset:3072 sc1\n\t" \
    "s_mov_b32 %[a], 1\n\t" \
    ".Lxs3_%=:\n\t" \
    "v_max_u32 v196, v216, v217\n\t" \
    "v_max3_u32 v196, v218, v219, v196\n\t" \
    "v_cmp_eq_u32 vcc, -1, v196\n\t" \
    "s_cbranch_vccz .Lxs4_%=\n\t" \
    "buffer_load_dwordx4 v[216:219], %[l16], %[rs], %[s1] offen offset:0 sc1\n\t" \
    "s_mov_b32 %[a], 1\n\t" \
    ".Lxs4_%=:\n\t" \
    "v_max_u32 v196, v220, v221\n\t" \
    "v_max3_u32 v196, v222, v223, v196\n\t" \
    "v_cmp_eq_u32 vcc, -1, v196\n\t" \
    "s_cbranch_vccz .Lxs5_%=\n\t" \
    "buffer_load_dwordx4 v[220:223], %[l16], %[rs], %[s1] offen offset:1024 sc1\n\t" \
    "s_mov_b32 %[a], 1\n\t" \
    ".Lxs5_%=:\n\t" \
    "v_max_u32 v196, v224, v225\n\t" \
    "v_max3_u32 v196, v226, v227, v196\n\t" \
    "v_cmp_eq_u32 vcc, -1, v196\n\t" \
    "s_cbranch_vccz .Lxs6_%=\n\t" \
    "buffer_load_dwordx4 v[224:227], %[l16], %[rs], %[s1] offen offset:2048 sc1\n\t" \
    "s_mov_b32 %[a], 1\n\t" \
    ".Lxs6_%=:\n\t" \
    "v_max_u32 v196, v228, v229\n\t" \
    "v_max3_u32 v196, v230, v231, v196\n\t" \
    "v_cmp_eq_u32 vcc, -1, v196\n\t" \
    "s_cbranch_vccz .Lxs7_%=\n\t" \
    "buffer_load_dwordx4 v[228:231], %[l16], %[rs], %[s1] offen offset:3072 sc1\n\t" \
    "s_mov_b32 %[a], 1\n\t" \
    ".Lxs7_%=:\n\t" \
    "s_cmp_eq_u32 %[a], 0\n\t" \
    "s_cbranch_scc1 .Lxd%=\n\t" \
    "s_waitcnt vmcnt(0)\n\t" \
    "s_sub_u32 %[n], %[n], 1\n\t" \
    "s_cmp_lg_u32 %[n], 0\n\t" \
    "s_cbranch_scc1 .Lxl%=\n\t" \
    "s_branch .Lxe%=\n\t" \
    ".Lxd%=:\n\t" \
    ASM_ST2 \
    "v_mfma_f32_16x16x32_bf16 v[120:123], v[128:131], v[200:203], 0\n\t" \
    "v_mfma_f32_16x16x32_bf16 v[124:127], v[144:147], v[200:203], 0\n\t" \
    "v_mfma_f32_16x16x32_bf16 v[120:123], v[132:135], v[204:207], v[120:123]\n\t" \
    "v_mfma_f32_16x16x32_bf16 v[124:127], v[148:151], v[204:207], v[124:127]\n\t" \
    "v_mfma_f32_16x16x32_bf16 v[120:123], v[136:139], v[208:211], v[120:123]\n\t" \
    "v_mfma_f32_16x16x32_bf16 v[124:127], v[152:155], v[208:211], v[124:127]\n\t" \
    "v_mfma_f32_16x16x32_bf16 v[120:123], v[140:143], v[212:215], v[120:123]\n\t" \
    "v_mfma_f32_16x16x32_bf16 v[124:127], v[156:159], v[212:215], v[124:127]\n\t" \
    "v_mfma_f32_16x16x32_bf16 v[112:115], v[96:99], v[220:223], 0\n\t" \
    "v_mfma_f32_16x16x32_bf16 v[116:119], v[104:107], v[220:223], 0\n\t" \
    "v_mfma_f32_16x16x32_bf16 v[120:123], v[176:179], v[216:219], v[120:123]\n\t" \
    "v_mfma_f32_16x16x32_bf16 v[124:127], v[180:183], v[216:219], v[124:127]\n\t" \
    "v_mfma_f32_16x16x32_bf16 v[112:115], v[100:103], v[224:227], v[112:115]\n\t" \
    "v_mfma_f32_16x16x32_bf16 v[116:119], v[108:111], v[224:227], v[116:119]\n\t" \
    "v_mfma_f32_16x16x32_bf16 v[112:115], v[176:179], v[228:231], v[112:115]\n\t" \
    "v_mfma_f32_16x16x32_bf16 v[116:119], v[180:183], v[228:231], v[116:119]\n\t" \
    "s_nop 1\n\t" \
    "ds_write_b128 %[pw], v[120:123] offset:16384\n\t" \
    "ds_write_b128 %[pw], v[124:127] offset:24576\n\t" \
    "s_nop 3\n\t" \
    "ds_write_b128 %[pw], v[112:115] offset:0\n\t" \
    "ds_write_b128 %[pw], v[116:119] offset:8192\n\t" \
    "v_cmp_ge_u32 vcc, %[tn], v248\n\t" \
    "s_mov_b32 %[a], 0\n\t" \
    "s_cbranch_vccnz .Lxv%=\n\t" \
    "buffer_load_dwordx4 v[128:131], %[l16], %[rp], %[sv0] offen offset:0 sc1\n\t" \
    "buffer_load_dwordx4 v[132:135], %[l16], %[rp], %[sv0] offen offset:1024 sc1\n\t" \
    "buffer_load_dwordx4 v[136:139], %[l16], %[rp], %[sv0] offen offset:2048 sc1\n\t" \
    "buffer_load_dwordx4 v[140:143], %[l16], %[rp], %[sv0] offen offset:3072 sc1\n\t" \
    "buffer_load_dwordx4 v[144:147], %[l16], %[rp], %[sv1] offen offset:0 sc1\n\t" \
    "buffer_load_dwordx4 v[148:151], %[l16], %[rp], %[sv1] offen offset:1024 sc1\n\t" \
    "buffer_load_dwordx4 v[152:155], %[l16], %[rp], %[sv1] offen offset:2048 sc1\n\t" \
    "buffer_load_dwordx4 v[156:159], %[l16], %[rp], %[sv1] offen offset:3072 sc1\n\t" \
    "s_mov_b32 %[a], 1\n\t" \
    ".Lxv%=:\n\t" \
    "s_cmp_eq_u32 %[fin], 0\n\t" \
    "s_waitcnt lgkmcnt(0)\n\t" \
    "s_barrier\n\t" ASM_ST \
    "s_cbranch_scc1 .Lxe%=\n\t" \
    "ds_read_b128 v[200:203], %[pr] offset:0\n\t" \
    "ds_read_b128 v[204:207], %[pr] offset:1024\n\t" \
    "ds_read_b128 v[208:211], %[pr] offset:2048\n\t" \
    "ds_read_b128 v[212:215], %[pr] offset:3072\n\t" \
    "ds_read_b128 v[216:219], %[pr] offset:4096\n\t" \
    "ds_read_b128 v[220:223], %[pr] offset:5120\n\t" \
    "ds_read_b128 v[224:227], %[pr] offset:6144\n\t" \
    "ds_read_b128 v[228:231], %[pr] offset:7168\n\t" \
    "v_lshlrev_b32 v240, 16, v250\n\t" \
    "v_and_b32 v241, 0xffff0000, v250\n\t" \
    "v_lshlrev_b32 v242, 16, v251\n\t" \
    "v_and_b32 v243, 0xffff0000, v251\n\t" \
    "s_waitcnt lgkmcnt(6)\n\t" \
    "v_pk_add_f32 v[200:201], v[200:201], v[204:205]\n\t" \
    "v_pk_add_f32 v[202:203], v[202:203], v[206:207]\n\t" \
    "s_waitcnt lgkmcnt(4)\n\t" \
    "v_pk_add_f32 v[208:209], v[208:209], v[212:213]\n\t" \
    "v_pk_add_f32 v[210:211], v[210:211], v[214:215]\n\t" \
    "v_pk_add_f32 v[200:201], v[200:201], v[208:209]\n\t" \
    "v_pk_add_f32 v[202:203], v[202:203], v[210:211]\n\t" \
    "s_waitcnt lgkmcnt(2)\n\t" \
    "v_pk_add_f32 v[216:217], v[216:217], v[220:221]\n\t" \
    "v_pk_add_f32 v[218:219], v[218:219], v[222:223]\n\t" \
    "s_waitcnt lgkmcnt(0)\n\t" \
    "v_pk_add_f32 v[224:225], v[224:225], v[228:229]\n\t" \
    "v_pk_add_f32 v[226:227], v[226:227], v[230:231]\n\t" \
    "v_pk_add_f32 v[216:217], v[216:217], v[224:225]\n\t" \
    "v_pk_add_f32 v[218:219], v[218:219], v[226:227]\n\t" \
    "v_pk_add_f32 v[200:201], v[200:201], v[216:217]\n\t" \
    "v_pk_add_f32 v[202:203], v[202:203], v[218:219]\n\t" \
    "v_pk_add_f32 v[200:201], v[200:201], v[240:241]\n\t" \
    "v_pk_add_f32 v[202:203], v[202:203], v[242:243]\n\t" \
    "v_cvt_pk_bf16_f32 v200, v200, v201\n\t" \
    "v_cvt_pk_bf16_f32 v201, v202, v203\n\t" \
    "v_min_u32 v200, -2, v200\n\t" \
    "v_min_u32 v201, -2, v201\n\t" \
    "buffer_store_dwordx2 v[200:201], %[xo], %[rd], %[sa] offen sc1\n\t" \
    ".Lxe%=:\n\t"
#define ASM_W4_M \
    ".Lc7_%=:\n\t" \
    "s_waitcnt vmcnt(0)\n\t" \
    "v_max_u32 v212, v188, v189\n\t" \
    "v_max3_u32 v212, v190, v191, v212\n\t" \
    "v_cmp_eq_u32 vcc, -1, v212\n\t" \
    "s_cbranch_vccnz .Ls7_%=\n\t" \
    ".Lk7_%=:\n\t" \
    ASM_ST2 \
    "buffer_load_dwordx4 v[184:187], %[l16], %[rs], %[s1] offen offset:2048 sc1\n\t" \
    "buffer_load_dwordx4 v[180:183], %[l16], %[rs], %[s1] offen offset:1024 sc1\n\t" \
    "buffer_load_dwordx4 v[176:179], %[l16], %[rs], %[s1] offen offset:0 sc1\n\t" \
    "buffer_load_dwordx4 v[172:175], %[l16], %[rs], %[s0] offen offset:3072 sc1\n\t" \
    "buffer_load_dwordx4 v[168:171], %[l16], %[rs], %[s0] offen offset:2048 sc1\n\t" \
    "buffer_load_dwordx4 v[164:167], %[l16], %[rs], %[s0] offen offset:1024 sc1\n\t" \
    "buffer_load_dwordx4 v[160:163], %[l16], %[rs], %[s0] offen offset:0 sc1\n\t" \
    "v_mfma_f32_16x16x32_bf16 v[208:211], v[156:159], v[188:191], 0\n\t" \
    "v_mfma_f32_16x16x32_bf16 v[204:207], v[124:127], v[188:191], 0\n\t" \
    "v_mfma_f32_16x16x32_bf16 v[200:203], v[92:95], v[188:191], 0\n\t" \
    "v_mfma_f32_16x16x32_bf16 v[196:199], v[60:63], v[188:191], 0\n\t" \
    "v_mfma_f32_16x16x32_bf16 v[192:195], v[28:31], v[188:191], 0\n\t" \
    ".Lc6_%=:\n\t" \
    "s_waitcnt vmcnt(6)\n\t" \
    "v_max_u32 v212, v184, v185\n\t" \
    "v_max3_u32 v212, v186, v187, v212\n\t" \
    "v_cmp_eq_u32 vcc, -1, v212\n\t" \
    "s_cbranch_vccnz .Ls6_%=\n\t" \
    ".Lk6_%=:\n\t" \
    ".Lc5_%=:\n\t" \
    "s_waitcnt vmcnt(5)\n\t" \
    "v_max_u32 v212, v180, v181\n\t" \
    "v_max3_u32 v212, v182, v183, v212\n\t" \
    "v_cmp_eq_u32 vcc, -1, v212\n\t" \
    "s_cbranch_vccnz .Ls5_%=\n\t" \
    ".Lk5_%=:\n\t" \
    "v_mfma_f32_16x16x32_bf16 v[192:195], v[24:27], v[184:187], v[192:195]\n\t" \
    "v_mfma_f32_16x16x32_bf16 v[192:195], v[20:23], v[180:183], v[192:195]\n\t" \
    "v_mfma_f32_16x16x32_bf16 v[196:199], v[56:59], v[184:187], v[196:199]\n\t" \
    "v_mfma_f32_16x16x32_bf16 v[196:199], v[52:55], v[180:183], v[196:199]\n\t" \
    "v_mfma_f32_16x16x32_bf16 v[200:203], v[88:91], v[184:187], v[200:203]\n\t" \
    "v_mfma_f32_16x16x32_bf16 v[200:203], v[84:87], v[180:183], v[200:203]\n\t" \
    "v_mfma_f32_16x16x32_bf16 v[204:207], v[120:123], v[184:187], v[204:207]\n\t" \
    "v_mfma_f32_16x16x32_bf16 v[204:207], v[116:119], v[180:183], v[204:207]\n\t" \
    "v_mfma_f32_16x16x32_bf16 v[208:211], v[152:155], v[184:187], v[208:211]\n\t" \
    "v_mfma_f32_16x16x32_bf16 v[208:211], v[148:151], v[180:183], v[208:211]\n\t" \
    ".Lc4_%=:\n\t" \
    "s_waitcnt vmcnt(4)\n\t" \
    "v_max_u32 v212, v176, v177\n\t" \
    "v_max3_u32 v212, v178, v179, v212\n\t" \
    "v_cmp_eq_u32 vcc, -1, v212\n\t" \
    "s_cbranch_vccnz .Ls4_%=\n\t" \
    ".Lk4_%=:\n\t" \
    ".Lc3_%=:\n\t" \
    "s_waitcnt vmcnt(3)\n\t" \
    "v_max_u32 v212, v172, v173\n\t" \
    "v_max3_u32 v212, v174, v175, v212\n\t" \
    "v_cmp_eq_u32 vcc, -1, v212\n\t" \
    "s_cbranch_vccnz .Ls3_%=\n\t" \
    ".Lk3_%=:\n\t" \
    "v_mfma_f32_16x16x32_bf16 v[208:211], v[144:147], v[176:179], v[208:211]\n\t" \
    "v_mfma_f32_16x16x32_bf16 v[208:211], v[140:143], v[172:175], v[208:211]\n\t" \
    "v_mfma_f32_16x16x32_bf16 v[204:207], v[112:115], v[176:179], v[204:207]\n\t" \
    "v_mfma_f32_16x16x32_bf16 v[204:207], v[108:111], v[172:175], v[204:207]\n\t" \
    "v_mfma_f32_16x16x32_bf16 v[200:203], v[80:83], v[176:179], v[200:203]\n\t" \
    "v_mfma_f32_16x16x32_bf16 v[200:203], v[76:79], v[172:175], v[200:203]\n\t" \
    "v_mfma_f32_16x16x32_bf16 v[196:199], v[48:51], v[176:179], v[196:199]\n\t" \
    "v_mfma_f32_16x16x32_bf16 v[196:199], v[44:47], v[172:175], v[196:199]\n\t" \
    "v_mfma_f32_16x16x32_bf16 v[192:195], v[16:19], v[176:179], v[192:195]\n\t" \
    "v_mfma_f32_16x16x32_bf16 v[192:195], v[12:15], v[172:175], v[192:195]\n\t" \
    ".Lc2_%=:\n\t" \
    "s_waitcnt vmcnt(2)\n\t" \
    "v_max_u32 v212, v168, v169\n\t" \
    "v_max3_u32 v212, v170, v171, v212\n\t" \
    "v_cmp_eq_u32 vcc, -1, v212\n\t" \
    "s_cbranch_vccnz .Ls2_%=\n\t" \
    ".Lk2_%=:\n\t" \
    "buffer_load_dwordx4 v[188:191], %[l16], %[rs], %[n1] offen offset:3072 sc1\n\t" \
    ".Lc1_%=:\n\t" \
    "s_waitcnt vmcnt(2)\n\t" \
    "v_max_u32 v212, v164, v165\n\t" \
    "v_max3_u32 v212, v166, v167, v212\n\t" \
    "v_cmp_eq_u32 vcc, -1, v212\n\t" \
    "s_cbranch_vccnz .Ls1_%=\n\t" \
    ".Lk1_%=:\n\t" \
    ".Lc0_%=:\n\t" \
    "s_waitcnt vmcnt(1)\n\t" \
    "v_max_u32 v212, v160, v161\n\t" \
    "v_max3_u32 v212, v162, v163, v212\n\t" \
    "v_cmp_eq_u32 vcc, -1, v212\n\t" \
    "s_cbranch_vccnz .Ls0_%=\n\t" \
    ".Lk0_%=:\n\t" \
    "v_mfma_f32_16x16x32_bf16 v[192:195], v[8:11], v[168:171], v[192:195]\n\t" \
    "v_mfma_f32_16x16x32_bf16 v[192:195], v[4:7], v[164:167], v[192:195]\n\t" \
    "v_mfma_f32_16x16x32_bf16 v[192:195], v[0:3], v[160:163], v[192:195]\n\t" \
    "v_mfma_f32_16x16x32_bf16 v[196:199], v[40:43], v[168:171], v[196:199]\n\t" \
    "v_mfma_f32_16x16x32_bf16 v[196:199], v[36:39], v[164:167], v[196:199]\n\t" \
    "v_mfma_f32_16x16x32_bf16 v[196:199], v[32:35], v[160:163], v[196:199]\n\t" \
    "v_mfma_f32_16x16x32_bf16 v[200:203], v[72:75], v[168:171], v[200:203]\n\t" \
    "v_mfma_f32_16x16x32_bf16 v[200:203], v[68:71], v[164:167], v[200:203]\n\t" \
    "v_mfma_f32_16x16x32_bf16 v[200:203], v[64:67], v[160:163], v[200:203]\n\t" \
    "v_mfma_f32_16x16x32_bf16 v[204:207], v[104:107], v[168:171], v[204:207]\n\t" \
    "v_mfma_f32_16x16x32_bf16 v[204:207], v[100:103], v[164:167], v[204:207]\n\t" \
    "v_mfma_f32_16x16x32_bf16 v[204:207], v[96:99], v[160:163], v[204:207]\n\t" \
    "v_mfma_f32_16x16x32_bf16 v[208:211], v[136:139], v[168:171], v[208:211]\n\t" \
    "v_mfma_f32_16x16x32_bf16 v[208:211], v[132:135], v[164:167], v[208:211]\n\t" \
    "v_mfma_f32_16x16x32_bf16 v[208:211], v[128:131], v[160:163], v[208:211]\n\t" \
    "v_mov_b32 v212, %[un]\n\t" \
    "ds_write_b128 %[ld], v[192:195] offset:0\n\t" \
    "ds_write_b128 %[ld], v[196:199] offset:6144\n\t" \
    "ds_write_b128 %[ld], v[200:203] offset:12288\n\t" \
    "ds_write_b128 %[ld], v[204:207] offset:18432\n\t" \
    "s_nop 2\n\t" \
    "ds_write_b128 %[ld], v[208:211] offset:24576\n\t" \
    "ds_write_b32 %[fl], v212\n\t" \
    "s_branch .Lme%=\n\t" \
    ".Ls7_%=:\n\t" \
    "s_cmp_eq_u32 %[n], 0\n\t" \
    "s_cbranch_scc1 .Lk7_%=\n\t" \
    "s_sub_u32 %[n], %[n], 1\n\t" \
    "s_sleep 1\n\t" \
    "buffer_load_dwordx4 v[188:191], %[l16], %[rs], %[s1] offen offset:3072 sc1\n\t" \
    "s_branch .Lc7_%=\n\t" \
    ".Ls6_%=:\n\t" \
    "s_cmp_eq_u32 %[n], 0\n\t" \
    "s_cbranch_scc1 .Lk6_%=\n\t" \
    "s_sub_u32 %[n], %[n], 1\n\t" \
    "buffer_load_dwordx4 v[184:187], %[l16], %[rs], %[s1] offen offset:2048 sc1\n\t" \
    "s_waitcnt vmcnt(0)\n\t" \
    "s_branch .Lc6_%=\n\t" \
    ".Ls5_%=:\n\t" \
    "s_cmp_eq_u32 %[n], 0\n\t" \
    "s_cbranch_scc1 .Lk5_%=\n\t" \
    "s_sub_u32 %[n], %[n], 1\n\t" \
    "buffer_load_dwordx4 v[180:183], %[l16], %[rs], %[s1] offen offset:1024 sc1\n\t" \
    "s_waitcnt vmcnt(0)\n\t" \
    "s_branch .Lc5_%=\n\t" \
    ".Ls4_%=:\n\t" \
    "s_cmp_eq_u32 %[n], 0\n\t" \
    "s_cbranch_scc1 .Lk4_%=\n\t" \
    "s_sub_u32 %[n], %[n], 1\n\t" \
    "buffer_load_dwordx4 v[176:179], %[l16], %[rs], %[s1] offen offset:0 sc1\n\t" \
    "s_waitcnt vmcnt(0)\n\t" \
    "s_branch .Lc4_%=\n\t" \
    ".Ls3_%=:\n\t" \
    "s_cmp_eq_u32 %[n], 0\n\t" \
    "s_cbranch_scc1 .Lk3_%=\n\t" \
    "s_sub_u32 %[n], %[n], 1\n\t" \
    "buffer_load_dwordx4 v[172:175], %[l16], %[rs], %[s0] offen offset:3072 sc1\n\t" \
    "s_waitcnt vmcnt(0)\n\t" \
    "s_branch .Lc3_%=\n\t" \
    ".Ls2_%=:\n\t" \
    "s_cmp_eq_u32 %[n], 0\n\t" \
    "s_cbranch_scc1 .Lk2_%=\n\t" \
    "s_sub_u32 %[n], %[n], 1\n\t" \
    "buffer_load_dwordx4 v[168:171], %[l16], %[rs], %[s0] offen offset:2048 sc1\n\t" \
    "s_waitcnt vmcnt(0)\n\t" \
    "s_branch .Lc2_%=\n\t" \
    ".Ls1_%=:\n\t" \
    "s_cmp_eq_u32 %[n], 0\n\t" \
    "s_cbranch_scc1 .Lk1_%=\n\t" \
    "s_sub_u32 %[n], %[n], 1\n\t" \
    "buffer_load_dwordx4 v[164:167], %[l16], %[rs], %[s0] offen offset:1024 sc1\n\t" \
    "s_waitcnt vmcnt(0)\n\t" \
    "s_branch .Lc1_%=\n\t" \
    ".Ls0_%=:\n\t" \
    "s_cmp_eq_u32 %[n], 0\n\t" \
    "s_cbranch_scc1 .Lk0_%=\n\t" \
    "s_sub_u32 %[n], %[n], 1\n\t" \
    "buffer_load_dwordx4 v[160:163], %[l16], %[rs], %[s0] offen offset:0 sc1\n\t" \
    "s_waitcnt vmcnt(0)\n\t" \
    "s_branch .Lc0_%=\n\t" \
    ".Lme%=:\n\t"
#define ASM_W4F_0 \
    "s_branch .Lf0%=\n\t" \
    ".Lfr%=:\n\t" \
    "s_cmp_eq_u32 %[n], 0\n\t" \
    "s_cbranch_scc1 .Lfb%=\n\t" \
    "s_sub_u32 %[n], %[n], 1\n\t" \
    "s_sleep 6\n\t" \
    "buffer_load_dwordx2 v[164:165], %[o0], %[rd], %[c1] offen sc1\n\t" \
    "buffer_load_dwordx2 v[166:167], %[o1], %[rd], %[c1] offen sc1\n\t" \
    "buffer_load_dwordx2 v[168:169], %[o2], %[rd], %[c1] offen sc1\n\t" \
    "buffer_load_dwordx2 v[170:171], %[o3], %[rd], %[c1] offen sc1\n\t" \
    "buffer_load_dwordx2 v[172:173], %[o4], %[rd], %[c1] offen sc1\n\t" \
    "buffer_load_dwordx2 v[174:175], %[o0], %[rd], %[ca] offen sc1\n\t" \
    "buffer_load_dwordx2 v[176:177], %[o1], %[rd], %[ca] offen sc1\n\t" \
    "buffer_load_dwordx2 v[178:179], %[o2], %[rd], %[ca] offen sc1\n\t" \
    "buffer_load_dwordx2 v[180:181], %[o3], %[rd], %[ca] offen sc1\n\t" \
    "buffer_load_dwordx2 v[182:183], %[o4], %[rd], %[ca] offen sc1\n\t" \
    "s_waitcnt vmcnt(0)\n\t" \
    "s_branch .Lfa%=\n\t" \
    ".Lfs0_%=:\n\t" \
    "s_cmp_eq_u32 %[n], 0\n\t" \
    "s_cbranch_scc1 .Lfu0_%=\n\t" \
    "s_sub_u32 %[n], %[n], 1\n\t" \
    "s_sleep 1\n\t" \
    "s_branch .Lft0_%=\n\t" \
    ".Lfs1_%=:\n\t" \
    "s_cmp_eq_u32 %[n], 0\n\t" \
    "s_cbranch_scc1 .Lfu1_%=\n\t" \
    "s_sub_u32 %[n], %[n], 1\n\t" \
    "s_sleep 1\n\t" \
    "s_branch .Lft1_%=\n\t" \
    ".Lfx%=:\n\t" \
    "s_cmp_eq_u32 %[n], 0\n\t" \
    "s_cbranch_scc1 .Lfz%=\n\t" \
    "s_sub_u32 %[n], %[n], 1\n\t" \
    "s_branch .Lfy%=\n\t" \
    ".Lf0%=:\n\t" \
    "s_waitcnt vmcnt(1)\n\t" \
    ".Lfa%=:\n\t" \
    "v_max_u32 v194, v164, v165\n\t" \
    "v_max3_u32 v194, v166, v167, v194\n\t" \
    "v_max3_u32 v194, v168, v169, v194\n\t" \
    "v_max3_u32 v194, v170, v171, v194\n\t" \
    "v_max3_u32 v194, v172, v173, v194\n\t" \
    "v_max3_u32 v194, v174, v175, v194\n\t" \
    "v_max3_u32 v194, v176, v177, v194\n\t" \
    "v_max3_u32 v194, v178, v179, v194\n\t" \
    "v_max3_u32 v194, v180, v181, v194\n\t" \
    "v_max3_u32 v194, v182, v183, v194\n\t" \
    "v_cmp_eq_u32 vcc, -1, v194\n\t" \
    "s_cbranch_vccnz .Lfr%=\n\t" \
    ".Lfb%=:\n\t" \
    "v_lshlrev_b32 v64, 16, v164\n\t" \
    "v_and_b32 v65, 0xffff0000, v164\n\t" \
    "v_lshlrev_b32 v84, 16, v174\n\t" \
    "v_and_b32 v85, 0xffff0000, v174\n\t" \
    "v_lshlrev_b32 v66, 16, v165\n\t" \
    "v_and_b32 v67, 0xffff0000, v165\n\t" \
    "v_lshlrev_b32 v86, 16, v175\n\t" \
    "v_and_b32 v87, 0xffff0000, v175\n\t" \
    "v_lshlrev_b32 v68, 16, v166\n\t" \
    "v_and_b32 v69, 0xffff0000, v166\n\t" \
    "v_lshlrev_b32 v88, 16, v176\n\t" \
    "v_and_b32 v89, 0xffff0000, v176\n\t" \
    "v_lshlrev_b32 v70, 16, v167\n\t" \
    "v_and_b32 v71, 0xffff0000, v167\n\t" \
    "v_lshlrev_b32 v90, 16, v177\n\t" \
    "v_and_b32 v91, 0xffff0000, v177\n\t" \
    "v_lshlrev_b32 v72, 16, v168\n\t" \
    "v_and_b32 v73, 0xffff0000, v168\n\t" \
    "v_lshlrev_b32 v92, 16, v178\n\t" \
    "v_and_b32 v93, 0xffff0000, v178\n\t" \
    "v_lshlrev_b32 v74, 16, v169\n\t" \
    "v_and_b32 v75, 0xffff0000, v169\n\t" \
    "v_lshlrev_b32 v94, 16, v179\n\t" \
    "v_and_b32 v95, 0xffff0000, v179\n\t" \
    "v_lshlrev_b32 v76, 16, v170\n\t" \
    "v_and_b32 v77, 0xffff0000, v170\n\t" \
    "v_lshlrev_b32 v96, 16, v180\n\t" \
    "v_and_b32 v97, 0xffff0000, v180\n\t" \
    "v_lshlrev_b32 v78, 16, v171\n\t" \
    "v_and_b32 v79, 0xffff0000, v171\n\t" \
    "v_lshlrev_b32 v98, 16, v181\n\t" \
    "v_and_b32 v99, 0xffff0000, v181\n\t" \
    "v_lshlrev_b32 v80, 16, v172\n\t" \
    "v_and_b32 v81, 0xffff0000, v172\n\t" \
    "v_lshlrev_b32 v100, 16, v182\n\t" \
    "v_and_b32 v101, 0xffff0000, v182\n\t" \
    "v_lshlrev_b32 v82, 16, v173\n\t" \
    "v_and_b32 v83, 0xffff0000, v173\n\t" \
    "v_lshlrev_b32 v102, 16, v183\n\t" \
    "v_and_b32 v103, 0xffff0000, v183\n\t" \
    "v_pk_add_f32 v[64:65], v[64:65], v[84:85]\n\t" \
    "v_pk_add_f32 v[66:67], v[66:67], v[86:87]\n\t" \
    "v_pk_add_f32 v[68:69], v[68:69], v[88:89]\n\t" \
    "v_pk_add_f32 v[70:71], v[70:71], v[90:91]\n\t" \
    "v_pk_add_f32 v[72:73], v[72:73], v[92:93]\n\t" \
    "v_pk_add_f32 v[74:75], v[74:75], v[94:95]\n\t" \
    "v_pk_add_f32 v[76:77], v[76:77], v[96:97]\n\t" \
    "v_pk_add_f32 v[78:79], v[78:79], v[98:99]\n\t" \
    "v_pk_add_f32 v[80:81], v[80:81], v[100:101]\n\t" \
    "v_pk_add_f32 v[82:83], v[82:83], v[102:103]\n\t" \
    ".Lft0_%=:\n\t" \
    "ds_read_b32 v194, %[f0]\n\t" \
    "s_waitcnt lgkmcnt(0)\n\t" \
    "v_cmp_ge_u32 vcc, %[u], v194\n\t" \
    "s_cbranch_vccnz .Lfs0_%=\n\t" \
    ".Lfu0_%=:\n\t" \
    "ds_read_b128 v[84:87], %[la] offset:0\n\t" \
    "ds_read_b128 v[88:91], %[la] offset:6144\n\t" \
    "ds_read_b128 v[92:95], %[la] offset:12288\n\t" \
    "ds_read_b128 v[96:99], %[la] offset:18432\n\t" \
    "ds_read_b128 v[100:103], %[la] offset:24576\n\t" \
    "ds_read_b128 v[104:107], %[la] offset:1024\n\t" \
    "ds_read_b128 v[108:111], %[la] offset:7168\n\t" \
    "ds_read_b128 v[112:115], %[la] offset:13312\n\t" \
    "ds_read_b128 v[116:119], %[la] offset:19456\n\t" \
    "ds_read_b128 v[120:123], %[la] offset:25600\n\t" \
    "ds_read_b128 v[124:127], %[la] offset:2048\n\t" \
    "s_waitcnt lgkmcnt(10)\n\t" \
    "v_pk_add_f32 v[64:65], v[64:65], v[84:85]\n\t" \
    "v_pk_add_f32 v[66:67], v[66:67], v[86:87]\n\t" \
    "ds_read_b128 v[128:131], %[la] offset:8192\n\t" \
    "s_waitcnt lgkmcnt(10)\n\t" \
    "v_pk_add_f32 v[68:69], v[68:69], v[88:89]\n\t" \
    "v_pk_add_f32 v[70:71], v[70:71], v[90:91]\n\t" \
    "ds_read_b128 v[132:135], %[la] offset:14336\n\t" \
    "s_waitcnt lgkmcnt(10)\n\t" \
    "v_pk_add_f32 v[72:73], v[72:73], v[92:93]\n\t" \
    "v_pk_add_f32 v[74:75], v[74:75], v[94:95]\n\t" \
    "ds_read_b128 v[136:139], %[la] offset:20480\n\t" \
    "s_waitcnt lgkmcnt(10)\n\t" \
    "v_pk_add_f32 v[76:77], v[76:77], v[96:97]\n\t" \
    "v_pk_add_f32 v[78:79], v[78:79], v[98:99]\n\t" \
    "ds_read_b128 v[140:143], %[la] offset:26624\n\t" \
    "s_waitcnt lgkmcnt(10)\n\t" \
    "v_pk_add_f32 v[80:81], v[80:81], v[100:101]\n\t" \
    "v_pk_add_f32 v[82:83], v[82:83], v[102:103]\n\t" \
    "ds_read_b128 v[144:147], %[la] offset:3072\n\t" \
    "s_waitcnt lgkmcnt(10)\n\t" \
    "v_pk_add_f32 v[64:65], v[64:65], v[104:105]\n\t" \
    "v_pk_add_f32 v[66:67], v[66:67], v[106:107]\n\t" \
    "ds_read_b128 v[148:151], %[la] offset:9216\n\t" \
    "s_waitcnt lgkmcnt(10)\n\t" \
    "v_pk_add_f32 v[68:69], v[68:69], v[108:109]\n\t" \
    "v_pk_add_f32 v[70:71], v[70:71], v[110:111]\n\t" \
    "ds_read_b128 v[152:155], %[la] offset:15360\n\t" \
    "s_waitcnt lgkmcnt(10)\n\t" \
    "v_pk_add_f32 v[72:73], v[72:73], v[112:113]\n\t" \
    "v_pk_add_f32 v[74:75], v[74:75], v[114:115]\n\t" \
    "ds_read_b128 v[156:159], %[la] offset:21504\n\t" \
    "s_waitcnt lgkmcnt(10)\n\t" \
    "v_pk_add_f32 v[76:77], v[76:77], v[116:117]\n\t" \
    "v_pk_add_f32 v[78:79], v[78:79], v[118:119]\n\t" \
    "ds_read_b128 v[160:163], %[la] offset:27648\n\t" \
    "s_waitcnt lgkmcnt(10)\n\t" \
    "v_pk_add_f32 v[80:81], v[80:81], v[120:121]\n\t" \
    "v_pk_add_f32 v[82:83], v[82:83], v[122:123]\n\t" \
    "s_waitcnt lgkmcnt(9)\n\t" \
    "v_pk_add_f32 v[64:65], v[64:65], v[124:125]\n\t" \
    "v_pk_add_f32 v[66:67], v[66:67], v[126:127]\n\t" \
    "s_waitcnt lgkmcnt(8)\n\t" \
    "v_pk_add_f32 v[68:69], v[68:69], v[128:129]\n\t" \
    "v_pk_add_f32 v[70:71], v[70:71], v[130:131]\n\t" \
    "s_waitcnt lgkmcnt(7)\n\t" \
    "v_pk_add_f32 v[72:73], v[72:73], v[132:133]\n\t" \
    "v_pk_add_f32 v[74:75], v[74:75], v[134:135]\n\t" \
    "s_waitcnt lgkmcnt(6)\n\t" \
    "v_pk_add_f32 v[76:77], v[76:77], v[136:137]\n\t" \
    "v_pk_add_f32 v[78:79], v[78:79], v[138:139]\n\t" \
    "s_waitcnt lgkmcnt(5)\n\t" \
    "v_pk_add_f32 v[80:81], v[80:81], v[140:141]\n\t" \
    "v_pk_add_f32 v[82:83], v[82:83], v[142:143]\n\t" \
    "s_waitcnt lgkmcnt(4)\n\t" \
    "v_pk_add_f32 v[64:65], v[64:65], v[144:145]\n\t" \
    "v_pk_add_f32 v[66:67], v[66:67], v[146:147]\n\t" \
    "s_waitcnt lgkmcnt(3)\n\t" \
    "v_pk_add_f32 v[68:69], v[68:69], v[148:149]\n\t" \
    "v_pk_add_f32 v[70:71], v[70:71], v[150:151]\n\t" \
    "s_waitcnt lgkmcnt(2)\n\t" \
    "v_pk_add_f32 v[72:73], v[72:73], v[152:153]\n\t" \
    "v_pk_add_f32 v[74:75], v[74:75], v[154:155]\n\t" \
    "s_waitcnt lgkmcnt(1)\n\t" \
    "v_pk_add_f32 v[76:77], v[76:77], v[156:157]\n\t" \
    "v_pk_add_f32 v[78:79], v[78:79], v[158:159]\n\t" \
    "s_waitcnt lgkmcnt(0)\n\t" \
    "v_pk_add_f32 v[80:81], v[80:81], v[160:161]\n\t" \
    "v_pk_add_f32 v[82:83], v[82:83], v[162:163]\n\t" \
    ".Lft1_%=:\n\t" \
    "ds_read_b32 v194, %[f1]\n\t" \
    "s_waitcnt lgkmcnt(0)\n\t" \
    "v_cmp_ge_u32 vcc, %[u], v194\n\t" \
    "s_cbranch_vccnz .Lfs1_%=\n\t" \
    ".Lfu1_%=:\n\t" \
    ASM_ST2 \
    "ds_read_b128 v[84:87], %[la] offset:4096\n\t" \
    "ds_read_b128 v[88:91], %[la] offset:10240\n\t" \
    "ds_read_b128 v[92:95], %[la] offset:16384\n\t" \
    "ds_read_b128 v[96:99], %[la] offset:22528\n\t" \
    "ds_read_b128 v[100:103], %[la] offset:28672\n\t" \
    "ds_read_b128 v[104:107], %[la] offset:5120\n\t" \
    "ds_read_b128 v[108:111], %[la] offset:11264\n\t" \
    "ds_read_b128 v[112:115], %[la] offset:17408\n\t" \
    "ds_read_b128 v[116:119], %[la] offset:23552\n\t" \
    "ds_read_b128 v[120:123], %[la] offset:29696\n\t" \
    "v_mov_b32 v194, %[un]\n\t" \
    "ds_write_b32 %[fd], v194\n\t" \
    "s_waitcnt lgkmcnt(10)\n\t" \
    "v_pk_add_f32 v[64:65], v[64:65], v[84:85]\n\t" \
    "v_pk_add_f32 v[66:67], v[66:67], v[86:87]\n\t" \
    "s_waitcnt lgkmcnt(9)\n\t" \
    "v_pk_add_f32 v[68:69], v[68:69], v[88:89]\n\t" \
    "v_pk_add_f32 v[70:71], v[70:71], v[90:91]\n\t" \
    "s_waitcnt lgkmcnt(8)\n\t" \
    "v_pk_add_f32 v[72:73], v[72:73], v[92:93]\n\t" \
    "v_pk_add_f32 v[74:75], v[74:75], v[94:95]\n\t" \
    "s_waitcnt lgkmcnt(7)\n\t" \
    "v_pk_add_f32 v[76:77], v[76:77], v[96:97]\n\t" \
    "v_pk_add_f32 v[78:79], v[78:79], v[98:99]\n\t" \
    "s_waitcnt lgkmcnt(6)\n\t" \
    "v_pk_add_f32 v[80:81], v[80:81], v[100:101]\n\t" \
    "v_pk_add_f32 v[82:83], v[82:83], v[102:103]\n\t" \
    "s_waitcnt lgkmcnt(5)\n\t" \
    "v_pk_add_f32 v[64:65], v[64:65], v[104:105]\n\t" \
    "v_pk_add_f32 v[66:67], v[66:67], v[106:107]\n\t" \
    "s_waitcnt lgkmcnt(4)\n\t" \
    "v_pk_add_f32 v[68:69], v[68:69], v[108:109]\n\t" \
    "v_pk_add_f32 v[70:71], v[70:71], v[110:111]\n\t" \
    "s_waitcnt lgkmcnt(3)\n\t" \
    "v_pk_add_f32 v[72:73], v[72:73], v[112:113]\n\t" \
    "v_pk_add_f32 v[74:75], v[74:75], v[114:115]\n\t" \
    "s_waitcnt lgkmcnt(2)\n\t" \
    "v_pk_add_f32 v[76:77], v[76:77], v[116:117]\n\t" \
    "v_pk_add_f32 v[78:79], v[78:79], v[118:119]\n\t" \
    "s_waitcnt lgkmcnt(1)\n\t" \
    "v_pk_add_f32 v[80:81], v[80:81], v[120:121]\n\t" \
    "v_pk_add_f32 v[82:83], v[82:83], v[122:123]\n\t" \
    "v_pk_add_f32 v[84:85], v[64:65], v[66:67]\n\t" \
    "v_pk_mul_f32 v[88:89], v[64:65], v[64:65]\n\t" \
    "v_pk_add_f32 v[86:87], v[68:69], v[70:71]\n\t" \
    "v_pk_mul_f32 v[90:91], v[66:67], v[66:67]\n\t" \
    "v_pk_fma_f32 v[88:89], v[68:69], v[68:69], v[88:89]\n\t" \
    "v_pk_fma_f32 v[90:91], v[70:71], v[70:71], v[90:91]\n\t" \
    "v_pk_add_f32 v[84:85], v[84:85], v[72:73]\n\t" \
    "v_pk_fma_f32 v[88:89], v[72:73], v[72:73], v[88:89]\n\t" \
    "v_pk_add_f32 v[86:87], v[86:87], v[74:75]\n\t" \
    "v_pk_fma_f32 v[90:91], v[74:75], v[74:75], v[90:91]\n\t" \
    "v_pk_add_f32 v[84:85], v[84:85], v[76:77]\n\t" \
    "v_pk_fma_f32 v[88:89], v[76:77], v[76:77], v[88:89]\n\t" \
    "v_pk_add_f32 v[86:87], v[86:87], v[78:79]\n\t" \
    "v_pk_fma_f32 v[90:91], v[78:79], v[78:79], v[90:91]\n\t" \
    "v_pk_add_f32 v[84:85], v[84:85], v[80:81]\n\t" \
    "v_pk_fma_f32 v[88:89], v[80:81], v[80:81], v[88:89]\n\t" \
    "v_pk_add_f32 v[86:87], v[86:87], v[82:83]\n\t" \
    "v_pk_fma_f32 v[90:91], v[82:83], v[82:83], v[90:91]\n\t" \
    "v_pk_add_f32 v[84:85], v[84:85], v[86:87]\n\t" \
    "v_pk_add_f32 v[88:89], v[88:89], v[90:91]\n\t" \
    "v_add_f32 v194, v84, v85\n\t" \
    "v_add_f32 v195, v88, v89\n\t" \
    "s_nop 1\n\t" \
    "v_permlane16_swap_b32 v194, v195\n\t" \
    "v_add_f32 v196, v194, v195\n\t" \
    "v_add_f32 v197, v194, v195\n\t" \
    "s_nop 1\n\t" \
    "v_permlane32_swap_b32 v196, v197\n\t" \
    "v_add_f32 v194, v196, v197\n\t" \
    "v_add_f32 v195, v196, v197\n\t" \
    "s_nop 1\n\t" \
    "v_permlane16_swap_b32 v194, v195\n\t" \
    "v_min_u32 v194, -2, v194\n\t" \
    "v_min_u32 v195, -2, v195\n\t" \
    "buffer_store_dwordx2 v[194:195], %[so], %[rr], %[sp] offen sc1\n\t" \
    ASM_ST3 \
    "v_cvt_pk_bf16_f32 v184, v64, v65\n\t" \
    "v_cvt_pk_bf16_f32 v185, v66, v67\n\t" \
    "v_cvt_pk_bf16_f32 v186, v68, v69\n\t" \
    "v_cvt_pk_bf16_f32 v187, v70, v71\n\t" \
    "v_cvt_pk_bf16_f32 v188, v72, v73\n\t" \
    "v_cvt_pk_bf16_f32 v189, v74, v75\n\t" \
    "v_cvt_pk_bf16_f32 v190, v76, v77\n\t" \
    "v_cvt_pk_bf16_f32 v191, v78, v79\n\t" \
    "v_cvt_pk_bf16_f32 v192, v80, v81\n\t" \
    "v_cvt_pk_bf16_f32 v193, v82, v83\n\t" \
    "v_permlane16_swap_b32 v184, v186\n\t" \
    "v_permlane16_swap_b32 v185, v187\n\t" \
    "v_permlane16_swap_b32 v188, v190\n\t" \
    "v_permlane16_swap_b32 v189, v191\n\t" \
    W4SLEEP \
    ".Lfy%=:\n\t" \
    "buffer_load_dwordx2 v[196:197], %[si], %[rr], %[sq] offen sc1\n\t" \
    "buffer_load_dwordx2 v[198:199], %[si], %[rr], %[sq] offen offset:512 sc1\n\t" \
    "s_waitcnt vmcnt(0)\n\t" \
    "v_max_u32 v200, v196, v197\n\t" \
    "v_max3_u32 v200, v198, v199, v200\n\t" \
    "v_cmp_eq_u32 vcc, -1, v200\n\t" \
    "s_cbranch_vccnz .Lfx%=\n\t" \
    ".Lfz%=:\n\t" \
    ASM_ST \
    "v_add_f32 v194, v196, v198\n\t" \
    "v_add_f32 v195, v197, v199\n\t" \
    "buffer_store_dwordx4 v[184:187], %[pa], %[rd], %[xr] offen sc1\n\t" \
    "buffer_store_dwordx4 v[188:191], %[pb], %[rd], %[xr] offen sc1\n\t" \
    "buffer_store_dwordx2 v[192:193], %[o4], %[rd], %[xr] offen sc1\n\t" \
    "s_nop 1\n\t" \
    "v_permlane16_swap_b32 v194, v195\n\t" \
    "v_add_f32 v196, v194, v195\n\t" \
    "v_add_f32 v197, v194, v195\n\t" \
    "s_nop 1\n\t" \
    "v_permlane32_swap_b32 v196, v197\n\t" \
    "v_add_f32 v194, v196, v197\n\t" \
    "v_add_f32 v195, v196, v197\n\t" \
    "s_nop 1\n\t" \
    "v_permlane16_swap_b32 v194, v195\n\t" \
    "v_mul_f32 v194, %[kd], v194\n\t" \
    "v_mul_f32 v195, %[kd], v195\n\t" \
    "v_fma_f32 v195, -v194, v194, v195\n\t" \
    "v_max_f32 v195, 0, v195\n\t" \
    "v_cvt_pk_bf16_f32 v195, v195, v195\n\t" \
    "v_lshlrev_b32 v195, 16, v195\n\t" \
    "v_add_f32 v195, %[ke], v195\n\t" \
    "v_cvt_pk_bf16_f32 v195, v195, v195\n\t" \
    "v_lshlrev_b32 v195, 16, v195\n\t" \
    "v_sqrt_f32 v195, v195\n\t" \
    "v_cvt_pk_bf16_f32 v194, v194, v194\n\t" \
    "v_lshlrev_b32 v194, 16, v194\n\t" \
    "v_cvt_pk_bf16_f32 v195, v195, v195\n\t" \
    "v_lshlrev_b32 v195, 16, v195\n\t" \
    "v_rcp_f32 v196, v195\n\t" \
    "v_rcp_f32 v197, v195\n\t" \
    "s_nop 0\n\t" \
    "v_mul_f32 v198, -v194, v196\n\t" \
    "v_mul_f32 v199, -v194, v196\n\t" \
    "v_pk_fma_f32 v[64:65], v[64:65], v[196:197], v[198:199]\n\t" \
    "v_pk_fma_f32 v[66:67], v[66:67], v[196:197], v[198:199]\n\t" \
    "v_pk_fma_f32 v[68:69], v[68:69], v[196:197], v[198:199]\n\t" \
    "v_pk_fma_f32 v[70:71], v[70:71], v[196:197], v[198:199]\n\t" \
    "v_pk_fma_f32 v[72:73], v[72:73], v[196:197], v[198:199]\n\t" \
    "v_pk_fma_f32 v[74:75], v[74:75], v[196:197], v[198:199]\n\t" \
    "v_pk_fma_f32 v[76:77], v[76:77], v[196:197], v[198:199]\n\t" \
    "v_pk_fma_f32 v[78:79], v[78:79], v[196:197], v[198:199]\n\t" \
    "v_pk_fma_f32 v[80:81], v[80:81], v[196:197], v[198:199]\n\t" \
    "v_pk_fma_f32 v[82:83], v[82:83], v[196:197], v[198:199]\n\t" \
    "v_cvt_pk_bf16_f32 v184, v64, v65\n\t" \
    "v_cvt_pk_bf16_f32 v185, v66, v67\n\t" \
    "v_cvt_pk_bf16_f32 v186, v68, v69\n\t" \
    "v_cvt_pk_bf16_f32 v187, v70, v71\n\t" \
    "v_cvt_pk_bf16_f32 v188, v72, v73\n\t" \
    "v_cvt_pk_bf16_f32 v189, v74, v75\n\t" \
    "v_cvt_pk_bf16_f32 v190, v76, v77\n\t" \
    "v_cvt_pk_bf16_f32 v191, v78, v79\n\t" \
    "v_cvt_pk_bf16_f32 v192, v80, v81\n\t" \
    "v_cvt_pk_bf16_f32 v193, v82, v83\n\t" \
    "v_permlane16_swap_b32 v184, v186\n\t" \
    "v_permlane16_swap_b32 v185, v187\n\t" \
    "v_permlane16_swap_b32 v188, v190\n\t" \
    "v_permlane16_swap_b32 v189, v191\n\t" \
    "buffer_store_dwordx4 v[184:187], %[pa], %[rd], %[xn] offen sc1\n\t" \
    "buffer_store_dwordx4 v[188:191], %[pb], %[rd], %[xn] offen sc1\n\t" \
    "buffer_store_dwordx2 v[192:193], %[o4], %[rd], %[xn] offen sc1\n\t" \
    "buffer_load_dwordx2 v[164:165], %[o0], %[rd], %[n1] offen sc1\n\t" \
    "buffer_load_dwordx2 v[166:167], %[o1], %[rd], %[n1] offen sc1\n\t" \
    "buffer_load_dwordx2 v[168:169], %[o2], %[rd], %[n1] offen sc1\n\t" \
    "buffer_load_dwordx2 v[170:171], %[o3], %[rd], %[n1] offen sc1\n\t" \
    "buffer_load_dwordx2 v[172:173], %[o4], %[rd], %[n1] offen sc1\n\t" \
    "buffer_load_dwordx2 v[174:175], %[o0], %[rd], %[na] offen sc1\n\t" \
    "buffer_load_dwordx2 v[176:177], %[o1], %[rd], %[na] offen sc1\n\t" \
    "buffer_load_dwordx2 v[178:179], %[o2], %[rd], %[na] offen sc1\n\t" \
    "buffer_load_dwordx2 v[180:181], %[o3], %[rd], %[na] offen sc1\n\t" \
    "buffer_load_dwordx2 v[182:183], %[o4], %[rd], %[na] offen sc1\n\t" \
    "v_mov_b32 v196, %[tn]\n\t" \
    "v_mov_b32 v197, %[tn]\n\t" \
    "v_mov_b32 v198, %[tn]\n\t" \
    "v_mov_b32 v199, %[tn]\n\t" \
    "s_waitcnt vmcnt(10)\n\t" \
    "buffer_store_dwordx4 v[196:199], %[fo], %[rn], %[fs] offen sc1\n\t" \
    "s_nop 1\n\t"
#define ASM_W4F_1 \
    "s_branch .Lf0%=\n\t" \
    ".Lfr%=:\n\t" \
    "s_cmp_eq_u32 %[n], 0\n\t" \
    "s_cbranch_scc1 .Lfb%=\n\t" \
    "s_sub_u32 %[n], %[n], 1\n\t" \
    "s_sleep 6\n\t" \
    "buffer_load_dwordx2 v[164:165], %[o0], %[rd], %[c1] offen sc1\n\t" \
    "buffer_load_dwordx2 v[166:167], %[o1], %[rd], %[c1] offen sc1\n\t" \
    "buffer_load_dwordx2 v[168:169], %[o2], %[rd], %[c1] offen sc1\n\t" \
    "buffer_load_dwordx2 v[170:171], %[o3], %[rd], %[c1] offen sc1\n\t" \
    "buffer_load_dwordx2 v[172:173], %[o4], %[rd], %[c1] offen sc1\n\t" \
    "buffer_load_dwordx2 v[174:175], %[o0], %[rd], %[ca] offen sc1\n\t" \
    "buffer_load_dwordx2 v[176:177], %[o1], %[rd], %[ca] offen sc1\n\t" \
    "buffer_load_dwordx2 v[178:179], %[o2], %[rd], %[ca] offen sc1\n\t" \
    "buffer_load_dwordx2 v[180:181], %[o3], %[rd], %[ca] offen sc1\n\t" \
    "buffer_load_dwordx2 v[182:183], %[o4], %[rd], %[ca] offen sc1\n\t" \
    "s_waitcnt vmcnt(0)\n\t" \
    "s_branch .Lfa%=\n\t" \
    ".Lfs0_%=:\n\t" \
    "s_cmp_eq_u32 %[n], 0\n\t" \
    "s_cbranch_scc1 .Lfu0_%=\n\t" \
    "s_sub_u32 %[n], %[n], 1\n\t" \
    "s_sleep 1\n\t" \
    "s_branch .Lft0_%=\n\t" \
    ".Lfs1_%=:\n\t" \
    "s_cmp_eq_u32 %[n], 0\n\t" \
    "s_cbranch_scc1 .Lfu1_%=\n\t" \
    "s_sub_u32 %[n], %[n], 1\n\t" \
    "s_sleep 1\n\t" \
    "s_branch .Lft1_%=\n\t" \
    ".Lf0%=:\n\t" \
    "s_waitcnt vmcnt(0)\n\t" \
    ".Lfa%=:\n\t" \
    "v_max_u32 v194, v164, v165\n\t" \
    "v_max3_u32 v194, v166, v167, v194\n\t" \
    "v_max3_u32 v194, v168, v169, v194\n\t" \
    "v_max3_u32 v194, v170, v171, v194\n\t" \
    "v_max3_u32 v194, v172, v173, v194\n\t" \
    "v_max3_u32 v194, v174, v175, v194\n\t" \
    "v_max3_u32 v194, v176, v177, v194\n\t" \
    "v_max3_u32 v194, v178, v179, v194\n\t" \
    "v_max3_u32 v194, v180, v181, v194\n\t" \
    "v_max3_u32 v194, v182, v183, v194\n\t" \
    "v_cmp_eq_u32 vcc, -1, v194\n\t" \
    "s_cbranch_vccnz .Lfr%=\n\t" \
    ".Lfb%=:\n\t" \
    "v_lshlrev_b32 v64, 16, v164\n\t" \
    "v_and_b32 v65, 0xffff0000, v164\n\t" \
    "v_lshlrev_b32 v84, 16, v174\n\t" \
    "v_and_b32 v85, 0xffff0000, v174\n\t" \
    "v_lshlrev_b32 v66, 16, v165\n\t" \
    "v_and_b32 v67, 0xffff0000, v165\n\t" \
    "v_lshlrev_b32 v86, 16, v175\n\t" \
    "v_and_b32 v87, 0xffff0000, v175\n\t" \
    "v_lshlrev_b32 v68, 16, v166\n\t" \
    "v_and_b32 v69, 0xffff0000, v166\n\t" \
    "v_lshlrev_b32 v88, 16, v176\n\t" \
    "v_and_b32 v89, 0xffff0000, v176\n\t" \
    "v_lshlrev_b32 v70, 16, v167\n\t" \
    "v_and_b32 v71, 0xffff0000, v167\n\t" \
    "v_lshlrev_b32 v90, 16, v177\n\t" \
    "v_and_b32 v91, 0xffff0000, v177\n\t" \
    "v_lshlrev_b32 v72, 16, v168\n\t" \
    "v_and_b32 v73, 0xffff0000, v168\n\t" \
    "v_lshlrev_b32 v92, 16, v178\n\t" \
    "v_and_b32 v93, 0xffff0000, v178\n\t" \
    "v_lshlrev_b32 v74, 16, v169\n\t" \
    "v_and_b32 v75, 0xffff0000, v169\n\t" \
    "v_lshlrev_b32 v94, 16, v179\n\t" \
    "v_and_b32 v95, 0xffff0000, v179\n\t" \
    "v_lshlrev_b32 v76, 16, v170\n\t" \
    "v_and_b32 v77, 0xffff0000, v170\n\t" \
    "v_lshlrev_b32 v96, 16, v180\n\t" \
    "v_and_b32 v97, 0xffff0000, v180\n\t" \
    "v_lshlrev_b32 v78, 16, v171\n\t" \
    "v_and_b32 v79, 0xffff0000, v171\n\t" \
    "v_lshlrev_b32 v98, 16, v181\n\t" \
    "v_and_b32 v99, 0xffff0000, v181\n\t" \
    "v_lshlrev_b32 v80, 16, v172\n\t" \
    "v_and_b32 v81, 0xffff0000, v172\n\t" \
    "v_lshlrev_b32 v100, 16, v182\n\t" \
    "v_and_b32 v101, 0xffff0000, v182\n\t" \
    "v_lshlrev_b32 v82, 16, v173\n\t" \
    "v_and_b32 v83, 0xffff0000, v173\n\t" \
    "v_lshlrev_b32 v102, 16, v183\n\t" \
    "v_and_b32 v103, 0xffff0000, v183\n\t" \
    "v_pk_add_f32 v[64:65], v[64:65], v[84:85]\n\t" \
    "v_pk_add_f32 v[66:67], v[66:67], v[86:87]\n\t" \
    "v_pk_add_f32 v[68:69], v[68:69], v[88:89]\n\t" \
    "v_pk_add_f32 v[70:71], v[70:71], v[90:91]\n\t" \
    "v_pk_add_f32 v[72:73], v[72:73], v[92:93]\n\t" \
    "v_pk_add_f32 v[74:75], v[74:75], v[94:95]\n\t" \
    "v_pk_add_f32 v[76:77], v[76:77], v[96:97]\n\t" \
    "v_pk_add_f32 v[78:79], v[78:79], v[98:99]\n\t" \
    "v_pk_add_f32 v[80:81], v[80:81], v[100:101]\n\t" \
    "v_pk_add_f32 v[82:83], v[82:83], v[102:103]\n\t" \
    ".Lft0_%=:\n\t" \
    "ds_read_b32 v194, %[f0]\n\t" \
    "s_waitcnt lgkmcnt(0)\n\t" \
    "v_cmp_ge_u32 vcc, %[u], v194\n\t" \
    "s_cbranch_vccnz .Lfs0_%=\n\t" \
    ".Lfu0_%=:\n\t" \
    "ds_read_b128 v[84:87], %[la] offset:0\n\t" \
    "ds_read_b128 v[88:91], %[la] offset:6144\n\t" \
    "ds_read_b128 v[92:95], %[la] offset:12288\n\t" \
    "ds_read_b128 v[96:99], %[la] offset:18432\n\t" \
    "ds_read_b128 v[100:103], %[la] offset:24576\n\t" \
    "ds_read_b128 v[104:107], %[la] offset:1024\n\t" \
    "ds_read_b128 v[108:111], %[la] offset:7168\n\t" \
    "ds_read_b128 v[112:115], %[la] offset:13312\n\t" \
    "ds_read_b128 v[116:119], %[la] offset:19456\n\t" \
    "ds_read_b128 v[120:123], %[la] offset:25600\n\t" \
    "ds_read_b128 v[124:127], %[la] offset:2048\n\t" \
    "s_waitcnt lgkmcnt(10)\n\t" \
    "v_pk_add_f32 v[64:65], v[64:65], v[84:85]\n\t" \
    "v_pk_add_f32 v[66:67], v[66:67], v[86:87]\n\t" \
    "ds_read_b128 v[128:131], %[la] offset:8192\n\t" \
    "s_waitcnt lgkmcnt(10)\n\t" \
    "v_pk_add_f32 v[68:69], v[68:69], v[88:89]\n\t" \
    "v_pk_add_f32 v[70:71], v[70:71], v[90:91]\n\t" \
    "ds_read_b128 v[132:135], %[la] offset:14336\n\t" \
    "s_waitcnt lgkmcnt(10)\n\t" \
    "v_pk_add_f32 v[72:73], v[72:73], v[92:93]\n\t" \
    "v_pk_add_f32 v[74:75], v[74:75], v[94:95]\n\t" \
    "ds_read_b128 v[136:139], %[la] offset:20480\n\t" \
    "s_waitcnt lgkmcnt(10)\n\t" \
    "v_pk_add_f32 v[76:77], v[76:77], v[96:97]\n\t" \
    "v_pk_add_f32 v[78:79], v[78:79], v[98:99]\n\t" \
    "ds_read_b128 v[140:143], %[la] offset:26624\n\t" \
    "s_waitcnt lgkmcnt(10)\n\t" \
    "v_pk_add_f32 v[80:81], v[80:81], v[100:101]\n\t" \
    "v_pk_add_f32 v[82:83], v[82:83], v[102:103]\n\t" \
    "ds_read_b128 v[144:147], %[la] offset:3072\n\t" \
    "s_waitcnt lgkmcnt(10)\n\t" \
    "v_pk_add_f32 v[64:65], v[64:65], v[104:105]\n\t" \
    "v_pk_add_f32 v[66:67], v[66:67], v[106:107]\n\t" \
    "ds_read_b128 v[148:151], %[la] offset:9216\n\t" \
    "s_waitcnt lgkmcnt(10)\n\t" \
    "v_pk_add_f32 v[68:69], v[68:69], v[108:109]\n\t" \
    "v_pk_add_f32 v[70:71], v[70:71], v[110:111]\n\t" \
    "ds_read_b128 v[152:155], %[la] offset:15360\n\t" \
    "s_waitcnt lgkmcnt(10)\n\t" \
    "v_pk_add_f32 v[72:73], v[72:73], v[112:113]\n\t" \
    "v_pk_add_f32 v[74:75], v[74:75], v[114:115]\n\t" \
    "ds_read_b128 v[156:159], %[la] offset:21504\n\t" \
    "s_waitcnt lgkmcnt(10)\n\t" \
    "v_pk_add_f32 v[76:77], v[76:77], v[116:117]\n\t" \
    "v_pk_add_f32 v[78:79], v[78:79], v[118:119]\n\t" \
    "ds_read_b128 v[160:163], %[la] offset:27648\n\t" \
    "s_waitcnt lgkmcnt(10)\n\t" \
    "v_pk_add_f32 v[80:81], v[80:81], v[120:121]\n\t" \
    "v_pk_add_f32 v[82:83], v[82:83], v[122:123]\n\t" \
    "s_waitcnt lgkmcnt(9)\n\t" \
    "v_pk_add_f32 v[64:65], v[64:65], v[124:125]\n\t" \
    "v_pk_add_f32 v[66:67], v[66:67], v[126:127]\n\t" \
    "s_waitcnt lgkmcnt(8)\n\t" \
    "v_pk_add_f32 v[68:69], v[68:69], v[128:129]\n\t" \
    "v_pk_add_f32 v[70:71], v[70:71], v[130:131]\n\t" \
    "s_waitcnt lgkmcnt(7)\n\t" \
    "v_pk_add_f32 v[72:73], v[72:73], v[132:133]\n\t" \
    "v_pk_add_f32 v[74:75], v[74:75], v[134:135]\n\t" \
    "s_waitcnt lgkmcnt(6)\n\t" \
    "v_pk_add_f32 v[76:77], v[76:77], v[136:137]\n\t" \
    "v_pk_add_f32 v[78:79], v[78:79], v[138:139]\n\t" \
    "s_waitcnt lgkmcnt(5)\n\t" \
    "v_pk_add_f32 v[80:81], v[80:81], v[140:141]\n\t" \
    "v_pk_add_f32 v[82:83], v[82:83], v[142:143]\n\t" \
    "s_waitcnt lgkmcnt(4)\n\t" \
    "v_pk_add_f32 v[64:65], v[64:65], v[144:145]\n\t" \
    "v_pk_add_f32 v[66:67], v[66:67], v[146:147]\n\t" \
    "s_waitcnt lgkmcnt(3)\n\t" \
    "v_pk_add_f32 v[68:69], v[68:69], v[148:149]\n\t" \
    "v_pk_add_f32 v[70:71], v[70:71], v[150:151]\n\t" \
    "s_waitcnt lgkmcnt(2)\n\t" \
    "v_pk_add_f32 v[72:73], v[72:73], v[152:153]\n\t" \
    "v_pk_add_f32 v[74:75], v[74:75], v[154:155]\n\t" \
    "s_waitcnt lgkmcnt(1)\n\t" \
    "v_pk_add_f32 v[76:77], v[76:77], v[156:157]\n\t" \
    "v_pk_add_f32 v[78:79], v[78:79], v[158:159]\n\t" \
    "s_waitcnt lgkmcnt(0)\n\t" \
    "v_pk_add_f32 v[80:81], v[80:81], v[160:161]\n\t" \
    "v_pk_add_f32 v[82:83], v[82:83], v[162:163]\n\t" \
    ".Lft1_%=:\n\t" \
    "ds_read_b32 v194, %[f1]\n\t" \
    "s_waitcnt lgkmcnt(0)\n\t" \
    "v_cmp_ge_u32 vcc, %[u], v194\n\t" \
    "s_cbranch_vccnz .Lfs1_%=\n\t" \
    ".Lfu1_%=:\n\t" \
    ASM_ST2 \
    "ds_read_b128 v[84:87], %[la] offset:4096\n\t" \
    "ds_read_b128 v[88:91], %[la] offset:10240\n\t" \
    "ds_read_b128 v[92:95], %[la] offset:16384\n\t" \
    "ds_read_b128 v[96:99], %[la] offset:22528\n\t" \
    "ds_read_b128 v[100:103], %[la] offset:28672\n\t" \
    "ds_read_b128 v[104:107], %[la] offset:5120\n\t" \
    "ds_read_b128 v[108:111], %[la] offset:11264\n\t" \
    "ds_read_b128 v[112:115], %[la] offset:17408\n\t" \
    "ds_read_b128 v[116:119], %[la] offset:23552\n\t" \
    "ds_read_b128 v[120:123], %[la] offset:29696\n\t" \
    "v_mov_b32 v194, %[un]\n\t" \
    "ds_write_b32 %[fd], v194\n\t" \
    "s_waitcnt lgkmcnt(10)\n\t" \
    "v_pk_add_f32 v[64:65], v[64:65], v[84:85]\n\t" \
    "v_pk_add_f32 v[66:67], v[66:67], v[86:87]\n\t" \
    "s_waitcnt lgkmcnt(9)\n\t" \
    "v_pk_add_f32 v[68:69], v[68:69], v[88:89]\n\t" \
    "v_pk_add_f32 v[70:71], v[70:71], v[90:91]\n\t" \
    "s_waitcnt lgkmcnt(8)\n\t" \
    "v_pk_add_f32 v[72:73], v[72:73], v[92:93]\n\t" \
    "v_pk_add_f32 v[74:75], v[74:75], v[94:95]\n\t" \
    "s_waitcnt lgkmcnt(7)\n\t" \
    "v_pk_add_f32 v[76:77], v[76:77], v[96:97]\n\t" \
    "v_pk_add_f32 v[78:79], v[78:79], v[98:99]\n\t" \
    "s_waitcnt lgkmcnt(6)\n\t" \
    "v_pk_add_f32 v[80:81], v[80:81], v[100:101]\n\t" \
    "v_pk_add_f32 v[82:83], v[82:83], v[102:103]\n\t" \
    "s_waitcnt lgkmcnt(5)\n\t" \
    "v_pk_add_f32 v[64:65], v[64:65], v[104:105]\n\t" \
    "v_pk_add_f32 v[66:67], v[66:67], v[106:107]\n\t" \
    "s_waitcnt lgkmcnt(4)\n\t" \
    "v_pk_add_f32 v[68:69], v[68:69], v[108:109]\n\t" \
    "v_pk_add_f32 v[70:71], v[70:71], v[110:111]\n\t" \
    "s_waitcnt lgkmcnt(3)\n\t" \
    "v_pk_add_f32 v[72:73], v[72:73], v[112:113]\n\t" \
    "v_pk_add_f32 v[74:75], v[74:75], v[114:115]\n\t" \
    "s_waitcnt lgkmcnt(2)\n\t" \
    "v_pk_add_f32 v[76:77], v[76:77], v[116:117]\n\t" \
    "v_pk_add_f32 v[78:79], v[78:79], v[118:119]\n\t" \
    "s_waitcnt lgkmcnt(1)\n\t" \
    "v_pk_add_f32 v[80:81], v[80:81], v[120:121]\n\t" \
    "v_pk_add_f32 v[82:83], v[82:83], v[122:123]\n\t" \
    "v_cvt_pk_bf16_f32 v184, v64, v65\n\t" \
    "v_cvt_pk_bf16_f32 v185, v66, v67\n\t" \
    "v_cvt_pk_bf16_f32 v186, v68, v69\n\t" \
    "v_cvt_pk_bf16_f32 v187, v70, v71\n\t" \
    "v_cvt_pk_bf16_f32 v188, v72, v73\n\t" \
    "v_cvt_pk_bf16_f32 v189, v74, v75\n\t" \
    "v_cvt_pk_bf16_f32 v190, v76, v77\n\t" \
    "v_cvt_pk_bf16_f32 v191, v78, v79\n\t" \
    "v_cvt_pk_bf16_f32 v192, v80, v81\n\t" \
    "v_cvt_pk_bf16_f32 v193, v82, v83\n\t" \
    "v_permlane16_swap_b32 v184, v186\n\t" \
    "v_permlane16_swap_b32 v185, v187\n\t" \
    "v_permlane16_swap_b32 v188, v190\n\t" \
    "v_permlane16_swap_b32 v189, v191\n\t" \
    "buffer_store_dwordx4 v[184:187], %[pa], %[ro], %[so] offen nt\n\t" \
    "buffer_store_dwordx4 v[188:191], %[pa], %[ro], %[so] offen offset:64 nt\n\t" \
    "buffer_store_dwordx2 v[192:193], %[pb], %[ro], %[so] offen offset:128 nt\n\t" \
    "buffer_load_dwordx2 v[164:165], %[o0], %[rd], %[n1] offen sc1\n\t" \
    "buffer_load_dwordx2 v[166:167], %[o1], %[rd], %[n1] offen sc1\n\t" \
    "buffer_load_dwordx2 v[168:169], %[o2], %[rd], %[n1] offen sc1\n\t" \
    "buffer_load_dwordx2 v[170:171], %[o3], %[rd], %[n1] offen sc1\n\t" \
    "buffer_load_dwordx2 v[172:173], %[o4], %[rd], %[n1] offen sc1\n\t" \
    "buffer_load_dwordx2 v[174:175], %[o0], %[rd], %[na] offen sc1\n\t" \
    "buffer_load_dwordx2 v[176:177], %[o1], %[rd], %[na] offen sc1\n\t" \
    "buffer_load_dwordx2 v[178:179], %[o2], %[rd], %[na] offen sc1\n\t" \
    "buffer_load_dwordx2 v[180:181], %[o3], %[rd], %[na] offen sc1\n\t" \
    "buffer_load_dwordx2 v[182:183], %[o4], %[rd], %[na] offen sc1\n\t"
#define ASM_WCS \
    "v_mfma_f32_16x16x32_bf16 v[196:199], v[0:3], v[216:219], 0\n\t" \
    "v_mfma_f32_16x16x32_bf16 v[200:203], v[80:83], v[216:219], 0\n\t" \
    "v_mfma_f32_16x16x32_bf16 v[196:199], v[4:7], v[216:219], v[196:199]\n\t" \
    "v_mfma_f32_16x16x32_bf16 v[200:203], v[84:87], v[216:219], v[200:203]\n\t" \
    "v_mfma_f32_16x16x32_bf16 v[196:199], v[8:11], v[216:219], v[196:199]\n\t" \
    "v_mfma_f32_16x16x32_bf16 v[200:203], v[88:91], v[216:219], v[200:203]\n\t" \
    "v_mfma_f32_16x16x32_bf16 v[196:199], v[12:15], v[216:219], v[196:199]\n\t" \
    "v_mfma_f32_16x16x32_bf16 v[200:203], v[92:95], v[216:219], v[200:203]\n\t" \
    "v_mfma_f32_16x16x32_bf16 v[196:199], v[16:19], v[216:219], v[196:199]\n\t" \
    "v_mfma_f32_16x16x32_bf16 v[200:203], v[96:99], v[216:219], v[200:203]\n\t" \
    "v_mfma_f32_16x16x32_bf16 v[196:199], v[20:23], v[216:219], v[196:199]\n\t" \
    "v_mfma_f32_16x16x32_bf16 v[200:203], v[100:103], v[216:219], v[200:203]\n\t" \
    "v_mfma_f32_16x16x32_bf16 v[196:199], v[24:27], v[216:219], v[196:199]\n\t" \
    "v_mfma_f32_16x16x32_bf16 v[200:203], v[104:107], v[216:219], v[200:203]\n\t" \
    "v_mfma_f32_16x16x32_bf16 v[196:199], v[28:31], v[216:219], v[196:199]\n\t" \
    "v_mfma_f32_16x16x32_bf16 v[200:203], v[108:111], v[216:219], v[200:203]\n\t" \
    "v_mfma_f32_16x16x32_bf16 v[196:199], v[32:35], v[216:219], v[196:199]\n\t" \
    "v_mfma_f32_16x16x32_bf16 v[200:203], v[112:115], v[216:219], v[200:203]\n\t" \
    "v_mfma_f32_16x16x32_bf16 v[196:199], v[36:39], v[216:219], v[196:199]\n\t" \
    "v_mfma_f32_16x16x32_bf16 v[200:203], v[116:119], v[216:219], v[200:203]\n\t" \
    "v_mfma_f32_16x16x32_bf16 v[196:199], v[40:43], v[216:219], v[196:199]\n\t" \
    "v_mfma_f32_16x16x32_bf16 v[200:203], v[120:123], v[216:219], v[200:203]\n\t" \
    "v_mfma_f32_16x16x32_bf16 v[196:199], v[44:47], v[216:219], v[196:199]\n\t" \
    "v_mfma_f32_16x16x32_bf16 v[200:203], v[124:127], v[216:219], v[200:203]\n\t" \
    "v_mfma_f32_16x16x32_bf16 v[196:199], v[48:51], v[216:219], v[196:199]\n\t" \
    "v_mfma_f32_16x16x32_bf16 v[200:203], v[128:131], v[216:219], v[200:203]\n\t" \
    "v_mfma_f32_16x16x32_bf16 v[196:199], v[52:55], v[216:219], v[196:199]\n\t" \
    "v_mfma_f32_16x16x32_bf16 v[200:203], v[132:135], v[216:219], v[200:203]\n\t" \
    "v_mfma_f32_16x16x32_bf16 v[196:199], v[56:59], v[216:219], v[196:199]\n\t" \
    "v_mfma_f32_16x16x32_bf16 v[200:203], v[136:139], v[216:219], v[200:203]\n\t" \
    "v_mfma_f32_16x16x32_bf16 v[196:199], v[60:63], v[216:219], v[196:199]\n\t" \
    "v_mfma_f32_16x16x32_bf16 v[200:203], v[140:143], v[216:219], v[200:203]\n\t" \
    "v_mfma_f32_16x16x32_bf16 v[196:199], v[64:67], v[216:219], v[196:199]\n\t" \
    "v_mfma_f32_16x16x32_bf16 v[200:203], v[144:147], v[216:219], v[200:203]\n\t" \
    "v_mfma_f32_16x16x32_bf16 v[196:199], v[68:71], v[216:219], v[196:199]\n\t" \
    "v_mfma_f32_16x16x32_bf16 v[200:203], v[148:151], v[216:219], v[200:203]\n\t" \
    "v_mfma_f32_16x16x32_bf16 v[196:199], v[72:75], v[216:219], v[196:199]\n\t" \
    "v_mfma_f32_16x16x32_bf16 v[200:203], v[152:155], v[216:219], v[200:203]\n\t" \
    "v_mfma_f32_16x16x32_bf16 v[196:199], v[76:79], v[216:219], v[196:199]\n\t" \
    "v_mfma_f32_16x16x32_bf16 v[200:203], v[156:159], v[216:219], v[200:203]\n\t" \
    "s_nop 7\n\t"
#define ASM_WT1_LO \
    "s_branch .Lt0%=\n\t" \
    ".Ltr%=:\n\t" \
    "s_cmp_gt_i32 %[n], 0\n\t" \
    "s_cbranch_scc0 .Ltg%=\n\t" \
    ".Lfw%=:\n\t" \
    "s_mov_b32 %[a], 0\n\t" \
    "v_max_u32 v252, v232, v233\n\t" \
    "v_max3_u32 v252, v234, v235, v252\n\t" \
    "v_cmp_eq_u32 vcc, -1, v252\n\t" \
    "s_cbranch_vccz .Lgw0_%=\n\t" \
    "buffer_load_dwordx4 v[232:235], %[l16], %[rs], %[i0] offen offset:0 sc1\n\t" \
    "s_mov_b32 %[a], 1\n\t" \
    ".Lgw0_%=:\n\t" \
    "v_max_u32 v252, v236, v237\n\t" \
    "v_max3_u32 v252, v238, v239, v252\n\t" \
    "v_cmp_eq_u32 vcc, -1, v252\n\t" \
    "s_cbranch_vccz .Lgw1_%=\n\t" \
    "buffer_load_dwordx4 v[236:239], %[l16], %[rs], %[i0] offen offset:1024 sc1\n\t" \
    "s_mov_b32 %[a], 1\n\t" \
    ".Lgw1_%=:\n\t" \
    "v_max_u32 v252, v240, v241\n\t" \
    "v_max3_u32 v252, v242, v243, v252\n\t" \
    "v_cmp_eq_u32 vcc, -1, v252\n\t" \
    "s_cbranch_vccz .Lgw2_%=\n\t" \
    "buffer_load_dwordx4 v[240:243], %[l16], %[rs], %[i0] offen offset:2048 sc1\n\t" \
    "s_mov_b32 %[a], 1\n\t" \
    ".Lgw2_%=:\n\t" \
    "v_max_u32 v252, v244, v245\n\t" \
    "v_max3_u32 v252, v246, v247, v252\n\t" \
    "v_cmp_eq_u32 vcc, -1, v252\n\t" \
    "s_cbranch_vccz .Lgw3_%=\n\t" \
    "buffer_load_dwordx4 v[244:247], %[l16], %[rs], %[i0] offen offset:3072 sc1\n\t" \
    "s_mov_b32 %[a], 1\n\t" \
    ".Lgw3_%=:\n\t" \
    "v_max_u32 v252, v248, v249\n\t" \
    "v_max3_u32 v252, v250, v251, v252\n\t" \
    "v_cmp_eq_u32 vcc, -1, v252\n\t" \
    "s_cbranch_vccz .Lgw4_%=\n\t" \
    "buffer_load_dwordx4 v[248:251], %[l16], %[rs], %[i1] offen offset:0 sc1\n\t" \
    "s_mov_b32 %[a], 1\n\t" \
    ".Lgw4_%=:\n\t" \
    "s_cmp_eq_u32 %[a], 0\n\t" \
    "s_cbranch_scc1 .Lhw%=\n\t" \
    "s_waitcnt vmcnt(20)\n\t" \
    ".Liw%=:\n\t" \
    "v_max_u32 v252, v232, v233\n\t" \
    "v_max3_u32 v252, v234, v235, v252\n\t" \
    "v_max3_u32 v252, v236, v237, v252\n\t" \
    "v_max3_u32 v252, v238, v239, v252\n\t" \
    "v_max3_u32 v252, v240, v241, v252\n\t" \
    "v_max3_u32 v252, v242, v243, v252\n\t" \
    "v_max3_u32 v252, v244, v245, v252\n\t" \
    "v_max3_u32 v252, v246, v247, v252\n\t" \
    "v_max3_u32 v252, v248, v249, v252\n\t" \
    "v_max3_u32 v252, v250, v251, v252\n\t" \
    "v_cmp_eq_u32 vcc, -1, v252\n\t" \
    "s_cbranch_vccz .Lhw%=\n\t" \
    "s_sleep 1\n\t" \
    "v_max_u32 v252, v232, v233\n\t" \
    "v_max3_u32 v252, v234, v235, v252\n\t" \
    "v_max3_u32 v252, v236, v237, v252\n\t" \
    "v_max3_u32 v252, v238, v239, v252\n\t" \
    "v_max3_u32 v252, v240, v241, v252\n\t" \
    "v_max3_u32 v252, v242, v243, v252\n\t" \
    "v_max3_u32 v252, v244, v245, v252\n\t" \
    "v_max3_u32 v252, v246, v247, v252\n\t" \
    "v_max3_u32 v252, v248, v249, v252\n\t" \
    "v_max3_u32 v252, v250, v251, v252\n\t" \
    "v_cmp_eq_u32 vcc, -1, v252\n\t" \
    "s_cbranch_vccz .Lhw%=\n\t" \
    "s_sleep 1\n\t" \
    "v_max_u32 v252, v232, v233\n\t" \
    "v_max3_u32 v252, v234, v235, v252\n\t" \
    "v_max3_u32 v252, v236, v237, v252\n\t" \
    "v_max3_u32 v252, v238, v239, v252\n\t" \
    "v_max3_u32 v252, v240, v241, v252\n\t" \
    "v_max3_u32 v252, v242, v243, v252\n\t" \
    "v_max3_u32 v252, v244, v245, v252\n\t" \
    "v_max3_u32 v252, v246, v247, v252\n\t" \
    "v_max3_u32 v252, v248, v249, v252\n\t" \
    "v_max3_u32 v252, v250, v251, v252\n\t" \
    "v_cmp_eq_u32 vcc, -1, v252\n\t" \
    "s_cbranch_vccz .Lhw%=\n\t" \
    "s_sleep 1\n\t" \
    "v_max_u32 v252, v232, v233\n\t" \
    "v_max3_u32 v252, v234, v235, v252\n\t" \
    "v_max3_u32 v252, v236, v237, v252\n\t" \
    "v_max3_u32 v252, v238, v239, v252\n\t" \
    "v_max3_u32 v252, v240, v241, v252\n\t" \
    "v_max3_u32 v252, v242, v243, v252\n\t" \
    "v_max3_u32 v252, v244, v245, v252\n\t" \
    "v_max3_u32 v252, v246, v247, v252\n\t" \
    "v_max3_u32 v252, v248, v249, v252\n\t" \
    "v_max3_u32 v252, v250, v251, v252\n\t" \
    "v_cmp_eq_u32 vcc, -1, v252\n\t" \
    "s_cbranch_vccz .Lhw%=\n\t" \
    "s_sleep 1\n\t" \
    "v_max_u32 v252, v232, v233\n\t" \
    "v_max3_u32 v252, v234, v235, v252\n\t" \
    "v_max3_u32 v252, v236, v237, v252\n\t" \
    "v_max3_u32 v252, v238, v239, v252\n\t" \
    "v_max3_u32 v252, v240, v241, v252\n\t" \
    "v_max3_u32 v252, v242, v243, v252\n\t" \
    "v_max3_u32 v252, v244, v245, v252\n\t" \
    "v_max3_u32 v252, v246, v247, v252\n\t" \
    "v_max3_u32 v252, v248, v249, v252\n\t" \
    "v_max3_u32 v252, v250, v251, v252\n\t" \
    "v_cmp_eq_u32 vcc, -1, v252\n\t" \
    "s_cbranch_vccz .Lhw%=\n\t" \
    "s_sleep 1\n\t" \
    "v_max_u32 v252, v232, v233\n\t" \
    "v_max3_u32 v252, v234, v235, v252\n\t" \
    "v_max3_u32 v252, v236, v237, v252\n\t" \
    "v_max3_u32 v252, v238, v239, v252\n\t" \
    "v_max3_u32 v252, v240, v241, v252\n\t" \
    "v_max3_u32 v252, v242, v243, v252\n\t" \
    "v_max3_u32 v252, v244, v245, v252\n\t" \
    "v_max3_u32 v252, v246, v247, v252\n\t" \
    "v_max3_u32 v252, v248, v249, v252\n\t" \
    "v_max3_u32 v252, v250, v251, v252\n\t" \
    "v_cmp_eq_u32 vcc, -1, v252\n\t" \
    "s_cbranch_vccz .Lhw%=\n\t" \
    "s_sleep 1\n\t" \
    "s_sub_u32 %[n], %[n], 1\n\t" \
    "s_cmp_gt_i32 %[n], 0\n\t" \
    "s_cbranch_scc1 .Lfw%=\n\t" \
    ".Lhw%=:\n\t" \
    "s_branch .Lta%=\n\t" \
    ".Lt0%=:\n\t" \
    "s_waitcnt vmcnt(1)\n\t" \
    ".Lta%=:\n\t" \
    "ds_write_b128 %[lw], v[232:235] offset:0\n\t" \
    "v_max_u32 v252, v232, v233\n\t" \
    "v_max3_u32 v252, v234, v235, v252\n\t" \
    "v_mfma_f32_16x16x32_bf16 v[188:191], v[232:235], v[232:235], 0\n\t" \
    "ds_write_b128 %[lw], v[236:239] offset:1024\n\t" \
    "v_max3_u32 v252, v236, v237, v252\n\t" \
    "v_max3_u32 v252, v238, v239, v252\n\t" \
    "v_mfma_f32_16x16x32_bf16 v[188:191], v[236:239], v[236:239], v[188:191]\n\t" \
    "ds_write_b128 %[lw], v[240:243] offset:2048\n\t" \
    "v_max3_u32 v252, v240, v241, v252\n\t" \
    "v_max3_u32 v252, v242, v243, v252\n\t" \
    "v_mfma_f32_16x16x32_bf16 v[188:191], v[240:243], v[240:243], v[188:191]\n\t" \
    "ds_write_b128 %[lw], v[244:247] offset:3072\n\t" \
    "v_max3_u32 v252, v244, v245, v252\n\t" \
    "v_max3_u32 v252, v246, v247, v252\n\t" \
    "v_mfma_f32_16x16x32_bf16 v[188:191], v[244:247], v[244:247], v[188:191]\n\t" \
    "ds_write_b128 %[lw], v[248:251] offset:4096\n\t" \
    "v_max3_u32 v252, v248, v249, v252\n\t" \
    "v_max3_u32 v252, v250, v251, v252\n\t" \
    "v_mfma_f32_16x16x32_bf16 v[188:191], v[248:251], v[248:251], v[188:191]\n\t" \
    "v_mfma_f32_16x16x32_bf16 v[192:195], v[216:219], v[232:235], 0\n\t" \
    "v_mfma_f32_16x16x32_bf16 v[192:195], v[216:219], v[236:239], v[192:195]\n\t" \
    "v_mfma_f32_16x16x32_bf16 v[192:195], v[216:219], v[240:243], v[192:195]\n\t" \
    "v_mfma_f32_16x16x32_bf16 v[192:195], v[216:219], v[244:247], v[192:195]\n\t" \
    "v_mfma_f32_16x16x32_bf16 v[192:195], v[216:219], v[248:251], v[192:195]\n\t" \
    "v_cmp_eq_u32 vcc, -1, v252\n\t" \
    "s_cbranch_vccnz .Ltr%=\n\t" \
    ".Ltg%=:\n\t" \
    ASM_ST2 \
    "s_nop 5\n\t" \
    "ds_write_b128 %[lw], v[188:191] offset:5120\n\t" \
    "ds_write_b128 %[lw], v[192:195] offset:6144\n\t" \
    "s_waitcnt lgkmcnt(0)\n\t" \
    "s_setprio 2\n\t" \
    "s_barrier\n\t" ASM_ST \
    "ds_read_b128 v[160:163], %[la] offset:0\n\t" \
    "ds_read_b128 v[164:167], %[la] offset:1024\n\t" \
    "ds_read_b128 v[168:171], %[la] offset:2048\n\t" \
    "ds_read2st64_b32 v[204:205], %[sr] offset0:20 offset1:24\n\t" \
    "ds_read_b128 v[172:175], %[la] offset:3072\n\t" \
    "ds_read_b128 v[176:179], %[la] offset:8192\n\t" \
    "ds_read_b128 v[180:183], %[la] offset:9216\n\t" \
    "ds_read_b128 v[184:187], %[la] offset:10240\n\t" \
    "s_waitcnt lgkmcnt(7)\n\t" \
    "v_mfma_f32_16x16x32_bf16 v[192:195], v[80:83], v[160:163], 0\n\t" \
    "s_waitcnt lgkmcnt(6)\n\t" \
    "v_mfma_f32_16x16x32_bf16 v[192:195], v[84:87], v[164:167], v[192:195]\n\t" \
    "v_mfma_f32_16x16x32_bf16 v[188:191], v[0:3], v[160:163], 0\n\t" \
    "ds_read_b128 v[160:163], %[la] offset:11264\n\t" \
    "v_mfma_f32_16x16x32_bf16 v[188:191], v[4:7], v[164:167], v[188:191]\n\t" \
    "ds_read_b128 v[164:167], %[la] offset:16384\n\t" \
    "s_waitcnt lgkmcnt(7)\n\t" \
    "v_mfma_f32_16x16x32_bf16 v[188:191], v[8:11], v[168:171], v[188:191]\n\t" \
    "s_waitcnt lgkmcnt(6)\n\t" \
    "s_waitcnt lgkmcnt(5)\n\t" \
    "v_mfma_f32_16x16x32_bf16 v[188:191], v[12:15], v[172:175], v[188:191]\n\t" \
    "v_permlane16_swap_b32 v204, v205\n\t" \
    "v_mfma_f32_16x16x32_bf16 v[192:195], v[88:91], v[168:171], v[192:195]\n\t" \
    "ds_read_b128 v[168:171], %[la] offset:17408\n\t" \
    "v_mfma_f32_16x16x32_bf16 v[192:195], v[92:95], v[172:175], v[192:195]\n\t" \
    "ds_read_b128 v[172:175], %[la] offset:18432\n\t" \
    "v_add_f32 v206, v204, v205\n\t" \
    "s_waitcnt lgkmcnt(6)\n\t" \
    "v_mfma_f32_16x16x32_bf16 v[192:195], v[96:99], v[176:179], v[192:195]\n\t" \
    "v_add_f32 v207, v204, v205\n\t" \
    "s_waitcnt lgkmcnt(5)\n\t" \
    "v_mfma_f32_16x16x32_bf16 v[192:195], v[100:103], v[180:183], v[192:195]\n\t" \
    "v_mfma_f32_16x16x32_bf16 v[188:191], v[16:19], v[176:179], v[188:191]\n\t" \
    "ds_read_b128 v[176:179], %[la] offset:19456\n\t" \
    "v_permlane32_swap_b32 v206, v207\n\t" \
    "v_mfma_f32_16x16x32_bf16 v[188:191], v[20:23], v[180:183], v[188:191]\n\t" \
    "ds_read_b128 v[180:183], %[la] offset:24576\n\t" \
    "s_waitcnt lgkmcnt(6)\n\t" \
    "v_mfma_f32_16x16x32_bf16 v[188:191], v[24:27], v[184:187], v[188:191]\n\t" \
    "v_add_f32 v204, v206, v207\n\t" \
    "s_waitcnt lgkmcnt(5)\n\t" \
    "v_mfma_f32_16x16x32_bf16 v[188:191], v[28:31], v[160:163], v[188:191]\n\t" \
    "v_add_f32 v205, v206, v207\n\t" \
    "v_mfma_f32_16x16x32_bf16 v[192:195], v[104:107], v[184:187], v[192:195]\n\t" \
    "ds_read_b128 v[184:187], %[la] offset:25600\n\t" \
    "v_mfma_f32_16x16x32_bf16 v[192:195], v[108:111], v[160:163], v[192:195]\n\t" \
    "ds_read_b128 v[160:163], %[la] offset:26624\n\t" \
    "v_permlane16_swap_b32 v204, v205\n\t" \
    "s_waitcnt lgkmcnt(6)\n\t" \
    "v_mfma_f32_16x16x32_bf16 v[192:195], v[112:115], v[164:167], v[192:195]\n\t" \
    "s_waitcnt lgkmcnt(5)\n\t" \
    "v_mfma_f32_16x16x32_bf16 v[192:195], v[116:119], v[168:171], v[192:195]\n\t" \
    "v_mul_f32 v205, %[kd], v205\n\t" \
    "v_mfma_f32_16x16x32_bf16 v[188:191], v[32:35], v[164:167], v[188:191]\n\t" \
    "ds_read_b128 v[164:167], %[la] offset:27648\n\t" \
    "v_mul_f32 v204, %[kd], v204\n\t" \
    "v_mfma_f32_16x16x32_bf16 v[188:191], v[36:39], v[168:171], v[188:191]\n\t" \
    "ds_read_b128 v[168:171], %[la] offset:4096\n\t" \
    "v_fma_f32 v204, -v205, v205, v204\n\t" \
    "s_waitcnt lgkmcnt(6)\n\t" \
    "v_mfma_f32_16x16x32_bf16 v[188:191], v[40:43], v[172:175], v[188:191]\n\t" \
    "v_max_f32 v204, 0, v204\n\t" \
    "s_waitcnt lgkmcnt(5)\n\t" \
    "v_mfma_f32_16x16x32_bf16 v[188:191], v[44:47], v[176:179], v[188:191]\n\t" \
    "v_add_f32 v206, %[ke], v204\n\t" \
    "v_mfma_f32_16x16x32_bf16 v[192:195], v[120:123], v[172:175], v[192:195]\n\t" \
    "ds_read_b128 v[172:175], %[la] offset:12288\n\t" \
    "v_rsq_f32 v206, v206\n\t" \
    "v_mfma_f32_16x16x32_bf16 v[192:195], v[124:127], v[176:179], v[192:195]\n\t" \
    "ds_read_b128 v[176:179], %[la] offset:20480\n\t" \
    "s_waitcnt lgkmcnt(6)\n\t" \
    "v_mfma_f32_16x16x32_bf16 v[192:195], v[128:131], v[180:183], v[192:195]\n\t" \
    "v_mul_f32 v207, v204, v206\n\t" \
    "s_waitcnt lgkmcnt(5)\n\t" \
    "v_mfma_f32_16x16x32_bf16 v[192:195], v[132:135], v[184:187], v[192:195]\n\t" \
    "v_fma_f32 v207, v207, v206, %[ke]\n\t" \
    "v_mfma_f32_16x16x32_bf16 v[188:191], v[48:51], v[180:183], v[188:191]\n\t" \
    "ds_read_b128 v[180:183], %[la] offset:28672\n\t" \
    "v_rsq_f32 v207, v207\n\t" \
    "v_mfma_f32_16x16x32_bf16 v[188:191], v[52:55], v[184:187], v[188:191]\n\t" \
    "v_mul_f32 v206, v206, v207\n\t" \
    "v_mul_f32 v207, -v205, v206\n\t" \
    "s_waitcnt lgkmcnt(5)\n\t" \
    "v_mfma_f32_16x16x32_bf16 v[188:191], v[56:59], v[160:163], v[188:191]\n\t" \
    "v_mul_f32 v208, v196, v207\n\t" \
    "v_mul_f32 v209, v197, v207\n\t" \
    "s_waitcnt lgkmcnt(4)\n\t" \
    "v_mfma_f32_16x16x32_bf16 v[188:191], v[60:63], v[164:167], v[188:191]\n\t" \
    "v_mul_f32 v210, v198, v207\n\t" \
    "v_mul_f32 v211, v199, v207\n\t" \
    "v_mfma_f32_16x16x32_bf16 v[192:195], v[136:139], v[160:163], v[192:195]\n\t" \
    "v_mul_f32 v212, v200, v207\n\t" \
    "v_mul_f32 v213, v201, v207\n\t" \
    "v_mfma_f32_16x16x32_bf16 v[192:195], v[140:143], v[164:167], v[192:195]\n\t" \
    "v_mul_f32 v214, v202, v207\n\t" \
    "v_mul_f32 v215, v203, v207\n\t" \
    "s_waitcnt lgkmcnt(3)\n\t" \
    "v_mfma_f32_16x16x32_bf16 v[192:195], v[144:147], v[168:171], v[192:195]\n\t" \
    "s_cmp_lg_u32 %[sw], 0\n\t" \
    "s_cbranch_scc1 .Lts%=\n\t" \
    "v_cvt_pk_bf16_f32 v217, v204, v204\n\t" \
    "v_lshlrev_b32 v217, 16, v217\n\t" \
    "v_add_f32 v217, %[ke], v217\n\t" \
    "v_cvt_pk_bf16_f32 v217, v217, v217\n\t" \
    "v_lshlrev_b32 v217, 16, v217\n\t" \
    "v_sqrt_f32 v217, v217\n\t" \
    "v_cvt_pk_bf16_f32 v216, v205, v205\n\t" \
    "v_lshlrev_b32 v216, 16, v216\n\t" \
    "v_cvt_pk_bf16_f32 v217, v217, v217\n\t" \
    "v_lshlrev_b32 v217, 16, v217\n\t" \
    "v_rcp_f32 v217, v217\n\t" \
    "v_lshrrev_b32 v218, 1, %[l16]\n\t" \
    "v_and_b32 v218, 0x78, v218\n\t" \
    "buffer_store_dwordx2 v[216:217], v218, %[rs], %[ss] offen sc1\n\t" \
    ".Lts%=:\n\t" \
    "v_mov_b64 v[216:217], -1\n\t" \
    "s_waitcnt lgkmcnt(2)\n\t" \
    "v_mfma_f32_16x16x32_bf16 v[192:195], v[148:151], v[172:175], v[192:195]\n\t" \
    "v_mov_b64 v[218:219], -1\n\t" \
    "buffer_store_dwordx4 v[216:219], %[ob], %[rs], %[sa] offen sc1\n\t" \
    "v_mfma_f32_16x16x32_bf16 v[188:191], v[64:67], v[168:171], v[188:191]\n\t" \
    "v_mfma_f32_16x16x32_bf16 v[188:191], v[68:71], v[172:175], v[188:191]\n\t" \
    "s_waitcnt lgkmcnt(1)\n\t" \
    "v_mfma_f32_16x16x32_bf16 v[188:191], v[72:75], v[176:179], v[188:191]\n\t" \
    "s_waitcnt lgkmcnt(0)\n\t" \
    "v_mfma_f32_16x16x32_bf16 v[188:191], v[76:79], v[180:183], v[188:191]\n\t" \
    "v_mfma_f32_16x16x32_bf16 v[192:195], v[152:155], v[176:179], v[192:195]\n\t" \
    "v_mfma_f32_16x16x32_bf16 v[192:195], v[156:159], v[180:183], v[192:195]\n\t" \
    "s_setprio 2\n\t" \
    "buffer_load_dwordx4 v[232:235], %[l16], %[rs], %[n0] offen offset:0 sc1\n\t" \
    "buffer_load_dwordx4 v[236:239], %[l16], %[rs], %[n0] offen offset:1024 sc1\n\t" \
    "buffer_load_dwordx4 v[240:243], %[l16], %[rs], %[n0] offen offset:2048 sc1\n\t" \
    "buffer_load_dwordx4 v[244:247], %[l16], %[rs], %[n0] offen offset:3072 sc1\n\t" \
    "buffer_load_dwordx4 v[248:251], %[l16], %[rs], %[n1] offen offset:0 sc1\n\t" \
    ASM_ST3 \
    "v_fma_f32 v188, v188, v206, v208\n\t" \
    "v_fma_f32 v189, v189, v206, v209\n\t" \
    "v_fma_f32 v190, v190, v206, v210\n\t" \
    "v_fma_f32 v191, v191, v206, v211\n\t" \
    "v_mul_f32 v160, v188, v188\n\t" \
    "v_mul_f32 v161, v189, v189\n\t" \
    "v_mul_f32 v162, v190, v190\n\t" \
    "v_mul_f32 v163, v191, v191\n\t" \
    "v_fma_f32 v160, v160, %[k1], v220\n\t" \
    "v_fma_f32 v161, v161, %[k1], v220\n\t" \
    "v_fma_f32 v162, v162, %[k1], v220\n\t" \
    "v_fma_f32 v163, v163, %[k1], v220\n\t" \
    "v_mul_f32 v160, v188, v160\n\t" \
    "v_mul_f32 v161, v189, v161\n\t" \
    "v_mul_f32 v162, v190, v162\n\t" \
    "v_mul_f32 v163, v191, v163\n\t" \
    "v_fma_f32 v192, v192, v206, v212\n\t" \
    "v_fma_f32 v193, v193, v206, v213\n\t" \
    "v_fma_f32 v194, v194, v206, v214\n\t" \
    "v_fma_f32 v195, v195, v206, v215\n\t" \
    "v_mul_f32 v164, v192, v192\n\t" \
    "v_mul_f32 v165, v193, v193\n\t" \
    "v_mul_f32 v166, v194, v194\n\t" \
    "v_mul_f32 v167, v195, v195\n\t" \
    "v_fma_f32 v164, v164, %[k1], v220\n\t" \
    "v_fma_f32 v165, v165, %[k1], v220\n\t" \
    "v_fma_f32 v166, v166, %[k1], v220\n\t" \
    "v_fma_f32 v167, v167, %[k1], v220\n\t" \
    "v_mul_f32 v164, v192, v164\n\t" \
    "v_mul_f32 v165, v193, v165\n\t" \
    "v_mul_f32 v166, v194, v166\n\t" \
    "v_mul_f32 v167, v195, v167\n\t" \
    "v_exp_f32 v160, v160\n\t" \
    "v_exp_f32 v161, v161\n\t" \
    "v_exp_f32 v162, v162\n\t" \
    "v_exp_f32 v163, v163\n\t" \
    "v_exp_f32 v164, v164\n\t" \
    "v_exp_f32 v165, v165\n\t" \
    "v_exp_f32 v166, v166\n\t" \
    "v_exp_f32 v167, v167\n\t" \
    "v_add_f32 v160, 1.0, v160\n\t" \
    "v_add_f32 v161, 1.0, v161\n\t" \
    "v_add_f32 v162, 1.0, v162\n\t" \
    "v_add_f32 v163, 1.0, v163\n\t" \
    "v_add_f32 v164, 1.0, v164\n\t" \
    "v_add_f32 v165, 1.0, v165\n\t" \
    "v_add_f32 v166, 1.0, v166\n\t" \
    "v_add_f32 v167, 1.0, v167\n\t" \
    "v_rcp_f32 v160, v160\n\t" \
    "v_rcp_f32 v161, v161\n\t" \
    "v_rcp_f32 v162, v162\n\t" \
    "v_rcp_f32 v163, v163\n\t" \
    "v_rcp_f32 v164, v164\n\t" \
    "v_rcp_f32 v165, v165\n\t" \
    "v_rcp_f32 v166, v166\n\t" \
    "v_rcp_f32 v167, v167\n\t" \
    "v_mul_f32 v188, v188, v160\n\t" \
    "v_mul_f32 v189, v189, v161\n\t" \
    "v_mul_f32 v190, v190, v162\n\t" \
    "v_mul_f32 v191, v191, v163\n\t" \
    "v_mul_f32 v192, v192, v164\n\t" \
    "v_mul_f32 v193, v193, v165\n\t" \
    "v_mul_f32 v194, v194, v166\n\t" \
    "v_mul_f32 v195, v195, v167\n\t" \
    "v_cvt_pk_bf16_f32 v188, v188, v189\n\t" \
    "v_cvt_pk_bf16_f32 v189, v190, v191\n\t" \
    "v_cvt_pk_bf16_f32 v190, v192, v193\n\t" \
    "v_cvt_pk_bf16_f32 v191, v194, v195\n\t" \
    "v_min_u32 v188, -2, v188\n\t" \
    "v_min_u32 v189, -2, v189\n\t" \
    "v_min_u32 v190, -2, v190\n\t" \
    "v_min_u32 v191, -2, v191\n\t" \
    "v_mov_b32 v216, 0x3f803f80\n\t" \
    "v_mov_b32 v217, 0x3f803f80\n\t" \
    "v_mov_b32 v218, 0x3f803f80\n\t" \
    "v_mov_b32 v219, 0x3f803f80\n\t" \
    "v_permlane16_swap_b32 v188, v190\n\t" \
    "v_permlane16_swap_b32 v189, v191\n\t" \
    "s_waitcnt vmcnt(6)\n\t" \
    "buffer_store_dwordx4 v[188:191], %[oa], %[rs], %[so] offen sc1\n\t" \
    "s_nop 1\n\t" \
    "s_setprio 0\n\t"
#define ASM_WT1_HI \
    "s_setprio 1\n\t" \
    "s_barrier\n\t" ASM_ST \
    "ds_read_b128 v[160:163], %[la] offset:0\n\t" \
    "ds_read_b128 v[164:167], %[la] offset:1024\n\t" \
    "ds_read_b128 v[168:171], %[la] offset:2048\n\t" \
    "ds_read2st64_b32 v[204:205], %[sr] offset0:20 offset1:24\n\t" \
    "ds_read_b128 v[172:175], %[la] offset:3072\n\t" \
    "ds_read_b128 v[176:179], %[la] offset:8192\n\t" \
    "ds_read_b128 v[180:183], %[la] offset:9216\n\t" \
    "ds_read_b128 v[184:187], %[la] offset:10240\n\t" \
    "s_waitcnt lgkmcnt(7)\n\t" \
    "v_mfma_f32_16x16x32_bf16 v[192:195], v[80:83], v[160:163], 0\n\t" \
    "s_waitcnt lgkmcnt(6)\n\t" \
    "v_mfma_f32_16x16x32_bf16 v[192:195], v[84:87], v[164:167], v[192:195]\n\t" \
    "v_mfma_f32_16x16x32_bf16 v[188:191], v[0:3], v[160:163], 0\n\t" \
    "ds_read_b128 v[160:163], %[la] offset:11264\n\t" \
    "v_mfma_f32_16x16x32_bf16 v[188:191], v[4:7], v[164:167], v[188:191]\n\t" \
    "ds_read_b128 v[164:167], %[la] offset:16384\n\t" \
    "s_waitcnt lgkmcnt(7)\n\t" \
    "v_mfma_f32_16x16x32_bf16 v[188:191], v[8:11], v[168:171], v[188:191]\n\t" \
    "s_waitcnt lgkmcnt(6)\n\t" \
    "s_waitcnt lgkmcnt(5)\n\t" \
    "v_mfma_f32_16x16x32_bf16 v[188:191], v[12:15], v[172:175], v[188:191]\n\t" \
    "v_permlane16_swap_b32 v204, v205\n\t" \
    "v_mfma_f32_16x16x32_bf16 v[192:195], v[88:91], v[168:171], v[192:195]\n\t" \
    "ds_read_b128 v[168:171], %[la] offset:17408\n\t" \
    "v_mfma_f32_16x16x32_bf16 v[192:195], v[92:95], v[172:175], v[192:195]\n\t" \
    "ds_read_b128 v[172:175], %[la] offset:18432\n\t" \
    "v_add_f32 v206, v204, v205\n\t" \
    "s_waitcnt lgkmcnt(6)\n\t" \
    "v_mfma_f32_16x16x32_bf16 v[192:195], v[96:99], v[176:179], v[192:195]\n\t" \
    "v_add_f32 v207, v204, v205\n\t" \
    "s_waitcnt lgkmcnt(5)\n\t" \
    "v_mfma_f32_16x16x32_bf16 v[192:195], v[100:103], v[180:183], v[192:195]\n\t" \
    "v_mfma_f32_16x16x32_bf16 v[188:191], v[16:19], v[176:179], v[188:191]\n\t" \
    "ds_read_b128 v[176:179], %[la] offset:19456\n\t" \
    "v_permlane32_swap_b32 v206, v207\n\t" \
    "v_mfma_f32_16x16x32_bf16 v[188:191], v[20:23], v[180:183], v[188:191]\n\t" \
    "ds_read_b128 v[180:183], %[la] offset:24576\n\t" \
    "s_waitcnt lgkmcnt(6)\n\t" \
    "v_mfma_f32_16x16x32_bf16 v[188:191], v[24:27], v[184:187], v[188:191]\n\t" \
    "v_add_f32 v204, v206, v207\n\t" \
    "s_waitcnt lgkmcnt(5)\n\t" \
    "v_mfma_f32_16x16x32_bf16 v[188:191], v[28:31], v[160:163], v[188:191]\n\t" \
    "v_add_f32 v205, v206, v207\n\t" \
    "v_mfma_f32_16x16x32_bf16 v[192:195], v[104:107], v[184:187], v[192:195]\n\t" \
    "ds_read_b128 v[184:187], %[la] offset:25600\n\t" \
    "v_mfma_f32_16x16x32_bf16 v[192:195], v[108:111], v[160:163], v[192:195]\n\t" \
    "ds_read_b128 v[160:163], %[la] offset:26624\n\t" \
    "v_permlane16_swap_b32 v204, v205\n\t" \
    "s_waitcnt lgkmcnt(6)\n\t" \
    "v_mfma_f32_16x16x32_bf16 v[192:195], v[112:115], v[164:167], v[192:195]\n\t" \
    "s_waitcnt lgkmcnt(5)\n\t" \
    "v_mfma_f32_16x16x32_bf16 v[192:195], v[116:119], v[168:171], v[192:195]\n\t" \
    "v_mul_f32 v205, %[kd], v205\n\t" \
    "v_mfma_f32_16x16x32_bf16 v[188:191], v[32:35], v[164:167], v[188:191]\n\t" \
    "ds_read_b128 v[164:167], %[la] offset:27648\n\t" \
    "v_mul_f32 v204, %[kd], v204\n\t" \
    "v_mfma_f32_16x16x32_bf16 v[188:191], v[36:39], v[168:171], v[188:191]\n\t" \
    "ds_read_b128 v[168:171], %[la] offset:4096\n\t" \
    "v_fma_f32 v204, -v205, v205, v204\n\t" \
    "s_waitcnt lgkmcnt(6)\n\t" \
    "v_mfma_f32_16x16x32_bf16 v[188:191], v[40:43], v[172:175], v[188:191]\n\t" \
    "v_max_f32 v204, 0, v204\n\t" \
    "s_waitcnt lgkmcnt(5)\n\t" \
    "v_mfma_f32_16x16x32_bf16 v[188:191], v[44:47], v[176:179], v[188:191]\n\t" \
    "v_add_f32 v206, %[ke], v204\n\t" \
    "v_mfma_f32_16x16x32_bf16 v[192:195], v[120:123], v[172:175], v[192:195]\n\t" \
    "ds_read_b128 v[172:175], %[la] offset:12288\n\t" \
    "v_rsq_f32 v206, v206\n\t" \
    "v_mfma_f32_16x16x32_bf16 v[192:195], v[124:127], v[176:179], v[192:195]\n\t" \
    "ds_read_b128 v[176:179], %[la] offset:20480\n\t" \
    "s_waitcnt lgkmcnt(6)\n\t" \
    "v_mfma_f32_16x16x32_bf16 v[192:195], v[128:131], v[180:183], v[192:195]\n\t" \
    "v_mul_f32 v207, v204, v206\n\t" \
    "s_waitcnt lgkmcnt(5)\n\t" \
    "v_mfma_f32_16x16x32_bf16 v[192:195], v[132:135], v[184:187], v[192:195]\n\t" \
    "v_fma_f32 v207, v207, v206, %[ke]\n\t" \
    "v_mfma_f32_16x16x32_bf16 v[188:191], v[48:51], v[180:183], v[188:191]\n\t" \
    "ds_read_b128 v[180:183], %[la] offset:28672\n\t" \
    "v_rsq_f32 v207, v207\n\t" \
    "v_mfma_f32_16x16x32_bf16 v[188:191], v[52:55], v[184:187], v[188:191]\n\t" \
    "v_mul_f32 v206, v206, v207\n\t" \
    "v_mul_f32 v207, -v205, v206\n\t" \
    "s_waitcnt lgkmcnt(5)\n\t" \
    "v_mfma_f32_16x16x32_bf16 v[188:191], v[56:59], v[160:163], v[188:191]\n\t" \
    "v_mul_f32 v208, v196, v207\n\t" \
    "v_mul_f32 v209, v197, v207\n\t" \
    "s_waitcnt lgkmcnt(4)\n\t" \
    "v_mfma_f32_16x16x32_bf16 v[188:191], v[60:63], v[164:167], v[188:191]\n\t" \
    "v_mul_f32 v210, v198, v207\n\t" \
    "v_mul_f32 v211, v199, v207\n\t" \
    "v_mfma_f32_16x16x32_bf16 v[192:195], v[136:139], v[160:163], v[192:195]\n\t" \
    "v_mul_f32 v212, v200, v207\n\t" \
    "v_mul_f32 v213, v201, v207\n\t" \
    "v_mfma_f32_16x16x32_bf16 v[192:195], v[140:143], v[164:167], v[192:195]\n\t" \
    "v_mul_f32 v214, v202, v207\n\t" \
    "v_mul_f32 v215, v203, v207\n\t" \
    "s_waitcnt lgkmcnt(3)\n\t" \
    "v_mfma_f32_16x16x32_bf16 v[192:195], v[144:147], v[168:171], v[192:195]\n\t" \
    "v_mov_b64 v[216:217], -1\n\t" \
    "v_mov_b64 v[218:219], -1\n\t" \
    "s_waitcnt lgkmcnt(2)\n\t" \
    "v_mfma_f32_16x16x32_bf16 v[192:195], v[148:151], v[172:175], v[192:195]\n\t" \
    "buffer_store_dwordx4 v[216:219], %[ob], %[rs], %[sa] offen sc1\n\t" \
    "v_mfma_f32_16x16x32_bf16 v[188:191], v[64:67], v[168:171], v[188:191]\n\t" \
    "v_mfma_f32_16x16x32_bf16 v[188:191], v[68:71], v[172:175], v[188:191]\n\t" \
    "s_waitcnt lgkmcnt(1)\n\t" \
    "v_mfma_f32_16x16x32_bf16 v[188:191], v[72:75], v[176:179], v[188:191]\n\t" \
    "s_waitcnt lgkmcnt(0)\n\t" \
    "v_mfma_f32_16x16x32_bf16 v[188:191], v[76:79], v[180:183], v[188:191]\n\t" \
    "v_mfma_f32_16x16x32_bf16 v[192:195], v[152:155], v[176:179], v[192:195]\n\t" \
    "v_mfma_f32_16x16x32_bf16 v[192:195], v[156:159], v[180:183], v[192:195]\n\t" \
    ASM_ST3 \
    "s_nop 1\n\t" \
    "v_fma_f32 v188, v188, v206, v208\n\t" \
    "v_fma_f32 v189, v189, v206, v209\n\t" \
    "v_fma_f32 v190, v190, v206, v210\n\t" \
    "v_fma_f32 v191, v191, v206, v211\n\t" \
    "v_mul_f32 v160, v188, v188\n\t" \
    "v_mul_f32 v161, v189, v189\n\t" \
    "v_mul_f32 v162, v190, v190\n\t" \
    "v_mul_f32 v163, v191, v191\n\t" \
    "v_fma_f32 v160, v160, %[k1], v220\n\t" \
    "v_fma_f32 v161, v161, %[k1], v220\n\t" \
    "v_fma_f32 v162, v162, %[k1], v220\n\t" \
    "v_fma_f32 v163, v163, %[k1], v220\n\t" \
    "v_mul_f32 v160, v188, v160\n\t" \
    "v_mul_f32 v161, v189, v161\n\t" \
    "v_mul_f32 v162, v190, v162\n\t" \
    "v_mul_f32 v163, v191, v163\n\t" \
    "v_fma_f32 v192, v192, v206, v212\n\t" \
    "v_fma_f32 v193, v193, v206, v213\n\t" \
    "v_fma_f32 v194, v194, v206, v214\n\t" \
    "v_fma_f32 v195, v195, v206, v215\n\t" \
    "v_mul_f32 v164, v192, v192\n\t" \
    "v_mul_f32 v165, v193, v193\n\t" \
    "v_mul_f32 v166, v194, v194\n\t" \
    "v_mul_f32 v167, v195, v195\n\t" \
    "v_fma_f32 v164, v164, %[k1], v220\n\t" \
    "v_fma_f32 v165, v165, %[k1], v220\n\t" \
    "v_fma_f32 v166, v166, %[k1], v220\n\t" \
    "v_fma_f32 v167, v167, %[k1], v220\n\t" \
    "v_mul_f32 v164, v192, v164\n\t" \
    "v_mul_f32 v165, v193, v165\n\t" \
    "v_mul_f32 v166, v194, v166\n\t" \
    "v_mul_f32 v167, v195, v167\n\t" \
    "v_exp_f32 v160, v160\n\t" \
    "v_exp_f32 v161, v161\n\t" \
    "v_exp_f32 v162, v162\n\t" \
    "v_exp_f32 v163, v163\n\t" \
    "v_exp_f32 v164, v164\n\t" \
    "v_exp_f32 v165, v165\n\t" \
    "v_exp_f32 v166, v166\n\t" \
    "v_exp_f32 v167, v167\n\t" \
    "v_add_f32 v160, 1.0, v160\n\t" \
    "v_add_f32 v161, 1.0, v161\n\t" \
    "v_add_f32 v162, 1.0, v162\n\t" \
    "v_add_f32 v163, 1.0, v163\n\t" \
    "v_add_f32 v164, 1.0, v164\n\t" \
    "v_add_f32 v165, 1.0, v165\n\t" \
    "v_add_f32 v166, 1.0, v166\n\t" \
    "v_add_f32 v167, 1.0, v167\n\t" \
    "v_rcp_f32 v160, v160\n\t" \
    "v_rcp_f32 v161, v161\n\t" \
    "v_rcp_f32 v162, v162\n\t" \
    "v_rcp_f32 v163, v163\n\t" \
    "v_rcp_f32 v164, v164\n\t" \
    "v_rcp_f32 v165, v165\n\t" \
    "v_rcp_f32 v166, v166\n\t" \
    "v_rcp_f32 v167, v167\n\t" \
    "v_mul_f32 v188, v188, v160\n\t" \
    "v_mul_f32 v189, v189, v161\n\t" \
    "v_mul_f32 v190, v190, v162\n\t" \
    "v_mul_f32 v191, v191, v163\n\t" \
    "v_mul_f32 v192, v192, v164\n\t" \
    "v_mul_f32 v193, v193, v165\n\t" \
    "v_mul_f32 v194, v194, v166\n\t" \
    "v_mul_f32 v195, v195, v167\n\t" \
    "v_cvt_pk_bf16_f32 v188, v188, v189\n\t" \
    "v_cvt_pk_bf16_f32 v189, v190, v191\n\t" \
    "v_cvt_pk_bf16_f32 v190, v192, v193\n\t" \
    "v_cvt_pk_bf16_f32 v191, v194, v195\n\t" \
    "v_min_u32 v188, -2, v188\n\t" \
    "v_min_u32 v189, -2, v189\n\t" \
    "v_min_u32 v190, -2, v190\n\t" \
    "v_min_u32 v191, -2, v191\n\t" \
    "s_nop 1\n\t" \
    "v_permlane16_swap_b32 v188, v190\n\t" \
    "v_permlane16_swap_b32 v189, v191\n\t" \
    "s_waitcnt vmcnt(1)\n\t" \
    "buffer_store_dwordx4 v[188:191], %[oa], %[rs], %[so] offen sc1\n\t" \
    "s_nop 1\n\t"
#define ASM_WT2_LO \
    "s_branch .Lt0%=\n\t" \
    ".Ltr%=:\n\t" \
    "s_cmp_gt_i32 %[n], 0\n\t" \
    "s_cbranch_scc0 .Ltg%=\n\t" \
    ".Lfw%=:\n\t" \
    "s_mov_b32 %[a], 0\n\t" \
    "v_max_u32 v252, v232, v233\n\t" \
    "v_max3_u32 v252, v234, v235, v252\n\t" \
    "v_cmp_eq_u32 vcc, -1, v252\n\t" \
    "s_cbranch_vccz .Lgw0_%=\n\t" \
    "buffer_load_dwordx4 v[232:235], %[l16], %[rs], %[i0] offen offset:0 sc1\n\t" \
    "s_mov_b32 %[a], 1\n\t" \
    ".Lgw0_%=:\n\t" \
    "v_max_u32 v252, v236, v237\n\t" \
    "v_max3_u32 v252, v238, v239, v252\n\t" \
    "v_cmp_eq_u32 vcc, -1, v252\n\t" \
    "s_cbranch_vccz .Lgw1_%=\n\t" \
    "buffer_load_dwordx4 v[236:239], %[l16], %[rs], %[i0] offen offset:1024 sc1\n\t" \
    "s_mov_b32 %[a], 1\n\t" \
    ".Lgw1_%=:\n\t" \
    "v_max_u32 v252, v240, v241\n\t" \
    "v_max3_u32 v252, v242, v243, v252\n\t" \
    "v_cmp_eq_u32 vcc, -1, v252\n\t" \
    "s_cbranch_vccz .Lgw2_%=\n\t" \
    "buffer_load_dwordx4 v[240:243], %[l16], %[rs], %[i0] offen offset:2048 sc1\n\t" \
    "s_mov_b32 %[a], 1\n\t" \
    ".Lgw2_%=:\n\t" \
    "v_max_u32 v252, v244, v245\n\t" \
    "v_max3_u32 v252, v246, v247, v252\n\t" \
    "v_cmp_eq_u32 vcc, -1, v252\n\t" \
    "s_cbranch_vccz .Lgw3_%=\n\t" \
    "buffer_load_dwordx4 v[244:247], %[l16], %[rs], %[i0] offen offset:3072 sc1\n\t" \
    "s_mov_b32 %[a], 1\n\t" \
    ".Lgw3_%=:\n\t" \
    "v_max_u32 v252, v248, v249\n\t" \
    "v_max3_u32 v252, v250, v251, v252\n\t" \
    "v_cmp_eq_u32 vcc, -1, v252\n\t" \
    "s_cbranch_vccz .Lgw4_%=\n\t" \
    "buffer_load_dwordx4 v[248:251], %[l16], %[rs], %[i1] offen offset:0 sc1\n\t" \
    "s_mov_b32 %[a], 1\n\t" \
    ".Lgw4_%=:\n\t" \
    "s_cmp_eq_u32 %[a], 0\n\t" \
    "s_cbranch_scc1 .Lhw%=\n\t" \
    "s_waitcnt vmcnt(20)\n\t" \
    ".Liw%=:\n\t" \
    "v_max_u32 v252, v232, v233\n\t" \
    "v_max3_u32 v252, v234, v235, v252\n\t" \
    "v_max3_u32 v252, v236, v237, v252\n\t" \
    "v_max3_u32 v252, v238, v239, v252\n\t" \
    "v_max3_u32 v252, v240, v241, v252\n\t" \
    "v_max3_u32 v252, v242, v243, v252\n\t" \
    "v_max3_u32 v252, v244, v245, v252\n\t" \
    "v_max3_u32 v252, v246, v247, v252\n\t" \
    "v_max3_u32 v252, v248, v249, v252\n\t" \
    "v_max3_u32 v252, v250, v251, v252\n\t" \
    "v_cmp_eq_u32 vcc, -1, v252\n\t" \
    "s_cbranch_vccz .Lhw%=\n\t" \
    "s_sleep 1\n\t" \
    "v_max_u32 v252, v232, v233\n\t" \
    "v_max3_u32 v252, v234, v235, v252\n\t" \
    "v_max3_u32 v252, v236, v237, v252\n\t" \
    "v_max3_u32 v252, v238, v239, v252\n\t" \
    "v_max3_u32 v252, v240, v241, v252\n\t" \
    "v_max3_u32 v252, v242, v243, v252\n\t" \
    "v_max3_u32 v252, v244, v245, v252\n\t" \
    "v_max3_u32 v252, v246, v247, v252\n\t" \
    "v_max3_u32 v252, v248, v249, v252\n\t" \
    "v_max3_u32 v252, v250, v251, v252\n\t" \
    "v_cmp_eq_u32 vcc, -1, v252\n\t" \
    "s_cbranch_vccz .Lhw%=\n\t" \
    "s_sleep 1\n\t" \
    "v_max_u32 v252, v232, v233\n\t" \
    "v_max3_u32 v252, v234, v235, v252\n\t" \
    "v_max3_u32 v252, v236, v237, v252\n\t" \
    "v_max3_u32 v252, v238, v239, v252\n\t" \
    "v_max3_u32 v252, v240, v241, v252\n\t" \
    "v_max3_u32 v252, v242, v243, v252\n\t" \
    "v_max3_u32 v252, v244, v245, v252\n\t" \
    "v_max3_u32 v252, v246, v247, v252\n\t" \
    "v_max3_u32 v252, v248, v249, v252\n\t" \
    "v_max3_u32 v252, v250, v251, v252\n\t" \
    "v_cmp_eq_u32 vcc, -1, v252\n\t" \
    "s_cbranch_vccz .Lhw%=\n\t" \
    "s_sleep 1\n\t" \
    "v_max_u32 v252, v232, v233\n\t" \
    "v_max3_u32 v252, v234, v235, v252\n\t" \
    "v_max3_u32 v252, v236, v237, v252\n\t" \
    "v_max3_u32 v252, v238, v239, v252\n\t" \
    "v_max3_u32 v252, v240, v241, v252\n\t" \
    "v_max3_u32 v252, v242, v243, v252\n\t" \
    "v_max3_u32 v252, v244, v245, v252\n\t" \
    "v_max3_u32 v252, v246, v247, v252\n\t" \
    "v_max3_u32 v252, v248, v249, v252\n\t" \
    "v_max3_u32 v252, v250, v251, v252\n\t" \
    "v_cmp_eq_u32 vcc, -1, v252\n\t" \
    "s_cbranch_vccz .Lhw%=\n\t" \
    "s_sleep 1\n\t" \
    "v_max_u32 v252, v232, v233\n\t" \
    "v_max3_u32 v252, v234, v235, v252\n\t" \
    "v_max3_u32 v252, v236, v237, v252\n\t" \
    "v_max3_u32 v252, v238, v239, v252\n\t" \
    "v_max3_u32 v252, v240, v241, v252\n\t" \
    "v_max3_u32 v252, v242, v243, v252\n\t" \
    "v_max3_u32 v252, v244, v245, v252\n\t" \
    "v_max3_u32 v252, v246, v247, v252\n\t" \
    "v_max3_u32 v252, v248, v249, v252\n\t" \
    "v_max3_u32 v252, v250, v251, v252\n\t" \
    "v_cmp_eq_u32 vcc, -1, v252\n\t" \
    "s_cbranch_vccz .Lhw%=\n\t" \
    "s_sleep 1\n\t" \
    "v_max_u32 v252, v232, v233\n\t" \
    "v_max3_u32 v252, v234, v235, v252\n\t" \
    "v_max3_u32 v252, v236, v237, v252\n\t" \
    "v_max3_u32 v252, v238, v239, v252\n\t" \
    "v_max3_u32 v252, v240, v241, v252\n\t" \
    "v_max3_u32 v252, v242, v243, v252\n\t" \
    "v_max3_u32 v252, v244, v245, v252\n\t" \
    "v_max3_u32 v252, v246, v247, v252\n\t" \
    "v_max3_u32 v252, v248, v249, v252\n\t" \
    "v_max3_u32 v252, v250, v251, v252\n\t" \
    "v_cmp_eq_u32 vcc, -1, v252\n\t" \
    "s_cbranch_vccz .Lhw%=\n\t" \
    "s_sleep 1\n\t" \
    "s_sub_u32 %[n], %[n], 1\n\t" \
    "s_cmp_gt_i32 %[n], 0\n\t" \
    "s_cbranch_scc1 .Lfw%=\n\t" \
    ".Lhw%=:\n\t" \
    "s_branch .Lta%=\n\t" \
    ".Lt0%=:\n\t" \
    "s_waitcnt vmcnt(1)\n\t" \
    ".Lta%=:\n\t" \
    "ds_write_b128 %[lw], v[232:235] offset:0\n\t" \
    "v_max_u32 v252, v232, v233\n\t" \
    "v_max3_u32 v252, v234, v235, v252\n\t" \
    "ds_write_b128 %[lw], v[236:239] offset:1024\n\t" \
    "v_max3_u32 v252, v236, v237, v252\n\t" \
    "v_max3_u32 v252, v238, v239, v252\n\t" \
    "ds_write_b128 %[lw], v[240:243] offset:2048\n\t" \
    "v_max3_u32 v252, v240, v241, v252\n\t" \
    "v_max3_u32 v252, v242, v243, v252\n\t" \
    "ds_write_b128 %[lw], v[244:247] offset:3072\n\t" \
    "v_max3_u32 v252, v244, v245, v252\n\t" \
    "v_max3_u32 v252, v246, v247, v252\n\t" \
    "ds_write_b128 %[lw], v[248:251] offset:4096\n\t" \
    "v_max3_u32 v252, v248, v249, v252\n\t" \
    "v_max3_u32 v252, v250, v251, v252\n\t" \
    "v_cmp_eq_u32 vcc, -1, v252\n\t" \
    "s_cbranch_vccnz .Ltr%=\n\t" \
    ".Ltg%=:\n\t" \
    ASM_ST2 \
    "s_waitcnt lgkmcnt(0)\n\t" \
    "s_setprio 2\n\t" \
    "s_barrier\n\t" ASM_ST \
    "ds_read_b128 v[160:163], %[la] offset:0\n\t" \
    "ds_read_b128 v[164:167], %[la] offset:1024\n\t" \
    "ds_read_b128 v[168:171], %[la] offset:2048\n\t" \
    "ds_read_b128 v[172:175], %[la] offset:3072\n\t" \
    "ds_read_b128 v[176:179], %[la] offset:8192\n\t" \
    "ds_read_b128 v[180:183], %[la] offset:9216\n\t" \
    "ds_read_b128 v[184:187], %[la] offset:10240\n\t" \
    "s_waitcnt lgkmcnt(6)\n\t" \
    "v_mfma_f32_16x16x32_bf16 v[192:195], v[80:83], v[160:163], 0\n\t" \
    "buffer_load_dwordx2 v[196:197], %[ro], %[rs], %[sx] offen sc1\n\t" \
    "s_waitcnt lgkmcnt(5)\n\t" \
    "v_mfma_f32_16x16x32_bf16 v[192:195], v[84:87], v[164:167], v[192:195]\n\t" \
    "buffer_load_dwordx2 v[198:199], %[ro], %[rs], %[sx] offen offset:512 sc1\n\t" \
    "v_mfma_f32_16x16x32_bf16 v[188:191], v[0:3], v[160:163], 0\n\t" \
    "ds_read_b128 v[160:163], %[la] offset:11264\n\t" \
    "v_lshrrev_b32 v204, 1, %[l16]\n\t" \
    "v_mfma_f32_16x16x32_bf16 v[188:191], v[4:7], v[164:167], v[188:191]\n\t" \
    "ds_read_b128 v[164:167], %[la] offset:16384\n\t" \
    "v_and_b32 v204, 0x78, v204\n\t" \
    "s_waitcnt lgkmcnt(6)\n\t" \
    "v_mfma_f32_16x16x32_bf16 v[188:191], v[8:11], v[168:171], v[188:191]\n\t" \
    "buffer_load_dwordx2 v[200:201], v204, %[rs], %[ss] offen sc1\n\t" \
    "s_waitcnt lgkmcnt(5)\n\t" \
    "v_mfma_f32_16x16x32_bf16 v[188:191], v[12:15], v[172:175], v[188:191]\n\t" \
    "v_mov_b64 v[216:217], -1\n\t" \
    "v_mfma_f32_16x16x32_bf16 v[192:195], v[88:91], v[168:171], v[192:195]\n\t" \
    "ds_read_b128 v[168:171], %[la] offset:17408\n\t" \
    "v_mov_b64 v[218:219], -1\n\t" \
    "v_mfma_f32_16x16x32_bf16 v[192:195], v[92:95], v[172:175], v[192:195]\n\t" \
    "ds_read_b128 v[172:175], %[la] offset:18432\n\t" \
    "buffer_store_dwordx4 v[216:219], %[l16], %[rs], %[sa] offen sc1\n\t" \
    "s_waitcnt lgkmcnt(6)\n\t" \
    "v_mfma_f32_16x16x32_bf16 v[192:195], v[96:99], v[176:179], v[192:195]\n\t" \
    "s_waitcnt lgkmcnt(5)\n\t" \
    "v_mfma_f32_16x16x32_bf16 v[192:195], v[100:103], v[180:183], v[192:195]\n\t" \
    "v_mfma_f32_16x16x32_bf16 v[188:191], v[16:19], v[176:179], v[188:191]\n\t" \
    "ds_read_b128 v[176:179], %[la] offset:19456\n\t" \
    "v_mfma_f32_16x16x32_bf16 v[188:191], v[20:23], v[180:183], v[188:191]\n\t" \
    "ds_read_b128 v[180:183], %[la] offset:24576\n\t" \
    "s_waitcnt lgkmcnt(6)\n\t" \
    "v_mfma_f32_16x16x32_bf16 v[188:191], v[24:27], v[184:187], v[188:191]\n\t" \
    "s_waitcnt lgkmcnt(5)\n\t" \
    "v_mfma_f32_16x16x32_bf16 v[188:191], v[28:31], v[160:163], v[188:191]\n\t" \
    "v_mfma_f32_16x16x32_bf16 v[192:195], v[104:107], v[184:187], v[192:195]\n\t" \
    "ds_read_b128 v[184:187], %[la] offset:25600\n\t" \
    "v_mfma_f32_16x16x32_bf16 v[192:195], v[108:111], v[160:163], v[192:195]\n\t" \
    "ds_read_b128 v[160:163], %[la] offset:26624\n\t" \
    "s_waitcnt lgkmcnt(6)\n\t" \
    "v_mfma_f32_16x16x32_bf16 v[192:195], v[112:115], v[164:167], v[192:195]\n\t" \
    "s_waitcnt lgkmcnt(5)\n\t" \
    "v_mfma_f32_16x16x32_bf16 v[192:195], v[116:119], v[168:171], v[192:195]\n\t" \
    "v_mfma_f32_16x16x32_bf16 v[188:191], v[32:35], v[164:167], v[188:191]\n\t" \
    "ds_read_b128 v[164:167], %[la] offset:27648\n\t" \
    "v_mfma_f32_16x16x32_bf16 v[188:191], v[36:39], v[168:171], v[188:191]\n\t" \
    "ds_read_b128 v[168:171], %[la] offset:4096\n\t" \
    "s_waitcnt lgkmcnt(6)\n\t" \
    "v_mfma_f32_16x16x32_bf16 v[188:191], v[40:43], v[172:175], v[188:191]\n\t" \
    "s_waitcnt lgkmcnt(5)\n\t" \
    "v_mfma_f32_16x16x32_bf16 v[188:191], v[44:47], v[176:179], v[188:191]\n\t" \
    "v_mfma_f32_16x16x32_bf16 v[192:195], v[120:123], v[172:175], v[192:195]\n\t" \
    "ds_read_b128 v[172:175], %[la] offset:12288\n\t" \
    "v_mfma_f32_16x16x32_bf16 v[192:195], v[124:127], v[176:179], v[192:195]\n\t" \
    "ds_read_b128 v[176:179], %[la] offset:20480\n\t" \
    "s_waitcnt lgkmcnt(6)\n\t" \
    "v_mfma_f32_16x16x32_bf16 v[192:195], v[128:131], v[180:183], v[192:195]\n\t" \
    "s_waitcnt lgkmcnt(5)\n\t" \
    "v_mfma_f32_16x16x32_bf16 v[192:195], v[132:135], v[184:187], v[192:195]\n\t" \
    "v_mfma_f32_16x16x32_bf16 v[188:191], v[48:51], v[180:183], v[188:191]\n\t" \
    "ds_read_b128 v[180:183], %[la] offset:28672\n\t" \
    "v_mfma_f32_16x16x32_bf16 v[188:191], v[52:55], v[184:187], v[188:191]\n\t" \
    "s_waitcnt lgkmcnt(5)\n\t" \
    "v_mfma_f32_16x16x32_bf16 v[188:191], v[56:59], v[160:163], v[188:191]\n\t" \
    "s_waitcnt lgkmcnt(4)\n\t" \
    "v_mfma_f32_16x16x32_bf16 v[188:191], v[60:63], v[164:167], v[188:191]\n\t" \
    "v_mfma_f32_16x16x32_bf16 v[192:195], v[136:139], v[160:163], v[192:195]\n\t" \
    "v_mfma_f32_16x16x32_bf16 v[192:195], v[140:143], v[164:167], v[192:195]\n\t" \
    "s_waitcnt lgkmcnt(3)\n\t" \
    "v_mfma_f32_16x16x32_bf16 v[192:195], v[144:147], v[168:171], v[192:195]\n\t" \
    "s_waitcnt lgkmcnt(2)\n\t" \
    "v_mfma_f32_16x16x32_bf16 v[192:195], v[148:151], v[172:175], v[192:195]\n\t" \
    "v_mfma_f32_16x16x32_bf16 v[188:191], v[64:67], v[168:171], v[188:191]\n\t" \
    "v_mfma_f32_16x16x32_bf16 v[188:191], v[68:71], v[172:175], v[188:191]\n\t" \
    "s_waitcnt lgkmcnt(1)\n\t" \
    "v_mfma_f32_16x16x32_bf16 v[188:191], v[72:75], v[176:179], v[188:191]\n\t" \
    "s_waitcnt lgkmcnt(0)\n\t" \
    "v_mfma_f32_16x16x32_bf16 v[188:191], v[76:79], v[180:183], v[188:191]\n\t" \
    "v_mfma_f32_16x16x32_bf16 v[192:195], v[152:155], v[176:179], v[192:195]\n\t" \
    "v_mfma_f32_16x16x32_bf16 v[192:195], v[156:159], v[180:183], v[192:195]\n\t" \
    "s_setprio 2\n\t" \
    "buffer_load_dwordx4 v[232:235], %[l16], %[rs], %[n0] offen offset:0 sc1\n\t" \
    "buffer_load_dwordx4 v[236:239], %[l16], %[rs], %[n0] offen offset:1024 sc1\n\t" \
    "buffer_load_dwordx4 v[240:243], %[l16], %[rs], %[n0] offen offset:2048 sc1\n\t" \
    "buffer_load_dwordx4 v[244:247], %[l16], %[rs], %[n0] offen offset:3072 sc1\n\t" \
    "buffer_load_dwordx4 v[248:251], %[l16], %[rs], %[n1] offen offset:0 sc1\n\t" \
    ASM_ST3 \
    "s_waitcnt vmcnt(6)\n\t" \
    "v_lshlrev_b32 v160, 16, v196\n\t" \
    "v_and_b32 v161, 0xffff0000, v196\n\t" \
    "v_lshlrev_b32 v162, 16, v197\n\t" \
    "v_and_b32 v163, 0xffff0000, v197\n\t" \
    "v_lshlrev_b32 v164, 16, v198\n\t" \
    "v_and_b32 v165, 0xffff0000, v198\n\t" \
    "v_lshlrev_b32 v166, 16, v199\n\t" \
    "v_and_b32 v167, 0xffff0000, v199\n\t" \
    "v_sub_f32 v160, v160, v200\n\t" \
    "v_sub_f32 v161, v161, v200\n\t" \
    "v_sub_f32 v162, v162, v200\n\t" \
    "v_sub_f32 v163, v163, v200\n\t" \
    "v_sub_f32 v164, v164, v200\n\t" \
    "v_sub_f32 v165, v165, v200\n\t" \
    "v_sub_f32 v166, v166, v200\n\t" \
    "v_sub_f32 v167, v167, v200\n\t" \
    "v_mul_f32 v160, v160, v201\n\t" \
    "v_mul_f32 v161, v161, v201\n\t" \
    "v_mul_f32 v162, v162, v201\n\t" \
    "v_mul_f32 v163, v163, v201\n\t" \
    "v_mul_f32 v164, v164, v201\n\t" \
    "v_mul_f32 v165, v165, v201\n\t" \
    "v_mul_f32 v166, v166, v201\n\t" \
    "v_mul_f32 v167, v167, v201\n\t" \
    "v_cvt_pk_bf16_f32 v160, v160, v161\n\t" \
    "v_cvt_pk_bf16_f32 v162, v162, v163\n\t" \
    "v_cvt_pk_bf16_f32 v164, v164, v165\n\t" \
    "v_cvt_pk_bf16_f32 v166, v166, v167\n\t" \
    "v_and_b32 v161, 0xffff0000, v160\n\t" \
    "v_lshlrev_b32 v160, 16, v160\n\t" \
    "v_and_b32 v163, 0xffff0000, v162\n\t" \
    "v_lshlrev_b32 v162, 16, v162\n\t" \
    "v_and_b32 v165, 0xffff0000, v164\n\t" \
    "v_lshlrev_b32 v164, 16, v164\n\t" \
    "v_and_b32 v167, 0xffff0000, v166\n\t" \
    "v_lshlrev_b32 v166, 16, v166\n\t" \
    "v_add_f32 v188, v188, v160\n\t" \
    "v_add_f32 v189, v189, v161\n\t" \
    "v_add_f32 v190, v190, v162\n\t" \
    "v_add_f32 v191, v191, v163\n\t" \
    "v_add_f32 v192, v192, v164\n\t" \
    "v_add_f32 v193, v193, v165\n\t" \
    "v_add_f32 v194, v194, v166\n\t" \
    "v_add_f32 v195, v195, v167\n\t" \
    "v_cvt_pk_bf16_f32 v188, v188, v189\n\t" \
    "v_cvt_pk_bf16_f32 v189, v190, v191\n\t" \
    "v_cvt_pk_bf16_f32 v190, v192, v193\n\t" \
    "v_cvt_pk_bf16_f32 v191, v194, v195\n\t" \
    "v_min_u32 v188, -2, v188\n\t" \
    "v_min_u32 v189, -2, v189\n\t" \
    "v_min_u32 v190, -2, v190\n\t" \
    "v_min_u32 v191, -2, v191\n\t" \
    "s_nop 1\n\t" \
    "v_permlane16_swap_b32 v188, v190\n\t" \
    "v_permlane16_swap_b32 v189, v191\n\t" \
    "s_waitcnt vmcnt(6)\n\t" \
    "buffer_store_dwordx4 v[188:191], %[oa], %[rs], %[so] offen sc1\n\t" \
    "s_nop 1\n\t" \
    "s_setprio 0\n\t"
#define ASM_WT2_HI \
    "s_setprio 1\n\t" \
    "s_barrier\n\t" ASM_ST \
    "ds_read_b128 v[160:163], %[la] offset:0\n\t" \
    "ds_read_b128 v[164:167], %[la] offset:1024\n\t" \
    "ds_read_b128 v[168:171], %[la] offset:2048\n\t" \
    "ds_read_b128 v[172:175], %[la] offset:3072\n\t" \
    "ds_read_b128 v[176:179], %[la] offset:8192\n\t" \
    "ds_read_b128 v[180:183], %[la] offset:9216\n\t" \
    "ds_read_b128 v[184:187], %[la] offset:10240\n\t" \
    "s_waitcnt lgkmcnt(6)\n\t" \
    "v_mfma_f32_16x16x32_bf16 v[192:195], v[80:83], v[160:163], 0\n\t" \
    "buffer_load_dwordx2 v[196:197], %[ro], %[rs], %[sx] offen sc1\n\t" \
    "s_waitcnt lgkmcnt(5)\n\t" \
    "v_mfma_f32_16x16x32_bf16 v[192:195], v[84:87], v[164:167], v[192:195]\n\t" \
    "buffer_load_dwordx2 v[198:199], %[ro], %[rs], %[sx] offen offset:512 sc1\n\t" \
    "v_mfma_f32_16x16x32_bf16 v[188:191], v[0:3], v[160:163], 0\n\t" \
    "ds_read_b128 v[160:163], %[la] offset:11264\n\t" \
    "v_lshrrev_b32 v204, 1, %[l16]\n\t" \
    "v_mfma_f32_16x16x32_bf16 v[188:191], v[4:7], v[164:167], v[188:191]\n\t" \
    "ds_read_b128 v[164:167], %[la] offset:16384\n\t" \
    "v_and_b32 v204, 0x78, v204\n\t" \
    "s_waitcnt lgkmcnt(6)\n\t" \
    "v_mfma_f32_16x16x32_bf16 v[188:191], v[8:11], v[168:171], v[188:191]\n\t" \
    "buffer_load_dwordx2 v[200:201], v204, %[rs], %[ss] offen sc1\n\t" \
    "s_waitcnt lgkmcnt(5)\n\t" \
    "v_mfma_f32_16x16x32_bf16 v[188:191], v[12:15], v[172:175], v[188:191]\n\t" \
    "v_mov_b64 v[216:217], -1\n\t" \
    "v_mfma_f32_16x16x32_bf16 v[192:195], v[88:91], v[168:171], v[192:195]\n\t" \
    "ds_read_b128 v[168:171], %[la] offset:17408\n\t" \
    "v_mov_b64 v[218:219], -1\n\t" \
    "v_mfma_f32_16x16x32_bf16 v[192:195], v[92:95], v[172:175], v[192:195]\n\t" \
    "ds_read_b128 v[172:175], %[la] offset:18432\n\t" \
    "buffer_store_dwordx4 v[216:219], %[l16], %[rs], %[sa] offen sc1\n\t" \
    "s_waitcnt lgkmcnt(6)\n\t" \
    "v_mfma_f32_16x16x32_bf16 v[192:195], v[96:99], v[176:179], v[192:195]\n\t" \
    "s_waitcnt lgkmcnt(5)\n\t" \
    "v_mfma_f32_16x16x32_bf16 v[192:195], v[100:103], v[180:183], v[192:195]\n\t" \
    "v_mfma_f32_16x16x32_bf16 v[188:191], v[16:19], v[176:179], v[188:191]\n\t" \
    "ds_read_b128 v[176:179], %[la] offset:19456\n\t" \
    "v_mfma_f32_16x16x32_bf16 v[188:191], v[20:23], v[180:183], v[188:191]\n\t" \
    "ds_read_b128 v[180:183], %[la] offset:24576\n\t" \
    "s_waitcnt lgkmcnt(6)\n\t" \
    "v_mfma_f32_16x16x32_bf16 v[188:191], v[24:27], v[184:187], v[188:191]\n\t" \
    "s_waitcnt lgkmcnt(5)\n\t" \
    "v_mfma_f32_16x16x32_bf16 v[188:191], v[28:31], v[160:163], v[188:191]\n\t" \
    "v_mfma_f32_16x16x32_bf16 v[192:195], v[104:107], v[184:187], v[192:195]\n\t" \
    "ds_read_b128 v[184:187], %[la] offset:25600\n\t" \
    "v_mfma_f32_16x16x32_bf16 v[192:195], v[108:111], v[160:163], v[192:195]\n\t" \
    "ds_read_b128 v[160:163], %[la] offset:26624\n\t" \
    "s_waitcnt lgkmcnt(6)\n\t" \
    "v_mfma_f32_16x16x32_bf16 v[192:195], v[112:115], v[164:167], v[192:195]\n\t" \
    "s_waitcnt lgkmcnt(5)\n\t" \
    "v_mfma_f32_16x16x32_bf16 v[192:195], v[116:119], v[168:171], v[192:195]\n\t" \
    "v_mfma_f32_16x16x32_bf16 v[188:191], v[32:35], v[164:167], v[188:191]\n\t" \
    "ds_read_b128 v[164:167], %[la] offset:27648\n\t" \
    "v_mfma_f32_16x16x32_bf16 v[188:191], v[36:39], v[168:171], v[188:191]\n\t" \
    "ds_read_b128 v[168:171], %[la] offset:4096\n\t" \
    "s_waitcnt lgkmcnt(6)\n\t" \
    "v_mfma_f32_16x16x32_bf16 v[188:191], v[40:43], v[172:175], v[188:191]\n\t" \
    "s_waitcnt lgkmcnt(5)\n\t" \
    "v_mfma_f32_16x16x32_bf16 v[188:191], v[44:47], v[176:179], v[188:191]\n\t" \
    "v_mfma_f32_16x16x32_bf16 v[192:195], v[120:123], v[172:175], v[192:195]\n\t" \
    "ds_read_b128 v[172:175], %[la] offset:12288\n\t" \
    "v_mfma_f32_16x16x32_bf16 v[192:195], v[124:127], v[176:179], v[192:195]\n\t" \
    "ds_read_b128 v[176:179], %[la] offset:20480\n\t" \
    "s_waitcnt lgkmcnt(6)\n\t" \
    "v_mfma_f32_16x16x32_bf16 v[192:195], v[128:131], v[180:183], v[192:195]\n\t" \
    "s_waitcnt lgkmcnt(5)\n\t" \
    "v_mfma_f32_16x16x32_bf16 v[192:195], v[132:135], v[184:187], v[192:195]\n\t" \
    "v_mfma_f32_16x16x32_bf16 v[188:191], v[48:51], v[180:183], v[188:191]\n\t" \
    "ds_read_b128 v[180:183], %[la] offset:28672\n\t" \
    "v_mfma_f32_16x16x32_bf16 v[188:191], v[52:55], v[184:187], v[188:191]\n\t" \
    "s_waitcnt lgkmcnt(5)\n\t" \
    "v_mfma_f32_16x16x32_bf16 v[188:191], v[56:59], v[160:163], v[188:191]\n\t" \
    "s_waitcnt lgkmcnt(4)\n\t" \
    "v_mfma_f32_16x16x32_bf16 v[188:191], v[60:63], v[164:167], v[188:191]\n\t" \
    "v_mfma_f32_16x16x32_bf16 v[192:195], v[136:139], v[160:163], v[192:195]\n\t" \
    "v_mfma_f32_16x16x32_bf16 v[192:195], v[140:143], v[164:167], v[192:195]\n\t" \
    "s_waitcnt lgkmcnt(3)\n\t" \
    "v_mfma_f32_16x16x32_bf16 v[192:195], v[144:147], v[168:171], v[192:195]\n\t" \
    "s_waitcnt lgkmcnt(2)\n\t" \
    "v_mfma_f32_16x16x32_bf16 v[192:195], v[148:151], v[172:175], v[192:195]\n\t" \
    "v_mfma_f32_16x16x32_bf16 v[188:191], v[64:67], v[168:171], v[188:191]\n\t" \
    "v_mfma_f32_16x16x32_bf16 v[188:191], v[68:71], v[172:175], v[188:191]\n\t" \
    "s_waitcnt lgkmcnt(1)\n\t" \
    "v_mfma_f32_16x16x32_bf16 v[188:191], v[72:75], v[176:179], v[188:191]\n\t" \
    "s_waitcnt lgkmcnt(0)\n\t" \
    "v_mfma_f32_16x16x32_bf16 v[188:191], v[76:79], v[180:183], v[188:191]\n\t" \
    "v_mfma_f32_16x16x32_bf16 v[192:195], v[152:155], v[176:179], v[192:195]\n\t" \
    "v_mfma_f32_16x16x32_bf16 v[192:195], v[156:159], v[180:183], v[192:195]\n\t" \
    ASM_ST3 \
    "s_waitcnt vmcnt(1)\n\t" \
    "v_lshlrev_b32 v160, 16, v196\n\t" \
    "v_and_b32 v161, 0xffff0000, v196\n\t" \
    "v_lshlrev_b32 v162, 16, v197\n\t" \
    "v_and_b32 v163, 0xffff0000, v197\n\t" \
    "v_lshlrev_b32 v164, 16, v198\n\t" \
    "v_and_b32 v165, 0xffff0000, v198\n\t" \
    "v_lshlrev_b32 v166, 16, v199\n\t" \
    "v_and_b32 v167, 0xffff0000, v199\n\t" \
    "v_sub_f32 v160, v160, v200\n\t" \
    "v_sub_f32 v161, v161, v200\n\t" \
    "v_sub_f32 v162, v162, v200\n\t" \
    "v_sub_f32 v163, v163, v200\n\t" \
    "v_sub_f32 v164, v164, v200\n\t" \
    "v_sub_f32 v165, v165, v200\n\t" \
    "v_sub_f32 v166, v166, v200\n\t" \
    "v_sub_f32 v167, v167, v200\n\t" \
    "v_mul_f32 v160, v160, v201\n\t" \
    "v_mul_f32 v161, v161, v201\n\t" \
    "v_mul_f32 v162, v162, v201\n\t" \
    "v_mul_f32 v163, v163, v201\n\t" \
    "v_mul_f32 v164, v164, v201\n\t" \
    "v_mul_f32 v165, v165, v201\n\t" \
    "v_mul_f32 v166, v166, v201\n\t" \
    "v_mul_f32 v167, v167, v201\n\t" \
    "v_cvt_pk_bf16_f32 v160, v160, v161\n\t" \
    "v_cvt_pk_bf16_f32 v162, v162, v163\n\t" \
    "v_cvt_pk_bf16_f32 v164, v164, v165\n\t" \
    "v_cvt_pk_bf16_f32 v166, v166, v167\n\t" \
    "v_and_b32 v161, 0xffff0000, v160\n\t" \
    "v_lshlrev_b32 v160, 16, v160\n\t" \
    "v_and_b32 v163, 0xffff0000, v162\n\t" \
    "v_lshlrev_b32 v162, 16, v162\n\t" \
    "v_and_b32 v165, 0xffff0000, v164\n\t" \
    "v_lshlrev_b32 v164, 16, v164\n\t" \
    "v_and_b32 v167, 0xffff0000, v166\n\t" \
    "v_lshlrev_b32 v166, 16, v166\n\t" \
    "v_add_f32 v188, v188, v160\n\t" \
    "v_add_f32 v189, v189, v161\n\t" \
    "v_add_f32 v190, v190, v162\n\t" \
    "v_add_f32 v191, v191, v163\n\t" \
    "v_add_f32 v192, v192, v164\n\t" \
    "v_add_f32 v193, v193, v165\n\t" \
    "v_add_f32 v194, v194, v166\n\t" \
    "v_add_f32 v195, v195, v167\n\t" \
    "v_cvt_pk_bf16_f32 v188, v188, v189\n\t" \
    "v_cvt_pk_bf16_f32 v189, v190, v191\n\t" \
    "v_cvt_pk_bf16_f32 v190, v192, v193\n\t" \
    "v_cvt_pk_bf16_f32 v191, v194, v195\n\t" \
    "v_min_u32 v188, -2, v188\n\t" \
    "v_min_u32 v189, -2, v189\n\t" \
    "v_min_u32 v190, -2, v190\n\t" \
    "v_min_u32 v191, -2, v191\n\t" \
    "s_nop 1\n\t" \
    "v_permlane16_swap_b32 v188, v190\n\t" \
    "v_permlane16_swap_b32 v189, v191\n\t" \
    "s_waitcnt vmcnt(1)\n\t" \
    "buffer_store_dwordx4 v[188:191], %[oa], %[rs], %[so] offen sc1\n\t" \
    "s_nop 1\n\t"
#define ASM_WT3_LO \
    "s_branch .Lt0%=\n\t" \
    ".Ltr%=:\n\t" \
    "s_cmp_gt_i32 %[n], 0\n\t" \
    "s_cbranch_scc0 .Ltg%=\n\t" \
    ".Lfw%=:\n\t" \
    "s_mov_b32 %[a], 0\n\t" \
    "v_max_u32 v252, v232, v233\n\t" \
    "v_max3_u32 v252, v234, v235, v252\n\t" \
    "v_cmp_eq_u32 vcc, -1, v252\n\t" \
    "s_cbranch_vccz .Lgw0_%=\n\t" \
    "buffer_load_dwordx4 v[232:235], %[l16], %[rs], %[i0] offen offset:0 sc1\n\t" \
    "s_mov_b32 %[a], 1\n\t" \
    ".Lgw0_%=:\n\t" \
    "v_max_u32 v252, v236, v237\n\t" \
    "v_max3_u32 v252, v238, v239, v252\n\t" \
    "v_cmp_eq_u32 vcc, -1, v252\n\t" \
    "s_cbranch_vccz .Lgw1_%=\n\t" \
    "buffer_load_dwordx4 v[236:239], %[l16], %[rs], %[i0] offen offset:1024 sc1\n\t" \
    "s_mov_b32 %[a], 1\n\t" \
    ".Lgw1_%=:\n\t" \
    "v_max_u32 v252, v240, v241\n\t" \
    "v_max3_u32 v252, v242, v243, v252\n\t" \
    "v_cmp_eq_u32 vcc, -1, v252\n\t" \
    "s_cbranch_vccz .Lgw2_%=\n\t" \
    "buffer_load_dwordx4 v[240:243], %[l16], %[rs], %[i0] offen offset:2048 sc1\n\t" \
    "s_mov_b32 %[a], 1\n\t" \
    ".Lgw2_%=:\n\t" \
    "v_max_u32 v252, v244, v245\n\t" \
    "v_max3_u32 v252, v246, v247, v252\n\t" \
    "v_cmp_eq_u32 vcc, -1, v252\n\t" \
    "s_cbranch_vccz .Lgw3_%=\n\t" \
    "buffer_load_dwordx4 v[244:247], %[l16], %[rs], %[i0] offen offset:3072 sc1\n\t" \
    "s_mov_b32 %[a], 1\n\t" \
    ".Lgw3_%=:\n\t" \
    "v_max_u32 v252, v248, v249\n\t" \
    "v_max3_u32 v252, v250, v251, v252\n\t" \
    "v_cmp_eq_u32 vcc, -1, v252\n\t" \
    "s_cbranch_vccz .Lgw4_%=\n\t" \
    "buffer_load_dwordx4 v[248:251], %[l16], %[rs], %[i1] offen offset:0 sc1\n\t" \
    "s_mov_b32 %[a], 1\n\t" \
    ".Lgw4_%=:\n\t" \
    "s_cmp_eq_u32 %[a], 0\n\t" \
    "s_cbranch_scc1 .Lhw%=\n\t" \
    "s_waitcnt vmcnt(20)\n\t" \
    ".Liw%=:\n\t" \
    "v_max_u32 v252, v232, v233\n\t" \
    "v_max3_u32 v252, v234, v235, v252\n\t" \
    "v_max3_u32 v252, v236, v237, v252\n\t" \
    "v_max3_u32 v252, v238, v239, v252\n\t" \
    "v_max3_u32 v252, v240, v241, v252\n\t" \
    "v_max3_u32 v252, v242, v243, v252\n\t" \
    "v_max3_u32 v252, v244, v245, v252\n\t" \
    "v_max3_u32 v252, v246, v247, v252\n\t" \
    "v_max3_u32 v252, v248, v249, v252\n\t" \
    "v_max3_u32 v252, v250, v251, v252\n\t" \
    "v_cmp_eq_u32 vcc, -1, v252\n\t" \
    "s_cbranch_vccz .Lhw%=\n\t" \
    "s_sleep 1\n\t" \
    "v_max_u32 v252, v232, v233\n\t" \
    "v_max3_u32 v252, v234, v235, v252\n\t" \
    "v_max3_u32 v252, v236, v237, v252\n\t" \
    "v_max3_u32 v252, v238, v239, v252\n\t" \
    "v_max3_u32 v252, v240, v241, v252\n\t" \
    "v_max3_u32 v252, v242, v243, v252\n\t" \
    "v_max3_u32 v252, v244, v245, v252\n\t" \
    "v_max3_u32 v252, v246, v247, v252\n\t" \
    "v_max3_u32 v252, v248, v249, v252\n\t" \
    "v_max3_u32 v252, v250, v251, v252\n\t" \
    "v_cmp_eq_u32 vcc, -1, v252\n\t" \
    "s_cbranch_vccz .Lhw%=\n\t" \
    "s_sleep 1\n\t" \
    "v_max_u32 v252, v232, v233\n\t" \
    "v_max3_u32 v252, v234, v235, v252\n\t" \
    "v_max3_u32 v252, v236, v237, v252\n\t" \
    "v_max3_u32 v252, v238, v239, v252\n\t" \
    "v_max3_u32 v252, v240, v241, v252\n\t" \
    "v_max3_u32 v252, v242, v243, v252\n\t" \
    "v_max3_u32 v252, v244, v245, v252\n\t" \
    "v_max3_u32 v252, v246, v247, v252\n\t" \
    "v_max3_u32 v252, v248, v249, v252\n\t" \
    "v_max3_u32 v252, v250, v251, v252\n\t" \
    "v_cmp_eq_u32 vcc, -1, v252\n\t" \
    "s_cbranch_vccz .Lhw%=\n\t" \
    "s_sleep 1\n\t" \
    "v_max_u32 v252, v232, v233\n\t" \
    "v_max3_u32 v252, v234, v235, v252\n\t" \
    "v_max3_u32 v252, v236, v237, v252\n\t" \
    "v_max3_u32 v252, v238, v239, v252\n\t" \
    "v_max3_u32 v252, v240, v241, v252\n\t" \
    "v_max3_u32 v252, v242, v243, v252\n\t" \
    "v_max3_u32 v252, v244, v245, v252\n\t" \
    "v_max3_u32 v252, v246, v247, v252\n\t" \
    "v_max3_u32 v252, v248, v249, v252\n\t" \
    "v_max3_u32 v252, v250, v251, v252\n\t" \
    "v_cmp_eq_u32 vcc, -1, v252\n\t" \
    "s_cbranch_vccz .Lhw%=\n\t" \
    "s_sleep 1\n\t" \
    "v_max_u32 v252, v232, v233\n\t" \
    "v_max3_u32 v252, v234, v235, v252\n\t" \
    "v_max3_u32 v252, v236, v237, v252\n\t" \
    "v_max3_u32 v252, v238, v239, v252\n\t" \
    "v_max3_u32 v252, v240, v241, v252\n\t" \
    "v_max3_u32 v252, v242, v243, v252\n\t" \
    "v_max3_u32 v252, v244, v245, v252\n\t" \
    "v_max3_u32 v252, v246, v247, v252\n\t" \
    "v_max3_u32 v252, v248, v249, v252\n\t" \
    "v_max3_u32 v252, v250, v251, v252\n\t" \
    "v_cmp_eq_u32 vcc, -1, v252\n\t" \
    "s_cbranch_vccz .Lhw%=\n\t" \
    "s_sleep 1\n\t" \
    "v_max_u32 v252, v232, v233\n\t" \
    "v_max3_u32 v252, v234, v235, v252\n\t" \
    "v_max3_u32 v252, v236, v237, v252\n\t" \
    "v_max3_u32 v252, v238, v239, v252\n\t" \
    "v_max3_u32 v252, v240, v241, v252\n\t" \
    "v_max3_u32 v252, v242, v243, v252\n\t" \
    "v_max3_u32 v252, v244, v245, v252\n\t" \
    "v_max3_u32 v252, v246, v247, v252\n\t" \
    "v_max3_u32 v252, v248, v249, v252\n\t" \
    "v_max3_u32 v252, v250, v251, v252\n\t" \
    "v_cmp_eq_u32 vcc, -1, v252\n\t" \
    "s_cbranch_vccz .Lhw%=\n\t" \
    "s_sleep 1\n\t" \
    "s_sub_u32 %[n], %[n], 1\n\t" \
    "s_cmp_gt_i32 %[n], 0\n\t" \
    "s_cbranch_scc1 .Lfw%=\n\t" \
    ".Lhw%=:\n\t" \
    "s_branch .Lta%=\n\t" \
    ".Lt0%=:\n\t" \
    "s_waitcnt vmcnt(1)\n\t" \
    ".Lta%=:\n\t" \
    "ds_write_b128 %[lw], v[232:235] offset:0\n\t" \
    "v_max_u32 v252, v232, v233\n\t" \
    "v_max3_u32 v252, v234, v235, v252\n\t" \
    "v_mfma_f32_16x16x32_bf16 v[188:191], v[232:235], v[232:235], 0\n\t" \
    "ds_write_b128 %[lw], v[236:239] offset:1024\n\t" \
    "v_max3_u32 v252, v236, v237, v252\n\t" \
    "v_max3_u32 v252, v238, v239, v252\n\t" \
    "v_mfma_f32_16x16x32_bf16 v[188:191], v[236:239], v[236:239], v[188:191]\n\t" \
    "ds_write_b128 %[lw], v[240:243] offset:2048\n\t" \
    "v_max3_u32 v252, v240, v241, v252\n\t" \
    "v_max3_u32 v252, v242, v243, v252\n\t" \
    "v_mfma_f32_16x16x32_bf16 v[188:191], v[240:243], v[240:243], v[188:191]\n\t" \
    "ds_write_b128 %[lw], v[244:247] offset:3072\n\t" \
    "v_max3_u32 v252, v244, v245, v252\n\t" \
    "v_max3_u32 v252, v246, v247, v252\n\t" \
    "v_mfma_f32_16x16x32_bf16 v[188:191], v[244:247], v[244:247], v[188:191]\n\t" \
    "ds_write_b128 %[lw], v[248:251] offset:4096\n\t" \
    "v_max3_u32 v252, v248, v249, v252\n\t" \
    "v_max3_u32 v252, v250, v251, v252\n\t" \
    "v_mfma_f32_16x16x32_bf16 v[188:191], v[248:251], v[248:251], v[188:191]\n\t" \
    "v_mfma_f32_16x16x32_bf16 v[192:195], v[216:219], v[232:235], 0\n\t" \
    "v_mfma_f32_16x16x32_bf16 v[192:195], v[216:219], v[236:239], v[192:195]\n\t" \
    "v_mfma_f32_16x16x32_bf16 v[192:195], v[216:219], v[240:243], v[192:195]\n\t" \
    "v_mfma_f32_16x16x32_bf16 v[192:195], v[216:219], v[244:247], v[192:195]\n\t" \
    "v_mfma_f32_16x16x32_bf16 v[192:195], v[216:219], v[248:251], v[192:195]\n\t" \
    "v_cmp_eq_u32 vcc, -1, v252\n\t" \
    "s_cbranch_vccnz .Ltr%=\n\t" \
    ".Ltg%=:\n\t" \
    ASM_ST2 \
    "s_nop 5\n\t" \
    "ds_write_b128 %[lw], v[188:191] offset:5120\n\t" \
    "ds_write_b128 %[lw], v[192:195] offset:6144\n\t" \
    "s_waitcnt lgkmcnt(0)\n\t" \
    "s_setprio 2\n\t" \
    "s_barrier\n\t" ASM_ST \
    "ds_read_b128 v[160:163], %[la] offset:0\n\t" \
    "ds_read_b128 v[164:167], %[la] offset:1024\n\t" \
    "ds_read_b128 v[168:171], %[la] offset:2048\n\t" \
    "ds_read2st64_b32 v[204:205], %[sr] offset0:20 offset1:24\n\t" \
    "ds_read_b128 v[172:175], %[la] offset:3072\n\t" \
    "ds_read_b128 v[176:179], %[la] offset:8192\n\t" \
    "ds_read_b128 v[180:183], %[la] offset:9216\n\t" \
    "ds_read_b128 v[184:187], %[la] offset:10240\n\t" \
    "s_waitcnt lgkmcnt(7)\n\t" \
    "v_mfma_f32_16x16x32_bf16 v[192:195], v[80:83], v[160:163], 0\n\t" \
    "s_waitcnt lgkmcnt(6)\n\t" \
    "v_mfma_f32_16x16x32_bf16 v[192:195], v[84:87], v[164:167], v[192:195]\n\t" \
    "v_mfma_f32_16x16x32_bf16 v[188:191], v[0:3], v[160:163], 0\n\t" \
    "ds_read_b128 v[160:163], %[la] offset:11264\n\t" \
    "v_mfma_f32_16x16x32_bf16 v[188:191], v[4:7], v[164:167], v[188:191]\n\t" \
    "ds_read_b128 v[164:167], %[la] offset:16384\n\t" \
    "s_waitcnt lgkmcnt(7)\n\t" \
    "v_mfma_f32_16x16x32_bf16 v[188:191], v[8:11], v[168:171], v[188:191]\n\t" \
    "s_waitcnt lgkmcnt(6)\n\t" \
    "s_waitcnt lgkmcnt(5)\n\t" \
    "v_mfma_f32_16x16x32_bf16 v[188:191], v[12:15], v[172:175], v[188:191]\n\t" \
    "v_permlane16_swap_b32 v204, v205\n\t" \
    "v_mfma_f32_16x16x32_bf16 v[192:195], v[88:91], v[168:171], v[192:195]\n\t" \
    "ds_read_b128 v[168:171], %[la] offset:17408\n\t" \
    "v_mfma_f32_16x16x32_bf16 v[192:195], v[92:95], v[172:175], v[192:195]\n\t" \
    "ds_read_b128 v[172:175], %[la] offset:18432\n\t" \
    "v_add_f32 v206, v204, v205\n\t" \
    "s_waitcnt lgkmcnt(6)\n\t" \
    "v_mfma_f32_16x16x32_bf16 v[192:195], v[96:99], v[176:179], v[192:195]\n\t" \
    "v_add_f32 v207, v204, v205\n\t" \
    "s_waitcnt lgkmcnt(5)\n\t" \
    "v_mfma_f32_16x16x32_bf16 v[192:195], v[100:103], v[180:183], v[192:195]\n\t" \
    "v_mfma_f32_16x16x32_bf16 v[188:191], v[16:19], v[176:179], v[188:191]\n\t" \
    "ds_read_b128 v[176:179], %[la] offset:19456\n\t" \
    "v_permlane32_swap_b32 v206, v207\n\t" \
    "v_mfma_f32_16x16x32_bf16 v[188:191], v[20:23], v[180:183], v[188:191]\n\t" \
    "ds_read_b128 v[180:183], %[la] offset:24576\n\t" \
    "s_waitcnt lgkmcnt(6)\n\t" \
    "v_mfma_f32_16x16x32_bf16 v[188:191], v[24:27], v[184:187], v[188:191]\n\t" \
    "v_add_f32 v204, v206, v207\n\t" \
    "s_waitcnt lgkmcnt(5)\n\t" \
    "v_mfma_f32_16x16x32_bf16 v[188:191], v[28:31], v[160:163], v[188:191]\n\t" \
    "v_add_f32 v205, v206, v207\n\t" \
    "v_mfma_f32_16x16x32_bf16 v[192:195], v[104:107], v[184:187], v[192:195]\n\t" \
    "ds_read_b128 v[184:187], %[la] offset:25600\n\t" \
    "v_mfma_f32_16x16x32_bf16 v[192:195], v[108:111], v[160:163], v[192:195]\n\t" \
    "ds_read_b128 v[160:163], %[la] offset:26624\n\t" \
    "v_permlane16_swap_b32 v204, v205\n\t" \
    "s_waitcnt lgkmcnt(6)\n\t" \
    "v_mfma_f32_16x16x32_bf16 v[192:195], v[112:115], v[164:167], v[192:195]\n\t" \
    "s_waitcnt lgkmcnt(5)\n\t" \
    "v_mfma_f32_16x16x32_bf16 v[192:195], v[116:119], v[168:171], v[192:195]\n\t" \
    "v_mul_f32 v205, %[kd], v205\n\t" \
    "v_mfma_f32_16x16x32_bf16 v[188:191], v[32:35], v[164:167], v[188:191]\n\t" \
    "ds_read_b128 v[164:167], %[la] offset:27648\n\t" \
    "v_mul_f32 v204, %[kd], v204\n\t" \
    "v_mfma_f32_16x16x32_bf16 v[188:191], v[36:39], v[168:171], v[188:191]\n\t" \
    "ds_read_b128 v[168:171], %[la] offset:4096\n\t" \
    "v_fma_f32 v204, -v205, v205, v204\n\t" \
    "s_waitcnt lgkmcnt(6)\n\t" \
    "v_mfma_f32_16x16x32_bf16 v[188:191], v[40:43], v[172:175], v[188:191]\n\t" \
    "v_max_f32 v204, 0, v204\n\t" \
    "s_waitcnt lgkmcnt(5)\n\t" \
    "v_mfma_f32_16x16x32_bf16 v[188:191], v[44:47], v[176:179], v[188:191]\n\t" \
    "v_add_f32 v206, %[ke], v204\n\t" \
    "v_mfma_f32_16x16x32_bf16 v[192:195], v[120:123], v[172:175], v[192:195]\n\t" \
    "ds_read_b128 v[172:175], %[la] offset:12288\n\t" \
    "v_rsq_f32 v206, v206\n\t" \
    "v_mfma_f32_16x16x32_bf16 v[192:195], v[124:127], v[176:179], v[192:195]\n\t" \
    "ds_read_b128 v[176:179], %[la] offset:20480\n\t" \
    "s_waitcnt lgkmcnt(6)\n\t" \
    "v_mfma_f32_16x16x32_bf16 v[192:195], v[128:131], v[180:183], v[192:195]\n\t" \
    "v_mul_f32 v207, -v205, v206\n\t" \
    "s_waitcnt lgkmcnt(5)\n\t" \
    "v_mfma_f32_16x16x32_bf16 v[192:195], v[132:135], v[184:187], v[192:195]\n\t" \
    "v_mul_f32 v208, v196, v207\n\t" \
    "v_mfma_f32_16x16x32_bf16 v[188:191], v[48:51], v[180:183], v[188:191]\n\t" \
    "ds_read_b128 v[180:183], %[la] offset:28672\n\t" \
    "v_mul_f32 v209, v197, v207\n\t" \
    "v_mul_f32 v210, v198, v207\n\t" \
    "v_mfma_f32_16x16x32_bf16 v[188:191], v[52:55], v[184:187], v[188:191]\n\t" \
    "v_mul_f32 v211, v199, v207\n\t" \
    "v_mul_f32 v212, v200, v207\n\t" \
    "s_waitcnt lgkmcnt(5)\n\t" \
    "v_mfma_f32_16x16x32_bf16 v[188:191], v[56:59], v[160:163], v[188:191]\n\t" \
    "v_mul_f32 v213, v201, v207\n\t" \
    "v_mul_f32 v214, v202, v207\n\t" \
    "s_waitcnt lgkmcnt(4)\n\t" \
    "v_mfma_f32_16x16x32_bf16 v[188:191], v[60:63], v[164:167], v[188:191]\n\t" \
    "v_mul_f32 v215, v203, v207\n\t" \
    "v_mov_b64 v[216:217], -1\n\t" \
    "v_mfma_f32_16x16x32_bf16 v[192:195], v[136:139], v[160:163], v[192:195]\n\t" \
    "v_mov_b64 v[218:219], -1\n\t" \
    "buffer_store_dwordx4 v[216:219], %[l16], %[rs], %[sa] offen sc1\n\t" \
    "v_mfma_f32_16x16x32_bf16 v[192:195], v[140:143], v[164:167], v[192:195]\n\t" \
    "s_waitcnt lgkmcnt(3)\n\t" \
    "v_mfma_f32_16x16x32_bf16 v[192:195], v[144:147], v[168:171], v[192:195]\n\t" \
    "s_waitcnt lgkmcnt(2)\n\t" \
    "v_mfma_f32_16x16x32_bf16 v[192:195], v[148:151], v[172:175], v[192:195]\n\t" \
    "v_mfma_f32_16x16x32_bf16 v[188:191], v[64:67], v[168:171], v[188:191]\n\t" \
    "v_mfma_f32_16x16x32_bf16 v[188:191], v[68:71], v[172:175], v[188:191]\n\t" \
    "s_waitcnt lgkmcnt(1)\n\t" \
    "v_mfma_f32_16x16x32_bf16 v[188:191], v[72:75], v[176:179], v[188:191]\n\t" \
    "s_waitcnt lgkmcnt(0)\n\t" \
    "v_mfma_f32_16x16x32_bf16 v[188:191], v[76:79], v[180:183], v[188:191]\n\t" \
    "v_mfma_f32_16x16x32_bf16 v[192:195], v[152:155], v[176:179], v[192:195]\n\t" \
    "v_mfma_f32_16x16x32_bf16 v[192:195], v[156:159], v[180:183], v[192:195]\n\t" \
    "s_setprio 2\n\t" \
    "buffer_load_dwordx4 v[232:235], %[l16], %[rs], %[n0] offen offset:0 sc1\n\t" \
    "buffer_load_dwordx4 v[236:239], %[l16], %[rs], %[n0] offen offset:1024 sc1\n\t" \
    "buffer_load_dwordx4 v[240:243], %[l16], %[rs], %[n0] offen offset:2048 sc1\n\t" \
    "buffer_load_dwordx4 v[244:247], %[l16], %[rs], %[n0] offen offset:3072 sc1\n\t" \
    "buffer_load_dwordx4 v[248:251], %[l16], %[rs], %[n1] offen offset:0 sc1\n\t" \
    ASM_ST3 \
    "v_fma_f32 v188, v188, v206, v208\n\t" \
    "v_fma_f32 v189, v189, v206, v209\n\t" \
    "v_fma_f32 v190, v190, v206, v210\n\t" \
    "v_fma_f32 v191, v191, v206, v211\n\t" \
    "v_mul_f32 v160, v188, v188\n\t" \
    "v_mul_f32 v161, v189, v189\n\t" \
    "v_mul_f32 v162, v190, v190\n\t" \
    "v_mul_f32 v163, v191, v191\n\t" \
    "v_fma_f32 v160, v160, %[k1], v220\n\t" \
    "v_fma_f32 v161, v161, %[k1], v220\n\t" \
    "v_fma_f32 v162, v162, %[k1], v220\n\t" \
    "v_fma_f32 v163, v163, %[k1], v220\n\t" \
    "v_mul_f32 v160, v188, v160\n\t" \
    "v_mul_f32 v161, v189, v161\n\t" \
    "v_mul_f32 v162, v190, v162\n\t" \
    "v_mul_f32 v163, v191, v163\n\t" \
    "v_fma_f32 v192, v192, v206, v212\n\t" \
    "v_fma_f32 v193, v193, v206, v213\n\t" \
    "v_fma_f32 v194, v194, v206, v214\n\t" \
    "v_fma_f32 v195, v195, v206, v215\n\t" \
    "v_mul_f32 v164, v192, v192\n\t" \
    "v_mul_f32 v165, v193, v193\n\t" \
    "v_mul_f32 v166, v194, v194\n\t" \
    "v_mul_f32 v167, v195, v195\n\t" \
    "v_fma_f32 v164, v164, %[k1], v220\n\t" \
    "v_fma_f32 v165, v165, %[k1], v220\n\t" \
    "v_fma_f32 v166, v166, %[k1], v220\n\t" \
    "v_fma_f32 v167, v167, %[k1], v220\n\t" \
    "v_mul_f32 v164, v192, v164\n\t" \
    "v_mul_f32 v165, v193, v165\n\t" \
    "v_mul_f32 v166, v194, v166\n\t" \
    "v_mul_f32 v167, v195, v167\n\t" \
    "v_exp_f32 v160, v160\n\t" \
    "v_exp_f32 v161, v161\n\t" \
    "v_exp_f32 v162, v162\n\t" \
    "v_exp_f32 v163, v163\n\t" \
    "v_exp_f32 v164, v164\n\t" \
    "v_exp_f32 v165, v165\n\t" \
    "v_exp_f32 v166, v166\n\t" \
    "v_exp_f32 v167, v167\n\t" \
    "v_add_f32 v160, 1.0, v160\n\t" \
    "v_add_f32 v161, 1.0, v161\n\t" \
    "v_add_f32 v162, 1.0, v162\n\t" \
    "v_add_f32 v163, 1.0, v163\n\t" \
    "v_add_f32 v164, 1.0, v164\n\t" \
    "v_add_f32 v165, 1.0, v165\n\t" \
    "v_add_f32 v166, 1.0, v166\n\t" \
    "v_add_f32 v167, 1.0, v167\n\t" \
    "v_rcp_f32 v160, v160\n\t" \
    "v_rcp_f32 v161, v161\n\t" \
    "v_rcp_f32 v162, v162\n\t" \
    "v_rcp_f32 v163, v163\n\t" \
    "v_rcp_f32 v164, v164\n\t" \
    "v_rcp_f32 v165, v165\n\t" \
    "v_rcp_f32 v166, v166\n\t" \
    "v_rcp_f32 v167, v167\n\t" \
    "v_mul_f32 v188, v188, v160\n\t" \
    "v_mul_f32 v189, v189, v161\n\t" \
    "v_mul_f32 v190, v190, v162\n\t" \
    "v_mul_f32 v191, v191, v163\n\t" \
    "v_mul_f32 v192, v192, v164\n\t" \
    "v_mul_f32 v193, v193, v165\n\t" \
    "v_mul_f32 v194, v194, v166\n\t" \
    "v_mul_f32 v195, v195, v167\n\t" \
    "v_cvt_pk_bf16_f32 v188, v188, v189\n\t" \
    "v_cvt_pk_bf16_f32 v189, v190, v191\n\t" \
    "v_cvt_pk_bf16_f32 v190, v192, v193\n\t" \
    "v_cvt_pk_bf16_f32 v191, v194, v195\n\t" \
    "v_min_u32 v188, -2, v188\n\t" \
    "v_min_u32 v189, -2, v189\n\t" \
    "v_min_u32 v190, -2, v190\n\t" \
    "v_min_u32 v191, -2, v191\n\t" \
    "v_mov_b32 v216, 0x3f803f80\n\t" \
    "v_mov_b32 v217, 0x3f803f80\n\t" \
    "v_mov_b32 v218, 0x3f803f80\n\t" \
    "v_mov_b32 v219, 0x3f803f80\n\t" \
    "v_permlane16_swap_b32 v188, v190\n\t" \
    "v_permlane16_swap_b32 v189, v191\n\t" \
    "s_waitcnt vmcnt(6)\n\t" \
    "buffer_store_dwordx4 v[188:191], %[oa], %[rs], %[so] offen sc1\n\t" \
    "s_nop 1\n\t" \
    "s_setprio 0\n\t"
#define ASM_WT3_HI \
    "s_setprio 1\n\t" \
    "s_barrier\n\t" ASM_ST \
    "ds_read_b128 v[160:163], %[la] offset:0\n\t" \
    "ds_read_b128 v[164:167], %[la] offset:1024\n\t" \
    "ds_read_b128 v[168:171], %[la] offset:2048\n\t" \
    "ds_read2st64_b32 v[204:205], %[sr] offset0:20 offset1:24\n\t" \
    "ds_read_b128 v[172:175], %[la] offset:3072\n\t" \
    "ds_read_b128 v[176:179], %[la] offset:8192\n\t" \
    "ds_read_b128 v[180:183], %[la] offset:9216\n\t" \
    "ds_read_b128 v[184:187], %[la] offset:10240\n\t" \
    "s_waitcnt lgkmcnt(7)\n\t" \
    "v_mfma_f32_16x16x32_bf16 v[192:195], v[80:83], v[160:163], 0\n\t" \
    "s_waitcnt lgkmcnt(6)\n\t" \
    "v_mfma_f32_16x16x32_bf16 v[192:195], v[84:87], v[164:167], v[192:195]\n\t" \
    "v_mfma_f32_16x16x32_bf16 v[188:191], v[0:3], v[160:163], 0\n\t" \
    "ds_read_b128 v[160:163], %[la] offset:11264\n\t" \
    "v_mfma_f32_16x16x32_bf16 v[188:191], v[4:7], v[164:167], v[188:191]\n\t" \
    "ds_read_b128 v[164:167], %[la] offset:16384\n\t" \
    "s_waitcnt lgkmcnt(7)\n\t" \
    "v_mfma_f32_16x16x32_bf16 v[188:191], v[8:11], v[168:171], v[188:191]\n\t" \
    "s_waitcnt lgkmcnt(6)\n\t" \
    "s_waitcnt lgkmcnt(5)\n\t" \
    "v_mfma_f32_16x16x32_bf16 v[188:191], v[12:15], v[172:175], v[188:191]\n\t" \
    "v_permlane16_swap_b32 v204, v205\n\t" \
    "v_mfma_f32_16x16x32_bf16 v[192:195], v[88:91], v[168:171], v[192:195]\n\t" \
    "ds_read_b128 v[168:171], %[la] offset:17408\n\t" \
    "v_mfma_f32_16x16x32_bf16 v[192:195], v[92:95], v[172:175], v[192:195]\n\t" \
    "ds_read_b128 v[172:175], %[la] offset:18432\n\t" \
    "v_add_f32 v206, v204, v205\n\t" \
    "s_waitcnt lgkmcnt(6)\n\t" \
    "v_mfma_f32_16x16x32_bf16 v[192:195], v[96:99], v[176:179], v[192:195]\n\t" \
    "v_add_f32 v207, v204, v205\n\t" \
    "s_waitcnt lgkmcnt(5)\n\t" \
    "v_mfma_f32_16x16x32_bf16 v[192:195], v[100:103], v[180:183], v[192:195]\n\t" \
    "v_mfma_f32_16x16x32_bf16 v[188:191], v[16:19], v[176:179], v[188:191]\n\t" \
    "ds_read_b128 v[176:179], %[la] offset:19456\n\t" \
    "v_permlane32_swap_b32 v206, v207\n\t" \
    "v_mfma_f32_16x16x32_bf16 v[188:191], v[20:23], v[180:183], v[188:191]\n\t" \
    "ds_read_b128 v[180:183], %[la] offset:24576\n\t" \
    "s_waitcnt lgkmcnt(6)\n\t" \
    "v_mfma_f32_16x16x32_bf16 v[188:191], v[24:27], v[184:187], v[188:191]\n\t" \
    "v_add_f32 v204, v206, v207\n\t" \
    "s_waitcnt lgkmcnt(5)\n\t" \
    "v_mfma_f32_16x16x32_bf16 v[188:191], v[28:31], v[160:163], v[188:191]\n\t" \
    "v_add_f32 v205, v206, v207\n\t" \
    "v_mfma_f32_16x16x32_bf16 v[192:195], v[104:107], v[184:187], v[192:195]\n\t" \
    "ds_read_b128 v[184:187], %[la] offset:25600\n\t" \
    "v_mfma_f32_16x16x32_bf16 v[192:195], v[108:111], v[160:163], v[192:195]\n\t" \
    "ds_read_b128 v[160:163], %[la] offset:26624\n\t" \
    "v_permlane16_swap_b32 v204, v205\n\t" \
    "s_waitcnt lgkmcnt(6)\n\t" \
    "v_mfma_f32_16x16x32_bf16 v[192:195], v[112:115], v[164:167], v[192:195]\n\t" \
    "s_waitcnt lgkmcnt(5)\n\t" \
    "v_mfma_f32_16x16x32_bf16 v[192:195], v[116:119], v[168:171], v[192:195]\n\t" \
    "v_mul_f32 v205, %[kd], v205\n\t" \
    "v_mfma_f32_16x16x32_bf16 v[188:191], v[32:35], v[164:167], v[188:191]\n\t" \
    "ds_read_b128 v[164:167], %[la] offset:27648\n\t" \
    "v_mul_f32 v204, %[kd], v204\n\t" \
    "v_mfma_f32_16x16x32_bf16 v[188:191], v[36:39], v[168:171], v[188:191]\n\t" \
    "ds_read_b128 v[168:171], %[la] offset:4096\n\t" \
    "v_fma_f32 v204, -v205, v205, v204\n\t" \
    "s_waitcnt lgkmcnt(6)\n\t" \
    "v_mfma_f32_16x16x32_bf16 v[188:191], v[40:43], v[172:175], v[188:191]\n\t" \
    "v_max_f32 v204, 0, v204\n\t" \
    "s_waitcnt lgkmcnt(5)\n\t" \
    "v_mfma_f32_16x16x32_bf16 v[188:191], v[44:47], v[176:179], v[188:191]\n\t" \
    "v_add_f32 v206, %[ke], v204\n\t" \
    "v_mfma_f32_16x16x32_bf16 v[192:195], v[120:123], v[172:175], v[192:195]\n\t" \
    "ds_read_b128 v[172:175], %[la] offset:12288\n\t" \
    "v_rsq_f32 v206, v206\n\t" \
    "v_mfma_f32_16x16x32_bf16 v[192:195], v[124:127], v[176:179], v[192:195]\n\t" \
    "ds_read_b128 v[176:179], %[la] offset:20480\n\t" \
    "s_waitcnt lgkmcnt(6)\n\t" \
    "v_mfma_f32_16x16x32_bf16 v[192:195], v[128:131], v[180:183], v[192:195]\n\t" \
    "v_mul_f32 v207, -v205, v206\n\t" \
    "s_waitcnt lgkmcnt(5)\n\t" \
    "v_mfma_f32_16x16x32_bf16 v[192:195], v[132:135], v[184:187], v[192:195]\n\t" \
    "v_mul_f32 v208, v196, v207\n\t" \
    "v_mfma_f32_16x16x32_bf16 v[188:191], v[48:51], v[180:183], v[188:191]\n\t" \
    "ds_read_b128 v[180:183], %[la] offset:28672\n\t" \
    "v_mul_f32 v209, v197, v207\n\t" \
    "v_mul_f32 v210, v198, v207\n\t" \
    "v_mfma_f32_16x16x32_bf16 v[188:191], v[52:55], v[184:187], v[188:191]\n\t" \
    "v_mul_f32 v211, v199, v207\n\t" \
    "v_mul_f32 v212, v200, v207\n\t" \
    "s_waitcnt lgkmcnt(5)\n\t" \
    "v_mfma_f32_16x16x32_bf16 v[188:191], v[56:59], v[160:163], v[188:191]\n\t" \
    "v_mul_f32 v213, v201, v207\n\t" \
    "v_mul_f32 v214, v202, v207\n\t" \
    "s_waitcnt lgkmcnt(4)\n\t" \
    "v_mfma_f32_16x16x32_bf16 v[188:191], v[60:63], v[164:167], v[188:191]\n\t" \
    "v_mul_f32 v215, v203, v207\n\t" \
    "v_mov_b64 v[216:217], -1\n\t" \
    "v_mfma_f32_16x16x32_bf16 v[192:195], v[136:139], v[160:163], v[192:195]\n\t" \
    "v_mov_b64 v[218:219], -1\n\t" \
    "buffer_store_dwordx4 v[216:219], %[l16], %[rs], %[sa] offen sc1\n\t" \
    "v_mfma_f32_16x16x32_bf16 v[192:195], v[140:143], v[164:167], v[192:195]\n\t" \
    "s_waitcnt lgkmcnt(3)\n\t" \
    "v_mfma_f32_16x16x32_bf16 v[192:195], v[144:147], v[168:171], v[192:195]\n\t" \
    "s_waitcnt lgkmcnt(2)\n\t" \
    "v_mfma_f32_16x16x32_bf16 v[192:195], v[148:151], v[172:175], v[192:195]\n\t" \
    "v_mfma_f32_16x16x32_bf16 v[188:191], v[64:67], v[168:171], v[188:191]\n\t" \
    "v_mfma_f32_16x16x32_bf16 v[188:191], v[68:71], v[172:175], v[188:191]\n\t" \
    "s_waitcnt lgkmcnt(1)\n\t" \
    "v_mfma_f32_16x16x32_bf16 v[188:191], v[72:75], v[176:179], v[188:191]\n\t" \
    "s_waitcnt lgkmcnt(0)\n\t" \
    "v_mfma_f32_16x16x32_bf16 v[188:191], v[76:79], v[180:183], v[188:191]\n\t" \
    "v_mfma_f32_16x16x32_bf16 v[192:195], v[152:155], v[176:179], v[192:195]\n\t" \
    "v_mfma_f32_16x16x32_bf16 v[192:195], v[156:159], v[180:183], v[192:195]\n\t" \
    ASM_ST3 \
    "s_nop 1\n\t" \
    "v_fma_f32 v188, v188, v206, v208\n\t" \
    "v_fma_f32 v189, v189, v206, v209\n\t" \
    "v_fma_f32 v190, v190, v206, v210\n\t" \
    "v_fma_f32 v191, v191, v206, v211\n\t" \
    "v_mul_f32 v160, v188, v188\n\t" \
    "v_mul_f32 v161, v189, v189\n\t" \
    "v_mul_f32 v162, v190, v190\n\t" \
    "v_mul_f32 v163, v191, v191\n\t" \
    "v_fma_f32 v160, v160, %[k1], v220\n\t" \
    "v_fma_f32 v161, v161, %[k1], v220\n\t" \
    "v_fma_f32 v162, v162, %[k1], v220\n\t" \
    "v_fma_f32 v163, v163, %[k1], v220\n\t" \
    "v_mul_f32 v160, v188, v160\n\t" \
    "v_mul_f32 v161, v189, v161\n\t" \
    "v_mul_f32 v162, v190, v162\n\t" \
    "v_mul_f32 v163, v191, v163\n\t" \
    "v_fma_f32 v192, v192, v206, v212\n\t" \
    "v_fma_f32 v193, v193, v206, v213\n\t" \
    "v_fma_f32 v194, v194, v206, v214\n\t" \
    "v_fma_f32 v195, v195, v206, v215\n\t" \
    "v_mul_f32 v164, v192, v192\n\t" \
    "v_mul_f32 v165, v193, v193\n\t" \
    "v_mul_f32 v166, v194, v194\n\t" \
    "v_mul_f32 v167, v195, v195\n\t" \
    "v_fma_f32 v164, v164, %[k1], v220\n\t" \
    "v_fma_f32 v165, v165, %[k1], v220\n\t" \
    "v_fma_f32 v166, v166, %[k1], v220\n\t" \
    "v_fma_f32 v167, v167, %[k1], v220\n\t" \
    "v_mul_f32 v164, v192, v164\n\t" \
    "v_mul_f32 v165, v193, v165\n\t" \
    "v_mul_f32 v166, v194, v166\n\t" \
    "v_mul_f32 v167, v195, v167\n\t" \
    "v_exp_f32 v160, v160\n\t" \
    "v_exp_f32 v161, v161\n\t" \
    "v_exp_f32 v162, v162\n\t" \
    "v_exp_f32 v163, v163\n\t" \
    "v_exp_f32 v164, v164\n\t" \
    "v_exp_f32 v165, v165\n\t" \
    "v_exp_f32 v166, v166\n\t" \
    "v_exp_f32 v167, v167\n\t" \
    "v_add_f32 v160, 1.0, v160\n\t" \
    "v_add_f32 v161, 1.0, v161\n\t" \
    "v_add_f32 v162, 1.0, v162\n\t" \
    "v_add_f32 v163, 1.0, v163\n\t" \
    "v_add_f32 v164, 1.0, v164\n\t" \
    "v_add_f32 v165, 1.0, v165\n\t" \
    "v_add_f32 v166, 1.0, v166\n\t" \
    "v_add_f32 v167, 1.0, v167\n\t" \
    "v_rcp_f32 v160, v160\n\t" \
    "v_rcp_f32 v161, v161\n\t" \
    "v_rcp_f32 v162, v162\n\t" \
    "v_rcp_f32 v163, v163\n\t" \
    "v_rcp_f32 v164, v164\n\t" \
    "v_rcp_f32 v165, v165\n\t" \
    "v_rcp_f32 v166, v166\n\t" \
    "v_rcp_f32 v167, v167\n\t" \
    "v_mul_f32 v188, v188, v160\n\t" \
    "v_mul_f32 v189, v189, v161\n\t" \
    "v_mul_f32 v190, v190, v162\n\t" \
    "v_mul_f32 v191, v191, v163\n\t" \
    "v_mul_f32 v192, v192, v164\n\t" \
    "v_mul_f32 v193, v193, v165\n\t" \
    "v_mul_f32 v194, v194, v166\n\t" \
    "v_mul_f32 v195, v195, v167\n\t" \
    "v_cvt_pk_bf16_f32 v188, v188, v189\n\t" \
    "v_cvt_pk_bf16_f32 v189, v190, v191\n\t" \
    "v_cvt_pk_bf16_f32 v190, v192, v193\n\t" \
    "v_cvt_pk_bf16_f32 v191, v194, v195\n\t" \
    "v_min_u32 v188, -2, v188\n\t" \
    "v_min_u32 v189, -2, v189\n\t" \
    "v_min_u32 v190, -2, v190\n\t" \
    "v_min_u32 v191, -2, v191\n\t" \
    "s_nop 1\n\t" \
    "v_permlane16_swap_b32 v188, v190\n\t" \
    "v_permlane16_swap_b32 v189, v191\n\t" \
    "s_waitcnt vmcnt(1)\n\t" \
    "buffer_store_dwordx4 v[188:191], %[oa], %[rs], %[so] offen sc1\n\t" \
    "s_nop 1\n\t"
#define ASM_POLL5_I \
    "buffer_load_dwordx4 v[232:235], %[l16], %[rs], %[s0] offen offset:0 sc1\n\t" \
    "buffer_load_dwordx4 v[236:239], %[l16], %[rs], %[s0] offen offset:1024 sc1\n\t" \
    "buffer_load_dwordx4 v[240:243], %[l16], %[rs], %[s0] offen offset:2048 sc1\n\t" \
    "buffer_load_dwordx4 v[244:247], %[l16], %[rs], %[s0] offen offset:3072 sc1\n\t" \
    "buffer_load_dwordx4 v[248:251], %[l16], %[rs], %[s1] offen offset:0 sc1\n\t"
using u32x8 = __attribute__((ext_vector_type(8))) unsigned;
using u32x16 = __attribute__((ext_vector_type(16))) unsigned;
using u32x32 = __attribute__((ext_vector_type(32))) unsigned;
struct WRegs { u32x32 w[6]; u32x8 t; };
#define WREGS(W) "{v[0:31]}"(W.w[0]), "{v[32:63]}"(W.w[1]), "{v[64:95]}"(W.w[2]), "{v[96:127]}"(W.w[3]), "{v[128:159]}"(W.w[4]), "{v[160:191]}"(W.w[5]), "{v[192:199]}"(W.t)
__device__ __forceinline__ u32x32 cat32(u32x8 a, u32x8 b, u32x8 c, u32x8 d) {
    const u32x16 lo = __builtin_shufflevector(a, b, 0, 1, 2, 3, 4, 5, 6, 7, 8, 9, 10, 11, 12, 13, 14, 15);
    const u32x16 hi = __builtin_shufflevector(c, d, 0, 1, 2, 3, 4, 5, 6, 7, 8, 9, 10, 11, 12, 13, 14, 15);
    return __builtin_shufflevector(lo, hi, 0, 1, 2, 3, 4, 5, 6, 7, 8, 9, 10, 11, 12, 13, 14, 15, 16, 17, 18, 19, 20, 21, 22, 23, 24, 25, 26, 27, 28, 29, 30, 31);
}
#define WOUT(W) "=&{v[0:31]}"(W.w[0]), "=&{v[32:63]}"(W.w[1]), "=&{v[64:95]}"(W.w[2]), "=&{v[96:127]}"(W.w[3]), "=&{v[128:159]}"(W.w[4]), "=&{v[160:191]}"(W.w[5]), "=&{v[192:199]}"(W.t)
#define WTMP "memory", "scc", "v200", "v201", "v202", "v203", "v204", "v205", "v206", "v207", "v208", "v209", "v210", "v211", "v212", "v213", "v214", "v215", "v216", "v217", "v218", "v219", "v220", "v221", "v222", "v223", "v224", "v225", "v226", "v227", "v228", "v229", "v230", "v231", "v232", "v233", "v234", "v235", "v236", "v237", "v238", "v239"
template <bool NT>
__device__ __forceinline__ void load_w3(WRegs& W, const bf16_t* m, int wave, unsigned c0, bool hq) {
    const int ln = lane_id();
    unsigned o[3], s[3], t;
#pragma unroll
    for (int k = 0; k < 3; ++k) {
        const int row = (k * 64 + ln) / 24, sg = (k * 64 + ln) % 24;
        o[k] = row * (HD * 2) + c0 + (hq ? ((sg >> 1) & 1) * 384 + ((0x140 >> ((sg >> 3) * 4)) & 15) * 32 + (sg & 5) * 16 : sg * 16); s[k] = wave * 13824 + row * 416 + sg * 16;
    }
    const unsigned tr = (ln >> 4) * 3456 + ((ln >> 2) & 3) * 416 + (wave & 3) * 32 + (ln & 3) * 8;
#undef WNT
#define WNT " nt"
    if constexpr (NT) {
    asm volatile(ASM_WLOAD3 : WOUT(W), [t] "=&s"(t) : [rs] "s"(mkrsrc(m, DM * HD * 2)), [wx] "s"(wave), [o0] "v"(o[0]), [o1] "v"(o[1]), [o2] "v"(o[2]), [s0] "v"(s[0]), [s1] "v"(s[1]), [s2] "v"(s[2]),
                 [t0] "v"(tr + wave * 13824), [t1] "v"(tr + (wave ^ 1) * 13824), [t2] "v"(tr + (wave ^ 2) * 13824), [t3] "v"(tr + (wave ^ 3) * 13824), [t4] "v"(tr + (wave & 4) * 13824),
                 [wl] "v"(L_W + wave * 10240 + ln * 16) : WTMP);
    } else {
#undef WNT
#define WNT ""
    asm volatile(ASM_WLOAD3 : WOUT(W), [t] "=&s"(t) : [rs] "s"(mkrsrc(m, DM * HD * 2)), [wx] "s"(wave), [o0] "v"(o[0]), [o1] "v"(o[1]), [o2] "v"(o[2]), [s0] "v"(s[0]), [s1] "v"(s[1]), [s2] "v"(s[2]),
                 [t0] "v"(tr + wave * 13824), [t1] "v"(tr + (wave ^ 1) * 13824), [t2] "v"(tr + (wave ^ 2) * 13824), [t3] "v"(tr + (wave ^ 3) * 13824), [t4] "v"(tr + (wave & 4) * 13824),
                 [wl] "v"(L_W + wave * 10240 + ln * 16) : WTMP);
    }
}
#undef WNT
#define WNT " nt"
__device__ __forceinline__ void load_wo(WRegs& W, const bf16_t* m, int wave, int h) {
    const int ln = lane_id();
    unsigned o[5], s[5], t;
#pragma unroll
    for (int k = 0; k < 5; ++k) {
        const int row = (k * 64 + ln) / 20, sg = (k * 64 + ln) % 20;
        o[k] = (h * DH + row) * (DM * 2) + sg * 16 + (wave & 3) * 320; s[k] = wave * 13824 + row * 352 + sg * 16;
    }
    asm volatile(ASM_WLOADO : WOUT(W), [t] "=&s"(t) : [rs] "s"(mkrsrc(m, DM * HD * 2)), [o0] "v"(o[0]), [o1] "v"(o[1]), [o2] "v"(o[2]), [o3] "v"(o[3]), [o4] "v"(o[4]),
                 [s0] "v"(s[0]), [s1] "v"(s[1]), [s2] "v"(s[2]), [s3] "v"(s[3]), [s4] "v"(s[4]), [tr] "v"(wave * 13824 + (ln >> 2) * 352 + (ln & 3) * 8), [wl] "v"(L_W + wave * 10240 + ln * 16) : WTMP);
}
using s16x4 = __attribute__((ext_vector_type(4))) short;
template <int NT, int KS, int N>
__device__ __forceinline__ void load_wn(u32x4 (&f)[NT * KS], const bf16_t* m, int rows, int wave, int c0, int k0) {
    const int ln = lane_id();
    const rsrc_t rs = mkrsrc(m, rows * N * 2);
#pragma unroll
    for (int k = 0; k < KS; ++k)
#pragma unroll
        for (int n = 0; n < NT; ++n) f[n * KS + k] = __builtin_amdgcn_raw_buffer_load_b128(rs, ((n * 64 + ln) / (2 * NT)) * (N * 2) + ((n * 64 + ln) % (2 * NT)) * 16, (k0 + k) * (N * 64) + c0 * 32, 0);
    const unsigned st = wave * 10240 + ln * 16, tr = wave * 10240 + ((ln >> 4) * 8 + ((ln >> 2) & 3)) * (NT * 32) + (ln & 3) * 8;
#pragma unroll
    for (int k = 0; k < KS; ++k) {
#pragma unroll
        for (int n = 0; n < NT; ++n) lds_st<u32x4>(st + n * 1024, f[n * KS + k]);
        asm volatile("" ::: "memory");
#pragma unroll
        for (int n = 0; n < NT; ++n) {
            const u32x2 a = __builtin_bit_cast(u32x2, __builtin_amdgcn_ds_read_tr16_b64_v4i16((LAS s16x4*)(tr + n * 32)));
            const u32x2 b = __builtin_bit_cast(u32x2, __builtin_amdgcn_ds_read_tr16_b64_v4i16((LAS s16x4*)(tr + n * 32 + NT * 128)));
            f[n * KS + k] = (u32x4){a[0], a[1], b[0], b[1]};
        }
        asm volatile("" ::: "memory");
    }
}
struct C1Regs { u32x8 lw0, lw1, z1; u32x4 z0; };
#define XG_OUT "=&{v[200:207]}"(C.lw0), "=&{v[224:227]}"(C.z0), "=&{v[228:235]}"(C.lw1), "=&{v[244:251]}"(C.z1)
#define XG_IN WREGS(W), [xa] "v"(xa), [wl] "v"(wl), [qs] "s"(qs)
#define XG_CLOB "memory", "scc", "v208", "v209", "v210", "v211", "v212", "v213", "v214", "v215", "v216", "v217", "v218", "v219", "v220", "v221", "v222", "v223", "v236", "v237", "v238", "v239", "v240", "v241", "v242", "v243", "v252"
constexpr int XS0 = 8;
__device__ __forceinline__ unsigned init_wait(const Params& P, int c, int ph, unsigned need = 0x220080u) {
    unsigned w;
    for (int spins = 0;; ++spins) {
        unsigned o;
        asm volatile("v_mov_b32 %0, 0" : "=v"(o));
        w = __builtin_amdgcn_readfirstlane(__builtin_amdgcn_raw_buffer_load_b32(mkrsrc(P.ws + FL0, FL_BYTES), o, FL_IN + (ph * 8 + (c & 7)) * 128, 16));
        if ((w & need) == need) break;
        __builtin_amdgcn_s_sleep(8);
        if (spin_fail(spins, P)) break;
    }
    asm volatile("" ::: "memory");
    return w;
}
#define PLACE() const unsigned pw_ = init_wait(P, wave, L); const int par = (L ^ (pw_ >> 16)) & 1, cpy = x ^ ((pw_ >> 20) & 1); const rsrc_t rr = mkrsrc(P.ws + par * 8192, RG_L); (void)cpy; (void)rr
__device__ __forceinline__ void xn_wait(const Params& P, rsrc_t rr, int seg, int cpy) {
    for (int spins = 0;; ++spins) {
        const unsigned f = __builtin_amdgcn_raw_buffer_load_b32(rr, (lane_id() & 15) * 128, RG_FX + (cpy & 3) * 2048, 16);
        if (__all(f > (unsigned)seg)) break;
        __builtin_amdgcn_s_sleep(4);
        if (spin_fail(spins, P)) break;
    }
    asm volatile("" ::: "memory");
}
__device__ __forceinline__ unsigned c1_dst(int wave, bool ktype) {
    const int ln = lane_id(), l15 = ln & 15, hf = (wave >> 1) & 1;
    const int rb16 = ((ln >> 2) & 4) * 16 + (ln >> 5) * 32;
    return (ktype ? L_KM + (l15 >> 2) * 128 + hf * 8 + (l15 & 3) * 2
                  : L_Q + (l15 >> 3) * 256 + ((l15 >> 1) & 1) * 128 + ((l15 >> 2) & 1) * 8 + hf * 4 + (l15 & 1) * 2) + (wave & 1) * 1536 + rb16;
}
template <int NP>
__device__ __forceinline__ void crit1(const WRegs& W, C1Regs& C, rsrc_t rr, unsigned sbase, int wave, unsigned wl, unsigned dsto, unsigned& budget, unsigned& fv, unsigned fs
) {
    unsigned abase;
    asm volatile("v_and_b32 %0, 0x3ff, %1\n\tv_or_b32 %0, %2, %0" : "=&v"(abase) : "v"(wl), "s"(wave * 1024));
    ASM_ST_DECL;
#define C1_OUT "+{v[224:227]}"(C.z0), "+{v[244:251]}"(C.z1), "+{v[200:207]}"(C.lw0), "+{v[228:235]}"(C.lw1)
#define C1_IN WREGS(W), [ab] "v"(abase), [wl] "v"(wl), [dst] "v"(dsto)
#define C1_CLOB "memory", "vcc", "scc", "v208", "v209", "v210", "v211", "v212", "v213", "v214", "v215", "v216", "v217", "v218", "v219", "v220", "v221", "v222", "v223", "v236", "v237", "v238", "v239", "v240", "v241", "v242", "v243"
#define C1_POLL [rs] "s"(rr), [s0] "s"(sbase), [s1] "s"(sbase + 4096), [s2] "s"(sbase + 2 * BLK)
    ST(1);
    if constexpr (NP == 13) asm volatile(ASM_CRIT1_V3 "s_waitcnt vmcnt(0)" : C1_OUT, [n] "+s"(budget), [fv] "+v"(fv) ASM_ST_OUT ASM_ST2_OUT : C1_IN, C1_POLL, [fs] "s"(fs) : C1_CLOB);
    else if constexpr (NP == 12) asm volatile(ASM_CRIT1_V2 "s_waitcnt vmcnt(0)" : C1_OUT, [n] "+s"(budget), [fv] "+v"(fv) ASM_ST_OUT ASM_ST2_OUT : C1_IN, C1_POLL, [fs] "s"(fs) : C1_CLOB);
    else if constexpr (NP == 3) asm volatile(ASM_CRIT1_3 : C1_OUT, [n] "+s"(budget) ASM_ST_OUT ASM_ST2_OUT : C1_IN, C1_POLL : C1_CLOB);
    else if constexpr (NP == 2) asm volatile(ASM_CRIT1_2 : C1_OUT, [n] "+s"(budget) ASM_ST_OUT ASM_ST2_OUT : C1_IN, C1_POLL : C1_CLOB);
    else asm volatile(ASM_CRIT1_N : C1_OUT ASM_ST_OUT : C1_IN : C1_CLOB);
    SB();
    ASM_ST_SAVE(3);
    if constexpr (NP != 0) { ASM_ST2_SAVE(2); }
}
#define STARG

#define SRG 5
#undef STROLE
#define STROLE 1
__device__ __forceinline__ void s_role(const Params& P, int wave, int L, int h, int x, unsigned t0_) {
    const char* xn = P.ws + PG0 + PG_XN + L * PG_LX;
    auto xdma = [&](int seg, int hb, int par) {
        const unsigned l16 = lane_id() * 16;
        const int mt = (wave >> 1) & 1;
        const char* g = xn + pgb(seg, par);
        if (hb == 0) {
            dma4(g + blin(mt * 20480 + (wave & 1) * 4096), l16, L_XA + mt * 16384 + (wave & 1) * 4096);
            dmaq<0>(g + blin(mt * 20480 + 16384) + (wave & 1) * 1024, l16, L_XB + mt * 4096 + (wave & 1) * 1024);
        } else {
            dma4(g + blin(mt * 20480 + 8192), l16, L_XA + mt * 16384 + 8192); dma4(g + blin(mt * 20480 + 12288), l16, L_XA + mt * 16384 + 12288);
            dmaq<2>(g + blin(mt * 20480 + 16384), l16, L_XB + mt * 4096); dmaq<3>(g + blin(mt * 20480 + 16384), l16, L_XB + mt * 4096);
        }
    };
    const float SCL = 0.07216878364870322f * 1.4426950408889634f;
    auto run = [&](auto Cc) {
    constexpr int CLS = decltype(Cc)::value;
    WRegs wf;
    MARK(!L && h == 0 && wave == 0, 0);
    unsigned hm_ = 0, hc_ = 0;
    if (HM_T == -2) hm_ = (unsigned)__builtin_amdgcn_s_memrealtime() - t0_;
    load_w3<(NTM & (CLS == 3 ? 2 : 1)) != 0>(wf, P.w[CLS == 3], wave, h * 384, false);
    MARKX(!L && wave == 0, 1);
    if (HM_T == -1) hm_ = (unsigned)__builtin_amdgcn_s_memrealtime() - t0_;
    PLACE();
    if (CLS == 3) {
        if (L) xn_wait(P, rr, 0, cpy);
        xdma(0, 0, par);
        if (wave == 4 || wave == 7) xdma(0, 1, par);
        wait_vm<0>();
    }
    bar();
    if (CLS == 3 && wave == 7 && L) xn_wait(P, rr, 1, cpy);
    const unsigned qs = (CLS == 3 ? L_KX : L_QX) + (wave & 1) * 3072 + ((wave >> 1) & 1) * 8;
    unsigned xa, wl, dsto;
    if (CLS == 3) { xa = L_XA + wave * 2048 + lane_id() * 16; wl = L_W + wave * 10240 + lane_id() * 16; dsto = c1_dst(wave, true); }
    __builtin_amdgcn_s_setprio(CLS == 3 ? 1 : 2);
    unsigned budget = 1u << 18, tb2 = 0;
    STDECL;
    LDECL;
#pragma unroll 1
    for (int t = 0; t < NSEG; ++t) {
        const unsigned slot = t & (NSLOT - 1);
        C1Regs C;
        {
            const WRegs& W = wf;
            ASM_ST_DECL;
            if constexpr (CLS == 3) {
                const int mt = (wave >> 1) & 1;
                const char* g = xn + pgb(t + 1 < NSEG ? t + 1 : t, par);
                asm volatile(ASM_XG_S_HI : XG_OUT ASM_ST_OUT : XG_IN, [slp] "s"(SLP4), [g0] "s"(g + blin(mt * 20480 + (wave & 1) * 4096)), [d0] "s"(L_XA + mt * 16384 + (wave & 1) * 4096),
                             [g1] "s"(g + blin(mt * 20480 + 16384) + (wave & 1) * 1024), [d1] "s"(L_XB + mt * 4096 + (wave & 1) * 1024) : XG_CLOB);
                wait_vm<0>();
                LB(t);
            } else {
                const unsigned l16 = lane_id() * 16;
                xa = L_XA + wave * 2048 + l16; wl = L_W + wave * 10240 + l16;
#define XG_Q(A) asm volatile("s_cmp_lt_u32 %[t], 2\n\ts_cbranch_scc1 .Lq3%=\n\t" A "s_branch .Lqe%=\n\t.Lq3%=:\n\t" ASM_XG_S_LO ".Lqe%=:" : XG_OUT ASM_ST_OUT : XG_IN, [t] "s"(t) : XG_CLOB)
                if constexpr (CLS == 0 || CLS == 4) XG_Q(ASM_XG_S_LO1); else XG_Q(ASM_XG_S_LO2);
                __builtin_amdgcn_s_setprio(0);
                if constexpr (CLS == 0) {
                    const int ln = lane_id();
                    const u32x4 s4 = sent4();
                    const unsigned poff = (ln * 16 + ((ln < 8) ? h * 128 : (1 + (h >> 4)) * 2 * BLK - 128 + (h & 15) * 512)) | ((ln >= 40 || t < 2) ? 0x40000000u : 0u);
#pragma unroll
                    for (int c = 0; c < (int)NCP; ++c) st128(s4, rr, poff, RG_XP + ((t - 2) & (NSLOT - 1)) * XP_SLOT + (c >> 1) * XP_PAIR + (c & 1) * BLK);
                    const unsigned pbase = RG_PX + ((t - 3) & (NSLOT - 1)) * PX_SLOT + (h >> 2) * BLK;
                    const unsigned o2 = ((ln & 16) ? 5120 + ((h >> 1) & 1) * 1024 + (h & 1) * 256 + (ln >> 5) * 512 : 4096 + (h & 3) * 256 + (ln >> 5) * 3072) + (ln & 15) * 16;
                    if (t >= 3) { st128(s4, rr, ln * 16, pbase + (h & 3) * 1024); st128(s4, rr, o2, pbase); }
                }
                if (CLS == 4 && t >= HT0) {
                    const int ln = lane_id(), u = h >> 2;
                    for (int spins = 0;; ++spins) {
                        const unsigned fl = __builtin_amdgcn_raw_buffer_load_b32(rr, (ln & 3) * 128, RG_FH + BLK + u * 512, 16);
                        if (__all(fl > (unsigned)t)) break;
                        __builtin_amdgcn_s_sleep(2);
                        if (spin_fail(spins, P)) break;
                    }
                    asm volatile("" ::: "memory");
                    const char* g = P.ws + PG0 + pgb(t, par) + PG_HV + (16 + u) * 3 * BLK;
                    unsigned t0, t1, vo;
                    static_assert(L_QX == 16384, "");
                    asm volatile("s_add_u32 %0, %4, 0\n\ts_and_b32 %1, %0, 0xe000\n\ts_add_u32 %0, %0, %1\n\tv_add_u32 %2, %0, %5\n\ts_mov_b32 m0, 16384\n\ts_nop 0\n\tglobal_load_lds_dwordx4 %2, %3 sc1\n\t"
                                 "s_add_u32 %0, %4, 1024\n\ts_and_b32 %1, %0, 0xe000\n\ts_add_u32 %0, %0, %1\n\tv_add_u32 %2, %0, %5\n\ts_mov_b32 m0, 17408\n\ts_nop 0\n\tglobal_load_lds_dwordx4 %2, %3 sc1\n\t"
                                 "s_add_u32 %0, %4, 2048\n\ts_and_b32 %1, %0, 0xe000\n\ts_add_u32 %0, %0, %1\n\tv_add_u32 %2, %0, %5\n\ts_mov_b32 m0, 19456\n\ts_nop 0\n\tglobal_load_lds_dwordx4 %2, %3 sc1\n\t"
                                 "s_add_u32 %0, %4, 6144\n\ts_and_b32 %1, %0, 0xe000\n\ts_add_u32 %0, %0, %1\n\tv_add_u32 %2, %0, %5\n\ts_mov_b32 m0, 22528\n\ts_nop 0\n\tglobal_load_lds_dwordx4 %2, %3 sc1\n\t"
                                 "s_add_u32 %0, %4, 7168\n\ts_and_b32 %1, %0, 0xe000\n\ts_add_u32 %0, %0, %1\n\tv_add_u32 %2, %0, %5\n\ts_mov_b32 m0, 23552\n\ts_nop 0\n\tglobal_load_lds_dwordx4 %2, %3 sc1\n\t"
                                 "s_add_u32 %0, %4, 8192\n\ts_and_b32 %1, %0, 0xe000\n\ts_add_u32 %0, %0, %1\n\tv_add_u32 %2, %0, %5\n\ts_mov_b32 m0, 25600\n\ts_nop 0\n\tglobal_load_lds_dwordx4 %2, %3 sc1\n\t"
                                 : "=&s"(t0), "=&s"(t1), "=&v"(vo) : "s"(g), "s"((((h >> 1) & 1) * 12 + (h & 1) * 3) * 1024), "v"(ln * 16) : "memory", "scc");
                }
                dsto = c1_dst(wave, false);
                while ((unsigned)__builtin_amdgcn_s_memtime() - tb2 < STD) __builtin_amdgcn_s_sleep(2);
            }
            ASM_ST_SAVE(7);
            ST(6);
        }
        ST(0);
        unsigned fv = 0;
        if (CLS == 2) fv = (lane_id() & 15) * 128;
        crit1<CLS == 3 ? 0 : CLS < 2 ? 3 : CLS == 2 ? 12 : 2>(wf, C, rr, RG_XM + slot * XM_SLOT + (cpy >> 1) * XM_PAIR + (cpy & 1) * BLK, wave, wl, dsto, budget, fv, RG_FX + (cpy & 3) * 2048 STARG);
        if (CLS != 3) tb2 = (unsigned)__builtin_amdgcn_s_memtime();
        if constexpr (CLS == 0) {
            const unsigned sp = RG_XP + slot * XP_SLOT, sx = sp + (1 + (h >> 4)) * 2 * BLK + (h & 15) * 512;
            ASM_ST_DECL;
            asm volatile(ASM_SM : [c0] "+s"(budget) ASM_ST_OUT : WREGS(wf), [rr] "s"(rr), [sx] "s"(sx), [sy] "s"(sx + XP_PAIR), [sm] "s"(sp + h * 128), [cs] "s"(BLK), [cp] "s"(XP_PAIR), [c70] "s"(0x70), [scl] "s"(SCL)
                         : "memory", "v200", "v201", "v202", "v203", "v204", "v205", "v206", "v207", "v208", "v209", "v210", "v211", "v212", "v213", "v214", "v215", "v216", "v217", "v218", "v219", "v220", "v221", "v222", "v223", "v224", "v225", "v226", "v227", "v228", "v229", "v230", "v231", "v232", "v233", "v234", "v235", "v236", "v237", "v238", "v239", "v240", "v241", "v242", "v243", "v244", "v245", "v246", "v247", "v248", "v249", "v250", "v251", "v252", "v253", "v254", "v255");
            ASM_ST_SAVE(4);
            ST(5);
        } else if constexpr (CLS == 1 || CLS == 4) {
            const int ln = lane_id(), l15 = ln & 15, l4 = ln >> 4;
            constexpr int mt = (CLS == 4);
            const unsigned qa = L_QX + mt * 6144 + ln * 16, kmo = L_KM + l4 * 128 + (ln & 7) * 16;
            const unsigned xo = mt ? (h & 3) * 1024 : 5120 + ((h >> 1) & 1) * 1024 + (h & 1) * 256;
            const unsigned pm = l4 < 2 ? (mt ? 4096 : 7168) + (h & 3) * 256 - xo + l15 * 16 + l4 * 8 : OOB;
            const unsigned so = RG_PX + slot * PX_SLOT + (h >> 2) * BLK + xo;
            f32x4 sd;
#pragma unroll
            for (int r = 0; r < 4; ++r) sd[r] = (l4 * 4 + r > l15) ? -3.0e38f : 0.f;
            ASM_ST_DECL;
#define P4_ARGS : "+{v[240:243]}"(sd) ASM_ST_OUT : WREGS(wf), [qa] "v"(qa), [km] "v"(kmo), [pm] "v"(pm), [rr] "s"(rr), [so] "s"(so), [scl] "s"(SCL) : "memory", "v200", "v201", "v202", "v203", "v204", "v205", "v206", "v207", "v208", "v209", "v210", "v211", "v212", "v213", "v214", "v215", "v216", "v217", "v218", "v219", "v220", "v221", "v222", "v223", "v224", "v225", "v226", "v227", "v228", "v229", "v230", "v231", "v232", "v233", "v234", "v235", "v236", "v237", "v238", "v239", "v244", "v245", "v246", "v247", "v248", "v249", "v250", "v251"
            if constexpr (mt) asm volatile(ASM_P4_1 P4_ARGS); else asm volatile(ASM_P4_0 P4_ARGS);
            ASM_ST_SAVE(4);
            ST(5);
        } else {
            bar();
            ST(4);
            if (CLS == 2 && L && t + 2 < NSEG && !__all(fv > (unsigned)t + 2)) xn_wait(P, rr, t + 2, cpy);
            if (CLS == 2) { int tt = t; asm volatile("" : "+s"(tt)); if (tt + 2 == XS0) init_wait(P, h, 1); }
            if (CLS == 3) LA();
            if (CLS == 3 && t == HM_T) hm_ = (unsigned)__builtin_amdgcn_s_memrealtime() - t0_;
            if (CLS == 3 && HM_C && t == 8) hc_ = (unsigned)__builtin_amdgcn_s_memtime();
            if (CLS == 3 && HM_C && t == 56) hm_ = ((unsigned)__builtin_amdgcn_s_memtime() - hc_) / 20u;
            {
                if (CLS == 3 && (wave == 4 || wave == 7)) {
                    if (t + 1 < NSEG) xdma(t + 1, 1, par);
                }
                ST(5);
            }
        }
        STFLUSH(t);
    }
    LF();
    if (CLS == 3 && wave == 5 && (h == HM_H || HM_H < 0) && L == HM_L) while ((unsigned)__builtin_amdgcn_s_memrealtime() - t0_ < HM_B + hm_) __builtin_amdgcn_s_sleep(1);
    };
    if (wave == 0) run(std::integral_constant<int, 0>{});
    else if (wave == 1) run(std::integral_constant<int, 1>{});
    else if (wave == 2) run(std::integral_constant<int, 4>{});
    else if (wave == 3) run(std::integral_constant<int, 2>{});
    else run(std::integral_constant<int, 3>{});
}

#undef STROLE
#define STROLE 8
__device__ __forceinline__ void h_role(const Params& P, int wave, int L, int i, int x) {
    const rsrc_t rp = mkrsrc(P.ws + PG0, PG_L);
    const char* xn = P.ws + PG0 + PG_XN + L * PG_LX;
    auto run = [&](auto Cc) {
    constexpr bool HI = decltype(Cc)::value;
    const int mt = (wave >> 1) & 1;
    WRegs wf;
    load_w3<(NTM & 16) != 0>(wf, i < 16 ? P.w[2] : P.w[0], wave, (i < 16 ? i * 2 + HI : (i - 16) * 4 + HI * 2) * 384, i >= 16);
    PLACE();
    auto xdma = [&](int seg, int hb) {
        const unsigned l16 = lane_id() * 16;
        const char* g = xn + pgb(seg, par);
        dma4(g + blin(mt * 20480 + hb * 8192 + (wave & 1) * 4096), l16, L_XA + mt * 16384 + hb * 8192 + (wave & 1) * 4096);
        dmaq<0>(g + blin(mt * 20480 + 16384) + hb * 2048 + (wave & 1) * 1024, l16, L_XB + mt * 4096 + hb * 2048 + (wave & 1) * 1024);
    };
    const unsigned qs = (HI ? L_KX : L_QX) + (wave & 1) * 3072 + mt * 8;
    const unsigned xa = L_XA + wave * 2048 + lane_id() * 16, wl = L_W + wave * 10240 + lane_id() * 16;
    const unsigned fp = RG_FH + (i >> 4) * BLK + ((i & 15) * 4 + (wave & 3)) * 128;
    const unsigned fb = L ? RG_FX + (cpy & 3) * 2048 : RG_FV + (i & 3) * 2048, fa = L ? 0 : HAH;
    unsigned go = 0, all = 0;
    STDECL;
    LDECL;
    for (int t = HT0; t <= NSEG; ++t) {
        ST(0);
        if constexpr (HI) {
            const unsigned l16 = lane_id() * 16;
            if (t > HT0) {
                u32x4 v[6];
                const int w4 = wave - 4;
#pragma unroll
                for (int k = 0; k < 6; ++k) v[k] = lds_ld<u32x4>(L_QX + ((k >> 1) * 8 + w4 * 2 + (k & 1)) * 1024 + l16);
                const unsigned so = pgb(t - 1, par) + PG_HV + i * 3 * BLK + w4 * 2048;
#pragma unroll
                for (int k = 0; k < 6; ++k) st128(v[k], rp, l16 + (k & 1) * 1024, so + (k >> 1) * BLK);
                if (!all) {
                    wait_vm<0>();
                    if (l16 < 512) __builtin_amdgcn_raw_buffer_store_b32(t, rr, l16 >> 2, fp, 16);
                }
            }
            ST(1);
            if (t == NSEG) break;
            if (!all) bar();
            xdma(t, 1);
        } else {
            if (t == NSEG) break;
            if (!go) {
                for (int spins = 0;; ++spins) {
                    if (__all(__builtin_amdgcn_raw_buffer_load_b32(rr, (lane_id() & 15) * 128, fb, 16) + fa > (unsigned)t)) break;
                    __builtin_amdgcn_s_sleep(HSL);
                    if (spin_fail(spins, P)) break;
                }
                asm volatile("" ::: "memory");
                xdma(t, 0); wait_vm<0>();
            }
            if (!all) bar();
        }
        ST(2);
        C1Regs C;
        const WRegs& W = wf;
        ASM_ST_DECL;
        if constexpr (HI) {
            const unsigned l4 = lane_id() * 4;
            asm volatile(ASM_XG_H_HI : XG_OUT ASM_ST_OUT : XG_IN, [rr] "s"(rr), [fp] "s"(fp), [tv] "s"(t), [fv] "v"((all && l4 < 128) ? l4 : OOB) : XG_CLOB);
        } else {
            asm volatile(ASM_XG_H_LO : XG_OUT ASM_ST_OUT : XG_IN : XG_CLOB);
            if (t + 1 == XS0) init_wait(P, wave, 1);
            const unsigned fv = __builtin_amdgcn_raw_buffer_load_b32(rr, (lane_id() & 15) * 128, fb, 16) + fa;
            go = __builtin_amdgcn_readfirstlane((t + 1 < NSEG && __all(fv > (unsigned)t + 1)) ? 1u : 0u);
            if (go) xdma(t + 1, 0);
            lds_st<unsigned>(L_MEM + wave * 4, go);
            wait_vm<0>();
        }
        ASM_ST_SAVE(3);
        ST(4);
        if (!HI) { LB(t); LA(); }
        PMARK(L && wave == 7, 6, t);
        bar();
        const u32x4 gv = lds_ld<u32x4>(L_MEM);
        all = __builtin_amdgcn_readfirstlane(gv[0] & gv[1] & gv[2] & gv[3]);
        ST(5);
        STFLUSH(t);
    }
    LF();
    };
    if (wave < 4) run(std::false_type{}); else run(std::true_type{});
}

#undef STROLE
#define STROLE 2
__device__ __forceinline__ void v_role(const Params& P, int wave, int L, int h, int x) {
    WRegs wf;
    STDECL;
    auto runv = [&](auto Cc) {
        load_w3<(NTM & 4) != 0>(wf, P.w[2], wave, h * 384, false);
        MARKX(!L && wave == 0, 6);
        PLACE();
        unsigned budget = 1u << 18;
        const unsigned wl = L_W + wave * 10240 + lane_id() * 16, dsto = c1_dst(wave, false);
        auto vfetch = [&](int seg, int tries) {
            const int ln = lane_id();
            for (int spins = 0; tries != -2; ++spins) {
                if (spins == tries) return false;
                const unsigned f = __builtin_amdgcn_raw_buffer_load_b32(rr, (ln & 3) * 128, RG_FH + (h >> 1) * 512, 16);
                if (__all(f > (unsigned)seg)) break;
                __builtin_amdgcn_s_sleep(4);
                if (spin_fail(spins, P)) break;
            }
            asm volatile("" ::: "memory");
            const char* g = P.ws + PG0 + pgb(seg, par) + PG_HV + (h >> 1) * 3 * BLK;
#pragma unroll
            for (int k = 0; k < 3; ++k) {
                const int f = (h & 1) * 12 + wave * 3 + k;
                dmaq<0>(g + (f >> 3) * BLK + (f & 7) * 1024, ln * 16, L_QX + (seg & 1) * 12288 + (wave * 3 + k) * 1024);
            }
            return true;
        };
#pragma unroll 1
        for (int t = 0; t < HT0; ++t) {
            if (L) xn_wait(P, rr, t, cpy);
            const unsigned l16 = lane_id() * 16;
            const char* g = P.ws + PG0 + PG_XN + L * PG_LX + pgb(t, par);
            dma4(g + blin(wave * 4096), l16, L_XA + wave * 4096); dma4(g + blin(20480 + wave * 4096), l16, L_XA + 16384 + wave * 4096);
            dmaq<0>(g + blin(16384) + wave * 1024, l16, L_XB + wave * 1024); dmaq<0>(g + blin(20480 + 16384) + wave * 1024, l16, L_XB + 4096 + wave * 1024);
            wait_vm<0>();
            bar();
            C1Regs C;
            const WRegs& W = wf;
            const unsigned xa = L_XA + wave * 2048 + l16, qs = L_QX + (wave & 1) * 3072 + ((wave >> 1) & 1) * 8 + t * 12288;
            asm volatile(ASM_XG_V : XG_OUT : XG_IN : XG_CLOB);
            if (t == 0) bar();
        }
        bool seen = false;
        unsigned tb2 = 0;
        for (int t = 0; t < NSEG; ++t) {
            if (t + 1 - V1A >= HT0 && t + 1 - V1A < NSEG) { vfetch(t + 1 - V1A, seen ? -2 : -1); wait_vm<0>(); }
            ST(7);
            bar();
            C1Regs C;
            asm volatile("ds_read_b128 v[200:203], %4\n\tds_read_b128 v[204:207], %4 offset:1024\n\tds_read_b128 v[228:231], %4 offset:2048\n\tds_read_b128 v[232:235], %4 offset:3072\n\t"
                         "v_mov_b64 v[224:225], 0\n\tv_mov_b64 v[226:227], 0\n\tv_mov_b64 v[244:245], 0\n\tv_mov_b64 v[246:247], 0\n\tv_mov_b64 v[248:249], 0\n\tv_mov_b64 v[250:251], 0\n\ts_waitcnt lgkmcnt(0)"
                         : XG_OUT : "v"(wl) : "memory");
            ST(6);
            while ((unsigned)__builtin_amdgcn_s_memtime() - tb2 < VTD) __builtin_amdgcn_s_sleep(1);
            unsigned fv = (lane_id() & 3) * 128;
            crit1<10 + decltype(Cc)::value>(wf, C, rr, RG_XM + (t & (NSLOT - 1)) * XM_SLOT + (cpy >> 1) * XM_PAIR + (cpy & 1) * BLK, wave, wl, dsto, budget, fv, RG_FH + (h >> 1) * 512 STARG);
            __builtin_amdgcn_s_setprio(0);
            bar();
            seen = __all(fv > (unsigned)t + 2 - V1A);
            tb2 = (unsigned)__builtin_amdgcn_s_memtime();
            ST(4);
            STC(5);
            STFLUSH(t);
        }
    };
    if (wave < 2) runv(std::integral_constant<int, 3>{});
    else if (wave < 4) runv(std::integral_constant<int, 2>{});
    else {
        const int w4 = wave - 4;
        const rsrc_t rd = mkrsrc(P.ws + PG0, PG_L);
        load_wo(wf, P.w[3], wave, h);
        PLACE();
        bar();
        bar();
        for (int t = -1; t < NSEG; ++t) {
            const unsigned slot = t & (NSLOT - 1);
            bar();
            ST(7);
#pragma unroll 1
            for (int n = V1A ? t + (t <= 0) : t + (t != 1) + (t == 0); n <= (V1A && t >= 0 ? t : t + 1) && n < NSEG; ++n) {
                const unsigned l16 = lane_id() * 16;
                const unsigned wl = L_W + wave * 10240 + l16, va = L_QX + (n & 1) * 12288 + l16;
                unsigned so = ((n & 7) << 22) + pgb(64 + (w4 >> 1), par) + ((w4 & 1) * 40 + (h >> 3)) * BLK + (h & 7) * 1024;
                asm volatile(ASM_VOX : [so] "+s"(so) : WREGS(wf), [l16] "v"(l16), [wl] "v"(wl), [va] "v"(va), [rd] "s"(rd) : "memory", "scc", "v200", "v201", "v202", "v203", "v204", "v205", "v206", "v207", "v208", "v209", "v210", "v211", "v212", "v213", "v214", "v215", "v216", "v217", "v218", "v219", "v220", "v221", "v222", "v223", "v224", "v225", "v226", "v227", "v228", "v229", "v230", "v231", "v232", "v233", "v234", "v235", "v236", "v237", "v238", "v239", "v240", "v241", "v242", "v243", "v244", "v245", "v246", "v247", "v248", "v249", "v250", "v251");
                if (V1A || n < 2) {
                    wait_vm<0>();
                    const unsigned l4 = lane_id() * 4;
                    if (l4 < 128) __builtin_amdgcn_raw_buffer_store_b32(n + 1, rr, l4, RG_FV + (w4 >> 1) * BLK + ((w4 & 1) * 32 + h) * 128, 16);
                }
            }
            if (t < 0) continue;
            ST(0);
            unsigned wl = L_W + wave * 10240 + lane_id() * 16;
            bf16x16 lw = lds_frag(wl, 1024);
            u32x32 cz;
            asm volatile("v_mov_b64 v[220:221], 0\n\tv_mov_b64 v[222:223], 0\n\tv_mov_b64 v[224:225], 0\n\tv_mov_b64 v[226:227], 0\n\tv_mov_b64 v[228:229], 0\n\tv_mov_b64 v[230:231], 0\n\tv_mov_b64 v[232:233], 0\n\tv_mov_b64 v[234:235], 0\n\tv_mov_b64 v[236:237], 0\n\tv_mov_b64 v[238:239], 0\n\tv_mov_b64 v[240:241], 0\n\tv_mov_b64 v[242:243], 0\n\tv_mov_b64 v[244:245], 0\n\tv_mov_b64 v[246:247], 0\n\tv_mov_b64 v[248:249], 0\n\tv_mov_b64 v[250:251], 0" : "={v[220:251]}"(cz));
            unsigned vo = ((wl >> 8) & 1) * (1 + (w4 & 1) * 2) * BLK + ((wl >> 9) & 1) * 1024 + (wl & 255);
            asm volatile("" : "+{v[212:219]}"(lw), "+{v[220:251]}"(cz), "+v"(wl), "+v"(vo));
            unsigned l16 = wl & 1023u;
            bar();
            wait_vm<0>();
            if (!V1A && l16 < 512 && t && t + 1 < NSEG) __builtin_amdgcn_raw_buffer_store_b32(t + 2, rr, l16 >> 2, RG_FV + (w4 >> 1) * BLK + ((w4 & 1) * 32 + h) * 128, 16);
            ST(1);
            const int sidx = (wl & 128u) ? 0xEEEE : 0x4444;
            unsigned so = RG_VM + slot * VM_SLOT + (unsigned)((w4 * 10 - (w4 & 1) + (h >> 4) * 2) * BLK + ((h >> 2) & 3) * 2048 + (h & 3) * 256);
            ST(3);
            ASM_ST_DECL;
            asm volatile(ASM_CRIT2 : [so] "+s"(so), "+{v[212:219]}"(lw), "+{v200}"(l16), "+{v[220:251]}"(cz) ASM_ST_OUT
                         : WREGS(wf), [wl] "v"(wl), [vo] "v"(vo), [sidx] "v"(sidx), [rr] "s"(rr)
                         : "memory", "scc", "v201", "v202", "v203", "v204", "v205", "v206", "v207", "v208", "v209", "v210", "v211");
            ASM_ST_SAVE(4);
            ST(5);
            STFLUSH(t);
        }
    }
}

#undef STROLE
#define STROLE 4
constexpr unsigned R_RED = 0;
constexpr unsigned R_PART = 16384;
__device__ __forceinline__ void r_role(const Params& P, int wave, int L, int r5, int x) {
    PLACE();
    const int jj = (r5 * 4 + cpy) ^ (r5 * 4 + x >= 6 ? 1 : 0);
    const int lane = lane_id(), l15 = lane & 15, l4 = lane >> 4;
    const unsigned l16 = lane * 16;
    const rsrc_t rd = mkrsrc(P.ws + PG0, PG_L);
    const int sidx = (l15 < 8) ? 0x4444 : 0xEEEE;
    unsigned offp[3];
    offp[0] = RG_XP + (cpy >> 1) * XP_PAIR + (cpy & 1) * BLK + (1 + (wave >> 2)) * 2 * BLK + (wave & 3) * 2048 + (PLAY ? (l4 >> 1) * 512 + (((l15 >> 2) & 1) + (l15 & 2)) * 128 + (l4 & 1) * 64 + (l15 >> 3) * 32 + (l15 & 1) * 16 : l16);
    offp[2] = (l15 < 8) ? RG_XP + (cpy >> 1) * XP_PAIR + (cpy & 1) * BLK + wave * 512 + l4 * 128 + l15 * 16 : OOB;
    const unsigned offv = RG_VM + ((jj >> 1) * 4 + (wave >> 2) * 2 + (jj & 1)) * BLK + (wave & 3) * 2048 + l16;
    const int j = jj * 2 + ((wave >> 1) & 1), fl = (wave & 1) * 32 + (lane & 31);
    const unsigned offo = RG_XM + (wave >> 2) * XM_PAIR + (lane >> 5) * BLK + (j & 31) * 256 + (j >> 5) * 2 * BLK + fl * 4;
    MARK(!L && jj == 0 && wave == 0, 5);
    {
        const int row = (lane >> 2) & 7, col = 16 * j + 4 * (lane & 3) + 2 * (wave & 1);
        __builtin_amdgcn_raw_buffer_store_b32(nosent(*(const unsigned*)(P.mem0 + row * DM + col)), rr, offo, 0, 16);
    }
    auto red_addr = [](int fl) { return R_RED + fl * 80 + (fl >> 4) * 16; };
    const unsigned rw0 = (lane >> 5) * 5248 + red_addr((((l15 >> 1) & 1) * 8 + 4 * (l4 & 1)) * 4 + (l15 >> 2)) + (l15 & 1) * 4 + (wave ^ (l15 >> 3)) * 8;
    const unsigned rrd = ((wave >> 1) & 1) * 5248 + red_addr(fl) + (lane >> 5) * 32;
    u32x32 vx;
    const unsigned fo = (lane & 3) * 128, fs = RG_FV + (jj / 10) * BLK + (((jj / 5) & 1) * 32 + wave * 4) * 128, vxo = pgb(64 + jj / 10, par) + (unsigned)((jj % 10) * 8 + (wave >> 1)) * BLK + (wave & 1) * 4096;
    const rsrc_t rp = mkrsrc(P.ws + PG0, PG_L);
    const unsigned xo = (l4 >> 1) * 256 + l15 * 16 + (l4 & 1) * 8, pw = R_PART + wave * 1024 + l16, pr = R_PART + (wave & 3) * 8192 + l16;
    const int fin = wave >> 2;
    asm volatile("" : "={v[128:159]}"(vx));
    auto vx_fetch = [&](int t) {
        for (int spins = 0;; ++spins) {
            const unsigned f = __builtin_amdgcn_raw_buffer_load_b32(rr, fo, fs, 16);
            if (__all(f > (unsigned)t)) break;
            __builtin_amdgcn_s_sleep(2);
            if (spin_fail(spins, P)) break;
        }
        const unsigned sv = ((t & 7) << 22) + vxo;
        asm volatile("buffer_load_dwordx4 v[128:131], %1, %2, %3 offen sc1\n\tbuffer_load_dwordx4 v[132:135], %1, %2, %3 offen offset:1024 sc1\n\t"
                     "buffer_load_dwordx4 v[136:139], %1, %2, %3 offen offset:2048 sc1\n\tbuffer_load_dwordx4 v[140:143], %1, %2, %3 offen offset:3072 sc1\n\t"
                     "buffer_load_dwordx4 v[144:147], %1, %2, %4 offen sc1\n\tbuffer_load_dwordx4 v[148:151], %1, %2, %4 offen offset:1024 sc1\n\t"
                     "buffer_load_dwordx4 v[152:155], %1, %2, %4 offen offset:2048 sc1\n\tbuffer_load_dwordx4 v[156:159], %1, %2, %4 offen offset:3072 sc1"
                     : "+{v[128:159]}"(vx) : "v"(l16), "s"(rp), "s"(sv), "s"(sv + 4 * BLK) : "memory");
    };
    vx_fetch(0);
    unsigned budget = 1u << 18, tpub = 0;
    STDECL;
    LDECL;
    for (int t = 0; t < NSEG; ++t) {
        const unsigned slot = t & (NSLOT - 1);
        ST(0);
        while ((unsigned)__builtin_amdgcn_s_memtime() - tpub < RTD) __builtin_amdgcn_s_sleep(1);
        u32x8 vm, pv;
        u32x4 pm;
        {
            unsigned a;
            ASM_ST_DECL;
            asm volatile(ASM_RCRIT : "=&{v[176:183]}"(vm), "=&{v[184:191]}"(pv), "=&{v[192:195]}"(pm), [a] "=&s"(a), [n] "+s"(budget) ASM_ST_OUT ASM_ST2_OUT ASM_ST3_OUT
                         : [rs] "s"(rr), [ov] "v"(offv), [sv] "s"(slot * VM_SLOT), [o0] "v"(offp[0]), [o2] "v"(offp[2]), [sp] "s"(slot * XP_SLOT),
                           "{v[128:159]}"(vx), [sidx] "v"(sidx), [rw] "v"(rw0), [rd] "v"(rrd), [oo] "v"(offo),
                           [so] "s"(((t + 1) & (NSLOT - 1)) * XM_SLOT), [pub] "s"(NSEG - 1 - t)
                         : "memory", "vcc", "scc", "v160", "v161", "v162", "v163", "v164", "v165", "v166", "v167", "v196", "v197", "v200", "v201", "v202", "v203", "v204", "v205", "v206", "v207");
            ASM_ST_SAVE(2); ASM_ST2_SAVE(1); ASM_ST3_SAVE(6);
        }
        ST(3);
        LA();
        MARK(!L && jj == 0 && wave == 0 && t < 4, 10 + t);
        PMARK(wave == 0, L * 7, t - 1 + L);
        tpub = (unsigned)__builtin_amdgcn_s_memtime();
        __builtin_amdgcn_s_sleep(RXDEL);
        unsigned early;
#pragma unroll 1
        for (int spins = 1023;; spins += 1024) {
            unsigned n;
            const unsigned sx = pgb(t, par) + blin(((wave >> 1) & 1) * 20480 + jj * 1024) + (wave & 1) * 512;
            const unsigned pxb = RG_PX + slot * PX_SLOT + wave * BLK, sv = (((t + 1) & 7) << 22) + vxo;
            ASM_ST_DECL;
            asm volatile(ASM_RX : "+{v[128:159]}"(vx), [n] "=&s"(n), [a] "=&s"(early) ASM_ST_OUT ASM_ST2_OUT
                         : "{v[176:183]}"(vm), "{v[184:191]}"(pv), "{v[192:195]}"(pm), [l16] "v"(l16), [rs] "s"(rr), [s0] "s"(pxb), [s1] "s"(pxb + 4096), [rd] "s"(rd), [rx] "s"(rd), [xo] "v"(xo),
                           [fo] "v"(fo), [fs] "s"(fs), [tn] "s"(t + 1), [sv0] "s"(sv), [sv1] "s"(sv + 4 * BLK), [rp] "s"(rp), [sx] "s"(PG_XR + L * PG_LX + sx), [sa] "s"(PG_XA + sx),
                           [pw] "v"(pw), [pr] "v"(pr), [fin] "s"(fin)
                         : "memory", "vcc", "scc", "v96", "v97", "v98", "v99", "v100", "v101", "v102", "v103", "v104", "v105", "v106", "v107", "v108", "v109", "v110", "v111", "v112", "v113", "v114", "v115", "v116", "v117", "v118", "v119", "v120", "v121", "v122", "v123", "v124", "v125", "v126", "v127", "v196", "v200", "v201", "v202", "v203", "v204", "v205", "v206", "v207", "v208", "v209", "v210", "v211", "v212", "v213", "v214", "v215", "v216", "v217", "v218", "v219", "v220", "v221", "v222", "v223", "v224", "v225", "v226", "v227", "v228", "v229", "v230", "v231", "v240", "v241", "v242", "v243", "v248", "v250", "v251");
            ASM_ST_SAVE(4); ASM_ST2_SAVE(5);
            if (__builtin_amdgcn_readfirstlane(n)) break;
            early = 0;
            if (spin_fail(spins, P)) { bar(); break; }
        }
        if (t + 1 < NSEG && !__builtin_amdgcn_readfirstlane(early)) vx_fetch(t + 1);
        PMARK(!L && wave == 7 && PMK == 1, 1, t - 1);
        st128(sent4(), rr, offv, slot * VM_SLOT); st128(sent4(), rr, offv + 1024, slot * VM_SLOT);
        __builtin_amdgcn_raw_buffer_store_b32(SENT, rr, offo, slot * XM_SLOT, 16);
        if (wave < 2 && t + AH < NSEG) st128(sent4(), rd, l16, PG_XA + pgb(t + AH, par) + blin(wave * 20480 + jj * 1024));
        ST(7);
        LB(t);
        STFLUSH(t);
    }
    LF();
}

#undef STROLE
#define STROLE 16
template <int WHICH>
__device__ __forceinline__ void w_role(const Params& P, int wave, int L, int cu, int x) {
    constexpr int KS = 20, N = (WHICH == 3) ? FF : DM, NU = 2 * NSEG;
    constexpr unsigned D_IN = WHICH == 1 ? PG_XA : WHICH == 2 ? PG_HS : PG_X1;
    constexpr unsigned D_OUT = WHICH == 1 ? PG_HS : WHICH == 2 ? PG_X1 : PG_H2;
    if (WHICH != 3 && cu * 8 + wave >= 20) {
        for (int u = 0; u <= NU; ++u) bar();
        return;
    }
    auto run = [&](auto Cc) {
    constexpr bool LO = decltype(Cc)::value;
    const int ln = lane_id(), l15 = ln & 15, l4 = ln >> 4;
    const unsigned l16 = ln * 16;
    const rsrc_t rd = mkrsrc(P.ws + PG0, PG_L);
    const int nt0 = (cu * 8 + wave) * 2;
    u32x32 w[5];
    {
        u32x4 f[2 * KS];
        load_wn<2, KS, N>(f, P.w[3 + WHICH], DM, wave, nt0, 0);
#pragma unroll
        for (int n = 0; n < 5; ++n) {
            u32x8 q[4];
#pragma unroll
            for (int i = 0; i < 4; ++i) q[i] = __builtin_shufflevector(f[n * 8 + 2 * i], f[n * 8 + 2 * i + 1], 0, 1, 2, 3, 4, 5, 6, 7);
            w[n] = cat32(q[0], q[1], q[2], q[3]);
        }
        asm volatile("" : "+{v[0:31]}"(w[0]), "+{v[32:63]}"(w[1]), "+{v[64:95]}"(w[2]), "+{v[96:127]}"(w[3]), "+{v[128:159]}"(w[4]));
    }
    PLACE();
    bar();
    const int ocol = nt0 * 16 + (l4 & 1) * 16 + (l4 >> 1) * 8;
    const unsigned o3 = (unsigned)((l4 & 1) * 512 + (l4 >> 1) * 256 + l15 * 16), ro = (l4 >> 1) * 256 + l15 * 16 + (l4 & 1) * 8;
    const int grp = nt0 / 10, sh = ((grp & 1) ? -1 : 1) * ((grp >> 1) + 1);
    if (WHICH == 1) {
        unsigned zv;
        asm volatile("v_mov_b32 %0, 0" : "=v"(zv));
        st128(((u32x4){zv, zv, zv, zv}), rd, l15 <= (grp >> 1) ? toff((grp & 1) ? S_ - 1 - l15 : l15, ocol, par) : OOB, PG_HS);
    }
    auto in0 = [&](int u) { return D_IN + pgb(u >> 1, par) + blin((u & 1) * 20480 + wave * 4096); };
    auto in1 = [&](int u) { return D_IN + pgb(u >> 1, par) + blin((u & 1) * 20480 + 16384) + wave * 1024; };
    u32x16 va;
    u32x4 vb;
    u32x8 cs, k8;
#define WT_W "{v[0:31]}"(w[0]), "{v[32:63]}"(w[1]), "{v[64:95]}"(w[2]), "{v[96:127]}"(w[3]), "{v[128:159]}"(w[4])
    asm volatile("v_mov_b32 v216, 0x3f803f80\n\tv_mov_b32 v217, v216\n\tv_mov_b32 v218, v216\n\tv_mov_b32 v219, v216\n\tv_mov_b32 v220, %[c0]\n\tv_mov_b32 v232, -1\n\tv_mov_b32 v236, -1\n\t"
                 "v_mov_b32 v240, -1\n\tv_mov_b32 v244, -1\n\tv_mov_b32 v248, -1\n\t" ASM_WCS
                 : "=&{v[196:203]}"(cs), "=&{v[216:223]}"(k8), "=&{v[232:247]}"(va), "=&{v[248:251]}"(vb) : WT_W, [c0] "s"(-2.0f * 0.7978845608028654f * 1.4426950408889634f));
    unsigned la = l16, lw = wave * 8192 + l16, sr = l4 * 8192 + ((l15 >> 2) * 16 + l15) * 16 + (l15 & 3) * 4, budget = 1u << 18;
    wait_vm<0>();
    STDECL;
    LDECL;
#pragma unroll 1
    for (int u = 0; u < NU; ++u) {
        const int mt = u & 1;
        const unsigned pg = pgb(u >> 1, par), c3 = D_OUT + blin(mt * (N * 32) + (nt0 >> 1) * 1024);
        unsigned oa = o3, ob = 0, so = pg + c3, sa = u + 2 * AH < NU ? pgb((u >> 1) + AH, par) + c3 : pg + PG_SS + 4096, a;
        if (WHICH == 1) {
            const int orow = u * 16 + l15 + sh;
            ob = toff(orow + AH * SEG, ocol, par);
            oa = (unsigned)orow < (unsigned)S_ ? (ob ^ 8192u) - (AH << 21) : OOB;
            if (orow + AH * SEG >= S_ + (sh < 0 ? sh : 0)) ob = OOB;
            so = sa = D_OUT;
        }
        if (mt) { ST(0); LB(u >> 1); } else { LA(); }
        ASM_ST_DECL;
#define WT_A WT_W, [la] "v"(la), [l16] "v"(l16), [oa] "v"(oa), [rs] "s"(rd), [so] "s"(so), [sa] "s"(sa)
#define WT_G [sr] "v"(sr), [kd] "s"(1.0f / DM), [ke] "s"(1e-5f), [k1] "s"(-2.0f * 0.7978845608028654f * 1.4426950408889634f * 0.044715f)
#define WT_1 WT_G, [ob] "v"(ob), [ss] "s"(pg + PG_SS + mt * 128), [sw] "s"(nt0)
#define WT_2 [ro] "v"(ro), [sx] "s"(pg + PG_XA + blin(mt * 20480 + nt0 * 512)), [ss] "s"(pg + PG_SS + mt * 128)
#define WT_C "memory", "vcc", "scc", "v160", "v161", "v162", "v163", "v164", "v165", "v166", "v167", "v168", "v169", "v170", "v171", "v172", "v173", "v174", "v175", "v176", "v177", "v178", "v179", "v180", "v181", "v182", "v183", "v184", "v185", "v186", "v187", "v188", "v189", "v190", "v191", "v192", "v193", "v194", "v195", "v204", "v205", "v206", "v207", "v208", "v209", "v210", "v211", "v212", "v213", "v214", "v215", "v252"
#define WT_LO(A, ...) asm volatile(A : "+{v[196:203]}"(cs), "+{v[216:223]}"(k8), "+{v[232:247]}"(va), "+{v[248:251]}"(vb), [n] "+s"(budget), [a] "=&s"(a) ASM_ST_OUT ASM_ST2_OUT ASM_ST3_OUT \
                              : WT_A, __VA_ARGS__, [lw] "v"(lw), [i0] "s"(in0(u)), [i1] "s"(in1(u)), [n0] "s"(in0(u + 1)), [n1] "s"(in1(u + 1)) : WT_C)
#define WT_HI(A, ...) asm volatile(A : "+{v[196:203]}"(cs), "+{v[216:223]}"(k8) ASM_ST_OUT ASM_ST3_OUT : WT_A, __VA_ARGS__ : WT_C)
        if constexpr (LO) { if constexpr (WHICH == 1) WT_LO(ASM_WT1_LO, WT_1); else if constexpr (WHICH == 2) WT_LO(ASM_WT2_LO, WT_2); else WT_LO(ASM_WT3_LO, WT_G); }
        else { if constexpr (WHICH == 1) WT_HI(ASM_WT1_HI, WT_1); else if constexpr (WHICH == 2) WT_HI(ASM_WT2_HI, WT_2); else WT_HI(ASM_WT3_HI, WT_G); }
        la ^= 32768u; lw ^= 32768u; sr ^= 32768u;
        PMARK(!L && mt == (WHICH != 1) && (PMK == 1 || WHICH == 3), PMK == 1 ? 1 + WHICH : 1, (u >> 1) - (WHICH == 1));
    }
    wait_vm<0>();
    if constexpr (LO) asm volatile("" :: "{v[232:247]}"(va), "{v[248:251]}"(vb));
    LF();
    };
    if (wave < 4) run(std::true_type{}); else run(std::false_type{});
}

__device__ __forceinline__ void w4_role(const Params& P, int wave, int L, int cu, int x) {
    constexpr unsigned W_FL = 122880;
    const int ln = lane_id(), l15 = ln & 15, l4 = ln >> 4;
    const unsigned l16 = ln * 16;
    const rsrc_t rd = mkrsrc(P.ws + PG0, PG_L);
    lds_st<unsigned>(W_FL + wave * 4, 0u);
    STDECL;
    if (wave < 6) {
        u32x32 w[5];
        {
            u32x4 g[40];
            load_wn<5, 8, DM>(g, P.w[7], FF, wave, cu * 5, wave * 8);
#pragma unroll
            for (int n = 0; n < 5; ++n) {
                u32x8 f[4];
#pragma unroll
                for (int i = 0; i < 4; ++i) f[i] = __builtin_shufflevector(g[n * 8 + 2 * i], g[n * 8 + 2 * i + 1], 0, 1, 2, 3, 4, 5, 6, 7);
                w[n] = cat32(f[0], f[1], f[2], f[3]);
            }
            asm volatile("" : "+{v[0:31]}"(w[0]), "+{v[32:63]}"(w[1]), "+{v[64:95]}"(w[2]), "+{v[96:127]}"(w[3]), "+{v[128:159]}"(w[4]));
        }
        PLACE();
        bar();
        unsigned budget = 1u << 18;
        u32x32 av;
        asm volatile("v_mov_b32 v160, -1\n\tv_mov_b32 v164, -1\n\tv_mov_b32 v168, -1\n\tv_mov_b32 v172, -1\n\tv_mov_b32 v176, -1\n\tv_mov_b32 v180, -1\n\tv_mov_b32 v184, -1\n\tv_mov_b32 v188, -1" : "={v[160:191]}"(av));
        for (int u = 0; u < 2 * NSEG; ++u) {
            if (u & 1) ST(0);
            for (int spins = 0;; ++spins) {
                asm volatile("" ::: "memory");
                if ((int)__builtin_amdgcn_readfirstlane(lds_ld<unsigned>(W_FL + 24 + (u & 1) * 4)) + 3 >= u) break;
                __builtin_amdgcn_s_sleep(2);
                if (spin_fail(spins, P)) break;
            }
            const unsigned gb = PG_H2 + pgb(u >> 1, par) + ((u & 1) * 6 + wave) * BLK, gn = PG_H2 + pgb((u + 1) >> 1, par) + ((~u & 1) * 6 + wave) * BLK;
            ASM_ST_DECL;
            asm volatile(ASM_W4_M : [n] "+s"(budget), "+{v[160:191]}"(av) ASM_ST2_OUT
                         : "{v[0:31]}"(w[0]), "{v[32:63]}"(w[1]), "{v[64:95]}"(w[2]), "{v[96:127]}"(w[3]), "{v[128:159]}"(w[4]),
                           [ld] "v"(l16 + wave * 1024 + (u & 3) * 30720), [l16] "v"(l16), [rs] "s"(rd), [s0] "s"(gb), [s1] "s"(gb + 4096), [n0] "s"(gn), [n1] "s"(gn + 4096), [un] "s"(u + 1), [fl] "v"(W_FL + wave * 4)
                         : "memory", "vcc", "scc", "v192", "v193", "v194", "v195", "v196", "v197", "v198", "v199", "v200", "v201", "v202", "v203", "v204", "v205", "v206", "v207", "v208", "v209", "v210", "v211", "v212");
            PMARK(!L && (u & 1) && PMK == 2, 2, u >> 1);
        }
        asm volatile("s_waitcnt vmcnt(0)" :: "{v[160:191]}"(av) : "memory");
    } else {
        const int mt = wave - 6;
        PLACE();
        bar();
        const unsigned c0 = mt * 20480 + cu * 2560, o8 = (l4 >> 1) * 256 + l15 * 16 + (l4 & 1) * 8;
        unsigned budget = 1u << 18;
        u32x16 ra;
        u32x4 rb;
        asm volatile("v_mov_b32 v164, -1" : "={v[164:179]}"(ra), "={v[180:183]}"(rb));
        __builtin_amdgcn_s_setprio(W4P);
#define W4F_OUT "+{v[164:179]}"(ra), "+{v[180:183]}"(rb), [n] "+s"(budget)
#define W4F_IN [o0] "v"(o8 + blin(c0)), [o1] "v"(o8 + blin(c0 + 512)), [o2] "v"(o8 + blin(c0 + 1024)), [o3] "v"(o8 + blin(c0 + 1536)), [o4] "v"(o8 + blin(c0 + 2048)), [rd] "s"(rd), \
               [c1] "s"(PG_X1 + pg), [ca] "s"(PG_XA + pg), [n1] "s"(PG_X1 + pn), [na] "s"(PG_XA + pn), [f0] "v"(W_FL + (ln & 3) * 4), [f1] "v"(W_FL + 16 + (ln & 1) * 4), [fd] "v"(W_FL + wave * 4), \
               [la] "v"((u & 3) * 30720 + l16), [u] "s"(u), [un] "s"(u + 1)
#define W4F_CLOB "memory", "vcc", "scc", "v64", "v65", "v66", "v67", "v68", "v69", "v70", "v71", "v72", "v73", "v74", "v75", "v76", "v77", "v78", "v79", "v80", "v81", "v82", "v83", "v84", "v85", "v86", "v87", "v88", "v89", "v90", "v91", "v92", "v93", "v94", "v95", "v96", "v97", "v98", "v99", "v100", "v101", "v102", "v103", "v104", "v105", "v106", "v107", "v108", "v109", "v110", "v111", "v112", "v113", "v114", "v115", "v116", "v117", "v118", "v119", "v120", "v121", "v122", "v123", "v124", "v125", "v126", "v127", "v128", "v129", "v130", "v131", "v132", "v133", "v134", "v135", "v136", "v137", "v138", "v139", "v140", "v141", "v142", "v143", "v144", "v145", "v146", "v147", "v148", "v149", "v150", "v151", "v152", "v153", "v154", "v155", "v156", "v157", "v158", "v159", "v160", "v161", "v162", "v163", "v184", "v185", "v186", "v187", "v188", "v189", "v190", "v191", "v192", "v193", "v194", "v195", "v196", "v197", "v198", "v199", "v200"
        if (L) {
            const rsrc_t ro = mkrsrc(P.out, ACT);
#pragma unroll 1
            for (int t = 0; t < NSEG; ++t) {
                const unsigned u = t * 2 + mt, pg = pgb(t, par), pn = pgb(t + 1, par);
                ASM_ST_DECL;
                asm volatile(ASM_W4F_1 : W4F_OUT ASM_ST2_OUT : W4F_IN, [pa] "v"(l15 * (DM * 2) + (l4 & 1) * 32 + (l4 >> 1) * 16), [pb] "v"(l15 * (DM * 2) + l4 * 8), [ro] "s"(ro), [so] "s"(u * 20480 + cu * 160) : W4F_CLOB);
            }
        } else {
            const rsrc_t rn = mkrsrc(P.ws + (par ^ 1) * 8192, RG_L);
#pragma unroll 1
            for (int t = 0; t < NSEG; ++t) {
                const unsigned u = t * 2 + mt, pg = pgb(t, par), pn = pgb(t + 1, par), sn = (pg ^ 8192) + PG_LX, st = RG_ST + (u >> 3) * BLK + (u & 7) * 1024;
                ASM_ST_DECL;
                asm volatile(ASM_W4F_0 : W4F_OUT ASM_ST_OUT ASM_ST2_OUT ASM_ST3_OUT
                             : W4F_IN, [pa] "v"(blin(c0 + (l4 & 1) * 512) + (l4 >> 1) * 256 + l15 * 16), [pb] "v"(blin(c0 + 1024 + (l4 & 1) * 512) + (l4 >> 1) * 256 + l15 * 16),
                               [so] "v"(ln < 16 ? ln * 8 : OOB), [si] "v"(ln * 8), [rr] "s"(rr), [sp] "s"(st + cu * 128), [sq] "s"(st), [xr] "s"(PG_XR + sn), [xn] "s"(PG_XN + sn),
                               [kd] "s"(1.0f / DM), [ke] "s"(1e-5f), [tn] "s"(t + 1), [fo] "v"(ln < 32 ? (ln >> 3) * 2048 + (ln & 7) * 16 : OOB), [rn] "s"(rn), [fs] "s"(RG_FX + (cu * 2 + mt) * 128) : W4F_CLOB);
            }
        }
        asm volatile("s_waitcnt vmcnt(0)" :: "{v[164:179]}"(ra), "{v[180:183]}"(rb) : "memory");
    }
}

__device__ __forceinline__ unsigned probe(char* ws, unsigned base, unsigned bit, int n) {
    const rsrc_t r = mkrsrc(ws, RG_SLOT);
    unsigned m0 = ~0u, m1 = ~0u;
    for (int i = 0; i < n; ++i) {
        const unsigned long long t0 = __builtin_amdgcn_s_memtime();
        __builtin_amdgcn_raw_buffer_store_b32(0u, r, 96 * BLK + base + (i & 1) * bit + ((i >> 2) & 1) * 4096 + (i >> 2) * 768, 0, 16);
        asm volatile("s_waitcnt vmcnt(0)" ::: "memory");
        const unsigned d = (unsigned)(__builtin_amdgcn_s_memtime() - t0);
        if (i & 1) m1 = d < m1 ? d : m1; else m0 = d < m0 ? d : m0;
    }
    return m0 > m1 ? 1u : 0u;
}
__device__ __forceinline__ void setup(const Params& P, int wave, int mt, int par0, int ph) {
    const bool xw = wave < 4 && (mt >= 2 * XS0) == (ph != 0);
    const int tid = threadIdx.x & 255, r15 = tid & 15, slot = tid >> 4;
    const rsrc_t rw = mkrsrc(P.ws, WS_END);
    u32x4 v[5];
    float s = 0.f, q = 0.f;
    if (xw) {
#pragma unroll
        for (int c = 0; c < 5; ++c) v[c] = *(const u32x4*)(P.x + (size_t)(mt * 16 + r15) * DM + (slot * 5 + c) * 8);
#pragma unroll
        for (int c = 0; c < 5; ++c)
#pragma unroll
            for (int e = 0; e < 4; ++e) s += lo2f(v[c][e]) + hi2f(v[c][e]);
        lds_st<float>((slot * 17 + r15) * 4, s);
    } else if (wave >= 4) {
        constexpr unsigned N0 = RG_ST / BLK, N1 = 4 * N0 + 16, N2 = PG_SS / BLK;
        const u32x4 s4 = sent4();
        for (unsigned k = mt; k < 2 * (N1 + 4 * N2); k += 128) {
            const unsigned i = k >> 1;
            const unsigned o = i < N1 ? (i < 4 * N0 ? (i / N0) * RG_SLOT + (i % N0) * BLK : (i - 3 * N0) * BLK) + (par0 ^ ph) * 8192 : PG0 + pgb((i - N1) / N2, par0 ^ ph) + ((i - N1) % N2) * BLK;
            st128(s4, rw, tid * 16, o + (k & 1) * 4096);
        }
        if (ph == 0 && mt >= 108) st128(~s4, rw, tid * 16, RG_FV + (mt - 108) * 4096);
    }
    bar();
    float mean = 0.f;
    if (xw) {
        s = 0.f;
#pragma unroll
        for (int k = 0; k < 16; ++k) s += lds_ld<float>((k * 17 + r15) * 4);
        mean = s * (1.0f / DM);
#pragma unroll
        for (int c = 0; c < 5; ++c)
#pragma unroll
            for (int e = 0; e < 4; ++e) { const float a = lo2f(v[c][e]) - mean, b = hi2f(v[c][e]) - mean; q += a * a + b * b; }
        lds_st<float>(2048 + (slot * 17 + r15) * 4, q);
    }
    bar();
    if (xw) {
        q = 0.f;
#pragma unroll
        for (int k = 0; k < 16; ++k) q += lds_ld<float>(2048 + (k * 17 + r15) * 4);
        const float m1 = rbf(mean), r = 1.0f / ln_den(q * (1.0f / DM));
#pragma unroll
        for (int c = 0; c < 5; ++c) {
            const unsigned o = toff(mt * 16 + r15, (slot * 5 + c) * 8, par0);
            st128(v[c], rw, o, PG0 + PG_XR);
            u32x4 n;
#pragma unroll
            for (int e = 0; e < 4; ++e) n[e] = pack2(rbf(lo2f(v[c][e]) - m1) * r, rbf(hi2f(v[c][e]) - m1) * r);
            st128(n, rw, o, PG0 + PG_XN);
        }
    }
    if (xw || wave >= 4) wait_vm<0>();
    bar();
    if (threadIdx.x < 8) __hip_atomic_fetch_add((unsigned*)(P.ws + FL0 + FL_IN + (ph * 8 + threadIdx.x) * 128), 1u, RLX_AGENT);
}

__global__ __launch_bounds__(512, 2) void mega_kernel(Params P) {
    const unsigned t0_ = (unsigned)__builtin_amdgcn_s_memrealtime();
    const int wave = __builtin_amdgcn_readfirstlane(threadIdx.x >> 6);
    u32x4 cpf;
    {
        unsigned long long pc;
        unsigned len;
        asm volatile("s_getpc_b64 %0\n\ts_mov_b32 %1, _Z8code_endv-_Z11mega_kernel6Params" : "=s"(pc), "=s"(len));
        cpf = __builtin_amdgcn_raw_buffer_load_b128(mkrsrc((const void*)(pc & ~255ull), len), (blockIdx.x >> 3) * 8192 + threadIdx.x * 16, 0, 0);
    }
    {
        const rsrc_t rf = mkrsrc(P.ws + FL0, FL_BYTES);
        if (blockIdx.x == 0) {
            unsigned h = 0;
            if (wave == 7) h = (probe(P.ws, 0, 8192, CAL1) ^ ((unsigned)__builtin_amdgcn_s_getreg((3 << 11) | 20) >> 2)) & 1u;
            if (wave == 0) {
                const unsigned l16 = lane_id() * 16;
                u32x4 z = {0u, 0u, 0u, 0u};
                asm volatile("" : "+v"(z));
#pragma unroll
                for (int k = 0; k < 4; ++k) st128(z, rf, k * 1024 + l16 < FL_HB ? k * 1024 + l16 : OOB, 0);
                wait_vm<0>();
                for (unsigned c = 1; c < 200000u; ++c) {
                    __builtin_amdgcn_raw_buffer_store_b32(c, rf, l16 < 128 ? l16 * 8 : OOB, FL_HB, 16);
                    unsigned n = __builtin_amdgcn_raw_buffer_load_b32(rf, (l16 & 127u) * 8, FL_TK, 16), sum = 0;
                    n = n < 32u ? n : 32u;
#pragma unroll
                    for (int k = 0; k < 8; ++k) sum += __builtin_amdgcn_readlane(n, k);
                    if (sum >= 255u) break;
                }
            }
            bar();
            if (wave == 7 && lane_id() < 16) __hip_atomic_fetch_add((unsigned*)(P.ws + FL0 + FL_IN + (threadIdx.x & 15) * 128), (2u + h) << 16, RLX_AGENT);
        } else if (wave == 0) {
            unsigned o;
            asm volatile("v_mov_b32 %0, 0" : "=v"(o));
            const unsigned v0 = __builtin_amdgcn_readfirstlane(__builtin_amdgcn_raw_buffer_load_b32(rf, o, FL_HB + (blockIdx.x & 7) * 128, 16));
            for (int spins = 0; spins < 400000; ++spins) {
                asm volatile("" ::: "memory");
                if (__builtin_amdgcn_readfirstlane(__builtin_amdgcn_raw_buffer_load_b32(rf, o, FL_HB + (blockIdx.x & 7) * 128, 16)) != v0) break;
                __builtin_amdgcn_s_sleep(1);
            }
            asm volatile("" ::: "memory");
        }
    }
    if (threadIdx.x == 0) {
        unsigned g = (unsigned)__builtin_amdgcn_s_getreg((3 << 11) | 20) & 7u, k;
        for (int n = 0; (k = __hip_atomic_fetch_add((unsigned*)(P.ws + FL0 + FL_TK + g * 128), 1u, RLX_AGENT)) >= 32u && n < 8; ++n) g = (g + 1) & 7u;
        lds_st<unsigned>(0, g << 5 | (k & 31u));
    }
    bar();
    const unsigned tk = __builtin_amdgcn_readfirstlane(lds_ld<unsigned>(0));
    asm volatile("" :: "v"(cpf));
    bar();
    const int g4 = (tk >> 5) & 3, k = tk & 31;
    const int L = tk >> 7, x = g4, r = k;
    if (L) {
        const int par0 = (init_wait(P, wave, 1, 0x20000u) >> 16) & 1;
        if (tk == 254 && wave == 0) {
            const unsigned xc = (unsigned)__builtin_amdgcn_s_getreg((3 << 11) | 20);
            const unsigned f = (probe(P.ws, ((par0 ^ (xc >> 2)) & 1) * 8192, BLK, CAL2) ^ 1u ^ xc) & 1u;
            if (threadIdx.x < 16) __hip_atomic_fetch_add((unsigned*)(P.ws + FL0 + FL_IN + threadIdx.x * 128), (2u + f) << 20, RLX_AGENT);
        }
        if (MPF && wave < 4 && k >= 4 && tk != 254) {
            const int wid = (k - 4) * 4 + g4;
            const rsrc_t rs = mkrsrc(wave == 0 ? P.w[0] : wave == 1 ? P.w[1] : wave == 2 ? P.w[2] : P.w[3], DM * HD * 2);
            const unsigned l16 = lane_id() * 16;
#pragma unroll 1
            for (int q = 0; q < MPF; ++q) {
                unsigned u = 7679 - (q * 112 + wid);
                if (wave == 1 && u < 6144) u = u < 3072 ? u + 3072 : u - 3072;
                asm volatile("buffer_load_dwordx4 v[252:255], %0, %1, %2 offen" :: "v"(l16), "s"(rs), "s"(u * 1024) : "memory", "v252", "v253", "v254", "v255");
            }
            if (MPW) {
                const rsrc_t rm = mkrsrc(wave == 0 ? P.w[4] : wave == 1 ? P.w[5] : wave == 2 ? P.w[6] : P.w[7], (wave < 2 ? DM : FF) * DM * 2);
#pragma unroll 1
                for (unsigned u = wid; u < (wave < 2 ? 800u : 1920u); u += 112)
                    asm volatile("buffer_load_dwordx4 v[252:255], %0, %1, %2 offen" :: "v"(l16), "s"(rm), "s"(u * 1024) : "memory", "v252", "v253", "v254", "v255");
            }
        }
#pragma unroll 1
        for (int ph = 0; ph < 2; ++ph) {
            setup(P, wave, (tk & 31) * 4 + g4, par0, ph);
            for (int k = ph * DLY_L; k < DLY_L; ++k) __builtin_amdgcn_s_sleep(32);
            wait_vm<0>();
        }
    }
    {
        const int i = (r - 26) * 4 + x;
        const int d = (r < 8 ? DLY_S : r < 21 ? 0 : i < 4 ? DLY_H : DLY_W + DLY_X * (i < 7 ? 0 : i < 10 ? 1 : i < 16 ? 2 : 3));
        for (int k = 0; k < d; ++k) __builtin_amdgcn_s_sleep(32);
    }
    if (r < 8) s_role(P, wave, L, x * 8 + r, x, t0_);
    else if (r < 16) v_role(P, wave ^ 4, L, x * 8 + r - 8, x);
    else if (r < 21) r_role(P, wave, L, r - 16, x);
    else {
        const int i = (r - 26) * 4 + x;
        if (i < 4) h_role(P, wave, L, r < 25 ? x * 4 + r - 21 : 16 + x * 2 + r - 25, x);
        else if (i < 7) w_role<1>(P, wave, L, i - 4, x);
        else if (i < 10) w_role<2>(P, wave, L, i - 7, x);
        else if (i < 16) w_role<3>(P, wave, L, i - 10, x);
        else w4_role(P, wave, L, i - 16, x);
    }
}

__global__ void code_end() {}

extern "C" void kernel_launch(void* const* d_in, const int* in_sizes, int n_in, void* d_out, int out_size, void* d_ws,
                              size_t ws_size, hipStream_t stream) {
    char* ws = (char*)d_ws;
    Params P;
    P.ws = ws; P.x = (const bf16_t*)d_in[0]; P.mem0 = (const bf16_t*)d_in[1]; P.out = (bf16_t*)d_out;
    for (int i = 0; i < 8; ++i) P.w[i] = (const bf16_t*)d_in[i < 4 ? 2 + i : 4 + i];
    (void)hipFuncSetAttribute((const void*)mega_kernel, hipFuncAttributeMaxDynamicSharedMemorySize, MEGA_LDS);
    mega_kernel<<<256, 512, MEGA_LDS, stream>>>(P);
}
```

```cpp
#include <hip/hip_runtime.h>
#include <stdint.h>
#include <type_traits>

typedef unsigned short bf16_t;
using bf16x2 = __attribute__((ext_vector_type(2))) __bf16;
using bf16x8 = __attribute__((ext_vector_type(8))) __bf16;
using bf16x16 = __attribute__((ext_vector_type(16))) __bf16;
using f32x2 = __attribute__((ext_vector_type(2))) float;
using f32x4 = __attribute__((ext_vector_type(4))) float;
using u32x2 = __attribute__((ext_vector_type(2))) unsigned;
using u32x4 = __attribute__((ext_vector_type(4))) unsigned;
typedef __amdgpu_buffer_rsrc_t rsrc_t;

__device__ __forceinline__ unsigned pack2(float a, float b) {
    bf16x2 r = __builtin_convertvector((f32x2){a, b}, bf16x2);
    return __builtin_bit_cast(unsigned, r);
}
__device__ __forceinline__ float rbf(float f) { return __uint_as_float(pack2(f, 0.f) << 16); }
__device__ __forceinline__ float lo2f(unsigned u) { return __uint_as_float(u << 16); }
__device__ __forceinline__ float hi2f(unsigned u) { return __uint_as_float(u & 0xFFFF0000u); }
__device__ __forceinline__ rsrc_t mkrsrc(const void* p, unsigned bytes) {
    return __builtin_amdgcn_make_buffer_rsrc((void*)p, 0, bytes, 0x00020000);
}

constexpr int S_ = 2048, DM = 640, NH = 32, DH = 192, HD = NH * DH  , SEG = 32, NSEG = 64;
constexpr int FF = 1536;

#define XMAP
#define NCPY 4
constexpr unsigned NSLOT = 4, NCP = NCPY;
#define FL_TK_DEF
#define DLY_L 10
#define DLY_S 0
#define DLY_W 10
#define DLY_H 3
#define EWAKE "s_nop 0"
#define LWAKE "s_wakeup\n\t"
#define SLP4 (wave == 4 ? t : 0)
#define WPF 12
#define WPRI 1
#define WPRI2 0
#define RTD 3500
#define VTD 3900
#define HAH 100
#define WEAK 0
#define BAL 0
#define HSL 4
#define OAUX 2
#define W4P 3
#define STD 3800
#define CAL1 6
#define CAL2 12
#define DLY_X 2
#define NTM 10
#define HTRY 1
#define CALN 12
#define RSL 9
#define VTRY 0
#define VSL 1
#define NRR 5
constexpr unsigned BLK = 16384, RG_SLOT = 4u << 20;
constexpr unsigned XP_PAIR = 6 * BLK, XP_SLOT = RG_SLOT;
constexpr unsigned XM_PAIR = 4 * BLK, XM_SLOT = RG_SLOT;
constexpr unsigned VM_SLOT = RG_SLOT;
constexpr unsigned PX_SLOT = RG_SLOT;
constexpr unsigned RG_XP = 0, RG_XM = 12 * BLK, RG_VM = 20 * BLK, RG_PX = 60 * BLK;
constexpr unsigned RG_ST = 68 * BLK, RG_FV = 84 * BLK, RG_FH = 86 * BLK, RG_FX = 88 * BLK, RG_L = NSLOT * RG_SLOT;
constexpr unsigned FL0 = 89 * BLK, FL_TK = 512, FL_IN = FL_TK + 8 * 128, FL_HB = FL_IN + 16 * 128, FL_BYTES = FL_HB + 8 * 128;
constexpr unsigned ACT = S_ * DM * 2;
#define PXROT 5
#define W4SL 5
#define RXDEL 1
#define VXD 32
#define VXAUX 16
constexpr unsigned PG0 = RG_L, PG_L = 80u << 21;
constexpr unsigned PG_XA = 0, PG_HS = 5 * BLK, PG_X1 = 10 * BLK, PG_H2 = 15 * BLK, PG_SS = 27 * BLK, PG_XR = 28 * BLK, PG_XN = 33 * BLK, PG_LX = 10 * BLK, PG_HV = 48 * BLK;
__device__ __forceinline__ unsigned pgb(unsigned p, int par) { return p << 21 | ((p ^ par) & 1) << 13; }
__device__ __forceinline__ constexpr unsigned blin(unsigned o) { return o + (o & ~8191u); }
constexpr int HT0 = 2;
constexpr int AH = 3;
constexpr unsigned WS_END = PG0 + PG_L;

#define SB() __builtin_amdgcn_sched_barrier(0)
#define LAS __attribute__((address_space(3)))
template <class T> __device__ __forceinline__ T lds_ld(unsigned a) { return *(const LAS T*)(a); }
template <class T> __device__ __forceinline__ void lds_st(unsigned a, T v) { *(LAS T*)(a) = v; }
constexpr unsigned OOB = 0x7FFFFF00u;
template <int N> __device__ __forceinline__ void wait_vm() { asm volatile("s_waitcnt vmcnt(%0)" :: "i"(N) : "memory"); }
__device__ __forceinline__ void bar() {
    asm volatile("s_waitcnt lgkmcnt(0)" ::: "memory");
    __builtin_amdgcn_s_barrier();
    asm volatile("" ::: "memory");
}
__device__ __forceinline__ int lane_id() {
    int ln;
    asm volatile("v_mbcnt_lo_u32_b32 %0, -1, 0\n\tv_mbcnt_hi_u32_b32 %0, -1, %0" : "=v"(ln));
    return ln;
}
__device__ __forceinline__ void swap16(unsigned& a, unsigned& b) { asm volatile("s_nop 3\n\tv_permlane16_swap_b32 %0, %1\n\ts_nop 1" : "+v"(a), "+v"(b)); }
__device__ __forceinline__ void swap32(unsigned& a, unsigned& b) { asm volatile("s_nop 3\n\tv_permlane32_swap_b32 %0, %1\n\ts_nop 1" : "+v"(a), "+v"(b)); }
__device__ __forceinline__ float sum_x16(float x) { unsigned a = __float_as_uint(x), b = a; swap16(a, b); return __uint_as_float(a) + __uint_as_float(b); }
__device__ __forceinline__ float sum_x32(float x) { unsigned a = __float_as_uint(x), b = a; swap32(a, b); return __uint_as_float(a) + __uint_as_float(b); }
__device__ __forceinline__ float max_x16(float x) { unsigned a = __float_as_uint(x), b = a; swap16(a, b); return __builtin_fmaxf(__uint_as_float(a), __uint_as_float(b)); }
__device__ __forceinline__ float max_x32(float x) { unsigned a = __float_as_uint(x), b = a; swap32(a, b); return __builtin_fmaxf(__uint_as_float(a), __uint_as_float(b)); }
__device__ __forceinline__ void sum2_rows(float& s, float& q) {
    unsigned a = __float_as_uint(s), b = a, c = __float_as_uint(q), d = c;
    asm volatile("s_nop 2\n\tv_permlane16_swap_b32 %0, %1\n\tv_permlane16_swap_b32 %2, %3\n\tv_add_f32 %0, %0, %1\n\tv_add_f32 %2, %2, %3\n\tv_mov_b32 %1, %0\n\tv_mov_b32 %3, %2\n\t"
                 "s_nop 1\n\tv_permlane32_swap_b32 %0, %1\n\tv_permlane32_swap_b32 %2, %3\n\tv_add_f32 %0, %0, %1\n\tv_add_f32 %2, %2, %3" : "+v"(a), "+v"(b), "+v"(c), "+v"(d));
    s = __uint_as_float(a); q = __uint_as_float(c);
}
__device__ __forceinline__ u32x4 merge16(u32x2 a, u32x2 b) {
    unsigned a0 = a[0], a1 = a[1], b0 = b[0], b1 = b[1];
    asm volatile("s_nop 3\n\tv_permlane16_swap_b32 %0, %2\n\tv_permlane16_swap_b32 %1, %3\n\ts_nop 1" : "+v"(a0), "+v"(a1), "+v"(b0), "+v"(b1));
    return (u32x4){a0, a1, b0, b1};
}
template <int AUXV>
__device__ __forceinline__ void st128_(u32x4 v, rsrc_t r, unsigned voff, unsigned soff) {
    __builtin_amdgcn_raw_buffer_store_b128(v, r, voff, soff, AUXV);
    asm volatile("s_nop 1" :: "v"(v) : "memory");
}
#define st128(V, R, VO_, SO_) st128_<16>(V, R, VO_, SO_)
#define LD128(R, VO_, SO_) __builtin_amdgcn_raw_buffer_load_b128(R, VO_, SO_, 16)
#define LD64(R, VO_, SO_) __builtin_amdgcn_raw_buffer_load_b64(R, VO_, SO_, 16)
#define BF8(X) __builtin_bit_cast(bf16x8, X)
__device__ __forceinline__ float gelu_tanh(float x) {
    const float c0 = -2.0f * 0.7978845608028654f * 1.4426950408889634f, c1 = c0 * 0.044715f;
    const float e = __builtin_amdgcn_exp2f(x * __builtin_fmaf(x * x, c1, c0));
    return x * __builtin_amdgcn_rcpf(1.0f + e);
}
#define MFMA(A_, B_, C_) C_ = __builtin_amdgcn_mfma_f32_16x16x32_bf16(A_, B_, C_, 0, 0, 0)
#define SMF(A_, B_, C_) C_ = __builtin_amdgcn_smfmac_f32_16x16x64_bf16(A_, B_, C_, sidx, 0, 0)
__device__ __forceinline__ float ln_den(float var) { return rbf(__builtin_amdgcn_sqrtf(rbf(rbf(var) + 1e-5f))); }
__device__ __forceinline__ float rcp(float x) { return __builtin_amdgcn_rcpf(x); }

struct Params {
    char* ws;
    const bf16_t* x;
    const bf16_t* mem0;
    const bf16_t* w[8];
    bf16_t* out;
};

#define RLX_AGENT __ATOMIC_RELAXED, __HIP_MEMORY_SCOPE_AGENT
#define SENT 0xFFFFFFFFu
constexpr int SPIN_CAP = 400000;
__device__ __forceinline__ bool spin_fail(int spins, const Params& P) {
    if ((spins & 1023) == 1023) {
        const rsrc_t rt = mkrsrc(P.ws + FL0, FL_BYTES);
        unsigned o;
        asm volatile("v_mov_b32 %0, 0x100" : "=v"(o));
        if (__builtin_amdgcn_readfirstlane(__builtin_amdgcn_raw_buffer_load_b32(rt, o, 0, 16)) != 0u) return true;
        if (spins > SPIN_CAP) { __builtin_amdgcn_raw_buffer_store_b32(o, rt, o, 0, 16); return true; }
    }
    return false;
}
__device__ __forceinline__ bool ok4(u32x4 v) { return v[0] != SENT && v[1] != SENT && v[2] != SENT && v[3] != SENT; }
__device__ __forceinline__ unsigned nosent(unsigned d) { return d < 0xFFFFFFFEu ? d : 0xFFFFFFFEu; }
__device__ __forceinline__ u32x4 nosent4(u32x4 v) { return (u32x4){nosent(v[0]), nosent(v[1]), nosent(v[2]), nosent(v[3])}; }
__device__ __forceinline__ unsigned umax3(unsigned a, unsigned b, unsigned c) { const unsigned m = a > b ? a : b; return m > c ? m : c; }
__device__ __forceinline__ unsigned umax4(unsigned m, u32x4 v) { return umax3(umax3(m, v[0], v[1]), v[2], v[3]); }
__device__ __forceinline__ u32x4 sent4() { unsigned sv; asm volatile("v_mov_b32 %0, -1" : "=v"(sv)); return (u32x4){sv, sv, sv, sv}; }
template <int N>
__device__ __forceinline__ void poll(rsrc_t rs, unsigned base, const unsigned (&off)[N], u32x4 (&v)[N], const Params& P) {
    for (int spins = 0;; ++spins) {
        unsigned m = 0;
#pragma unroll
        for (int k = 0; k < N; ++k) v[k] = LD128(rs, off[k], base);
#pragma unroll
        for (int k = 0; k < N; ++k) m = umax4(m, v[k]);
        if (__all(m != SENT)) return;
        asm volatile("" ::: "memory");
        if (spin_fail(spins, P)) return;
    }
}
__device__ __forceinline__ bf16x16 cat16(bf16x8 a, bf16x8 b) {
    return __builtin_shufflevector(a, b, 0, 1, 2, 3, 4, 5, 6, 7, 8, 9, 10, 11, 12, 13, 14, 15);
}
__device__ __forceinline__ bf16x8 half8(bf16x16 v, int h) {
    return h ? __builtin_shufflevector(v, v, 8, 9, 10, 11, 12, 13, 14, 15) : __builtin_shufflevector(v, v, 0, 1, 2, 3, 4, 5, 6, 7);
}
__device__ __forceinline__ bf16x16 lds_frag(unsigned a, unsigned step) { return cat16(lds_ld<bf16x8>(a), lds_ld<bf16x8>(a + step)); }
template <int I>
__device__ __forceinline__ void dmaq(const char* g, unsigned l16, unsigned dst) {
    asm volatile("s_mov_b32 m0, %2\n\ts_nop 0\n\tglobal_load_lds_dwordx4 %0, %1 offset:%3 sc1" :: "v"(l16), "s"(g), "s"(dst), "i"(I * 1024) : "memory");
}
__device__ __forceinline__ void dma4(const char* g, unsigned l16, unsigned dst) {
    asm volatile("s_mov_b32 m0, %2\n\ts_nop 0\n\tglobal_load_lds_dwordx4 %0, %1 sc1\n\tglobal_load_lds_dwordx4 %0, %1 offset:1024 sc1\n\t"
                 "global_load_lds_dwordx4 %0, %1 offset:2048 sc1\n\tglobal_load_lds_dwordx4 %0, %1 offset:3072 sc1" :: "v"(l16), "s"(g), "s"(dst) : "memory");
}
__device__ __forceinline__ unsigned toff(int row, int col, int par) {
    return pgb(row >> 5, par) + blin((unsigned)((row >> 4) & 1) * 20480u + (unsigned)(col >> 3) * 256u) + (unsigned)(row & 15) * 16u;
}

#define ASM_ST
#define ASM_ST_DECL
#define ASM_ST_OUT
#define ASM_ST_SAVE(k)
#define ASM_ST2
#define ASM_ST3
#define ASM_ST3_OUT
#define ASM_ST3_SAVE(k)
#define ASM_ST2_OUT
#define ASM_ST2_SAVE(k)
#define STDECL
#define ST(k)
#define STC(k)
#define STFLUSH(t)

#define MARK(c, k)
#define MARKX(c, k)
#define PMARK(c, st, seg)
#define LDECL
#define LA()
#define LB(t)
#define LF()
#define STROLE 3
constexpr unsigned L_MEM = 0;
constexpr unsigned L_Q = 10240;
constexpr unsigned L_KM = 13312;
constexpr unsigned L_QX = 16384;
constexpr unsigned L_KX = 28672;
constexpr unsigned L_XB = 40960;
constexpr unsigned L_XA = 49152;
constexpr unsigned L_W = 81920;
constexpr int MEGA_LDS = 163840;
constexpr int wi(int i, int j) { return (i >> 1) * 5 + ((i & 1) ? 2 + j : j - 1); }

#define RPUB2
#define WNT ""
#define PRIUP "s_setprio 2\n\t"
#define PRISM PRIUP
#define STR2(x) #x
#define STRX(x) STR2(x)
#define HSLEEP "s_sleep " STRX(HSL) "\n\t"
#define W4SLEEP "s_sleep " STRX(W4SL) "\n\t"
#define PLAY 0
#define V1A 0
#define ASM_WLOAD3 \
    "s_xor_b32 %[t], %[wx], 0\n\t" \
    "s_mul_i32 %[t], %[t], 0xc0000\n\t" \
    "buffer_load_dwordx4 v[200:203], %[o0], %[rs], %[t] offen" WNT "\n\t" \
    "buffer_load_dwordx4 v[0:3], %[o1], %[rs], %[t] offen" WNT "\n\t" \
    "buffer_load_dwordx4 v[8:11], %[o2], %[rs], %[t] offen" WNT "\n\t" \
    "s_add_u32 %[t], %[t], 0x18000\n\t" \
    "buffer_load_dwordx4 v[16:19], %[o0], %[rs], %[t] offen" WNT "\n\t" \
    "buffer_load_dwordx4 v[24:27], %[o1], %[rs], %[t] offen" WNT "\n\t" \
    "buffer_load_dwordx4 v[32:35], %[o2], %[rs], %[t] offen" WNT "\n\t" \
    "s_add_u32 %[t], %[t], 0x18000\n\t" \
    "buffer_load_dwordx4 v[208:211], %[o0], %[rs], %[t] offen" WNT "\n\t" \
    "buffer_load_dwordx4 v[40:43], %[o1], %[rs], %[t] offen" WNT "\n\t" \
    "buffer_load_dwordx4 v[48:51], %[o2], %[rs], %[t] offen" WNT "\n\t" \
    "s_add_u32 %[t], %[t], 0x18000\n\t" \
    "buffer_load_dwordx4 v[56:59], %[o0], %[rs], %[t] offen" WNT "\n\t" \
    "buffer_load_dwordx4 v[64:67], %[o1], %[rs], %[t] offen" WNT "\n\t" \
    "buffer_load_dwordx4 v[72:75], %[o2], %[rs], %[t] offen" WNT "\n\t" \
    "s_add_u32 %[t], %[t], 0x18000\n\t" \
    "buffer_load_dwordx4 v[204:207], %[o0], %[rs], %[t] offen" WNT "\n\t" \
    "buffer_load_dwordx4 v[4:7], %[o1], %[rs], %[t] offen" WNT "\n\t" \
    "buffer_load_dwordx4 v[12:15], %[o2], %[rs], %[t] offen" WNT "\n\t" \
    "s_add_u32 %[t], %[t], 0x18000\n\t" \
    "buffer_load_dwordx4 v[20:23], %[o0], %[rs], %[t] offen" WNT "\n\t" \
    "buffer_load_dwordx4 v[28:31], %[o1], %[rs], %[t] offen" WNT "\n\t" \
    "buffer_load_dwordx4 v[36:39], %[o2], %[rs], %[t] offen" WNT "\n\t" \
    "s_add_u32 %[t], %[t], 0x18000\n\t" \
    "buffer_load_dwordx4 v[212:215], %[o0], %[rs], %[t] offen" WNT "\n\t" \
    "buffer_load_dwordx4 v[44:47], %[o1], %[rs], %[t] offen" WNT "\n\t" \
    "buffer_load_dwordx4 v[52:55], %[o2], %[rs], %[t] offen" WNT "\n\t" \
    "s_add_u32 %[t], %[t], 0x18000\n\t" \
    "buffer_load_dwordx4 v[60:63], %[o0], %[rs], %[t] offen" WNT "\n\t" \
    "buffer_load_dwordx4 v[68:71], %[o1], %[rs], %[t] offen" WNT "\n\t" \
    "buffer_load_dwordx4 v[76:79], %[o2], %[rs], %[t] offen" WNT "\n\t" \
    "s_add_u32 %[t], %[t], 0x18000\n\t" \
    "s_waitcnt vmcnt(21)\n\t" \
    "ds_write_b128 %[s0], v[200:203] offset:0\n\t" \
    "ds_write_b128 %[s1], v[0:3] offset:0\n\t" \
    "ds_write_b128 %[s2], v[8:11] offset:0\n\t" \
    "s_waitcnt vmcnt(18)\n\t" \
    "ds_write_b128 %[s0], v[16:19] offset:3456\n\t" \
    "ds_write_b128 %[s1], v[24:27] offset:3456\n\t" \
    "ds_write_b128 %[s2], v[32:35] offset:3456\n\t" \
    "s_waitcnt vmcnt(15)\n\t" \
    "ds_write_b128 %[s0], v[208:211] offset:6912\n\t" \
    "ds_write_b128 %[s1], v[40:43] offset:6912\n\t" \
    "ds_write_b128 %[s2], v[48:51] offset:6912\n\t" \
    "s_waitcnt vmcnt(12)\n\t" \
    "ds_write_b128 %[s0], v[56:59] offset:10368\n\t" \
    "ds_write_b128 %[s1], v[64:67] offset:10368\n\t" \
    "ds_write_b128 %[s2], v[72:75] offset:10368\n\t" \
    "s_waitcnt lgkmcnt(0)\n\t" \
    "s_barrier \n\t" \
    "ds_read_b64_tr_b16 v[200:201], %[t0] offset:0\n\t" \
    "ds_read_b64_tr_b16 v[202:203], %[t0] offset:1664\n\t" \
    "ds_read_b64_tr_b16 v[0:1], %[t0] offset:128\n\t" \
    "ds_read_b64_tr_b16 v[2:3], %[t0] offset:1792\n\t" \
    "ds_read_b64_tr_b16 v[8:9], %[t0] offset:256\n\t" \
    "ds_read_b64_tr_b16 v[10:11], %[t0] offset:1920\n\t" \
    "ds_read_b64_tr_b16 v[16:17], %[t1] offset:0\n\t" \
    "ds_read_b64_tr_b16 v[18:19], %[t1] offset:1664\n\t" \
    "ds_read_b64_tr_b16 v[24:25], %[t1] offset:128\n\t" \
    "ds_read_b64_tr_b16 v[26:27], %[t1] offset:1792\n\t" \
    "ds_read_b64_tr_b16 v[32:33], %[t1] offset:256\n\t" \
    "ds_read_b64_tr_b16 v[34:35], %[t1] offset:1920\n\t" \
    "ds_read_b64_tr_b16 v[208:209], %[t2] offset:0\n\t" \
    "ds_read_b64_tr_b16 v[210:211], %[t2] offset:1664\n\t" \
    "ds_read_b64_tr_b16 v[40:41], %[t2] offset:128\n\t" \
    "ds_read_b64_tr_b16 v[42:43], %[t2] offset:1792\n\t" \
    "ds_read_b64_tr_b16 v[48:49], %[t2] offset:256\n\t" \
    "ds_read_b64_tr_b16 v[50:51], %[t2] offset:1920\n\t" \
    "ds_read_b64_tr_b16 v[56:57], %[t3] offset:0\n\t" \
    "ds_read_b64_tr_b16 v[58:59], %[t3] offset:1664\n\t" \
    "ds_read_b64_tr_b16 v[64:65], %[t3] offset:128\n\t" \
    "ds_read_b64_tr_b16 v[66:67], %[t3] offset:1792\n\t" \
    "ds_read_b64_tr_b16 v[72:73], %[t3] offset:256\n\t" \
    "ds_read_b64_tr_b16 v[74:75], %[t3] offset:1920\n\t" \
    "s_xor_b32 %[t], %[wx], 4\n\t" \
    "s_mul_i32 %[t], %[t], 0xc0000\n\t" \
    "buffer_load_dwordx4 v[216:219], %[o0], %[rs], %[t] offen" WNT "\n\t" \
    "buffer_load_dwordx4 v[80:83], %[o1], %[rs], %[t] offen" WNT "\n\t" \
    "buffer_load_dwordx4 v[88:91], %[o2], %[rs], %[t] offen" WNT "\n\t" \
    "s_add_u32 %[t], %[t], 0x18000\n\t" \
    "buffer_load_dwordx4 v[96:99], %[o0], %[rs], %[t] offen" WNT "\n\t" \
    "buffer_load_dwordx4 v[104:107], %[o1], %[rs], %[t] offen" WNT "\n\t" \
    "buffer_load_dwordx4 v[112:115], %[o2], %[rs], %[t] offen" WNT "\n\t" \
    "s_add_u32 %[t], %[t], 0x18000\n\t" \
    "buffer_load_dwordx4 v[224:227], %[o0], %[rs], %[t] offen" WNT "\n\t" \
    "buffer_load_dwordx4 v[120:123], %[o1], %[rs], %[t] offen" WNT "\n\t" \
    "buffer_load_dwordx4 v[128:131], %[o2], %[rs], %[t] offen" WNT "\n\t" \
    "s_add_u32 %[t], %[t], 0x18000\n\t" \
    "buffer_load_dwordx4 v[136:139], %[o0], %[rs], %[t] offen" WNT "\n\t" \
    "buffer_load_dwordx4 v[144:147], %[o1], %[rs], %[t] offen" WNT "\n\t" \
    "buffer_load_dwordx4 v[152:155], %[o2], %[rs], %[t] offen" WNT "\n\t" \
    "s_add_u32 %[t], %[t], 0x18000\n\t" \
    "s_waitcnt lgkmcnt(0)\n\t" \
    "s_barrier \n\t" \
    "s_waitcnt vmcnt(21)\n\t" \
    "ds_write_b128 %[s0], v[204:207] offset:0\n\t" \
    "ds_write_b128 %[s1], v[4:7] offset:0\n\t" \
    "ds_write_b128 %[s2], v[12:15] offset:0\n\t" \
    "s_waitcnt vmcnt(18)\n\t" \
    "ds_write_b128 %[s0], v[20:23] offset:3456\n\t" \
    "ds_write_b128 %[s1], v[28:31] offset:3456\n\t" \
    "ds_write_b128 %[s2], v[36:39] offset:3456\n\t" \
    "s_waitcnt vmcnt(15)\n\t" \
    "ds_write_b128 %[s0], v[212:215] offset:6912\n\t" \
    "ds_write_b128 %[s1], v[44:47] offset:6912\n\t" \
    "ds_write_b128 %[s2], v[52:55] offset:6912\n\t" \
    "s_waitcnt vmcnt(12)\n\t" \
    "ds_write_b128 %[s0], v[60:63] offset:10368\n\t" \
    "ds_write_b128 %[s1], v[68:71] offset:10368\n\t" \
    "ds_write_b128 %[s2], v[76:79] offset:10368\n\t" \
    "s_waitcnt lgkmcnt(0)\n\t" \
    "s_barrier \n\t" \
    "ds_read_b64_tr_b16 v[204:205], %[t0] offset:0\n\t" \
    "ds_read_b64_tr_b16 v[206:207], %[t0] offset:1664\n\t" \
    "ds_read_b64_tr_b16 v[4:5], %[t0] offset:128\n\t" \
    "ds_read_b64_tr_b16 v[6:7], %[t0] offset:1792\n\t" \
    "ds_read_b64_tr_b16 v[12:13], %[t0] offset:256\n\t" \
    "ds_read_b64_tr_b16 v[14:15], %[t0] offset:1920\n\t" \
    "ds_read_b64_tr_b16 v[20:21], %[t1] offset:0\n\t" \
    "ds_read_b64_tr_b16 v[22:23], %[t1] offset:1664\n\t" \
    "ds_read_b64_tr_b16 v[28:29], %[t1] offset:128\n\t" \
    "ds_read_b64_tr_b16 v[30:31], %[t1] offset:1792\n\t" \
    "ds_read_b64_tr_b16 v[36:37], %[t1] offset:256\n\t" \
    "ds_read_b64_tr_b16 v[38:39], %[t1] offset:1920\n\t" \
    "ds_read_b64_tr_b16 v[212:213], %[t2] offset:0\n\t" \
    "ds_read_b64_tr_b16 v[214:215], %[t2] offset:1664\n\t" \
    "ds_read_b64_tr_b16 v[44:45], %[t2] offset:128\n\t" \
    "ds_read_b64_tr_b16 v[46:47], %[t2] offset:1792\n\t" \
    "ds_read_b64_tr_b16 v[52:53], %[t2] offset:256\n\t" \
    "ds_read_b64_tr_b16 v[54:55], %[t2] offset:1920\n\t" \
    "ds_read_b64_tr_b16 v[60:61], %[t3] offset:0\n\t" \
    "ds_read_b64_tr_b16 v[62:63], %[t3] offset:1664\n\t" \
    "ds_read_b64_tr_b16 v[68:69], %[t3] offset:128\n\t" \
    "ds_read_b64_tr_b16 v[70:71], %[t3] offset:1792\n\t" \
    "ds_read_b64_tr_b16 v[76:77], %[t3] offset:256\n\t" \
    "ds_read_b64_tr_b16 v[78:79], %[t3] offset:1920\n\t" \
    "buffer_load_dwordx4 v[220:223], %[o0], %[rs], %[t] offen" WNT "\n\t" \
    "buffer_load_dwordx4 v[84:87], %[o1], %[rs], %[t] offen" WNT "\n\t" \
    "buffer_load_dwordx4 v[92:95], %[o2], %[rs], %[t] offen" WNT "\n\t" \
    "s_add_u32 %[t], %[t], 0x18000\n\t" \
    "buffer_load_dwordx4 v[100:103], %[o0], %[rs], %[t] offen" WNT "\n\t" \
    "buffer_load_dwordx4 v[108:111], %[o1], %[rs], %[t] offen" WNT "\n\t" \
    "buffer_load_dwordx4 v[116:119], %[o2], %[rs], %[t] offen" WNT "\n\t" \
    "s_add_u32 %[t], %[t], 0x18000\n\t" \
    "buffer_load_dwordx4 v[228:231], %[o0], %[rs], %[t] offen" WNT "\n\t" \
    "buffer_load_dwordx4 v[124:127], %[o1], %[rs], %[t] offen" WNT "\n\t" \
    "buffer_load_dwordx4 v[132:135], %[o2], %[rs], %[t] offen" WNT "\n\t" \
    "s_add_u32 %[t], %[t], 0x18000\n\t" \
    "buffer_load_dwordx4 v[140:143], %[o0], %[rs], %[t] offen" WNT "\n\t" \
    "buffer_load_dwordx4 v[148:151], %[o1], %[rs], %[t] offen" WNT "\n\t" \
    "buffer_load_dwordx4 v[156:159], %[o2], %[rs], %[t] offen" WNT "\n\t" \
    "s_add_u32 %[t], %[t], 0x18000\n\t" \
    "s_waitcnt lgkmcnt(0)\n\t" \
    "s_barrier \n\t" \
    "s_waitcnt vmcnt(21)\n\t" \
    "ds_write_b128 %[s0], v[216:219] offset:0\n\t" \
    "ds_write_b128 %[s1], v[80:83] offset:0\n\t" \
    "ds_write_b128 %[s2], v[88:91] offset:0\n\t" \
    "s_waitcnt vmcnt(18)\n\t" \
    "ds_write_b128 %[s0], v[96:99] offset:3456\n\t" \
    "ds_write_b128 %[s1], v[104:107] offset:3456\n\t" \
    "ds_write_b128 %[s2], v[112:115] offset:3456\n\t" \
    "s_waitcnt vmcnt(15)\n\t" \
    "ds_write_b128 %[s0], v[224:227] offset:6912\n\t" \
    "ds_write_b128 %[s1], v[120:123] offset:6912\n\t" \
    "ds_write_b128 %[s2], v[128:131] offset:6912\n\t" \
    "s_waitcnt vmcnt(12)\n\t" \
    "ds_write_b128 %[s0], v[136:139] offset:10368\n\t" \
    "ds_write_b128 %[s1], v[144:147] offset:10368\n\t" \
    "ds_write_b128 %[s2], v[152:155] offset:10368\n\t" \
    "s_waitcnt lgkmcnt(0)\n\t" \
    "s_barrier \n\t" \
    "ds_read_b64_tr_b16 v[216:217], %[t0] offset:0\n\t" \
    "ds_read_b64_tr_b16 v[218:219], %[t0] offset:1664\n\t" \
    "ds_read_b64_tr_b16 v[80:81], %[t0] offset:128\n\t" \
    "ds_read_b64_tr_b16 v[82:83], %[t0] offset:1792\n\t" \
    "ds_read_b64_tr_b16 v[88:89], %[t0] offset:256\n\t" \
    "ds_read_b64_tr_b16 v[90:91], %[t0] offset:1920\n\t" \
    "ds_read_b64_tr_b16 v[96:97], %[t1] offset:0\n\t" \
    "ds_read_b64_tr_b16 v[98:99], %[t1] offset:1664\n\t" \
    "ds_read_b64_tr_b16 v[104:105], %[t1] offset:128\n\t" \
    "ds_read_b64_tr_b16 v[106:107], %[t1] offset:1792\n\t" \
    "ds_read_b64_tr_b16 v[112:113], %[t1] offset:256\n\t" \
    "ds_read_b64_tr_b16 v[114:115], %[t1] offset:1920\n\t" \
    "ds_read_b64_tr_b16 v[224:225], %[t2] offset:0\n\t" \
    "ds_read_b64_tr_b16 v[226:227], %[t2] offset:1664\n\t" \
    "ds_read_b64_tr_b16 v[120:121], %[t2] offset:128\n\t" \
    "ds_read_b64_tr_b16 v[122:123], %[t2] offset:1792\n\t" \
    "ds_read_b64_tr_b16 v[128:129], %[t2] offset:256\n\t" \
    "ds_read_b64_tr_b16 v[130:131], %[t2] offset:1920\n\t" \
    "ds_read_b64_tr_b16 v[136:137], %[t3] offset:0\n\t" \
    "ds_read_b64_tr_b16 v[138:139], %[t3] offset:1664\n\t" \
    "ds_read_b64_tr_b16 v[144:145], %[t3] offset:128\n\t" \
    "ds_read_b64_tr_b16 v[146:147], %[t3] offset:1792\n\t" \
    "ds_read_b64_tr_b16 v[152:153], %[t3] offset:256\n\t" \
    "ds_read_b64_tr_b16 v[154:155], %[t3] offset:1920\n\t" \
    "s_and_b32 %[t], %[wx], 3\n\t" \
    "s_mul_i32 %[t], %[t], 0x60000\n\t" \
    "s_add_u32 %[t], %[t], 0x600000\n\t" \
    "buffer_load_dwordx4 v[232:235], %[o0], %[rs], %[t] offen" WNT "\n\t" \
    "buffer_load_dwordx4 v[160:163], %[o1], %[rs], %[t] offen" WNT "\n\t" \
    "buffer_load_dwordx4 v[168:171], %[o2], %[rs], %[t] offen" WNT "\n\t" \
    "s_add_u32 %[t], %[t], 0x18000\n\t" \
    "buffer_load_dwordx4 v[236:239], %[o0], %[rs], %[t] offen" WNT "\n\t" \
    "buffer_load_dwordx4 v[164:167], %[o1], %[rs], %[t] offen" WNT "\n\t" \
    "buffer_load_dwordx4 v[172:175], %[o2], %[rs], %[t] offen" WNT "\n\t" \
    "s_add_u32 %[t], %[t], 0x18000\n\t" \
    "buffer_load_dwordx4 v[176:179], %[o0], %[rs], %[t] offen" WNT "\n\t" \
    "buffer_load_dwordx4 v[184:187], %[o1], %[rs], %[t] offen" WNT "\n\t" \
    "buffer_load_dwordx4 v[192:195], %[o2], %[rs], %[t] offen" WNT "\n\t" \
    "s_add_u32 %[t], %[t], 0x18000\n\t" \
    "buffer_load_dwordx4 v[180:183], %[o0], %[rs], %[t] offen" WNT "\n\t" \
    "buffer_load_dwordx4 v[188:191], %[o1], %[rs], %[t] offen" WNT "\n\t" \
    "buffer_load_dwordx4 v[196:199], %[o2], %[rs], %[t] offen" WNT "\n\t" \
    "s_add_u32 %[t], %[t], 0x18000\n\t" \
    "s_waitcnt lgkmcnt(0)\n\t" \
    "s_barrier \n\t" \
    "s_waitcnt vmcnt(21)\n\t" \
    "ds_write_b128 %[s0], v[220:223] offset:0\n\t" \
    "ds_write_b128 %[s1], v[84:87] offset:0\n\t" \
    "ds_write_b128 %[s2], v[92:95] offset:0\n\t" \
    "s_waitcnt vmcnt(18)\n\t" \
    "ds_write_b128 %[s0], v[100:103] offset:3456\n\t" \
    "ds_write_b128 %[s1], v[108:111] offset:3456\n\t" \
    "ds_write_b128 %[s2], v[116:119] offset:3456\n\t" \
    "s_waitcnt vmcnt(15)\n\t" \
    "ds_write_b128 %[s0], v[228:231] offset:6912\n\t" \
    "ds_write_b128 %[s1], v[124:127] offset:6912\n\t" \
    "ds_write_b128 %[s2], v[132:135] offset:6912\n\t" \
    "s_waitcnt vmcnt(12)\n\t" \
    "ds_write_b128 %[s0], v[140:143] offset:10368\n\t" \
    "ds_write_b128 %[s1], v[148:151] offset:10368\n\t" \
    "ds_write_b128 %[s2], v[156:159] offset:10368\n\t" \
    "s_waitcnt lgkmcnt(0)\n\t" \
    "s_barrier \n\t" \
    "ds_read_b64_tr_b16 v[220:221], %[t0] offset:0\n\t" \
    "ds_read_b64_tr_b16 v[222:223], %[t0] offset:1664\n\t" \
    "ds_read_b64_tr_b16 v[84:85], %[t0] offset:128\n\t" \
    "ds_read_b64_tr_b16 v[86:87], %[t0] offset:1792\n\t" \
    "ds_read_b64_tr_b16 v[92:93], %[t0] offset:256\n\t" \
    "ds_read_b64_tr_b16 v[94:95], %[t0] offset:1920\n\t" \
    "ds_read_b64_tr_b16 v[100:101], %[t1] offset:0\n\t" \
    "ds_read_b64_tr_b16 v[102:103], %[t1] offset:1664\n\t" \
    "ds_read_b64_tr_b16 v[108:109], %[t1] offset:128\n\t" \
    "ds_read_b64_tr_b16 v[110:111], %[t1] offset:1792\n\t" \
    "ds_read_b64_tr_b16 v[116:117], %[t1] offset:256\n\t" \
    "ds_read_b64_tr_b16 v[118:119], %[t1] offset:1920\n\t" \
    "ds_read_b64_tr_b16 v[228:229], %[t2] offset:0\n\t" \
    "ds_read_b64_tr_b16 v[230:231], %[t2] offset:1664\n\t" \
    "ds_read_b64_tr_b16 v[124:125], %[t2] offset:128\n\t" \
    "ds_read_b64_tr_b16 v[126:127], %[t2] offset:1792\n\t" \
    "ds_read_b64_tr_b16 v[132:133], %[t2] offset:256\n\t" \
    "ds_read_b64_tr_b16 v[134:135], %[t2] offset:1920\n\t" \
    "ds_read_b64_tr_b16 v[140:141], %[t3] offset:0\n\t" \
    "ds_read_b64_tr_b16 v[142:143], %[t3] offset:1664\n\t" \
    "ds_read_b64_tr_b16 v[148:149], %[t3] offset:128\n\t" \
    "ds_read_b64_tr_b16 v[150:151], %[t3] offset:1792\n\t" \
    "ds_read_b64_tr_b16 v[156:157], %[t3] offset:256\n\t" \
    "ds_read_b64_tr_b16 v[158:159], %[t3] offset:1920\n\t" \
    "s_waitcnt lgkmcnt(0)\n\t" \
    "s_barrier \n\t" \
    "s_waitcnt vmcnt(9)\n\t" \
    "ds_write_b128 %[s0], v[232:235] offset:0\n\t" \
    "ds_write_b128 %[s1], v[160:163] offset:0\n\t" \
    "ds_write_b128 %[s2], v[168:171] offset:0\n\t" \
    "s_waitcnt vmcnt(6)\n\t" \
    "ds_write_b128 %[s0], v[236:239] offset:3456\n\t" \
    "ds_write_b128 %[s1], v[164:167] offset:3456\n\t" \
    "ds_write_b128 %[s2], v[172:175] offset:3456\n\t" \
    "s_waitcnt vmcnt(3)\n\t" \
    "ds_write_b128 %[s0], v[176:179] offset:6912\n\t" \
    "ds_write_b128 %[s1], v[184:187] offset:6912\n\t" \
    "ds_write_b128 %[s2], v[192:195] offset:6912\n\t" \
    "s_waitcnt vmcnt(0)\n\t" \
    "ds_write_b128 %[s0], v[180:183] offset:10368\n\t" \
    "ds_write_b128 %[s1], v[188:191] offset:10368\n\t" \
    "ds_write_b128 %[s2], v[196:199] offset:10368\n\t" \
    "s_waitcnt lgkmcnt(0)\n\t" \
    "s_barrier \n\t" \
    "ds_read_b64_tr_b16 v[232:233], %[t4] offset:0\n\t" \
    "ds_read_b64_tr_b16 v[234:235], %[t4] offset:1664\n\t" \
    "ds_read_b64_tr_b16 v[160:161], %[t4] offset:128\n\t" \
    "ds_read_b64_tr_b16 v[162:163], %[t4] offset:1792\n\t" \
    "ds_read_b64_tr_b16 v[168:169], %[t4] offset:256\n\t" \
    "ds_read_b64_tr_b16 v[170:171], %[t4] offset:1920\n\t" \
    "ds_read_b64_tr_b16 v[236:237], %[t4] offset:13824\n\t" \
    "ds_read_b64_tr_b16 v[238:239], %[t4] offset:15488\n\t" \
    "ds_read_b64_tr_b16 v[164:165], %[t4] offset:13952\n\t" \
    "ds_read_b64_tr_b16 v[166:167], %[t4] offset:15616\n\t" \
    "ds_read_b64_tr_b16 v[172:173], %[t4] offset:14080\n\t" \
    "ds_read_b64_tr_b16 v[174:175], %[t4] offset:15744\n\t" \
    "ds_read_b64_tr_b16 v[176:177], %[t4] offset:27648\n\t" \
    "ds_read_b64_tr_b16 v[178:179], %[t4] offset:29312\n\t" \
    "ds_read_b64_tr_b16 v[184:185], %[t4] offset:27776\n\t" \
    "ds_read_b64_tr_b16 v[186:187], %[t4] offset:29440\n\t" \
    "ds_read_b64_tr_b16 v[192:193], %[t4] offset:27904\n\t" \
    "ds_read_b64_tr_b16 v[194:195], %[t4] offset:29568\n\t" \
    "ds_read_b64_tr_b16 v[180:181], %[t4] offset:41472\n\t" \
    "ds_read_b64_tr_b16 v[182:183], %[t4] offset:43136\n\t" \
    "ds_read_b64_tr_b16 v[188:189], %[t4] offset:41600\n\t" \
    "ds_read_b64_tr_b16 v[190:191], %[t4] offset:43264\n\t" \
    "ds_read_b64_tr_b16 v[196:197], %[t4] offset:41728\n\t" \
    "ds_read_b64_tr_b16 v[198:199], %[t4] offset:43392\n\t" \
    "s_waitcnt lgkmcnt(0)\n\t" \
    "s_barrier \n\t" \
    "ds_write_b128 %[wl], v[200:203] offset:0\n\t" \
    "ds_write_b128 %[wl], v[204:207] offset:1024\n\t" \
    "ds_write_b128 %[wl], v[208:211] offset:2048\n\t" \
    "ds_write_b128 %[wl], v[212:215] offset:3072\n\t" \
    "ds_write_b128 %[wl], v[216:219] offset:4096\n\t" \
    "ds_write_b128 %[wl], v[220:223] offset:5120\n\t" \
    "ds_write_b128 %[wl], v[224:227] offset:6144\n\t" \
    "ds_write_b128 %[wl], v[228:231] offset:7168\n\t" \
    "ds_write_b128 %[wl], v[232:235] offset:8192\n\t" \
    "ds_write_b128 %[wl], v[236:239] offset:9216\n\t" \
    "s_waitcnt lgkmcnt(0)\n\t"
#define ASM_WLOADO \
    "s_mov_b32 %[t], 0x0\n\t" \
    "buffer_load_dwordx4 v[200:203], %[o0], %[rs], %[t] offen" WNT "\n\t" \
    "buffer_load_dwordx4 v[16:19], %[o1], %[rs], %[t] offen" WNT "\n\t" \
    "buffer_load_dwordx4 v[208:211], %[o2], %[rs], %[t] offen" WNT "\n\t" \
    "buffer_load_dwordx4 v[56:59], %[o3], %[rs], %[t] offen" WNT "\n\t" \
    "buffer_load_dwordx4 v[216:219], %[o4], %[rs], %[t] offen" WNT "\n\t" \
    "s_mov_b32 %[t], 0xa000\n\t" \
    "buffer_load_dwordx4 v[96:99], %[o0], %[rs], %[t] offen" WNT "\n\t" \
    "buffer_load_dwordx4 v[224:227], %[o1], %[rs], %[t] offen" WNT "\n\t" \
    "buffer_load_dwordx4 v[136:139], %[o2], %[rs], %[t] offen" WNT "\n\t" \
    "buffer_load_dwordx4 v[232:235], %[o3], %[rs], %[t] offen" WNT "\n\t" \
    "buffer_load_dwordx4 v[176:179], %[o4], %[rs], %[t] offen" WNT "\n\t" \
    "s_mov_b32 %[t], 0x14000\n\t" \
    "buffer_load_dwordx4 v[204:207], %[o0], %[rs], %[t] offen" WNT "\n\t" \
    "buffer_load_dwordx4 v[20:23], %[o1], %[rs], %[t] offen" WNT "\n\t" \
    "buffer_load_dwordx4 v[212:215], %[o2], %[rs], %[t] offen" WNT "\n\t" \
    "buffer_load_dwordx4 v[60:63], %[o3], %[rs], %[t] offen" WNT "\n\t" \
    "buffer_load_dwordx4 v[220:223], %[o4], %[rs], %[t] offen" WNT "\n\t" \
    "s_mov_b32 %[t], 0x1e000\n\t" \
    "buffer_load_dwordx4 v[100:103], %[o0], %[rs], %[t] offen" WNT "\n\t" \
    "buffer_load_dwordx4 v[228:231], %[o1], %[rs], %[t] offen" WNT "\n\t" \
    "buffer_load_dwordx4 v[140:143], %[o2], %[rs], %[t] offen" WNT "\n\t" \
    "buffer_load_dwordx4 v[236:239], %[o3], %[rs], %[t] offen" WNT "\n\t" \
    "buffer_load_dwordx4 v[180:183], %[o4], %[rs], %[t] offen" WNT "\n\t" \
    "s_waitcnt vmcnt(15)\n\t" \
    "ds_write_b128 %[s0], v[200:203] offset:0\n\t" \
    "ds_write_b128 %[s1], v[16:19] offset:0\n\t" \
    "ds_write_b128 %[s2], v[208:211] offset:0\n\t" \
    "ds_write_b128 %[s3], v[56:59] offset:0\n\t" \
    "ds_write_b128 %[s4], v[216:219] offset:0\n\t" \
    "s_waitcnt vmcnt(10)\n\t" \
    "ds_write_b128 %[s0], v[96:99] offset:5632\n\t" \
    "ds_write_b128 %[s1], v[224:227] offset:5632\n\t" \
    "ds_write_b128 %[s2], v[136:139] offset:5632\n\t" \
    "ds_write_b128 %[s3], v[232:235] offset:5632\n\t" \
    "ds_write_b128 %[s4], v[176:179] offset:5632\n\t" \
    "ds_read_b64_tr_b16 v[200:201], %[tr] offset:0\n\t" \
    "ds_read_b64_tr_b16 v[202:203], %[tr] offset:5632\n\t" \
    "ds_read_b64_tr_b16 v[16:17], %[tr] offset:32\n\t" \
    "ds_read_b64_tr_b16 v[18:19], %[tr] offset:5664\n\t" \
    "ds_read_b64_tr_b16 v[208:209], %[tr] offset:64\n\t" \
    "ds_read_b64_tr_b16 v[210:211], %[tr] offset:5696\n\t" \
    "ds_read_b64_tr_b16 v[56:57], %[tr] offset:96\n\t" \
    "ds_read_b64_tr_b16 v[58:59], %[tr] offset:5728\n\t" \
    "ds_read_b64_tr_b16 v[216:217], %[tr] offset:128\n\t" \
    "ds_read_b64_tr_b16 v[218:219], %[tr] offset:5760\n\t" \
    "ds_read_b64_tr_b16 v[96:97], %[tr] offset:160\n\t" \
    "ds_read_b64_tr_b16 v[98:99], %[tr] offset:5792\n\t" \
    "ds_read_b64_tr_b16 v[224:225], %[tr] offset:192\n\t" \
    "ds_read_b64_tr_b16 v[226:227], %[tr] offset:5824\n\t" \
    "ds_read_b64_tr_b16 v[136:137], %[tr] offset:224\n\t" \
    "ds_read_b64_tr_b16 v[138:139], %[tr] offset:5856\n\t" \
    "ds_read_b64_tr_b16 v[232:233], %[tr] offset:256\n\t" \
    "ds_read_b64_tr_b16 v[234:235], %[tr] offset:5888\n\t" \
    "ds_read_b64_tr_b16 v[176:177], %[tr] offset:288\n\t" \
    "ds_read_b64_tr_b16 v[178:179], %[tr] offset:5920\n\t" \
    "s_barrier \n\t" \
    "s_mov_b32 %[t], 0x28000\n\t" \
    "buffer_load_dwordx4 v[0:3], %[o0], %[rs], %[t] offen" WNT "\n\t" \
    "buffer_load_dwordx4 v[24:27], %[o1], %[rs], %[t] offen" WNT "\n\t" \
    "buffer_load_dwordx4 v[40:43], %[o2], %[rs], %[t] offen" WNT "\n\t" \
    "buffer_load_dwordx4 v[64:67], %[o3], %[rs], %[t] offen" WNT "\n\t" \
    "buffer_load_dwordx4 v[80:83], %[o4], %[rs], %[t] offen" WNT "\n\t" \
    "s_mov_b32 %[t], 0x32000\n\t" \
    "buffer_load_dwordx4 v[104:107], %[o0], %[rs], %[t] offen" WNT "\n\t" \
    "buffer_load_dwordx4 v[120:123], %[o1], %[rs], %[t] offen" WNT "\n\t" \
    "buffer_load_dwordx4 v[144:147], %[o2], %[rs], %[t] offen" WNT "\n\t" \
    "buffer_load_dwordx4 v[160:163], %[o3], %[rs], %[t] offen" WNT "\n\t" \
    "buffer_load_dwordx4 v[184:187], %[o4], %[rs], %[t] offen" WNT "\n\t" \
    "s_waitcnt lgkmcnt(0)\n\t" \
    "s_barrier \n\t" \
    "s_waitcnt vmcnt(15)\n\t" \
    "ds_write_b128 %[s0], v[204:207] offset:0\n\t" \
    "ds_write_b128 %[s1], v[20:23] offset:0\n\t" \
    "ds_write_b128 %[s2], v[212:215] offset:0\n\t" \
    "ds_write_b128 %[s3], v[60:63] offset:0\n\t" \
    "ds_write_b128 %[s4], v[220:223] offset:0\n\t" \
    "s_waitcnt vmcnt(10)\n\t" \
    "ds_write_b128 %[s0], v[100:103] offset:5632\n\t" \
    "ds_write_b128 %[s1], v[228:231] offset:5632\n\t" \
    "ds_write_b128 %[s2], v[140:143] offset:5632\n\t" \
    "ds_write_b128 %[s3], v[236:239] offset:5632\n\t" \
    "ds_write_b128 %[s4], v[180:183] offset:5632\n\t" \
    "ds_read_b64_tr_b16 v[204:205], %[tr] offset:0\n\t" \
    "ds_read_b64_tr_b16 v[206:207], %[tr] offset:5632\n\t" \
    "ds_read_b64_tr_b16 v[20:21], %[tr] offset:32\n\t" \
    "ds_read_b64_tr_b16 v[22:23], %[tr] offset:5664\n\t" \
    "ds_read_b64_tr_b16 v[212:213], %[tr] offset:64\n\t" \
    "ds_read_b64_tr_b16 v[214:215], %[tr] offset:5696\n\t" \
    "ds_read_b64_tr_b16 v[60:61], %[tr] offset:96\n\t" \
    "ds_read_b64_tr_b16 v[62:63], %[tr] offset:5728\n\t" \
    "ds_read_b64_tr_b16 v[220:221], %[tr] offset:128\n\t" \
    "ds_read_b64_tr_b16 v[222:223], %[tr] offset:5760\n\t" \
    "ds_read_b64_tr_b16 v[100:101], %[tr] offset:160\n\t" \
    "ds_read_b64_tr_b16 v[102:103], %[tr] offset:5792\n\t" \
    "ds_read_b64_tr_b16 v[228:229], %[tr] offset:192\n\t" \
    "ds_read_b64_tr_b16 v[230:231], %[tr] offset:5824\n\t" \
    "ds_read_b64_tr_b16 v[140:141], %[tr] offset:224\n\t" \
    "ds_read_b64_tr_b16 v[142:143], %[tr] offset:5856\n\t" \
    "ds_read_b64_tr_b16 v[236:237], %[tr] offset:256\n\t" \
    "ds_read_b64_tr_b16 v[238:239], %[tr] offset:5888\n\t" \
    "ds_read_b64_tr_b16 v[180:181], %[tr] offset:288\n\t" \
    "ds_read_b64_tr_b16 v[182:183], %[tr] offset:5920\n\t" \
    "s_barrier \n\t" \
    "s_mov_b32 %[t], 0x5000\n\t" \
    "buffer_load_dwordx4 v[4:7], %[o0], %[rs], %[t] offen" WNT "\n\t" \
    "buffer_load_dwordx4 v[28:31], %[o1], %[rs], %[t] offen" WNT "\n\t" \
    "buffer_load_dwordx4 v[44:47], %[o2], %[rs], %[t] offen" WNT "\n\t" \
    "buffer_load_dwordx4 v[68:71], %[o3], %[rs], %[t] offen" WNT "\n\t" \
    "buffer_load_dwordx4 v[84:87], %[o4], %[rs], %[t] offen" WNT "\n\t" \
    "s_mov_b32 %[t], 0xf000\n\t" \
    "buffer_load_dwordx4 v[108:111], %[o0], %[rs], %[t] offen" WNT "\n\t" \
    "buffer_load_dwordx4 v[124:127], %[o1], %[rs], %[t] offen" WNT "\n\t" \
    "buffer_load_dwordx4 v[148:151], %[o2], %[rs], %[t] offen" WNT "\n\t" \
    "buffer_load_dwordx4 v[164:167], %[o3], %[rs], %[t] offen" WNT "\n\t" \
    "buffer_load_dwordx4 v[188:191], %[o4], %[rs], %[t] offen" WNT "\n\t" \
    "s_waitcnt lgkmcnt(0)\n\t" \
    "s_barrier \n\t" \
    "s_waitcnt vmcnt(15)\n\t" \
    "ds_write_b128 %[s0], v[0:3] offset:0\n\t" \
    "ds_write_b128 %[s1], v[24:27] offset:0\n\t" \
    "ds_write_b128 %[s2], v[40:43] offset:0\n\t" \
    "ds_write_b128 %[s3], v[64:67] offset:0\n\t" \
    "ds_write_b128 %[s4], v[80:83] offset:0\n\t" \
    "s_waitcnt vmcnt(10)\n\t" \
    "ds_write_b128 %[s0], v[104:107] offset:5632\n\t" \
    "ds_write_b128 %[s1], v[120:123] offset:5632\n\t" \
    "ds_write_b128 %[s2], v[144:147] offset:5632\n\t" \
    "ds_write_b128 %[s3], v[160:163] offset:5632\n\t" \
    "ds_write_b128 %[s4], v[184:187] offset:5632\n\t" \
    "ds_read_b64_tr_b16 v[0:1], %[tr] offset:0\n\t" \
    "ds_read_b64_tr_b16 v[2:3], %[tr] offset:5632\n\t" \
    "ds_read_b64_tr_b16 v[24:25], %[tr] offset:32\n\t" \
    "ds_read_b64_tr_b16 v[26:27], %[tr] offset:5664\n\t" \
    "ds_read_b64_tr_b16 v[40:41], %[tr] offset:64\n\t" \
    "ds_read_b64_tr_b16 v[42:43], %[tr] offset:5696\n\t" \
    "ds_read_b64_tr_b16 v[64:65], %[tr] offset:96\n\t" \
    "ds_read_b64_tr_b16 v[66:67], %[tr] offset:5728\n\t" \
    "ds_read_b64_tr_b16 v[80:81], %[tr] offset:128\n\t" \
    "ds_read_b64_tr_b16 v[82:83], %[tr] offset:5760\n\t" \
    "ds_read_b64_tr_b16 v[104:105], %[tr] offset:160\n\t" \
    "ds_read_b64_tr_b16 v[106:107], %[tr] offset:5792\n\t" \
    "ds_read_b64_tr_b16 v[120:121], %[tr] offset:192\n\t" \
    "ds_read_b64_tr_b16 v[122:123], %[tr] offset:5824\n\t" \
    "ds_read_b64_tr_b16 v[144:145], %[tr] offset:224\n\t" \
    "ds_read_b64_tr_b16 v[146:147], %[tr] offset:5856\n\t" \
    "ds_read_b64_tr_b16 v[160:161], %[tr] offset:256\n\t" \
    "ds_read_b64_tr_b16 v[162:163], %[tr] offset:5888\n\t" \
    "ds_read_b64_tr_b16 v[184:185], %[tr] offset:288\n\t" \
    "ds_read_b64_tr_b16 v[186:187], %[tr] offset:5920\n\t" \
    "s_barrier \n\t" \
    "s_mov_b32 %[t], 0x19000\n\t" \
    "buffer_load_dwordx4 v[8:11], %[o0], %[rs], %[t] offen" WNT "\n\t" \
    "buffer_load_dwordx4 v[32:35], %[o1], %[rs], %[t] offen" WNT "\n\t" \
    "buffer_load_dwordx4 v[48:51], %[o2], %[rs], %[t] offen" WNT "\n\t" \
    "buffer_load_dwordx4 v[72:75], %[o3], %[rs], %[t] offen" WNT "\n\t" \
    "buffer_load_dwordx4 v[88:91], %[o4], %[rs], %[t] offen" WNT "\n\t" \
    "s_mov_b32 %[t], 0x23000\n\t" \
    "buffer_load_dwordx4 v[112:115], %[o0], %[rs], %[t] offen" WNT "\n\t" \
    "buffer_load_dwordx4 v[128:131], %[o1], %[rs], %[t] offen" WNT "\n\t" \
    "buffer_load_dwordx4 v[152:155], %[o2], %[rs], %[t] offen" WNT "\n\t" \
    "buffer_load_dwordx4 v[168:171], %[o3], %[rs], %[t] offen" WNT "\n\t" \
    "buffer_load_dwordx4 v[192:195], %[o4], %[rs], %[t] offen" WNT "\n\t" \
    "s_waitcnt lgkmcnt(0)\n\t" \
    "s_barrier \n\t" \
    "s_waitcnt vmcnt(15)\n\t" \
    "ds_write_b128 %[s0], v[4:7] offset:0\n\t" \
    "ds_write_b128 %[s1], v[28:31] offset:0\n\t" \
    "ds_write_b128 %[s2], v[44:47] offset:0\n\t" \
    "ds_write_b128 %[s3], v[68:71] offset:0\n\t" \
    "ds_write_b128 %[s4], v[84:87] offset:0\n\t" \
    "s_waitcnt vmcnt(10)\n\t" \
    "ds_write_b128 %[s0], v[108:111] offset:5632\n\t" \
    "ds_write_b128 %[s1], v[124:127] offset:5632\n\t" \
    "ds_write_b128 %[s2], v[148:151] offset:5632\n\t" \
    "ds_write_b128 %[s3], v[164:167] offset:5632\n\t" \
    "ds_write_b128 %[s4], v[188:191] offset:5632\n\t" \
    "ds_read_b64_tr_b16 v[4:5], %[tr] offset:0\n\t" \
    "ds_read_b64_tr_b16 v[6:7], %[tr] offset:5632\n\t" \
    "ds_read_b64_tr_b16 v[28:29], %[tr] offset:32\n\t" \
    "ds_read_b64_tr_b16 v[30:31], %[tr] offset:5664\n\t" \
    "ds_read_b64_tr_b16 v[44:45], %[tr] offset:64\n\t" \
    "ds_read_b64_tr_b16 v[46:47], %[tr] offset:5696\n\t" \
    "ds_read_b64_tr_b16 v[68:69], %[tr] offset:96\n\t" \
    "ds_read_b64_tr_b16 v[70:71], %[tr] offset:5728\n\t" \
    "ds_read_b64_tr_b16 v[84:85], %[tr] offset:128\n\t" \
    "ds_read_b64_tr_b16 v[86:87], %[tr] offset:5760\n\t" \
    "ds_read_b64_tr_b16 v[108:109], %[tr] offset:160\n\t" \
    "ds_read_b64_tr_b16 v[110:111], %[tr] offset:5792\n\t" \
    "ds_read_b64_tr_b16 v[124:125], %[tr] offset:192\n\t" \
    "ds_read_b64_tr_b16 v[126:127], %[tr] offset:5824\n\t" \
    "ds_read_b64_tr_b16 v[148:149], %[tr] offset:224\n\t" \
    "ds_read_b64_tr_b16 v[150:151], %[tr] offset:5856\n\t" \
    "ds_read_b64_tr_b16 v[164:165], %[tr] offset:256\n\t" \
    "ds_read_b64_tr_b16 v[166:167], %[tr] offset:5888\n\t" \
    "ds_read_b64_tr_b16 v[188:189], %[tr] offset:288\n\t" \
    "ds_read_b64_tr_b16 v[190:191], %[tr] offset:5920\n\t" \
    "s_barrier \n\t" \
    "s_mov_b32 %[t], 0x2d000\n\t" \
    "buffer_load_dwordx4 v[12:15], %[o0], %[rs], %[t] offen" WNT "\n\t" \
    "buffer_load_dwordx4 v[36:39], %[o1], %[rs], %[t] offen" WNT "\n\t" \
    "buffer_load_dwordx4 v[52:55], %[o2], %[rs], %[t] offen" WNT "\n\t" \
    "buffer_load_dwordx4 v[76:79], %[o3], %[rs], %[t] offen" WNT "\n\t" \
    "buffer_load_dwordx4 v[92:95], %[o4], %[rs], %[t] offen" WNT "\n\t" \
    "s_mov_b32 %[t], 0x37000\n\t" \
    "buffer_load_dwordx4 v[116:119], %[o0], %[rs], %[t] offen" WNT "\n\t" \
    "buffer_load_dwordx4 v[132:135], %[o1], %[rs], %[t] offen" WNT "\n\t" \
    "buffer_load_dwordx4 v[156:159], %[o2], %[rs], %[t] offen" WNT "\n\t" \
    "buffer_load_dwordx4 v[172:175], %[o3], %[rs], %[t] offen" WNT "\n\t" \
    "buffer_load_dwordx4 v[196:199], %[o4], %[rs], %[t] offen" WNT "\n\t" \
    "s_waitcnt lgkmcnt(0)\n\t" \
    "s_barrier \n\t" \
    "s_waitcnt vmcnt(15)\n\t" \
    "ds_write_b128 %[s0], v[8:11] offset:0\n\t" \
    "ds_write_b128 %[s1], v[32:35] offset:0\n\t" \
    "ds_write_b128 %[s2], v[48:51] offset:0\n\t" \
    "ds_write_b128 %[s3], v[72:75] offset:0\n\t" \
    "ds_write_b128 %[s4], v[88:91] offset:0\n\t" \
    "s_waitcnt vmcnt(10)\n\t" \
    "ds_write_b128 %[s0], v[112:115] offset:5632\n\t" \
    "ds_write_b128 %[s1], v[128:131] offset:5632\n\t" \
    "ds_write_b128 %[s2], v[152:155] offset:5632\n\t" \
    "ds_write_b128 %[s3], v[168:171] offset:5632\n\t" \
    "ds_write_b128 %[s4], v[192:195] offset:5632\n\t" \
    "ds_read_b64_tr_b16 v[8:9], %[tr] offset:0\n\t" \
    "ds_read_b64_tr_b16 v[10:11], %[tr] offset:5632\n\t" \
    "ds_read_b64_tr_b16 v[32:33], %[tr] offset:32\n\t" \
    "ds_read_b64_tr_b16 v[34:35], %[tr] offset:5664\n\t" \
    "ds_read_b64_tr_b16 v[48:49], %[tr] offset:64\n\t" \
    "ds_read_b64_tr_b16 v[50:51], %[tr] offset:5696\n\t" \
    "ds_read_b64_tr_b16 v[72:73], %[tr] offset:96\n\t" \
    "ds_read_b64_tr_b16 v[74:75], %[tr] offset:5728\n\t" \
    "ds_read_b64_tr_b16 v[88:89], %[tr] offset:128\n\t" \
    "ds_read_b64_tr_b16 v[90:91], %[tr] offset:5760\n\t" \
    "ds_read_b64_tr_b16 v[112:113], %[tr] offset:160\n\t" \
    "ds_read_b64_tr_b16 v[114:115], %[tr] offset:5792\n\t" \
    "ds_read_b64_tr_b16 v[128:129], %[tr] offset:192\n\t" \
    "ds_read_b64_tr_b16 v[130:131], %[tr] offset:5824\n\t" \
    "ds_read_b64_tr_b16 v[152:153], %[tr] offset:224\n\t" \
    "ds_read_b64_tr_b16 v[154:155], %[tr] offset:5856\n\t" \
    "ds_read_b64_tr_b16 v[168:169], %[tr] offset:256\n\t" \
    "ds_read_b64_tr_b16 v[170:171], %[tr] offset:5888\n\t" \
    "ds_read_b64_tr_b16 v[192:193], %[tr] offset:288\n\t" \
    "ds_read_b64_tr_b16 v[194:195], %[tr] offset:5920\n\t" \
    "s_waitcnt lgkmcnt(0)\n\t" \
    "s_barrier \n\t" \
    "s_waitcnt vmcnt(5)\n\t" \
    "ds_write_b128 %[s0], v[12:15] offset:0\n\t" \
    "ds_write_b128 %[s1], v[36:39] offset:0\n\t" \
    "ds_write_b128 %[s2], v[52:55] offset:0\n\t" \
    "ds_write_b128 %[s3], v[76:79] offset:0\n\t" \
    "ds_write_b128 %[s4], v[92:95] offset:0\n\t" \
    "s_waitcnt vmcnt(0)\n\t" \
    "ds_write_b128 %[s0], v[116:119] offset:5632\n\t" \
    "ds_write_b128 %[s1], v[132:135] offset:5632\n\t" \
    "ds_write_b128 %[s2], v[156:159] offset:5632\n\t" \
    "ds_write_b128 %[s3], v[172:175] offset:5632\n\t" \
    "ds_write_b128 %[s4], v[196:199] offset:5632\n\t" \
    "ds_read_b64_tr_b16 v[12:13], %[tr] offset:0\n\t" \
    "ds_read_b64_tr_b16 v[14:15], %[tr] offset:5632\n\t" \
    "ds_read_b64_tr_b16 v[36:37], %[tr] offset:32\n\t" \
    "ds_read_b64_tr_b16 v[38:39], %[tr] offset:5664\n\t" \
    "ds_read_b64_tr_b16 v[52:53], %[tr] offset:64\n\t" \
    "ds_read_b64_tr_b16 v[54:55], %[tr] offset:5696\n\t" \
    "ds_read_b64_tr_b16 v[76:77], %[tr] offset:96\n\t" \
    "ds_read_b64_tr_b16 v[78:79], %[tr] offset:5728\n\t" \
    "ds_read_b64_tr_b16 v[92:93], %[tr] offset:128\n\t" \
    "ds_read_b64_tr_b16 v[94:95], %[tr] offset:5760\n\t" \
    "ds_read_b64_tr_b16 v[116:117], %[tr] offset:160\n\t" \
    "ds_read_b64_tr_b16 v[118:119], %[tr] offset:5792\n\t" \
    "ds_read_b64_tr_b16 v[132:133], %[tr] offset:192\n\t" \
    "ds_read_b64_tr_b16 v[134:135], %[tr] offset:5824\n\t" \
    "ds_read_b64_tr_b16 v[156:157], %[tr] offset:224\n\t" \
    "ds_read_b64_tr_b16 v[158:159], %[tr] offset:5856\n\t" \
    "ds_read_b64_tr_b16 v[172:173], %[tr] offset:256\n\t" \
    "ds_read_b64_tr_b16 v[174:175], %[tr] offset:5888\n\t" \
    "ds_read_b64_tr_b16 v[196:197], %[tr] offset:288\n\t" \
    "ds_read_b64_tr_b16 v[198:199], %[tr] offset:5920\n\t" \
    "s_waitcnt lgkmcnt(0)\n\t" \
    "s_barrier \n\t" \
    "ds_write_b128 %[wl], v[200:203] offset:0\n\t" \
    "ds_write_b128 %[wl], v[204:207] offset:1024\n\t" \
    "ds_write_b128 %[wl], v[208:211] offset:2048\n\t" \
    "ds_write_b128 %[wl], v[212:215] offset:3072\n\t" \
    "ds_write_b128 %[wl], v[216:219] offset:4096\n\t" \
    "ds_write_b128 %[wl], v[220:223] offset:5120\n\t" \
    "ds_write_b128 %[wl], v[224:227] offset:6144\n\t" \
    "ds_write_b128 %[wl], v[228:231] offset:7168\n\t" \
    "ds_write_b128 %[wl], v[232:235] offset:8192\n\t" \
    "ds_write_b128 %[wl], v[236:239] offset:9216\n\t" \
    "s_waitcnt lgkmcnt(0)\n\t"
#define ASM_XG_S_LO \
    "s_waitcnt lgkmcnt(0)\n\t" \
    "ds_read_b128 v[224:227], %[xa] offset:16384\n\t" \
    "ds_read_b128 v[228:231], %[xa] offset:17408\n\t" \
    "ds_read_b128 v[244:247], %[wl] offset:0\n\t" \
    "ds_read_b128 v[248:251], %[wl] offset:1024\n\t" \
    "ds_read_b128 v[232:235], %[xa] offset:0\n\t" \
    "ds_read_b128 v[236:239], %[xa] offset:1024\n\t" \
    "v_xor_b32 v252, 0x800, %[xa]\n\t" \
    "ds_read_b128 v[240:243], v252 offset:0\n\t" \
    "s_waitcnt lgkmcnt(6)\n\t" \
    "v_mfma_f32_16x16x32_bf16 v[220:223], v[8:11], v[224:227], 0\n\t" \
    "s_waitcnt lgkmcnt(5)\n\t" \
    "v_mfma_f32_16x16x32_bf16 v[220:223], v[12:15], v[228:231], v[220:223]\n\t" \
    "v_mfma_f32_16x16x32_bf16 v[216:219], v[0:3], v[224:227], 0\n\t" \
    "v_mfma_f32_16x16x32_bf16 v[216:219], v[4:7], v[228:231], v[216:219]\n\t" \
    "s_waitcnt lgkmcnt(4)\n\t" \
    "v_mfma_f32_16x16x32_bf16 v[212:215], v[244:247], v[224:227], 0\n\t" \
    "ds_read_b128 v[224:227], v252 offset:1024\n\t" \
    "s_waitcnt lgkmcnt(4)\n\t" \
    "v_mfma_f32_16x16x32_bf16 v[212:215], v[248:251], v[228:231], v[212:215]\n\t" \
    "ds_read_b128 v[228:231], v252 offset:16384\n\t" \
    "s_waitcnt lgkmcnt(4)\n\t" \
    "v_mfma_f32_16x16x32_bf16 v[200:203], v[244:247], v[232:235], 0\n\t" \
    "ds_read_b128 v[244:247], %[wl] offset:2048\n\t" \
    "s_waitcnt lgkmcnt(4)\n\t" \
    "v_mfma_f32_16x16x32_bf16 v[200:203], v[248:251], v[236:239], v[200:203]\n\t" \
    "ds_read_b128 v[248:251], %[wl] offset:3072\n\t" \
    "v_mfma_f32_16x16x32_bf16 v[204:207], v[0:3], v[232:235], 0\n\t" \
    "v_mfma_f32_16x16x32_bf16 v[204:207], v[4:7], v[236:239], v[204:207]\n\t" \
    "v_mfma_f32_16x16x32_bf16 v[208:211], v[8:11], v[232:235], 0\n\t" \
    "ds_read_b128 v[232:235], v252 offset:17408\n\t" \
    "v_mfma_f32_16x16x32_bf16 v[208:211], v[12:15], v[236:239], v[208:211]\n\t" \
    "v_xor_b32 v252, 0x1000, %[xa]\n\t" \
    "ds_read_b128 v[236:239], v252 offset:16384\n\t" \
    "s_waitcnt lgkmcnt(6)\n\t" \
    "v_mfma_f32_16x16x32_bf16 v[208:211], v[32:35], v[240:243], v[208:211]\n\t" \
    "s_waitcnt lgkmcnt(5)\n\t" \
    "v_mfma_f32_16x16x32_bf16 v[208:211], v[36:39], v[224:227], v[208:211]\n\t" \
    "v_mfma_f32_16x16x32_bf16 v[204:207], v[24:27], v[240:243], v[204:207]\n\t" \
    "v_mfma_f32_16x16x32_bf16 v[204:207], v[28:31], v[224:227], v[204:207]\n\t" \
    "v_mfma_f32_16x16x32_bf16 v[200:203], v[16:19], v[240:243], v[200:203]\n\t" \
    "ds_read_b128 v[240:243], v252 offset:17408\n\t" \
    "v_mfma_f32_16x16x32_bf16 v[200:203], v[20:23], v[224:227], v[200:203]\n\t" \
    "ds_read_b128 v[224:227], v252 offset:0\n\t" \
    "s_waitcnt lgkmcnt(6)\n\t" \
    "v_mfma_f32_16x16x32_bf16 v[212:215], v[16:19], v[228:231], v[212:215]\n\t" \
    "s_waitcnt lgkmcnt(3)\n\t" \
    "v_mfma_f32_16x16x32_bf16 v[212:215], v[20:23], v[232:235], v[212:215]\n\t" \
    "v_mfma_f32_16x16x32_bf16 v[216:219], v[24:27], v[228:231], v[216:219]\n\t" \
    "v_mfma_f32_16x16x32_bf16 v[216:219], v[28:31], v[232:235], v[216:219]\n\t" \
    "v_mfma_f32_16x16x32_bf16 v[220:223], v[32:35], v[228:231], v[220:223]\n\t" \
    "ds_read_b128 v[228:231], v252 offset:1024\n\t" \
    "v_mfma_f32_16x16x32_bf16 v[220:223], v[36:39], v[232:235], v[220:223]\n\t" \
    "v_xor_b32 v252, 0x1800, %[xa]\n\t" \
    "ds_read_b128 v[232:235], v252 offset:0\n\t" \
    "s_waitcnt lgkmcnt(4)\n\t" \
    "v_mfma_f32_16x16x32_bf16 v[220:223], v[48:51], v[236:239], v[220:223]\n\t" \
    "s_waitcnt lgkmcnt(3)\n\t" \
    "v_mfma_f32_16x16x32_bf16 v[220:223], v[52:55], v[240:243], v[220:223]\n\t" \
    "v_mfma_f32_16x16x32_bf16 v[216:219], v[40:43], v[236:239], v[216:219]\n\t" \
    "v_mfma_f32_16x16x32_bf16 v[216:219], v[44:47], v[240:243], v[216:219]\n\t" \
    "v_mfma_f32_16x16x32_bf16 v[212:215], v[244:247], v[236:239], v[212:215]\n\t" \
    "ds_read_b128 v[236:239], v252 offset:1024\n\t" \
    "v_mfma_f32_16x16x32_bf16 v[212:215], v[248:251], v[240:243], v[212:215]\n\t" \
    "ds_read_b128 v[240:243], v252 offset:16384\n\t" \
    "s_waitcnt lgkmcnt(4)\n\t" \
    "v_mfma_f32_16x16x32_bf16 v[200:203], v[244:247], v[224:227], v[200:203]\n\t" \
    "ds_read_b128 v[244:247], %[wl] offset:8192\n\t" \
    "s_waitcnt lgkmcnt(4)\n\t" \
    "v_mfma_f32_16x16x32_bf16 v[200:203], v[248:251], v[228:231], v[200:203]\n\t" \
    "ds_read_b128 v[248:251], %[wl] offset:9216\n\t" \
    "v_mfma_f32_16x16x32_bf16 v[204:207], v[40:43], v[224:227], v[204:207]\n\t" \
    "v_mfma_f32_16x16x32_bf16 v[204:207], v[44:47], v[228:231], v[204:207]\n\t" \
    "v_mfma_f32_16x16x32_bf16 v[208:211], v[48:51], v[224:227], v[208:211]\n\t" \
    "ds_read_b128 v[224:227], v252 offset:17408\n\t" \
    "v_mfma_f32_16x16x32_bf16 v[208:211], v[52:55], v[228:231], v[208:211]\n\t" \
    "v_and_b32 v252, 0x3ff, %[xa]\n\t" \
    "ds_read_b128 v[228:231], v252 offset:45056\n\t" \
    "s_waitcnt lgkmcnt(6)\n\t" \
    "v_mfma_f32_16x16x32_bf16 v[208:211], v[72:75], v[232:235], v[208:211]\n\t" \
    "s_waitcnt lgkmcnt(5)\n\t" \
    "v_mfma_f32_16x16x32_bf16 v[208:211], v[76:79], v[236:239], v[208:211]\n\t" \
    "v_mfma_f32_16x16x32_bf16 v[204:207], v[64:67], v[232:235], v[204:207]\n\t" \
    "v_mfma_f32_16x16x32_bf16 v[204:207], v[68:71], v[236:239], v[204:207]\n\t" \
    "v_mfma_f32_16x16x32_bf16 v[200:203], v[56:59], v[232:235], v[200:203]\n\t" \
    "ds_read_b128 v[232:235], v252 offset:46080\n\t" \
    "v_mfma_f32_16x16x32_bf16 v[200:203], v[60:63], v[236:239], v[200:203]\n\t" \
    "ds_read_b128 v[236:239], v252 offset:40960\n\t" \
    "s_waitcnt lgkmcnt(6)\n\t" \
    "v_mfma_f32_16x16x32_bf16 v[212:215], v[56:59], v[240:243], v[212:215]\n\t" \
    "s_waitcnt lgkmcnt(3)\n\t" \
    "v_mfma_f32_16x16x32_bf16 v[212:215], v[60:63], v[224:227], v[212:215]\n\t" \
    "v_mfma_f32_16x16x32_bf16 v[216:219], v[64:67], v[240:243], v[216:219]\n\t" \
    "v_mfma_f32_16x16x32_bf16 v[216:219], v[68:71], v[224:227], v[216:219]\n\t" \
    "v_mfma_f32_16x16x32_bf16 v[220:223], v[72:75], v[240:243], v[220:223]\n\t" \
    "ds_read_b128 v[240:243], v252 offset:41984\n\t" \
    "v_mfma_f32_16x16x32_bf16 v[220:223], v[76:79], v[224:227], v[220:223]\n\t" \
    "s_waitcnt lgkmcnt(0)\n\t" \
    "s_barrier\n\t" ASM_ST \
    "v_xor_b32 v252, 0x2000, %[xa]\n\t" \
    "ds_read_b128 v[224:227], v252 offset:0\n\t" \
    "v_mfma_f32_16x16x32_bf16 v[220:223], v[168:171], v[228:231], v[220:223]\n\t" \
    "v_mfma_f32_16x16x32_bf16 v[220:223], v[172:175], v[232:235], v[220:223]\n\t" \
    "v_mfma_f32_16x16x32_bf16 v[216:219], v[160:163], v[228:231], v[216:219]\n\t" \
    "v_mfma_f32_16x16x32_bf16 v[216:219], v[164:167], v[232:235], v[216:219]\n\t" \
    "v_mfma_f32_16x16x32_bf16 v[212:215], v[244:247], v[228:231], v[212:215]\n\t" \
    "ds_read_b128 v[228:231], v252 offset:1024\n\t" \
    "v_mfma_f32_16x16x32_bf16 v[212:215], v[248:251], v[232:235], v[212:215]\n\t" \
    "ds_read_b128 v[232:235], v252 offset:16384\n\t" \
    "v_mfma_f32_16x16x32_bf16 v[200:203], v[244:247], v[236:239], v[200:203]\n\t" \
    "ds_read_b128 v[244:247], %[wl] offset:4096\n\t" \
    "v_mfma_f32_16x16x32_bf16 v[200:203], v[248:251], v[240:243], v[200:203]\n\t" \
    "ds_read_b128 v[248:251], %[wl] offset:5120\n\t" \
    "v_mfma_f32_16x16x32_bf16 v[204:207], v[160:163], v[236:239], v[204:207]\n\t" \
    "v_mfma_f32_16x16x32_bf16 v[204:207], v[164:167], v[240:243], v[204:207]\n\t" \
    "v_mfma_f32_16x16x32_bf16 v[208:211], v[168:171], v[236:239], v[208:211]\n\t" \
    "ds_read_b128 v[236:239], v252 offset:17408\n\t" \
    "v_mfma_f32_16x16x32_bf16 v[208:211], v[172:175], v[240:243], v[208:211]\n\t" \
    "v_xor_b32 v252, 0x2800, %[xa]\n\t" \
    "ds_read_b128 v[240:243], v252 offset:16384\n\t" \
    "s_waitcnt lgkmcnt(6)\n\t" \
    "v_mfma_f32_16x16x32_bf16 v[208:211], v[88:91], v[224:227], v[208:211]\n\t" \
    "s_waitcnt lgkmcnt(5)\n\t" \
    "v_mfma_f32_16x16x32_bf16 v[208:211], v[92:95], v[228:231], v[208:211]\n\t" \
    "v_mfma_f32_16x16x32_bf16 v[204:207], v[80:83], v[224:227], v[204:207]\n\t" \
    "v_mfma_f32_16x16x32_bf16 v[204:207], v[84:87], v[228:231], v[204:207]\n\t" \
    "s_waitcnt lgkmcnt(3)\n\t" \
    "v_mfma_f32_16x16x32_bf16 v[200:203], v[244:247], v[224:227], v[200:203]\n\t" \
    "ds_read_b128 v[224:227], v252 offset:17408\n\t" \
    "s_waitcnt lgkmcnt(3)\n\t" \
    "v_mfma_f32_16x16x32_bf16 v[200:203], v[248:251], v[228:231], v[200:203]\n\t" \
    "ds_read_b128 v[228:231], v252 offset:0\n\t" \
    "v_mfma_f32_16x16x32_bf16 v[212:215], v[244:247], v[232:235], v[212:215]\n\t" \
    "ds_read_b128 v[244:247], %[wl] offset:6144\n\t" \
    "s_waitcnt lgkmcnt(4)\n\t" \
    "v_mfma_f32_16x16x32_bf16 v[212:215], v[248:251], v[236:239], v[212:215]\n\t" \
    "ds_read_b128 v[248:251], %[wl] offset:7168\n\t" \
    "v_mfma_f32_16x16x32_bf16 v[216:219], v[80:83], v[232:235], v[216:219]\n\t" \
    "v_mfma_f32_16x16x32_bf16 v[216:219], v[84:87], v[236:239], v[216:219]\n\t" \
    "v_mfma_f32_16x16x32_bf16 v[220:223], v[88:91], v[232:235], v[220:223]\n\t" \
    "ds_read_b128 v[232:235], v252 offset:1024\n\t" \
    "v_mfma_f32_16x16x32_bf16 v[220:223], v[92:95], v[236:239], v[220:223]\n\t" \
    "v_xor_b32 v252, 0x3000, %[xa]\n\t" \
    "ds_read_b128 v[236:239], v252 offset:0\n\t" \
    "s_waitcnt lgkmcnt(6)\n\t" \
    "v_mfma_f32_16x16x32_bf16 v[220:223], v[112:115], v[240:243], v[220:223]\n\t" \
    "s_waitcnt lgkmcnt(5)\n\t" \
    "v_mfma_f32_16x16x32_bf16 v[220:223], v[116:119], v[224:227], v[220:223]\n\t" \
    "v_mfma_f32_16x16x32_bf16 v[216:219], v[104:107], v[240:243], v[216:219]\n\t" \
    "v_mfma_f32_16x16x32_bf16 v[216:219], v[108:111], v[224:227], v[216:219]\n\t" \
    "v_mfma_f32_16x16x32_bf16 v[212:215], v[96:99], v[240:243], v[212:215]\n\t" \
    "ds_read_b128 v[240:243], v252 offset:1024\n\t" \
    "v_mfma_f32_16x16x32_bf16 v[212:215], v[100:103], v[224:227], v[212:215]\n\t" \
    "ds_read_b128 v[224:227], v252 offset:16384\n\t" \
    "s_waitcnt lgkmcnt(6)\n\t" \
    "v_mfma_f32_16x16x32_bf16 v[200:203], v[96:99], v[228:231], v[200:203]\n\t" \
    "s_waitcnt lgkmcnt(3)\n\t" \
    "v_mfma_f32_16x16x32_bf16 v[200:203], v[100:103], v[232:235], v[200:203]\n\t" \
    "v_mfma_f32_16x16x32_bf16 v[204:207], v[104:107], v[228:231], v[204:207]\n\t" \
    "v_mfma_f32_16x16x32_bf16 v[204:207], v[108:111], v[232:235], v[204:207]\n\t" \
    "v_mfma_f32_16x16x32_bf16 v[208:211], v[112:115], v[228:231], v[208:211]\n\t" \
    "ds_read_b128 v[228:231], v252 offset:17408\n\t" \
    "v_mfma_f32_16x16x32_bf16 v[208:211], v[116:119], v[232:235], v[208:211]\n\t" \
    "v_xor_b32 v252, 0x3800, %[xa]\n\t" \
    "ds_read_b128 v[232:235], v252 offset:16384\n\t" \
    "s_waitcnt lgkmcnt(4)\n\t" \
    "v_mfma_f32_16x16x32_bf16 v[208:211], v[128:131], v[236:239], v[208:211]\n\t" \
    "s_waitcnt lgkmcnt(3)\n\t" \
    "v_mfma_f32_16x16x32_bf16 v[208:211], v[132:135], v[240:243], v[208:211]\n\t" \
    "v_mfma_f32_16x16x32_bf16 v[204:207], v[120:123], v[236:239], v[204:207]\n\t" \
    "v_mfma_f32_16x16x32_bf16 v[204:207], v[124:127], v[240:243], v[204:207]\n\t" \
    "v_mfma_f32_16x16x32_bf16 v[200:203], v[244:247], v[236:239], v[200:203]\n\t" \
    "ds_read_b128 v[236:239], v252 offset:17408\n\t" \
    "v_mfma_f32_16x16x32_bf16 v[200:203], v[248:251], v[240:243], v[200:203]\n\t" \
    "ds_read_b128 v[240:243], v252 offset:0\n\t" \
    "s_waitcnt lgkmcnt(4)\n\t" \
    "v_mfma_f32_16x16x32_bf16 v[212:215], v[244:247], v[224:227], v[212:215]\n\t" \
    "s_waitcnt lgkmcnt(3)\n\t" \
    "v_mfma_f32_16x16x32_bf16 v[212:215], v[248:251], v[228:231], v[212:215]\n\t" \
    "v_mfma_f32_16x16x32_bf16 v[216:219], v[120:123], v[224:227], v[216:219]\n\t" \
    "v_mfma_f32_16x16x32_bf16 v[216:219], v[124:127], v[228:231], v[216:219]\n\t" \
    "v_mfma_f32_16x16x32_bf16 v[220:223], v[128:131], v[224:227], v[220:223]\n\t" \
    "ds_read_b128 v[224:227], v252 offset:1024\n\t" \
    "v_mfma_f32_16x16x32_bf16 v[220:223], v[132:135], v[228:231], v[220:223]\n\t" \
    "v_and_b32 v252, 0x3ff, %[xa]\n\t" \
    "ds_read_b128 v[228:231], v252 offset:43008\n\t" \
    "s_waitcnt lgkmcnt(4)\n\t" \
    "v_mfma_f32_16x16x32_bf16 v[220:223], v[152:155], v[232:235], v[220:223]\n\t" \
    "v_mov_b64 v[244:245], 0\n\t" \
    "s_waitcnt lgkmcnt(3)\n\t" \
    "v_mfma_f32_16x16x32_bf16 v[220:223], v[156:159], v[236:239], v[220:223]\n\t" \
    "v_mov_b64 v[246:247], 0\n\t" \
    "v_mfma_f32_16x16x32_bf16 v[216:219], v[144:147], v[232:235], v[216:219]\n\t" \
    "v_mov_b64 v[248:249], 0\n\t" \
    "v_mfma_f32_16x16x32_bf16 v[216:219], v[148:151], v[236:239], v[216:219]\n\t" \
    "v_mov_b64 v[250:251], 0\n\t" \
    "v_mfma_f32_16x16x32_bf16 v[212:215], v[136:139], v[232:235], v[212:215]\n\t" \
    "ds_read_b128 v[232:235], v252 offset:44032\n\t" \
    "v_mfma_f32_16x16x32_bf16 v[212:215], v[140:143], v[236:239], v[212:215]\n\t" \
    "ds_read_b128 v[236:239], v252 offset:47104\n\t" \
    "s_waitcnt lgkmcnt(4)\n\t" \
    "v_mfma_f32_16x16x32_bf16 v[200:203], v[136:139], v[240:243], v[200:203]\n\t" \
    "s_waitcnt lgkmcnt(3)\n\t" \
    "v_mfma_f32_16x16x32_bf16 v[200:203], v[140:143], v[224:227], v[200:203]\n\t" \
    "v_mfma_f32_16x16x32_bf16 v[204:207], v[144:147], v[240:243], v[204:207]\n\t" \
    "v_mfma_f32_16x16x32_bf16 v[204:207], v[148:151], v[224:227], v[204:207]\n\t" \
    "v_mfma_f32_16x16x32_bf16 v[208:211], v[152:155], v[240:243], v[208:211]\n\t" \
    "ds_read_b128 v[240:243], v252 offset:48128\n\t" \
    "v_mfma_f32_16x16x32_bf16 v[208:211], v[156:159], v[224:227], v[208:211]\n\t" \
    "s_waitcnt lgkmcnt(3)\n\t" \
    "v_mfma_f32_16x16x32_bf16 v[208:211], v[192:195], v[228:231], v[208:211]\n\t" \
    "v_add_u32 v252, %[qs], v252\n\t" \
    "v_mov_b64 v[224:225], 0\n\t" \
    "s_waitcnt lgkmcnt(2)\n\t" \
    "v_mfma_f32_16x16x32_bf16 v[208:211], v[196:199], v[232:235], v[208:211]\n\t" \
    "v_mov_b64 v[226:227], 0\n\t" \
    "v_mfma_f32_16x16x32_bf16 v[204:207], v[184:187], v[228:231], v[204:207]\n\t" \
    "v_mfma_f32_16x16x32_bf16 v[204:207], v[188:191], v[232:235], v[204:207]\n\t" \
    "v_mfma_f32_16x16x32_bf16 v[200:203], v[176:179], v[228:231], v[200:203]\n\t" \
    "v_mfma_f32_16x16x32_bf16 v[200:203], v[180:183], v[232:235], v[200:203]\n\t" \
    "s_waitcnt lgkmcnt(1)\n\t" \
    "v_mfma_f32_16x16x32_bf16 v[212:215], v[176:179], v[236:239], v[212:215]\n\t" \
    "ds_read_b128 v[228:231], %[wl] offset:2048\n\t" \
    "ds_read_b128 v[232:235], %[wl] offset:3072\n\t" \
    "s_waitcnt lgkmcnt(2)\n\t" \
    "v_mfma_f32_16x16x32_bf16 v[212:215], v[180:183], v[240:243], v[212:215]\n\t" \
    "v_cvt_pk_bf16_f32 v208, v208, v209\n\t" \
    "v_cvt_pk_bf16_f32 v209, v210, v211\n\t" \
    "v_mfma_f32_16x16x32_bf16 v[216:219], v[184:187], v[236:239], v[216:219]\n\t" \
    "ds_write_b64 v252, v[208:209] offset:2048\n\t" \
    "v_cvt_pk_bf16_f32 v204, v204, v205\n\t" \
    "v_mfma_f32_16x16x32_bf16 v[216:219], v[188:191], v[240:243], v[216:219]\n\t" \
    "v_cvt_pk_bf16_f32 v205, v206, v207\n\t" \
    "ds_write_b64 v252, v[204:205] offset:1024\n\t" \
    "v_mfma_f32_16x16x32_bf16 v[220:223], v[192:195], v[236:239], v[220:223]\n\t" \
    "v_cvt_pk_bf16_f32 v200, v200, v201\n\t" \
    "v_cvt_pk_bf16_f32 v201, v202, v203\n\t" \
    "v_mfma_f32_16x16x32_bf16 v[220:223], v[196:199], v[240:243], v[220:223]\n\t" \
    "ds_write_b64 v252, v[200:201] offset:0\n\t" \
    "ds_read_b128 v[200:203], %[wl] offset:0\n\t" \
    "ds_read_b128 v[204:207], %[wl] offset:1024\n\t" \
    "v_cvt_pk_bf16_f32 v212, v212, v213\n\t" \
    "v_cvt_pk_bf16_f32 v213, v214, v215\n\t" \
    "ds_write_b64 v252, v[212:213] offset:6144\n\t" \
    "v_cvt_pk_bf16_f32 v216, v216, v217\n\t" \
    "v_cvt_pk_bf16_f32 v217, v218, v219\n\t" \
    "ds_write_b64 v252, v[216:217] offset:7168\n\t" \
    "v_cvt_pk_bf16_f32 v220, v220, v221\n\t" \
    "v_cvt_pk_bf16_f32 v221, v222, v223\n\t" \
    "ds_write_b64 v252, v[220:221] offset:8192\n\t" \
    "s_waitcnt lgkmcnt(0)\n\t"
#define ASM_XG_S_HI \
    "s_waitcnt lgkmcnt(0)\n\t" \
    "v_xor_b32 v252, 0x2000, %[xa]\n\t" \
    "ds_read_b128 v[224:227], v252 offset:16384\n\t" \
    "ds_read_b128 v[228:231], v252 offset:17408\n\t" \
    "ds_read_b128 v[244:247], %[wl] offset:4096\n\t" \
    "ds_read_b128 v[248:251], %[wl] offset:5120\n\t" \
    "ds_read_b128 v[232:235], v252 offset:0\n\t" \
    "ds_read_b128 v[236:239], v252 offset:1024\n\t" \
    "v_xor_b32 v252, 0x2800, %[xa]\n\t" \
    "ds_read_b128 v[240:243], v252 offset:0\n\t" \
    "s_cmp_eq_u32 %[slp], 0\n\t" \
    "s_cbranch_scc1 .Lxs%=\n\t" \
    "s_sleep 10\n\t" \
    ".Lxs%=:\n\t" \
    "s_waitcnt lgkmcnt(6)\n\t" \
    "v_mfma_f32_16x16x32_bf16 v[220:223], v[88:91], v[224:227], 0\n\t" \
    "s_waitcnt lgkmcnt(5)\n\t" \
    "v_mfma_f32_16x16x32_bf16 v[220:223], v[92:95], v[228:231], v[220:223]\n\t" \
    "v_mfma_f32_16x16x32_bf16 v[216:219], v[80:83], v[224:227], 0\n\t" \
    "v_mfma_f32_16x16x32_bf16 v[216:219], v[84:87], v[228:231], v[216:219]\n\t" \
    "s_waitcnt lgkmcnt(4)\n\t" \
    "v_mfma_f32_16x16x32_bf16 v[212:215], v[244:247], v[224:227], 0\n\t" \
    "ds_read_b128 v[224:227], v252 offset:1024\n\t" \
    "s_waitcnt lgkmcnt(4)\n\t" \
    "v_mfma_f32_16x16x32_bf16 v[212:215], v[248:251], v[228:231], v[212:215]\n\t" \
    "ds_read_b128 v[228:231], v252 offset:16384\n\t" \
    "s_waitcnt lgkmcnt(4)\n\t" \
    "v_mfma_f32_16x16x32_bf16 v[200:203], v[244:247], v[232:235], 0\n\t" \
    "ds_read_b128 v[244:247], %[wl] offset:6144\n\t" \
    "s_waitcnt lgkmcnt(4)\n\t" \
    "v_mfma_f32_16x16x32_bf16 v[200:203], v[248:251], v[236:239], v[200:203]\n\t" \
    "ds_read_b128 v[248:251], %[wl] offset:7168\n\t" \
    "v_mfma_f32_16x16x32_bf16 v[204:207], v[80:83], v[232:235], 0\n\t" \
    "v_mfma_f32_16x16x32_bf16 v[204:207], v[84:87], v[236:239], v[204:207]\n\t" \
    "v_mfma_f32_16x16x32_bf16 v[208:211], v[88:91], v[232:235], 0\n\t" \
    "ds_read_b128 v[232:235], v252 offset:17408\n\t" \
    "v_mfma_f32_16x16x32_bf16 v[208:211], v[92:95], v[236:239], v[208:211]\n\t" \
    "v_xor_b32 v252, 0x3000, %[xa]\n\t" \
    "ds_read_b128 v[236:239], v252 offset:16384\n\t" \
    "s_waitcnt lgkmcnt(6)\n\t" \
    "v_mfma_f32_16x16x32_bf16 v[208:211], v[112:115], v[240:243], v[208:211]\n\t" \
    "s_waitcnt lgkmcnt(5)\n\t" \
    "v_mfma_f32_16x16x32_bf16 v[208:211], v[116:119], v[224:227], v[208:211]\n\t" \
    "v_mfma_f32_16x16x32_bf16 v[204:207], v[104:107], v[240:243], v[204:207]\n\t" \
    "v_mfma_f32_16x16x32_bf16 v[204:207], v[108:111], v[224:227], v[204:207]\n\t" \
    "v_mfma_f32_16x16x32_bf16 v[200:203], v[96:99], v[240:243], v[200:203]\n\t" \
    "ds_read_b128 v[240:243], v252 offset:17408\n\t" \
    "v_mfma_f32_16x16x32_bf16 v[200:203], v[100:103], v[224:227], v[200:203]\n\t" \
    "ds_read_b128 v[224:227], v252 offset:0\n\t" \
    "s_waitcnt lgkmcnt(6)\n\t" \
    "v_mfma_f32_16x16x32_bf16 v[212:215], v[96:99], v[228:231], v[212:215]\n\t" \
    "s_waitcnt lgkmcnt(3)\n\t" \
    "v_mfma_f32_16x16x32_bf16 v[212:215], v[100:103], v[232:235], v[212:215]\n\t" \
    "v_mfma_f32_16x16x32_bf16 v[216:219], v[104:107], v[228:231], v[216:219]\n\t" \
    "v_mfma_f32_16x16x32_bf16 v[216:219], v[108:111], v[232:235], v[216:219]\n\t" \
    "v_mfma_f32_16x16x32_bf16 v[220:223], v[112:115], v[228:231], v[220:223]\n\t" \
    "ds_read_b128 v[228:231], v252 offset:1024\n\t" \
    "v_mfma_f32_16x16x32_bf16 v[220:223], v[116:119], v[232:235], v[220:223]\n\t" \
    "v_xor_b32 v252, 0x3800, %[xa]\n\t" \
    "ds_read_b128 v[232:235], v252 offset:0\n\t" \
    "s_waitcnt lgkmcnt(4)\n\t" \
    "v_mfma_f32_16x16x32_bf16 v[220:223], v[128:131], v[236:239], v[220:223]\n\t" \
    "s_waitcnt lgkmcnt(3)\n\t" \
    "v_mfma_f32_16x16x32_bf16 v[220:223], v[132:135], v[240:243], v[220:223]\n\t" \
    "v_mfma_f32_16x16x32_bf16 v[216:219], v[120:123], v[236:239], v[216:219]\n\t" \
    "v_mfma_f32_16x16x32_bf16 v[216:219], v[124:127], v[240:243], v[216:219]\n\t" \
    "v_mfma_f32_16x16x32_bf16 v[212:215], v[244:247], v[236:239], v[212:215]\n\t" \
    "ds_read_b128 v[236:239], v252 offset:1024\n\t" \
    "v_mfma_f32_16x16x32_bf16 v[212:215], v[248:251], v[240:243], v[212:215]\n\t" \
    "ds_read_b128 v[240:243], v252 offset:16384\n\t" \
    "s_waitcnt lgkmcnt(4)\n\t" \
    "v_mfma_f32_16x16x32_bf16 v[200:203], v[244:247], v[224:227], v[200:203]\n\t" \
    "ds_read_b128 v[244:247], %[wl] offset:8192\n\t" \
    "s_waitcnt lgkmcnt(4)\n\t" \
    "v_mfma_f32_16x16x32_bf16 v[200:203], v[248:251], v[228:231], v[200:203]\n\t" \
    "ds_read_b128 v[248:251], %[wl] offset:9216\n\t" \
    "v_mfma_f32_16x16x32_bf16 v[204:207], v[120:123], v[224:227], v[204:207]\n\t" \
    "v_mfma_f32_16x16x32_bf16 v[204:207], v[124:127], v[228:231], v[204:207]\n\t" \
    "v_mfma_f32_16x16x32_bf16 v[208:211], v[128:131], v[224:227], v[208:211]\n\t" \
    "ds_read_b128 v[224:227], v252 offset:17408\n\t" \
    "v_mfma_f32_16x16x32_bf16 v[208:211], v[132:135], v[228:231], v[208:211]\n\t" \
    "v_and_b32 v252, 0x3ff, %[xa]\n\t" \
    "ds_read_b128 v[228:231], v252 offset:45056\n\t" \
    "s_waitcnt lgkmcnt(6)\n\t" \
    "v_mfma_f32_16x16x32_bf16 v[208:211], v[152:155], v[232:235], v[208:211]\n\t" \
    "s_waitcnt lgkmcnt(5)\n\t" \
    "v_mfma_f32_16x16x32_bf16 v[208:211], v[156:159], v[236:239], v[208:211]\n\t" \
    "v_mfma_f32_16x16x32_bf16 v[204:207], v[144:147], v[232:235], v[204:207]\n\t" \
    "v_mfma_f32_16x16x32_bf16 v[204:207], v[148:151], v[236:239], v[204:207]\n\t" \
    "v_mfma_f32_16x16x32_bf16 v[200:203], v[136:139], v[232:235], v[200:203]\n\t" \
    "ds_read_b128 v[232:235], v252 offset:46080\n\t" \
    "v_mfma_f32_16x16x32_bf16 v[200:203], v[140:143], v[236:239], v[200:203]\n\t" \
    "ds_read_b128 v[236:239], v252 offset:40960\n\t" \
    "s_waitcnt lgkmcnt(6)\n\t" \
    "v_mfma_f32_16x16x32_bf16 v[212:215], v[136:139], v[240:243], v[212:215]\n\t" \
    "s_waitcnt lgkmcnt(3)\n\t" \
    "v_mfma_f32_16x16x32_bf16 v[212:215], v[140:143], v[224:227], v[212:215]\n\t" \
    "v_mfma_f32_16x16x32_bf16 v[216:219], v[144:147], v[240:243], v[216:219]\n\t" \
    "v_mfma_f32_16x16x32_bf16 v[216:219], v[148:151], v[224:227], v[216:219]\n\t" \
    "v_mfma_f32_16x16x32_bf16 v[220:223], v[152:155], v[240:243], v[220:223]\n\t" \
    "ds_read_b128 v[240:243], v252 offset:41984\n\t" \
    "v_mfma_f32_16x16x32_bf16 v[220:223], v[156:159], v[224:227], v[220:223]\n\t" \
    "s_waitcnt vmcnt(0) lgkmcnt(0)\n\t" \
    "s_barrier\n\t" ASM_ST \
    "s_mov_b32 m0, %[d0]\n\t" \
    "s_nop 0\n\t" \
    "global_load_lds_dwordx4 v252, %[g0] offset:0 sc1\n\t" \
    "global_load_lds_dwordx4 v252, %[g0] offset:1024 sc1\n\t" \
    "global_load_lds_dwordx4 v252, %[g0] offset:2048 sc1\n\t" \
    "global_load_lds_dwordx4 v252, %[g0] offset:3072 sc1\n\t" \
    "s_mov_b32 m0, %[d1]\n\t" \
    "s_nop 0\n\t" \
    "global_load_lds_dwordx4 v252, %[g1] sc1\n\t" \
    "ds_read_b128 v[224:227], %[xa] offset:0\n\t" \
    "v_mfma_f32_16x16x32_bf16 v[220:223], v[168:171], v[228:231], v[220:223]\n\t" \
    "v_mfma_f32_16x16x32_bf16 v[220:223], v[172:175], v[232:235], v[220:223]\n\t" \
    "v_mfma_f32_16x16x32_bf16 v[216:219], v[160:163], v[228:231], v[216:219]\n\t" \
    "v_mfma_f32_16x16x32_bf16 v[216:219], v[164:167], v[232:235], v[216:219]\n\t" \
    "v_mfma_f32_16x16x32_bf16 v[212:215], v[244:247], v[228:231], v[212:215]\n\t" \
    "ds_read_b128 v[228:231], %[xa] offset:1024\n\t" \
    "v_mfma_f32_16x16x32_bf16 v[212:215], v[248:251], v[232:235], v[212:215]\n\t" \
    "ds_read_b128 v[232:235], %[xa] offset:16384\n\t" \
    "v_mfma_f32_16x16x32_bf16 v[200:203], v[244:247], v[236:239], v[200:203]\n\t" \
    "ds_read_b128 v[244:247], %[wl] offset:0\n\t" \
    "v_mfma_f32_16x16x32_bf16 v[200:203], v[248:251], v[240:243], v[200:203]\n\t" \
    "ds_read_b128 v[248:251], %[wl] offset:1024\n\t" \
    "v_mfma_f32_16x16x32_bf16 v[204:207], v[160:163], v[236:239], v[204:207]\n\t" \
    "v_mfma_f32_16x16x32_bf16 v[204:207], v[164:167], v[240:243], v[204:207]\n\t" \
    "v_mfma_f32_16x16x32_bf16 v[208:211], v[168:171], v[236:239], v[208:211]\n\t" \
    "ds_read_b128 v[236:239], %[xa] offset:17408\n\t" \
    "v_mfma_f32_16x16x32_bf16 v[208:211], v[172:175], v[240:243], v[208:211]\n\t" \
    "v_xor_b32 v252, 0x800, %[xa]\n\t" \
    "ds_read_b128 v[240:243], v252 offset:16384\n\t" \
    "s_waitcnt lgkmcnt(6)\n\t" \
    "v_mfma_f32_16x16x32_bf16 v[208:211], v[8:11], v[224:227], v[208:211]\n\t" \
    "s_waitcnt lgkmcnt(5)\n\t" \
    "v_mfma_f32_16x16x32_bf16 v[208:211], v[12:15], v[228:231], v[208:211]\n\t" \
    "v_mfma_f32_16x16x32_bf16 v[204:207], v[0:3], v[224:227], v[204:207]\n\t" \
    "v_mfma_f32_16x16x32_bf16 v[204:207], v[4:7], v[228:231], v[204:207]\n\t" \
    "s_waitcnt lgkmcnt(3)\n\t" \
    "v_mfma_f32_16x16x32_bf16 v[200:203], v[244:247], v[224:227], v[200:203]\n\t" \
    "ds_read_b128 v[224:227], v252 offset:17408\n\t" \
    "s_waitcnt lgkmcnt(3)\n\t" \
    "v_mfma_f32_16x16x32_bf16 v[200:203], v[248:251], v[228:231], v[200:203]\n\t" \
    "ds_read_b128 v[228:231], v252 offset:0\n\t" \
    "v_mfma_f32_16x16x32_bf16 v[212:215], v[244:247], v[232:235], v[212:215]\n\t" \
    "ds_read_b128 v[244:247], %[wl] offset:2048\n\t" \
    "s_waitcnt lgkmcnt(4)\n\t" \
    "v_mfma_f32_16x16x32_bf16 v[212:215], v[248:251], v[236:239], v[212:215]\n\t" \
    "ds_read_b128 v[248:251], %[wl] offset:3072\n\t" \
    "v_mfma_f32_16x16x32_bf16 v[216:219], v[0:3], v[232:235], v[216:219]\n\t" \
    "v_mfma_f32_16x16x32_bf16 v[216:219], v[4:7], v[236:239], v[216:219]\n\t" \
    "v_mfma_f32_16x16x32_bf16 v[220:223], v[8:11], v[232:235], v[220:223]\n\t" \
    "ds_read_b128 v[232:235], v252 offset:1024\n\t" \
    "v_mfma_f32_16x16x32_bf16 v[220:223], v[12:15], v[236:239], v[220:223]\n\t" \
    "v_xor_b32 v252, 0x1000, %[xa]\n\t" \
    "ds_read_b128 v[236:239], v252 offset:0\n\t" \
    "s_waitcnt lgkmcnt(6)\n\t" \
    "v_mfma_f32_16x16x32_bf16 v[220:223], v[32:35], v[240:243], v[220:223]\n\t" \
    "s_waitcnt lgkmcnt(5)\n\t" \
    "v_mfma_f32_16x16x32_bf16 v[220:223], v[36:39], v[224:227], v[220:223]\n\t" \
    "v_mfma_f32_16x16x32_bf16 v[216:219], v[24:27], v[240:243], v[216:219]\n\t" \
    "v_mfma_f32_16x16x32_bf16 v[216:219], v[28:31], v[224:227], v[216:219]\n\t" \
    "v_mfma_f32_16x16x32_bf16 v[212:215], v[16:19], v[240:243], v[212:215]\n\t" \
    "ds_read_b128 v[240:243], v252 offset:1024\n\t" \
    "v_mfma_f32_16x16x32_bf16 v[212:215], v[20:23], v[224:227], v[212:215]\n\t" \
    "ds_read_b128 v[224:227], v252 offset:16384\n\t" \
    "s_waitcnt lgkmcnt(6)\n\t" \
    "v_mfma_f32_16x16x32_bf16 v[200:203], v[16:19], v[228:231], v[200:203]\n\t" \
    "s_waitcnt lgkmcnt(3)\n\t" \
    "v_mfma_f32_16x16x32_bf16 v[200:203], v[20:23], v[232:235], v[200:203]\n\t" \
    "v_mfma_f32_16x16x32_bf16 v[204:207], v[24:27], v[228:231], v[204:207]\n\t" \
    "v_mfma_f32_16x16x32_bf16 v[204:207], v[28:31], v[232:235], v[204:207]\n\t" \
    "v_mfma_f32_16x16x32_bf16 v[208:211], v[32:35], v[228:231], v[208:211]\n\t" \
    "ds_read_b128 v[228:231], v252 offset:17408\n\t" \
    "v_mfma_f32_16x16x32_bf16 v[208:211], v[36:39], v[232:235], v[208:211]\n\t" \
    "v_xor_b32 v252, 0x1800, %[xa]\n\t" \
    "ds_read_b128 v[232:235], v252 offset:16384\n\t" \
    "s_waitcnt lgkmcnt(4)\n\t" \
    "v_mfma_f32_16x16x32_bf16 v[208:211], v[48:51], v[236:239], v[208:211]\n\t" \
    "s_waitcnt lgkmcnt(3)\n\t" \
    "v_mfma_f32_16x16x32_bf16 v[208:211], v[52:55], v[240:243], v[208:211]\n\t" \
    "v_mfma_f32_16x16x32_bf16 v[204:207], v[40:43], v[236:239], v[204:207]\n\t" \
    "v_mfma_f32_16x16x32_bf16 v[204:207], v[44:47], v[240:243], v[204:207]\n\t" \
    "v_mfma_f32_16x16x32_bf16 v[200:203], v[244:247], v[236:239], v[200:203]\n\t" \
    "ds_read_b128 v[236:239], v252 offset:17408\n\t" \
    "v_mfma_f32_16x16x32_bf16 v[200:203], v[248:251], v[240:243], v[200:203]\n\t" \
    "ds_read_b128 v[240:243], v252 offset:0\n\t" \
    "s_waitcnt lgkmcnt(4)\n\t" \
    "v_mfma_f32_16x16x32_bf16 v[212:215], v[244:247], v[224:227], v[212:215]\n\t" \
    "s_waitcnt lgkmcnt(3)\n\t" \
    "v_mfma_f32_16x16x32_bf16 v[212:215], v[248:251], v[228:231], v[212:215]\n\t" \
    "v_mfma_f32_16x16x32_bf16 v[216:219], v[40:43], v[224:227], v[216:219]\n\t" \
    "v_mfma_f32_16x16x32_bf16 v[216:219], v[44:47], v[228:231], v[216:219]\n\t" \
    "v_mfma_f32_16x16x32_bf16 v[220:223], v[48:51], v[224:227], v[220:223]\n\t" \
    "ds_read_b128 v[224:227], v252 offset:1024\n\t" \
    "v_mfma_f32_16x16x32_bf16 v[220:223], v[52:55], v[228:231], v[220:223]\n\t" \
    "v_and_b32 v252, 0x3ff, %[xa]\n\t" \
    "ds_read_b128 v[228:231], v252 offset:43008\n\t" \
    "s_waitcnt lgkmcnt(4)\n\t" \
    "v_mfma_f32_16x16x32_bf16 v[220:223], v[72:75], v[232:235], v[220:223]\n\t" \
    "v_mov_b64 v[244:245], 0\n\t" \
    "s_waitcnt lgkmcnt(3)\n\t" \
    "v_mfma_f32_16x16x32_bf16 v[220:223], v[76:79], v[236:239], v[220:223]\n\t" \
    "v_mov_b64 v[246:247], 0\n\t" \
    "v_mfma_f32_16x16x32_bf16 v[216:219], v[64:67], v[232:235], v[216:219]\n\t" \
    "v_mov_b64 v[248:249], 0\n\t" \
    "v_mfma_f32_16x16x32_bf16 v[216:219], v[68:71], v[236:239], v[216:219]\n\t" \
    "v_mov_b64 v[250:251], 0\n\t" \
    "v_mfma_f32_16x16x32_bf16 v[212:215], v[56:59], v[232:235], v[212:215]\n\t" \
    "ds_read_b128 v[232:235], v252 offset:44032\n\t" \
    "v_mfma_f32_16x16x32_bf16 v[212:215], v[60:63], v[236:239], v[212:215]\n\t" \
    "ds_read_b128 v[236:239], v252 offset:47104\n\t" \
    "s_waitcnt lgkmcnt(4)\n\t" \
    "v_mfma_f32_16x16x32_bf16 v[200:203], v[56:59], v[240:243], v[200:203]\n\t" \
    "s_waitcnt lgkmcnt(3)\n\t" \
    "v_mfma_f32_16x16x32_bf16 v[200:203], v[60:63], v[224:227], v[200:203]\n\t" \
    "v_mfma_f32_16x16x32_bf16 v[204:207], v[64:67], v[240:243], v[204:207]\n\t" \
    "v_mfma_f32_16x16x32_bf16 v[204:207], v[68:71], v[224:227], v[204:207]\n\t" \
    "v_mfma_f32_16x16x32_bf16 v[208:211], v[72:75], v[240:243], v[208:211]\n\t" \
    "ds_read_b128 v[240:243], v252 offset:48128\n\t" \
    "v_mfma_f32_16x16x32_bf16 v[208:211], v[76:79], v[224:227], v[208:211]\n\t" \
    "s_waitcnt lgkmcnt(3)\n\t" \
    "v_mfma_f32_16x16x32_bf16 v[208:211], v[192:195], v[228:231], v[208:211]\n\t" \
    "v_add_u32 v252, %[qs], v252\n\t" \
    "v_mov_b64 v[224:225], 0\n\t" \
    "s_waitcnt lgkmcnt(2)\n\t" \
    "v_mfma_f32_16x16x32_bf16 v[208:211], v[196:199], v[232:235], v[208:211]\n\t" \
    "v_mov_b64 v[226:227], 0\n\t" \
    "v_mfma_f32_16x16x32_bf16 v[204:207], v[184:187], v[228:231], v[204:207]\n\t" \
    "v_mfma_f32_16x16x32_bf16 v[204:207], v[188:191], v[232:235], v[204:207]\n\t" \
    "v_mfma_f32_16x16x32_bf16 v[200:203], v[176:179], v[228:231], v[200:203]\n\t" \
    "v_mfma_f32_16x16x32_bf16 v[200:203], v[180:183], v[232:235], v[200:203]\n\t" \
    "s_waitcnt lgkmcnt(1)\n\t" \
    "v_mfma_f32_16x16x32_bf16 v[212:215], v[176:179], v[236:239], v[212:215]\n\t" \
    "ds_read_b128 v[228:231], %[wl] offset:2048\n\t" \
    "ds_read_b128 v[232:235], %[wl] offset:3072\n\t" \
    "s_waitcnt lgkmcnt(2)\n\t" \
    "v_mfma_f32_16x16x32_bf16 v[212:215], v[180:183], v[240:243], v[212:215]\n\t" \
    "v_cvt_pk_bf16_f32 v208, v208, v209\n\t" \
    "v_cvt_pk_bf16_f32 v209, v210, v211\n\t" \
    "v_mfma_f32_16x16x32_bf16 v[216:219], v[184:187], v[236:239], v[216:219]\n\t" \
    "ds_write_b64 v252, v[208:209] offset:2048\n\t" \
    "v_cvt_pk_bf16_f32 v204, v204, v205\n\t" \
    "v_mfma_f32_16x16x32_bf16 v[216:219], v[188:191], v[240:243], v[216:219]\n\t" \
    "v_cvt_pk_bf16_f32 v205, v206, v207\n\t" \
    "ds_write_b64 v252, v[204:205] offset:1024\n\t" \
    "v_mfma_f32_16x16x32_bf16 v[220:223], v[192:195], v[236:239], v[220:223]\n\t" \
    "v_cvt_pk_bf16_f32 v200, v200, v201\n\t" \
    "v_cvt_pk_bf16_f32 v201, v202, v203\n\t" \
    "v_mfma_f32_16x16x32_bf16 v[220:223], v[196:199], v[240:243], v[220:223]\n\t" \
    "ds_write_b64 v252, v[200:201] offset:0\n\t" \
    "ds_read_b128 v[200:203], %[wl] offset:0\n\t" \
    "ds_read_b128 v[204:207], %[wl] offset:1024\n\t" \
    "v_cvt_pk_bf16_f32 v212, v212, v213\n\t" \
    "v_cvt_pk_bf16_f32 v213, v214, v215\n\t" \
    "ds_write_b64 v252, v[212:213] offset:6144\n\t" \
    "v_cvt_pk_bf16_f32 v216, v216, v217\n\t" \
    "v_cvt_pk_bf16_f32 v217, v218, v219\n\t" \
    "ds_write_b64 v252, v[216:217] offset:7168\n\t" \
    "v_cvt_pk_bf16_f32 v220, v220, v221\n\t" \
    "v_cvt_pk_bf16_f32 v221, v222, v223\n\t" \
    "ds_write_b64 v252, v[220:221] offset:8192\n\t" \
    "s_waitcnt lgkmcnt(0)\n\t"
#define ASM_XG_S_LO2 \
    "s_waitcnt lgkmcnt(0)\n\t" \
    "ds_read_b128 v[224:227], %[xa] offset:16384\n\t" \
    "ds_read_b128 v[228:231], %[xa] offset:17408\n\t" \
    "v_xor_b32 v252, 0x800, %[xa]\n\t" \
    "ds_read_b128 v[232:235], v252 offset:16384\n\t" \
    "ds_read_b128 v[236:239], v252 offset:17408\n\t" \
    "v_xor_b32 v252, 0x1000, %[xa]\n\t" \
    "ds_read_b128 v[240:243], v252 offset:16384\n\t" \
    "ds_read_b128 v[212:215], v252 offset:17408\n\t" \
    "v_xor_b32 v252, 0x1800, %[xa]\n\t" \
    "ds_read_b128 v[200:203], v252 offset:16384\n\t" \
    "s_waitcnt lgkmcnt(6)\n\t" \
    "v_mfma_f32_16x16x32_bf16 v[220:223], v[8:11], v[224:227], 0\n\t" \
    "s_waitcnt lgkmcnt(5)\n\t" \
    "v_mfma_f32_16x16x32_bf16 v[220:223], v[12:15], v[228:231], v[220:223]\n\t" \
    "v_mfma_f32_16x16x32_bf16 v[216:219], v[0:3], v[224:227], 0\n\t" \
    "ds_read_b128 v[224:227], v252 offset:17408\n\t" \
    "v_mfma_f32_16x16x32_bf16 v[216:219], v[4:7], v[228:231], v[216:219]\n\t" \
    "v_and_b32 v252, 0x3ff, %[xa]\n\t" \
    "ds_read_b128 v[228:231], v252 offset:45056\n\t" \
    "s_waitcnt lgkmcnt(6)\n\t" \
    "v_mfma_f32_16x16x32_bf16 v[216:219], v[24:27], v[232:235], v[216:219]\n\t" \
    "s_waitcnt lgkmcnt(5)\n\t" \
    "v_mfma_f32_16x16x32_bf16 v[216:219], v[28:31], v[236:239], v[216:219]\n\t" \
    "v_mfma_f32_16x16x32_bf16 v[220:223], v[32:35], v[232:235], v[220:223]\n\t" \
    "ds_read_b128 v[232:235], v252 offset:46080\n\t" \
    "v_mfma_f32_16x16x32_bf16 v[220:223], v[36:39], v[236:239], v[220:223]\n\t" \
    "ds_read_b128 v[236:239], %[xa] offset:0\n\t" \
    "s_waitcnt lgkmcnt(6)\n\t" \
    "v_mfma_f32_16x16x32_bf16 v[220:223], v[48:51], v[240:243], v[220:223]\n\t" \
    "s_waitcnt lgkmcnt(5)\n\t" \
    "v_mfma_f32_16x16x32_bf16 v[220:223], v[52:55], v[212:215], v[220:223]\n\t" \
    "v_mfma_f32_16x16x32_bf16 v[216:219], v[40:43], v[240:243], v[216:219]\n\t" \
    "ds_read_b128 v[240:243], %[xa] offset:1024\n\t" \
    "v_mfma_f32_16x16x32_bf16 v[216:219], v[44:47], v[212:215], v[216:219]\n\t" \
    "v_xor_b32 v252, 0x800, %[xa]\n\t" \
    "ds_read_b128 v[212:215], v252 offset:0\n\t" \
    "s_waitcnt lgkmcnt(6)\n\t" \
    "v_mfma_f32_16x16x32_bf16 v[216:219], v[64:67], v[200:203], v[216:219]\n\t" \
    "s_waitcnt lgkmcnt(5)\n\t" \
    "v_mfma_f32_16x16x32_bf16 v[216:219], v[68:71], v[224:227], v[216:219]\n\t" \
    "v_mfma_f32_16x16x32_bf16 v[220:223], v[72:75], v[200:203], v[220:223]\n\t" \
    "ds_read_b128 v[200:203], v252 offset:1024\n\t" \
    "v_mfma_f32_16x16x32_bf16 v[220:223], v[76:79], v[224:227], v[220:223]\n\t" \
    "v_xor_b32 v252, 0x1000, %[xa]\n\t" \
    "ds_read_b128 v[224:227], v252 offset:0\n\t" \
    "s_waitcnt lgkmcnt(6)\n\t" \
    "v_mfma_f32_16x16x32_bf16 v[220:223], v[168:171], v[228:231], v[220:223]\n\t" \
    "s_waitcnt lgkmcnt(5)\n\t" \
    "v_mfma_f32_16x16x32_bf16 v[220:223], v[172:175], v[232:235], v[220:223]\n\t" \
    "v_mfma_f32_16x16x32_bf16 v[216:219], v[160:163], v[228:231], v[216:219]\n\t" \
    "ds_read_b128 v[228:231], v252 offset:1024\n\t" \
    "v_mfma_f32_16x16x32_bf16 v[216:219], v[164:167], v[232:235], v[216:219]\n\t" \
    "v_xor_b32 v252, 0x1800, %[xa]\n\t" \
    "ds_read_b128 v[232:235], v252 offset:0\n\t" \
    "s_waitcnt lgkmcnt(6)\n\t" \
    "v_mfma_f32_16x16x32_bf16 v[204:207], v[0:3], v[236:239], 0\n\t" \
    "s_waitcnt lgkmcnt(5)\n\t" \
    "v_mfma_f32_16x16x32_bf16 v[204:207], v[4:7], v[240:243], v[204:207]\n\t" \
    "v_mfma_f32_16x16x32_bf16 v[208:211], v[8:11], v[236:239], 0\n\t" \
    "ds_read_b128 v[236:239], v252 offset:1024\n\t" \
    "v_mfma_f32_16x16x32_bf16 v[208:211], v[12:15], v[240:243], v[208:211]\n\t" \
    "v_and_b32 v252, 0x3ff, %[xa]\n\t" \
    "ds_read_b128 v[240:243], v252 offset:40960\n\t" \
    "s_waitcnt lgkmcnt(6)\n\t" \
    "v_mfma_f32_16x16x32_bf16 v[208:211], v[32:35], v[212:215], v[208:211]\n\t" \
    "s_waitcnt lgkmcnt(5)\n\t" \
    "v_mfma_f32_16x16x32_bf16 v[208:211], v[36:39], v[200:203], v[208:211]\n\t" \
    "v_mfma_f32_16x16x32_bf16 v[204:207], v[24:27], v[212:215], v[204:207]\n\t" \
    "ds_read_b128 v[212:215], v252 offset:41984\n\t" \
    "v_mfma_f32_16x16x32_bf16 v[204:207], v[28:31], v[200:203], v[204:207]\n\t" \
    "s_waitcnt lgkmcnt(0)\n\t" \
    "s_barrier\n\t" ASM_ST \
    "v_xor_b32 v252, 0x2000, %[xa]\n\t" \
    "ds_read_b128 v[200:203], v252 offset:0\n\t" \
    "v_mfma_f32_16x16x32_bf16 v[204:207], v[40:43], v[224:227], v[204:207]\n\t" \
    "v_mfma_f32_16x16x32_bf16 v[204:207], v[44:47], v[228:231], v[204:207]\n\t" \
    "v_mfma_f32_16x16x32_bf16 v[208:211], v[48:51], v[224:227], v[208:211]\n\t" \
    "ds_read_b128 v[224:227], v252 offset:1024\n\t" \
    "v_mfma_f32_16x16x32_bf16 v[208:211], v[52:55], v[228:231], v[208:211]\n\t" \
    "v_xor_b32 v252, 0x2800, %[xa]\n\t" \
    "ds_read_b128 v[228:231], v252 offset:0\n\t" \
    "v_mfma_f32_16x16x32_bf16 v[208:211], v[72:75], v[232:235], v[208:211]\n\t" \
    "v_mfma_f32_16x16x32_bf16 v[208:211], v[76:79], v[236:239], v[208:211]\n\t" \
    "v_mfma_f32_16x16x32_bf16 v[204:207], v[64:67], v[232:235], v[204:207]\n\t" \
    "ds_read_b128 v[232:235], v252 offset:1024\n\t" \
    "v_mfma_f32_16x16x32_bf16 v[204:207], v[68:71], v[236:239], v[204:207]\n\t" \
    "v_xor_b32 v252, 0x3000, %[xa]\n\t" \
    "ds_read_b128 v[236:239], v252 offset:0\n\t" \
    "v_mfma_f32_16x16x32_bf16 v[204:207], v[160:163], v[240:243], v[204:207]\n\t" \
    "v_mfma_f32_16x16x32_bf16 v[204:207], v[164:167], v[212:215], v[204:207]\n\t" \
    "v_mfma_f32_16x16x32_bf16 v[208:211], v[168:171], v[240:243], v[208:211]\n\t" \
    "ds_read_b128 v[240:243], v252 offset:1024\n\t" \
    "v_mfma_f32_16x16x32_bf16 v[208:211], v[172:175], v[212:215], v[208:211]\n\t" \
    "v_xor_b32 v252, 0x3800, %[xa]\n\t" \
    "ds_read_b128 v[212:215], v252 offset:0\n\t" \
    "s_waitcnt lgkmcnt(6)\n\t" \
    "v_mfma_f32_16x16x32_bf16 v[208:211], v[88:91], v[200:203], v[208:211]\n\t" \
    "s_waitcnt lgkmcnt(5)\n\t" \
    "v_mfma_f32_16x16x32_bf16 v[208:211], v[92:95], v[224:227], v[208:211]\n\t" \
    "v_mfma_f32_16x16x32_bf16 v[204:207], v[80:83], v[200:203], v[204:207]\n\t" \
    "ds_read_b128 v[200:203], v252 offset:1024\n\t" \
    "v_mfma_f32_16x16x32_bf16 v[204:207], v[84:87], v[224:227], v[204:207]\n\t" \
    "v_and_b32 v252, 0x3ff, %[xa]\n\t" \
    "ds_read_b128 v[224:227], v252 offset:43008\n\t" \
    "s_waitcnt lgkmcnt(6)\n\t" \
    "v_mfma_f32_16x16x32_bf16 v[204:207], v[104:107], v[228:231], v[204:207]\n\t" \
    "s_waitcnt lgkmcnt(5)\n\t" \
    "v_mfma_f32_16x16x32_bf16 v[204:207], v[108:111], v[232:235], v[204:207]\n\t" \
    "v_mfma_f32_16x16x32_bf16 v[208:211], v[112:115], v[228:231], v[208:211]\n\t" \
    "ds_read_b128 v[228:231], v252 offset:44032\n\t" \
    "v_mfma_f32_16x16x32_bf16 v[208:211], v[116:119], v[232:235], v[208:211]\n\t" \
    "v_xor_b32 v252, 0x2000, %[xa]\n\t" \
    "ds_read_b128 v[232:235], v252 offset:16384\n\t" \
    "s_waitcnt lgkmcnt(6)\n\t" \
    "v_mfma_f32_16x16x32_bf16 v[208:211], v[128:131], v[236:239], v[208:211]\n\t" \
    "s_waitcnt lgkmcnt(5)\n\t" \
    "v_mfma_f32_16x16x32_bf16 v[208:211], v[132:135], v[240:243], v[208:211]\n\t" \
    "v_mfma_f32_16x16x32_bf16 v[204:207], v[120:123], v[236:239], v[204:207]\n\t" \
    "ds_read_b128 v[236:239], v252 offset:17408\n\t" \
    "v_mfma_f32_16x16x32_bf16 v[204:207], v[124:127], v[240:243], v[204:207]\n\t" \
    "v_xor_b32 v252, 0x2800, %[xa]\n\t" \
    "ds_read_b128 v[240:243], v252 offset:16384\n\t" \
    "s_waitcnt lgkmcnt(6)\n\t" \
    "v_mfma_f32_16x16x32_bf16 v[204:207], v[144:147], v[212:215], v[204:207]\n\t" \
    "s_waitcnt lgkmcnt(5)\n\t" \
    "v_mfma_f32_16x16x32_bf16 v[204:207], v[148:151], v[200:203], v[204:207]\n\t" \
    "v_mfma_f32_16x16x32_bf16 v[208:211], v[152:155], v[212:215], v[208:211]\n\t" \
    "ds_read_b128 v[212:215], v252 offset:17408\n\t" \
    "v_mfma_f32_16x16x32_bf16 v[208:211], v[156:159], v[200:203], v[208:211]\n\t" \
    "v_xor_b32 v252, 0x3000, %[xa]\n\t" \
    "ds_read_b128 v[200:203], v252 offset:16384\n\t" \
    "s_waitcnt lgkmcnt(6)\n\t" \
    "v_mfma_f32_16x16x32_bf16 v[208:211], v[192:195], v[224:227], v[208:211]\n\t" \
    "s_waitcnt lgkmcnt(5)\n\t" \
    "v_mfma_f32_16x16x32_bf16 v[208:211], v[196:199], v[228:231], v[208:211]\n\t" \
    "v_mfma_f32_16x16x32_bf16 v[204:207], v[184:187], v[224:227], v[204:207]\n\t" \
    "ds_read_b128 v[224:227], v252 offset:17408\n\t" \
    "v_mfma_f32_16x16x32_bf16 v[204:207], v[188:191], v[228:231], v[204:207]\n\t" \
    "v_xor_b32 v252, 0x3800, %[xa]\n\t" \
    "ds_read_b128 v[228:231], v252 offset:16384\n\t" \
    "s_waitcnt lgkmcnt(6)\n\t" \
    "v_mfma_f32_16x16x32_bf16 v[216:219], v[80:83], v[232:235], v[216:219]\n\t" \
    "s_waitcnt lgkmcnt(5)\n\t" \
    "v_mfma_f32_16x16x32_bf16 v[216:219], v[84:87], v[236:239], v[216:219]\n\t" \
    "v_mfma_f32_16x16x32_bf16 v[220:223], v[88:91], v[232:235], v[220:223]\n\t" \
    "ds_read_b128 v[232:235], v252 offset:17408\n\t" \
    "v_mfma_f32_16x16x32_bf16 v[220:223], v[92:95], v[236:239], v[220:223]\n\t" \
    "v_and_b32 v252, 0x3ff, %[xa]\n\t" \
    "ds_read_b128 v[236:239], v252 offset:47104\n\t" \
    "s_waitcnt lgkmcnt(6)\n\t" \
    "v_mfma_f32_16x16x32_bf16 v[220:223], v[112:115], v[240:243], v[220:223]\n\t" \
    "v_mov_b64 v[244:245], 0\n\t" \
    "s_waitcnt lgkmcnt(5)\n\t" \
    "v_mfma_f32_16x16x32_bf16 v[220:223], v[116:119], v[212:215], v[220:223]\n\t" \
    "v_mov_b64 v[246:247], 0\n\t" \
    "v_mfma_f32_16x16x32_bf16 v[216:219], v[104:107], v[240:243], v[216:219]\n\t" \
    "ds_read_b128 v[240:243], v252 offset:48128\n\t" \
    "v_mov_b64 v[248:249], 0\n\t" \
    "v_mfma_f32_16x16x32_bf16 v[216:219], v[108:111], v[212:215], v[216:219]\n\t" \
    "v_mov_b64 v[250:251], 0\n\t" \
    "s_waitcnt lgkmcnt(5)\n\t" \
    "v_mfma_f32_16x16x32_bf16 v[216:219], v[120:123], v[200:203], v[216:219]\n\t" \
    "s_waitcnt lgkmcnt(4)\n\t" \
    "v_mfma_f32_16x16x32_bf16 v[216:219], v[124:127], v[224:227], v[216:219]\n\t" \
    "v_mfma_f32_16x16x32_bf16 v[220:223], v[128:131], v[200:203], v[220:223]\n\t" \
    "v_mfma_f32_16x16x32_bf16 v[220:223], v[132:135], v[224:227], v[220:223]\n\t" \
    "s_waitcnt lgkmcnt(3)\n\t" \
    "v_mfma_f32_16x16x32_bf16 v[220:223], v[152:155], v[228:231], v[220:223]\n\t" \
    "v_add_u32 v252, %[qs], v252\n\t" \
    "v_mov_b64 v[224:225], 0\n\t" \
    "s_waitcnt lgkmcnt(2)\n\t" \
    "v_mfma_f32_16x16x32_bf16 v[220:223], v[156:159], v[232:235], v[220:223]\n\t" \
    "v_mov_b64 v[226:227], 0\n\t" \
    "v_cvt_pk_bf16_f32 v208, v208, v209\n\t" \
    "v_mfma_f32_16x16x32_bf16 v[216:219], v[144:147], v[228:231], v[216:219]\n\t" \
    "v_cvt_pk_bf16_f32 v209, v210, v211\n\t" \
    "ds_write_b64 v252, v[208:209] offset:2048\n\t" \
    "v_mfma_f32_16x16x32_bf16 v[216:219], v[148:151], v[232:235], v[216:219]\n\t" \
    "v_cvt_pk_bf16_f32 v204, v204, v205\n\t" \
    "v_cvt_pk_bf16_f32 v205, v206, v207\n\t" \
    "s_waitcnt lgkmcnt(2)\n\t" \
    "v_mfma_f32_16x16x32_bf16 v[216:219], v[184:187], v[236:239], v[216:219]\n\t" \
    "ds_write_b64 v252, v[204:205] offset:1024\n\t" \
    "ds_read_b128 v[228:231], %[wl] offset:2048\n\t" \
    "s_waitcnt lgkmcnt(3)\n\t" \
    "v_mfma_f32_16x16x32_bf16 v[216:219], v[188:191], v[240:243], v[216:219]\n\t" \
    "ds_read_b128 v[232:235], %[wl] offset:3072\n\t" \
    "v_mfma_f32_16x16x32_bf16 v[220:223], v[192:195], v[236:239], v[220:223]\n\t" \
    "v_mfma_f32_16x16x32_bf16 v[220:223], v[196:199], v[240:243], v[220:223]\n\t" \
    "ds_read_b128 v[200:203], %[wl] offset:0\n\t" \
    "ds_read_b128 v[204:207], %[wl] offset:1024\n\t" \
    "v_cvt_pk_bf16_f32 v216, v216, v217\n\t" \
    "v_cvt_pk_bf16_f32 v217, v218, v219\n\t" \
    "ds_write_b64 v252, v[216:217] offset:7168\n\t" \
    "s_nop 2\n\t" \
    "v_cvt_pk_bf16_f32 v220, v220, v221\n\t" \
    "v_cvt_pk_bf16_f32 v221, v222, v223\n\t" \
    "ds_write_b64 v252, v[220:221] offset:8192\n\t" \
    "s_waitcnt lgkmcnt(0)\n\t"
#define ASM_XG_S_LO1 \
    "s_waitcnt lgkmcnt(0)\n\t" \
    "ds_read_b128 v[224:227], %[xa] offset:16384\n\t" \
    "ds_read_b128 v[228:231], %[xa] offset:17408\n\t" \
    "v_xor_b32 v252, 0x800, %[xa]\n\t" \
    "ds_read_b128 v[232:235], v252 offset:16384\n\t" \
    "ds_read_b128 v[236:239], v252 offset:17408\n\t" \
    "v_xor_b32 v252, 0x1000, %[xa]\n\t" \
    "ds_read_b128 v[240:243], v252 offset:16384\n\t" \
    "ds_read_b128 v[212:215], v252 offset:17408\n\t" \
    "v_xor_b32 v252, 0x1800, %[xa]\n\t" \
    "ds_read_b128 v[200:203], v252 offset:16384\n\t" \
    "s_waitcnt lgkmcnt(6)\n\t" \
    "v_mfma_f32_16x16x32_bf16 v[220:223], v[8:11], v[224:227], 0\n\t" \
    "ds_read_b128 v[224:227], v252 offset:17408\n\t" \
    "s_waitcnt lgkmcnt(6)\n\t" \
    "v_mfma_f32_16x16x32_bf16 v[220:223], v[12:15], v[228:231], v[220:223]\n\t" \
    "v_and_b32 v252, 0x3ff, %[xa]\n\t" \
    "ds_read_b128 v[228:231], v252 offset:45056\n\t" \
    "s_waitcnt lgkmcnt(6)\n\t" \
    "v_mfma_f32_16x16x32_bf16 v[220:223], v[32:35], v[232:235], v[220:223]\n\t" \
    "ds_read_b128 v[232:235], v252 offset:46080\n\t" \
    "s_waitcnt lgkmcnt(6)\n\t" \
    "v_mfma_f32_16x16x32_bf16 v[220:223], v[36:39], v[236:239], v[220:223]\n\t" \
    "ds_read_b128 v[236:239], %[xa] offset:0\n\t" \
    "s_waitcnt lgkmcnt(6)\n\t" \
    "v_mfma_f32_16x16x32_bf16 v[220:223], v[48:51], v[240:243], v[220:223]\n\t" \
    "ds_read_b128 v[240:243], %[xa] offset:1024\n\t" \
    "s_waitcnt lgkmcnt(6)\n\t" \
    "v_mfma_f32_16x16x32_bf16 v[220:223], v[52:55], v[212:215], v[220:223]\n\t" \
    "v_xor_b32 v252, 0x800, %[xa]\n\t" \
    "ds_read_b128 v[212:215], v252 offset:0\n\t" \
    "s_waitcnt lgkmcnt(6)\n\t" \
    "v_mfma_f32_16x16x32_bf16 v[220:223], v[72:75], v[200:203], v[220:223]\n\t" \
    "ds_read_b128 v[200:203], v252 offset:1024\n\t" \
    "s_waitcnt lgkmcnt(6)\n\t" \
    "v_mfma_f32_16x16x32_bf16 v[220:223], v[76:79], v[224:227], v[220:223]\n\t" \
    "v_xor_b32 v252, 0x1000, %[xa]\n\t" \
    "ds_read_b128 v[224:227], v252 offset:0\n\t" \
    "s_waitcnt lgkmcnt(6)\n\t" \
    "v_mfma_f32_16x16x32_bf16 v[220:223], v[168:171], v[228:231], v[220:223]\n\t" \
    "ds_read_b128 v[228:231], v252 offset:1024\n\t" \
    "s_waitcnt lgkmcnt(6)\n\t" \
    "v_mfma_f32_16x16x32_bf16 v[220:223], v[172:175], v[232:235], v[220:223]\n\t" \
    "v_xor_b32 v252, 0x1800, %[xa]\n\t" \
    "ds_read_b128 v[232:235], v252 offset:0\n\t" \
    "s_waitcnt lgkmcnt(6)\n\t" \
    "v_mfma_f32_16x16x32_bf16 v[208:211], v[8:11], v[236:239], 0\n\t" \
    "ds_read_b128 v[236:239], v252 offset:1024\n\t" \
    "s_waitcnt lgkmcnt(6)\n\t" \
    "v_mfma_f32_16x16x32_bf16 v[208:211], v[12:15], v[240:243], v[208:211]\n\t" \
    "v_and_b32 v252, 0x3ff, %[xa]\n\t" \
    "ds_read_b128 v[240:243], v252 offset:40960\n\t" \
    "s_waitcnt lgkmcnt(6)\n\t" \
    "v_mfma_f32_16x16x32_bf16 v[208:211], v[32:35], v[212:215], v[208:211]\n\t" \
    "ds_read_b128 v[212:215], v252 offset:41984\n\t" \
    "s_waitcnt lgkmcnt(6)\n\t" \
    "v_mfma_f32_16x16x32_bf16 v[208:211], v[36:39], v[200:203], v[208:211]\n\t" \
    "s_waitcnt lgkmcnt(0)\n\t" \
    "s_barrier\n\t" ASM_ST \
    "v_xor_b32 v252, 0x2000, %[xa]\n\t" \
    "ds_read_b128 v[200:203], v252 offset:0\n\t" \
    "v_mfma_f32_16x16x32_bf16 v[208:211], v[48:51], v[224:227], v[208:211]\n\t" \
    "ds_read_b128 v[224:227], v252 offset:1024\n\t" \
    "v_mfma_f32_16x16x32_bf16 v[208:211], v[52:55], v[228:231], v[208:211]\n\t" \
    "v_xor_b32 v252, 0x2800, %[xa]\n\t" \
    "ds_read_b128 v[228:231], v252 offset:0\n\t" \
    "v_mfma_f32_16x16x32_bf16 v[208:211], v[72:75], v[232:235], v[208:211]\n\t" \
    "ds_read_b128 v[232:235], v252 offset:1024\n\t" \
    "v_mfma_f32_16x16x32_bf16 v[208:211], v[76:79], v[236:239], v[208:211]\n\t" \
    "v_xor_b32 v252, 0x3000, %[xa]\n\t" \
    "ds_read_b128 v[236:239], v252 offset:0\n\t" \
    "v_mfma_f32_16x16x32_bf16 v[208:211], v[168:171], v[240:243], v[208:211]\n\t" \
    "ds_read_b128 v[240:243], v252 offset:1024\n\t" \
    "v_mfma_f32_16x16x32_bf16 v[208:211], v[172:175], v[212:215], v[208:211]\n\t" \
    "v_xor_b32 v252, 0x3800, %[xa]\n\t" \
    "ds_read_b128 v[212:215], v252 offset:0\n\t" \
    "s_waitcnt lgkmcnt(6)\n\t" \
    "v_mfma_f32_16x16x32_bf16 v[208:211], v[88:91], v[200:203], v[208:211]\n\t" \
    "ds_read_b128 v[200:203], v252 offset:1024\n\t" \
    "s_waitcnt lgkmcnt(6)\n\t" \
    "v_mfma_f32_16x16x32_bf16 v[208:211], v[92:95], v[224:227], v[208:211]\n\t" \
    "v_and_b32 v252, 0x3ff, %[xa]\n\t" \
    "ds_read_b128 v[224:227], v252 offset:43008\n\t" \
    "s_waitcnt lgkmcnt(6)\n\t" \
    "v_mfma_f32_16x16x32_bf16 v[208:211], v[112:115], v[228:231], v[208:211]\n\t" \
    "ds_read_b128 v[228:231], v252 offset:44032\n\t" \
    "s_waitcnt lgkmcnt(6)\n\t" \
    "v_mfma_f32_16x16x32_bf16 v[208:211], v[116:119], v[232:235], v[208:211]\n\t" \
    "v_xor_b32 v252, 0x2000, %[xa]\n\t" \
    "ds_read_b128 v[232:235], v252 offset:16384\n\t" \
    "s_waitcnt lgkmcnt(6)\n\t" \
    "v_mfma_f32_16x16x32_bf16 v[208:211], v[128:131], v[236:239], v[208:211]\n\t" \
    "ds_read_b128 v[236:239], v252 offset:17408\n\t" \
    "s_waitcnt lgkmcnt(6)\n\t" \
    "v_mfma_f32_16x16x32_bf16 v[208:211], v[132:135], v[240:243], v[208:211]\n\t" \
    "v_xor_b32 v252, 0x2800, %[xa]\n\t" \
    "ds_read_b128 v[240:243], v252 offset:16384\n\t" \
    "s_waitcnt lgkmcnt(6)\n\t" \
    "v_mfma_f32_16x16x32_bf16 v[208:211], v[152:155], v[212:215], v[208:211]\n\t" \
    "ds_read_b128 v[212:215], v252 offset:17408\n\t" \
    "s_waitcnt lgkmcnt(6)\n\t" \
    "v_mfma_f32_16x16x32_bf16 v[208:211], v[156:159], v[200:203], v[208:211]\n\t" \
    "v_xor_b32 v252, 0x3000, %[xa]\n\t" \
    "ds_read_b128 v[200:203], v252 offset:16384\n\t" \
    "s_waitcnt lgkmcnt(6)\n\t" \
    "v_mfma_f32_16x16x32_bf16 v[208:211], v[192:195], v[224:227], v[208:211]\n\t" \
    "ds_read_b128 v[224:227], v252 offset:17408\n\t" \
    "s_waitcnt lgkmcnt(6)\n\t" \
    "v_mfma_f32_16x16x32_bf16 v[208:211], v[196:199], v[228:231], v[208:211]\n\t" \
    "v_xor_b32 v252, 0x3800, %[xa]\n\t" \
    "ds_read_b128 v[228:231], v252 offset:16384\n\t" \
    "s_waitcnt lgkmcnt(6)\n\t" \
    "v_mfma_f32_16x16x32_bf16 v[220:223], v[88:91], v[232:235], v[220:223]\n\t" \
    "ds_read_b128 v[232:235], v252 offset:17408\n\t" \
    "s_waitcnt lgkmcnt(6)\n\t" \
    "v_mfma_f32_16x16x32_bf16 v[220:223], v[92:95], v[236:239], v[220:223]\n\t" \
    "v_and_b32 v252, 0x3ff, %[xa]\n\t" \
    "ds_read_b128 v[236:239], v252 offset:47104\n\t" \
    "s_waitcnt lgkmcnt(6)\n\t" \
    "v_mfma_f32_16x16x32_bf16 v[220:223], v[112:115], v[240:243], v[220:223]\n\t" \
    "ds_read_b128 v[240:243], v252 offset:48128\n\t" \
    "v_mov_b64 v[244:245], 0\n\t" \
    "s_waitcnt lgkmcnt(6)\n\t" \
    "v_mfma_f32_16x16x32_bf16 v[220:223], v[116:119], v[212:215], v[220:223]\n\t" \
    "v_mov_b64 v[246:247], 0\n\t" \
    "s_waitcnt lgkmcnt(5)\n\t" \
    "v_mfma_f32_16x16x32_bf16 v[220:223], v[128:131], v[200:203], v[220:223]\n\t" \
    "v_mov_b64 v[248:249], 0\n\t" \
    "s_waitcnt lgkmcnt(4)\n\t" \
    "v_mfma_f32_16x16x32_bf16 v[220:223], v[132:135], v[224:227], v[220:223]\n\t" \
    "v_mov_b64 v[250:251], 0\n\t" \
    "s_waitcnt lgkmcnt(3)\n\t" \
    "v_mfma_f32_16x16x32_bf16 v[220:223], v[152:155], v[228:231], v[220:223]\n\t" \
    "v_add_u32 v252, %[qs], v252\n\t" \
    "v_mov_b64 v[224:225], 0\n\t" \
    "s_waitcnt lgkmcnt(2)\n\t" \
    "v_mfma_f32_16x16x32_bf16 v[220:223], v[156:159], v[232:235], v[220:223]\n\t" \
    "v_mov_b64 v[226:227], 0\n\t" \
    "v_cvt_pk_bf16_f32 v208, v208, v209\n\t" \
    "s_waitcnt lgkmcnt(1)\n\t" \
    "v_mfma_f32_16x16x32_bf16 v[220:223], v[192:195], v[236:239], v[220:223]\n\t" \
    "v_cvt_pk_bf16_f32 v209, v210, v211\n\t" \
    "ds_write_b64 v252, v[208:209] offset:2048\n\t" \
    "s_waitcnt lgkmcnt(1)\n\t" \
    "v_mfma_f32_16x16x32_bf16 v[220:223], v[196:199], v[240:243], v[220:223]\n\t" \
    "ds_read_b128 v[228:231], %[wl] offset:2048\n\t" \
    "ds_read_b128 v[232:235], %[wl] offset:3072\n\t" \
    "ds_read_b128 v[200:203], %[wl] offset:0\n\t" \
    "ds_read_b128 v[204:207], %[wl] offset:1024\n\t" \
    "s_nop 3\n\t" \
    "v_cvt_pk_bf16_f32 v220, v220, v221\n\t" \
    "v_cvt_pk_bf16_f32 v221, v222, v223\n\t" \
    "ds_write_b64 v252, v[220:221] offset:8192\n\t" \
    "s_waitcnt lgkmcnt(0)\n\t"
#define ASM_XG_V \
    "s_waitcnt lgkmcnt(0)\n\t" \
    "ds_read_b128 v[224:227], %[xa] offset:16384\n\t" \
    "ds_read_b128 v[228:231], %[xa] offset:17408\n\t" \
    "ds_read_b128 v[244:247], %[wl] offset:0\n\t" \
    "ds_read_b128 v[248:251], %[wl] offset:1024\n\t" \
    "ds_read_b128 v[232:235], %[xa] offset:0\n\t" \
    "ds_read_b128 v[236:239], %[xa] offset:1024\n\t" \
    "v_xor_b32 v252, 0x800, %[xa]\n\t" \
    "ds_read_b128 v[240:243], v252 offset:0\n\t" \
    "s_waitcnt lgkmcnt(6)\n\t" \
    "v_mfma_f32_16x16x32_bf16 v[220:223], v[8:11], v[224:227], 0\n\t" \
    "s_waitcnt lgkmcnt(5)\n\t" \
    "v_mfma_f32_16x16x32_bf16 v[220:223], v[12:15], v[228:231], v[220:223]\n\t" \
    "v_mfma_f32_16x16x32_bf16 v[216:219], v[0:3], v[224:227], 0\n\t" \
    "v_mfma_f32_16x16x32_bf16 v[216:219], v[4:7], v[228:231], v[216:219]\n\t" \
    "s_waitcnt lgkmcnt(4)\n\t" \
    "v_mfma_f32_16x16x32_bf16 v[212:215], v[244:247], v[224:227], 0\n\t" \
    "ds_read_b128 v[224:227], v252 offset:1024\n\t" \
    "s_waitcnt lgkmcnt(4)\n\t" \
    "v_mfma_f32_16x16x32_bf16 v[212:215], v[248:251], v[228:231], v[212:215]\n\t" \
    "ds_read_b128 v[228:231], v252 offset:16384\n\t" \
    "s_waitcnt lgkmcnt(4)\n\t" \
    "v_mfma_f32_16x16x32_bf16 v[200:203], v[244:247], v[232:235], 0\n\t" \
    "ds_read_b128 v[244:247], %[wl] offset:2048\n\t" \
    "s_waitcnt lgkmcnt(4)\n\t" \
    "v_mfma_f32_16x16x32_bf16 v[200:203], v[248:251], v[236:239], v[200:203]\n\t" \
    "ds_read_b128 v[248:251], %[wl] offset:3072\n\t" \
    "v_mfma_f32_16x16x32_bf16 v[204:207], v[0:3], v[232:235], 0\n\t" \
    "v_mfma_f32_16x16x32_bf16 v[204:207], v[4:7], v[236:239], v[204:207]\n\t" \
    "v_mfma_f32_16x16x32_bf16 v[208:211], v[8:11], v[232:235], 0\n\t" \
    "ds_read_b128 v[232:235], v252 offset:17408\n\t" \
    "v_mfma_f32_16x16x32_bf16 v[208:211], v[12:15], v[236:239], v[208:211]\n\t" \
    "v_xor_b32 v252, 0x1000, %[xa]\n\t" \
    "ds_read_b128 v[236:239], v252 offset:16384\n\t" \
    "s_waitcnt lgkmcnt(6)\n\t" \
    "v_mfma_f32_16x16x32_bf16 v[208:211], v[32:35], v[240:243], v[208:211]\n\t" \
    "s_waitcnt lgkmcnt(5)\n\t" \
    "v_mfma_f32_16x16x32_bf16 v[208:211], v[36:39], v[224:227], v[208:211]\n\t" \
    "v_mfma_f32_16x16x32_bf16 v[204:207], v[24:27], v[240:243], v[204:207]\n\t" \
    "v_mfma_f32_16x16x32_bf16 v[204:207], v[28:31], v[224:227], v[204:207]\n\t" \
    "v_mfma_f32_16x16x32_bf16 v[200:203], v[16:19], v[240:243], v[200:203]\n\t" \
    "ds_read_b128 v[240:243], v252 offset:17408\n\t" \
    "v_mfma_f32_16x16x32_bf16 v[200:203], v[20:23], v[224:227], v[200:203]\n\t" \
    "ds_read_b128 v[224:227], v252 offset:0\n\t" \
    "s_waitcnt lgkmcnt(6)\n\t" \
    "v_mfma_f32_16x16x32_bf16 v[212:215], v[16:19], v[228:231], v[212:215]\n\t" \
    "s_waitcnt lgkmcnt(3)\n\t" \
    "v_mfma_f32_16x16x32_bf16 v[212:215], v[20:23], v[232:235], v[212:215]\n\t" \
    "v_mfma_f32_16x16x32_bf16 v[216:219], v[24:27], v[228:231], v[216:219]\n\t" \
    "v_mfma_f32_16x16x32_bf16 v[216:219], v[28:31], v[232:235], v[216:219]\n\t" \
    "v_mfma_f32_16x16x32_bf16 v[220:223], v[32:35], v[228:231], v[220:223]\n\t" \
    "ds_read_b128 v[228:231], v252 offset:1024\n\t" \
    "v_mfma_f32_16x16x32_bf16 v[220:223], v[36:39], v[232:235], v[220:223]\n\t" \
    "v_xor_b32 v252, 0x1800, %[xa]\n\t" \
    "ds_read_b128 v[232:235], v252 offset:0\n\t" \
    "s_waitcnt lgkmcnt(4)\n\t" \
    "v_mfma_f32_16x16x32_bf16 v[220:223], v[48:51], v[236:239], v[220:223]\n\t" \
    "s_waitcnt lgkmcnt(3)\n\t" \
    "v_mfma_f32_16x16x32_bf16 v[220:223], v[52:55], v[240:243], v[220:223]\n\t" \
    "v_mfma_f32_16x16x32_bf16 v[216:219], v[40:43], v[236:239], v[216:219]\n\t" \
    "v_mfma_f32_16x16x32_bf16 v[216:219], v[44:47], v[240:243], v[216:219]\n\t" \
    "v_mfma_f32_16x16x32_bf16 v[212:215], v[244:247], v[236:239], v[212:215]\n\t" \
    "ds_read_b128 v[236:239], v252 offset:1024\n\t" \
    "v_mfma_f32_16x16x32_bf16 v[212:215], v[248:251], v[240:243], v[212:215]\n\t" \
    "ds_read_b128 v[240:243], v252 offset:16384\n\t" \
    "s_waitcnt lgkmcnt(4)\n\t" \
    "v_mfma_f32_16x16x32_bf16 v[200:203], v[244:247], v[224:227], v[200:203]\n\t" \
    "ds_read_b128 v[244:247], %[wl] offset:8192\n\t" \
    "s_waitcnt lgkmcnt(4)\n\t" \
    "v_mfma_f32_16x16x32_bf16 v[200:203], v[248:251], v[228:231], v[200:203]\n\t" \
    "ds_read_b128 v[248:251], %[wl] offset:9216\n\t" \
    "v_mfma_f32_16x16x32_bf16 v[204:207], v[40:43], v[224:227], v[204:207]\n\t" \
    "v_mfma_f32_16x16x32_bf16 v[204:207], v[44:47], v[228:231], v[204:207]\n\t" \
    "v_mfma_f32_16x16x32_bf16 v[208:211], v[48:51], v[224:227], v[208:211]\n\t" \
    "ds_read_b128 v[224:227], v252 offset:17408\n\t" \
    "v_mfma_f32_16x16x32_bf16 v[208:211], v[52:55], v[228:231], v[208:211]\n\t" \
    "v_and_b32 v252, 0x3ff, %[xa]\n\t" \
    "ds_read_b128 v[228:231], v252 offset:45056\n\t" \
    "s_waitcnt lgkmcnt(6)\n\t" \
    "v_mfma_f32_16x16x32_bf16 v[208:211], v[72:75], v[232:235], v[208:211]\n\t" \
    "s_waitcnt lgkmcnt(5)\n\t" \
    "v_mfma_f32_16x16x32_bf16 v[208:211], v[76:79], v[236:239], v[208:211]\n\t" \
    "v_mfma_f32_16x16x32_bf16 v[204:207], v[64:67], v[232:235], v[204:207]\n\t" \
    "v_mfma_f32_16x16x32_bf16 v[204:207], v[68:71], v[236:239], v[204:207]\n\t" \
    "v_mfma_f32_16x16x32_bf16 v[200:203], v[56:59], v[232:235], v[200:203]\n\t" \
    "ds_read_b128 v[232:235], v252 offset:46080\n\t" \
    "v_mfma_f32_16x16x32_bf16 v[200:203], v[60:63], v[236:239], v[200:203]\n\t" \
    "ds_read_b128 v[236:239], v252 offset:40960\n\t" \
    "s_waitcnt lgkmcnt(6)\n\t" \
    "v_mfma_f32_16x16x32_bf16 v[212:215], v[56:59], v[240:243], v[212:215]\n\t" \
    "s_waitcnt lgkmcnt(3)\n\t" \
    "v_mfma_f32_16x16x32_bf16 v[212:215], v[60:63], v[224:227], v[212:215]\n\t" \
    "v_mfma_f32_16x16x32_bf16 v[216:219], v[64:67], v[240:243], v[216:219]\n\t" \
    "v_mfma_f32_16x16x32_bf16 v[216:219], v[68:71], v[224:227], v[216:219]\n\t" \
    "v_mfma_f32_16x16x32_bf16 v[220:223], v[72:75], v[240:243], v[220:223]\n\t" \
    "ds_read_b128 v[240:243], v252 offset:41984\n\t" \
    "v_mfma_f32_16x16x32_bf16 v[220:223], v[76:79], v[224:227], v[220:223]\n\t" \
    "v_xor_b32 v252, 0x2000, %[xa]\n\t" \
    "ds_read_b128 v[224:227], v252 offset:0\n\t" \
    "s_waitcnt lgkmcnt(4)\n\t" \
    "v_mfma_f32_16x16x32_bf16 v[220:223], v[168:171], v[228:231], v[220:223]\n\t" \
    "s_waitcnt lgkmcnt(3)\n\t" \
    "v_mfma_f32_16x16x32_bf16 v[220:223], v[172:175], v[232:235], v[220:223]\n\t" \
    "v_mfma_f32_16x16x32_bf16 v[216:219], v[160:163], v[228:231], v[216:219]\n\t" \
    "v_mfma_f32_16x16x32_bf16 v[216:219], v[164:167], v[232:235], v[216:219]\n\t" \
    "v_mfma_f32_16x16x32_bf16 v[212:215], v[244:247], v[228:231], v[212:215]\n\t" \
    "ds_read_b128 v[228:231], v252 offset:1024\n\t" \
    "v_mfma_f32_16x16x32_bf16 v[212:215], v[248:251], v[232:235], v[212:215]\n\t" \
    "ds_read_b128 v[232:235], v252 offset:16384\n\t" \
    "s_waitcnt lgkmcnt(4)\n\t" \
    "v_mfma_f32_16x16x32_bf16 v[200:203], v[244:247], v[236:239], v[200:203]\n\t" \
    "ds_read_b128 v[244:247], %[wl] offset:4096\n\t" \
    "s_waitcnt lgkmcnt(4)\n\t" \
    "v_mfma_f32_16x16x32_bf16 v[200:203], v[248:251], v[240:243], v[200:203]\n\t" \
    "ds_read_b128 v[248:251], %[wl] offset:5120\n\t" \
    "v_mfma_f32_16x16x32_bf16 v[204:207], v[160:163], v[236:239], v[204:207]\n\t" \
    "v_mfma_f32_16x16x32_bf16 v[204:207], v[164:167], v[240:243], v[204:207]\n\t" \
    "v_mfma_f32_16x16x32_bf16 v[208:211], v[168:171], v[236:239], v[208:211]\n\t" \
    "ds_read_b128 v[236:239], v252 offset:17408\n\t" \
    "v_mfma_f32_16x16x32_bf16 v[208:211], v[172:175], v[240:243], v[208:211]\n\t" \
    "v_xor_b32 v252, 0x2800, %[xa]\n\t" \
    "ds_read_b128 v[240:243], v252 offset:16384\n\t" \
    "s_waitcnt lgkmcnt(6)\n\t" \
    "v_mfma_f32_16x16x32_bf16 v[208:211], v[88:91], v[224:227], v[208:211]\n\t" \
    "s_waitcnt lgkmcnt(5)\n\t" \
    "v_mfma_f32_16x16x32_bf16 v[208:211], v[92:95], v[228:231], v[208:211]\n\t" \
    "v_mfma_f32_16x16x32_bf16 v[204:207], v[80:83], v[224:227], v[204:207]\n\t" \
    "v_mfma_f32_16x16x32_bf16 v[204:207], v[84:87], v[228:231], v[204:207]\n\t" \
    "s_waitcnt lgkmcnt(3)\n\t" \
    "v_mfma_f32_16x16x32_bf16 v[200:203], v[244:247], v[224:227], v[200:203]\n\t" \
    "ds_read_b128 v[224:227], v252 offset:17408\n\t" \
    "s_waitcnt lgkmcnt(3)\n\t" \
    "v_mfma_f32_16x16x32_bf16 v[200:203], v[248:251], v[228:231], v[200:203]\n\t" \
    "ds_read_b128 v[228:231], v252 offset:0\n\t" \
    "v_mfma_f32_16x16x32_bf16 v[212:215], v[244:247], v[232:235], v[212:215]\n\t" \
    "ds_read_b128 v[244:247], %[wl] offset:6144\n\t" \
    "s_waitcnt lgkmcnt(4)\n\t" \
    "v_mfma_f32_16x16x32_bf16 v[212:215], v[248:251], v[236:239], v[212:215]\n\t" \
    "ds_read_b128 v[248:251], %[wl] offset:7168\n\t" \
    "v_mfma_f32_16x16x32_bf16 v[216:219], v[80:83], v[232:235], v[216:219]\n\t" \
    "v_mfma_f32_16x16x32_bf16 v[216:219], v[84:87], v[236:239], v[216:219]\n\t" \
    "v_mfma_f32_16x16x32_bf16 v[220:223], v[88:91], v[232:235], v[220:223]\n\t" \
    "ds_read_b128 v[232:235], v252 offset:1024\n\t" \
    "v_mfma_f32_16x16x32_bf16 v[220:223], v[92:95], v[236:239], v[220:223]\n\t" \
    "v_xor_b32 v252, 0x3000, %[xa]\n\t" \
    "ds_read_b128 v[236:239], v252 offset:0\n\t" \
    "s_waitcnt lgkmcnt(6)\n\t" \
    "v_mfma_f32_16x16x32_bf16 v[220:223], v[112:115], v[240:243], v[220:223]\n\t" \
    "s_waitcnt lgkmcnt(5)\n\t" \
    "v_mfma_f32_16x16x32_bf16 v[220:223], v[116:119], v[224:227], v[220:223]\n\t" \
    "v_mfma_f32_16x16x32_bf16 v[216:219], v[104:107], v[240:243], v[216:219]\n\t" \
    "v_mfma_f32_16x16x32_bf16 v[216:219], v[108:111], v[224:227], v[216:219]\n\t" \
    "v_mfma_f32_16x16x32_bf16 v[212:215], v[96:99], v[240:243], v[212:215]\n\t" \
    "ds_read_b128 v[240:243], v252 offset:1024\n\t" \
    "v_mfma_f32_16x16x32_bf16 v[212:215], v[100:103], v[224:227], v[212:215]\n\t" \
    "ds_read_b128 v[224:227], v252 offset:16384\n\t" \
    "s_waitcnt lgkmcnt(6)\n\t" \
    "v_mfma_f32_16x16x32_bf16 v[200:203], v[96:99], v[228:231], v[200:203]\n\t" \
    "s_waitcnt lgkmcnt(3)\n\t" \
    "v_mfma_f32_16x16x32_bf16 v[200:203], v[100:103], v[232:235], v[200:203]\n\t" \
    "v_mfma_f32_16x16x32_bf16 v[204:207], v[104:107], v[228:231], v[204:207]\n\t" \
    "v_mfma_f32_16x16x32_bf16 v[204:207], v[108:111], v[232:235], v[204:207]\n\t" \
    "v_mfma_f32_16x16x32_bf16 v[208:211], v[112:115], v[228:231], v[208:211]\n\t" \
    "ds_read_b128 v[228:231], v252 offset:17408\n\t" \
    "v_mfma_f32_16x16x32_bf16 v[208:211], v[116:119], v[232:235], v[208:211]\n\t" \
    "v_xor_b32 v252, 0x3800, %[xa]\n\t" \
    "ds_read_b128 v[232:235], v252 offset:16384\n\t" \
    "s_waitcnt lgkmcnt(4)\n\t" \
    "v_mfma_f32_16x16x32_bf16 v[208:211], v[128:131], v[236:239], v[208:211]\n\t" \
    "s_waitcnt lgkmcnt(3)\n\t" \
    "v_mfma_f32_16x16x32_bf16 v[208:211], v[132:135], v[240:243], v[208:211]\n\t" \
    "v_mfma_f32_16x16x32_bf16 v[204:207], v[120:123], v[236:239], v[204:207]\n\t" \
    "v_mfma_f32_16x16x32_bf16 v[204:207], v[124:127], v[240:243], v[204:207]\n\t" \
    "v_mfma_f32_16x16x32_bf16 v[200:203], v[244:247], v[236:239], v[200:203]\n\t" \
    "ds_read_b128 v[236:239], v252 offset:17408\n\t" \
    "v_mfma_f32_16x16x32_bf16 v[200:203], v[248:251], v[240:243], v[200:203]\n\t" \
    "ds_read_b128 v[240:243], v252 offset:0\n\t" \
    "s_waitcnt lgkmcnt(4)\n\t" \
    "v_mfma_f32_16x16x32_bf16 v[212:215], v[244:247], v[224:227], v[212:215]\n\t" \
    "s_waitcnt lgkmcnt(3)\n\t" \
    "v_mfma_f32_16x16x32_bf16 v[212:215], v[248:251], v[228:231], v[212:215]\n\t" \
    "v_mfma_f32_16x16x32_bf16 v[216:219], v[120:123], v[224:227], v[216:219]\n\t" \
    "v_mfma_f32_16x16x32_bf16 v[216:219], v[124:127], v[228:231], v[216:219]\n\t" \
    "v_mfma_f32_16x16x32_bf16 v[220:223], v[128:131], v[224:227], v[220:223]\n\t" \
    "ds_read_b128 v[224:227], v252 offset:1024\n\t" \
    "v_mfma_f32_16x16x32_bf16 v[220:223], v[132:135], v[228:231], v[220:223]\n\t" \
    "v_and_b32 v252, 0x3ff, %[xa]\n\t" \
    "ds_read_b128 v[228:231], v252 offset:43008\n\t" \
    "s_waitcnt lgkmcnt(4)\n\t" \
    "v_mfma_f32_16x16x32_bf16 v[220:223], v[152:155], v[232:235], v[220:223]\n\t" \
    "s_waitcnt lgkmcnt(3)\n\t" \
    "v_mfma_f32_16x16x32_bf16 v[220:223], v[156:159], v[236:239], v[220:223]\n\t" \
    "v_mfma_f32_16x16x32_bf16 v[216:219], v[144:147], v[232:235], v[216:219]\n\t" \
    "v_mfma_f32_16x16x32_bf16 v[216:219], v[148:151], v[236:239], v[216:219]\n\t" \
    "v_mfma_f32_16x16x32_bf16 v[212:215], v[136:139], v[232:235], v[212:215]\n\t" \
    "ds_read_b128 v[232:235], v252 offset:44032\n\t" \
    "v_mfma_f32_16x16x32_bf16 v[212:215], v[140:143], v[236:239], v[212:215]\n\t" \
    "ds_read_b128 v[236:239], v252 offset:47104\n\t" \
    "s_waitcnt lgkmcnt(4)\n\t" \
    "v_mfma_f32_16x16x32_bf16 v[200:203], v[136:139], v[240:243], v[200:203]\n\t" \
    "s_waitcnt lgkmcnt(3)\n\t" \
    "v_mfma_f32_16x16x32_bf16 v[200:203], v[140:143], v[224:227], v[200:203]\n\t" \
    "v_mfma_f32_16x16x32_bf16 v[204:207], v[144:147], v[240:243], v[204:207]\n\t" \
    "v_mfma_f32_16x16x32_bf16 v[204:207], v[148:151], v[224:227], v[204:207]\n\t" \
    "v_mfma_f32_16x16x32_bf16 v[208:211], v[152:155], v[240:243], v[208:211]\n\t" \
    "ds_read_b128 v[240:243], v252 offset:48128\n\t" \
    "v_mfma_f32_16x16x32_bf16 v[208:211], v[156:159], v[224:227], v[208:211]\n\t" \
    "s_waitcnt lgkmcnt(3)\n\t" \
    "v_mfma_f32_16x16x32_bf16 v[208:211], v[192:195], v[228:231], v[208:211]\n\t" \
    "v_add_u32 v252, %[qs], v252\n\t" \
    "s_waitcnt lgkmcnt(2)\n\t" \
    "v_mfma_f32_16x16x32_bf16 v[208:211], v[196:199], v[232:235], v[208:211]\n\t" \
    "v_mfma_f32_16x16x32_bf16 v[204:207], v[184:187], v[228:231], v[204:207]\n\t" \
    "v_mfma_f32_16x16x32_bf16 v[204:207], v[188:191], v[232:235], v[204:207]\n\t" \
    "v_mfma_f32_16x16x32_bf16 v[200:203], v[176:179], v[228:231], v[200:203]\n\t" \
    "v_mfma_f32_16x16x32_bf16 v[200:203], v[180:183], v[232:235], v[200:203]\n\t" \
    "s_waitcnt lgkmcnt(1)\n\t" \
    "v_mfma_f32_16x16x32_bf16 v[212:215], v[176:179], v[236:239], v[212:215]\n\t" \
    "v_cvt_pk_bf16_f32 v208, v208, v209\n\t" \
    "v_cvt_pk_bf16_f32 v209, v210, v211\n\t" \
    "s_waitcnt lgkmcnt(0)\n\t" \
    "v_mfma_f32_16x16x32_bf16 v[212:215], v[180:183], v[240:243], v[212:215]\n\t" \
    "ds_write_b64 v252, v[208:209] offset:2048\n\t" \
    "v_cvt_pk_bf16_f32 v204, v204, v205\n\t" \
    "v_mfma_f32_16x16x32_bf16 v[216:219], v[184:187], v[236:239], v[216:219]\n\t" \
    "v_cvt_pk_bf16_f32 v205, v206, v207\n\t" \
    "ds_write_b64 v252, v[204:205] offset:1024\n\t" \
    "v_mfma_f32_16x16x32_bf16 v[216:219], v[188:191], v[240:243], v[216:219]\n\t" \
    "v_cvt_pk_bf16_f32 v200, v200, v201\n\t" \
    "v_cvt_pk_bf16_f32 v201, v202, v203\n\t" \
    "v_mfma_f32_16x16x32_bf16 v[220:223], v[192:195], v[236:239], v[220:223]\n\t" \
    "ds_write_b64 v252, v[200:201] offset:0\n\t" \
    "v_mfma_f32_16x16x32_bf16 v[220:223], v[196:199], v[240:243], v[220:223]\n\t" \
    "v_cvt_pk_bf16_f32 v212, v212, v213\n\t" \
    "v_cvt_pk_bf16_f32 v213, v214, v215\n\t" \
    "ds_write_b64 v252, v[212:213] offset:6144\n\t" \
    "v_cvt_pk_bf16_f32 v216, v216, v217\n\t" \
    "v_cvt_pk_bf16_f32 v217, v218, v219\n\t" \
    "ds_write_b64 v252, v[216:217] offset:7168\n\t" \
    "s_nop 1\n\t" \
    "v_cvt_pk_bf16_f32 v220, v220, v221\n\t" \
    "v_cvt_pk_bf16_f32 v221, v222, v223\n\t" \
    "ds_write_b64 v252, v[220:221] offset:8192\n\t" \
    "s_waitcnt lgkmcnt(0)\n\t"
#define ASM_HT_LO \
    "s_branch .Lh0%=\n\t" \
    ".Lhs%=:\n\t" \
    "s_cmp_lg_u32 %[go], 0\n\t" \
    "s_cbranch_scc1 .Lhu%=\n\t" \
    ".Lht%=:\n\t" \
    "v_lshlrev_b32 v204, 3, %[l16]\n\t" \
    "v_and_b32 v204, 0x780, v204\n\t" \
    "buffer_load_dword v204, v204, %[rr], %[fs] offen sc1\n\t" \
    "v_bfe_u32 v205, %[l16], 4, 1\n\t" \
    "v_sub_u32 v205, %[u1], v205\n\t" \
    "v_lshrrev_b32 v205, 1, v205\n\t" \
    "s_waitcnt vmcnt(0)\n\t" \
    "v_or_b32 v204, %[fl], v204\n\t" \
    "v_cmp_gt_u32 vcc, v204, v205\n\t" \
    "s_lshr_b32 %[a], vcc_lo, %[m]\n\t" \
    "s_and_b32 %[a], %[a], 0x5555\n\t" \
    "s_cmp_eq_u32 %[a], 0x5555\n\t" \
    "s_cbranch_scc1 .Lhv%=\n\t" \
    HSLEEP \
    "s_sub_u32 %[n], %[n], 1\n\t" \
    "s_cmp_gt_i32 %[n], 0\n\t" \
    "s_cbranch_scc1 .Lht%=\n\t" \
    ".Lhv%=:\n\t" \
    "s_mov_b32 m0, %[e0]\n\t" \
    "s_nop 0\n\t" \
    "global_load_lds_dwordx4 %[l16], %[c0] offset:0 sc1\n\t" \
    "global_load_lds_dwordx4 %[l16], %[c0] offset:1024 sc1\n\t" \
    "global_load_lds_dwordx4 %[l16], %[c0] offset:2048 sc1\n\t" \
    "global_load_lds_dwordx4 %[l16], %[c0] offset:3072 sc1\n\t" \
    "s_mov_b32 m0, %[e1]\n\t" \
    "s_nop 0\n\t" \
    "global_load_lds_dwordx4 %[l16], %[c1] sc1\n\t" \
    ".Lhu%=:\n\t" \
    "s_waitcnt vmcnt(0) lgkmcnt(0)\n\t" \
    "s_barrier \n\t" \
    "ds_read_b128 v[212:215], %[xa] offset:0\n\t" \
    "ds_read_b128 v[216:219], %[xa] offset:1024\n\t" \
    "v_xor_b32 v252, 0x800, %[xa]\n\t" \
    "ds_read_b128 v[220:223], v252 offset:0\n\t" \
    "ds_read_b128 v[224:227], v252 offset:1024\n\t" \
    "ds_read_b128 v[244:247], %[wl] offset:0\n\t" \
    "ds_read_b128 v[248:251], %[wl] offset:1024\n\t" \
    "v_xor_b32 v252, 0x1000, %[xa]\n\t" \
    "ds_read_b128 v[228:231], v252 offset:0\n\t" \
    "ds_read_b128 v[232:235], v252 offset:1024\n\t" \
    "v_xor_b32 v252, 0x1800, %[xa]\n\t" \
    "ds_read_b128 v[236:239], v252 offset:0\n\t" \
    "ds_read_b128 v[240:243], v252 offset:1024\n\t" \
    "s_branch .Lhg%=\n\t" \
    ".Lh0%=:\n\t" \
    "v_mov_b32 v252, %[gw]\n\t" \
    "s_waitcnt vmcnt(0) lgkmcnt(0)\n\t" \
    "s_barrier \n\t" \
    "ds_read_b128 v[200:203], v252\n\t" \
    "ds_read_b128 v[212:215], %[xa] offset:0\n\t" \
    "ds_read_b128 v[216:219], %[xa] offset:1024\n\t" \
    "v_xor_b32 v252, 0x800, %[xa]\n\t" \
    "ds_read_b128 v[220:223], v252 offset:0\n\t" \
    "ds_read_b128 v[224:227], v252 offset:1024\n\t" \
    "ds_read_b128 v[244:247], %[wl] offset:0\n\t" \
    "ds_read_b128 v[248:251], %[wl] offset:1024\n\t" \
    "v_xor_b32 v252, 0x1000, %[xa]\n\t" \
    "ds_read_b128 v[228:231], v252 offset:0\n\t" \
    "ds_read_b128 v[232:235], v252 offset:1024\n\t" \
    "v_xor_b32 v252, 0x1800, %[xa]\n\t" \
    "ds_read_b128 v[236:239], v252 offset:0\n\t" \
    "ds_read_b128 v[240:243], v252 offset:1024\n\t" \
    "s_waitcnt lgkmcnt(10)\n\t" \
    "v_and_b32 v200, v200, v201\n\t" \
    "v_and_b32 v202, v202, v203\n\t" \
    "v_and_b32 v200, v200, v202\n\t" \
    "v_cmp_eq_u32 vcc, 0, v200\n\t" \
    "s_cbranch_vccnz .Lhs%=\n\t" \
    ".Lhg%=:\n\t" \
    "s_mov_b32 %[go], %[nx]\n\t" \
    "s_waitcnt lgkmcnt(9)\n\t" \
    "v_mfma_f32_16x16x32_bf16 v[208:211], v[8:11], v[212:215], 0\n\t" \
    "s_waitcnt lgkmcnt(8)\n\t" \
    "v_mfma_f32_16x16x32_bf16 v[208:211], v[12:15], v[216:219], v[208:211]\n\t" \
    "s_waitcnt lgkmcnt(7)\n\t" \
    "v_mfma_f32_16x16x32_bf16 v[208:211], v[32:35], v[220:223], v[208:211]\n\t" \
    "s_waitcnt lgkmcnt(6)\n\t" \
    "v_mfma_f32_16x16x32_bf16 v[208:211], v[36:39], v[224:227], v[208:211]\n\t" \
    "s_waitcnt lgkmcnt(5)\n\t" \
    "v_mfma_f32_16x16x32_bf16 v[200:203], v[244:247], v[212:215], 0\n\t" \
    "ds_read_b128 v[244:247], %[wl] offset:2048\n\t" \
    "s_waitcnt lgkmcnt(5)\n\t" \
    "v_mfma_f32_16x16x32_bf16 v[200:203], v[248:251], v[216:219], v[200:203]\n\t" \
    "ds_read_b128 v[248:251], %[wl] offset:3072\n\t" \
    "v_mfma_f32_16x16x32_bf16 v[200:203], v[16:19], v[220:223], v[200:203]\n\t" \
    "v_mfma_f32_16x16x32_bf16 v[200:203], v[20:23], v[224:227], v[200:203]\n\t" \
    "v_mfma_f32_16x16x32_bf16 v[204:207], v[0:3], v[212:215], 0\n\t" \
    "v_xor_b32 v252, 0x2000, %[xa]\n\t" \
    "ds_read_b128 v[212:215], v252 offset:0\n\t" \
    "v_mfma_f32_16x16x32_bf16 v[204:207], v[4:7], v[216:219], v[204:207]\n\t" \
    "ds_read_b128 v[216:219], v252 offset:1024\n\t" \
    "v_mfma_f32_16x16x32_bf16 v[204:207], v[24:27], v[220:223], v[204:207]\n\t" \
    "v_xor_b32 v252, 0x2800, %[xa]\n\t" \
    "ds_read_b128 v[220:223], v252 offset:0\n\t" \
    "v_mfma_f32_16x16x32_bf16 v[204:207], v[28:31], v[224:227], v[204:207]\n\t" \
    "ds_read_b128 v[224:227], v252 offset:1024\n\t" \
    "s_cmp_eq_u32 %[go], 0\n\t" \
    "s_cbranch_scc1 .Lhn%=\n\t" \
    "s_mov_b32 m0, %[d0]\n\t" \
    "s_nop 0\n\t" \
    "global_load_lds_dwordx4 %[l16], %[g0] offset:0 sc1\n\t" \
    "global_load_lds_dwordx4 %[l16], %[g0] offset:1024 sc1\n\t" \
    "global_load_lds_dwordx4 %[l16], %[g0] offset:2048 sc1\n\t" \
    "global_load_lds_dwordx4 %[l16], %[g0] offset:3072 sc1\n\t" \
    "s_mov_b32 m0, %[d1]\n\t" \
    "s_nop 0\n\t" \
    "global_load_lds_dwordx4 %[l16], %[g1] sc1\n\t" \
    ".Lhn%=:\n\t" \
    "s_waitcnt lgkmcnt(9)\n\t" \
    "v_mfma_f32_16x16x32_bf16 v[204:207], v[40:43], v[228:231], v[204:207]\n\t" \
    "s_waitcnt lgkmcnt(8)\n\t" \
    "v_mfma_f32_16x16x32_bf16 v[204:207], v[44:47], v[232:235], v[204:207]\n\t" \
    "s_waitcnt lgkmcnt(7)\n\t" \
    "v_mfma_f32_16x16x32_bf16 v[204:207], v[64:67], v[236:239], v[204:207]\n\t" \
    "s_waitcnt lgkmcnt(6)\n\t" \
    "v_mfma_f32_16x16x32_bf16 v[204:207], v[68:71], v[240:243], v[204:207]\n\t" \
    "s_waitcnt lgkmcnt(5)\n\t" \
    "v_mfma_f32_16x16x32_bf16 v[200:203], v[244:247], v[228:231], v[200:203]\n\t" \
    "ds_read_b128 v[244:247], %[wl] offset:4096\n\t" \
    "s_waitcnt lgkmcnt(5)\n\t" \
    "v_mfma_f32_16x16x32_bf16 v[200:203], v[248:251], v[232:235], v[200:203]\n\t" \
    "ds_read_b128 v[248:251], %[wl] offset:5120\n\t" \
    "v_mfma_f32_16x16x32_bf16 v[200:203], v[56:59], v[236:239], v[200:203]\n\t" \
    "v_mfma_f32_16x16x32_bf16 v[200:203], v[60:63], v[240:243], v[200:203]\n\t" \
    "v_mfma_f32_16x16x32_bf16 v[208:211], v[48:51], v[228:231], v[208:211]\n\t" \
    "v_xor_b32 v252, 0x3000, %[xa]\n\t" \
    "ds_read_b128 v[228:231], v252 offset:0\n\t" \
    "v_mfma_f32_16x16x32_bf16 v[208:211], v[52:55], v[232:235], v[208:211]\n\t" \
    "ds_read_b128 v[232:235], v252 offset:1024\n\t" \
    "v_mfma_f32_16x16x32_bf16 v[208:211], v[72:75], v[236:239], v[208:211]\n\t" \
    "v_xor_b32 v252, 0x3800, %[xa]\n\t" \
    "ds_read_b128 v[236:239], v252 offset:0\n\t" \
    "v_mfma_f32_16x16x32_bf16 v[208:211], v[76:79], v[240:243], v[208:211]\n\t" \
    "ds_read_b128 v[240:243], v252 offset:1024\n\t" \
    "s_waitcnt lgkmcnt(9)\n\t" \
    "v_mfma_f32_16x16x32_bf16 v[208:211], v[88:91], v[212:215], v[208:211]\n\t" \
    "s_waitcnt lgkmcnt(8)\n\t" \
    "v_mfma_f32_16x16x32_bf16 v[208:211], v[92:95], v[216:219], v[208:211]\n\t" \
    "s_waitcnt lgkmcnt(7)\n\t" \
    "v_mfma_f32_16x16x32_bf16 v[208:211], v[112:115], v[220:223], v[208:211]\n\t" \
    "s_waitcnt lgkmcnt(6)\n\t" \
    "v_mfma_f32_16x16x32_bf16 v[208:211], v[116:119], v[224:227], v[208:211]\n\t" \
    "s_waitcnt lgkmcnt(5)\n\t" \
    "v_mfma_f32_16x16x32_bf16 v[200:203], v[244:247], v[212:215], v[200:203]\n\t" \
    "ds_read_b128 v[244:247], %[wl] offset:6144\n\t" \
    "s_waitcnt lgkmcnt(5)\n\t" \
    "v_mfma_f32_16x16x32_bf16 v[200:203], v[248:251], v[216:219], v[200:203]\n\t" \
    "ds_read_b128 v[248:251], %[wl] offset:7168\n\t" \
    "v_mfma_f32_16x16x32_bf16 v[200:203], v[96:99], v[220:223], v[200:203]\n\t" \
    "v_mfma_f32_16x16x32_bf16 v[200:203], v[100:103], v[224:227], v[200:203]\n\t" \
    "v_mfma_f32_16x16x32_bf16 v[204:207], v[80:83], v[212:215], v[204:207]\n\t" \
    "v_add_u32 v252, %[xb], %[l16]\n\t" \
    "ds_read_b128 v[212:215], v252 offset:0\n\t" \
    "v_mfma_f32_16x16x32_bf16 v[204:207], v[84:87], v[216:219], v[204:207]\n\t" \
    "ds_read_b128 v[216:219], v252 offset:1024\n\t" \
    "v_mfma_f32_16x16x32_bf16 v[204:207], v[104:107], v[220:223], v[204:207]\n\t" \
    "ds_read_b128 v[220:223], v252 offset:2048\n\t" \
    "v_mfma_f32_16x16x32_bf16 v[204:207], v[108:111], v[224:227], v[204:207]\n\t" \
    "ds_read_b128 v[224:227], v252 offset:3072\n\t" \
    "s_waitcnt lgkmcnt(9)\n\t" \
    "v_mfma_f32_16x16x32_bf16 v[204:207], v[120:123], v[228:231], v[204:207]\n\t" \
    "s_waitcnt lgkmcnt(8)\n\t" \
    "v_mfma_f32_16x16x32_bf16 v[204:207], v[124:127], v[232:235], v[204:207]\n\t" \
    "s_waitcnt lgkmcnt(7)\n\t" \
    "v_mfma_f32_16x16x32_bf16 v[204:207], v[144:147], v[236:239], v[204:207]\n\t" \
    "s_waitcnt lgkmcnt(6)\n\t" \
    "v_mfma_f32_16x16x32_bf16 v[204:207], v[148:151], v[240:243], v[204:207]\n\t" \
    "s_waitcnt lgkmcnt(5)\n\t" \
    "v_mfma_f32_16x16x32_bf16 v[200:203], v[244:247], v[228:231], v[200:203]\n\t" \
    "ds_read_b128 v[244:247], %[wl] offset:8192\n\t" \
    "s_waitcnt lgkmcnt(5)\n\t" \
    "v_mfma_f32_16x16x32_bf16 v[200:203], v[248:251], v[232:235], v[200:203]\n\t" \
    "ds_read_b128 v[248:251], %[wl] offset:9216\n\t" \
    "v_mfma_f32_16x16x32_bf16 v[200:203], v[136:139], v[236:239], v[200:203]\n\t" \
    "v_mfma_f32_16x16x32_bf16 v[200:203], v[140:143], v[240:243], v[200:203]\n\t" \
    "v_mfma_f32_16x16x32_bf16 v[208:211], v[128:131], v[228:231], v[208:211]\n\t" \
    "v_mfma_f32_16x16x32_bf16 v[208:211], v[132:135], v[232:235], v[208:211]\n\t" \
    "v_mfma_f32_16x16x32_bf16 v[208:211], v[152:155], v[236:239], v[208:211]\n\t" \
    "v_mfma_f32_16x16x32_bf16 v[208:211], v[156:159], v[240:243], v[208:211]\n\t" \
    "v_add_u32 v252, %[qd], %[l16]\n\t" \
    "s_waitcnt lgkmcnt(5)\n\t" \
    "v_mfma_f32_16x16x32_bf16 v[208:211], v[168:171], v[212:215], v[208:211]\n\t" \
    "s_waitcnt lgkmcnt(4)\n\t" \
    "v_mfma_f32_16x16x32_bf16 v[208:211], v[172:175], v[216:219], v[208:211]\n\t" \
    "s_waitcnt lgkmcnt(3)\n\t" \
    "v_mfma_f32_16x16x32_bf16 v[208:211], v[192:195], v[220:223], v[208:211]\n\t" \
    "s_waitcnt lgkmcnt(2)\n\t" \
    "v_mfma_f32_16x16x32_bf16 v[208:211], v[196:199], v[224:227], v[208:211]\n\t" \
    "s_waitcnt lgkmcnt(1)\n\t" \
    "v_mfma_f32_16x16x32_bf16 v[200:203], v[244:247], v[212:215], v[200:203]\n\t" \
    "s_waitcnt lgkmcnt(0)\n\t" \
    "v_mfma_f32_16x16x32_bf16 v[200:203], v[248:251], v[216:219], v[200:203]\n\t" \
    "s_nop 1\n\t" \
    "v_cvt_pk_bf16_f32 v208, v208, v209\n\t" \
    "v_mfma_f32_16x16x32_bf16 v[200:203], v[176:179], v[220:223], v[200:203]\n\t" \
    "v_cvt_pk_bf16_f32 v209, v210, v211\n\t" \
    "v_mfma_f32_16x16x32_bf16 v[200:203], v[180:183], v[224:227], v[200:203]\n\t" \
    "ds_write_b64 v252, v[208:209] offset:2048\n\t" \
    "v_mfma_f32_16x16x32_bf16 v[204:207], v[160:163], v[212:215], v[204:207]\n\t" \
    "v_mfma_f32_16x16x32_bf16 v[204:207], v[164:167], v[216:219], v[204:207]\n\t" \
    "s_nop 0\n\t" \
    "v_cvt_pk_bf16_f32 v200, v200, v201\n\t" \
    "v_mfma_f32_16x16x32_bf16 v[204:207], v[184:187], v[220:223], v[204:207]\n\t" \
    "v_cvt_pk_bf16_f32 v201, v202, v203\n\t" \
    "v_mfma_f32_16x16x32_bf16 v[204:207], v[188:191], v[224:227], v[204:207]\n\t" \
    "ds_write_b64 v252, v[200:201] offset:0\n\t" \
    "s_nop 6\n\t" \
    "v_cvt_pk_bf16_f32 v204, v204, v205\n\t" \
    "v_cvt_pk_bf16_f32 v205, v206, v207\n\t" \
    "ds_write_b64 v252, v[204:205] offset:1024\n\t" \
    "s_mov_b32 vcc_lo, -1\n\t" \
    "s_cmp_lg_u32 %[fl], 0\n\t" \
    "s_cbranch_scc1 .Lhx%=\n\t" \
    "v_lshlrev_b32 v212, 3, %[l16]\n\t" \
    "v_and_b32 v212, 0x780, v212\n\t" \
    "buffer_load_dword v212, v212, %[rr], %[fs] offen sc1\n\t" \
    "v_bfe_u32 v213, %[l16], 4, 1\n\t" \
    "v_sub_u32 v213, %[u2], v213\n\t" \
    "v_lshrrev_b32 v213, 1, v213\n\t" \
    "s_waitcnt vmcnt(0)\n\t" \
    "v_or_b32 v212, %[fl], v212\n\t" \
    "v_cmp_gt_u32 vcc, v212, v213\n\t" \
    ".Lhx%=:\n\t" \
    "s_lshr_b32 %[a], vcc_lo, %[m]\n\t" \
    "s_and_b32 %[a], %[a], 0x5555\n\t" \
    "s_cmp_eq_u32 %[a], %[k2]\n\t" \
    "s_cselect_b32 %[nx], 1, 0\n\t" \
    "s_cmp_lg_u32 %[go], 0\n\t" \
    "s_cbranch_scc1 .Lhw%=\n\t" \
    "s_lshl_b32 %[a], vcc_lo, %[m]\n\t" \
    "s_lshr_b32 %[a], %[a], 1\n\t" \
    "s_and_b32 %[a], %[a], 0x5555\n\t" \
    "s_cmp_eq_u32 %[a], %[k1]\n\t" \
    "s_cbranch_scc0 .Lhw%=\n\t" \
    "s_mov_b32 m0, %[d0]\n\t" \
    "s_nop 0\n\t" \
    "global_load_lds_dwordx4 %[l16], %[g0] offset:0 sc1\n\t" \
    "global_load_lds_dwordx4 %[l16], %[g0] offset:1024 sc1\n\t" \
    "global_load_lds_dwordx4 %[l16], %[g0] offset:2048 sc1\n\t" \
    "global_load_lds_dwordx4 %[l16], %[g0] offset:3072 sc1\n\t" \
    "s_mov_b32 m0, %[d1]\n\t" \
    "s_nop 0\n\t" \
    "global_load_lds_dwordx4 %[l16], %[g1] sc1\n\t" \
    "s_mov_b32 %[go], 1\n\t" \
    ".Lhw%=:\n\t" \
    "v_mov_b32 v212, %[gn]\n\t" \
    "v_mov_b32 v213, %[go]\n\t" \
    "ds_write_b32 v212, v213\n\t"
#define ASM_HT_HI \
    "s_branch .Lh0%=\n\t" \
    ".Lhs%=:\n\t" \
    "s_cmp_eq_u32 %[pb], 0\n\t" \
    "s_cbranch_scc1 .Lht%=\n\t" \
    "v_lshrrev_b32 v204, 2, %[l16]\n\t" \
    "v_mov_b32 v205, %[tv]\n\t" \
    "s_mov_b32 exec_hi, 0\n\t" \
    "s_waitcnt vmcnt(0)\n\t" \
    "buffer_store_dword v205, v204, %[rr], %[fp] offen sc1\n\t" \
    "s_mov_b32 exec_hi, -1\n\t" \
    "s_mov_b32 %[pb], 0\n\t" \
    ".Lht%=:\n\t" \
    "s_cmp_lg_u32 %[end], 0\n\t" \
    "s_cbranch_scc1 .Lhe%=\n\t" \
    "s_barrier \n\t" \
    "s_branch .Lhg%=\n\t" \
    ".Lh0%=:\n\t" \
    "v_mov_b32 v252, %[gw]\n\t" \
    "v_add_u32 v204, %[ro], %[l16]\n\t" \
    "ds_read_b128 v[200:203], v252\n\t" \
    "ds_read_b128 v[212:215], v204 offset:0\n\t" \
    "ds_read_b128 v[216:219], v204 offset:1024\n\t" \
    "ds_read_b128 v[220:223], v204 offset:2048\n\t" \
    "s_waitcnt lgkmcnt(3)\n\t" \
    "v_and_b32 v200, v200, v201\n\t" \
    "v_and_b32 v202, v202, v203\n\t" \
    "v_and_b32 v200, v200, v202\n\t" \
    "v_cmp_eq_u32 vcc, 0, v200\n\t" \
    "s_waitcnt lgkmcnt(2)\n\t" \
    "buffer_store_dwordx4 v[212:215], %[l16], %[rp], %[s0] offen sc1\n\t" \
    "s_waitcnt lgkmcnt(1)\n\t" \
    "buffer_store_dwordx4 v[216:219], %[l16], %[rp], %[s1] offen sc1\n\t" \
    "s_waitcnt lgkmcnt(0)\n\t" \
    "buffer_store_dwordx4 v[220:223], %[l16], %[rp], %[s2] offen sc1\n\t" \
    "s_cbranch_vccnz .Lhs%=\n\t" \
    ".Lhg%=:\n\t" \
    "s_nop 0\n\t" \
    "ds_read_b128 v[212:215], %[xa] offset:0\n\t" \
    "ds_read_b128 v[216:219], %[xa] offset:1024\n\t" \
    "v_xor_b32 v252, 0x800, %[xa]\n\t" \
    "ds_read_b128 v[220:223], v252 offset:0\n\t" \
    "ds_read_b128 v[224:227], v252 offset:1024\n\t" \
    "ds_read_b128 v[244:247], %[wl] offset:0\n\t" \
    "ds_read_b128 v[248:251], %[wl] offset:1024\n\t" \
    "v_xor_b32 v252, 0x1000, %[xa]\n\t" \
    "ds_read_b128 v[228:231], v252 offset:0\n\t" \
    "ds_read_b128 v[232:235], v252 offset:1024\n\t" \
    "v_xor_b32 v252, 0x1800, %[xa]\n\t" \
    "ds_read_b128 v[236:239], v252 offset:0\n\t" \
    "ds_read_b128 v[240:243], v252 offset:1024\n\t" \
    "s_setprio 1\n\t" \
    "s_cmp_eq_u32 %[pb], 0\n\t" \
    "s_cbranch_scc1 .Lhn%=\n\t" \
    "v_mov_b32 v252, %[tv]\n\t" \
    "s_mov_b64 exec, 0xff\n\t" \
    "s_waitcnt vmcnt(0)\n\t" \
    "buffer_store_dword v252, %[l16], %[rr], %[fp] offen sc1\n\t" \
    "s_mov_b64 exec, -1\n\t" \
    ".Lhn%=:\n\t" \
    "s_waitcnt lgkmcnt(9)\n\t" \
    "v_mfma_f32_16x16x32_bf16 v[208:211], v[8:11], v[212:215], 0\n\t" \
    "s_waitcnt lgkmcnt(8)\n\t" \
    "v_mfma_f32_16x16x32_bf16 v[208:211], v[12:15], v[216:219], v[208:211]\n\t" \
    "s_waitcnt lgkmcnt(7)\n\t" \
    "v_mfma_f32_16x16x32_bf16 v[208:211], v[32:35], v[220:223], v[208:211]\n\t" \
    "s_waitcnt lgkmcnt(6)\n\t" \
    "v_mfma_f32_16x16x32_bf16 v[208:211], v[36:39], v[224:227], v[208:211]\n\t" \
    "s_waitcnt lgkmcnt(5)\n\t" \
    "v_mfma_f32_16x16x32_bf16 v[200:203], v[244:247], v[212:215], 0\n\t" \
    "ds_read_b128 v[244:247], %[wl] offset:2048\n\t" \
    "s_waitcnt lgkmcnt(5)\n\t" \
    "v_mfma_f32_16x16x32_bf16 v[200:203], v[248:251], v[216:219], v[200:203]\n\t" \
    "ds_read_b128 v[248:251], %[wl] offset:3072\n\t" \
    "v_mfma_f32_16x16x32_bf16 v[200:203], v[16:19], v[220:223], v[200:203]\n\t" \
    "v_mfma_f32_16x16x32_bf16 v[200:203], v[20:23], v[224:227], v[200:203]\n\t" \
    "v_mfma_f32_16x16x32_bf16 v[204:207], v[0:3], v[212:215], 0\n\t" \
    "v_xor_b32 v252, 0x2000, %[xa]\n\t" \
    "ds_read_b128 v[212:215], v252 offset:0\n\t" \
    "v_mfma_f32_16x16x32_bf16 v[204:207], v[4:7], v[216:219], v[204:207]\n\t" \
    "ds_read_b128 v[216:219], v252 offset:1024\n\t" \
    "v_mfma_f32_16x16x32_bf16 v[204:207], v[24:27], v[220:223], v[204:207]\n\t" \
    "v_xor_b32 v252, 0x2800, %[xa]\n\t" \
    "ds_read_b128 v[220:223], v252 offset:0\n\t" \
    "v_mfma_f32_16x16x32_bf16 v[204:207], v[28:31], v[224:227], v[204:207]\n\t" \
    "ds_read_b128 v[224:227], v252 offset:1024\n\t" \
    "s_waitcnt lgkmcnt(9)\n\t" \
    "v_mfma_f32_16x16x32_bf16 v[204:207], v[40:43], v[228:231], v[204:207]\n\t" \
    "s_waitcnt lgkmcnt(8)\n\t" \
    "v_mfma_f32_16x16x32_bf16 v[204:207], v[44:47], v[232:235], v[204:207]\n\t" \
    "s_waitcnt lgkmcnt(7)\n\t" \
    "v_mfma_f32_16x16x32_bf16 v[204:207], v[64:67], v[236:239], v[204:207]\n\t" \
    "s_waitcnt lgkmcnt(6)\n\t" \
    "v_mfma_f32_16x16x32_bf16 v[204:207], v[68:71], v[240:243], v[204:207]\n\t" \
    "s_waitcnt lgkmcnt(5)\n\t" \
    "v_mfma_f32_16x16x32_bf16 v[200:203], v[244:247], v[228:231], v[200:203]\n\t" \
    "ds_read_b128 v[244:247], %[wl] offset:4096\n\t" \
    "s_waitcnt lgkmcnt(5)\n\t" \
    "v_mfma_f32_16x16x32_bf16 v[200:203], v[248:251], v[232:235], v[200:203]\n\t" \
    "ds_read_b128 v[248:251], %[wl] offset:5120\n\t" \
    "v_mfma_f32_16x16x32_bf16 v[200:203], v[56:59], v[236:239], v[200:203]\n\t" \
    "v_mfma_f32_16x16x32_bf16 v[200:203], v[60:63], v[240:243], v[200:203]\n\t" \
    "v_mfma_f32_16x16x32_bf16 v[208:211], v[48:51], v[228:231], v[208:211]\n\t" \
    "v_xor_b32 v252, 0x3000, %[xa]\n\t" \
    "ds_read_b128 v[228:231], v252 offset:0\n\t" \
    "v_mfma_f32_16x16x32_bf16 v[208:211], v[52:55], v[232:235], v[208:211]\n\t" \
    "ds_read_b128 v[232:235], v252 offset:1024\n\t" \
    "v_mfma_f32_16x16x32_bf16 v[208:211], v[72:75], v[236:239], v[208:211]\n\t" \
    "v_xor_b32 v252, 0x3800, %[xa]\n\t" \
    "ds_read_b128 v[236:239], v252 offset:0\n\t" \
    "v_mfma_f32_16x16x32_bf16 v[208:211], v[76:79], v[240:243], v[208:211]\n\t" \
    "ds_read_b128 v[240:243], v252 offset:1024\n\t" \
    "s_waitcnt lgkmcnt(9)\n\t" \
    "v_mfma_f32_16x16x32_bf16 v[208:211], v[88:91], v[212:215], v[208:211]\n\t" \
    "s_waitcnt lgkmcnt(8)\n\t" \
    "v_mfma_f32_16x16x32_bf16 v[208:211], v[92:95], v[216:219], v[208:211]\n\t" \
    "s_waitcnt lgkmcnt(7)\n\t" \
    "v_mfma_f32_16x16x32_bf16 v[208:211], v[112:115], v[220:223], v[208:211]\n\t" \
    "s_waitcnt lgkmcnt(6)\n\t" \
    "v_mfma_f32_16x16x32_bf16 v[208:211], v[116:119], v[224:227], v[208:211]\n\t" \
    "s_waitcnt lgkmcnt(5)\n\t" \
    "v_mfma_f32_16x16x32_bf16 v[200:203], v[244:247], v[212:215], v[200:203]\n\t" \
    "ds_read_b128 v[244:247], %[wl] offset:6144\n\t" \
    "s_waitcnt lgkmcnt(5)\n\t" \
    "v_mfma_f32_16x16x32_bf16 v[200:203], v[248:251], v[216:219], v[200:203]\n\t" \
    "ds_read_b128 v[248:251], %[wl] offset:7168\n\t" \
    "v_mfma_f32_16x16x32_bf16 v[200:203], v[96:99], v[220:223], v[200:203]\n\t" \
    "v_mfma_f32_16x16x32_bf16 v[200:203], v[100:103], v[224:227], v[200:203]\n\t" \
    "v_mfma_f32_16x16x32_bf16 v[204:207], v[80:83], v[212:215], v[204:207]\n\t" \
    "v_add_u32 v252, %[xb], %[l16]\n\t" \
    "ds_read_b128 v[212:215], v252 offset:0\n\t" \
    "v_mfma_f32_16x16x32_bf16 v[204:207], v[84:87], v[216:219], v[204:207]\n\t" \
    "ds_read_b128 v[216:219], v252 offset:1024\n\t" \
    "v_mfma_f32_16x16x32_bf16 v[204:207], v[104:107], v[220:223], v[204:207]\n\t" \
    "ds_read_b128 v[220:223], v252 offset:2048\n\t" \
    "v_mfma_f32_16x16x32_bf16 v[204:207], v[108:111], v[224:227], v[204:207]\n\t" \
    "ds_read_b128 v[224:227], v252 offset:3072\n\t" \
    "s_waitcnt lgkmcnt(9)\n\t" \
    "v_mfma_f32_16x16x32_bf16 v[204:207], v[120:123], v[228:231], v[204:207]\n\t" \
    "s_waitcnt lgkmcnt(8)\n\t" \
    "v_mfma_f32_16x16x32_bf16 v[204:207], v[124:127], v[232:235], v[204:207]\n\t" \
    "s_waitcnt lgkmcnt(7)\n\t" \
    "v_mfma_f32_16x16x32_bf16 v[204:207], v[144:147], v[236:239], v[204:207]\n\t" \
    "s_waitcnt lgkmcnt(6)\n\t" \
    "v_mfma_f32_16x16x32_bf16 v[204:207], v[148:151], v[240:243], v[204:207]\n\t" \
    "s_waitcnt lgkmcnt(5)\n\t" \
    "v_mfma_f32_16x16x32_bf16 v[200:203], v[244:247], v[228:231], v[200:203]\n\t" \
    "ds_read_b128 v[244:247], %[wl] offset:8192\n\t" \
    "s_waitcnt lgkmcnt(5)\n\t" \
    "v_mfma_f32_16x16x32_bf16 v[200:203], v[248:251], v[232:235], v[200:203]\n\t" \
    "ds_read_b128 v[248:251], %[wl] offset:9216\n\t" \
    "v_mfma_f32_16x16x32_bf16 v[200:203], v[136:139], v[236:239], v[200:203]\n\t" \
    "v_mfma_f32_16x16x32_bf16 v[200:203], v[140:143], v[240:243], v[200:203]\n\t" \
    "v_mfma_f32_16x16x32_bf16 v[208:211], v[128:131], v[228:231], v[208:211]\n\t" \
    "v_mfma_f32_16x16x32_bf16 v[208:211], v[132:135], v[232:235], v[208:211]\n\t" \
    "v_mfma_f32_16x16x32_bf16 v[208:211], v[152:155], v[236:239], v[208:211]\n\t" \
    "v_mfma_f32_16x16x32_bf16 v[208:211], v[156:159], v[240:243], v[208:211]\n\t" \
    "v_add_u32 v252, %[qd], %[l16]\n\t" \
    "s_waitcnt lgkmcnt(5)\n\t" \
    "v_mfma_f32_16x16x32_bf16 v[208:211], v[168:171], v[212:215], v[208:211]\n\t" \
    "s_waitcnt lgkmcnt(4)\n\t" \
    "v_mfma_f32_16x16x32_bf16 v[208:211], v[172:175], v[216:219], v[208:211]\n\t" \
    "s_waitcnt lgkmcnt(3)\n\t" \
    "v_mfma_f32_16x16x32_bf16 v[208:211], v[192:195], v[220:223], v[208:211]\n\t" \
    "s_waitcnt lgkmcnt(2)\n\t" \
    "v_mfma_f32_16x16x32_bf16 v[208:211], v[196:199], v[224:227], v[208:211]\n\t" \
    "s_waitcnt lgkmcnt(1)\n\t" \
    "v_mfma_f32_16x16x32_bf16 v[200:203], v[244:247], v[212:215], v[200:203]\n\t" \
    "s_waitcnt lgkmcnt(0)\n\t" \
    "v_mfma_f32_16x16x32_bf16 v[200:203], v[248:251], v[216:219], v[200:203]\n\t" \
    "s_nop 1\n\t" \
    "v_cvt_pk_bf16_f32 v208, v208, v209\n\t" \
    "v_mfma_f32_16x16x32_bf16 v[200:203], v[176:179], v[220:223], v[200:203]\n\t" \
    "v_cvt_pk_bf16_f32 v209, v210, v211\n\t" \
    "v_mfma_f32_16x16x32_bf16 v[200:203], v[180:183], v[224:227], v[200:203]\n\t" \
    "ds_write_b64 v252, v[208:209] offset:2048\n\t" \
    "v_mfma_f32_16x16x32_bf16 v[204:207], v[160:163], v[212:215], v[204:207]\n\t" \
    "v_mfma_f32_16x16x32_bf16 v[204:207], v[164:167], v[216:219], v[204:207]\n\t" \
    "s_nop 0\n\t" \
    "v_cvt_pk_bf16_f32 v200, v200, v201\n\t" \
    "v_mfma_f32_16x16x32_bf16 v[204:207], v[184:187], v[220:223], v[204:207]\n\t" \
    "v_cvt_pk_bf16_f32 v201, v202, v203\n\t" \
    "v_mfma_f32_16x16x32_bf16 v[204:207], v[188:191], v[224:227], v[204:207]\n\t" \
    "ds_write_b64 v252, v[200:201] offset:0\n\t" \
    "s_nop 6\n\t" \
    "v_cvt_pk_bf16_f32 v204, v204, v205\n\t" \
    "v_cvt_pk_bf16_f32 v205, v206, v207\n\t" \
    "ds_write_b64 v252, v[204:205] offset:1024\n\t" \
    "s_setprio 3\n\t" \
    "s_waitcnt lgkmcnt(0)\n\t" \
    "s_barrier \n\t" \
    ".Lhe%=:\n\t"
#define ASM_CRIT1_3 \
    "s_branch .Lc0%=\n\t" \
    ".Lcr%=:\n\t" \
    "s_cmp_eq_u32 %[n], 0\n\t" \
    "s_cbranch_scc1 .Lcb%=\n\t" \
    "s_sub_u32 %[n], %[n], 1\n\t" \
    "v_max_u32 v243, v208, v209\n\t" \
    "v_max3_u32 v243, v210, v211, v243\n\t" \
    "v_cmp_eq_u32 vcc, -1, v243\n\t" \
    "s_cbranch_vccz .Lcs0_%=\n\t" \
    "buffer_load_dwordx4 v[208:211], %[ab], %[rs], %[s0] offen sc1\n\t" \
    ".Lcs0_%=:\n\t" \
    "v_max_u32 v243, v236, v237\n\t" \
    "v_max3_u32 v243, v238, v239, v243\n\t" \
    "v_cmp_eq_u32 vcc, -1, v243\n\t" \
    "s_cbranch_vccz .Lcs1_%=\n\t" \
    "buffer_load_dwordx4 v[236:239], %[ab], %[rs], %[s1] offen sc1\n\t" \
    ".Lcs1_%=:\n\t" \
    "v_max_u32 v243, v220, v221\n\t" \
    "v_max3_u32 v243, v222, v223, v243\n\t" \
    "v_cmp_eq_u32 vcc, -1, v243\n\t" \
    "s_cbranch_vccz .Lca%=\n\t" \
    "buffer_load_dwordx4 v[220:223], %[ab], %[rs], %[s2] offen sc1\n\t" \
    "s_branch .Lca%=\n\t" \
    ".Lc0%=:\n\t" \
    "buffer_load_dwordx4 v[208:211], %[ab], %[rs], %[s0] offen sc1\n\t" \
    "buffer_load_dwordx4 v[236:239], %[ab], %[rs], %[s1] offen sc1\n\t" \
    "buffer_load_dwordx4 v[220:223], %[ab], %[rs], %[s2] offen sc1\n\t" \
    "v_bfe_i32 v241, %[ab], 7, 1\n\t" \
    "v_and_b32 v241, 0xaaaa, v241\n\t" \
    "v_or_b32 v241, 0x4444, v241\n\t" \
    "v_xor_b32 v212, 0x400, %[ab]\n\t" \
    "v_xor_b32 v216, 0x800, %[ab]\n\t" \
    "v_xor_b32 v242, 0xc00, %[ab]\n\t" \
    "v_xor_b32 v240, 0x1400, %[ab]\n\t" \
    ".Lca%=:\n\t" \
    "s_waitcnt vmcnt(0)\n\t" \
    "ds_write_b128 %[ab], v[208:211] offset:0\n\t" \
    "ds_write_b128 %[ab], v[236:239] offset:4096\n\t" \
    "ds_write_b128 %[ab], v[220:223] offset:8192\n\t" \
    "v_max_u32 v243, v208, v209\n\t" \
    "v_max3_u32 v243, v210, v211, v243\n\t" \
    "v_max3_u32 v243, v236, v237, v243\n\t" \
    "v_max3_u32 v243, v238, v239, v243\n\t" \
    "v_max3_u32 v243, v220, v221, v243\n\t" \
    "v_max3_u32 v243, v222, v223, v243\n\t" \
    "v_cmp_eq_u32 vcc, -1, v243\n\t" \
    "s_cbranch_vccnz .Lcr%=\n\t" \
    ".Lcb%=:\n\t" \
    PRIUP \
    ASM_ST2 \
    "v_smfmac_f32_16x16x64_bf16 v[224:227], v[208:211], v[200:207], v241\n\t" \
    "ds_read_b128 v[200:203], %[wl] offset:4096\n\t" \
    "ds_read_b128 v[204:207], %[wl] offset:5120\n\t" \
    "v_smfmac_f32_16x16x64_bf16 v[244:247], v[208:211], v[0:7], v241\n\t" \
    "v_smfmac_f32_16x16x64_bf16 v[248:251], v[208:211], v[8:15], v241\n\t" \
    "s_waitcnt lgkmcnt(2)\n\t" \
    "s_barrier\n\t" ASM_ST \
    "ds_read_b128 v[212:215], v212 offset:0\n\t" \
    "ds_read_b128 v[216:219], v216 offset:0\n\t" \
    "ds_read_b128 v[220:223], v242 offset:0\n\t" \
    "ds_read_b128 v[208:211], v240 offset:0\n\t" \
    "v_smfmac_f32_16x16x64_bf16 v[244:247], v[236:239], v[80:87], v241\n\t" \
    "v_xor_b32 v240, 0x1800, %[ab]\n\t" \
    "v_smfmac_f32_16x16x64_bf16 v[248:251], v[236:239], v[88:95], v241\n\t" \
    "s_waitcnt lgkmcnt(4)\n\t" \
    "v_smfmac_f32_16x16x64_bf16 v[224:227], v[236:239], v[200:207], v241\n\t" \
    "ds_read_b128 v[200:203], %[wl] offset:6144\n\t" \
    "ds_read_b128 v[204:207], %[wl] offset:7168\n\t" \
    "ds_read_b128 v[236:239], v240 offset:0\n\t" \
    "s_waitcnt lgkmcnt(6)\n\t" \
    "v_smfmac_f32_16x16x64_bf16 v[224:227], v[212:215], v[16:23], v241\n\t" \
    "v_smfmac_f32_16x16x64_bf16 v[244:247], v[212:215], v[24:31], v241\n\t" \
    "v_xor_b32 v240, 0x1c00, %[ab]\n\t" \
    "v_smfmac_f32_16x16x64_bf16 v[248:251], v[212:215], v[32:39], v241\n\t" \
    "ds_read_b128 v[212:215], v240 offset:0\n\t" \
    "s_waitcnt lgkmcnt(6)\n\t" \
    "v_smfmac_f32_16x16x64_bf16 v[224:227], v[216:219], v[228:235], v241\n\t" \
    "ds_read_b128 v[228:231], %[wl] offset:8192\n\t" \
    "ds_read_b128 v[232:235], %[wl] offset:9216\n\t" \
    "v_smfmac_f32_16x16x64_bf16 v[244:247], v[216:219], v[40:47], v241\n\t" \
    "v_and_b32 v240, 0x3ff, %[ab]\n\t" \
    "v_smfmac_f32_16x16x64_bf16 v[248:251], v[216:219], v[48:55], v241\n\t" \
    "ds_read_b128 v[216:219], v240 offset:8192\n\t" \
    "s_waitcnt lgkmcnt(8)\n\t" \
    "v_smfmac_f32_16x16x64_bf16 v[224:227], v[220:223], v[56:63], v241\n\t" \
    "v_smfmac_f32_16x16x64_bf16 v[244:247], v[220:223], v[64:71], v241\n\t" \
    "v_smfmac_f32_16x16x64_bf16 v[248:251], v[220:223], v[72:79], v241\n\t" \
    "ds_read_b128 v[220:223], v240 offset:9216\n\t" \
    "s_waitcnt lgkmcnt(8)\n\t" \
    "v_smfmac_f32_16x16x64_bf16 v[244:247], v[208:211], v[104:111], v241\n\t" \
    "s_waitcnt lgkmcnt(5)\n\t" \
    "v_smfmac_f32_16x16x64_bf16 v[244:247], v[236:239], v[120:127], v241\n\t" \
    "s_waitcnt lgkmcnt(4)\n\t" \
    "v_smfmac_f32_16x16x64_bf16 v[244:247], v[212:215], v[144:151], v241\n\t" \
    "s_waitcnt lgkmcnt(1)\n\t" \
    "v_smfmac_f32_16x16x64_bf16 v[244:247], v[216:219], v[160:167], v241\n\t" \
    "s_waitcnt lgkmcnt(0)\n\t" \
    "v_smfmac_f32_16x16x64_bf16 v[244:247], v[220:223], v[184:191], v241\n\t" \
    "v_smfmac_f32_16x16x64_bf16 v[248:251], v[208:211], v[112:119], v241\n\t" \
    "v_smfmac_f32_16x16x64_bf16 v[248:251], v[236:239], v[128:135], v241\n\t" \
    "v_smfmac_f32_16x16x64_bf16 v[248:251], v[212:215], v[152:159], v241\n\t" \
    "v_permlane32_swap_b32 v244, v246\n\t" \
    "v_permlane32_swap_b32 v245, v247\n\t" \
    "v_smfmac_f32_16x16x64_bf16 v[248:251], v[216:219], v[168:175], v241\n\t" \
    "v_add_f32 v244, v244, v246\n\t" \
    "v_add_f32 v245, v245, v247\n\t" \
    "v_smfmac_f32_16x16x64_bf16 v[248:251], v[220:223], v[192:199], v241\n\t" \
    "v_cvt_pk_bf16_f32 v244, v244, v245\n\t" \
    "ds_write_b16 %[dst], v244 offset:512\n\t" \
    "ds_write_b16_d16_hi %[dst], v244 offset:528\n\t" \
    "v_smfmac_f32_16x16x64_bf16 v[224:227], v[208:211], v[96:103], v241\n\t" \
    "v_smfmac_f32_16x16x64_bf16 v[224:227], v[236:239], v[200:207], v241\n\t" \
    "v_smfmac_f32_16x16x64_bf16 v[224:227], v[212:215], v[136:143], v241\n\t" \
    "v_permlane32_swap_b32 v248, v250\n\t" \
    "v_permlane32_swap_b32 v249, v251\n\t" \
    "v_smfmac_f32_16x16x64_bf16 v[224:227], v[216:219], v[228:235], v241\n\t" \
    "v_add_f32 v248, v248, v250\n\t" \
    "v_add_f32 v249, v249, v251\n\t" \
    "v_smfmac_f32_16x16x64_bf16 v[224:227], v[220:223], v[176:183], v241\n\t" \
    "v_cvt_pk_bf16_f32 v248, v248, v249\n\t" \
    "ds_write_b16 %[dst], v248 offset:1024\n\t" \
    "ds_write_b16_d16_hi %[dst], v248 offset:1040\n\t" \
    "s_nop 4\n\t" \
    "v_permlane32_swap_b32 v224, v226\n\t" \
    "v_permlane32_swap_b32 v225, v227\n\t" \
    "v_add_f32 v224, v224, v226\n\t" \
    "v_add_f32 v225, v225, v227\n\t" \
    "v_cvt_pk_bf16_f32 v224, v224, v225\n\t" \
    "ds_write_b16 %[dst], v224 offset:0\n\t" \
    "ds_write_b16_d16_hi %[dst], v224 offset:16\n\t"
#define ASM_CRIT1_2 \
    "s_branch .Lc0%=\n\t" \
    ".Lcr%=:\n\t" \
    "s_cmp_eq_u32 %[n], 0\n\t" \
    "s_cbranch_scc1 .Lcb%=\n\t" \
    "s_sub_u32 %[n], %[n], 1\n\t" \
    "v_max_u32 v243, v208, v209\n\t" \
    "v_max3_u32 v243, v210, v211, v243\n\t" \
    "v_cmp_eq_u32 vcc, -1, v243\n\t" \
    "s_cbranch_vccz .Lcs0_%=\n\t" \
    "buffer_load_dwordx4 v[208:211], %[ab], %[rs], %[s0] offen sc1\n\t" \
    ".Lcs0_%=:\n\t" \
    "v_max_u32 v243, v236, v237\n\t" \
    "v_max3_u32 v243, v238, v239, v243\n\t" \
    "v_cmp_eq_u32 vcc, -1, v243\n\t" \
    "s_cbranch_vccz .Lca%=\n\t" \
    "buffer_load_dwordx4 v[236:239], %[ab], %[rs], %[s1] offen sc1\n\t" \
    "s_branch .Lca%=\n\t" \
    ".Lc0%=:\n\t" \
    "buffer_load_dwordx4 v[208:211], %[ab], %[rs], %[s0] offen sc1\n\t" \
    "buffer_load_dwordx4 v[236:239], %[ab], %[rs], %[s1] offen sc1\n\t" \
    "v_bfe_i32 v241, %[ab], 7, 1\n\t" \
    "v_and_b32 v241, 0xaaaa, v241\n\t" \
    "v_or_b32 v241, 0x4444, v241\n\t" \
    "v_xor_b32 v212, 0x400, %[ab]\n\t" \
    "v_xor_b32 v216, 0x800, %[ab]\n\t" \
    "v_xor_b32 v242, 0xc00, %[ab]\n\t" \
    "v_xor_b32 v240, 0x1400, %[ab]\n\t" \
    ".Lca%=:\n\t" \
    "s_waitcnt vmcnt(0)\n\t" \
    "ds_write_b128 %[ab], v[208:211] offset:0\n\t" \
    "ds_write_b128 %[ab], v[236:239] offset:4096\n\t" \
    "v_max_u32 v243, v208, v209\n\t" \
    "v_max3_u32 v243, v210, v211, v243\n\t" \
    "v_max3_u32 v243, v236, v237, v243\n\t" \
    "v_max3_u32 v243, v238, v239, v243\n\t" \
    "v_cmp_eq_u32 vcc, -1, v243\n\t" \
    "s_cbranch_vccnz .Lcr%=\n\t" \
    ".Lcb%=:\n\t" \
    PRIUP \
    ASM_ST2 \
    "v_smfmac_f32_16x16x64_bf16 v[224:227], v[208:211], v[200:207], v241\n\t" \
    "ds_read_b128 v[200:203], %[wl] offset:4096\n\t" \
    "ds_read_b128 v[204:207], %[wl] offset:5120\n\t" \
    "v_smfmac_f32_16x16x64_bf16 v[244:247], v[208:211], v[0:7], v241\n\t" \
    "v_smfmac_f32_16x16x64_bf16 v[248:251], v[208:211], v[8:15], v241\n\t" \
    "s_waitcnt lgkmcnt(2)\n\t" \
    "s_barrier\n\t" ASM_ST \
    "ds_read_b128 v[212:215], v212 offset:0\n\t" \
    "ds_read_b128 v[216:219], v216 offset:0\n\t" \
    "ds_read_b128 v[220:223], v242 offset:0\n\t" \
    "ds_read_b128 v[208:211], v240 offset:0\n\t" \
    "v_smfmac_f32_16x16x64_bf16 v[244:247], v[236:239], v[80:87], v241\n\t" \
    "v_xor_b32 v240, 0x1800, %[ab]\n\t" \
    "v_smfmac_f32_16x16x64_bf16 v[248:251], v[236:239], v[88:95], v241\n\t" \
    "s_waitcnt lgkmcnt(4)\n\t" \
    "v_smfmac_f32_16x16x64_bf16 v[224:227], v[236:239], v[200:207], v241\n\t" \
    "ds_read_b128 v[200:203], %[wl] offset:6144\n\t" \
    "ds_read_b128 v[204:207], %[wl] offset:7168\n\t" \
    "ds_read_b128 v[236:239], v240 offset:0\n\t" \
    "s_waitcnt lgkmcnt(6)\n\t" \
    "v_smfmac_f32_16x16x64_bf16 v[224:227], v[212:215], v[16:23], v241\n\t" \
    "v_smfmac_f32_16x16x64_bf16 v[244:247], v[212:215], v[24:31], v241\n\t" \
    "v_xor_b32 v240, 0x1c00, %[ab]\n\t" \
    "v_smfmac_f32_16x16x64_bf16 v[248:251], v[212:215], v[32:39], v241\n\t" \
    "ds_read_b128 v[212:215], v240 offset:0\n\t" \
    "s_waitcnt lgkmcnt(6)\n\t" \
    "v_smfmac_f32_16x16x64_bf16 v[224:227], v[216:219], v[228:235], v241\n\t" \
    "ds_read_b128 v[228:231], %[wl] offset:8192\n\t" \
    "ds_read_b128 v[232:235], %[wl] offset:9216\n\t" \
    "v_smfmac_f32_16x16x64_bf16 v[244:247], v[216:219], v[40:47], v241\n\t" \
    "v_and_b32 v240, 0x3ff, %[ab]\n\t" \
    "v_smfmac_f32_16x16x64_bf16 v[248:251], v[216:219], v[48:55], v241\n\t" \
    "ds_read_b128 v[216:219], v240 offset:8192\n\t" \
    "s_waitcnt lgkmcnt(8)\n\t" \
    "v_smfmac_f32_16x16x64_bf16 v[224:227], v[220:223], v[56:63], v241\n\t" \
    "v_smfmac_f32_16x16x64_bf16 v[244:247], v[220:223], v[64:71], v241\n\t" \
    "v_smfmac_f32_16x16x64_bf16 v[248:251], v[220:223], v[72:79], v241\n\t" \
    "ds_read_b128 v[220:223], v240 offset:9216\n\t" \
    "s_waitcnt lgkmcnt(8)\n\t" \
    "v_smfmac_f32_16x16x64_bf16 v[244:247], v[208:211], v[104:111], v241\n\t" \
    "s_waitcnt lgkmcnt(5)\n\t" \
    "v_smfmac_f32_16x16x64_bf16 v[244:247], v[236:239], v[120:127], v241\n\t" \
    "s_waitcnt lgkmcnt(4)\n\t" \
    "v_smfmac_f32_16x16x64_bf16 v[244:247], v[212:215], v[144:151], v241\n\t" \
    "s_waitcnt lgkmcnt(1)\n\t" \
    "v_smfmac_f32_16x16x64_bf16 v[244:247], v[216:219], v[160:167], v241\n\t" \
    "s_waitcnt lgkmcnt(0)\n\t" \
    "v_smfmac_f32_16x16x64_bf16 v[244:247], v[220:223], v[184:191], v241\n\t" \
    "v_smfmac_f32_16x16x64_bf16 v[248:251], v[208:211], v[112:119], v241\n\t" \
    "v_smfmac_f32_16x16x64_bf16 v[248:251], v[236:239], v[128:135], v241\n\t" \
    "v_smfmac_f32_16x16x64_bf16 v[248:251], v[212:215], v[152:159], v241\n\t" \
    "v_permlane32_swap_b32 v244, v246\n\t" \
    "v_permlane32_swap_b32 v245, v247\n\t" \
    "v_smfmac_f32_16x16x64_bf16 v[248:251], v[216:219], v[168:175], v241\n\t" \
    "v_add_f32 v244, v244, v246\n\t" \
    "v_add_f32 v245, v245, v247\n\t" \
    "v_smfmac_f32_16x16x64_bf16 v[248:251], v[220:223], v[192:199], v241\n\t" \
    "v_cvt_pk_bf16_f32 v244, v244, v245\n\t" \
    "ds_write_b16 %[dst], v244 offset:512\n\t" \
    "ds_write_b16_d16_hi %[dst], v244 offset:528\n\t" \
    "v_smfmac_f32_16x16x64_bf16 v[224:227], v[208:211], v[96:103], v241\n\t" \
    "v_smfmac_f32_16x16x64_bf16 v[224:227], v[236:239], v[200:207], v241\n\t" \
    "v_smfmac_f32_16x16x64_bf16 v[224:227], v[212:215], v[136:143], v241\n\t" \
    "v_permlane32_swap_b32 v248, v250\n\t" \
    "v_permlane32_swap_b32 v249, v251\n\t" \
    "v_smfmac_f32_16x16x64_bf16 v[224:227], v[216:219], v[228:235], v241\n\t" \
    "v_add_f32 v248, v248, v250\n\t" \
    "v_add_f32 v249, v249, v251\n\t" \
    "v_smfmac_f32_16x16x64_bf16 v[224:227], v[220:223], v[176:183], v241\n\t" \
    "v_cvt_pk_bf16_f32 v248, v248, v249\n\t" \
    "ds_write_b16 %[dst], v248 offset:1024\n\t" \
    "ds_write_b16_d16_hi %[dst], v248 offset:1040\n\t" \
    "s_nop 4\n\t" \
    "v_permlane32_swap_b32 v224, v226\n\t" \
    "v_permlane32_swap_b32 v225, v227\n\t" \
    "v_add_f32 v224, v224, v226\n\t" \
    "v_add_f32 v225, v225, v227\n\t" \
    "v_cvt_pk_bf16_f32 v224, v224, v225\n\t" \
    "ds_write_b16 %[dst], v224 offset:0\n\t" \
    "ds_write_b16_d16_hi %[dst], v224 offset:16\n\t"
#define ASM_CRIT1_N \
    "v_bfe_i32 v241, %[ab], 7, 1\n\t" \
    "v_and_b32 v241, 0xaaaa, v241\n\t" \
    "v_or_b32 v241, 0x4444, v241\n\t" \
    "v_xor_b32 v212, 0x400, %[ab]\n\t" \
    "v_xor_b32 v216, 0x800, %[ab]\n\t" \
    "v_xor_b32 v242, 0xc00, %[ab]\n\t" \
    "v_xor_b32 v240, 0x1400, %[ab]\n\t" \
    "v_xor_b32 v236, 0x1000, %[ab]\n\t" \
    "s_waitcnt lgkmcnt(0)\n\t" \
    "s_barrier\n\t" ASM_ST \
    "ds_read_b128 v[208:211], %[ab] offset:0\n\t" \
    "ds_read_b128 v[212:215], v212 offset:0\n\t" \
    "ds_read_b128 v[216:219], v216 offset:0\n\t" \
    "ds_read_b128 v[220:223], v242 offset:0\n\t" \
    "ds_read_b128 v[236:239], v236 offset:0\n\t" \
    "s_waitcnt lgkmcnt(4)\n\t" \
    "v_smfmac_f32_16x16x64_bf16 v[224:227], v[208:211], v[200:207], v241\n\t" \
    "ds_read_b128 v[200:203], %[wl] offset:4096\n\t" \
    "ds_read_b128 v[204:207], %[wl] offset:5120\n\t" \
    "v_smfmac_f32_16x16x64_bf16 v[244:247], v[208:211], v[0:7], v241\n\t" \
    "v_smfmac_f32_16x16x64_bf16 v[248:251], v[208:211], v[8:15], v241\n\t" \
    "ds_read_b128 v[208:211], v240 offset:0\n\t" \
    "s_waitcnt lgkmcnt(6)\n\t" \
    "v_smfmac_f32_16x16x64_bf16 v[224:227], v[212:215], v[16:23], v241\n\t" \
    "v_smfmac_f32_16x16x64_bf16 v[244:247], v[212:215], v[24:31], v241\n\t" \
    "v_xor_b32 v240, 0x1800, %[ab]\n\t" \
    "v_smfmac_f32_16x16x64_bf16 v[248:251], v[212:215], v[32:39], v241\n\t" \
    "ds_read_b128 v[212:215], v240 offset:0\n\t" \
    "s_waitcnt lgkmcnt(6)\n\t" \
    "v_smfmac_f32_16x16x64_bf16 v[224:227], v[216:219], v[228:235], v241\n\t" \
    "ds_read_b128 v[228:231], %[wl] offset:6144\n\t" \
    "ds_read_b128 v[232:235], %[wl] offset:7168\n\t" \
    "v_smfmac_f32_16x16x64_bf16 v[244:247], v[216:219], v[40:47], v241\n\t" \
    "v_xor_b32 v240, 0x1c00, %[ab]\n\t" \
    "v_smfmac_f32_16x16x64_bf16 v[248:251], v[216:219], v[48:55], v241\n\t" \
    "ds_read_b128 v[216:219], v240 offset:0\n\t" \
    "s_waitcnt lgkmcnt(8)\n\t" \
    "v_smfmac_f32_16x16x64_bf16 v[224:227], v[220:223], v[56:63], v241\n\t" \
    "v_smfmac_f32_16x16x64_bf16 v[244:247], v[220:223], v[64:71], v241\n\t" \
    "v_and_b32 v240, 0x3ff, %[ab]\n\t" \
    "v_smfmac_f32_16x16x64_bf16 v[248:251], v[220:223], v[72:79], v241\n\t" \
    "ds_read_b128 v[220:223], v240 offset:8192\n\t" \
    "s_waitcnt lgkmcnt(6)\n\t" \
    "v_smfmac_f32_16x16x64_bf16 v[224:227], v[236:239], v[200:207], v241\n\t" \
    "ds_read_b128 v[200:203], %[wl] offset:8192\n\t" \
    "ds_read_b128 v[204:207], %[wl] offset:9216\n\t" \
    "v_smfmac_f32_16x16x64_bf16 v[244:247], v[236:239], v[80:87], v241\n\t" \
    "v_smfmac_f32_16x16x64_bf16 v[248:251], v[236:239], v[88:95], v241\n\t" \
    "ds_read_b128 v[236:239], v240 offset:9216\n\t" \
    "s_waitcnt lgkmcnt(8)\n\t" \
    "v_smfmac_f32_16x16x64_bf16 v[244:247], v[208:211], v[104:111], v241\n\t" \
    "s_waitcnt lgkmcnt(7)\n\t" \
    "v_smfmac_f32_16x16x64_bf16 v[244:247], v[212:215], v[120:127], v241\n\t" \
    "s_waitcnt lgkmcnt(4)\n\t" \
    "v_smfmac_f32_16x16x64_bf16 v[244:247], v[216:219], v[144:151], v241\n\t" \
    "s_waitcnt lgkmcnt(3)\n\t" \
    "v_smfmac_f32_16x16x64_bf16 v[244:247], v[220:223], v[160:167], v241\n\t" \
    "s_waitcnt lgkmcnt(0)\n\t" \
    "v_smfmac_f32_16x16x64_bf16 v[244:247], v[236:239], v[184:191], v241\n\t" \
    "v_smfmac_f32_16x16x64_bf16 v[248:251], v[208:211], v[112:119], v241\n\t" \
    "v_smfmac_f32_16x16x64_bf16 v[248:251], v[212:215], v[128:135], v241\n\t" \
    "v_smfmac_f32_16x16x64_bf16 v[248:251], v[216:219], v[152:159], v241\n\t" \
    "v_permlane32_swap_b32 v244, v246\n\t" \
    "v_permlane32_swap_b32 v245, v247\n\t" \
    "v_smfmac_f32_16x16x64_bf16 v[248:251], v[220:223], v[168:175], v241\n\t" \
    "v_add_f32 v244, v244, v246\n\t" \
    "v_add_f32 v245, v245, v247\n\t" \
    "v_smfmac_f32_16x16x64_bf16 v[248:251], v[236:239], v[192:199], v241\n\t" \
    "v_cvt_pk_bf16_f32 v244, v244, v245\n\t" \
    "ds_write_b16 %[dst], v244 offset:512\n\t" \
    "ds_write_b16_d16_hi %[dst], v244 offset:528\n\t" \
    "v_smfmac_f32_16x16x64_bf16 v[224:227], v[208:211], v[96:103], v241\n\t" \
    "v_smfmac_f32_16x16x64_bf16 v[224:227], v[212:215], v[228:235], v241\n\t" \
    "v_smfmac_f32_16x16x64_bf16 v[224:227], v[216:219], v[136:143], v241\n\t" \
    "v_permlane32_swap_b32 v248, v250\n\t" \
    "v_permlane32_swap_b32 v249, v251\n\t" \
    "v_smfmac_f32_16x16x64_bf16 v[224:227], v[220:223], v[200:207], v241\n\t" \
    "v_add_f32 v248, v248, v250\n\t" \
    "v_add_f32 v249, v249, v251\n\t" \
    "v_smfmac_f32_16x16x64_bf16 v[224:227], v[236:239], v[176:183], v241\n\t" \
    "v_cvt_pk_bf16_f32 v248, v248, v249\n\t" \
    "ds_write_b16 %[dst], v248 offset:1024\n\t" \
    "ds_write_b16_d16_hi %[dst], v248 offset:1040\n\t" \
    "s_nop 4\n\t" \
    "v_permlane32_swap_b32 v224, v226\n\t" \
    "v_permlane32_swap_b32 v225, v227\n\t" \
    "v_add_f32 v224, v224, v226\n\t" \
    "v_add_f32 v225, v225, v227\n\t" \
    "v_cvt_pk_bf16_f32 v224, v224, v225\n\t" \
    "ds_write_b16 %[dst], v224 offset:0\n\t" \
    "ds_write_b16_d16_hi %[dst], v224 offset:16\n\t"
#define ASM_CRIT1_V3 \
    "s_branch .Lc0%=\n\t" \
    ".Lcr%=:\n\t" \
    "s_cmp_eq_u32 %[n], 0\n\t" \
    "s_cbranch_scc1 .Lcb%=\n\t" \
    "s_sub_u32 %[n], %[n], 1\n\t" \
    "v_max_u32 v243, v208, v209\n\t" \
    "v_max3_u32 v243, v210, v211, v243\n\t" \
    "v_cmp_eq_u32 vcc, -1, v243\n\t" \
    "s_cbranch_vccz .Lcs0_%=\n\t" \
    "buffer_load_dwordx4 v[208:211], %[ab], %[rs], %[s0] offen sc1\n\t" \
    ".Lcs0_%=:\n\t" \
    "v_max_u32 v243, v236, v237\n\t" \
    "v_max3_u32 v243, v238, v239, v243\n\t" \
    "v_cmp_eq_u32 vcc, -1, v243\n\t" \
    "s_cbranch_vccz .Lcs1_%=\n\t" \
    "buffer_load_dwordx4 v[236:239], %[ab], %[rs], %[s1] offen sc1\n\t" \
    ".Lcs1_%=:\n\t" \
    "v_max_u32 v243, v220, v221\n\t" \
    "v_max3_u32 v243, v222, v223, v243\n\t" \
    "v_cmp_eq_u32 vcc, -1, v243\n\t" \
    "s_cbranch_vccz .Lca%=\n\t" \
    "buffer_load_dwordx4 v[220:223], %[ab], %[rs], %[s2] offen sc1\n\t" \
    "s_branch .Lca%=\n\t" \
    ".Lc0%=:\n\t" \
    "buffer_load_dwordx4 v[208:211], %[ab], %[rs], %[s0] offen sc1\n\t" \
    "buffer_load_dwordx4 v[236:239], %[ab], %[rs], %[s1] offen sc1\n\t" \
    "buffer_load_dwordx4 v[220:223], %[ab], %[rs], %[s2] offen sc1\n\t" \
    "v_bfe_i32 v241, %[ab], 7, 1\n\t" \
    "v_and_b32 v241, 0xaaaa, v241\n\t" \
    "v_or_b32 v241, 0x4444, v241\n\t" \
    "v_xor_b32 v212, 0x400, %[ab]\n\t" \
    "v_xor_b32 v216, 0x800, %[ab]\n\t" \
    "v_xor_b32 v242, 0xc00, %[ab]\n\t" \
    "v_xor_b32 v240, 0x1400, %[ab]\n\t" \
    ".Lca%=:\n\t" \
    "s_waitcnt vmcnt(0)\n\t" \
    "ds_write_b128 %[ab], v[208:211] offset:0\n\t" \
    "ds_write_b128 %[ab], v[236:239] offset:4096\n\t" \
    "ds_write_b128 %[ab], v[220:223] offset:8192\n\t" \
    "v_max_u32 v243, v208, v209\n\t" \
    "v_max3_u32 v243, v210, v211, v243\n\t" \
    "v_max3_u32 v243, v236, v237, v243\n\t" \
    "v_max3_u32 v243, v238, v239, v243\n\t" \
    "v_max3_u32 v243, v220, v221, v243\n\t" \
    "v_max3_u32 v243, v222, v223, v243\n\t" \
    "v_cmp_eq_u32 vcc, -1, v243\n\t" \
    "s_cbranch_vccnz .Lcr%=\n\t" \
    ".Lcb%=:\n\t" \
    PRIUP \
    ASM_ST2 \
    "v_smfmac_f32_16x16x64_bf16 v[224:227], v[208:211], v[200:207], v241\n\t" \
    "ds_read_b128 v[200:203], %[wl] offset:4096\n\t" \
    "ds_read_b128 v[204:207], %[wl] offset:5120\n\t" \
    "v_smfmac_f32_16x16x64_bf16 v[244:247], v[208:211], v[0:7], v241\n\t" \
    "v_smfmac_f32_16x16x64_bf16 v[248:251], v[208:211], v[8:15], v241\n\t" \
    "s_waitcnt lgkmcnt(2)\n\t" \
    "buffer_load_dword %[fv], %[fv], %[rs], %[fs] offen sc1\n\t" \
    "s_barrier\n\t" ASM_ST \
    "ds_read_b128 v[212:215], v212 offset:0\n\t" \
    "ds_read_b128 v[216:219], v216 offset:0\n\t" \
    "ds_read_b128 v[220:223], v242 offset:0\n\t" \
    "ds_read_b128 v[208:211], v240 offset:0\n\t" \
    "v_smfmac_f32_16x16x64_bf16 v[244:247], v[236:239], v[80:87], v241\n\t" \
    "v_xor_b32 v240, 0x1800, %[ab]\n\t" \
    "v_smfmac_f32_16x16x64_bf16 v[248:251], v[236:239], v[88:95], v241\n\t" \
    "s_waitcnt lgkmcnt(4)\n\t" \
    "v_smfmac_f32_16x16x64_bf16 v[224:227], v[236:239], v[200:207], v241\n\t" \
    "ds_read_b128 v[200:203], %[wl] offset:6144\n\t" \
    "ds_read_b128 v[204:207], %[wl] offset:7168\n\t" \
    "ds_read_b128 v[236:239], v240 offset:0\n\t" \
    "s_waitcnt lgkmcnt(6)\n\t" \
    "v_smfmac_f32_16x16x64_bf16 v[224:227], v[212:215], v[16:23], v241\n\t" \
    "v_smfmac_f32_16x16x64_bf16 v[244:247], v[212:215], v[24:31], v241\n\t" \
    "v_xor_b32 v240, 0x1c00, %[ab]\n\t" \
    "v_smfmac_f32_16x16x64_bf16 v[248:251], v[212:215], v[32:39], v241\n\t" \
    "ds_read_b128 v[212:215], v240 offset:0\n\t" \
    "s_waitcnt lgkmcnt(6)\n\t" \
    "v_smfmac_f32_16x16x64_bf16 v[224:227], v[216:219], v[228:235], v241\n\t" \
    "ds_read_b128 v[228:231], %[wl] offset:8192\n\t" \
    "ds_read_b128 v[232:235], %[wl] offset:9216\n\t" \
    "v_smfmac_f32_16x16x64_bf16 v[244:247], v[216:219], v[40:47], v241\n\t" \
    "v_and_b32 v240, 0x3ff, %[ab]\n\t" \
    "v_smfmac_f32_16x16x64_bf16 v[248:251], v[216:219], v[48:55], v241\n\t" \
    "ds_read_b128 v[216:219], v240 offset:8192\n\t" \
    "s_waitcnt lgkmcnt(8)\n\t" \
    "v_smfmac_f32_16x16x64_bf16 v[224:227], v[220:223], v[56:63], v241\n\t" \
    "v_smfmac_f32_16x16x64_bf16 v[244:247], v[220:223], v[64:71], v241\n\t" \
    "v_smfmac_f32_16x16x64_bf16 v[248:251], v[220:223], v[72:79], v241\n\t" \
    "ds_read_b128 v[220:223], v240 offset:9216\n\t" \
    "s_waitcnt lgkmcnt(8)\n\t" \
    "v_smfmac_f32_16x16x64_bf16 v[244:247], v[208:211], v[104:111], v241\n\t" \
    "s_waitcnt lgkmcnt(5)\n\t" \
    "v_smfmac_f32_16x16x64_bf16 v[244:247], v[236:239], v[120:127], v241\n\t" \
    "s_waitcnt lgkmcnt(4)\n\t" \
    "v_smfmac_f32_16x16x64_bf16 v[244:247], v[212:215], v[144:151], v241\n\t" \
    "s_waitcnt lgkmcnt(1)\n\t" \
    "v_smfmac_f32_16x16x64_bf16 v[244:247], v[216:219], v[160:167], v241\n\t" \
    "s_waitcnt lgkmcnt(0)\n\t" \
    "v_smfmac_f32_16x16x64_bf16 v[244:247], v[220:223], v[184:191], v241\n\t" \
    "v_smfmac_f32_16x16x64_bf16 v[248:251], v[208:211], v[112:119], v241\n\t" \
    "v_smfmac_f32_16x16x64_bf16 v[248:251], v[236:239], v[128:135], v241\n\t" \
    "v_smfmac_f32_16x16x64_bf16 v[248:251], v[212:215], v[152:159], v241\n\t" \
    "v_permlane32_swap_b32 v244, v246\n\t" \
    "v_permlane32_swap_b32 v245, v247\n\t" \
    "v_smfmac_f32_16x16x64_bf16 v[248:251], v[216:219], v[168:175], v241\n\t" \
    "v_add_f32 v244, v244, v246\n\t" \
    "v_add_f32 v245, v245, v247\n\t" \
    "v_smfmac_f32_16x16x64_bf16 v[248:251], v[220:223], v[192:199], v241\n\t" \
    "v_cvt_pk_bf16_f32 v244, v244, v245\n\t" \
    "ds_write_b16 %[dst], v244 offset:512\n\t" \
    "ds_write_b16_d16_hi %[dst], v244 offset:528\n\t" \
    "v_smfmac_f32_16x16x64_bf16 v[224:227], v[208:211], v[96:103], v241\n\t" \
    "v_smfmac_f32_16x16x64_bf16 v[224:227], v[236:239], v[200:207], v241\n\t" \
    "v_smfmac_f32_16x16x64_bf16 v[224:227], v[212:215], v[136:143], v241\n\t" \
    "v_permlane32_swap_b32 v248, v250\n\t" \
    "v_permlane32_swap_b32 v249, v251\n\t" \
    "v_smfmac_f32_16x16x64_bf16 v[224:227], v[216:219], v[228:235], v241\n\t" \
    "v_add_f32 v248, v248, v250\n\t" \
    "v_add_f32 v249, v249, v251\n\t" \
    "v_smfmac_f32_16x16x64_bf16 v[224:227], v[220:223], v[176:183], v241\n\t" \
    "v_cvt_pk_bf16_f32 v248, v248, v249\n\t" \
    "ds_write_b16 %[dst], v248 offset:1024\n\t" \
    "ds_write_b16_d16_hi %[dst], v248 offset:1040\n\t" \
    "s_nop 4\n\t" \
    "v_permlane32_swap_b32 v224, v226\n\t" \
    "v_permlane32_swap_b32 v225, v227\n\t" \
    "v_add_f32 v224, v224, v226\n\t" \
    "v_add_f32 v225, v225, v227\n\t" \
    "v_cvt_pk_bf16_f32 v224, v224, v225\n\t" \
    "ds_write_b16 %[dst], v224 offset:0\n\t" \
    "ds_write_b16_d16_hi %[dst], v224 offset:16\n\t"
#define ASM_CRIT1_V2 \
    "s_branch .Lc0%=\n\t" \
    ".Lcr%=:\n\t" \
    "s_cmp_eq_u32 %[n], 0\n\t" \
    "s_cbranch_scc1 .Lcb%=\n\t" \
    "s_sub_u32 %[n], %[n], 1\n\t" \
    "v_max_u32 v243, v208, v209\n\t" \
    "v_max3_u32 v243, v210, v211, v243\n\t" \
    "v_cmp_eq_u32 vcc, -1, v243\n\t" \
    "s_cbranch_vccz .Lcs0_%=\n\t" \
    "buffer_load_dwordx4 v[208:211], %[ab], %[rs], %[s0] offen sc1\n\t" \
    ".Lcs0_%=:\n\t" \
    "v_max_u32 v243, v236, v237\n\t" \
    "v_max3_u32 v243, v238, v239, v243\n\t" \
    "v_cmp_eq_u32 vcc, -1, v243\n\t" \
    "s_cbranch_vccz .Lca%=\n\t" \
    "buffer_load_dwordx4 v[236:239], %[ab], %[rs], %[s1] offen sc1\n\t" \
    "s_branch .Lca%=\n\t" \
    ".Lc0%=:\n\t" \
    "buffer_load_dwordx4 v[208:211], %[ab], %[rs], %[s0] offen sc1\n\t" \
    "buffer_load_dwordx4 v[236:239], %[ab], %[rs], %[s1] offen sc1\n\t" \
    "v_bfe_i32 v241, %[ab], 7, 1\n\t" \
    "v_and_b32 v241, 0xaaaa, v241\n\t" \
    "v_or_b32 v241, 0x4444, v241\n\t" \
    "v_xor_b32 v212, 0x400, %[ab]\n\t" \
    "v_xor_b32 v216, 0x800, %[ab]\n\t" \
    "v_xor_b32 v242, 0xc00, %[ab]\n\t" \
    "v_xor_b32 v240, 0x1400, %[ab]\n\t" \
    ".Lca%=:\n\t" \
    "s_waitcnt vmcnt(0)\n\t" \
    "ds_write_b128 %[ab], v[208:211] offset:0\n\t" \
    "ds_write_b128 %[ab], v[236:239] offset:4096\n\t" \
    "v_max_u32 v243, v208, v209\n\t" \
    "v_max3_u32 v243, v210, v211, v243\n\t" \
    "v_max3_u32 v243, v236, v237, v243\n\t" \
    "v_max3_u32 v243, v238, v239, v243\n\t" \
    "v_cmp_eq_u32 vcc, -1, v243\n\t" \
    "s_cbranch_vccnz .Lcr%=\n\t" \
    ".Lcb%=:\n\t" \
    PRIUP \
    ASM_ST2 \
    "v_smfmac_f32_16x16x64_bf16 v[224:227], v[208:211], v[200:207], v241\n\t" \
    "ds_read_b128 v[200:203], %[wl] offset:4096\n\t" \
    "ds_read_b128 v[204:207], %[wl] offset:5120\n\t" \
    "v_smfmac_f32_16x16x64_bf16 v[244:247], v[208:211], v[0:7], v241\n\t" \
    "v_smfmac_f32_16x16x64_bf16 v[248:251], v[208:211], v[8:15], v241\n\t" \
    "s_waitcnt lgkmcnt(2)\n\t" \
    "buffer_load_dword %[fv], %[fv], %[rs], %[fs] offen sc1\n\t" \
    "s_barrier\n\t" ASM_ST \
    "ds_read_b128 v[212:215], v212 offset:0\n\t" \
    "ds_read_b128 v[216:219], v216 offset:0\n\t" \
    "ds_read_b128 v[220:223], v242 offset:0\n\t" \
    "ds_read_b128 v[208:211], v240 offset:0\n\t" \
    "v_smfmac_f32_16x16x64_bf16 v[244:247], v[236:239], v[80:87], v241\n\t" \
    "v_xor_b32 v240, 0x1800, %[ab]\n\t" \
    "v_smfmac_f32_16x16x64_bf16 v[248:251], v[236:239], v[88:95], v241\n\t" \
    "s_waitcnt lgkmcnt(4)\n\t" \
    "v_smfmac_f32_16x16x64_bf16 v[224:227], v[236:239], v[200:207], v241\n\t" \
    "ds_read_b128 v[200:203], %[wl] offset:6144\n\t" \
    "ds_read_b128 v[204:207], %[wl] offset:7168\n\t" \
    "ds_read_b128 v[236:239], v240 offset:0\n\t" \
    "s_waitcnt lgkmcnt(6)\n\t" \
    "v_smfmac_f32_16x16x64_bf16 v[224:227], v[212:215], v[16:23], v241\n\t" \
    "v_smfmac_f32_16x16x64_bf16 v[244:247], v[212:215], v[24:31], v241\n\t" \
    "v_xor_b32 v240, 0x1c00, %[ab]\n\t" \
    "v_smfmac_f32_16x16x64_bf16 v[248:251], v[212:215], v[32:39], v241\n\t" \
    "ds_read_b128 v[212:215], v240 offset:0\n\t" \
    "s_waitcnt lgkmcnt(6)\n\t" \
    "v_smfmac_f32_16x16x64_bf16 v[224:227], v[216:219], v[228:235], v241\n\t" \
    "ds_read_b128 v[228:231], %[wl] offset:8192\n\t" \
    "ds_read_b128 v[232:235], %[wl] offset:9216\n\t" \
    "v_smfmac_f32_16x16x64_bf16 v[244:247], v[216:219], v[40:47], v241\n\t" \
    "v_and_b32 v240, 0x3ff, %[ab]\n\t" \
    "v_smfmac_f32_16x16x64_bf16 v[248:251], v[216:219], v[48:55], v241\n\t" \
    "ds_read_b128 v[216:219], v240 offset:8192\n\t" \
    "s_waitcnt lgkmcnt(8)\n\t" \
    "v_smfmac_f32_16x16x64_bf16 v[224:227], v[220:223], v[56:63], v241\n\t" \
    "v_smfmac_f32_16x16x64_bf16 v[244:247], v[220:223], v[64:71], v241\n\t" \
    "v_smfmac_f32_16x16x64_bf16 v[248:251], v[220:223], v[72:79], v241\n\t" \
    "ds_read_b128 v[220:223], v240 offset:9216\n\t" \
    "s_waitcnt lgkmcnt(8)\n\t" \
    "v_smfmac_f32_16x16x64_bf16 v[244:247], v[208:211], v[104:111], v241\n\t" \
    "s_waitcnt lgkmcnt(5)\n\t" \
    "v_smfmac_f32_16x16x64_bf16 v[244:247], v[236:239], v[120:127], v241\n\t" \
    "s_waitcnt lgkmcnt(4)\n\t" \
    "v_smfmac_f32_16x16x64_bf16 v[244:247], v[212:215], v[144:151], v241\n\t" \
    "s_waitcnt lgkmcnt(1)\n\t" \
    "v_smfmac_f32_16x16x64_bf16 v[244:247], v[216:219], v[160:167], v241\n\t" \
    "s_waitcnt lgkmcnt(0)\n\t" \
    "v_smfmac_f32_16x16x64_bf16 v[244:247], v[220:223], v[184:191], v241\n\t" \
    "v_smfmac_f32_16x16x64_bf16 v[248:251], v[208:211], v[112:119], v241\n\t" \
    "v_smfmac_f32_16x16x64_bf16 v[248:251], v[236:239], v[128:135], v241\n\t" \
    "v_smfmac_f32_16x16x64_bf16 v[248:251], v[212:215], v[152:159], v241\n\t" \
    "v_permlane32_swap_b32 v244, v246\n\t" \
    "v_permlane32_swap_b32 v245, v247\n\t" \
    "v_smfmac_f32_16x16x64_bf16 v[248:251], v[216:219], v[168:175], v241\n\t" \
    "v_add_f32 v244, v244, v246\n\t" \
    "v_add_f32 v245, v245, v247\n\t" \
    "v_smfmac_f32_16x16x64_bf16 v[248:251], v[220:223], v[192:199], v241\n\t" \
    "v_cvt_pk_bf16_f32 v244, v244, v245\n\t" \
    "ds_write_b16 %[dst], v244 offset:512\n\t" \
    "ds_write_b16_d16_hi %[dst], v244 offset:528\n\t" \
    "v_smfmac_f32_16x16x64_bf16 v[224:227], v[208:211], v[96:103], v241\n\t" \
    "v_smfmac_f32_16x16x64_bf16 v[224:227], v[236:239], v[200:207], v241\n\t" \
    "v_smfmac_f32_16x16x64_bf16 v[224:227], v[212:215], v[136:143], v241\n\t" \
    "v_permlane32_swap_b32 v248, v250\n\t" \
    "v_permlane32_swap_b32 v249, v251\n\t" \
    "v_smfmac_f32_16x16x64_bf16 v[224:227], v[216:219], v[228:235], v241\n\t" \
    "v_add_f32 v248, v248, v250\n\t" \
    "v_add_f32 v249, v249, v251\n\t" \
    "v_smfmac_f32_16x16x64_bf16 v[224:227], v[220:223], v[176:183], v241\n\t" \
    "v_cvt_pk_bf16_f32 v248, v248, v249\n\t" \
    "ds_write_b16 %[dst], v248 offset:1024\n\t" \
    "ds_write_b16_d16_hi %[dst], v248 offset:1040\n\t" \
    "s_nop 4\n\t" \
    "v_permlane32_swap_b32 v224, v226\n\t" \
    "v_permlane32_swap_b32 v225, v227\n\t" \
    "v_add_f32 v224, v224, v226\n\t" \
    "v_add_f32 v225, v225, v227\n\t" \
    "v_cvt_pk_bf16_f32 v224, v224, v225\n\t" \
    "ds_write_b16 %[dst], v224 offset:0\n\t" \
    "ds_write_b16_d16_hi %[dst], v224 offset:16\n\t"
#define ASM_VOX \
    "s_waitcnt lgkmcnt(0)\n\t" \
    "ds_read_b128 v[208:211], %[va] offset:2048\n\t" \
    "ds_read_b128 v[212:215], %[va] offset:3072\n\t" \
    "ds_read_b128 v[216:219], %[va] offset:4096\n\t" \
    "ds_read_b128 v[220:223], %[va] offset:5120\n\t" \
    "ds_read_b128 v[200:203], %[va] offset:0\n\t" \
    "ds_read_b128 v[204:207], %[va] offset:1024\n\t" \
    "ds_read_b128 v[236:239], %[wl] offset:0\n\t" \
    "ds_read_b128 v[240:243], %[wl] offset:1024\n\t" \
    "s_waitcnt lgkmcnt(7)\n\t" \
    "v_mfma_f32_16x16x32_bf16 v[244:247], v[208:211], v[0:3], 0\n\t" \
    "s_waitcnt lgkmcnt(6)\n\t" \
    "v_mfma_f32_16x16x32_bf16 v[244:247], v[212:215], v[4:7], v[244:247]\n\t" \
    "s_waitcnt lgkmcnt(5)\n\t" \
    "v_mfma_f32_16x16x32_bf16 v[244:247], v[216:219], v[8:11], v[244:247]\n\t" \
    "s_waitcnt lgkmcnt(4)\n\t" \
    "v_mfma_f32_16x16x32_bf16 v[244:247], v[220:223], v[12:15], v[244:247]\n\t" \
    "s_waitcnt lgkmcnt(1)\n\t" \
    "v_mfma_f32_16x16x32_bf16 v[244:247], v[200:203], v[236:239], v[244:247]\n\t" \
    "ds_read_b128 v[236:239], %[wl] offset:2048\n\t" \
    "s_waitcnt lgkmcnt(1)\n\t" \
    "v_mfma_f32_16x16x32_bf16 v[244:247], v[204:207], v[240:243], v[244:247]\n\t" \
    "ds_read_b128 v[240:243], %[wl] offset:3072\n\t" \
    "v_mfma_f32_16x16x32_bf16 v[248:251], v[208:211], v[40:43], 0\n\t" \
    "v_mfma_f32_16x16x32_bf16 v[248:251], v[212:215], v[44:47], v[248:251]\n\t" \
    "v_mfma_f32_16x16x32_bf16 v[248:251], v[216:219], v[48:51], v[248:251]\n\t" \
    "v_cvt_pk_bf16_f32 v224, v244, v245\n\t" \
    "v_mfma_f32_16x16x32_bf16 v[248:251], v[220:223], v[52:55], v[248:251]\n\t" \
    "v_cvt_pk_bf16_f32 v225, v246, v247\n\t" \
    "s_waitcnt lgkmcnt(1)\n\t" \
    "v_mfma_f32_16x16x32_bf16 v[248:251], v[200:203], v[236:239], v[248:251]\n\t" \
    "ds_read_b128 v[236:239], %[wl] offset:4096\n\t" \
    "s_waitcnt lgkmcnt(1)\n\t" \
    "v_mfma_f32_16x16x32_bf16 v[248:251], v[204:207], v[240:243], v[248:251]\n\t" \
    "ds_read_b128 v[240:243], %[wl] offset:5120\n\t" \
    "v_mfma_f32_16x16x32_bf16 v[244:247], v[208:211], v[80:83], 0\n\t" \
    "v_mfma_f32_16x16x32_bf16 v[244:247], v[212:215], v[84:87], v[244:247]\n\t" \
    "v_mfma_f32_16x16x32_bf16 v[244:247], v[216:219], v[88:91], v[244:247]\n\t" \
    "v_cvt_pk_bf16_f32 v226, v248, v249\n\t" \
    "v_mfma_f32_16x16x32_bf16 v[244:247], v[220:223], v[92:95], v[244:247]\n\t" \
    "v_cvt_pk_bf16_f32 v227, v250, v251\n\t" \
    "s_waitcnt lgkmcnt(1)\n\t" \
    "v_mfma_f32_16x16x32_bf16 v[244:247], v[200:203], v[236:239], v[244:247]\n\t" \
    "ds_read_b128 v[236:239], %[wl] offset:6144\n\t" \
    "s_waitcnt lgkmcnt(1)\n\t" \
    "v_mfma_f32_16x16x32_bf16 v[244:247], v[204:207], v[240:243], v[244:247]\n\t" \
    "ds_read_b128 v[240:243], %[wl] offset:7168\n\t" \
    "v_mfma_f32_16x16x32_bf16 v[248:251], v[208:211], v[120:123], 0\n\t" \
    "v_mfma_f32_16x16x32_bf16 v[248:251], v[212:215], v[124:127], v[248:251]\n\t" \
    "v_mfma_f32_16x16x32_bf16 v[248:251], v[216:219], v[128:131], v[248:251]\n\t" \
    "v_cvt_pk_bf16_f32 v228, v244, v245\n\t" \
    "v_mfma_f32_16x16x32_bf16 v[248:251], v[220:223], v[132:135], v[248:251]\n\t" \
    "v_cvt_pk_bf16_f32 v229, v246, v247\n\t" \
    "s_waitcnt lgkmcnt(1)\n\t" \
    "v_mfma_f32_16x16x32_bf16 v[248:251], v[200:203], v[236:239], v[248:251]\n\t" \
    "ds_read_b128 v[236:239], %[wl] offset:8192\n\t" \
    "s_waitcnt lgkmcnt(1)\n\t" \
    "v_mfma_f32_16x16x32_bf16 v[248:251], v[204:207], v[240:243], v[248:251]\n\t" \
    "ds_read_b128 v[240:243], %[wl] offset:9216\n\t" \
    "v_mfma_f32_16x16x32_bf16 v[244:247], v[208:211], v[160:163], 0\n\t" \
    "v_mfma_f32_16x16x32_bf16 v[244:247], v[212:215], v[164:167], v[244:247]\n\t" \
    "v_mfma_f32_16x16x32_bf16 v[244:247], v[216:219], v[168:171], v[244:247]\n\t" \
    "v_cvt_pk_bf16_f32 v230, v248, v249\n\t" \
    "v_mfma_f32_16x16x32_bf16 v[244:247], v[220:223], v[172:175], v[244:247]\n\t" \
    "v_cvt_pk_bf16_f32 v231, v250, v251\n\t" \
    "s_waitcnt lgkmcnt(1)\n\t" \
    "v_mfma_f32_16x16x32_bf16 v[244:247], v[200:203], v[236:239], v[244:247]\n\t" \
    "s_waitcnt lgkmcnt(0)\n\t" \
    "v_mfma_f32_16x16x32_bf16 v[244:247], v[204:207], v[240:243], v[244:247]\n\t" \
    "v_mfma_f32_16x16x32_bf16 v[248:251], v[208:211], v[24:27], 0\n\t" \
    "v_mfma_f32_16x16x32_bf16 v[248:251], v[212:215], v[28:31], v[248:251]\n\t" \
    "v_mfma_f32_16x16x32_bf16 v[248:251], v[216:219], v[32:35], v[248:251]\n\t" \
    "v_cvt_pk_bf16_f32 v232, v244, v245\n\t" \
    "v_mfma_f32_16x16x32_bf16 v[248:251], v[220:223], v[36:39], v[248:251]\n\t" \
    "v_cvt_pk_bf16_f32 v233, v246, v247\n\t" \
    "v_mfma_f32_16x16x32_bf16 v[248:251], v[200:203], v[16:19], v[248:251]\n\t" \
    "v_mfma_f32_16x16x32_bf16 v[248:251], v[204:207], v[20:23], v[248:251]\n\t" \
    "v_mfma_f32_16x16x32_bf16 v[244:247], v[208:211], v[64:67], 0\n\t" \
    "v_mfma_f32_16x16x32_bf16 v[244:247], v[212:215], v[68:71], v[244:247]\n\t" \
    "v_mfma_f32_16x16x32_bf16 v[244:247], v[216:219], v[72:75], v[244:247]\n\t" \
    "v_cvt_pk_bf16_f32 v236, v248, v249\n\t" \
    "v_mfma_f32_16x16x32_bf16 v[244:247], v[220:223], v[76:79], v[244:247]\n\t" \
    "v_cvt_pk_bf16_f32 v237, v250, v251\n\t" \
    "v_mfma_f32_16x16x32_bf16 v[244:247], v[200:203], v[56:59], v[244:247]\n\t" \
    "v_mfma_f32_16x16x32_bf16 v[244:247], v[204:207], v[60:63], v[244:247]\n\t" \
    "v_mfma_f32_16x16x32_bf16 v[248:251], v[208:211], v[104:107], 0\n\t" \
    "v_mfma_f32_16x16x32_bf16 v[248:251], v[212:215], v[108:111], v[248:251]\n\t" \
    "v_mfma_f32_16x16x32_bf16 v[248:251], v[216:219], v[112:115], v[248:251]\n\t" \
    "v_cvt_pk_bf16_f32 v238, v244, v245\n\t" \
    "v_mfma_f32_16x16x32_bf16 v[248:251], v[220:223], v[116:119], v[248:251]\n\t" \
    "v_cvt_pk_bf16_f32 v239, v246, v247\n\t" \
    "v_mfma_f32_16x16x32_bf16 v[248:251], v[200:203], v[96:99], v[248:251]\n\t" \
    "v_mfma_f32_16x16x32_bf16 v[248:251], v[204:207], v[100:103], v[248:251]\n\t" \
    "v_mfma_f32_16x16x32_bf16 v[244:247], v[208:211], v[144:147], 0\n\t" \
    "v_mfma_f32_16x16x32_bf16 v[244:247], v[212:215], v[148:151], v[244:247]\n\t" \
    "v_mfma_f32_16x16x32_bf16 v[244:247], v[216:219], v[152:155], v[244:247]\n\t" \
    "v_cvt_pk_bf16_f32 v240, v248, v249\n\t" \
    "v_mfma_f32_16x16x32_bf16 v[244:247], v[220:223], v[156:159], v[244:247]\n\t" \
    "v_cvt_pk_bf16_f32 v241, v250, v251\n\t" \
    "v_mfma_f32_16x16x32_bf16 v[244:247], v[200:203], v[136:139], v[244:247]\n\t" \
    "v_mfma_f32_16x16x32_bf16 v[244:247], v[204:207], v[140:143], v[244:247]\n\t" \
    "s_cmp_eq_u32 %[sy], 0\n\t" \
    "s_cbranch_scc1 .Lvb%=\n\t" \
    "s_barrier \n\t" \
    ".Lvb%=:\n\t" \
    "v_mfma_f32_16x16x32_bf16 v[248:251], v[208:211], v[184:187], 0\n\t" \
    "ds_read_b128 v[208:211], %[va] offset:8192\n\t" \
    "v_mfma_f32_16x16x32_bf16 v[248:251], v[212:215], v[188:191], v[248:251]\n\t" \
    "ds_read_b128 v[212:215], %[va] offset:9216\n\t" \
    "v_mfma_f32_16x16x32_bf16 v[248:251], v[216:219], v[192:195], v[248:251]\n\t" \
    "ds_read_b128 v[216:219], %[va] offset:10240\n\t" \
    "v_cvt_pk_bf16_f32 v242, v244, v245\n\t" \
    "v_mfma_f32_16x16x32_bf16 v[248:251], v[220:223], v[196:199], v[248:251]\n\t" \
    "ds_read_b128 v[220:223], %[va] offset:11264\n\t" \
    "v_cvt_pk_bf16_f32 v243, v246, v247\n\t" \
    "v_mfma_f32_16x16x32_bf16 v[248:251], v[200:203], v[176:179], v[248:251]\n\t" \
    "ds_read_b128 v[200:203], %[va] offset:6144\n\t" \
    "v_mfma_f32_16x16x32_bf16 v[248:251], v[204:207], v[180:183], v[248:251]\n\t" \
    "ds_read_b128 v[204:207], %[va] offset:7168\n\t" \
    "s_waitcnt lgkmcnt(5)\n\t" \
    "v_mfma_f32_16x16x32_bf16 v[244:247], v[208:211], v[24:27], 0\n\t" \
    "s_waitcnt lgkmcnt(4)\n\t" \
    "v_mfma_f32_16x16x32_bf16 v[244:247], v[212:215], v[28:31], v[244:247]\n\t" \
    "s_waitcnt lgkmcnt(3)\n\t" \
    "v_mfma_f32_16x16x32_bf16 v[244:247], v[216:219], v[32:35], v[244:247]\n\t" \
    "v_cvt_pk_bf16_f32 v234, v248, v249\n\t" \
    "s_waitcnt lgkmcnt(2)\n\t" \
    "v_mfma_f32_16x16x32_bf16 v[244:247], v[220:223], v[36:39], v[244:247]\n\t" \
    "v_cvt_pk_bf16_f32 v235, v250, v251\n\t" \
    "s_waitcnt lgkmcnt(1)\n\t" \
    "v_mfma_f32_16x16x32_bf16 v[244:247], v[200:203], v[16:19], v[244:247]\n\t" \
    "s_waitcnt lgkmcnt(0)\n\t" \
    "v_mfma_f32_16x16x32_bf16 v[244:247], v[204:207], v[20:23], v[244:247]\n\t" \
    "v_mfma_f32_16x16x32_bf16 v[248:251], v[208:211], v[64:67], 0\n\t" \
    "s_add_i32 %[so], %[so], 65536\n\t" \
    "v_mfma_f32_16x16x32_bf16 v[248:251], v[212:215], v[68:71], v[248:251]\n\t" \
    "s_nop 1\n\t" \
    "v_cvt_pk_bf16_f32 v247, v246, v247\n\t" \
    "v_mfma_f32_16x16x32_bf16 v[248:251], v[216:219], v[72:75], v[248:251]\n\t" \
    "v_cvt_pk_bf16_f32 v246, v244, v245\n\t" \
    "v_mfma_f32_16x16x32_bf16 v[248:251], v[220:223], v[76:79], v[248:251]\n\t" \
    "v_mov_b64 v[244:245], v[236:237]\n\t" \
    "buffer_store_dwordx4 v[244:247], %[l16], %[rd], %[so] offen sc1\n\t" \
    "v_mfma_f32_16x16x32_bf16 v[248:251], v[200:203], v[56:59], v[248:251]\n\t" \
    "v_mfma_f32_16x16x32_bf16 v[248:251], v[204:207], v[60:63], v[248:251]\n\t" \
    "v_mfma_f32_16x16x32_bf16 v[244:247], v[208:211], v[104:107], 0\n\t" \
    "s_add_i32 %[so], %[so], 131072\n\t" \
    "v_mfma_f32_16x16x32_bf16 v[244:247], v[212:215], v[108:111], v[244:247]\n\t" \
    "s_nop 1\n\t" \
    "v_cvt_pk_bf16_f32 v251, v250, v251\n\t" \
    "v_mfma_f32_16x16x32_bf16 v[244:247], v[216:219], v[112:115], v[244:247]\n\t" \
    "v_cvt_pk_bf16_f32 v250, v248, v249\n\t" \
    "v_mfma_f32_16x16x32_bf16 v[244:247], v[220:223], v[116:119], v[244:247]\n\t" \
    "v_mov_b64 v[248:249], v[238:239]\n\t" \
    "buffer_store_dwordx4 v[248:251], %[l16], %[rd], %[so] offen sc1\n\t" \
    "v_mfma_f32_16x16x32_bf16 v[244:247], v[200:203], v[96:99], v[244:247]\n\t" \
    "v_mfma_f32_16x16x32_bf16 v[244:247], v[204:207], v[100:103], v[244:247]\n\t" \
    "v_mfma_f32_16x16x32_bf16 v[248:251], v[208:211], v[144:147], 0\n\t" \
    "s_add_i32 %[so], %[so], 131072\n\t" \
    "v_mfma_f32_16x16x32_bf16 v[248:251], v[212:215], v[148:151], v[248:251]\n\t" \
    "s_nop 1\n\t" \
    "v_cvt_pk_bf16_f32 v247, v246, v247\n\t" \
    "v_mfma_f32_16x16x32_bf16 v[248:251], v[216:219], v[152:155], v[248:251]\n\t" \
    "v_cvt_pk_bf16_f32 v246, v244, v245\n\t" \
    "v_mfma_f32_16x16x32_bf16 v[248:251], v[220:223], v[156:159], v[248:251]\n\t" \
    "v_mov_b64 v[244:245], v[240:241]\n\t" \
    "buffer_store_dwordx4 v[244:247], %[l16], %[rd], %[so] offen sc1\n\t" \
    "v_mfma_f32_16x16x32_bf16 v[248:251], v[200:203], v[136:139], v[248:251]\n\t" \
    "v_mfma_f32_16x16x32_bf16 v[248:251], v[204:207], v[140:143], v[248:251]\n\t" \
    "v_mfma_f32_16x16x32_bf16 v[244:247], v[208:211], v[184:187], 0\n\t" \
    "s_add_i32 %[so], %[so], 131072\n\t" \
    "v_mfma_f32_16x16x32_bf16 v[244:247], v[212:215], v[188:191], v[244:247]\n\t" \
    "s_nop 1\n\t" \
    "v_cvt_pk_bf16_f32 v251, v250, v251\n\t" \
    "v_mfma_f32_16x16x32_bf16 v[244:247], v[216:219], v[192:195], v[244:247]\n\t" \
    "v_cvt_pk_bf16_f32 v250, v248, v249\n\t" \
    "v_mfma_f32_16x16x32_bf16 v[244:247], v[220:223], v[196:199], v[244:247]\n\t" \
    "v_mov_b64 v[248:249], v[242:243]\n\t" \
    "buffer_store_dwordx4 v[248:251], %[l16], %[rd], %[so] offen sc1\n\t" \
    "v_mfma_f32_16x16x32_bf16 v[244:247], v[200:203], v[176:179], v[244:247]\n\t" \
    "v_mfma_f32_16x16x32_bf16 v[244:247], v[204:207], v[180:183], v[244:247]\n\t" \
    "ds_read_b128 v[236:239], %[wl] offset:0\n\t" \
    "ds_read_b128 v[240:243], %[wl] offset:1024\n\t" \
    "v_mfma_f32_16x16x32_bf16 v[248:251], v[208:211], v[0:3], 0\n\t" \
    "s_add_i32 %[so], %[so], 131072\n\t" \
    "v_mfma_f32_16x16x32_bf16 v[248:251], v[212:215], v[4:7], v[248:251]\n\t" \
    "v_cvt_pk_bf16_f32 v247, v246, v247\n\t" \
    "v_mfma_f32_16x16x32_bf16 v[248:251], v[216:219], v[8:11], v[248:251]\n\t" \
    "v_cvt_pk_bf16_f32 v246, v244, v245\n\t" \
    "v_mfma_f32_16x16x32_bf16 v[248:251], v[220:223], v[12:15], v[248:251]\n\t" \
    "v_mov_b64 v[244:245], v[234:235]\n\t" \
    "buffer_store_dwordx4 v[244:247], %[l16], %[rd], %[so] offen sc1\n\t" \
    "s_waitcnt lgkmcnt(1)\n\t" \
    "v_mfma_f32_16x16x32_bf16 v[248:251], v[200:203], v[236:239], v[248:251]\n\t" \
    "ds_read_b128 v[236:239], %[wl] offset:2048\n\t" \
    "s_waitcnt lgkmcnt(1)\n\t" \
    "v_mfma_f32_16x16x32_bf16 v[248:251], v[204:207], v[240:243], v[248:251]\n\t" \
    "ds_read_b128 v[240:243], %[wl] offset:3072\n\t" \
    "v_mfma_f32_16x16x32_bf16 v[244:247], v[208:211], v[40:43], 0\n\t" \
    "s_add_i32 %[so], %[so], -589824\n\t" \
    "v_mfma_f32_16x16x32_bf16 v[244:247], v[212:215], v[44:47], v[244:247]\n\t" \
    "s_nop 0\n\t" \
    "v_cvt_pk_bf16_f32 v251, v250, v251\n\t" \
    "v_mfma_f32_16x16x32_bf16 v[244:247], v[216:219], v[48:51], v[244:247]\n\t" \
    "v_cvt_pk_bf16_f32 v250, v248, v249\n\t" \
    "v_mfma_f32_16x16x32_bf16 v[244:247], v[220:223], v[52:55], v[244:247]\n\t" \
    "v_mov_b64 v[248:249], v[224:225]\n\t" \
    "buffer_store_dwordx4 v[248:251], %[l16], %[rd], %[so] offen sc1\n\t" \
    "s_waitcnt lgkmcnt(1)\n\t" \
    "v_mfma_f32_16x16x32_bf16 v[244:247], v[200:203], v[236:239], v[244:247]\n\t" \
    "ds_read_b128 v[236:239], %[wl] offset:4096\n\t" \
    "s_waitcnt lgkmcnt(1)\n\t" \
    "v_mfma_f32_16x16x32_bf16 v[244:247], v[204:207], v[240:243], v[244:247]\n\t" \
    "ds_read_b128 v[240:243], %[wl] offset:5120\n\t" \
    "v_mfma_f32_16x16x32_bf16 v[248:251], v[208:211], v[80:83], 0\n\t" \
    "s_add_i32 %[so], %[so], 131072\n\t" \
    "v_mfma_f32_16x16x32_bf16 v[248:251], v[212:215], v[84:87], v[248:251]\n\t" \
    "s_nop 0\n\t" \
    "v_cvt_pk_bf16_f32 v247, v246, v247\n\t" \
    "v_mfma_f32_16x16x32_bf16 v[248:251], v[216:219], v[88:91], v[248:251]\n\t" \
    "v_cvt_pk_bf16_f32 v246, v244, v245\n\t" \
    "v_mfma_f32_16x16x32_bf16 v[248:251], v[220:223], v[92:95], v[248:251]\n\t" \
    "v_mov_b64 v[244:245], v[226:227]\n\t" \
    "buffer_store_dwordx4 v[244:247], %[l16], %[rd], %[so] offen sc1\n\t" \
    "s_waitcnt lgkmcnt(1)\n\t" \
    "v_mfma_f32_16x16x32_bf16 v[248:251], v[200:203], v[236:239], v[248:251]\n\t" \
    "ds_read_b128 v[236:239], %[wl] offset:6144\n\t" \
    "s_waitcnt lgkmcnt(1)\n\t" \
    "v_mfma_f32_16x16x32_bf16 v[248:251], v[204:207], v[240:243], v[248:251]\n\t" \
    "ds_read_b128 v[240:243], %[wl] offset:7168\n\t" \
    "v_mfma_f32_16x16x32_bf16 v[244:247], v[208:211], v[120:123], 0\n\t" \
    "s_add_i32 %[so], %[so], 131072\n\t" \
    "v_mfma_f32_16x16x32_bf16 v[244:247], v[212:215], v[124:127], v[244:247]\n\t" \
    "s_nop 0\n\t" \
    "v_cvt_pk_bf16_f32 v251, v250, v251\n\t" \
    "v_mfma_f32_16x16x32_bf16 v[244:247], v[216:219], v[128:131], v[244:247]\n\t" \
    "v_cvt_pk_bf16_f32 v250, v248, v249\n\t" \
    "v_mfma_f32_16x16x32_bf16 v[244:247], v[220:223], v[132:135], v[244:247]\n\t" \
    "v_mov_b64 v[248:249], v[228:229]\n\t" \
    "buffer_store_dwordx4 v[248:251], %[l16], %[rd], %[so] offen sc1\n\t" \
    "s_waitcnt lgkmcnt(1)\n\t" \
    "v_mfma_f32_16x16x32_bf16 v[244:247], v[200:203], v[236:239], v[244:247]\n\t" \
    "ds_read_b128 v[236:239], %[wl] offset:8192\n\t" \
    "s_waitcnt lgkmcnt(1)\n\t" \
    "v_mfma_f32_16x16x32_bf16 v[244:247], v[204:207], v[240:243], v[244:247]\n\t" \
    "ds_read_b128 v[240:243], %[wl] offset:9216\n\t" \
    "v_mfma_f32_16x16x32_bf16 v[248:251], v[208:211], v[160:163], 0\n\t" \
    "s_add_i32 %[so], %[so], 131072\n\t" \
    "v_mfma_f32_16x16x32_bf16 v[248:251], v[212:215], v[164:167], v[248:251]\n\t" \
    "s_nop 0\n\t" \
    "v_cvt_pk_bf16_f32 v247, v246, v247\n\t" \
    "v_mfma_f32_16x16x32_bf16 v[248:251], v[216:219], v[168:171], v[248:251]\n\t" \
    "v_cvt_pk_bf16_f32 v246, v244, v245\n\t" \
    "v_mfma_f32_16x16x32_bf16 v[248:251], v[220:223], v[172:175], v[248:251]\n\t" \
    "v_mov_b64 v[244:245], v[230:231]\n\t" \
    "buffer_store_dwordx4 v[244:247], %[l16], %[rd], %[so] offen sc1\n\t" \
    "s_waitcnt lgkmcnt(1)\n\t" \
    "v_mfma_f32_16x16x32_bf16 v[248:251], v[200:203], v[236:239], v[248:251]\n\t" \
    "s_waitcnt lgkmcnt(0)\n\t" \
    "v_mfma_f32_16x16x32_bf16 v[248:251], v[204:207], v[240:243], v[248:251]\n\t" \
    "s_add_i32 %[so], %[so], 131072\n\t" \
    "s_nop 7\n\t" \
    "v_cvt_pk_bf16_f32 v251, v250, v251\n\t" \
    "v_cvt_pk_bf16_f32 v250, v248, v249\n\t" \
    "v_mov_b64 v[248:249], v[232:233]\n\t" \
    "buffer_store_dwordx4 v[248:251], %[l16], %[rd], %[so] offen sc1\n\t" \
    "s_nop 1\n\t"
#define ASM_CRIT2 \
    "s_waitcnt lgkmcnt(0)\n\t" \
    "s_barrier\n\t" ASM_ST \
    "ds_read_b128 v[204:207], v200 offset:11264\n\t" \
    "ds_read_b128 v[208:211], v200 offset:12288\n\t" \
    "ds_read_b128 v[200:203], v200 offset:10240\n\t" \
    "s_waitcnt lgkmcnt(2)\n\t" \
    "v_smfmac_f32_16x16x64_bf16 v[224:227], v[204:207], v[24:31], %[sidx]\n\t" \
    "s_waitcnt lgkmcnt(1)\n\t" \
    "v_smfmac_f32_16x16x64_bf16 v[224:227], v[208:211], v[32:39], %[sidx]\n\t" \
    "s_waitcnt lgkmcnt(0)\n\t" \
    "v_smfmac_f32_16x16x64_bf16 v[224:227], v[200:203], v[16:23], %[sidx]\n\t" \
    "v_smfmac_f32_16x16x64_bf16 v[220:223], v[204:207], v[0:7], %[sidx]\n\t" \
    "v_smfmac_f32_16x16x64_bf16 v[220:223], v[208:211], v[8:15], %[sidx]\n\t" \
    "v_smfmac_f32_16x16x64_bf16 v[220:223], v[200:203], v[212:219], %[sidx]\n\t" \
    "ds_read_b128 v[212:215], %[wl] offset:2048\n\t" \
    "ds_read_b128 v[216:219], %[wl] offset:3072\n\t" \
    "v_smfmac_f32_16x16x64_bf16 v[232:235], v[204:207], v[64:71], %[sidx]\n\t" \
    "v_smfmac_f32_16x16x64_bf16 v[232:235], v[208:211], v[72:79], %[sidx]\n\t" \
    "v_smfmac_f32_16x16x64_bf16 v[232:235], v[200:203], v[56:63], %[sidx]\n\t" \
    "v_permlane32_swap_b32 v220, v224\n\t" \
    "v_permlane32_swap_b32 v221, v225\n\t" \
    "v_permlane32_swap_b32 v222, v226\n\t" \
    "v_smfmac_f32_16x16x64_bf16 v[228:231], v[204:207], v[40:47], %[sidx]\n\t" \
    "v_permlane32_swap_b32 v223, v227\n\t" \
    "v_add_f32 v220, v220, v224\n\t" \
    "v_add_f32 v221, v221, v225\n\t" \
    "v_smfmac_f32_16x16x64_bf16 v[228:231], v[208:211], v[48:55], %[sidx]\n\t" \
    "v_add_f32 v222, v222, v226\n\t" \
    "v_add_f32 v223, v223, v227\n\t" \
    "v_cvt_pk_bf16_f32 v226, v220, v221\n\t" \
    "s_waitcnt lgkmcnt(0)\n\t" \
    "v_smfmac_f32_16x16x64_bf16 v[228:231], v[200:203], v[212:219], %[sidx]\n\t" \
    "ds_read_b128 v[212:215], %[wl] offset:4096\n\t" \
    "ds_read_b128 v[216:219], %[wl] offset:5120\n\t" \
    "v_cvt_pk_bf16_f32 v227, v222, v223\n\t" \
    "v_min_u32 v226, -2, v226\n\t" \
    "v_min_u32 v227, -2, v227\n\t" \
    "v_smfmac_f32_16x16x64_bf16 v[240:243], v[204:207], v[104:111], %[sidx]\n\t" \
    "v_smfmac_f32_16x16x64_bf16 v[240:243], v[208:211], v[112:119], %[sidx]\n\t" \
    "v_smfmac_f32_16x16x64_bf16 v[240:243], v[200:203], v[96:103], %[sidx]\n\t" \
    "v_permlane32_swap_b32 v228, v232\n\t" \
    "v_permlane32_swap_b32 v229, v233\n\t" \
    "v_permlane32_swap_b32 v230, v234\n\t" \
    "v_smfmac_f32_16x16x64_bf16 v[236:239], v[204:207], v[80:87], %[sidx]\n\t" \
    "v_permlane32_swap_b32 v231, v235\n\t" \
    "v_add_f32 v228, v228, v232\n\t" \
    "v_add_f32 v229, v229, v233\n\t" \
    "v_smfmac_f32_16x16x64_bf16 v[236:239], v[208:211], v[88:95], %[sidx]\n\t" \
    "v_add_f32 v230, v230, v234\n\t" \
    "v_add_f32 v231, v231, v235\n\t" \
    "v_cvt_pk_bf16_f32 v228, v228, v229\n\t" \
    "s_waitcnt lgkmcnt(0)\n\t" \
    "v_smfmac_f32_16x16x64_bf16 v[236:239], v[200:203], v[212:219], %[sidx]\n\t" \
    "ds_read_b128 v[212:215], %[wl] offset:6144\n\t" \
    "ds_read_b128 v[216:219], %[wl] offset:7168\n\t" \
    "v_cvt_pk_bf16_f32 v229, v230, v231\n\t" \
    "v_min_u32 v228, -2, v228\n\t" \
    "v_min_u32 v229, -2, v229\n\t" \
    "s_nop 1\n\t" \
    "v_permlane16_swap_b32 v226, v228\n\t" \
    "v_permlane16_swap_b32 v227, v229\n\t" \
    "buffer_store_dwordx4 v[226:229], %[vo], %[rr], %[so] offen sc1\n\t" \
    "s_add_u32 %[so], %[so], 0x10000\n\t" \
    "v_smfmac_f32_16x16x64_bf16 v[248:251], v[204:207], v[144:151], %[sidx]\n\t" \
    "v_mov_b64 v[220:221], 0\n\t" \
    "v_mov_b64 v[222:223], 0\n\t" \
    "v_smfmac_f32_16x16x64_bf16 v[248:251], v[208:211], v[152:159], %[sidx]\n\t" \
    "v_mov_b64 v[224:225], 0\n\t" \
    "v_mov_b64 v[226:227], 0\n\t" \
    "v_smfmac_f32_16x16x64_bf16 v[248:251], v[200:203], v[136:143], %[sidx]\n\t" \
    "v_permlane32_swap_b32 v236, v240\n\t" \
    "v_permlane32_swap_b32 v237, v241\n\t" \
    "v_permlane32_swap_b32 v238, v242\n\t" \
    "v_smfmac_f32_16x16x64_bf16 v[244:247], v[204:207], v[120:127], %[sidx]\n\t" \
    "v_permlane32_swap_b32 v239, v243\n\t" \
    "v_add_f32 v236, v236, v240\n\t" \
    "v_add_f32 v237, v237, v241\n\t" \
    "v_smfmac_f32_16x16x64_bf16 v[244:247], v[208:211], v[128:135], %[sidx]\n\t" \
    "v_add_f32 v238, v238, v242\n\t" \
    "v_add_f32 v239, v239, v243\n\t" \
    "v_cvt_pk_bf16_f32 v242, v236, v237\n\t" \
    "s_waitcnt lgkmcnt(0)\n\t" \
    "v_smfmac_f32_16x16x64_bf16 v[244:247], v[200:203], v[212:219], %[sidx]\n\t" \
    "ds_read_b128 v[212:215], %[wl] offset:8192\n\t" \
    "ds_read_b128 v[216:219], %[wl] offset:9216\n\t" \
    "v_cvt_pk_bf16_f32 v243, v238, v239\n\t" \
    "v_min_u32 v242, -2, v242\n\t" \
    "v_min_u32 v243, -2, v243\n\t" \
    "v_smfmac_f32_16x16x64_bf16 v[224:227], v[204:207], v[184:191], %[sidx]\n\t" \
    "v_and_b32 v232, 0x3fff, %[vo]\n\t" \
    "v_bfe_u32 v233, %[vo], 14, 1\n\t" \
    "v_smfmac_f32_16x16x64_bf16 v[224:227], v[208:211], v[192:199], %[sidx]\n\t" \
    "v_lshl_add_u32 v232, v233, 3, v232\n\t" \
    "v_permlane32_swap_b32 v244, v248\n\t" \
    "v_smfmac_f32_16x16x64_bf16 v[224:227], v[200:203], v[176:183], %[sidx]\n\t" \
    "v_permlane32_swap_b32 v245, v249\n\t" \
    "v_permlane32_swap_b32 v246, v250\n\t" \
    "v_permlane32_swap_b32 v247, v251\n\t" \
    "v_smfmac_f32_16x16x64_bf16 v[220:223], v[204:207], v[160:167], %[sidx]\n\t" \
    "v_add_f32 v244, v244, v248\n\t" \
    "v_add_f32 v245, v245, v249\n\t" \
    "v_add_f32 v246, v246, v250\n\t" \
    "v_smfmac_f32_16x16x64_bf16 v[220:223], v[208:211], v[168:175], %[sidx]\n\t" \
    "v_add_f32 v247, v247, v251\n\t" \
    "v_cvt_pk_bf16_f32 v244, v244, v245\n\t" \
    "v_cvt_pk_bf16_f32 v245, v246, v247\n\t" \
    "s_waitcnt lgkmcnt(0)\n\t" \
    "v_smfmac_f32_16x16x64_bf16 v[220:223], v[200:203], v[212:219], %[sidx]\n\t" \
    "v_min_u32 v244, -2, v244\n\t" \
    "v_min_u32 v245, -2, v245\n\t" \
    "s_nop 1\n\t" \
    "v_permlane16_swap_b32 v242, v244\n\t" \
    "v_permlane16_swap_b32 v243, v245\n\t" \
    "buffer_store_dwordx4 v[242:245], %[vo], %[rr], %[so] offen sc1\n\t" \
    "s_add_u32 %[so], %[so], 0x10000\n\t" \
    "s_nop 0\n\t" \
    "v_permlane32_swap_b32 v220, v224\n\t" \
    "v_permlane32_swap_b32 v221, v225\n\t" \
    "v_permlane32_swap_b32 v222, v226\n\t" \
    "v_permlane32_swap_b32 v223, v227\n\t" \
    "v_add_f32 v220, v220, v224\n\t" \
    "v_add_f32 v221, v221, v225\n\t" \
    "v_add_f32 v222, v222, v226\n\t" \
    "v_add_f32 v223, v223, v227\n\t" \
    "v_cvt_pk_bf16_f32 v220, v220, v221\n\t" \
    "v_cvt_pk_bf16_f32 v221, v222, v223\n\t" \
    "v_min_u32 v220, -2, v220\n\t" \
    "v_min_u32 v221, -2, v221\n\t" \
    "buffer_store_dwordx2 v[220:221], v232, %[rr], %[so] offen sc1\n\t" \
    "s_nop 1\n\t"
#define ASM_SM \
    "v_mbcnt_lo_u32_b32 v252, -1, 0\n\t" \
    "v_mbcnt_hi_u32_b32 v252, -1, v252\n\t" \
    "v_lshlrev_b32 v252, 4, v252\n\t" \
    "v_lshrrev_b32 v253, 1, v252\n\t" \
    "v_xor_b32 v253, v253, v252\n\t" \
    "v_and_b32 v253, 0x20, v253\n\t" \
    "v_mul_u32_u24 v253, 3, v253\n\t" \
    "v_xor_b32 v253, v253, v252\n\t" \
    "ds_read_b128 v[200:203], v253 offset:28672\n\t" \
    "ds_read_b128 v[204:207], v253 offset:29696\n\t" \
    "ds_read_b128 v[208:211], v253 offset:34816\n\t" \
    "ds_read_b128 v[212:215], v253 offset:35840\n\t" \
    "ds_read_b128 v[216:219], v253 offset:30720\n\t" \
    "ds_read_b128 v[220:223], v253 offset:31744\n\t" \
    "ds_read_b128 v[224:227], v253 offset:36864\n\t" \
    "ds_read_b128 v[228:231], v253 offset:37888\n\t" \
    "v_lshrrev_b32 v254, 1, v252\n\t" \
    "v_and_b32 v254, 0x180, v254\n\t" \
    "v_and_or_b32 v254, v252, %[c70], v254\n\t" \
    "v_bfe_i32 v255, v252, 7, 1\n\t" \
    "v_and_b32 v255, 0xaaaa, v255\n\t" \
    "v_or_b32 v255, 0x4444, v255\n\t" \
    "v_mov_b64 v[232:233], 0\n\t" \
    "v_mov_b64 v[234:235], 0\n\t" \
    "v_mov_b64 v[236:237], 0\n\t" \
    "v_mov_b64 v[238:239], 0\n\t" \
    "s_waitcnt lgkmcnt(0)\n\t" \
    "s_barrier\n\t" ASM_ST \
    "s_setprio 3\n\t" \
    "ds_read_b128 v[240:243], v252 offset:10240\n\t" \
    "ds_read_b128 v[244:247], v252 offset:11264\n\t" \
    "ds_read_b128 v[248:251], v252 offset:12288\n\t" \
    "s_waitcnt lgkmcnt(2)\n\t" \
    "v_smfmac_f32_16x16x64_bf16 v[232:235], v[240:243], v[200:207], v255\n\t" \
    "ds_read_b128 v[200:203], v253 offset:32768\n\t" \
    "ds_read_b128 v[204:207], v253 offset:33792\n\t" \
    "v_smfmac_f32_16x16x64_bf16 v[236:239], v[240:243], v[208:215], v255\n\t" \
    "ds_read_b128 v[208:211], v253 offset:38912\n\t" \
    "ds_read_b128 v[212:215], v253 offset:39936\n\t" \
    "s_waitcnt lgkmcnt(5)\n\t" \
    "v_smfmac_f32_16x16x64_bf16 v[232:235], v[244:247], v[216:223], v255\n\t" \
    "ds_read_b128 v[216:219], v254 offset:13312\n\t" \
    "ds_read_b128 v[220:223], v254 offset:13824\n\t" \
    "v_smfmac_f32_16x16x64_bf16 v[236:239], v[244:247], v[224:231], v255\n\t" \
    "ds_read_b128 v[224:227], v254 offset:14336\n\t" \
    "ds_read_b128 v[228:231], v254 offset:14848\n\t" \
    "s_waitcnt lgkmcnt(6)\n\t" \
    "v_smfmac_f32_16x16x64_bf16 v[232:235], v[248:251], v[200:207], v255\n\t" \
    "s_waitcnt lgkmcnt(4)\n\t" \
    "v_smfmac_f32_16x16x64_bf16 v[236:239], v[248:251], v[208:215], v255\n\t" \
    "ds_read_b128 v[208:211], v254 offset:15360\n\t" \
    "ds_read_b128 v[212:215], v254 offset:15872\n\t" \
    "v_mov_b64 v[204:205], 0\n\t" \
    "v_mov_b64 v[206:207], 0\n\t" \
    "s_nop 1\n\t" \
    "s_waitcnt lgkmcnt(4)\n\t" \
    "v_smfmac_f32_16x16x64_bf16 v[204:207], v[240:243], v[216:223], v255\n\t" \
    "s_waitcnt lgkmcnt(2)\n\t" \
    "v_smfmac_f32_16x16x64_bf16 v[204:207], v[244:247], v[224:231], v255\n\t" \
    "v_bfe_u32 v253, v252, 5, 1\n\t" \
    "v_bfe_i32 v202, v252, 4, 1\n\t" \
    "v_lshlrev_b32 v200, 1, v252\n\t" \
    "v_and_b32 v200, 0x180, v200\n\t" \
    "v_permlane32_swap_b32 v232, v234\n\t" \
    "v_permlane32_swap_b32 v233, v235\n\t" \
    "v_permlane32_swap_b32 v236, v238\n\t" \
    "v_permlane32_swap_b32 v237, v239\n\t" \
    "v_lshrrev_b32 v201, 2, v252\n\t" \
    "v_lshrrev_b32 v203, 4, v252\n\t" \
    "v_and_b32 v201, 0x40, v201\n\t" \
    "v_and_or_b32 v201, v203, 32, v201\n\t" \
    "v_and_or_b32 v201, v252, 16, v201\n\t" \
    "v_pk_add_f32 v[232:233], v[232:233], v[234:235]\n\t" \
    "v_pk_add_f32 v[234:235], v[236:237], v[238:239]\n\t" \
    "v_or_b32 v200, v200, v201\n\t" \
    "v_mad_u32_u24 v200, v253, %[cs], v200\n\t" \
    "v_mad_u32_u24 v201, v253, %[cs], v201\n\t" \
    "v_lshrrev_b32 v203, 3, v252\n\t" \
    "v_bfe_u32 v253, v252, 7, 1\n\t" \
    "s_waitcnt lgkmcnt(0)\n\t" \
    "v_smfmac_f32_16x16x64_bf16 v[204:207], v[248:251], v[208:215], v255\n\t" \
    "v_and_or_b32 v201, v203, 8, v201\n\t" \
    "v_and_b32 v202, 0x06060606, v202\n\t" \
    "v_mad_u32_u24 v201, v253, %[cp], v201\n\t" \
    "v_xor_b32 v202, 0x01000504, v202\n\t" \
    "v_max_f32 v236, v232, v234\n\t" \
    "v_max_f32 v237, v233, v235\n\t" \
    "s_nop 1\n\t" \
    "v_permlane32_swap_b32 v204, v206\n\t" \
    "v_permlane32_swap_b32 v205, v207\n\t" \
    "v_add_f32 v204, v204, v206\n\t" \
    "v_add_f32 v205, v205, v207\n\t" \
    "v_max_f32 v236, v236, v204\n\t" \
    "v_max_f32 v237, v237, v205\n\t" \
    "s_nop 0\n\t" \
    "v_max_f32_dpp v236, v236, v236 quad_perm:[1,0,3,2] row_mask:0xf bank_mask:0xf\n\t" \
    "v_max_f32_dpp v237, v237, v237 quad_perm:[1,0,3,2] row_mask:0xf bank_mask:0xf\n\t" \
    "s_nop 0\n\t" \
    "v_max_f32_dpp v236, v236, v236 quad_perm:[2,3,0,1] row_mask:0xf bank_mask:0xf\n\t" \
    "v_max_f32_dpp v237, v237, v237 quad_perm:[2,3,0,1] row_mask:0xf bank_mask:0xf\n\t" \
    "s_nop 0\n\t" \
    "v_max_f32_dpp v236, v236, v236 row_half_mirror row_mask:0xf bank_mask:0xf\n\t" \
    "v_max_f32_dpp v237, v237, v237 row_half_mirror row_mask:0xf bank_mask:0xf\n\t" \
    "s_nop 0\n\t" \
    "v_max_f32_dpp v236, v236, v236 row_mirror row_mask:0xf bank_mask:0xf\n\t" \
    "v_max_f32_dpp v237, v237, v237 row_mirror row_mask:0xf bank_mask:0xf\n\t" \
    "v_mul_f32 v236, %[scl], v236\n\t" \
    "v_mul_f32 v237, %[scl], v237\n\t" \
    "v_fma_f32 v232, v232, %[scl], -v236\n\t" \
    "v_fma_f32 v233, v233, %[scl], -v237\n\t" \
    "v_fma_f32 v234, v234, %[scl], -v236\n\t" \
    "v_fma_f32 v235, v235, %[scl], -v237\n\t" \
    "v_exp_f32 v232, v232\n\t" \
    "v_exp_f32 v233, v233\n\t" \
    "v_fma_f32 v204, v204, %[scl], -v236\n\t" \
    "v_fma_f32 v205, v205, %[scl], -v237\n\t" \
    "v_exp_f32 v234, v234\n\t" \
    "v_exp_f32 v235, v235\n\t" \
    "v_exp_f32 v204, v204\n\t" \
    "v_exp_f32 v205, v205\n\t" \
    "v_add_f32 v238, v232, v234\n\t" \
    "v_add_f32 v239, v233, v235\n\t" \
    "v_fma_f32 v238, v204, 0.5, v238\n\t" \
    "v_fma_f32 v239, v205, 0.5, v239\n\t" \
    "s_nop 0\n\t" \
    "v_add_f32_dpp v238, v238, v238 quad_perm:[1,0,3,2] row_mask:0xf bank_mask:0xf\n\t" \
    "v_add_f32_dpp v239, v239, v239 quad_perm:[1,0,3,2] row_mask:0xf bank_mask:0xf\n\t" \
    "s_nop 0\n\t" \
    "v_add_f32_dpp v238, v238, v238 quad_perm:[2,3,0,1] row_mask:0xf bank_mask:0xf\n\t" \
    "v_add_f32_dpp v239, v239, v239 quad_perm:[2,3,0,1] row_mask:0xf bank_mask:0xf\n\t" \
    "s_nop 0\n\t" \
    "v_add_f32_dpp v238, v238, v238 row_half_mirror row_mask:0xf bank_mask:0xf\n\t" \
    "v_add_f32_dpp v239, v239, v239 row_half_mirror row_mask:0xf bank_mask:0xf\n\t" \
    "s_nop 0\n\t" \
    "v_add_f32_dpp v238, v238, v238 row_mirror row_mask:0xf bank_mask:0xf\n\t" \
    "v_add_f32_dpp v239, v239, v239 row_mirror row_mask:0xf bank_mask:0xf\n\t" \
    "v_rcp_f32 v238, v238\n\t" \
    "v_rcp_f32 v239, v239\n\t" \
    "s_nop 0\n\t" \
    "v_pk_mul_f32 v[232:233], v[232:233], v[238:239]\n\t" \
    "v_pk_mul_f32 v[234:235], v[234:235], v[238:239]\n\t" \
    "v_pk_mul_f32 v[204:205], v[204:205], v[238:239]\n\t" \
    "v_cvt_pk_bf16_f32 v232, |v232|, |v233|\n\t" \
    "v_cvt_pk_bf16_f32 v234, |v234|, |v235|\n\t" \
    "v_cvt_pk_bf16_f32 v204, |v204|, |v205|\n\t" \
    "v_mov_b32_dpp v233, v232 quad_perm:[1,0,3,2] row_mask:0xf bank_mask:0xf\n\t" \
    "v_mov_b32_dpp v235, v234 quad_perm:[1,0,3,2] row_mask:0xf bank_mask:0xf\n\t" \
    "v_mov_b32_dpp v205, v204 quad_perm:[1,0,3,2] row_mask:0xf bank_mask:0xf\n\t" \
    "v_perm_b32 v232, v232, v233, v202\n\t" \
    "v_perm_b32 v234, v234, v235, v202\n\t" \
    "v_perm_b32 v204, v204, v205, v202\n\t" \
    "v_mov_b32_dpp v240, v232 quad_perm:[0,1,0,1] row_mask:0xf bank_mask:0xf\n\t" \
    "v_mov_b32_dpp v241, v234 quad_perm:[0,1,0,1] row_mask:0xf bank_mask:0xf\n\t" \
    "v_mov_b32_dpp v242, v232 quad_perm:[2,3,2,3] row_mask:0xf bank_mask:0xf\n\t" \
    "v_mov_b32_dpp v243, v234 quad_perm:[2,3,2,3] row_mask:0xf bank_mask:0xf\n\t" \
    "buffer_store_dwordx4 v[240:243], v200, %[rr], %[sx] offen sc1\n\t" \
    "v_mov_b32_dpp v244, v204 quad_perm:[0,1,0,1] row_mask:0xf bank_mask:0xf\n\t" \
    "v_mov_b32_dpp v245, v204 quad_perm:[2,3,2,3] row_mask:0xf bank_mask:0xf\n\t" \
    "buffer_store_dwordx4 v[240:243], v200, %[rr], %[sy] offen sc1\n\t" \
    "buffer_store_dwordx2 v[244:245], v201, %[rr], %[sm] offen sc1\n\t" \
    "s_wakeup\n\t" \
    PRISM
#define ASM_P4_0 \
    "ds_read_b128 v[200:203], %[qa] offset:0\n\t" \
    "ds_read_b128 v[204:207], %[qa] offset:1024\n\t" \
    "ds_read_b128 v[208:211], %[qa] offset:2048\n\t" \
    "ds_read_b128 v[212:215], %[qa] offset:3072\n\t" \
    "ds_read_b128 v[216:219], %[qa] offset:4096\n\t" \
    "ds_read_b128 v[220:223], %[qa] offset:5120\n\t" \
    "ds_read_b128 v[224:227], %[qa] offset:12288\n\t" \
    "ds_read_b128 v[228:231], %[qa] offset:13312\n\t" \
    "ds_read_b128 v[232:235], %[qa] offset:14336\n\t" \
    "ds_read_b128 v[236:239], %[qa] offset:15360\n\t" \
    "s_waitcnt lgkmcnt(0)\n\t" \
    "s_barrier\n\t" ASM_ST \
    "v_mfma_f32_16x16x32_bf16 v[240:243], v[224:227], v[200:203], v[240:243]\n\t" \
    "ds_read_b128 v[224:227], %[qa] offset:16384\n\t" \
    "v_mfma_f32_16x16x32_bf16 v[240:243], v[228:231], v[204:207], v[240:243]\n\t" \
    "ds_read_b128 v[228:231], %[qa] offset:17408\n\t" \
    "v_mfma_f32_16x16x32_bf16 v[240:243], v[232:235], v[208:211], v[240:243]\n\t" \
    "ds_read_b128 v[232:235], %[km] offset:0\n\t" \
    "v_mfma_f32_16x16x32_bf16 v[240:243], v[236:239], v[212:215], v[240:243]\n\t" \
    "ds_read_b128 v[236:239], %[km] offset:512\n\t" \
    "s_waitcnt lgkmcnt(3)\n\t" \
    "v_mfma_f32_16x16x32_bf16 v[240:243], v[224:227], v[216:219], v[240:243]\n\t" \
    "ds_read_b128 v[224:227], %[km] offset:1024\n\t" \
    "s_waitcnt lgkmcnt(3)\n\t" \
    "v_mfma_f32_16x16x32_bf16 v[240:243], v[228:231], v[220:223], v[240:243]\n\t" \
    "ds_read_b128 v[228:231], %[km] offset:1536\n\t" \
    "s_waitcnt lgkmcnt(3)\n\t" \
    "v_mfma_f32_16x16x32_bf16 v[248:251], v[232:235], v[200:203], 0\n\t" \
    "ds_read_b128 v[232:235], %[km] offset:2048\n\t" \
    "s_waitcnt lgkmcnt(3)\n\t" \
    "v_mfma_f32_16x16x32_bf16 v[248:251], v[236:239], v[204:207], v[248:251]\n\t" \
    "ds_read_b128 v[236:239], %[km] offset:2560\n\t" \
    "s_waitcnt lgkmcnt(3)\n\t" \
    "v_mfma_f32_16x16x32_bf16 v[248:251], v[224:227], v[208:211], v[248:251]\n\t" \
    "s_waitcnt lgkmcnt(2)\n\t" \
    "v_mfma_f32_16x16x32_bf16 v[248:251], v[228:231], v[212:215], v[248:251]\n\t" \
    "s_waitcnt lgkmcnt(1)\n\t" \
    "v_mfma_f32_16x16x32_bf16 v[248:251], v[232:235], v[216:219], v[248:251]\n\t" \
    "s_waitcnt lgkmcnt(0)\n\t" \
    "v_mfma_f32_16x16x32_bf16 v[248:251], v[236:239], v[220:223], v[248:251]\n\t" \
    "v_max3_f32 v224, v240, v241, v242\n\t" \
    "v_mov_b32 v225, v243\n\t" \
    "v_lshlrev_b32 v201, 23, %[qa]\n\t" \
    "v_and_b32 v201, 0x80000000, v201\n\t" \
    "v_and_b32 v200, 0x3ff, %[qa]\n\t" \
    "v_or_b32 v200, v201, v200\n\t" \
    "s_nop 1\n\t" \
    "v_max3_f32 v224, v224, v248, v249\n\t" \
    "v_max3_f32 v225, v225, v250, v251\n\t" \
    "v_max_f32 v224, v224, v225\n\t" \
    "v_mov_b32 v225, v224\n\t" \
    "s_nop 1\n\t" \
    "v_permlane16_swap_b32 v224, v225\n\t" \
    "v_max_f32 v224, v224, v225\n\t" \
    "v_mov_b32 v225, v224\n\t" \
    "s_nop 1\n\t" \
    "v_permlane32_swap_b32 v224, v225\n\t" \
    "v_max_f32 v224, v224, v225\n\t" \
    "v_mul_f32 v224, %[scl], v224\n\t" \
    "v_fma_f32 v240, v240, %[scl], -v224\n\t" \
    "v_fma_f32 v241, v241, %[scl], -v224\n\t" \
    "v_exp_f32 v240, v240\n\t" \
    "v_fma_f32 v242, v242, %[scl], -v224\n\t" \
    "v_exp_f32 v241, v241\n\t" \
    "v_fma_f32 v243, v243, %[scl], -v224\n\t" \
    "v_exp_f32 v242, v242\n\t" \
    "v_fma_f32 v248, v248, %[scl], -v224\n\t" \
    "v_exp_f32 v243, v243\n\t" \
    "v_fma_f32 v249, v249, %[scl], -v224\n\t" \
    "v_exp_f32 v248, v248\n\t" \
    "v_fma_f32 v250, v250, %[scl], -v224\n\t" \
    "v_exp_f32 v249, v249\n\t" \
    "v_fma_f32 v251, v251, %[scl], -v224\n\t" \
    "v_exp_f32 v250, v250\n\t" \
    "v_exp_f32 v251, v251\n\t" \
    "v_add_f32 v224, v240, v241\n\t" \
    "v_add_f32 v225, v242, v243\n\t" \
    "v_add_f32 v232, v248, v249\n\t" \
    "v_add_f32 v233, v250, v251\n\t" \
    "v_add_f32 v224, v224, v225\n\t" \
    "v_add_f32 v232, v232, v233\n\t" \
    "v_fma_f32 v224, v232, 0.5, v224\n\t" \
    "v_mov_b32 v225, v224\n\t" \
    "s_nop 1\n\t" \
    "v_permlane16_swap_b32 v224, v225\n\t" \
    "v_add_f32 v224, v224, v225\n\t" \
    "v_mov_b32 v225, v224\n\t" \
    "s_nop 1\n\t" \
    "v_permlane32_swap_b32 v224, v225\n\t" \
    "v_add_f32 v224, v224, v225\n\t" \
    "v_rcp_f32 v224, v224\n\t" \
    "s_nop 0\n\t" \
    "v_mul_f32 v240, v240, v224\n\t" \
    "v_mul_f32 v241, v241, v224\n\t" \
    "v_mul_f32 v242, v242, v224\n\t" \
    "v_mul_f32 v243, v243, v224\n\t" \
    "v_mul_f32 v248, v248, v224\n\t" \
    "v_mul_f32 v249, v249, v224\n\t" \
    "v_mul_f32 v250, v250, v224\n\t" \
    "v_mul_f32 v251, v251, v224\n\t" \
    "v_cvt_pk_bf16_f32 v228, |v240|, |v241|\n\t" \
    "v_cvt_pk_bf16_f32 v229, |v242|, |v243|\n\t" \
    "v_cvt_pk_bf16_f32 v232, |v248|, |v249|\n\t" \
    "v_cvt_pk_bf16_f32 v233, |v250|, |v251|\n\t" \
    "v_permlane16_swap_b32 v228, v230\n\t" \
    "v_permlane16_swap_b32 v229, v231\n\t" \
    "buffer_store_dwordx4 v[228:231], v200, %[rr], %[so] offen sc1\n\t" \
    "buffer_store_dwordx2 v[232:233], %[pm], %[rr], %[so] offen sc1\n\t" \
    "s_nop 1\n\t"
#define ASM_P4_1 \
    "ds_read_b128 v[200:203], %[qa] offset:0\n\t" \
    "ds_read_b128 v[204:207], %[qa] offset:1024\n\t" \
    "ds_read_b128 v[208:211], %[qa] offset:2048\n\t" \
    "ds_read_b128 v[212:215], %[qa] offset:3072\n\t" \
    "ds_read_b128 v[216:219], %[qa] offset:4096\n\t" \
    "ds_read_b128 v[220:223], %[qa] offset:5120\n\t" \
    "ds_read_b128 v[224:227], %[qa] offset:12288\n\t" \
    "ds_read_b128 v[228:231], %[qa] offset:13312\n\t" \
    "ds_read_b128 v[232:235], %[qa] offset:14336\n\t" \
    "ds_read_b128 v[236:239], %[qa] offset:15360\n\t" \
    "s_waitcnt lgkmcnt(0)\n\t" \
    "s_barrier\n\t" ASM_ST \
    "v_mfma_f32_16x16x32_bf16 v[240:243], v[224:227], v[200:203], v[240:243]\n\t" \
    "ds_read_b128 v[224:227], %[qa] offset:16384\n\t" \
    "v_mfma_f32_16x16x32_bf16 v[240:243], v[228:231], v[204:207], v[240:243]\n\t" \
    "ds_read_b128 v[228:231], %[qa] offset:17408\n\t" \
    "v_mfma_f32_16x16x32_bf16 v[240:243], v[232:235], v[208:211], v[240:243]\n\t" \
    "ds_read_b128 v[232:235], %[qa] offset:6144\n\t" \
    "v_mfma_f32_16x16x32_bf16 v[240:243], v[236:239], v[212:215], v[240:243]\n\t" \
    "ds_read_b128 v[236:239], %[qa] offset:7168\n\t" \
    "s_waitcnt lgkmcnt(3)\n\t" \
    "v_mfma_f32_16x16x32_bf16 v[240:243], v[224:227], v[216:219], v[240:243]\n\t" \
    "ds_read_b128 v[224:227], %[qa] offset:8192\n\t" \
    "s_waitcnt lgkmcnt(3)\n\t" \
    "v_mfma_f32_16x16x32_bf16 v[240:243], v[228:231], v[220:223], v[240:243]\n\t" \
    "ds_read_b128 v[228:231], %[qa] offset:9216\n\t" \
    "s_waitcnt lgkmcnt(3)\n\t" \
    "v_mfma_f32_16x16x32_bf16 v[244:247], v[232:235], v[200:203], 0\n\t" \
    "ds_read_b128 v[232:235], %[qa] offset:10240\n\t" \
    "s_waitcnt lgkmcnt(3)\n\t" \
    "v_mfma_f32_16x16x32_bf16 v[244:247], v[236:239], v[204:207], v[244:247]\n\t" \
    "ds_read_b128 v[236:239], %[qa] offset:11264\n\t" \
    "s_waitcnt lgkmcnt(3)\n\t" \
    "v_mfma_f32_16x16x32_bf16 v[244:247], v[224:227], v[208:211], v[244:247]\n\t" \
    "ds_read_b128 v[224:227], %[km] offset:0\n\t" \
    "s_waitcnt lgkmcnt(3)\n\t" \
    "v_mfma_f32_16x16x32_bf16 v[244:247], v[228:231], v[212:215], v[244:247]\n\t" \
    "ds_read_b128 v[228:231], %[km] offset:512\n\t" \
    "s_waitcnt lgkmcnt(3)\n\t" \
    "v_mfma_f32_16x16x32_bf16 v[244:247], v[232:235], v[216:219], v[244:247]\n\t" \
    "ds_read_b128 v[232:235], %[km] offset:1024\n\t" \
    "s_waitcnt lgkmcnt(3)\n\t" \
    "v_mfma_f32_16x16x32_bf16 v[244:247], v[236:239], v[220:223], v[244:247]\n\t" \
    "ds_read_b128 v[236:239], %[km] offset:1536\n\t" \
    "s_waitcnt lgkmcnt(3)\n\t" \
    "v_mfma_f32_16x16x32_bf16 v[248:251], v[224:227], v[200:203], 0\n\t" \
    "ds_read_b128 v[224:227], %[km] offset:2048\n\t" \
    "s_waitcnt lgkmcnt(3)\n\t" \
    "v_mfma_f32_16x16x32_bf16 v[248:251], v[228:231], v[204:207], v[248:251]\n\t" \
    "ds_read_b128 v[228:231], %[km] offset:2560\n\t" \
    "s_waitcnt lgkmcnt(3)\n\t" \
    "v_mfma_f32_16x16x32_bf16 v[248:251], v[232:235], v[208:211], v[248:251]\n\t" \
    "s_waitcnt lgkmcnt(2)\n\t" \
    "v_mfma_f32_16x16x32_bf16 v[248:251], v[236:239], v[212:215], v[248:251]\n\t" \
    "s_waitcnt lgkmcnt(1)\n\t" \
    "v_mfma_f32_16x16x32_bf16 v[248:251], v[224:227], v[216:219], v[248:251]\n\t" \
    "s_waitcnt lgkmcnt(0)\n\t" \
    "v_mfma_f32_16x16x32_bf16 v[248:251], v[228:231], v[220:223], v[248:251]\n\t" \
    "v_max3_f32 v224, v240, v241, v242\n\t" \
    "v_max3_f32 v225, v244, v245, v246\n\t" \
    "v_max3_f32 v224, v224, v243, v247\n\t" \
    "v_and_b32 v200, 0x3ff, %[qa]\n\t" \
    "s_nop 3\n\t" \
    "v_max3_f32 v224, v224, v248, v249\n\t" \
    "v_max3_f32 v225, v225, v250, v251\n\t" \
    "v_max_f32 v224, v224, v225\n\t" \
    "v_mov_b32 v225, v224\n\t" \
    "s_nop 1\n\t" \
    "v_permlane16_swap_b32 v224, v225\n\t" \
    "v_max_f32 v224, v224, v225\n\t" \
    "v_mov_b32 v225, v224\n\t" \
    "s_nop 1\n\t" \
    "v_permlane32_swap_b32 v224, v225\n\t" \
    "v_max_f32 v224, v224, v225\n\t" \
    "v_mul_f32 v224, %[scl], v224\n\t" \
    "v_fma_f32 v240, v240, %[scl], -v224\n\t" \
    "v_fma_f32 v241, v241, %[scl], -v224\n\t" \
    "v_exp_f32 v240, v240\n\t" \
    "v_fma_f32 v242, v242, %[scl], -v224\n\t" \
    "v_exp_f32 v241, v241\n\t" \
    "v_fma_f32 v243, v243, %[scl], -v224\n\t" \
    "v_exp_f32 v242, v242\n\t" \
    "v_fma_f32 v244, v244, %[scl], -v224\n\t" \
    "v_exp_f32 v243, v243\n\t" \
    "v_fma_f32 v245, v245, %[scl], -v224\n\t" \
    "v_exp_f32 v244, v244\n\t" \
    "v_fma_f32 v246, v246, %[scl], -v224\n\t" \
    "v_exp_f32 v245, v245\n\t" \
    "v_fma_f32 v247, v247, %[scl], -v224\n\t" \
    "v_exp_f32 v246, v246\n\t" \
    "v_fma_f32 v248, v248, %[scl], -v224\n\t" \
    "v_exp_f32 v247, v247\n\t" \
    "v_fma_f32 v249, v249, %[scl], -v224\n\t" \
    "v_exp_f32 v248, v248\n\t" \
    "v_fma_f32 v250, v250, %[scl], -v224\n\t" \
    "v_exp_f32 v249, v249\n\t" \
    "v_fma_f32 v251, v251, %[scl], -v224\n\t" \
    "v_exp_f32 v250, v250\n\t" \
    "v_exp_f32 v251, v251\n\t" \
    "v_add_f32 v224, v240, v241\n\t" \
    "v_add_f32 v225, v242, v243\n\t" \
    "v_add_f32 v226, v244, v245\n\t" \
    "v_add_f32 v227, v246, v247\n\t" \
    "v_add_f32 v232, v248, v249\n\t" \
    "v_add_f32 v233, v250, v251\n\t" \
    "v_add_f32 v224, v224, v225\n\t" \
    "v_add_f32 v226, v226, v227\n\t" \
    "v_add_f32 v232, v232, v233\n\t" \
    "v_add_f32 v224, v224, v226\n\t" \
    "v_fma_f32 v224, v232, 0.5, v224\n\t" \
    "v_mov_b32 v225, v224\n\t" \
    "s_nop 1\n\t" \
    "v_permlane16_swap_b32 v224, v225\n\t" \
    "v_add_f32 v224, v224, v225\n\t" \
    "v_mov_b32 v225, v224\n\t" \
    "s_nop 1\n\t" \
    "v_permlane32_swap_b32 v224, v225\n\t" \
    "v_add_f32 v224, v224, v225\n\t" \
    "v_rcp_f32 v224, v224\n\t" \
    "s_nop 0\n\t" \
    "v_mul_f32 v240, v240, v224\n\t" \
    "v_mul_f32 v241, v241, v224\n\t" \
    "v_mul_f32 v242, v242, v224\n\t" \
    "v_mul_f32 v243, v243, v224\n\t" \
    "v_mul_f32 v244, v244, v224\n\t" \
    "v_mul_f32 v245, v245, v224\n\t" \
    "v_mul_f32 v246, v246, v224\n\t" \
    "v_mul_f32 v247, v247, v224\n\t" \
    "v_mul_f32 v248, v248, v224\n\t" \
    "v_mul_f32 v249, v249, v224\n\t" \
    "v_mul_f32 v250, v250, v224\n\t" \
    "v_mul_f32 v251, v251, v224\n\t" \
    "v_cvt_pk_bf16_f32 v230, |v240|, |v241|\n\t" \
    "v_cvt_pk_bf16_f32 v231, |v242|, |v243|\n\t" \
    "v_cvt_pk_bf16_f32 v228, |v244|, |v245|\n\t" \
    "v_cvt_pk_bf16_f32 v229, |v246|, |v247|\n\t" \
    "v_cvt_pk_bf16_f32 v232, |v248|, |v249|\n\t" \
    "v_cvt_pk_bf16_f32 v233, |v250|, |v251|\n\t" \
    "buffer_store_dwordx4 v[228:231], v200, %[rr], %[so] offen sc1\n\t" \
    "buffer_store_dwordx2 v[232:233], %[pm], %[rr], %[so] offen sc1\n\t" \
    "s_nop 1\n\t"
#define ASM_RCRIT \
    "v_mov_b64 v[176:177], -1\n\t" \
    "v_mov_b64 v[178:179], -1\n\t" \
    "v_mov_b64 v[180:181], -1\n\t" \
    "v_mov_b64 v[182:183], -1\n\t" \
    "v_mov_b64 v[184:185], -1\n\t" \
    "v_mov_b64 v[186:187], -1\n\t" \
    "v_mov_b64 v[188:189], -1\n\t" \
    "v_mov_b64 v[190:191], -1\n\t" \
    "v_mov_b64 v[192:193], -1\n\t" \
    "v_mov_b64 v[194:195], -1\n\t" \
    ".Lfa%=:\n\t" \
    "s_mov_b32 %[a], 0\n\t" \
    "v_max_u32 v196, v176, v177\n\t" \
    "v_max3_u32 v196, v178, v179, v196\n\t" \
    "v_cmp_eq_u32 vcc, -1, v196\n\t" \
    "s_cbranch_vccz .Lga0_%=\n\t" \
    "buffer_load_dwordx4 v[176:179], %[ov], %[rs], %[sv] offen sc1\n\t" \
    "s_mov_b32 %[a], 1\n\t" \
    ".Lga0_%=:\n\t" \
    "v_max_u32 v196, v180, v181\n\t" \
    "v_max3_u32 v196, v182, v183, v196\n\t" \
    "v_cmp_eq_u32 vcc, -1, v196\n\t" \
    "s_cbranch_vccz .Lga1_%=\n\t" \
    "buffer_load_dwordx4 v[180:183], %[ov], %[rs], %[sv] offen offset:1024 sc1\n\t" \
    "s_mov_b32 %[a], 1\n\t" \
    ".Lga1_%=:\n\t" \
    "s_cmp_eq_u32 %[a], 0\n\t" \
    "s_cbranch_scc1 .Lha%=\n\t" \
    "s_waitcnt vmcnt(12)\n\t" \
    ".Lia%=:\n\t" \
    "v_max_u32 v196, v176, v177\n\t" \
    "v_max3_u32 v196, v178, v179, v196\n\t" \
    "v_max3_u32 v196, v180, v181, v196\n\t" \
    "v_max3_u32 v196, v182, v183, v196\n\t" \
    "v_cmp_eq_u32 vcc, -1, v196\n\t" \
    "s_cbranch_vccz .Lha%=\n\t" \
    "s_nop 7\n\t" \
    "v_max_u32 v196, v176, v177\n\t" \
    "v_max3_u32 v196, v178, v179, v196\n\t" \
    "v_max3_u32 v196, v180, v181, v196\n\t" \
    "v_max3_u32 v196, v182, v183, v196\n\t" \
    "v_cmp_eq_u32 vcc, -1, v196\n\t" \
    "s_cbranch_vccz .Lha%=\n\t" \
    "s_nop 7\n\t" \
    "v_max_u32 v196, v176, v177\n\t" \
    "v_max3_u32 v196, v178, v179, v196\n\t" \
    "v_max3_u32 v196, v180, v181, v196\n\t" \
    "v_max3_u32 v196, v182, v183, v196\n\t" \
    "v_cmp_eq_u32 vcc, -1, v196\n\t" \
    "s_cbranch_vccz .Lha%=\n\t" \
    "s_nop 7\n\t" \
    "v_max_u32 v196, v176, v177\n\t" \
    "v_max3_u32 v196, v178, v179, v196\n\t" \
    "v_max3_u32 v196, v180, v181, v196\n\t" \
    "v_max3_u32 v196, v182, v183, v196\n\t" \
    "v_cmp_eq_u32 vcc, -1, v196\n\t" \
    "s_cbranch_vccz .Lha%=\n\t" \
    "s_nop 7\n\t" \
    "s_sub_u32 %[n], %[n], 1\n\t" \
    "s_cmp_gt_i32 %[n], 0\n\t" \
    "s_cbranch_scc1 .Lfa%=\n\t" \
    ".Lha%=:\n\t" \
    ASM_ST3 \
    "buffer_load_dwordx4 v[184:187], %[o0], %[rs], %[sp] offen sc1\n\t" \
    "buffer_load_dwordx4 v[188:191], %[o0], %[rs], %[sp] offen offset:1024 sc1\n\t" \
    "buffer_load_dwordx4 v[192:195], %[o2], %[rs], %[sp] offen sc1\n\t" \
    "s_waitcnt vmcnt(0)\n\t" \
    "v_max_u32 v196, v184, v185\n\t" \
    "v_max3_u32 v196, v186, v187, v196\n\t" \
    "v_max3_u32 v196, v188, v189, v196\n\t" \
    "v_max3_u32 v196, v190, v191, v196\n\t" \
    "v_max3_u32 v196, v192, v193, v196\n\t" \
    "v_max3_u32 v196, v194, v195, v196\n\t" \
    "v_cmp_eq_u32 vcc, -1, v196\n\t" \
    "s_cbranch_vccz .Lhb%=\n\t" \
    ".Lfb%=:\n\t" \
    "s_mov_b32 %[a], 0\n\t" \
    "v_max_u32 v196, v184, v185\n\t" \
    "v_max3_u32 v196, v186, v187, v196\n\t" \
    "v_cmp_eq_u32 vcc, -1, v196\n\t" \
    "s_cbranch_vccz .Lgb0_%=\n\t" \
    "buffer_load_dwordx4 v[184:187], %[o0], %[rs], %[sp] offen sc1\n\t" \
    "s_mov_b32 %[a], 1\n\t" \
    ".Lgb0_%=:\n\t" \
    "v_max_u32 v196, v188, v189\n\t" \
    "v_max3_u32 v196, v190, v191, v196\n\t" \
    "v_cmp_eq_u32 vcc, -1, v196\n\t" \
    "s_cbranch_vccz .Lgb1_%=\n\t" \
    "buffer_load_dwordx4 v[188:191], %[o0], %[rs], %[sp] offen offset:1024 sc1\n\t" \
    "s_mov_b32 %[a], 1\n\t" \
    ".Lgb1_%=:\n\t" \
    "v_max_u32 v196, v192, v193\n\t" \
    "v_max3_u32 v196, v194, v195, v196\n\t" \
    "v_cmp_eq_u32 vcc, -1, v196\n\t" \
    "s_cbranch_vccz .Lgb2_%=\n\t" \
    "buffer_load_dwordx4 v[192:195], %[o2], %[rs], %[sp] offen sc1\n\t" \
    "s_mov_b32 %[a], 1\n\t" \
    ".Lgb2_%=:\n\t" \
    "s_cmp_eq_u32 %[a], 0\n\t" \
    "s_cbranch_scc1 .Lhb%=\n\t" \
    "s_waitcnt vmcnt(12)\n\t" \
    ".Lib%=:\n\t" \
    "v_max_u32 v196, v184, v185\n\t" \
    "v_max3_u32 v196, v186, v187, v196\n\t" \
    "v_max3_u32 v196, v188, v189, v196\n\t" \
    "v_max3_u32 v196, v190, v191, v196\n\t" \
    "v_max3_u32 v196, v192, v193, v196\n\t" \
    "v_max3_u32 v196, v194, v195, v196\n\t" \
    "v_cmp_eq_u32 vcc, -1, v196\n\t" \
    "s_cbranch_vccz .Lhb%=\n\t" \
    "s_nop 7\n\t" \
    "v_max_u32 v196, v184, v185\n\t" \
    "v_max3_u32 v196, v186, v187, v196\n\t" \
    "v_max3_u32 v196, v188, v189, v196\n\t" \
    "v_max3_u32 v196, v190, v191, v196\n\t" \
    "v_max3_u32 v196, v192, v193, v196\n\t" \
    "v_max3_u32 v196, v194, v195, v196\n\t" \
    "v_cmp_eq_u32 vcc, -1, v196\n\t" \
    "s_cbranch_vccz .Lhb%=\n\t" \
    "s_nop 7\n\t" \
    "v_max_u32 v196, v184, v185\n\t" \
    "v_max3_u32 v196, v186, v187, v196\n\t" \
    "v_max3_u32 v196, v188, v189, v196\n\t" \
    "v_max3_u32 v196, v190, v191, v196\n\t" \
    "v_max3_u32 v196, v192, v193, v196\n\t" \
    "v_max3_u32 v196, v194, v195, v196\n\t" \
    "v_cmp_eq_u32 vcc, -1, v196\n\t" \
    "s_cbranch_vccz .Lhb%=\n\t" \
    "s_nop 7\n\t" \
    "v_max_u32 v196, v184, v185\n\t" \
    "v_max3_u32 v196, v186, v187, v196\n\t" \
    "v_max3_u32 v196, v188, v189, v196\n\t" \
    "v_max3_u32 v196, v190, v191, v196\n\t" \
    "v_max3_u32 v196, v192, v193, v196\n\t" \
    "v_max3_u32 v196, v194, v195, v196\n\t" \
    "v_cmp_eq_u32 vcc, -1, v196\n\t" \
    "s_cbranch_vccz .Lhb%=\n\t" \
    "s_nop 7\n\t" \
    "s_sub_u32 %[n], %[n], 1\n\t" \
    "s_cmp_gt_i32 %[n], 0\n\t" \
    "s_cbranch_scc1 .Lfb%=\n\t" \
    ".Lhb%=:\n\t" \
    ASM_ST2 \
    "v_mfma_f32_16x16x32_bf16 v[160:163], v[192:195], v[176:179], 0\n\t" \
    "v_mfma_f32_16x16x32_bf16 v[164:167], v[192:195], v[180:183], 0\n\t" \
    "v_smfmac_f32_16x16x64_bf16 v[160:163], v[184:187], v[128:135], %[sidx]\n\t" \
    "v_smfmac_f32_16x16x64_bf16 v[164:167], v[184:187], v[144:151], %[sidx]\n\t" \
    "v_smfmac_f32_16x16x64_bf16 v[160:163], v[188:191], v[136:143], %[sidx]\n\t" \
    "v_smfmac_f32_16x16x64_bf16 v[164:167], v[188:191], v[152:159], %[sidx]\n\t" \
    "s_cmp_eq_u32 %[pub], 0\n\t" \
    "s_nop 7\n\t" \
    "v_permlane32_swap_b32 v160, v164\n\t" \
    "v_permlane32_swap_b32 v161, v165\n\t" \
    "v_permlane32_swap_b32 v162, v166\n\t" \
    "v_permlane32_swap_b32 v163, v167\n\t" \
    "v_add_f32 v160, v160, v164\n\t" \
    "v_add_f32 v161, v161, v165\n\t" \
    "ds_write2_b32 %[rw], v160, v161 offset1:80\n\t" \
    "v_add_f32 v162, v162, v166\n\t" \
    "v_add_f32 v163, v163, v167\n\t" \
    "ds_write2_b32 %[rw], v162, v163 offset0:160 offset1:240\n\t" \
    "s_waitcnt lgkmcnt(0)\n\t" \
    "s_barrier\n\t" ASM_ST \
    "s_cbranch_scc1 .Lre%=\n\t" \
    "ds_read_b128 v[200:203], %[rd] offset:0\n\t" \
    "ds_read_b128 v[204:207], %[rd] offset:16\n\t" \
    "s_waitcnt lgkmcnt(1)\n\t" \
    "v_pk_add_f32 v[200:201], v[200:201], v[202:203]\n\t" \
    "s_waitcnt lgkmcnt(0)\n\t" \
    "v_pk_add_f32 v[204:205], v[204:205], v[206:207]\n\t" \
    "v_pk_add_f32 v[200:201], v[200:201], v[204:205]\n\t" \
    "s_nop 1\n\t" \
    "v_permlane32_swap_b32 v200, v201\n\t" \
    "v_add_f32 v202, v200, v201\n\t" \
    "v_add_f32 v203, v200, v201\n\t" \
    "s_nop 1\n\t" \
    "v_permlane32_swap_b32 v202, v203\n\t" \
    "v_cvt_pk_bf16_f32 v200, v202, v203\n\t" \
    "v_min_u32 v200, -2, v200\n\t" \
    "buffer_store_dword v200, %[oo], %[rs], %[so] offen sc1\n\t" \
    ".Lre%=:\n\t"
#define ASM_RX \
    "s_cmp_eq_u32 %[fin], 0\n\t" \
    "s_cbranch_scc1 .Lx0%=\n\t" \
    "buffer_load_dwordx2 v[250:251], %[xo], %[rx], %[sx] offen sc1\n\t" \
    ".Lx0%=:\n\t" \
    "buffer_load_dword v248, %[fo], %[rs], %[fs] offen sc1\n\t" \
    "buffer_load_dwordx4 v[200:203], %[l16], %[rs], %[s0] offen offset:0 sc1\n\t" \
    "buffer_load_dwordx4 v[204:207], %[l16], %[rs], %[s0] offen offset:1024 sc1\n\t" \
    "buffer_load_dwordx4 v[208:211], %[l16], %[rs], %[s0] offen offset:2048 sc1\n\t" \
    "buffer_load_dwordx4 v[212:215], %[l16], %[rs], %[s0] offen offset:3072 sc1\n\t" \
    "buffer_load_dwordx4 v[216:219], %[l16], %[rs], %[s1] offen offset:0 sc1\n\t" \
    "buffer_load_dwordx4 v[220:223], %[l16], %[rs], %[s1] offen offset:1024 sc1\n\t" \
    "buffer_load_dwordx4 v[224:227], %[l16], %[rs], %[s1] offen offset:2048 sc1\n\t" \
    "buffer_load_dwordx4 v[228:231], %[l16], %[rs], %[s1] offen offset:3072 sc1\n\t" \
    "v_mov_b64 v[96:97], v[128:129]\n\t" \
    "v_mov_b64 v[98:99], v[132:133]\n\t" \
    "v_mov_b64 v[100:101], v[136:137]\n\t" \
    "v_mov_b64 v[102:103], v[140:141]\n\t" \
    "v_mov_b64 v[104:105], v[144:145]\n\t" \
    "v_mov_b64 v[106:107], v[148:149]\n\t" \
    "v_mov_b64 v[108:109], v[152:153]\n\t" \
    "v_mov_b64 v[110:111], v[156:157]\n\t" \
    "s_movk_i32 %[n], 0x200\n\t" \
    "s_nop 1\n\t" \
    "v_permlane16_swap_b32 v96, v98\n\t" \
    "v_permlane16_swap_b32 v97, v99\n\t" \
    "v_permlane16_swap_b32 v100, v102\n\t" \
    "v_permlane16_swap_b32 v101, v103\n\t" \
    "v_permlane16_swap_b32 v104, v106\n\t" \
    "v_permlane16_swap_b32 v105, v107\n\t" \
    "v_permlane16_swap_b32 v108, v110\n\t" \
    "v_permlane16_swap_b32 v109, v111\n\t" \
    "s_waitcnt vmcnt(7)\n\t" \
    "v_max_u32 v196, v200, v201\n\t" \
    "v_max3_u32 v196, v202, v203, v196\n\t" \
    "s_waitcnt vmcnt(6)\n\t" \
    "v_max3_u32 v196, v204, v205, v196\n\t" \
    "v_max3_u32 v196, v206, v207, v196\n\t" \
    "s_waitcnt vmcnt(5)\n\t" \
    "v_max3_u32 v196, v208, v209, v196\n\t" \
    "v_max3_u32 v196, v210, v211, v196\n\t" \
    "s_waitcnt vmcnt(4)\n\t" \
    "v_max3_u32 v196, v212, v213, v196\n\t" \
    "v_max3_u32 v196, v214, v215, v196\n\t" \
    "s_waitcnt vmcnt(3)\n\t" \
    "v_max3_u32 v196, v216, v217, v196\n\t" \
    "v_max3_u32 v196, v218, v219, v196\n\t" \
    "s_waitcnt vmcnt(2)\n\t" \
    "v_max3_u32 v196, v220, v221, v196\n\t" \
    "v_max3_u32 v196, v222, v223, v196\n\t" \
    "s_waitcnt vmcnt(1)\n\t" \
    "v_max3_u32 v196, v224, v225, v196\n\t" \
    "v_max3_u32 v196, v226, v227, v196\n\t" \
    "s_waitcnt vmcnt(0)\n\t" \
    "v_max3_u32 v196, v228, v229, v196\n\t" \
    "v_max3_u32 v196, v230, v231, v196\n\t" \
    "v_cmp_eq_u32 vcc, -1, v196\n\t" \
    "s_cbranch_vccz .Lxd%=\n\t" \
    ".Lxl%=:\n\t" \
    "s_mov_b32 %[a], 0\n\t" \
    "v_max_u32 v196, v200, v201\n\t" \
    "v_max3_u32 v196, v202, v203, v196\n\t" \
    "v_cmp_eq_u32 vcc, -1, v196\n\t" \
    "s_cbranch_vccz .Lxs0_%=\n\t" \
    "buffer_load_dwordx4 v[200:203], %[l16], %[rs], %[s0] offen offset:0 sc1\n\t" \
    "s_mov_b32 %[a], 1\n\t" \
    ".Lxs0_%=:\n\t" \
    "v_max_u32 v196, v204, v205\n\t" \
    "v_max3_u32 v196, v206, v207, v196\n\t" \
    "v_cmp_eq_u32 vcc, -1, v196\n\t" \
    "s_cbranch_vccz .Lxs1_%=\n\t" \
    "buffer_load_dwordx4 v[204:207], %[l16], %[rs], %[s0] offen offset:1024 sc1\n\t" \
    "s_mov_b32 %[a], 1\n\t" \
    ".Lxs1_%=:\n\t" \
    "v_max_u32 v196, v208, v209\n\t" \
    "v_max3_u32 v196, v210, v211, v196\n\t" \
    "v_cmp_eq_u32 vcc, -1, v196\n\t" \
    "s_cbranch_vccz .Lxs2_%=\n\t" \
    "buffer_load_dwordx4 v[208:211], %[l16], %[rs], %[s0] offen offset:2048 sc1\n\t" \
    "s_mov_b32 %[a], 1\n\t" \
    ".Lxs2_%=:\n\t" \
    "v_max_u32 v196, v212, v213\n\t" \
    "v_max3_u32 v196, v214, v215, v196\n\t" \
    "v_cmp_eq_u32 vcc, -1, v196\n\t" \
    "s_cbranch_vccz .Lxs3_%=\n\t" \
    "buffer_load_dwordx4 v[212:215], %[l16], %[rs], %[s0] offen offset:3072 sc1\n\t" \
    "s_mov_b32 %[a], 1\n\t" \
    ".Lxs3_%=:\n\t" \
    "v_max_u32 v196, v216, v217\n\t" \
    "v_max3_u32 v196, v218, v219, v196\n\t" \
    "v_cmp_eq_u32 vcc, -1, v196\n\t" \
    "s_cbranch_vccz .Lxs4_%=\n\t" \
    "buffer_load_dwordx4 v[216:219], %[l16], %[rs], %[s1] offen offset:0 sc1\n\t" \
    "s_mov_b32 %[a], 1\n\t" \
    ".Lxs4_%=:\n\t" \
    "v_max_u32 v196, v220, v221\n\t" \
    "v_max3_u32 v196, v222, v223, v196\n\t" \
    "v_cmp_eq_u32 vcc, -1, v196\n\t" \
    "s_cbranch_vccz .Lxs5_%=\n\t" \
    "buffer_load_dwordx4 v[220:223], %[l16], %[rs], %[s1] offen offset:1024 sc1\n\t" \
    "s_mov_b32 %[a], 1\n\t" \
    ".Lxs5_%=:\n\t" \
    "v_max_u32 v196, v224, v225\n\t" \
    "v_max3_u32 v196, v226, v227, v196\n\t" \
    "v_cmp_eq_u32 vcc, -1, v196\n\t" \
    "s_cbranch_vccz .Lxs6_%=\n\t" \
    "buffer_load_dwordx4 v[224:227], %[l16], %[rs], %[s1] offen offset:2048 sc1\n\t" \
    "s_mov_b32 %[a], 1\n\t" \
    ".Lxs6_%=:\n\t" \
    "v_max_u32 v196, v228, v229\n\t" \
    "v_max3_u32 v196, v230, v231, v196\n\t" \
    "v_cmp_eq_u32 vcc, -1, v196\n\t" \
    "s_cbranch_vccz .Lxs7_%=\n\t" \
    "buffer_load_dwordx4 v[228:231], %[l16], %[rs], %[s1] offen offset:3072 sc1\n\t" \
    "s_mov_b32 %[a], 1\n\t" \
    ".Lxs7_%=:\n\t" \
    "s_cmp_eq_u32 %[a], 0\n\t" \
    "s_cbranch_scc1 .Lxd%=\n\t" \
    "s_waitcnt vmcnt(0)\n\t" \
    "s_sub_u32 %[n], %[n], 1\n\t" \
    "s_cmp_lg_u32 %[n], 0\n\t" \
    "s_cbranch_scc1 .Lxl%=\n\t" \
    "s_branch .Lxe%=\n\t" \
    ".Lxd%=:\n\t" \
    ASM_ST2 \
    "v_mfma_f32_16x16x32_bf16 v[120:123], v[128:131], v[200:203], 0\n\t" \
    "v_mfma_f32_16x16x32_bf16 v[124:127], v[144:147], v[200:203], 0\n\t" \
    "v_mfma_f32_16x16x32_bf16 v[120:123], v[132:135], v[204:207], v[120:123]\n\t" \
    "v_mfma_f32_16x16x32_bf16 v[124:127], v[148:151], v[204:207], v[124:127]\n\t" \
    "v_mfma_f32_16x16x32_bf16 v[120:123], v[136:139], v[208:211], v[120:123]\n\t" \
    "v_mfma_f32_16x16x32_bf16 v[124:127], v[152:155], v[208:211], v[124:127]\n\t" \
    "v_mfma_f32_16x16x32_bf16 v[120:123], v[140:143], v[212:215], v[120:123]\n\t" \
    "v_mfma_f32_16x16x32_bf16 v[124:127], v[156:159], v[212:215], v[124:127]\n\t" \
    "v_mfma_f32_16x16x32_bf16 v[112:115], v[96:99], v[220:223], 0\n\t" \
    "v_mfma_f32_16x16x32_bf16 v[116:119], v[104:107], v[220:223], 0\n\t" \
    "v_mfma_f32_16x16x32_bf16 v[120:123], v[176:179], v[216:219], v[120:123]\n\t" \
    "v_mfma_f32_16x16x32_bf16 v[124:127], v[180:183], v[216:219], v[124:127]\n\t" \
    "v_mfma_f32_16x16x32_bf16 v[112:115], v[100:103], v[224:227], v[112:115]\n\t" \
    "v_mfma_f32_16x16x32_bf16 v[116:119], v[108:111], v[224:227], v[116:119]\n\t" \
    "v_mfma_f32_16x16x32_bf16 v[112:115], v[176:179], v[228:231], v[112:115]\n\t" \
    "v_mfma_f32_16x16x32_bf16 v[116:119], v[180:183], v[228:231], v[116:119]\n\t" \
    "s_nop 1\n\t" \
    "ds_write_b128 %[pw], v[120:123] offset:16384\n\t" \
    "ds_write_b128 %[pw], v[124:127] offset:24576\n\t" \
    "s_nop 3\n\t" \
    "ds_write_b128 %[pw], v[112:115] offset:0\n\t" \
    "ds_write_b128 %[pw], v[116:119] offset:8192\n\t" \
    "v_cmp_ge_u32 vcc, %[tn], v248\n\t" \
    "s_mov_b32 %[a], 0\n\t" \
    "s_cbranch_vccnz .Lxv%=\n\t" \
    "buffer_load_dwordx4 v[128:131], %[l16], %[rp], %[sv0] offen offset:0 sc1\n\t" \
    "buffer_load_dwordx4 v[132:135], %[l16], %[rp], %[sv0] offen offset:1024 sc1\n\t" \
    "buffer_load_dwordx4 v[136:139], %[l16], %[rp], %[sv0] offen offset:2048 sc1\n\t" \
    "buffer_load_dwordx4 v[140:143], %[l16], %[rp], %[sv0] offen offset:3072 sc1\n\t" \
    "buffer_load_dwordx4 v[144:147], %[l16], %[rp], %[sv1] offen offset:0 sc1\n\t" \
    "buffer_load_dwordx4 v[148:151], %[l16], %[rp], %[sv1] offen offset:1024 sc1\n\t" \
    "buffer_load_dwordx4 v[152:155], %[l16], %[rp], %[sv1] offen offset:2048 sc1\n\t" \
    "buffer_load_dwordx4 v[156:159], %[l16], %[rp], %[sv1] offen offset:3072 sc1\n\t" \
    "s_mov_b32 %[a], 1\n\t" \
    ".Lxv%=:\n\t" \
    "s_cmp_eq_u32 %[fin], 0\n\t" \
    "s_waitcnt lgkmcnt(0)\n\t" \
    "s_barrier\n\t" ASM_ST \
    "s_cbranch_scc1 .Lxe%=\n\t" \
    "ds_read_b128 v[200:203], %[pr] offset:0\n\t" \
    "ds_read_b128 v[204:207], %[pr] offset:1024\n\t" \
    "ds_read_b128 v[208:211], %[pr] offset:2048\n\t" \
    "ds_read_b128 v[212:215], %[pr] offset:3072\n\t" \
    "ds_read_b128 v[216:219], %[pr] offset:4096\n\t" \
    "ds_read_b128 v[220:223], %[pr] offset:5120\n\t" \
    "ds_read_b128 v[224:227], %[pr] offset:6144\n\t" \
    "ds_read_b128 v[228:231], %[pr] offset:7168\n\t" \
    "v_lshlrev_b32 v240, 16, v250\n\t" \
    "v_and_b32 v241, 0xffff0000, v250\n\t" \
    "v_lshlrev_b32 v242, 16, v251\n\t" \
    "v_and_b32 v243, 0xffff0000, v251\n\t" \
    "s_waitcnt lgkmcnt(6)\n\t" \
    "v_pk_add_f32 v[200:201], v[200:201], v[204:205]\n\t" \
    "v_pk_add_f32 v[202:203], v[202:203], v[206:207]\n\t" \
    "s_waitcnt lgkmcnt(4)\n\t" \
    "v_pk_add_f32 v[208:209], v[208:209], v[212:213]\n\t" \
    "v_pk_add_f32 v[210:211], v[210:211], v[214:215]\n\t" \
    "v_pk_add_f32 v[200:201], v[200:201], v[208:209]\n\t" \
    "v_pk_add_f32 v[202:203], v[202:203], v[210:211]\n\t" \
    "s_waitcnt lgkmcnt(2)\n\t" \
    "v_pk_add_f32 v[216:217], v[216:217], v[220:221]\n\t" \
    "v_pk_add_f32 v[218:219], v[218:219], v[222:223]\n\t" \
    "s_waitcnt lgkmcnt(0)\n\t" \
    "v_pk_add_f32 v[224:225], v[224:225], v[228:229]\n\t" \
    "v_pk_add_f32 v[226:227], v[226:227], v[230:231]\n\t" \
    "v_pk_add_f32 v[216:217], v[216:217], v[224:225]\n\t" \
    "v_pk_add_f32 v[218:219], v[218:219], v[226:227]\n\t" \
    "v_pk_add_f32 v[200:201], v[200:201], v[216:217]\n\t" \
    "v_pk_add_f32 v[202:203], v[202:203], v[218:219]\n\t" \
    "v_pk_add_f32 v[200:201], v[200:201], v[240:241]\n\t" \
    "v_pk_add_f32 v[202:203], v[202:203], v[242:243]\n\t" \
    "v_cvt_pk_bf16_f32 v200, v200, v201\n\t" \
    "v_cvt_pk_bf16_f32 v201, v202, v203\n\t" \
    "v_min_u32 v200, -2, v200\n\t" \
    "v_min_u32 v201, -2, v201\n\t" \
    "buffer_store_dwordx2 v[200:201], %[xo], %[rd], %[sa] offen sc1\n\t" \
    ".Lxe%=:\n\t"
#define ASM_W4_M \
    ".Lc7_%=:\n\t" \
    "s_waitcnt vmcnt(0)\n\t" \
    "v_max_u32 v212, v188, v189\n\t" \
    "v_max3_u32 v212, v190, v191, v212\n\t" \
    "v_cmp_eq_u32 vcc, -1, v212\n\t" \
    "s_cbranch_vccnz .Ls7_%=\n\t" \
    ".Lk7_%=:\n\t" \
    ASM_ST2 \
    "buffer_load_dwordx4 v[184:187], %[l16], %[rs], %[s1] offen offset:2048 sc1\n\t" \
    "buffer_load_dwordx4 v[180:183], %[l16], %[rs], %[s1] offen offset:1024 sc1\n\t" \
    "buffer_load_dwordx4 v[176:179], %[l16], %[rs], %[s1] offen offset:0 sc1\n\t" \
    "buffer_load_dwordx4 v[172:175], %[l16], %[rs], %[s0] offen offset:3072 sc1\n\t" \
    "buffer_load_dwordx4 v[168:171], %[l16], %[rs], %[s0] offen offset:2048 sc1\n\t" \
    "buffer_load_dwordx4 v[164:167], %[l16], %[rs], %[s0] offen offset:1024 sc1\n\t" \
    "buffer_load_dwordx4 v[160:163], %[l16], %[rs], %[s0] offen offset:0 sc1\n\t" \
    "v_mfma_f32_16x16x32_bf16 v[208:211], v[156:159], v[188:191], 0\n\t" \
    "v_mfma_f32_16x16x32_bf16 v[204:207], v[124:127], v[188:191], 0\n\t" \
    "v_mfma_f32_16x16x32_bf16 v[200:203], v[92:95], v[188:191], 0\n\t" \
    "v_mfma_f32_16x16x32_bf16 v[196:199], v[60:63], v[188:191], 0\n\t" \
    "v_mfma_f32_16x16x32_bf16 v[192:195], v[28:31], v[188:191], 0\n\t" \
    ".Lc6_%=:\n\t" \
    "s_waitcnt vmcnt(6)\n\t" \
    "v_max_u32 v212, v184, v185\n\t" \
    "v_max3_u32 v212, v186, v187, v212\n\t" \
    "v_cmp_eq_u32 vcc, -1, v212\n\t" \
    "s_cbranch_vccnz .Ls6_%=\n\t" \
    ".Lk6_%=:\n\t" \
    ".Lc5_%=:\n\t" \
    "s_waitcnt vmcnt(5)\n\t" \
    "v_max_u32 v212, v180, v181\n\t" \
    "v_max3_u32 v212, v182, v183, v212\n\t" \
    "v_cmp_eq_u32 vcc, -1, v212\n\t" \
    "s_cbranch_vccnz .Ls5_%=\n\t" \
    ".Lk5_%=:\n\t" \
    "v_mfma_f32_16x16x32_bf16 v[192:195], v[24:27], v[184:187], v[192:195]\n\t" \
    "v_mfma_f32_16x16x32_bf16 v[192:195], v[20:23], v[180:183], v[192:195]\n\t" \
    "v_mfma_f32_16x16x32_bf16 v[196:199], v[56:59], v[184:187], v[196:199]\n\t" \
    "v_mfma_f32_16x16x32_bf16 v[196:199], v[52:55], v[180:183], v[196:199]\n\t" \
    "v_mfma_f32_16x16x32_bf16 v[200:203], v[88:91], v[184:187], v[200:203]\n\t" \
    "v_mfma_f32_16x16x32_bf16 v[200:203], v[84:87], v[180:183], v[200:203]\n\t" \
    "v_mfma_f32_16x16x32_bf16 v[204:207], v[120:123], v[184:187], v[204:207]\n\t" \
    "v_mfma_f32_16x16x32_bf16 v[204:207], v[116:119], v[180:183], v[204:207]\n\t" \
    "v_mfma_f32_16x16x32_bf16 v[208:211], v[152:155], v[184:187], v[208:211]\n\t" \
    "v_mfma_f32_16x16x32_bf16 v[208:211], v[148:151], v[180:183], v[208:211]\n\t" \
    ".Lc4_%=:\n\t" \
    "s_waitcnt vmcnt(4)\n\t" \
    "v_max_u32 v212, v176, v177\n\t" \
    "v_max3_u32 v212, v178, v179, v212\n\t" \
    "v_cmp_eq_u32 vcc, -1, v212\n\t" \
    "s_cbranch_vccnz .Ls4_%=\n\t" \
    ".Lk4_%=:\n\t" \
    ".Lc3_%=:\n\t" \
    "s_waitcnt vmcnt(3)\n\t" \
    "v_max_u32 v212, v172, v173\n\t" \
    "v_max3_u32 v212, v174, v175, v212\n\t" \
    "v_cmp_eq_u32 vcc, -1, v212\n\t" \
    "s_cbranch_vccnz .Ls3_%=\n\t" \
    ".Lk3_%=:\n\t" \
    "v_mfma_f32_16x16x32_bf16 v[208:211], v[144:147], v[176:179], v[208:211]\n\t" \
    "v_mfma_f32_16x16x32_bf16 v[208:211], v[140:143], v[172:175], v[208:211]\n\t" \
    "v_mfma_f32_16x16x32_bf16 v[204:207], v[112:115], v[176:179], v[204:207]\n\t" \
    "v_mfma_f32_16x16x32_bf16 v[204:207], v[108:111], v[172:175], v[204:207]\n\t" \
    "v_mfma_f32_16x16x32_bf16 v[200:203], v[80:83], v[176:179], v[200:203]\n\t" \
    "v_mfma_f32_16x16x32_bf16 v[200:203], v[76:79], v[172:175], v[200:203]\n\t" \
    "v_mfma_f32_16x16x32_bf16 v[196:199], v[48:51], v[176:179], v[196:199]\n\t" \
    "v_mfma_f32_16x16x32_bf16 v[196:199], v[44:47], v[172:175], v[196:199]\n\t" \
    "v_mfma_f32_16x16x32_bf16 v[192:195], v[16:19], v[176:179], v[192:195]\n\t" \
    "v_mfma_f32_16x16x32_bf16 v[192:195], v[12:15], v[172:175], v[192:195]\n\t" \
    ".Lc2_%=:\n\t" \
    "s_waitcnt vmcnt(2)\n\t" \
    "v_max_u32 v212, v168, v169\n\t" \
    "v_max3_u32 v212, v170, v171, v212\n\t" \
    "v_cmp_eq_u32 vcc, -1, v212\n\t" \
    "s_cbranch_vccnz .Ls2_%=\n\t" \
    ".Lk2_%=:\n\t" \
    "buffer_load_dwordx4 v[188:191], %[l16], %[rs], %[n1] offen offset:3072 sc1\n\t" \
    ".Lc1_%=:\n\t" \
    "s_waitcnt vmcnt(2)\n\t" \
    "v_max_u32 v212, v164, v165\n\t" \
    "v_max3_u32 v212, v166, v167, v212\n\t" \
    "v_cmp_eq_u32 vcc, -1, v212\n\t" \
    "s_cbranch_vccnz .Ls1_%=\n\t" \
    ".Lk1_%=:\n\t" \
    ".Lc0_%=:\n\t" \
    "s_waitcnt vmcnt(1)\n\t" \
    "v_max_u32 v212, v160, v161\n\t" \
    "v_max3_u32 v212, v162, v163, v212\n\t" \
    "v_cmp_eq_u32 vcc, -1, v212\n\t" \
    "s_cbranch_vccnz .Ls0_%=\n\t" \
    ".Lk0_%=:\n\t" \
    "v_mfma_f32_16x16x32_bf16 v[192:195], v[8:11], v[168:171], v[192:195]\n\t" \
    "v_mfma_f32_16x16x32_bf16 v[192:195], v[4:7], v[164:167], v[192:195]\n\t" \
    "v_mfma_f32_16x16x32_bf16 v[192:195], v[0:3], v[160:163], v[192:195]\n\t" \
    "v_mfma_f32_16x16x32_bf16 v[196:199], v[40:43], v[168:171], v[196:199]\n\t" \
    "v_mfma_f32_16x16x32_bf16 v[196:199], v[36:39], v[164:167], v[196:199]\n\t" \
    "v_mfma_f32_16x16x32_bf16 v[196:199], v[32:35], v[160:163], v[196:199]\n\t" \
    "v_mfma_f32_16x16x32_bf16 v[200:203], v[72:75], v[168:171], v[200:203]\n\t" \
    "v_mfma_f32_16x16x32_bf16 v[200:203], v[68:71], v[164:167], v[200:203]\n\t" \
    "v_mfma_f32_16x16x32_bf16 v[200:203], v[64:67], v[160:163], v[200:203]\n\t" \
    "v_mfma_f32_16x16x32_bf16 v[204:207], v[104:107], v[168:171], v[204:207]\n\t" \
    "v_mfma_f32_16x16x32_bf16 v[204:207], v[100:103], v[164:167], v[204:207]\n\t" \
    "v_mfma_f32_16x16x32_bf16 v[204:207], v[96:99], v[160:163], v[204:207]\n\t" \
    "v_mfma_f32_16x16x32_bf16 v[208:211], v[136:139], v[168:171], v[208:211]\n\t" \
    "v_mfma_f32_16x16x32_bf16 v[208:211], v[132:135], v[164:167], v[208:211]\n\t" \
    "v_mfma_f32_16x16x32_bf16 v[208:211], v[128:131], v[160:163], v[208:211]\n\t" \
    "v_mov_b32 v212, %[un]\n\t" \
    "ds_write_b128 %[ld], v[192:195] offset:0\n\t" \
    "ds_write_b128 %[ld], v[196:199] offset:6144\n\t" \
    "ds_write_b128 %[ld], v[200:203] offset:12288\n\t" \
    "ds_write_b128 %[ld], v[204:207] offset:18432\n\t" \
    "s_nop 2\n\t" \
    "ds_write_b128 %[ld], v[208:211] offset:24576\n\t" \
    "ds_write_b32 %[fl], v212\n\t" \
    "s_branch .Lme%=\n\t" \
    ".Ls7_%=:\n\t" \
    "s_cmp_eq_u32 %[n], 0\n\t" \
    "s_cbranch_scc1 .Lk7_%=\n\t" \
    "s_sub_u32 %[n], %[n], 1\n\t" \
    "s_sleep 1\n\t" \
    "buffer_load_dwordx4 v[188:191], %[l16], %[rs], %[s1] offen offset:3072 sc1\n\t" \
    "s_branch .Lc7_%=\n\t" \
    ".Ls6_%=:\n\t" \
    "s_cmp_eq_u32 %[n], 0\n\t" \
    "s_cbranch_scc1 .Lk6_%=\n\t" \
    "s_sub_u32 %[n], %[n], 1\n\t" \
    "buffer_load_dwordx4 v[184:187], %[l16], %[rs], %[s1] offen offset:2048 sc1\n\t" \
    "s_waitcnt vmcnt(0)\n\t" \
    "s_branch .Lc6_%=\n\t" \
    ".Ls5_%=:\n\t" \
    "s_cmp_eq_u32 %[n], 0\n\t" \
    "s_cbranch_scc1 .Lk5_%=\n\t" \
    "s_sub_u32 %[n], %[n], 1\n\t" \
    "buffer_load_dwordx4 v[180:183], %[l16], %[rs], %[s1] offen offset:1024 sc1\n\t" \
    "s_waitcnt vmcnt(0)\n\t" \
    "s_branch .Lc5_%=\n\t" \
    ".Ls4_%=:\n\t" \
    "s_cmp_eq_u32 %[n], 0\n\t" \
    "s_cbranch_scc1 .Lk4_%=\n\t" \
    "s_sub_u32 %[n], %[n], 1\n\t" \
    "buffer_load_dwordx4 v[176:179], %[l16], %[rs], %[s1] offen offset:0 sc1\n\t" \
    "s_waitcnt vmcnt(0)\n\t" \
    "s_branch .Lc4_%=\n\t" \
    ".Ls3_%=:\n\t" \
    "s_cmp_eq_u32 %[n], 0\n\t" \
    "s_cbranch_scc1 .Lk3_%=\n\t" \
    "s_sub_u32 %[n], %[n], 1\n\t" \
    "buffer_load_dwordx4 v[172:175], %[l16], %[rs], %[s0] offen offset:3072 sc1\n\t" \
    "s_waitcnt vmcnt(0)\n\t" \
    "s_branch .Lc3_%=\n\t" \
    ".Ls2_%=:\n\t" \
    "s_cmp_eq_u32 %[n], 0\n\t" \
    "s_cbranch_scc1 .Lk2_%=\n\t" \
    "s_sub_u32 %[n], %[n], 1\n\t" \
    "buffer_load_dwordx4 v[168:171], %[l16], %[rs], %[s0] offen offset:2048 sc1\n\t" \
    "s_waitcnt vmcnt(0)\n\t" \
    "s_branch .Lc2_%=\n\t" \
    ".Ls1_%=:\n\t" \
    "s_cmp_eq_u32 %[n], 0\n\t" \
    "s_cbranch_scc1 .Lk1_%=\n\t" \
    "s_sub_u32 %[n], %[n], 1\n\t" \
    "buffer_load_dwordx4 v[164:167], %[l16], %[rs], %[s0] offen offset:1024 sc1\n\t" \
    "s_waitcnt vmcnt(0)\n\t" \
    "s_branch .Lc1_%=\n\t" \
    ".Ls0_%=:\n\t" \
    "s_cmp_eq_u32 %[n], 0\n\t" \
    "s_cbranch_scc1 .Lk0_%=\n\t" \
    "s_sub_u32 %[n], %[n], 1\n\t" \
    "buffer_load_dwordx4 v[160:163], %[l16], %[rs], %[s0] offen offset:0 sc1\n\t" \
    "s_waitcnt vmcnt(0)\n\t" \
    "s_branch .Lc0_%=\n\t" \
    ".Lme%=:\n\t"
#define ASM_W4F_0 \
    "s_branch .Lf0%=\n\t" \
    ".Lfr%=:\n\t" \
    "s_cmp_eq_u32 %[n], 0\n\t" \
    "s_cbranch_scc1 .Lfb%=\n\t" \
    "s_sub_u32 %[n], %[n], 1\n\t" \
    "s_sleep 6\n\t" \
    "buffer_load_dwordx2 v[164:165], %[o0], %[rd], %[c1] offen sc1\n\t" \
    "buffer_load_dwordx2 v[166:167], %[o1], %[rd], %[c1] offen sc1\n\t" \
    "buffer_load_dwordx2 v[168:169], %[o2], %[rd], %[c1] offen sc1\n\t" \
    "buffer_load_dwordx2 v[170:171], %[o3], %[rd], %[c1] offen sc1\n\t" \
    "buffer_load_dwordx2 v[172:173], %[o4], %[rd], %[c1] offen sc1\n\t" \
    "buffer_load_dwordx2 v[174:175], %[o0], %[rd], %[ca] offen sc1\n\t" \
    "buffer_load_dwordx2 v[176:177], %[o1], %[rd], %[ca] offen sc1\n\t" \
    "buffer_load_dwordx2 v[178:179], %[o2], %[rd], %[ca] offen sc1\n\t" \
    "buffer_load_dwordx2 v[180:181], %[o3], %[rd], %[ca] offen sc1\n\t" \
    "buffer_load_dwordx2 v[182:183], %[o4], %[rd], %[ca] offen sc1\n\t" \
    "s_waitcnt vmcnt(0)\n\t" \
    "s_branch .Lfa%=\n\t" \
    ".Lfs0_%=:\n\t" \
    "s_cmp_eq_u32 %[n], 0\n\t" \
    "s_cbranch_scc1 .Lfu0_%=\n\t" \
    "s_sub_u32 %[n], %[n], 1\n\t" \
    "s_sleep 1\n\t" \
    "s_branch .Lft0_%=\n\t" \
    ".Lfs1_%=:\n\t" \
    "s_cmp_eq_u32 %[n], 0\n\t" \
    "s_cbranch_scc1 .Lfu1_%=\n\t" \
    "s_sub_u32 %[n], %[n], 1\n\t" \
    "s_sleep 1\n\t" \
    "s_branch .Lft1_%=\n\t" \
    ".Lfx%=:\n\t" \
    "s_cmp_eq_u32 %[n], 0\n\t" \
    "s_cbranch_scc1 .Lfz%=\n\t" \
    "s_sub_u32 %[n], %[n], 1\n\t" \
    "s_branch .Lfy%=\n\t" \
    ".Lf0%=:\n\t" \
    "s_waitcnt vmcnt(1)\n\t" \
    ".Lfa%=:\n\t" \
    "v_max_u32 v194, v164, v165\n\t" \
    "v_max3_u32 v194, v166, v167, v194\n\t" \
    "v_max3_u32 v194, v168, v169, v194\n\t" \
    "v_max3_u32 v194, v170, v171, v194\n\t" \
    "v_max3_u32 v194, v172, v173, v194\n\t" \
    "v_max3_u32 v194, v174, v175, v194\n\t" \
    "v_max3_u32 v194, v176, v177, v194\n\t" \
    "v_max3_u32 v194, v178, v179, v194\n\t" \
    "v_max3_u32 v194, v180, v181, v194\n\t" \
    "v_max3_u32 v194, v182, v183, v194\n\t" \
    "v_cmp_eq_u32 vcc, -1, v194\n\t" \
    "s_cbranch_vccnz .Lfr%=\n\t" \
    ".Lfb%=:\n\t" \
    "v_lshlrev_b32 v64, 16, v164\n\t" \
    "v_and_b32 v65, 0xffff0000, v164\n\t" \
    "v_lshlrev_b32 v84, 16, v174\n\t" \
    "v_and_b32 v85, 0xffff0000, v174\n\t" \
    "v_lshlrev_b32 v66, 16, v165\n\t" \
    "v_and_b32 v67, 0xffff0000, v165\n\t" \
    "v_lshlrev_b32 v86, 16, v175\n\t" \
    "v_and_b32 v87, 0xffff0000, v175\n\t" \
    "v_lshlrev_b32 v68, 16, v166\n\t" \
    "v_and_b32 v69, 0xffff0000, v166\n\t" \
    "v_lshlrev_b32 v88, 16, v176\n\t" \
    "v_and_b32 v89, 0xffff0000, v176\n\t" \
    "v_lshlrev_b32 v70, 16, v167\n\t" \
    "v_and_b32 v71, 0xffff0000, v167\n\t" \
    "v_lshlrev_b32 v90, 16, v177\n\t" \
    "v_and_b32 v91, 0xffff0000, v177\n\t" \
    "v_lshlrev_b32 v72, 16, v168\n\t" \
    "v_and_b32 v73, 0xffff0000, v168\n\t" \
    "v_lshlrev_b32 v92, 16, v178\n\t" \
    "v_and_b32 v93, 0xffff0000, v178\n\t" \
    "v_lshlrev_b32 v74, 16, v169\n\t" \
    "v_and_b32 v75, 0xffff0000, v169\n\t" \
    "v_lshlrev_b32 v94, 16, v179\n\t" \
    "v_and_b32 v95, 0xffff0000, v179\n\t" \
    "v_lshlrev_b32 v76, 16, v170\n\t" \
    "v_and_b32 v77, 0xffff0000, v170\n\t" \
    "v_lshlrev_b32 v96, 16, v180\n\t" \
    "v_and_b32 v97, 0xffff0000, v180\n\t" \
    "v_lshlrev_b32 v78, 16, v171\n\t" \
    "v_and_b32 v79, 0xffff0000, v171\n\t" \
    "v_lshlrev_b32 v98, 16, v181\n\t" \
    "v_and_b32 v99, 0xffff0000, v181\n\t" \
    "v_lshlrev_b32 v80, 16, v172\n\t" \
    "v_and_b32 v81, 0xffff0000, v172\n\t" \
    "v_lshlrev_b32 v100, 16, v182\n\t" \
    "v_and_b32 v101, 0xffff0000, v182\n\t" \
    "v_lshlrev_b32 v82, 16, v173\n\t" \
    "v_and_b32 v83, 0xffff0000, v173\n\t" \
    "v_lshlrev_b32 v102, 16, v183\n\t" \
    "v_and_b32 v103, 0xffff0000, v183\n\t" \
    "v_pk_add_f32 v[64:65], v[64:65], v[84:85]\n\t" \
    "v_pk_add_f32 v[66:67], v[66:67], v[86:87]\n\t" \
    "v_pk_add_f32 v[68:69], v[68:69], v[88:89]\n\t" \
    "v_pk_add_f32 v[70:71], v[70:71], v[90:91]\n\t" \
    "v_pk_add_f32 v[72:73], v[72:73], v[92:93]\n\t" \
    "v_pk_add_f32 v[74:75], v[74:75], v[94:95]\n\t" \
    "v_pk_add_f32 v[76:77], v[76:77], v[96:97]\n\t" \
    "v_pk_add_f32 v[78:79], v[78:79], v[98:99]\n\t" \
    "v_pk_add_f32 v[80:81], v[80:81], v[100:101]\n\t" \
    "v_pk_add_f32 v[82:83], v[82:83], v[102:103]\n\t" \
    ".Lft0_%=:\n\t" \
    "ds_read_b32 v194, %[f0]\n\t" \
    "s_waitcnt lgkmcnt(0)\n\t" \
    "v_cmp_ge_u32 vcc, %[u], v194\n\t" \
    "s_cbranch_vccnz .Lfs0_%=\n\t" \
    ".Lfu0_%=:\n\t" \
    "ds_read_b128 v[84:87], %[la] offset:0\n\t" \
    "ds_read_b128 v[88:91], %[la] offset:6144\n\t" \
    "ds_read_b128 v[92:95], %[la] offset:12288\n\t" \
    "ds_read_b128 v[96:99], %[la] offset:18432\n\t" \
    "ds_read_b128 v[100:103], %[la] offset:24576\n\t" \
    "ds_read_b128 v[104:107], %[la] offset:1024\n\t" \
    "ds_read_b128 v[108:111], %[la] offset:7168\n\t" \
    "ds_read_b128 v[112:115], %[la] offset:13312\n\t" \
    "ds_read_b128 v[116:119], %[la] offset:19456\n\t" \
    "ds_read_b128 v[120:123], %[la] offset:25600\n\t" \
    "ds_read_b128 v[124:127], %[la] offset:2048\n\t" \
    "s_waitcnt lgkmcnt(10)\n\t" \
    "v_pk_add_f32 v[64:65], v[64:65], v[84:85]\n\t" \
    "v_pk_add_f32 v[66:67], v[66:67], v[86:87]\n\t" \
    "ds_read_b128 v[128:131], %[la] offset:8192\n\t" \
    "s_waitcnt lgkmcnt(10)\n\t" \
    "v_pk_add_f32 v[68:69], v[68:69], v[88:89]\n\t" \
    "v_pk_add_f32 v[70:71], v[70:71], v[90:91]\n\t" \
    "ds_read_b128 v[132:135], %[la] offset:14336\n\t" \
    "s_waitcnt lgkmcnt(10)\n\t" \
    "v_pk_add_f32 v[72:73], v[72:73], v[92:93]\n\t" \
    "v_pk_add_f32 v[74:75], v[74:75], v[94:95]\n\t" \
    "ds_read_b128 v[136:139], %[la] offset:20480\n\t" \
    "s_waitcnt lgkmcnt(10)\n\t" \
    "v_pk_add_f32 v[76:77], v[76:77], v[96:97]\n\t" \
    "v_pk_add_f32 v[78:79], v[78:79], v[98:99]\n\t" \
    "ds_read_b128 v[140:143], %[la] offset:26624\n\t" \
    "s_waitcnt lgkmcnt(10)\n\t" \
    "v_pk_add_f32 v[80:81], v[80:81], v[100:101]\n\t" \
    "v_pk_add_f32 v[82:83], v[82:83], v[102:103]\n\t" \
    "ds_read_b128 v[144:147], %[la] offset:3072\n\t" \
    "s_waitcnt lgkmcnt(10)\n\t" \
    "v_pk_add_f32 v[64:65], v[64:65], v[104:105]\n\t" \
    "v_pk_add_f32 v[66:67], v[66:67], v[106:107]\n\t" \
    "ds_read_b128 v[148:151], %[la] offset:9216\n\t" \
    "s_waitcnt lgkmcnt(10)\n\t" \
    "v_pk_add_f32 v[68:69], v[68:69], v[108:109]\n\t" \
    "v_pk_add_f32 v[70:71], v[70:71], v[110:111]\n\t" \
    "ds_read_b128 v[152:155], %[la] offset:15360\n\t" \
    "s_waitcnt lgkmcnt(10)\n\t" \
    "v_pk_add_f32 v[72:73], v[72:73], v[112:113]\n\t" \
    "v_pk_add_f32 v[74:75], v[74:75], v[114:115]\n\t" \
    "ds_read_b128 v[156:159], %[la] offset:21504\n\t" \
    "s_waitcnt lgkmcnt(10)\n\t" \
    "v_pk_add_f32 v[76:77], v[76:77], v[116:117]\n\t" \
    "v_pk_add_f32 v[78:79], v[78:79], v[118:119]\n\t" \
    "ds_read_b128 v[160:163], %[la] offset:27648\n\t" \
    "s_waitcnt lgkmcnt(10)\n\t" \
    "v_pk_add_f32 v[80:81], v[80:81], v[120:121]\n\t" \
    "v_pk_add_f32 v[82:83], v[82:83], v[122:123]\n\t" \
    "s_waitcnt lgkmcnt(9)\n\t" \
    "v_pk_add_f32 v[64:65], v[64:65], v[124:125]\n\t" \
    "v_pk_add_f32 v[66:67], v[66:67], v[126:127]\n\t" \
    "s_waitcnt lgkmcnt(8)\n\t" \
    "v_pk_add_f32 v[68:69], v[68:69], v[128:129]\n\t" \
    "v_pk_add_f32 v[70:71], v[70:71], v[130:131]\n\t" \
    "s_waitcnt lgkmcnt(7)\n\t" \
    "v_pk_add_f32 v[72:73], v[72:73], v[132:133]\n\t" \
    "v_pk_add_f32 v[74:75], v[74:75], v[134:135]\n\t" \
    "s_waitcnt lgkmcnt(6)\n\t" \
    "v_pk_add_f32 v[76:77], v[76:77], v[136:137]\n\t" \
    "v_pk_add_f32 v[78:79], v[78:79], v[138:139]\n\t" \
    "s_waitcnt lgkmcnt(5)\n\t" \
    "v_pk_add_f32 v[80:81], v[80:81], v[140:141]\n\t" \
    "v_pk_add_f32 v[82:83], v[82:83], v[142:143]\n\t" \
    "s_waitcnt lgkmcnt(4)\n\t" \
    "v_pk_add_f32 v[64:65], v[64:65], v[144:145]\n\t" \
    "v_pk_add_f32 v[66:67], v[66:67], v[146:147]\n\t" \
    "s_waitcnt lgkmcnt(3)\n\t" \
    "v_pk_add_f32 v[68:69], v[68:69], v[148:149]\n\t" \
    "v_pk_add_f32 v[70:71], v[70:71], v[150:151]\n\t" \
    "s_waitcnt lgkmcnt(2)\n\t" \
    "v_pk_add_f32 v[72:73], v[72:73], v[152:153]\n\t" \
    "v_pk_add_f32 v[74:75], v[74:75], v[154:155]\n\t" \
    "s_waitcnt lgkmcnt(1)\n\t" \
    "v_pk_add_f32 v[76:77], v[76:77], v[156:157]\n\t" \
    "v_pk_add_f32 v[78:79], v[78:79], v[158:159]\n\t" \
    "s_waitcnt lgkmcnt(0)\n\t" \
    "v_pk_add_f32 v[80:81], v[80:81], v[160:161]\n\t" \
    "v_pk_add_f32 v[82:83], v[82:83], v[162:163]\n\t" \
    ".Lft1_%=:\n\t" \
    "ds_read_b32 v194, %[f1]\n\t" \
    "s_waitcnt lgkmcnt(0)\n\t" \
    "v_cmp_ge_u32 vcc, %[u], v194\n\t" \
    "s_cbranch_vccnz .Lfs1_%=\n\t" \
    ".Lfu1_%=:\n\t" \
    ASM_ST2 \
    "ds_read_b128 v[84:87], %[la] offset:4096\n\t" \
    "ds_read_b128 v[88:91], %[la] offset:10240\n\t" \
    "ds_read_b128 v[92:95], %[la] offset:16384\n\t" \
    "ds_read_b128 v[96:99], %[la] offset:22528\n\t" \
    "ds_read_b128 v[100:103], %[la] offset:28672\n\t" \
    "ds_read_b128 v[104:107], %[la] offset:5120\n\t" \
    "ds_read_b128 v[108:111], %[la] offset:11264\n\t" \
    "ds_read_b128 v[112:115], %[la] offset:17408\n\t" \
    "ds_read_b128 v[116:119], %[la] offset:23552\n\t" \
    "ds_read_b128 v[120:123], %[la] offset:29696\n\t" \
    "v_mov_b32 v194, %[un]\n\t" \
    "ds_write_b32 %[fd], v194\n\t" \
    "s_waitcnt lgkmcnt(10)\n\t" \
    "v_pk_add_f32 v[64:65], v[64:65], v[84:85]\n\t" \
    "v_pk_add_f32 v[66:67], v[66:67], v[86:87]\n\t" \
    "s_waitcnt lgkmcnt(9)\n\t" \
    "v_pk_add_f32 v[68:69], v[68:69], v[88:89]\n\t" \
    "v_pk_add_f32 v[70:71], v[70:71], v[90:91]\n\t" \
    "s_waitcnt lgkmcnt(8)\n\t" \
    "v_pk_add_f32 v[72:73], v[72:73], v[92:93]\n\t" \
    "v_pk_add_f32 v[74:75], v[74:75], v[94:95]\n\t" \
    "s_waitcnt lgkmcnt(7)\n\t" \
    "v_pk_add_f32 v[76:77], v[76:77], v[96:97]\n\t" \
    "v_pk_add_f32 v[78:79], v[78:79], v[98:99]\n\t" \
    "s_waitcnt lgkmcnt(6)\n\t" \
    "v_pk_add_f32 v[80:81], v[80:81], v[100:101]\n\t" \
    "v_pk_add_f32 v[82:83], v[82:83], v[102:103]\n\t" \
    "s_waitcnt lgkmcnt(5)\n\t" \
    "v_pk_add_f32 v[64:65], v[64:65], v[104:105]\n\t" \
    "v_pk_add_f32 v[66:67], v[66:67], v[106:107]\n\t" \
    "s_waitcnt lgkmcnt(4)\n\t" \
    "v_pk_add_f32 v[68:69], v[68:69], v[108:109]\n\t" \
    "v_pk_add_f32 v[70:71], v[70:71], v[110:111]\n\t" \
    "s_waitcnt lgkmcnt(3)\n\t" \
    "v_pk_add_f32 v[72:73], v[72:73], v[112:113]\n\t" \
    "v_pk_add_f32 v[74:75], v[74:75], v[114:115]\n\t" \
    "s_waitcnt lgkmcnt(2)\n\t" \
    "v_pk_add_f32 v[76:77], v[76:77], v[116:117]\n\t" \
    "v_pk_add_f32 v[78:79], v[78:79], v[118:119]\n\t" \
    "s_waitcnt lgkmcnt(1)\n\t" \
    "v_pk_add_f32 v[80:81], v[80:81], v[120:121]\n\t" \
    "v_pk_add_f32 v[82:83], v[82:83], v[122:123]\n\t" \
    "v_pk_add_f32 v[84:85], v[64:65], v[66:67]\n\t" \
    "v_pk_mul_f32 v[88:89], v[64:65], v[64:65]\n\t" \
    "v_pk_add_f32 v[86:87], v[68:69], v[70:71]\n\t" \
    "v_pk_mul_f32 v[90:91], v[66:67], v[66:67]\n\t" \
    "v_pk_fma_f32 v[88:89], v[68:69], v[68:69], v[88:89]\n\t" \
    "v_pk_fma_f32 v[90:91], v[70:71], v[70:71], v[90:91]\n\t" \
    "v_pk_add_f32 v[84:85], v[84:85], v[72:73]\n\t" \
    "v_pk_fma_f32 v[88:89], v[72:73], v[72:73], v[88:89]\n\t" \
    "v_pk_add_f32 v[86:87], v[86:87], v[74:75]\n\t" \
    "v_pk_fma_f32 v[90:91], v[74:75], v[74:75], v[90:91]\n\t" \
    "v_pk_add_f32 v[84:85], v[84:85], v[76:77]\n\t" \
    "v_pk_fma_f32 v[88:89], v[76:77], v[76:77], v[88:89]\n\t" \
    "v_pk_add_f32 v[86:87], v[86:87], v[78:79]\n\t" \
    "v_pk_fma_f32 v[90:91], v[78:79], v[78:79], v[90:91]\n\t" \
    "v_pk_add_f32 v[84:85], v[84:85], v[80:81]\n\t" \
    "v_pk_fma_f32 v[88:89], v[80:81], v[80:81], v[88:89]\n\t" \
    "v_pk_add_f32 v[86:87], v[86:87], v[82:83]\n\t" \
    "v_pk_fma_f32 v[90:91], v[82:83], v[82:83], v[90:91]\n\t" \
    "v_pk_add_f32 v[84:85], v[84:85], v[86:87]\n\t" \
    "v_pk_add_f32 v[88:89], v[88:89], v[90:91]\n\t" \
    "v_add_f32 v194, v84, v85\n\t" \
    "v_add_f32 v195, v88, v89\n\t" \
    "s_nop 1\n\t" \
    "v_permlane16_swap_b32 v194, v195\n\t" \
    "v_add_f32 v196, v194, v195\n\t" \
    "v_add_f32 v197, v194, v195\n\t" \
    "s_nop 1\n\t" \
    "v_permlane32_swap_b32 v196, v197\n\t" \
    "v_add_f32 v194, v196, v197\n\t" \
    "v_add_f32 v195, v196, v197\n\t" \
    "s_nop 1\n\t" \
    "v_permlane16_swap_b32 v194, v195\n\t" \
    "v_min_u32 v194, -2, v194\n\t" \
    "v_min_u32 v195, -2, v195\n\t" \
    "buffer_store_dwordx2 v[194:195], %[so], %[rr], %[sp] offen sc1\n\t" \
    ASM_ST3 \
    "v_cvt_pk_bf16_f32 v184, v64, v65\n\t" \
    "v_cvt_pk_bf16_f32 v185, v66, v67\n\t" \
    "v_cvt_pk_bf16_f32 v186, v68, v69\n\t" \
    "v_cvt_pk_bf16_f32 v187, v70, v71\n\t" \
    "v_cvt_pk_bf16_f32 v188, v72, v73\n\t" \
    "v_cvt_pk_bf16_f32 v189, v74, v75\n\t" \
    "v_cvt_pk_bf16_f32 v190, v76, v77\n\t" \
    "v_cvt_pk_bf16_f32 v191, v78, v79\n\t" \
    "v_cvt_pk_bf16_f32 v192, v80, v81\n\t" \
    "v_cvt_pk_bf16_f32 v193, v82, v83\n\t" \
    "v_permlane16_swap_b32 v184, v186\n\t" \
    "v_permlane16_swap_b32 v185, v187\n\t" \
    "v_permlane16_swap_b32 v188, v190\n\t" \
    "v_permlane16_swap_b32 v189, v191\n\t" \
    W4SLEEP \
    ".Lfy%=:\n\t" \
    "buffer_load_dwordx2 v[196:197], %[si], %[rr], %[sq] offen sc1\n\t" \
    "buffer_load_dwordx2 v[198:199], %[si], %[rr], %[sq] offen offset:512 sc1\n\t" \
    "s_waitcnt vmcnt(0)\n\t" \
    "v_max_u32 v200, v196, v197\n\t" \
    "v_max3_u32 v200, v198, v199, v200\n\t" \
    "v_cmp_eq_u32 vcc, -1, v200\n\t" \
    "s_cbranch_vccnz .Lfx%=\n\t" \
    ".Lfz%=:\n\t" \
    ASM_ST \
    "v_add_f32 v194, v196, v198\n\t" \
    "v_add_f32 v195, v197, v199\n\t" \
    "buffer_store_dwordx4 v[184:187], %[pa], %[rd], %[xr] offen sc1\n\t" \
    "buffer_store_dwordx4 v[188:191], %[pb], %[rd], %[xr] offen sc1\n\t" \
    "buffer_store_dwordx2 v[192:193], %[o4], %[rd], %[xr] offen sc1\n\t" \
    "s_nop 1\n\t" \
    "v_permlane16_swap_b32 v194, v195\n\t" \
    "v_add_f32 v196, v194, v195\n\t" \
    "v_add_f32 v197, v194, v195\n\t" \
    "s_nop 1\n\t" \
    "v_permlane32_swap_b32 v196, v197\n\t" \
    "v_add_f32 v194, v196, v197\n\t" \
    "v_add_f32 v195, v196, v197\n\t" \
    "s_nop 1\n\t" \
    "v_permlane16_swap_b32 v194, v195\n\t" \
    "v_mul_f32 v194, %[kd], v194\n\t" \
    "v_mul_f32 v195, %[kd], v195\n\t" \
    "v_fma_f32 v195, -v194, v194, v195\n\t" \
    "v_max_f32 v195, 0, v195\n\t" \
    "v_cvt_pk_bf16_f32 v195, v195, v195\n\t" \
    "v_lshlrev_b32 v195, 16, v195\n\t" \
    "v_add_f32 v195, %[ke], v195\n\t" \
    "v_cvt_pk_bf16_f32 v195, v195, v195\n\t" \
    "v_lshlrev_b32 v195, 16, v195\n\t" \
    "v_sqrt_f32 v195, v195\n\t" \
    "v_cvt_pk_bf16_f32 v194, v194, v194\n\t" \
    "v_lshlrev_b32 v194, 16, v194\n\t" \
    "v_cvt_pk_bf16_f32 v195, v195, v195\n\t" \
    "v_lshlrev_b32 v195, 16, v195\n\t" \
    "v_rcp_f32 v196, v195\n\t" \
    "v_rcp_f32 v197, v195\n\t" \
    "s_nop 0\n\t" \
    "v_mul_f32 v198, -v194, v196\n\t" \
    "v_mul_f32 v199, -v194, v196\n\t" \
    "v_pk_fma_f32 v[64:65], v[64:65], v[196:197], v[198:199]\n\t" \
    "v_pk_fma_f32 v[66:67], v[66:67], v[196:197], v[198:199]\n\t" \
    "v_pk_fma_f32 v[68:69], v[68:69], v[196:197], v[198:199]\n\t" \
    "v_pk_fma_f32 v[70:71], v[70:71], v[196:197], v[198:199]\n\t" \
    "v_pk_fma_f32 v[72:73], v[72:73], v[196:197], v[198:199]\n\t" \
    "v_pk_fma_f32 v[74:75], v[74:75], v[196:197], v[198:199]\n\t" \
    "v_pk_fma_f32 v[76:77], v[76:77], v[196:197], v[198:199]\n\t" \
    "v_pk_fma_f32 v[78:79], v[78:79], v[196:197], v[198:199]\n\t" \
    "v_pk_fma_f32 v[80:81], v[80:81], v[196:197], v[198:199]\n\t" \
    "v_pk_fma_f32 v[82:83], v[82:83], v[196:197], v[198:199]\n\t" \
    "v_cvt_pk_bf16_f32 v184, v64, v65\n\t" \
    "v_cvt_pk_bf16_f32 v185, v66, v67\n\t" \
    "v_cvt_pk_bf16_f32 v186, v68, v69\n\t" \
    "v_cvt_pk_bf16_f32 v187, v70, v71\n\t" \
    "v_cvt_pk_bf16_f32 v188, v72, v73\n\t" \
    "v_cvt_pk_bf16_f32 v189, v74, v75\n\t" \
    "v_cvt_pk_bf16_f32 v190, v76, v77\n\t" \
    "v_cvt_pk_bf16_f32 v191, v78, v79\n\t" \
    "v_cvt_pk_bf16_f32 v192, v80, v81\n\t" \
    "v_cvt_pk_bf16_f32 v193, v82, v83\n\t" \
    "v_permlane16_swap_b32 v184, v186\n\t" \
    "v_permlane16_swap_b32 v185, v187\n\t" \
    "v_permlane16_swap_b32 v188, v190\n\t" \
    "v_permlane16_swap_b32 v189, v191\n\t" \
    "buffer_store_dwordx4 v[184:187], %[pa], %[rd], %[xn] offen sc1\n\t" \
    "buffer_store_dwordx4 v[188:191], %[pb], %[rd], %[xn] offen sc1\n\t" \
    "buffer_store_dwordx2 v[192:193], %[o4], %[rd], %[xn] offen sc1\n\t" \
    "buffer_load_dwordx2 v[164:165], %[o0], %[rd], %[n1] offen sc1\n\t" \
    "buffer_load_dwordx2 v[166:167], %[o1], %[rd], %[n1] offen sc1\n\t" \
    "buffer_load_dwordx2 v[168:169], %[o2], %[rd], %[n1] offen sc1\n\t" \
    "buffer_load_dwordx2 v[170:171], %[o3], %[rd], %[n1] offen sc1\n\t" \
    "buffer_load_dwordx2 v[172:173], %[o4], %[rd], %[n1] offen sc1\n\t" \
    "buffer_load_dwordx2 v[174:175], %[o0], %[rd], %[na] offen sc1\n\t" \
    "buffer_load_dwordx2 v[176:177], %[o1], %[rd], %[na] offen sc1\n\t" \
    "buffer_load_dwordx2 v[178:179], %[o2], %[rd], %[na] offen sc1\n\t" \
    "buffer_load_dwordx2 v[180:181], %[o3], %[rd], %[na] offen sc1\n\t" \
    "buffer_load_dwordx2 v[182:183], %[o4], %[rd], %[na] offen sc1\n\t" \
    "v_mov_b32 v196, %[tn]\n\t" \
    "v_mov_b32 v197, %[tn]\n\t" \
    "v_mov_b32 v198, %[tn]\n\t" \
    "v_mov_b32 v199, %[tn]\n\t" \
    "s_waitcnt vmcnt(10)\n\t" \
    "buffer_store_dwordx4 v[196:199], %[fo], %[rn], %[fs] offen sc1\n\t" \
    "s_nop 1\n\t"
#define ASM_W4F_1 \
    "s_branch .Lf0%=\n\t" \
    ".Lfr%=:\n\t" \
    "s_cmp_eq_u32 %[n], 0\n\t" \
    "s_cbranch_scc1 .Lfb%=\n\t" \
    "s_sub_u32 %[n], %[n], 1\n\t" \
    "s_sleep 6\n\t" \
    "buffer_load_dwordx2 v[164:165], %[o0], %[rd], %[c1] offen sc1\n\t" \
    "buffer_load_dwordx2 v[166:167], %[o1], %[rd], %[c1] offen sc1\n\t" \
    "buffer_load_dwordx2 v[168:169], %[o2], %[rd], %[c1] offen sc1\n\t" \
    "buffer_load_dwordx2 v[170:171], %[o3], %[rd], %[c1] offen sc1\n\t" \
    "buffer_load_dwordx2 v[172:173], %[o4], %[rd], %[c1] offen sc1\n\t" \
    "buffer_load_dwordx2 v[174:175], %[o0], %[rd], %[ca] offen sc1\n\t" \
    "buffer_load_dwordx2 v[176:177], %[o1], %[rd], %[ca] offen sc1\n\t" \
    "buffer_load_dwordx2 v[178:179], %[o2], %[rd], %[ca] offen sc1\n\t" \
    "buffer_load_dwordx2 v[180:181], %[o3], %[rd], %[ca] offen sc1\n\t" \
    "buffer_load_dwordx2 v[182:183], %[o4], %[rd], %[ca] offen sc1\n\t" \
    "s_waitcnt vmcnt(0)\n\t" \
    "s_branch .Lfa%=\n\t" \
    ".Lfs0_%=:\n\t" \
    "s_cmp_eq_u32 %[n], 0\n\t" \
    "s_cbranch_scc1 .Lfu0_%=\n\t" \
    "s_sub_u32 %[n], %[n], 1\n\t" \
    "s_sleep 1\n\t" \
    "s_branch .Lft0_%=\n\t" \
    ".Lfs1_%=:\n\t" \
    "s_cmp_eq_u32 %[n], 0\n\t" \
    "s_cbranch_scc1 .Lfu1_%=\n\t" \
    "s_sub_u32 %[n], %[n], 1\n\t" \
    "s_sleep 1\n\t" \
    "s_branch .Lft1_%=\n\t" \
    ".Lf0%=:\n\t" \
    "s_waitcnt vmcnt(0)\n\t" \
    ".Lfa%=:\n\t" \
    "v_max_u32 v194, v164, v165\n\t" \
    "v_max3_u32 v194, v166, v167, v194\n\t" \
    "v_max3_u32 v194, v168, v169, v194\n\t" \
    "v_max3_u32 v194, v170, v171, v194\n\t" \
    "v_max3_u32 v194, v172, v173, v194\n\t" \
    "v_max3_u32 v194, v174, v175, v194\n\t" \
    "v_max3_u32 v194, v176, v177, v194\n\t" \
    "v_max3_u32 v194, v178, v179, v194\n\t" \
    "v_max3_u32 v194, v180, v181, v194\n\t" \
    "v_max3_u32 v194, v182, v183, v194\n\t" \
    "v_cmp_eq_u32 vcc, -1, v194\n\t" \
    "s_cbranch_vccnz .Lfr%=\n\t" \
    ".Lfb%=:\n\t" \
    "v_lshlrev_b32 v64, 16, v164\n\t" \
    "v_and_b32 v65, 0xffff0000, v164\n\t" \
    "v_lshlrev_b32 v84, 16, v174\n\t" \
    "v_and_b32 v85, 0xffff0000, v174\n\t" \
    "v_lshlrev_b32 v66, 16, v165\n\t" \
    "v_and_b32 v67, 0xffff0000, v165\n\t" \
    "v_lshlrev_b32 v86, 16, v175\n\t" \
    "v_and_b32 v87, 0xffff0000, v175\n\t" \
    "v_lshlrev_b32 v68, 16, v166\n\t" \
    "v_and_b32 v69, 0xffff0000, v166\n\t" \
    "v_lshlrev_b32 v88, 16, v176\n\t" \
    "v_and_b32 v89, 0xffff0000, v176\n\t" \
    "v_lshlrev_b32 v70, 16, v167\n\t" \
    "v_and_b32 v71, 0xffff0000, v167\n\t" \
    "v_lshlrev_b32 v90, 16, v177\n\t" \
    "v_and_b32 v91, 0xffff0000, v177\n\t" \
    "v_lshlrev_b32 v72, 16, v168\n\t" \
    "v_and_b32 v73, 0xffff0000, v168\n\t" \
    "v_lshlrev_b32 v92, 16, v178\n\t" \
    "v_and_b32 v93, 0xffff0000, v178\n\t" \
    "v_lshlrev_b32 v74, 16, v169\n\t" \
    "v_and_b32 v75, 0xffff0000, v169\n\t" \
    "v_lshlrev_b32 v94, 16, v179\n\t" \
    "v_and_b32 v95, 0xffff0000, v179\n\t" \
    "v_lshlrev_b32 v76, 16, v170\n\t" \
    "v_and_b32 v77, 0xffff0000, v170\n\t" \
    "v_lshlrev_b32 v96, 16, v180\n\t" \
    "v_and_b32 v97, 0xffff0000, v180\n\t" \
    "v_lshlrev_b32 v78, 16, v171\n\t" \
    "v_and_b32 v79, 0xffff0000, v171\n\t" \
    "v_lshlrev_b32 v98, 16, v181\n\t" \
    "v_and_b32 v99, 0xffff0000, v181\n\t" \
    "v_lshlrev_b32 v80, 16, v172\n\t" \
    "v_and_b32 v81, 0xffff0000, v172\n\t" \
    "v_lshlrev_b32 v100, 16, v182\n\t" \
    "v_and_b32 v101, 0xffff0000, v182\n\t" \
    "v_lshlrev_b32 v82, 16, v173\n\t" \
    "v_and_b32 v83, 0xffff0000, v173\n\t" \
    "v_lshlrev_b32 v102, 16, v183\n\t" \
    "v_and_b32 v103, 0xffff0000, v183\n\t" \
    "v_pk_add_f32 v[64:65], v[64:65], v[84:85]\n\t" \
    "v_pk_add_f32 v[66:67], v[66:67], v[86:87]\n\t" \
    "v_pk_add_f32 v[68:69], v[68:69], v[88:89]\n\t" \
    "v_pk_add_f32 v[70:71], v[70:71], v[90:91]\n\t" \
    "v_pk_add_f32 v[72:73], v[72:73], v[92:93]\n\t" \
    "v_pk_add_f32 v[74:75], v[74:75], v[94:95]\n\t" \
    "v_pk_add_f32 v[76:77], v[76:77], v[96:97]\n\t" \
    "v_pk_add_f32 v[78:79], v[78:79], v[98:99]\n\t" \
    "v_pk_add_f32 v[80:81], v[80:81], v[100:101]\n\t" \
    "v_pk_add_f32 v[82:83], v[82:83], v[102:103]\n\t" \
    ".Lft0_%=:\n\t" \
    "ds_read_b32 v194, %[f0]\n\t" \
    "s_waitcnt lgkmcnt(0)\n\t" \
    "v_cmp_ge_u32 vcc, %[u], v194\n\t" \
    "s_cbranch_vccnz .Lfs0_%=\n\t" \
    ".Lfu0_%=:\n\t" \
    "ds_read_b128 v[84:87], %[la] offset:0\n\t" \
    "ds_read_b128 v[88:91], %[la] offset:6144\n\t" \
    "ds_read_b128 v[92:95], %[la] offset:12288\n\t" \
    "ds_read_b128 v[96:99], %[la] offset:18432\n\t" \
    "ds_read_b128 v[100:103], %[la] offset:24576\n\t" \
    "ds_read_b128 v[104:107], %[la] offset:1024\n\t" \
    "ds_read_b128 v[108:111], %[la] offset:7168\n\t" \
    "ds_read_b128 v[112:115], %[la] offset:13312\n\t" \
    "ds_read_b128 v[116:119], %[la] offset:19456\n\t" \
    "ds_read_b128 v[120:123], %[la] offset:25600\n\t" \
    "ds_read_b128 v[124:127], %[la] offset:2048\n\t" \
    "s_waitcnt lgkmcnt(10)\n\t" \
    "v_pk_add_f32 v[64:65], v[64:65], v[84:85]\n\t" \
    "v_pk_add_f32 v[66:67], v[66:67], v[86:87]\n\t" \
    "ds_read_b128 v[128:131], %[la] offset:8192\n\t" \
    "s_waitcnt lgkmcnt(10)\n\t" \
    "v_pk_add_f32 v[68:69], v[68:69], v[88:89]\n\t" \
    "v_pk_add_f32 v[70:71], v[70:71], v[90:91]\n\t" \
    "ds_read_b128 v[132:135], %[la] offset:14336\n\t" \
    "s_waitcnt lgkmcnt(10)\n\t" \
    "v_pk_add_f32 v[72:73], v[72:73], v[92:93]\n\t" \
    "v_pk_add_f32 v[74:75], v[74:75], v[94:95]\n\t" \
    "ds_read_b128 v[136:139], %[la] offset:20480\n\t" \
    "s_waitcnt lgkmcnt(10)\n\t" \
    "v_pk_add_f32 v[76:77], v[76:77], v[96:97]\n\t" \
    "v_pk_add_f32 v[78:79], v[78:79], v[98:99]\n\t" \
    "ds_read_b128 v[140:143], %[la] offset:26624\n\t" \
    "s_waitcnt lgkmcnt(10)\n\t" \
    "v_pk_add_f32 v[80:81], v[80:81], v[100:101]\n\t" \
    "v_pk_add_f32 v[82:83], v[82:83], v[102:103]\n\t" \
    "ds_read_b128 v[144:147], %[la] offset:3072\n\t" \
    "s_waitcnt lgkmcnt(10)\n\t" \
    "v_pk_add_f32 v[64:65], v[64:65], v[104:105]\n\t" \
    "v_pk_add_f32 v[66:67], v[66:67], v[106:107]\n\t" \
    "ds_read_b128 v[148:151], %[la] offset:9216\n\t" \
    "s_waitcnt lgkmcnt(10)\n\t" \
    "v_pk_add_f32 v[68:69], v[68:69], v[108:109]\n\t" \
    "v_pk_add_f32 v[70:71], v[70:71], v[110:111]\n\t" \
    "ds_read_b128 v[152:155], %[la] offset:15360\n\t" \
    "s_waitcnt lgkmcnt(10)\n\t" \
    "v_pk_add_f32 v[72:73], v[72:73], v[112:113]\n\t" \
    "v_pk_add_f32 v[74:75], v[74:75], v[114:115]\n\t" \
    "ds_read_b128 v[156:159], %[la] offset:21504\n\t" \
    "s_waitcnt lgkmcnt(10)\n\t" \
    "v_pk_add_f32 v[76:77], v[76:77], v[116:117]\n\t" \
    "v_pk_add_f32 v[78:79], v[78:79], v[118:119]\n\t" \
    "ds_read_b128 v[160:163], %[la] offset:27648\n\t" \
    "s_waitcnt lgkmcnt(10)\n\t" \
    "v_pk_add_f32 v[80:81], v[80:81], v[120:121]\n\t" \
    "v_pk_add_f32 v[82:83], v[82:83], v[122:123]\n\t" \
    "s_waitcnt lgkmcnt(9)\n\t" \
    "v_pk_add_f32 v[64:65], v[64:65], v[124:125]\n\t" \
    "v_pk_add_f32 v[66:67], v[66:67], v[126:127]\n\t" \
    "s_waitcnt lgkmcnt(8)\n\t" \
    "v_pk_add_f32 v[68:69], v[68:69], v[128:129]\n\t" \
    "v_pk_add_f32 v[70:71], v[70:71], v[130:131]\n\t" \
    "s_waitcnt lgkmcnt(7)\n\t" \
    "v_pk_add_f32 v[72:73], v[72:73], v[132:133]\n\t" \
    "v_pk_add_f32 v[74:75], v[74:75], v[134:135]\n\t" \
    "s_waitcnt lgkmcnt(6)\n\t" \
    "v_pk_add_f32 v[76:77], v[76:77], v[136:137]\n\t" \
    "v_pk_add_f32 v[78:79], v[78:79], v[138:139]\n\t" \
    "s_waitcnt lgkmcnt(5)\n\t" \
    "v_pk_add_f32 v[80:81], v[80:81], v[140:141]\n\t" \
    "v_pk_add_f32 v[82:83], v[82:83], v[142:143]\n\t" \
    "s_waitcnt lgkmcnt(4)\n\t" \
    "v_pk_add_f32 v[64:65], v[64:65], v[144:145]\n\t" \
    "v_pk_add_f32 v[66:67], v[66:67], v[146:147]\n\t" \
    "s_waitcnt lgkmcnt(3)\n\t" \
    "v_pk_add_f32 v[68:69], v[68:69], v[148:149]\n\t" \
    "v_pk_add_f32 v[70:71], v[70:71], v[150:151]\n\t" \
    "s_waitcnt lgkmcnt(2)\n\t" \
    "v_pk_add_f32 v[72:73], v[72:73], v[152:153]\n\t" \
    "v_pk_add_f32 v[74:75], v[74:75], v[154:155]\n\t" \
    "s_waitcnt lgkmcnt(1)\n\t" \
    "v_pk_add_f32 v[76:77], v[76:77], v[156:157]\n\t" \
    "v_pk_add_f32 v[78:79], v[78:79], v[158:159]\n\t" \
    "s_waitcnt lgkmcnt(0)\n\t" \
    "v_pk_add_f32 v[80:81], v[80:81], v[160:161]\n\t" \
    "v_pk_add_f32 v[82:83], v[82:83], v[162:163]\n\t" \
    ".Lft1_%=:\n\t" \
    "ds_read_b32 v194, %[f1]\n\t" \
    "s_waitcnt lgkmcnt(0)\n\t" \
    "v_cmp_ge_u32 vcc, %[u], v194\n\t" \
    "s_cbranch_vccnz .Lfs1_%=\n\t" \
    ".Lfu1_%=:\n\t" \
    ASM_ST2 \
    "ds_read_b128 v[84:87], %[la] offset:4096\n\t" \
    "ds_read_b128 v[88:91], %[la] offset:10240\n\t" \
    "ds_read_b128 v[92:95], %[la] offset:16384\n\t" \
    "ds_read_b128 v[96:99], %[la] offset:22528\n\t" \
    "ds_read_b128 v[100:103], %[la] offset:28672\n\t" \
    "ds_read_b128 v[104:107], %[la] offset:5120\n\t" \
    "ds_read_b128 v[108:111], %[la] offset:11264\n\t" \
    "ds_read_b128 v[112:115], %[la] offset:17408\n\t" \
    "ds_read_b128 v[116:119], %[la] offset:23552\n\t" \
    "ds_read_b128 v[120:123], %[la] offset:29696\n\t" \
    "v_mov_b32 v194, %[un]\n\t" \
    "ds_write_b32 %[fd], v194\n\t" \
    "s_waitcnt lgkmcnt(10)\n\t" \
    "v_pk_add_f32 v[64:65], v[64:65], v[84:85]\n\t" \
    "v_pk_add_f32 v[66:67], v[66:67], v[86:87]\n\t" \
    "s_waitcnt lgkmcnt(9)\n\t" \
    "v_pk_add_f32 v[68:69], v[68:69], v[88:89]\n\t" \
    "v_pk_add_f32 v[70:71], v[70:71], v[90:91]\n\t" \
    "s_waitcnt lgkmcnt(8)\n\t" \
    "v_pk_add_f32 v[72:73], v[72:73], v[92:93]\n\t" \
    "v_pk_add_f32 v[74:75], v[74:75], v[94:95]\n\t" \
    "s_waitcnt lgkmcnt(7)\n\t" \
    "v_pk_add_f32 v[76:77], v[76:77], v[96:97]\n\t" \
    "v_pk_add_f32 v[78:79], v[78:79], v[98:99]\n\t" \
    "s_waitcnt lgkmcnt(6)\n\t" \
    "v_pk_add_f32 v[80:81], v[80:81], v[100:101]\n\t" \
    "v_pk_add_f32 v[82:83], v[82:83], v[102:103]\n\t" \
    "s_waitcnt lgkmcnt(5)\n\t" \
    "v_pk_add_f32 v[64:65], v[64:65], v[104:105]\n\t" \
    "v_pk_add_f32 v[66:67], v[66:67], v[106:107]\n\t" \
    "s_waitcnt lgkmcnt(4)\n\t" \
    "v_pk_add_f32 v[68:69], v[68:69], v[108:109]\n\t" \
    "v_pk_add_f32 v[70:71], v[70:71], v[110:111]\n\t" \
    "s_waitcnt lgkmcnt(3)\n\t" \
    "v_pk_add_f32 v[72:73], v[72:73], v[112:113]\n\t" \
    "v_pk_add_f32 v[74:75], v[74:75], v[114:115]\n\t" \
    "s_waitcnt lgkmcnt(2)\n\t" \
    "v_pk_add_f32 v[76:77], v[76:77], v[116:117]\n\t" \
    "v_pk_add_f32 v[78:79], v[78:79], v[118:119]\n\t" \
    "s_waitcnt lgkmcnt(1)\n\t" \
    "v_pk_add_f32 v[80:81], v[80:81], v[120:121]\n\t" \
    "v_pk_add_f32 v[82:83], v[82:83], v[122:123]\n\t" \
    "v_cvt_pk_bf16_f32 v184, v64, v65\n\t" \
    "v_cvt_pk_bf16_f32 v185, v66, v67\n\t" \
    "v_cvt_pk_bf16_f32 v186, v68, v69\n\t" \
    "v_cvt_pk_bf16_f32 v187, v70, v71\n\t" \
    "v_cvt_pk_bf16_f32 v188, v72, v73\n\t" \
    "v_cvt_pk_bf16_f32 v189, v74, v75\n\t" \
    "v_cvt_pk_bf16_f32 v190, v76, v77\n\t" \
    "v_cvt_pk_bf16_f32 v191, v78, v79\n\t" \
    "v_cvt_pk_bf16_f32 v192, v80, v81\n\t" \
    "v_cvt_pk_bf16_f32 v193, v82, v83\n\t" \
    "v_permlane16_swap_b32 v184, v186\n\t" \
    "v_permlane16_swap_b32 v185, v187\n\t" \
    "v_permlane16_swap_b32 v188, v190\n\t" \
    "v_permlane16_swap_b32 v189, v191\n\t" \
    "buffer_store_dwordx4 v[184:187], %[pa], %[ro], %[so] offen nt\n\t" \
    "buffer_store_dwordx4 v[188:191], %[pa], %[ro], %[so] offen offset:64 nt\n\t" \
    "buffer_store_dwordx2 v[192:193], %[pb], %[ro], %[so] offen offset:128 nt\n\t" \
    "buffer_load_dwordx2 v[164:165], %[o0], %[rd], %[n1] offen sc1\n\t" \
    "buffer_load_dwordx2 v[166:167], %[o1], %[rd], %[n1] offen sc1\n\t" \
    "buffer_load_dwordx2 v[168:169], %[o2], %[rd], %[n1] offen sc1\n\t" \
    "buffer_load_dwordx2 v[170:171], %[o3], %[rd], %[n1] offen sc1\n\t" \
    "buffer_load_dwordx2 v[172:173], %[o4], %[rd], %[n1] offen sc1\n\t" \
    "buffer_load_dwordx2 v[174:175], %[o0], %[rd], %[na] offen sc1\n\t" \
    "buffer_load_dwordx2 v[176:177], %[o1], %[rd], %[na] offen sc1\n\t" \
    "buffer_load_dwordx2 v[178:179], %[o2], %[rd], %[na] offen sc1\n\t" \
    "buffer_load_dwordx2 v[180:181], %[o3], %[rd], %[na] offen sc1\n\t" \
    "buffer_load_dwordx2 v[182:183], %[o4], %[rd], %[na] offen sc1\n\t"
#define ASM_WCS \
    "v_mfma_f32_16x16x32_bf16 v[196:199], v[0:3], v[216:219], 0\n\t" \
    "v_mfma_f32_16x16x32_bf16 v[200:203], v[80:83], v[216:219], 0\n\t" \
    "v_mfma_f32_16x16x32_bf16 v[196:199], v[4:7], v[216:219], v[196:199]\n\t" \
    "v_mfma_f32_16x16x32_bf16 v[200:203], v[84:87], v[216:219], v[200:203]\n\t" \
    "v_mfma_f32_16x16x32_bf16 v[196:199], v[8:11], v[216:219], v[196:199]\n\t" \
    "v_mfma_f32_16x16x32_bf16 v[200:203], v[88:91], v[216:219], v[200:203]\n\t" \
    "v_mfma_f32_16x16x32_bf16 v[196:199], v[12:15], v[216:219], v[196:199]\n\t" \
    "v_mfma_f32_16x16x32_bf16 v[200:203], v[92:95], v[216:219], v[200:203]\n\t" \
    "v_mfma_f32_16x16x32_bf16 v[196:199], v[16:19], v[216:219], v[196:199]\n\t" \
    "v_mfma_f32_16x16x32_bf16 v[200:203], v[96:99], v[216:219], v[200:203]\n\t" \
    "v_mfma_f32_16x16x32_bf16 v[196:199], v[20:23], v[216:219], v[196:199]\n\t" \
    "v_mfma_f32_16x16x32_bf16 v[200:203], v[100:103], v[216:219], v[200:203]\n\t" \
    "v_mfma_f32_16x16x32_bf16 v[196:199], v[24:27], v[216:219], v[196:199]\n\t" \
    "v_mfma_f32_16x16x32_bf16 v[200:203], v[104:107], v[216:219], v[200:203]\n\t" \
    "v_mfma_f32_16x16x32_bf16 v[196:199], v[28:31], v[216:219], v[196:199]\n\t" \
    "v_mfma_f32_16x16x32_bf16 v[200:203], v[108:111], v[216:219], v[200:203]\n\t" \
    "v_mfma_f32_16x16x32_bf16 v[196:199], v[32:35], v[216:219], v[196:199]\n\t" \
    "v_mfma_f32_16x16x32_bf16 v[200:203], v[112:115], v[216:219], v[200:203]\n\t" \
    "v_mfma_f32_16x16x32_bf16 v[196:199], v[36:39], v[216:219], v[196:199]\n\t" \
    "v_mfma_f32_16x16x32_bf16 v[200:203], v[116:119], v[216:219], v[200:203]\n\t" \
    "v_mfma_f32_16x16x32_bf16 v[196:199], v[40:43], v[216:219], v[196:199]\n\t" \
    "v_mfma_f32_16x16x32_bf16 v[200:203], v[120:123], v[216:219], v[200:203]\n\t" \
    "v_mfma_f32_16x16x32_bf16 v[196:199], v[44:47], v[216:219], v[196:199]\n\t" \
    "v_mfma_f32_16x16x32_bf16 v[200:203], v[124:127], v[216:219], v[200:203]\n\t" \
    "v_mfma_f32_16x16x32_bf16 v[196:199], v[48:51], v[216:219], v[196:199]\n\t" \
    "v_mfma_f32_16x16x32_bf16 v[200:203], v[128:131], v[216:219], v[200:203]\n\t" \
    "v_mfma_f32_16x16x32_bf16 v[196:199], v[52:55], v[216:219], v[196:199]\n\t" \
    "v_mfma_f32_16x16x32_bf16 v[200:203], v[132:135], v[216:219], v[200:203]\n\t" \
    "v_mfma_f32_16x16x32_bf16 v[196:199], v[56:59], v[216:219], v[196:199]\n\t" \
    "v_mfma_f32_16x16x32_bf16 v[200:203], v[136:139], v[216:219], v[200:203]\n\t" \
    "v_mfma_f32_16x16x32_bf16 v[196:199], v[60:63], v[216:219], v[196:199]\n\t" \
    "v_mfma_f32_16x16x32_bf16 v[200:203], v[140:143], v[216:219], v[200:203]\n\t" \
    "v_mfma_f32_16x16x32_bf16 v[196:199], v[64:67], v[216:219], v[196:199]\n\t" \
    "v_mfma_f32_16x16x32_bf16 v[200:203], v[144:147], v[216:219], v[200:203]\n\t" \
    "v_mfma_f32_16x16x32_bf16 v[196:199], v[68:71], v[216:219], v[196:199]\n\t" \
    "v_mfma_f32_16x16x32_bf16 v[200:203], v[148:151], v[216:219], v[200:203]\n\t" \
    "v_mfma_f32_16x16x32_bf16 v[196:199], v[72:75], v[216:219], v[196:199]\n\t" \
    "v_mfma_f32_16x16x32_bf16 v[200:203], v[152:155], v[216:219], v[200:203]\n\t" \
    "v_mfma_f32_16x16x32_bf16 v[196:199], v[76:79], v[216:219], v[196:199]\n\t" \
    "v_mfma_f32_16x16x32_bf16 v[200:203], v[156:159], v[216:219], v[200:203]\n\t" \
    "s_nop 7\n\t"
#define ASM_WT1_LO \
    "s_branch .Lt0%=\n\t" \
    ".Ltr%=:\n\t" \
    "s_cmp_gt_i32 %[n], 0\n\t" \
    "s_cbranch_scc0 .Ltg%=\n\t" \
    ".Lfw%=:\n\t" \
    "s_mov_b32 %[a], 0\n\t" \
    "v_max_u32 v252, v232, v233\n\t" \
    "v_max3_u32 v252, v234, v235, v252\n\t" \
    "v_cmp_eq_u32 vcc, -1, v252\n\t" \
    "s_cbranch_vccz .Lgw0_%=\n\t" \
    "buffer_load_dwordx4 v[232:235], %[l16], %[rs], %[i0] offen offset:0 sc1\n\t" \
    "s_mov_b32 %[a], 1\n\t" \
    ".Lgw0_%=:\n\t" \
    "v_max_u32 v252, v236, v237\n\t" \
    "v_max3_u32 v252, v238, v239, v252\n\t" \
    "v_cmp_eq_u32 vcc, -1, v252\n\t" \
    "s_cbranch_vccz .Lgw1_%=\n\t" \
    "buffer_load_dwordx4 v[236:239], %[l16], %[rs], %[i0] offen offset:1024 sc1\n\t" \
    "s_mov_b32 %[a], 1\n\t" \
    ".Lgw1_%=:\n\t" \
    "v_max_u32 v252, v240, v241\n\t" \
    "v_max3_u32 v252, v242, v243, v252\n\t" \
    "v_cmp_eq_u32 vcc, -1, v252\n\t" \
    "s_cbranch_vccz .Lgw2_%=\n\t" \
    "buffer_load_dwordx4 v[240:243], %[l16], %[rs], %[i0] offen offset:2048 sc1\n\t" \
    "s_mov_b32 %[a], 1\n\t" \
    ".Lgw2_%=:\n\t" \
    "v_max_u32 v252, v244, v245\n\t" \
    "v_max3_u32 v252, v246, v247, v252\n\t" \
    "v_cmp_eq_u32 vcc, -1, v252\n\t" \
    "s_cbranch_vccz .Lgw3_%=\n\t" \
    "buffer_load_dwordx4 v[244:247], %[l16], %[rs], %[i0] offen offset:3072 sc1\n\t" \
    "s_mov_b32 %[a], 1\n\t" \
    ".Lgw3_%=:\n\t" \
    "v_max_u32 v252, v248, v249\n\t" \
    "v_max3_u32 v252, v250, v251, v252\n\t" \
    "v_cmp_eq_u32 vcc, -1, v252\n\t" \
    "s_cbranch_vccz .Lgw4_%=\n\t" \
    "buffer_load_dwordx4 v[248:251], %[l16], %[rs], %[i1] offen offset:0 sc1\n\t" \
    "s_mov_b32 %[a], 1\n\t" \
    ".Lgw4_%=:\n\t" \
    "s_cmp_eq_u32 %[a], 0\n\t" \
    "s_cbranch_scc1 .Lhw%=\n\t" \
    "s_waitcnt vmcnt(20)\n\t" \
    ".Liw%=:\n\t" \
    "v_max_u32 v252, v232, v233\n\t" \
    "v_max3_u32 v252, v234, v235, v252\n\t" \
    "v_max3_u32 v252, v236, v237, v252\n\t" \
    "v_max3_u32 v252, v238, v239, v252\n\t" \
    "v_max3_u32 v252, v240, v241, v252\n\t" \
    "v_max3_u32 v252, v242, v243, v252\n\t" \
    "v_max3_u32 v252, v244, v245, v252\n\t" \
    "v_max3_u32 v252, v246, v247, v252\n\t" \
    "v_max3_u32 v252, v248, v249, v252\n\t" \
    "v_max3_u32 v252, v250, v251, v252\n\t" \
    "v_cmp_eq_u32 vcc, -1, v252\n\t" \
    "s_cbranch_vccz .Lhw%=\n\t" \
    "s_sleep 1\n\t" \
    "v_max_u32 v252, v232, v233\n\t" \
    "v_max3_u32 v252, v234, v235, v252\n\t" \
    "v_max3_u32 v252, v236, v237, v252\n\t" \
    "v_max3_u32 v252, v238, v239, v252\n\t" \
    "v_max3_u32 v252, v240, v241, v252\n\t" \
    "v_max3_u32 v252, v242, v243, v252\n\t" \
    "v_max3_u32 v252, v244, v245, v252\n\t" \
    "v_max3_u32 v252, v246, v247, v252\n\t" \
    "v_max3_u32 v252, v248, v249, v252\n\t" \
    "v_max3_u32 v252, v250, v251, v252\n\t" \
    "v_cmp_eq_u32 vcc, -1, v252\n\t" \
    "s_cbranch_vccz .Lhw%=\n\t" \
    "s_sleep 1\n\t" \
    "v_max_u32 v252, v232, v233\n\t" \
    "v_max3_u32 v252, v234, v235, v252\n\t" \
    "v_max3_u32 v252, v236, v237, v252\n\t" \
    "v_max3_u32 v252, v238, v239, v252\n\t" \
    "v_max3_u32 v252, v240, v241, v252\n\t" \
    "v_max3_u32 v252, v242, v243, v252\n\t" \
    "v_max3_u32 v252, v244, v245, v252\n\t" \
    "v_max3_u32 v252, v246, v247, v252\n\t" \
    "v_max3_u32 v252, v248, v249, v252\n\t" \
    "v_max3_u32 v252, v250, v251, v252\n\t" \
    "v_cmp_eq_u32 vcc, -1, v252\n\t" \
    "s_cbranch_vccz .Lhw%=\n\t" \
    "s_sleep 1\n\t" \
    "v_max_u32 v252, v232, v233\n\t" \
    "v_max3_u32 v252, v234, v235, v252\n\t" \
    "v_max3_u32 v252, v236, v237, v252\n\t" \
    "v_max3_u32 v252, v238, v239, v252\n\t" \
    "v_max3_u32 v252, v240, v241, v252\n\t" \
    "v_max3_u32 v252, v242, v243, v252\n\t" \
    "v_max3_u32 v252, v244, v245, v252\n\t" \
    "v_max3_u32 v252, v246, v247, v252\n\t" \
    "v_max3_u32 v252, v248, v249, v252\n\t" \
    "v_max3_u32 v252, v250, v251, v252\n\t" \
    "v_cmp_eq_u32 vcc, -1, v252\n\t" \
    "s_cbranch_vccz .Lhw%=\n\t" \
    "s_sleep 1\n\t" \
    "v_max_u32 v252, v232, v233\n\t" \
    "v_max3_u32 v252, v234, v235, v252\n\t" \
    "v_max3_u32 v252, v236, v237, v252\n\t" \
    "v_max3_u32 v252, v238, v239, v252\n\t" \
    "v_max3_u32 v252, v240, v241, v252\n\t" \
    "v_max3_u32 v252, v242, v243, v252\n\t" \
    "v_max3_u32 v252, v244, v245, v252\n\t" \
    "v_max3_u32 v252, v246, v247, v252\n\t" \
    "v_max3_u32 v252, v248, v249, v252\n\t" \
    "v_max3_u32 v252, v250, v251, v252\n\t" \
    "v_cmp_eq_u32 vcc, -1, v252\n\t" \
    "s_cbranch_vccz .Lhw%=\n\t" \
    "s_sleep 1\n\t" \
    "v_max_u32 v252, v232, v233\n\t" \
    "v_max3_u32 v252, v234, v235, v252\n\t" \
    "v_max3_u32 v252, v236, v237, v252\n\t" \
    "v_max3_u32 v252, v238, v239, v252\n\t" \
    "v_max3_u32 v252, v240, v241, v252\n\t" \
    "v_max3_u32 v252, v242, v243, v252\n\t" \
    "v_max3_u32 v252, v244, v245, v252\n\t" \
    "v_max3_u32 v252, v246, v247, v252\n\t" \
    "v_max3_u32 v252, v248, v249, v252\n\t" \
    "v_max3_u32 v252, v250, v251, v252\n\t" \
    "v_cmp_eq_u32 vcc, -1, v252\n\t" \
    "s_cbranch_vccz .Lhw%=\n\t" \
    "s_sleep 1\n\t" \
    "s_sub_u32 %[n], %[n], 1\n\t" \
    "s_cmp_gt_i32 %[n], 0\n\t" \
    "s_cbranch_scc1 .Lfw%=\n\t" \
    ".Lhw%=:\n\t" \
    "s_branch .Lta%=\n\t" \
    ".Lt0%=:\n\t" \
    "s_waitcnt vmcnt(1)\n\t" \
    ".Lta%=:\n\t" \
    "ds_write_b128 %[lw], v[232:235] offset:0\n\t" \
    "v_max_u32 v252, v232, v233\n\t" \
    "v_max3_u32 v252, v234, v235, v252\n\t" \
    "v_mfma_f32_16x16x32_bf16 v[188:191], v[232:235], v[232:235], 0\n\t" \
    "ds_write_b128 %[lw], v[236:239] offset:1024\n\t" \
    "v_max3_u32 v252, v236, v237, v252\n\t" \
    "v_max3_u32 v252, v238, v239, v252\n\t" \
    "v_mfma_f32_16x16x32_bf16 v[188:191], v[236:239], v[236:239], v[188:191]\n\t" \
    "ds_write_b128 %[lw], v[240:243] offset:2048\n\t" \
    "v_max3_u32 v252, v240, v241, v252\n\t" \
    "v_max3_u32 v252, v242, v243, v252\n\t" \
    "v_mfma_f32_16x16x32_bf16 v[188:191], v[240:243], v[240:243], v[188:191]\n\t" \
    "ds_write_b128 %[lw], v[244:247] offset:3072\n\t" \
    "v_max3_u32 v252, v244, v245, v252\n\t" \
    "v_max3_u32 v252, v246, v247, v252\n\t" \
    "v_mfma_f32_16x16x32_bf16 v[188:191], v[244:247], v[244:247], v[188:191]\n\t" \
    "ds_write_b128 %[lw], v[248:251] offset:4096\n\t" \
    "v_max3_u32 v252, v248, v249, v252\n\t" \
    "v_max3_u32 v252, v250, v251, v252\n\t" \
    "v_mfma_f32_16x16x32_bf16 v[188:191], v[248:251], v[248:251], v[188:191]\n\t" \
    "v_mfma_f32_16x16x32_bf16 v[192:195], v[216:219], v[232:235], 0\n\t" \
    "v_mfma_f32_16x16x32_bf16 v[192:195], v[216:219], v[236:239], v[192:195]\n\t" \
    "v_mfma_f32_16x16x32_bf16 v[192:195], v[216:219], v[240:243], v[192:195]\n\t" \
    "v_mfma_f32_16x16x32_bf16 v[192:195], v[216:219], v[244:247], v[192:195]\n\t" \
    "v_mfma_f32_16x16x32_bf16 v[192:195], v[216:219], v[248:251], v[192:195]\n\t" \
    "v_cmp_eq_u32 vcc, -1, v252\n\t" \
    "s_cbranch_vccnz .Ltr%=\n\t" \
    ".Ltg%=:\n\t" \
    ASM_ST2 \
    "s_nop 5\n\t" \
    "ds_write_b128 %[lw], v[188:191] offset:5120\n\t" \
    "ds_write_b128 %[lw], v[192:195] offset:6144\n\t" \
    "s_waitcnt lgkmcnt(0)\n\t" \
    "s_setprio 2\n\t" \
    "s_barrier\n\t" ASM_ST \
    "ds_read_b128 v[160:163], %[la] offset:0\n\t" \
    "ds_read_b128 v[164:167], %[la] offset:1024\n\t" \
    "ds_read_b128 v[168:171], %[la] offset:2048\n\t" \
    "ds_read2st64_b32 v[204:205], %[sr] offset0:20 offset1:24\n\t" \
    "ds_read_b128 v[172:175], %[la] offset:3072\n\t" \
    "ds_read_b128 v[176:179], %[la] offset:8192\n\t" \
    "ds_read_b128 v[180:183], %[la] offset:9216\n\t" \
    "ds_read_b128 v[184:187], %[la] offset:10240\n\t" \
    "s_waitcnt lgkmcnt(7)\n\t" \
    "v_mfma_f32_16x16x32_bf16 v[192:195], v[80:83], v[160:163], 0\n\t" \
    "s_waitcnt lgkmcnt(6)\n\t" \
    "v_mfma_f32_16x16x32_bf16 v[192:195], v[84:87], v[164:167], v[192:195]\n\t" \
    "v_mfma_f32_16x16x32_bf16 v[188:191], v[0:3], v[160:163], 0\n\t" \
    "ds_read_b128 v[160:163], %[la] offset:11264\n\t" \
    "v_mfma_f32_16x16x32_bf16 v[188:191], v[4:7], v[164:167], v[188:191]\n\t" \
    "ds_read_b128 v[164:167], %[la] offset:16384\n\t" \
    "s_waitcnt lgkmcnt(7)\n\t" \
    "v_mfma_f32_16x16x32_bf16 v[188:191], v[8:11], v[168:171], v[188:191]\n\t" \
    "s_waitcnt lgkmcnt(6)\n\t" \
    "s_waitcnt lgkmcnt(5)\n\t" \
    "v_mfma_f32_16x16x32_bf16 v[188:191], v[12:15], v[172:175], v[188:191]\n\t" \
    "v_permlane16_swap_b32 v204, v205\n\t" \
    "v_mfma_f32_16x16x32_bf16 v[192:195], v[88:91], v[168:171], v[192:195]\n\t" \
    "ds_read_b128 v[168:171], %[la] offset:17408\n\t" \
    "v_mfma_f32_16x16x32_bf16 v[192:195], v[92:95], v[172:175], v[192:195]\n\t" \
    "ds_read_b128 v[172:175], %[la] offset:18432\n\t" \
    "v_add_f32 v206, v204, v205\n\t" \
    "s_waitcnt lgkmcnt(6)\n\t" \
    "v_mfma_f32_16x16x32_bf16 v[192:195], v[96:99], v[176:179], v[192:195]\n\t" \
    "v_add_f32 v207, v204, v205\n\t" \
    "s_waitcnt lgkmcnt(5)\n\t" \
    "v_mfma_f32_16x16x32_bf16 v[192:195], v[100:103], v[180:183], v[192:195]\n\t" \
    "v_mfma_f32_16x16x32_bf16 v[188:191], v[16:19], v[176:179], v[188:191]\n\t" \
    "ds_read_b128 v[176:179], %[la] offset:19456\n\t" \
    "v_permlane32_swap_b32 v206, v207\n\t" \
    "v_mfma_f32_16x16x32_bf16 v[188:191], v[20:23], v[180:183], v[188:191]\n\t" \
    "ds_read_b128 v[180:183], %[la] offset:24576\n\t" \
    "s_waitcnt lgkmcnt(6)\n\t" \
    "v_mfma_f32_16x16x32_bf16 v[188:191], v[24:27], v[184:187], v[188:191]\n\t" \
    "v_add_f32 v204, v206, v207\n\t" \
    "s_waitcnt lgkmcnt(5)\n\t" \
    "v_mfma_f32_16x16x32_bf16 v[188:191], v[28:31], v[160:163], v[188:191]\n\t" \
    "v_add_f32 v205, v206, v207\n\t" \
    "v_mfma_f32_16x16x32_bf16 v[192:195], v[104:107], v[184:187], v[192:195]\n\t" \
    "ds_read_b128 v[184:187], %[la] offset:25600\n\t" \
    "v_mfma_f32_16x16x32_bf16 v[192:195], v[108:111], v[160:163], v[192:195]\n\t" \
    "ds_read_b128 v[160:163], %[la] offset:26624\n\t" \
    "v_permlane16_swap_b32 v204, v205\n\t" \
    "s_waitcnt lgkmcnt(6)\n\t" \
    "v_mfma_f32_16x16x32_bf16 v[192:195], v[112:115], v[164:167], v[192:195]\n\t" \
    "s_waitcnt lgkmcnt(5)\n\t" \
    "v_mfma_f32_16x16x32_bf16 v[192:195], v[116:119], v[168:171], v[192:195]\n\t" \
    "v_mul_f32 v205, %[kd], v205\n\t" \
    "v_mfma_f32_16x16x32_bf16 v[188:191], v[32:35], v[164:167], v[188:191]\n\t" \
    "ds_read_b128 v[164:167], %[la] offset:27648\n\t" \
    "v_mul_f32 v204, %[kd], v204\n\t" \
    "v_mfma_f32_16x16x32_bf16 v[188:191], v[36:39], v[168:171], v[188:191]\n\t" \
    "ds_read_b128 v[168:171], %[la] offset:4096\n\t" \
    "v_fma_f32 v204, -v205, v205, v204\n\t" \
    "s_waitcnt lgkmcnt(6)\n\t" \
    "v_mfma_f32_16x16x32_bf16 v[188:191], v[40:43], v[172:175], v[188:191]\n\t" \
    "v_max_f32 v204, 0, v204\n\t" \
    "s_waitcnt lgkmcnt(5)\n\t" \
    "v_mfma_f32_16x16x32_bf16 v[188:191], v[44:47], v[176:179], v[188:191]\n\t" \
    "v_add_f32 v206, %[ke], v204\n\t" \
    "v_mfma_f32_16x16x32_bf16 v[192:195], v[120:123], v[172:175], v[192:195]\n\t" \
    "ds_read_b128 v[172:175], %[la] offset:12288\n\t" \
    "v_rsq_f32 v206, v206\n\t" \
    "v_mfma_f32_16x16x32_bf16 v[192:195], v[124:127], v[176:179], v[192:195]\n\t" \
    "ds_read_b128 v[176:179], %[la] offset:20480\n\t" \
    "s_waitcnt lgkmcnt(6)\n\t" \
    "v_mfma_f32_16x16x32_bf16 v[192:195], v[128:131], v[180:183], v[192:195]\n\t" \
    "v_mul_f32 v207, v204, v206\n\t" \
    "s_waitcnt lgkmcnt(5)\n\t" \
    "v_mfma_f32_16x16x32_bf16 v[192:195], v[132:135], v[184:187], v[192:195]\n\t" \
    "v_fma_f32 v207, v207, v206, %[ke]\n\t" \
    "v_mfma_f32_16x16x32_bf16 v[188:191], v[48:51], v[180:183], v[188:191]\n\t" \
    "ds_read_b128 v[180:183], %[la] offset:28672\n\t" \
    "v_rsq_f32 v207, v207\n\t" \
    "v_mfma_f32_16x16x32_bf16 v[188:191], v[52:55], v[184:187], v[188:191]\n\t" \
    "v_mul_f32 v206, v206, v207\n\t" \
    "v_mul_f32 v207, -v205, v206\n\t" \
    "s_waitcnt lgkmcnt(5)\n\t" \
    "v_mfma_f32_16x16x32_bf16 v[188:191], v[56:59], v[160:163], v[188:191]\n\t" \
    "v_mul_f32 v208, v196, v207\n\t" \
    "v_mul_f32 v209, v197, v207\n\t" \
    "s_waitcnt lgkmcnt(4)\n\t" \
    "v_mfma_f32_16x16x32_bf16 v[188:191], v[60:63], v[164:167], v[188:191]\n\t" \
    "v_mul_f32 v210, v198, v207\n\t" \
    "v_mul_f32 v211, v199, v207\n\t" \
    "v_mfma_f32_16x16x32_bf16 v[192:195], v[136:139], v[160:163], v[192:195]\n\t" \
    "v_mul_f32 v212, v200, v207\n\t" \
    "v_mul_f32 v213, v201, v207\n\t" \
    "v_mfma_f32_16x16x32_bf16 v[192:195], v[140:143], v[164:167], v[192:195]\n\t" \
    "v_mul_f32 v214, v202, v207\n\t" \
    "v_mul_f32 v215, v203, v207\n\t" \
    "s_waitcnt lgkmcnt(3)\n\t" \
    "v_mfma_f32_16x16x32_bf16 v[192:195], v[144:147], v[168:171], v[192:195]\n\t" \
    "s_cmp_lg_u32 %[sw], 0\n\t" \
    "s_cbranch_scc1 .Lts%=\n\t" \
    "v_cvt_pk_bf16_f32 v217, v204, v204\n\t" \
    "v_lshlrev_b32 v217, 16, v217\n\t" \
    "v_add_f32 v217, %[ke], v217\n\t" \
    "v_cvt_pk_bf16_f32 v217, v217, v217\n\t" \
    "v_lshlrev_b32 v217, 16, v217\n\t" \
    "v_sqrt_f32 v217, v217\n\t" \
    "v_cvt_pk_bf16_f32 v216, v205, v205\n\t" \
    "v_lshlrev_b32 v216, 16, v216\n\t" \
    "v_cvt_pk_bf16_f32 v217, v217, v217\n\t" \
    "v_lshlrev_b32 v217, 16, v217\n\t" \
    "v_rcp_f32 v217, v217\n\t" \
    "v_lshrrev_b32 v218, 1, %[l16]\n\t" \
    "v_and_b32 v218, 0x78, v218\n\t" \
    "buffer_store_dwordx2 v[216:217], v218, %[rs], %[ss] offen sc1\n\t" \
    ".Lts%=:\n\t" \
    "v_mov_b64 v[216:217], -1\n\t" \
    "s_waitcnt lgkmcnt(2)\n\t" \
    "v_mfma_f32_16x16x32_bf16 v[192:195], v[148:151], v[172:175], v[192:195]\n\t" \
    "v_mov_b64 v[218:219], -1\n\t" \
    "buffer_store_dwordx4 v[216:219], %[ob], %[rs], %[sa] offen sc1\n\t" \
    "v_mfma_f32_16x16x32_bf16 v[188:191], v[64:67], v[168:171], v[188:191]\n\t" \
    "v_mfma_f32_16x16x32_bf16 v[188:191], v[68:71], v[172:175], v[188:191]\n\t" \
    "s_waitcnt lgkmcnt(1)\n\t" \
    "v_mfma_f32_16x16x32_bf16 v[188:191], v[72:75], v[176:179], v[188:191]\n\t" \
    "s_waitcnt lgkmcnt(0)\n\t" \
    "v_mfma_f32_16x16x32_bf16 v[188:191], v[76:79], v[180:183], v[188:191]\n\t" \
    "v_mfma_f32_16x16x32_bf16 v[192:195], v[152:155], v[176:179], v[192:195]\n\t" \
    "v_mfma_f32_16x16x32_bf16 v[192:195], v[156:159], v[180:183], v[192:195]\n\t" \
    "s_setprio 2\n\t" \
    "buffer_load_dwordx4 v[232:235], %[l16], %[rs], %[n0] offen offset:0 sc1\n\t" \
    "buffer_load_dwordx4 v[236:239], %[l16], %[rs], %[n0] offen offset:1024 sc1\n\t" \
    "buffer_load_dwordx4 v[240:243], %[l16], %[rs], %[n0] offen offset:2048 sc1\n\t" \
    "buffer_load_dwordx4 v[244:247], %[l16], %[rs], %[n0] offen offset:3072 sc1\n\t" \
    "buffer_load_dwordx4 v[248:251], %[l16], %[rs], %[n1] offen offset:0 sc1\n\t" \
    ASM_ST3 \
    "v_fma_f32 v188, v188, v206, v208\n\t" \
    "v_fma_f32 v189, v189, v206, v209\n\t" \
    "v_fma_f32 v190, v190, v206, v210\n\t" \
    "v_fma_f32 v191, v191, v206, v211\n\t" \
    "v_mul_f32 v160, v188, v188\n\t" \
    "v_mul_f32 v161, v189, v189\n\t" \
    "v_mul_f32 v162, v190, v190\n\t" \
    "v_mul_f32 v163, v191, v191\n\t" \
    "v_fma_f32 v160, v160, %[k1], v220\n\t" \
    "v_fma_f32 v161, v161, %[k1], v220\n\t" \
    "v_fma_f32 v162, v162, %[k1], v220\n\t" \
    "v_fma_f32 v163, v163, %[k1], v220\n\t" \
    "v_mul_f32 v160, v188, v160\n\t" \
    "v_mul_f32 v161, v189, v161\n\t" \
    "v_mul_f32 v162, v190, v162\n\t" \
    "v_mul_f32 v163, v191, v163\n\t" \
    "v_fma_f32 v192, v192, v206, v212\n\t" \
    "v_fma_f32 v193, v193, v206, v213\n\t" \
    "v_fma_f32 v194, v194, v206, v214\n\t" \
    "v_fma_f32 v195, v195, v206, v215\n\t" \
    "v_mul_f32 v164, v192, v192\n\t" \
    "v_mul_f32 v165, v193, v193\n\t" \
    "v_mul_f32 v166, v194, v194\n\t" \
    "v_mul_f32 v167, v195, v195\n\t" \
    "v_fma_f32 v164, v164, %[k1], v220\n\t" \
    "v_fma_f32 v165, v165, %[k1], v220\n\t" \
    "v_fma_f32 v166, v166, %[k1], v220\n\t" \
    "v_fma_f32 v167, v167, %[k1], v220\n\t" \
    "v_mul_f32 v164, v192, v164\n\t" \
    "v_mul_f32 v165, v193, v165\n\t" \
    "v_mul_f32 v166, v194, v166\n\t" \
    "v_mul_f32 v167, v195, v167\n\t" \
    "v_exp_f32 v160, v160\n\t" \
    "v_exp_f32 v161, v161\n\t" \
    "v_exp_f32 v162, v162\n\t" \
    "v_exp_f32 v163, v163\n\t" \
    "v_exp_f32 v164, v164\n\t" \
    "v_exp_f32 v165, v165\n\t" \
    "v_exp_f32 v166, v166\n\t" \
    "v_exp_f32 v167, v167\n\t" \
    "v_add_f32 v160, 1.0, v160\n\t" \
    "v_add_f32 v161, 1.0, v161\n\t" \
    "v_add_f32 v162, 1.0, v162\n\t" \
    "v_add_f32 v163, 1.0, v163\n\t" \
    "v_add_f32 v164, 1.0, v164\n\t" \
    "v_add_f32 v165, 1.0, v165\n\t" \
    "v_add_f32 v166, 1.0, v166\n\t" \
    "v_add_f32 v167, 1.0, v167\n\t" \
    "v_rcp_f32 v160, v160\n\t" \
    "v_rcp_f32 v161, v161\n\t" \
    "v_rcp_f32 v162, v162\n\t" \
    "v_rcp_f32 v163, v163\n\t" \
    "v_rcp_f32 v164, v164\n\t" \
    "v_rcp_f32 v165, v165\n\t" \
    "v_rcp_f32 v166, v166\n\t" \
    "v_rcp_f32 v167, v167\n\t" \
    "v_mul_f32 v188, v188, v160\n\t" \
    "v_mul_f32 v189, v189, v161\n\t" \
    "v_mul_f32 v190, v190, v162\n\t" \
    "v_mul_f32 v191, v191, v163\n\t" \
    "v_mul_f32 v192, v192, v164\n\t" \
    "v_mul_f32 v193, v193, v165\n\t" \
    "v_mul_f32 v194, v194, v166\n\t" \
    "v_mul_f32 v195, v195, v167\n\t" \
    "v_cvt_pk_bf16_f32 v188, v188, v189\n\t" \
    "v_cvt_pk_bf16_f32 v189, v190, v191\n\t" \
    "v_cvt_pk_bf16_f32 v190, v192, v193\n\t" \
    "v_cvt_pk_bf16_f32 v191, v194, v195\n\t" \
    "v_min_u32 v188, -2, v188\n\t" \
    "v_min_u32 v189, -2, v189\n\t" \
    "v_min_u32 v190, -2, v190\n\t" \
    "v_min_u32 v191, -2, v191\n\t" \
    "v_mov_b32 v216, 0x3f803f80\n\t" \
    "v_mov_b32 v217, 0x3f803f80\n\t" \
    "v_mov_b32 v218, 0x3f803f80\n\t" \
    "v_mov_b32 v219, 0x3f803f80\n\t" \
    "v_permlane16_swap_b32 v188, v190\n\t" \
    "v_permlane16_swap_b32 v189, v191\n\t" \
    "s_waitcnt vmcnt(6)\n\t" \
    "buffer_store_dwordx4 v[188:191], %[oa], %[rs], %[so] offen sc1\n\t" \
    "s_nop 1\n\t" \
    "s_setprio 0\n\t"
#define ASM_WT1_HI \
    "s_setprio 1\n\t" \
    "s_barrier\n\t" ASM_ST \
    "ds_read_b128 v[160:163], %[la] offset:0\n\t" \
    "ds_read_b128 v[164:167], %[la] offset:1024\n\t" \
    "ds_read_b128 v[168:171], %[la] offset:2048\n\t" \
    "ds_read2st64_b32 v[204:205], %[sr] offset0:20 offset1:24\n\t" \
    "ds_read_b128 v[172:175], %[la] offset:3072\n\t" \
    "ds_read_b128 v[176:179], %[la] offset:8192\n\t" \
    "ds_read_b128 v[180:183], %[la] offset:9216\n\t" \
    "ds_read_b128 v[184:187], %[la] offset:10240\n\t" \
    "s_waitcnt lgkmcnt(7)\n\t" \
    "v_mfma_f32_16x16x32_bf16 v[192:195], v[80:83], v[160:163], 0\n\t" \
    "s_waitcnt lgkmcnt(6)\n\t" \
    "v_mfma_f32_16x16x32_bf16 v[192:195], v[84:87], v[164:167], v[192:195]\n\t" \
    "v_mfma_f32_16x16x32_bf16 v[188:191], v[0:3], v[160:163], 0\n\t" \
    "ds_read_b128 v[160:163], %[la] offset:11264\n\t" \
    "v_mfma_f32_16x16x32_bf16 v[188:191], v[4:7], v[164:167], v[188:191]\n\t" \
    "ds_read_b128 v[164:167], %[la] offset:16384\n\t" \
    "s_waitcnt lgkmcnt(7)\n\t" \
    "v_mfma_f32_16x16x32_bf16 v[188:191], v[8:11], v[168:171], v[188:191]\n\t" \
    "s_waitcnt lgkmcnt(6)\n\t" \
    "s_waitcnt lgkmcnt(5)\n\t" \
    "v_mfma_f32_16x16x32_bf16 v[188:191], v[12:15], v[172:175], v[188:191]\n\t" \
    "v_permlane16_swap_b32 v204, v205\n\t" \
    "v_mfma_f32_16x16x32_bf16 v[192:195], v[88:91], v[168:171], v[192:195]\n\t" \
    "ds_read_b128 v[168:171], %[la] offset:17408\n\t" \
    "v_mfma_f32_16x16x32_bf16 v[192:195], v[92:95], v[172:175], v[192:195]\n\t" \
    "ds_read_b128 v[172:175], %[la] offset:18432\n\t" \
    "v_add_f32 v206, v204, v205\n\t" \
    "s_waitcnt lgkmcnt(6)\n\t" \
    "v_mfma_f32_16x16x32_bf16 v[192:195], v[96:99], v[176:179], v[192:195]\n\t" \
    "v_add_f32 v207, v204, v205\n\t" \
    "s_waitcnt lgkmcnt(5)\n\t" \
    "v_mfma_f32_16x16x32_bf16 v[192:195], v[100:103], v[180:183], v[192:195]\n\t" \
    "v_mfma_f32_16x16x32_bf16 v[188:191], v[16:19], v[176:179], v[188:191]\n\t" \
    "ds_read_b128 v[176:179], %[la] offset:19456\n\t" \
    "v_permlane32_swap_b32 v206, v207\n\t" \
    "v_mfma_f32_16x16x32_bf16 v[188:191], v[20:23], v[180:183], v[188:191]\n\t" \
    "ds_read_b128 v[180:183], %[la] offset:24576\n\t" \
    "s_waitcnt lgkmcnt(6)\n\t" \
    "v_mfma_f32_16x16x32_bf16 v[188:191], v[24:27], v[184:187], v[188:191]\n\t" \
    "v_add_f32 v204, v206, v207\n\t" \
    "s_waitcnt lgkmcnt(5)\n\t" \
    "v_mfma_f32_16x16x32_bf16 v[188:191], v[28:31], v[160:163], v[188:191]\n\t" \
    "v_add_f32 v205, v206, v207\n\t" \
    "v_mfma_f32_16x16x32_bf16 v[192:195], v[104:107], v[184:187], v[192:195]\n\t" \
    "ds_read_b128 v[184:187], %[la] offset:25600\n\t" \
    "v_mfma_f32_16x16x32_bf16 v[192:195], v[108:111], v[160:163], v[192:195]\n\t" \
    "ds_read_b128 v[160:163], %[la] offset:26624\n\t" \
    "v_permlane16_swap_b32 v204, v205\n\t" \
    "s_waitcnt lgkmcnt(6)\n\t" \
    "v_mfma_f32_16x16x32_bf16 v[192:195], v[112:115], v[164:167], v[192:195]\n\t" \
    "s_waitcnt lgkmcnt(5)\n\t" \
    "v_mfma_f32_16x16x32_bf16 v[192:195], v[116:119], v[168:171], v[192:195]\n\t" \
    "v_mul_f32 v205, %[kd], v205\n\t" \
    "v_mfma_f32_16x16x32_bf16 v[188:191], v[32:35], v[164:167], v[188:191]\n\t" \
    "ds_read_b128 v[164:167], %[la] offset:27648\n\t" \
    "v_mul_f32 v204, %[kd], v204\n\t" \
    "v_mfma_f32_16x16x32_bf16 v[188:191], v[36:39], v[168:171], v[188:191]\n\t" \
    "ds_read_b128 v[168:171], %[la] offset:4096\n\t" \
    "v_fma_f32 v204, -v205, v205, v204\n\t" \
    "s_waitcnt lgkmcnt(6)\n\t" \
    "v_mfma_f32_16x16x32_bf16 v[188:191], v[40:43], v[172:175], v[188:191]\n\t" \
    "v_max_f32 v204, 0, v204\n\t" \
    "s_waitcnt lgkmcnt(5)\n\t" \
    "v_mfma_f32_16x16x32_bf16 v[188:191], v[44:47], v[176:179], v[188:191]\n\t" \
    "v_add_f32 v206, %[ke], v204\n\t" \
    "v_mfma_f32_16x16x32_bf16 v[192:195], v[120:123], v[172:175], v[192:195]\n\t" \
    "ds_read_b128 v[172:175], %[la] offset:12288\n\t" \
    "v_rsq_f32 v206, v206\n\t" \
    "v_mfma_f32_16x16x32_bf16 v[192:195], v[124:127], v[176:179], v[192:195]\n\t" \
    "ds_read_b128 v[176:179], %[la] offset:20480\n\t" \
    "s_waitcnt lgkmcnt(6)\n\t" \
    "v_mfma_f32_16x16x32_bf16 v[192:195], v[128:131], v[180:183], v[192:195]\n\t" \
    "v_mul_f32 v207, v204, v206\n\t" \
    "s_waitcnt lgkmcnt(5)\n\t" \
    "v_mfma_f32_16x16x32_bf16 v[192:195], v[132:135], v[184:187], v[192:195]\n\t" \
    "v_fma_f32 v207, v207, v206, %[ke]\n\t" \
    "v_mfma_f32_16x16x32_bf16 v[188:191], v[48:51], v[180:183], v[188:191]\n\t" \
    "ds_read_b128 v[180:183], %[la] offset:28672\n\t" \
    "v_rsq_f32 v207, v207\n\t" \
    "v_mfma_f32_16x16x32_bf16 v[188:191], v[52:55], v[184:187], v[188:191]\n\t" \
    "v_mul_f32 v206, v206, v207\n\t" \
    "v_mul_f32 v207, -v205, v206\n\t" \
    "s_waitcnt lgkmcnt(5)\n\t" \
    "v_mfma_f32_16x16x32_bf16 v[188:191], v[56:59], v[160:163], v[188:191]\n\t" \
    "v_mul_f32 v208, v196, v207\n\t" \
    "v_mul_f32 v209, v197, v207\n\t" \
    "s_waitcnt lgkmcnt(4)\n\t" \
    "v_mfma_f32_16x16x32_bf16 v[188:191], v[60:63], v[164:167], v[188:191]\n\t" \
    "v_mul_f32 v210, v198, v207\n\t" \
    "v_mul_f32 v211, v199, v207\n\t" \
    "v_mfma_f32_16x16x32_bf16 v[192:195], v[136:139], v[160:163], v[192:195]\n\t" \
    "v_mul_f32 v212, v200, v207\n\t" \
    "v_mul_f32 v213, v201, v207\n\t" \
    "v_mfma_f32_16x16x32_bf16 v[192:195], v[140:143], v[164:167], v[192:195]\n\t" \
    "v_mul_f32 v214, v202, v207\n\t" \
    "v_mul_f32 v215, v203, v207\n\t" \
    "s_waitcnt lgkmcnt(3)\n\t" \
    "v_mfma_f32_16x16x32_bf16 v[192:195], v[144:147], v[168:171], v[192:195]\n\t" \
    "v_mov_b64 v[216:217], -1\n\t" \
    "v_mov_b64 v[218:219], -1\n\t" \
    "s_waitcnt lgkmcnt(2)\n\t" \
    "v_mfma_f32_16x16x32_bf16 v[192:195], v[148:151], v[172:175], v[192:195]\n\t" \
    "buffer_store_dwordx4 v[216:219], %[ob], %[rs], %[sa] offen sc1\n\t" \
    "v_mfma_f32_16x16x32_bf16 v[188:191], v[64:67], v[168:171], v[188:191]\n\t" \
    "v_mfma_f32_16x16x32_bf16 v[188:191], v[68:71], v[172:175], v[188:191]\n\t" \
    "s_waitcnt lgkmcnt(1)\n\t" \
    "v_mfma_f32_16x16x32_bf16 v[188:191], v[72:75], v[176:179], v[188:191]\n\t" \
    "s_waitcnt lgkmcnt(0)\n\t" \
    "v_mfma_f32_16x16x32_bf16 v[188:191], v[76:79], v[180:183], v[188:191]\n\t" \
    "v_mfma_f32_16x16x32_bf16 v[192:195], v[152:155], v[176:179], v[192:195]\n\t" \
    "v_mfma_f32_16x16x32_bf16 v[192:195], v[156:159], v[180:183], v[192:195]\n\t" \
    ASM_ST3 \
    "s_nop 1\n\t" \
    "v_fma_f32 v188, v188, v206, v208\n\t" \
    "v_fma_f32 v189, v189, v206, v209\n\t" \
    "v_fma_f32 v190, v190, v206, v210\n\t" \
    "v_fma_f32 v191, v191, v206, v211\n\t" \
    "v_mul_f32 v160, v188, v188\n\t" \
    "v_mul_f32 v161, v189, v189\n\t" \
    "v_mul_f32 v162, v190, v190\n\t" \
    "v_mul_f32 v163, v191, v191\n\t" \
    "v_fma_f32 v160, v160, %[k1], v220\n\t" \
    "v_fma_f32 v161, v161, %[k1], v220\n\t" \
    "v_fma_f32 v162, v162, %[k1], v220\n\t" \
    "v_fma_f32 v163, v163, %[k1], v220\n\t" \
    "v_mul_f32 v160, v188, v160\n\t" \
    "v_mul_f32 v161, v189, v161\n\t" \
    "v_mul_f32 v162, v190, v162\n\t" \
    "v_mul_f32 v163, v191, v163\n\t" \
    "v_fma_f32 v192, v192, v206, v212\n\t" \
    "v_fma_f32 v193, v193, v206, v213\n\t" \
    "v_fma_f32 v194, v194, v206, v214\n\t" \
    "v_fma_f32 v195, v195, v206, v215\n\t" \
    "v_mul_f32 v164, v192, v192\n\t" \
    "v_mul_f32 v165, v193, v193\n\t" \
    "v_mul_f32 v166, v194, v194\n\t" \
    "v_mul_f32 v167, v195, v195\n\t" \
    "v_fma_f32 v164, v164, %[k1], v220\n\t" \
    "v_fma_f32 v165, v165, %[k1], v220\n\t" \
    "v_fma_f32 v166, v166, %[k1], v220\n\t" \
    "v_fma_f32 v167, v167, %[k1], v220\n\t" \
    "v_mul_f32 v164, v192, v164\n\t" \
    "v_mul_f32 v165, v193, v165\n\t" \
    "v_mul_f32 v166, v194, v166\n\t" \
    "v_mul_f32 v167, v195, v167\n\t" \
    "v_exp_f32 v160, v160\n\t" \
    "v_exp_f32 v161, v161\n\t" \
    "v_exp_f32 v162, v162\n\t" \
    "v_exp_f32 v163, v163\n\t" \
    "v_exp_f32 v164, v164\n\t" \
    "v_exp_f32 v165, v165\n\t" \
    "v_exp_f32 v166, v166\n\t" \
    "v_exp_f32 v167, v167\n\t" \
    "v_add_f32 v160, 1.0, v160\n\t" \
    "v_add_f32 v161, 1.0, v161\n\t" \
    "v_add_f32 v162, 1.0, v162\n\t" \
    "v_add_f32 v163, 1.0, v163\n\t" \
    "v_add_f32 v164, 1.0, v164\n\t" \
    "v_add_f32 v165, 1.0, v165\n\t" \
    "v_add_f32 v166, 1.0, v166\n\t" \
    "v_add_f32 v167, 1.0, v167\n\t" \
    "v_rcp_f32 v160, v160\n\t" \
    "v_rcp_f32 v161, v161\n\t" \
    "v_rcp_f32 v162, v162\n\t" \
    "v_rcp_f32 v163, v163\n\t" \
    "v_rcp_f32 v164, v164\n\t" \
    "v_rcp_f32 v165, v165\n\t" \
    "v_rcp_f32 v166, v166\n\t" \
    "v_rcp_f32 v167, v167\n\t" \
    "v_mul_f32 v188, v188, v160\n\t" \
    "v_mul_f32 v189, v189, v161\n\t" \
    "v_mul_f32 v190, v190, v162\n\t" \
    "v_mul_f32 v191, v191, v163\n\t" \
    "v_mul_f32 v192, v192, v164\n\t" \
    "v_mul_f32 v193, v193, v165\n\t" \
    "v_mul_f32 v194, v194, v166\n\t" \
    "v_mul_f32 v195, v195, v167\n\t" \
    "v_cvt_pk_bf16_f32 v188, v188, v189\n\t" \
    "v_cvt_pk_bf16_f32 v189, v190, v191\n\t" \
    "v_cvt_pk_bf16_f32 v190, v192, v193\n\t" \
    "v_cvt_pk_bf16_f32 v191, v194, v195\n\t" \
    "v_min_u32 v188, -2, v188\n\t" \
    "v_min_u32 v189, -2, v189\n\t" \
    "v_min_u32 v190, -2, v190\n\t" \
    "v_min_u32 v191, -2, v191\n\t" \
    "s_nop 1\n\t" \
    "v_permlane16_swap_b32 v188, v190\n\t" \
    "v_permlane16_swap_b32 v189, v191\n\t" \
    "s_waitcnt vmcnt(1)\n\t" \
    "buffer_store_dwordx4 v[188:191], %[oa], %[rs], %[so] offen sc1\n\t" \
    "s_nop 1\n\t"
#define ASM_WT2_LO \
    "s_branch .Lt0%=\n\t" \
    ".Ltr%=:\n\t" \
    "s_cmp_gt_i32 %[n], 0\n\t" \
    "s_cbranch_scc0 .Ltg%=\n\t" \
    ".Lfw%=:\n\t" \
    "s_mov_b32 %[a], 0\n\t" \
    "v_max_u32 v252, v232, v233\n\t" \
    "v_max3_u32 v252, v234, v235, v252\n\t" \
    "v_cmp_eq_u32 vcc, -1, v252\n\t" \
    "s_cbranch_vccz .Lgw0_%=\n\t" \
    "buffer_load_dwordx4 v[232:235], %[l16], %[rs], %[i0] offen offset:0 sc1\n\t" \
    "s_mov_b32 %[a], 1\n\t" \
    ".Lgw0_%=:\n\t" \
    "v_max_u32 v252, v236, v237\n\t" \
    "v_max3_u32 v252, v238, v239, v252\n\t" \
    "v_cmp_eq_u32 vcc, -1, v252\n\t" \
    "s_cbranch_vccz .Lgw1_%=\n\t" \
    "buffer_load_dwordx4 v[236:239], %[l16], %[rs], %[i0] offen offset:1024 sc1\n\t" \
    "s_mov_b32 %[a], 1\n\t" \
    ".Lgw1_%=:\n\t" \
    "v_max_u32 v252, v240, v241\n\t" \
    "v_max3_u32 v252, v242, v243, v252\n\t" \
    "v_cmp_eq_u32 vcc, -1, v252\n\t" \
    "s_cbranch_vccz .Lgw2_%=\n\t" \
    "buffer_load_dwordx4 v[240:243], %[l16], %[rs], %[i0] offen offset:2048 sc1\n\t" \
    "s_mov_b32 %[a], 1\n\t" \
    ".Lgw2_%=:\n\t" \
    "v_max_u32 v252, v244, v245\n\t" \
    "v_max3_u32 v252, v246, v247, v252\n\t" \
    "v_cmp_eq_u32 vcc, -1, v252\n\t" \
    "s_cbranch_vccz .Lgw3_%=\n\t" \
    "buffer_load_dwordx4 v[244:247], %[l16], %[rs], %[i0] offen offset:3072 sc1\n\t" \
    "s_mov_b32 %[a], 1\n\t" \
    ".Lgw3_%=:\n\t" \
    "v_max_u32 v252, v248, v249\n\t" \
    "v_max3_u32 v252, v250, v251, v252\n\t" \
    "v_cmp_eq_u32 vcc, -1, v252\n\t" \
    "s_cbranch_vccz .Lgw4_%=\n\t" \
    "buffer_load_dwordx4 v[248:251], %[l16], %[rs], %[i1] offen offset:0 sc1\n\t" \
    "s_mov_b32 %[a], 1\n\t" \
    ".Lgw4_%=:\n\t" \
    "s_cmp_eq_u32 %[a], 0\n\t" \
    "s_cbranch_scc1 .Lhw%=\n\t" \
    "s_waitcnt vmcnt(20)\n\t" \
    ".Liw%=:\n\t" \
    "v_max_u32 v252, v232, v233\n\t" \
    "v_max3_u32 v252, v234, v235, v252\n\t" \
    "v_max3_u32 v252, v236, v237, v252\n\t" \
    "v_max3_u32 v252, v238, v239, v252\n\t" \
    "v_max3_u32 v252, v240, v241, v252\n\t" \
    "v_max3_u32 v252, v242, v243, v252\n\t" \
    "v_max3_u32 v252, v244, v245, v252\n\t" \
    "v_max3_u32 v252, v246, v247, v252\n\t" \
    "v_max3_u32 v252, v248, v249, v252\n\t" \
    "v_max3_u32 v252, v250, v251, v252\n\t" \
    "v_cmp_eq_u32 vcc, -1, v252\n\t" \
    "s_cbranch_vccz .Lhw%=\n\t" \
    "s_sleep 1\n\t" \
    "v_max_u32 v252, v232, v233\n\t" \
    "v_max3_u32 v252, v234, v235, v252\n\t" \
    "v_max3_u32 v252, v236, v237, v252\n\t" \
    "v_max3_u32 v252, v238, v239, v252\n\t" \
    "v_max3_u32 v252, v240, v241, v252\n\t" \
    "v_max3_u32 v252, v242, v243, v252\n\t" \
    "v_max3_u32 v252, v244, v245, v252\n\t" \
    "v_max3_u32 v252, v246, v247, v252\n\t" \
    "v_max3_u32 v252, v248, v249, v252\n\t" \
    "v_max3_u32 v252, v250, v251, v252\n\t" \
    "v_cmp_eq_u32 vcc, -1, v252\n\t" \
    "s_cbranch_vccz .Lhw%=\n\t" \
    "s_sleep 1\n\t" \
    "v_max_u32 v252, v232, v233\n\t" \
    "v_max3_u32 v252, v234, v235, v252\n\t" \
    "v_max3_u32 v252, v236, v237, v252\n\t" \
    "v_max3_u32 v252, v238, v239, v252\n\t" \
    "v_max3_u32 v252, v240, v241, v252\n\t" \
    "v_max3_u32 v252, v242, v243, v252\n\t" \
    "v_max3_u32 v252, v244, v245, v252\n\t" \
    "v_max3_u32 v252, v246, v247, v252\n\t" \
    "v_max3_u32 v252, v248, v249, v252\n\t" \
    "v_max3_u32 v252, v250, v251, v252\n\t" \
    "v_cmp_eq_u32 vcc, -1, v252\n\t" \
    "s_cbranch_vccz .Lhw%=\n\t" \
    "s_sleep 1\n\t" \
    "v_max_u32 v252, v232, v233\n\t" \
    "v_max3_u32 v252, v234, v235, v252\n\t" \
    "v_max3_u32 v252, v236, v237, v252\n\t" \
    "v_max3_u32 v252, v238, v239, v252\n\t" \
    "v_max3_u32 v252, v240, v241, v252\n\t" \
    "v_max3_u32 v252, v242, v243, v252\n\t" \
    "v_max3_u32 v252, v244, v245, v252\n\t" \
    "v_max3_u32 v252, v246, v247, v252\n\t" \
    "v_max3_u32 v252, v248, v249, v252\n\t" \
    "v_max3_u32 v252, v250, v251, v252\n\t" \
    "v_cmp_eq_u32 vcc, -1, v252\n\t" \
    "s_cbranch_vccz .Lhw%=\n\t" \
    "s_sleep 1\n\t" \
    "v_max_u32 v252, v232, v233\n\t" \
    "v_max3_u32 v252, v234, v235, v252\n\t" \
    "v_max3_u32 v252, v236, v237, v252\n\t" \
    "v_max3_u32 v252, v238, v239, v252\n\t" \
    "v_max3_u32 v252, v240, v241, v252\n\t" \
    "v_max3_u32 v252, v242, v243, v252\n\t" \
    "v_max3_u32 v252, v244, v245, v252\n\t" \
    "v_max3_u32 v252, v246, v247, v252\n\t" \
    "v_max3_u32 v252, v248, v249, v252\n\t" \
    "v_max3_u32 v252, v250, v251, v252\n\t" \
    "v_cmp_eq_u32 vcc, -1, v252\n\t" \
    "s_cbranch_vccz .Lhw%=\n\t" \
    "s_sleep 1\n\t" \
    "v_max_u32 v252, v232, v233\n\t" \
    "v_max3_u32 v252, v234, v235, v252\n\t" \
    "v_max3_u32 v252, v236, v237, v252\n\t" \
    "v_max3_u32 v252, v238, v239, v252\n\t" \
    "v_max3_u32 v252, v240, v241, v252\n\t" \
    "v_max3_u32 v252, v242, v243, v252\n\t" \
    "v_max3_u32 v252, v244, v245, v252\n\t" \
    "v_max3_u32 v252, v246, v247, v252\n\t" \
    "v_max3_u32 v252, v248, v249, v252\n\t" \
    "v_max3_u32 v252, v250, v251, v252\n\t" \
    "v_cmp_eq_u32 vcc, -1, v252\n\t" \
    "s_cbranch_vccz .Lhw%=\n\t" \
    "s_sleep 1\n\t" \
    "s_sub_u32 %[n], %[n], 1\n\t" \
    "s_cmp_gt_i32 %[n], 0\n\t" \
    "s_cbranch_scc1 .Lfw%=\n\t" \
    ".Lhw%=:\n\t" \
    "s_branch .Lta%=\n\t" \
    ".Lt0%=:\n\t" \
    "s_waitcnt vmcnt(1)\n\t" \
    ".Lta%=:\n\t" \
    "ds_write_b128 %[lw], v[232:235] offset:0\n\t" \
    "v_max_u32 v252, v232, v233\n\t" \
    "v_max3_u32 v252, v234, v235, v252\n\t" \
    "ds_write_b128 %[lw], v[236:239] offset:1024\n\t" \
    "v_max3_u32 v252, v236, v237, v252\n\t" \
    "v_max3_u32 v252, v238, v239, v252\n\t" \
    "ds_write_b128 %[lw], v[240:243] offset:2048\n\t" \
    "v_max3_u32 v252, v240, v241, v252\n\t" \
    "v_max3_u32 v252, v242, v243, v252\n\t" \
    "ds_write_b128 %[lw], v[244:247] offset:3072\n\t" \
    "v_max3_u32 v252, v244, v245, v252\n\t" \
    "v_max3_u32 v252, v246, v247, v252\n\t" \
    "ds_write_b128 %[lw], v[248:251] offset:4096\n\t" \
    "v_max3_u32 v252, v248, v249, v252\n\t" \
    "v_max3_u32 v252, v250, v251, v252\n\t" \
    "v_cmp_eq_u32 vcc, -1, v252\n\t" \
    "s_cbranch_vccnz .Ltr%=\n\t" \
    ".Ltg%=:\n\t" \
    ASM_ST2 \
    "s_waitcnt lgkmcnt(0)\n\t" \
    "s_setprio 2\n\t" \
    "s_barrier\n\t" ASM_ST \
    "ds_read_b128 v[160:163], %[la] offset:0\n\t" \
    "ds_read_b128 v[164:167], %[la] offset:1024\n\t" \
    "ds_read_b128 v[168:171], %[la] offset:2048\n\t" \
    "ds_read_b128 v[172:175], %[la] offset:3072\n\t" \
    "ds_read_b128 v[176:179], %[la] offset:8192\n\t" \
    "ds_read_b128 v[180:183], %[la] offset:9216\n\t" \
    "ds_read_b128 v[184:187], %[la] offset:10240\n\t" \
    "s_waitcnt lgkmcnt(6)\n\t" \
    "v_mfma_f32_16x16x32_bf16 v[192:195], v[80:83], v[160:163], 0\n\t" \
    "buffer_load_dwordx2 v[196:197], %[ro], %[rs], %[sx] offen sc1\n\t" \
    "s_waitcnt lgkmcnt(5)\n\t" \
    "v_mfma_f32_16x16x32_bf16 v[192:195], v[84:87], v[164:167], v[192:195]\n\t" \
    "buffer_load_dwordx2 v[198:199], %[ro], %[rs], %[sx] offen offset:512 sc1\n\t" \
    "v_mfma_f32_16x16x32_bf16 v[188:191], v[0:3], v[160:163], 0\n\t" \
    "ds_read_b128 v[160:163], %[la] offset:11264\n\t" \
    "v_lshrrev_b32 v204, 1, %[l16]\n\t" \
    "v_mfma_f32_16x16x32_bf16 v[188:191], v[4:7], v[164:167], v[188:191]\n\t" \
    "ds_read_b128 v[164:167], %[la] offset:16384\n\t" \
    "v_and_b32 v204, 0x78, v204\n\t" \
    "s_waitcnt lgkmcnt(6)\n\t" \
    "v_mfma_f32_16x16x32_bf16 v[188:191], v[8:11], v[168:171], v[188:191]\n\t" \
    "buffer_load_dwordx2 v[200:201], v204, %[rs], %[ss] offen sc1\n\t" \
    "s_waitcnt lgkmcnt(5)\n\t" \
    "v_mfma_f32_16x16x32_bf16 v[188:191], v[12:15], v[172:175], v[188:191]\n\t" \
    "v_mov_b64 v[216:217], -1\n\t" \
    "v_mfma_f32_16x16x32_bf16 v[192:195], v[88:91], v[168:171], v[192:195]\n\t" \
    "ds_read_b128 v[168:171], %[la] offset:17408\n\t" \
    "v_mov_b64 v[218:219], -1\n\t" \
    "v_mfma_f32_16x16x32_bf16 v[192:195], v[92:95], v[172:175], v[192:195]\n\t" \
    "ds_read_b128 v[172:175], %[la] offset:18432\n\t" \
    "buffer_store_dwordx4 v[216:219], %[l16], %[rs], %[sa] offen sc1\n\t" \
    "s_waitcnt lgkmcnt(6)\n\t" \
    "v_mfma_f32_16x16x32_bf16 v[192:195], v[96:99], v[176:179], v[192:195]\n\t" \
    "s_waitcnt lgkmcnt(5)\n\t" \
    "v_mfma_f32_16x16x32_bf16 v[192:195], v[100:103], v[180:183], v[192:195]\n\t" \
    "v_mfma_f32_16x16x32_bf16 v[188:191], v[16:19], v[176:179], v[188:191]\n\t" \
    "ds_read_b128 v[176:179], %[la] offset:19456\n\t" \
    "v_mfma_f32_16x16x32_bf16 v[188:191], v[20:23], v[180:183], v[188:191]\n\t" \
    "ds_read_b128 v[180:183], %[la] offset:24576\n\t" \
    "s_waitcnt lgkmcnt(6)\n\t" \
    "v_mfma_f32_16x16x32_bf16 v[188:191], v[24:27], v[184:187], v[188:191]\n\t" \
    "s_waitcnt lgkmcnt(5)\n\t" \
    "v_mfma_f32_16x16x32_bf16 v[188:191], v[28:31], v[160:163], v[188:191]\n\t" \
    "v_mfma_f32_16x16x32_bf16 v[192:195], v[104:107], v[184:187], v[192:195]\n\t" \
    "ds_read_b128 v[184:187], %[la] offset:25600\n\t" \
    "v_mfma_f32_16x16x32_bf16 v[192:195], v[108:111], v[160:163], v[192:195]\n\t" \
    "ds_read_b128 v[160:163], %[la] offset:26624\n\t" \
    "s_waitcnt lgkmcnt(6)\n\t" \
    "v_mfma_f32_16x16x32_bf16 v[192:195], v[112:115], v[164:167], v[192:195]\n\t" \
    "s_waitcnt lgkmcnt(5)\n\t" \
    "v_mfma_f32_16x16x32_bf16 v[192:195], v[116:119], v[168:171], v[192:195]\n\t" \
    "v_mfma_f32_16x16x32_bf16 v[188:191], v[32:35], v[164:167], v[188:191]\n\t" \
    "ds_read_b128 v[164:167], %[la] offset:27648\n\t" \
    "v_mfma_f32_16x16x32_bf16 v[188:191], v[36:39], v[168:171], v[188:191]\n\t" \
    "ds_read_b128 v[168:171], %[la] offset:4096\n\t" \
    "s_waitcnt lgkmcnt(6)\n\t" \
    "v_mfma_f32_16x16x32_bf16 v[188:191], v[40:43], v[172:175], v[188:191]\n\t" \
    "s_waitcnt lgkmcnt(5)\n\t" \
    "v_mfma_f32_16x16x32_bf16 v[188:191], v[44:47], v[176:179], v[188:191]\n\t" \
    "v_mfma_f32_16x16x32_bf16 v[192:195], v[120:123], v[172:175], v[192:195]\n\t" \
    "ds_read_b128 v[172:175], %[la] offset:12288\n\t" \
    "v_mfma_f32_16x16x32_bf16 v[192:195], v[124:127], v[176:179], v[192:195]\n\t" \
    "ds_read_b128 v[176:179], %[la] offset:20480\n\t" \
    "s_waitcnt lgkmcnt(6)\n\t" \
    "v_mfma_f32_16x16x32_bf16 v[192:195], v[128:131], v[180:183], v[192:195]\n\t" \
    "s_waitcnt lgkmcnt(5)\n\t" \
    "v_mfma_f32_16x16x32_bf16 v[192:195], v[132:135], v[184:187], v[192:195]\n\t" \
    "v_mfma_f32_16x16x32_bf16 v[188:191], v[48:51], v[180:183], v[188:191]\n\t" \
    "ds_read_b128 v[180:183], %[la] offset:28672\n\t" \
    "v_mfma_f32_16x16x32_bf16 v[188:191], v[52:55], v[184:187], v[188:191]\n\t" \
    "s_waitcnt lgkmcnt(5)\n\t" \
    "v_mfma_f32_16x16x32_bf16 v[188:191], v[56:59], v[160:163], v[188:191]\n\t" \
    "s_waitcnt lgkmcnt(4)\n\t" \
    "v_mfma_f32_16x16x32_bf16 v[188:191], v[60:63], v[164:167], v[188:191]\n\t" \
    "v_mfma_f32_16x16x32_bf16 v[192:195], v[136:139], v[160:163], v[192:195]\n\t" \
    "v_mfma_f32_16x16x32_bf16 v[192:195], v[140:143], v[164:167], v[192:195]\n\t" \
    "s_waitcnt lgkmcnt(3)\n\t" \
    "v_mfma_f32_16x16x32_bf16 v[192:195], v[144:147], v[168:171], v[192:195]\n\t" \
    "s_waitcnt lgkmcnt(2)\n\t" \
    "v_mfma_f32_16x16x32_bf16 v[192:195], v[148:151], v[172:175], v[192:195]\n\t" \
    "v_mfma_f32_16x16x32_bf16 v[188:191], v[64:67], v[168:171], v[188:191]\n\t" \
    "v_mfma_f32_16x16x32_bf16 v[188:191], v[68:71], v[172:175], v[188:191]\n\t" \
    "s_waitcnt lgkmcnt(1)\n\t" \
    "v_mfma_f32_16x16x32_bf16 v[188:191], v[72:75], v[176:179], v[188:191]\n\t" \
    "s_waitcnt lgkmcnt(0)\n\t" \
    "v_mfma_f32_16x16x32_bf16 v[188:191], v[76:79], v[180:183], v[188:191]\n\t" \
    "v_mfma_f32_16x16x32_bf16 v[192:195], v[152:155], v[176:179], v[192:195]\n\t" \
    "v_mfma_f32_16x16x32_bf16 v[192:195], v[156:159], v[180:183], v[192:195]\n\t" \
    "s_setprio 2\n\t" \
    "buffer_load_dwordx4 v[232:235], %[l16], %[rs], %[n0] offen offset:0 sc1\n\t" \
    "buffer_load_dwordx4 v[236:239], %[l16], %[rs], %[n0] offen offset:1024 sc1\n\t" \
    "buffer_load_dwordx4 v[240:243], %[l16], %[rs], %[n0] offen offset:2048 sc1\n\t" \
    "buffer_load_dwordx4 v[244:247], %[l16], %[rs], %[n0] offen offset:3072 sc1\n\t" \
    "buffer_load_dwordx4 v[248:251], %[l16], %[rs], %[n1] offen offset:0 sc1\n\t" \
    ASM_ST3 \
    "s_waitcnt vmcnt(6)\n\t" \
    "v_lshlrev_b32 v160, 16, v196\n\t" \
    "v_and_b32 v161, 0xffff0000, v196\n\t" \
    "v_lshlrev_b32 v162, 16, v197\n\t" \
    "v_and_b32 v163, 0xffff0000, v197\n\t" \
    "v_lshlrev_b32 v164, 16, v198\n\t" \
    "v_and_b32 v165, 0xffff0000, v198\n\t" \
    "v_lshlrev_b32 v166, 16, v199\n\t" \
    "v_and_b32 v167, 0xffff0000, v199\n\t" \
    "v_sub_f32 v160, v160, v200\n\t" \
    "v_sub_f32 v161, v161, v200\n\t" \
    "v_sub_f32 v162, v162, v200\n\t" \
    "v_sub_f32 v163, v163, v200\n\t" \
    "v_sub_f32 v164, v164, v200\n\t" \
    "v_sub_f32 v165, v165, v200\n\t" \
    "v_sub_f32 v166, v166, v200\n\t" \
    "v_sub_f32 v167, v167, v200\n\t" \
    "v_mul_f32 v160, v160, v201\n\t" \
    "v_mul_f32 v161, v161, v201\n\t" \
    "v_mul_f32 v162, v162, v201\n\t" \
    "v_mul_f32 v163, v163, v201\n\t" \
    "v_mul_f32 v164, v164, v201\n\t" \
    "v_mul_f32 v165, v165, v201\n\t" \
    "v_mul_f32 v166, v166, v201\n\t" \
    "v_mul_f32 v167, v167, v201\n\t" \
    "v_cvt_pk_bf16_f32 v160, v160, v161\n\t" \
    "v_cvt_pk_bf16_f32 v162, v162, v163\n\t" \
    "v_cvt_pk_bf16_f32 v164, v164, v165\n\t" \
    "v_cvt_pk_bf16_f32 v166, v166, v167\n\t" \
    "v_and_b32 v161, 0xffff0000, v160\n\t" \
    "v_lshlrev_b32 v160, 16, v160\n\t" \
    "v_and_b32 v163, 0xffff0000, v162\n\t" \
    "v_lshlrev_b32 v162, 16, v162\n\t" \
    "v_and_b32 v165, 0xffff0000, v164\n\t" \
    "v_lshlrev_b32 v164, 16, v164\n\t" \
    "v_and_b32 v167, 0xffff0000, v166\n\t" \
    "v_lshlrev_b32 v166, 16, v166\n\t" \
    "v_add_f32 v188, v188, v160\n\t" \
    "v_add_f32 v189, v189, v161\n\t" \
    "v_add_f32 v190, v190, v162\n\t" \
    "v_add_f32 v191, v191, v163\n\t" \
    "v_add_f32 v192, v192, v164\n\t" \
    "v_add_f32 v193, v193, v165\n\t" \
    "v_add_f32 v194, v194, v166\n\t" \
    "v_add_f32 v195, v195, v167\n\t" \
    "v_cvt_pk_bf16_f32 v188, v188, v189\n\t" \
    "v_cvt_pk_bf16_f32 v189, v190, v191\n\t" \
    "v_cvt_pk_bf16_f32 v190, v192, v193\n\t" \
    "v_cvt_pk_bf16_f32 v191, v194, v195\n\t" \
    "v_min_u32 v188, -2, v188\n\t" \
    "v_min_u32 v189, -2, v189\n\t" \
    "v_min_u32 v190, -2, v190\n\t" \
    "v_min_u32 v191, -2, v191\n\t" \
    "s_nop 1\n\t" \
    "v_permlane16_swap_b32 v188, v190\n\t" \
    "v_permlane16_swap_b32 v189, v191\n\t" \
    "s_waitcnt vmcnt(6)\n\t" \
    "buffer_store_dwordx4 v[188:191], %[oa], %[rs], %[so] offen sc1\n\t" \
    "s_nop 1\n\t" \
    "s_setprio 0\n\t"
#define ASM_WT2_HI \
    "s_setprio 1\n\t" \
    "s_barrier\n\t" ASM_ST \
    "ds_read_b128 v[160:163], %[la] offset:0\n\t" \
    "ds_read_b128 v[164:167], %[la] offset:1024\n\t" \
    "ds_read_b128 v[168:171], %[la] offset:2048\n\t" \
    "ds_read_b128 v[172:175], %[la] offset:3072\n\t" \
    "ds_read_b128 v[176:179], %[la] offset:8192\n\t" \
    "ds_read_b128 v[180:183], %[la] offset:9216\n\t" \
    "ds_read_b128 v[184:187], %[la] offset:10240\n\t" \
    "s_waitcnt lgkmcnt(6)\n\t" \
    "v_mfma_f32_16x16x32_bf16 v[192:195], v[80:83], v[160:163], 0\n\t" \
    "buffer_load_dwordx2 v[196:197], %[ro], %[rs], %[sx] offen sc1\n\t" \
    "s_waitcnt lgkmcnt(5)\n\t" \
    "v_mfma_f32_16x16x32_bf16 v[192:195], v[84:87], v[164:167], v[192:195]\n\t" \
    "buffer_load_dwordx2 v[198:199], %[ro], %[rs], %[sx] offen offset:512 sc1\n\t" \
    "v_mfma_f32_16x16x32_bf16 v[188:191], v[0:3], v[160:163], 0\n\t" \
    "ds_read_b128 v[160:163], %[la] offset:11264\n\t" \
    "v_lshrrev_b32 v204, 1, %[l16]\n\t" \
    "v_mfma_f32_16x16x32_bf16 v[188:191], v[4:7], v[164:167], v[188:191]\n\t" \
    "ds_read_b128 v[164:167], %[la] offset:16384\n\t" \
    "v_and_b32 v204, 0x78, v204\n\t" \
    "s_waitcnt lgkmcnt(6)\n\t" \
    "v_mfma_f32_16x16x32_bf16 v[188:191], v[8:11], v[168:171], v[188:191]\n\t" \
    "buffer_load_dwordx2 v[200:201], v204, %[rs], %[ss] offen sc1\n\t" \
    "s_waitcnt lgkmcnt(5)\n\t" \
    "v_mfma_f32_16x16x32_bf16 v[188:191], v[12:15], v[172:175], v[188:191]\n\t" \
    "v_mov_b64 v[216:217], -1\n\t" \
    "v_mfma_f32_16x16x32_bf16 v[192:195], v[88:91], v[168:171], v[192:195]\n\t" \
    "ds_read_b128 v[168:171], %[la] offset:17408\n\t" \
    "v_mov_b64 v[218:219], -1\n\t" \
    "v_mfma_f32_16x16x32_bf16 v[192:195], v[92:95], v[172:175], v[192:195]\n\t" \
    "ds_read_b128 v[172:175], %[la] offset:18432\n\t" \
    "buffer_store_dwordx4 v[216:219], %[l16], %[rs], %[sa] offen sc1\n\t" \
    "s_waitcnt lgkmcnt(6)\n\t" \
    "v_mfma_f32_16x16x32_bf16 v[192:195], v[96:99], v[176:179], v[192:195]\n\t" \
    "s_waitcnt lgkmcnt(5)\n\t" \
    "v_mfma_f32_16x16x32_bf16 v[192:195], v[100:103], v[180:183], v[192:195]\n\t" \
    "v_mfma_f32_16x16x32_bf16 v[188:191], v[16:19], v[176:179], v[188:191]\n\t" \
    "ds_read_b128 v[176:179], %[la] offset:19456\n\t" \
    "v_mfma_f32_16x16x32_bf16 v[188:191], v[20:23], v[180:183], v[188:191]\n\t" \
    "ds_read_b128 v[180:183], %[la] offset:24576\n\t" \
    "s_waitcnt lgkmcnt(6)\n\t" \
    "v_mfma_f32_16x16x32_bf16 v[188:191], v[24:27], v[184:187], v[188:191]\n\t" \
    "s_waitcnt lgkmcnt(5)\n\t" \
    "v_mfma_f32_16x16x32_bf16 v[188:191], v[28:31], v[160:163], v[188:191]\n\t" \
    "v_mfma_f32_16x16x32_bf16 v[192:195], v[104:107], v[184:187], v[192:195]\n\t" \
    "ds_read_b128 v[184:187], %[la] offset:25600\n\t" \
    "v_mfma_f32_16x16x32_bf16 v[192:195], v[108:111], v[160:163], v[192:195]\n\t" \
    "ds_read_b128 v[160:163], %[la] offset:26624\n\t" \
    "s_waitcnt lgkmcnt(6)\n\t" \
    "v_mfma_f32_16x16x32_bf16 v[192:195], v[112:115], v[164:167], v[192:195]\n\t" \
    "s_waitcnt lgkmcnt(5)\n\t" \
    "v_mfma_f32_16x16x32_bf16 v[192:195], v[116:119], v[168:171], v[192:195]\n\t" \
    "v_mfma_f32_16x16x32_bf16 v[188:191], v[32:35], v[164:167], v[188:191]\n\t" \
    "ds_read_b128 v[164:167], %[la] offset:27648\n\t" \
    "v_mfma_f32_16x16x32_bf16 v[188:191], v[36:39], v[168:171], v[188:191]\n\t" \
    "ds_read_b128 v[168:171], %[la] offset:4096\n\t" \
    "s_waitcnt lgkmcnt(6)\n\t" \
    "v_mfma_f32_16x16x32_bf16 v[188:191], v[40:43], v[172:175], v[188:191]\n\t" \
    "s_waitcnt lgkmcnt(5)\n\t" \
    "v_mfma_f32_16x16x32_bf16 v[188:191], v[44:47], v[176:179], v[188:191]\n\t" \
    "v_mfma_f32_16x16x32_bf16 v[192:195], v[120:123], v[172:175], v[192:195]\n\t" \
    "ds_read_b128 v[172:175], %[la] offset:12288\n\t" \
    "v_mfma_f32_16x16x32_bf16 v[192:195], v[124:127], v[176:179], v[192:195]\n\t" \
    "ds_read_b128 v[176:179], %[la] offset:20480\n\t" \
    "s_waitcnt lgkmcnt(6)\n\t" \
    "v_mfma_f32_16x16x32_bf16 v[192:195], v[128:131], v[180:183], v[192:195]\n\t" \
    "s_waitcnt lgkmcnt(5)\n\t" \
    "v_mfma_f32_16x16x32_bf16 v[192:195], v[132:135], v[184:187], v[192:195]\n\t" \
    "v_mfma_f32_16x16x32_bf16 v[188:191], v[48:51], v[180:183], v[188:191]\n\t" \
    "ds_read_b128 v[180:183], %[la] offset:28672\n\t" \
    "v_mfma_f32_16x16x32_bf16 v[188:191], v[52:55], v[184:187], v[188:191]\n\t" \
    "s_waitcnt lgkmcnt(5)\n\t" \
    "v_mfma_f32_16x16x32_bf16 v[188:191], v[56:59], v[160:163], v[188:191]\n\t" \
    "s_waitcnt lgkmcnt(4)\n\t" \
    "v_mfma_f32_16x16x32_bf16 v[188:191], v[60:63], v[164:167], v[188:191]\n\t" \
    "v_mfma_f32_16x16x32_bf16 v[192:195], v[136:139], v[160:163], v[192:195]\n\t" \
    "v_mfma_f32_16x16x32_bf16 v[192:195], v[140:143], v[164:167], v[192:195]\n\t" \
    "s_waitcnt lgkmcnt(3)\n\t" \
    "v_mfma_f32_16x16x32_bf16 v[192:195], v[144:147], v[168:171], v[192:195]\n\t" \
    "s_waitcnt lgkmcnt(2)\n\t" \
    "v_mfma_f32_16x16x32_bf16 v[192:195], v[148:151], v[172:175], v[192:195]\n\t" \
    "v_mfma_f32_16x16x32_bf16 v[188:191], v[64:67], v[168:171], v[188:191]\n\t" \
    "v_mfma_f32_16x16x32_bf16 v[188:191], v[68:71], v[172:175], v[188:191]\n\t" \
    "s_waitcnt lgkmcnt(1)\n\t" \
    "v_mfma_f32_16x16x32_bf16 v[188:191], v[72:75], v[176:179], v[188:191]\n\t" \
    "s_waitcnt lgkmcnt(0)\n\t" \
    "v_mfma_f32_16x16x32_bf16 v[188:191], v[76:79], v[180:183], v[188:191]\n\t" \
    "v_mfma_f32_16x16x32_bf16 v[192:195], v[152:155], v[176:179], v[192:195]\n\t" \
    "v_mfma_f32_16x16x32_bf16 v[192:195], v[156:159], v[180:183], v[192:195]\n\t" \
    ASM_ST3 \
    "s_waitcnt vmcnt(1)\n\t" \
    "v_lshlrev_b32 v160, 16, v196\n\t" \
    "v_and_b32 v161, 0xffff0000, v196\n\t" \
    "v_lshlrev_b32 v162, 16, v197\n\t" \
    "v_and_b32 v163, 0xffff0000, v197\n\t" \
    "v_lshlrev_b32 v164, 16, v198\n\t" \
    "v_and_b32 v165, 0xffff0000, v198\n\t" \
    "v_lshlrev_b32 v166, 16, v199\n\t" \
    "v_and_b32 v167, 0xffff0000, v199\n\t" \
    "v_sub_f32 v160, v160, v200\n\t" \
    "v_sub_f32 v161, v161, v200\n\t" \
    "v_sub_f32 v162, v162, v200\n\t" \
    "v_sub_f32 v163, v163, v200\n\t" \
    "v_sub_f32 v164, v164, v200\n\t" \
    "v_sub_f32 v165, v165, v200\n\t" \
    "v_sub_f32 v166, v166, v200\n\t" \
    "v_sub_f32 v167, v167, v200\n\t" \
    "v_mul_f32 v160, v160, v201\n\t" \
    "v_mul_f32 v161, v161, v201\n\t" \
    "v_mul_f32 v162, v162, v201\n\t" \
    "v_mul_f32 v163, v163, v201\n\t" \
    "v_mul_f32 v164, v164, v201\n\t" \
    "v_mul_f32 v165, v165, v201\n\t" \
    "v_mul_f32 v166, v166, v201\n\t" \
    "v_mul_f32 v167, v167, v201\n\t" \
    "v_cvt_pk_bf16_f32 v160, v160, v161\n\t" \
    "v_cvt_pk_bf16_f32 v162, v162, v163\n\t" \
    "v_cvt_pk_bf16_f32 v164, v164, v165\n\t" \
    "v_cvt_pk_bf16_f32 v166, v166, v167\n\t" \
    "v_and_b32 v161, 0xffff0000, v160\n\t" \
    "v_lshlrev_b32 v160, 16, v160\n\t" \
    "v_and_b32 v163, 0xffff0000, v162\n\t" \
    "v_lshlrev_b32 v162, 16, v162\n\t" \
    "v_and_b32 v165, 0xffff0000, v164\n\t" \
    "v_lshlrev_b32 v164, 16, v164\n\t" \
    "v_and_b32 v167, 0xffff0000, v166\n\t" \
    "v_lshlrev_b32 v166, 16, v166\n\t" \
    "v_add_f32 v188, v188, v160\n\t" \
    "v_add_f32 v189, v189, v161\n\t" \
    "v_add_f32 v190, v190, v162\n\t" \
    "v_add_f32 v191, v191, v163\n\t" \
    "v_add_f32 v192, v192, v164\n\t" \
    "v_add_f32 v193, v193, v165\n\t" \
    "v_add_f32 v194, v194, v166\n\t" \
    "v_add_f32 v195, v195, v167\n\t" \
    "v_cvt_pk_bf16_f32 v188, v188, v189\n\t" \
    "v_cvt_pk_bf16_f32 v189, v190, v191\n\t" \
    "v_cvt_pk_bf16_f32 v190, v192, v193\n\t" \
    "v_cvt_pk_bf16_f32 v191, v194, v195\n\t" \
    "v_min_u32 v188, -2, v188\n\t" \
    "v_min_u32 v189, -2, v189\n\t" \
    "v_min_u32 v190, -2, v190\n\t" \
    "v_min_u32 v191, -2, v191\n\t" \
    "s_nop 1\n\t" \
    "v_permlane16_swap_b32 v188, v190\n\t" \
    "v_permlane16_swap_b32 v189, v191\n\t" \
    "s_waitcnt vmcnt(1)\n\t" \
    "buffer_store_dwordx4 v[188:191], %[oa], %[rs], %[so] offen sc1\n\t" \
    "s_nop 1\n\t"
#define ASM_WT3_LO \
    "s_branch .Lt0%=\n\t" \
    ".Ltr%=:\n\t" \
    "s_cmp_gt_i32 %[n], 0\n\t" \
    "s_cbranch_scc0 .Ltg%=\n\t" \
    ".Lfw%=:\n\t" \
    "s_mov_b32 %[a], 0\n\t" \
    "v_max_u32 v252, v232, v233\n\t" \
    "v_max3_u32 v252, v234, v235, v252\n\t" \
    "v_cmp_eq_u32 vcc, -1, v252\n\t" \
    "s_cbranch_vccz .Lgw0_%=\n\t" \
    "buffer_load_dwordx4 v[232:235], %[l16], %[rs], %[i0] offen offset:0 sc1\n\t" \
    "s_mov_b32 %[a], 1\n\t" \
    ".Lgw0_%=:\n\t" \
    "v_max_u32 v252, v236, v237\n\t" \
    "v_max3_u32 v252, v238, v239, v252\n\t" \
    "v_cmp_eq_u32 vcc, -1, v252\n\t" \
    "s_cbranch_vccz .Lgw1_%=\n\t" \
    "buffer_load_dwordx4 v[236:239], %[l16], %[rs], %[i0] offen offset:1024 sc1\n\t" \
    "s_mov_b32 %[a], 1\n\t" \
    ".Lgw1_%=:\n\t" \
    "v_max_u32 v252, v240, v241\n\t" \
    "v_max3_u32 v252, v242, v243, v252\n\t" \
    "v_cmp_eq_u32 vcc, -1, v252\n\t" \
    "s_cbranch_vccz .Lgw2_%=\n\t" \
    "buffer_load_dwordx4 v[240:243], %[l16], %[rs], %[i0] offen offset:2048 sc1\n\t" \
    "s_mov_b32 %[a], 1\n\t" \
    ".Lgw2_%=:\n\t" \
    "v_max_u32 v252, v244, v245\n\t" \
    "v_max3_u32 v252, v246, v247, v252\n\t" \
    "v_cmp_eq_u32 vcc, -1, v252\n\t" \
    "s_cbranch_vccz .Lgw3_%=\n\t" \
    "buffer_load_dwordx4 v[244:247], %[l16], %[rs], %[i0] offen offset:3072 sc1\n\t" \
    "s_mov_b32 %[a], 1\n\t" \
    ".Lgw3_%=:\n\t" \
    "v_max_u32 v252, v248, v249\n\t" \
    "v_max3_u32 v252, v250, v251, v252\n\t" \
    "v_cmp_eq_u32 vcc, -1, v252\n\t" \
    "s_cbranch_vccz .Lgw4_%=\n\t" \
    "buffer_load_dwordx4 v[248:251], %[l16], %[rs], %[i1] offen offset:0 sc1\n\t" \
    "s_mov_b32 %[a], 1\n\t" \
    ".Lgw4_%=:\n\t" \
    "s_cmp_eq_u32 %[a], 0\n\t" \
    "s_cbranch_scc1 .Lhw%=\n\t" \
    "s_waitcnt vmcnt(20)\n\t" \
    ".Liw%=:\n\t" \
    "v_max_u32 v252, v232, v233\n\t" \
    "v_max3_u32 v252, v234, v235, v252\n\t" \
    "v_max3_u32 v252, v236, v237, v252\n\t" \
    "v_max3_u32 v252, v238, v239, v252\n\t" \
    "v_max3_u32 v252, v240, v241, v252\n\t" \
    "v_max3_u32 v252, v242, v243, v252\n\t" \
    "v_max3_u32 v252, v244, v245, v252\n\t" \
    "v_max3_u32 v252, v246, v247, v252\n\t" \
    "v_max3_u32 v252, v248, v249, v252\n\t" \
    "v_max3_u32 v252, v250, v251, v252\n\t" \
    "v_cmp_eq_u32 vcc, -1, v252\n\t" \
    "s_cbranch_vccz .Lhw%=\n\t" \
    "s_sleep 1\n\t" \
    "v_max_u32 v252, v232, v233\n\t" \
    "v_max3_u32 v252, v234, v235, v252\n\t" \
    "v_max3_u32 v252, v236, v237, v252\n\t" \
    "v_max3_u32 v252, v238, v239, v252\n\t" \
    "v_max3_u32 v252, v240, v241, v252\n\t" \
    "v_max3_u32 v252, v242, v243, v252\n\t" \
    "v_max3_u32 v252, v244, v245, v252\n\t" \
    "v_max3_u32 v252, v246, v247, v252\n\t" \
    "v_max3_u32 v252, v248, v249, v252\n\t" \
    "v_max3_u32 v252, v250, v251, v252\n\t" \
    "v_cmp_eq_u32 vcc, -1, v252\n\t" \
    "s_cbranch_vccz .Lhw%=\n\t" \
    "s_sleep 1\n\t" \
    "v_max_u32 v252, v232, v233\n\t" \
    "v_max3_u32 v252, v234, v235, v252\n\t" \
    "v_max3_u32 v252, v236, v237, v252\n\t" \
    "v_max3_u32 v252, v238, v239, v252\n\t" \
    "v_max3_u32 v252, v240, v241, v252\n\t" \
    "v_max3_u32 v252, v242, v243, v252\n\t" \
    "v_max3_u32 v252, v244, v245, v252\n\t" \
    "v_max3_u32 v252, v246, v247, v252\n\t" \
    "v_max3_u32 v252, v248, v249, v252\n\t" \
    "v_max3_u32 v252, v250, v251, v252\n\t" \
    "v_cmp_eq_u32 vcc, -1, v252\n\t" \
    "s_cbranch_vccz .Lhw%=\n\t" \
    "s_sleep 1\n\t" \
    "v_max_u32 v252, v232, v233\n\t" \
    "v_max3_u32 v252, v234, v235, v252\n\t" \
    "v_max3_u32 v252, v236, v237, v252\n\t" \
    "v_max3_u32 v252, v238, v239, v252\n\t" \
    "v_max3_u32 v252, v240, v241, v252\n\t" \
    "v_max3_u32 v252, v242, v243, v252\n\t" \
    "v_max3_u32 v252, v244, v245, v252\n\t" \
    "v_max3_u32 v252, v246, v247, v252\n\t" \
    "v_max3_u32 v252, v248, v249, v252\n\t" \
    "v_max3_u32 v252, v250, v251, v252\n\t" \
    "v_cmp_eq_u32 vcc, -1, v252\n\t" \
    "s_cbranch_vccz .Lhw%=\n\t" \
    "s_sleep 1\n\t" \
    "v_max_u32 v252, v232, v233\n\t" \
    "v_max3_u32 v252, v234, v235, v252\n\t" \
    "v_max3_u32 v252, v236, v237, v252\n\t" \
    "v_max3_u32 v252, v238, v239, v252\n\t" \
    "v_max3_u32 v252, v240, v241, v252\n\t" \
    "v_max3_u32 v252, v242, v243, v252\n\t" \
    "v_max3_u32 v252, v244, v245, v252\n\t" \
    "v_max3_u32 v252, v246, v247, v252\n\t" \
    "v_max3_u32 v252, v248, v249, v252\n\t" \
    "v_max3_u32 v252, v250, v251, v252\n\t" \
    "v_cmp_eq_u32 vcc, -1, v252\n\t" \
    "s_cbranch_vccz .Lhw%=\n\t" \
    "s_sleep 1\n\t" \
    "v_max_u32 v252, v232, v233\n\t" \
    "v_max3_u32 v252, v234, v235, v252\n\t" \
    "v_max3_u32 v252, v236, v237, v252\n\t" \
    "v_max3_u32 v252, v238, v239, v252\n\t" \
    "v_max3_u32 v252, v240, v241, v252\n\t" \
    "v_max3_u32 v252, v242, v243, v252\n\t" \
    "v_max3_u32 v252, v244, v245, v252\n\t" \
    "v_max3_u32 v252, v246, v247, v252\n\t" \
    "v_max3_u32 v252, v248, v249, v252\n\t" \
    "v_max3_u32 v252, v250, v251, v252\n\t" \
    "v_cmp_eq_u32 vcc, -1, v252\n\t" \
    "s_cbranch_vccz .Lhw%=\n\t" \
    "s_sleep 1\n\t" \
    "s_sub_u32 %[n], %[n], 1\n\t" \
    "s_cmp_gt_i32 %[n], 0\n\t" \
    "s_cbranch_scc1 .Lfw%=\n\t" \
    ".Lhw%=:\n\t" \
    "s_branch .Lta%=\n\t" \
    ".Lt0%=:\n\t" \
    "s_waitcnt vmcnt(1)\n\t" \
    ".Lta%=:\n\t" \
    "ds_write_b128 %[lw], v[232:235] offset:0\n\t" \
    "v_max_u32 v252, v232, v233\n\t" \
    "v_max3_u32 v252, v234, v235, v252\n\t" \
    "v_mfma_f32_16x16x32_bf16 v[188:191], v[232:235], v[232:235], 0\n\t" \
    "ds_write_b128 %[lw], v[236:239] offset:1024\n\t" \
    "v_max3_u32 v252, v236, v237, v252\n\t" \
    "v_max3_u32 v252, v238, v239, v252\n\t" \
    "v_mfma_f32_16x16x32_bf16 v[188:191], v[236:239], v[236:239], v[188:191]\n\t" \
    "ds_write_b128 %[lw], v[240:243] offset:2048\n\t" \
    "v_max3_u32 v252, v240, v241, v252\n\t" \
    "v_max3_u32 v252, v242, v243, v252\n\t" \
    "v_mfma_f32_16x16x32_bf16 v[188:191], v[240:243], v[240:243], v[188:191]\n\t" \
    "ds_write_b128 %[lw], v[244:247] offset:3072\n\t" \
    "v_max3_u32 v252, v244, v245, v252\n\t" \
    "v_max3_u32 v252, v246, v247, v252\n\t" \
    "v_mfma_f32_16x16x32_bf16 v[188:191], v[244:247], v[244:247], v[188:191]\n\t" \
    "ds_write_b128 %[lw], v[248:251] offset:4096\n\t" \
    "v_max3_u32 v252, v248, v249, v252\n\t" \
    "v_max3_u32 v252, v250, v251, v252\n\t" \
    "v_mfma_f32_16x16x32_bf16 v[188:191], v[248:251], v[248:251], v[188:191]\n\t" \
    "v_mfma_f32_16x16x32_bf16 v[192:195], v[216:219], v[232:235], 0\n\t" \
    "v_mfma_f32_16x16x32_bf16 v[192:195], v[216:219], v[236:239], v[192:195]\n\t" \
    "v_mfma_f32_16x16x32_bf16 v[192:195], v[216:219], v[240:243], v[192:195]\n\t" \
    "v_mfma_f32_16x16x32_bf16 v[192:195], v[216:219], v[244:247], v[192:195]\n\t" \
    "v_mfma_f32_16x16x32_bf16 v[192:195], v[216:219], v[248:251], v[192:195]\n\t" \
    "v_cmp_eq_u32 vcc, -1, v252\n\t" \
    "s_cbranch_vccnz .Ltr%=\n\t" \
    ".Ltg%=:\n\t" \
    ASM_ST2 \
    "s_nop 5\n\t" \
    "ds_write_b128 %[lw], v[188:191] offset:5120\n\t" \
    "ds_write_b128 %[lw], v[192:195] offset:6144\n\t" \
    "s_waitcnt lgkmcnt(0)\n\t" \
    "s_setprio 2\n\t" \
    "s_barrier\n\t" ASM_ST \
    "ds_read_b128 v[160:163], %[la] offset:0\n\t" \
    "ds_read_b128 v[164:167], %[la] offset:1024\n\t" \
    "ds_read_b128 v[168:171], %[la] offset:2048\n\t" \
    "ds_read2st64_b32 v[204:205], %[sr] offset0:20 offset1:24\n\t" \
    "ds_read_b128 v[172:175], %[la] offset:3072\n\t" \
    "ds_read_b128 v[176:179], %[la] offset:8192\n\t" \
    "ds_read_b128 v[180:183], %[la] offset:9216\n\t" \
    "ds_read_b128 v[184:187], %[la] offset:10240\n\t" \
    "s_waitcnt lgkmcnt(7)\n\t" \
    "v_mfma_f32_16x16x32_bf16 v[192:195], v[80:83], v[160:163], 0\n\t" \
    "s_waitcnt lgkmcnt(6)\n\t" \
    "v_mfma_f32_16x16x32_bf16 v[192:195], v[84:87], v[164:167], v[192:195]\n\t" \
    "v_mfma_f32_16x16x32_bf16 v[188:191], v[0:3], v[160:163], 0\n\t" \
    "ds_read_b128 v[160:163], %[la] offset:11264\n\t" \
    "v_mfma_f32_16x16x32_bf16 v[188:191], v[4:7], v[164:167], v[188:191]\n\t" \
    "ds_read_b128 v[164:167], %[la] offset:16384\n\t" \
    "s_waitcnt lgkmcnt(7)\n\t" \
    "v_mfma_f32_16x16x32_bf16 v[188:191], v[8:11], v[168:171], v[188:191]\n\t" \
    "s_waitcnt lgkmcnt(6)\n\t" \
    "s_waitcnt lgkmcnt(5)\n\t" \
    "v_mfma_f32_16x16x32_bf16 v[188:191], v[12:15], v[172:175], v[188:191]\n\t" \
    "v_permlane16_swap_b32 v204, v205\n\t" \
    "v_mfma_f32_16x16x32_bf16 v[192:195], v[88:91], v[168:171], v[192:195]\n\t" \
    "ds_read_b128 v[168:171], %[la] offset:17408\n\t" \
    "v_mfma_f32_16x16x32_bf16 v[192:195], v[92:95], v[172:175], v[192:195]\n\t" \
    "ds_read_b128 v[172:175], %[la] offset:18432\n\t" \
    "v_add_f32 v206, v204, v205\n\t" \
    "s_waitcnt lgkmcnt(6)\n\t" \
    "v_mfma_f32_16x16x32_bf16 v[192:195], v[96:99], v[176:179], v[192:195]\n\t" \
    "v_add_f32 v207, v204, v205\n\t" \
    "s_waitcnt lgkmcnt(5)\n\t" \
    "v_mfma_f32_16x16x32_bf16 v[192:195], v[100:103], v[180:183], v[192:195]\n\t" \
    "v_mfma_f32_16x16x32_bf16 v[188:191], v[16:19], v[176:179], v[188:191]\n\t" \
    "ds_read_b128 v[176:179], %[la] offset:19456\n\t" \
    "v_permlane32_swap_b32 v206, v207\n\t" \
    "v_mfma_f32_16x16x32_bf16 v[188:191], v[20:23], v[180:183], v[188:191]\n\t" \
    "ds_read_b128 v[180:183], %[la] offset:24576\n\t" \
    "s_waitcnt lgkmcnt(6)\n\t" \
    "v_mfma_f32_16x16x32_bf16 v[188:191], v[24:27], v[184:187], v[188:191]\n\t" \
    "v_add_f32 v204, v206, v207\n\t" \
    "s_waitcnt lgkmcnt(5)\n\t" \
    "v_mfma_f32_16x16x32_bf16 v[188:191], v[28:31], v[160:163], v[188:191]\n\t" \
    "v_add_f32 v205, v206, v207\n\t" \
    "v_mfma_f32_16x16x32_bf16 v[192:195], v[104:107], v[184:187], v[192:195]\n\t" \
    "ds_read_b128 v[184:187], %[la] offset:25600\n\t" \
    "v_mfma_f32_16x16x32_bf16 v[192:195], v[108:111], v[160:163], v[192:195]\n\t" \
    "ds_read_b128 v[160:163], %[la] offset:26624\n\t" \
    "v_permlane16_swap_b32 v204, v205\n\t" \
    "s_waitcnt lgkmcnt(6)\n\t" \
    "v_mfma_f32_16x16x32_bf16 v[192:195], v[112:115], v[164:167], v[192:195]\n\t" \
    "s_waitcnt lgkmcnt(5)\n\t" \
    "v_mfma_f32_16x16x32_bf16 v[192:195], v[116:119], v[168:171], v[192:195]\n\t" \
    "v_mul_f32 v205, %[kd], v205\n\t" \
    "v_mfma_f32_16x16x32_bf16 v[188:191], v[32:35], v[164:167], v[188:191]\n\t" \
    "ds_read_b128 v[164:167], %[la] offset:27648\n\t" \
    "v_mul_f32 v204, %[kd], v204\n\t" \
    "v_mfma_f32_16x16x32_bf16 v[188:191], v[36:39], v[168:171], v[188:191]\n\t" \
    "ds_read_b128 v[168:171], %[la] offset:4096\n\t" \
    "v_fma_f32 v204, -v205, v205, v204\n\t" \
    "s_waitcnt lgkmcnt(6)\n\t" \
    "v_mfma_f32_16x16x32_bf16 v[188:191], v[40:43], v[172:175], v[188:191]\n\t" \
    "v_max_f32 v204, 0, v204\n\t" \
    "s_waitcnt lgkmcnt(5)\n\t" \
    "v_mfma_f32_16x16x32_bf16 v[188:191], v[44:47], v[176:179], v[188:191]\n\t" \
    "v_add_f32 v206, %[ke], v204\n\t" \
    "v_mfma_f32_16x16x32_bf16 v[192:195], v[120:123], v[172:175], v[192:195]\n\t" \
    "ds_read_b128 v[172:175], %[la] offset:12288\n\t" \
    "v_rsq_f32 v206, v206\n\t" \
    "v_mfma_f32_16x16x32_bf16 v[192:195], v[124:127], v[176:179], v[192:195]\n\t" \
    "ds_read_b128 v[176:179], %[la] offset:20480\n\t" \
    "s_waitcnt lgkmcnt(6)\n\t" \
    "v_mfma_f32_16x16x32_bf16 v[192:195], v[128:131], v[180:183], v[192:195]\n\t" \
    "v_mul_f32 v207, -v205, v206\n\t" \
    "s_waitcnt lgkmcnt(5)\n\t" \
    "v_mfma_f32_16x16x32_bf16 v[192:195], v[132:135], v[184:187], v[192:195]\n\t" \
    "v_mul_f32 v208, v196, v207\n\t" \
    "v_mfma_f32_16x16x32_bf16 v[188:191], v[48:51], v[180:183], v[188:191]\n\t" \
    "ds_read_b128 v[180:183], %[la] offset:28672\n\t" \
    "v_mul_f32 v209, v197, v207\n\t" \
    "v_mul_f32 v210, v198, v207\n\t" \
    "v_mfma_f32_16x16x32_bf16 v[188:191], v[52:55], v[184:187], v[188:191]\n\t" \
    "v_mul_f32 v211, v199, v207\n\t" \
    "v_mul_f32 v212, v200, v207\n\t" \
    "s_waitcnt lgkmcnt(5)\n\t" \
    "v_mfma_f32_16x16x32_bf16 v[188:191], v[56:59], v[160:163], v[188:191]\n\t" \
    "v_mul_f32 v213, v201, v207\n\t" \
    "v_mul_f32 v214, v202, v207\n\t" \
    "s_waitcnt lgkmcnt(4)\n\t" \
    "v_mfma_f32_16x16x32_bf16 v[188:191], v[60:63], v[164:167], v[188:191]\n\t" \
    "v_mul_f32 v215, v203, v207\n\t" \
    "v_mov_b64 v[216:217], -1\n\t" \
    "v_mfma_f32_16x16x32_bf16 v[192:195], v[136:139], v[160:163], v[192:195]\n\t" \
    "v_mov_b64 v[218:219], -1\n\t" \
    "buffer_store_dwordx4 v[216:219], %[l16], %[rs], %[sa] offen sc1\n\t" \
    "v_mfma_f32_16x16x32_bf16 v[192:195], v[140:143], v[164:167], v[192:195]\n\t" \
    "s_waitcnt lgkmcnt(3)\n\t" \
    "v_mfma_f32_16x16x32_bf16 v[192:195], v[144:147], v[168:171], v[192:195]\n\t" \
    "s_waitcnt lgkmcnt(2)\n\t" \
    "v_mfma_f32_16x16x32_bf16 v[192:195], v[148:151], v[172:175], v[192:195]\n\t" \
    "v_mfma_f32_16x16x32_bf16 v[188:191], v[64:67], v[168:171], v[188:191]\n\t" \
    "v_mfma_f32_16x16x32_bf16 v[188:191], v[68:71], v[172:175], v[188:191]\n\t" \
    "s_waitcnt lgkmcnt(1)\n\t" \
    "v_mfma_f32_16x16x32_bf16 v[188:191], v[72:75], v[176:179], v[188:191]\n\t" \
    "s_waitcnt lgkmcnt(0)\n\t" \
    "v_mfma_f32_16x16x32_bf16 v[188:191], v[76:79], v[180:183], v[188:191]\n\t" \
    "v_mfma_f32_16x16x32_bf16 v[192:195], v[152:155], v[176:179], v[192:195]\n\t" \
    "v_mfma_f32_16x16x32_bf16 v[192:195], v[156:159], v[180:183], v[192:195]\n\t" \
    "s_setprio 2\n\t" \
    "buffer_load_dwordx4 v[232:235], %[l16], %[rs], %[n0] offen offset:0 sc1\n\t" \
    "buffer_load_dwordx4 v[236:239], %[l16], %[rs], %[n0] offen offset:1024 sc1\n\t" \
    "buffer_load_dwordx4 v[240:243], %[l16], %[rs], %[n0] offen offset:2048 sc1\n\t" \
    "buffer_load_dwordx4 v[244:247], %[l16], %[rs], %[n0] offen offset:3072 sc1\n\t" \
    "buffer_load_dwordx4 v[248:251], %[l16], %[rs], %[n1] offen offset:0 sc1\n\t" \
    ASM_ST3 \
    "v_fma_f32 v188, v188, v206, v208\n\t" \
    "v_fma_f32 v189, v189, v206, v209\n\t" \
    "v_fma_f32 v190, v190, v206, v210\n\t" \
    "v_fma_f32 v191, v191, v206, v211\n\t" \
    "v_mul_f32 v160, v188, v188\n\t" \
    "v_mul_f32 v161, v189, v189\n\t" \
    "v_mul_f32 v162, v190, v190\n\t" \
    "v_mul_f32 v163, v191, v191\n\t" \
    "v_fma_f32 v160, v160, %[k1], v220\n\t" \
    "v_fma_f32 v161, v161, %[k1], v220\n\t" \
    "v_fma_f32 v162, v162, %[k1], v220\n\t" \
    "v_fma_f32 v163, v163, %[k1], v220\n\t" \
    "v_mul_f32 v160, v188, v160\n\t" \
    "v_mul_f32 v161, v189, v161\n\t" \
    "v_mul_f32 v162, v190, v162\n\t" \
    "v_mul_f32 v163, v191, v163\n\t" \
    "v_fma_f32 v192, v192, v206, v212\n\t" \
    "v_fma_f32 v193, v193, v206, v213\n\t" \
    "v_fma_f32 v194, v194, v206, v214\n\t" \
    "v_fma_f32 v195, v195, v206, v215\n\t" \
    "v_mul_f32 v164, v192, v192\n\t" \
    "v_mul_f32 v165, v193, v193\n\t" \
    "v_mul_f32 v166, v194, v194\n\t" \
    "v_mul_f32 v167, v195, v195\n\t" \
    "v_fma_f32 v164, v164, %[k1], v220\n\t" \
    "v_fma_f32 v165, v165, %[k1], v220\n\t" \
    "v_fma_f32 v166, v166, %[k1], v220\n\t" \
    "v_fma_f32 v167, v167, %[k1], v220\n\t" \
    "v_mul_f32 v164, v192, v164\n\t" \
    "v_mul_f32 v165, v193, v165\n\t" \
    "v_mul_f32 v166, v194, v166\n\t" \
    "v_mul_f32 v167, v195, v167\n\t" \
    "v_exp_f32 v160, v160\n\t" \
    "v_exp_f32 v161, v161\n\t" \
    "v_exp_f32 v162, v162\n\t" \
    "v_exp_f32 v163, v163\n\t" \
    "v_exp_f32 v164, v164\n\t" \
    "v_exp_f32 v165, v165\n\t" \
    "v_exp_f32 v166, v166\n\t" \
    "v_exp_f32 v167, v167\n\t" \
    "v_add_f32 v160, 1.0, v160\n\t" \
    "v_add_f32 v161, 1.0, v161\n\t" \
    "v_add_f32 v162, 1.0, v162\n\t" \
    "v_add_f32 v163, 1.0, v163\n\t" \
    "v_add_f32 v164, 1.0, v164\n\t" \
    "v_add_f32 v165, 1.0, v165\n\t" \
    "v_add_f32 v166, 1.0, v166\n\t" \
    "v_add_f32 v167, 1.0, v167\n\t" \
    "v_rcp_f32 v160, v160\n\t" \
    "v_rcp_f32 v161, v161\n\t" \
    "v_rcp_f32 v162, v162\n\t" \
    "v_rcp_f32 v163, v163\n\t" \
    "v_rcp_f32 v164, v164\n\t" \
    "v_rcp_f32 v165, v165\n\t" \
    "v_rcp_f32 v166, v166\n\t" \
    "v_rcp_f32 v167, v167\n\t" \
    "v_mul_f32 v188, v188, v160\n\t" \
    "v_mul_f32 v189, v189, v161\n\t" \
    "v_mul_f32 v190, v190, v162\n\t" \
    "v_mul_f32 v191, v191, v163\n\t" \
    "v_mul_f32 v192, v192, v164\n\t" \
    "v_mul_f32 v193, v193, v165\n\t" \
    "v_mul_f32 v194, v194, v166\n\t" \
    "v_mul_f32 v195, v195, v167\n\t" \
    "v_cvt_pk_bf16_f32 v188, v188, v189\n\t" \
    "v_cvt_pk_bf16_f32 v189, v190, v191\n\t" \
    "v_cvt_pk_bf16_f32 v190, v192, v193\n\t" \
    "v_cvt_pk_bf16_f32 v191, v194, v195\n\t" \
    "v_min_u32 v188, -2, v188\n\t" \
    "v_min_u32 v189, -2, v189\n\t" \
    "v_min_u32 v190, -2, v190\n\t" \
    "v_min_u32 v191, -2, v191\n\t" \
    "v_mov_b32 v216, 0x3f803f80\n\t" \
    "v_mov_b32 v217, 0x3f803f80\n\t" \
    "v_mov_b32 v218, 0x3f803f80\n\t" \
    "v_mov_b32 v219, 0x3f803f80\n\t" \
    "v_permlane16_swap_b32 v188, v190\n\t" \
    "v_permlane16_swap_b32 v189, v191\n\t" \
    "s_waitcnt vmcnt(6)\n\t" \
    "buffer_store_dwordx4 v[188:191], %[oa], %[rs], %[so] offen sc1\n\t" \
    "s_nop 1\n\t" \
    "s_setprio 0\n\t"
#define ASM_WT3_HI \
    "s_setprio 1\n\t" \
    "s_barrier\n\t" ASM_ST \
    "ds_read_b128 v[160:163], %[la] offset:0\n\t" \
    "ds_read_b128 v[164:167], %[la] offset:1024\n\t" \
    "ds_read_b128 v[168:171], %[la] offset:2048\n\t" \
    "ds_read2st64_b32 v[204:205], %[sr] offset0:20 offset1:24\n\t" \
    "ds_read_b128 v[172:175], %[la] offset:3072\n\t" \
    "ds_read_b128 v[176:179], %[la] offset:8192\n\t" \
    "ds_read_b128 v[180:183], %[la] offset:9216\n\t" \
    "ds_read_b128 v[184:187], %[la] offset:10240\n\t" \
    "s_waitcnt lgkmcnt(7)\n\t" \
    "v_mfma_f32_16x16x32_bf16 v[192:195], v[80:83], v[160:163], 0\n\t" \
    "s_waitcnt lgkmcnt(6)\n\t" \
    "v_mfma_f32_16x16x32_bf16 v[192:195], v[84:87], v[164:167], v[192:195]\n\t" \
    "v_mfma_f32_16x16x32_bf16 v[188:191], v[0:3], v[160:163], 0\n\t" \
    "ds_read_b128 v[160:163], %[la] offset:11264\n\t" \
    "v_mfma_f32_16x16x32_bf16 v[188:191], v[4:7], v[164:167], v[188:191]\n\t" \
    "ds_read_b128 v[164:167], %[la] offset:16384\n\t" \
    "s_waitcnt lgkmcnt(7)\n\t" \
    "v_mfma_f32_16x16x32_bf16 v[188:191], v[8:11], v[168:171], v[188:191]\n\t" \
    "s_waitcnt lgkmcnt(6)\n\t" \
    "s_waitcnt lgkmcnt(5)\n\t" \
    "v_mfma_f32_16x16x32_bf16 v[188:191], v[12:15], v[172:175], v[188:191]\n\t" \
    "v_permlane16_swap_b32 v204, v205\n\t" \
    "v_mfma_f32_16x16x32_bf16 v[192:195], v[88:91], v[168:171], v[192:195]\n\t" \
    "ds_read_b128 v[168:171], %[la] offset:17408\n\t" \
    "v_mfma_f32_16x16x32_bf16 v[192:195], v[92:95], v[172:175], v[192:195]\n\t" \
    "ds_read_b128 v[172:175], %[la] offset:18432\n\t" \
    "v_add_f32 v206, v204, v205\n\t" \
    "s_waitcnt lgkmcnt(6)\n\t" \
    "v_mfma_f32_16x16x32_bf16 v[192:195], v[96:99], v[176:179], v[192:195]\n\t" \
    "v_add_f32 v207, v204, v205\n\t" \
    "s_waitcnt lgkmcnt(5)\n\t" \
    "v_mfma_f32_16x16x32_bf16 v[192:195], v[100:103], v[180:183], v[192:195]\n\t" \
    "v_mfma_f32_16x16x32_bf16 v[188:191], v[16:19], v[176:179], v[188:191]\n\t" \
    "ds_read_b128 v[176:179], %[la] offset:19456\n\t" \
    "v_permlane32_swap_b32 v206, v207\n\t" \
    "v_mfma_f32_16x16x32_bf16 v[188:191], v[20:23], v[180:183], v[188:191]\n\t" \
    "ds_read_b128 v[180:183], %[la] offset:24576\n\t" \
    "s_waitcnt lgkmcnt(6)\n\t" \
    "v_mfma_f32_16x16x32_bf16 v[188:191], v[24:27], v[184:187], v[188:191]\n\t" \
    "v_add_f32 v204, v206, v207\n\t" \
    "s_waitcnt lgkmcnt(5)\n\t" \
    "v_mfma_f32_16x16x32_bf16 v[188:191], v[28:31], v[160:163], v[188:191]\n\t" \
    "v_add_f32 v205, v206, v207\n\t" \
    "v_mfma_f32_16x16x32_bf16 v[192:195], v[104:107], v[184:187], v[192:195]\n\t" \
    "ds_read_b128 v[184:187], %[la] offset:25600\n\t" \
    "v_mfma_f32_16x16x32_bf16 v[192:195], v[108:111], v[160:163], v[192:195]\n\t" \
    "ds_read_b128 v[160:163], %[la] offset:26624\n\t" \
    "v_permlane16_swap_b32 v204, v205\n\t" \
    "s_waitcnt lgkmcnt(6)\n\t" \
    "v_mfma_f32_16x16x32_bf16 v[192:195], v[112:115], v[164:167], v[192:195]\n\t" \
    "s_waitcnt lgkmcnt(5)\n\t" \
    "v_mfma_f32_16x16x32_bf16 v[192:195], v[116:119], v[168:171], v[192:195]\n\t" \
    "v_mul_f32 v205, %[kd], v205\n\t" \
    "v_mfma_f32_16x16x32_bf16 v[188:191], v[32:35], v[164:167], v[188:191]\n\t" \
    "ds_read_b128 v[164:167], %[la] offset:27648\n\t" \
    "v_mul_f32 v204, %[kd], v204\n\t" \
    "v_mfma_f32_16x16x32_bf16 v[188:191], v[36:39], v[168:171], v[188:191]\n\t" \
    "ds_read_b128 v[168:171], %[la] offset:4096\n\t" \
    "v_fma_f32 v204, -v205, v205, v204\n\t" \
    "s_waitcnt lgkmcnt(6)\n\t" \
    "v_mfma_f32_16x16x32_bf16 v[188:191], v[40:43], v[172:175], v[188:191]\n\t" \
    "v_max_f32 v204, 0, v204\n\t" \
    "s_waitcnt lgkmcnt(5)\n\t" \
    "v_mfma_f32_16x16x32_bf16 v[188:191], v[44:47], v[176:179], v[188:191]\n\t" \
    "v_add_f32 v206, %[ke], v204\n\t" \
    "v_mfma_f32_16x16x32_bf16 v[192:195], v[120:123], v[172:175], v[192:195]\n\t" \
    "ds_read_b128 v[172:175], %[la] offset:12288\n\t" \
    "v_rsq_f32 v206, v206\n\t" \
    "v_mfma_f32_16x16x32_bf16 v[192:195], v[124:127], v[176:179], v[192:195]\n\t" \
    "ds_read_b128 v[176:179], %[la] offset:20480\n\t" \
    "s_waitcnt lgkmcnt(6)\n\t" \
    "v_mfma_f32_16x16x32_bf16 v[192:195], v[128:131], v[180:183], v[192:195]\n\t" \
    "v_mul_f32 v207, -v205, v206\n\t" \
    "s_waitcnt lgkmcnt(5)\n\t" \
    "v_mfma_f32_16x16x32_bf16 v[192:195], v[132:135], v[184:187], v[192:195]\n\t" \
    "v_mul_f32 v208, v196, v207\n\t" \
    "v_mfma_f32_16x16x32_bf16 v[188:191], v[48:51], v[180:183], v[188:191]\n\t" \
    "ds_read_b128 v[180:183], %[la] offset:28672\n\t" \
    "v_mul_f32 v209, v197, v207\n\t" \
    "v_mul_f32 v210, v198, v207\n\t" \
    "v_mfma_f32_16x16x32_bf16 v[188:191], v[52:55], v[184:187], v[188:191]\n\t" \
    "v_mul_f32 v211, v199, v207\n\t" \
    "v_mul_f32 v212, v200, v207\n\t" \
    "s_waitcnt lgkmcnt(5)\n\t" \
    "v_mfma_f32_16x16x32_bf16 v[188:191], v[56:59], v[160:163], v[188:191]\n\t" \
    "v_mul_f32 v213, v201, v207\n\t" \
    "v_mul_f32 v214, v202, v207\n\t" \
    "s_waitcnt lgkmcnt(4)\n\t" \
    "v_mfma_f32_16x16x32_bf16 v[188:191], v[60:63], v[164:167], v[188:191]\n\t" \
    "v_mul_f32 v215, v203, v207\n\t" \
    "v_mov_b64 v[216:217], -1\n\t" \
    "v_mfma_f32_16x16x32_bf16 v[192:195], v[136:139], v[160:163], v[192:195]\n\t" \
    "v_mov_b64 v[218:219], -1\n\t" \
    "buffer_store_dwordx4 v[216:219], %[l16], %[rs], %[sa] offen sc1\n\t" \
    "v_mfma_f32_16x16x32_bf16 v[192:195], v[140:143], v[164:167], v[192:195]\n\t" \
    "s_waitcnt lgkmcnt(3)\n\t" \
    "v_mfma_f32_16x16x32_bf16 v[192:195], v[144:147], v[168:171], v[192:195]\n\t" \
    "s_waitcnt lgkmcnt(2)\n\t" \
    "v_mfma_f32_16x16x32_bf16 v[192:195], v[148:151], v[172:175], v[192:195]\n\t" \
    "v_mfma_f32_16x16x32_bf16 v[188:191], v[64:67], v[168:171], v[188:191]\n\t" \
    "v_mfma_f32_16x16x32_bf16 v[188:191], v[68:71], v[172:175], v[188:191]\n\t" \
    "s_waitcnt lgkmcnt(1)\n\t" \
    "v_mfma_f32_16x16x32_bf16 v[188:191], v[72:75], v[176:179], v[188:191]\n\t" \
    "s_waitcnt lgkmcnt(0)\n\t" \
    "v_mfma_f32_16x16x32_bf16 v[188:191], v[76:79], v[180:183], v[188:191]\n\t" \
    "v_mfma_f32_16x16x32_bf16 v[192:195], v[152:155], v[176:179], v[192:195]\n\t" \
    "v_mfma_f32_16x16x32_bf16 v[192:195], v[156:159], v[180:183], v[192:195]\n\t" \
    ASM_ST3 \
    "s_nop 1\n\t" \
    "v_fma_f32 v188, v188, v206, v208\n\t" \
    "v_fma_f32 v189, v189, v206, v209\n\t" \
    "v_fma_f32 v190, v190, v206, v210\n\t" \
    "v_fma_f32 v191, v191, v206, v211\n\t" \
    "v_mul_f32 v160, v188, v188\n\t" \
    "v_mul_f32 v161, v189, v189\n\t" \
    "v_mul_f32 v162, v190, v190\n\t" \
    "v_mul_f32 v163, v191, v191\n\t" \
    "v_fma_f32 v160, v160, %[k1], v220\n\t" \
    "v_fma_f32 v161, v161, %[k1], v220\n\t" \
    "v_fma_f32 v162, v162, %[k1], v220\n\t" \
    "v_fma_f32 v163, v163, %[k1], v220\n\t" \
    "v_mul_f32 v160, v188, v160\n\t" \
    "v_mul_f32 v161, v189, v161\n\t" \
    "v_mul_f32 v162, v190, v162\n\t" \
    "v_mul_f32 v163, v191, v163\n\t" \
    "v_fma_f32 v192, v192, v206, v212\n\t" \
    "v_fma_f32 v193, v193, v206, v213\n\t" \
    "v_fma_f32 v194, v194, v206, v214\n\t" \
    "v_fma_f32 v195, v195, v206, v215\n\t" \
    "v_mul_f32 v164, v192, v192\n\t" \
    "v_mul_f32 v165, v193, v193\n\t" \
    "v_mul_f32 v166, v194, v194\n\t" \
    "v_mul_f32 v167, v195, v195\n\t" \
    "v_fma_f32 v164, v164, %[k1], v220\n\t" \
    "v_fma_f32 v165, v165, %[k1], v220\n\t" \
    "v_fma_f32 v166, v166, %[k1], v220\n\t" \
    "v_fma_f32 v167, v167, %[k1], v220\n\t" \
    "v_mul_f32 v164, v192, v164\n\t" \
    "v_mul_f32 v165, v193, v165\n\t" \
    "v_mul_f32 v166, v194, v166\n\t" \
    "v_mul_f32 v167, v195, v167\n\t" \
    "v_exp_f32 v160, v160\n\t" \
    "v_exp_f32 v161, v161\n\t" \
    "v_exp_f32 v162, v162\n\t" \
    "v_exp_f32 v163, v163\n\t" \
    "v_exp_f32 v164, v164\n\t" \
    "v_exp_f32 v165, v165\n\t" \
    "v_exp_f32 v166, v166\n\t" \
    "v_exp_f32 v167, v167\n\t" \
    "v_add_f32 v160, 1.0, v160\n\t" \
    "v_add_f32 v161, 1.0, v161\n\t" \
    "v_add_f32 v162, 1.0, v162\n\t" \
    "v_add_f32 v163, 1.0, v163\n\t" \
    "v_add_f32 v164, 1.0, v164\n\t" \
    "v_add_f32 v165, 1.0, v165\n\t" \
    "v_add_f32 v166, 1.0, v166\n\t" \
    "v_add_f32 v167, 1.0, v167\n\t" \
    "v_rcp_f32 v160, v160\n\t" \
    "v_rcp_f32 v161, v161\n\t" \
    "v_rcp_f32 v162, v162\n\t" \
    "v_rcp_f32 v163, v163\n\t" \
    "v_rcp_f32 v164, v164\n\t" \
    "v_rcp_f32 v165, v165\n\t" \
    "v_rcp_f32 v166, v166\n\t" \
    "v_rcp_f32 v167, v167\n\t" \
    "v_mul_f32 v188, v188, v160\n\t" \
    "v_mul_f32 v189, v189, v161\n\t" \
    "v_mul_f32 v190, v190, v162\n\t" \
    "v_mul_f32 v191, v191, v163\n\t" \
    "v_mul_f32 v192, v192, v164\n\t" \
    "v_mul_f32 v193, v193, v165\n\t" \
    "v_mul_f32 v194, v194, v166\n\t" \
    "v_mul_f32 v195, v195, v167\n\t" \
    "v_cvt_pk_bf16_f32 v188, v188, v189\n\t" \
    "v_cvt_pk_bf16_f32 v189, v190, v191\n\t" \
    "v_cvt_pk_bf16_f32 v190, v192, v193\n\t" \
    "v_cvt_pk_bf16_f32 v191, v194, v195\n\t" \
    "v_min_u32 v188, -2, v188\n\t" \
    "v_min_u32 v189, -2, v189\n\t" \
    "v_min_u32 v190, -2, v190\n\t" \
    "v_min_u32 v191, -2, v191\n\t" \
    "s_nop 1\n\t" \
    "v_permlane16_swap_b32 v188, v190\n\t" \
    "v_permlane16_swap_b32 v189, v191\n\t" \
    "s_waitcnt vmcnt(1)\n\t" \
    "buffer_store_dwordx4 v[188:191], %[oa], %[rs], %[so] offen sc1\n\t" \
    "s_nop 1\n\t"
#define ASM_POLL5_I \
    "buffer_load_dwordx4 v[232:235], %[l16], %[rs], %[s0] offen offset:0 sc1\n\t" \
    "buffer_load_dwordx4 v[236:239], %[l16], %[rs], %[s0] offen offset:1024 sc1\n\t" \
    "buffer_load_dwordx4 v[240:243], %[l16], %[rs], %[s0] offen offset:2048 sc1\n\t" \
    "buffer_load_dwordx4 v[244:247], %[l16], %[rs], %[s0] offen offset:3072 sc1\n\t" \
    "buffer_load_dwordx4 v[248:251], %[l16], %[rs], %[s1] offen offset:0 sc1\n\t"
using u32x8 = __attribute__((ext_vector_type(8))) unsigned;
using u32x16 = __attribute__((ext_vector_type(16))) unsigned;
using u32x32 = __attribute__((ext_vector_type(32))) unsigned;
struct WRegs { u32x32 w[6]; u32x8 t; };
#define WREGS(W) "{v[0:31]}"(W.w[0]), "{v[32:63]}"(W.w[1]), "{v[64:95]}"(W.w[2]), "{v[96:127]}"(W.w[3]), "{v[128:159]}"(W.w[4]), "{v[160:191]}"(W.w[5]), "{v[192:199]}"(W.t)
__device__ __forceinline__ u32x32 cat32(u32x8 a, u32x8 b, u32x8 c, u32x8 d) {
    const u32x16 lo = __builtin_shufflevector(a, b, 0, 1, 2, 3, 4, 5, 6, 7, 8, 9, 10, 11, 12, 13, 14, 15);
    const u32x16 hi = __builtin_shufflevector(c, d, 0, 1, 2, 3, 4, 5, 6, 7, 8, 9, 10, 11, 12, 13, 14, 15);
    return __builtin_shufflevector(lo, hi, 0, 1, 2, 3, 4, 5, 6, 7, 8, 9, 10, 11, 12, 13, 14, 15, 16, 17, 18, 19, 20, 21, 22, 23, 24, 25, 26, 27, 28, 29, 30, 31);
}
#define WOUT(W) "=&{v[0:31]}"(W.w[0]), "=&{v[32:63]}"(W.w[1]), "=&{v[64:95]}"(W.w[2]), "=&{v[96:127]}"(W.w[3]), "=&{v[128:159]}"(W.w[4]), "=&{v[160:191]}"(W.w[5]), "=&{v[192:199]}"(W.t)
#define WTMP "memory", "scc", "v200", "v201", "v202", "v203", "v204", "v205", "v206", "v207", "v208", "v209", "v210", "v211", "v212", "v213", "v214", "v215", "v216", "v217", "v218", "v219", "v220", "v221", "v222", "v223", "v224", "v225", "v226", "v227", "v228", "v229", "v230", "v231", "v232", "v233", "v234", "v235", "v236", "v237", "v238", "v239"
template <bool NT>
__device__ __forceinline__ void load_w3(WRegs& W, const bf16_t* m, int wave, unsigned c0, bool hq) {
    const int ln = lane_id();
    unsigned o[3], s[3], t;
#pragma unroll
    for (int k = 0; k < 3; ++k) {
        const int row = (k * 64 + ln) / 24, sg = (k * 64 + ln) % 24;
        o[k] = row * (HD * 2) + c0 + (hq ? ((sg >> 1) & 1) * 384 + ((0x140 >> ((sg >> 3) * 4)) & 15) * 32 + (sg & 5) * 16 : sg * 16); s[k] = wave * 13824 + row * 416 + sg * 16;
    }
    const unsigned tr = (ln >> 4) * 3456 + ((ln >> 2) & 3) * 416 + (wave & 3) * 32 + (ln & 3) * 8;
#undef WNT
#define WNT " nt"
    if constexpr (NT) {
    asm volatile(ASM_WLOAD3 : WOUT(W), [t] "=&s"(t) : [rs] "s"(mkrsrc(m, DM * HD * 2)), [wx] "s"(wave), [o0] "v"(o[0]), [o1] "v"(o[1]), [o2] "v"(o[2]), [s0] "v"(s[0]), [s1] "v"(s[1]), [s2] "v"(s[2]),
                 [t0] "v"(tr + wave * 13824), [t1] "v"(tr + (wave ^ 1) * 13824), [t2] "v"(tr + (wave ^ 2) * 13824), [t3] "v"(tr + (wave ^ 3) * 13824), [t4] "v"(tr + (wave & 4) * 13824),
                 [wl] "v"(L_W + wave * 10240 + ln * 16) : WTMP);
    } else {
#undef WNT
#define WNT ""
    asm volatile(ASM_WLOAD3 : WOUT(W), [t] "=&s"(t) : [rs] "s"(mkrsrc(m, DM * HD * 2)), [wx] "s"(wave), [o0] "v"(o[0]), [o1] "v"(o[1]), [o2] "v"(o[2]), [s0] "v"(s[0]), [s1] "v"(s[1]), [s2] "v"(s[2]),
                 [t0] "v"(tr + wave * 13824), [t1] "v"(tr + (wave ^ 1) * 13824), [t2] "v"(tr + (wave ^ 2) * 13824), [t3] "v"(tr + (wave ^ 3) * 13824), [t4] "v"(tr + (wave & 4) * 13824),
                 [wl] "v"(L_W + wave * 10240 + ln * 16) : WTMP);
    }
}
#undef WNT
#define WNT " nt"
__device__ __forceinline__ void load_wo(WRegs& W, const bf16_t* m, int wave, int h) {
    const int ln = lane_id();
    unsigned o[5], s[5], t;
#pragma unroll
    for (int k = 0; k < 5; ++k) {
        const int row = (k * 64 + ln) / 20, sg = (k * 64 + ln) % 20;
        o[k] = (h * DH + row) * (DM * 2) + sg * 16 + (wave & 3) * 320; s[k] = wave * 13824 + row * 352 + sg * 16;
    }
    asm volatile(ASM_WLOADO : WOUT(W), [t] "=&s"(t) : [rs] "s"(mkrsrc(m, DM * HD * 2)), [o0] "v"(o[0]), [o1] "v"(o[1]), [o2] "v"(o[2]), [o3] "v"(o[3]), [o4] "v"(o[4]),
                 [s0] "v"(s[0]), [s1] "v"(s[1]), [s2] "v"(s[2]), [s3] "v"(s[3]), [s4] "v"(s[4]), [tr] "v"(wave * 13824 + (ln >> 2) * 352 + (ln & 3) * 8), [wl] "v"(L_W + wave * 10240 + ln * 16) : WTMP);
}
using s16x4 = __attribute__((ext_vector_type(4))) short;
template <int NT, int KS, int N>
__device__ __forceinline__ void load_wn(u32x4 (&f)[NT * KS], const bf16_t* m, int rows, int wave, int c0, int k0) {
    const int ln = lane_id();
    const rsrc_t rs = mkrsrc(m, rows * N * 2);
#pragma unroll
    for (int k = 0; k < KS; ++k)
#pragma unroll
        for (int n = 0; n < NT; ++n) f[n * KS + k] = __builtin_amdgcn_raw_buffer_load_b128(rs, ((n * 64 + ln) / (2 * NT)) * (N * 2) + ((n * 64 + ln) % (2 * NT)) * 16, (k0 + k) * (N * 64) + c0 * 32, 0);
    const unsigned st = wave * 10240 + ln * 16, tr = wave * 10240 + ((ln >> 4) * 8 + ((ln >> 2) & 3)) * (NT * 32) + (ln & 3) * 8;
#pragma unroll
    for (int k = 0; k < KS; ++k) {
#pragma unroll
        for (int n = 0; n < NT; ++n) lds_st<u32x4>(st + n * 1024, f[n * KS + k]);
        asm volatile("" ::: "memory");
#pragma unroll
        for (int n = 0; n < NT; ++n) {
            const u32x2 a = __builtin_bit_cast(u32x2, __builtin_amdgcn_ds_read_tr16_b64_v4i16((LAS s16x4*)(tr + n * 32)));
            const u32x2 b = __builtin_bit_cast(u32x2, __builtin_amdgcn_ds_read_tr16_b64_v4i16((LAS s16x4*)(tr + n * 32 + NT * 128)));
            f[n * KS + k] = (u32x4){a[0], a[1], b[0], b[1]};
        }
        asm volatile("" ::: "memory");
    }
}
struct C1Regs { u32x8 lw0, lw1, z1; u32x4 z0; };
#define XG_OUT "=&{v[200:207]}"(C.lw0), "=&{v[224:227]}"(C.z0), "=&{v[228:235]}"(C.lw1), "=&{v[244:251]}"(C.z1)
#define XG_IN WREGS(W), [xa] "v"(xa), [wl] "v"(wl), [qs] "s"(qs)
#define XG_CLOB "memory", "scc", "v208", "v209", "v210", "v211", "v212", "v213", "v214", "v215", "v216", "v217", "v218", "v219", "v220", "v221", "v222", "v223", "v236", "v237", "v238", "v239", "v240", "v241", "v242", "v243", "v252"
constexpr int XS0 = 8;
__device__ __forceinline__ unsigned init_wait(const Params& P, int c, int ph, unsigned need = 0x220080u) {
    unsigned w;
    for (int spins = 0;; ++spins) {
        unsigned o;
        asm volatile("v_mov_b32 %0, 0" : "=v"(o));
        w = __builtin_amdgcn_readfirstlane(__builtin_amdgcn_raw_buffer_load_b32(mkrsrc(P.ws + FL0, FL_BYTES), o, FL_IN + (ph * 8 + (c & 7)) * 128, 16));
        if ((w & need) == need) break;
        __builtin_amdgcn_s_sleep(8);
        if (spin_fail(spins, P)) break;
    }
    asm volatile("" ::: "memory");
    return w;
}
#define PLACE() const unsigned pw_ = init_wait(P, wave, L); const int par = (L ^ (pw_ >> 16)) & 1, cpy = x ^ ((pw_ >> 20) & 1); const rsrc_t rr = mkrsrc(P.ws + par * 8192, RG_L); (void)cpy; (void)rr
__device__ __forceinline__ void xn_wait(const Params& P, rsrc_t rr, int seg, int cpy) {
    for (int spins = 0;; ++spins) {
        const unsigned f = __builtin_amdgcn_raw_buffer_load_b32(rr, (lane_id() & 15) * 128, RG_FX + (cpy & 3) * 2048, 16);
        if (__all(f > (unsigned)seg)) break;
        __builtin_amdgcn_s_sleep(4);
        if (spin_fail(spins, P)) break;
    }
    asm volatile("" ::: "memory");
}
__device__ __forceinline__ unsigned c1_dst(int wave, bool ktype) {
    const int ln = lane_id(), l15 = ln & 15, hf = (wave >> 1) & 1;
    const int rb16 = ((ln >> 2) & 4) * 16 + (ln >> 5) * 32;
    return (ktype ? L_KM + (l15 >> 2) * 128 + hf * 8 + (l15 & 3) * 2
                  : L_Q + (l15 >> 3) * 256 + ((l15 >> 1) & 1) * 128 + ((l15 >> 2) & 1) * 8 + hf * 4 + (l15 & 1) * 2) + (wave & 1) * 1536 + rb16;
}
template <int NP>
__device__ __forceinline__ void crit1(const WRegs& W, C1Regs& C, rsrc_t rr, unsigned sbase, int wave, unsigned wl, unsigned dsto, unsigned& budget, unsigned& fv, unsigned fs
) {
    unsigned abase;
    asm volatile("v_and_b32 %0, 0x3ff, %1\n\tv_or_b32 %0, %2, %0" : "=&v"(abase) : "v"(wl), "s"(wave * 1024));
    ASM_ST_DECL;
#define C1_OUT "+{v[224:227]}"(C.z0), "+{v[244:251]}"(C.z1), "+{v[200:207]}"(C.lw0), "+{v[228:235]}"(C.lw1)
#define C1_IN WREGS(W), [ab] "v"(abase), [wl] "v"(wl), [dst] "v"(dsto)
#define C1_CLOB "memory", "vcc", "scc", "v208", "v209", "v210", "v211", "v212", "v213", "v214", "v215", "v216", "v217", "v218", "v219", "v220", "v221", "v222", "v223", "v236", "v237", "v238", "v239", "v240", "v241", "v242", "v243"
#define C1_POLL [rs] "s"(rr), [s0] "s"(sbase), [s1] "s"(sbase + 4096), [s2] "s"(sbase + 2 * BLK)
    ST(1);
    if constexpr (NP == 13) asm volatile(ASM_CRIT1_V3 "s_waitcnt vmcnt(0)" : C1_OUT, [n] "+s"(budget), [fv] "+v"(fv) ASM_ST_OUT ASM_ST2_OUT : C1_IN, C1_POLL, [fs] "s"(fs) : C1_CLOB);
    else if constexpr (NP == 12) asm volatile(ASM_CRIT1_V2 "s_waitcnt vmcnt(0)" : C1_OUT, [n] "+s"(budget), [fv] "+v"(fv) ASM_ST_OUT ASM_ST2_OUT : C1_IN, C1_POLL, [fs] "s"(fs) : C1_CLOB);
    else if constexpr (NP == 3) asm volatile(ASM_CRIT1_3 : C1_OUT, [n] "+s"(budget) ASM_ST_OUT ASM_ST2_OUT : C1_IN, C1_POLL : C1_CLOB);
    else if constexpr (NP == 2) asm volatile(ASM_CRIT1_2 : C1_OUT, [n] "+s"(budget) ASM_ST_OUT ASM_ST2_OUT : C1_IN, C1_POLL : C1_CLOB);
    else asm volatile(ASM_CRIT1_N : C1_OUT ASM_ST_OUT : C1_IN : C1_CLOB);
    SB();
    ASM_ST_SAVE(3);
    if constexpr (NP != 0) { ASM_ST2_SAVE(2); }
}
#define STARG

#define SRG 5
#undef STROLE
#define STROLE 1
__device__ __forceinline__ void s_role(const Params& P, int wave, int L, int h, int x) {
    const char* xn = P.ws + PG0 + PG_XN + L * PG_LX;
    auto xdma = [&](int seg, int hb, int par) {
        const unsigned l16 = lane_id() * 16;
        const int mt = (wave >> 1) & 1;
        const char* g = xn + pgb(seg, par);
        if (hb == 0) {
            dma4(g + blin(mt * 20480 + (wave & 1) * 4096), l16, L_XA + mt * 16384 + (wave & 1) * 4096);
            dmaq<0>(g + blin(mt * 20480 + 16384) + (wave & 1) * 1024, l16, L_XB + mt * 4096 + (wave & 1) * 1024);
        } else {
            dma4(g + blin(mt * 20480 + 8192), l16, L_XA + mt * 16384 + 8192); dma4(g + blin(mt * 20480 + 12288), l16, L_XA + mt * 16384 + 12288);
            dmaq<2>(g + blin(mt * 20480 + 16384), l16, L_XB + mt * 4096); dmaq<3>(g + blin(mt * 20480 + 16384), l16, L_XB + mt * 4096);
        }
    };
    const float SCL = 0.07216878364870322f * 1.4426950408889634f;
    auto run = [&](auto Cc) {
    constexpr int CLS = decltype(Cc)::value;
    WRegs wf;
    MARK(!L && h == 0 && wave == 0, 0);
    load_w3<(NTM & (CLS == 3 ? 2 : 1)) != 0>(wf, P.w[CLS == 3], wave, h * 384, false);
    MARKX(!L && wave == 0, 1);
    PLACE();
    if (CLS == 3) {
        if (L) xn_wait(P, rr, 0, cpy);
        xdma(0, 0, par);
        if (wave == 4 || wave == 7) xdma(0, 1, par);
        wait_vm<0>();
    }
    bar();
    if (CLS == 3 && wave == 7 && L) xn_wait(P, rr, 1, cpy);
    const unsigned qs = (CLS == 3 ? L_KX : L_QX) + (wave & 1) * 3072 + ((wave >> 1) & 1) * 8;
    unsigned xa, wl, dsto;
    if (CLS == 3) { xa = L_XA + wave * 2048 + lane_id() * 16; wl = L_W + wave * 10240 + lane_id() * 16; dsto = c1_dst(wave, true); }
    __builtin_amdgcn_s_setprio(CLS == 3 ? 1 : 2);
    unsigned budget = 1u << 18, tb2 = 0;
    STDECL;
    LDECL;
#pragma unroll 1
    for (int t = 0; t < NSEG; ++t) {
        const unsigned slot = t & (NSLOT - 1);
        C1Regs C;
        {
            const WRegs& W = wf;
            ASM_ST_DECL;
            if constexpr (CLS == 3) {
                const int mt = (wave >> 1) & 1;
                const char* g = xn + pgb(t + 1 < NSEG ? t + 1 : t, par);
                asm volatile(ASM_XG_S_HI : XG_OUT ASM_ST_OUT : XG_IN, [slp] "s"(SLP4), [g0] "s"(g + blin(mt * 20480 + (wave & 1) * 4096)), [d0] "s"(L_XA + mt * 16384 + (wave & 1) * 4096),
                             [g1] "s"(g + blin(mt * 20480 + 16384) + (wave & 1) * 1024), [d1] "s"(L_XB + mt * 4096 + (wave & 1) * 1024) : XG_CLOB);
                wait_vm<0>();
                LB(t);
            } else {
                const unsigned l16 = lane_id() * 16;
                xa = L_XA + wave * 2048 + l16; wl = L_W + wave * 10240 + l16;
#define XG_Q(A) asm volatile("s_cmp_lt_u32 %[t], 2\n\ts_cbranch_scc1 .Lq3%=\n\t" A "s_branch .Lqe%=\n\t.Lq3%=:\n\t" ASM_XG_S_LO ".Lqe%=:" : XG_OUT ASM_ST_OUT : XG_IN, [t] "s"(t) : XG_CLOB)
                if constexpr (CLS == 0 || CLS == 4) XG_Q(ASM_XG_S_LO1); else XG_Q(ASM_XG_S_LO2);
                __builtin_amdgcn_s_setprio(0);
                if constexpr (CLS == 0) {
                    const int ln = lane_id();
                    const u32x4 s4 = sent4();
                    const unsigned poff = (ln * 16 + ((ln < 8) ? h * 128 : (1 + (h >> 4)) * 2 * BLK - 128 + (h & 15) * 512)) | ((ln >= 40 || t < 2) ? 0x40000000u : 0u);
#pragma unroll
                    for (int c = 0; c < (int)NCP; ++c) st128(s4, rr, poff, RG_XP + ((t - 2) & (NSLOT - 1)) * XP_SLOT + (c >> 1) * XP_PAIR + (c & 1) * BLK);
                    const unsigned pbase = RG_PX + ((t - 3) & (NSLOT - 1)) * PX_SLOT + (h >> 2) * BLK;
                    const unsigned o2 = ((ln & 16) ? 5120 + ((h >> 1) & 1) * 1024 + (h & 1) * 256 + (ln >> 5) * 512 : 4096 + (h & 3) * 256 + (ln >> 5) * 3072) + (ln & 15) * 16;
                    if (t >= 3) { st128(s4, rr, ln * 16, pbase + (h & 3) * 1024); st128(s4, rr, o2, pbase); }
                }
                if (CLS == 4 && t >= HT0) {
                    const int ln = lane_id(), u = h >> 2;
                    for (int spins = 0;; ++spins) {
                        const unsigned fl = __builtin_amdgcn_raw_buffer_load_b32(rr, (ln & 3) * 128, RG_FH + BLK + u * 512, 16);
                        if (__all(fl > 2u * t + 1u)) break;
                        __builtin_amdgcn_s_sleep(2);
                        if (spin_fail(spins, P)) break;
                    }
                    asm volatile("" ::: "memory");
                    const char* g = P.ws + PG0 + pgb(t, par) + PG_HV + (16 + u) * 3 * BLK;
                    unsigned t0, t1, vo;
                    static_assert(L_QX == 16384, "");
                    asm volatile("s_add_u32 %0, %4, 0\n\ts_and_b32 %1, %0, 0xe000\n\ts_add_u32 %0, %0, %1\n\tv_add_u32 %2, %0, %5\n\ts_mov_b32 m0, 16384\n\ts_nop 0\n\tglobal_load_lds_dwordx4 %2, %3 sc1\n\t"
                                 "s_add_u32 %0, %4, 1024\n\ts_and_b32 %1, %0, 0xe000\n\ts_add_u32 %0, %0, %1\n\tv_add_u32 %2, %0, %5\n\ts_mov_b32 m0, 17408\n\ts_nop 0\n\tglobal_load_lds_dwordx4 %2, %3 sc1\n\t"
                                 "s_add_u32 %0, %4, 2048\n\ts_and_b32 %1, %0, 0xe000\n\ts_add_u32 %0, %0, %1\n\tv_add_u32 %2, %0, %5\n\ts_mov_b32 m0, 19456\n\ts_nop 0\n\tglobal_load_lds_dwordx4 %2, %3 sc1\n\t"
                                 "s_add_u32 %0, %4, 6144\n\ts_and_b32 %1, %0, 0xe000\n\ts_add_u32 %0, %0, %1\n\tv_add_u32 %2, %0, %5\n\ts_mov_b32 m0, 22528\n\ts_nop 0\n\tglobal_load_lds_dwordx4 %2, %3 sc1\n\t"
                                 "s_add_u32 %0, %4, 7168\n\ts_and_b32 %1, %0, 0xe000\n\ts_add_u32 %0, %0, %1\n\tv_add_u32 %2, %0, %5\n\ts_mov_b32 m0, 23552\n\ts_nop 0\n\tglobal_load_lds_dwordx4 %2, %3 sc1\n\t"
                                 "s_add_u32 %0, %4, 8192\n\ts_and_b32 %1, %0, 0xe000\n\ts_add_u32 %0, %0, %1\n\tv_add_u32 %2, %0, %5\n\ts_mov_b32 m0, 25600\n\ts_nop 0\n\tglobal_load_lds_dwordx4 %2, %3 sc1\n\t"
                                 : "=&s"(t0), "=&s"(t1), "=&v"(vo) : "s"(g), "s"((((h >> 1) & 1) * 12 + (h & 1) * 3) * 1024), "v"(ln * 16) : "memory", "scc");
                }
                dsto = c1_dst(wave, false);
                while ((unsigned)__builtin_amdgcn_s_memtime() - tb2 < STD) __builtin_amdgcn_s_sleep(2);
            }
            ASM_ST_SAVE(7);
            ST(6);
        }
        ST(0);
        unsigned fv = 0;
        if (CLS == 2) fv = (lane_id() & 15) * 128;
        crit1<CLS == 3 ? 0 : CLS < 2 ? 3 : CLS == 2 ? 12 : 2>(wf, C, rr, RG_XM + slot * XM_SLOT + (cpy >> 1) * XM_PAIR + (cpy & 1) * BLK, wave, wl, dsto, budget, fv, RG_FX + (cpy & 3) * 2048 STARG);
        if (CLS != 3) tb2 = (unsigned)__builtin_amdgcn_s_memtime();
        if constexpr (CLS == 0) {
            const unsigned sp = RG_XP + slot * XP_SLOT, sx = sp + (1 + (h >> 4)) * 2 * BLK + (h & 15) * 512;
            ASM_ST_DECL;
            asm volatile(ASM_SM : [c0] "+s"(budget) ASM_ST_OUT : WREGS(wf), [rr] "s"(rr), [sx] "s"(sx), [sy] "s"(sx + XP_PAIR), [sm] "s"(sp + h * 128), [cs] "s"(BLK), [cp] "s"(XP_PAIR), [c70] "s"(0x70), [scl] "s"(SCL)
                         : "memory", "v200", "v201", "v202", "v203", "v204", "v205", "v206", "v207", "v208", "v209", "v210", "v211", "v212", "v213", "v214", "v215", "v216", "v217", "v218", "v219", "v220", "v221", "v222", "v223", "v224", "v225", "v226", "v227", "v228", "v229", "v230", "v231", "v232", "v233", "v234", "v235", "v236", "v237", "v238", "v239", "v240", "v241", "v242", "v243", "v244", "v245", "v246", "v247", "v248", "v249", "v250", "v251", "v252", "v253", "v254", "v255");
            ASM_ST_SAVE(4);
            ST(5);
        } else if constexpr (CLS == 1 || CLS == 4) {
            const int ln = lane_id(), l15 = ln & 15, l4 = ln >> 4;
            constexpr int mt = (CLS == 4);
            const unsigned qa = L_QX + mt * 6144 + ln * 16, kmo = L_KM + l4 * 128 + (ln & 7) * 16;
            const unsigned xo = mt ? (h & 3) * 1024 : 5120 + ((h >> 1) & 1) * 1024 + (h & 1) * 256;
            const unsigned pm = l4 < 2 ? (mt ? 4096 : 7168) + (h & 3) * 256 - xo + l15 * 16 + l4 * 8 : OOB;
            const unsigned so = RG_PX + slot * PX_SLOT + (h >> 2) * BLK + xo;
            f32x4 sd;
#pragma unroll
            for (int r = 0; r < 4; ++r) sd[r] = (l4 * 4 + r > l15) ? -3.0e38f : 0.f;
            ASM_ST_DECL;
#define P4_ARGS : "+{v[240:243]}"(sd) ASM_ST_OUT : WREGS(wf), [qa] "v"(qa), [km] "v"(kmo), [pm] "v"(pm), [rr] "s"(rr), [so] "s"(so), [scl] "s"(SCL) : "memory", "v200", "v201", "v202", "v203", "v204", "v205", "v206", "v207", "v208", "v209", "v210", "v211", "v212", "v213", "v214", "v215", "v216", "v217", "v218", "v219", "v220", "v221", "v222", "v223", "v224", "v225", "v226", "v227", "v228", "v229", "v230", "v231", "v232", "v233", "v234", "v235", "v236", "v237", "v238", "v239", "v244", "v245", "v246", "v247", "v248", "v249", "v250", "v251"
            if constexpr (mt) asm volatile(ASM_P4_1 P4_ARGS); else asm volatile(ASM_P4_0 P4_ARGS);
            ASM_ST_SAVE(4);
            ST(5);
        } else {
            bar();
            ST(4);
            if (CLS == 2 && L && t + 2 < NSEG && !__all(fv > (unsigned)t + 2)) xn_wait(P, rr, t + 2, cpy);
            if (CLS == 2) { int tt = t; asm volatile("" : "+s"(tt)); if (tt + 2 == XS0) init_wait(P, h, 1); }
            if (CLS == 3) LA();
            {
                if (CLS == 3 && (wave == 4 || wave == 7)) {
                    if (t + 1 < NSEG) xdma(t + 1, 1, par);
                }
                ST(5);
            }
        }
        STFLUSH(t);
    }
    LF();
    };
    if (wave == 0) run(std::integral_constant<int, 0>{});
    else if (wave == 1) run(std::integral_constant<int, 1>{});
    else if (wave == 2) run(std::integral_constant<int, 4>{});
    else if (wave == 3) run(std::integral_constant<int, 2>{});
    else run(std::integral_constant<int, 3>{});
}

#undef STROLE
#define STROLE 8
__device__ __forceinline__ void h_role(const Params& P, int wave, int L, int i, int x) {
    const rsrc_t rp = mkrsrc(P.ws + PG0, PG_L);
    const char* xn = P.ws + PG0 + PG_XN + L * PG_LX;
    auto run = [&](auto Cc) {
    constexpr bool HI = decltype(Cc)::value;
    const int mt = (wave >> 1) & 1;
    WRegs wf;
    load_w3<(NTM & 16) != 0>(wf, i < 16 ? P.w[2] : P.w[0], wave, (i < 16 ? i * 2 + HI : (i - 16) * 4 + HI * 2) * 384, i >= 16);
    PLACE();
    const unsigned qs = (HI ? L_KX : L_QX) + (wave & 1) * 3072 + mt * 8, l16 = lane_id() * 16, wl = L_W + wave * 10240 + l16;
    unsigned xa = L_XA + wave * 2048 + l16;
    lds_st<unsigned>(L_MEM + wave * 4, 0u);
    __builtin_amdgcn_s_setprio(HI ? 1 : 2);
    if (HI) bar();
    LDECL;
#define HT_IN WREGS(wf), [xa] "v"(xa), [wl] "v"(wl), [l16] "v"(l16), [xb] "s"(L_XB + m * 4096), [qd] "s"(qs + m * 6144), [gw] "s"(L_MEM + m * 16), [rr] "s"(rr)
#define HT_CLOB "memory", "vcc", "scc", "v200", "v201", "v202", "v203", "v204", "v205", "v206", "v207", "v208", "v209", "v210", "v211", "v212", "v213", "v214", "v215", "v216", "v217", "v218", "v219", "v220", "v221", "v222", "v223", "v224", "v225", "v226", "v227", "v228", "v229", "v230", "v231", "v232", "v233", "v234", "v235", "v236", "v237", "v238", "v239", "v240", "v241", "v242", "v243", "v244", "v245", "v246", "v247", "v248", "v249", "v250", "v251", "v252"
    if constexpr (HI) {
#pragma unroll 1
        for (int u = 2 * HT0; u <= 2 * NSEG; ++u) {
            const int m = u & 1;
            const unsigned f = (mt * 12 + (m ^ 1) * 6 + (wave & 1) * 3) * 1024, so = pgb((u - 1) >> 1, par) + PG_HV + i * 3 * BLK;
            unsigned pb = 1;
            asm volatile("" : "+s"(pb));
            asm volatile(ASM_HT_HI : [pb] "+s"(pb) : HT_IN, [rp] "s"(rp), [ro] "s"(L_QX + f), [s0] "s"(so + blin(f)), [s1] "s"(so + blin(f + 1024)), [s2] "s"(so + blin(f + 2048)),
                         [fp] "s"(RG_FH + (i >> 4) * BLK + ((i & 15) * 4 + (wave & 3)) * 128), [tv] "s"(u), [end] "s"(u >> 7) : HT_CLOB);
            xa ^= L_XA ^ (L_XA + 16384);
            PMARK(L && wave == 7 && m, 6, u >> 1);
        }
    } else {
        unsigned go = 0, nx = 0, budget = 1u << 18, fl;
        asm volatile("s_sub_u32 %0, %1, 1" : "=s"(fl) : "s"(L) : "scc");
#pragma unroll 1
        for (int u = 2 * HT0; u < 2 * NSEG; ++u) {
            const int m = u & 1;
            const char *c = xn + pgb(u >> 1, par), *g = xn + pgb((u + 1) >> 1, par);
            unsigned a;
            { int uu = u; asm volatile("" : "+s"(uu)); if (uu + 1 == 2 * XS0) init_wait(P, wave, 1); }
            asm volatile(ASM_HT_LO : [go] "+s"(go), [nx] "+s"(nx), [n] "+s"(budget), [a] "=&s"(a)
                         : HT_IN, [c0] "s"(c + blin(m * 20480 + wave * 4096)), [e0] "s"(L_XA + m * 16384 + wave * 4096), [c1] "s"(c + blin(m * 20480 + 16384) + wave * 1024), [e1] "s"(L_XB + m * 4096 + wave * 1024),
                           [g0] "s"(g + blin((m ^ 1) * 20480 + wave * 4096)), [d0] "s"(L_XA + (m ^ 1) * 16384 + wave * 4096), [g1] "s"(g + blin((m ^ 1) * 20480 + 16384) + wave * 1024),
                           [d1] "s"(L_XB + (m ^ 1) * 4096 + wave * 1024), [fs] "s"(RG_FX + (cpy & 3) * 2048), [fl] "s"(fl), [m] "s"(m), [u1] "s"(u + 1), [u2] "s"(u + 2),
                           [k1] "s"(0x5555 + ((u + 1) & 128) * 512), [k2] "s"(0x5555 + ((u + 2) & 128) * 512), [gn] "s"(L_MEM + (m ^ 1) * 16 + wave * 4) : HT_CLOB);
            xa ^= L_XA ^ (L_XA + 16384);
            if (m) { LB(u >> 1); LA(); }
        }
        bar();
    }
    LF();
    };
    if (wave < 4) run(std::false_type{}); else run(std::true_type{});
}

#undef STROLE
#define STROLE 2
__device__ __forceinline__ void v_role(const Params& P, int wave, int L, int h, int x) {
    WRegs wf;
    STDECL;
    auto runv = [&](auto Cc) {
        constexpr unsigned LATE = decltype(Cc)::value == 2;
        load_w3<(NTM & 4) != 0>(wf, P.w[2], wave, h * 384, false);
        MARKX(!L && wave == 0, 6);
        PLACE();
        unsigned budget = 1u << 18;
        const unsigned wl = L_W + wave * 10240 + lane_id() * 16, dsto = c1_dst(wave, false);
        auto vfetch = [&](int seg, int tries) {
            const int ln = lane_id();
            for (int spins = 0; tries != -2; ++spins) {
                if (spins == tries) return false;
                const unsigned f = __builtin_amdgcn_raw_buffer_load_b32(rr, (ln & 3) * 128, RG_FH + (h >> 1) * 512, 16);
                if (__all(f > 2u * seg + LATE)) break;
                __builtin_amdgcn_s_sleep(4);
                if (spin_fail(spins, P)) break;
            }
            asm volatile("" ::: "memory");
            const char* g = P.ws + PG0 + pgb(seg, par) + PG_HV + (h >> 1) * 3 * BLK;
#pragma unroll
            for (int k = 0; k < 3; ++k) {
                const int f = (h & 1) * 12 + wave * 3 + k;
                dmaq<0>(g + (f >> 3) * BLK + (f & 7) * 1024, ln * 16, L_QX + (seg & 1) * 12288 + (wave * 3 + k) * 1024);
            }
            return true;
        };
#pragma unroll 1
        for (int t = 0; t < HT0; ++t) {
            if (L) xn_wait(P, rr, t, cpy);
            const unsigned l16 = lane_id() * 16;
            const char* g = P.ws + PG0 + PG_XN + L * PG_LX + pgb(t, par);
            dma4(g + blin(wave * 4096), l16, L_XA + wave * 4096); dma4(g + blin(20480 + wave * 4096), l16, L_XA + 16384 + wave * 4096);
            dmaq<0>(g + blin(16384) + wave * 1024, l16, L_XB + wave * 1024); dmaq<0>(g + blin(20480 + 16384) + wave * 1024, l16, L_XB + 4096 + wave * 1024);
            wait_vm<0>();
            bar();
            C1Regs C;
            const WRegs& W = wf;
            const unsigned xa = L_XA + wave * 2048 + l16, qs = L_QX + (wave & 1) * 3072 + ((wave >> 1) & 1) * 8 + t * 12288;
            asm volatile(ASM_XG_V : XG_OUT : XG_IN : XG_CLOB);
            if (t == 0) bar();
        }
        bool seen = false;
        unsigned tb2 = 0;
        for (int t = 0; t < NSEG; ++t) {
            const bool fe = t + 1 >= HT0 && t + 1 < NSEG;
            if (fe && (seen || !LATE)) vfetch(t + 1, seen ? -2 : -1);
            if (!LATE) wait_vm<0>();
            ST(7);
            bar();
            if (fe) {
                if (LATE) { if (!seen) vfetch(t + 1, -1); wait_vm<0>(); }
                bar();
            }
            C1Regs C;
            asm volatile("ds_read_b128 v[200:203], %4\n\tds_read_b128 v[204:207], %4 offset:1024\n\tds_read_b128 v[228:231], %4 offset:2048\n\tds_read_b128 v[232:235], %4 offset:3072\n\t"
                         "v_mov_b64 v[224:225], 0\n\tv_mov_b64 v[226:227], 0\n\tv_mov_b64 v[244:245], 0\n\tv_mov_b64 v[246:247], 0\n\tv_mov_b64 v[248:249], 0\n\tv_mov_b64 v[250:251], 0\n\ts_waitcnt lgkmcnt(0)"
                         : XG_OUT : "v"(wl) : "memory");
            ST(6);
            while ((unsigned)__builtin_amdgcn_s_memtime() - tb2 < VTD) __builtin_amdgcn_s_sleep(1);
            unsigned fv = (lane_id() & 3) * 128;
            crit1<10 + decltype(Cc)::value>(wf, C, rr, RG_XM + (t & (NSLOT - 1)) * XM_SLOT + (cpy >> 1) * XM_PAIR + (cpy & 1) * BLK, wave, wl, dsto, budget, fv, RG_FH + (h >> 1) * 512 STARG);
            __builtin_amdgcn_s_setprio(0);
            bar();
            seen = __all(fv > 2u * t + 4u + LATE);
            tb2 = (unsigned)__builtin_amdgcn_s_memtime();
            ST(4);
            STC(5);
            STFLUSH(t);
        }
    };
    if (wave < 2) runv(std::integral_constant<int, 3>{});
    else if (wave < 4) runv(std::integral_constant<int, 2>{});
    else {
        const int w4 = wave - 4;
        const rsrc_t rd = mkrsrc(P.ws + PG0, PG_L);
        load_wo(wf, P.w[3], wave, h);
        PLACE();
        bar();
        bar();
        for (int t = -1; t < NSEG; ++t) {
            const unsigned slot = t & (NSLOT - 1);
            bar();
            ST(7);
#pragma unroll 1
            for (int n = V1A ? t + (t <= 0) : t + (t != 1) + (t == 0); n <= (V1A && t >= 0 ? t : t + 1) && n < NSEG; ++n) {
                const unsigned l16 = lane_id() * 16;
                const unsigned wl = L_W + wave * 10240 + l16, va = L_QX + (n & 1) * 12288 + l16;
                unsigned so = ((n & 7) << 22) + pgb(64 + (w4 >> 1), par) + ((w4 & 1) * 40 + (h >> 3)) * BLK + (h & 7) * 1024;
                asm volatile(ASM_VOX : [so] "+s"(so) : WREGS(wf), [l16] "v"(l16), [wl] "v"(wl), [va] "v"(va), [rd] "s"(rd), [sy] "s"(n >> 1) : "memory", "scc", "v200", "v201", "v202", "v203", "v204", "v205", "v206", "v207", "v208", "v209", "v210", "v211", "v212", "v213", "v214", "v215", "v216", "v217", "v218", "v219", "v220", "v221", "v222", "v223", "v224", "v225", "v226", "v227", "v228", "v229", "v230", "v231", "v232", "v233", "v234", "v235", "v236", "v237", "v238", "v239", "v240", "v241", "v242", "v243", "v244", "v245", "v246", "v247", "v248", "v249", "v250", "v251");
                if (V1A || n < 2) {
                    wait_vm<0>();
                    const unsigned l4 = lane_id() * 4;
                    if (l4 < 128) __builtin_amdgcn_raw_buffer_store_b32(n + 1, rr, l4, RG_FV + (w4 >> 1) * BLK + ((w4 & 1) * 32 + h) * 128, 16);
                }
            }
            if (t < 0) continue;
            ST(0);
            unsigned wl = L_W + wave * 10240 + lane_id() * 16;
            bf16x16 lw = lds_frag(wl, 1024);
            u32x32 cz;
            asm volatile("v_mov_b64 v[220:221], 0\n\tv_mov_b64 v[222:223], 0\n\tv_mov_b64 v[224:225], 0\n\tv_mov_b64 v[226:227], 0\n\tv_mov_b64 v[228:229], 0\n\tv_mov_b64 v[230:231], 0\n\tv_mov_b64 v[232:233], 0\n\tv_mov_b64 v[234:235], 0\n\tv_mov_b64 v[236:237], 0\n\tv_mov_b64 v[238:239], 0\n\tv_mov_b64 v[240:241], 0\n\tv_mov_b64 v[242:243], 0\n\tv_mov_b64 v[244:245], 0\n\tv_mov_b64 v[246:247], 0\n\tv_mov_b64 v[248:249], 0\n\tv_mov_b64 v[250:251], 0" : "={v[220:251]}"(cz));
            unsigned vo = ((wl >> 8) & 1) * (1 + (w4 & 1) * 2) * BLK + ((wl >> 9) & 1) * 1024 + (wl & 255);
            asm volatile("" : "+{v[212:219]}"(lw), "+{v[220:251]}"(cz), "+v"(wl), "+v"(vo));
            unsigned l16 = wl & 1023u;
            bar();
            wait_vm<0>();
            if (!V1A && l16 < 512 && t && t + 1 < NSEG) __builtin_amdgcn_raw_buffer_store_b32(t + 2, rr, l16 >> 2, RG_FV + (w4 >> 1) * BLK + ((w4 & 1) * 32 + h) * 128, 16);
            ST(1);
            const int sidx = (wl & 128u) ? 0xEEEE : 0x4444;
            unsigned so = RG_VM + slot * VM_SLOT + (unsigned)((w4 * 10 - (w4 & 1) + (h >> 4) * 2) * BLK + ((h >> 2) & 3) * 2048 + (h & 3) * 256);
            ST(3);
            ASM_ST_DECL;
            asm volatile(ASM_CRIT2 : [so] "+s"(so), "+{v[212:219]}"(lw), "+{v200}"(l16), "+{v[220:251]}"(cz) ASM_ST_OUT
                         : WREGS(wf), [wl] "v"(wl), [vo] "v"(vo), [sidx] "v"(sidx), [rr] "s"(rr)
                         : "memory", "scc", "v201", "v202", "v203", "v204", "v205", "v206", "v207", "v208", "v209", "v210", "v211");
            ASM_ST_SAVE(4);
            ST(5);
            STFLUSH(t);
        }
    }
}

#undef STROLE
#define STROLE 4
constexpr unsigned R_RED = 0;
constexpr unsigned R_PART = 16384;
__device__ __forceinline__ void r_role(const Params& P, int wave, int L, int r5, int x) {
    PLACE();
    const int jj = (r5 * 4 + cpy) ^ (r5 * 4 + x >= 6 ? 1 : 0);
    const int lane = lane_id(), l15 = lane & 15, l4 = lane >> 4;
    const unsigned l16 = lane * 16;
    const rsrc_t rd = mkrsrc(P.ws + PG0, PG_L);
    const int sidx = (l15 < 8) ? 0x4444 : 0xEEEE;
    unsigned offp[3];
    offp[0] = RG_XP + (cpy >> 1) * XP_PAIR + (cpy & 1) * BLK + (1 + (wave >> 2)) * 2 * BLK + (wave & 3) * 2048 + (PLAY ? (l4 >> 1) * 512 + (((l15 >> 2) & 1) + (l15 & 2)) * 128 + (l4 & 1) * 64 + (l15 >> 3) * 32 + (l15 & 1) * 16 : l16);
    offp[2] = (l15 < 8) ? RG_XP + (cpy >> 1) * XP_PAIR + (cpy & 1) * BLK + wave * 512 + l4 * 128 + l15 * 16 : OOB;
    const unsigned offv = RG_VM + ((jj >> 1) * 4 + (wave >> 2) * 2 + (jj & 1)) * BLK + (wave & 3) * 2048 + l16;
    const int j = jj * 2 + ((wave >> 1) & 1), fl = (wave & 1) * 32 + (lane & 31);
    const unsigned offo = RG_XM + (wave >> 2) * XM_PAIR + (lane >> 5) * BLK + (j & 31) * 256 + (j >> 5) * 2 * BLK + fl * 4;
    MARK(!L && jj == 0 && wave == 0, 5);
    {
        const int row = (lane >> 2) & 7, col = 16 * j + 4 * (lane & 3) + 2 * (wave & 1);
        __builtin_amdgcn_raw_buffer_store_b32(nosent(*(const unsigned*)(P.mem0 + row * DM + col)), rr, offo, 0, 16);
    }
    auto red_addr = [](int fl) { return R_RED + fl * 80 + (fl >> 4) * 16; };
    const unsigned rw0 = (lane >> 5) * 5248 + red_addr((((l15 >> 1) & 1) * 8 + 4 * (l4 & 1)) * 4 + (l15 >> 2)) + (l15 & 1) * 4 + (wave ^ (l15 >> 3)) * 8;
    const unsigned rrd = ((wave >> 1) & 1) * 5248 + red_addr(fl) + (lane >> 5) * 32;
    u32x32 vx;
    const unsigned fo = (lane & 3) * 128, fs = RG_FV + (jj / 10) * BLK + (((jj / 5) & 1) * 32 + wave * 4) * 128, vxo = pgb(64 + jj / 10, par) + (unsigned)((jj % 10) * 8 + (wave >> 1)) * BLK + (wave & 1) * 4096;
    const rsrc_t rp = mkrsrc(P.ws + PG0, PG_L);
    const unsigned xo = (l4 >> 1) * 256 + l15 * 16 + (l4 & 1) * 8, pw = R_PART + wave * 1024 + l16, pr = R_PART + (wave & 3) * 8192 + l16;
    const int fin = wave >> 2;
    asm volatile("" : "={v[128:159]}"(vx));
    auto vx_fetch = [&](int t) {
        for (int spins = 0;; ++spins) {
            const unsigned f = __builtin_amdgcn_raw_buffer_load_b32(rr, fo, fs, 16);
            if (__all(f > (unsigned)t)) break;
            __builtin_amdgcn_s_sleep(2);
            if (spin_fail(spins, P)) break;
        }
        const unsigned sv = ((t & 7) << 22) + vxo;
        asm volatile("buffer_load_dwordx4 v[128:131], %1, %2, %3 offen sc1\n\tbuffer_load_dwordx4 v[132:135], %1, %2, %3 offen offset:1024 sc1\n\t"
                     "buffer_load_dwordx4 v[136:139], %1, %2, %3 offen offset:2048 sc1\n\tbuffer_load_dwordx4 v[140:143], %1, %2, %3 offen offset:3072 sc1\n\t"
                     "buffer_load_dwordx4 v[144:147], %1, %2, %4 offen sc1\n\tbuffer_load_dwordx4 v[148:151], %1, %2, %4 offen offset:1024 sc1\n\t"
                     "buffer_load_dwordx4 v[152:155], %1, %2, %4 offen offset:2048 sc1\n\tbuffer_load_dwordx4 v[156:159], %1, %2, %4 offen offset:3072 sc1"
                     : "+{v[128:159]}"(vx) : "v"(l16), "s"(rp), "s"(sv), "s"(sv + 4 * BLK) : "memory");
    };
    vx_fetch(0);
    unsigned budget = 1u << 18, tpub = 0;
    STDECL;
    LDECL;
    for (int t = 0; t < NSEG; ++t) {
        const unsigned slot = t & (NSLOT - 1);
        ST(0);
        while ((unsigned)__builtin_amdgcn_s_memtime() - tpub < RTD) __builtin_amdgcn_s_sleep(1);
        u32x8 vm, pv;
        u32x4 pm;
        {
            unsigned a;
            ASM_ST_DECL;
            asm volatile(ASM_RCRIT : "=&{v[176:183]}"(vm), "=&{v[184:191]}"(pv), "=&{v[192:195]}"(pm), [a] "=&s"(a), [n] "+s"(budget) ASM_ST_OUT ASM_ST2_OUT ASM_ST3_OUT
                         : [rs] "s"(rr), [ov] "v"(offv), [sv] "s"(slot * VM_SLOT), [o0] "v"(offp[0]), [o2] "v"(offp[2]), [sp] "s"(slot * XP_SLOT),
                           "{v[128:159]}"(vx), [sidx] "v"(sidx), [rw] "v"(rw0), [rd] "v"(rrd), [oo] "v"(offo),
                           [so] "s"(((t + 1) & (NSLOT - 1)) * XM_SLOT), [pub] "s"(NSEG - 1 - t)
                         : "memory", "vcc", "scc", "v160", "v161", "v162", "v163", "v164", "v165", "v166", "v167", "v196", "v197", "v200", "v201", "v202", "v203", "v204", "v205", "v206", "v207");
            ASM_ST_SAVE(2); ASM_ST2_SAVE(1); ASM_ST3_SAVE(6);
        }
        ST(3);
        LA();
        MARK(!L && jj == 0 && wave == 0 && t < 4, 10 + t);
        PMARK(wave == 0, L * 7, t - 1 + L);
        tpub = (unsigned)__builtin_amdgcn_s_memtime();
        __builtin_amdgcn_s_sleep(RXDEL);
        unsigned early;
#pragma unroll 1
        for (int spins = 1023;; spins += 1024) {
            unsigned n;
            const unsigned sx = pgb(t, par) + blin(((wave >> 1) & 1) * 20480 + jj * 1024) + (wave & 1) * 512;
            const unsigned pxb = RG_PX + slot * PX_SLOT + wave * BLK, sv = (((t + 1) & 7) << 22) + vxo;
            ASM_ST_DECL;
            asm volatile(ASM_RX : "+{v[128:159]}"(vx), [n] "=&s"(n), [a] "=&s"(early) ASM_ST_OUT ASM_ST2_OUT
                         : "{v[176:183]}"(vm), "{v[184:191]}"(pv), "{v[192:195]}"(pm), [l16] "v"(l16), [rs] "s"(rr), [s0] "s"(pxb), [s1] "s"(pxb + 4096), [rd] "s"(rd), [rx] "s"(rd), [xo] "v"(xo),
                           [fo] "v"(fo), [fs] "s"(fs), [tn] "s"(t + 1), [sv0] "s"(sv), [sv1] "s"(sv + 4 * BLK), [rp] "s"(rp), [sx] "s"(PG_XR + L * PG_LX + sx), [sa] "s"(PG_XA + sx),
                           [pw] "v"(pw), [pr] "v"(pr), [fin] "s"(fin)
                         : "memory", "vcc", "scc", "v96", "v97", "v98", "v99", "v100", "v101", "v102", "v103", "v104", "v105", "v106", "v107", "v108", "v109", "v110", "v111", "v112", "v113", "v114", "v115", "v116", "v117", "v118", "v119", "v120", "v121", "v122", "v123", "v124", "v125", "v126", "v127", "v196", "v200", "v201", "v202", "v203", "v204", "v205", "v206", "v207", "v208", "v209", "v210", "v211", "v212", "v213", "v214", "v215", "v216", "v217", "v218", "v219", "v220", "v221", "v222", "v223", "v224", "v225", "v226", "v227", "v228", "v229", "v230", "v231", "v240", "v241", "v242", "v243", "v248", "v250", "v251");
            ASM_ST_SAVE(4); ASM_ST2_SAVE(5);
            if (__builtin_amdgcn_readfirstlane(n)) break;
            early = 0;
            if (spin_fail(spins, P)) { bar(); break; }
        }
        if (t + 1 < NSEG && !__builtin_amdgcn_readfirstlane(early)) vx_fetch(t + 1);
        PMARK(!L && wave == 7 && PMK == 1, 1, t - 1);
        st128(sent4(), rr, offv, slot * VM_SLOT); st128(sent4(), rr, offv + 1024, slot * VM_SLOT);
        __builtin_amdgcn_raw_buffer_store_b32(SENT, rr, offo, slot * XM_SLOT, 16);
        if (wave < 2 && t + AH < NSEG) st128(sent4(), rd, l16, PG_XA + pgb(t + AH, par) + blin(wave * 20480 + jj * 1024));
        ST(7);
        LB(t);
        STFLUSH(t);
    }
    LF();
}

#undef STROLE
#define STROLE 16
template <int WHICH>
__device__ __forceinline__ void w_role(const Params& P, int wave, int L, int cu, int x) {
    constexpr int KS = 20, N = (WHICH == 3) ? FF : DM, NU = 2 * NSEG;
    constexpr unsigned D_IN = WHICH == 1 ? PG_XA : WHICH == 2 ? PG_HS : PG_X1;
    constexpr unsigned D_OUT = WHICH == 1 ? PG_HS : WHICH == 2 ? PG_X1 : PG_H2;
    if (WHICH != 3 && cu * 8 + wave >= 20) {
        for (int u = 0; u <= NU; ++u) bar();
        return;
    }
    auto run = [&](auto Cc) {
    constexpr bool LO = decltype(Cc)::value;
    const int ln = lane_id(), l15 = ln & 15, l4 = ln >> 4;
    const unsigned l16 = ln * 16;
    const rsrc_t rd = mkrsrc(P.ws + PG0, PG_L);
    const int nt0 = (cu * 8 + wave) * 2;
    u32x32 w[5];
    {
        u32x4 f[2 * KS];
        load_wn<2, KS, N>(f, P.w[3 + WHICH], DM, wave, nt0, 0);
#pragma unroll
        for (int n = 0; n < 5; ++n) {
            u32x8 q[4];
#pragma unroll
            for (int i = 0; i < 4; ++i) q[i] = __builtin_shufflevector(f[n * 8 + 2 * i], f[n * 8 + 2 * i + 1], 0, 1, 2, 3, 4, 5, 6, 7);
            w[n] = cat32(q[0], q[1], q[2], q[3]);
        }
        asm volatile("" : "+{v[0:31]}"(w[0]), "+{v[32:63]}"(w[1]), "+{v[64:95]}"(w[2]), "+{v[96:127]}"(w[3]), "+{v[128:159]}"(w[4]));
    }
    PLACE();
    bar();
    const int ocol = nt0 * 16 + (l4 & 1) * 16 + (l4 >> 1) * 8;
    const unsigned o3 = (unsigned)((l4 & 1) * 512 + (l4 >> 1) * 256 + l15 * 16), ro = (l4 >> 1) * 256 + l15 * 16 + (l4 & 1) * 8;
    const int grp = nt0 / 10, sh = ((grp & 1) ? -1 : 1) * ((grp >> 1) + 1);
    if (WHICH == 1) {
        unsigned zv;
        asm volatile("v_mov_b32 %0, 0" : "=v"(zv));
        st128(((u32x4){zv, zv, zv, zv}), rd, l15 <= (grp >> 1) ? toff((grp & 1) ? S_ - 1 - l15 : l15, ocol, par) : OOB, PG_HS);
    }
    auto in0 = [&](int u) { return D_IN + pgb(u >> 1, par) + blin((u & 1) * 20480 + wave * 4096); };
    auto in1 = [&](int u) { return D_IN + pgb(u >> 1, par) + blin((u & 1) * 20480 + 16384) + wave * 1024; };
    u32x16 va;
    u32x4 vb;
    u32x8 cs, k8;
#define WT_W "{v[0:31]}"(w[0]), "{v[32:63]}"(w[1]), "{v[64:95]}"(w[2]), "{v[96:127]}"(w[3]), "{v[128:159]}"(w[4])
    asm volatile("v_mov_b32 v216, 0x3f803f80\n\tv_mov_b32 v217, v216\n\tv_mov_b32 v218, v216\n\tv_mov_b32 v219, v216\n\tv_mov_b32 v220, %[c0]\n\tv_mov_b32 v232, -1\n\tv_mov_b32 v236, -1\n\t"
                 "v_mov_b32 v240, -1\n\tv_mov_b32 v244, -1\n\tv_mov_b32 v248, -1\n\t" ASM_WCS
                 : "=&{v[196:203]}"(cs), "=&{v[216:223]}"(k8), "=&{v[232:247]}"(va), "=&{v[248:251]}"(vb) : WT_W, [c0] "s"(-2.0f * 0.7978845608028654f * 1.4426950408889634f));
    unsigned la = l16, lw = wave * 8192 + l16, sr = l4 * 8192 + ((l15 >> 2) * 16 + l15) * 16 + (l15 & 3) * 4, budget = 1u << 18;
    wait_vm<0>();
    STDECL;
    LDECL;
#pragma unroll 1
    for (int u = 0; u < NU; ++u) {
        const int mt = u & 1;
        const unsigned pg = pgb(u >> 1, par), c3 = D_OUT + blin(mt * (N * 32) + (nt0 >> 1) * 1024);
        unsigned oa = o3, ob = 0, so = pg + c3, sa = u + 2 * AH < NU ? pgb((u >> 1) + AH, par) + c3 : pg + PG_SS + 4096, a;
        if (WHICH == 1) {
            const int orow = u * 16 + l15 + sh;
            ob = toff(orow + AH * SEG, ocol, par);
            oa = (unsigned)orow < (unsigned)S_ ? (ob ^ 8192u) - (AH << 21) : OOB;
            if (orow + AH * SEG >= S_ + (sh < 0 ? sh : 0)) ob = OOB;
            so = sa = D_OUT;
        }
        if (mt) { ST(0); LB(u >> 1); } else { LA(); }
        ASM_ST_DECL;
#define WT_A WT_W, [la] "v"(la), [l16] "v"(l16), [oa] "v"(oa), [rs] "s"(rd), [so] "s"(so), [sa] "s"(sa)
#define WT_G [sr] "v"(sr), [kd] "s"(1.0f / DM), [ke] "s"(1e-5f), [k1] "s"(-2.0f * 0.7978845608028654f * 1.4426950408889634f * 0.044715f)
#define WT_1 WT_G, [ob] "v"(ob), [ss] "s"(pg + PG_SS + mt * 128), [sw] "s"(nt0)
#define WT_2 [ro] "v"(ro), [sx] "s"(pg + PG_XA + blin(mt * 20480 + nt0 * 512)), [ss] "s"(pg + PG_SS + mt * 128)
#define WT_C "memory", "vcc", "scc", "v160", "v161", "v162", "v163", "v164", "v165", "v166", "v167", "v168", "v169", "v170", "v171", "v172", "v173", "v174", "v175", "v176", "v177", "v178", "v179", "v180", "v181", "v182", "v183", "v184", "v185", "v186", "v187", "v188", "v189", "v190", "v191", "v192", "v193", "v194", "v195", "v204", "v205", "v206", "v207", "v208", "v209", "v210", "v211", "v212", "v213", "v214", "v215", "v252"
#define WT_LO(A, ...) asm volatile(A : "+{v[196:203]}"(cs), "+{v[216:223]}"(k8), "+{v[232:247]}"(va), "+{v[248:251]}"(vb), [n] "+s"(budget), [a] "=&s"(a) ASM_ST_OUT ASM_ST2_OUT ASM_ST3_OUT \
                              : WT_A, __VA_ARGS__, [lw] "v"(lw), [i0] "s"(in0(u)), [i1] "s"(in1(u)), [n0] "s"(in0(u + 1)), [n1] "s"(in1(u + 1)) : WT_C)
#define WT_HI(A, ...) asm volatile(A : "+{v[196:203]}"(cs), "+{v[216:223]}"(k8) ASM_ST_OUT ASM_ST3_OUT : WT_A, __VA_ARGS__ : WT_C)
        if constexpr (LO) { if constexpr (WHICH == 1) WT_LO(ASM_WT1_LO, WT_1); else if constexpr (WHICH == 2) WT_LO(ASM_WT2_LO, WT_2); else WT_LO(ASM_WT3_LO, WT_G); }
        else { if constexpr (WHICH == 1) WT_HI(ASM_WT1_HI, WT_1); else if constexpr (WHICH == 2) WT_HI(ASM_WT2_HI, WT_2); else WT_HI(ASM_WT3_HI, WT_G); }
        la ^= 32768u; lw ^= 32768u; sr ^= 32768u;
        PMARK(!L && mt == (WHICH != 1) && (PMK == 1 || WHICH == 3), PMK == 1 ? 1 + WHICH : 1, (u >> 1) - (WHICH == 1));
    }
    wait_vm<0>();
    if constexpr (LO) asm volatile("" :: "{v[232:247]}"(va), "{v[248:251]}"(vb));
    LF();
    };
    if (wave < 4) run(std::true_type{}); else run(std::false_type{});
}

__device__ __forceinline__ void w4_role(const Params& P, int wave, int L, int cu, int x) {
    constexpr unsigned W_FL = 122880;
    const int ln = lane_id(), l15 = ln & 15, l4 = ln >> 4;
    const unsigned l16 = ln * 16;
    const rsrc_t rd = mkrsrc(P.ws + PG0, PG_L);
    lds_st<unsigned>(W_FL + wave * 4, 0u);
    STDECL;
    if (wave < 6) {
        u32x32 w[5];
        {
            u32x4 g[40];
            load_wn<5, 8, DM>(g, P.w[7], FF, wave, cu * 5, wave * 8);
#pragma unroll
            for (int n = 0; n < 5; ++n) {
                u32x8 f[4];
#pragma unroll
                for (int i = 0; i < 4; ++i) f[i] = __builtin_shufflevector(g[n * 8 + 2 * i], g[n * 8 + 2 * i + 1], 0, 1, 2, 3, 4, 5, 6, 7);
                w[n] = cat32(f[0], f[1], f[2], f[3]);
            }
            asm volatile("" : "+{v[0:31]}"(w[0]), "+{v[32:63]}"(w[1]), "+{v[64:95]}"(w[2]), "+{v[96:127]}"(w[3]), "+{v[128:159]}"(w[4]));
        }
        PLACE();
        bar();
        unsigned budget = 1u << 18;
        u32x32 av;
        asm volatile("v_mov_b32 v160, -1\n\tv_mov_b32 v164, -1\n\tv_mov_b32 v168, -1\n\tv_mov_b32 v172, -1\n\tv_mov_b32 v176, -1\n\tv_mov_b32 v180, -1\n\tv_mov_b32 v184, -1\n\tv_mov_b32 v188, -1" : "={v[160:191]}"(av));
        for (int u = 0; u < 2 * NSEG; ++u) {
            if (u & 1) ST(0);
            for (int spins = 0;; ++spins) {
                asm volatile("" ::: "memory");
                if ((int)__builtin_amdgcn_readfirstlane(lds_ld<unsigned>(W_FL + 24 + (u & 1) * 4)) + 3 >= u) break;
                __builtin_amdgcn_s_sleep(2);
                if (spin_fail(spins, P)) break;
            }
            const unsigned gb = PG_H2 + pgb(u >> 1, par) + ((u & 1) * 6 + wave) * BLK, gn = PG_H2 + pgb((u + 1) >> 1, par) + ((~u & 1) * 6 + wave) * BLK;
            ASM_ST_DECL;
            asm volatile(ASM_W4_M : [n] "+s"(budget), "+{v[160:191]}"(av) ASM_ST2_OUT
                         : "{v[0:31]}"(w[0]), "{v[32:63]}"(w[1]), "{v[64:95]}"(w[2]), "{v[96:127]}"(w[3]), "{v[128:159]}"(w[4]),
                           [ld] "v"(l16 + wave * 1024 + (u & 3) * 30720), [l16] "v"(l16), [rs] "s"(rd), [s0] "s"(gb), [s1] "s"(gb + 4096), [n0] "s"(gn), [n1] "s"(gn + 4096), [un] "s"(u + 1), [fl] "v"(W_FL + wave * 4)
                         : "memory", "vcc", "scc", "v192", "v193", "v194", "v195", "v196", "v197", "v198", "v199", "v200", "v201", "v202", "v203", "v204", "v205", "v206", "v207", "v208", "v209", "v210", "v211", "v212");
            PMARK(!L && (u & 1) && PMK == 2, 2, u >> 1);
        }
        asm volatile("s_waitcnt vmcnt(0)" :: "{v[160:191]}"(av) : "memory");
    } else {
        const int mt = wave - 6;
        PLACE();
        bar();
        const unsigned c0 = mt * 20480 + cu * 2560, o8 = (l4 >> 1) * 256 + l15 * 16 + (l4 & 1) * 8;
        unsigned budget = 1u << 18;
        u32x16 ra;
        u32x4 rb;
        asm volatile("v_mov_b32 v164, -1" : "={v[164:179]}"(ra), "={v[180:183]}"(rb));
        __builtin_amdgcn_s_setprio(W4P);
#define W4F_OUT "+{v[164:179]}"(ra), "+{v[180:183]}"(rb), [n] "+s"(budget)
#define W4F_IN [o0] "v"(o8 + blin(c0)), [o1] "v"(o8 + blin(c0 + 512)), [o2] "v"(o8 + blin(c0 + 1024)), [o3] "v"(o8 + blin(c0 + 1536)), [o4] "v"(o8 + blin(c0 + 2048)), [rd] "s"(rd), \
               [c1] "s"(PG_X1 + pg), [ca] "s"(PG_XA + pg), [n1] "s"(PG_X1 + pn), [na] "s"(PG_XA + pn), [f0] "v"(W_FL + (ln & 3) * 4), [f1] "v"(W_FL + 16 + (ln & 1) * 4), [fd] "v"(W_FL + wave * 4), \
               [la] "v"((u & 3) * 30720 + l16), [u] "s"(u), [un] "s"(u + 1)
#define W4F_CLOB "memory", "vcc", "scc", "v64", "v65", "v66", "v67", "v68", "v69", "v70", "v71", "v72", "v73", "v74", "v75", "v76", "v77", "v78", "v79", "v80", "v81", "v82", "v83", "v84", "v85", "v86", "v87", "v88", "v89", "v90", "v91", "v92", "v93", "v94", "v95", "v96", "v97", "v98", "v99", "v100", "v101", "v102", "v103", "v104", "v105", "v106", "v107", "v108", "v109", "v110", "v111", "v112", "v113", "v114", "v115", "v116", "v117", "v118", "v119", "v120", "v121", "v122", "v123", "v124", "v125", "v126", "v127", "v128", "v129", "v130", "v131", "v132", "v133", "v134", "v135", "v136", "v137", "v138", "v139", "v140", "v141", "v142", "v143", "v144", "v145", "v146", "v147", "v148", "v149", "v150", "v151", "v152", "v153", "v154", "v155", "v156", "v157", "v158", "v159", "v160", "v161", "v162", "v163", "v184", "v185", "v186", "v187", "v188", "v189", "v190", "v191", "v192", "v193", "v194", "v195", "v196", "v197", "v198", "v199", "v200"
        if (L) {
            const rsrc_t ro = mkrsrc(P.out, ACT);
#pragma unroll 1
            for (int t = 0; t < NSEG; ++t) {
                const unsigned u = t * 2 + mt, pg = pgb(t, par), pn = pgb(t + 1, par);
                ASM_ST_DECL;
                asm volatile(ASM_W4F_1 : W4F_OUT ASM_ST2_OUT : W4F_IN, [pa] "v"(l15 * (DM * 2) + (l4 & 1) * 32 + (l4 >> 1) * 16), [pb] "v"(l15 * (DM * 2) + l4 * 8), [ro] "s"(ro), [so] "s"(u * 20480 + cu * 160) : W4F_CLOB);
            }
        } else {
            const rsrc_t rn = mkrsrc(P.ws + (par ^ 1) * 8192, RG_L);
#pragma unroll 1
            for (int t = 0; t < NSEG; ++t) {
                const unsigned u = t * 2 + mt, pg = pgb(t, par), pn = pgb(t + 1, par), sn = (pg ^ 8192) + PG_LX, st = RG_ST + (u >> 3) * BLK + (u & 7) * 1024;
                ASM_ST_DECL;
                asm volatile(ASM_W4F_0 : W4F_OUT ASM_ST_OUT ASM_ST2_OUT ASM_ST3_OUT
                             : W4F_IN, [pa] "v"(blin(c0 + (l4 & 1) * 512) + (l4 >> 1) * 256 + l15 * 16), [pb] "v"(blin(c0 + 1024 + (l4 & 1) * 512) + (l4 >> 1) * 256 + l15 * 16),
                               [so] "v"(ln < 16 ? ln * 8 : OOB), [si] "v"(ln * 8), [rr] "s"(rr), [sp] "s"(st + cu * 128), [sq] "s"(st), [xr] "s"(PG_XR + sn), [xn] "s"(PG_XN + sn),
                               [kd] "s"(1.0f / DM), [ke] "s"(1e-5f), [tn] "s"(t + 1), [fo] "v"(ln < 32 ? (ln >> 3) * 2048 + (ln & 7) * 16 : OOB), [rn] "s"(rn), [fs] "s"(RG_FX + (cu * 2 + mt) * 128) : W4F_CLOB);
            }
        }
        asm volatile("s_waitcnt vmcnt(0)" :: "{v[164:179]}"(ra), "{v[180:183]}"(rb) : "memory");
    }
}

__device__ __forceinline__ unsigned probe(char* ws, unsigned base, unsigned bit, int n) {
    const rsrc_t r = mkrsrc(ws, RG_SLOT);
    unsigned m0 = ~0u, m1 = ~0u;
    for (int i = 0; i < n; ++i) {
        const unsigned long long t0 = __builtin_amdgcn_s_memtime();
        __builtin_amdgcn_raw_buffer_store_b32(0u, r, 96 * BLK + base + (i & 1) * bit + ((i >> 2) & 1) * 4096 + (i >> 2) * 768, 0, 16);
        asm volatile("s_waitcnt vmcnt(0)" ::: "memory");
        const unsigned d = (unsigned)(__builtin_amdgcn_s_memtime() - t0);
        if (i & 1) m1 = d < m1 ? d : m1; else m0 = d < m0 ? d : m0;
    }
    return m0 > m1 ? 1u : 0u;
}
__device__ __forceinline__ void setup(const Params& P, int wave, int mt, int par0, int ph) {
    const bool xw = wave < 4 && (mt >= 2 * XS0) == (ph != 0);
    const int tid = threadIdx.x & 255, r15 = tid & 15, slot = tid >> 4;
    const rsrc_t rw = mkrsrc(P.ws, WS_END);
    u32x4 v[5];
    float s = 0.f, q = 0.f;
    if (xw) {
#pragma unroll
        for (int c = 0; c < 5; ++c) v[c] = *(const u32x4*)(P.x + (size_t)(mt * 16 + r15) * DM + (slot * 5 + c) * 8);
#pragma unroll
        for (int c = 0; c < 5; ++c)
#pragma unroll
            for (int e = 0; e < 4; ++e) s += lo2f(v[c][e]) + hi2f(v[c][e]);
        lds_st<float>((slot * 17 + r15) * 4, s);
    } else if (wave >= 4) {
        constexpr unsigned N0 = RG_ST / BLK, N1 = 4 * N0 + 16, N2 = PG_SS / BLK;
        const u32x4 s4 = sent4();
        for (unsigned k = mt; k < 2 * (N1 + 4 * N2); k += 128) {
            const unsigned i = k >> 1;
            const unsigned o = i < N1 ? (i < 4 * N0 ? (i / N0) * RG_SLOT + (i % N0) * BLK : (i - 3 * N0) * BLK) + (par0 ^ ph) * 8192 : PG0 + pgb((i - N1) / N2, par0 ^ ph) + ((i - N1) % N2) * BLK;
            st128(s4, rw, tid * 16, o + (k & 1) * 4096);
        }
        if (ph == 0 && mt >= 108) st128(~s4, rw, tid * 16, RG_FV + (mt - 108) * 4096);
    }
    bar();
    float mean = 0.f;
    if (xw) {
        s = 0.f;
#pragma unroll
        for (int k = 0; k < 16; ++k) s += lds_ld<float>((k * 17 + r15) * 4);
        mean = s * (1.0f / DM);
#pragma unroll
        for (int c = 0; c < 5; ++c)
#pragma unroll
            for (int e = 0; e < 4; ++e) { const float a = lo2f(v[c][e]) - mean, b = hi2f(v[c][e]) - mean; q += a * a + b * b; }
        lds_st<float>(2048 + (slot * 17 + r15) * 4, q);
    }
    bar();
    if (xw) {
        q = 0.f;
#pragma unroll
        for (int k = 0; k < 16; ++k) q += lds_ld<float>(2048 + (k * 17 + r15) * 4);
        const float m1 = rbf(mean), r = 1.0f / ln_den(q * (1.0f / DM));
#pragma unroll
        for (int c = 0; c < 5; ++c) {
            const unsigned o = toff(mt * 16 + r15, (slot * 5 + c) * 8, par0);
            st128(v[c], rw, o, PG0 + PG_XR);
            u32x4 n;
#pragma unroll
            for (int e = 0; e < 4; ++e) n[e] = pack2(rbf(lo2f(v[c][e]) - m1) * r, rbf(hi2f(v[c][e]) - m1) * r);
            st128(n, rw, o, PG0 + PG_XN);
        }
    }
    wait_vm<0>();
    bar();
    if (threadIdx.x < 8) __hip_atomic_fetch_add((unsigned*)(P.ws + FL0 + FL_IN + (ph * 8 + threadIdx.x) * 128), 1u, RLX_AGENT);
}

__global__ __launch_bounds__(512, 2) void mega_kernel(Params P) {
    const int wave = __builtin_amdgcn_readfirstlane(threadIdx.x >> 6);
    u32x4 cpf;
    {
        unsigned long long pc;
        unsigned len;
        asm volatile("s_getpc_b64 %0\n\ts_mov_b32 %1, _Z8code_endv-_Z11mega_kernel6Params" : "=s"(pc), "=s"(len));
        cpf = __builtin_amdgcn_raw_buffer_load_b128(mkrsrc((const void*)(pc & ~255ull), len), (blockIdx.x >> 3) * 8192 + threadIdx.x * 16, 0, 0);
    }
    {
        const rsrc_t rf = mkrsrc(P.ws + FL0, FL_BYTES);
        if (blockIdx.x == 0) {
            unsigned h = 0;
            if (wave == 7) h = (probe(P.ws, 0, 8192, CAL1) ^ ((unsigned)__builtin_amdgcn_s_getreg((3 << 11) | 20) >> 2)) & 1u;
            if (wave == 0) {
                const unsigned l16 = lane_id() * 16;
                u32x4 z = {0u, 0u, 0u, 0u};
                asm volatile("" : "+v"(z));
#pragma unroll
                for (int k = 0; k < 4; ++k) st128(z, rf, k * 1024 + l16 < FL_HB ? k * 1024 + l16 : OOB, 0);
                wait_vm<0>();
                for (unsigned c = 1; c < 200000u; ++c) {
                    __builtin_amdgcn_raw_buffer_store_b32(c, rf, l16 < 128 ? l16 * 8 : OOB, FL_HB, 16);
                    unsigned n = __builtin_amdgcn_raw_buffer_load_b32(rf, (l16 & 127u) * 8, FL_TK, 16), sum = 0;
                    n = n < 32u ? n : 32u;
#pragma unroll
                    for (int k = 0; k < 8; ++k) sum += __builtin_amdgcn_readlane(n, k);
                    if (sum >= 255u) break;
                }
            }
            bar();
            if (wave == 7 && lane_id() < 16) __hip_atomic_fetch_add((unsigned*)(P.ws + FL0 + FL_IN + (threadIdx.x & 15) * 128), (2u + h) << 16, RLX_AGENT);
        } else if (wave == 0) {
            unsigned o;
            asm volatile("v_mov_b32 %0, 0" : "=v"(o));
            const unsigned v0 = __builtin_amdgcn_readfirstlane(__builtin_amdgcn_raw_buffer_load_b32(rf, o, FL_HB + (blockIdx.x & 7) * 128, 16));
            for (int spins = 0; spins < 400000; ++spins) {
                asm volatile("" ::: "memory");
                if (__builtin_amdgcn_readfirstlane(__builtin_amdgcn_raw_buffer_load_b32(rf, o, FL_HB + (blockIdx.x & 7) * 128, 16)) != v0) break;
                __builtin_amdgcn_s_sleep(1);
            }
            asm volatile("" ::: "memory");
        }
    }
    if (threadIdx.x == 0) {
        unsigned g = (unsigned)__builtin_amdgcn_s_getreg((3 << 11) | 20) & 7u, k;
        for (int n = 0; (k = __hip_atomic_fetch_add((unsigned*)(P.ws + FL0 + FL_TK + g * 128), 1u, RLX_AGENT)) >= 32u && n < 8; ++n) g = (g + 1) & 7u;
        lds_st<unsigned>(0, g << 5 | (k & 31u));
    }
    bar();
    const unsigned tk = __builtin_amdgcn_readfirstlane(lds_ld<unsigned>(0));
    asm volatile("" :: "v"(cpf));
    bar();
    const int g4 = (tk >> 5) & 3, k = tk & 31;
    const int L = tk >> 7, x = g4, r = k;
    if (L) {
        const int par0 = (init_wait(P, wave, 1, 0x20000u) >> 16) & 1;
        if (tk == 254 && wave == 0) {
            const unsigned xc = (unsigned)__builtin_amdgcn_s_getreg((3 << 11) | 20);
            const unsigned f = (probe(P.ws, ((par0 ^ (xc >> 2)) & 1) * 8192, BLK, CAL2) ^ 1u ^ xc) & 1u;
            if (threadIdx.x < 16) __hip_atomic_fetch_add((unsigned*)(P.ws + FL0 + FL_IN + threadIdx.x * 128), (2u + f) << 20, RLX_AGENT);
        }
#pragma unroll 1
        for (int ph = 0; ph < 2; ++ph) {
            setup(P, wave, (tk & 31) * 4 + g4, par0, ph);
            for (int k = ph * DLY_L; k < DLY_L; ++k) __builtin_amdgcn_s_sleep(32);
        }
    }
    {
        const int i = (r - 26) * 4 + x;
        const int d = (r < 8 ? DLY_S : r < 21 ? 0 : i < 4 ? (L ? 0 : DLY_H) : DLY_W + DLY_X * (i < 7 ? 0 : i < 10 ? 1 : i < 16 ? 2 : 3));
        for (int k = 0; k < d; ++k) __builtin_amdgcn_s_sleep(32);
    }
    if (r < 8) s_role(P, wave, L, x * 8 + r, x);
    else if (r < 16) v_role(P, wave ^ 4, L, x * 8 + r - 8, x);
    else if (r < 21) r_role(P, wave, L, r - 16, x);
    else {
        const int i = (r - 26) * 4 + x;
        if (i < 4) h_role(P, wave, L, r < 25 ? x * 4 + r - 21 : 16 + x * 2 + r - 25, x);
        else if (i < 7) w_role<1>(P, wave, L, i - 4, x);
        else if (i < 10) w_role<2>(P, wave, L, i - 7, x);
        else if (i < 16) w_role<3>(P, wave, L, i - 10, x);
        else w4_role(P, wave, L, i - 16, x);
    }
}

__global__ void code_end() {}

extern "C" void kernel_launch(void* const* d_in, const int* in_sizes, int n_in, void* d_out, int out_size, void* d_ws,
                              size_t ws_size, hipStream_t stream) {
    char* ws = (char*)d_ws;
    Params P;
    P.ws = ws; P.x = (const bf16_t*)d_in[0]; P.mem0 = (const bf16_t*)d_in[1]; P.out = (bf16_t*)d_out;
    for (int i = 0; i < 8; ++i) P.w[i] = (const bf16_t*)d_in[i < 4 ? 2 + i : 4 + i];
    (void)hipFuncSetAttribute((const void*)mega_kernel, hipFuncAttributeMaxDynamicSharedMemorySize, MEGA_LDS);
    mega_kernel<<<256, 512, MEGA_LDS, stream>>>(P);
}
```

```cpp
#include <hip/hip_runtime.h>
#include <stdint.h>
#include <type_traits>

typedef unsigned short bf16_t;
using bf16x2 = __attribute__((ext_vector_type(2))) __bf16;
using bf16x8 = __attribute__((ext_vector_type(8))) __bf16;
using bf16x16 = __attribute__((ext_vector_type(16))) __bf16;
using f32x2 = __attribute__((ext_vector_type(2))) float;
using f32x4 = __attribute__((ext_vector_type(4))) float;
using u32x2 = __attribute__((ext_vector_type(2))) unsigned;
using u32x4 = __attribute__((ext_vector_type(4))) unsigned;
typedef __amdgpu_buffer_rsrc_t rsrc_t;

__device__ __forceinline__ unsigned pack2(float a, float b) {
    bf16x2 r = __builtin_convertvector((f32x2){a, b}, bf16x2);
    return __builtin_bit_cast(unsigned, r);
}
__device__ __forceinline__ float rbf(float f) { return __uint_as_float(pack2(f, 0.f) << 16); }
__device__ __forceinline__ float lo2f(unsigned u) { return __uint_as_float(u << 16); }
__device__ __forceinline__ float hi2f(unsigned u) { return __uint_as_float(u & 0xFFFF0000u); }
__device__ __forceinline__ rsrc_t mkrsrc(const void* p, unsigned bytes) {
    return __builtin_amdgcn_make_buffer_rsrc((void*)p, 0, bytes, 0x00020000);
}

constexpr int S_ = 2048, DM = 640, NH = 32, DH = 192, HD = NH * DH  , SEG = 32, NSEG = 64;
constexpr int FF = 1536;

#define XMAP
#define NCPY 4
constexpr unsigned NSLOT = 4, NCP = NCPY;
#define FL_TK_DEF
#define DLY_L 10
#define DLY_S 0
#define DLY_W 10
#define DLY_H 3
#define EWAKE "s_nop 0"
#define LWAKE "s_wakeup\n\t"
#define SLP4 (wave == 4 ? t : 0)
#define WPF 12
#define WPRI 1
#define WPRI2 0
#define RTD 3350
#define VTD 3850
#define HAH 100
#define WEAK 0
#define BAL 0
#define HSL 4
#define OAUX 2
#define W4P 3
#define STD 3750
#define CAL1 6
#define CAL2 12
#define DLY_X 2
#define NTM 10
#define HTRY 1
#define EO 0
#define CALN 12
#define RSL 9
#define VTRY 0
#define VSL 1
#define NRR 5
constexpr unsigned BLK = 16384, RG_SLOT = 4u << 20;
constexpr unsigned XP_PAIR = 6 * BLK, XP_SLOT = RG_SLOT;
constexpr unsigned XM_PAIR = 4 * BLK, XM_SLOT = RG_SLOT;
constexpr unsigned VM_SLOT = RG_SLOT;
constexpr unsigned PX_SLOT = RG_SLOT;
constexpr unsigned RG_XP = 0, RG_XM = 12 * BLK, RG_VM = 20 * BLK, RG_PX = 60 * BLK;
constexpr unsigned RG_ST = 68 * BLK, RG_FV = 84 * BLK, RG_FH = 86 * BLK, RG_FX = 88 * BLK, RG_L = NSLOT * RG_SLOT;
constexpr unsigned FL0 = 89 * BLK, FL_TK = 512, FL_IN = FL_TK + 8 * 128, FL_HB = FL_IN + 16 * 128, FL_BYTES = FL_HB + 8 * 128;
constexpr unsigned ACT = S_ * DM * 2;
#define PXROT 5
#define W4SL 5
#define RXDEL 1
#define VXD 32
#define VXAUX 16
constexpr unsigned PG0 = RG_L, PG_L = 80u << 21;
constexpr unsigned PG_XA = 0, PG_HS = 5 * BLK, PG_X1 = 10 * BLK, PG_H2 = 15 * BLK, PG_SS = 27 * BLK, PG_XR = 28 * BLK, PG_XN = 33 * BLK, PG_LX = 10 * BLK, PG_HV = 48 * BLK;
__device__ __forceinline__ unsigned pgb(unsigned p, int par) { return p << 21 | ((p ^ par) & 1) << 13; }
__device__ __forceinline__ constexpr unsigned blin(unsigned o) { return o + (o & ~8191u); }
constexpr int HT0 = 2;
constexpr int AH = 3;
constexpr unsigned WS_END = PG0 + PG_L;

#define SB() __builtin_amdgcn_sched_barrier(0)
#define LAS __attribute__((address_space(3)))
template <class T> __device__ __forceinline__ T lds_ld(unsigned a) { return *(const LAS T*)(a); }
template <class T> __device__ __forceinline__ void lds_st(unsigned a, T v) { *(LAS T*)(a) = v; }
constexpr unsigned OOB = 0x7FFFFF00u;
template <int N> __device__ __forceinline__ void wait_vm() { asm volatile("s_waitcnt vmcnt(%0)" :: "i"(N) : "memory"); }
__device__ __forceinline__ void bar() {
    asm volatile("s_waitcnt lgkmcnt(0)" ::: "memory");
    __builtin_amdgcn_s_barrier();
    asm volatile("" ::: "memory");
}
__device__ __forceinline__ int lane_id() {
    int ln;
    asm volatile("v_mbcnt_lo_u32_b32 %0, -1, 0\n\tv_mbcnt_hi_u32_b32 %0, -1, %0" : "=v"(ln));
    return ln;
}
__device__ __forceinline__ void swap16(unsigned& a, unsigned& b) { asm volatile("s_nop 3\n\tv_permlane16_swap_b32 %0, %1\n\ts_nop 1" : "+v"(a), "+v"(b)); }
__device__ __forceinline__ void swap32(unsigned& a, unsigned& b) { asm volatile("s_nop 3\n\tv_permlane32_swap_b32 %0, %1\n\ts_nop 1" : "+v"(a), "+v"(b)); }
__device__ __forceinline__ float sum_x16(float x) { unsigned a = __float_as_uint(x), b = a; swap16(a, b); return __uint_as_float(a) + __uint_as_float(b); }
__device__ __forceinline__ float sum_x32(float x) { unsigned a = __float_as_uint(x), b = a; swap32(a, b); return __uint_as_float(a) + __uint_as_float(b); }
__device__ __forceinline__ float max_x16(float x) { unsigned a = __float_as_uint(x), b = a; swap16(a, b); return __builtin_fmaxf(__uint_as_float(a), __uint_as_float(b)); }
__device__ __forceinline__ float max_x32(float x) { unsigned a = __float_as_uint(x), b = a; swap32(a, b); return __builtin_fmaxf(__uint_as_float(a), __uint_as_float(b)); }
__device__ __forceinline__ void sum2_rows(float& s, float& q) {
    unsigned a = __float_as_uint(s), b = a, c = __float_as_uint(q), d = c;
    asm volatile("s_nop 2\n\tv_permlane16_swap_b32 %0, %1\n\tv_permlane16_swap_b32 %2, %3\n\tv_add_f32 %0, %0, %1\n\tv_add_f32 %2, %2, %3\n\tv_mov_b32 %1, %0\n\tv_mov_b32 %3, %2\n\t"
                 "s_nop 1\n\tv_permlane32_swap_b32 %0, %1\n\tv_permlane32_swap_b32 %2, %3\n\tv_add_f32 %0, %0, %1\n\tv_add_f32 %2, %2, %3" : "+v"(a), "+v"(b), "+v"(c), "+v"(d));
    s = __uint_as_float(a); q = __uint_as_float(c);
}
__device__ __forceinline__ u32x4 merge16(u32x2 a, u32x2 b) {
    unsigned a0 = a[0], a1 = a[1], b0 = b[0], b1 = b[1];
    asm volatile("s_nop 3\n\tv_permlane16_swap_b32 %0, %2\n\tv_permlane16_swap_b32 %1, %3\n\ts_nop 1" : "+v"(a0), "+v"(a1), "+v"(b0), "+v"(b1));
    return (u32x4){a0, a1, b0, b1};
}
template <int AUXV>
__device__ __forceinline__ void st128_(u32x4 v, rsrc_t r, unsigned voff, unsigned soff) {
    __builtin_amdgcn_raw_buffer_store_b128(v, r, voff, soff, AUXV);
    asm volatile("s_nop 1" :: "v"(v) : "memory");
}
#define st128(V, R, VO_, SO_) st128_<16>(V, R, VO_, SO_)
#define LD128(R, VO_, SO_) __builtin_amdgcn_raw_buffer_load_b128(R, VO_, SO_, 16)
#define LD64(R, VO_, SO_) __builtin_amdgcn_raw_buffer_load_b64(R, VO_, SO_, 16)
#define BF8(X) __builtin_bit_cast(bf16x8, X)
__device__ __forceinline__ float gelu_tanh(float x) {
    const float c0 = -2.0f * 0.7978845608028654f * 1.4426950408889634f, c1 = c0 * 0.044715f;
    const float e = __builtin_amdgcn_exp2f(x * __builtin_fmaf(x * x, c1, c0));
    return x * __builtin_amdgcn_rcpf(1.0f + e);
}
#define MFMA(A_, B_, C_) C_ = __builtin_amdgcn_mfma_f32_16x16x32_bf16(A_, B_, C_, 0, 0, 0)
#define SMF(A_, B_, C_) C_ = __builtin_amdgcn_smfmac_f32_16x16x64_bf16(A_, B_, C_, sidx, 0, 0)
__device__ __forceinline__ float ln_den(float var) { return rbf(__builtin_amdgcn_sqrtf(rbf(rbf(var) + 1e-5f))); }
__device__ __forceinline__ float rcp(float x) { return __builtin_amdgcn_rcpf(x); }

struct Params {
    char* ws;
    const bf16_t* x;
    const bf16_t* mem0;
    const bf16_t* w[8];
    bf16_t* out;
};

#define RLX_AGENT __ATOMIC_RELAXED, __HIP_MEMORY_SCOPE_AGENT
#define SENT 0xFFFFFFFFu
constexpr int SPIN_CAP = 400000;
__device__ __forceinline__ bool spin_fail(int spins, const Params& P) {
    if ((spins & 1023) == 1023) {
        const rsrc_t rt = mkrsrc(P.ws + FL0, FL_BYTES);
        unsigned o;
        asm volatile("v_mov_b32 %0, 0x100" : "=v"(o));
        if (__builtin_amdgcn_readfirstlane(__builtin_amdgcn_raw_buffer_load_b32(rt, o, 0, 16)) != 0u) return true;
        if (spins > SPIN_CAP) { __builtin_amdgcn_raw_buffer_store_b32(o, rt, o, 0, 16); return true; }
    }
    return false;
}
__device__ __forceinline__ bool ok4(u32x4 v) { return v[0] != SENT && v[1] != SENT && v[2] != SENT && v[3] != SENT; }
__device__ __forceinline__ unsigned nosent(unsigned d) { return d < 0xFFFFFFFEu ? d : 0xFFFFFFFEu; }
__device__ __forceinline__ u32x4 nosent4(u32x4 v) { return (u32x4){nosent(v[0]), nosent(v[1]), nosent(v[2]), nosent(v[3])}; }
__device__ __forceinline__ unsigned umax3(unsigned a, unsigned b, unsigned c) { const unsigned m = a > b ? a : b; return m > c ? m : c; }
__device__ __forceinline__ unsigned umax4(unsigned m, u32x4 v) { return umax3(umax3(m, v[0], v[1]), v[2], v[3]); }
__device__ __forceinline__ u32x4 sent4() { unsigned sv; asm volatile("v_mov_b32 %0, -1" : "=v"(sv)); return (u32x4){sv, sv, sv, sv}; }
template <int N>
__device__ __forceinline__ void poll(rsrc_t rs, unsigned base, const unsigned (&off)[N], u32x4 (&v)[N], const Params& P) {
    for (int spins = 0;; ++spins) {
        unsigned m = 0;
#pragma unroll
        for (int k = 0; k < N; ++k) v[k] = LD128(rs, off[k], base);
#pragma unroll
        for (int k = 0; k < N; ++k) m = umax4(m, v[k]);
        if (__all(m != SENT)) return;
        asm volatile("" ::: "memory");
        if (spin_fail(spins, P)) return;
    }
}
__device__ __forceinline__ bf16x16 cat16(bf16x8 a, bf16x8 b) {
    return __builtin_shufflevector(a, b, 0, 1, 2, 3, 4, 5, 6, 7, 8, 9, 10, 11, 12, 13, 14, 15);
}
__device__ __forceinline__ bf16x8 half8(bf16x16 v, int h) {
    return h ? __builtin_shufflevector(v, v, 8, 9, 10, 11, 12, 13, 14, 15) : __builtin_shufflevector(v, v, 0, 1, 2, 3, 4, 5, 6, 7);
}
__device__ __forceinline__ bf16x16 lds_frag(unsigned a, unsigned step) { return cat16(lds_ld<bf16x8>(a), lds_ld<bf16x8>(a + step)); }
template <int I>
__device__ __forceinline__ void dmaq(const char* g, unsigned l16, unsigned dst) {
    asm volatile("s_mov_b32 m0, %2\n\ts_nop 0\n\tglobal_load_lds_dwordx4 %0, %1 offset:%3 sc1" :: "v"(l16), "s"(g), "s"(dst), "i"(I * 1024) : "memory");
}
__device__ __forceinline__ void dma4(const char* g, unsigned l16, unsigned dst) {
    asm volatile("s_mov_b32 m0, %2\n\ts_nop 0\n\tglobal_load_lds_dwordx4 %0, %1 sc1\n\tglobal_load_lds_dwordx4 %0, %1 offset:1024 sc1\n\t"
                 "global_load_lds_dwordx4 %0, %1 offset:2048 sc1\n\tglobal_load_lds_dwordx4 %0, %1 offset:3072 sc1" :: "v"(l16), "s"(g), "s"(dst) : "memory");
}
__device__ __forceinline__ unsigned toff(int row, int col, int par) {
    return pgb(row >> 5, par) + blin((unsigned)((row >> 4) & 1) * 20480u + (unsigned)(col >> 3) * 256u) + (unsigned)(row & 15) * 16u;
}

#define ASM_ST
#define ASM_ST_DECL
#define ASM_ST_OUT
#define ASM_ST_SAVE(k)
#define ASM_ST2
#define ASM_ST3
#define ASM_ST3_OUT
#define ASM_ST3_SAVE(k)
#define ASM_ST2_OUT
#define ASM_ST2_SAVE(k)
#define STDECL
#define ST(k)
#define STC(k)
#define STFLUSH(t)

#define MARK(c, k)
#define MARKX(c, k)
#define PMARK(c, st, seg)
#define LDECL
#define LA()
#define LB(t)
#define LF()
#define STROLE 3
constexpr unsigned L_MEM = 0;
constexpr unsigned L_Q = 10240;
constexpr unsigned L_KM = 13312;
constexpr unsigned L_QX = 16384;
constexpr unsigned L_KX = 28672;
constexpr unsigned L_XB = 40960;
constexpr unsigned L_XA = 49152;
constexpr unsigned L_W = 81920;
constexpr int MEGA_LDS = 163840;
constexpr int wi(int i, int j) { return (i >> 1) * 5 + ((i & 1) ? 2 + j : j - 1); }

#define RPUB2
#define WNT ""
#define PRIUP "s_setprio 2\n\t"
#define PRISM PRIUP
#define STR2(x) #x
#define STRX(x) STR2(x)
#define HSLEEP "s_sleep " STRX(HSL) "\n\t"
#define W4SLEEP "s_sleep " STRX(W4SL) "\n\t"
#define SMSY "buffer_store_dwordx4 v[240:243], v200, %[rr], %[sy] offen sc1\n\t"
#define PLAY 0
#define V1A 0
#define ASM_WLOAD3 \
    "s_xor_b32 %[t], %[wx], 0\n\t" \
    "s_mul_i32 %[t], %[t], 0xc0000\n\t" \
    "buffer_load_dwordx4 v[200:203], %[o0], %[rs], %[t] offen" WNT "\n\t" \
    "buffer_load_dwordx4 v[0:3], %[o1], %[rs], %[t] offen" WNT "\n\t" \
    "buffer_load_dwordx4 v[8:11], %[o2], %[rs], %[t] offen" WNT "\n\t" \
    "s_add_u32 %[t], %[t], 0x18000\n\t" \
    "buffer_load_dwordx4 v[16:19], %[o0], %[rs], %[t] offen" WNT "\n\t" \
    "buffer_load_dwordx4 v[24:27], %[o1], %[rs], %[t] offen" WNT "\n\t" \
    "buffer_load_dwordx4 v[32:35], %[o2], %[rs], %[t] offen" WNT "\n\t" \
    "s_add_u32 %[t], %[t], 0x18000\n\t" \
    "buffer_load_dwordx4 v[208:211], %[o0], %[rs], %[t] offen" WNT "\n\t" \
    "buffer_load_dwordx4 v[40:43], %[o1], %[rs], %[t] offen" WNT "\n\t" \
    "buffer_load_dwordx4 v[48:51], %[o2], %[rs], %[t] offen" WNT "\n\t" \
    "s_add_u32 %[t], %[t], 0x18000\n\t" \
    "buffer_load_dwordx4 v[56:59], %[o0], %[rs], %[t] offen" WNT "\n\t" \
    "buffer_load_dwordx4 v[64:67], %[o1], %[rs], %[t] offen" WNT "\n\t" \
    "buffer_load_dwordx4 v[72:75], %[o2], %[rs], %[t] offen" WNT "\n\t" \
    "s_add_u32 %[t], %[t], 0x18000\n\t" \
    "buffer_load_dwordx4 v[204:207], %[o0], %[rs], %[t] offen" WNT "\n\t" \
    "buffer_load_dwordx4 v[4:7], %[o1], %[rs], %[t] offen" WNT "\n\t" \
    "buffer_load_dwordx4 v[12:15], %[o2], %[rs], %[t] offen" WNT "\n\t" \
    "s_add_u32 %[t], %[t], 0x18000\n\t" \
    "buffer_load_dwordx4 v[20:23], %[o0], %[rs], %[t] offen" WNT "\n\t" \
    "buffer_load_dwordx4 v[28:31], %[o1], %[rs], %[t] offen" WNT "\n\t" \
    "buffer_load_dwordx4 v[36:39], %[o2], %[rs], %[t] offen" WNT "\n\t" \
    "s_add_u32 %[t], %[t], 0x18000\n\t" \
    "buffer_load_dwordx4 v[212:215], %[o0], %[rs], %[t] offen" WNT "\n\t" \
    "buffer_load_dwordx4 v[44:47], %[o1], %[rs], %[t] offen" WNT "\n\t" \
    "buffer_load_dwordx4 v[52:55], %[o2], %[rs], %[t] offen" WNT "\n\t" \
    "s_add_u32 %[t], %[t], 0x18000\n\t" \
    "buffer_load_dwordx4 v[60:63], %[o0], %[rs], %[t] offen" WNT "\n\t" \
    "buffer_load_dwordx4 v[68:71], %[o1], %[rs], %[t] offen" WNT "\n\t" \
    "buffer_load_dwordx4 v[76:79], %[o2], %[rs], %[t] offen" WNT "\n\t" \
    "s_add_u32 %[t], %[t], 0x18000\n\t" \
    "s_waitcnt vmcnt(21)\n\t" \
    "ds_write_b128 %[s0], v[200:203] offset:0\n\t" \
    "ds_write_b128 %[s1], v[0:3] offset:0\n\t" \
    "ds_write_b128 %[s2], v[8:11] offset:0\n\t" \
    "s_waitcnt vmcnt(18)\n\t" \
    "ds_write_b128 %[s0], v[16:19] offset:3456\n\t" \
    "ds_write_b128 %[s1], v[24:27] offset:3456\n\t" \
    "ds_write_b128 %[s2], v[32:35] offset:3456\n\t" \
    "s_waitcnt vmcnt(15)\n\t" \
    "ds_write_b128 %[s0], v[208:211] offset:6912\n\t" \
    "ds_write_b128 %[s1], v[40:43] offset:6912\n\t" \
    "ds_write_b128 %[s2], v[48:51] offset:6912\n\t" \
    "s_waitcnt vmcnt(12)\n\t" \
    "ds_write_b128 %[s0], v[56:59] offset:10368\n\t" \
    "ds_write_b128 %[s1], v[64:67] offset:10368\n\t" \
    "ds_write_b128 %[s2], v[72:75] offset:10368\n\t" \
    "s_waitcnt lgkmcnt(0)\n\t" \
    "s_barrier \n\t" \
    "ds_read_b64_tr_b16 v[200:201], %[t0] offset:0\n\t" \
    "ds_read_b64_tr_b16 v[202:203], %[t0] offset:1664\n\t" \
    "ds_read_b64_tr_b16 v[0:1], %[t0] offset:128\n\t" \
    "ds_read_b64_tr_b16 v[2:3], %[t0] offset:1792\n\t" \
    "ds_read_b64_tr_b16 v[8:9], %[t0] offset:256\n\t" \
    "ds_read_b64_tr_b16 v[10:11], %[t0] offset:1920\n\t" \
    "ds_read_b64_tr_b16 v[16:17], %[t1] offset:0\n\t" \
    "ds_read_b64_tr_b16 v[18:19], %[t1] offset:1664\n\t" \
    "ds_read_b64_tr_b16 v[24:25], %[t1] offset:128\n\t" \
    "ds_read_b64_tr_b16 v[26:27], %[t1] offset:1792\n\t" \
    "ds_read_b64_tr_b16 v[32:33], %[t1] offset:256\n\t" \
    "ds_read_b64_tr_b16 v[34:35], %[t1] offset:1920\n\t" \
    "ds_read_b64_tr_b16 v[208:209], %[t2] offset:0\n\t" \
    "ds_read_b64_tr_b16 v[210:211], %[t2] offset:1664\n\t" \
    "ds_read_b64_tr_b16 v[40:41], %[t2] offset:128\n\t" \
    "ds_read_b64_tr_b16 v[42:43], %[t2] offset:1792\n\t" \
    "ds_read_b64_tr_b16 v[48:49], %[t2] offset:256\n\t" \
    "ds_read_b64_tr_b16 v[50:51], %[t2] offset:1920\n\t" \
    "ds_read_b64_tr_b16 v[56:57], %[t3] offset:0\n\t" \
    "ds_read_b64_tr_b16 v[58:59], %[t3] offset:1664\n\t" \
    "ds_read_b64_tr_b16 v[64:65], %[t3] offset:128\n\t" \
    "ds_read_b64_tr_b16 v[66:67], %[t3] offset:1792\n\t" \
    "ds_read_b64_tr_b16 v[72:73], %[t3] offset:256\n\t" \
    "ds_read_b64_tr_b16 v[74:75], %[t3] offset:1920\n\t" \
    "s_xor_b32 %[t], %[wx], 4\n\t" \
    "s_mul_i32 %[t], %[t], 0xc0000\n\t" \
    "buffer_load_dwordx4 v[216:219], %[o0], %[rs], %[t] offen" WNT "\n\t" \
    "buffer_load_dwordx4 v[80:83], %[o1], %[rs], %[t] offen" WNT "\n\t" \
    "buffer_load_dwordx4 v[88:91], %[o2], %[rs], %[t] offen" WNT "\n\t" \
    "s_add_u32 %[t], %[t], 0x18000\n\t" \
    "buffer_load_dwordx4 v[96:99], %[o0], %[rs], %[t] offen" WNT "\n\t" \
    "buffer_load_dwordx4 v[104:107], %[o1], %[rs], %[t] offen" WNT "\n\t" \
    "buffer_load_dwordx4 v[112:115], %[o2], %[rs], %[t] offen" WNT "\n\t" \
    "s_add_u32 %[t], %[t], 0x18000\n\t" \
    "buffer_load_dwordx4 v[224:227], %[o0], %[rs], %[t] offen" WNT "\n\t" \
    "buffer_load_dwordx4 v[120:123], %[o1], %[rs], %[t] offen" WNT "\n\t" \
    "buffer_load_dwordx4 v[128:131], %[o2], %[rs], %[t] offen" WNT "\n\t" \
    "s_add_u32 %[t], %[t], 0x18000\n\t" \
    "buffer_load_dwordx4 v[136:139], %[o0], %[rs], %[t] offen" WNT "\n\t" \
    "buffer_load_dwordx4 v[144:147], %[o1], %[rs], %[t] offen" WNT "\n\t" \
    "buffer_load_dwordx4 v[152:155], %[o2], %[rs], %[t] offen" WNT "\n\t" \
    "s_add_u32 %[t], %[t], 0x18000\n\t" \
    "s_waitcnt lgkmcnt(0)\n\t" \
    "s_barrier \n\t" \
    "s_waitcnt vmcnt(21)\n\t" \
    "ds_write_b128 %[s0], v[204:207] offset:0\n\t" \
    "ds_write_b128 %[s1], v[4:7] offset:0\n\t" \
    "ds_write_b128 %[s2], v[12:15] offset:0\n\t" \
    "s_waitcnt vmcnt(18)\n\t" \
    "ds_write_b128 %[s0], v[20:23] offset:3456\n\t" \
    "ds_write_b128 %[s1], v[28:31] offset:3456\n\t" \
    "ds_write_b128 %[s2], v[36:39] offset:3456\n\t" \
    "s_waitcnt vmcnt(15)\n\t" \
    "ds_write_b128 %[s0], v[212:215] offset:6912\n\t" \
    "ds_write_b128 %[s1], v[44:47] offset:6912\n\t" \
    "ds_write_b128 %[s2], v[52:55] offset:6912\n\t" \
    "s_waitcnt vmcnt(12)\n\t" \
    "ds_write_b128 %[s0], v[60:63] offset:10368\n\t" \
    "ds_write_b128 %[s1], v[68:71] offset:10368\n\t" \
    "ds_write_b128 %[s2], v[76:79] offset:10368\n\t" \
    "s_waitcnt lgkmcnt(0)\n\t" \
    "s_barrier \n\t" \
    "ds_read_b64_tr_b16 v[204:205], %[t0] offset:0\n\t" \
    "ds_read_b64_tr_b16 v[206:207], %[t0] offset:1664\n\t" \
    "ds_read_b64_tr_b16 v[4:5], %[t0] offset:128\n\t" \
    "ds_read_b64_tr_b16 v[6:7], %[t0] offset:1792\n\t" \
    "ds_read_b64_tr_b16 v[12:13], %[t0] offset:256\n\t" \
    "ds_read_b64_tr_b16 v[14:15], %[t0] offset:1920\n\t" \
    "ds_read_b64_tr_b16 v[20:21], %[t1] offset:0\n\t" \
    "ds_read_b64_tr_b16 v[22:23], %[t1] offset:1664\n\t" \
    "ds_read_b64_tr_b16 v[28:29], %[t1] offset:128\n\t" \
    "ds_read_b64_tr_b16 v[30:31], %[t1] offset:1792\n\t" \
    "ds_read_b64_tr_b16 v[36:37], %[t1] offset:256\n\t" \
    "ds_read_b64_tr_b16 v[38:39], %[t1] offset:1920\n\t" \
    "ds_read_b64_tr_b16 v[212:213], %[t2] offset:0\n\t" \
    "ds_read_b64_tr_b16 v[214:215], %[t2] offset:1664\n\t" \
    "ds_read_b64_tr_b16 v[44:45], %[t2] offset:128\n\t" \
    "ds_read_b64_tr_b16 v[46:47], %[t2] offset:1792\n\t" \
    "ds_read_b64_tr_b16 v[52:53], %[t2] offset:256\n\t" \
    "ds_read_b64_tr_b16 v[54:55], %[t2] offset:1920\n\t" \
    "ds_read_b64_tr_b16 v[60:61], %[t3] offset:0\n\t" \
    "ds_read_b64_tr_b16 v[62:63], %[t3] offset:1664\n\t" \
    "ds_read_b64_tr_b16 v[68:69], %[t3] offset:128\n\t" \
    "ds_read_b64_tr_b16 v[70:71], %[t3] offset:1792\n\t" \
    "ds_read_b64_tr_b16 v[76:77], %[t3] offset:256\n\t" \
    "ds_read_b64_tr_b16 v[78:79], %[t3] offset:1920\n\t" \
    "buffer_load_dwordx4 v[220:223], %[o0], %[rs], %[t] offen" WNT "\n\t" \
    "buffer_load_dwordx4 v[84:87], %[o1], %[rs], %[t] offen" WNT "\n\t" \
    "buffer_load_dwordx4 v[92:95], %[o2], %[rs], %[t] offen" WNT "\n\t" \
    "s_add_u32 %[t], %[t], 0x18000\n\t" \
    "buffer_load_dwordx4 v[100:103], %[o0], %[rs], %[t] offen" WNT "\n\t" \
    "buffer_load_dwordx4 v[108:111], %[o1], %[rs], %[t] offen" WNT "\n\t" \
    "buffer_load_dwordx4 v[116:119], %[o2], %[rs], %[t] offen" WNT "\n\t" \
    "s_add_u32 %[t], %[t], 0x18000\n\t" \
    "buffer_load_dwordx4 v[228:231], %[o0], %[rs], %[t] offen" WNT "\n\t" \
    "buffer_load_dwordx4 v[124:127], %[o1], %[rs], %[t] offen" WNT "\n\t" \
    "buffer_load_dwordx4 v[132:135], %[o2], %[rs], %[t] offen" WNT "\n\t" \
    "s_add_u32 %[t], %[t], 0x18000\n\t" \
    "buffer_load_dwordx4 v[140:143], %[o0], %[rs], %[t] offen" WNT "\n\t" \
    "buffer_load_dwordx4 v[148:151], %[o1], %[rs], %[t] offen" WNT "\n\t" \
    "buffer_load_dwordx4 v[156:159], %[o2], %[rs], %[t] offen" WNT "\n\t" \
    "s_add_u32 %[t], %[t], 0x18000\n\t" \
    "s_waitcnt lgkmcnt(0)\n\t" \
    "s_barrier \n\t" \
    "s_waitcnt vmcnt(21)\n\t" \
    "ds_write_b128 %[s0], v[216:219] offset:0\n\t" \
    "ds_write_b128 %[s1], v[80:83] offset:0\n\t" \
    "ds_write_b128 %[s2], v[88:91] offset:0\n\t" \
    "s_waitcnt vmcnt(18)\n\t" \
    "ds_write_b128 %[s0], v[96:99] offset:3456\n\t" \
    "ds_write_b128 %[s1], v[104:107] offset:3456\n\t" \
    "ds_write_b128 %[s2], v[112:115] offset:3456\n\t" \
    "s_waitcnt vmcnt(15)\n\t" \
    "ds_write_b128 %[s0], v[224:227] offset:6912\n\t" \
    "ds_write_b128 %[s1], v[120:123] offset:6912\n\t" \
    "ds_write_b128 %[s2], v[128:131] offset:6912\n\t" \
    "s_waitcnt vmcnt(12)\n\t" \
    "ds_write_b128 %[s0], v[136:139] offset:10368\n\t" \
    "ds_write_b128 %[s1], v[144:147] offset:10368\n\t" \
    "ds_write_b128 %[s2], v[152:155] offset:10368\n\t" \
    "s_waitcnt lgkmcnt(0)\n\t" \
    "s_barrier \n\t" \
    "ds_read_b64_tr_b16 v[216:217], %[t0] offset:0\n\t" \
    "ds_read_b64_tr_b16 v[218:219], %[t0] offset:1664\n\t" \
    "ds_read_b64_tr_b16 v[80:81], %[t0] offset:128\n\t" \
    "ds_read_b64_tr_b16 v[82:83], %[t0] offset:1792\n\t" \
    "ds_read_b64_tr_b16 v[88:89], %[t0] offset:256\n\t" \
    "ds_read_b64_tr_b16 v[90:91], %[t0] offset:1920\n\t" \
    "ds_read_b64_tr_b16 v[96:97], %[t1] offset:0\n\t" \
    "ds_read_b64_tr_b16 v[98:99], %[t1] offset:1664\n\t" \
    "ds_read_b64_tr_b16 v[104:105], %[t1] offset:128\n\t" \
    "ds_read_b64_tr_b16 v[106:107], %[t1] offset:1792\n\t" \
    "ds_read_b64_tr_b16 v[112:113], %[t1] offset:256\n\t" \
    "ds_read_b64_tr_b16 v[114:115], %[t1] offset:1920\n\t" \
    "ds_read_b64_tr_b16 v[224:225], %[t2] offset:0\n\t" \
    "ds_read_b64_tr_b16 v[226:227], %[t2] offset:1664\n\t" \
    "ds_read_b64_tr_b16 v[120:121], %[t2] offset:128\n\t" \
    "ds_read_b64_tr_b16 v[122:123], %[t2] offset:1792\n\t" \
    "ds_read_b64_tr_b16 v[128:129], %[t2] offset:256\n\t" \
    "ds_read_b64_tr_b16 v[130:131], %[t2] offset:1920\n\t" \
    "ds_read_b64_tr_b16 v[136:137], %[t3] offset:0\n\t" \
    "ds_read_b64_tr_b16 v[138:139], %[t3] offset:1664\n\t" \
    "ds_read_b64_tr_b16 v[144:145], %[t3] offset:128\n\t" \
    "ds_read_b64_tr_b16 v[146:147], %[t3] offset:1792\n\t" \
    "ds_read_b64_tr_b16 v[152:153], %[t3] offset:256\n\t" \
    "ds_read_b64_tr_b16 v[154:155], %[t3] offset:1920\n\t" \
    "s_and_b32 %[t], %[wx], 3\n\t" \
    "s_mul_i32 %[t], %[t], 0x60000\n\t" \
    "s_add_u32 %[t], %[t], 0x600000\n\t" \
    "buffer_load_dwordx4 v[232:235], %[o0], %[rs], %[t] offen" WNT "\n\t" \
    "buffer_load_dwordx4 v[160:163], %[o1], %[rs], %[t] offen" WNT "\n\t" \
    "buffer_load_dwordx4 v[168:171], %[o2], %[rs], %[t] offen" WNT "\n\t" \
    "s_add_u32 %[t], %[t], 0x18000\n\t" \
    "buffer_load_dwordx4 v[236:239], %[o0], %[rs], %[t] offen" WNT "\n\t" \
    "buffer_load_dwordx4 v[164:167], %[o1], %[rs], %[t] offen" WNT "\n\t" \
    "buffer_load_dwordx4 v[172:175], %[o2], %[rs], %[t] offen" WNT "\n\t" \
    "s_add_u32 %[t], %[t], 0x18000\n\t" \
    "buffer_load_dwordx4 v[176:179], %[o0], %[rs], %[t] offen" WNT "\n\t" \
    "buffer_load_dwordx4 v[184:187], %[o1], %[rs], %[t] offen" WNT "\n\t" \
    "buffer_load_dwordx4 v[192:195], %[o2], %[rs], %[t] offen" WNT "\n\t" \
    "s_add_u32 %[t], %[t], 0x18000\n\t" \
    "buffer_load_dwordx4 v[180:183], %[o0], %[rs], %[t] offen" WNT "\n\t" \
    "buffer_load_dwordx4 v[188:191], %[o1], %[rs], %[t] offen" WNT "\n\t" \
    "buffer_load_dwordx4 v[196:199], %[o2], %[rs], %[t] offen" WNT "\n\t" \
    "s_add_u32 %[t], %[t], 0x18000\n\t" \
    "s_waitcnt lgkmcnt(0)\n\t" \
    "s_barrier \n\t" \
    "s_waitcnt vmcnt(21)\n\t" \
    "ds_write_b128 %[s0], v[220:223] offset:0\n\t" \
    "ds_write_b128 %[s1], v[84:87] offset:0\n\t" \
    "ds_write_b128 %[s2], v[92:95] offset:0\n\t" \
    "s_waitcnt vmcnt(18)\n\t" \
    "ds_write_b128 %[s0], v[100:103] offset:3456\n\t" \
    "ds_write_b128 %[s1], v[108:111] offset:3456\n\t" \
    "ds_write_b128 %[s2], v[116:119] offset:3456\n\t" \
    "s_waitcnt vmcnt(15)\n\t" \
    "ds_write_b128 %[s0], v[228:231] offset:6912\n\t" \
    "ds_write_b128 %[s1], v[124:127] offset:6912\n\t" \
    "ds_write_b128 %[s2], v[132:135] offset:6912\n\t" \
    "s_waitcnt vmcnt(12)\n\t" \
    "ds_write_b128 %[s0], v[140:143] offset:10368\n\t" \
    "ds_write_b128 %[s1], v[148:151] offset:10368\n\t" \
    "ds_write_b128 %[s2], v[156:159] offset:10368\n\t" \
    "s_waitcnt lgkmcnt(0)\n\t" \
    "s_barrier \n\t" \
    "ds_read_b64_tr_b16 v[220:221], %[t0] offset:0\n\t" \
    "ds_read_b64_tr_b16 v[222:223], %[t0] offset:1664\n\t" \
    "ds_read_b64_tr_b16 v[84:85], %[t0] offset:128\n\t" \
    "ds_read_b64_tr_b16 v[86:87], %[t0] offset:1792\n\t" \
    "ds_read_b64_tr_b16 v[92:93], %[t0] offset:256\n\t" \
    "ds_read_b64_tr_b16 v[94:95], %[t0] offset:1920\n\t" \
    "ds_read_b64_tr_b16 v[100:101], %[t1] offset:0\n\t" \
    "ds_read_b64_tr_b16 v[102:103], %[t1] offset:1664\n\t" \
    "ds_read_b64_tr_b16 v[108:109], %[t1] offset:128\n\t" \
    "ds_read_b64_tr_b16 v[110:111], %[t1] offset:1792\n\t" \
    "ds_read_b64_tr_b16 v[116:117], %[t1] offset:256\n\t" \
    "ds_read_b64_tr_b16 v[118:119], %[t1] offset:1920\n\t" \
    "ds_read_b64_tr_b16 v[228:229], %[t2] offset:0\n\t" \
    "ds_read_b64_tr_b16 v[230:231], %[t2] offset:1664\n\t" \
    "ds_read_b64_tr_b16 v[124:125], %[t2] offset:128\n\t" \
    "ds_read_b64_tr_b16 v[126:127], %[t2] offset:1792\n\t" \
    "ds_read_b64_tr_b16 v[132:133], %[t2] offset:256\n\t" \
    "ds_read_b64_tr_b16 v[134:135], %[t2] offset:1920\n\t" \
    "ds_read_b64_tr_b16 v[140:141], %[t3] offset:0\n\t" \
    "ds_read_b64_tr_b16 v[142:143], %[t3] offset:1664\n\t" \
    "ds_read_b64_tr_b16 v[148:149], %[t3] offset:128\n\t" \
    "ds_read_b64_tr_b16 v[150:151], %[t3] offset:1792\n\t" \
    "ds_read_b64_tr_b16 v[156:157], %[t3] offset:256\n\t" \
    "ds_read_b64_tr_b16 v[158:159], %[t3] offset:1920\n\t" \
    "s_waitcnt lgkmcnt(0)\n\t" \
    "s_barrier \n\t" \
    "s_waitcnt vmcnt(9)\n\t" \
    "ds_write_b128 %[s0], v[232:235] offset:0\n\t" \
    "ds_write_b128 %[s1], v[160:163] offset:0\n\t" \
    "ds_write_b128 %[s2], v[168:171] offset:0\n\t" \
    "s_waitcnt vmcnt(6)\n\t" \
    "ds_write_b128 %[s0], v[236:239] offset:3456\n\t" \
    "ds_write_b128 %[s1], v[164:167] offset:3456\n\t" \
    "ds_write_b128 %[s2], v[172:175] offset:3456\n\t" \
    "s_waitcnt vmcnt(3)\n\t" \
    "ds_write_b128 %[s0], v[176:179] offset:6912\n\t" \
    "ds_write_b128 %[s1], v[184:187] offset:6912\n\t" \
    "ds_write_b128 %[s2], v[192:195] offset:6912\n\t" \
    "s_waitcnt vmcnt(0)\n\t" \
    "ds_write_b128 %[s0], v[180:183] offset:10368\n\t" \
    "ds_write_b128 %[s1], v[188:191] offset:10368\n\t" \
    "ds_write_b128 %[s2], v[196:199] offset:10368\n\t" \
    "s_waitcnt lgkmcnt(0)\n\t" \
    "s_barrier \n\t" \
    "ds_read_b64_tr_b16 v[232:233], %[t4] offset:0\n\t" \
    "ds_read_b64_tr_b16 v[234:235], %[t4] offset:1664\n\t" \
    "ds_read_b64_tr_b16 v[160:161], %[t4] offset:128\n\t" \
    "ds_read_b64_tr_b16 v[162:163], %[t4] offset:1792\n\t" \
    "ds_read_b64_tr_b16 v[168:169], %[t4] offset:256\n\t" \
    "ds_read_b64_tr_b16 v[170:171], %[t4] offset:1920\n\t" \
    "ds_read_b64_tr_b16 v[236:237], %[t4] offset:13824\n\t" \
    "ds_read_b64_tr_b16 v[238:239], %[t4] offset:15488\n\t" \
    "ds_read_b64_tr_b16 v[164:165], %[t4] offset:13952\n\t" \
    "ds_read_b64_tr_b16 v[166:167], %[t4] offset:15616\n\t" \
    "ds_read_b64_tr_b16 v[172:173], %[t4] offset:14080\n\t" \
    "ds_read_b64_tr_b16 v[174:175], %[t4] offset:15744\n\t" \
    "ds_read_b64_tr_b16 v[176:177], %[t4] offset:27648\n\t" \
    "ds_read_b64_tr_b16 v[178:179], %[t4] offset:29312\n\t" \
    "ds_read_b64_tr_b16 v[184:185], %[t4] offset:27776\n\t" \
    "ds_read_b64_tr_b16 v[186:187], %[t4] offset:29440\n\t" \
    "ds_read_b64_tr_b16 v[192:193], %[t4] offset:27904\n\t" \
    "ds_read_b64_tr_b16 v[194:195], %[t4] offset:29568\n\t" \
    "ds_read_b64_tr_b16 v[180:181], %[t4] offset:41472\n\t" \
    "ds_read_b64_tr_b16 v[182:183], %[t4] offset:43136\n\t" \
    "ds_read_b64_tr_b16 v[188:189], %[t4] offset:41600\n\t" \
    "ds_read_b64_tr_b16 v[190:191], %[t4] offset:43264\n\t" \
    "ds_read_b64_tr_b16 v[196:197], %[t4] offset:41728\n\t" \
    "ds_read_b64_tr_b16 v[198:199], %[t4] offset:43392\n\t" \
    "s_waitcnt lgkmcnt(0)\n\t" \
    "s_barrier \n\t" \
    "ds_write_b128 %[wl], v[200:203] offset:0\n\t" \
    "ds_write_b128 %[wl], v[204:207] offset:1024\n\t" \
    "ds_write_b128 %[wl], v[208:211] offset:2048\n\t" \
    "ds_write_b128 %[wl], v[212:215] offset:3072\n\t" \
    "ds_write_b128 %[wl], v[216:219] offset:4096\n\t" \
    "ds_write_b128 %[wl], v[220:223] offset:5120\n\t" \
    "ds_write_b128 %[wl], v[224:227] offset:6144\n\t" \
    "ds_write_b128 %[wl], v[228:231] offset:7168\n\t" \
    "ds_write_b128 %[wl], v[232:235] offset:8192\n\t" \
    "ds_write_b128 %[wl], v[236:239] offset:9216\n\t" \
    "s_waitcnt lgkmcnt(0)\n\t"
#define ASM_WLOADO \
    "s_mov_b32 %[t], 0x0\n\t" \
    "buffer_load_dwordx4 v[200:203], %[o0], %[rs], %[t] offen" WNT "\n\t" \
    "buffer_load_dwordx4 v[16:19], %[o1], %[rs], %[t] offen" WNT "\n\t" \
    "buffer_load_dwordx4 v[208:211], %[o2], %[rs], %[t] offen" WNT "\n\t" \
    "buffer_load_dwordx4 v[56:59], %[o3], %[rs], %[t] offen" WNT "\n\t" \
    "buffer_load_dwordx4 v[216:219], %[o4], %[rs], %[t] offen" WNT "\n\t" \
    "s_mov_b32 %[t], 0xa000\n\t" \
    "buffer_load_dwordx4 v[96:99], %[o0], %[rs], %[t] offen" WNT "\n\t" \
    "buffer_load_dwordx4 v[224:227], %[o1], %[rs], %[t] offen" WNT "\n\t" \
    "buffer_load_dwordx4 v[136:139], %[o2], %[rs], %[t] offen" WNT "\n\t" \
    "buffer_load_dwordx4 v[232:235], %[o3], %[rs], %[t] offen" WNT "\n\t" \
    "buffer_load_dwordx4 v[176:179], %[o4], %[rs], %[t] offen" WNT "\n\t" \
    "s_mov_b32 %[t], 0x14000\n\t" \
    "buffer_load_dwordx4 v[204:207], %[o0], %[rs], %[t] offen" WNT "\n\t" \
    "buffer_load_dwordx4 v[20:23], %[o1], %[rs], %[t] offen" WNT "\n\t" \
    "buffer_load_dwordx4 v[212:215], %[o2], %[rs], %[t] offen" WNT "\n\t" \
    "buffer_load_dwordx4 v[60:63], %[o3], %[rs], %[t] offen" WNT "\n\t" \
    "buffer_load_dwordx4 v[220:223], %[o4], %[rs], %[t] offen" WNT "\n\t" \
    "s_mov_b32 %[t], 0x1e000\n\t" \
    "buffer_load_dwordx4 v[100:103], %[o0], %[rs], %[t] offen" WNT "\n\t" \
    "buffer_load_dwordx4 v[228:231], %[o1], %[rs], %[t] offen" WNT "\n\t" \
    "buffer_load_dwordx4 v[140:143], %[o2], %[rs], %[t] offen" WNT "\n\t" \
    "buffer_load_dwordx4 v[236:239], %[o3], %[rs], %[t] offen" WNT "\n\t" \
    "buffer_load_dwordx4 v[180:183], %[o4], %[rs], %[t] offen" WNT "\n\t" \
    "s_waitcnt vmcnt(15)\n\t" \
    "ds_write_b128 %[s0], v[200:203] offset:0\n\t" \
    "ds_write_b128 %[s1], v[16:19] offset:0\n\t" \
    "ds_write_b128 %[s2], v[208:211] offset:0\n\t" \
    "ds_write_b128 %[s3], v[56:59] offset:0\n\t" \
    "ds_write_b128 %[s4], v[216:219] offset:0\n\t" \
    "s_waitcnt vmcnt(10)\n\t" \
    "ds_write_b128 %[s0], v[96:99] offset:5632\n\t" \
    "ds_write_b128 %[s1], v[224:227] offset:5632\n\t" \
    "ds_write_b128 %[s2], v[136:139] offset:5632\n\t" \
    "ds_write_b128 %[s3], v[232:235] offset:5632\n\t" \
    "ds_write_b128 %[s4], v[176:179] offset:5632\n\t" \
    "ds_read_b64_tr_b16 v[200:201], %[tr] offset:0\n\t" \
    "ds_read_b64_tr_b16 v[202:203], %[tr] offset:5632\n\t" \
    "ds_read_b64_tr_b16 v[16:17], %[tr] offset:32\n\t" \
    "ds_read_b64_tr_b16 v[18:19], %[tr] offset:5664\n\t" \
    "ds_read_b64_tr_b16 v[208:209], %[tr] offset:64\n\t" \
    "ds_read_b64_tr_b16 v[210:211], %[tr] offset:5696\n\t" \
    "ds_read_b64_tr_b16 v[56:57], %[tr] offset:96\n\t" \
    "ds_read_b64_tr_b16 v[58:59], %[tr] offset:5728\n\t" \
    "ds_read_b64_tr_b16 v[216:217], %[tr] offset:128\n\t" \
    "ds_read_b64_tr_b16 v[218:219], %[tr] offset:5760\n\t" \
    "ds_read_b64_tr_b16 v[96:97], %[tr] offset:160\n\t" \
    "ds_read_b64_tr_b16 v[98:99], %[tr] offset:5792\n\t" \
    "ds_read_b64_tr_b16 v[224:225], %[tr] offset:192\n\t" \
    "ds_read_b64_tr_b16 v[226:227], %[tr] offset:5824\n\t" \
    "ds_read_b64_tr_b16 v[136:137], %[tr] offset:224\n\t" \
    "ds_read_b64_tr_b16 v[138:139], %[tr] offset:5856\n\t" \
    "ds_read_b64_tr_b16 v[232:233], %[tr] offset:256\n\t" \
    "ds_read_b64_tr_b16 v[234:235], %[tr] offset:5888\n\t" \
    "ds_read_b64_tr_b16 v[176:177], %[tr] offset:288\n\t" \
    "ds_read_b64_tr_b16 v[178:179], %[tr] offset:5920\n\t" \
    "s_barrier \n\t" \
    "s_mov_b32 %[t], 0x28000\n\t" \
    "buffer_load_dwordx4 v[0:3], %[o0], %[rs], %[t] offen" WNT "\n\t" \
    "buffer_load_dwordx4 v[24:27], %[o1], %[rs], %[t] offen" WNT "\n\t" \
    "buffer_load_dwordx4 v[40:43], %[o2], %[rs], %[t] offen" WNT "\n\t" \
    "buffer_load_dwordx4 v[64:67], %[o3], %[rs], %[t] offen" WNT "\n\t" \
    "buffer_load_dwordx4 v[80:83], %[o4], %[rs], %[t] offen" WNT "\n\t" \
    "s_mov_b32 %[t], 0x32000\n\t" \
    "buffer_load_dwordx4 v[104:107], %[o0], %[rs], %[t] offen" WNT "\n\t" \
    "buffer_load_dwordx4 v[120:123], %[o1], %[rs], %[t] offen" WNT "\n\t" \
    "buffer_load_dwordx4 v[144:147], %[o2], %[rs], %[t] offen" WNT "\n\t" \
    "buffer_load_dwordx4 v[160:163], %[o3], %[rs], %[t] offen" WNT "\n\t" \
    "buffer_load_dwordx4 v[184:187], %[o4], %[rs], %[t] offen" WNT "\n\t" \
    "s_waitcnt lgkmcnt(0)\n\t" \
    "s_barrier \n\t" \
    "s_waitcnt vmcnt(15)\n\t" \
    "ds_write_b128 %[s0], v[204:207] offset:0\n\t" \
    "ds_write_b128 %[s1], v[20:23] offset:0\n\t" \
    "ds_write_b128 %[s2], v[212:215] offset:0\n\t" \
    "ds_write_b128 %[s3], v[60:63] offset:0\n\t" \
    "ds_write_b128 %[s4], v[220:223] offset:0\n\t" \
    "s_waitcnt vmcnt(10)\n\t" \
    "ds_write_b128 %[s0], v[100:103] offset:5632\n\t" \
    "ds_write_b128 %[s1], v[228:231] offset:5632\n\t" \
    "ds_write_b128 %[s2], v[140:143] offset:5632\n\t" \
    "ds_write_b128 %[s3], v[236:239] offset:5632\n\t" \
    "ds_write_b128 %[s4], v[180:183] offset:5632\n\t" \
    "ds_read_b64_tr_b16 v[204:205], %[tr] offset:0\n\t" \
    "ds_read_b64_tr_b16 v[206:207], %[tr] offset:5632\n\t" \
    "ds_read_b64_tr_b16 v[20:21], %[tr] offset:32\n\t" \
    "ds_read_b64_tr_b16 v[22:23], %[tr] offset:5664\n\t" \
    "ds_read_b64_tr_b16 v[212:213], %[tr] offset:64\n\t" \
    "ds_read_b64_tr_b16 v[214:215], %[tr] offset:5696\n\t" \
    "ds_read_b64_tr_b16 v[60:61], %[tr] offset:96\n\t" \
    "ds_read_b64_tr_b16 v[62:63], %[tr] offset:5728\n\t" \
    "ds_read_b64_tr_b16 v[220:221], %[tr] offset:128\n\t" \
    "ds_read_b64_tr_b16 v[222:223], %[tr] offset:5760\n\t" \
    "ds_read_b64_tr_b16 v[100:101], %[tr] offset:160\n\t" \
    "ds_read_b64_tr_b16 v[102:103], %[tr] offset:5792\n\t" \
    "ds_read_b64_tr_b16 v[228:229], %[tr] offset:192\n\t" \
    "ds_read_b64_tr_b16 v[230:231], %[tr] offset:5824\n\t" \
    "ds_read_b64_tr_b16 v[140:141], %[tr] offset:224\n\t" \
    "ds_read_b64_tr_b16 v[142:143], %[tr] offset:5856\n\t" \
    "ds_read_b64_tr_b16 v[236:237], %[tr] offset:256\n\t" \
    "ds_read_b64_tr_b16 v[238:239], %[tr] offset:5888\n\t" \
    "ds_read_b64_tr_b16 v[180:181], %[tr] offset:288\n\t" \
    "ds_read_b64_tr_b16 v[182:183], %[tr] offset:5920\n\t" \
    "s_barrier \n\t" \
    "s_mov_b32 %[t], 0x5000\n\t" \
    "buffer_load_dwordx4 v[4:7], %[o0], %[rs], %[t] offen" WNT "\n\t" \
    "buffer_load_dwordx4 v[28:31], %[o1], %[rs], %[t] offen" WNT "\n\t" \
    "buffer_load_dwordx4 v[44:47], %[o2], %[rs], %[t] offen" WNT "\n\t" \
    "buffer_load_dwordx4 v[68:71], %[o3], %[rs], %[t] offen" WNT "\n\t" \
    "buffer_load_dwordx4 v[84:87], %[o4], %[rs], %[t] offen" WNT "\n\t" \
    "s_mov_b32 %[t], 0xf000\n\t" \
    "buffer_load_dwordx4 v[108:111], %[o0], %[rs], %[t] offen" WNT "\n\t" \
    "buffer_load_dwordx4 v[124:127], %[o1], %[rs], %[t] offen" WNT "\n\t" \
    "buffer_load_dwordx4 v[148:151], %[o2], %[rs], %[t] offen" WNT "\n\t" \
    "buffer_load_dwordx4 v[164:167], %[o3], %[rs], %[t] offen" WNT "\n\t" \
    "buffer_load_dwordx4 v[188:191], %[o4], %[rs], %[t] offen" WNT "\n\t" \
    "s_waitcnt lgkmcnt(0)\n\t" \
    "s_barrier \n\t" \
    "s_waitcnt vmcnt(15)\n\t" \
    "ds_write_b128 %[s0], v[0:3] offset:0\n\t" \
    "ds_write_b128 %[s1], v[24:27] offset:0\n\t" \
    "ds_write_b128 %[s2], v[40:43] offset:0\n\t" \
    "ds_write_b128 %[s3], v[64:67] offset:0\n\t" \
    "ds_write_b128 %[s4], v[80:83] offset:0\n\t" \
    "s_waitcnt vmcnt(10)\n\t" \
    "ds_write_b128 %[s0], v[104:107] offset:5632\n\t" \
    "ds_write_b128 %[s1], v[120:123] offset:5632\n\t" \
    "ds_write_b128 %[s2], v[144:147] offset:5632\n\t" \
    "ds_write_b128 %[s3], v[160:163] offset:5632\n\t" \
    "ds_write_b128 %[s4], v[184:187] offset:5632\n\t" \
    "ds_read_b64_tr_b16 v[0:1], %[tr] offset:0\n\t" \
    "ds_read_b64_tr_b16 v[2:3], %[tr] offset:5632\n\t" \
    "ds_read_b64_tr_b16 v[24:25], %[tr] offset:32\n\t" \
    "ds_read_b64_tr_b16 v[26:27], %[tr] offset:5664\n\t" \
    "ds_read_b64_tr_b16 v[40:41], %[tr] offset:64\n\t" \
    "ds_read_b64_tr_b16 v[42:43], %[tr] offset:5696\n\t" \
    "ds_read_b64_tr_b16 v[64:65], %[tr] offset:96\n\t" \
    "ds_read_b64_tr_b16 v[66:67], %[tr] offset:5728\n\t" \
    "ds_read_b64_tr_b16 v[80:81], %[tr] offset:128\n\t" \
    "ds_read_b64_tr_b16 v[82:83], %[tr] offset:5760\n\t" \
    "ds_read_b64_tr_b16 v[104:105], %[tr] offset:160\n\t" \
    "ds_read_b64_tr_b16 v[106:107], %[tr] offset:5792\n\t" \
    "ds_read_b64_tr_b16 v[120:121], %[tr] offset:192\n\t" \
    "ds_read_b64_tr_b16 v[122:123], %[tr] offset:5824\n\t" \
    "ds_read_b64_tr_b16 v[144:145], %[tr] offset:224\n\t" \
    "ds_read_b64_tr_b16 v[146:147], %[tr] offset:5856\n\t" \
    "ds_read_b64_tr_b16 v[160:161], %[tr] offset:256\n\t" \
    "ds_read_b64_tr_b16 v[162:163], %[tr] offset:5888\n\t" \
    "ds_read_b64_tr_b16 v[184:185], %[tr] offset:288\n\t" \
    "ds_read_b64_tr_b16 v[186:187], %[tr] offset:5920\n\t" \
    "s_barrier \n\t" \
    "s_mov_b32 %[t], 0x19000\n\t" \
    "buffer_load_dwordx4 v[8:11], %[o0], %[rs], %[t] offen" WNT "\n\t" \
    "buffer_load_dwordx4 v[32:35], %[o1], %[rs], %[t] offen" WNT "\n\t" \
    "buffer_load_dwordx4 v[48:51], %[o2], %[rs], %[t] offen" WNT "\n\t" \
    "buffer_load_dwordx4 v[72:75], %[o3], %[rs], %[t] offen" WNT "\n\t" \
    "buffer_load_dwordx4 v[88:91], %[o4], %[rs], %[t] offen" WNT "\n\t" \
    "s_mov_b32 %[t], 0x23000\n\t" \
    "buffer_load_dwordx4 v[112:115], %[o0], %[rs], %[t] offen" WNT "\n\t" \
    "buffer_load_dwordx4 v[128:131], %[o1], %[rs], %[t] offen" WNT "\n\t" \
    "buffer_load_dwordx4 v[152:155], %[o2], %[rs], %[t] offen" WNT "\n\t" \
    "buffer_load_dwordx4 v[168:171], %[o3], %[rs], %[t] offen" WNT "\n\t" \
    "buffer_load_dwordx4 v[192:195], %[o4], %[rs], %[t] offen" WNT "\n\t" \
    "s_waitcnt lgkmcnt(0)\n\t" \
    "s_barrier \n\t" \
    "s_waitcnt vmcnt(15)\n\t" \
    "ds_write_b128 %[s0], v[4:7] offset:0\n\t" \
    "ds_write_b128 %[s1], v[28:31] offset:0\n\t" \
    "ds_write_b128 %[s2], v[44:47] offset:0\n\t" \
    "ds_write_b128 %[s3], v[68:71] offset:0\n\t" \
    "ds_write_b128 %[s4], v[84:87] offset:0\n\t" \
    "s_waitcnt vmcnt(10)\n\t" \
    "ds_write_b128 %[s0], v[108:111] offset:5632\n\t" \
    "ds_write_b128 %[s1], v[124:127] offset:5632\n\t" \
    "ds_write_b128 %[s2], v[148:151] offset:5632\n\t" \
    "ds_write_b128 %[s3], v[164:167] offset:5632\n\t" \
    "ds_write_b128 %[s4], v[188:191] offset:5632\n\t" \
    "ds_read_b64_tr_b16 v[4:5], %[tr] offset:0\n\t" \
    "ds_read_b64_tr_b16 v[6:7], %[tr] offset:5632\n\t" \
    "ds_read_b64_tr_b16 v[28:29], %[tr] offset:32\n\t" \
    "ds_read_b64_tr_b16 v[30:31], %[tr] offset:5664\n\t" \
    "ds_read_b64_tr_b16 v[44:45], %[tr] offset:64\n\t" \
    "ds_read_b64_tr_b16 v[46:47], %[tr] offset:5696\n\t" \
    "ds_read_b64_tr_b16 v[68:69], %[tr] offset:96\n\t" \
    "ds_read_b64_tr_b16 v[70:71], %[tr] offset:5728\n\t" \
    "ds_read_b64_tr_b16 v[84:85], %[tr] offset:128\n\t" \
    "ds_read_b64_tr_b16 v[86:87], %[tr] offset:5760\n\t" \
    "ds_read_b64_tr_b16 v[108:109], %[tr] offset:160\n\t" \
    "ds_read_b64_tr_b16 v[110:111], %[tr] offset:5792\n\t" \
    "ds_read_b64_tr_b16 v[124:125], %[tr] offset:192\n\t" \
    "ds_read_b64_tr_b16 v[126:127], %[tr] offset:5824\n\t" \
    "ds_read_b64_tr_b16 v[148:149], %[tr] offset:224\n\t" \
    "ds_read_b64_tr_b16 v[150:151], %[tr] offset:5856\n\t" \
    "ds_read_b64_tr_b16 v[164:165], %[tr] offset:256\n\t" \
    "ds_read_b64_tr_b16 v[166:167], %[tr] offset:5888\n\t" \
    "ds_read_b64_tr_b16 v[188:189], %[tr] offset:288\n\t" \
    "ds_read_b64_tr_b16 v[190:191], %[tr] offset:5920\n\t" \
    "s_barrier \n\t" \
    "s_mov_b32 %[t], 0x2d000\n\t" \
    "buffer_load_dwordx4 v[12:15], %[o0], %[rs], %[t] offen" WNT "\n\t" \
    "buffer_load_dwordx4 v[36:39], %[o1], %[rs], %[t] offen" WNT "\n\t" \
    "buffer_load_dwordx4 v[52:55], %[o2], %[rs], %[t] offen" WNT "\n\t" \
    "buffer_load_dwordx4 v[76:79], %[o3], %[rs], %[t] offen" WNT "\n\t" \
    "buffer_load_dwordx4 v[92:95], %[o4], %[rs], %[t] offen" WNT "\n\t" \
    "s_mov_b32 %[t], 0x37000\n\t" \
    "buffer_load_dwordx4 v[116:119], %[o0], %[rs], %[t] offen" WNT "\n\t" \
    "buffer_load_dwordx4 v[132:135], %[o1], %[rs], %[t] offen" WNT "\n\t" \
    "buffer_load_dwordx4 v[156:159], %[o2], %[rs], %[t] offen" WNT "\n\t" \
    "buffer_load_dwordx4 v[172:175], %[o3], %[rs], %[t] offen" WNT "\n\t" \
    "buffer_load_dwordx4 v[196:199], %[o4], %[rs], %[t] offen" WNT "\n\t" \
    "s_waitcnt lgkmcnt(0)\n\t" \
    "s_barrier \n\t" \
    "s_waitcnt vmcnt(15)\n\t" \
    "ds_write_b128 %[s0], v[8:11] offset:0\n\t" \
    "ds_write_b128 %[s1], v[32:35] offset:0\n\t" \
    "ds_write_b128 %[s2], v[48:51] offset:0\n\t" \
    "ds_write_b128 %[s3], v[72:75] offset:0\n\t" \
    "ds_write_b128 %[s4], v[88:91] offset:0\n\t" \
    "s_waitcnt vmcnt(10)\n\t" \
    "ds_write_b128 %[s0], v[112:115] offset:5632\n\t" \
    "ds_write_b128 %[s1], v[128:131] offset:5632\n\t" \
    "ds_write_b128 %[s2], v[152:155] offset:5632\n\t" \
    "ds_write_b128 %[s3], v[168:171] offset:5632\n\t" \
    "ds_write_b128 %[s4], v[192:195] offset:5632\n\t" \
    "ds_read_b64_tr_b16 v[8:9], %[tr] offset:0\n\t" \
    "ds_read_b64_tr_b16 v[10:11], %[tr] offset:5632\n\t" \
    "ds_read_b64_tr_b16 v[32:33], %[tr] offset:32\n\t" \
    "ds_read_b64_tr_b16 v[34:35], %[tr] offset:5664\n\t" \
    "ds_read_b64_tr_b16 v[48:49], %[tr] offset:64\n\t" \
    "ds_read_b64_tr_b16 v[50:51], %[tr] offset:5696\n\t" \
    "ds_read_b64_tr_b16 v[72:73], %[tr] offset:96\n\t" \
    "ds_read_b64_tr_b16 v[74:75], %[tr] offset:5728\n\t" \
    "ds_read_b64_tr_b16 v[88:89], %[tr] offset:128\n\t" \
    "ds_read_b64_tr_b16 v[90:91], %[tr] offset:5760\n\t" \
    "ds_read_b64_tr_b16 v[112:113], %[tr] offset:160\n\t" \
    "ds_read_b64_tr_b16 v[114:115], %[tr] offset:5792\n\t" \
    "ds_read_b64_tr_b16 v[128:129], %[tr] offset:192\n\t" \
    "ds_read_b64_tr_b16 v[130:131], %[tr] offset:5824\n\t" \
    "ds_read_b64_tr_b16 v[152:153], %[tr] offset:224\n\t" \
    "ds_read_b64_tr_b16 v[154:155], %[tr] offset:5856\n\t" \
    "ds_read_b64_tr_b16 v[168:169], %[tr] offset:256\n\t" \
    "ds_read_b64_tr_b16 v[170:171], %[tr] offset:5888\n\t" \
    "ds_read_b64_tr_b16 v[192:193], %[tr] offset:288\n\t" \
    "ds_read_b64_tr_b16 v[194:195], %[tr] offset:5920\n\t" \
    "s_waitcnt lgkmcnt(0)\n\t" \
    "s_barrier \n\t" \
    "s_waitcnt vmcnt(5)\n\t" \
    "ds_write_b128 %[s0], v[12:15] offset:0\n\t" \
    "ds_write_b128 %[s1], v[36:39] offset:0\n\t" \
    "ds_write_b128 %[s2], v[52:55] offset:0\n\t" \
    "ds_write_b128 %[s3], v[76:79] offset:0\n\t" \
    "ds_write_b128 %[s4], v[92:95] offset:0\n\t" \
    "s_waitcnt vmcnt(0)\n\t" \
    "ds_write_b128 %[s0], v[116:119] offset:5632\n\t" \
    "ds_write_b128 %[s1], v[132:135] offset:5632\n\t" \
    "ds_write_b128 %[s2], v[156:159] offset:5632\n\t" \
    "ds_write_b128 %[s3], v[172:175] offset:5632\n\t" \
    "ds_write_b128 %[s4], v[196:199] offset:5632\n\t" \
    "ds_read_b64_tr_b16 v[12:13], %[tr] offset:0\n\t" \
    "ds_read_b64_tr_b16 v[14:15], %[tr] offset:5632\n\t" \
    "ds_read_b64_tr_b16 v[36:37], %[tr] offset:32\n\t" \
    "ds_read_b64_tr_b16 v[38:39], %[tr] offset:5664\n\t" \
    "ds_read_b64_tr_b16 v[52:53], %[tr] offset:64\n\t" \
    "ds_read_b64_tr_b16 v[54:55], %[tr] offset:5696\n\t" \
    "ds_read_b64_tr_b16 v[76:77], %[tr] offset:96\n\t" \
    "ds_read_b64_tr_b16 v[78:79], %[tr] offset:5728\n\t" \
    "ds_read_b64_tr_b16 v[92:93], %[tr] offset:128\n\t" \
    "ds_read_b64_tr_b16 v[94:95], %[tr] offset:5760\n\t" \
    "ds_read_b64_tr_b16 v[116:117], %[tr] offset:160\n\t" \
    "ds_read_b64_tr_b16 v[118:119], %[tr] offset:5792\n\t" \
    "ds_read_b64_tr_b16 v[132:133], %[tr] offset:192\n\t" \
    "ds_read_b64_tr_b16 v[134:135], %[tr] offset:5824\n\t" \
    "ds_read_b64_tr_b16 v[156:157], %[tr] offset:224\n\t" \
    "ds_read_b64_tr_b16 v[158:159], %[tr] offset:5856\n\t" \
    "ds_read_b64_tr_b16 v[172:173], %[tr] offset:256\n\t" \
    "ds_read_b64_tr_b16 v[174:175], %[tr] offset:5888\n\t" \
    "ds_read_b64_tr_b16 v[196:197], %[tr] offset:288\n\t" \
    "ds_read_b64_tr_b16 v[198:199], %[tr] offset:5920\n\t" \
    "s_waitcnt lgkmcnt(0)\n\t" \
    "s_barrier \n\t" \
    "ds_write_b128 %[wl], v[200:203] offset:0\n\t" \
    "ds_write_b128 %[wl], v[204:207] offset:1024\n\t" \
    "ds_write_b128 %[wl], v[208:211] offset:2048\n\t" \
    "ds_write_b128 %[wl], v[212:215] offset:3072\n\t" \
    "ds_write_b128 %[wl], v[216:219] offset:4096\n\t" \
    "ds_write_b128 %[wl], v[220:223] offset:5120\n\t" \
    "ds_write_b128 %[wl], v[224:227] offset:6144\n\t" \
    "ds_write_b128 %[wl], v[228:231] offset:7168\n\t" \
    "ds_write_b128 %[wl], v[232:235] offset:8192\n\t" \
    "ds_write_b128 %[wl], v[236:239] offset:9216\n\t" \
    "s_waitcnt lgkmcnt(0)\n\t"
#define ASM_XG_S_LO \
    "s_waitcnt lgkmcnt(0)\n\t" \
    "ds_read_b128 v[224:227], %[xa] offset:16384\n\t" \
    "ds_read_b128 v[228:231], %[xa] offset:17408\n\t" \
    "ds_read_b128 v[244:247], %[wl] offset:0\n\t" \
    "ds_read_b128 v[248:251], %[wl] offset:1024\n\t" \
    "ds_read_b128 v[232:235], %[xa] offset:0\n\t" \
    "ds_read_b128 v[236:239], %[xa] offset:1024\n\t" \
    "v_xor_b32 v252, 0x800, %[xa]\n\t" \
    "ds_read_b128 v[240:243], v252 offset:0\n\t" \
    "s_waitcnt lgkmcnt(6)\n\t" \
    "v_mfma_f32_16x16x32_bf16 v[220:223], v[8:11], v[224:227], 0\n\t" \
    "s_waitcnt lgkmcnt(5)\n\t" \
    "v_mfma_f32_16x16x32_bf16 v[220:223], v[12:15], v[228:231], v[220:223]\n\t" \
    "v_mfma_f32_16x16x32_bf16 v[216:219], v[0:3], v[224:227], 0\n\t" \
    "v_mfma_f32_16x16x32_bf16 v[216:219], v[4:7], v[228:231], v[216:219]\n\t" \
    "s_waitcnt lgkmcnt(4)\n\t" \
    "v_mfma_f32_16x16x32_bf16 v[212:215], v[244:247], v[224:227], 0\n\t" \
    "ds_read_b128 v[224:227], v252 offset:1024\n\t" \
    "s_waitcnt lgkmcnt(4)\n\t" \
    "v_mfma_f32_16x16x32_bf16 v[212:215], v[248:251], v[228:231], v[212:215]\n\t" \
    "ds_read_b128 v[228:231], v252 offset:16384\n\t" \
    "s_waitcnt lgkmcnt(4)\n\t" \
    "v_mfma_f32_16x16x32_bf16 v[200:203], v[244:247], v[232:235], 0\n\t" \
    "ds_read_b128 v[244:247], %[wl] offset:2048\n\t" \
    "s_waitcnt lgkmcnt(4)\n\t" \
    "v_mfma_f32_16x16x32_bf16 v[200:203], v[248:251], v[236:239], v[200:203]\n\t" \
    "ds_read_b128 v[248:251], %[wl] offset:3072\n\t" \
    "v_mfma_f32_16x16x32_bf16 v[204:207], v[0:3], v[232:235], 0\n\t" \
    "v_mfma_f32_16x16x32_bf16 v[204:207], v[4:7], v[236:239], v[204:207]\n\t" \
    "v_mfma_f32_16x16x32_bf16 v[208:211], v[8:11], v[232:235], 0\n\t" \
    "ds_read_b128 v[232:235], v252 offset:17408\n\t" \
    "v_mfma_f32_16x16x32_bf16 v[208:211], v[12:15], v[236:239], v[208:211]\n\t" \
    "v_xor_b32 v252, 0x1000, %[xa]\n\t" \
    "ds_read_b128 v[236:239], v252 offset:16384\n\t" \
    "s_waitcnt lgkmcnt(6)\n\t" \
    "v_mfma_f32_16x16x32_bf16 v[208:211], v[32:35], v[240:243], v[208:211]\n\t" \
    "s_waitcnt lgkmcnt(5)\n\t" \
    "v_mfma_f32_16x16x32_bf16 v[208:211], v[36:39], v[224:227], v[208:211]\n\t" \
    "v_mfma_f32_16x16x32_bf16 v[204:207], v[24:27], v[240:243], v[204:207]\n\t" \
    "v_mfma_f32_16x16x32_bf16 v[204:207], v[28:31], v[224:227], v[204:207]\n\t" \
    "v_mfma_f32_16x16x32_bf16 v[200:203], v[16:19], v[240:243], v[200:203]\n\t" \
    "ds_read_b128 v[240:243], v252 offset:17408\n\t" \
    "v_mfma_f32_16x16x32_bf16 v[200:203], v[20:23], v[224:227], v[200:203]\n\t" \
    "ds_read_b128 v[224:227], v252 offset:0\n\t" \
    "s_waitcnt lgkmcnt(6)\n\t" \
    "v_mfma_f32_16x16x32_bf16 v[212:215], v[16:19], v[228:231], v[212:215]\n\t" \
    "s_waitcnt lgkmcnt(3)\n\t" \
    "v_mfma_f32_16x16x32_bf16 v[212:215], v[20:23], v[232:235], v[212:215]\n\t" \
    "v_mfma_f32_16x16x32_bf16 v[216:219], v[24:27], v[228:231], v[216:219]\n\t" \
    "v_mfma_f32_16x16x32_bf16 v[216:219], v[28:31], v[232:235], v[216:219]\n\t" \
    "v_mfma_f32_16x16x32_bf16 v[220:223], v[32:35], v[228:231], v[220:223]\n\t" \
    "ds_read_b128 v[228:231], v252 offset:1024\n\t" \
    "v_mfma_f32_16x16x32_bf16 v[220:223], v[36:39], v[232:235], v[220:223]\n\t" \
    "v_xor_b32 v252, 0x1800, %[xa]\n\t" \
    "ds_read_b128 v[232:235], v252 offset:0\n\t" \
    "s_waitcnt lgkmcnt(4)\n\t" \
    "v_mfma_f32_16x16x32_bf16 v[220:223], v[48:51], v[236:239], v[220:223]\n\t" \
    "s_waitcnt lgkmcnt(3)\n\t" \
    "v_mfma_f32_16x16x32_bf16 v[220:223], v[52:55], v[240:243], v[220:223]\n\t" \
    "v_mfma_f32_16x16x32_bf16 v[216:219], v[40:43], v[236:239], v[216:219]\n\t" \
    "v_mfma_f32_16x16x32_bf16 v[216:219], v[44:47], v[240:243], v[216:219]\n\t" \
    "v_mfma_f32_16x16x32_bf16 v[212:215], v[244:247], v[236:239], v[212:215]\n\t" \
    "ds_read_b128 v[236:239], v252 offset:1024\n\t" \
    "v_mfma_f32_16x16x32_bf16 v[212:215], v[248:251], v[240:243], v[212:215]\n\t" \
    "ds_read_b128 v[240:243], v252 offset:16384\n\t" \
    "s_waitcnt lgkmcnt(4)\n\t" \
    "v_mfma_f32_16x16x32_bf16 v[200:203], v[244:247], v[224:227], v[200:203]\n\t" \
    "ds_read_b128 v[244:247], %[wl] offset:8192\n\t" \
    "s_waitcnt lgkmcnt(4)\n\t" \
    "v_mfma_f32_16x16x32_bf16 v[200:203], v[248:251], v[228:231], v[200:203]\n\t" \
    "ds_read_b128 v[248:251], %[wl] offset:9216\n\t" \
    "v_mfma_f32_16x16x32_bf16 v[204:207], v[40:43], v[224:227], v[204:207]\n\t" \
    "v_mfma_f32_16x16x32_bf16 v[204:207], v[44:47], v[228:231], v[204:207]\n\t" \
    "v_mfma_f32_16x16x32_bf16 v[208:211], v[48:51], v[224:227], v[208:211]\n\t" \
    "ds_read_b128 v[224:227], v252 offset:17408\n\t" \
    "v_mfma_f32_16x16x32_bf16 v[208:211], v[52:55], v[228:231], v[208:211]\n\t" \
    "v_and_b32 v252, 0x3ff, %[xa]\n\t" \
    "ds_read_b128 v[228:231], v252 offset:45056\n\t" \
    "s_waitcnt lgkmcnt(6)\n\t" \
    "v_mfma_f32_16x16x32_bf16 v[208:211], v[72:75], v[232:235], v[208:211]\n\t" \
    "s_waitcnt lgkmcnt(5)\n\t" \
    "v_mfma_f32_16x16x32_bf16 v[208:211], v[76:79], v[236:239], v[208:211]\n\t" \
    "v_mfma_f32_16x16x32_bf16 v[204:207], v[64:67], v[232:235], v[204:207]\n\t" \
    "v_mfma_f32_16x16x32_bf16 v[204:207], v[68:71], v[236:239], v[204:207]\n\t" \
    "v_mfma_f32_16x16x32_bf16 v[200:203], v[56:59], v[232:235], v[200:203]\n\t" \
    "ds_read_b128 v[232:235], v252 offset:46080\n\t" \
    "v_mfma_f32_16x16x32_bf16 v[200:203], v[60:63], v[236:239], v[200:203]\n\t" \
    "ds_read_b128 v[236:239], v252 offset:40960\n\t" \
    "s_waitcnt lgkmcnt(6)\n\t" \
    "v_mfma_f32_16x16x32_bf16 v[212:215], v[56:59], v[240:243], v[212:215]\n\t" \
    "s_waitcnt lgkmcnt(3)\n\t" \
    "v_mfma_f32_16x16x32_bf16 v[212:215], v[60:63], v[224:227], v[212:215]\n\t" \
    "v_mfma_f32_16x16x32_bf16 v[216:219], v[64:67], v[240:243], v[216:219]\n\t" \
    "v_mfma_f32_16x16x32_bf16 v[216:219], v[68:71], v[224:227], v[216:219]\n\t" \
    "v_mfma_f32_16x16x32_bf16 v[220:223], v[72:75], v[240:243], v[220:223]\n\t" \
    "ds_read_b128 v[240:243], v252 offset:41984\n\t" \
    "v_mfma_f32_16x16x32_bf16 v[220:223], v[76:79], v[224:227], v[220:223]\n\t" \
    "s_waitcnt lgkmcnt(0)\n\t" \
    "s_barrier\n\t" ASM_ST \
    "v_xor_b32 v252, 0x2000, %[xa]\n\t" \
    "ds_read_b128 v[224:227], v252 offset:0\n\t" \
    "v_mfma_f32_16x16x32_bf16 v[220:223], v[168:171], v[228:231], v[220:223]\n\t" \
    "v_mfma_f32_16x16x32_bf16 v[220:223], v[172:175], v[232:235], v[220:223]\n\t" \
    "v_mfma_f32_16x16x32_bf16 v[216:219], v[160:163], v[228:231], v[216:219]\n\t" \
    "v_mfma_f32_16x16x32_bf16 v[216:219], v[164:167], v[232:235], v[216:219]\n\t" \
    "v_mfma_f32_16x16x32_bf16 v[212:215], v[244:247], v[228:231], v[212:215]\n\t" \
    "ds_read_b128 v[228:231], v252 offset:1024\n\t" \
    "v_mfma_f32_16x16x32_bf16 v[212:215], v[248:251], v[232:235], v[212:215]\n\t" \
    "ds_read_b128 v[232:235], v252 offset:16384\n\t" \
    "v_mfma_f32_16x16x32_bf16 v[200:203], v[244:247], v[236:239], v[200:203]\n\t" \
    "ds_read_b128 v[244:247], %[wl] offset:4096\n\t" \
    "v_mfma_f32_16x16x32_bf16 v[200:203], v[248:251], v[240:243], v[200:203]\n\t" \
    "ds_read_b128 v[248:251], %[wl] offset:5120\n\t" \
    "v_mfma_f32_16x16x32_bf16 v[204:207], v[160:163], v[236:239], v[204:207]\n\t" \
    "v_mfma_f32_16x16x32_bf16 v[204:207], v[164:167], v[240:243], v[204:207]\n\t" \
    "v_mfma_f32_16x16x32_bf16 v[208:211], v[168:171], v[236:239], v[208:211]\n\t" \
    "ds_read_b128 v[236:239], v252 offset:17408\n\t" \
    "v_mfma_f32_16x16x32_bf16 v[208:211], v[172:175], v[240:243], v[208:211]\n\t" \
    "v_xor_b32 v252, 0x2800, %[xa]\n\t" \
    "ds_read_b128 v[240:243], v252 offset:16384\n\t" \
    "s_waitcnt lgkmcnt(6)\n\t" \
    "v_mfma_f32_16x16x32_bf16 v[208:211], v[88:91], v[224:227], v[208:211]\n\t" \
    "s_waitcnt lgkmcnt(5)\n\t" \
    "v_mfma_f32_16x16x32_bf16 v[208:211], v[92:95], v[228:231], v[208:211]\n\t" \
    "v_mfma_f32_16x16x32_bf16 v[204:207], v[80:83], v[224:227], v[204:207]\n\t" \
    "v_mfma_f32_16x16x32_bf16 v[204:207], v[84:87], v[228:231], v[204:207]\n\t" \
    "s_waitcnt lgkmcnt(3)\n\t" \
    "v_mfma_f32_16x16x32_bf16 v[200:203], v[244:247], v[224:227], v[200:203]\n\t" \
    "ds_read_b128 v[224:227], v252 offset:17408\n\t" \
    "s_waitcnt lgkmcnt(3)\n\t" \
    "v_mfma_f32_16x16x32_bf16 v[200:203], v[248:251], v[228:231], v[200:203]\n\t" \
    "ds_read_b128 v[228:231], v252 offset:0\n\t" \
    "v_mfma_f32_16x16x32_bf16 v[212:215], v[244:247], v[232:235], v[212:215]\n\t" \
    "ds_read_b128 v[244:247], %[wl] offset:6144\n\t" \
    "s_waitcnt lgkmcnt(4)\n\t" \
    "v_mfma_f32_16x16x32_bf16 v[212:215], v[248:251], v[236:239], v[212:215]\n\t" \
    "ds_read_b128 v[248:251], %[wl] offset:7168\n\t" \
    "v_mfma_f32_16x16x32_bf16 v[216:219], v[80:83], v[232:235], v[216:219]\n\t" \
    "v_mfma_f32_16x16x32_bf16 v[216:219], v[84:87], v[236:239], v[216:219]\n\t" \
    "v_mfma_f32_16x16x32_bf16 v[220:223], v[88:91], v[232:235], v[220:223]\n\t" \
    "ds_read_b128 v[232:235], v252 offset:1024\n\t" \
    "v_mfma_f32_16x16x32_bf16 v[220:223], v[92:95], v[236:239], v[220:223]\n\t" \
    "v_xor_b32 v252, 0x3000, %[xa]\n\t" \
    "ds_read_b128 v[236:239], v252 offset:0\n\t" \
    "s_waitcnt lgkmcnt(6)\n\t" \
    "v_mfma_f32_16x16x32_bf16 v[220:223], v[112:115], v[240:243], v[220:223]\n\t" \
    "s_waitcnt lgkmcnt(5)\n\t" \
    "v_mfma_f32_16x16x32_bf16 v[220:223], v[116:119], v[224:227], v[220:223]\n\t" \
    "v_mfma_f32_16x16x32_bf16 v[216:219], v[104:107], v[240:243], v[216:219]\n\t" \
    "v_mfma_f32_16x16x32_bf16 v[216:219], v[108:111], v[224:227], v[216:219]\n\t" \
    "v_mfma_f32_16x16x32_bf16 v[212:215], v[96:99], v[240:243], v[212:215]\n\t" \
    "ds_read_b128 v[240:243], v252 offset:1024\n\t" \
    "v_mfma_f32_16x16x32_bf16 v[212:215], v[100:103], v[224:227], v[212:215]\n\t" \
    "ds_read_b128 v[224:227], v252 offset:16384\n\t" \
    "s_waitcnt lgkmcnt(6)\n\t" \
    "v_mfma_f32_16x16x32_bf16 v[200:203], v[96:99], v[228:231], v[200:203]\n\t" \
    "s_waitcnt lgkmcnt(3)\n\t" \
    "v_mfma_f32_16x16x32_bf16 v[200:203], v[100:103], v[232:235], v[200:203]\n\t" \
    "v_mfma_f32_16x16x32_bf16 v[204:207], v[104:107], v[228:231], v[204:207]\n\t" \
    "v_mfma_f32_16x16x32_bf16 v[204:207], v[108:111], v[232:235], v[204:207]\n\t" \
    "v_mfma_f32_16x16x32_bf16 v[208:211], v[112:115], v[228:231], v[208:211]\n\t" \
    "ds_read_b128 v[228:231], v252 offset:17408\n\t" \
    "v_mfma_f32_16x16x32_bf16 v[208:211], v[116:119], v[232:235], v[208:211]\n\t" \
    "v_xor_b32 v252, 0x3800, %[xa]\n\t" \
    "ds_read_b128 v[232:235], v252 offset:16384\n\t" \
    "s_waitcnt lgkmcnt(4)\n\t" \
    "v_mfma_f32_16x16x32_bf16 v[208:211], v[128:131], v[236:239], v[208:211]\n\t" \
    "s_waitcnt lgkmcnt(3)\n\t" \
    "v_mfma_f32_16x16x32_bf16 v[208:211], v[132:135], v[240:243], v[208:211]\n\t" \
    "v_mfma_f32_16x16x32_bf16 v[204:207], v[120:123], v[236:239], v[204:207]\n\t" \
    "v_mfma_f32_16x16x32_bf16 v[204:207], v[124:127], v[240:243], v[204:207]\n\t" \
    "v_mfma_f32_16x16x32_bf16 v[200:203], v[244:247], v[236:239], v[200:203]\n\t" \
    "ds_read_b128 v[236:239], v252 offset:17408\n\t" \
    "v_mfma_f32_16x16x32_bf16 v[200:203], v[248:251], v[240:243], v[200:203]\n\t" \
    "ds_read_b128 v[240:243], v252 offset:0\n\t" \
    "s_waitcnt lgkmcnt(4)\n\t" \
    "v_mfma_f32_16x16x32_bf16 v[212:215], v[244:247], v[224:227], v[212:215]\n\t" \
    "s_waitcnt lgkmcnt(3)\n\t" \
    "v_mfma_f32_16x16x32_bf16 v[212:215], v[248:251], v[228:231], v[212:215]\n\t" \
    "v_mfma_f32_16x16x32_bf16 v[216:219], v[120:123], v[224:227], v[216:219]\n\t" \
    "v_mfma_f32_16x16x32_bf16 v[216:219], v[124:127], v[228:231], v[216:219]\n\t" \
    "v_mfma_f32_16x16x32_bf16 v[220:223], v[128:131], v[224:227], v[220:223]\n\t" \
    "ds_read_b128 v[224:227], v252 offset:1024\n\t" \
    "v_mfma_f32_16x16x32_bf16 v[220:223], v[132:135], v[228:231], v[220:223]\n\t" \
    "v_and_b32 v252, 0x3ff, %[xa]\n\t" \
    "ds_read_b128 v[228:231], v252 offset:43008\n\t" \
    "s_waitcnt lgkmcnt(4)\n\t" \
    "v_mfma_f32_16x16x32_bf16 v[220:223], v[152:155], v[232:235], v[220:223]\n\t" \
    "v_mov_b64 v[244:245], 0\n\t" \
    "s_waitcnt lgkmcnt(3)\n\t" \
    "v_mfma_f32_16x16x32_bf16 v[220:223], v[156:159], v[236:239], v[220:223]\n\t" \
    "v_mov_b64 v[246:247], 0\n\t" \
    "v_mfma_f32_16x16x32_bf16 v[216:219], v[144:147], v[232:235], v[216:219]\n\t" \
    "v_mov_b64 v[248:249], 0\n\t" \
    "v_mfma_f32_16x16x32_bf16 v[216:219], v[148:151], v[236:239], v[216:219]\n\t" \
    "v_mov_b64 v[250:251], 0\n\t" \
    "v_mfma_f32_16x16x32_bf16 v[212:215], v[136:139], v[232:235], v[212:215]\n\t" \
    "ds_read_b128 v[232:235], v252 offset:44032\n\t" \
    "v_mfma_f32_16x16x32_bf16 v[212:215], v[140:143], v[236:239], v[212:215]\n\t" \
    "ds_read_b128 v[236:239], v252 offset:47104\n\t" \
    "s_waitcnt lgkmcnt(4)\n\t" \
    "v_mfma_f32_16x16x32_bf16 v[200:203], v[136:139], v[240:243], v[200:203]\n\t" \
    "s_waitcnt lgkmcnt(3)\n\t" \
    "v_mfma_f32_16x16x32_bf16 v[200:203], v[140:143], v[224:227], v[200:203]\n\t" \
    "v_mfma_f32_16x16x32_bf16 v[204:207], v[144:147], v[240:243], v[204:207]\n\t" \
    "v_mfma_f32_16x16x32_bf16 v[204:207], v[148:151], v[224:227], v[204:207]\n\t" \
    "v_mfma_f32_16x16x32_bf16 v[208:211], v[152:155], v[240:243], v[208:211]\n\t" \
    "ds_read_b128 v[240:243], v252 offset:48128\n\t" \
    "v_mfma_f32_16x16x32_bf16 v[208:211], v[156:159], v[224:227], v[208:211]\n\t" \
    "s_waitcnt lgkmcnt(3)\n\t" \
    "v_mfma_f32_16x16x32_bf16 v[208:211], v[192:195], v[228:231], v[208:211]\n\t" \
    "v_add_u32 v252, %[qs], v252\n\t" \
    "v_mov_b64 v[224:225], 0\n\t" \
    "s_waitcnt lgkmcnt(2)\n\t" \
    "v_mfma_f32_16x16x32_bf16 v[208:211], v[196:199], v[232:235], v[208:211]\n\t" \
    "v_mov_b64 v[226:227], 0\n\t" \
    "v_mfma_f32_16x16x32_bf16 v[204:207], v[184:187], v[228:231], v[204:207]\n\t" \
    "v_mfma_f32_16x16x32_bf16 v[204:207], v[188:191], v[232:235], v[204:207]\n\t" \
    "v_mfma_f32_16x16x32_bf16 v[200:203], v[176:179], v[228:231], v[200:203]\n\t" \
    "v_mfma_f32_16x16x32_bf16 v[200:203], v[180:183], v[232:235], v[200:203]\n\t" \
    "s_waitcnt lgkmcnt(1)\n\t" \
    "v_mfma_f32_16x16x32_bf16 v[212:215], v[176:179], v[236:239], v[212:215]\n\t" \
    "ds_read_b128 v[228:231], %[wl] offset:2048\n\t" \
    "ds_read_b128 v[232:235], %[wl] offset:3072\n\t" \
    "s_waitcnt lgkmcnt(2)\n\t" \
    "v_mfma_f32_16x16x32_bf16 v[212:215], v[180:183], v[240:243], v[212:215]\n\t" \
    "v_cvt_pk_bf16_f32 v208, v208, v209\n\t" \
    "v_cvt_pk_bf16_f32 v209, v210, v211\n\t" \
    "v_mfma_f32_16x16x32_bf16 v[216:219], v[184:187], v[236:239], v[216:219]\n\t" \
    "ds_write_b64 v252, v[208:209] offset:2048\n\t" \
    "v_cvt_pk_bf16_f32 v204, v204, v205\n\t" \
    "v_mfma_f32_16x16x32_bf16 v[216:219], v[188:191], v[240:243], v[216:219]\n\t" \
    "v_cvt_pk_bf16_f32 v205, v206, v207\n\t" \
    "ds_write_b64 v252, v[204:205] offset:1024\n\t" \
    "v_mfma_f32_16x16x32_bf16 v[220:223], v[192:195], v[236:239], v[220:223]\n\t" \
    "v_cvt_pk_bf16_f32 v200, v200, v201\n\t" \
    "v_cvt_pk_bf16_f32 v201, v202, v203\n\t" \
    "v_mfma_f32_16x16x32_bf16 v[220:223], v[196:199], v[240:243], v[220:223]\n\t" \
    "ds_write_b64 v252, v[200:201] offset:0\n\t" \
    "ds_read_b128 v[200:203], %[wl] offset:0\n\t" \
    "ds_read_b128 v[204:207], %[wl] offset:1024\n\t" \
    "v_cvt_pk_bf16_f32 v212, v212, v213\n\t" \
    "v_cvt_pk_bf16_f32 v213, v214, v215\n\t" \
    "ds_write_b64 v252, v[212:213] offset:6144\n\t" \
    "v_cvt_pk_bf16_f32 v216, v216, v217\n\t" \
    "v_cvt_pk_bf16_f32 v217, v218, v219\n\t" \
    "ds_write_b64 v252, v[216:217] offset:7168\n\t" \
    "v_cvt_pk_bf16_f32 v220, v220, v221\n\t" \
    "v_cvt_pk_bf16_f32 v221, v222, v223\n\t" \
    "ds_write_b64 v252, v[220:221] offset:8192\n\t" \
    "s_waitcnt lgkmcnt(0)\n\t"
#define ASM_XG_S_HI \
    "s_waitcnt lgkmcnt(0)\n\t" \
    "v_xor_b32 v252, 0x2000, %[xa]\n\t" \
    "ds_read_b128 v[224:227], v252 offset:16384\n\t" \
    "ds_read_b128 v[228:231], v252 offset:17408\n\t" \
    "ds_read_b128 v[244:247], %[wl] offset:4096\n\t" \
    "ds_read_b128 v[248:251], %[wl] offset:5120\n\t" \
    "ds_read_b128 v[232:235], v252 offset:0\n\t" \
    "ds_read_b128 v[236:239], v252 offset:1024\n\t" \
    "v_xor_b32 v252, 0x2800, %[xa]\n\t" \
    "ds_read_b128 v[240:243], v252 offset:0\n\t" \
    "s_cmp_eq_u32 %[slp], 0\n\t" \
    "s_cbranch_scc1 .Lxs%=\n\t" \
    "s_sleep 10\n\t" \
    ".Lxs%=:\n\t" \
    "s_waitcnt lgkmcnt(6)\n\t" \
    "v_mfma_f32_16x16x32_bf16 v[220:223], v[88:91], v[224:227], 0\n\t" \
    "s_waitcnt lgkmcnt(5)\n\t" \
    "v_mfma_f32_16x16x32_bf16 v[220:223], v[92:95], v[228:231], v[220:223]\n\t" \
    "v_mfma_f32_16x16x32_bf16 v[216:219], v[80:83], v[224:227], 0\n\t" \
    "v_mfma_f32_16x16x32_bf16 v[216:219], v[84:87], v[228:231], v[216:219]\n\t" \
    "s_waitcnt lgkmcnt(4)\n\t" \
    "v_mfma_f32_16x16x32_bf16 v[212:215], v[244:247], v[224:227], 0\n\t" \
    "ds_read_b128 v[224:227], v252 offset:1024\n\t" \
    "s_waitcnt lgkmcnt(4)\n\t" \
    "v_mfma_f32_16x16x32_bf16 v[212:215], v[248:251], v[228:231], v[212:215]\n\t" \
    "ds_read_b128 v[228:231], v252 offset:16384\n\t" \
    "s_waitcnt lgkmcnt(4)\n\t" \
    "v_mfma_f32_16x16x32_bf16 v[200:203], v[244:247], v[232:235], 0\n\t" \
    "ds_read_b128 v[244:247], %[wl] offset:6144\n\t" \
    "s_waitcnt lgkmcnt(4)\n\t" \
    "v_mfma_f32_16x16x32_bf16 v[200:203], v[248:251], v[236:239], v[200:203]\n\t" \
    "ds_read_b128 v[248:251], %[wl] offset:7168\n\t" \
    "v_mfma_f32_16x16x32_bf16 v[204:207], v[80:83], v[232:235], 0\n\t" \
    "v_mfma_f32_16x16x32_bf16 v[204:207], v[84:87], v[236:239], v[204:207]\n\t" \
    "v_mfma_f32_16x16x32_bf16 v[208:211], v[88:91], v[232:235], 0\n\t" \
    "ds_read_b128 v[232:235], v252 offset:17408\n\t" \
    "v_mfma_f32_16x16x32_bf16 v[208:211], v[92:95], v[236:239], v[208:211]\n\t" \
    "v_xor_b32 v252, 0x3000, %[xa]\n\t" \
    "ds_read_b128 v[236:239], v252 offset:16384\n\t" \
    "s_waitcnt lgkmcnt(6)\n\t" \
    "v_mfma_f32_16x16x32_bf16 v[208:211], v[112:115], v[240:243], v[208:211]\n\t" \
    "s_waitcnt lgkmcnt(5)\n\t" \
    "v_mfma_f32_16x16x32_bf16 v[208:211], v[116:119], v[224:227], v[208:211]\n\t" \
    "v_mfma_f32_16x16x32_bf16 v[204:207], v[104:107], v[240:243], v[204:207]\n\t" \
    "v_mfma_f32_16x16x32_bf16 v[204:207], v[108:111], v[224:227], v[204:207]\n\t" \
    "v_mfma_f32_16x16x32_bf16 v[200:203], v[96:99], v[240:243], v[200:203]\n\t" \
    "ds_read_b128 v[240:243], v252 offset:17408\n\t" \
    "v_mfma_f32_16x16x32_bf16 v[200:203], v[100:103], v[224:227], v[200:203]\n\t" \
    "ds_read_b128 v[224:227], v252 offset:0\n\t" \
    "s_waitcnt lgkmcnt(6)\n\t" \
    "v_mfma_f32_16x16x32_bf16 v[212:215], v[96:99], v[228:231], v[212:215]\n\t" \
    "s_waitcnt lgkmcnt(3)\n\t" \
    "v_mfma_f32_16x16x32_bf16 v[212:215], v[100:103], v[232:235], v[212:215]\n\t" \
    "v_mfma_f32_16x16x32_bf16 v[216:219], v[104:107], v[228:231], v[216:219]\n\t" \
    "v_mfma_f32_16x16x32_bf16 v[216:219], v[108:111], v[232:235], v[216:219]\n\t" \
    "v_mfma_f32_16x16x32_bf16 v[220:223], v[112:115], v[228:231], v[220:223]\n\t" \
    "ds_read_b128 v[228:231], v252 offset:1024\n\t" \
    "v_mfma_f32_16x16x32_bf16 v[220:223], v[116:119], v[232:235], v[220:223]\n\t" \
    "v_xor_b32 v252, 0x3800, %[xa]\n\t" \
    "ds_read_b128 v[232:235], v252 offset:0\n\t" \
    "s_waitcnt lgkmcnt(4)\n\t" \
    "v_mfma_f32_16x16x32_bf16 v[220:223], v[128:131], v[236:239], v[220:223]\n\t" \
    "s_waitcnt lgkmcnt(3)\n\t" \
    "v_mfma_f32_16x16x32_bf16 v[220:223], v[132:135], v[240:243], v[220:223]\n\t" \
    "v_mfma_f32_16x16x32_bf16 v[216:219], v[120:123], v[236:239], v[216:219]\n\t" \
    "v_mfma_f32_16x16x32_bf16 v[216:219], v[124:127], v[240:243], v[216:219]\n\t" \
    "v_mfma_f32_16x16x32_bf16 v[212:215], v[244:247], v[236:239], v[212:215]\n\t" \
    "ds_read_b128 v[236:239], v252 offset:1024\n\t" \
    "v_mfma_f32_16x16x32_bf16 v[212:215], v[248:251], v[240:243], v[212:215]\n\t" \
    "ds_read_b128 v[240:243], v252 offset:16384\n\t" \
    "s_waitcnt lgkmcnt(4)\n\t" \
    "v_mfma_f32_16x16x32_bf16 v[200:203], v[244:247], v[224:227], v[200:203]\n\t" \
    "ds_read_b128 v[244:247], %[wl] offset:8192\n\t" \
    "s_waitcnt lgkmcnt(4)\n\t" \
    "v_mfma_f32_16x16x32_bf16 v[200:203], v[248:251], v[228:231], v[200:203]\n\t" \
    "ds_read_b128 v[248:251], %[wl] offset:9216\n\t" \
    "v_mfma_f32_16x16x32_bf16 v[204:207], v[120:123], v[224:227], v[204:207]\n\t" \
    "v_mfma_f32_16x16x32_bf16 v[204:207], v[124:127], v[228:231], v[204:207]\n\t" \
    "v_mfma_f32_16x16x32_bf16 v[208:211], v[128:131], v[224:227], v[208:211]\n\t" \
    "ds_read_b128 v[224:227], v252 offset:17408\n\t" \
    "v_mfma_f32_16x16x32_bf16 v[208:211], v[132:135], v[228:231], v[208:211]\n\t" \
    "v_and_b32 v252, 0x3ff, %[xa]\n\t" \
    "ds_read_b128 v[228:231], v252 offset:45056\n\t" \
    "s_waitcnt lgkmcnt(6)\n\t" \
    "v_mfma_f32_16x16x32_bf16 v[208:211], v[152:155], v[232:235], v[208:211]\n\t" \
    "s_waitcnt lgkmcnt(5)\n\t" \
    "v_mfma_f32_16x16x32_bf16 v[208:211], v[156:159], v[236:239], v[208:211]\n\t" \
    "v_mfma_f32_16x16x32_bf16 v[204:207], v[144:147], v[232:235], v[204:207]\n\t" \
    "v_mfma_f32_16x16x32_bf16 v[204:207], v[148:151], v[236:239], v[204:207]\n\t" \
    "v_mfma_f32_16x16x32_bf16 v[200:203], v[136:139], v[232:235], v[200:203]\n\t" \
    "ds_read_b128 v[232:235], v252 offset:46080\n\t" \
    "v_mfma_f32_16x16x32_bf16 v[200:203], v[140:143], v[236:239], v[200:203]\n\t" \
    "ds_read_b128 v[236:239], v252 offset:40960\n\t" \
    "s_waitcnt lgkmcnt(6)\n\t" \
    "v_mfma_f32_16x16x32_bf16 v[212:215], v[136:139], v[240:243], v[212:215]\n\t" \
    "s_waitcnt lgkmcnt(3)\n\t" \
    "v_mfma_f32_16x16x32_bf16 v[212:215], v[140:143], v[224:227], v[212:215]\n\t" \
    "v_mfma_f32_16x16x32_bf16 v[216:219], v[144:147], v[240:243], v[216:219]\n\t" \
    "v_mfma_f32_16x16x32_bf16 v[216:219], v[148:151], v[224:227], v[216:219]\n\t" \
    "v_mfma_f32_16x16x32_bf16 v[220:223], v[152:155], v[240:243], v[220:223]\n\t" \
    "ds_read_b128 v[240:243], v252 offset:41984\n\t" \
    "v_mfma_f32_16x16x32_bf16 v[220:223], v[156:159], v[224:227], v[220:223]\n\t" \
    "s_waitcnt vmcnt(0) lgkmcnt(0)\n\t" \
    "s_barrier\n\t" ASM_ST \
    "s_mov_b32 m0, %[d0]\n\t" \
    "s_nop 0\n\t" \
    "global_load_lds_dwordx4 v252, %[g0] offset:0 sc1\n\t" \
    "global_load_lds_dwordx4 v252, %[g0] offset:1024 sc1\n\t" \
    "global_load_lds_dwordx4 v252, %[g0] offset:2048 sc1\n\t" \
    "global_load_lds_dwordx4 v252, %[g0] offset:3072 sc1\n\t" \
    "s_mov_b32 m0, %[d1]\n\t" \
    "s_nop 0\n\t" \
    "global_load_lds_dwordx4 v252, %[g1] sc1\n\t" \
    "ds_read_b128 v[224:227], %[xa] offset:0\n\t" \
    "v_mfma_f32_16x16x32_bf16 v[220:223], v[168:171], v[228:231], v[220:223]\n\t" \
    "v_mfma_f32_16x16x32_bf16 v[220:223], v[172:175], v[232:235], v[220:223]\n\t" \
    "v_mfma_f32_16x16x32_bf16 v[216:219], v[160:163], v[228:231], v[216:219]\n\t" \
    "v_mfma_f32_16x16x32_bf16 v[216:219], v[164:167], v[232:235], v[216:219]\n\t" \
    "v_mfma_f32_16x16x32_bf16 v[212:215], v[244:247], v[228:231], v[212:215]\n\t" \
    "ds_read_b128 v[228:231], %[xa] offset:1024\n\t" \
    "v_mfma_f32_16x16x32_bf16 v[212:215], v[248:251], v[232:235], v[212:215]\n\t" \
    "ds_read_b128 v[232:235], %[xa] offset:16384\n\t" \
    "v_mfma_f32_16x16x32_bf16 v[200:203], v[244:247], v[236:239], v[200:203]\n\t" \
    "ds_read_b128 v[244:247], %[wl] offset:0\n\t" \
    "v_mfma_f32_16x16x32_bf16 v[200:203], v[248:251], v[240:243], v[200:203]\n\t" \
    "ds_read_b128 v[248:251], %[wl] offset:1024\n\t" \
    "v_mfma_f32_16x16x32_bf16 v[204:207], v[160:163], v[236:239], v[204:207]\n\t" \
    "v_mfma_f32_16x16x32_bf16 v[204:207], v[164:167], v[240:243], v[204:207]\n\t" \
    "v_mfma_f32_16x16x32_bf16 v[208:211], v[168:171], v[236:239], v[208:211]\n\t" \
    "ds_read_b128 v[236:239], %[xa] offset:17408\n\t" \
    "v_mfma_f32_16x16x32_bf16 v[208:211], v[172:175], v[240:243], v[208:211]\n\t" \
    "v_xor_b32 v252, 0x800, %[xa]\n\t" \
    "ds_read_b128 v[240:243], v252 offset:16384\n\t" \
    "s_waitcnt lgkmcnt(6)\n\t" \
    "v_mfma_f32_16x16x32_bf16 v[208:211], v[8:11], v[224:227], v[208:211]\n\t" \
    "s_waitcnt lgkmcnt(5)\n\t" \
    "v_mfma_f32_16x16x32_bf16 v[208:211], v[12:15], v[228:231], v[208:211]\n\t" \
    "v_mfma_f32_16x16x32_bf16 v[204:207], v[0:3], v[224:227], v[204:207]\n\t" \
    "v_mfma_f32_16x16x32_bf16 v[204:207], v[4:7], v[228:231], v[204:207]\n\t" \
    "s_waitcnt lgkmcnt(3)\n\t" \
    "v_mfma_f32_16x16x32_bf16 v[200:203], v[244:247], v[224:227], v[200:203]\n\t" \
    "ds_read_b128 v[224:227], v252 offset:17408\n\t" \
    "s_waitcnt lgkmcnt(3)\n\t" \
    "v_mfma_f32_16x16x32_bf16 v[200:203], v[248:251], v[228:231], v[200:203]\n\t" \
    "ds_read_b128 v[228:231], v252 offset:0\n\t" \
    "v_mfma_f32_16x16x32_bf16 v[212:215], v[244:247], v[232:235], v[212:215]\n\t" \
    "ds_read_b128 v[244:247], %[wl] offset:2048\n\t" \
    "s_waitcnt lgkmcnt(4)\n\t" \
    "v_mfma_f32_16x16x32_bf16 v[212:215], v[248:251], v[236:239], v[212:215]\n\t" \
    "ds_read_b128 v[248:251], %[wl] offset:3072\n\t" \
    "v_mfma_f32_16x16x32_bf16 v[216:219], v[0:3], v[232:235], v[216:219]\n\t" \
    "v_mfma_f32_16x16x32_bf16 v[216:219], v[4:7], v[236:239], v[216:219]\n\t" \
    "v_mfma_f32_16x16x32_bf16 v[220:223], v[8:11], v[232:235], v[220:223]\n\t" \
    "ds_read_b128 v[232:235], v252 offset:1024\n\t" \
    "v_mfma_f32_16x16x32_bf16 v[220:223], v[12:15], v[236:239], v[220:223]\n\t" \
    "v_xor_b32 v252, 0x1000, %[xa]\n\t" \
    "ds_read_b128 v[236:239], v252 offset:0\n\t" \
    "s_waitcnt lgkmcnt(6)\n\t" \
    "v_mfma_f32_16x16x32_bf16 v[220:223], v[32:35], v[240:243], v[220:223]\n\t" \
    "s_waitcnt lgkmcnt(5)\n\t" \
    "v_mfma_f32_16x16x32_bf16 v[220:223], v[36:39], v[224:227], v[220:223]\n\t" \
    "v_mfma_f32_16x16x32_bf16 v[216:219], v[24:27], v[240:243], v[216:219]\n\t" \
    "v_mfma_f32_16x16x32_bf16 v[216:219], v[28:31], v[224:227], v[216:219]\n\t" \
    "v_mfma_f32_16x16x32_bf16 v[212:215], v[16:19], v[240:243], v[212:215]\n\t" \
    "ds_read_b128 v[240:243], v252 offset:1024\n\t" \
    "v_mfma_f32_16x16x32_bf16 v[212:215], v[20:23], v[224:227], v[212:215]\n\t" \
    "ds_read_b128 v[224:227], v252 offset:16384\n\t" \
    "s_waitcnt lgkmcnt(6)\n\t" \
    "v_mfma_f32_16x16x32_bf16 v[200:203], v[16:19], v[228:231], v[200:203]\n\t" \
    "s_waitcnt lgkmcnt(3)\n\t" \
    "v_mfma_f32_16x16x32_bf16 v[200:203], v[20:23], v[232:235], v[200:203]\n\t" \
    "v_mfma_f32_16x16x32_bf16 v[204:207], v[24:27], v[228:231], v[204:207]\n\t" \
    "v_mfma_f32_16x16x32_bf16 v[204:207], v[28:31], v[232:235], v[204:207]\n\t" \
    "v_mfma_f32_16x16x32_bf16 v[208:211], v[32:35], v[228:231], v[208:211]\n\t" \
    "ds_read_b128 v[228:231], v252 offset:17408\n\t" \
    "v_mfma_f32_16x16x32_bf16 v[208:211], v[36:39], v[232:235], v[208:211]\n\t" \
    "v_xor_b32 v252, 0x1800, %[xa]\n\t" \
    "ds_read_b128 v[232:235], v252 offset:16384\n\t" \
    "s_waitcnt lgkmcnt(4)\n\t" \
    "v_mfma_f32_16x16x32_bf16 v[208:211], v[48:51], v[236:239], v[208:211]\n\t" \
    "s_waitcnt lgkmcnt(3)\n\t" \
    "v_mfma_f32_16x16x32_bf16 v[208:211], v[52:55], v[240:243], v[208:211]\n\t" \
    "v_mfma_f32_16x16x32_bf16 v[204:207], v[40:43], v[236:239], v[204:207]\n\t" \
    "v_mfma_f32_16x16x32_bf16 v[204:207], v[44:47], v[240:243], v[204:207]\n\t" \
    "v_mfma_f32_16x16x32_bf16 v[200:203], v[244:247], v[236:239], v[200:203]\n\t" \
    "ds_read_b128 v[236:239], v252 offset:17408\n\t" \
    "v_mfma_f32_16x16x32_bf16 v[200:203], v[248:251], v[240:243], v[200:203]\n\t" \
    "ds_read_b128 v[240:243], v252 offset:0\n\t" \
    "s_waitcnt lgkmcnt(4)\n\t" \
    "v_mfma_f32_16x16x32_bf16 v[212:215], v[244:247], v[224:227], v[212:215]\n\t" \
    "s_waitcnt lgkmcnt(3)\n\t" \
    "v_mfma_f32_16x16x32_bf16 v[212:215], v[248:251], v[228:231], v[212:215]\n\t" \
    "v_mfma_f32_16x16x32_bf16 v[216:219], v[40:43], v[224:227], v[216:219]\n\t" \
    "v_mfma_f32_16x16x32_bf16 v[216:219], v[44:47], v[228:231], v[216:219]\n\t" \
    "v_mfma_f32_16x16x32_bf16 v[220:223], v[48:51], v[224:227], v[220:223]\n\t" \
    "ds_read_b128 v[224:227], v252 offset:1024\n\t" \
    "v_mfma_f32_16x16x32_bf16 v[220:223], v[52:55], v[228:231], v[220:223]\n\t" \
    "v_and_b32 v252, 0x3ff, %[xa]\n\t" \
    "ds_read_b128 v[228:231], v252 offset:43008\n\t" \
    "s_waitcnt lgkmcnt(4)\n\t" \
    "v_mfma_f32_16x16x32_bf16 v[220:223], v[72:75], v[232:235], v[220:223]\n\t" \
    "v_mov_b64 v[244:245], 0\n\t" \
    "s_waitcnt lgkmcnt(3)\n\t" \
    "v_mfma_f32_16x16x32_bf16 v[220:223], v[76:79], v[236:239], v[220:223]\n\t" \
    "v_mov_b64 v[246:247], 0\n\t" \
    "v_mfma_f32_16x16x32_bf16 v[216:219], v[64:67], v[232:235], v[216:219]\n\t" \
    "v_mov_b64 v[248:249], 0\n\t" \
    "v_mfma_f32_16x16x32_bf16 v[216:219], v[68:71], v[236:239], v[216:219]\n\t" \
    "v_mov_b64 v[250:251], 0\n\t" \
    "v_mfma_f32_16x16x32_bf16 v[212:215], v[56:59], v[232:235], v[212:215]\n\t" \
    "ds_read_b128 v[232:235], v252 offset:44032\n\t" \
    "v_mfma_f32_16x16x32_bf16 v[212:215], v[60:63], v[236:239], v[212:215]\n\t" \
    "ds_read_b128 v[236:239], v252 offset:47104\n\t" \
    "s_waitcnt lgkmcnt(4)\n\t" \
    "v_mfma_f32_16x16x32_bf16 v[200:203], v[56:59], v[240:243], v[200:203]\n\t" \
    "s_waitcnt lgkmcnt(3)\n\t" \
    "v_mfma_f32_16x16x32_bf16 v[200:203], v[60:63], v[224:227], v[200:203]\n\t" \
    "v_mfma_f32_16x16x32_bf16 v[204:207], v[64:67], v[240:243], v[204:207]\n\t" \
    "v_mfma_f32_16x16x32_bf16 v[204:207], v[68:71], v[224:227], v[204:207]\n\t" \
    "v_mfma_f32_16x16x32_bf16 v[208:211], v[72:75], v[240:243], v[208:211]\n\t" \
    "ds_read_b128 v[240:243], v252 offset:48128\n\t" \
    "v_mfma_f32_16x16x32_bf16 v[208:211], v[76:79], v[224:227], v[208:211]\n\t" \
    "s_waitcnt lgkmcnt(3)\n\t" \
    "v_mfma_f32_16x16x32_bf16 v[208:211], v[192:195], v[228:231], v[208:211]\n\t" \
    "v_add_u32 v252, %[qs], v252\n\t" \
    "v_mov_b64 v[224:225], 0\n\t" \
    "s_waitcnt lgkmcnt(2)\n\t" \
    "v_mfma_f32_16x16x32_bf16 v[208:211], v[196:199], v[232:235], v[208:211]\n\t" \
    "v_mov_b64 v[226:227], 0\n\t" \
    "v_mfma_f32_16x16x32_bf16 v[204:207], v[184:187], v[228:231], v[204:207]\n\t" \
    "v_mfma_f32_16x16x32_bf16 v[204:207], v[188:191], v[232:235], v[204:207]\n\t" \
    "v_mfma_f32_16x16x32_bf16 v[200:203], v[176:179], v[228:231], v[200:203]\n\t" \
    "v_mfma_f32_16x16x32_bf16 v[200:203], v[180:183], v[232:235], v[200:203]\n\t" \
    "s_waitcnt lgkmcnt(1)\n\t" \
    "v_mfma_f32_16x16x32_bf16 v[212:215], v[176:179], v[236:239], v[212:215]\n\t" \
    "ds_read_b128 v[228:231], %[wl] offset:2048\n\t" \
    "ds_read_b128 v[232:235], %[wl] offset:3072\n\t" \
    "s_waitcnt lgkmcnt(2)\n\t" \
    "v_mfma_f32_16x16x32_bf16 v[212:215], v[180:183], v[240:243], v[212:215]\n\t" \
    "v_cvt_pk_bf16_f32 v208, v208, v209\n\t" \
    "v_cvt_pk_bf16_f32 v209, v210, v211\n\t" \
    "v_mfma_f32_16x16x32_bf16 v[216:219], v[184:187], v[236:239], v[216:219]\n\t" \
    "ds_write_b64 v252, v[208:209] offset:2048\n\t" \
    "v_cvt_pk_bf16_f32 v204, v204, v205\n\t" \
    "v_mfma_f32_16x16x32_bf16 v[216:219], v[188:191], v[240:243], v[216:219]\n\t" \
    "v_cvt_pk_bf16_f32 v205, v206, v207\n\t" \
    "ds_write_b64 v252, v[204:205] offset:1024\n\t" \
    "v_mfma_f32_16x16x32_bf16 v[220:223], v[192:195], v[236:239], v[220:223]\n\t" \
    "v_cvt_pk_bf16_f32 v200, v200, v201\n\t" \
    "v_cvt_pk_bf16_f32 v201, v202, v203\n\t" \
    "v_mfma_f32_16x16x32_bf16 v[220:223], v[196:199], v[240:243], v[220:223]\n\t" \
    "ds_write_b64 v252, v[200:201] offset:0\n\t" \
    "ds_read_b128 v[200:203], %[wl] offset:0\n\t" \
    "ds_read_b128 v[204:207], %[wl] offset:1024\n\t" \
    "v_cvt_pk_bf16_f32 v212, v212, v213\n\t" \
    "v_cvt_pk_bf16_f32 v213, v214, v215\n\t" \
    "ds_write_b64 v252, v[212:213] offset:6144\n\t" \
    "v_cvt_pk_bf16_f32 v216, v216, v217\n\t" \
    "v_cvt_pk_bf16_f32 v217, v218, v219\n\t" \
    "ds_write_b64 v252, v[216:217] offset:7168\n\t" \
    "v_cvt_pk_bf16_f32 v220, v220, v221\n\t" \
    "v_cvt_pk_bf16_f32 v221, v222, v223\n\t" \
    "ds_write_b64 v252, v[220:221] offset:8192\n\t" \
    "s_waitcnt lgkmcnt(0)\n\t"
#define ASM_XG_S_LO2 \
    "s_waitcnt lgkmcnt(0)\n\t" \
    "ds_read_b128 v[224:227], %[xa] offset:16384\n\t" \
    "ds_read_b128 v[228:231], %[xa] offset:17408\n\t" \
    "v_xor_b32 v252, 0x800, %[xa]\n\t" \
    "ds_read_b128 v[232:235], v252 offset:16384\n\t" \
    "ds_read_b128 v[236:239], v252 offset:17408\n\t" \
    "v_xor_b32 v252, 0x1000, %[xa]\n\t" \
    "ds_read_b128 v[240:243], v252 offset:16384\n\t" \
    "ds_read_b128 v[212:215], v252 offset:17408\n\t" \
    "v_xor_b32 v252, 0x1800, %[xa]\n\t" \
    "ds_read_b128 v[200:203], v252 offset:16384\n\t" \
    "s_waitcnt lgkmcnt(6)\n\t" \
    "v_mfma_f32_16x16x32_bf16 v[220:223], v[8:11], v[224:227], 0\n\t" \
    "s_waitcnt lgkmcnt(5)\n\t" \
    "v_mfma_f32_16x16x32_bf16 v[220:223], v[12:15], v[228:231], v[220:223]\n\t" \
    "v_mfma_f32_16x16x32_bf16 v[216:219], v[0:3], v[224:227], 0\n\t" \
    "ds_read_b128 v[224:227], v252 offset:17408\n\t" \
    "v_mfma_f32_16x16x32_bf16 v[216:219], v[4:7], v[228:231], v[216:219]\n\t" \
    "v_and_b32 v252, 0x3ff, %[xa]\n\t" \
    "ds_read_b128 v[228:231], v252 offset:45056\n\t" \
    "s_waitcnt lgkmcnt(6)\n\t" \
    "v_mfma_f32_16x16x32_bf16 v[216:219], v[24:27], v[232:235], v[216:219]\n\t" \
    "s_waitcnt lgkmcnt(5)\n\t" \
    "v_mfma_f32_16x16x32_bf16 v[216:219], v[28:31], v[236:239], v[216:219]\n\t" \
    "v_mfma_f32_16x16x32_bf16 v[220:223], v[32:35], v[232:235], v[220:223]\n\t" \
    "ds_read_b128 v[232:235], v252 offset:46080\n\t" \
    "v_mfma_f32_16x16x32_bf16 v[220:223], v[36:39], v[236:239], v[220:223]\n\t" \
    "ds_read_b128 v[236:239], %[xa] offset:0\n\t" \
    "s_waitcnt lgkmcnt(6)\n\t" \
    "v_mfma_f32_16x16x32_bf16 v[220:223], v[48:51], v[240:243], v[220:223]\n\t" \
    "s_waitcnt lgkmcnt(5)\n\t" \
    "v_mfma_f32_16x16x32_bf16 v[220:223], v[52:55], v[212:215], v[220:223]\n\t" \
    "v_mfma_f32_16x16x32_bf16 v[216:219], v[40:43], v[240:243], v[216:219]\n\t" \
    "ds_read_b128 v[240:243], %[xa] offset:1024\n\t" \
    "v_mfma_f32_16x16x32_bf16 v[216:219], v[44:47], v[212:215], v[216:219]\n\t" \
    "v_xor_b32 v252, 0x800, %[xa]\n\t" \
    "ds_read_b128 v[212:215], v252 offset:0\n\t" \
    "s_waitcnt lgkmcnt(6)\n\t" \
    "v_mfma_f32_16x16x32_bf16 v[216:219], v[64:67], v[200:203], v[216:219]\n\t" \
    "s_waitcnt lgkmcnt(5)\n\t" \
    "v_mfma_f32_16x16x32_bf16 v[216:219], v[68:71], v[224:227], v[216:219]\n\t" \
    "v_mfma_f32_16x16x32_bf16 v[220:223], v[72:75], v[200:203], v[220:223]\n\t" \
    "ds_read_b128 v[200:203], v252 offset:1024\n\t" \
    "v_mfma_f32_16x16x32_bf16 v[220:223], v[76:79], v[224:227], v[220:223]\n\t" \
    "v_xor_b32 v252, 0x1000, %[xa]\n\t" \
    "ds_read_b128 v[224:227], v252 offset:0\n\t" \
    "s_waitcnt lgkmcnt(6)\n\t" \
    "v_mfma_f32_16x16x32_bf16 v[220:223], v[168:171], v[228:231], v[220:223]\n\t" \
    "s_waitcnt lgkmcnt(5)\n\t" \
    "v_mfma_f32_16x16x32_bf16 v[220:223], v[172:175], v[232:235], v[220:223]\n\t" \
    "v_mfma_f32_16x16x32_bf16 v[216:219], v[160:163], v[228:231], v[216:219]\n\t" \
    "ds_read_b128 v[228:231], v252 offset:1024\n\t" \
    "v_mfma_f32_16x16x32_bf16 v[216:219], v[164:167], v[232:235], v[216:219]\n\t" \
    "v_xor_b32 v252, 0x1800, %[xa]\n\t" \
    "ds_read_b128 v[232:235], v252 offset:0\n\t" \
    "s_waitcnt lgkmcnt(6)\n\t" \
    "v_mfma_f32_16x16x32_bf16 v[204:207], v[0:3], v[236:239], 0\n\t" \
    "s_waitcnt lgkmcnt(5)\n\t" \
    "v_mfma_f32_16x16x32_bf16 v[204:207], v[4:7], v[240:243], v[204:207]\n\t" \
    "v_mfma_f32_16x16x32_bf16 v[208:211], v[8:11], v[236:239], 0\n\t" \
    "ds_read_b128 v[236:239], v252 offset:1024\n\t" \
    "v_mfma_f32_16x16x32_bf16 v[208:211], v[12:15], v[240:243], v[208:211]\n\t" \
    "v_and_b32 v252, 0x3ff, %[xa]\n\t" \
    "ds_read_b128 v[240:243], v252 offset:40960\n\t" \
    "s_waitcnt lgkmcnt(6)\n\t" \
    "v_mfma_f32_16x16x32_bf16 v[208:211], v[32:35], v[212:215], v[208:211]\n\t" \
    "s_waitcnt lgkmcnt(5)\n\t" \
    "v_mfma_f32_16x16x32_bf16 v[208:211], v[36:39], v[200:203], v[208:211]\n\t" \
    "v_mfma_f32_16x16x32_bf16 v[204:207], v[24:27], v[212:215], v[204:207]\n\t" \
    "ds_read_b128 v[212:215], v252 offset:41984\n\t" \
    "v_mfma_f32_16x16x32_bf16 v[204:207], v[28:31], v[200:203], v[204:207]\n\t" \
    "s_waitcnt lgkmcnt(0)\n\t" \
    "s_barrier\n\t" ASM_ST \
    "v_xor_b32 v252, 0x2000, %[xa]\n\t" \
    "ds_read_b128 v[200:203], v252 offset:0\n\t" \
    "v_mfma_f32_16x16x32_bf16 v[204:207], v[40:43], v[224:227], v[204:207]\n\t" \
    "v_mfma_f32_16x16x32_bf16 v[204:207], v[44:47], v[228:231], v[204:207]\n\t" \
    "v_mfma_f32_16x16x32_bf16 v[208:211], v[48:51], v[224:227], v[208:211]\n\t" \
    "ds_read_b128 v[224:227], v252 offset:1024\n\t" \
    "v_mfma_f32_16x16x32_bf16 v[208:211], v[52:55], v[228:231], v[208:211]\n\t" \
    "v_xor_b32 v252, 0x2800, %[xa]\n\t" \
    "ds_read_b128 v[228:231], v252 offset:0\n\t" \
    "v_mfma_f32_16x16x32_bf16 v[208:211], v[72:75], v[232:235], v[208:211]\n\t" \
    "v_mfma_f32_16x16x32_bf16 v[208:211], v[76:79], v[236:239], v[208:211]\n\t" \
    "v_mfma_f32_16x16x32_bf16 v[204:207], v[64:67], v[232:235], v[204:207]\n\t" \
    "ds_read_b128 v[232:235], v252 offset:1024\n\t" \
    "v_mfma_f32_16x16x32_bf16 v[204:207], v[68:71], v[236:239], v[204:207]\n\t" \
    "v_xor_b32 v252, 0x3000, %[xa]\n\t" \
    "ds_read_b128 v[236:239], v252 offset:0\n\t" \
    "v_mfma_f32_16x16x32_bf16 v[204:207], v[160:163], v[240:243], v[204:207]\n\t" \
    "v_mfma_f32_16x16x32_bf16 v[204:207], v[164:167], v[212:215], v[204:207]\n\t" \
    "v_mfma_f32_16x16x32_bf16 v[208:211], v[168:171], v[240:243], v[208:211]\n\t" \
    "ds_read_b128 v[240:243], v252 offset:1024\n\t" \
    "v_mfma_f32_16x16x32_bf16 v[208:211], v[172:175], v[212:215], v[208:211]\n\t" \
    "v_xor_b32 v252, 0x3800, %[xa]\n\t" \
    "ds_read_b128 v[212:215], v252 offset:0\n\t" \
    "s_waitcnt lgkmcnt(6)\n\t" \
    "v_mfma_f32_16x16x32_bf16 v[208:211], v[88:91], v[200:203], v[208:211]\n\t" \
    "s_waitcnt lgkmcnt(5)\n\t" \
    "v_mfma_f32_16x16x32_bf16 v[208:211], v[92:95], v[224:227], v[208:211]\n\t" \
    "v_mfma_f32_16x16x32_bf16 v[204:207], v[80:83], v[200:203], v[204:207]\n\t" \
    "ds_read_b128 v[200:203], v252 offset:1024\n\t" \
    "v_mfma_f32_16x16x32_bf16 v[204:207], v[84:87], v[224:227], v[204:207]\n\t" \
    "v_and_b32 v252, 0x3ff, %[xa]\n\t" \
    "ds_read_b128 v[224:227], v252 offset:43008\n\t" \
    "s_waitcnt lgkmcnt(6)\n\t" \
    "v_mfma_f32_16x16x32_bf16 v[204:207], v[104:107], v[228:231], v[204:207]\n\t" \
    "s_waitcnt lgkmcnt(5)\n\t" \
    "v_mfma_f32_16x16x32_bf16 v[204:207], v[108:111], v[232:235], v[204:207]\n\t" \
    "v_mfma_f32_16x16x32_bf16 v[208:211], v[112:115], v[228:231], v[208:211]\n\t" \
    "ds_read_b128 v[228:231], v252 offset:44032\n\t" \
    "v_mfma_f32_16x16x32_bf16 v[208:211], v[116:119], v[232:235], v[208:211]\n\t" \
    "v_xor_b32 v252, 0x2000, %[xa]\n\t" \
    "ds_read_b128 v[232:235], v252 offset:16384\n\t" \
    "s_waitcnt lgkmcnt(6)\n\t" \
    "v_mfma_f32_16x16x32_bf16 v[208:211], v[128:131], v[236:239], v[208:211]\n\t" \
    "s_waitcnt lgkmcnt(5)\n\t" \
    "v_mfma_f32_16x16x32_bf16 v[208:211], v[132:135], v[240:243], v[208:211]\n\t" \
    "v_mfma_f32_16x16x32_bf16 v[204:207], v[120:123], v[236:239], v[204:207]\n\t" \
    "ds_read_b128 v[236:239], v252 offset:17408\n\t" \
    "v_mfma_f32_16x16x32_bf16 v[204:207], v[124:127], v[240:243], v[204:207]\n\t" \
    "v_xor_b32 v252, 0x2800, %[xa]\n\t" \
    "ds_read_b128 v[240:243], v252 offset:16384\n\t" \
    "s_waitcnt lgkmcnt(6)\n\t" \
    "v_mfma_f32_16x16x32_bf16 v[204:207], v[144:147], v[212:215], v[204:207]\n\t" \
    "s_waitcnt lgkmcnt(5)\n\t" \
    "v_mfma_f32_16x16x32_bf16 v[204:207], v[148:151], v[200:203], v[204:207]\n\t" \
    "v_mfma_f32_16x16x32_bf16 v[208:211], v[152:155], v[212:215], v[208:211]\n\t" \
    "ds_read_b128 v[212:215], v252 offset:17408\n\t" \
    "v_mfma_f32_16x16x32_bf16 v[208:211], v[156:159], v[200:203], v[208:211]\n\t" \
    "v_xor_b32 v252, 0x3000, %[xa]\n\t" \
    "ds_read_b128 v[200:203], v252 offset:16384\n\t" \
    "s_waitcnt lgkmcnt(6)\n\t" \
    "v_mfma_f32_16x16x32_bf16 v[208:211], v[192:195], v[224:227], v[208:211]\n\t" \
    "s_waitcnt lgkmcnt(5)\n\t" \
    "v_mfma_f32_16x16x32_bf16 v[208:211], v[196:199], v[228:231], v[208:211]\n\t" \
    "v_mfma_f32_16x16x32_bf16 v[204:207], v[184:187], v[224:227], v[204:207]\n\t" \
    "ds_read_b128 v[224:227], v252 offset:17408\n\t" \
    "v_mfma_f32_16x16x32_bf16 v[204:207], v[188:191], v[228:231], v[204:207]\n\t" \
    "v_xor_b32 v252, 0x3800, %[xa]\n\t" \
    "ds_read_b128 v[228:231], v252 offset:16384\n\t" \
    "s_waitcnt lgkmcnt(6)\n\t" \
    "v_mfma_f32_16x16x32_bf16 v[216:219], v[80:83], v[232:235], v[216:219]\n\t" \
    "s_waitcnt lgkmcnt(5)\n\t" \
    "v_mfma_f32_16x16x32_bf16 v[216:219], v[84:87], v[236:239], v[216:219]\n\t" \
    "v_mfma_f32_16x16x32_bf16 v[220:223], v[88:91], v[232:235], v[220:223]\n\t" \
    "ds_read_b128 v[232:235], v252 offset:17408\n\t" \
    "v_mfma_f32_16x16x32_bf16 v[220:223], v[92:95], v[236:239], v[220:223]\n\t" \
    "v_and_b32 v252, 0x3ff, %[xa]\n\t" \
    "ds_read_b128 v[236:239], v252 offset:47104\n\t" \
    "s_waitcnt lgkmcnt(6)\n\t" \
    "v_mfma_f32_16x16x32_bf16 v[220:223], v[112:115], v[240:243], v[220:223]\n\t" \
    "v_mov_b64 v[244:245], 0\n\t" \
    "s_waitcnt lgkmcnt(5)\n\t" \
    "v_mfma_f32_16x16x32_bf16 v[220:223], v[116:119], v[212:215], v[220:223]\n\t" \
    "v_mov_b64 v[246:247], 0\n\t" \
    "v_mfma_f32_16x16x32_bf16 v[216:219], v[104:107], v[240:243], v[216:219]\n\t" \
    "ds_read_b128 v[240:243], v252 offset:48128\n\t" \
    "v_mov_b64 v[248:249], 0\n\t" \
    "v_mfma_f32_16x16x32_bf16 v[216:219], v[108:111], v[212:215], v[216:219]\n\t" \
    "v_mov_b64 v[250:251], 0\n\t" \
    "s_waitcnt lgkmcnt(5)\n\t" \
    "v_mfma_f32_16x16x32_bf16 v[216:219], v[120:123], v[200:203], v[216:219]\n\t" \
    "s_waitcnt lgkmcnt(4)\n\t" \
    "v_mfma_f32_16x16x32_bf16 v[216:219], v[124:127], v[224:227], v[216:219]\n\t" \
    "v_mfma_f32_16x16x32_bf16 v[220:223], v[128:131], v[200:203], v[220:223]\n\t" \
    "v_mfma_f32_16x16x32_bf16 v[220:223], v[132:135], v[224:227], v[220:223]\n\t" \
    "s_waitcnt lgkmcnt(3)\n\t" \
    "v_mfma_f32_16x16x32_bf16 v[220:223], v[152:155], v[228:231], v[220:223]\n\t" \
    "v_add_u32 v252, %[qs], v252\n\t" \
    "v_mov_b64 v[224:225], 0\n\t" \
    "s_waitcnt lgkmcnt(2)\n\t" \
    "v_mfma_f32_16x16x32_bf16 v[220:223], v[156:159], v[232:235], v[220:223]\n\t" \
    "v_mov_b64 v[226:227], 0\n\t" \
    "v_cvt_pk_bf16_f32 v208, v208, v209\n\t" \
    "v_mfma_f32_16x16x32_bf16 v[216:219], v[144:147], v[228:231], v[216:219]\n\t" \
    "v_cvt_pk_bf16_f32 v209, v210, v211\n\t" \
    "ds_write_b64 v252, v[208:209] offset:2048\n\t" \
    "v_mfma_f32_16x16x32_bf16 v[216:219], v[148:151], v[232:235], v[216:219]\n\t" \
    "v_cvt_pk_bf16_f32 v204, v204, v205\n\t" \
    "v_cvt_pk_bf16_f32 v205, v206, v207\n\t" \
    "s_waitcnt lgkmcnt(2)\n\t" \
    "v_mfma_f32_16x16x32_bf16 v[216:219], v[184:187], v[236:239], v[216:219]\n\t" \
    "ds_write_b64 v252, v[204:205] offset:1024\n\t" \
    "ds_read_b128 v[228:231], %[wl] offset:2048\n\t" \
    "s_waitcnt lgkmcnt(3)\n\t" \
    "v_mfma_f32_16x16x32_bf16 v[216:219], v[188:191], v[240:243], v[216:219]\n\t" \
    "ds_read_b128 v[232:235], %[wl] offset:3072\n\t" \
    "v_mfma_f32_16x16x32_bf16 v[220:223], v[192:195], v[236:239], v[220:223]\n\t" \
    "v_mfma_f32_16x16x32_bf16 v[220:223], v[196:199], v[240:243], v[220:223]\n\t" \
    "ds_read_b128 v[200:203], %[wl] offset:0\n\t" \
    "ds_read_b128 v[204:207], %[wl] offset:1024\n\t" \
    "v_cvt_pk_bf16_f32 v216, v216, v217\n\t" \
    "v_cvt_pk_bf16_f32 v217, v218, v219\n\t" \
    "ds_write_b64 v252, v[216:217] offset:7168\n\t" \
    "s_nop 2\n\t" \
    "v_cvt_pk_bf16_f32 v220, v220, v221\n\t" \
    "v_cvt_pk_bf16_f32 v221, v222, v223\n\t" \
    "ds_write_b64 v252, v[220:221] offset:8192\n\t" \
    "s_waitcnt lgkmcnt(0)\n\t"
#define ASM_XG_S_LO1 \
    "s_waitcnt lgkmcnt(0)\n\t" \
    "ds_read_b128 v[224:227], %[xa] offset:16384\n\t" \
    "ds_read_b128 v[228:231], %[xa] offset:17408\n\t" \
    "v_xor_b32 v252, 0x800, %[xa]\n\t" \
    "ds_read_b128 v[232:235], v252 offset:16384\n\t" \
    "ds_read_b128 v[236:239], v252 offset:17408\n\t" \
    "v_xor_b32 v252, 0x1000, %[xa]\n\t" \
    "ds_read_b128 v[240:243], v252 offset:16384\n\t" \
    "ds_read_b128 v[212:215], v252 offset:17408\n\t" \
    "v_xor_b32 v252, 0x1800, %[xa]\n\t" \
    "ds_read_b128 v[200:203], v252 offset:16384\n\t" \
    "s_waitcnt lgkmcnt(6)\n\t" \
    "v_mfma_f32_16x16x32_bf16 v[220:223], v[8:11], v[224:227], 0\n\t" \
    "ds_read_b128 v[224:227], v252 offset:17408\n\t" \
    "s_waitcnt lgkmcnt(6)\n\t" \
    "v_mfma_f32_16x16x32_bf16 v[220:223], v[12:15], v[228:231], v[220:223]\n\t" \
    "v_and_b32 v252, 0x3ff, %[xa]\n\t" \
    "ds_read_b128 v[228:231], v252 offset:45056\n\t" \
    "s_waitcnt lgkmcnt(6)\n\t" \
    "v_mfma_f32_16x16x32_bf16 v[220:223], v[32:35], v[232:235], v[220:223]\n\t" \
    "ds_read_b128 v[232:235], v252 offset:46080\n\t" \
    "s_waitcnt lgkmcnt(6)\n\t" \
    "v_mfma_f32_16x16x32_bf16 v[220:223], v[36:39], v[236:239], v[220:223]\n\t" \
    "ds_read_b128 v[236:239], %[xa] offset:0\n\t" \
    "s_waitcnt lgkmcnt(6)\n\t" \
    "v_mfma_f32_16x16x32_bf16 v[220:223], v[48:51], v[240:243], v[220:223]\n\t" \
    "ds_read_b128 v[240:243], %[xa] offset:1024\n\t" \
    "s_waitcnt lgkmcnt(6)\n\t" \
    "v_mfma_f32_16x16x32_bf16 v[220:223], v[52:55], v[212:215], v[220:223]\n\t" \
    "v_xor_b32 v252, 0x800, %[xa]\n\t" \
    "ds_read_b128 v[212:215], v252 offset:0\n\t" \
    "s_waitcnt lgkmcnt(6)\n\t" \
    "v_mfma_f32_16x16x32_bf16 v[220:223], v[72:75], v[200:203], v[220:223]\n\t" \
    "ds_read_b128 v[200:203], v252 offset:1024\n\t" \
    "s_waitcnt lgkmcnt(6)\n\t" \
    "v_mfma_f32_16x16x32_bf16 v[220:223], v[76:79], v[224:227], v[220:223]\n\t" \
    "v_xor_b32 v252, 0x1000, %[xa]\n\t" \
    "ds_read_b128 v[224:227], v252 offset:0\n\t" \
    "s_waitcnt lgkmcnt(6)\n\t" \
    "v_mfma_f32_16x16x32_bf16 v[220:223], v[168:171], v[228:231], v[220:223]\n\t" \
    "ds_read_b128 v[228:231], v252 offset:1024\n\t" \
    "s_waitcnt lgkmcnt(6)\n\t" \
    "v_mfma_f32_16x16x32_bf16 v[220:223], v[172:175], v[232:235], v[220:223]\n\t" \
    "v_xor_b32 v252, 0x1800, %[xa]\n\t" \
    "ds_read_b128 v[232:235], v252 offset:0\n\t" \
    "s_waitcnt lgkmcnt(6)\n\t" \
    "v_mfma_f32_16x16x32_bf16 v[208:211], v[8:11], v[236:239], 0\n\t" \
    "ds_read_b128 v[236:239], v252 offset:1024\n\t" \
    "s_waitcnt lgkmcnt(6)\n\t" \
    "v_mfma_f32_16x16x32_bf16 v[208:211], v[12:15], v[240:243], v[208:211]\n\t" \
    "v_and_b32 v252, 0x3ff, %[xa]\n\t" \
    "ds_read_b128 v[240:243], v252 offset:40960\n\t" \
    "s_waitcnt lgkmcnt(6)\n\t" \
    "v_mfma_f32_16x16x32_bf16 v[208:211], v[32:35], v[212:215], v[208:211]\n\t" \
    "ds_read_b128 v[212:215], v252 offset:41984\n\t" \
    "s_waitcnt lgkmcnt(6)\n\t" \
    "v_mfma_f32_16x16x32_bf16 v[208:211], v[36:39], v[200:203], v[208:211]\n\t" \
    "s_waitcnt lgkmcnt(0)\n\t" \
    "s_barrier\n\t" ASM_ST \
    "v_xor_b32 v252, 0x2000, %[xa]\n\t" \
    "ds_read_b128 v[200:203], v252 offset:0\n\t" \
    "v_mfma_f32_16x16x32_bf16 v[208:211], v[48:51], v[224:227], v[208:211]\n\t" \
    "ds_read_b128 v[224:227], v252 offset:1024\n\t" \
    "v_mfma_f32_16x16x32_bf16 v[208:211], v[52:55], v[228:231], v[208:211]\n\t" \
    "v_xor_b32 v252, 0x2800, %[xa]\n\t" \
    "ds_read_b128 v[228:231], v252 offset:0\n\t" \
    "v_mfma_f32_16x16x32_bf16 v[208:211], v[72:75], v[232:235], v[208:211]\n\t" \
    "ds_read_b128 v[232:235], v252 offset:1024\n\t" \
    "v_mfma_f32_16x16x32_bf16 v[208:211], v[76:79], v[236:239], v[208:211]\n\t" \
    "v_xor_b32 v252, 0x3000, %[xa]\n\t" \
    "ds_read_b128 v[236:239], v252 offset:0\n\t" \
    "v_mfma_f32_16x16x32_bf16 v[208:211], v[168:171], v[240:243], v[208:211]\n\t" \
    "ds_read_b128 v[240:243], v252 offset:1024\n\t" \
    "v_mfma_f32_16x16x32_bf16 v[208:211], v[172:175], v[212:215], v[208:211]\n\t" \
    "v_xor_b32 v252, 0x3800, %[xa]\n\t" \
    "ds_read_b128 v[212:215], v252 offset:0\n\t" \
    "s_waitcnt lgkmcnt(6)\n\t" \
    "v_mfma_f32_16x16x32_bf16 v[208:211], v[88:91], v[200:203], v[208:211]\n\t" \
    "ds_read_b128 v[200:203], v252 offset:1024\n\t" \
    "s_waitcnt lgkmcnt(6)\n\t" \
    "v_mfma_f32_16x16x32_bf16 v[208:211], v[92:95], v[224:227], v[208:211]\n\t" \
    "v_and_b32 v252, 0x3ff, %[xa]\n\t" \
    "ds_read_b128 v[224:227], v252 offset:43008\n\t" \
    "s_waitcnt lgkmcnt(6)\n\t" \
    "v_mfma_f32_16x16x32_bf16 v[208:211], v[112:115], v[228:231], v[208:211]\n\t" \
    "ds_read_b128 v[228:231], v252 offset:44032\n\t" \
    "s_waitcnt lgkmcnt(6)\n\t" \
    "v_mfma_f32_16x16x32_bf16 v[208:211], v[116:119], v[232:235], v[208:211]\n\t" \
    "v_xor_b32 v252, 0x2000, %[xa]\n\t" \
    "ds_read_b128 v[232:235], v252 offset:16384\n\t" \
    "s_waitcnt lgkmcnt(6)\n\t" \
    "v_mfma_f32_16x16x32_bf16 v[208:211], v[128:131], v[236:239], v[208:211]\n\t" \
    "ds_read_b128 v[236:239], v252 offset:17408\n\t" \
    "s_waitcnt lgkmcnt(6)\n\t" \
    "v_mfma_f32_16x16x32_bf16 v[208:211], v[132:135], v[240:243], v[208:211]\n\t" \
    "v_xor_b32 v252, 0x2800, %[xa]\n\t" \
    "ds_read_b128 v[240:243], v252 offset:16384\n\t" \
    "s_waitcnt lgkmcnt(6)\n\t" \
    "v_mfma_f32_16x16x32_bf16 v[208:211], v[152:155], v[212:215], v[208:211]\n\t" \
    "ds_read_b128 v[212:215], v252 offset:17408\n\t" \
    "s_waitcnt lgkmcnt(6)\n\t" \
    "v_mfma_f32_16x16x32_bf16 v[208:211], v[156:159], v[200:203], v[208:211]\n\t" \
    "v_xor_b32 v252, 0x3000, %[xa]\n\t" \
    "ds_read_b128 v[200:203], v252 offset:16384\n\t" \
    "s_waitcnt lgkmcnt(6)\n\t" \
    "v_mfma_f32_16x16x32_bf16 v[208:211], v[192:195], v[224:227], v[208:211]\n\t" \
    "ds_read_b128 v[224:227], v252 offset:17408\n\t" \
    "s_waitcnt lgkmcnt(6)\n\t" \
    "v_mfma_f32_16x16x32_bf16 v[208:211], v[196:199], v[228:231], v[208:211]\n\t" \
    "v_xor_b32 v252, 0x3800, %[xa]\n\t" \
    "ds_read_b128 v[228:231], v252 offset:16384\n\t" \
    "s_waitcnt lgkmcnt(6)\n\t" \
    "v_mfma_f32_16x16x32_bf16 v[220:223], v[88:91], v[232:235], v[220:223]\n\t" \
    "ds_read_b128 v[232:235], v252 offset:17408\n\t" \
    "s_waitcnt lgkmcnt(6)\n\t" \
    "v_mfma_f32_16x16x32_bf16 v[220:223], v[92:95], v[236:239], v[220:223]\n\t" \
    "v_and_b32 v252, 0x3ff, %[xa]\n\t" \
    "ds_read_b128 v[236:239], v252 offset:47104\n\t" \
    "s_waitcnt lgkmcnt(6)\n\t" \
    "v_mfma_f32_16x16x32_bf16 v[220:223], v[112:115], v[240:243], v[220:223]\n\t" \
    "ds_read_b128 v[240:243], v252 offset:48128\n\t" \
    "v_mov_b64 v[244:245], 0\n\t" \
    "s_waitcnt lgkmcnt(6)\n\t" \
    "v_mfma_f32_16x16x32_bf16 v[220:223], v[116:119], v[212:215], v[220:223]\n\t" \
    "v_mov_b64 v[246:247], 0\n\t" \
    "s_waitcnt lgkmcnt(5)\n\t" \
    "v_mfma_f32_16x16x32_bf16 v[220:223], v[128:131], v[200:203], v[220:223]\n\t" \
    "v_mov_b64 v[248:249], 0\n\t" \
    "s_waitcnt lgkmcnt(4)\n\t" \
    "v_mfma_f32_16x16x32_bf16 v[220:223], v[132:135], v[224:227], v[220:223]\n\t" \
    "v_mov_b64 v[250:251], 0\n\t" \
    "s_waitcnt lgkmcnt(3)\n\t" \
    "v_mfma_f32_16x16x32_bf16 v[220:223], v[152:155], v[228:231], v[220:223]\n\t" \
    "v_add_u32 v252, %[qs], v252\n\t" \
    "v_mov_b64 v[224:225], 0\n\t" \
    "s_waitcnt lgkmcnt(2)\n\t" \
    "v_mfma_f32_16x16x32_bf16 v[220:223], v[156:159], v[232:235], v[220:223]\n\t" \
    "v_mov_b64 v[226:227], 0\n\t" \
    "v_cvt_pk_bf16_f32 v208, v208, v209\n\t" \
    "s_waitcnt lgkmcnt(1)\n\t" \
    "v_mfma_f32_16x16x32_bf16 v[220:223], v[192:195], v[236:239], v[220:223]\n\t" \
    "v_cvt_pk_bf16_f32 v209, v210, v211\n\t" \
    "ds_write_b64 v252, v[208:209] offset:2048\n\t" \
    "s_waitcnt lgkmcnt(1)\n\t" \
    "v_mfma_f32_16x16x32_bf16 v[220:223], v[196:199], v[240:243], v[220:223]\n\t" \
    "ds_read_b128 v[228:231], %[wl] offset:2048\n\t" \
    "ds_read_b128 v[232:235], %[wl] offset:3072\n\t" \
    "ds_read_b128 v[200:203], %[wl] offset:0\n\t" \
    "ds_read_b128 v[204:207], %[wl] offset:1024\n\t" \
    "s_nop 3\n\t" \
    "v_cvt_pk_bf16_f32 v220, v220, v221\n\t" \
    "v_cvt_pk_bf16_f32 v221, v222, v223\n\t" \
    "ds_write_b64 v252, v[220:221] offset:8192\n\t" \
    "s_waitcnt lgkmcnt(0)\n\t"
#define ASM_XG_V \
    "s_waitcnt lgkmcnt(0)\n\t" \
    "ds_read_b128 v[224:227], %[xa] offset:16384\n\t" \
    "ds_read_b128 v[228:231], %[xa] offset:17408\n\t" \
    "ds_read_b128 v[244:247], %[wl] offset:0\n\t" \
    "ds_read_b128 v[248:251], %[wl] offset:1024\n\t" \
    "ds_read_b128 v[232:235], %[xa] offset:0\n\t" \
    "ds_read_b128 v[236:239], %[xa] offset:1024\n\t" \
    "v_xor_b32 v252, 0x800, %[xa]\n\t" \
    "ds_read_b128 v[240:243], v252 offset:0\n\t" \
    "s_waitcnt lgkmcnt(6)\n\t" \
    "v_mfma_f32_16x16x32_bf16 v[220:223], v[8:11], v[224:227], 0\n\t" \
    "s_waitcnt lgkmcnt(5)\n\t" \
    "v_mfma_f32_16x16x32_bf16 v[220:223], v[12:15], v[228:231], v[220:223]\n\t" \
    "v_mfma_f32_16x16x32_bf16 v[216:219], v[0:3], v[224:227], 0\n\t" \
    "v_mfma_f32_16x16x32_bf16 v[216:219], v[4:7], v[228:231], v[216:219]\n\t" \
    "s_waitcnt lgkmcnt(4)\n\t" \
    "v_mfma_f32_16x16x32_bf16 v[212:215], v[244:247], v[224:227], 0\n\t" \
    "ds_read_b128 v[224:227], v252 offset:1024\n\t" \
    "s_waitcnt lgkmcnt(4)\n\t" \
    "v_mfma_f32_16x16x32_bf16 v[212:215], v[248:251], v[228:231], v[212:215]\n\t" \
    "ds_read_b128 v[228:231], v252 offset:16384\n\t" \
    "s_waitcnt lgkmcnt(4)\n\t" \
    "v_mfma_f32_16x16x32_bf16 v[200:203], v[244:247], v[232:235], 0\n\t" \
    "ds_read_b128 v[244:247], %[wl] offset:2048\n\t" \
    "s_waitcnt lgkmcnt(4)\n\t" \
    "v_mfma_f32_16x16x32_bf16 v[200:203], v[248:251], v[236:239], v[200:203]\n\t" \
    "ds_read_b128 v[248:251], %[wl] offset:3072\n\t" \
    "v_mfma_f32_16x16x32_bf16 v[204:207], v[0:3], v[232:235], 0\n\t" \
    "v_mfma_f32_16x16x32_bf16 v[204:207], v[4:7], v[236:239], v[204:207]\n\t" \
    "v_mfma_f32_16x16x32_bf16 v[208:211], v[8:11], v[232:235], 0\n\t" \
    "ds_read_b128 v[232:235], v252 offset:17408\n\t" \
    "v_mfma_f32_16x16x32_bf16 v[208:211], v[12:15], v[236:239], v[208:211]\n\t" \
    "v_xor_b32 v252, 0x1000, %[xa]\n\t" \
    "ds_read_b128 v[236:239], v252 offset:16384\n\t" \
    "s_waitcnt lgkmcnt(6)\n\t" \
    "v_mfma_f32_16x16x32_bf16 v[208:211], v[32:35], v[240:243], v[208:211]\n\t" \
    "s_waitcnt lgkmcnt(5)\n\t" \
    "v_mfma_f32_16x16x32_bf16 v[208:211], v[36:39], v[224:227], v[208:211]\n\t" \
    "v_mfma_f32_16x16x32_bf16 v[204:207], v[24:27], v[240:243], v[204:207]\n\t" \
    "v_mfma_f32_16x16x32_bf16 v[204:207], v[28:31], v[224:227], v[204:207]\n\t" \
    "v_mfma_f32_16x16x32_bf16 v[200:203], v[16:19], v[240:243], v[200:203]\n\t" \
    "ds_read_b128 v[240:243], v252 offset:17408\n\t" \
    "v_mfma_f32_16x16x32_bf16 v[200:203], v[20:23], v[224:227], v[200:203]\n\t" \
    "ds_read_b128 v[224:227], v252 offset:0\n\t" \
    "s_waitcnt lgkmcnt(6)\n\t" \
    "v_mfma_f32_16x16x32_bf16 v[212:215], v[16:19], v[228:231], v[212:215]\n\t" \
    "s_waitcnt lgkmcnt(3)\n\t" \
    "v_mfma_f32_16x16x32_bf16 v[212:215], v[20:23], v[232:235], v[212:215]\n\t" \
    "v_mfma_f32_16x16x32_bf16 v[216:219], v[24:27], v[228:231], v[216:219]\n\t" \
    "v_mfma_f32_16x16x32_bf16 v[216:219], v[28:31], v[232:235], v[216:219]\n\t" \
    "v_mfma_f32_16x16x32_bf16 v[220:223], v[32:35], v[228:231], v[220:223]\n\t" \
    "ds_read_b128 v[228:231], v252 offset:1024\n\t" \
    "v_mfma_f32_16x16x32_bf16 v[220:223], v[36:39], v[232:235], v[220:223]\n\t" \
    "v_xor_b32 v252, 0x1800, %[xa]\n\t" \
    "ds_read_b128 v[232:235], v252 offset:0\n\t" \
    "s_waitcnt lgkmcnt(4)\n\t" \
    "v_mfma_f32_16x16x32_bf16 v[220:223], v[48:51], v[236:239], v[220:223]\n\t" \
    "s_waitcnt lgkmcnt(3)\n\t" \
    "v_mfma_f32_16x16x32_bf16 v[220:223], v[52:55], v[240:243], v[220:223]\n\t" \
    "v_mfma_f32_16x16x32_bf16 v[216:219], v[40:43], v[236:239], v[216:219]\n\t" \
    "v_mfma_f32_16x16x32_bf16 v[216:219], v[44:47], v[240:243], v[216:219]\n\t" \
    "v_mfma_f32_16x16x32_bf16 v[212:215], v[244:247], v[236:239], v[212:215]\n\t" \
    "ds_read_b128 v[236:239], v252 offset:1024\n\t" \
    "v_mfma_f32_16x16x32_bf16 v[212:215], v[248:251], v[240:243], v[212:215]\n\t" \
    "ds_read_b128 v[240:243], v252 offset:16384\n\t" \
    "s_waitcnt lgkmcnt(4)\n\t" \
    "v_mfma_f32_16x16x32_bf16 v[200:203], v[244:247], v[224:227], v[200:203]\n\t" \
    "ds_read_b128 v[244:247], %[wl] offset:8192\n\t" \
    "s_waitcnt lgkmcnt(4)\n\t" \
    "v_mfma_f32_16x16x32_bf16 v[200:203], v[248:251], v[228:231], v[200:203]\n\t" \
    "ds_read_b128 v[248:251], %[wl] offset:9216\n\t" \
    "v_mfma_f32_16x16x32_bf16 v[204:207], v[40:43], v[224:227], v[204:207]\n\t" \
    "v_mfma_f32_16x16x32_bf16 v[204:207], v[44:47], v[228:231], v[204:207]\n\t" \
    "v_mfma_f32_16x16x32_bf16 v[208:211], v[48:51], v[224:227], v[208:211]\n\t" \
    "ds_read_b128 v[224:227], v252 offset:17408\n\t" \
    "v_mfma_f32_16x16x32_bf16 v[208:211], v[52:55], v[228:231], v[208:211]\n\t" \
    "v_and_b32 v252, 0x3ff, %[xa]\n\t" \
    "ds_read_b128 v[228:231], v252 offset:45056\n\t" \
    "s_waitcnt lgkmcnt(6)\n\t" \
    "v_mfma_f32_16x16x32_bf16 v[208:211], v[72:75], v[232:235], v[208:211]\n\t" \
    "s_waitcnt lgkmcnt(5)\n\t" \
    "v_mfma_f32_16x16x32_bf16 v[208:211], v[76:79], v[236:239], v[208:211]\n\t" \
    "v_mfma_f32_16x16x32_bf16 v[204:207], v[64:67], v[232:235], v[204:207]\n\t" \
    "v_mfma_f32_16x16x32_bf16 v[204:207], v[68:71], v[236:239], v[204:207]\n\t" \
    "v_mfma_f32_16x16x32_bf16 v[200:203], v[56:59], v[232:235], v[200:203]\n\t" \
    "ds_read_b128 v[232:235], v252 offset:46080\n\t" \
    "v_mfma_f32_16x16x32_bf16 v[200:203], v[60:63], v[236:239], v[200:203]\n\t" \
    "ds_read_b128 v[236:239], v252 offset:40960\n\t" \
    "s_waitcnt lgkmcnt(6)\n\t" \
    "v_mfma_f32_16x16x32_bf16 v[212:215], v[56:59], v[240:243], v[212:215]\n\t" \
    "s_waitcnt lgkmcnt(3)\n\t" \
    "v_mfma_f32_16x16x32_bf16 v[212:215], v[60:63], v[224:227], v[212:215]\n\t" \
    "v_mfma_f32_16x16x32_bf16 v[216:219], v[64:67], v[240:243], v[216:219]\n\t" \
    "v_mfma_f32_16x16x32_bf16 v[216:219], v[68:71], v[224:227], v[216:219]\n\t" \
    "v_mfma_f32_16x16x32_bf16 v[220:223], v[72:75], v[240:243], v[220:223]\n\t" \
    "ds_read_b128 v[240:243], v252 offset:41984\n\t" \
    "v_mfma_f32_16x16x32_bf16 v[220:223], v[76:79], v[224:227], v[220:223]\n\t" \
    "v_xor_b32 v252, 0x2000, %[xa]\n\t" \
    "ds_read_b128 v[224:227], v252 offset:0\n\t" \
    "s_waitcnt lgkmcnt(4)\n\t" \
    "v_mfma_f32_16x16x32_bf16 v[220:223], v[168:171], v[228:231], v[220:223]\n\t" \
    "s_waitcnt lgkmcnt(3)\n\t" \
    "v_mfma_f32_16x16x32_bf16 v[220:223], v[172:175], v[232:235], v[220:223]\n\t" \
    "v_mfma_f32_16x16x32_bf16 v[216:219], v[160:163], v[228:231], v[216:219]\n\t" \
    "v_mfma_f32_16x16x32_bf16 v[216:219], v[164:167], v[232:235], v[216:219]\n\t" \
    "v_mfma_f32_16x16x32_bf16 v[212:215], v[244:247], v[228:231], v[212:215]\n\t" \
    "ds_read_b128 v[228:231], v252 offset:1024\n\t" \
    "v_mfma_f32_16x16x32_bf16 v[212:215], v[248:251], v[232:235], v[212:215]\n\t" \
    "ds_read_b128 v[232:235], v252 offset:16384\n\t" \
    "s_waitcnt lgkmcnt(4)\n\t" \
    "v_mfma_f32_16x16x32_bf16 v[200:203], v[244:247], v[236:239], v[200:203]\n\t" \
    "ds_read_b128 v[244:247], %[wl] offset:4096\n\t" \
    "s_waitcnt lgkmcnt(4)\n\t" \
    "v_mfma_f32_16x16x32_bf16 v[200:203], v[248:251], v[240:243], v[200:203]\n\t" \
    "ds_read_b128 v[248:251], %[wl] offset:5120\n\t" \
    "v_mfma_f32_16x16x32_bf16 v[204:207], v[160:163], v[236:239], v[204:207]\n\t" \
    "v_mfma_f32_16x16x32_bf16 v[204:207], v[164:167], v[240:243], v[204:207]\n\t" \
    "v_mfma_f32_16x16x32_bf16 v[208:211], v[168:171], v[236:239], v[208:211]\n\t" \
    "ds_read_b128 v[236:239], v252 offset:17408\n\t" \
    "v_mfma_f32_16x16x32_bf16 v[208:211], v[172:175], v[240:243], v[208:211]\n\t" \
    "v_xor_b32 v252, 0x2800, %[xa]\n\t" \
    "ds_read_b128 v[240:243], v252 offset:16384\n\t" \
    "s_waitcnt lgkmcnt(6)\n\t" \
    "v_mfma_f32_16x16x32_bf16 v[208:211], v[88:91], v[224:227], v[208:211]\n\t" \
    "s_waitcnt lgkmcnt(5)\n\t" \
    "v_mfma_f32_16x16x32_bf16 v[208:211], v[92:95], v[228:231], v[208:211]\n\t" \
    "v_mfma_f32_16x16x32_bf16 v[204:207], v[80:83], v[224:227], v[204:207]\n\t" \
    "v_mfma_f32_16x16x32_bf16 v[204:207], v[84:87], v[228:231], v[204:207]\n\t" \
    "s_waitcnt lgkmcnt(3)\n\t" \
    "v_mfma_f32_16x16x32_bf16 v[200:203], v[244:247], v[224:227], v[200:203]\n\t" \
    "ds_read_b128 v[224:227], v252 offset:17408\n\t" \
    "s_waitcnt lgkmcnt(3)\n\t" \
    "v_mfma_f32_16x16x32_bf16 v[200:203], v[248:251], v[228:231], v[200:203]\n\t" \
    "ds_read_b128 v[228:231], v252 offset:0\n\t" \
    "v_mfma_f32_16x16x32_bf16 v[212:215], v[244:247], v[232:235], v[212:215]\n\t" \
    "ds_read_b128 v[244:247], %[wl] offset:6144\n\t" \
    "s_waitcnt lgkmcnt(4)\n\t" \
    "v_mfma_f32_16x16x32_bf16 v[212:215], v[248:251], v[236:239], v[212:215]\n\t" \
    "ds_read_b128 v[248:251], %[wl] offset:7168\n\t" \
    "v_mfma_f32_16x16x32_bf16 v[216:219], v[80:83], v[232:235], v[216:219]\n\t" \
    "v_mfma_f32_16x16x32_bf16 v[216:219], v[84:87], v[236:239], v[216:219]\n\t" \
    "v_mfma_f32_16x16x32_bf16 v[220:223], v[88:91], v[232:235], v[220:223]\n\t" \
    "ds_read_b128 v[232:235], v252 offset:1024\n\t" \
    "v_mfma_f32_16x16x32_bf16 v[220:223], v[92:95], v[236:239], v[220:223]\n\t" \
    "v_xor_b32 v252, 0x3000, %[xa]\n\t" \
    "ds_read_b128 v[236:239], v252 offset:0\n\t" \
    "s_waitcnt lgkmcnt(6)\n\t" \
    "v_mfma_f32_16x16x32_bf16 v[220:223], v[112:115], v[240:243], v[220:223]\n\t" \
    "s_waitcnt lgkmcnt(5)\n\t" \
    "v_mfma_f32_16x16x32_bf16 v[220:223], v[116:119], v[224:227], v[220:223]\n\t" \
    "v_mfma_f32_16x16x32_bf16 v[216:219], v[104:107], v[240:243], v[216:219]\n\t" \
    "v_mfma_f32_16x16x32_bf16 v[216:219], v[108:111], v[224:227], v[216:219]\n\t" \
    "v_mfma_f32_16x16x32_bf16 v[212:215], v[96:99], v[240:243], v[212:215]\n\t" \
    "ds_read_b128 v[240:243], v252 offset:1024\n\t" \
    "v_mfma_f32_16x16x32_bf16 v[212:215], v[100:103], v[224:227], v[212:215]\n\t" \
    "ds_read_b128 v[224:227], v252 offset:16384\n\t" \
    "s_waitcnt lgkmcnt(6)\n\t" \
    "v_mfma_f32_16x16x32_bf16 v[200:203], v[96:99], v[228:231], v[200:203]\n\t" \
    "s_waitcnt lgkmcnt(3)\n\t" \
    "v_mfma_f32_16x16x32_bf16 v[200:203], v[100:103], v[232:235], v[200:203]\n\t" \
    "v_mfma_f32_16x16x32_bf16 v[204:207], v[104:107], v[228:231], v[204:207]\n\t" \
    "v_mfma_f32_16x16x32_bf16 v[204:207], v[108:111], v[232:235], v[204:207]\n\t" \
    "v_mfma_f32_16x16x32_bf16 v[208:211], v[112:115], v[228:231], v[208:211]\n\t" \
    "ds_read_b128 v[228:231], v252 offset:17408\n\t" \
    "v_mfma_f32_16x16x32_bf16 v[208:211], v[116:119], v[232:235], v[208:211]\n\t" \
    "v_xor_b32 v252, 0x3800, %[xa]\n\t" \
    "ds_read_b128 v[232:235], v252 offset:16384\n\t" \
    "s_waitcnt lgkmcnt(4)\n\t" \
    "v_mfma_f32_16x16x32_bf16 v[208:211], v[128:131], v[236:239], v[208:211]\n\t" \
    "s_waitcnt lgkmcnt(3)\n\t" \
    "v_mfma_f32_16x16x32_bf16 v[208:211], v[132:135], v[240:243], v[208:211]\n\t" \
    "v_mfma_f32_16x16x32_bf16 v[204:207], v[120:123], v[236:239], v[204:207]\n\t" \
    "v_mfma_f32_16x16x32_bf16 v[204:207], v[124:127], v[240:243], v[204:207]\n\t" \
    "v_mfma_f32_16x16x32_bf16 v[200:203], v[244:247], v[236:239], v[200:203]\n\t" \
    "ds_read_b128 v[236:239], v252 offset:17408\n\t" \
    "v_mfma_f32_16x16x32_bf16 v[200:203], v[248:251], v[240:243], v[200:203]\n\t" \
    "ds_read_b128 v[240:243], v252 offset:0\n\t" \
    "s_waitcnt lgkmcnt(4)\n\t" \
    "v_mfma_f32_16x16x32_bf16 v[212:215], v[244:247], v[224:227], v[212:215]\n\t" \
    "s_waitcnt lgkmcnt(3)\n\t" \
    "v_mfma_f32_16x16x32_bf16 v[212:215], v[248:251], v[228:231], v[212:215]\n\t" \
    "v_mfma_f32_16x16x32_bf16 v[216:219], v[120:123], v[224:227], v[216:219]\n\t" \
    "v_mfma_f32_16x16x32_bf16 v[216:219], v[124:127], v[228:231], v[216:219]\n\t" \
    "v_mfma_f32_16x16x32_bf16 v[220:223], v[128:131], v[224:227], v[220:223]\n\t" \
    "ds_read_b128 v[224:227], v252 offset:1024\n\t" \
    "v_mfma_f32_16x16x32_bf16 v[220:223], v[132:135], v[228:231], v[220:223]\n\t" \
    "v_and_b32 v252, 0x3ff, %[xa]\n\t" \
    "ds_read_b128 v[228:231], v252 offset:43008\n\t" \
    "s_waitcnt lgkmcnt(4)\n\t" \
    "v_mfma_f32_16x16x32_bf16 v[220:223], v[152:155], v[232:235], v[220:223]\n\t" \
    "s_waitcnt lgkmcnt(3)\n\t" \
    "v_mfma_f32_16x16x32_bf16 v[220:223], v[156:159], v[236:239], v[220:223]\n\t" \
    "v_mfma_f32_16x16x32_bf16 v[216:219], v[144:147], v[232:235], v[216:219]\n\t" \
    "v_mfma_f32_16x16x32_bf16 v[216:219], v[148:151], v[236:239], v[216:219]\n\t" \
    "v_mfma_f32_16x16x32_bf16 v[212:215], v[136:139], v[232:235], v[212:215]\n\t" \
    "ds_read_b128 v[232:235], v252 offset:44032\n\t" \
    "v_mfma_f32_16x16x32_bf16 v[212:215], v[140:143], v[236:239], v[212:215]\n\t" \
    "ds_read_b128 v[236:239], v252 offset:47104\n\t" \
    "s_waitcnt lgkmcnt(4)\n\t" \
    "v_mfma_f32_16x16x32_bf16 v[200:203], v[136:139], v[240:243], v[200:203]\n\t" \
    "s_waitcnt lgkmcnt(3)\n\t" \
    "v_mfma_f32_16x16x32_bf16 v[200:203], v[140:143], v[224:227], v[200:203]\n\t" \
    "v_mfma_f32_16x16x32_bf16 v[204:207], v[144:147], v[240:243], v[204:207]\n\t" \
    "v_mfma_f32_16x16x32_bf16 v[204:207], v[148:151], v[224:227], v[204:207]\n\t" \
    "v_mfma_f32_16x16x32_bf16 v[208:211], v[152:155], v[240:243], v[208:211]\n\t" \
    "ds_read_b128 v[240:243], v252 offset:48128\n\t" \
    "v_mfma_f32_16x16x32_bf16 v[208:211], v[156:159], v[224:227], v[208:211]\n\t" \
    "s_waitcnt lgkmcnt(3)\n\t" \
    "v_mfma_f32_16x16x32_bf16 v[208:211], v[192:195], v[228:231], v[208:211]\n\t" \
    "v_add_u32 v252, %[qs], v252\n\t" \
    "s_waitcnt lgkmcnt(2)\n\t" \
    "v_mfma_f32_16x16x32_bf16 v[208:211], v[196:199], v[232:235], v[208:211]\n\t" \
    "v_mfma_f32_16x16x32_bf16 v[204:207], v[184:187], v[228:231], v[204:207]\n\t" \
    "v_mfma_f32_16x16x32_bf16 v[204:207], v[188:191], v[232:235], v[204:207]\n\t" \
    "v_mfma_f32_16x16x32_bf16 v[200:203], v[176:179], v[228:231], v[200:203]\n\t" \
    "v_mfma_f32_16x16x32_bf16 v[200:203], v[180:183], v[232:235], v[200:203]\n\t" \
    "s_waitcnt lgkmcnt(1)\n\t" \
    "v_mfma_f32_16x16x32_bf16 v[212:215], v[176:179], v[236:239], v[212:215]\n\t" \
    "v_cvt_pk_bf16_f32 v208, v208, v209\n\t" \
    "v_cvt_pk_bf16_f32 v209, v210, v211\n\t" \
    "s_waitcnt lgkmcnt(0)\n\t" \
    "v_mfma_f32_16x16x32_bf16 v[212:215], v[180:183], v[240:243], v[212:215]\n\t" \
    "ds_write_b64 v252, v[208:209] offset:2048\n\t" \
    "v_cvt_pk_bf16_f32 v204, v204, v205\n\t" \
    "v_mfma_f32_16x16x32_bf16 v[216:219], v[184:187], v[236:239], v[216:219]\n\t" \
    "v_cvt_pk_bf16_f32 v205, v206, v207\n\t" \
    "ds_write_b64 v252, v[204:205] offset:1024\n\t" \
    "v_mfma_f32_16x16x32_bf16 v[216:219], v[188:191], v[240:243], v[216:219]\n\t" \
    "v_cvt_pk_bf16_f32 v200, v200, v201\n\t" \
    "v_cvt_pk_bf16_f32 v201, v202, v203\n\t" \
    "v_mfma_f32_16x16x32_bf16 v[220:223], v[192:195], v[236:239], v[220:223]\n\t" \
    "ds_write_b64 v252, v[200:201] offset:0\n\t" \
    "v_mfma_f32_16x16x32_bf16 v[220:223], v[196:199], v[240:243], v[220:223]\n\t" \
    "v_cvt_pk_bf16_f32 v212, v212, v213\n\t" \
    "v_cvt_pk_bf16_f32 v213, v214, v215\n\t" \
    "ds_write_b64 v252, v[212:213] offset:6144\n\t" \
    "v_cvt_pk_bf16_f32 v216, v216, v217\n\t" \
    "v_cvt_pk_bf16_f32 v217, v218, v219\n\t" \
    "ds_write_b64 v252, v[216:217] offset:7168\n\t" \
    "s_nop 1\n\t" \
    "v_cvt_pk_bf16_f32 v220, v220, v221\n\t" \
    "v_cvt_pk_bf16_f32 v221, v222, v223\n\t" \
    "ds_write_b64 v252, v[220:221] offset:8192\n\t" \
    "s_waitcnt lgkmcnt(0)\n\t"
#define ASM_HT_LO \
    "s_branch .Lh0%=\n\t" \
    ".Lhs%=:\n\t" \
    "s_cmp_lg_u32 %[go], 0\n\t" \
    "s_cbranch_scc1 .Lhu%=\n\t" \
    ".Lht%=:\n\t" \
    "v_lshlrev_b32 v204, 3, %[l16]\n\t" \
    "v_and_b32 v204, 0x780, v204\n\t" \
    "buffer_load_dword v204, v204, %[rr], %[fs] offen sc1\n\t" \
    "v_bfe_u32 v205, %[l16], 4, 1\n\t" \
    "v_sub_u32 v205, %[u1], v205\n\t" \
    "v_lshrrev_b32 v205, 1, v205\n\t" \
    "s_waitcnt vmcnt(0)\n\t" \
    "v_or_b32 v204, %[fl], v204\n\t" \
    "v_cmp_gt_u32 vcc, v204, v205\n\t" \
    "s_lshr_b32 %[a], vcc_lo, %[m]\n\t" \
    "s_and_b32 %[a], %[a], 0x5555\n\t" \
    "s_cmp_eq_u32 %[a], 0x5555\n\t" \
    "s_cbranch_scc1 .Lhv%=\n\t" \
    HSLEEP \
    "s_sub_u32 %[n], %[n], 1\n\t" \
    "s_cmp_gt_i32 %[n], 0\n\t" \
    "s_cbranch_scc1 .Lht%=\n\t" \
    ".Lhv%=:\n\t" \
    "s_mov_b32 m0, %[e0]\n\t" \
    "s_nop 0\n\t" \
    "global_load_lds_dwordx4 %[l16], %[c0] offset:0 sc1\n\t" \
    "global_load_lds_dwordx4 %[l16], %[c0] offset:1024 sc1\n\t" \
    "global_load_lds_dwordx4 %[l16], %[c0] offset:2048 sc1\n\t" \
    "global_load_lds_dwordx4 %[l16], %[c0] offset:3072 sc1\n\t" \
    "s_mov_b32 m0, %[e1]\n\t" \
    "s_nop 0\n\t" \
    "global_load_lds_dwordx4 %[l16], %[c1] sc1\n\t" \
    ".Lhu%=:\n\t" \
    "s_waitcnt vmcnt(0) lgkmcnt(0)\n\t" \
    "s_barrier \n\t" \
    "ds_read_b128 v[212:215], %[xa] offset:0\n\t" \
    "ds_read_b128 v[216:219], %[xa] offset:1024\n\t" \
    "v_xor_b32 v252, 0x800, %[xa]\n\t" \
    "ds_read_b128 v[220:223], v252 offset:0\n\t" \
    "ds_read_b128 v[224:227], v252 offset:1024\n\t" \
    "ds_read_b128 v[244:247], %[wl] offset:0\n\t" \
    "ds_read_b128 v[248:251], %[wl] offset:1024\n\t" \
    "v_xor_b32 v252, 0x1000, %[xa]\n\t" \
    "ds_read_b128 v[228:231], v252 offset:0\n\t" \
    "ds_read_b128 v[232:235], v252 offset:1024\n\t" \
    "v_xor_b32 v252, 0x1800, %[xa]\n\t" \
    "ds_read_b128 v[236:239], v252 offset:0\n\t" \
    "ds_read_b128 v[240:243], v252 offset:1024\n\t" \
    "s_branch .Lhg%=\n\t" \
    ".Lh0%=:\n\t" \
    "v_mov_b32 v252, %[gw]\n\t" \
    "s_waitcnt vmcnt(0) lgkmcnt(0)\n\t" \
    "s_barrier \n\t" \
    "ds_read_b128 v[200:203], v252\n\t" \
    "ds_read_b128 v[212:215], %[xa] offset:0\n\t" \
    "ds_read_b128 v[216:219], %[xa] offset:1024\n\t" \
    "v_xor_b32 v252, 0x800, %[xa]\n\t" \
    "ds_read_b128 v[220:223], v252 offset:0\n\t" \
    "ds_read_b128 v[224:227], v252 offset:1024\n\t" \
    "ds_read_b128 v[244:247], %[wl] offset:0\n\t" \
    "ds_read_b128 v[248:251], %[wl] offset:1024\n\t" \
    "v_xor_b32 v252, 0x1000, %[xa]\n\t" \
    "ds_read_b128 v[228:231], v252 offset:0\n\t" \
    "ds_read_b128 v[232:235], v252 offset:1024\n\t" \
    "v_xor_b32 v252, 0x1800, %[xa]\n\t" \
    "ds_read_b128 v[236:239], v252 offset:0\n\t" \
    "ds_read_b128 v[240:243], v252 offset:1024\n\t" \
    "s_waitcnt lgkmcnt(10)\n\t" \
    "v_and_b32 v200, v200, v201\n\t" \
    "v_and_b32 v202, v202, v203\n\t" \
    "v_and_b32 v200, v200, v202\n\t" \
    "v_cmp_eq_u32 vcc, 0, v200\n\t" \
    "s_cbranch_vccnz .Lhs%=\n\t" \
    ".Lhg%=:\n\t" \
    "s_mov_b32 %[go], %[nx]\n\t" \
    "s_waitcnt lgkmcnt(9)\n\t" \
    "v_mfma_f32_16x16x32_bf16 v[208:211], v[8:11], v[212:215], 0\n\t" \
    "s_waitcnt lgkmcnt(8)\n\t" \
    "v_mfma_f32_16x16x32_bf16 v[208:211], v[12:15], v[216:219], v[208:211]\n\t" \
    "s_waitcnt lgkmcnt(7)\n\t" \
    "v_mfma_f32_16x16x32_bf16 v[208:211], v[32:35], v[220:223], v[208:211]\n\t" \
    "s_waitcnt lgkmcnt(6)\n\t" \
    "v_mfma_f32_16x16x32_bf16 v[208:211], v[36:39], v[224:227], v[208:211]\n\t" \
    "s_waitcnt lgkmcnt(5)\n\t" \
    "v_mfma_f32_16x16x32_bf16 v[200:203], v[244:247], v[212:215], 0\n\t" \
    "ds_read_b128 v[244:247], %[wl] offset:2048\n\t" \
    "s_waitcnt lgkmcnt(5)\n\t" \
    "v_mfma_f32_16x16x32_bf16 v[200:203], v[248:251], v[216:219], v[200:203]\n\t" \
    "ds_read_b128 v[248:251], %[wl] offset:3072\n\t" \
    "v_mfma_f32_16x16x32_bf16 v[200:203], v[16:19], v[220:223], v[200:203]\n\t" \
    "v_mfma_f32_16x16x32_bf16 v[200:203], v[20:23], v[224:227], v[200:203]\n\t" \
    "v_mfma_f32_16x16x32_bf16 v[204:207], v[0:3], v[212:215], 0\n\t" \
    "v_xor_b32 v252, 0x2000, %[xa]\n\t" \
    "ds_read_b128 v[212:215], v252 offset:0\n\t" \
    "v_mfma_f32_16x16x32_bf16 v[204:207], v[4:7], v[216:219], v[204:207]\n\t" \
    "ds_read_b128 v[216:219], v252 offset:1024\n\t" \
    "v_mfma_f32_16x16x32_bf16 v[204:207], v[24:27], v[220:223], v[204:207]\n\t" \
    "v_xor_b32 v252, 0x2800, %[xa]\n\t" \
    "ds_read_b128 v[220:223], v252 offset:0\n\t" \
    "v_mfma_f32_16x16x32_bf16 v[204:207], v[28:31], v[224:227], v[204:207]\n\t" \
    "ds_read_b128 v[224:227], v252 offset:1024\n\t" \
    "s_cmp_eq_u32 %[go], 0\n\t" \
    "s_cbranch_scc1 .Lhn%=\n\t" \
    "s_mov_b32 m0, %[d0]\n\t" \
    "s_nop 0\n\t" \
    "global_load_lds_dwordx4 %[l16], %[g0] offset:0 sc1\n\t" \
    "global_load_lds_dwordx4 %[l16], %[g0] offset:1024 sc1\n\t" \
    "global_load_lds_dwordx4 %[l16], %[g0] offset:2048 sc1\n\t" \
    "global_load_lds_dwordx4 %[l16], %[g0] offset:3072 sc1\n\t" \
    "s_mov_b32 m0, %[d1]\n\t" \
    "s_nop 0\n\t" \
    "global_load_lds_dwordx4 %[l16], %[g1] sc1\n\t" \
    ".Lhn%=:\n\t" \
    "s_waitcnt lgkmcnt(9)\n\t" \
    "v_mfma_f32_16x16x32_bf16 v[204:207], v[40:43], v[228:231], v[204:207]\n\t" \
    "s_waitcnt lgkmcnt(8)\n\t" \
    "v_mfma_f32_16x16x32_bf16 v[204:207], v[44:47], v[232:235], v[204:207]\n\t" \
    "s_waitcnt lgkmcnt(7)\n\t" \
    "v_mfma_f32_16x16x32_bf16 v[204:207], v[64:67], v[236:239], v[204:207]\n\t" \
    "s_waitcnt lgkmcnt(6)\n\t" \
    "v_mfma_f32_16x16x32_bf16 v[204:207], v[68:71], v[240:243], v[204:207]\n\t" \
    "s_waitcnt lgkmcnt(5)\n\t" \
    "v_mfma_f32_16x16x32_bf16 v[200:203], v[244:247], v[228:231], v[200:203]\n\t" \
    "ds_read_b128 v[244:247], %[wl] offset:4096\n\t" \
    "s_waitcnt lgkmcnt(5)\n\t" \
    "v_mfma_f32_16x16x32_bf16 v[200:203], v[248:251], v[232:235], v[200:203]\n\t" \
    "ds_read_b128 v[248:251], %[wl] offset:5120\n\t" \
    "v_mfma_f32_16x16x32_bf16 v[200:203], v[56:59], v[236:239], v[200:203]\n\t" \
    "v_mfma_f32_16x16x32_bf16 v[200:203], v[60:63], v[240:243], v[200:203]\n\t" \
    "v_mfma_f32_16x16x32_bf16 v[208:211], v[48:51], v[228:231], v[208:211]\n\t" \
    "v_xor_b32 v252, 0x3000, %[xa]\n\t" \
    "ds_read_b128 v[228:231], v252 offset:0\n\t" \
    "v_mfma_f32_16x16x32_bf16 v[208:211], v[52:55], v[232:235], v[208:211]\n\t" \
    "ds_read_b128 v[232:235], v252 offset:1024\n\t" \
    "v_mfma_f32_16x16x32_bf16 v[208:211], v[72:75], v[236:239], v[208:211]\n\t" \
    "v_xor_b32 v252, 0x3800, %[xa]\n\t" \
    "ds_read_b128 v[236:239], v252 offset:0\n\t" \
    "v_mfma_f32_16x16x32_bf16 v[208:211], v[76:79], v[240:243], v[208:211]\n\t" \
    "ds_read_b128 v[240:243], v252 offset:1024\n\t" \
    "s_waitcnt lgkmcnt(9)\n\t" \
    "v_mfma_f32_16x16x32_bf16 v[208:211], v[88:91], v[212:215], v[208:211]\n\t" \
    "s_waitcnt lgkmcnt(8)\n\t" \
    "v_mfma_f32_16x16x32_bf16 v[208:211], v[92:95], v[216:219], v[208:211]\n\t" \
    "s_waitcnt lgkmcnt(7)\n\t" \
    "v_mfma_f32_16x16x32_bf16 v[208:211], v[112:115], v[220:223], v[208:211]\n\t" \
    "s_waitcnt lgkmcnt(6)\n\t" \
    "v_mfma_f32_16x16x32_bf16 v[208:211], v[116:119], v[224:227], v[208:211]\n\t" \
    "s_waitcnt lgkmcnt(5)\n\t" \
    "v_mfma_f32_16x16x32_bf16 v[200:203], v[244:247], v[212:215], v[200:203]\n\t" \
    "ds_read_b128 v[244:247], %[wl] offset:6144\n\t" \
    "s_waitcnt lgkmcnt(5)\n\t" \
    "v_mfma_f32_16x16x32_bf16 v[200:203], v[248:251], v[216:219], v[200:203]\n\t" \
    "ds_read_b128 v[248:251], %[wl] offset:7168\n\t" \
    "v_mfma_f32_16x16x32_bf16 v[200:203], v[96:99], v[220:223], v[200:203]\n\t" \
    "v_mfma_f32_16x16x32_bf16 v[200:203], v[100:103], v[224:227], v[200:203]\n\t" \
    "v_mfma_f32_16x16x32_bf16 v[204:207], v[80:83], v[212:215], v[204:207]\n\t" \
    "v_add_u32 v252, %[xb], %[l16]\n\t" \
    "ds_read_b128 v[212:215], v252 offset:0\n\t" \
    "v_mfma_f32_16x16x32_bf16 v[204:207], v[84:87], v[216:219], v[204:207]\n\t" \
    "ds_read_b128 v[216:219], v252 offset:1024\n\t" \
    "v_mfma_f32_16x16x32_bf16 v[204:207], v[104:107], v[220:223], v[204:207]\n\t" \
    "ds_read_b128 v[220:223], v252 offset:2048\n\t" \
    "v_mfma_f32_16x16x32_bf16 v[204:207], v[108:111], v[224:227], v[204:207]\n\t" \
    "ds_read_b128 v[224:227], v252 offset:3072\n\t" \
    "s_waitcnt lgkmcnt(9)\n\t" \
    "v_mfma_f32_16x16x32_bf16 v[204:207], v[120:123], v[228:231], v[204:207]\n\t" \
    "s_waitcnt lgkmcnt(8)\n\t" \
    "v_mfma_f32_16x16x32_bf16 v[204:207], v[124:127], v[232:235], v[204:207]\n\t" \
    "s_waitcnt lgkmcnt(7)\n\t" \
    "v_mfma_f32_16x16x32_bf16 v[204:207], v[144:147], v[236:239], v[204:207]\n\t" \
    "s_waitcnt lgkmcnt(6)\n\t" \
    "v_mfma_f32_16x16x32_bf16 v[204:207], v[148:151], v[240:243], v[204:207]\n\t" \
    "s_waitcnt lgkmcnt(5)\n\t" \
    "v_mfma_f32_16x16x32_bf16 v[200:203], v[244:247], v[228:231], v[200:203]\n\t" \
    "ds_read_b128 v[244:247], %[wl] offset:8192\n\t" \
    "s_waitcnt lgkmcnt(5)\n\t" \
    "v_mfma_f32_16x16x32_bf16 v[200:203], v[248:251], v[232:235], v[200:203]\n\t" \
    "ds_read_b128 v[248:251], %[wl] offset:9216\n\t" \
    "v_mfma_f32_16x16x32_bf16 v[200:203], v[136:139], v[236:239], v[200:203]\n\t" \
    "v_mfma_f32_16x16x32_bf16 v[200:203], v[140:143], v[240:243], v[200:203]\n\t" \
    "v_mfma_f32_16x16x32_bf16 v[208:211], v[128:131], v[228:231], v[208:211]\n\t" \
    "v_mfma_f32_16x16x32_bf16 v[208:211], v[132:135], v[232:235], v[208:211]\n\t" \
    "v_mfma_f32_16x16x32_bf16 v[208:211], v[152:155], v[236:239], v[208:211]\n\t" \
    "v_mfma_f32_16x16x32_bf16 v[208:211], v[156:159], v[240:243], v[208:211]\n\t" \
    "v_add_u32 v252, %[qd], %[l16]\n\t" \
    "s_waitcnt lgkmcnt(5)\n\t" \
    "v_mfma_f32_16x16x32_bf16 v[208:211], v[168:171], v[212:215], v[208:211]\n\t" \
    "s_waitcnt lgkmcnt(4)\n\t" \
    "v_mfma_f32_16x16x32_bf16 v[208:211], v[172:175], v[216:219], v[208:211]\n\t" \
    "s_waitcnt lgkmcnt(3)\n\t" \
    "v_mfma_f32_16x16x32_bf16 v[208:211], v[192:195], v[220:223], v[208:211]\n\t" \
    "s_waitcnt lgkmcnt(2)\n\t" \
    "v_mfma_f32_16x16x32_bf16 v[208:211], v[196:199], v[224:227], v[208:211]\n\t" \
    "s_waitcnt lgkmcnt(1)\n\t" \
    "v_mfma_f32_16x16x32_bf16 v[200:203], v[244:247], v[212:215], v[200:203]\n\t" \
    "s_waitcnt lgkmcnt(0)\n\t" \
    "v_mfma_f32_16x16x32_bf16 v[200:203], v[248:251], v[216:219], v[200:203]\n\t" \
    "s_nop 1\n\t" \
    "v_cvt_pk_bf16_f32 v208, v208, v209\n\t" \
    "v_mfma_f32_16x16x32_bf16 v[200:203], v[176:179], v[220:223], v[200:203]\n\t" \
    "v_cvt_pk_bf16_f32 v209, v210, v211\n\t" \
    "v_mfma_f32_16x16x32_bf16 v[200:203], v[180:183], v[224:227], v[200:203]\n\t" \
    "ds_write_b64 v252, v[208:209] offset:2048\n\t" \
    "v_mfma_f32_16x16x32_bf16 v[204:207], v[160:163], v[212:215], v[204:207]\n\t" \
    "v_mfma_f32_16x16x32_bf16 v[204:207], v[164:167], v[216:219], v[204:207]\n\t" \
    "s_nop 0\n\t" \
    "v_cvt_pk_bf16_f32 v200, v200, v201\n\t" \
    "v_mfma_f32_16x16x32_bf16 v[204:207], v[184:187], v[220:223], v[204:207]\n\t" \
    "v_cvt_pk_bf16_f32 v201, v202, v203\n\t" \
    "v_mfma_f32_16x16x32_bf16 v[204:207], v[188:191], v[224:227], v[204:207]\n\t" \
    "ds_write_b64 v252, v[200:201] offset:0\n\t" \
    "s_nop 6\n\t" \
    "v_cvt_pk_bf16_f32 v204, v204, v205\n\t" \
    "v_cvt_pk_bf16_f32 v205, v206, v207\n\t" \
    "ds_write_b64 v252, v[204:205] offset:1024\n\t" \
    "s_mov_b32 vcc_lo, -1\n\t" \
    "s_cmp_lg_u32 %[fl], 0\n\t" \
    "s_cbranch_scc1 .Lhx%=\n\t" \
    "v_lshlrev_b32 v212, 3, %[l16]\n\t" \
    "v_and_b32 v212, 0x780, v212\n\t" \
    "buffer_load_dword v212, v212, %[rr], %[fs] offen sc1\n\t" \
    "v_bfe_u32 v213, %[l16], 4, 1\n\t" \
    "v_sub_u32 v213, %[u2], v213\n\t" \
    "v_lshrrev_b32 v213, 1, v213\n\t" \
    "s_waitcnt vmcnt(0)\n\t" \
    "v_or_b32 v212, %[fl], v212\n\t" \
    "v_cmp_gt_u32 vcc, v212, v213\n\t" \
    ".Lhx%=:\n\t" \
    "s_lshr_b32 %[a], vcc_lo, %[m]\n\t" \
    "s_and_b32 %[a], %[a], 0x5555\n\t" \
    "s_cmp_eq_u32 %[a], %[k2]\n\t" \
    "s_cselect_b32 %[nx], 1, 0\n\t" \
    "s_cmp_lg_u32 %[go], 0\n\t" \
    "s_cbranch_scc1 .Lhw%=\n\t" \
    "s_lshl_b32 %[a], vcc_lo, %[m]\n\t" \
    "s_lshr_b32 %[a], %[a], 1\n\t" \
    "s_and_b32 %[a], %[a], 0x5555\n\t" \
    "s_cmp_eq_u32 %[a], %[k1]\n\t" \
    "s_cbranch_scc0 .Lhw%=\n\t" \
    "s_mov_b32 m0, %[d0]\n\t" \
    "s_nop 0\n\t" \
    "global_load_lds_dwordx4 %[l16], %[g0] offset:0 sc1\n\t" \
    "global_load_lds_dwordx4 %[l16], %[g0] offset:1024 sc1\n\t" \
    "global_load_lds_dwordx4 %[l16], %[g0] offset:2048 sc1\n\t" \
    "global_load_lds_dwordx4 %[l16], %[g0] offset:3072 sc1\n\t" \
    "s_mov_b32 m0, %[d1]\n\t" \
    "s_nop 0\n\t" \
    "global_load_lds_dwordx4 %[l16], %[g1] sc1\n\t" \
    "s_mov_b32 %[go], 1\n\t" \
    ".Lhw%=:\n\t" \
    "v_mov_b32 v212, %[gn]\n\t" \
    "v_mov_b32 v213, %[go]\n\t" \
    "ds_write_b32 v212, v213\n\t"
#define ASM_HT_HI \
    "s_branch .Lh0%=\n\t" \
    ".Lhs%=:\n\t" \
    "s_cmp_eq_u32 %[pb], 0\n\t" \
    "s_cbranch_scc1 .Lht%=\n\t" \
    "v_lshrrev_b32 v204, 2, %[l16]\n\t" \
    "v_mov_b32 v205, %[tv]\n\t" \
    "s_mov_b32 exec_hi, 0\n\t" \
    "s_waitcnt vmcnt(0)\n\t" \
    "buffer_store_dword v205, v204, %[rr], %[fp] offen sc1\n\t" \
    "s_mov_b32 exec_hi, -1\n\t" \
    "s_mov_b32 %[pb], 0\n\t" \
    ".Lht%=:\n\t" \
    "s_cmp_lg_u32 %[end], 0\n\t" \
    "s_cbranch_scc1 .Lhe%=\n\t" \
    "s_barrier \n\t" \
    "s_branch .Lhg%=\n\t" \
    ".Lh0%=:\n\t" \
    "v_mov_b32 v252, %[gw]\n\t" \
    "v_add_u32 v204, %[ro], %[l16]\n\t" \
    "ds_read_b128 v[200:203], v252\n\t" \
    "ds_read_b128 v[212:215], v204 offset:0\n\t" \
    "ds_read_b128 v[216:219], v204 offset:1024\n\t" \
    "ds_read_b128 v[220:223], v204 offset:2048\n\t" \
    "s_waitcnt lgkmcnt(3)\n\t" \
    "v_and_b32 v200, v200, v201\n\t" \
    "v_and_b32 v202, v202, v203\n\t" \
    "v_and_b32 v200, v200, v202\n\t" \
    "v_cmp_eq_u32 vcc, 0, v200\n\t" \
    "s_waitcnt lgkmcnt(2)\n\t" \
    "buffer_store_dwordx4 v[212:215], %[l16], %[rp], %[s0] offen sc1\n\t" \
    "s_waitcnt lgkmcnt(1)\n\t" \
    "buffer_store_dwordx4 v[216:219], %[l16], %[rp], %[s1] offen sc1\n\t" \
    "s_waitcnt lgkmcnt(0)\n\t" \
    "buffer_store_dwordx4 v[220:223], %[l16], %[rp], %[s2] offen sc1\n\t" \
    "s_cbranch_vccnz .Lhs%=\n\t" \
    ".Lhg%=:\n\t" \
    "s_nop 0\n\t" \
    "ds_read_b128 v[212:215], %[xa] offset:0\n\t" \
    "ds_read_b128 v[216:219], %[xa] offset:1024\n\t" \
    "v_xor_b32 v252, 0x800, %[xa]\n\t" \
    "ds_read_b128 v[220:223], v252 offset:0\n\t" \
    "ds_read_b128 v[224:227], v252 offset:1024\n\t" \
    "ds_read_b128 v[244:247], %[wl] offset:0\n\t" \
    "ds_read_b128 v[248:251], %[wl] offset:1024\n\t" \
    "v_xor_b32 v252, 0x1000, %[xa]\n\t" \
    "ds_read_b128 v[228:231], v252 offset:0\n\t" \
    "ds_read_b128 v[232:235], v252 offset:1024\n\t" \
    "v_xor_b32 v252, 0x1800, %[xa]\n\t" \
    "ds_read_b128 v[236:239], v252 offset:0\n\t" \
    "ds_read_b128 v[240:243], v252 offset:1024\n\t" \
    "s_setprio 1\n\t" \
    "s_cmp_eq_u32 %[pb], 0\n\t" \
    "s_cbranch_scc1 .Lhn%=\n\t" \
    "v_mov_b32 v252, %[tv]\n\t" \
    "s_mov_b64 exec, 0xff\n\t" \
    "s_waitcnt vmcnt(0)\n\t" \
    "buffer_store_dword v252, %[l16], %[rr], %[fp] offen sc1\n\t" \
    "s_mov_b64 exec, -1\n\t" \
    ".Lhn%=:\n\t" \
    "s_waitcnt lgkmcnt(9)\n\t" \
    "v_mfma_f32_16x16x32_bf16 v[208:211], v[8:11], v[212:215], 0\n\t" \
    "s_waitcnt lgkmcnt(8)\n\t" \
    "v_mfma_f32_16x16x32_bf16 v[208:211], v[12:15], v[216:219], v[208:211]\n\t" \
    "s_waitcnt lgkmcnt(7)\n\t" \
    "v_mfma_f32_16x16x32_bf16 v[208:211], v[32:35], v[220:223], v[208:211]\n\t" \
    "s_waitcnt lgkmcnt(6)\n\t" \
    "v_mfma_f32_16x16x32_bf16 v[208:211], v[36:39], v[224:227], v[208:211]\n\t" \
    "s_waitcnt lgkmcnt(5)\n\t" \
    "v_mfma_f32_16x16x32_bf16 v[200:203], v[244:247], v[212:215], 0\n\t" \
    "ds_read_b128 v[244:247], %[wl] offset:2048\n\t" \
    "s_waitcnt lgkmcnt(5)\n\t" \
    "v_mfma_f32_16x16x32_bf16 v[200:203], v[248:251], v[216:219], v[200:203]\n\t" \
    "ds_read_b128 v[248:251], %[wl] offset:3072\n\t" \
    "v_mfma_f32_16x16x32_bf16 v[200:203], v[16:19], v[220:223], v[200:203]\n\t" \
    "v_mfma_f32_16x16x32_bf16 v[200:203], v[20:23], v[224:227], v[200:203]\n\t" \
    "v_mfma_f32_16x16x32_bf16 v[204:207], v[0:3], v[212:215], 0\n\t" \
    "v_xor_b32 v252, 0x2000, %[xa]\n\t" \
    "ds_read_b128 v[212:215], v252 offset:0\n\t" \
    "v_mfma_f32_16x16x32_bf16 v[204:207], v[4:7], v[216:219], v[204:207]\n\t" \
    "ds_read_b128 v[216:219], v252 offset:1024\n\t" \
    "v_mfma_f32_16x16x32_bf16 v[204:207], v[24:27], v[220:223], v[204:207]\n\t" \
    "v_xor_b32 v252, 0x2800, %[xa]\n\t" \
    "ds_read_b128 v[220:223], v252 offset:0\n\t" \
    "v_mfma_f32_16x16x32_bf16 v[204:207], v[28:31], v[224:227], v[204:207]\n\t" \
    "ds_read_b128 v[224:227], v252 offset:1024\n\t" \
    "s_waitcnt lgkmcnt(9)\n\t" \
    "v_mfma_f32_16x16x32_bf16 v[204:207], v[40:43], v[228:231], v[204:207]\n\t" \
    "s_waitcnt lgkmcnt(8)\n\t" \
    "v_mfma_f32_16x16x32_bf16 v[204:207], v[44:47], v[232:235], v[204:207]\n\t" \
    "s_waitcnt lgkmcnt(7)\n\t" \
    "v_mfma_f32_16x16x32_bf16 v[204:207], v[64:67], v[236:239], v[204:207]\n\t" \
    "s_waitcnt lgkmcnt(6)\n\t" \
    "v_mfma_f32_16x16x32_bf16 v[204:207], v[68:71], v[240:243], v[204:207]\n\t" \
    "s_waitcnt lgkmcnt(5)\n\t" \
    "v_mfma_f32_16x16x32_bf16 v[200:203], v[244:247], v[228:231], v[200:203]\n\t" \
    "ds_read_b128 v[244:247], %[wl] offset:4096\n\t" \
    "s_waitcnt lgkmcnt(5)\n\t" \
    "v_mfma_f32_16x16x32_bf16 v[200:203], v[248:251], v[232:235], v[200:203]\n\t" \
    "ds_read_b128 v[248:251], %[wl] offset:5120\n\t" \
    "v_mfma_f32_16x16x32_bf16 v[200:203], v[56:59], v[236:239], v[200:203]\n\t" \
    "v_mfma_f32_16x16x32_bf16 v[200:203], v[60:63], v[240:243], v[200:203]\n\t" \
    "v_mfma_f32_16x16x32_bf16 v[208:211], v[48:51], v[228:231], v[208:211]\n\t" \
    "v_xor_b32 v252, 0x3000, %[xa]\n\t" \
    "ds_read_b128 v[228:231], v252 offset:0\n\t" \
    "v_mfma_f32_16x16x32_bf16 v[208:211], v[52:55], v[232:235], v[208:211]\n\t" \
    "ds_read_b128 v[232:235], v252 offset:1024\n\t" \
    "v_mfma_f32_16x16x32_bf16 v[208:211], v[72:75], v[236:239], v[208:211]\n\t" \
    "v_xor_b32 v252, 0x3800, %[xa]\n\t" \
    "ds_read_b128 v[236:239], v252 offset:0\n\t" \
    "v_mfma_f32_16x16x32_bf16 v[208:211], v[76:79], v[240:243], v[208:211]\n\t" \
    "ds_read_b128 v[240:243], v252 offset:1024\n\t" \
    "s_waitcnt lgkmcnt(9)\n\t" \
    "v_mfma_f32_16x16x32_bf16 v[208:211], v[88:91], v[212:215], v[208:211]\n\t" \
    "s_waitcnt lgkmcnt(8)\n\t" \
    "v_mfma_f32_16x16x32_bf16 v[208:211], v[92:95], v[216:219], v[208:211]\n\t" \
    "s_waitcnt lgkmcnt(7)\n\t" \
    "v_mfma_f32_16x16x32_bf16 v[208:211], v[112:115], v[220:223], v[208:211]\n\t" \
    "s_waitcnt lgkmcnt(6)\n\t" \
    "v_mfma_f32_16x16x32_bf16 v[208:211], v[116:119], v[224:227], v[208:211]\n\t" \
    "s_waitcnt lgkmcnt(5)\n\t" \
    "v_mfma_f32_16x16x32_bf16 v[200:203], v[244:247], v[212:215], v[200:203]\n\t" \
    "ds_read_b128 v[244:247], %[wl] offset:6144\n\t" \
    "s_waitcnt lgkmcnt(5)\n\t" \
    "v_mfma_f32_16x16x32_bf16 v[200:203], v[248:251], v[216:219], v[200:203]\n\t" \
    "ds_read_b128 v[248:251], %[wl] offset:7168\n\t" \
    "v_mfma_f32_16x16x32_bf16 v[200:203], v[96:99], v[220:223], v[200:203]\n\t" \
    "v_mfma_f32_16x16x32_bf16 v[200:203], v[100:103], v[224:227], v[200:203]\n\t" \
    "v_mfma_f32_16x16x32_bf16 v[204:207], v[80:83], v[212:215], v[204:207]\n\t" \
    "v_add_u32 v252, %[xb], %[l16]\n\t" \
    "ds_read_b128 v[212:215], v252 offset:0\n\t" \
    "v_mfma_f32_16x16x32_bf16 v[204:207], v[84:87], v[216:219], v[204:207]\n\t" \
    "ds_read_b128 v[216:219], v252 offset:1024\n\t" \
    "v_mfma_f32_16x16x32_bf16 v[204:207], v[104:107], v[220:223], v[204:207]\n\t" \
    "ds_read_b128 v[220:223], v252 offset:2048\n\t" \
    "v_mfma_f32_16x16x32_bf16 v[204:207], v[108:111], v[224:227], v[204:207]\n\t" \
    "ds_read_b128 v[224:227], v252 offset:3072\n\t" \
    "s_waitcnt lgkmcnt(9)\n\t" \
    "v_mfma_f32_16x16x32_bf16 v[204:207], v[120:123], v[228:231], v[204:207]\n\t" \
    "s_waitcnt lgkmcnt(8)\n\t" \
    "v_mfma_f32_16x16x32_bf16 v[204:207], v[124:127], v[232:235], v[204:207]\n\t" \
    "s_waitcnt lgkmcnt(7)\n\t" \
    "v_mfma_f32_16x16x32_bf16 v[204:207], v[144:147], v[236:239], v[204:207]\n\t" \
    "s_waitcnt lgkmcnt(6)\n\t" \
    "v_mfma_f32_16x16x32_bf16 v[204:207], v[148:151], v[240:243], v[204:207]\n\t" \
    "s_waitcnt lgkmcnt(5)\n\t" \
    "v_mfma_f32_16x16x32_bf16 v[200:203], v[244:247], v[228:231], v[200:203]\n\t" \
    "ds_read_b128 v[244:247], %[wl] offset:8192\n\t" \
    "s_waitcnt lgkmcnt(5)\n\t" \
    "v_mfma_f32_16x16x32_bf16 v[200:203], v[248:251], v[232:235], v[200:203]\n\t" \
    "ds_read_b128 v[248:251], %[wl] offset:9216\n\t" \
    "v_mfma_f32_16x16x32_bf16 v[200:203], v[136:139], v[236:239], v[200:203]\n\t" \
    "v_mfma_f32_16x16x32_bf16 v[200:203], v[140:143], v[240:243], v[200:203]\n\t" \
    "v_mfma_f32_16x16x32_bf16 v[208:211], v[128:131], v[228:231], v[208:211]\n\t" \
    "v_mfma_f32_16x16x32_bf16 v[208:211], v[132:135], v[232:235], v[208:211]\n\t" \
    "v_mfma_f32_16x16x32_bf16 v[208:211], v[152:155], v[236:239], v[208:211]\n\t" \
    "v_mfma_f32_16x16x32_bf16 v[208:211], v[156:159], v[240:243], v[208:211]\n\t" \
    "v_add_u32 v252, %[qd], %[l16]\n\t" \
    "s_waitcnt lgkmcnt(5)\n\t" \
    "v_mfma_f32_16x16x32_bf16 v[208:211], v[168:171], v[212:215], v[208:211]\n\t" \
    "s_waitcnt lgkmcnt(4)\n\t" \
    "v_mfma_f32_16x16x32_bf16 v[208:211], v[172:175], v[216:219], v[208:211]\n\t" \
    "s_waitcnt lgkmcnt(3)\n\t" \
    "v_mfma_f32_16x16x32_bf16 v[208:211], v[192:195], v[220:223], v[208:211]\n\t" \
    "s_waitcnt lgkmcnt(2)\n\t" \
    "v_mfma_f32_16x16x32_bf16 v[208:211], v[196:199], v[224:227], v[208:211]\n\t" \
    "s_waitcnt lgkmcnt(1)\n\t" \
    "v_mfma_f32_16x16x32_bf16 v[200:203], v[244:247], v[212:215], v[200:203]\n\t" \
    "s_waitcnt lgkmcnt(0)\n\t" \
    "v_mfma_f32_16x16x32_bf16 v[200:203], v[248:251], v[216:219], v[200:203]\n\t" \
    "s_nop 1\n\t" \
    "v_cvt_pk_bf16_f32 v208, v208, v209\n\t" \
    "v_mfma_f32_16x16x32_bf16 v[200:203], v[176:179], v[220:223], v[200:203]\n\t" \
    "v_cvt_pk_bf16_f32 v209, v210, v211\n\t" \
    "v_mfma_f32_16x16x32_bf16 v[200:203], v[180:183], v[224:227], v[200:203]\n\t" \
    "ds_write_b64 v252, v[208:209] offset:2048\n\t" \
    "v_mfma_f32_16x16x32_bf16 v[204:207], v[160:163], v[212:215], v[204:207]\n\t" \
    "v_mfma_f32_16x16x32_bf16 v[204:207], v[164:167], v[216:219], v[204:207]\n\t" \
    "s_nop 0\n\t" \
    "v_cvt_pk_bf16_f32 v200, v200, v201\n\t" \
    "v_mfma_f32_16x16x32_bf16 v[204:207], v[184:187], v[220:223], v[204:207]\n\t" \
    "v_cvt_pk_bf16_f32 v201, v202, v203\n\t" \
    "v_mfma_f32_16x16x32_bf16 v[204:207], v[188:191], v[224:227], v[204:207]\n\t" \
    "ds_write_b64 v252, v[200:201] offset:0\n\t" \
    "s_nop 6\n\t" \
    "v_cvt_pk_bf16_f32 v204, v204, v205\n\t" \
    "v_cvt_pk_bf16_f32 v205, v206, v207\n\t" \
    "ds_write_b64 v252, v[204:205] offset:1024\n\t" \
    "s_setprio 3\n\t" \
    "s_waitcnt lgkmcnt(0)\n\t" \
    "s_barrier \n\t" \
    ".Lhe%=:\n\t"
#define ASM_CRIT1_3 \
    "s_branch .Lc0%=\n\t" \
    ".Lcr%=:\n\t" \
    "s_cmp_eq_u32 %[n], 0\n\t" \
    "s_cbranch_scc1 .Lcb%=\n\t" \
    "s_sub_u32 %[n], %[n], 1\n\t" \
    "v_max_u32 v243, v208, v209\n\t" \
    "v_max3_u32 v243, v210, v211, v243\n\t" \
    "v_cmp_eq_u32 vcc, -1, v243\n\t" \
    "s_cbranch_vccz .Lcs0_%=\n\t" \
    "buffer_load_dwordx4 v[208:211], %[ab], %[rs], %[s0] offen sc1\n\t" \
    ".Lcs0_%=:\n\t" \
    "v_max_u32 v243, v236, v237\n\t" \
    "v_max3_u32 v243, v238, v239, v243\n\t" \
    "v_cmp_eq_u32 vcc, -1, v243\n\t" \
    "s_cbranch_vccz .Lcs1_%=\n\t" \
    "buffer_load_dwordx4 v[236:239], %[ab], %[rs], %[s1] offen sc1\n\t" \
    ".Lcs1_%=:\n\t" \
    "v_max_u32 v243, v220, v221\n\t" \
    "v_max3_u32 v243, v222, v223, v243\n\t" \
    "v_cmp_eq_u32 vcc, -1, v243\n\t" \
    "s_cbranch_vccz .Lca%=\n\t" \
    "buffer_load_dwordx4 v[220:223], %[ab], %[rs], %[s2] offen sc1\n\t" \
    "s_branch .Lca%=\n\t" \
    ".Lc0%=:\n\t" \
    "buffer_load_dwordx4 v[208:211], %[ab], %[rs], %[s0] offen sc1\n\t" \
    "buffer_load_dwordx4 v[236:239], %[ab], %[rs], %[s1] offen sc1\n\t" \
    "buffer_load_dwordx4 v[220:223], %[ab], %[rs], %[s2] offen sc1\n\t" \
    "v_bfe_i32 v241, %[ab], 5, 1\n\t" \
    "v_and_b32 v241, 0xaaaa, v241\n\t" \
    "v_or_b32 v241, 0x4444, v241\n\t" \
    "v_xor_b32 v212, 0x400, %[ab]\n\t" \
    "v_xor_b32 v216, 0x800, %[ab]\n\t" \
    "v_xor_b32 v242, 0xc00, %[ab]\n\t" \
    "v_xor_b32 v240, 0x1400, %[ab]\n\t" \
    ".Lca%=:\n\t" \
    "s_waitcnt vmcnt(0)\n\t" \
    "ds_write_b128 %[ab], v[208:211] offset:0\n\t" \
    "ds_write_b128 %[ab], v[236:239] offset:4096\n\t" \
    "ds_write_b128 %[ab], v[220:223] offset:8192\n\t" \
    "v_max_u32 v243, v208, v209\n\t" \
    "v_max3_u32 v243, v210, v211, v243\n\t" \
    "v_max3_u32 v243, v236, v237, v243\n\t" \
    "v_max3_u32 v243, v238, v239, v243\n\t" \
    "v_max3_u32 v243, v220, v221, v243\n\t" \
    "v_max3_u32 v243, v222, v223, v243\n\t" \
    "v_cmp_eq_u32 vcc, -1, v243\n\t" \
    "s_cbranch_vccnz .Lcr%=\n\t" \
    ".Lcb%=:\n\t" \
    PRIUP \
    ASM_ST2 \
    "v_smfmac_f32_16x16x64_bf16 v[224:227], v[208:211], v[200:207], v241\n\t" \
    "ds_read_b128 v[200:203], %[wl] offset:4096\n\t" \
    "ds_read_b128 v[204:207], %[wl] offset:5120\n\t" \
    "v_smfmac_f32_16x16x64_bf16 v[244:247], v[208:211], v[0:7], v241\n\t" \
    "v_smfmac_f32_16x16x64_bf16 v[248:251], v[208:211], v[8:15], v241\n\t" \
    "s_waitcnt lgkmcnt(2)\n\t" \
    "s_barrier\n\t" ASM_ST \
    "ds_read_b128 v[212:215], v212 offset:0\n\t" \
    "ds_read_b128 v[216:219], v216 offset:0\n\t" \
    "ds_read_b128 v[220:223], v242 offset:0\n\t" \
    "ds_read_b128 v[208:211], v240 offset:0\n\t" \
    "v_smfmac_f32_16x16x64_bf16 v[244:247], v[236:239], v[80:87], v241\n\t" \
    "v_xor_b32 v240, 0x1800, %[ab]\n\t" \
    "v_smfmac_f32_16x16x64_bf16 v[248:251], v[236:239], v[88:95], v241\n\t" \
    "s_waitcnt lgkmcnt(4)\n\t" \
    "v_smfmac_f32_16x16x64_bf16 v[224:227], v[236:239], v[200:207], v241\n\t" \
    "ds_read_b128 v[200:203], %[wl] offset:6144\n\t" \
    "ds_read_b128 v[204:207], %[wl] offset:7168\n\t" \
    "ds_read_b128 v[236:239], v240 offset:0\n\t" \
    "s_waitcnt lgkmcnt(6)\n\t" \
    "v_smfmac_f32_16x16x64_bf16 v[224:227], v[212:215], v[16:23], v241\n\t" \
    "v_smfmac_f32_16x16x64_bf16 v[244:247], v[212:215], v[24:31], v241\n\t" \
    "v_xor_b32 v240, 0x1c00, %[ab]\n\t" \
    "v_smfmac_f32_16x16x64_bf16 v[248:251], v[212:215], v[32:39], v241\n\t" \
    "ds_read_b128 v[212:215], v240 offset:0\n\t" \
    "s_waitcnt lgkmcnt(6)\n\t" \
    "v_smfmac_f32_16x16x64_bf16 v[224:227], v[216:219], v[228:235], v241\n\t" \
    "ds_read_b128 v[228:231], %[wl] offset:8192\n\t" \
    "ds_read_b128 v[232:235], %[wl] offset:9216\n\t" \
    "v_smfmac_f32_16x16x64_bf16 v[244:247], v[216:219], v[40:47], v241\n\t" \
    "v_and_b32 v240, 0x3ff, %[ab]\n\t" \
    "v_smfmac_f32_16x16x64_bf16 v[248:251], v[216:219], v[48:55], v241\n\t" \
    "ds_read_b128 v[216:219], v240 offset:8192\n\t" \
    "s_waitcnt lgkmcnt(8)\n\t" \
    "v_smfmac_f32_16x16x64_bf16 v[224:227], v[220:223], v[56:63], v241\n\t" \
    "v_smfmac_f32_16x16x64_bf16 v[244:247], v[220:223], v[64:71], v241\n\t" \
    "v_smfmac_f32_16x16x64_bf16 v[248:251], v[220:223], v[72:79], v241\n\t" \
    "ds_read_b128 v[220:223], v240 offset:9216\n\t" \
    "s_waitcnt lgkmcnt(8)\n\t" \
    "v_smfmac_f32_16x16x64_bf16 v[244:247], v[208:211], v[104:111], v241\n\t" \
    "s_waitcnt lgkmcnt(5)\n\t" \
    "v_smfmac_f32_16x16x64_bf16 v[244:247], v[236:239], v[120:127], v241\n\t" \
    "s_waitcnt lgkmcnt(4)\n\t" \
    "v_smfmac_f32_16x16x64_bf16 v[244:247], v[212:215], v[144:151], v241\n\t" \
    "s_waitcnt lgkmcnt(1)\n\t" \
    "v_smfmac_f32_16x16x64_bf16 v[244:247], v[216:219], v[160:167], v241\n\t" \
    "s_waitcnt lgkmcnt(0)\n\t" \
    "v_smfmac_f32_16x16x64_bf16 v[244:247], v[220:223], v[184:191], v241\n\t" \
    "v_smfmac_f32_16x16x64_bf16 v[248:251], v[208:211], v[112:119], v241\n\t" \
    "v_smfmac_f32_16x16x64_bf16 v[248:251], v[236:239], v[128:135], v241\n\t" \
    "v_smfmac_f32_16x16x64_bf16 v[248:251], v[212:215], v[152:159], v241\n\t" \
    "v_add_f32 v244, v244, v246\n\t" \
    "v_add_f32 v245, v245, v247\n\t" \
    "v_smfmac_f32_16x16x64_bf16 v[248:251], v[216:219], v[168:175], v241\n\t" \
    "v_cvt_pk_bf16_f32 v244, v244, v245\n\t" \
    "ds_write_b16 %[dst], v244 offset:512\n\t" \
    "v_smfmac_f32_16x16x64_bf16 v[248:251], v[220:223], v[192:199], v241\n\t" \
    "ds_write_b16_d16_hi %[dst], v244 offset:528\n\t" \
    "v_smfmac_f32_16x16x64_bf16 v[224:227], v[208:211], v[96:103], v241\n\t" \
    "v_smfmac_f32_16x16x64_bf16 v[224:227], v[236:239], v[200:207], v241\n\t" \
    "v_smfmac_f32_16x16x64_bf16 v[224:227], v[212:215], v[136:143], v241\n\t" \
    "v_add_f32 v248, v248, v250\n\t" \
    "v_add_f32 v249, v249, v251\n\t" \
    "v_smfmac_f32_16x16x64_bf16 v[224:227], v[216:219], v[228:235], v241\n\t" \
    "v_cvt_pk_bf16_f32 v248, v248, v249\n\t" \
    "ds_write_b16 %[dst], v248 offset:1024\n\t" \
    "v_smfmac_f32_16x16x64_bf16 v[224:227], v[220:223], v[176:183], v241\n\t" \
    "ds_write_b16_d16_hi %[dst], v248 offset:1040\n\t" \
    "s_nop 6\n\t" \
    "v_add_f32 v224, v224, v226\n\t" \
    "v_add_f32 v225, v225, v227\n\t" \
    "v_cvt_pk_bf16_f32 v224, v224, v225\n\t" \
    "ds_write_b16 %[dst], v224 offset:0\n\t" \
    "ds_write_b16_d16_hi %[dst], v224 offset:16\n\t"
#define ASM_CRIT1_2 \
    "s_branch .Lc0%=\n\t" \
    ".Lcr%=:\n\t" \
    "s_cmp_eq_u32 %[n], 0\n\t" \
    "s_cbranch_scc1 .Lcb%=\n\t" \
    "s_sub_u32 %[n], %[n], 1\n\t" \
    "v_max_u32 v243, v208, v209\n\t" \
    "v_max3_u32 v243, v210, v211, v243\n\t" \
    "v_cmp_eq_u32 vcc, -1, v243\n\t" \
    "s_cbranch_vccz .Lcs0_%=\n\t" \
    "buffer_load_dwordx4 v[208:211], %[ab], %[rs], %[s0] offen sc1\n\t" \
    ".Lcs0_%=:\n\t" \
    "v_max_u32 v243, v236, v237\n\t" \
    "v_max3_u32 v243, v238, v239, v243\n\t" \
    "v_cmp_eq_u32 vcc, -1, v243\n\t" \
    "s_cbranch_vccz .Lca%=\n\t" \
    "buffer_load_dwordx4 v[236:239], %[ab], %[rs], %[s1] offen sc1\n\t" \
    "s_branch .Lca%=\n\t" \
    ".Lc0%=:\n\t" \
    "buffer_load_dwordx4 v[208:211], %[ab], %[rs], %[s0] offen sc1\n\t" \
    "buffer_load_dwordx4 v[236:239], %[ab], %[rs], %[s1] offen sc1\n\t" \
    "v_bfe_i32 v241, %[ab], 5, 1\n\t" \
    "v_and_b32 v241, 0xaaaa, v241\n\t" \
    "v_or_b32 v241, 0x4444, v241\n\t" \
    "v_xor_b32 v212, 0x400, %[ab]\n\t" \
    "v_xor_b32 v216, 0x800, %[ab]\n\t" \
    "v_xor_b32 v242, 0xc00, %[ab]\n\t" \
    "v_xor_b32 v240, 0x1400, %[ab]\n\t" \
    ".Lca%=:\n\t" \
    "s_waitcnt vmcnt(0)\n\t" \
    "ds_write_b128 %[ab], v[208:211] offset:0\n\t" \
    "ds_write_b128 %[ab], v[236:239] offset:4096\n\t" \
    "v_max_u32 v243, v208, v209\n\t" \
    "v_max3_u32 v243, v210, v211, v243\n\t" \
    "v_max3_u32 v243, v236, v237, v243\n\t" \
    "v_max3_u32 v243, v238, v239, v243\n\t" \
    "v_cmp_eq_u32 vcc, -1, v243\n\t" \
    "s_cbranch_vccnz .Lcr%=\n\t" \
    ".Lcb%=:\n\t" \
    PRIUP \
    ASM_ST2 \
    "v_smfmac_f32_16x16x64_bf16 v[224:227], v[208:211], v[200:207], v241\n\t" \
    "ds_read_b128 v[200:203], %[wl] offset:4096\n\t" \
    "ds_read_b128 v[204:207], %[wl] offset:5120\n\t" \
    "v_smfmac_f32_16x16x64_bf16 v[244:247], v[208:211], v[0:7], v241\n\t" \
    "v_smfmac_f32_16x16x64_bf16 v[248:251], v[208:211], v[8:15], v241\n\t" \
    "s_waitcnt lgkmcnt(2)\n\t" \
    "s_barrier\n\t" ASM_ST \
    "ds_read_b128 v[212:215], v212 offset:0\n\t" \
    "ds_read_b128 v[216:219], v216 offset:0\n\t" \
    "ds_read_b128 v[220:223], v242 offset:0\n\t" \
    "ds_read_b128 v[208:211], v240 offset:0\n\t" \
    "v_smfmac_f32_16x16x64_bf16 v[244:247], v[236:239], v[80:87], v241\n\t" \
    "v_xor_b32 v240, 0x1800, %[ab]\n\t" \
    "v_smfmac_f32_16x16x64_bf16 v[248:251], v[236:239], v[88:95], v241\n\t" \
    "s_waitcnt lgkmcnt(4)\n\t" \
    "v_smfmac_f32_16x16x64_bf16 v[224:227], v[236:239], v[200:207], v241\n\t" \
    "ds_read_b128 v[200:203], %[wl] offset:6144\n\t" \
    "ds_read_b128 v[204:207], %[wl] offset:7168\n\t" \
    "ds_read_b128 v[236:239], v240 offset:0\n\t" \
    "s_waitcnt lgkmcnt(6)\n\t" \
    "v_smfmac_f32_16x16x64_bf16 v[224:227], v[212:215], v[16:23], v241\n\t" \
    "v_smfmac_f32_16x16x64_bf16 v[244:247], v[212:215], v[24:31], v241\n\t" \
    "v_xor_b32 v240, 0x1c00, %[ab]\n\t" \
    "v_smfmac_f32_16x16x64_bf16 v[248:251], v[212:215], v[32:39], v241\n\t" \
    "ds_read_b128 v[212:215], v240 offset:0\n\t" \
    "s_waitcnt lgkmcnt(6)\n\t" \
    "v_smfmac_f32_16x16x64_bf16 v[224:227], v[216:219], v[228:235], v241\n\t" \
    "ds_read_b128 v[228:231], %[wl] offset:8192\n\t" \
    "ds_read_b128 v[232:235], %[wl] offset:9216\n\t" \
    "v_smfmac_f32_16x16x64_bf16 v[244:247], v[216:219], v[40:47], v241\n\t" \
    "v_and_b32 v240, 0x3ff, %[ab]\n\t" \
    "v_smfmac_f32_16x16x64_bf16 v[248:251], v[216:219], v[48:55], v241\n\t" \
    "ds_read_b128 v[216:219], v240 offset:8192\n\t" \
    "s_waitcnt lgkmcnt(8)\n\t" \
    "v_smfmac_f32_16x16x64_bf16 v[224:227], v[220:223], v[56:63], v241\n\t" \
    "v_smfmac_f32_16x16x64_bf16 v[244:247], v[220:223], v[64:71], v241\n\t" \
    "v_smfmac_f32_16x16x64_bf16 v[248:251], v[220:223], v[72:79], v241\n\t" \
    "ds_read_b128 v[220:223], v240 offset:9216\n\t" \
    "s_waitcnt lgkmcnt(8)\n\t" \
    "v_smfmac_f32_16x16x64_bf16 v[244:247], v[208:211], v[104:111], v241\n\t" \
    "s_waitcnt lgkmcnt(5)\n\t" \
    "v_smfmac_f32_16x16x64_bf16 v[244:247], v[236:239], v[120:127], v241\n\t" \
    "s_waitcnt lgkmcnt(4)\n\t" \
    "v_smfmac_f32_16x16x64_bf16 v[244:247], v[212:215], v[144:151], v241\n\t" \
    "s_waitcnt lgkmcnt(1)\n\t" \
    "v_smfmac_f32_16x16x64_bf16 v[244:247], v[216:219], v[160:167], v241\n\t" \
    "s_waitcnt lgkmcnt(0)\n\t" \
    "v_smfmac_f32_16x16x64_bf16 v[244:247], v[220:223], v[184:191], v241\n\t" \
    "v_smfmac_f32_16x16x64_bf16 v[248:251], v[208:211], v[112:119], v241\n\t" \
    "v_smfmac_f32_16x16x64_bf16 v[248:251], v[236:239], v[128:135], v241\n\t" \
    "v_smfmac_f32_16x16x64_bf16 v[248:251], v[212:215], v[152:159], v241\n\t" \
    "v_add_f32 v244, v244, v246\n\t" \
    "v_add_f32 v245, v245, v247\n\t" \
    "v_smfmac_f32_16x16x64_bf16 v[248:251], v[216:219], v[168:175], v241\n\t" \
    "v_cvt_pk_bf16_f32 v244, v244, v245\n\t" \
    "ds_write_b16 %[dst], v244 offset:512\n\t" \
    "v_smfmac_f32_16x16x64_bf16 v[248:251], v[220:223], v[192:199], v241\n\t" \
    "ds_write_b16_d16_hi %[dst], v244 offset:528\n\t" \
    "v_smfmac_f32_16x16x64_bf16 v[224:227], v[208:211], v[96:103], v241\n\t" \
    "v_smfmac_f32_16x16x64_bf16 v[224:227], v[236:239], v[200:207], v241\n\t" \
    "v_smfmac_f32_16x16x64_bf16 v[224:227], v[212:215], v[136:143], v241\n\t" \
    "v_add_f32 v248, v248, v250\n\t" \
    "v_add_f32 v249, v249, v251\n\t" \
    "v_smfmac_f32_16x16x64_bf16 v[224:227], v[216:219], v[228:235], v241\n\t" \
    "v_cvt_pk_bf16_f32 v248, v248, v249\n\t" \
    "ds_write_b16 %[dst], v248 offset:1024\n\t" \
    "v_smfmac_f32_16x16x64_bf16 v[224:227], v[220:223], v[176:183], v241\n\t" \
    "ds_write_b16_d16_hi %[dst], v248 offset:1040\n\t" \
    "s_nop 6\n\t" \
    "v_add_f32 v224, v224, v226\n\t" \
    "v_add_f32 v225, v225, v227\n\t" \
    "v_cvt_pk_bf16_f32 v224, v224, v225\n\t" \
    "ds_write_b16 %[dst], v224 offset:0\n\t" \
    "ds_write_b16_d16_hi %[dst], v224 offset:16\n\t"
#define ASM_CRIT1_N \
    "v_bfe_i32 v241, %[ab], 5, 1\n\t" \
    "v_and_b32 v241, 0xaaaa, v241\n\t" \
    "v_or_b32 v241, 0x4444, v241\n\t" \
    "v_xor_b32 v212, 0x400, %[ab]\n\t" \
    "v_xor_b32 v216, 0x800, %[ab]\n\t" \
    "v_xor_b32 v242, 0xc00, %[ab]\n\t" \
    "v_xor_b32 v240, 0x1400, %[ab]\n\t" \
    "v_xor_b32 v236, 0x1000, %[ab]\n\t" \
    "s_waitcnt lgkmcnt(0)\n\t" \
    "s_barrier\n\t" ASM_ST \
    "ds_read_b128 v[208:211], %[ab] offset:0\n\t" \
    "ds_read_b128 v[212:215], v212 offset:0\n\t" \
    "ds_read_b128 v[216:219], v216 offset:0\n\t" \
    "ds_read_b128 v[220:223], v242 offset:0\n\t" \
    "ds_read_b128 v[236:239], v236 offset:0\n\t" \
    "s_waitcnt lgkmcnt(4)\n\t" \
    "v_smfmac_f32_16x16x64_bf16 v[224:227], v[208:211], v[200:207], v241\n\t" \
    "ds_read_b128 v[200:203], %[wl] offset:4096\n\t" \
    "ds_read_b128 v[204:207], %[wl] offset:5120\n\t" \
    "v_smfmac_f32_16x16x64_bf16 v[244:247], v[208:211], v[0:7], v241\n\t" \
    "v_smfmac_f32_16x16x64_bf16 v[248:251], v[208:211], v[8:15], v241\n\t" \
    "ds_read_b128 v[208:211], v240 offset:0\n\t" \
    "s_waitcnt lgkmcnt(6)\n\t" \
    "v_smfmac_f32_16x16x64_bf16 v[224:227], v[212:215], v[16:23], v241\n\t" \
    "v_smfmac_f32_16x16x64_bf16 v[244:247], v[212:215], v[24:31], v241\n\t" \
    "v_xor_b32 v240, 0x1800, %[ab]\n\t" \
    "v_smfmac_f32_16x16x64_bf16 v[248:251], v[212:215], v[32:39], v241\n\t" \
    "ds_read_b128 v[212:215], v240 offset:0\n\t" \
    "s_waitcnt lgkmcnt(6)\n\t" \
    "v_smfmac_f32_16x16x64_bf16 v[224:227], v[216:219], v[228:235], v241\n\t" \
    "ds_read_b128 v[228:231], %[wl] offset:6144\n\t" \
    "ds_read_b128 v[232:235], %[wl] offset:7168\n\t" \
    "v_smfmac_f32_16x16x64_bf16 v[244:247], v[216:219], v[40:47], v241\n\t" \
    "v_xor_b32 v240, 0x1c00, %[ab]\n\t" \
    "v_smfmac_f32_16x16x64_bf16 v[248:251], v[216:219], v[48:55], v241\n\t" \
    "ds_read_b128 v[216:219], v240 offset:0\n\t" \
    "s_waitcnt lgkmcnt(8)\n\t" \
    "v_smfmac_f32_16x16x64_bf16 v[224:227], v[220:223], v[56:63], v241\n\t" \
    "v_smfmac_f32_16x16x64_bf16 v[244:247], v[220:223], v[64:71], v241\n\t" \
    "v_and_b32 v240, 0x3ff, %[ab]\n\t" \
    "v_smfmac_f32_16x16x64_bf16 v[248:251], v[220:223], v[72:79], v241\n\t" \
    "ds_read_b128 v[220:223], v240 offset:8192\n\t" \
    "s_waitcnt lgkmcnt(6)\n\t" \
    "v_smfmac_f32_16x16x64_bf16 v[224:227], v[236:239], v[200:207], v241\n\t" \
    "ds_read_b128 v[200:203], %[wl] offset:8192\n\t" \
    "ds_read_b128 v[204:207], %[wl] offset:9216\n\t" \
    "v_smfmac_f32_16x16x64_bf16 v[244:247], v[236:239], v[80:87], v241\n\t" \
    "v_smfmac_f32_16x16x64_bf16 v[248:251], v[236:239], v[88:95], v241\n\t" \
    "ds_read_b128 v[236:239], v240 offset:9216\n\t" \
    "s_waitcnt lgkmcnt(8)\n\t" \
    "v_smfmac_f32_16x16x64_bf16 v[244:247], v[208:211], v[104:111], v241\n\t" \
    "s_waitcnt lgkmcnt(7)\n\t" \
    "v_smfmac_f32_16x16x64_bf16 v[244:247], v[212:215], v[120:127], v241\n\t" \
    "s_waitcnt lgkmcnt(4)\n\t" \
    "v_smfmac_f32_16x16x64_bf16 v[244:247], v[216:219], v[144:151], v241\n\t" \
    "s_waitcnt lgkmcnt(3)\n\t" \
    "v_smfmac_f32_16x16x64_bf16 v[244:247], v[220:223], v[160:167], v241\n\t" \
    "s_waitcnt lgkmcnt(0)\n\t" \
    "v_smfmac_f32_16x16x64_bf16 v[244:247], v[236:239], v[184:191], v241\n\t" \
    "v_smfmac_f32_16x16x64_bf16 v[248:251], v[208:211], v[112:119], v241\n\t" \
    "v_smfmac_f32_16x16x64_bf16 v[248:251], v[212:215], v[128:135], v241\n\t" \
    "v_smfmac_f32_16x16x64_bf16 v[248:251], v[216:219], v[152:159], v241\n\t" \
    "v_add_f32 v244, v244, v246\n\t" \
    "v_add_f32 v245, v245, v247\n\t" \
    "v_smfmac_f32_16x16x64_bf16 v[248:251], v[220:223], v[168:175], v241\n\t" \
    "v_cvt_pk_bf16_f32 v244, v244, v245\n\t" \
    "ds_write_b16 %[dst], v244 offset:512\n\t" \
    "v_smfmac_f32_16x16x64_bf16 v[248:251], v[236:239], v[192:199], v241\n\t" \
    "ds_write_b16_d16_hi %[dst], v244 offset:528\n\t" \
    "v_smfmac_f32_16x16x64_bf16 v[224:227], v[208:211], v[96:103], v241\n\t" \
    "v_smfmac_f32_16x16x64_bf16 v[224:227], v[212:215], v[228:235], v241\n\t" \
    "v_smfmac_f32_16x16x64_bf16 v[224:227], v[216:219], v[136:143], v241\n\t" \
    "v_add_f32 v248, v248, v250\n\t" \
    "v_add_f32 v249, v249, v251\n\t" \
    "v_smfmac_f32_16x16x64_bf16 v[224:227], v[220:223], v[200:207], v241\n\t" \
    "v_cvt_pk_bf16_f32 v248, v248, v249\n\t" \
    "ds_write_b16 %[dst], v248 offset:1024\n\t" \
    "v_smfmac_f32_16x16x64_bf16 v[224:227], v[236:239], v[176:183], v241\n\t" \
    "ds_write_b16_d16_hi %[dst], v248 offset:1040\n\t" \
    "s_nop 6\n\t" \
    "v_add_f32 v224, v224, v226\n\t" \
    "v_add_f32 v225, v225, v227\n\t" \
    "v_cvt_pk_bf16_f32 v224, v224, v225\n\t" \
    "ds_write_b16 %[dst], v224 offset:0\n\t" \
    "ds_write_b16_d16_hi %[dst], v224 offset:16\n\t"
#define ASM_CRIT1_V3 \
    "s_branch .Lc0%=\n\t" \
    ".Lcr%=:\n\t" \
    "s_cmp_eq_u32 %[n], 0\n\t" \
    "s_cbranch_scc1 .Lcb%=\n\t" \
    "s_sub_u32 %[n], %[n], 1\n\t" \
    "v_max_u32 v243, v208, v209\n\t" \
    "v_max3_u32 v243, v210, v211, v243\n\t" \
    "v_cmp_eq_u32 vcc, -1, v243\n\t" \
    "s_cbranch_vccz .Lcs0_%=\n\t" \
    "buffer_load_dwordx4 v[208:211], %[ab], %[rs], %[s0] offen sc1\n\t" \
    ".Lcs0_%=:\n\t" \
    "v_max_u32 v243, v236, v237\n\t" \
    "v_max3_u32 v243, v238, v239, v243\n\t" \
    "v_cmp_eq_u32 vcc, -1, v243\n\t" \
    "s_cbranch_vccz .Lcs1_%=\n\t" \
    "buffer_load_dwordx4 v[236:239], %[ab], %[rs], %[s1] offen sc1\n\t" \
    ".Lcs1_%=:\n\t" \
    "v_max_u32 v243, v220, v221\n\t" \
    "v_max3_u32 v243, v222, v223, v243\n\t" \
    "v_cmp_eq_u32 vcc, -1, v243\n\t" \
    "s_cbranch_vccz .Lca%=\n\t" \
    "buffer_load_dwordx4 v[220:223], %[ab], %[rs], %[s2] offen sc1\n\t" \
    "s_branch .Lca%=\n\t" \
    ".Lc0%=:\n\t" \
    "buffer_load_dwordx4 v[208:211], %[ab], %[rs], %[s0] offen sc1\n\t" \
    "buffer_load_dwordx4 v[236:239], %[ab], %[rs], %[s1] offen sc1\n\t" \
    "buffer_load_dwordx4 v[220:223], %[ab], %[rs], %[s2] offen sc1\n\t" \
    "v_bfe_i32 v241, %[ab], 5, 1\n\t" \
    "v_and_b32 v241, 0xaaaa, v241\n\t" \
    "v_or_b32 v241, 0x4444, v241\n\t" \
    "v_xor_b32 v212, 0x400, %[ab]\n\t" \
    "v_xor_b32 v216, 0x800, %[ab]\n\t" \
    "v_xor_b32 v242, 0xc00, %[ab]\n\t" \
    "v_xor_b32 v240, 0x1400, %[ab]\n\t" \
    ".Lca%=:\n\t" \
    "s_waitcnt vmcnt(0)\n\t" \
    "ds_write_b128 %[ab], v[208:211] offset:0\n\t" \
    "ds_write_b128 %[ab], v[236:239] offset:4096\n\t" \
    "ds_write_b128 %[ab], v[220:223] offset:8192\n\t" \
    "v_max_u32 v243, v208, v209\n\t" \
    "v_max3_u32 v243, v210, v211, v243\n\t" \
    "v_max3_u32 v243, v236, v237, v243\n\t" \
    "v_max3_u32 v243, v238, v239, v243\n\t" \
    "v_max3_u32 v243, v220, v221, v243\n\t" \
    "v_max3_u32 v243, v222, v223, v243\n\t" \
    "v_cmp_eq_u32 vcc, -1, v243\n\t" \
    "s_cbranch_vccnz .Lcr%=\n\t" \
    ".Lcb%=:\n\t" \
    PRIUP \
    ASM_ST2 \
    "v_smfmac_f32_16x16x64_bf16 v[224:227], v[208:211], v[200:207], v241\n\t" \
    "ds_read_b128 v[200:203], %[wl] offset:4096\n\t" \
    "ds_read_b128 v[204:207], %[wl] offset:5120\n\t" \
    "v_smfmac_f32_16x16x64_bf16 v[244:247], v[208:211], v[0:7], v241\n\t" \
    "v_smfmac_f32_16x16x64_bf16 v[248:251], v[208:211], v[8:15], v241\n\t" \
    "s_waitcnt lgkmcnt(2)\n\t" \
    "buffer_load_dword %[fv], %[fv], %[rs], %[fs] offen sc1\n\t" \
    "s_barrier\n\t" ASM_ST \
    "ds_read_b128 v[212:215], v212 offset:0\n\t" \
    "ds_read_b128 v[216:219], v216 offset:0\n\t" \
    "ds_read_b128 v[220:223], v242 offset:0\n\t" \
    "ds_read_b128 v[208:211], v240 offset:0\n\t" \
    "v_smfmac_f32_16x16x64_bf16 v[244:247], v[236:239], v[80:87], v241\n\t" \
    "v_xor_b32 v240, 0x1800, %[ab]\n\t" \
    "v_smfmac_f32_16x16x64_bf16 v[248:251], v[236:239], v[88:95], v241\n\t" \
    "s_waitcnt lgkmcnt(4)\n\t" \
    "v_smfmac_f32_16x16x64_bf16 v[224:227], v[236:239], v[200:207], v241\n\t" \
    "ds_read_b128 v[200:203], %[wl] offset:6144\n\t" \
    "ds_read_b128 v[204:207], %[wl] offset:7168\n\t" \
    "ds_read_b128 v[236:239], v240 offset:0\n\t" \
    "s_waitcnt lgkmcnt(6)\n\t" \
    "v_smfmac_f32_16x16x64_bf16 v[224:227], v[212:215], v[16:23], v241\n\t" \
    "v_smfmac_f32_16x16x64_bf16 v[244:247], v[212:215], v[24:31], v241\n\t" \
    "v_xor_b32 v240, 0x1c00, %[ab]\n\t" \
    "v_smfmac_f32_16x16x64_bf16 v[248:251], v[212:215], v[32:39], v241\n\t" \
    "ds_read_b128 v[212:215], v240 offset:0\n\t" \
    "s_waitcnt lgkmcnt(6)\n\t" \
    "v_smfmac_f32_16x16x64_bf16 v[224:227], v[216:219], v[228:235], v241\n\t" \
    "ds_read_b128 v[228:231], %[wl] offset:8192\n\t" \
    "ds_read_b128 v[232:235], %[wl] offset:9216\n\t" \
    "v_smfmac_f32_16x16x64_bf16 v[244:247], v[216:219], v[40:47], v241\n\t" \
    "v_and_b32 v240, 0x3ff, %[ab]\n\t" \
    "v_smfmac_f32_16x16x64_bf16 v[248:251], v[216:219], v[48:55], v241\n\t" \
    "ds_read_b128 v[216:219], v240 offset:8192\n\t" \
    "s_waitcnt lgkmcnt(8)\n\t" \
    "v_smfmac_f32_16x16x64_bf16 v[224:227], v[220:223], v[56:63], v241\n\t" \
    "v_smfmac_f32_16x16x64_bf16 v[244:247], v[220:223], v[64:71], v241\n\t" \
    "v_smfmac_f32_16x16x64_bf16 v[248:251], v[220:223], v[72:79], v241\n\t" \
    "ds_read_b128 v[220:223], v240 offset:9216\n\t" \
    "s_waitcnt lgkmcnt(8)\n\t" \
    "v_smfmac_f32_16x16x64_bf16 v[244:247], v[208:211], v[104:111], v241\n\t" \
    "s_waitcnt lgkmcnt(5)\n\t" \
    "v_smfmac_f32_16x16x64_bf16 v[244:247], v[236:239], v[120:127], v241\n\t" \
    "s_waitcnt lgkmcnt(4)\n\t" \
    "v_smfmac_f32_16x16x64_bf16 v[244:247], v[212:215], v[144:151], v241\n\t" \
    "s_waitcnt lgkmcnt(1)\n\t" \
    "v_smfmac_f32_16x16x64_bf16 v[244:247], v[216:219], v[160:167], v241\n\t" \
    "s_waitcnt lgkmcnt(0)\n\t" \
    "v_smfmac_f32_16x16x64_bf16 v[244:247], v[220:223], v[184:191], v241\n\t" \
    "v_smfmac_f32_16x16x64_bf16 v[248:251], v[208:211], v[112:119], v241\n\t" \
    "v_smfmac_f32_16x16x64_bf16 v[248:251], v[236:239], v[128:135], v241\n\t" \
    "v_smfmac_f32_16x16x64_bf16 v[248:251], v[212:215], v[152:159], v241\n\t" \
    "v_add_f32 v244, v244, v246\n\t" \
    "v_add_f32 v245, v245, v247\n\t" \
    "v_smfmac_f32_16x16x64_bf16 v[248:251], v[216:219], v[168:175], v241\n\t" \
    "v_cvt_pk_bf16_f32 v244, v244, v245\n\t" \
    "ds_write_b16 %[dst], v244 offset:512\n\t" \
    "v_smfmac_f32_16x16x64_bf16 v[248:251], v[220:223], v[192:199], v241\n\t" \
    "ds_write_b16_d16_hi %[dst], v244 offset:528\n\t" \
    "v_smfmac_f32_16x16x64_bf16 v[224:227], v[208:211], v[96:103], v241\n\t" \
    "v_smfmac_f32_16x16x64_bf16 v[224:227], v[236:239], v[200:207], v241\n\t" \
    "v_smfmac_f32_16x16x64_bf16 v[224:227], v[212:215], v[136:143], v241\n\t" \
    "v_add_f32 v248, v248, v250\n\t" \
    "v_add_f32 v249, v249, v251\n\t" \
    "v_smfmac_f32_16x16x64_bf16 v[224:227], v[216:219], v[228:235], v241\n\t" \
    "v_cvt_pk_bf16_f32 v248, v248, v249\n\t" \
    "ds_write_b16 %[dst], v248 offset:1024\n\t" \
    "v_smfmac_f32_16x16x64_bf16 v[224:227], v[220:223], v[176:183], v241\n\t" \
    "ds_write_b16_d16_hi %[dst], v248 offset:1040\n\t" \
    "s_nop 6\n\t" \
    "v_add_f32 v224, v224, v226\n\t" \
    "v_add_f32 v225, v225, v227\n\t" \
    "v_cvt_pk_bf16_f32 v224, v224, v225\n\t" \
    "ds_write_b16 %[dst], v224 offset:0\n\t" \
    "ds_write_b16_d16_hi %[dst], v224 offset:16\n\t"
#define ASM_CRIT1_V2 \
    "s_branch .Lc0%=\n\t" \
    ".Lcr%=:\n\t" \
    "s_cmp_eq_u32 %[n], 0\n\t" \
    "s_cbranch_scc1 .Lcb%=\n\t" \
    "s_sub_u32 %[n], %[n], 1\n\t" \
    "v_max_u32 v243, v208, v209\n\t" \
    "v_max3_u32 v243, v210, v211, v243\n\t" \
    "v_cmp_eq_u32 vcc, -1, v243\n\t" \
    "s_cbranch_vccz .Lcs0_%=\n\t" \
    "buffer_load_dwordx4 v[208:211], %[ab], %[rs], %[s0] offen sc1\n\t" \
    ".Lcs0_%=:\n\t" \
    "v_max_u32 v243, v236, v237\n\t" \
    "v_max3_u32 v243, v238, v239, v243\n\t" \
    "v_cmp_eq_u32 vcc, -1, v243\n\t" \
    "s_cbranch_vccz .Lca%=\n\t" \
    "buffer_load_dwordx4 v[236:239], %[ab], %[rs], %[s1] offen sc1\n\t" \
    "s_branch .Lca%=\n\t" \
    ".Lc0%=:\n\t" \
    "buffer_load_dwordx4 v[208:211], %[ab], %[rs], %[s0] offen sc1\n\t" \
    "buffer_load_dwordx4 v[236:239], %[ab], %[rs], %[s1] offen sc1\n\t" \
    "v_bfe_i32 v241, %[ab], 5, 1\n\t" \
    "v_and_b32 v241, 0xaaaa, v241\n\t" \
    "v_or_b32 v241, 0x4444, v241\n\t" \
    "v_xor_b32 v212, 0x400, %[ab]\n\t" \
    "v_xor_b32 v216, 0x800, %[ab]\n\t" \
    "v_xor_b32 v242, 0xc00, %[ab]\n\t" \
    "v_xor_b32 v240, 0x1400, %[ab]\n\t" \
    ".Lca%=:\n\t" \
    "s_waitcnt vmcnt(0)\n\t" \
    "ds_write_b128 %[ab], v[208:211] offset:0\n\t" \
    "ds_write_b128 %[ab], v[236:239] offset:4096\n\t" \
    "v_max_u32 v243, v208, v209\n\t" \
    "v_max3_u32 v243, v210, v211, v243\n\t" \
    "v_max3_u32 v243, v236, v237, v243\n\t" \
    "v_max3_u32 v243, v238, v239, v243\n\t" \
    "v_cmp_eq_u32 vcc, -1, v243\n\t" \
    "s_cbranch_vccnz .Lcr%=\n\t" \
    ".Lcb%=:\n\t" \
    PRIUP \
    ASM_ST2 \
    "v_smfmac_f32_16x16x64_bf16 v[224:227], v[208:211], v[200:207], v241\n\t" \
    "ds_read_b128 v[200:203], %[wl] offset:4096\n\t" \
    "ds_read_b128 v[204:207], %[wl] offset:5120\n\t" \
    "v_smfmac_f32_16x16x64_bf16 v[244:247], v[208:211], v[0:7], v241\n\t" \
    "v_smfmac_f32_16x16x64_bf16 v[248:251], v[208:211], v[8:15], v241\n\t" \
    "s_waitcnt lgkmcnt(2)\n\t" \
    "buffer_load_dword %[fv], %[fv], %[rs], %[fs] offen sc1\n\t" \
    "s_barrier\n\t" ASM_ST \
    "ds_read_b128 v[212:215], v212 offset:0\n\t" \
    "ds_read_b128 v[216:219], v216 offset:0\n\t" \
    "ds_read_b128 v[220:223], v242 offset:0\n\t" \
    "ds_read_b128 v[208:211], v240 offset:0\n\t" \
    "v_smfmac_f32_16x16x64_bf16 v[244:247], v[236:239], v[80:87], v241\n\t" \
    "v_xor_b32 v240, 0x1800, %[ab]\n\t" \
    "v_smfmac_f32_16x16x64_bf16 v[248:251], v[236:239], v[88:95], v241\n\t" \
    "s_waitcnt lgkmcnt(4)\n\t" \
    "v_smfmac_f32_16x16x64_bf16 v[224:227], v[236:239], v[200:207], v241\n\t" \
    "ds_read_b128 v[200:203], %[wl] offset:6144\n\t" \
    "ds_read_b128 v[204:207], %[wl] offset:7168\n\t" \
    "ds_read_b128 v[236:239], v240 offset:0\n\t" \
    "s_waitcnt lgkmcnt(6)\n\t" \
    "v_smfmac_f32_16x16x64_bf16 v[224:227], v[212:215], v[16:23], v241\n\t" \
    "v_smfmac_f32_16x16x64_bf16 v[244:247], v[212:215], v[24:31], v241\n\t" \
    "v_xor_b32 v240, 0x1c00, %[ab]\n\t" \
    "v_smfmac_f32_16x16x64_bf16 v[248:251], v[212:215], v[32:39], v241\n\t" \
    "ds_read_b128 v[212:215], v240 offset:0\n\t" \
    "s_waitcnt lgkmcnt(6)\n\t" \
    "v_smfmac_f32_16x16x64_bf16 v[224:227], v[216:219], v[228:235], v241\n\t" \
    "ds_read_b128 v[228:231], %[wl] offset:8192\n\t" \
    "ds_read_b128 v[232:235], %[wl] offset:9216\n\t" \
    "v_smfmac_f32_16x16x64_bf16 v[244:247], v[216:219], v[40:47], v241\n\t" \
    "v_and_b32 v240, 0x3ff, %[ab]\n\t" \
    "v_smfmac_f32_16x16x64_bf16 v[248:251], v[216:219], v[48:55], v241\n\t" \
    "ds_read_b128 v[216:219], v240 offset:8192\n\t" \
    "s_waitcnt lgkmcnt(8)\n\t" \
    "v_smfmac_f32_16x16x64_bf16 v[224:227], v[220:223], v[56:63], v241\n\t" \
    "v_smfmac_f32_16x16x64_bf16 v[244:247], v[220:223], v[64:71], v241\n\t" \
    "v_smfmac_f32_16x16x64_bf16 v[248:251], v[220:223], v[72:79], v241\n\t" \
    "ds_read_b128 v[220:223], v240 offset:9216\n\t" \
    "s_waitcnt lgkmcnt(8)\n\t" \
    "v_smfmac_f32_16x16x64_bf16 v[244:247], v[208:211], v[104:111], v241\n\t" \
    "s_waitcnt lgkmcnt(5)\n\t" \
    "v_smfmac_f32_16x16x64_bf16 v[244:247], v[236:239], v[120:127], v241\n\t" \
    "s_waitcnt lgkmcnt(4)\n\t" \
    "v_smfmac_f32_16x16x64_bf16 v[244:247], v[212:215], v[144:151], v241\n\t" \
    "s_waitcnt lgkmcnt(1)\n\t" \
    "v_smfmac_f32_16x16x64_bf16 v[244:247], v[216:219], v[160:167], v241\n\t" \
    "s_waitcnt lgkmcnt(0)\n\t" \
    "v_smfmac_f32_16x16x64_bf16 v[244:247], v[220:223], v[184:191], v241\n\t" \
    "v_smfmac_f32_16x16x64_bf16 v[248:251], v[208:211], v[112:119], v241\n\t" \
    "v_smfmac_f32_16x16x64_bf16 v[248:251], v[236:239], v[128:135], v241\n\t" \
    "v_smfmac_f32_16x16x64_bf16 v[248:251], v[212:215], v[152:159], v241\n\t" \
    "v_add_f32 v244, v244, v246\n\t" \
    "v_add_f32 v245, v245, v247\n\t" \
    "v_smfmac_f32_16x16x64_bf16 v[248:251], v[216:219], v[168:175], v241\n\t" \
    "v_cvt_pk_bf16_f32 v244, v244, v245\n\t" \
    "ds_write_b16 %[dst], v244 offset:512\n\t" \
    "v_smfmac_f32_16x16x64_bf16 v[248:251], v[220:223], v[192:199], v241\n\t" \
    "ds_write_b16_d16_hi %[dst], v244 offset:528\n\t" \
    "v_smfmac_f32_16x16x64_bf16 v[224:227], v[208:211], v[96:103], v241\n\t" \
    "v_smfmac_f32_16x16x64_bf16 v[224:227], v[236:239], v[200:207], v241\n\t" \
    "v_smfmac_f32_16x16x64_bf16 v[224:227], v[212:215], v[136:143], v241\n\t" \
    "v_add_f32 v248, v248, v250\n\t" \
    "v_add_f32 v249, v249, v251\n\t" \
    "v_smfmac_f32_16x16x64_bf16 v[224:227], v[216:219], v[228:235], v241\n\t" \
    "v_cvt_pk_bf16_f32 v248, v248, v249\n\t" \
    "ds_write_b16 %[dst], v248 offset:1024\n\t" \
    "v_smfmac_f32_16x16x64_bf16 v[224:227], v[220:223], v[176:183], v241\n\t" \
    "ds_write_b16_d16_hi %[dst], v248 offset:1040\n\t" \
    "s_nop 6\n\t" \
    "v_add_f32 v224, v224, v226\n\t" \
    "v_add_f32 v225, v225, v227\n\t" \
    "v_cvt_pk_bf16_f32 v224, v224, v225\n\t" \
    "ds_write_b16 %[dst], v224 offset:0\n\t" \
    "ds_write_b16_d16_hi %[dst], v224 offset:16\n\t"
#define ASM_VOX \
    "s_waitcnt lgkmcnt(0)\n\t" \
    "ds_read_b128 v[208:211], %[va] offset:2048\n\t" \
    "ds_read_b128 v[212:215], %[va] offset:3072\n\t" \
    "ds_read_b128 v[216:219], %[va] offset:4096\n\t" \
    "ds_read_b128 v[220:223], %[va] offset:5120\n\t" \
    "ds_read_b128 v[200:203], %[va] offset:0\n\t" \
    "ds_read_b128 v[204:207], %[va] offset:1024\n\t" \
    "ds_read_b128 v[236:239], %[wl] offset:0\n\t" \
    "ds_read_b128 v[240:243], %[wl] offset:1024\n\t" \
    "s_waitcnt lgkmcnt(7)\n\t" \
    "v_mfma_f32_16x16x32_bf16 v[244:247], v[208:211], v[0:3], 0\n\t" \
    "s_waitcnt lgkmcnt(6)\n\t" \
    "v_mfma_f32_16x16x32_bf16 v[244:247], v[212:215], v[4:7], v[244:247]\n\t" \
    "s_waitcnt lgkmcnt(5)\n\t" \
    "v_mfma_f32_16x16x32_bf16 v[244:247], v[216:219], v[8:11], v[244:247]\n\t" \
    "s_waitcnt lgkmcnt(4)\n\t" \
    "v_mfma_f32_16x16x32_bf16 v[244:247], v[220:223], v[12:15], v[244:247]\n\t" \
    "s_waitcnt lgkmcnt(1)\n\t" \
    "v_mfma_f32_16x16x32_bf16 v[244:247], v[200:203], v[236:239], v[244:247]\n\t" \
    "ds_read_b128 v[236:239], %[wl] offset:2048\n\t" \
    "s_waitcnt lgkmcnt(1)\n\t" \
    "v_mfma_f32_16x16x32_bf16 v[244:247], v[204:207], v[240:243], v[244:247]\n\t" \
    "ds_read_b128 v[240:243], %[wl] offset:3072\n\t" \
    "v_mfma_f32_16x16x32_bf16 v[248:251], v[208:211], v[40:43], 0\n\t" \
    "v_mfma_f32_16x16x32_bf16 v[248:251], v[212:215], v[44:47], v[248:251]\n\t" \
    "v_mfma_f32_16x16x32_bf16 v[248:251], v[216:219], v[48:51], v[248:251]\n\t" \
    "v_cvt_pk_bf16_f32 v224, v244, v245\n\t" \
    "v_mfma_f32_16x16x32_bf16 v[248:251], v[220:223], v[52:55], v[248:251]\n\t" \
    "v_cvt_pk_bf16_f32 v225, v246, v247\n\t" \
    "s_waitcnt lgkmcnt(1)\n\t" \
    "v_mfma_f32_16x16x32_bf16 v[248:251], v[200:203], v[236:239], v[248:251]\n\t" \
    "ds_read_b128 v[236:239], %[wl] offset:4096\n\t" \
    "s_waitcnt lgkmcnt(1)\n\t" \
    "v_mfma_f32_16x16x32_bf16 v[248:251], v[204:207], v[240:243], v[248:251]\n\t" \
    "ds_read_b128 v[240:243], %[wl] offset:5120\n\t" \
    "v_mfma_f32_16x16x32_bf16 v[244:247], v[208:211], v[80:83], 0\n\t" \
    "v_mfma_f32_16x16x32_bf16 v[244:247], v[212:215], v[84:87], v[244:247]\n\t" \
    "v_mfma_f32_16x16x32_bf16 v[244:247], v[216:219], v[88:91], v[244:247]\n\t" \
    "v_cvt_pk_bf16_f32 v226, v248, v249\n\t" \
    "v_mfma_f32_16x16x32_bf16 v[244:247], v[220:223], v[92:95], v[244:247]\n\t" \
    "v_cvt_pk_bf16_f32 v227, v250, v251\n\t" \
    "s_waitcnt lgkmcnt(1)\n\t" \
    "v_mfma_f32_16x16x32_bf16 v[244:247], v[200:203], v[236:239], v[244:247]\n\t" \
    "ds_read_b128 v[236:239], %[wl] offset:6144\n\t" \
    "s_waitcnt lgkmcnt(1)\n\t" \
    "v_mfma_f32_16x16x32_bf16 v[244:247], v[204:207], v[240:243], v[244:247]\n\t" \
    "ds_read_b128 v[240:243], %[wl] offset:7168\n\t" \
    "v_mfma_f32_16x16x32_bf16 v[248:251], v[208:211], v[120:123], 0\n\t" \
    "v_mfma_f32_16x16x32_bf16 v[248:251], v[212:215], v[124:127], v[248:251]\n\t" \
    "v_mfma_f32_16x16x32_bf16 v[248:251], v[216:219], v[128:131], v[248:251]\n\t" \
    "v_cvt_pk_bf16_f32 v228, v244, v245\n\t" \
    "v_mfma_f32_16x16x32_bf16 v[248:251], v[220:223], v[132:135], v[248:251]\n\t" \
    "v_cvt_pk_bf16_f32 v229, v246, v247\n\t" \
    "s_waitcnt lgkmcnt(1)\n\t" \
    "v_mfma_f32_16x16x32_bf16 v[248:251], v[200:203], v[236:239], v[248:251]\n\t" \
    "ds_read_b128 v[236:239], %[wl] offset:8192\n\t" \
    "s_waitcnt lgkmcnt(1)\n\t" \
    "v_mfma_f32_16x16x32_bf16 v[248:251], v[204:207], v[240:243], v[248:251]\n\t" \
    "ds_read_b128 v[240:243], %[wl] offset:9216\n\t" \
    "v_mfma_f32_16x16x32_bf16 v[244:247], v[208:211], v[160:163], 0\n\t" \
    "v_mfma_f32_16x16x32_bf16 v[244:247], v[212:215], v[164:167], v[244:247]\n\t" \
    "v_mfma_f32_16x16x32_bf16 v[244:247], v[216:219], v[168:171], v[244:247]\n\t" \
    "v_cvt_pk_bf16_f32 v230, v248, v249\n\t" \
    "v_mfma_f32_16x16x32_bf16 v[244:247], v[220:223], v[172:175], v[244:247]\n\t" \
    "v_cvt_pk_bf16_f32 v231, v250, v251\n\t" \
    "s_waitcnt lgkmcnt(1)\n\t" \
    "v_mfma_f32_16x16x32_bf16 v[244:247], v[200:203], v[236:239], v[244:247]\n\t" \
    "s_waitcnt lgkmcnt(0)\n\t" \
    "v_mfma_f32_16x16x32_bf16 v[244:247], v[204:207], v[240:243], v[244:247]\n\t" \
    "v_mfma_f32_16x16x32_bf16 v[248:251], v[208:211], v[24:27], 0\n\t" \
    "v_mfma_f32_16x16x32_bf16 v[248:251], v[212:215], v[28:31], v[248:251]\n\t" \
    "v_mfma_f32_16x16x32_bf16 v[248:251], v[216:219], v[32:35], v[248:251]\n\t" \
    "v_cvt_pk_bf16_f32 v232, v244, v245\n\t" \
    "v_mfma_f32_16x16x32_bf16 v[248:251], v[220:223], v[36:39], v[248:251]\n\t" \
    "v_cvt_pk_bf16_f32 v233, v246, v247\n\t" \
    "v_mfma_f32_16x16x32_bf16 v[248:251], v[200:203], v[16:19], v[248:251]\n\t" \
    "v_mfma_f32_16x16x32_bf16 v[248:251], v[204:207], v[20:23], v[248:251]\n\t" \
    "v_mfma_f32_16x16x32_bf16 v[244:247], v[208:211], v[64:67], 0\n\t" \
    "v_mfma_f32_16x16x32_bf16 v[244:247], v[212:215], v[68:71], v[244:247]\n\t" \
    "v_mfma_f32_16x16x32_bf16 v[244:247], v[216:219], v[72:75], v[244:247]\n\t" \
    "v_cvt_pk_bf16_f32 v236, v248, v249\n\t" \
    "v_mfma_f32_16x16x32_bf16 v[244:247], v[220:223], v[76:79], v[244:247]\n\t" \
    "v_cvt_pk_bf16_f32 v237, v250, v251\n\t" \
    "v_mfma_f32_16x16x32_bf16 v[244:247], v[200:203], v[56:59], v[244:247]\n\t" \
    "v_mfma_f32_16x16x32_bf16 v[244:247], v[204:207], v[60:63], v[244:247]\n\t" \
    "v_mfma_f32_16x16x32_bf16 v[248:251], v[208:211], v[104:107], 0\n\t" \
    "v_mfma_f32_16x16x32_bf16 v[248:251], v[212:215], v[108:111], v[248:251]\n\t" \
    "v_mfma_f32_16x16x32_bf16 v[248:251], v[216:219], v[112:115], v[248:251]\n\t" \
    "v_cvt_pk_bf16_f32 v238, v244, v245\n\t" \
    "v_mfma_f32_16x16x32_bf16 v[248:251], v[220:223], v[116:119], v[248:251]\n\t" \
    "v_cvt_pk_bf16_f32 v239, v246, v247\n\t" \
    "v_mfma_f32_16x16x32_bf16 v[248:251], v[200:203], v[96:99], v[248:251]\n\t" \
    "v_mfma_f32_16x16x32_bf16 v[248:251], v[204:207], v[100:103], v[248:251]\n\t" \
    "v_mfma_f32_16x16x32_bf16 v[244:247], v[208:211], v[144:147], 0\n\t" \
    "v_mfma_f32_16x16x32_bf16 v[244:247], v[212:215], v[148:151], v[244:247]\n\t" \
    "v_mfma_f32_16x16x32_bf16 v[244:247], v[216:219], v[152:155], v[244:247]\n\t" \
    "v_cvt_pk_bf16_f32 v240, v248, v249\n\t" \
    "v_mfma_f32_16x16x32_bf16 v[244:247], v[220:223], v[156:159], v[244:247]\n\t" \
    "v_cvt_pk_bf16_f32 v241, v250, v251\n\t" \
    "v_mfma_f32_16x16x32_bf16 v[244:247], v[200:203], v[136:139], v[244:247]\n\t" \
    "v_mfma_f32_16x16x32_bf16 v[244:247], v[204:207], v[140:143], v[244:247]\n\t" \
    "s_cmp_eq_u32 %[sy], 0\n\t" \
    "s_cbranch_scc1 .Lvb%=\n\t" \
    "s_barrier \n\t" \
    ".Lvb%=:\n\t" \
    "v_mfma_f32_16x16x32_bf16 v[248:251], v[208:211], v[184:187], 0\n\t" \
    "ds_read_b128 v[208:211], %[va] offset:8192\n\t" \
    "v_mfma_f32_16x16x32_bf16 v[248:251], v[212:215], v[188:191], v[248:251]\n\t" \
    "ds_read_b128 v[212:215], %[va] offset:9216\n\t" \
    "v_mfma_f32_16x16x32_bf16 v[248:251], v[216:219], v[192:195], v[248:251]\n\t" \
    "ds_read_b128 v[216:219], %[va] offset:10240\n\t" \
    "v_cvt_pk_bf16_f32 v242, v244, v245\n\t" \
    "v_mfma_f32_16x16x32_bf16 v[248:251], v[220:223], v[196:199], v[248:251]\n\t" \
    "ds_read_b128 v[220:223], %[va] offset:11264\n\t" \
    "v_cvt_pk_bf16_f32 v243, v246, v247\n\t" \
    "v_mfma_f32_16x16x32_bf16 v[248:251], v[200:203], v[176:179], v[248:251]\n\t" \
    "ds_read_b128 v[200:203], %[va] offset:6144\n\t" \
    "v_mfma_f32_16x16x32_bf16 v[248:251], v[204:207], v[180:183], v[248:251]\n\t" \
    "ds_read_b128 v[204:207], %[va] offset:7168\n\t" \
    "s_waitcnt lgkmcnt(5)\n\t" \
    "v_mfma_f32_16x16x32_bf16 v[244:247], v[208:211], v[24:27], 0\n\t" \
    "s_waitcnt lgkmcnt(4)\n\t" \
    "v_mfma_f32_16x16x32_bf16 v[244:247], v[212:215], v[28:31], v[244:247]\n\t" \
    "s_waitcnt lgkmcnt(3)\n\t" \
    "v_mfma_f32_16x16x32_bf16 v[244:247], v[216:219], v[32:35], v[244:247]\n\t" \
    "v_cvt_pk_bf16_f32 v234, v248, v249\n\t" \
    "s_waitcnt lgkmcnt(2)\n\t" \
    "v_mfma_f32_16x16x32_bf16 v[244:247], v[220:223], v[36:39], v[244:247]\n\t" \
    "v_cvt_pk_bf16_f32 v235, v250, v251\n\t" \
    "s_waitcnt lgkmcnt(1)\n\t" \
    "v_mfma_f32_16x16x32_bf16 v[244:247], v[200:203], v[16:19], v[244:247]\n\t" \
    "s_waitcnt lgkmcnt(0)\n\t" \
    "v_mfma_f32_16x16x32_bf16 v[244:247], v[204:207], v[20:23], v[244:247]\n\t" \
    "v_mfma_f32_16x16x32_bf16 v[248:251], v[208:211], v[64:67], 0\n\t" \
    "s_add_i32 %[so], %[so], 65536\n\t" \
    "v_mfma_f32_16x16x32_bf16 v[248:251], v[212:215], v[68:71], v[248:251]\n\t" \
    "s_nop 1\n\t" \
    "v_cvt_pk_bf16_f32 v247, v246, v247\n\t" \
    "v_mfma_f32_16x16x32_bf16 v[248:251], v[216:219], v[72:75], v[248:251]\n\t" \
    "v_cvt_pk_bf16_f32 v246, v244, v245\n\t" \
    "v_mfma_f32_16x16x32_bf16 v[248:251], v[220:223], v[76:79], v[248:251]\n\t" \
    "v_mov_b64 v[244:245], v[236:237]\n\t" \
    "buffer_store_dwordx4 v[244:247], %[l16], %[rd], %[so] offen sc1\n\t" \
    "v_mfma_f32_16x16x32_bf16 v[248:251], v[200:203], v[56:59], v[248:251]\n\t" \
    "v_mfma_f32_16x16x32_bf16 v[248:251], v[204:207], v[60:63], v[248:251]\n\t" \
    "v_mfma_f32_16x16x32_bf16 v[244:247], v[208:211], v[104:107], 0\n\t" \
    "s_add_i32 %[so], %[so], 131072\n\t" \
    "v_mfma_f32_16x16x32_bf16 v[244:247], v[212:215], v[108:111], v[244:247]\n\t" \
    "s_nop 1\n\t" \
    "v_cvt_pk_bf16_f32 v251, v250, v251\n\t" \
    "v_mfma_f32_16x16x32_bf16 v[244:247], v[216:219], v[112:115], v[244:247]\n\t" \
    "v_cvt_pk_bf16_f32 v250, v248, v249\n\t" \
    "v_mfma_f32_16x16x32_bf16 v[244:247], v[220:223], v[116:119], v[244:247]\n\t" \
    "v_mov_b64 v[248:249], v[238:239]\n\t" \
    "buffer_store_dwordx4 v[248:251], %[l16], %[rd], %[so] offen sc1\n\t" \
    "v_mfma_f32_16x16x32_bf16 v[244:247], v[200:203], v[96:99], v[244:247]\n\t" \
    "v_mfma_f32_16x16x32_bf16 v[244:247], v[204:207], v[100:103], v[244:247]\n\t" \
    "v_mfma_f32_16x16x32_bf16 v[248:251], v[208:211], v[144:147], 0\n\t" \
    "s_add_i32 %[so], %[so], 131072\n\t" \
    "v_mfma_f32_16x16x32_bf16 v[248:251], v[212:215], v[148:151], v[248:251]\n\t" \
    "s_nop 1\n\t" \
    "v_cvt_pk_bf16_f32 v247, v246, v247\n\t" \
    "v_mfma_f32_16x16x32_bf16 v[248:251], v[216:219], v[152:155], v[248:251]\n\t" \
    "v_cvt_pk_bf16_f32 v246, v244, v245\n\t" \
    "v_mfma_f32_16x16x32_bf16 v[248:251], v[220:223], v[156:159], v[248:251]\n\t" \
    "v_mov_b64 v[244:245], v[240:241]\n\t" \
    "buffer_store_dwordx4 v[244:247], %[l16], %[rd], %[so] offen sc1\n\t" \
    "v_mfma_f32_16x16x32_bf16 v[248:251], v[200:203], v[136:139], v[248:251]\n\t" \
    "v_mfma_f32_16x16x32_bf16 v[248:251], v[204:207], v[140:143], v[248:251]\n\t" \
    "v_mfma_f32_16x16x32_bf16 v[244:247], v[208:211], v[184:187], 0\n\t" \
    "s_add_i32 %[so], %[so], 131072\n\t" \
    "v_mfma_f32_16x16x32_bf16 v[244:247], v[212:215], v[188:191], v[244:247]\n\t" \
    "s_nop 1\n\t" \
    "v_cvt_pk_bf16_f32 v251, v250, v251\n\t" \
    "v_mfma_f32_16x16x32_bf16 v[244:247], v[216:219], v[192:195], v[244:247]\n\t" \
    "v_cvt_pk_bf16_f32 v250, v248, v249\n\t" \
    "v_mfma_f32_16x16x32_bf16 v[244:247], v[220:223], v[196:199], v[244:247]\n\t" \
    "v_mov_b64 v[248:249], v[242:243]\n\t" \
    "buffer_store_dwordx4 v[248:251], %[l16], %[rd], %[so] offen sc1\n\t" \
    "v_mfma_f32_16x16x32_bf16 v[244:247], v[200:203], v[176:179], v[244:247]\n\t" \
    "v_mfma_f32_16x16x32_bf16 v[244:247], v[204:207], v[180:183], v[244:247]\n\t" \
    "ds_read_b128 v[236:239], %[wl] offset:0\n\t" \
    "ds_read_b128 v[240:243], %[wl] offset:1024\n\t" \
    "v_mfma_f32_16x16x32_bf16 v[248:251], v[208:211], v[0:3], 0\n\t" \
    "s_add_i32 %[so], %[so], 131072\n\t" \
    "v_mfma_f32_16x16x32_bf16 v[248:251], v[212:215], v[4:7], v[248:251]\n\t" \
    "v_cvt_pk_bf16_f32 v247, v246, v247\n\t" \
    "v_mfma_f32_16x16x32_bf16 v[248:251], v[216:219], v[8:11], v[248:251]\n\t" \
    "v_cvt_pk_bf16_f32 v246, v244, v245\n\t" \
    "v_mfma_f32_16x16x32_bf16 v[248:251], v[220:223], v[12:15], v[248:251]\n\t" \
    "v_mov_b64 v[244:245], v[234:235]\n\t" \
    "buffer_store_dwordx4 v[244:247], %[l16], %[rd], %[so] offen sc1\n\t" \
    "s_waitcnt lgkmcnt(1)\n\t" \
    "v_mfma_f32_16x16x32_bf16 v[248:251], v[200:203], v[236:239], v[248:251]\n\t" \
    "ds_read_b128 v[236:239], %[wl] offset:2048\n\t" \
    "s_waitcnt lgkmcnt(1)\n\t" \
    "v_mfma_f32_16x16x32_bf16 v[248:251], v[204:207], v[240:243], v[248:251]\n\t" \
    "ds_read_b128 v[240:243], %[wl] offset:3072\n\t" \
    "v_mfma_f32_16x16x32_bf16 v[244:247], v[208:211], v[40:43], 0\n\t" \
    "s_add_i32 %[so], %[so], -589824\n\t" \
    "v_mfma_f32_16x16x32_bf16 v[244:247], v[212:215], v[44:47], v[244:247]\n\t" \
    "s_nop 0\n\t" \
    "v_cvt_pk_bf16_f32 v251, v250, v251\n\t" \
    "v_mfma_f32_16x16x32_bf16 v[244:247], v[216:219], v[48:51], v[244:247]\n\t" \
    "v_cvt_pk_bf16_f32 v250, v248, v249\n\t" \
    "v_mfma_f32_16x16x32_bf16 v[244:247], v[220:223], v[52:55], v[244:247]\n\t" \
    "v_mov_b64 v[248:249], v[224:225]\n\t" \
    "buffer_store_dwordx4 v[248:251], %[l16], %[rd], %[so] offen sc1\n\t" \
    "s_waitcnt lgkmcnt(1)\n\t" \
    "v_mfma_f32_16x16x32_bf16 v[244:247], v[200:203], v[236:239], v[244:247]\n\t" \
    "ds_read_b128 v[236:239], %[wl] offset:4096\n\t" \
    "s_waitcnt lgkmcnt(1)\n\t" \
    "v_mfma_f32_16x16x32_bf16 v[244:247], v[204:207], v[240:243], v[244:247]\n\t" \
    "ds_read_b128 v[240:243], %[wl] offset:5120\n\t" \
    "v_mfma_f32_16x16x32_bf16 v[248:251], v[208:211], v[80:83], 0\n\t" \
    "s_add_i32 %[so], %[so], 131072\n\t" \
    "v_mfma_f32_16x16x32_bf16 v[248:251], v[212:215], v[84:87], v[248:251]\n\t" \
    "s_nop 0\n\t" \
    "v_cvt_pk_bf16_f32 v247, v246, v247\n\t" \
    "v_mfma_f32_16x16x32_bf16 v[248:251], v[216:219], v[88:91], v[248:251]\n\t" \
    "v_cvt_pk_bf16_f32 v246, v244, v245\n\t" \
    "v_mfma_f32_16x16x32_bf16 v[248:251], v[220:223], v[92:95], v[248:251]\n\t" \
    "v_mov_b64 v[244:245], v[226:227]\n\t" \
    "buffer_store_dwordx4 v[244:247], %[l16], %[rd], %[so] offen sc1\n\t" \
    "s_waitcnt lgkmcnt(1)\n\t" \
    "v_mfma_f32_16x16x32_bf16 v[248:251], v[200:203], v[236:239], v[248:251]\n\t" \
    "ds_read_b128 v[236:239], %[wl] offset:6144\n\t" \
    "s_waitcnt lgkmcnt(1)\n\t" \
    "v_mfma_f32_16x16x32_bf16 v[248:251], v[204:207], v[240:243], v[248:251]\n\t" \
    "ds_read_b128 v[240:243], %[wl] offset:7168\n\t" \
    "v_mfma_f32_16x16x32_bf16 v[244:247], v[208:211], v[120:123], 0\n\t" \
    "s_add_i32 %[so], %[so], 131072\n\t" \
    "v_mfma_f32_16x16x32_bf16 v[244:247], v[212:215], v[124:127], v[244:247]\n\t" \
    "s_nop 0\n\t" \
    "v_cvt_pk_bf16_f32 v251, v250, v251\n\t" \
    "v_mfma_f32_16x16x32_bf16 v[244:247], v[216:219], v[128:131], v[244:247]\n\t" \
    "v_cvt_pk_bf16_f32 v250, v248, v249\n\t" \
    "v_mfma_f32_16x16x32_bf16 v[244:247], v[220:223], v[132:135], v[244:247]\n\t" \
    "v_mov_b64 v[248:249], v[228:229]\n\t" \
    "buffer_store_dwordx4 v[248:251], %[l16], %[rd], %[so] offen sc1\n\t" \
    "s_waitcnt lgkmcnt(1)\n\t" \
    "v_mfma_f32_16x16x32_bf16 v[244:247], v[200:203], v[236:239], v[244:247]\n\t" \
    "ds_read_b128 v[236:239], %[wl] offset:8192\n\t" \
    "s_waitcnt lgkmcnt(1)\n\t" \
    "v_mfma_f32_16x16x32_bf16 v[244:247], v[204:207], v[240:243], v[244:247]\n\t" \
    "ds_read_b128 v[240:243], %[wl] offset:9216\n\t" \
    "v_mfma_f32_16x16x32_bf16 v[248:251], v[208:211], v[160:163], 0\n\t" \
    "s_add_i32 %[so], %[so], 131072\n\t" \
    "v_mfma_f32_16x16x32_bf16 v[248:251], v[212:215], v[164:167], v[248:251]\n\t" \
    "s_nop 0\n\t" \
    "v_cvt_pk_bf16_f32 v247, v246, v247\n\t" \
    "v_mfma_f32_16x16x32_bf16 v[248:251], v[216:219], v[168:171], v[248:251]\n\t" \
    "v_cvt_pk_bf16_f32 v246, v244, v245\n\t" \
    "v_mfma_f32_16x16x32_bf16 v[248:251], v[220:223], v[172:175], v[248:251]\n\t" \
    "v_mov_b64 v[244:245], v[230:231]\n\t" \
    "buffer_store_dwordx4 v[244:247], %[l16], %[rd], %[so] offen sc1\n\t" \
    "s_waitcnt lgkmcnt(1)\n\t" \
    "v_mfma_f32_16x16x32_bf16 v[248:251], v[200:203], v[236:239], v[248:251]\n\t" \
    "s_waitcnt lgkmcnt(0)\n\t" \
    "v_mfma_f32_16x16x32_bf16 v[248:251], v[204:207], v[240:243], v[248:251]\n\t" \
    "s_add_i32 %[so], %[so], 131072\n\t" \
    "s_nop 7\n\t" \
    "v_cvt_pk_bf16_f32 v251, v250, v251\n\t" \
    "v_cvt_pk_bf16_f32 v250, v248, v249\n\t" \
    "v_mov_b64 v[248:249], v[232:233]\n\t" \
    "buffer_store_dwordx4 v[248:251], %[l16], %[rd], %[so] offen sc1\n\t" \
    "s_nop 1\n\t"
#define ASM_CRIT2 \
    "s_waitcnt lgkmcnt(0)\n\t" \
    "s_barrier\n\t" ASM_ST \
    "ds_read_b128 v[204:207], v200 offset:11264\n\t" \
    "ds_read_b128 v[208:211], v200 offset:12288\n\t" \
    "ds_read_b128 v[200:203], v200 offset:10240\n\t" \
    "s_waitcnt lgkmcnt(2)\n\t" \
    "v_smfmac_f32_16x16x64_bf16 v[224:227], v[204:207], v[24:31], %[sidx]\n\t" \
    "s_waitcnt lgkmcnt(1)\n\t" \
    "v_smfmac_f32_16x16x64_bf16 v[224:227], v[208:211], v[32:39], %[sidx]\n\t" \
    "s_waitcnt lgkmcnt(0)\n\t" \
    "v_smfmac_f32_16x16x64_bf16 v[224:227], v[200:203], v[16:23], %[sidx]\n\t" \
    "v_smfmac_f32_16x16x64_bf16 v[220:223], v[204:207], v[0:7], %[sidx]\n\t" \
    "v_smfmac_f32_16x16x64_bf16 v[220:223], v[208:211], v[8:15], %[sidx]\n\t" \
    "v_smfmac_f32_16x16x64_bf16 v[220:223], v[200:203], v[212:219], %[sidx]\n\t" \
    "ds_read_b128 v[212:215], %[wl] offset:2048\n\t" \
    "ds_read_b128 v[216:219], %[wl] offset:3072\n\t" \
    "v_smfmac_f32_16x16x64_bf16 v[232:235], v[204:207], v[64:71], %[sidx]\n\t" \
    "v_add_f32 v224, v224, v226\n\t" \
    "v_add_f32 v225, v225, v227\n\t" \
    "v_smfmac_f32_16x16x64_bf16 v[232:235], v[208:211], v[72:79], %[sidx]\n\t" \
    "v_smfmac_f32_16x16x64_bf16 v[232:235], v[200:203], v[56:63], %[sidx]\n\t" \
    "v_add_f32 v220, v220, v222\n\t" \
    "v_add_f32 v221, v221, v223\n\t" \
    "v_cvt_pk_bf16_f32 v226, v220, v221\n\t" \
    "v_cvt_pk_bf16_f32 v227, v224, v225\n\t" \
    "v_min_u32 v226, -2, v226\n\t" \
    "v_min_u32 v227, -2, v227\n\t" \
    "v_smfmac_f32_16x16x64_bf16 v[228:231], v[204:207], v[40:47], %[sidx]\n\t" \
    "v_smfmac_f32_16x16x64_bf16 v[228:231], v[208:211], v[48:55], %[sidx]\n\t" \
    "s_waitcnt lgkmcnt(0)\n\t" \
    "v_smfmac_f32_16x16x64_bf16 v[228:231], v[200:203], v[212:219], %[sidx]\n\t" \
    "ds_read_b128 v[212:215], %[wl] offset:4096\n\t" \
    "ds_read_b128 v[216:219], %[wl] offset:5120\n\t" \
    "v_smfmac_f32_16x16x64_bf16 v[240:243], v[204:207], v[104:111], %[sidx]\n\t" \
    "v_add_f32 v232, v232, v234\n\t" \
    "v_add_f32 v233, v233, v235\n\t" \
    "v_smfmac_f32_16x16x64_bf16 v[240:243], v[208:211], v[112:119], %[sidx]\n\t" \
    "v_smfmac_f32_16x16x64_bf16 v[240:243], v[200:203], v[96:103], %[sidx]\n\t" \
    "v_add_f32 v228, v228, v230\n\t" \
    "v_add_f32 v229, v229, v231\n\t" \
    "v_cvt_pk_bf16_f32 v228, v228, v229\n\t" \
    "v_cvt_pk_bf16_f32 v229, v232, v233\n\t" \
    "v_min_u32 v228, -2, v228\n\t" \
    "v_min_u32 v229, -2, v229\n\t" \
    "s_nop 0\n\t" \
    "v_smfmac_f32_16x16x64_bf16 v[236:239], v[204:207], v[80:87], %[sidx]\n\t" \
    "v_permlane32_swap_b32 v226, v228\n\t" \
    "v_permlane32_swap_b32 v227, v229\n\t" \
    "s_nop 0\n\t" \
    "v_smfmac_f32_16x16x64_bf16 v[236:239], v[208:211], v[88:95], %[sidx]\n\t" \
    "v_permlane16_swap_b32 v226, v227\n\t" \
    "v_permlane16_swap_b32 v228, v229\n\t" \
    "buffer_store_dwordx4 v[226:229], %[vo], %[rr], %[so] offen sc1\n\t" \
    "s_add_u32 %[so], %[so], 0x10000\n\t" \
    "s_waitcnt lgkmcnt(0)\n\t" \
    "v_smfmac_f32_16x16x64_bf16 v[236:239], v[200:203], v[212:219], %[sidx]\n\t" \
    "ds_read_b128 v[212:215], %[wl] offset:6144\n\t" \
    "ds_read_b128 v[216:219], %[wl] offset:7168\n\t" \
    "v_smfmac_f32_16x16x64_bf16 v[248:251], v[204:207], v[144:151], %[sidx]\n\t" \
    "v_mov_b64 v[220:221], 0\n\t" \
    "v_mov_b64 v[222:223], 0\n\t" \
    "v_smfmac_f32_16x16x64_bf16 v[248:251], v[208:211], v[152:159], %[sidx]\n\t" \
    "v_mov_b64 v[224:225], 0\n\t" \
    "v_mov_b64 v[226:227], 0\n\t" \
    "v_smfmac_f32_16x16x64_bf16 v[248:251], v[200:203], v[136:143], %[sidx]\n\t" \
    "v_add_f32 v240, v240, v242\n\t" \
    "v_add_f32 v241, v241, v243\n\t" \
    "v_add_f32 v236, v236, v238\n\t" \
    "v_add_f32 v237, v237, v239\n\t" \
    "v_cvt_pk_bf16_f32 v242, v236, v237\n\t" \
    "v_cvt_pk_bf16_f32 v243, v240, v241\n\t" \
    "v_min_u32 v242, -2, v242\n\t" \
    "v_min_u32 v243, -2, v243\n\t" \
    "v_smfmac_f32_16x16x64_bf16 v[244:247], v[204:207], v[120:127], %[sidx]\n\t" \
    "v_smfmac_f32_16x16x64_bf16 v[244:247], v[208:211], v[128:135], %[sidx]\n\t" \
    "s_waitcnt lgkmcnt(0)\n\t" \
    "v_smfmac_f32_16x16x64_bf16 v[244:247], v[200:203], v[212:219], %[sidx]\n\t" \
    "ds_read_b128 v[212:215], %[wl] offset:8192\n\t" \
    "ds_read_b128 v[216:219], %[wl] offset:9216\n\t" \
    "v_smfmac_f32_16x16x64_bf16 v[224:227], v[204:207], v[184:191], %[sidx]\n\t" \
    "v_add_f32 v248, v248, v250\n\t" \
    "v_add_f32 v249, v249, v251\n\t" \
    "v_smfmac_f32_16x16x64_bf16 v[224:227], v[208:211], v[192:199], %[sidx]\n\t" \
    "v_smfmac_f32_16x16x64_bf16 v[224:227], v[200:203], v[176:183], %[sidx]\n\t" \
    "v_add_f32 v244, v244, v246\n\t" \
    "v_add_f32 v245, v245, v247\n\t" \
    "v_cvt_pk_bf16_f32 v244, v244, v245\n\t" \
    "v_cvt_pk_bf16_f32 v245, v248, v249\n\t" \
    "v_min_u32 v244, -2, v244\n\t" \
    "v_min_u32 v245, -2, v245\n\t" \
    "s_nop 0\n\t" \
    "v_smfmac_f32_16x16x64_bf16 v[220:223], v[204:207], v[160:167], %[sidx]\n\t" \
    "v_permlane32_swap_b32 v242, v244\n\t" \
    "v_permlane32_swap_b32 v243, v245\n\t" \
    "s_nop 0\n\t" \
    "v_smfmac_f32_16x16x64_bf16 v[220:223], v[208:211], v[168:175], %[sidx]\n\t" \
    "v_permlane16_swap_b32 v242, v243\n\t" \
    "v_permlane16_swap_b32 v244, v245\n\t" \
    "buffer_store_dwordx4 v[242:245], %[vo], %[rr], %[so] offen sc1\n\t" \
    "s_add_u32 %[so], %[so], 0x10000\n\t" \
    "s_waitcnt lgkmcnt(0)\n\t" \
    "v_smfmac_f32_16x16x64_bf16 v[220:223], v[200:203], v[212:219], %[sidx]\n\t" \
    "v_add_f32 v224, v224, v226\n\t" \
    "v_add_f32 v225, v225, v227\n\t" \
    "s_nop 5\n\t" \
    "v_add_f32 v220, v220, v222\n\t" \
    "v_add_f32 v221, v221, v223\n\t" \
    "v_cvt_pk_bf16_f32 v220, v220, v221\n\t" \
    "v_cvt_pk_bf16_f32 v221, v224, v225\n\t" \
    "v_min_u32 v220, -2, v220\n\t" \
    "v_min_u32 v221, -2, v221\n\t" \
    "s_nop 1\n\t" \
    "v_permlane16_swap_b32 v220, v221\n\t" \
    "buffer_store_dwordx2 v[220:221], %[vl], %[rr], %[so] offen sc1\n\t" \
    "s_nop 1\n\t"
#define ASM_SM \
    "v_mbcnt_lo_u32_b32 v252, -1, 0\n\t" \
    "v_mbcnt_hi_u32_b32 v252, -1, v252\n\t" \
    "v_lshlrev_b32 v252, 4, v252\n\t" \
    "v_lshrrev_b32 v253, 1, v252\n\t" \
    "v_xor_b32 v253, v253, v252\n\t" \
    "v_and_b32 v253, 0x20, v253\n\t" \
    "v_mul_u32_u24 v253, 3, v253\n\t" \
    "v_xor_b32 v253, v253, v252\n\t" \
    "ds_read_b128 v[200:203], v253 offset:28672\n\t" \
    "ds_read_b128 v[204:207], v253 offset:29696\n\t" \
    "ds_read_b128 v[208:211], v253 offset:34816\n\t" \
    "ds_read_b128 v[212:215], v253 offset:35840\n\t" \
    "ds_read_b128 v[216:219], v253 offset:30720\n\t" \
    "ds_read_b128 v[220:223], v253 offset:31744\n\t" \
    "ds_read_b128 v[224:227], v253 offset:36864\n\t" \
    "ds_read_b128 v[228:231], v253 offset:37888\n\t" \
    "v_lshrrev_b32 v254, 1, v252\n\t" \
    "v_and_b32 v254, 0x180, v254\n\t" \
    "v_and_or_b32 v254, v252, %[c70], v254\n\t" \
    "v_bfe_i32 v255, v252, 5, 1\n\t" \
    "v_and_b32 v255, 0xaaaa, v255\n\t" \
    "v_or_b32 v255, 0x4444, v255\n\t" \
    "v_mov_b64 v[232:233], 0\n\t" \
    "v_mov_b64 v[234:235], 0\n\t" \
    "v_mov_b64 v[236:237], 0\n\t" \
    "v_mov_b64 v[238:239], 0\n\t" \
    "s_waitcnt lgkmcnt(0)\n\t" \
    "s_barrier\n\t" ASM_ST \
    "s_setprio 3\n\t" \
    "ds_read_b128 v[240:243], v252 offset:10240\n\t" \
    "ds_read_b128 v[244:247], v252 offset:11264\n\t" \
    "ds_read_b128 v[248:251], v252 offset:12288\n\t" \
    "s_waitcnt lgkmcnt(2)\n\t" \
    "v_smfmac_f32_16x16x64_bf16 v[232:235], v[240:243], v[200:207], v255\n\t" \
    "ds_read_b128 v[200:203], v253 offset:32768\n\t" \
    "ds_read_b128 v[204:207], v253 offset:33792\n\t" \
    "v_smfmac_f32_16x16x64_bf16 v[236:239], v[240:243], v[208:215], v255\n\t" \
    "ds_read_b128 v[208:211], v253 offset:38912\n\t" \
    "ds_read_b128 v[212:215], v253 offset:39936\n\t" \
    "s_waitcnt lgkmcnt(5)\n\t" \
    "v_smfmac_f32_16x16x64_bf16 v[232:235], v[244:247], v[216:223], v255\n\t" \
    "ds_read_b128 v[216:219], v254 offset:13312\n\t" \
    "ds_read_b128 v[220:223], v254 offset:13824\n\t" \
    "v_smfmac_f32_16x16x64_bf16 v[236:239], v[244:247], v[224:231], v255\n\t" \
    "ds_read_b128 v[224:227], v254 offset:14336\n\t" \
    "ds_read_b128 v[228:231], v254 offset:14848\n\t" \
    "s_waitcnt lgkmcnt(6)\n\t" \
    "v_smfmac_f32_16x16x64_bf16 v[232:235], v[248:251], v[200:207], v255\n\t" \
    "s_waitcnt lgkmcnt(4)\n\t" \
    "v_smfmac_f32_16x16x64_bf16 v[236:239], v[248:251], v[208:215], v255\n\t" \
    "ds_read_b128 v[208:211], v254 offset:15360\n\t" \
    "ds_read_b128 v[212:215], v254 offset:15872\n\t" \
    "v_mov_b64 v[204:205], 0\n\t" \
    "v_mov_b64 v[206:207], 0\n\t" \
    "s_nop 1\n\t" \
    "s_waitcnt lgkmcnt(4)\n\t" \
    "v_smfmac_f32_16x16x64_bf16 v[204:207], v[240:243], v[216:223], v255\n\t" \
    "s_waitcnt lgkmcnt(2)\n\t" \
    "v_smfmac_f32_16x16x64_bf16 v[204:207], v[244:247], v[224:231], v255\n\t" \
    "v_bfe_u32 v253, v252, 5, 1\n\t" \
    "v_bfe_i32 v202, v252, 4, 1\n\t" \
    "v_lshlrev_b32 v200, 1, v252\n\t" \
    "v_and_b32 v200, 0x100, v200\n\t" \
    "v_lshrrev_b32 v201, 1, v252\n\t" \
    "v_and_or_b32 v200, v201, 32, v200\n\t" \
    "v_and_or_b32 v200, v252, 16, v200\n\t" \
    "v_lshrrev_b32 v203, 8, v252\n\t" \
    "v_and_b32 v201, 16, v252\n\t" \
    "v_pk_add_f32 v[232:233], v[232:233], v[234:235]\n\t" \
    "v_pk_add_f32 v[234:235], v[236:237], v[238:239]\n\t" \
    "v_lshl_or_b32 v200, v203, 6, v200\n\t" \
    "v_lshl_or_b32 v201, v203, 5, v201\n\t" \
    "v_mad_u32_u24 v200, v253, %[cs], v200\n\t" \
    "v_mad_u32_u24 v201, v253, %[cs], v201\n\t" \
    "v_lshrrev_b32 v203, 3, v252\n\t" \
    "v_bfe_u32 v253, v252, 7, 1\n\t" \
    "s_waitcnt lgkmcnt(0)\n\t" \
    "v_smfmac_f32_16x16x64_bf16 v[204:207], v[248:251], v[208:215], v255\n\t" \
    "v_and_or_b32 v201, v203, 8, v201\n\t" \
    "v_and_b32 v202, 0x06060606, v202\n\t" \
    "v_mad_u32_u24 v201, v253, %[cp], v201\n\t" \
    "v_xor_b32 v202, 0x01000504, v202\n\t" \
    "v_max_f32 v236, v232, v234\n\t" \
    "v_max_f32 v237, v233, v235\n\t" \
    "s_nop 1\n\t" \
    "v_add_f32 v204, v204, v206\n\t" \
    "v_add_f32 v205, v205, v207\n\t" \
    "v_max_f32 v236, v236, v204\n\t" \
    "v_max_f32 v237, v237, v205\n\t" \
    "s_nop 0\n\t" \
    "v_max_f32_dpp v236, v236, v236 quad_perm:[1,0,3,2] row_mask:0xf bank_mask:0xf\n\t" \
    "v_max_f32_dpp v237, v237, v237 quad_perm:[1,0,3,2] row_mask:0xf bank_mask:0xf\n\t" \
    "s_nop 0\n\t" \
    "v_max_f32_dpp v236, v236, v236 quad_perm:[2,3,0,1] row_mask:0xf bank_mask:0xf\n\t" \
    "v_max_f32_dpp v237, v237, v237 quad_perm:[2,3,0,1] row_mask:0xf bank_mask:0xf\n\t" \
    "s_nop 0\n\t" \
    "v_max_f32_dpp v236, v236, v236 row_half_mirror row_mask:0xf bank_mask:0xf\n\t" \
    "v_max_f32_dpp v237, v237, v237 row_half_mirror row_mask:0xf bank_mask:0xf\n\t" \
    "s_nop 0\n\t" \
    "v_max_f32_dpp v236, v236, v236 row_mirror row_mask:0xf bank_mask:0xf\n\t" \
    "v_max_f32_dpp v237, v237, v237 row_mirror row_mask:0xf bank_mask:0xf\n\t" \
    "v_mul_f32 v236, %[scl], v236\n\t" \
    "v_mul_f32 v237, %[scl], v237\n\t" \
    "v_fma_f32 v232, v232, %[scl], -v236\n\t" \
    "v_fma_f32 v233, v233, %[scl], -v237\n\t" \
    "v_fma_f32 v234, v234, %[scl], -v236\n\t" \
    "v_fma_f32 v235, v235, %[scl], -v237\n\t" \
    "v_exp_f32 v232, v232\n\t" \
    "v_exp_f32 v233, v233\n\t" \
    "v_fma_f32 v204, v204, %[scl], -v236\n\t" \
    "v_fma_f32 v205, v205, %[scl], -v237\n\t" \
    "v_exp_f32 v234, v234\n\t" \
    "v_exp_f32 v235, v235\n\t" \
    "v_exp_f32 v204, v204\n\t" \
    "v_exp_f32 v205, v205\n\t" \
    "v_add_f32 v238, v232, v234\n\t" \
    "v_add_f32 v239, v233, v235\n\t" \
    "v_fma_f32 v238, v204, 0.5, v238\n\t" \
    "v_fma_f32 v239, v205, 0.5, v239\n\t" \
    "s_nop 0\n\t" \
    "v_add_f32_dpp v238, v238, v238 quad_perm:[1,0,3,2] row_mask:0xf bank_mask:0xf\n\t" \
    "v_add_f32_dpp v239, v239, v239 quad_perm:[1,0,3,2] row_mask:0xf bank_mask:0xf\n\t" \
    "s_nop 0\n\t" \
    "v_add_f32_dpp v238, v238, v238 quad_perm:[2,3,0,1] row_mask:0xf bank_mask:0xf\n\t" \
    "v_add_f32_dpp v239, v239, v239 quad_perm:[2,3,0,1] row_mask:0xf bank_mask:0xf\n\t" \
    "s_nop 0\n\t" \
    "v_add_f32_dpp v238, v238, v238 row_half_mirror row_mask:0xf bank_mask:0xf\n\t" \
    "v_add_f32_dpp v239, v239, v239 row_half_mirror row_mask:0xf bank_mask:0xf\n\t" \
    "s_nop 0\n\t" \
    "v_add_f32_dpp v238, v238, v238 row_mirror row_mask:0xf bank_mask:0xf\n\t" \
    "v_add_f32_dpp v239, v239, v239 row_mirror row_mask:0xf bank_mask:0xf\n\t" \
    "v_rcp_f32 v238, v238\n\t" \
    "v_rcp_f32 v239, v239\n\t" \
    "s_nop 0\n\t" \
    "v_pk_mul_f32 v[232:233], v[232:233], v[238:239]\n\t" \
    "v_pk_mul_f32 v[234:235], v[234:235], v[238:239]\n\t" \
    "v_pk_mul_f32 v[204:205], v[204:205], v[238:239]\n\t" \
    "v_cvt_pk_bf16_f32 v232, |v232|, |v233|\n\t" \
    "v_cvt_pk_bf16_f32 v234, |v234|, |v235|\n\t" \
    "v_cvt_pk_bf16_f32 v204, |v204|, |v205|\n\t" \
    "v_mov_b32_dpp v233, v232 quad_perm:[1,0,3,2] row_mask:0xf bank_mask:0xf\n\t" \
    "v_mov_b32_dpp v235, v234 quad_perm:[1,0,3,2] row_mask:0xf bank_mask:0xf\n\t" \
    "v_mov_b32_dpp v205, v204 quad_perm:[1,0,3,2] row_mask:0xf bank_mask:0xf\n\t" \
    "v_perm_b32 v232, v232, v233, v202\n\t" \
    "v_perm_b32 v234, v234, v235, v202\n\t" \
    "v_perm_b32 v204, v204, v205, v202\n\t" \
    "v_mov_b32_dpp v240, v232 quad_perm:[0,1,0,1] row_mask:0xf bank_mask:0xf\n\t" \
    "v_mov_b32_dpp v241, v234 quad_perm:[0,1,0,1] row_mask:0xf bank_mask:0xf\n\t" \
    "v_mov_b32_dpp v242, v232 quad_perm:[2,3,2,3] row_mask:0xf bank_mask:0xf\n\t" \
    "v_mov_b32_dpp v243, v234 quad_perm:[2,3,2,3] row_mask:0xf bank_mask:0xf\n\t" \
    "buffer_store_dwordx4 v[240:243], v200, %[rr], %[sx] offen sc1\n\t" \
    "v_mov_b32_dpp v244, v204 quad_perm:[0,1,0,1] row_mask:0xf bank_mask:0xf\n\t" \
    "v_mov_b32_dpp v245, v204 quad_perm:[2,3,2,3] row_mask:0xf bank_mask:0xf\n\t" \
    SMSY \
    "buffer_store_dwordx2 v[244:245], v201, %[rr], %[sm] offen sc1\n\t" \
    "s_wakeup\n\t" \
    PRISM
#define ASM_P4_0 \
    "ds_read_b128 v[200:203], %[qa] offset:0\n\t" \
    "ds_read_b128 v[204:207], %[qa] offset:1024\n\t" \
    "ds_read_b128 v[208:211], %[qa] offset:2048\n\t" \
    "ds_read_b128 v[212:215], %[qa] offset:3072\n\t" \
    "ds_read_b128 v[216:219], %[qa] offset:4096\n\t" \
    "ds_read_b128 v[220:223], %[qa] offset:5120\n\t" \
    "ds_read_b128 v[224:227], %[qa] offset:12288\n\t" \
    "ds_read_b128 v[228:231], %[qa] offset:13312\n\t" \
    "ds_read_b128 v[232:235], %[qa] offset:14336\n\t" \
    "ds_read_b128 v[236:239], %[qa] offset:15360\n\t" \
    "s_waitcnt lgkmcnt(0)\n\t" \
    "s_barrier\n\t" ASM_ST \
    "v_mfma_f32_16x16x32_bf16 v[240:243], v[224:227], v[200:203], v[240:243]\n\t" \
    "ds_read_b128 v[224:227], %[qa] offset:16384\n\t" \
    "v_mfma_f32_16x16x32_bf16 v[240:243], v[228:231], v[204:207], v[240:243]\n\t" \
    "ds_read_b128 v[228:231], %[qa] offset:17408\n\t" \
    "v_mfma_f32_16x16x32_bf16 v[240:243], v[232:235], v[208:211], v[240:243]\n\t" \
    "ds_read_b128 v[232:235], %[km] offset:0\n\t" \
    "v_mfma_f32_16x16x32_bf16 v[240:243], v[236:239], v[212:215], v[240:243]\n\t" \
    "ds_read_b128 v[236:239], %[km] offset:512\n\t" \
    "s_waitcnt lgkmcnt(3)\n\t" \
    "v_mfma_f32_16x16x32_bf16 v[240:243], v[224:227], v[216:219], v[240:243]\n\t" \
    "ds_read_b128 v[224:227], %[km] offset:1024\n\t" \
    "s_waitcnt lgkmcnt(3)\n\t" \
    "v_mfma_f32_16x16x32_bf16 v[240:243], v[228:231], v[220:223], v[240:243]\n\t" \
    "ds_read_b128 v[228:231], %[km] offset:1536\n\t" \
    "s_waitcnt lgkmcnt(3)\n\t" \
    "v_mfma_f32_16x16x32_bf16 v[248:251], v[232:235], v[200:203], 0\n\t" \
    "ds_read_b128 v[232:235], %[km] offset:2048\n\t" \
    "s_waitcnt lgkmcnt(3)\n\t" \
    "v_mfma_f32_16x16x32_bf16 v[248:251], v[236:239], v[204:207], v[248:251]\n\t" \
    "ds_read_b128 v[236:239], %[km] offset:2560\n\t" \
    "s_waitcnt lgkmcnt(3)\n\t" \
    "v_mfma_f32_16x16x32_bf16 v[248:251], v[224:227], v[208:211], v[248:251]\n\t" \
    "s_waitcnt lgkmcnt(2)\n\t" \
    "v_mfma_f32_16x16x32_bf16 v[248:251], v[228:231], v[212:215], v[248:251]\n\t" \
    "s_waitcnt lgkmcnt(1)\n\t" \
    "v_mfma_f32_16x16x32_bf16 v[248:251], v[232:235], v[216:219], v[248:251]\n\t" \
    "s_waitcnt lgkmcnt(0)\n\t" \
    "v_mfma_f32_16x16x32_bf16 v[248:251], v[236:239], v[220:223], v[248:251]\n\t" \
    "v_max3_f32 v224, v240, v241, v242\n\t" \
    "v_mov_b32 v225, v243\n\t" \
    "v_lshlrev_b32 v201, 23, %[qa]\n\t" \
    "v_and_b32 v201, 0x80000000, v201\n\t" \
    "v_and_b32 v200, 0x3ff, %[qa]\n\t" \
    "v_or_b32 v200, v201, v200\n\t" \
    "s_nop 1\n\t" \
    "v_max3_f32 v224, v224, v248, v249\n\t" \
    "v_max3_f32 v225, v225, v250, v251\n\t" \
    "v_max_f32 v224, v224, v225\n\t" \
    "v_mov_b32 v225, v224\n\t" \
    "s_nop 1\n\t" \
    "v_permlane16_swap_b32 v224, v225\n\t" \
    "v_max_f32 v224, v224, v225\n\t" \
    "v_mov_b32 v225, v224\n\t" \
    "s_nop 1\n\t" \
    "v_permlane32_swap_b32 v224, v225\n\t" \
    "v_max_f32 v224, v224, v225\n\t" \
    "v_mul_f32 v224, %[scl], v224\n\t" \
    "v_fma_f32 v240, v240, %[scl], -v224\n\t" \
    "v_fma_f32 v241, v241, %[scl], -v224\n\t" \
    "v_exp_f32 v240, v240\n\t" \
    "v_fma_f32 v242, v242, %[scl], -v224\n\t" \
    "v_exp_f32 v241, v241\n\t" \
    "v_fma_f32 v243, v243, %[scl], -v224\n\t" \
    "v_exp_f32 v242, v242\n\t" \
    "v_fma_f32 v248, v248, %[scl], -v224\n\t" \
    "v_exp_f32 v243, v243\n\t" \
    "v_fma_f32 v249, v249, %[scl], -v224\n\t" \
    "v_exp_f32 v248, v248\n\t" \
    "v_fma_f32 v250, v250, %[scl], -v224\n\t" \
    "v_exp_f32 v249, v249\n\t" \
    "v_fma_f32 v251, v251, %[scl], -v224\n\t" \
    "v_exp_f32 v250, v250\n\t" \
    "v_exp_f32 v251, v251\n\t" \
    "v_add_f32 v224, v240, v241\n\t" \
    "v_add_f32 v225, v242, v243\n\t" \
    "v_add_f32 v232, v248, v249\n\t" \
    "v_add_f32 v233, v250, v251\n\t" \
    "v_add_f32 v224, v224, v225\n\t" \
    "v_add_f32 v232, v232, v233\n\t" \
    "v_fma_f32 v224, v232, 0.5, v224\n\t" \
    "v_mov_b32 v225, v224\n\t" \
    "s_nop 1\n\t" \
    "v_permlane16_swap_b32 v224, v225\n\t" \
    "v_add_f32 v224, v224, v225\n\t" \
    "v_mov_b32 v225, v224\n\t" \
    "s_nop 1\n\t" \
    "v_permlane32_swap_b32 v224, v225\n\t" \
    "v_add_f32 v224, v224, v225\n\t" \
    "v_rcp_f32 v224, v224\n\t" \
    "s_nop 0\n\t" \
    "v_mul_f32 v240, v240, v224\n\t" \
    "v_mul_f32 v241, v241, v224\n\t" \
    "v_mul_f32 v242, v242, v224\n\t" \
    "v_mul_f32 v243, v243, v224\n\t" \
    "v_mul_f32 v248, v248, v224\n\t" \
    "v_mul_f32 v249, v249, v224\n\t" \
    "v_mul_f32 v250, v250, v224\n\t" \
    "v_mul_f32 v251, v251, v224\n\t" \
    "v_cvt_pk_bf16_f32 v228, |v240|, |v241|\n\t" \
    "v_cvt_pk_bf16_f32 v229, |v242|, |v243|\n\t" \
    "v_cvt_pk_bf16_f32 v232, |v248|, |v249|\n\t" \
    "v_cvt_pk_bf16_f32 v233, |v250|, |v251|\n\t" \
    "v_permlane16_swap_b32 v228, v230\n\t" \
    "v_permlane16_swap_b32 v229, v231\n\t" \
    "buffer_store_dwordx4 v[228:231], v200, %[rr], %[so] offen sc1\n\t" \
    "buffer_store_dwordx2 v[232:233], %[pm], %[rr], %[so] offen sc1\n\t" \
    "s_nop 1\n\t"
#define ASM_P4_1 \
    "ds_read_b128 v[200:203], %[qa] offset:0\n\t" \
    "ds_read_b128 v[204:207], %[qa] offset:1024\n\t" \
    "ds_read_b128 v[208:211], %[qa] offset:2048\n\t" \
    "ds_read_b128 v[212:215], %[qa] offset:3072\n\t" \
    "ds_read_b128 v[216:219], %[qa] offset:4096\n\t" \
    "ds_read_b128 v[220:223], %[qa] offset:5120\n\t" \
    "ds_read_b128 v[224:227], %[qa] offset:12288\n\t" \
    "ds_read_b128 v[228:231], %[qa] offset:13312\n\t" \
    "ds_read_b128 v[232:235], %[qa] offset:14336\n\t" \
    "ds_read_b128 v[236:239], %[qa] offset:15360\n\t" \
    "s_waitcnt lgkmcnt(0)\n\t" \
    "s_barrier\n\t" ASM_ST \
    "v_mfma_f32_16x16x32_bf16 v[240:243], v[224:227], v[200:203], v[240:243]\n\t" \
    "ds_read_b128 v[224:227], %[qa] offset:16384\n\t" \
    "v_mfma_f32_16x16x32_bf16 v[240:243], v[228:231], v[204:207], v[240:243]\n\t" \
    "ds_read_b128 v[228:231], %[qa] offset:17408\n\t" \
    "v_mfma_f32_16x16x32_bf16 v[240:243], v[232:235], v[208:211], v[240:243]\n\t" \
    "ds_read_b128 v[232:235], %[qa] offset:6144\n\t" \
    "v_mfma_f32_16x16x32_bf16 v[240:243], v[236:239], v[212:215], v[240:243]\n\t" \
    "ds_read_b128 v[236:239], %[qa] offset:7168\n\t" \
    "s_waitcnt lgkmcnt(3)\n\t" \
    "v_mfma_f32_16x16x32_bf16 v[240:243], v[224:227], v[216:219], v[240:243]\n\t" \
    "ds_read_b128 v[224:227], %[qa] offset:8192\n\t" \
    "s_waitcnt lgkmcnt(3)\n\t" \
    "v_mfma_f32_16x16x32_bf16 v[240:243], v[228:231], v[220:223], v[240:243]\n\t" \
    "ds_read_b128 v[228:231], %[qa] offset:9216\n\t" \
    "s_waitcnt lgkmcnt(3)\n\t" \
    "v_mfma_f32_16x16x32_bf16 v[244:247], v[232:235], v[200:203], 0\n\t" \
    "ds_read_b128 v[232:235], %[qa] offset:10240\n\t" \
    "s_waitcnt lgkmcnt(3)\n\t" \
    "v_mfma_f32_16x16x32_bf16 v[244:247], v[236:239], v[204:207], v[244:247]\n\t" \
    "ds_read_b128 v[236:239], %[qa] offset:11264\n\t" \
    "s_waitcnt lgkmcnt(3)\n\t" \
    "v_mfma_f32_16x16x32_bf16 v[244:247], v[224:227], v[208:211], v[244:247]\n\t" \
    "ds_read_b128 v[224:227], %[km] offset:0\n\t" \
    "s_waitcnt lgkmcnt(3)\n\t" \
    "v_mfma_f32_16x16x32_bf16 v[244:247], v[228:231], v[212:215], v[244:247]\n\t" \
    "ds_read_b128 v[228:231], %[km] offset:512\n\t" \
    "s_waitcnt lgkmcnt(3)\n\t" \
    "v_mfma_f32_16x16x32_bf16 v[244:247], v[232:235], v[216:219], v[244:247]\n\t" \
    "ds_read_b128 v[232:235], %[km] offset:1024\n\t" \
    "s_waitcnt lgkmcnt(3)\n\t" \
    "v_mfma_f32_16x16x32_bf16 v[244:247], v[236:239], v[220:223], v[244:247]\n\t" \
    "ds_read_b128 v[236:239], %[km] offset:1536\n\t" \
    "s_waitcnt lgkmcnt(3)\n\t" \
    "v_mfma_f32_16x16x32_bf16 v[248:251], v[224:227], v[200:203], 0\n\t" \
    "ds_read_b128 v[224:227], %[km] offset:2048\n\t" \
    "s_waitcnt lgkmcnt(3)\n\t" \
    "v_mfma_f32_16x16x32_bf16 v[248:251], v[228:231], v[204:207], v[248:251]\n\t" \
    "ds_read_b128 v[228:231], %[km] offset:2560\n\t" \
    "s_waitcnt lgkmcnt(3)\n\t" \
    "v_mfma_f32_16x16x32_bf16 v[248:251], v[232:235], v[208:211], v[248:251]\n\t" \
    "s_waitcnt lgkmcnt(2)\n\t" \
    "v_mfma_f32_16x16x32_bf16 v[248:251], v[236:239], v[212:215], v[248:251]\n\t" \
    "s_waitcnt lgkmcnt(1)\n\t" \
    "v_mfma_f32_16x16x32_bf16 v[248:251], v[224:227], v[216:219], v[248:251]\n\t" \
    "s_waitcnt lgkmcnt(0)\n\t" \
    "v_mfma_f32_16x16x32_bf16 v[248:251], v[228:231], v[220:223], v[248:251]\n\t" \
    "v_max3_f32 v224, v240, v241, v242\n\t" \
    "v_max3_f32 v225, v244, v245, v246\n\t" \
    "v_max3_f32 v224, v224, v243, v247\n\t" \
    "v_and_b32 v200, 0x3ff, %[qa]\n\t" \
    "s_nop 3\n\t" \
    "v_max3_f32 v224, v224, v248, v249\n\t" \
    "v_max3_f32 v225, v225, v250, v251\n\t" \
    "v_max_f32 v224, v224, v225\n\t" \
    "v_mov_b32 v225, v224\n\t" \
    "s_nop 1\n\t" \
    "v_permlane16_swap_b32 v224, v225\n\t" \
    "v_max_f32 v224, v224, v225\n\t" \
    "v_mov_b32 v225, v224\n\t" \
    "s_nop 1\n\t" \
    "v_permlane32_swap_b32 v224, v225\n\t" \
    "v_max_f32 v224, v224, v225\n\t" \
    "v_mul_f32 v224, %[scl], v224\n\t" \
    "v_fma_f32 v240, v240, %[scl], -v224\n\t" \
    "v_fma_f32 v241, v241, %[scl], -v224\n\t" \
    "v_exp_f32 v240, v240\n\t" \
    "v_fma_f32 v242, v242, %[scl], -v224\n\t" \
    "v_exp_f32 v241, v241\n\t" \
    "v_fma_f32 v243, v243, %[scl], -v224\n\t" \
    "v_exp_f32 v242, v242\n\t" \
    "v_fma_f32 v244, v244, %[scl], -v224\n\t" \
    "v_exp_f32 v243, v243\n\t" \
    "v_fma_f32 v245, v245, %[scl], -v224\n\t" \
    "v_exp_f32 v244, v244\n\t" \
    "v_fma_f32 v246, v246, %[scl], -v224\n\t" \
    "v_exp_f32 v245, v245\n\t" \
    "v_fma_f32 v247, v247, %[scl], -v224\n\t" \
    "v_exp_f32 v246, v246\n\t" \
    "v_fma_f32 v248, v248, %[scl], -v224\n\t" \
    "v_exp_f32 v247, v247\n\t" \
    "v_fma_f32 v249, v249, %[scl], -v224\n\t" \
    "v_exp_f32 v248, v248\n\t" \
    "v_fma_f32 v250, v250, %[scl], -v224\n\t" \
    "v_exp_f32 v249, v249\n\t" \
    "v_fma_f32 v251, v251, %[scl], -v224\n\t" \
    "v_exp_f32 v250, v250\n\t" \
    "v_exp_f32 v251, v251\n\t" \
    "v_add_f32 v224, v240, v241\n\t" \
    "v_add_f32 v225, v242, v243\n\t" \
    "v_add_f32 v226, v244, v245\n\t" \
    "v_add_f32 v227, v246, v247\n\t" \
    "v_add_f32 v232, v248, v249\n\t" \
    "v_add_f32 v233, v250, v251\n\t" \
    "v_add_f32 v224, v224, v225\n\t" \
    "v_add_f32 v226, v226, v227\n\t" \
    "v_add_f32 v232, v232, v233\n\t" \
    "v_add_f32 v224, v224, v226\n\t" \
    "v_fma_f32 v224, v232, 0.5, v224\n\t" \
    "v_mov_b32 v225, v224\n\t" \
    "s_nop 1\n\t" \
    "v_permlane16_swap_b32 v224, v225\n\t" \
    "v_add_f32 v224, v224, v225\n\t" \
    "v_mov_b32 v225, v224\n\t" \
    "s_nop 1\n\t" \
    "v_permlane32_swap_b32 v224, v225\n\t" \
    "v_add_f32 v224, v224, v225\n\t" \
    "v_rcp_f32 v224, v224\n\t" \
    "s_nop 0\n\t" \
    "v_mul_f32 v240, v240, v224\n\t" \
    "v_mul_f32 v241, v241, v224\n\t" \
    "v_mul_f32 v242, v242, v224\n\t" \
    "v_mul_f32 v243, v243, v224\n\t" \
    "v_mul_f32 v244, v244, v224\n\t" \
    "v_mul_f32 v245, v245, v224\n\t" \
    "v_mul_f32 v246, v246, v224\n\t" \
    "v_mul_f32 v247, v247, v224\n\t" \
    "v_mul_f32 v248, v248, v224\n\t" \
    "v_mul_f32 v249, v249, v224\n\t" \
    "v_mul_f32 v250, v250, v224\n\t" \
    "v_mul_f32 v251, v251, v224\n\t" \
    "v_cvt_pk_bf16_f32 v230, |v240|, |v241|\n\t" \
    "v_cvt_pk_bf16_f32 v231, |v242|, |v243|\n\t" \
    "v_cvt_pk_bf16_f32 v228, |v244|, |v245|\n\t" \
    "v_cvt_pk_bf16_f32 v229, |v246|, |v247|\n\t" \
    "v_cvt_pk_bf16_f32 v232, |v248|, |v249|\n\t" \
    "v_cvt_pk_bf16_f32 v233, |v250|, |v251|\n\t" \
    "buffer_store_dwordx4 v[228:231], v200, %[rr], %[so] offen sc1\n\t" \
    "buffer_store_dwordx2 v[232:233], %[pm], %[rr], %[so] offen sc1\n\t" \
    "s_nop 1\n\t"
#define ASM_RCRIT \
    "v_mov_b64 v[176:177], -1\n\t" \
    "v_mov_b64 v[178:179], -1\n\t" \
    "v_mov_b64 v[180:181], -1\n\t" \
    "v_mov_b64 v[182:183], -1\n\t" \
    "v_mov_b64 v[184:185], -1\n\t" \
    "v_mov_b64 v[186:187], -1\n\t" \
    "v_mov_b64 v[188:189], -1\n\t" \
    "v_mov_b64 v[190:191], -1\n\t" \
    "v_mov_b64 v[192:193], -1\n\t" \
    "v_mov_b64 v[194:195], -1\n\t" \
    ".Lfa%=:\n\t" \
    "s_mov_b32 %[a], 0\n\t" \
    "v_max_u32 v196, v176, v177\n\t" \
    "v_max3_u32 v196, v178, v179, v196\n\t" \
    "v_cmp_eq_u32 vcc, -1, v196\n\t" \
    "s_cbranch_vccz .Lga0_%=\n\t" \
    "buffer_load_dwordx4 v[176:179], %[ov], %[rs], %[sv] offen sc1\n\t" \
    "s_mov_b32 %[a], 1\n\t" \
    ".Lga0_%=:\n\t" \
    "v_max_u32 v196, v180, v181\n\t" \
    "v_max3_u32 v196, v182, v183, v196\n\t" \
    "v_cmp_eq_u32 vcc, -1, v196\n\t" \
    "s_cbranch_vccz .Lga1_%=\n\t" \
    "buffer_load_dwordx4 v[180:183], %[ov], %[rs], %[sv] offen offset:1024 sc1\n\t" \
    "s_mov_b32 %[a], 1\n\t" \
    ".Lga1_%=:\n\t" \
    "s_cmp_eq_u32 %[a], 0\n\t" \
    "s_cbranch_scc1 .Lha%=\n\t" \
    "s_waitcnt vmcnt(12)\n\t" \
    ".Lia%=:\n\t" \
    "v_max_u32 v196, v176, v177\n\t" \
    "v_max3_u32 v196, v178, v179, v196\n\t" \
    "v_max3_u32 v196, v180, v181, v196\n\t" \
    "v_max3_u32 v196, v182, v183, v196\n\t" \
    "v_cmp_eq_u32 vcc, -1, v196\n\t" \
    "s_cbranch_vccz .Lha%=\n\t" \
    "s_nop 7\n\t" \
    "v_max_u32 v196, v176, v177\n\t" \
    "v_max3_u32 v196, v178, v179, v196\n\t" \
    "v_max3_u32 v196, v180, v181, v196\n\t" \
    "v_max3_u32 v196, v182, v183, v196\n\t" \
    "v_cmp_eq_u32 vcc, -1, v196\n\t" \
    "s_cbranch_vccz .Lha%=\n\t" \
    "s_nop 7\n\t" \
    "v_max_u32 v196, v176, v177\n\t" \
    "v_max3_u32 v196, v178, v179, v196\n\t" \
    "v_max3_u32 v196, v180, v181, v196\n\t" \
    "v_max3_u32 v196, v182, v183, v196\n\t" \
    "v_cmp_eq_u32 vcc, -1, v196\n\t" \
    "s_cbranch_vccz .Lha%=\n\t" \
    "s_nop 7\n\t" \
    "v_max_u32 v196, v176, v177\n\t" \
    "v_max3_u32 v196, v178, v179, v196\n\t" \
    "v_max3_u32 v196, v180, v181, v196\n\t" \
    "v_max3_u32 v196, v182, v183, v196\n\t" \
    "v_cmp_eq_u32 vcc, -1, v196\n\t" \
    "s_cbranch_vccz .Lha%=\n\t" \
    "s_nop 7\n\t" \
    "s_sub_u32 %[n], %[n], 1\n\t" \
    "s_cmp_gt_i32 %[n], 0\n\t" \
    "s_cbranch_scc1 .Lfa%=\n\t" \
    ".Lha%=:\n\t" \
    ASM_ST3 \
    "buffer_load_dwordx4 v[184:187], %[o0], %[rs], %[sp] offen sc1\n\t" \
    "buffer_load_dwordx4 v[188:191], %[o0], %[rs], %[sp] offen offset:1024 sc1\n\t" \
    "buffer_load_dwordx4 v[192:195], %[o2], %[rs], %[sp] offen sc1\n\t" \
    "s_waitcnt vmcnt(0)\n\t" \
    "v_max_u32 v196, v184, v185\n\t" \
    "v_max3_u32 v196, v186, v187, v196\n\t" \
    "v_max3_u32 v196, v188, v189, v196\n\t" \
    "v_max3_u32 v196, v190, v191, v196\n\t" \
    "v_max3_u32 v196, v192, v193, v196\n\t" \
    "v_max3_u32 v196, v194, v195, v196\n\t" \
    "v_cmp_eq_u32 vcc, -1, v196\n\t" \
    "s_cbranch_vccz .Lhb%=\n\t" \
    ".Lfb%=:\n\t" \
    "s_mov_b32 %[a], 0\n\t" \
    "v_max_u32 v196, v184, v185\n\t" \
    "v_max3_u32 v196, v186, v187, v196\n\t" \
    "v_cmp_eq_u32 vcc, -1, v196\n\t" \
    "s_cbranch_vccz .Lgb0_%=\n\t" \
    "buffer_load_dwordx4 v[184:187], %[o0], %[rs], %[sp] offen sc1\n\t" \
    "s_mov_b32 %[a], 1\n\t" \
    ".Lgb0_%=:\n\t" \
    "v_max_u32 v196, v188, v189\n\t" \
    "v_max3_u32 v196, v190, v191, v196\n\t" \
    "v_cmp_eq_u32 vcc, -1, v196\n\t" \
    "s_cbranch_vccz .Lgb1_%=\n\t" \
    "buffer_load_dwordx4 v[188:191], %[o0], %[rs], %[sp] offen offset:1024 sc1\n\t" \
    "s_mov_b32 %[a], 1\n\t" \
    ".Lgb1_%=:\n\t" \
    "v_max_u32 v196, v192, v193\n\t" \
    "v_max3_u32 v196, v194, v195, v196\n\t" \
    "v_cmp_eq_u32 vcc, -1, v196\n\t" \
    "s_cbranch_vccz .Lgb2_%=\n\t" \
    "buffer_load_dwordx4 v[192:195], %[o2], %[rs], %[sp] offen sc1\n\t" \
    "s_mov_b32 %[a], 1\n\t" \
    ".Lgb2_%=:\n\t" \
    "s_cmp_eq_u32 %[a], 0\n\t" \
    "s_cbranch_scc1 .Lhb%=\n\t" \
    "s_waitcnt vmcnt(12)\n\t" \
    ".Lib%=:\n\t" \
    "v_max_u32 v196, v184, v185\n\t" \
    "v_max3_u32 v196, v186, v187, v196\n\t" \
    "v_max3_u32 v196, v188, v189, v196\n\t" \
    "v_max3_u32 v196, v190, v191, v196\n\t" \
    "v_max3_u32 v196, v192, v193, v196\n\t" \
    "v_max3_u32 v196, v194, v195, v196\n\t" \
    "v_cmp_eq_u32 vcc, -1, v196\n\t" \
    "s_cbranch_vccz .Lhb%=\n\t" \
    "s_nop 7\n\t" \
    "v_max_u32 v196, v184, v185\n\t" \
    "v_max3_u32 v196, v186, v187, v196\n\t" \
    "v_max3_u32 v196, v188, v189, v196\n\t" \
    "v_max3_u32 v196, v190, v191, v196\n\t" \
    "v_max3_u32 v196, v192, v193, v196\n\t" \
    "v_max3_u32 v196, v194, v195, v196\n\t" \
    "v_cmp_eq_u32 vcc, -1, v196\n\t" \
    "s_cbranch_vccz .Lhb%=\n\t" \
    "s_nop 7\n\t" \
    "v_max_u32 v196, v184, v185\n\t" \
    "v_max3_u32 v196, v186, v187, v196\n\t" \
    "v_max3_u32 v196, v188, v189, v196\n\t" \
    "v_max3_u32 v196, v190, v191, v196\n\t" \
    "v_max3_u32 v196, v192, v193, v196\n\t" \
    "v_max3_u32 v196, v194, v195, v196\n\t" \
    "v_cmp_eq_u32 vcc, -1, v196\n\t" \
    "s_cbranch_vccz .Lhb%=\n\t" \
    "s_nop 7\n\t" \
    "v_max_u32 v196, v184, v185\n\t" \
    "v_max3_u32 v196, v186, v187, v196\n\t" \
    "v_max3_u32 v196, v188, v189, v196\n\t" \
    "v_max3_u32 v196, v190, v191, v196\n\t" \
    "v_max3_u32 v196, v192, v193, v196\n\t" \
    "v_max3_u32 v196, v194, v195, v196\n\t" \
    "v_cmp_eq_u32 vcc, -1, v196\n\t" \
    "s_cbranch_vccz .Lhb%=\n\t" \
    "s_nop 7\n\t" \
    "s_sub_u32 %[n], %[n], 1\n\t" \
    "s_cmp_gt_i32 %[n], 0\n\t" \
    "s_cbranch_scc1 .Lfb%=\n\t" \
    ".Lhb%=:\n\t" \
    ASM_ST2 \
    "v_mfma_f32_16x16x32_bf16 v[160:163], v[192:195], v[176:179], 0\n\t" \
    "v_smfmac_f32_16x16x64_bf16 v[160:163], v[184:187], v[128:135], %[sidx]\n\t" \
    "v_smfmac_f32_16x16x64_bf16 v[160:163], v[188:191], v[136:143], %[sidx]\n\t" \
    "v_mfma_f32_16x16x32_bf16 v[164:167], v[192:195], v[180:183], 0\n\t" \
    "v_smfmac_f32_16x16x64_bf16 v[164:167], v[184:187], v[144:151], %[sidx]\n\t" \
    "v_smfmac_f32_16x16x64_bf16 v[164:167], v[188:191], v[152:159], %[sidx]\n\t" \
    "s_cmp_eq_u32 %[pub], 0\n\t" \
    "v_add_f32 v160, v160, v162\n\t" \
    "v_add_f32 v161, v161, v163\n\t" \
    "ds_write2_b32 %[rw], v160, v161 offset1:144\n\t" \
    "s_nop 4\n\t" \
    "v_add_f32 v164, v164, v166\n\t" \
    "v_add_f32 v165, v165, v167\n\t" \
    "ds_write2_b32 %[rw], v164, v165 offset0:16 offset1:160\n\t" \
    "s_waitcnt lgkmcnt(0)\n\t" \
    "s_barrier\n\t" ASM_ST \
    "s_cbranch_scc1 .Lre%=\n\t" \
    "ds_read_b128 v[200:203], %[rd] offset:0\n\t" \
    "ds_read_b128 v[204:207], %[rd] offset:16\n\t" \
    "s_waitcnt lgkmcnt(1)\n\t" \
    "v_pk_add_f32 v[200:201], v[200:201], v[202:203]\n\t" \
    "s_waitcnt lgkmcnt(0)\n\t" \
    "v_pk_add_f32 v[204:205], v[204:205], v[206:207]\n\t" \
    "v_pk_add_f32 v[200:201], v[200:201], v[204:205]\n\t" \
    "s_nop 1\n\t" \
    "v_permlane32_swap_b32 v200, v201\n\t" \
    "v_add_f32 v202, v200, v201\n\t" \
    "v_add_f32 v203, v200, v201\n\t" \
    "s_nop 1\n\t" \
    "v_permlane32_swap_b32 v202, v203\n\t" \
    "v_cvt_pk_bf16_f32 v200, v202, v203\n\t" \
    "v_min_u32 v200, -2, v200\n\t" \
    "buffer_store_dword v200, %[oo], %[rs], %[so] offen sc1\n\t" \
    ".Lre%=:\n\t"
#define ASM_RX \
    "s_cmp_eq_u32 %[fin], 0\n\t" \
    "s_cbranch_scc1 .Lx0%=\n\t" \
    "buffer_load_dwordx2 v[250:251], %[xo], %[rx], %[sx] offen sc1\n\t" \
    ".Lx0%=:\n\t" \
    "buffer_load_dword v248, %[fo], %[rs], %[fs] offen sc1\n\t" \
    "buffer_load_dwordx4 v[200:203], %[l16], %[rs], %[s0] offen offset:0 sc1\n\t" \
    "buffer_load_dwordx4 v[204:207], %[l16], %[rs], %[s0] offen offset:1024 sc1\n\t" \
    "buffer_load_dwordx4 v[208:211], %[l16], %[rs], %[s0] offen offset:2048 sc1\n\t" \
    "buffer_load_dwordx4 v[212:215], %[l16], %[rs], %[s0] offen offset:3072 sc1\n\t" \
    "buffer_load_dwordx4 v[216:219], %[l16], %[rs], %[s1] offen offset:0 sc1\n\t" \
    "buffer_load_dwordx4 v[220:223], %[l16], %[rs], %[s1] offen offset:1024 sc1\n\t" \
    "buffer_load_dwordx4 v[224:227], %[l16], %[rs], %[s1] offen offset:2048 sc1\n\t" \
    "buffer_load_dwordx4 v[228:231], %[l16], %[rs], %[s1] offen offset:3072 sc1\n\t" \
    "v_mov_b64 v[96:97], v[128:129]\n\t" \
    "v_mov_b64 v[98:99], v[132:133]\n\t" \
    "v_mov_b64 v[100:101], v[136:137]\n\t" \
    "v_mov_b64 v[102:103], v[140:141]\n\t" \
    "v_mov_b64 v[104:105], v[144:145]\n\t" \
    "v_mov_b64 v[106:107], v[148:149]\n\t" \
    "v_mov_b64 v[108:109], v[152:153]\n\t" \
    "v_mov_b64 v[110:111], v[156:157]\n\t" \
    "s_movk_i32 %[n], 0x200\n\t" \
    "s_nop 1\n\t" \
    "v_permlane16_swap_b32 v96, v98\n\t" \
    "v_permlane16_swap_b32 v97, v99\n\t" \
    "v_permlane16_swap_b32 v100, v102\n\t" \
    "v_permlane16_swap_b32 v101, v103\n\t" \
    "v_permlane16_swap_b32 v104, v106\n\t" \
    "v_permlane16_swap_b32 v105, v107\n\t" \
    "v_permlane16_swap_b32 v108, v110\n\t" \
    "v_permlane16_swap_b32 v109, v111\n\t" \
    "s_waitcnt vmcnt(7)\n\t" \
    "v_max_u32 v196, v200, v201\n\t" \
    "v_max3_u32 v196, v202, v203, v196\n\t" \
    "s_waitcnt vmcnt(6)\n\t" \
    "v_max3_u32 v196, v204, v205, v196\n\t" \
    "v_max3_u32 v196, v206, v207, v196\n\t" \
    "s_waitcnt vmcnt(5)\n\t" \
    "v_max3_u32 v196, v208, v209, v196\n\t" \
    "v_max3_u32 v196, v210, v211, v196\n\t" \
    "s_waitcnt vmcnt(4)\n\t" \
    "v_max3_u32 v196, v212, v213, v196\n\t" \
    "v_max3_u32 v196, v214, v215, v196\n\t" \
    "s_waitcnt vmcnt(3)\n\t" \
    "v_max3_u32 v196, v216, v217, v196\n\t" \
    "v_max3_u32 v196, v218, v219, v196\n\t" \
    "s_waitcnt vmcnt(2)\n\t" \
    "v_max3_u32 v196, v220, v221, v196\n\t" \
    "v_max3_u32 v196, v222, v223, v196\n\t" \
    "s_waitcnt vmcnt(1)\n\t" \
    "v_max3_u32 v196, v224, v225, v196\n\t" \
    "v_max3_u32 v196, v226, v227, v196\n\t" \
    "s_waitcnt vmcnt(0)\n\t" \
    "v_max3_u32 v196, v228, v229, v196\n\t" \
    "v_max3_u32 v196, v230, v231, v196\n\t" \
    "v_cmp_eq_u32 vcc, -1, v196\n\t" \
    "s_cbranch_vccz .Lxd%=\n\t" \
    ".Lxl%=:\n\t" \
    "s_mov_b32 %[a], 0\n\t" \
    "v_max_u32 v196, v200, v201\n\t" \
    "v_max3_u32 v196, v202, v203, v196\n\t" \
    "v_cmp_eq_u32 vcc, -1, v196\n\t" \
    "s_cbranch_vccz .Lxs0_%=\n\t" \
    "buffer_load_dwordx4 v[200:203], %[l16], %[rs], %[s0] offen offset:0 sc1\n\t" \
    "s_mov_b32 %[a], 1\n\t" \
    ".Lxs0_%=:\n\t" \
    "v_max_u32 v196, v204, v205\n\t" \
    "v_max3_u32 v196, v206, v207, v196\n\t" \
    "v_cmp_eq_u32 vcc, -1, v196\n\t" \
    "s_cbranch_vccz .Lxs1_%=\n\t" \
    "buffer_load_dwordx4 v[204:207], %[l16], %[rs], %[s0] offen offset:1024 sc1\n\t" \
    "s_mov_b32 %[a], 1\n\t" \
    ".Lxs1_%=:\n\t" \
    "v_max_u32 v196, v208, v209\n\t" \
    "v_max3_u32 v196, v210, v211, v196\n\t" \
    "v_cmp_eq_u32 vcc, -1, v196\n\t" \
    "s_cbranch_vccz .Lxs2_%=\n\t" \
    "buffer_load_dwordx4 v[208:211], %[l16], %[rs], %[s0] offen offset:2048 sc1\n\t" \
    "s_mov_b32 %[a], 1\n\t" \
    ".Lxs2_%=:\n\t" \
    "v_max_u32 v196, v212, v213\n\t" \
    "v_max3_u32 v196, v214, v215, v196\n\t" \
    "v_cmp_eq_u32 vcc, -1, v196\n\t" \
    "s_cbranch_vccz .Lxs3_%=\n\t" \
    "buffer_load_dwordx4 v[212:215], %[l16], %[rs], %[s0] offen offset:3072 sc1\n\t" \
    "s_mov_b32 %[a], 1\n\t" \
    ".Lxs3_%=:\n\t" \
    "v_max_u32 v196, v216, v217\n\t" \
    "v_max3_u32 v196, v218, v219, v196\n\t" \
    "v_cmp_eq_u32 vcc, -1, v196\n\t" \
    "s_cbranch_vccz .Lxs4_%=\n\t" \
    "buffer_load_dwordx4 v[216:219], %[l16], %[rs], %[s1] offen offset:0 sc1\n\t" \
    "s_mov_b32 %[a], 1\n\t" \
    ".Lxs4_%=:\n\t" \
    "v_max_u32 v196, v220, v221\n\t" \
    "v_max3_u32 v196, v222, v223, v196\n\t" \
    "v_cmp_eq_u32 vcc, -1, v196\n\t" \
    "s_cbranch_vccz .Lxs5_%=\n\t" \
    "buffer_load_dwordx4 v[220:223], %[l16], %[rs], %[s1] offen offset:1024 sc1\n\t" \
    "s_mov_b32 %[a], 1\n\t" \
    ".Lxs5_%=:\n\t" \
    "v_max_u32 v196, v224, v225\n\t" \
    "v_max3_u32 v196, v226, v227, v196\n\t" \
    "v_cmp_eq_u32 vcc, -1, v196\n\t" \
    "s_cbranch_vccz .Lxs6_%=\n\t" \
    "buffer_load_dwordx4 v[224:227], %[l16], %[rs], %[s1] offen offset:2048 sc1\n\t" \
    "s_mov_b32 %[a], 1\n\t" \
    ".Lxs6_%=:\n\t" \
    "v_max_u32 v196, v228, v229\n\t" \
    "v_max3_u32 v196, v230, v231, v196\n\t" \
    "v_cmp_eq_u32 vcc, -1, v196\n\t" \
    "s_cbranch_vccz .Lxs7_%=\n\t" \
    "buffer_load_dwordx4 v[228:231], %[l16], %[rs], %[s1] offen offset:3072 sc1\n\t" \
    "s_mov_b32 %[a], 1\n\t" \
    ".Lxs7_%=:\n\t" \
    "s_cmp_eq_u32 %[a], 0\n\t" \
    "s_cbranch_scc1 .Lxd%=\n\t" \
    "s_waitcnt vmcnt(0)\n\t" \
    "s_sub_u32 %[n], %[n], 1\n\t" \
    "s_cmp_lg_u32 %[n], 0\n\t" \
    "s_cbranch_scc1 .Lxl%=\n\t" \
    "s_branch .Lxe%=\n\t" \
    ".Lxd%=:\n\t" \
    ASM_ST2 \
    "v_mfma_f32_16x16x32_bf16 v[120:123], v[128:131], v[200:203], 0\n\t" \
    "v_mfma_f32_16x16x32_bf16 v[124:127], v[144:147], v[200:203], 0\n\t" \
    "v_mfma_f32_16x16x32_bf16 v[120:123], v[132:135], v[204:207], v[120:123]\n\t" \
    "v_mfma_f32_16x16x32_bf16 v[124:127], v[148:151], v[204:207], v[124:127]\n\t" \
    "v_mfma_f32_16x16x32_bf16 v[120:123], v[136:139], v[208:211], v[120:123]\n\t" \
    "v_mfma_f32_16x16x32_bf16 v[124:127], v[152:155], v[208:211], v[124:127]\n\t" \
    "v_mfma_f32_16x16x32_bf16 v[120:123], v[140:143], v[212:215], v[120:123]\n\t" \
    "v_mfma_f32_16x16x32_bf16 v[124:127], v[156:159], v[212:215], v[124:127]\n\t" \
    "v_mfma_f32_16x16x32_bf16 v[112:115], v[96:99], v[220:223], 0\n\t" \
    "v_mfma_f32_16x16x32_bf16 v[116:119], v[104:107], v[220:223], 0\n\t" \
    "v_mfma_f32_16x16x32_bf16 v[120:123], v[176:179], v[216:219], v[120:123]\n\t" \
    "v_mfma_f32_16x16x32_bf16 v[124:127], v[180:183], v[216:219], v[124:127]\n\t" \
    "v_mfma_f32_16x16x32_bf16 v[112:115], v[100:103], v[224:227], v[112:115]\n\t" \
    "v_mfma_f32_16x16x32_bf16 v[116:119], v[108:111], v[224:227], v[116:119]\n\t" \
    "v_mfma_f32_16x16x32_bf16 v[112:115], v[176:179], v[228:231], v[112:115]\n\t" \
    "v_mfma_f32_16x16x32_bf16 v[116:119], v[180:183], v[228:231], v[116:119]\n\t" \
    "s_nop 1\n\t" \
    "ds_write_b128 %[pw], v[120:123] offset:16384\n\t" \
    "ds_write_b128 %[pw], v[124:127] offset:24576\n\t" \
    "s_nop 3\n\t" \
    "ds_write_b128 %[pw], v[112:115] offset:0\n\t" \
    "ds_write_b128 %[pw], v[116:119] offset:8192\n\t" \
    "v_cmp_ge_u32 vcc, %[tn], v248\n\t" \
    "s_mov_b32 %[a], 0\n\t" \
    "s_cbranch_vccnz .Lxv%=\n\t" \
    "buffer_load_dwordx4 v[128:131], %[l16], %[rp], %[sv0] offen offset:0 sc1\n\t" \
    "buffer_load_dwordx4 v[132:135], %[l16], %[rp], %[sv0] offen offset:1024 sc1\n\t" \
    "buffer_load_dwordx4 v[136:139], %[l16], %[rp], %[sv0] offen offset:2048 sc1\n\t" \
    "buffer_load_dwordx4 v[140:143], %[l16], %[rp], %[sv0] offen offset:3072 sc1\n\t" \
    "buffer_load_dwordx4 v[144:147], %[l16], %[rp], %[sv1] offen offset:0 sc1\n\t" \
    "buffer_load_dwordx4 v[148:151], %[l16], %[rp], %[sv1] offen offset:1024 sc1\n\t" \
    "buffer_load_dwordx4 v[152:155], %[l16], %[rp], %[sv1] offen offset:2048 sc1\n\t" \
    "buffer_load_dwordx4 v[156:159], %[l16], %[rp], %[sv1] offen offset:3072 sc1\n\t" \
    "s_mov_b32 %[a], 1\n\t" \
    ".Lxv%=:\n\t" \
    "s_cmp_eq_u32 %[fin], 0\n\t" \
    "s_waitcnt lgkmcnt(0)\n\t" \
    "s_barrier\n\t" ASM_ST \
    "s_cbranch_scc1 .Lxe%=\n\t" \
    "ds_read_b128 v[200:203], %[pr] offset:0\n\t" \
    "ds_read_b128 v[204:207], %[pr] offset:1024\n\t" \
    "ds_read_b128 v[208:211], %[pr] offset:2048\n\t" \
    "ds_read_b128 v[212:215], %[pr] offset:3072\n\t" \
    "ds_read_b128 v[216:219], %[pr] offset:4096\n\t" \
    "ds_read_b128 v[220:223], %[pr] offset:5120\n\t" \
    "ds_read_b128 v[224:227], %[pr] offset:6144\n\t" \
    "ds_read_b128 v[228:231], %[pr] offset:7168\n\t" \
    "v_lshlrev_b32 v240, 16, v250\n\t" \
    "v_and_b32 v241, 0xffff0000, v250\n\t" \
    "v_lshlrev_b32 v242, 16, v251\n\t" \
    "v_and_b32 v243, 0xffff0000, v251\n\t" \
    "s_waitcnt lgkmcnt(6)\n\t" \
    "v_pk_add_f32 v[200:201], v[200:201], v[204:205]\n\t" \
    "v_pk_add_f32 v[202:203], v[202:203], v[206:207]\n\t" \
    "s_waitcnt lgkmcnt(4)\n\t" \
    "v_pk_add_f32 v[208:209], v[208:209], v[212:213]\n\t" \
    "v_pk_add_f32 v[210:211], v[210:211], v[214:215]\n\t" \
    "v_pk_add_f32 v[200:201], v[200:201], v[208:209]\n\t" \
    "v_pk_add_f32 v[202:203], v[202:203], v[210:211]\n\t" \
    "s_waitcnt lgkmcnt(2)\n\t" \
    "v_pk_add_f32 v[216:217], v[216:217], v[220:221]\n\t" \
    "v_pk_add_f32 v[218:219], v[218:219], v[222:223]\n\t" \
    "s_waitcnt lgkmcnt(0)\n\t" \
    "v_pk_add_f32 v[224:225], v[224:225], v[228:229]\n\t" \
    "v_pk_add_f32 v[226:227], v[226:227], v[230:231]\n\t" \
    "v_pk_add_f32 v[216:217], v[216:217], v[224:225]\n\t" \
    "v_pk_add_f32 v[218:219], v[218:219], v[226:227]\n\t" \
    "v_pk_add_f32 v[200:201], v[200:201], v[216:217]\n\t" \
    "v_pk_add_f32 v[202:203], v[202:203], v[218:219]\n\t" \
    "v_pk_add_f32 v[200:201], v[200:201], v[240:241]\n\t" \
    "v_pk_add_f32 v[202:203], v[202:203], v[242:243]\n\t" \
    "v_cvt_pk_bf16_f32 v200, v200, v201\n\t" \
    "v_cvt_pk_bf16_f32 v201, v202, v203\n\t" \
    "v_min_u32 v200, -2, v200\n\t" \
    "v_min_u32 v201, -2, v201\n\t" \
    "buffer_store_dwordx2 v[200:201], %[xo], %[rd], %[sa] offen sc1\n\t" \
    ".Lxe%=:\n\t"
#define ASM_W4_M \
    ".Lc7_%=:\n\t" \
    "s_waitcnt vmcnt(0)\n\t" \
    "v_max_u32 v212, v188, v189\n\t" \
    "v_max3_u32 v212, v190, v191, v212\n\t" \
    "v_cmp_eq_u32 vcc, -1, v212\n\t" \
    "s_cbranch_vccnz .Ls7_%=\n\t" \
    ".Lk7_%=:\n\t" \
    ASM_ST2 \
    "buffer_load_dwordx4 v[184:187], %[l16], %[rs], %[s1] offen offset:2048 sc1\n\t" \
    "buffer_load_dwordx4 v[180:183], %[l16], %[rs], %[s1] offen offset:1024 sc1\n\t" \
    "buffer_load_dwordx4 v[176:179], %[l16], %[rs], %[s1] offen offset:0 sc1\n\t" \
    "buffer_load_dwordx4 v[172:175], %[l16], %[rs], %[s0] offen offset:3072 sc1\n\t" \
    "buffer_load_dwordx4 v[168:171], %[l16], %[rs], %[s0] offen offset:2048 sc1\n\t" \
    "buffer_load_dwordx4 v[164:167], %[l16], %[rs], %[s0] offen offset:1024 sc1\n\t" \
    "buffer_load_dwordx4 v[160:163], %[l16], %[rs], %[s0] offen offset:0 sc1\n\t" \
    "v_mfma_f32_16x16x32_bf16 v[208:211], v[156:159], v[188:191], 0\n\t" \
    "v_mfma_f32_16x16x32_bf16 v[204:207], v[124:127], v[188:191], 0\n\t" \
    "v_mfma_f32_16x16x32_bf16 v[200:203], v[92:95], v[188:191], 0\n\t" \
    "v_mfma_f32_16x16x32_bf16 v[196:199], v[60:63], v[188:191], 0\n\t" \
    "v_mfma_f32_16x16x32_bf16 v[192:195], v[28:31], v[188:191], 0\n\t" \
    ".Lc6_%=:\n\t" \
    "s_waitcnt vmcnt(6)\n\t" \
    "v_max_u32 v212, v184, v185\n\t" \
    "v_max3_u32 v212, v186, v187, v212\n\t" \
    "v_cmp_eq_u32 vcc, -1, v212\n\t" \
    "s_cbranch_vccnz .Ls6_%=\n\t" \
    ".Lk6_%=:\n\t" \
    ".Lc5_%=:\n\t" \
    "s_waitcnt vmcnt(5)\n\t" \
    "v_max_u32 v212, v180, v181\n\t" \
    "v_max3_u32 v212, v182, v183, v212\n\t" \
    "v_cmp_eq_u32 vcc, -1, v212\n\t" \
    "s_cbranch_vccnz .Ls5_%=\n\t" \
    ".Lk5_%=:\n\t" \
    "v_mfma_f32_16x16x32_bf16 v[192:195], v[24:27], v[184:187], v[192:195]\n\t" \
    "v_mfma_f32_16x16x32_bf16 v[192:195], v[20:23], v[180:183], v[192:195]\n\t" \
    "v_mfma_f32_16x16x32_bf16 v[196:199], v[56:59], v[184:187], v[196:199]\n\t" \
    "v_mfma_f32_16x16x32_bf16 v[196:199], v[52:55], v[180:183], v[196:199]\n\t" \
    "v_mfma_f32_16x16x32_bf16 v[200:203], v[88:91], v[184:187], v[200:203]\n\t" \
    "v_mfma_f32_16x16x32_bf16 v[200:203], v[84:87], v[180:183], v[200:203]\n\t" \
    "v_mfma_f32_16x16x32_bf16 v[204:207], v[120:123], v[184:187], v[204:207]\n\t" \
    "v_mfma_f32_16x16x32_bf16 v[204:207], v[116:119], v[180:183], v[204:207]\n\t" \
    "v_mfma_f32_16x16x32_bf16 v[208:211], v[152:155], v[184:187], v[208:211]\n\t" \
    "v_mfma_f32_16x16x32_bf16 v[208:211], v[148:151], v[180:183], v[208:211]\n\t" \
    ".Lc4_%=:\n\t" \
    "s_waitcnt vmcnt(4)\n\t" \
    "v_max_u32 v212, v176, v177\n\t" \
    "v_max3_u32 v212, v178, v179, v212\n\t" \
    "v_cmp_eq_u32 vcc, -1, v212\n\t" \
    "s_cbranch_vccnz .Ls4_%=\n\t" \
    ".Lk4_%=:\n\t" \
    ".Lc3_%=:\n\t" \
    "s_waitcnt vmcnt(3)\n\t" \
    "v_max_u32 v212, v172, v173\n\t" \
    "v_max3_u32 v212, v174, v175, v212\n\t" \
    "v_cmp_eq_u32 vcc, -1, v212\n\t" \
    "s_cbranch_vccnz .Ls3_%=\n\t" \
    ".Lk3_%=:\n\t" \
    "v_mfma_f32_16x16x32_bf16 v[208:211], v[144:147], v[176:179], v[208:211]\n\t" \
    "v_mfma_f32_16x16x32_bf16 v[208:211], v[140:143], v[172:175], v[208:211]\n\t" \
    "v_mfma_f32_16x16x32_bf16 v[204:207], v[112:115], v[176:179], v[204:207]\n\t" \
    "v_mfma_f32_16x16x32_bf16 v[204:207], v[108:111], v[172:175], v[204:207]\n\t" \
    "v_mfma_f32_16x16x32_bf16 v[200:203], v[80:83], v[176:179], v[200:203]\n\t" \
    "v_mfma_f32_16x16x32_bf16 v[200:203], v[76:79], v[172:175], v[200:203]\n\t" \
    "v_mfma_f32_16x16x32_bf16 v[196:199], v[48:51], v[176:179], v[196:199]\n\t" \
    "v_mfma_f32_16x16x32_bf16 v[196:199], v[44:47], v[172:175], v[196:199]\n\t" \
    "v_mfma_f32_16x16x32_bf16 v[192:195], v[16:19], v[176:179], v[192:195]\n\t" \
    "v_mfma_f32_16x16x32_bf16 v[192:195], v[12:15], v[172:175], v[192:195]\n\t" \
    ".Lc2_%=:\n\t" \
    "s_waitcnt vmcnt(2)\n\t" \
    "v_max_u32 v212, v168, v169\n\t" \
    "v_max3_u32 v212, v170, v171, v212\n\t" \
    "v_cmp_eq_u32 vcc, -1, v212\n\t" \
    "s_cbranch_vccnz .Ls2_%=\n\t" \
    ".Lk2_%=:\n\t" \
    "buffer_load_dwordx4 v[188:191], %[l16], %[rs], %[n1] offen offset:3072 sc1\n\t" \
    ".Lc1_%=:\n\t" \
    "s_waitcnt vmcnt(2)\n\t" \
    "v_max_u32 v212, v164, v165\n\t" \
    "v_max3_u32 v212, v166, v167, v212\n\t" \
    "v_cmp_eq_u32 vcc, -1, v212\n\t" \
    "s_cbranch_vccnz .Ls1_%=\n\t" \
    ".Lk1_%=:\n\t" \
    ".Lc0_%=:\n\t" \
    "s_waitcnt vmcnt(1)\n\t" \
    "v_max_u32 v212, v160, v161\n\t" \
    "v_max3_u32 v212, v162, v163, v212\n\t" \
    "v_cmp_eq_u32 vcc, -1, v212\n\t" \
    "s_cbranch_vccnz .Ls0_%=\n\t" \
    ".Lk0_%=:\n\t" \
    "v_mfma_f32_16x16x32_bf16 v[192:195], v[8:11], v[168:171], v[192:195]\n\t" \
    "v_mfma_f32_16x16x32_bf16 v[192:195], v[4:7], v[164:167], v[192:195]\n\t" \
    "v_mfma_f32_16x16x32_bf16 v[192:195], v[0:3], v[160:163], v[192:195]\n\t" \
    "v_mfma_f32_16x16x32_bf16 v[196:199], v[40:43], v[168:171], v[196:199]\n\t" \
    "v_mfma_f32_16x16x32_bf16 v[196:199], v[36:39], v[164:167], v[196:199]\n\t" \
    "v_mfma_f32_16x16x32_bf16 v[196:199], v[32:35], v[160:163], v[196:199]\n\t" \
    "v_mfma_f32_16x16x32_bf16 v[200:203], v[72:75], v[168:171], v[200:203]\n\t" \
    "v_mfma_f32_16x16x32_bf16 v[200:203], v[68:71], v[164:167], v[200:203]\n\t" \
    "v_mfma_f32_16x16x32_bf16 v[200:203], v[64:67], v[160:163], v[200:203]\n\t" \
    "v_mfma_f32_16x16x32_bf16 v[204:207], v[104:107], v[168:171], v[204:207]\n\t" \
    "v_mfma_f32_16x16x32_bf16 v[204:207], v[100:103], v[164:167], v[204:207]\n\t" \
    "v_mfma_f32_16x16x32_bf16 v[204:207], v[96:99], v[160:163], v[204:207]\n\t" \
    "v_mfma_f32_16x16x32_bf16 v[208:211], v[136:139], v[168:171], v[208:211]\n\t" \
    "v_mfma_f32_16x16x32_bf16 v[208:211], v[132:135], v[164:167], v[208:211]\n\t" \
    "v_mfma_f32_16x16x32_bf16 v[208:211], v[128:131], v[160:163], v[208:211]\n\t" \
    "v_mov_b32 v212, %[un]\n\t" \
    "ds_write_b128 %[ld], v[192:195] offset:0\n\t" \
    "ds_write_b128 %[ld], v[196:199] offset:6144\n\t" \
    "ds_write_b128 %[ld], v[200:203] offset:12288\n\t" \
    "ds_write_b128 %[ld], v[204:207] offset:18432\n\t" \
    "s_nop 2\n\t" \
    "ds_write_b128 %[ld], v[208:211] offset:24576\n\t" \
    "ds_write_b32 %[fl], v212\n\t" \
    "s_branch .Lme%=\n\t" \
    ".Ls7_%=:\n\t" \
    "s_cmp_eq_u32 %[n], 0\n\t" \
    "s_cbranch_scc1 .Lk7_%=\n\t" \
    "s_sub_u32 %[n], %[n], 1\n\t" \
    "s_sleep 1\n\t" \
    "buffer_load_dwordx4 v[188:191], %[l16], %[rs], %[s1] offen offset:3072 sc1\n\t" \
    "s_branch .Lc7_%=\n\t" \
    ".Ls6_%=:\n\t" \
    "s_cmp_eq_u32 %[n], 0\n\t" \
    "s_cbranch_scc1 .Lk6_%=\n\t" \
    "s_sub_u32 %[n], %[n], 1\n\t" \
    "buffer_load_dwordx4 v[184:187], %[l16], %[rs], %[s1] offen offset:2048 sc1\n\t" \
    "s_waitcnt vmcnt(0)\n\t" \
    "s_branch .Lc6_%=\n\t" \
    ".Ls5_%=:\n\t" \
    "s_cmp_eq_u32 %[n], 0\n\t" \
    "s_cbranch_scc1 .Lk5_%=\n\t" \
    "s_sub_u32 %[n], %[n], 1\n\t" \
    "buffer_load_dwordx4 v[180:183], %[l16], %[rs], %[s1] offen offset:1024 sc1\n\t" \
    "s_waitcnt vmcnt(0)\n\t" \
    "s_branch .Lc5_%=\n\t" \
    ".Ls4_%=:\n\t" \
    "s_cmp_eq_u32 %[n], 0\n\t" \
    "s_cbranch_scc1 .Lk4_%=\n\t" \
    "s_sub_u32 %[n], %[n], 1\n\t" \
    "buffer_load_dwordx4 v[176:179], %[l16], %[rs], %[s1] offen offset:0 sc1\n\t" \
    "s_waitcnt vmcnt(0)\n\t" \
    "s_branch .Lc4_%=\n\t" \
    ".Ls3_%=:\n\t" \
    "s_cmp_eq_u32 %[n], 0\n\t" \
    "s_cbranch_scc1 .Lk3_%=\n\t" \
    "s_sub_u32 %[n], %[n], 1\n\t" \
    "buffer_load_dwordx4 v[172:175], %[l16], %[rs], %[s0] offen offset:3072 sc1\n\t" \
    "s_waitcnt vmcnt(0)\n\t" \
    "s_branch .Lc3_%=\n\t" \
    ".Ls2_%=:\n\t" \
    "s_cmp_eq_u32 %[n], 0\n\t" \
    "s_cbranch_scc1 .Lk2_%=\n\t" \
    "s_sub_u32 %[n], %[n], 1\n\t" \
    "buffer_load_dwordx4 v[168:171], %[l16], %[rs], %[s0] offen offset:2048 sc1\n\t" \
    "s_waitcnt vmcnt(0)\n\t" \
    "s_branch .Lc2_%=\n\t" \
    ".Ls1_%=:\n\t" \
    "s_cmp_eq_u32 %[n], 0\n\t" \
    "s_cbranch_scc1 .Lk1_%=\n\t" \
    "s_sub_u32 %[n], %[n], 1\n\t" \
    "buffer_load_dwordx4 v[164:167], %[l16], %[rs], %[s0] offen offset:1024 sc1\n\t" \
    "s_waitcnt vmcnt(0)\n\t" \
    "s_branch .Lc1_%=\n\t" \
    ".Ls0_%=:\n\t" \
    "s_cmp_eq_u32 %[n], 0\n\t" \
    "s_cbranch_scc1 .Lk0_%=\n\t" \
    "s_sub_u32 %[n], %[n], 1\n\t" \
    "buffer_load_dwordx4 v[160:163], %[l16], %[rs], %[s0] offen offset:0 sc1\n\t" \
    "s_waitcnt vmcnt(0)\n\t" \
    "s_branch .Lc0_%=\n\t" \
    ".Lme%=:\n\t"
#define ASM_W4F_0 \
    "s_branch .Lf0%=\n\t" \
    ".Lfr%=:\n\t" \
    "s_cmp_eq_u32 %[n], 0\n\t" \
    "s_cbranch_scc1 .Lfb%=\n\t" \
    "s_sub_u32 %[n], %[n], 1\n\t" \
    "s_sleep 6\n\t" \
    "buffer_load_dwordx2 v[164:165], %[o0], %[rd], %[c1] offen sc1\n\t" \
    "buffer_load_dwordx2 v[166:167], %[o1], %[rd], %[c1] offen sc1\n\t" \
    "buffer_load_dwordx2 v[168:169], %[o2], %[rd], %[c1] offen sc1\n\t" \
    "buffer_load_dwordx2 v[170:171], %[o3], %[rd], %[c1] offen sc1\n\t" \
    "buffer_load_dwordx2 v[172:173], %[o4], %[rd], %[c1] offen sc1\n\t" \
    "buffer_load_dwordx2 v[174:175], %[o0], %[rd], %[ca] offen sc1\n\t" \
    "buffer_load_dwordx2 v[176:177], %[o1], %[rd], %[ca] offen sc1\n\t" \
    "buffer_load_dwordx2 v[178:179], %[o2], %[rd], %[ca] offen sc1\n\t" \
    "buffer_load_dwordx2 v[180:181], %[o3], %[rd], %[ca] offen sc1\n\t" \
    "buffer_load_dwordx2 v[182:183], %[o4], %[rd], %[ca] offen sc1\n\t" \
    "s_waitcnt vmcnt(0)\n\t" \
    "s_branch .Lfa%=\n\t" \
    ".Lfs0_%=:\n\t" \
    "s_cmp_eq_u32 %[n], 0\n\t" \
    "s_cbranch_scc1 .Lfu0_%=\n\t" \
    "s_sub_u32 %[n], %[n], 1\n\t" \
    "s_sleep 1\n\t" \
    "s_branch .Lft0_%=\n\t" \
    ".Lfs1_%=:\n\t" \
    "s_cmp_eq_u32 %[n], 0\n\t" \
    "s_cbranch_scc1 .Lfu1_%=\n\t" \
    "s_sub_u32 %[n], %[n], 1\n\t" \
    "s_sleep 1\n\t" \
    "s_branch .Lft1_%=\n\t" \
    ".Lfx%=:\n\t" \
    "s_cmp_eq_u32 %[n], 0\n\t" \
    "s_cbranch_scc1 .Lfz%=\n\t" \
    "s_sub_u32 %[n], %[n], 1\n\t" \
    "s_branch .Lfy%=\n\t" \
    ".Lf0%=:\n\t" \
    "s_waitcnt vmcnt(1)\n\t" \
    ".Lfa%=:\n\t" \
    "v_max_u32 v194, v164, v165\n\t" \
    "v_max3_u32 v194, v166, v167, v194\n\t" \
    "v_max3_u32 v194, v168, v169, v194\n\t" \
    "v_max3_u32 v194, v170, v171, v194\n\t" \
    "v_max3_u32 v194, v172, v173, v194\n\t" \
    "v_max3_u32 v194, v174, v175, v194\n\t" \
    "v_max3_u32 v194, v176, v177, v194\n\t" \
    "v_max3_u32 v194, v178, v179, v194\n\t" \
    "v_max3_u32 v194, v180, v181, v194\n\t" \
    "v_max3_u32 v194, v182, v183, v194\n\t" \
    "v_cmp_eq_u32 vcc, -1, v194\n\t" \
    "s_cbranch_vccnz .Lfr%=\n\t" \
    ".Lfb%=:\n\t" \
    "v_lshlrev_b32 v64, 16, v164\n\t" \
    "v_and_b32 v65, 0xffff0000, v164\n\t" \
    "v_lshlrev_b32 v84, 16, v174\n\t" \
    "v_and_b32 v85, 0xffff0000, v174\n\t" \
    "v_lshlrev_b32 v66, 16, v165\n\t" \
    "v_and_b32 v67, 0xffff0000, v165\n\t" \
    "v_lshlrev_b32 v86, 16, v175\n\t" \
    "v_and_b32 v87, 0xffff0000, v175\n\t" \
    "v_lshlrev_b32 v68, 16, v166\n\t" \
    "v_and_b32 v69, 0xffff0000, v166\n\t" \
    "v_lshlrev_b32 v88, 16, v176\n\t" \
    "v_and_b32 v89, 0xffff0000, v176\n\t" \
    "v_lshlrev_b32 v70, 16, v167\n\t" \
    "v_and_b32 v71, 0xffff0000, v167\n\t" \
    "v_lshlrev_b32 v90, 16, v177\n\t" \
    "v_and_b32 v91, 0xffff0000, v177\n\t" \
    "v_lshlrev_b32 v72, 16, v168\n\t" \
    "v_and_b32 v73, 0xffff0000, v168\n\t" \
    "v_lshlrev_b32 v92, 16, v178\n\t" \
    "v_and_b32 v93, 0xffff0000, v178\n\t" \
    "v_lshlrev_b32 v74, 16, v169\n\t" \
    "v_and_b32 v75, 0xffff0000, v169\n\t" \
    "v_lshlrev_b32 v94, 16, v179\n\t" \
    "v_and_b32 v95, 0xffff0000, v179\n\t" \
    "v_lshlrev_b32 v76, 16, v170\n\t" \
    "v_and_b32 v77, 0xffff0000, v170\n\t" \
    "v_lshlrev_b32 v96, 16, v180\n\t" \
    "v_and_b32 v97, 0xffff0000, v180\n\t" \
    "v_lshlrev_b32 v78, 16, v171\n\t" \
    "v_and_b32 v79, 0xffff0000, v171\n\t" \
    "v_lshlrev_b32 v98, 16, v181\n\t" \
    "v_and_b32 v99, 0xffff0000, v181\n\t" \
    "v_lshlrev_b32 v80, 16, v172\n\t" \
    "v_and_b32 v81, 0xffff0000, v172\n\t" \
    "v_lshlrev_b32 v100, 16, v182\n\t" \
    "v_and_b32 v101, 0xffff0000, v182\n\t" \
    "v_lshlrev_b32 v82, 16, v173\n\t" \
    "v_and_b32 v83, 0xffff0000, v173\n\t" \
    "v_lshlrev_b32 v102, 16, v183\n\t" \
    "v_and_b32 v103, 0xffff0000, v183\n\t" \
    "v_pk_add_f32 v[64:65], v[64:65], v[84:85]\n\t" \
    "v_pk_add_f32 v[66:67], v[66:67], v[86:87]\n\t" \
    "v_pk_add_f32 v[68:69], v[68:69], v[88:89]\n\t" \
    "v_pk_add_f32 v[70:71], v[70:71], v[90:91]\n\t" \
    "v_pk_add_f32 v[72:73], v[72:73], v[92:93]\n\t" \
    "v_pk_add_f32 v[74:75], v[74:75], v[94:95]\n\t" \
    "v_pk_add_f32 v[76:77], v[76:77], v[96:97]\n\t" \
    "v_pk_add_f32 v[78:79], v[78:79], v[98:99]\n\t" \
    "v_pk_add_f32 v[80:81], v[80:81], v[100:101]\n\t" \
    "v_pk_add_f32 v[82:83], v[82:83], v[102:103]\n\t" \
    ".Lft0_%=:\n\t" \
    "ds_read_b32 v194, %[f0]\n\t" \
    "s_waitcnt lgkmcnt(0)\n\t" \
    "v_cmp_ge_u32 vcc, %[u], v194\n\t" \
    "s_cbranch_vccnz .Lfs0_%=\n\t" \
    ".Lfu0_%=:\n\t" \
    "ds_read_b128 v[84:87], %[la] offset:0\n\t" \
    "ds_read_b128 v[88:91], %[la] offset:6144\n\t" \
    "ds_read_b128 v[92:95], %[la] offset:12288\n\t" \
    "ds_read_b128 v[96:99], %[la] offset:18432\n\t" \
    "ds_read_b128 v[100:103], %[la] offset:24576\n\t" \
    "ds_read_b128 v[104:107], %[la] offset:1024\n\t" \
    "ds_read_b128 v[108:111], %[la] offset:7168\n\t" \
    "ds_read_b128 v[112:115], %[la] offset:13312\n\t" \
    "ds_read_b128 v[116:119], %[la] offset:19456\n\t" \
    "ds_read_b128 v[120:123], %[la] offset:25600\n\t" \
    "ds_read_b128 v[124:127], %[la] offset:2048\n\t" \
    "s_waitcnt lgkmcnt(10)\n\t" \
    "v_pk_add_f32 v[64:65], v[64:65], v[84:85]\n\t" \
    "v_pk_add_f32 v[66:67], v[66:67], v[86:87]\n\t" \
    "ds_read_b128 v[128:131], %[la] offset:8192\n\t" \
    "s_waitcnt lgkmcnt(10)\n\t" \
    "v_pk_add_f32 v[68:69], v[68:69], v[88:89]\n\t" \
    "v_pk_add_f32 v[70:71], v[70:71], v[90:91]\n\t" \
    "ds_read_b128 v[132:135], %[la] offset:14336\n\t" \
    "s_waitcnt lgkmcnt(10)\n\t" \
    "v_pk_add_f32 v[72:73], v[72:73], v[92:93]\n\t" \
    "v_pk_add_f32 v[74:75], v[74:75], v[94:95]\n\t" \
    "ds_read_b128 v[136:139], %[la] offset:20480\n\t" \
    "s_waitcnt lgkmcnt(10)\n\t" \
    "v_pk_add_f32 v[76:77], v[76:77], v[96:97]\n\t" \
    "v_pk_add_f32 v[78:79], v[78:79], v[98:99]\n\t" \
    "ds_read_b128 v[140:143], %[la] offset:26624\n\t" \
    "s_waitcnt lgkmcnt(10)\n\t" \
    "v_pk_add_f32 v[80:81], v[80:81], v[100:101]\n\t" \
    "v_pk_add_f32 v[82:83], v[82:83], v[102:103]\n\t" \
    "ds_read_b128 v[144:147], %[la] offset:3072\n\t" \
    "s_waitcnt lgkmcnt(10)\n\t" \
    "v_pk_add_f32 v[64:65], v[64:65], v[104:105]\n\t" \
    "v_pk_add_f32 v[66:67], v[66:67], v[106:107]\n\t" \
    "ds_read_b128 v[148:151], %[la] offset:9216\n\t" \
    "s_waitcnt lgkmcnt(10)\n\t" \
    "v_pk_add_f32 v[68:69], v[68:69], v[108:109]\n\t" \
    "v_pk_add_f32 v[70:71], v[70:71], v[110:111]\n\t" \
    "ds_read_b128 v[152:155], %[la] offset:15360\n\t" \
    "s_waitcnt lgkmcnt(10)\n\t" \
    "v_pk_add_f32 v[72:73], v[72:73], v[112:113]\n\t" \
    "v_pk_add_f32 v[74:75], v[74:75], v[114:115]\n\t" \
    "ds_read_b128 v[156:159], %[la] offset:21504\n\t" \
    "s_waitcnt lgkmcnt(10)\n\t" \
    "v_pk_add_f32 v[76:77], v[76:77], v[116:117]\n\t" \
    "v_pk_add_f32 v[78:79], v[78:79], v[118:119]\n\t" \
    "ds_read_b128 v[160:163], %[la] offset:27648\n\t" \
    "s_waitcnt lgkmcnt(10)\n\t" \
    "v_pk_add_f32 v[80:81], v[80:81], v[120:121]\n\t" \
    "v_pk_add_f32 v[82:83], v[82:83], v[122:123]\n\t" \
    "s_waitcnt lgkmcnt(9)\n\t" \
    "v_pk_add_f32 v[64:65], v[64:65], v[124:125]\n\t" \
    "v_pk_add_f32 v[66:67], v[66:67], v[126:127]\n\t" \
    "s_waitcnt lgkmcnt(8)\n\t" \
    "v_pk_add_f32 v[68:69], v[68:69], v[128:129]\n\t" \
    "v_pk_add_f32 v[70:71], v[70:71], v[130:131]\n\t" \
    "s_waitcnt lgkmcnt(7)\n\t" \
    "v_pk_add_f32 v[72:73], v[72:73], v[132:133]\n\t" \
    "v_pk_add_f32 v[74:75], v[74:75], v[134:135]\n\t" \
    "s_waitcnt lgkmcnt(6)\n\t" \
    "v_pk_add_f32 v[76:77], v[76:77], v[136:137]\n\t" \
    "v_pk_add_f32 v[78:79], v[78:79], v[138:139]\n\t" \
    "s_waitcnt lgkmcnt(5)\n\t" \
    "v_pk_add_f32 v[80:81], v[80:81], v[140:141]\n\t" \
    "v_pk_add_f32 v[82:83], v[82:83], v[142:143]\n\t" \
    "s_waitcnt lgkmcnt(4)\n\t" \
    "v_pk_add_f32 v[64:65], v[64:65], v[144:145]\n\t" \
    "v_pk_add_f32 v[66:67], v[66:67], v[146:147]\n\t" \
    "s_waitcnt lgkmcnt(3)\n\t" \
    "v_pk_add_f32 v[68:69], v[68:69], v[148:149]\n\t" \
    "v_pk_add_f32 v[70:71], v[70:71], v[150:151]\n\t" \
    "s_waitcnt lgkmcnt(2)\n\t" \
    "v_pk_add_f32 v[72:73], v[72:73], v[152:153]\n\t" \
    "v_pk_add_f32 v[74:75], v[74:75], v[154:155]\n\t" \
    "s_waitcnt lgkmcnt(1)\n\t" \
    "v_pk_add_f32 v[76:77], v[76:77], v[156:157]\n\t" \
    "v_pk_add_f32 v[78:79], v[78:79], v[158:159]\n\t" \
    "s_waitcnt lgkmcnt(0)\n\t" \
    "v_pk_add_f32 v[80:81], v[80:81], v[160:161]\n\t" \
    "v_pk_add_f32 v[82:83], v[82:83], v[162:163]\n\t" \
    ".Lft1_%=:\n\t" \
    "ds_read_b32 v194, %[f1]\n\t" \
    "s_waitcnt lgkmcnt(0)\n\t" \
    "v_cmp_ge_u32 vcc, %[u], v194\n\t" \
    "s_cbranch_vccnz .Lfs1_%=\n\t" \
    ".Lfu1_%=:\n\t" \
    ASM_ST2 \
    "ds_read_b128 v[84:87], %[la] offset:4096\n\t" \
    "ds_read_b128 v[88:91], %[la] offset:10240\n\t" \
    "ds_read_b128 v[92:95], %[la] offset:16384\n\t" \
    "ds_read_b128 v[96:99], %[la] offset:22528\n\t" \
    "ds_read_b128 v[100:103], %[la] offset:28672\n\t" \
    "ds_read_b128 v[104:107], %[la] offset:5120\n\t" \
    "ds_read_b128 v[108:111], %[la] offset:11264\n\t" \
    "ds_read_b128 v[112:115], %[la] offset:17408\n\t" \
    "ds_read_b128 v[116:119], %[la] offset:23552\n\t" \
    "ds_read_b128 v[120:123], %[la] offset:29696\n\t" \
    "v_mov_b32 v194, %[un]\n\t" \
    "ds_write_b32 %[fd], v194\n\t" \
    "s_waitcnt lgkmcnt(10)\n\t" \
    "v_pk_add_f32 v[64:65], v[64:65], v[84:85]\n\t" \
    "v_pk_add_f32 v[66:67], v[66:67], v[86:87]\n\t" \
    "s_waitcnt lgkmcnt(9)\n\t" \
    "v_pk_add_f32 v[68:69], v[68:69], v[88:89]\n\t" \
    "v_pk_add_f32 v[70:71], v[70:71], v[90:91]\n\t" \
    "s_waitcnt lgkmcnt(8)\n\t" \
    "v_pk_add_f32 v[72:73], v[72:73], v[92:93]\n\t" \
    "v_pk_add_f32 v[74:75], v[74:75], v[94:95]\n\t" \
    "s_waitcnt lgkmcnt(7)\n\t" \
    "v_pk_add_f32 v[76:77], v[76:77], v[96:97]\n\t" \
    "v_pk_add_f32 v[78:79], v[78:79], v[98:99]\n\t" \
    "s_waitcnt lgkmcnt(6)\n\t" \
    "v_pk_add_f32 v[80:81], v[80:81], v[100:101]\n\t" \
    "v_pk_add_f32 v[82:83], v[82:83], v[102:103]\n\t" \
    "s_waitcnt lgkmcnt(5)\n\t" \
    "v_pk_add_f32 v[64:65], v[64:65], v[104:105]\n\t" \
    "v_pk_add_f32 v[66:67], v[66:67], v[106:107]\n\t" \
    "s_waitcnt lgkmcnt(4)\n\t" \
    "v_pk_add_f32 v[68:69], v[68:69], v[108:109]\n\t" \
    "v_pk_add_f32 v[70:71], v[70:71], v[110:111]\n\t" \
    "s_waitcnt lgkmcnt(3)\n\t" \
    "v_pk_add_f32 v[72:73], v[72:73], v[112:113]\n\t" \
    "v_pk_add_f32 v[74:75], v[74:75], v[114:115]\n\t" \
    "s_waitcnt lgkmcnt(2)\n\t" \
    "v_pk_add_f32 v[76:77], v[76:77], v[116:117]\n\t" \
    "v_pk_add_f32 v[78:79], v[78:79], v[118:119]\n\t" \
    "s_waitcnt lgkmcnt(1)\n\t" \
    "v_pk_add_f32 v[80:81], v[80:81], v[120:121]\n\t" \
    "v_pk_add_f32 v[82:83], v[82:83], v[122:123]\n\t" \
    "v_pk_add_f32 v[84:85], v[64:65], v[66:67]\n\t" \
    "v_pk_mul_f32 v[88:89], v[64:65], v[64:65]\n\t" \
    "v_pk_add_f32 v[86:87], v[68:69], v[70:71]\n\t" \
    "v_pk_mul_f32 v[90:91], v[66:67], v[66:67]\n\t" \
    "v_pk_fma_f32 v[88:89], v[68:69], v[68:69], v[88:89]\n\t" \
    "v_pk_fma_f32 v[90:91], v[70:71], v[70:71], v[90:91]\n\t" \
    "v_pk_add_f32 v[84:85], v[84:85], v[72:73]\n\t" \
    "v_pk_fma_f32 v[88:89], v[72:73], v[72:73], v[88:89]\n\t" \
    "v_pk_add_f32 v[86:87], v[86:87], v[74:75]\n\t" \
    "v_pk_fma_f32 v[90:91], v[74:75], v[74:75], v[90:91]\n\t" \
    "v_pk_add_f32 v[84:85], v[84:85], v[76:77]\n\t" \
    "v_pk_fma_f32 v[88:89], v[76:77], v[76:77], v[88:89]\n\t" \
    "v_pk_add_f32 v[86:87], v[86:87], v[78:79]\n\t" \
    "v_pk_fma_f32 v[90:91], v[78:79], v[78:79], v[90:91]\n\t" \
    "v_pk_add_f32 v[84:85], v[84:85], v[80:81]\n\t" \
    "v_pk_fma_f32 v[88:89], v[80:81], v[80:81], v[88:89]\n\t" \
    "v_pk_add_f32 v[86:87], v[86:87], v[82:83]\n\t" \
    "v_pk_fma_f32 v[90:91], v[82:83], v[82:83], v[90:91]\n\t" \
    "v_pk_add_f32 v[84:85], v[84:85], v[86:87]\n\t" \
    "v_pk_add_f32 v[88:89], v[88:89], v[90:91]\n\t" \
    "v_add_f32 v194, v84, v85\n\t" \
    "v_add_f32 v195, v88, v89\n\t" \
    "s_nop 1\n\t" \
    "v_permlane16_swap_b32 v194, v195\n\t" \
    "v_add_f32 v196, v194, v195\n\t" \
    "v_add_f32 v197, v194, v195\n\t" \
    "s_nop 1\n\t" \
    "v_permlane32_swap_b32 v196, v197\n\t" \
    "v_add_f32 v194, v196, v197\n\t" \
    "v_add_f32 v195, v196, v197\n\t" \
    "s_nop 1\n\t" \
    "v_permlane16_swap_b32 v194, v195\n\t" \
    "v_min_u32 v194, -2, v194\n\t" \
    "v_min_u32 v195, -2, v195\n\t" \
    "buffer_store_dwordx2 v[194:195], %[so], %[rr], %[sp] offen sc1\n\t" \
    ASM_ST3 \
    "v_cvt_pk_bf16_f32 v184, v64, v65\n\t" \
    "v_cvt_pk_bf16_f32 v185, v66, v67\n\t" \
    "v_cvt_pk_bf16_f32 v186, v68, v69\n\t" \
    "v_cvt_pk_bf16_f32 v187, v70, v71\n\t" \
    "v_cvt_pk_bf16_f32 v188, v72, v73\n\t" \
    "v_cvt_pk_bf16_f32 v189, v74, v75\n\t" \
    "v_cvt_pk_bf16_f32 v190, v76, v77\n\t" \
    "v_cvt_pk_bf16_f32 v191, v78, v79\n\t" \
    "v_cvt_pk_bf16_f32 v192, v80, v81\n\t" \
    "v_cvt_pk_bf16_f32 v193, v82, v83\n\t" \
    "v_permlane16_swap_b32 v184, v186\n\t" \
    "v_permlane16_swap_b32 v185, v187\n\t" \
    "v_permlane16_swap_b32 v188, v190\n\t" \
    "v_permlane16_swap_b32 v189, v191\n\t" \
    W4SLEEP \
    ".Lfy%=:\n\t" \
    "buffer_load_dwordx2 v[196:197], %[si], %[rr], %[sq] offen sc1\n\t" \
    "buffer_load_dwordx2 v[198:199], %[si], %[rr], %[sq] offen offset:512 sc1\n\t" \
    "s_waitcnt vmcnt(0)\n\t" \
    "v_max_u32 v200, v196, v197\n\t" \
    "v_max3_u32 v200, v198, v199, v200\n\t" \
    "v_cmp_eq_u32 vcc, -1, v200\n\t" \
    "s_cbranch_vccnz .Lfx%=\n\t" \
    ".Lfz%=:\n\t" \
    ASM_ST \
    "v_add_f32 v194, v196, v198\n\t" \
    "v_add_f32 v195, v197, v199\n\t" \
    "buffer_store_dwordx4 v[184:187], %[pa], %[rd], %[xr] offen sc1\n\t" \
    "buffer_store_dwordx4 v[188:191], %[pb], %[rd], %[xr] offen sc1\n\t" \
    "buffer_store_dwordx2 v[192:193], %[o4], %[rd], %[xr] offen sc1\n\t" \
    "s_nop 1\n\t" \
    "v_permlane16_swap_b32 v194, v195\n\t" \
    "v_add_f32 v196, v194, v195\n\t" \
    "v_add_f32 v197, v194, v195\n\t" \
    "s_nop 1\n\t" \
    "v_permlane32_swap_b32 v196, v197\n\t" \
    "v_add_f32 v194, v196, v197\n\t" \
    "v_add_f32 v195, v196, v197\n\t" \
    "s_nop 1\n\t" \
    "v_permlane16_swap_b32 v194, v195\n\t" \
    "v_mul_f32 v194, %[kd], v194\n\t" \
    "v_mul_f32 v195, %[kd], v195\n\t" \
    "v_fma_f32 v195, -v194, v194, v195\n\t" \
    "v_max_f32 v195, 0, v195\n\t" \
    "v_cvt_pk_bf16_f32 v195, v195, v195\n\t" \
    "v_lshlrev_b32 v195, 16, v195\n\t" \
    "v_add_f32 v195, %[ke], v195\n\t" \
    "v_cvt_pk_bf16_f32 v195, v195, v195\n\t" \
    "v_lshlrev_b32 v195, 16, v195\n\t" \
    "v_sqrt_f32 v195, v195\n\t" \
    "v_cvt_pk_bf16_f32 v194, v194, v194\n\t" \
    "v_lshlrev_b32 v194, 16, v194\n\t" \
    "v_cvt_pk_bf16_f32 v195, v195, v195\n\t" \
    "v_lshlrev_b32 v195, 16, v195\n\t" \
    "v_rcp_f32 v196, v195\n\t" \
    "v_rcp_f32 v197, v195\n\t" \
    "s_nop 0\n\t" \
    "v_mul_f32 v198, -v194, v196\n\t" \
    "v_mul_f32 v199, -v194, v196\n\t" \
    "v_pk_fma_f32 v[64:65], v[64:65], v[196:197], v[198:199]\n\t" \
    "v_pk_fma_f32 v[66:67], v[66:67], v[196:197], v[198:199]\n\t" \
    "v_pk_fma_f32 v[68:69], v[68:69], v[196:197], v[198:199]\n\t" \
    "v_pk_fma_f32 v[70:71], v[70:71], v[196:197], v[198:199]\n\t" \
    "v_pk_fma_f32 v[72:73], v[72:73], v[196:197], v[198:199]\n\t" \
    "v_pk_fma_f32 v[74:75], v[74:75], v[196:197], v[198:199]\n\t" \
    "v_pk_fma_f32 v[76:77], v[76:77], v[196:197], v[198:199]\n\t" \
    "v_pk_fma_f32 v[78:79], v[78:79], v[196:197], v[198:199]\n\t" \
    "v_pk_fma_f32 v[80:81], v[80:81], v[196:197], v[198:199]\n\t" \
    "v_pk_fma_f32 v[82:83], v[82:83], v[196:197], v[198:199]\n\t" \
    "v_cvt_pk_bf16_f32 v184, v64, v65\n\t" \
    "v_cvt_pk_bf16_f32 v185, v66, v67\n\t" \
    "v_cvt_pk_bf16_f32 v186, v68, v69\n\t" \
    "v_cvt_pk_bf16_f32 v187, v70, v71\n\t" \
    "v_cvt_pk_bf16_f32 v188, v72, v73\n\t" \
    "v_cvt_pk_bf16_f32 v189, v74, v75\n\t" \
    "v_cvt_pk_bf16_f32 v190, v76, v77\n\t" \
    "v_cvt_pk_bf16_f32 v191, v78, v79\n\t" \
    "v_cvt_pk_bf16_f32 v192, v80, v81\n\t" \
    "v_cvt_pk_bf16_f32 v193, v82, v83\n\t" \
    "v_permlane16_swap_b32 v184, v186\n\t" \
    "v_permlane16_swap_b32 v185, v187\n\t" \
    "v_permlane16_swap_b32 v188, v190\n\t" \
    "v_permlane16_swap_b32 v189, v191\n\t" \
    "buffer_store_dwordx4 v[184:187], %[pa], %[rd], %[xn] offen sc1\n\t" \
    "buffer_store_dwordx4 v[188:191], %[pb], %[rd], %[xn] offen sc1\n\t" \
    "buffer_store_dwordx2 v[192:193], %[o4], %[rd], %[xn] offen sc1\n\t" \
    "buffer_load_dwordx2 v[164:165], %[o0], %[rd], %[n1] offen sc1\n\t" \
    "buffer_load_dwordx2 v[166:167], %[o1], %[rd], %[n1] offen sc1\n\t" \
    "buffer_load_dwordx2 v[168:169], %[o2], %[rd], %[n1] offen sc1\n\t" \
    "buffer_load_dwordx2 v[170:171], %[o3], %[rd], %[n1] offen sc1\n\t" \
    "buffer_load_dwordx2 v[172:173], %[o4], %[rd], %[n1] offen sc1\n\t" \
    "buffer_load_dwordx2 v[174:175], %[o0], %[rd], %[na] offen sc1\n\t" \
    "buffer_load_dwordx2 v[176:177], %[o1], %[rd], %[na] offen sc1\n\t" \
    "buffer_load_dwordx2 v[178:179], %[o2], %[rd], %[na] offen sc1\n\t" \
    "buffer_load_dwordx2 v[180:181], %[o3], %[rd], %[na] offen sc1\n\t" \
    "buffer_load_dwordx2 v[182:183], %[o4], %[rd], %[na] offen sc1\n\t" \
    "v_mov_b32 v196, %[tn]\n\t" \
    "v_mov_b32 v197, %[tn]\n\t" \
    "v_mov_b32 v198, %[tn]\n\t" \
    "v_mov_b32 v199, %[tn]\n\t" \
    "s_waitcnt vmcnt(10)\n\t" \
    "buffer_store_dwordx4 v[196:199], %[fo], %[rn], %[fs] offen sc1\n\t" \
    "s_nop 1\n\t"
#define ASM_W4F_1 \
    "s_branch .Lf0%=\n\t" \
    ".Lfr%=:\n\t" \
    "s_cmp_eq_u32 %[n], 0\n\t" \
    "s_cbranch_scc1 .Lfb%=\n\t" \
    "s_sub_u32 %[n], %[n], 1\n\t" \
    "s_sleep 6\n\t" \
    "buffer_load_dwordx2 v[164:165], %[o0], %[rd], %[c1] offen sc1\n\t" \
    "buffer_load_dwordx2 v[166:167], %[o1], %[rd], %[c1] offen sc1\n\t" \
    "buffer_load_dwordx2 v[168:169], %[o2], %[rd], %[c1] offen sc1\n\t" \
    "buffer_load_dwordx2 v[170:171], %[o3], %[rd], %[c1] offen sc1\n\t" \
    "buffer_load_dwordx2 v[172:173], %[o4], %[rd], %[c1] offen sc1\n\t" \
    "buffer_load_dwordx2 v[174:175], %[o0], %[rd], %[ca] offen sc1\n\t" \
    "buffer_load_dwordx2 v[176:177], %[o1], %[rd], %[ca] offen sc1\n\t" \
    "buffer_load_dwordx2 v[178:179], %[o2], %[rd], %[ca] offen sc1\n\t" \
    "buffer_load_dwordx2 v[180:181], %[o3], %[rd], %[ca] offen sc1\n\t" \
    "buffer_load_dwordx2 v[182:183], %[o4], %[rd], %[ca] offen sc1\n\t" \
    "s_waitcnt vmcnt(0)\n\t" \
    "s_branch .Lfa%=\n\t" \
    ".Lfs0_%=:\n\t" \
    "s_cmp_eq_u32 %[n], 0\n\t" \
    "s_cbranch_scc1 .Lfu0_%=\n\t" \
    "s_sub_u32 %[n], %[n], 1\n\t" \
    "s_sleep 1\n\t" \
    "s_branch .Lft0_%=\n\t" \
    ".Lfs1_%=:\n\t" \
    "s_cmp_eq_u32 %[n], 0\n\t" \
    "s_cbranch_scc1 .Lfu1_%=\n\t" \
    "s_sub_u32 %[n], %[n], 1\n\t" \
    "s_sleep 1\n\t" \
    "s_branch .Lft1_%=\n\t" \
    ".Lf0%=:\n\t" \
    "s_waitcnt vmcnt(0)\n\t" \
    ".Lfa%=:\n\t" \
    "v_max_u32 v194, v164, v165\n\t" \
    "v_max3_u32 v194, v166, v167, v194\n\t" \
    "v_max3_u32 v194, v168, v169, v194\n\t" \
    "v_max3_u32 v194, v170, v171, v194\n\t" \
    "v_max3_u32 v194, v172, v173, v194\n\t" \
    "v_max3_u32 v194, v174, v175, v194\n\t" \
    "v_max3_u32 v194, v176, v177, v194\n\t" \
    "v_max3_u32 v194, v178, v179, v194\n\t" \
    "v_max3_u32 v194, v180, v181, v194\n\t" \
    "v_max3_u32 v194, v182, v183, v194\n\t" \
    "v_cmp_eq_u32 vcc, -1, v194\n\t" \
    "s_cbranch_vccnz .Lfr%=\n\t" \
    ".Lfb%=:\n\t" \
    "v_lshlrev_b32 v64, 16, v164\n\t" \
    "v_and_b32 v65, 0xffff0000, v164\n\t" \
    "v_lshlrev_b32 v84, 16, v174\n\t" \
    "v_and_b32 v85, 0xffff0000, v174\n\t" \
    "v_lshlrev_b32 v66, 16, v165\n\t" \
    "v_and_b32 v67, 0xffff0000, v165\n\t" \
    "v_lshlrev_b32 v86, 16, v175\n\t" \
    "v_and_b32 v87, 0xffff0000, v175\n\t" \
    "v_lshlrev_b32 v68, 16, v166\n\t" \
    "v_and_b32 v69, 0xffff0000, v166\n\t" \
    "v_lshlrev_b32 v88, 16, v176\n\t" \
    "v_and_b32 v89, 0xffff0000, v176\n\t" \
    "v_lshlrev_b32 v70, 16, v167\n\t" \
    "v_and_b32 v71, 0xffff0000, v167\n\t" \
    "v_lshlrev_b32 v90, 16, v177\n\t" \
    "v_and_b32 v91, 0xffff0000, v177\n\t" \
    "v_lshlrev_b32 v72, 16, v168\n\t" \
    "v_and_b32 v73, 0xffff0000, v168\n\t" \
    "v_lshlrev_b32 v92, 16, v178\n\t" \
    "v_and_b32 v93, 0xffff0000, v178\n\t" \
    "v_lshlrev_b32 v74, 16, v169\n\t" \
    "v_and_b32 v75, 0xffff0000, v169\n\t" \
    "v_lshlrev_b32 v94, 16, v179\n\t" \
    "v_and_b32 v95, 0xffff0000, v179\n\t" \
    "v_lshlrev_b32 v76, 16, v170\n\t" \
    "v_and_b32 v77, 0xffff0000, v170\n\t" \
    "v_lshlrev_b32 v96, 16, v180\n\t" \
    "v_and_b32 v97, 0xffff0000, v180\n\t" \
    "v_lshlrev_b32 v78, 16, v171\n\t" \
    "v_and_b32 v79, 0xffff0000, v171\n\t" \
    "v_lshlrev_b32 v98, 16, v181\n\t" \
    "v_and_b32 v99, 0xffff0000, v181\n\t" \
    "v_lshlrev_b32 v80, 16, v172\n\t" \
    "v_and_b32 v81, 0xffff0000, v172\n\t" \
    "v_lshlrev_b32 v100, 16, v182\n\t" \
    "v_and_b32 v101, 0xffff0000, v182\n\t" \
    "v_lshlrev_b32 v82, 16, v173\n\t" \
    "v_and_b32 v83, 0xffff0000, v173\n\t" \
    "v_lshlrev_b32 v102, 16, v183\n\t" \
    "v_and_b32 v103, 0xffff0000, v183\n\t" \
    "v_pk_add_f32 v[64:65], v[64:65], v[84:85]\n\t" \
    "v_pk_add_f32 v[66:67], v[66:67], v[86:87]\n\t" \
    "v_pk_add_f32 v[68:69], v[68:69], v[88:89]\n\t" \
    "v_pk_add_f32 v[70:71], v[70:71], v[90:91]\n\t" \
    "v_pk_add_f32 v[72:73], v[72:73], v[92:93]\n\t" \
    "v_pk_add_f32 v[74:75], v[74:75], v[94:95]\n\t" \
    "v_pk_add_f32 v[76:77], v[76:77], v[96:97]\n\t" \
    "v_pk_add_f32 v[78:79], v[78:79], v[98:99]\n\t" \
    "v_pk_add_f32 v[80:81], v[80:81], v[100:101]\n\t" \
    "v_pk_add_f32 v[82:83], v[82:83], v[102:103]\n\t" \
    ".Lft0_%=:\n\t" \
    "ds_read_b32 v194, %[f0]\n\t" \
    "s_waitcnt lgkmcnt(0)\n\t" \
    "v_cmp_ge_u32 vcc, %[u], v194\n\t" \
    "s_cbranch_vccnz .Lfs0_%=\n\t" \
    ".Lfu0_%=:\n\t" \
    "ds_read_b128 v[84:87], %[la] offset:0\n\t" \
    "ds_read_b128 v[88:91], %[la] offset:6144\n\t" \
    "ds_read_b128 v[92:95], %[la] offset:12288\n\t" \
    "ds_read_b128 v[96:99], %[la] offset:18432\n\t" \
    "ds_read_b128 v[100:103], %[la] offset:24576\n\t" \
    "ds_read_b128 v[104:107], %[la] offset:1024\n\t" \
    "ds_read_b128 v[108:111], %[la] offset:7168\n\t" \
    "ds_read_b128 v[112:115], %[la] offset:13312\n\t" \
    "ds_read_b128 v[116:119], %[la] offset:19456\n\t" \
    "ds_read_b128 v[120:123], %[la] offset:25600\n\t" \
    "ds_read_b128 v[124:127], %[la] offset:2048\n\t" \
    "s_waitcnt lgkmcnt(10)\n\t" \
    "v_pk_add_f32 v[64:65], v[64:65], v[84:85]\n\t" \
    "v_pk_add_f32 v[66:67], v[66:67], v[86:87]\n\t" \
    "ds_read_b128 v[128:131], %[la] offset:8192\n\t" \
    "s_waitcnt lgkmcnt(10)\n\t" \
    "v_pk_add_f32 v[68:69], v[68:69], v[88:89]\n\t" \
    "v_pk_add_f32 v[70:71], v[70:71], v[90:91]\n\t" \
    "ds_read_b128 v[132:135], %[la] offset:14336\n\t" \
    "s_waitcnt lgkmcnt(10)\n\t" \
    "v_pk_add_f32 v[72:73], v[72:73], v[92:93]\n\t" \
    "v_pk_add_f32 v[74:75], v[74:75], v[94:95]\n\t" \
    "ds_read_b128 v[136:139], %[la] offset:20480\n\t" \
    "s_waitcnt lgkmcnt(10)\n\t" \
    "v_pk_add_f32 v[76:77], v[76:77], v[96:97]\n\t" \
    "v_pk_add_f32 v[78:79], v[78:79], v[98:99]\n\t" \
    "ds_read_b128 v[140:143], %[la] offset:26624\n\t" \
    "s_waitcnt lgkmcnt(10)\n\t" \
    "v_pk_add_f32 v[80:81], v[80:81], v[100:101]\n\t" \
    "v_pk_add_f32 v[82:83], v[82:83], v[102:103]\n\t" \
    "ds_read_b128 v[144:147], %[la] offset:3072\n\t" \
    "s_waitcnt lgkmcnt(10)\n\t" \
    "v_pk_add_f32 v[64:65], v[64:65], v[104:105]\n\t" \
    "v_pk_add_f32 v[66:67], v[66:67], v[106:107]\n\t" \
    "ds_read_b128 v[148:151], %[la] offset:9216\n\t" \
    "s_waitcnt lgkmcnt(10)\n\t" \
    "v_pk_add_f32 v[68:69], v[68:69], v[108:109]\n\t" \
    "v_pk_add_f32 v[70:71], v[70:71], v[110:111]\n\t" \
    "ds_read_b128 v[152:155], %[la] offset:15360\n\t" \
    "s_waitcnt lgkmcnt(10)\n\t" \
    "v_pk_add_f32 v[72:73], v[72:73], v[112:113]\n\t" \
    "v_pk_add_f32 v[74:75], v[74:75], v[114:115]\n\t" \
    "ds_read_b128 v[156:159], %[la] offset:21504\n\t" \
    "s_waitcnt lgkmcnt(10)\n\t" \
    "v_pk_add_f32 v[76:77], v[76:77], v[116:117]\n\t" \
    "v_pk_add_f32 v[78:79], v[78:79], v[118:119]\n\t" \
    "ds_read_b128 v[160:163], %[la] offset:27648\n\t" \
    "s_waitcnt lgkmcnt(10)\n\t" \
    "v_pk_add_f32 v[80:81], v[80:81], v[120:121]\n\t" \
    "v_pk_add_f32 v[82:83], v[82:83], v[122:123]\n\t" \
    "s_waitcnt lgkmcnt(9)\n\t" \
    "v_pk_add_f32 v[64:65], v[64:65], v[124:125]\n\t" \
    "v_pk_add_f32 v[66:67], v[66:67], v[126:127]\n\t" \
    "s_waitcnt lgkmcnt(8)\n\t" \
    "v_pk_add_f32 v[68:69], v[68:69], v[128:129]\n\t" \
    "v_pk_add_f32 v[70:71], v[70:71], v[130:131]\n\t" \
    "s_waitcnt lgkmcnt(7)\n\t" \
    "v_pk_add_f32 v[72:73], v[72:73], v[132:133]\n\t" \
    "v_pk_add_f32 v[74:75], v[74:75], v[134:135]\n\t" \
    "s_waitcnt lgkmcnt(6)\n\t" \
    "v_pk_add_f32 v[76:77], v[76:77], v[136:137]\n\t" \
    "v_pk_add_f32 v[78:79], v[78:79], v[138:139]\n\t" \
    "s_waitcnt lgkmcnt(5)\n\t" \
    "v_pk_add_f32 v[80:81], v[80:81], v[140:141]\n\t" \
    "v_pk_add_f32 v[82:83], v[82:83], v[142:143]\n\t" \
    "s_waitcnt lgkmcnt(4)\n\t" \
    "v_pk_add_f32 v[64:65], v[64:65], v[144:145]\n\t" \
    "v_pk_add_f32 v[66:67], v[66:67], v[146:147]\n\t" \
    "s_waitcnt lgkmcnt(3)\n\t" \
    "v_pk_add_f32 v[68:69], v[68:69], v[148:149]\n\t" \
    "v_pk_add_f32 v[70:71], v[70:71], v[150:151]\n\t" \
    "s_waitcnt lgkmcnt(2)\n\t" \
    "v_pk_add_f32 v[72:73], v[72:73], v[152:153]\n\t" \
    "v_pk_add_f32 v[74:75], v[74:75], v[154:155]\n\t" \
    "s_waitcnt lgkmcnt(1)\n\t" \
    "v_pk_add_f32 v[76:77], v[76:77], v[156:157]\n\t" \
    "v_pk_add_f32 v[78:79], v[78:79], v[158:159]\n\t" \
    "s_waitcnt lgkmcnt(0)\n\t" \
    "v_pk_add_f32 v[80:81], v[80:81], v[160:161]\n\t" \
    "v_pk_add_f32 v[82:83], v[82:83], v[162:163]\n\t" \
    ".Lft1_%=:\n\t" \
    "ds_read_b32 v194, %[f1]\n\t" \
    "s_waitcnt lgkmcnt(0)\n\t" \
    "v_cmp_ge_u32 vcc, %[u], v194\n\t" \
    "s_cbranch_vccnz .Lfs1_%=\n\t" \
    ".Lfu1_%=:\n\t" \
    ASM_ST2 \
    "ds_read_b128 v[84:87], %[la] offset:4096\n\t" \
    "ds_read_b128 v[88:91], %[la] offset:10240\n\t" \
    "ds_read_b128 v[92:95], %[la] offset:16384\n\t" \
    "ds_read_b128 v[96:99], %[la] offset:22528\n\t" \
    "ds_read_b128 v[100:103], %[la] offset:28672\n\t" \
    "ds_read_b128 v[104:107], %[la] offset:5120\n\t" \
    "ds_read_b128 v[108:111], %[la] offset:11264\n\t" \
    "ds_read_b128 v[112:115], %[la] offset:17408\n\t" \
    "ds_read_b128 v[116:119], %[la] offset:23552\n\t" \
    "ds_read_b128 v[120:123], %[la] offset:29696\n\t" \
    "v_mov_b32 v194, %[un]\n\t" \
    "ds_write_b32 %[fd], v194\n\t" \
    "s_waitcnt lgkmcnt(10)\n\t" \
    "v_pk_add_f32 v[64:65], v[64:65], v[84:85]\n\t" \
    "v_pk_add_f32 v[66:67], v[66:67], v[86:87]\n\t" \
    "s_waitcnt lgkmcnt(9)\n\t" \
    "v_pk_add_f32 v[68:69], v[68:69], v[88:89]\n\t" \
    "v_pk_add_f32 v[70:71], v[70:71], v[90:91]\n\t" \
    "s_waitcnt lgkmcnt(8)\n\t" \
    "v_pk_add_f32 v[72:73], v[72:73], v[92:93]\n\t" \
    "v_pk_add_f32 v[74:75], v[74:75], v[94:95]\n\t" \
    "s_waitcnt lgkmcnt(7)\n\t" \
    "v_pk_add_f32 v[76:77], v[76:77], v[96:97]\n\t" \
    "v_pk_add_f32 v[78:79], v[78:79], v[98:99]\n\t" \
    "s_waitcnt lgkmcnt(6)\n\t" \
    "v_pk_add_f32 v[80:81], v[80:81], v[100:101]\n\t" \
    "v_pk_add_f32 v[82:83], v[82:83], v[102:103]\n\t" \
    "s_waitcnt lgkmcnt(5)\n\t" \
    "v_pk_add_f32 v[64:65], v[64:65], v[104:105]\n\t" \
    "v_pk_add_f32 v[66:67], v[66:67], v[106:107]\n\t" \
    "s_waitcnt lgkmcnt(4)\n\t" \
    "v_pk_add_f32 v[68:69], v[68:69], v[108:109]\n\t" \
    "v_pk_add_f32 v[70:71], v[70:71], v[110:111]\n\t" \
    "s_waitcnt lgkmcnt(3)\n\t" \
    "v_pk_add_f32 v[72:73], v[72:73], v[112:113]\n\t" \
    "v_pk_add_f32 v[74:75], v[74:75], v[114:115]\n\t" \
    "s_waitcnt lgkmcnt(2)\n\t" \
    "v_pk_add_f32 v[76:77], v[76:77], v[116:117]\n\t" \
    "v_pk_add_f32 v[78:79], v[78:79], v[118:119]\n\t" \
    "s_waitcnt lgkmcnt(1)\n\t" \
    "v_pk_add_f32 v[80:81], v[80:81], v[120:121]\n\t" \
    "v_pk_add_f32 v[82:83], v[82:83], v[122:123]\n\t" \
    "v_cvt_pk_bf16_f32 v184, v64, v65\n\t" \
    "v_cvt_pk_bf16_f32 v185, v66, v67\n\t" \
    "v_cvt_pk_bf16_f32 v186, v68, v69\n\t" \
    "v_cvt_pk_bf16_f32 v187, v70, v71\n\t" \
    "v_cvt_pk_bf16_f32 v188, v72, v73\n\t" \
    "v_cvt_pk_bf16_f32 v189, v74, v75\n\t" \
    "v_cvt_pk_bf16_f32 v190, v76, v77\n\t" \
    "v_cvt_pk_bf16_f32 v191, v78, v79\n\t" \
    "v_cvt_pk_bf16_f32 v192, v80, v81\n\t" \
    "v_cvt_pk_bf16_f32 v193, v82, v83\n\t" \
    "v_permlane16_swap_b32 v184, v186\n\t" \
    "v_permlane16_swap_b32 v185, v187\n\t" \
    "v_permlane16_swap_b32 v188, v190\n\t" \
    "v_permlane16_swap_b32 v189, v191\n\t" \
    "buffer_store_dwordx4 v[184:187], %[pa], %[ro], %[so] offen nt\n\t" \
    "buffer_store_dwordx4 v[188:191], %[pa], %[ro], %[so] offen offset:64 nt\n\t" \
    "buffer_store_dwordx2 v[192:193], %[pb], %[ro], %[so] offen offset:128 nt\n\t" \
    "buffer_load_dwordx2 v[164:165], %[o0], %[rd], %[n1] offen sc1\n\t" \
    "buffer_load_dwordx2 v[166:167], %[o1], %[rd], %[n1] offen sc1\n\t" \
    "buffer_load_dwordx2 v[168:169], %[o2], %[rd], %[n1] offen sc1\n\t" \
    "buffer_load_dwordx2 v[170:171], %[o3], %[rd], %[n1] offen sc1\n\t" \
    "buffer_load_dwordx2 v[172:173], %[o4], %[rd], %[n1] offen sc1\n\t" \
    "buffer_load_dwordx2 v[174:175], %[o0], %[rd], %[na] offen sc1\n\t" \
    "buffer_load_dwordx2 v[176:177], %[o1], %[rd], %[na] offen sc1\n\t" \
    "buffer_load_dwordx2 v[178:179], %[o2], %[rd], %[na] offen sc1\n\t" \
    "buffer_load_dwordx2 v[180:181], %[o3], %[rd], %[na] offen sc1\n\t" \
    "buffer_load_dwordx2 v[182:183], %[o4], %[rd], %[na] offen sc1\n\t"
#define ASM_WCS \
    "v_mfma_f32_16x16x32_bf16 v[196:199], v[0:3], v[216:219], 0\n\t" \
    "v_mfma_f32_16x16x32_bf16 v[200:203], v[80:83], v[216:219], 0\n\t" \
    "v_mfma_f32_16x16x32_bf16 v[196:199], v[4:7], v[216:219], v[196:199]\n\t" \
    "v_mfma_f32_16x16x32_bf16 v[200:203], v[84:87], v[216:219], v[200:203]\n\t" \
    "v_mfma_f32_16x16x32_bf16 v[196:199], v[8:11], v[216:219], v[196:199]\n\t" \
    "v_mfma_f32_16x16x32_bf16 v[200:203], v[88:91], v[216:219], v[200:203]\n\t" \
    "v_mfma_f32_16x16x32_bf16 v[196:199], v[12:15], v[216:219], v[196:199]\n\t" \
    "v_mfma_f32_16x16x32_bf16 v[200:203], v[92:95], v[216:219], v[200:203]\n\t" \
    "v_mfma_f32_16x16x32_bf16 v[196:199], v[16:19], v[216:219], v[196:199]\n\t" \
    "v_mfma_f32_16x16x32_bf16 v[200:203], v[96:99], v[216:219], v[200:203]\n\t" \
    "v_mfma_f32_16x16x32_bf16 v[196:199], v[20:23], v[216:219], v[196:199]\n\t" \
    "v_mfma_f32_16x16x32_bf16 v[200:203], v[100:103], v[216:219], v[200:203]\n\t" \
    "v_mfma_f32_16x16x32_bf16 v[196:199], v[24:27], v[216:219], v[196:199]\n\t" \
    "v_mfma_f32_16x16x32_bf16 v[200:203], v[104:107], v[216:219], v[200:203]\n\t" \
    "v_mfma_f32_16x16x32_bf16 v[196:199], v[28:31], v[216:219], v[196:199]\n\t" \
    "v_mfma_f32_16x16x32_bf16 v[200:203], v[108:111], v[216:219], v[200:203]\n\t" \
    "v_mfma_f32_16x16x32_bf16 v[196:199], v[32:35], v[216:219], v[196:199]\n\t" \
    "v_mfma_f32_16x16x32_bf16 v[200:203], v[112:115], v[216:219], v[200:203]\n\t" \
    "v_mfma_f32_16x16x32_bf16 v[196:199], v[36:39], v[216:219], v[196:199]\n\t" \
    "v_mfma_f32_16x16x32_bf16 v[200:203], v[116:119], v[216:219], v[200:203]\n\t" \
    "v_mfma_f32_16x16x32_bf16 v[196:199], v[40:43], v[216:219], v[196:199]\n\t" \
    "v_mfma_f32_16x16x32_bf16 v[200:203], v[120:123], v[216:219], v[200:203]\n\t" \
    "v_mfma_f32_16x16x32_bf16 v[196:199], v[44:47], v[216:219], v[196:199]\n\t" \
    "v_mfma_f32_16x16x32_bf16 v[200:203], v[124:127], v[216:219], v[200:203]\n\t" \
    "v_mfma_f32_16x16x32_bf16 v[196:199], v[48:51], v[216:219], v[196:199]\n\t" \
    "v_mfma_f32_16x16x32_bf16 v[200:203], v[128:131], v[216:219], v[200:203]\n\t" \
    "v_mfma_f32_16x16x32_bf16 v[196:199], v[52:55], v[216:219], v[196:199]\n\t" \
    "v_mfma_f32_16x16x32_bf16 v[200:203], v[132:135], v[216:219], v[200:203]\n\t" \
    "v_mfma_f32_16x16x32_bf16 v[196:199], v[56:59], v[216:219], v[196:199]\n\t" \
    "v_mfma_f32_16x16x32_bf16 v[200:203], v[136:139], v[216:219], v[200:203]\n\t" \
    "v_mfma_f32_16x16x32_bf16 v[196:199], v[60:63], v[216:219], v[196:199]\n\t" \
    "v_mfma_f32_16x16x32_bf16 v[200:203], v[140:143], v[216:219], v[200:203]\n\t" \
    "v_mfma_f32_16x16x32_bf16 v[196:199], v[64:67], v[216:219], v[196:199]\n\t" \
    "v_mfma_f32_16x16x32_bf16 v[200:203], v[144:147], v[216:219], v[200:203]\n\t" \
    "v_mfma_f32_16x16x32_bf16 v[196:199], v[68:71], v[216:219], v[196:199]\n\t" \
    "v_mfma_f32_16x16x32_bf16 v[200:203], v[148:151], v[216:219], v[200:203]\n\t" \
    "v_mfma_f32_16x16x32_bf16 v[196:199], v[72:75], v[216:219], v[196:199]\n\t" \
    "v_mfma_f32_16x16x32_bf16 v[200:203], v[152:155], v[216:219], v[200:203]\n\t" \
    "v_mfma_f32_16x16x32_bf16 v[196:199], v[76:79], v[216:219], v[196:199]\n\t" \
    "v_mfma_f32_16x16x32_bf16 v[200:203], v[156:159], v[216:219], v[200:203]\n\t" \
    "s_nop 7\n\t"
#define ASM_WT1_LO \
    "s_branch .Lt0%=\n\t" \
    ".Ltr%=:\n\t" \
    "s_cmp_gt_i32 %[n], 0\n\t" \
    "s_cbranch_scc0 .Ltg%=\n\t" \
    ".Lfw%=:\n\t" \
    "s_mov_b32 %[a], 0\n\t" \
    "v_max_u32 v252, v232, v233\n\t" \
    "v_max3_u32 v252, v234, v235, v252\n\t" \
    "v_cmp_eq_u32 vcc, -1, v252\n\t" \
    "s_cbranch_vccz .Lgw0_%=\n\t" \
    "buffer_load_dwordx4 v[232:235], %[l16], %[rs], %[i0] offen offset:0 sc1\n\t" \
    "s_mov_b32 %[a], 1\n\t" \
    ".Lgw0_%=:\n\t" \
    "v_max_u32 v252, v236, v237\n\t" \
    "v_max3_u32 v252, v238, v239, v252\n\t" \
    "v_cmp_eq_u32 vcc, -1, v252\n\t" \
    "s_cbranch_vccz .Lgw1_%=\n\t" \
    "buffer_load_dwordx4 v[236:239], %[l16], %[rs], %[i0] offen offset:1024 sc1\n\t" \
    "s_mov_b32 %[a], 1\n\t" \
    ".Lgw1_%=:\n\t" \
    "v_max_u32 v252, v240, v241\n\t" \
    "v_max3_u32 v252, v242, v243, v252\n\t" \
    "v_cmp_eq_u32 vcc, -1, v252\n\t" \
    "s_cbranch_vccz .Lgw2_%=\n\t" \
    "buffer_load_dwordx4 v[240:243], %[l16], %[rs], %[i0] offen offset:2048 sc1\n\t" \
    "s_mov_b32 %[a], 1\n\t" \
    ".Lgw2_%=:\n\t" \
    "v_max_u32 v252, v244, v245\n\t" \
    "v_max3_u32 v252, v246, v247, v252\n\t" \
    "v_cmp_eq_u32 vcc, -1, v252\n\t" \
    "s_cbranch_vccz .Lgw3_%=\n\t" \
    "buffer_load_dwordx4 v[244:247], %[l16], %[rs], %[i0] offen offset:3072 sc1\n\t" \
    "s_mov_b32 %[a], 1\n\t" \
    ".Lgw3_%=:\n\t" \
    "v_max_u32 v252, v248, v249\n\t" \
    "v_max3_u32 v252, v250, v251, v252\n\t" \
    "v_cmp_eq_u32 vcc, -1, v252\n\t" \
    "s_cbranch_vccz .Lgw4_%=\n\t" \
    "buffer_load_dwordx4 v[248:251], %[l16], %[rs], %[i1] offen offset:0 sc1\n\t" \
    "s_mov_b32 %[a], 1\n\t" \
    ".Lgw4_%=:\n\t" \
    "s_cmp_eq_u32 %[a], 0\n\t" \
    "s_cbranch_scc1 .Lhw%=\n\t" \
    "s_waitcnt vmcnt(20)\n\t" \
    ".Liw%=:\n\t" \
    "v_max_u32 v252, v232, v233\n\t" \
    "v_max3_u32 v252, v234, v235, v252\n\t" \
    "v_max3_u32 v252, v236, v237, v252\n\t" \
    "v_max3_u32 v252, v238, v239, v252\n\t" \
    "v_max3_u32 v252, v240, v241, v252\n\t" \
    "v_max3_u32 v252, v242, v243, v252\n\t" \
    "v_max3_u32 v252, v244, v245, v252\n\t" \
    "v_max3_u32 v252, v246, v247, v252\n\t" \
    "v_max3_u32 v252, v248, v249, v252\n\t" \
    "v_max3_u32 v252, v250, v251, v252\n\t" \
    "v_cmp_eq_u32 vcc, -1, v252\n\t" \
    "s_cbranch_vccz .Lhw%=\n\t" \
    "s_sleep 1\n\t" \
    "v_max_u32 v252, v232, v233\n\t" \
    "v_max3_u32 v252, v234, v235, v252\n\t" \
    "v_max3_u32 v252, v236, v237, v252\n\t" \
    "v_max3_u32 v252, v238, v239, v252\n\t" \
    "v_max3_u32 v252, v240, v241, v252\n\t" \
    "v_max3_u32 v252, v242, v243, v252\n\t" \
    "v_max3_u32 v252, v244, v245, v252\n\t" \
    "v_max3_u32 v252, v246, v247, v252\n\t" \
    "v_max3_u32 v252, v248, v249, v252\n\t" \
    "v_max3_u32 v252, v250, v251, v252\n\t" \
    "v_cmp_eq_u32 vcc, -1, v252\n\t" \
    "s_cbranch_vccz .Lhw%=\n\t" \
    "s_sleep 1\n\t" \
    "v_max_u32 v252, v232, v233\n\t" \
    "v_max3_u32 v252, v234, v235, v252\n\t" \
    "v_max3_u32 v252, v236, v237, v252\n\t" \
    "v_max3_u32 v252, v238, v239, v252\n\t" \
    "v_max3_u32 v252, v240, v241, v252\n\t" \
    "v_max3_u32 v252, v242, v243, v252\n\t" \
    "v_max3_u32 v252, v244, v245, v252\n\t" \
    "v_max3_u32 v252, v246, v247, v252\n\t" \
    "v_max3_u32 v252, v248, v249, v252\n\t" \
    "v_max3_u32 v252, v250, v251, v252\n\t" \
    "v_cmp_eq_u32 vcc, -1, v252\n\t" \
    "s_cbranch_vccz .Lhw%=\n\t" \
    "s_sleep 1\n\t" \
    "v_max_u32 v252, v232, v233\n\t" \
    "v_max3_u32 v252, v234, v235, v252\n\t" \
    "v_max3_u32 v252, v236, v237, v252\n\t" \
    "v_max3_u32 v252, v238, v239, v252\n\t" \
    "v_max3_u32 v252, v240, v241, v252\n\t" \
    "v_max3_u32 v252, v242, v243, v252\n\t" \
    "v_max3_u32 v252, v244, v245, v252\n\t" \
    "v_max3_u32 v252, v246, v247, v252\n\t" \
    "v_max3_u32 v252, v248, v249, v252\n\t" \
    "v_max3_u32 v252, v250, v251, v252\n\t" \
    "v_cmp_eq_u32 vcc, -1, v252\n\t" \
    "s_cbranch_vccz .Lhw%=\n\t" \
    "s_sleep 1\n\t" \
    "v_max_u32 v252, v232, v233\n\t" \
    "v_max3_u32 v252, v234, v235, v252\n\t" \
    "v_max3_u32 v252, v236, v237, v252\n\t" \
    "v_max3_u32 v252, v238, v239, v252\n\t" \
    "v_max3_u32 v252, v240, v241, v252\n\t" \
    "v_max3_u32 v252, v242, v243, v252\n\t" \
    "v_max3_u32 v252, v244, v245, v252\n\t" \
    "v_max3_u32 v252, v246, v247, v252\n\t" \
    "v_max3_u32 v252, v248, v249, v252\n\t" \
    "v_max3_u32 v252, v250, v251, v252\n\t" \
    "v_cmp_eq_u32 vcc, -1, v252\n\t" \
    "s_cbranch_vccz .Lhw%=\n\t" \
    "s_sleep 1\n\t" \
    "v_max_u32 v252, v232, v233\n\t" \
    "v_max3_u32 v252, v234, v235, v252\n\t" \
    "v_max3_u32 v252, v236, v237, v252\n\t" \
    "v_max3_u32 v252, v238, v239, v252\n\t" \
    "v_max3_u32 v252, v240, v241, v252\n\t" \
    "v_max3_u32 v252, v242, v243, v252\n\t" \
    "v_max3_u32 v252, v244, v245, v252\n\t" \
    "v_max3_u32 v252, v246, v247, v252\n\t" \
    "v_max3_u32 v252, v248, v249, v252\n\t" \
    "v_max3_u32 v252, v250, v251, v252\n\t" \
    "v_cmp_eq_u32 vcc, -1, v252\n\t" \
    "s_cbranch_vccz .Lhw%=\n\t" \
    "s_sleep 1\n\t" \
    "s_sub_u32 %[n], %[n], 1\n\t" \
    "s_cmp_gt_i32 %[n], 0\n\t" \
    "s_cbranch_scc1 .Lfw%=\n\t" \
    ".Lhw%=:\n\t" \
    "s_branch .Lta%=\n\t" \
    ".Lt0%=:\n\t" \
    "s_waitcnt vmcnt(1)\n\t" \
    ".Lta%=:\n\t" \
    "ds_write_b128 %[lw], v[232:235] offset:0\n\t" \
    "v_max_u32 v252, v232, v233\n\t" \
    "v_max3_u32 v252, v234, v235, v252\n\t" \
    "v_mfma_f32_16x16x32_bf16 v[188:191], v[232:235], v[232:235], 0\n\t" \
    "ds_write_b128 %[lw], v[236:239] offset:1024\n\t" \
    "v_max3_u32 v252, v236, v237, v252\n\t" \
    "v_max3_u32 v252, v238, v239, v252\n\t" \
    "v_mfma_f32_16x16x32_bf16 v[188:191], v[236:239], v[236:239], v[188:191]\n\t" \
    "ds_write_b128 %[lw], v[240:243] offset:2048\n\t" \
    "v_max3_u32 v252, v240, v241, v252\n\t" \
    "v_max3_u32 v252, v242, v243, v252\n\t" \
    "v_mfma_f32_16x16x32_bf16 v[188:191], v[240:243], v[240:243], v[188:191]\n\t" \
    "ds_write_b128 %[lw], v[244:247] offset:3072\n\t" \
    "v_max3_u32 v252, v244, v245, v252\n\t" \
    "v_max3_u32 v252, v246, v247, v252\n\t" \
    "v_mfma_f32_16x16x32_bf16 v[188:191], v[244:247], v[244:247], v[188:191]\n\t" \
    "ds_write_b128 %[lw], v[248:251] offset:4096\n\t" \
    "v_max3_u32 v252, v248, v249, v252\n\t" \
    "v_max3_u32 v252, v250, v251, v252\n\t" \
    "v_mfma_f32_16x16x32_bf16 v[188:191], v[248:251], v[248:251], v[188:191]\n\t" \
    "v_mfma_f32_16x16x32_bf16 v[192:195], v[216:219], v[232:235], 0\n\t" \
    "v_mfma_f32_16x16x32_bf16 v[192:195], v[216:219], v[236:239], v[192:195]\n\t" \
    "v_mfma_f32_16x16x32_bf16 v[192:195], v[216:219], v[240:243], v[192:195]\n\t" \
    "v_mfma_f32_16x16x32_bf16 v[192:195], v[216:219], v[244:247], v[192:195]\n\t" \
    "v_mfma_f32_16x16x32_bf16 v[192:195], v[216:219], v[248:251], v[192:195]\n\t" \
    "v_cmp_eq_u32 vcc, -1, v252\n\t" \
    "s_cbranch_vccnz .Ltr%=\n\t" \
    ".Ltg%=:\n\t" \
    ASM_ST2 \
    "s_nop 5\n\t" \
    "ds_write_b128 %[lw], v[188:191] offset:5120\n\t" \
    "ds_write_b128 %[lw], v[192:195] offset:6144\n\t" \
    "s_waitcnt lgkmcnt(0)\n\t" \
    "s_setprio 2\n\t" \
    "s_barrier\n\t" ASM_ST \
    "ds_read_b128 v[160:163], %[la] offset:0\n\t" \
    "ds_read_b128 v[164:167], %[la] offset:1024\n\t" \
    "ds_read_b128 v[168:171], %[la] offset:2048\n\t" \
    "ds_read2st64_b32 v[204:205], %[sr] offset0:20 offset1:24\n\t" \
    "ds_read_b128 v[172:175], %[la] offset:3072\n\t" \
    "ds_read_b128 v[176:179], %[la] offset:8192\n\t" \
    "ds_read_b128 v[180:183], %[la] offset:9216\n\t" \
    "ds_read_b128 v[184:187], %[la] offset:10240\n\t" \
    "s_waitcnt lgkmcnt(7)\n\t" \
    "v_mfma_f32_16x16x32_bf16 v[192:195], v[80:83], v[160:163], 0\n\t" \
    "s_waitcnt lgkmcnt(6)\n\t" \
    "v_mfma_f32_16x16x32_bf16 v[192:195], v[84:87], v[164:167], v[192:195]\n\t" \
    "v_mfma_f32_16x16x32_bf16 v[188:191], v[0:3], v[160:163], 0\n\t" \
    "ds_read_b128 v[160:163], %[la] offset:11264\n\t" \
    "v_mfma_f32_16x16x32_bf16 v[188:191], v[4:7], v[164:167], v[188:191]\n\t" \
    "ds_read_b128 v[164:167], %[la] offset:16384\n\t" \
    "s_waitcnt lgkmcnt(7)\n\t" \
    "v_mfma_f32_16x16x32_bf16 v[188:191], v[8:11], v[168:171], v[188:191]\n\t" \
    "s_waitcnt lgkmcnt(6)\n\t" \
    "s_waitcnt lgkmcnt(5)\n\t" \
    "v_mfma_f32_16x16x32_bf16 v[188:191], v[12:15], v[172:175], v[188:191]\n\t" \
    "v_permlane16_swap_b32 v204, v205\n\t" \
    "v_mfma_f32_16x16x32_bf16 v[192:195], v[88:91], v[168:171], v[192:195]\n\t" \
    "ds_read_b128 v[168:171], %[la] offset:17408\n\t" \
    "v_mfma_f32_16x16x32_bf16 v[192:195], v[92:95], v[172:175], v[192:195]\n\t" \
    "ds_read_b128 v[172:175], %[la] offset:18432\n\t" \
    "v_add_f32 v206, v204, v205\n\t" \
    "s_waitcnt lgkmcnt(6)\n\t" \
    "v_mfma_f32_16x16x32_bf16 v[192:195], v[96:99], v[176:179], v[192:195]\n\t" \
    "v_add_f32 v207, v204, v205\n\t" \
    "s_waitcnt lgkmcnt(5)\n\t" \
    "v_mfma_f32_16x16x32_bf16 v[192:195], v[100:103], v[180:183], v[192:195]\n\t" \
    "v_mfma_f32_16x16x32_bf16 v[188:191], v[16:19], v[176:179], v[188:191]\n\t" \
    "ds_read_b128 v[176:179], %[la] offset:19456\n\t" \
    "v_permlane32_swap_b32 v206, v207\n\t" \
    "v_mfma_f32_16x16x32_bf16 v[188:191], v[20:23], v[180:183], v[188:191]\n\t" \
    "ds_read_b128 v[180:183], %[la] offset:24576\n\t" \
    "s_waitcnt lgkmcnt(6)\n\t" \
    "v_mfma_f32_16x16x32_bf16 v[188:191], v[24:27], v[184:187], v[188:191]\n\t" \
    "v_add_f32 v204, v206, v207\n\t" \
    "s_waitcnt lgkmcnt(5)\n\t" \
    "v_mfma_f32_16x16x32_bf16 v[188:191], v[28:31], v[160:163], v[188:191]\n\t" \
    "v_add_f32 v205, v206, v207\n\t" \
    "v_mfma_f32_16x16x32_bf16 v[192:195], v[104:107], v[184:187], v[192:195]\n\t" \
    "ds_read_b128 v[184:187], %[la] offset:25600\n\t" \
    "v_mfma_f32_16x16x32_bf16 v[192:195], v[108:111], v[160:163], v[192:195]\n\t" \
    "ds_read_b128 v[160:163], %[la] offset:26624\n\t" \
    "v_permlane16_swap_b32 v204, v205\n\t" \
    "s_waitcnt lgkmcnt(6)\n\t" \
    "v_mfma_f32_16x16x32_bf16 v[192:195], v[112:115], v[164:167], v[192:195]\n\t" \
    "s_waitcnt lgkmcnt(5)\n\t" \
    "v_mfma_f32_16x16x32_bf16 v[192:195], v[116:119], v[168:171], v[192:195]\n\t" \
    "v_mul_f32 v205, %[kd], v205\n\t" \
    "v_mfma_f32_16x16x32_bf16 v[188:191], v[32:35], v[164:167], v[188:191]\n\t" \
    "ds_read_b128 v[164:167], %[la] offset:27648\n\t" \
    "v_mul_f32 v204, %[kd], v204\n\t" \
    "v_mfma_f32_16x16x32_bf16 v[188:191], v[36:39], v[168:171], v[188:191]\n\t" \
    "ds_read_b128 v[168:171], %[la] offset:4096\n\t" \
    "v_fma_f32 v204, -v205, v205, v204\n\t" \
    "s_waitcnt lgkmcnt(6)\n\t" \
    "v_mfma_f32_16x16x32_bf16 v[188:191], v[40:43], v[172:175], v[188:191]\n\t" \
    "v_max_f32 v204, 0, v204\n\t" \
    "s_waitcnt lgkmcnt(5)\n\t" \
    "v_mfma_f32_16x16x32_bf16 v[188:191], v[44:47], v[176:179], v[188:191]\n\t" \
    "v_add_f32 v206, %[ke], v204\n\t" \
    "v_mfma_f32_16x16x32_bf16 v[192:195], v[120:123], v[172:175], v[192:195]\n\t" \
    "ds_read_b128 v[172:175], %[la] offset:12288\n\t" \
    "v_rsq_f32 v206, v206\n\t" \
    "v_mfma_f32_16x16x32_bf16 v[192:195], v[124:127], v[176:179], v[192:195]\n\t" \
    "ds_read_b128 v[176:179], %[la] offset:20480\n\t" \
    "s_waitcnt lgkmcnt(6)\n\t" \
    "v_mfma_f32_16x16x32_bf16 v[192:195], v[128:131], v[180:183], v[192:195]\n\t" \
    "v_mul_f32 v207, v204, v206\n\t" \
    "s_waitcnt lgkmcnt(5)\n\t" \
    "v_mfma_f32_16x16x32_bf16 v[192:195], v[132:135], v[184:187], v[192:195]\n\t" \
    "v_fma_f32 v207, v207, v206, %[ke]\n\t" \
    "v_mfma_f32_16x16x32_bf16 v[188:191], v[48:51], v[180:183], v[188:191]\n\t" \
    "ds_read_b128 v[180:183], %[la] offset:28672\n\t" \
    "v_rsq_f32 v207, v207\n\t" \
    "v_mfma_f32_16x16x32_bf16 v[188:191], v[52:55], v[184:187], v[188:191]\n\t" \
    "v_mul_f32 v206, v206, v207\n\t" \
    "v_mul_f32 v207, -v205, v206\n\t" \
    "s_waitcnt lgkmcnt(5)\n\t" \
    "v_mfma_f32_16x16x32_bf16 v[188:191], v[56:59], v[160:163], v[188:191]\n\t" \
    "v_mul_f32 v208, v196, v207\n\t" \
    "v_mul_f32 v209, v197, v207\n\t" \
    "s_waitcnt lgkmcnt(4)\n\t" \
    "v_mfma_f32_16x16x32_bf16 v[188:191], v[60:63], v[164:167], v[188:191]\n\t" \
    "v_mul_f32 v210, v198, v207\n\t" \
    "v_mul_f32 v211, v199, v207\n\t" \
    "v_mfma_f32_16x16x32_bf16 v[192:195], v[136:139], v[160:163], v[192:195]\n\t" \
    "v_mul_f32 v212, v200, v207\n\t" \
    "v_mul_f32 v213, v201, v207\n\t" \
    "v_mfma_f32_16x16x32_bf16 v[192:195], v[140:143], v[164:167], v[192:195]\n\t" \
    "v_mul_f32 v214, v202, v207\n\t" \
    "v_mul_f32 v215, v203, v207\n\t" \
    "s_waitcnt lgkmcnt(3)\n\t" \
    "v_mfma_f32_16x16x32_bf16 v[192:195], v[144:147], v[168:171], v[192:195]\n\t" \
    "s_cmp_lg_u32 %[sw], 0\n\t" \
    "s_cbranch_scc1 .Lts%=\n\t" \
    "v_cvt_pk_bf16_f32 v217, v204, v204\n\t" \
    "v_lshlrev_b32 v217, 16, v217\n\t" \
    "v_add_f32 v217, %[ke], v217\n\t" \
    "v_cvt_pk_bf16_f32 v217, v217, v217\n\t" \
    "v_lshlrev_b32 v217, 16, v217\n\t" \
    "v_sqrt_f32 v217, v217\n\t" \
    "v_cvt_pk_bf16_f32 v216, v205, v205\n\t" \
    "v_lshlrev_b32 v216, 16, v216\n\t" \
    "v_cvt_pk_bf16_f32 v217, v217, v217\n\t" \
    "v_lshlrev_b32 v217, 16, v217\n\t" \
    "v_rcp_f32 v217, v217\n\t" \
    "v_lshrrev_b32 v218, 1, %[l16]\n\t" \
    "v_and_b32 v218, 0x78, v218\n\t" \
    "buffer_store_dwordx2 v[216:217], v218, %[rs], %[ss] offen sc1\n\t" \
    ".Lts%=:\n\t" \
    "v_mov_b64 v[216:217], -1\n\t" \
    "s_waitcnt lgkmcnt(2)\n\t" \
    "v_mfma_f32_16x16x32_bf16 v[192:195], v[148:151], v[172:175], v[192:195]\n\t" \
    "v_mov_b64 v[218:219], -1\n\t" \
    "buffer_store_dwordx4 v[216:219], %[ob], %[rs], %[sa] offen sc1\n\t" \
    "v_mfma_f32_16x16x32_bf16 v[188:191], v[64:67], v[168:171], v[188:191]\n\t" \
    "v_mfma_f32_16x16x32_bf16 v[188:191], v[68:71], v[172:175], v[188:191]\n\t" \
    "s_waitcnt lgkmcnt(1)\n\t" \
    "v_mfma_f32_16x16x32_bf16 v[188:191], v[72:75], v[176:179], v[188:191]\n\t" \
    "s_waitcnt lgkmcnt(0)\n\t" \
    "v_mfma_f32_16x16x32_bf16 v[188:191], v[76:79], v[180:183], v[188:191]\n\t" \
    "v_mfma_f32_16x16x32_bf16 v[192:195], v[152:155], v[176:179], v[192:195]\n\t" \
    "v_mfma_f32_16x16x32_bf16 v[192:195], v[156:159], v[180:183], v[192:195]\n\t" \
    "s_setprio 2\n\t" \
    "buffer_load_dwordx4 v[232:235], %[l16], %[rs], %[n0] offen offset:0 sc1\n\t" \
    "buffer_load_dwordx4 v[236:239], %[l16], %[rs], %[n0] offen offset:1024 sc1\n\t" \
    "buffer_load_dwordx4 v[240:243], %[l16], %[rs], %[n0] offen offset:2048 sc1\n\t" \
    "buffer_load_dwordx4 v[244:247], %[l16], %[rs], %[n0] offen offset:3072 sc1\n\t" \
    "buffer_load_dwordx4 v[248:251], %[l16], %[rs], %[n1] offen offset:0 sc1\n\t" \
    ASM_ST3 \
    "v_fma_f32 v188, v188, v206, v208\n\t" \
    "v_fma_f32 v189, v189, v206, v209\n\t" \
    "v_fma_f32 v190, v190, v206, v210\n\t" \
    "v_fma_f32 v191, v191, v206, v211\n\t" \
    "v_mul_f32 v160, v188, v188\n\t" \
    "v_mul_f32 v161, v189, v189\n\t" \
    "v_mul_f32 v162, v190, v190\n\t" \
    "v_mul_f32 v163, v191, v191\n\t" \
    "v_fma_f32 v160, v160, %[k1], v220\n\t" \
    "v_fma_f32 v161, v161, %[k1], v220\n\t" \
    "v_fma_f32 v162, v162, %[k1], v220\n\t" \
    "v_fma_f32 v163, v163, %[k1], v220\n\t" \
    "v_mul_f32 v160, v188, v160\n\t" \
    "v_mul_f32 v161, v189, v161\n\t" \
    "v_mul_f32 v162, v190, v162\n\t" \
    "v_mul_f32 v163, v191, v163\n\t" \
    "v_fma_f32 v192, v192, v206, v212\n\t" \
    "v_fma_f32 v193, v193, v206, v213\n\t" \
    "v_fma_f32 v194, v194, v206, v214\n\t" \
    "v_fma_f32 v195, v195, v206, v215\n\t" \
    "v_mul_f32 v164, v192, v192\n\t" \
    "v_mul_f32 v165, v193, v193\n\t" \
    "v_mul_f32 v166, v194, v194\n\t" \
    "v_mul_f32 v167, v195, v195\n\t" \
    "v_fma_f32 v164, v164, %[k1], v220\n\t" \
    "v_fma_f32 v165, v165, %[k1], v220\n\t" \
    "v_fma_f32 v166, v166, %[k1], v220\n\t" \
    "v_fma_f32 v167, v167, %[k1], v220\n\t" \
    "v_mul_f32 v164, v192, v164\n\t" \
    "v_mul_f32 v165, v193, v165\n\t" \
    "v_mul_f32 v166, v194, v166\n\t" \
    "v_mul_f32 v167, v195, v167\n\t" \
    "v_exp_f32 v160, v160\n\t" \
    "v_exp_f32 v161, v161\n\t" \
    "v_exp_f32 v162, v162\n\t" \
    "v_exp_f32 v163, v163\n\t" \
    "v_exp_f32 v164, v164\n\t" \
    "v_exp_f32 v165, v165\n\t" \
    "v_exp_f32 v166, v166\n\t" \
    "v_exp_f32 v167, v167\n\t" \
    "v_add_f32 v160, 1.0, v160\n\t" \
    "v_add_f32 v161, 1.0, v161\n\t" \
    "v_add_f32 v162, 1.0, v162\n\t" \
    "v_add_f32 v163, 1.0, v163\n\t" \
    "v_add_f32 v164, 1.0, v164\n\t" \
    "v_add_f32 v165, 1.0, v165\n\t" \
    "v_add_f32 v166, 1.0, v166\n\t" \
    "v_add_f32 v167, 1.0, v167\n\t" \
    "v_rcp_f32 v160, v160\n\t" \
    "v_rcp_f32 v161, v161\n\t" \
    "v_rcp_f32 v162, v162\n\t" \
    "v_rcp_f32 v163, v163\n\t" \
    "v_rcp_f32 v164, v164\n\t" \
    "v_rcp_f32 v165, v165\n\t" \
    "v_rcp_f32 v166, v166\n\t" \
    "v_rcp_f32 v167, v167\n\t" \
    "v_mul_f32 v188, v188, v160\n\t" \
    "v_mul_f32 v189, v189, v161\n\t" \
    "v_mul_f32 v190, v190, v162\n\t" \
    "v_mul_f32 v191, v191, v163\n\t" \
    "v_mul_f32 v192, v192, v164\n\t" \
    "v_mul_f32 v193, v193, v165\n\t" \
    "v_mul_f32 v194, v194, v166\n\t" \
    "v_mul_f32 v195, v195, v167\n\t" \
    "v_cvt_pk_bf16_f32 v188, v188, v189\n\t" \
    "v_cvt_pk_bf16_f32 v189, v190, v191\n\t" \
    "v_cvt_pk_bf16_f32 v190, v192, v193\n\t" \
    "v_cvt_pk_bf16_f32 v191, v194, v195\n\t" \
    "v_min_u32 v188, -2, v188\n\t" \
    "v_min_u32 v189, -2, v189\n\t" \
    "v_min_u32 v190, -2, v190\n\t" \
    "v_min_u32 v191, -2, v191\n\t" \
    "v_mov_b32 v216, 0x3f803f80\n\t" \
    "v_mov_b32 v217, 0x3f803f80\n\t" \
    "v_mov_b32 v218, 0x3f803f80\n\t" \
    "v_mov_b32 v219, 0x3f803f80\n\t" \
    "v_permlane16_swap_b32 v188, v190\n\t" \
    "v_permlane16_swap_b32 v189, v191\n\t" \
    "s_waitcnt vmcnt(6)\n\t" \
    "buffer_store_dwordx4 v[188:191], %[oa], %[rs], %[so] offen sc1\n\t" \
    "s_nop 1\n\t" \
    "s_setprio 0\n\t"
#define ASM_WT1_HI \
    "s_setprio 1\n\t" \
    "s_barrier\n\t" ASM_ST \
    "ds_read_b128 v[160:163], %[la] offset:0\n\t" \
    "ds_read_b128 v[164:167], %[la] offset:1024\n\t" \
    "ds_read_b128 v[168:171], %[la] offset:2048\n\t" \
    "ds_read2st64_b32 v[204:205], %[sr] offset0:20 offset1:24\n\t" \
    "ds_read_b128 v[172:175], %[la] offset:3072\n\t" \
    "ds_read_b128 v[176:179], %[la] offset:8192\n\t" \
    "ds_read_b128 v[180:183], %[la] offset:9216\n\t" \
    "ds_read_b128 v[184:187], %[la] offset:10240\n\t" \
    "s_waitcnt lgkmcnt(7)\n\t" \
    "v_mfma_f32_16x16x32_bf16 v[192:195], v[80:83], v[160:163], 0\n\t" \
    "s_waitcnt lgkmcnt(6)\n\t" \
    "v_mfma_f32_16x16x32_bf16 v[192:195], v[84:87], v[164:167], v[192:195]\n\t" \
    "v_mfma_f32_16x16x32_bf16 v[188:191], v[0:3], v[160:163], 0\n\t" \
    "ds_read_b128 v[160:163], %[la] offset:11264\n\t" \
    "v_mfma_f32_16x16x32_bf16 v[188:191], v[4:7], v[164:167], v[188:191]\n\t" \
    "ds_read_b128 v[164:167], %[la] offset:16384\n\t" \
    "s_waitcnt lgkmcnt(7)\n\t" \
    "v_mfma_f32_16x16x32_bf16 v[188:191], v[8:11], v[168:171], v[188:191]\n\t" \
    "s_waitcnt lgkmcnt(6)\n\t" \
    "s_waitcnt lgkmcnt(5)\n\t" \
    "v_mfma_f32_16x16x32_bf16 v[188:191], v[12:15], v[172:175], v[188:191]\n\t" \
    "v_permlane16_swap_b32 v204, v205\n\t" \
    "v_mfma_f32_16x16x32_bf16 v[192:195], v[88:91], v[168:171], v[192:195]\n\t" \
    "ds_read_b128 v[168:171], %[la] offset:17408\n\t" \
    "v_mfma_f32_16x16x32_bf16 v[192:195], v[92:95], v[172:175], v[192:195]\n\t" \
    "ds_read_b128 v[172:175], %[la] offset:18432\n\t" \
    "v_add_f32 v206, v204, v205\n\t" \
    "s_waitcnt lgkmcnt(6)\n\t" \
    "v_mfma_f32_16x16x32_bf16 v[192:195], v[96:99], v[176:179], v[192:195]\n\t" \
    "v_add_f32 v207, v204, v205\n\t" \
    "s_waitcnt lgkmcnt(5)\n\t" \
    "v_mfma_f32_16x16x32_bf16 v[192:195], v[100:103], v[180:183], v[192:195]\n\t" \
    "v_mfma_f32_16x16x32_bf16 v[188:191], v[16:19], v[176:179], v[188:191]\n\t" \
    "ds_read_b128 v[176:179], %[la] offset:19456\n\t" \
    "v_permlane32_swap_b32 v206, v207\n\t" \
    "v_mfma_f32_16x16x32_bf16 v[188:191], v[20:23], v[180:183], v[188:191]\n\t" \
    "ds_read_b128 v[180:183], %[la] offset:24576\n\t" \
    "s_waitcnt lgkmcnt(6)\n\t" \
    "v_mfma_f32_16x16x32_bf16 v[188:191], v[24:27], v[184:187], v[188:191]\n\t" \
    "v_add_f32 v204, v206, v207\n\t" \
    "s_waitcnt lgkmcnt(5)\n\t" \
    "v_mfma_f32_16x16x32_bf16 v[188:191], v[28:31], v[160:163], v[188:191]\n\t" \
    "v_add_f32 v205, v206, v207\n\t" \
    "v_mfma_f32_16x16x32_bf16 v[192:195], v[104:107], v[184:187], v[192:195]\n\t" \
    "ds_read_b128 v[184:187], %[la] offset:25600\n\t" \
    "v_mfma_f32_16x16x32_bf16 v[192:195], v[108:111], v[160:163], v[192:195]\n\t" \
    "ds_read_b128 v[160:163], %[la] offset:26624\n\t" \
    "v_permlane16_swap_b32 v204, v205\n\t" \
    "s_waitcnt lgkmcnt(6)\n\t" \
    "v_mfma_f32_16x16x32_bf16 v[192:195], v[112:115], v[164:167], v[192:195]\n\t" \
    "s_waitcnt lgkmcnt(5)\n\t" \
    "v_mfma_f32_16x16x32_bf16 v[192:195], v[116:119], v[168:171], v[192:195]\n\t" \
    "v_mul_f32 v205, %[kd], v205\n\t" \
    "v_mfma_f32_16x16x32_bf16 v[188:191], v[32:35], v[164:167], v[188:191]\n\t" \
    "ds_read_b128 v[164:167], %[la] offset:27648\n\t" \
    "v_mul_f32 v204, %[kd], v204\n\t" \
    "v_mfma_f32_16x16x32_bf16 v[188:191], v[36:39], v[168:171], v[188:191]\n\t" \
    "ds_read_b128 v[168:171], %[la] offset:4096\n\t" \
    "v_fma_f32 v204, -v205, v205, v204\n\t" \
    "s_waitcnt lgkmcnt(6)\n\t" \
    "v_mfma_f32_16x16x32_bf16 v[188:191], v[40:43], v[172:175], v[188:191]\n\t" \
    "v_max_f32 v204, 0, v204\n\t" \
    "s_waitcnt lgkmcnt(5)\n\t" \
    "v_mfma_f32_16x16x32_bf16 v[188:191], v[44:47], v[176:179], v[188:191]\n\t" \
    "v_add_f32 v206, %[ke], v204\n\t" \
    "v_mfma_f32_16x16x32_bf16 v[192:195], v[120:123], v[172:175], v[192:195]\n\t" \
    "ds_read_b128 v[172:175], %[la] offset:12288\n\t" \
    "v_rsq_f32 v206, v206\n\t" \
    "v_mfma_f32_16x16x32_bf16 v[192:195], v[124:127], v[176:179], v[192:195]\n\t" \
    "ds_read_b128 v[176:179], %[la] offset:20480\n\t" \
    "s_waitcnt lgkmcnt(6)\n\t" \
    "v_mfma_f32_16x16x32_bf16 v[192:195], v[128:131], v[180:183], v[192:195]\n\t" \
    "v_mul_f32 v207, v204, v206\n\t" \
    "s_waitcnt lgkmcnt(5)\n\t" \
    "v_mfma_f32_16x16x32_bf16 v[192:195], v[132:135], v[184:187], v[192:195]\n\t" \
    "v_fma_f32 v207, v207, v206, %[ke]\n\t" \
    "v_mfma_f32_16x16x32_bf16 v[188:191], v[48:51], v[180:183], v[188:191]\n\t" \
    "ds_read_b128 v[180:183], %[la] offset:28672\n\t" \
    "v_rsq_f32 v207, v207\n\t" \
    "v_mfma_f32_16x16x32_bf16 v[188:191], v[52:55], v[184:187], v[188:191]\n\t" \
    "v_mul_f32 v206, v206, v207\n\t" \
    "v_mul_f32 v207, -v205, v206\n\t" \
    "s_waitcnt lgkmcnt(5)\n\t" \
    "v_mfma_f32_16x16x32_bf16 v[188:191], v[56:59], v[160:163], v[188:191]\n\t" \
    "v_mul_f32 v208, v196, v207\n\t" \
    "v_mul_f32 v209, v197, v207\n\t" \
    "s_waitcnt lgkmcnt(4)\n\t" \
    "v_mfma_f32_16x16x32_bf16 v[188:191], v[60:63], v[164:167], v[188:191]\n\t" \
    "v_mul_f32 v210, v198, v207\n\t" \
    "v_mul_f32 v211, v199, v207\n\t" \
    "v_mfma_f32_16x16x32_bf16 v[192:195], v[136:139], v[160:163], v[192:195]\n\t" \
    "v_mul_f32 v212, v200, v207\n\t" \
    "v_mul_f32 v213, v201, v207\n\t" \
    "v_mfma_f32_16x16x32_bf16 v[192:195], v[140:143], v[164:167], v[192:195]\n\t" \
    "v_mul_f32 v214, v202, v207\n\t" \
    "v_mul_f32 v215, v203, v207\n\t" \
    "s_waitcnt lgkmcnt(3)\n\t" \
    "v_mfma_f32_16x16x32_bf16 v[192:195], v[144:147], v[168:171], v[192:195]\n\t" \
    "v_mov_b64 v[216:217], -1\n\t" \
    "v_mov_b64 v[218:219], -1\n\t" \
    "s_waitcnt lgkmcnt(2)\n\t" \
    "v_mfma_f32_16x16x32_bf16 v[192:195], v[148:151], v[172:175], v[192:195]\n\t" \
    "buffer_store_dwordx4 v[216:219], %[ob], %[rs], %[sa] offen sc1\n\t" \
    "v_mfma_f32_16x16x32_bf16 v[188:191], v[64:67], v[168:171], v[188:191]\n\t" \
    "v_mfma_f32_16x16x32_bf16 v[188:191], v[68:71], v[172:175], v[188:191]\n\t" \
    "s_waitcnt lgkmcnt(1)\n\t" \
    "v_mfma_f32_16x16x32_bf16 v[188:191], v[72:75], v[176:179], v[188:191]\n\t" \
    "s_waitcnt lgkmcnt(0)\n\t" \
    "v_mfma_f32_16x16x32_bf16 v[188:191], v[76:79], v[180:183], v[188:191]\n\t" \
    "v_mfma_f32_16x16x32_bf16 v[192:195], v[152:155], v[176:179], v[192:195]\n\t" \
    "v_mfma_f32_16x16x32_bf16 v[192:195], v[156:159], v[180:183], v[192:195]\n\t" \
    ASM_ST3 \
    "s_nop 1\n\t" \
    "v_fma_f32 v188, v188, v206, v208\n\t" \
    "v_fma_f32 v189, v189, v206, v209\n\t" \
    "v_fma_f32 v190, v190, v206, v210\n\t" \
    "v_fma_f32 v191, v191, v206, v211\n\t" \
    "v_mul_f32 v160, v188, v188\n\t" \
    "v_mul_f32 v161, v189, v189\n\t" \
    "v_mul_f32 v162, v190, v190\n\t" \
    "v_mul_f32 v163, v191, v191\n\t" \
    "v_fma_f32 v160, v160, %[k1], v220\n\t" \
    "v_fma_f32 v161, v161, %[k1], v220\n\t" \
    "v_fma_f32 v162, v162, %[k1], v220\n\t" \
    "v_fma_f32 v163, v163, %[k1], v220\n\t" \
    "v_mul_f32 v160, v188, v160\n\t" \
    "v_mul_f32 v161, v189, v161\n\t" \
    "v_mul_f32 v162, v190, v162\n\t" \
    "v_mul_f32 v163, v191, v163\n\t" \
    "v_fma_f32 v192, v192, v206, v212\n\t" \
    "v_fma_f32 v193, v193, v206, v213\n\t" \
    "v_fma_f32 v194, v194, v206, v214\n\t" \
    "v_fma_f32 v195, v195, v206, v215\n\t" \
    "v_mul_f32 v164, v192, v192\n\t" \
    "v_mul_f32 v165, v193, v193\n\t" \
    "v_mul_f32 v166, v194, v194\n\t" \
    "v_mul_f32 v167, v195, v195\n\t" \
    "v_fma_f32 v164, v164, %[k1], v220\n\t" \
    "v_fma_f32 v165, v165, %[k1], v220\n\t" \
    "v_fma_f32 v166, v166, %[k1], v220\n\t" \
    "v_fma_f32 v167, v167, %[k1], v220\n\t" \
    "v_mul_f32 v164, v192, v164\n\t" \
    "v_mul_f32 v165, v193, v165\n\t" \
    "v_mul_f32 v166, v194, v166\n\t" \
    "v_mul_f32 v167, v195, v167\n\t" \
    "v_exp_f32 v160, v160\n\t" \
    "v_exp_f32 v161, v161\n\t" \
    "v_exp_f32 v162, v162\n\t" \
    "v_exp_f32 v163, v163\n\t" \
    "v_exp_f32 v164, v164\n\t" \
    "v_exp_f32 v165, v165\n\t" \
    "v_exp_f32 v166, v166\n\t" \
    "v_exp_f32 v167, v167\n\t" \
    "v_add_f32 v160, 1.0, v160\n\t" \
    "v_add_f32 v161, 1.0, v161\n\t" \
    "v_add_f32 v162, 1.0, v162\n\t" \
    "v_add_f32 v163, 1.0, v163\n\t" \
    "v_add_f32 v164, 1.0, v164\n\t" \
    "v_add_f32 v165, 1.0, v165\n\t" \
    "v_add_f32 v166, 1.0, v166\n\t" \
    "v_add_f32 v167, 1.0, v167\n\t" \
    "v_rcp_f32 v160, v160\n\t" \
    "v_rcp_f32 v161, v161\n\t" \
    "v_rcp_f32 v162, v162\n\t" \
    "v_rcp_f32 v163, v163\n\t" \
    "v_rcp_f32 v164, v164\n\t" \
    "v_rcp_f32 v165, v165\n\t" \
    "v_rcp_f32 v166, v166\n\t" \
    "v_rcp_f32 v167, v167\n\t" \
    "v_mul_f32 v188, v188, v160\n\t" \
    "v_mul_f32 v189, v189, v161\n\t" \
    "v_mul_f32 v190, v190, v162\n\t" \
    "v_mul_f32 v191, v191, v163\n\t" \
    "v_mul_f32 v192, v192, v164\n\t" \
    "v_mul_f32 v193, v193, v165\n\t" \
    "v_mul_f32 v194, v194, v166\n\t" \
    "v_mul_f32 v195, v195, v167\n\t" \
    "v_cvt_pk_bf16_f32 v188, v188, v189\n\t" \
    "v_cvt_pk_bf16_f32 v189, v190, v191\n\t" \
    "v_cvt_pk_bf16_f32 v190, v192, v193\n\t" \
    "v_cvt_pk_bf16_f32 v191, v194, v195\n\t" \
    "v_min_u32 v188, -2, v188\n\t" \
    "v_min_u32 v189, -2, v189\n\t" \
    "v_min_u32 v190, -2, v190\n\t" \
    "v_min_u32 v191, -2, v191\n\t" \
    "s_nop 1\n\t" \
    "v_permlane16_swap_b32 v188, v190\n\t" \
    "v_permlane16_swap_b32 v189, v191\n\t" \
    "s_waitcnt vmcnt(1)\n\t" \
    "buffer_store_dwordx4 v[188:191], %[oa], %[rs], %[so] offen sc1\n\t" \
    "s_nop 1\n\t"
#define ASM_WT2_LO \
    "s_branch .Lt0%=\n\t" \
    ".Ltr%=:\n\t" \
    "s_cmp_gt_i32 %[n], 0\n\t" \
    "s_cbranch_scc0 .Ltg%=\n\t" \
    ".Lfw%=:\n\t" \
    "s_mov_b32 %[a], 0\n\t" \
    "v_max_u32 v252, v232, v233\n\t" \
    "v_max3_u32 v252, v234, v235, v252\n\t" \
    "v_cmp_eq_u32 vcc, -1, v252\n\t" \
    "s_cbranch_vccz .Lgw0_%=\n\t" \
    "buffer_load_dwordx4 v[232:235], %[l16], %[rs], %[i0] offen offset:0 sc1\n\t" \
    "s_mov_b32 %[a], 1\n\t" \
    ".Lgw0_%=:\n\t" \
    "v_max_u32 v252, v236, v237\n\t" \
    "v_max3_u32 v252, v238, v239, v252\n\t" \
    "v_cmp_eq_u32 vcc, -1, v252\n\t" \
    "s_cbranch_vccz .Lgw1_%=\n\t" \
    "buffer_load_dwordx4 v[236:239], %[l16], %[rs], %[i0] offen offset:1024 sc1\n\t" \
    "s_mov_b32 %[a], 1\n\t" \
    ".Lgw1_%=:\n\t" \
    "v_max_u32 v252, v240, v241\n\t" \
    "v_max3_u32 v252, v242, v243, v252\n\t" \
    "v_cmp_eq_u32 vcc, -1, v252\n\t" \
    "s_cbranch_vccz .Lgw2_%=\n\t" \
    "buffer_load_dwordx4 v[240:243], %[l16], %[rs], %[i0] offen offset:2048 sc1\n\t" \
    "s_mov_b32 %[a], 1\n\t" \
    ".Lgw2_%=:\n\t" \
    "v_max_u32 v252, v244, v245\n\t" \
    "v_max3_u32 v252, v246, v247, v252\n\t" \
    "v_cmp_eq_u32 vcc, -1, v252\n\t" \
    "s_cbranch_vccz .Lgw3_%=\n\t" \
    "buffer_load_dwordx4 v[244:247], %[l16], %[rs], %[i0] offen offset:3072 sc1\n\t" \
    "s_mov_b32 %[a], 1\n\t" \
    ".Lgw3_%=:\n\t" \
    "v_max_u32 v252, v248, v249\n\t" \
    "v_max3_u32 v252, v250, v251, v252\n\t" \
    "v_cmp_eq_u32 vcc, -1, v252\n\t" \
    "s_cbranch_vccz .Lgw4_%=\n\t" \
    "buffer_load_dwordx4 v[248:251], %[l16], %[rs], %[i1] offen offset:0 sc1\n\t" \
    "s_mov_b32 %[a], 1\n\t" \
    ".Lgw4_%=:\n\t" \
    "s_cmp_eq_u32 %[a], 0\n\t" \
    "s_cbranch_scc1 .Lhw%=\n\t" \
    "s_waitcnt vmcnt(20)\n\t" \
    ".Liw%=:\n\t" \
    "v_max_u32 v252, v232, v233\n\t" \
    "v_max3_u32 v252, v234, v235, v252\n\t" \
    "v_max3_u32 v252, v236, v237, v252\n\t" \
    "v_max3_u32 v252, v238, v239, v252\n\t" \
    "v_max3_u32 v252, v240, v241, v252\n\t" \
    "v_max3_u32 v252, v242, v243, v252\n\t" \
    "v_max3_u32 v252, v244, v245, v252\n\t" \
    "v_max3_u32 v252, v246, v247, v252\n\t" \
    "v_max3_u32 v252, v248, v249, v252\n\t" \
    "v_max3_u32 v252, v250, v251, v252\n\t" \
    "v_cmp_eq_u32 vcc, -1, v252\n\t" \
    "s_cbranch_vccz .Lhw%=\n\t" \
    "s_sleep 1\n\t" \
    "v_max_u32 v252, v232, v233\n\t" \
    "v_max3_u32 v252, v234, v235, v252\n\t" \
    "v_max3_u32 v252, v236, v237, v252\n\t" \
    "v_max3_u32 v252, v238, v239, v252\n\t" \
    "v_max3_u32 v252, v240, v241, v252\n\t" \
    "v_max3_u32 v252, v242, v243, v252\n\t" \
    "v_max3_u32 v252, v244, v245, v252\n\t" \
    "v_max3_u32 v252, v246, v247, v252\n\t" \
    "v_max3_u32 v252, v248, v249, v252\n\t" \
    "v_max3_u32 v252, v250, v251, v252\n\t" \
    "v_cmp_eq_u32 vcc, -1, v252\n\t" \
    "s_cbranch_vccz .Lhw%=\n\t" \
    "s_sleep 1\n\t" \
    "v_max_u32 v252, v232, v233\n\t" \
    "v_max3_u32 v252, v234, v235, v252\n\t" \
    "v_max3_u32 v252, v236, v237, v252\n\t" \
    "v_max3_u32 v252, v238, v239, v252\n\t" \
    "v_max3_u32 v252, v240, v241, v252\n\t" \
    "v_max3_u32 v252, v242, v243, v252\n\t" \
    "v_max3_u32 v252, v244, v245, v252\n\t" \
    "v_max3_u32 v252, v246, v247, v252\n\t" \
    "v_max3_u32 v252, v248, v249, v252\n\t" \
    "v_max3_u32 v252, v250, v251, v252\n\t" \
    "v_cmp_eq_u32 vcc, -1, v252\n\t" \
    "s_cbranch_vccz .Lhw%=\n\t" \
    "s_sleep 1\n\t" \
    "v_max_u32 v252, v232, v233\n\t" \
    "v_max3_u32 v252, v234, v235, v252\n\t" \
    "v_max3_u32 v252, v236, v237, v252\n\t" \
    "v_max3_u32 v252, v238, v239, v252\n\t" \
    "v_max3_u32 v252, v240, v241, v252\n\t" \
    "v_max3_u32 v252, v242, v243, v252\n\t" \
    "v_max3_u32 v252, v244, v245, v252\n\t" \
    "v_max3_u32 v252, v246, v247, v252\n\t" \
    "v_max3_u32 v252, v248, v249, v252\n\t" \
    "v_max3_u32 v252, v250, v251, v252\n\t" \
    "v_cmp_eq_u32 vcc, -1, v252\n\t" \
    "s_cbranch_vccz .Lhw%=\n\t" \
    "s_sleep 1\n\t" \
    "v_max_u32 v252, v232, v233\n\t" \
    "v_max3_u32 v252, v234, v235, v252\n\t" \
    "v_max3_u32 v252, v236, v237, v252\n\t" \
    "v_max3_u32 v252, v238, v239, v252\n\t" \
    "v_max3_u32 v252, v240, v241, v252\n\t" \
    "v_max3_u32 v252, v242, v243, v252\n\t" \
    "v_max3_u32 v252, v244, v245, v252\n\t" \
    "v_max3_u32 v252, v246, v247, v252\n\t" \
    "v_max3_u32 v252, v248, v249, v252\n\t" \
    "v_max3_u32 v252, v250, v251, v252\n\t" \
    "v_cmp_eq_u32 vcc, -1, v252\n\t" \
    "s_cbranch_vccz .Lhw%=\n\t" \
    "s_sleep 1\n\t" \
    "v_max_u32 v252, v232, v233\n\t" \
    "v_max3_u32 v252, v234, v235, v252\n\t" \
    "v_max3_u32 v252, v236, v237, v252\n\t" \
    "v_max3_u32 v252, v238, v239, v252\n\t" \
    "v_max3_u32 v252, v240, v241, v252\n\t" \
    "v_max3_u32 v252, v242, v243, v252\n\t" \
    "v_max3_u32 v252, v244, v245, v252\n\t" \
    "v_max3_u32 v252, v246, v247, v252\n\t" \
    "v_max3_u32 v252, v248, v249, v252\n\t" \
    "v_max3_u32 v252, v250, v251, v252\n\t" \
    "v_cmp_eq_u32 vcc, -1, v252\n\t" \
    "s_cbranch_vccz .Lhw%=\n\t" \
    "s_sleep 1\n\t" \
    "s_sub_u32 %[n], %[n], 1\n\t" \
    "s_cmp_gt_i32 %[n], 0\n\t" \
    "s_cbranch_scc1 .Lfw%=\n\t" \
    ".Lhw%=:\n\t" \
    "s_branch .Lta%=\n\t" \
    ".Lt0%=:\n\t" \
    "s_waitcnt vmcnt(1)\n\t" \
    ".Lta%=:\n\t" \
    "ds_write_b128 %[lw], v[232:235] offset:0\n\t" \
    "v_max_u32 v252, v232, v233\n\t" \
    "v_max3_u32 v252, v234, v235, v252\n\t" \
    "ds_write_b128 %[lw], v[236:239] offset:1024\n\t" \
    "v_max3_u32 v252, v236, v237, v252\n\t" \
    "v_max3_u32 v252, v238, v239, v252\n\t" \
    "ds_write_b128 %[lw], v[240:243] offset:2048\n\t" \
    "v_max3_u32 v252, v240, v241, v252\n\t" \
    "v_max3_u32 v252, v242, v243, v252\n\t" \
    "ds_write_b128 %[lw], v[244:247] offset:3072\n\t" \
    "v_max3_u32 v252, v244, v245, v252\n\t" \
    "v_max3_u32 v252, v246, v247, v252\n\t" \
    "ds_write_b128 %[lw], v[248:251] offset:4096\n\t" \
    "v_max3_u32 v252, v248, v249, v252\n\t" \
    "v_max3_u32 v252, v250, v251, v252\n\t" \
    "v_cmp_eq_u32 vcc, -1, v252\n\t" \
    "s_cbranch_vccnz .Ltr%=\n\t" \
    ".Ltg%=:\n\t" \
    ASM_ST2 \
    "s_waitcnt lgkmcnt(0)\n\t" \
    "s_setprio 2\n\t" \
    "s_barrier\n\t" ASM_ST \
    "ds_read_b128 v[160:163], %[la] offset:0\n\t" \
    "ds_read_b128 v[164:167], %[la] offset:1024\n\t" \
    "ds_read_b128 v[168:171], %[la] offset:2048\n\t" \
    "ds_read_b128 v[172:175], %[la] offset:3072\n\t" \
    "ds_read_b128 v[176:179], %[la] offset:8192\n\t" \
    "ds_read_b128 v[180:183], %[la] offset:9216\n\t" \
    "ds_read_b128 v[184:187], %[la] offset:10240\n\t" \
    "s_waitcnt lgkmcnt(6)\n\t" \
    "v_mfma_f32_16x16x32_bf16 v[192:195], v[80:83], v[160:163], 0\n\t" \
    "buffer_load_dwordx2 v[196:197], %[ro], %[rs], %[sx] offen sc1\n\t" \
    "s_waitcnt lgkmcnt(5)\n\t" \
    "v_mfma_f32_16x16x32_bf16 v[192:195], v[84:87], v[164:167], v[192:195]\n\t" \
    "buffer_load_dwordx2 v[198:199], %[ro], %[rs], %[sx] offen offset:512 sc1\n\t" \
    "v_mfma_f32_16x16x32_bf16 v[188:191], v[0:3], v[160:163], 0\n\t" \
    "ds_read_b128 v[160:163], %[la] offset:11264\n\t" \
    "v_lshrrev_b32 v204, 1, %[l16]\n\t" \
    "v_mfma_f32_16x16x32_bf16 v[188:191], v[4:7], v[164:167], v[188:191]\n\t" \
    "ds_read_b128 v[164:167], %[la] offset:16384\n\t" \
    "v_and_b32 v204, 0x78, v204\n\t" \
    "s_waitcnt lgkmcnt(6)\n\t" \
    "v_mfma_f32_16x16x32_bf16 v[188:191], v[8:11], v[168:171], v[188:191]\n\t" \
    "buffer_load_dwordx2 v[200:201], v204, %[rs], %[ss] offen sc1\n\t" \
    "s_waitcnt lgkmcnt(5)\n\t" \
    "v_mfma_f32_16x16x32_bf16 v[188:191], v[12:15], v[172:175], v[188:191]\n\t" \
    "v_mov_b64 v[216:217], -1\n\t" \
    "v_mfma_f32_16x16x32_bf16 v[192:195], v[88:91], v[168:171], v[192:195]\n\t" \
    "ds_read_b128 v[168:171], %[la] offset:17408\n\t" \
    "v_mov_b64 v[218:219], -1\n\t" \
    "v_mfma_f32_16x16x32_bf16 v[192:195], v[92:95], v[172:175], v[192:195]\n\t" \
    "ds_read_b128 v[172:175], %[la] offset:18432\n\t" \
    "buffer_store_dwordx4 v[216:219], %[l16], %[rs], %[sa] offen sc1\n\t" \
    "s_waitcnt lgkmcnt(6)\n\t" \
    "v_mfma_f32_16x16x32_bf16 v[192:195], v[96:99], v[176:179], v[192:195]\n\t" \
    "s_waitcnt lgkmcnt(5)\n\t" \
    "v_mfma_f32_16x16x32_bf16 v[192:195], v[100:103], v[180:183], v[192:195]\n\t" \
    "v_mfma_f32_16x16x32_bf16 v[188:191], v[16:19], v[176:179], v[188:191]\n\t" \
    "ds_read_b128 v[176:179], %[la] offset:19456\n\t" \
    "v_mfma_f32_16x16x32_bf16 v[188:191], v[20:23], v[180:183], v[188:191]\n\t" \
    "ds_read_b128 v[180:183], %[la] offset:24576\n\t" \
    "s_waitcnt lgkmcnt(6)\n\t" \
    "v_mfma_f32_16x16x32_bf16 v[188:191], v[24:27], v[184:187], v[188:191]\n\t" \
    "s_waitcnt lgkmcnt(5)\n\t" \
    "v_mfma_f32_16x16x32_bf16 v[188:191], v[28:31], v[160:163], v[188:191]\n\t" \
    "v_mfma_f32_16x16x32_bf16 v[192:195], v[104:107], v[184:187], v[192:195]\n\t" \
    "ds_read_b128 v[184:187], %[la] offset:25600\n\t" \
    "v_mfma_f32_16x16x32_bf16 v[192:195], v[108:111], v[160:163], v[192:195]\n\t" \
    "ds_read_b128 v[160:163], %[la] offset:26624\n\t" \
    "s_waitcnt lgkmcnt(6)\n\t" \
    "v_mfma_f32_16x16x32_bf16 v[192:195], v[112:115], v[164:167], v[192:195]\n\t" \
    "s_waitcnt lgkmcnt(5)\n\t" \
    "v_mfma_f32_16x16x32_bf16 v[192:195], v[116:119], v[168:171], v[192:195]\n\t" \
    "v_mfma_f32_16x16x32_bf16 v[188:191], v[32:35], v[164:167], v[188:191]\n\t" \
    "ds_read_b128 v[164:167], %[la] offset:27648\n\t" \
    "v_mfma_f32_16x16x32_bf16 v[188:191], v[36:39], v[168:171], v[188:191]\n\t" \
    "ds_read_b128 v[168:171], %[la] offset:4096\n\t" \
    "s_waitcnt lgkmcnt(6)\n\t" \
    "v_mfma_f32_16x16x32_bf16 v[188:191], v[40:43], v[172:175], v[188:191]\n\t" \
    "s_waitcnt lgkmcnt(5)\n\t" \
    "v_mfma_f32_16x16x32_bf16 v[188:191], v[44:47], v[176:179], v[188:191]\n\t" \
    "v_mfma_f32_16x16x32_bf16 v[192:195], v[120:123], v[172:175], v[192:195]\n\t" \
    "ds_read_b128 v[172:175], %[la] offset:12288\n\t" \
    "v_mfma_f32_16x16x32_bf16 v[192:195], v[124:127], v[176:179], v[192:195]\n\t" \
    "ds_read_b128 v[176:179], %[la] offset:20480\n\t" \
    "s_waitcnt lgkmcnt(6)\n\t" \
    "v_mfma_f32_16x16x32_bf16 v[192:195], v[128:131], v[180:183], v[192:195]\n\t" \
    "s_waitcnt lgkmcnt(5)\n\t" \
    "v_mfma_f32_16x16x32_bf16 v[192:195], v[132:135], v[184:187], v[192:195]\n\t" \
    "v_mfma_f32_16x16x32_bf16 v[188:191], v[48:51], v[180:183], v[188:191]\n\t" \
    "ds_read_b128 v[180:183], %[la] offset:28672\n\t" \
    "v_mfma_f32_16x16x32_bf16 v[188:191], v[52:55], v[184:187], v[188:191]\n\t" \
    "s_waitcnt lgkmcnt(5)\n\t" \
    "v_mfma_f32_16x16x32_bf16 v[188:191], v[56:59], v[160:163], v[188:191]\n\t" \
    "s_waitcnt lgkmcnt(4)\n\t" \
    "v_mfma_f32_16x16x32_bf16 v[188:191], v[60:63], v[164:167], v[188:191]\n\t" \
    "v_mfma_f32_16x16x32_bf16 v[192:195], v[136:139], v[160:163], v[192:195]\n\t" \
    "v_mfma_f32_16x16x32_bf16 v[192:195], v[140:143], v[164:167], v[192:195]\n\t" \
    "s_waitcnt lgkmcnt(3)\n\t" \
    "v_mfma_f32_16x16x32_bf16 v[192:195], v[144:147], v[168:171], v[192:195]\n\t" \
    "s_waitcnt lgkmcnt(2)\n\t" \
    "v_mfma_f32_16x16x32_bf16 v[192:195], v[148:151], v[172:175], v[192:195]\n\t" \
    "v_mfma_f32_16x16x32_bf16 v[188:191], v[64:67], v[168:171], v[188:191]\n\t" \
    "v_mfma_f32_16x16x32_bf16 v[188:191], v[68:71], v[172:175], v[188:191]\n\t" \
    "s_waitcnt lgkmcnt(1)\n\t" \
    "v_mfma_f32_16x16x32_bf16 v[188:191], v[72:75], v[176:179], v[188:191]\n\t" \
    "s_waitcnt lgkmcnt(0)\n\t" \
    "v_mfma_f32_16x16x32_bf16 v[188:191], v[76:79], v[180:183], v[188:191]\n\t" \
    "v_mfma_f32_16x16x32_bf16 v[192:195], v[152:155], v[176:179], v[192:195]\n\t" \
    "v_mfma_f32_16x16x32_bf16 v[192:195], v[156:159], v[180:183], v[192:195]\n\t" \
    "s_setprio 2\n\t" \
    "buffer_load_dwordx4 v[232:235], %[l16], %[rs], %[n0] offen offset:0 sc1\n\t" \
    "buffer_load_dwordx4 v[236:239], %[l16], %[rs], %[n0] offen offset:1024 sc1\n\t" \
    "buffer_load_dwordx4 v[240:243], %[l16], %[rs], %[n0] offen offset:2048 sc1\n\t" \
    "buffer_load_dwordx4 v[244:247], %[l16], %[rs], %[n0] offen offset:3072 sc1\n\t" \
    "buffer_load_dwordx4 v[248:251], %[l16], %[rs], %[n1] offen offset:0 sc1\n\t" \
    ASM_ST3 \
    "s_waitcnt vmcnt(6)\n\t" \
    "v_lshlrev_b32 v160, 16, v196\n\t" \
    "v_and_b32 v161, 0xffff0000, v196\n\t" \
    "v_lshlrev_b32 v162, 16, v197\n\t" \
    "v_and_b32 v163, 0xffff0000, v197\n\t" \
    "v_lshlrev_b32 v164, 16, v198\n\t" \
    "v_and_b32 v165, 0xffff0000, v198\n\t" \
    "v_lshlrev_b32 v166, 16, v199\n\t" \
    "v_and_b32 v167, 0xffff0000, v199\n\t" \
    "v_sub_f32 v160, v160, v200\n\t" \
    "v_sub_f32 v161, v161, v200\n\t" \
    "v_sub_f32 v162, v162, v200\n\t" \
    "v_sub_f32 v163, v163, v200\n\t" \
    "v_sub_f32 v164, v164, v200\n\t" \
    "v_sub_f32 v165, v165, v200\n\t" \
    "v_sub_f32 v166, v166, v200\n\t" \
    "v_sub_f32 v167, v167, v200\n\t" \
    "v_mul_f32 v160, v160, v201\n\t" \
    "v_mul_f32 v161, v161, v201\n\t" \
    "v_mul_f32 v162, v162, v201\n\t" \
    "v_mul_f32 v163, v163, v201\n\t" \
    "v_mul_f32 v164, v164, v201\n\t" \
    "v_mul_f32 v165, v165, v201\n\t" \
    "v_mul_f32 v166, v166, v201\n\t" \
    "v_mul_f32 v167, v167, v201\n\t" \
    "v_cvt_pk_bf16_f32 v160, v160, v161\n\t" \
    "v_cvt_pk_bf16_f32 v162, v162, v163\n\t" \
    "v_cvt_pk_bf16_f32 v164, v164, v165\n\t" \
    "v_cvt_pk_bf16_f32 v166, v166, v167\n\t" \
    "v_and_b32 v161, 0xffff0000, v160\n\t" \
    "v_lshlrev_b32 v160, 16, v160\n\t" \
    "v_and_b32 v163, 0xffff0000, v162\n\t" \
    "v_lshlrev_b32 v162, 16, v162\n\t" \
    "v_and_b32 v165, 0xffff0000, v164\n\t" \
    "v_lshlrev_b32 v164, 16, v164\n\t" \
    "v_and_b32 v167, 0xffff0000, v166\n\t" \
    "v_lshlrev_b32 v166, 16, v166\n\t" \
    "v_add_f32 v188, v188, v160\n\t" \
    "v_add_f32 v189, v189, v161\n\t" \
    "v_add_f32 v190, v190, v162\n\t" \
    "v_add_f32 v191, v191, v163\n\t" \
    "v_add_f32 v192, v192, v164\n\t" \
    "v_add_f32 v193, v193, v165\n\t" \
    "v_add_f32 v194, v194, v166\n\t" \
    "v_add_f32 v195, v195, v167\n\t" \
    "v_cvt_pk_bf16_f32 v188, v188, v189\n\t" \
    "v_cvt_pk_bf16_f32 v189, v190, v191\n\t" \
    "v_cvt_pk_bf16_f32 v190, v192, v193\n\t" \
    "v_cvt_pk_bf16_f32 v191, v194, v195\n\t" \
    "v_min_u32 v188, -2, v188\n\t" \
    "v_min_u32 v189, -2, v189\n\t" \
    "v_min_u32 v190, -2, v190\n\t" \
    "v_min_u32 v191, -2, v191\n\t" \
    "s_nop 1\n\t" \
    "v_permlane16_swap_b32 v188, v190\n\t" \
    "v_permlane16_swap_b32 v189, v191\n\t" \
    "s_waitcnt vmcnt(6)\n\t" \
    "buffer_store_dwordx4 v[188:191], %[oa], %[rs], %[so] offen sc1\n\t" \
    "s_nop 1\n\t" \
    "s_setprio 0\n\t"
#define ASM_WT2_HI \
    "s_setprio 1\n\t" \
    "s_barrier\n\t" ASM_ST \
    "ds_read_b128 v[160:163], %[la] offset:0\n\t" \
    "ds_read_b128 v[164:167], %[la] offset:1024\n\t" \
    "ds_read_b128 v[168:171], %[la] offset:2048\n\t" \
    "ds_read_b128 v[172:175], %[la] offset:3072\n\t" \
    "ds_read_b128 v[176:179], %[la] offset:8192\n\t" \
    "ds_read_b128 v[180:183], %[la] offset:9216\n\t" \
    "ds_read_b128 v[184:187], %[la] offset:10240\n\t" \
    "s_waitcnt lgkmcnt(6)\n\t" \
    "v_mfma_f32_16x16x32_bf16 v[192:195], v[80:83], v[160:163], 0\n\t" \
    "buffer_load_dwordx2 v[196:197], %[ro], %[rs], %[sx] offen sc1\n\t" \
    "s_waitcnt lgkmcnt(5)\n\t" \
    "v_mfma_f32_16x16x32_bf16 v[192:195], v[84:87], v[164:167], v[192:195]\n\t" \
    "buffer_load_dwordx2 v[198:199], %[ro], %[rs], %[sx] offen offset:512 sc1\n\t" \
    "v_mfma_f32_16x16x32_bf16 v[188:191], v[0:3], v[160:163], 0\n\t" \
    "ds_read_b128 v[160:163], %[la] offset:11264\n\t" \
    "v_lshrrev_b32 v204, 1, %[l16]\n\t" \
    "v_mfma_f32_16x16x32_bf16 v[188:191], v[4:7], v[164:167], v[188:191]\n\t" \
    "ds_read_b128 v[164:167], %[la] offset:16384\n\t" \
    "v_and_b32 v204, 0x78, v204\n\t" \
    "s_waitcnt lgkmcnt(6)\n\t" \
    "v_mfma_f32_16x16x32_bf16 v[188:191], v[8:11], v[168:171], v[188:191]\n\t" \
    "buffer_load_dwordx2 v[200:201], v204, %[rs], %[ss] offen sc1\n\t" \
    "s_waitcnt lgkmcnt(5)\n\t" \
    "v_mfma_f32_16x16x32_bf16 v[188:191], v[12:15], v[172:175], v[188:191]\n\t" \
    "v_mov_b64 v[216:217], -1\n\t" \
    "v_mfma_f32_16x16x32_bf16 v[192:195], v[88:91], v[168:171], v[192:195]\n\t" \
    "ds_read_b128 v[168:171], %[la] offset:17408\n\t" \
    "v_mov_b64 v[218:219], -1\n\t" \
    "v_mfma_f32_16x16x32_bf16 v[192:195], v[92:95], v[172:175], v[192:195]\n\t" \
    "ds_read_b128 v[172:175], %[la] offset:18432\n\t" \
    "buffer_store_dwordx4 v[216:219], %[l16], %[rs], %[sa] offen sc1\n\t" \
    "s_waitcnt lgkmcnt(6)\n\t" \
    "v_mfma_f32_16x16x32_bf16 v[192:195], v[96:99], v[176:179], v[192:195]\n\t" \
    "s_waitcnt lgkmcnt(5)\n\t" \
    "v_mfma_f32_16x16x32_bf16 v[192:195], v[100:103], v[180:183], v[192:195]\n\t" \
    "v_mfma_f32_16x16x32_bf16 v[188:191], v[16:19], v[176:179], v[188:191]\n\t" \
    "ds_read_b128 v[176:179], %[la] offset:19456\n\t" \
    "v_mfma_f32_16x16x32_bf16 v[188:191], v[20:23], v[180:183], v[188:191]\n\t" \
    "ds_read_b128 v[180:183], %[la] offset:24576\n\t" \
    "s_waitcnt lgkmcnt(6)\n\t" \
    "v_mfma_f32_16x16x32_bf16 v[188:191], v[24:27], v[184:187], v[188:191]\n\t" \
    "s_waitcnt lgkmcnt(5)\n\t" \
    "v_mfma_f32_16x16x32_bf16 v[188:191], v[28:31], v[160:163], v[188:191]\n\t" \
    "v_mfma_f32_16x16x32_bf16 v[192:195], v[104:107], v[184:187], v[192:195]\n\t" \
    "ds_read_b128 v[184:187], %[la] offset:25600\n\t" \
    "v_mfma_f32_16x16x32_bf16 v[192:195], v[108:111], v[160:163], v[192:195]\n\t" \
    "ds_read_b128 v[160:163], %[la] offset:26624\n\t" \
    "s_waitcnt lgkmcnt(6)\n\t" \
    "v_mfma_f32_16x16x32_bf16 v[192:195], v[112:115], v[164:167], v[192:195]\n\t" \
    "s_waitcnt lgkmcnt(5)\n\t" \
    "v_mfma_f32_16x16x32_bf16 v[192:195], v[116:119], v[168:171], v[192:195]\n\t" \
    "v_mfma_f32_16x16x32_bf16 v[188:191], v[32:35], v[164:167], v[188:191]\n\t" \
    "ds_read_b128 v[164:167], %[la] offset:27648\n\t" \
    "v_mfma_f32_16x16x32_bf16 v[188:191], v[36:39], v[168:171], v[188:191]\n\t" \
    "ds_read_b128 v[168:171], %[la] offset:4096\n\t" \
    "s_waitcnt lgkmcnt(6)\n\t" \
    "v_mfma_f32_16x16x32_bf16 v[188:191], v[40:43], v[172:175], v[188:191]\n\t" \
    "s_waitcnt lgkmcnt(5)\n\t" \
    "v_mfma_f32_16x16x32_bf16 v[188:191], v[44:47], v[176:179], v[188:191]\n\t" \
    "v_mfma_f32_16x16x32_bf16 v[192:195], v[120:123], v[172:175], v[192:195]\n\t" \
    "ds_read_b128 v[172:175], %[la] offset:12288\n\t" \
    "v_mfma_f32_16x16x32_bf16 v[192:195], v[124:127], v[176:179], v[192:195]\n\t" \
    "ds_read_b128 v[176:179], %[la] offset:20480\n\t" \
    "s_waitcnt lgkmcnt(6)\n\t" \
    "v_mfma_f32_16x16x32_bf16 v[192:195], v[128:131], v[180:183], v[192:195]\n\t" \
    "s_waitcnt lgkmcnt(5)\n\t" \
    "v_mfma_f32_16x16x32_bf16 v[192:195], v[132:135], v[184:187], v[192:195]\n\t" \
    "v_mfma_f32_16x16x32_bf16 v[188:191], v[48:51], v[180:183], v[188:191]\n\t" \
    "ds_read_b128 v[180:183], %[la] offset:28672\n\t" \
    "v_mfma_f32_16x16x32_bf16 v[188:191], v[52:55], v[184:187], v[188:191]\n\t" \
    "s_waitcnt lgkmcnt(5)\n\t" \
    "v_mfma_f32_16x16x32_bf16 v[188:191], v[56:59], v[160:163], v[188:191]\n\t" \
    "s_waitcnt lgkmcnt(4)\n\t" \
    "v_mfma_f32_16x16x32_bf16 v[188:191], v[60:63], v[164:167], v[188:191]\n\t" \
    "v_mfma_f32_16x16x32_bf16 v[192:195], v[136:139], v[160:163], v[192:195]\n\t" \
    "v_mfma_f32_16x16x32_bf16 v[192:195], v[140:143], v[164:167], v[192:195]\n\t" \
    "s_waitcnt lgkmcnt(3)\n\t" \
    "v_mfma_f32_16x16x32_bf16 v[192:195], v[144:147], v[168:171], v[192:195]\n\t" \
    "s_waitcnt lgkmcnt(2)\n\t" \
    "v_mfma_f32_16x16x32_bf16 v[192:195], v[148:151], v[172:175], v[192:195]\n\t" \
    "v_mfma_f32_16x16x32_bf16 v[188:191], v[64:67], v[168:171], v[188:191]\n\t" \
    "v_mfma_f32_16x16x32_bf16 v[188:191], v[68:71], v[172:175], v[188:191]\n\t" \
    "s_waitcnt lgkmcnt(1)\n\t" \
    "v_mfma_f32_16x16x32_bf16 v[188:191], v[72:75], v[176:179], v[188:191]\n\t" \
    "s_waitcnt lgkmcnt(0)\n\t" \
    "v_mfma_f32_16x16x32_bf16 v[188:191], v[76:79], v[180:183], v[188:191]\n\t" \
    "v_mfma_f32_16x16x32_bf16 v[192:195], v[152:155], v[176:179], v[192:195]\n\t" \
    "v_mfma_f32_16x16x32_bf16 v[192:195], v[156:159], v[180:183], v[192:195]\n\t" \
    ASM_ST3 \
    "s_waitcnt vmcnt(1)\n\t" \
    "v_lshlrev_b32 v160, 16, v196\n\t" \
    "v_and_b32 v161, 0xffff0000, v196\n\t" \
    "v_lshlrev_b32 v162, 16, v197\n\t" \
    "v_and_b32 v163, 0xffff0000, v197\n\t" \
    "v_lshlrev_b32 v164, 16, v198\n\t" \
    "v_and_b32 v165, 0xffff0000, v198\n\t" \
    "v_lshlrev_b32 v166, 16, v199\n\t" \
    "v_and_b32 v167, 0xffff0000, v199\n\t" \
    "v_sub_f32 v160, v160, v200\n\t" \
    "v_sub_f32 v161, v161, v200\n\t" \
    "v_sub_f32 v162, v162, v200\n\t" \
    "v_sub_f32 v163, v163, v200\n\t" \
    "v_sub_f32 v164, v164, v200\n\t" \
    "v_sub_f32 v165, v165, v200\n\t" \
    "v_sub_f32 v166, v166, v200\n\t" \
    "v_sub_f32 v167, v167, v200\n\t" \
    "v_mul_f32 v160, v160, v201\n\t" \
    "v_mul_f32 v161, v161, v201\n\t" \
    "v_mul_f32 v162, v162, v201\n\t" \
    "v_mul_f32 v163, v163, v201\n\t" \
    "v_mul_f32 v164, v164, v201\n\t" \
    "v_mul_f32 v165, v165, v201\n\t" \
    "v_mul_f32 v166, v166, v201\n\t" \
    "v_mul_f32 v167, v167, v201\n\t" \
    "v_cvt_pk_bf16_f32 v160, v160, v161\n\t" \
    "v_cvt_pk_bf16_f32 v162, v162, v163\n\t" \
    "v_cvt_pk_bf16_f32 v164, v164, v165\n\t" \
    "v_cvt_pk_bf16_f32 v166, v166, v167\n\t" \
    "v_and_b32 v161, 0xffff0000, v160\n\t" \
    "v_lshlrev_b32 v160, 16, v160\n\t" \
    "v_and_b32 v163, 0xffff0000, v162\n\t" \
    "v_lshlrev_b32 v162, 16, v162\n\t" \
    "v_and_b32 v165, 0xffff0000, v164\n\t" \
    "v_lshlrev_b32 v164, 16, v164\n\t" \
    "v_and_b32 v167, 0xffff0000, v166\n\t" \
    "v_lshlrev_b32 v166, 16, v166\n\t" \
    "v_add_f32 v188, v188, v160\n\t" \
    "v_add_f32 v189, v189, v161\n\t" \
    "v_add_f32 v190, v190, v162\n\t" \
    "v_add_f32 v191, v191, v163\n\t" \
    "v_add_f32 v192, v192, v164\n\t" \
    "v_add_f32 v193, v193, v165\n\t" \
    "v_add_f32 v194, v194, v166\n\t" \
    "v_add_f32 v195, v195, v167\n\t" \
    "v_cvt_pk_bf16_f32 v188, v188, v189\n\t" \
    "v_cvt_pk_bf16_f32 v189, v190, v191\n\t" \
    "v_cvt_pk_bf16_f32 v190, v192, v193\n\t" \
    "v_cvt_pk_bf16_f32 v191, v194, v195\n\t" \
    "v_min_u32 v188, -2, v188\n\t" \
    "v_min_u32 v189, -2, v189\n\t" \
    "v_min_u32 v190, -2, v190\n\t" \
    "v_min_u32 v191, -2, v191\n\t" \
    "s_nop 1\n\t" \
    "v_permlane16_swap_b32 v188, v190\n\t" \
    "v_permlane16_swap_b32 v189, v191\n\t" \
    "s_waitcnt vmcnt(1)\n\t" \
    "buffer_store_dwordx4 v[188:191], %[oa], %[rs], %[so] offen sc1\n\t" \
    "s_nop 1\n\t"
#define ASM_WT3_LO \
    "s_branch .Lt0%=\n\t" \
    ".Ltr%=:\n\t" \
    "s_cmp_gt_i32 %[n], 0\n\t" \
    "s_cbranch_scc0 .Ltg%=\n\t" \
    ".Lfw%=:\n\t" \
    "s_mov_b32 %[a], 0\n\t" \
    "v_max_u32 v252, v232, v233\n\t" \
    "v_max3_u32 v252, v234, v235, v252\n\t" \
    "v_cmp_eq_u32 vcc, -1, v252\n\t" \
    "s_cbranch_vccz .Lgw0_%=\n\t" \
    "buffer_load_dwordx4 v[232:235], %[l16], %[rs], %[i0] offen offset:0 sc1\n\t" \
    "s_mov_b32 %[a], 1\n\t" \
    ".Lgw0_%=:\n\t" \
    "v_max_u32 v252, v236, v237\n\t" \
    "v_max3_u32 v252, v238, v239, v252\n\t" \
    "v_cmp_eq_u32 vcc, -1, v252\n\t" \
    "s_cbranch_vccz .Lgw1_%=\n\t" \
    "buffer_load_dwordx4 v[236:239], %[l16], %[rs], %[i0] offen offset:1024 sc1\n\t" \
    "s_mov_b32 %[a], 1\n\t" \
    ".Lgw1_%=:\n\t" \
    "v_max_u32 v252, v240, v241\n\t" \
    "v_max3_u32 v252, v242, v243, v252\n\t" \
    "v_cmp_eq_u32 vcc, -1, v252\n\t" \
    "s_cbranch_vccz .Lgw2_%=\n\t" \
    "buffer_load_dwordx4 v[240:243], %[l16], %[rs], %[i0] offen offset:2048 sc1\n\t" \
    "s_mov_b32 %[a], 1\n\t" \
    ".Lgw2_%=:\n\t" \
    "v_max_u32 v252, v244, v245\n\t" \
    "v_max3_u32 v252, v246, v247, v252\n\t" \
    "v_cmp_eq_u32 vcc, -1, v252\n\t" \
    "s_cbranch_vccz .Lgw3_%=\n\t" \
    "buffer_load_dwordx4 v[244:247], %[l16], %[rs], %[i0] offen offset:3072 sc1\n\t" \
    "s_mov_b32 %[a], 1\n\t" \
    ".Lgw3_%=:\n\t" \
    "v_max_u32 v252, v248, v249\n\t" \
    "v_max3_u32 v252, v250, v251, v252\n\t" \
    "v_cmp_eq_u32 vcc, -1, v252\n\t" \
    "s_cbranch_vccz .Lgw4_%=\n\t" \
    "buffer_load_dwordx4 v[248:251], %[l16], %[rs], %[i1] offen offset:0 sc1\n\t" \
    "s_mov_b32 %[a], 1\n\t" \
    ".Lgw4_%=:\n\t" \
    "s_cmp_eq_u32 %[a], 0\n\t" \
    "s_cbranch_scc1 .Lhw%=\n\t" \
    "s_waitcnt vmcnt(20)\n\t" \
    ".Liw%=:\n\t" \
    "v_max_u32 v252, v232, v233\n\t" \
    "v_max3_u32 v252, v234, v235, v252\n\t" \
    "v_max3_u32 v252, v236, v237, v252\n\t" \
    "v_max3_u32 v252, v238, v239, v252\n\t" \
    "v_max3_u32 v252, v240, v241, v252\n\t" \
    "v_max3_u32 v252, v242, v243, v252\n\t" \
    "v_max3_u32 v252, v244, v245, v252\n\t" \
    "v_max3_u32 v252, v246, v247, v252\n\t" \
    "v_max3_u32 v252, v248, v249, v252\n\t" \
    "v_max3_u32 v252, v250, v251, v252\n\t" \
    "v_cmp_eq_u32 vcc, -1, v252\n\t" \
    "s_cbranch_vccz .Lhw%=\n\t" \
    "s_sleep 1\n\t" \
    "v_max_u32 v252, v232, v233\n\t" \
    "v_max3_u32 v252, v234, v235, v252\n\t" \
    "v_max3_u32 v252, v236, v237, v252\n\t" \
    "v_max3_u32 v252, v238, v239, v252\n\t" \
    "v_max3_u32 v252, v240, v241, v252\n\t" \
    "v_max3_u32 v252, v242, v243, v252\n\t" \
    "v_max3_u32 v252, v244, v245, v252\n\t" \
    "v_max3_u32 v252, v246, v247, v252\n\t" \
    "v_max3_u32 v252, v248, v249, v252\n\t" \
    "v_max3_u32 v252, v250, v251, v252\n\t" \
    "v_cmp_eq_u32 vcc, -1, v252\n\t" \
    "s_cbranch_vccz .Lhw%=\n\t" \
    "s_sleep 1\n\t" \
    "v_max_u32 v252, v232, v233\n\t" \
    "v_max3_u32 v252, v234, v235, v252\n\t" \
    "v_max3_u32 v252, v236, v237, v252\n\t" \
    "v_max3_u32 v252, v238, v239, v252\n\t" \
    "v_max3_u32 v252, v240, v241, v252\n\t" \
    "v_max3_u32 v252, v242, v243, v252\n\t" \
    "v_max3_u32 v252, v244, v245, v252\n\t" \
    "v_max3_u32 v252, v246, v247, v252\n\t" \
    "v_max3_u32 v252, v248, v249, v252\n\t" \
    "v_max3_u32 v252, v250, v251, v252\n\t" \
    "v_cmp_eq_u32 vcc, -1, v252\n\t" \
    "s_cbranch_vccz .Lhw%=\n\t" \
    "s_sleep 1\n\t" \
    "v_max_u32 v252, v232, v233\n\t" \
    "v_max3_u32 v252, v234, v235, v252\n\t" \
    "v_max3_u32 v252, v236, v237, v252\n\t" \
    "v_max3_u32 v252, v238, v239, v252\n\t" \
    "v_max3_u32 v252, v240, v241, v252\n\t" \
    "v_max3_u32 v252, v242, v243, v252\n\t" \
    "v_max3_u32 v252, v244, v245, v252\n\t" \
    "v_max3_u32 v252, v246, v247, v252\n\t" \
    "v_max3_u32 v252, v248, v249, v252\n\t" \
    "v_max3_u32 v252, v250, v251, v252\n\t" \
    "v_cmp_eq_u32 vcc, -1, v252\n\t" \
    "s_cbranch_vccz .Lhw%=\n\t" \
    "s_sleep 1\n\t" \
    "v_max_u32 v252, v232, v233\n\t" \
    "v_max3_u32 v252, v234, v235, v252\n\t" \
    "v_max3_u32 v252, v236, v237, v252\n\t" \
    "v_max3_u32 v252, v238, v239, v252\n\t" \
    "v_max3_u32 v252, v240, v241, v252\n\t" \
    "v_max3_u32 v252, v242, v243, v252\n\t" \
    "v_max3_u32 v252, v244, v245, v252\n\t" \
    "v_max3_u32 v252, v246, v247, v252\n\t" \
    "v_max3_u32 v252, v248, v249, v252\n\t" \
    "v_max3_u32 v252, v250, v251, v252\n\t" \
    "v_cmp_eq_u32 vcc, -1, v252\n\t" \
    "s_cbranch_vccz .Lhw%=\n\t" \
    "s_sleep 1\n\t" \
    "v_max_u32 v252, v232, v233\n\t" \
    "v_max3_u32 v252, v234, v235, v252\n\t" \
    "v_max3_u32 v252, v236, v237, v252\n\t" \
    "v_max3_u32 v252, v238, v239, v252\n\t" \
    "v_max3_u32 v252, v240, v241, v252\n\t" \
    "v_max3_u32 v252, v242, v243, v252\n\t" \
    "v_max3_u32 v252, v244, v245, v252\n\t" \
    "v_max3_u32 v252, v246, v247, v252\n\t" \
    "v_max3_u32 v252, v248, v249, v252\n\t" \
    "v_max3_u32 v252, v250, v251, v252\n\t" \
    "v_cmp_eq_u32 vcc, -1, v252\n\t" \
    "s_cbranch_vccz .Lhw%=\n\t" \
    "s_sleep 1\n\t" \
    "s_sub_u32 %[n], %[n], 1\n\t" \
    "s_cmp_gt_i32 %[n], 0\n\t" \
    "s_cbranch_scc1 .Lfw%=\n\t" \
    ".Lhw%=:\n\t" \
    "s_branch .Lta%=\n\t" \
    ".Lt0%=:\n\t" \
    "s_waitcnt vmcnt(1)\n\t" \
    ".Lta%=:\n\t" \
    "ds_write_b128 %[lw], v[232:235] offset:0\n\t" \
    "v_max_u32 v252, v232, v233\n\t" \
    "v_max3_u32 v252, v234, v235, v252\n\t" \
    "v_mfma_f32_16x16x32_bf16 v[188:191], v[232:235], v[232:235], 0\n\t" \
    "ds_write_b128 %[lw], v[236:239] offset:1024\n\t" \
    "v_max3_u32 v252, v236, v237, v252\n\t" \
    "v_max3_u32 v252, v238, v239, v252\n\t" \
    "v_mfma_f32_16x16x32_bf16 v[188:191], v[236:239], v[236:239], v[188:191]\n\t" \
    "ds_write_b128 %[lw], v[240:243] offset:2048\n\t" \
    "v_max3_u32 v252, v240, v241, v252\n\t" \
    "v_max3_u32 v252, v242, v243, v252\n\t" \
    "v_mfma_f32_16x16x32_bf16 v[188:191], v[240:243], v[240:243], v[188:191]\n\t" \
    "ds_write_b128 %[lw], v[244:247] offset:3072\n\t" \
    "v_max3_u32 v252, v244, v245, v252\n\t" \
    "v_max3_u32 v252, v246, v247, v252\n\t" \
    "v_mfma_f32_16x16x32_bf16 v[188:191], v[244:247], v[244:247], v[188:191]\n\t" \
    "ds_write_b128 %[lw], v[248:251] offset:4096\n\t" \
    "v_max3_u32 v252, v248, v249, v252\n\t" \
    "v_max3_u32 v252, v250, v251, v252\n\t" \
    "v_mfma_f32_16x16x32_bf16 v[188:191], v[248:251], v[248:251], v[188:191]\n\t" \
    "v_mfma_f32_16x16x32_bf16 v[192:195], v[216:219], v[232:235], 0\n\t" \
    "v_mfma_f32_16x16x32_bf16 v[192:195], v[216:219], v[236:239], v[192:195]\n\t" \
    "v_mfma_f32_16x16x32_bf16 v[192:195], v[216:219], v[240:243], v[192:195]\n\t" \
    "v_mfma_f32_16x16x32_bf16 v[192:195], v[216:219], v[244:247], v[192:195]\n\t" \
    "v_mfma_f32_16x16x32_bf16 v[192:195], v[216:219], v[248:251], v[192:195]\n\t" \
    "v_cmp_eq_u32 vcc, -1, v252\n\t" \
    "s_cbranch_vccnz .Ltr%=\n\t" \
    ".Ltg%=:\n\t" \
    ASM_ST2 \
    "s_nop 5\n\t" \
    "ds_write_b128 %[lw], v[188:191] offset:5120\n\t" \
    "ds_write_b128 %[lw], v[192:195] offset:6144\n\t" \
    "s_waitcnt lgkmcnt(0)\n\t" \
    "s_setprio 2\n\t" \
    "s_barrier\n\t" ASM_ST \
    "ds_read_b128 v[160:163], %[la] offset:0\n\t" \
    "ds_read_b128 v[164:167], %[la] offset:1024\n\t" \
    "ds_read_b128 v[168:171], %[la] offset:2048\n\t" \
    "ds_read2st64_b32 v[204:205], %[sr] offset0:20 offset1:24\n\t" \
    "ds_read_b128 v[172:175], %[la] offset:3072\n\t" \
    "ds_read_b128 v[176:179], %[la] offset:8192\n\t" \
    "ds_read_b128 v[180:183], %[la] offset:9216\n\t" \
    "ds_read_b128 v[184:187], %[la] offset:10240\n\t" \
    "s_waitcnt lgkmcnt(7)\n\t" \
    "v_mfma_f32_16x16x32_bf16 v[192:195], v[80:83], v[160:163], 0\n\t" \
    "s_waitcnt lgkmcnt(6)\n\t" \
    "v_mfma_f32_16x16x32_bf16 v[192:195], v[84:87], v[164:167], v[192:195]\n\t" \
    "v_mfma_f32_16x16x32_bf16 v[188:191], v[0:3], v[160:163], 0\n\t" \
    "ds_read_b128 v[160:163], %[la] offset:11264\n\t" \
    "v_mfma_f32_16x16x32_bf16 v[188:191], v[4:7], v[164:167], v[188:191]\n\t" \
    "ds_read_b128 v[164:167], %[la] offset:16384\n\t" \
    "s_waitcnt lgkmcnt(7)\n\t" \
    "v_mfma_f32_16x16x32_bf16 v[188:191], v[8:11], v[168:171], v[188:191]\n\t" \
    "s_waitcnt lgkmcnt(6)\n\t" \
    "s_waitcnt lgkmcnt(5)\n\t" \
    "v_mfma_f32_16x16x32_bf16 v[188:191], v[12:15], v[172:175], v[188:191]\n\t" \
    "v_permlane16_swap_b32 v204, v205\n\t" \
    "v_mfma_f32_16x16x32_bf16 v[192:195], v[88:91], v[168:171], v[192:195]\n\t" \
    "ds_read_b128 v[168:171], %[la] offset:17408\n\t" \
    "v_mfma_f32_16x16x32_bf16 v[192:195], v[92:95], v[172:175], v[192:195]\n\t" \
    "ds_read_b128 v[172:175], %[la] offset:18432\n\t" \
    "v_add_f32 v206, v204, v205\n\t" \
    "s_waitcnt lgkmcnt(6)\n\t" \
    "v_mfma_f32_16x16x32_bf16 v[192:195], v[96:99], v[176:179], v[192:195]\n\t" \
    "v_add_f32 v207, v204, v205\n\t" \
    "s_waitcnt lgkmcnt(5)\n\t" \
    "v_mfma_f32_16x16x32_bf16 v[192:195], v[100:103], v[180:183], v[192:195]\n\t" \
    "v_mfma_f32_16x16x32_bf16 v[188:191], v[16:19], v[176:179], v[188:191]\n\t" \
    "ds_read_b128 v[176:179], %[la] offset:19456\n\t" \
    "v_permlane32_swap_b32 v206, v207\n\t" \
    "v_mfma_f32_16x16x32_bf16 v[188:191], v[20:23], v[180:183], v[188:191]\n\t" \
    "ds_read_b128 v[180:183], %[la] offset:24576\n\t" \
    "s_waitcnt lgkmcnt(6)\n\t" \
    "v_mfma_f32_16x16x32_bf16 v[188:191], v[24:27], v[184:187], v[188:191]\n\t" \
    "v_add_f32 v204, v206, v207\n\t" \
    "s_waitcnt lgkmcnt(5)\n\t" \
    "v_mfma_f32_16x16x32_bf16 v[188:191], v[28:31], v[160:163], v[188:191]\n\t" \
    "v_add_f32 v205, v206, v207\n\t" \
    "v_mfma_f32_16x16x32_bf16 v[192:195], v[104:107], v[184:187], v[192:195]\n\t" \
    "ds_read_b128 v[184:187], %[la] offset:25600\n\t" \
    "v_mfma_f32_16x16x32_bf16 v[192:195], v[108:111], v[160:163], v[192:195]\n\t" \
    "ds_read_b128 v[160:163], %[la] offset:26624\n\t" \
    "v_permlane16_swap_b32 v204, v205\n\t" \
    "s_waitcnt lgkmcnt(6)\n\t" \
    "v_mfma_f32_16x16x32_bf16 v[192:195], v[112:115], v[164:167], v[192:195]\n\t" \
    "s_waitcnt lgkmcnt(5)\n\t" \
    "v_mfma_f32_16x16x32_bf16 v[192:195], v[116:119], v[168:171], v[192:195]\n\t" \
    "v_mul_f32 v205, %[kd], v205\n\t" \
    "v_mfma_f32_16x16x32_bf16 v[188:191], v[32:35], v[164:167], v[188:191]\n\t" \
    "ds_read_b128 v[164:167], %[la] offset:27648\n\t" \
    "v_mul_f32 v204, %[kd], v204\n\t" \
    "v_mfma_f32_16x16x32_bf16 v[188:191], v[36:39], v[168:171], v[188:191]\n\t" \
    "ds_read_b128 v[168:171], %[la] offset:4096\n\t" \
    "v_fma_f32 v204, -v205, v205, v204\n\t" \
    "s_waitcnt lgkmcnt(6)\n\t" \
    "v_mfma_f32_16x16x32_bf16 v[188:191], v[40:43], v[172:175], v[188:191]\n\t" \
    "v_max_f32 v204, 0, v204\n\t" \
    "s_waitcnt lgkmcnt(5)\n\t" \
    "v_mfma_f32_16x16x32_bf16 v[188:191], v[44:47], v[176:179], v[188:191]\n\t" \
    "v_add_f32 v206, %[ke], v204\n\t" \
    "v_mfma_f32_16x16x32_bf16 v[192:195], v[120:123], v[172:175], v[192:195]\n\t" \
    "ds_read_b128 v[172:175], %[la] offset:12288\n\t" \
    "v_rsq_f32 v206, v206\n\t" \
    "v_mfma_f32_16x16x32_bf16 v[192:195], v[124:127], v[176:179], v[192:195]\n\t" \
    "ds_read_b128 v[176:179], %[la] offset:20480\n\t" \
    "s_waitcnt lgkmcnt(6)\n\t" \
    "v_mfma_f32_16x16x32_bf16 v[192:195], v[128:131], v[180:183], v[192:195]\n\t" \
    "v_mul_f32 v207, -v205, v206\n\t" \
    "s_waitcnt lgkmcnt(5)\n\t" \
    "v_mfma_f32_16x16x32_bf16 v[192:195], v[132:135], v[184:187], v[192:195]\n\t" \
    "v_mul_f32 v208, v196, v207\n\t" \
    "v_mfma_f32_16x16x32_bf16 v[188:191], v[48:51], v[180:183], v[188:191]\n\t" \
    "ds_read_b128 v[180:183], %[la] offset:28672\n\t" \
    "v_mul_f32 v209, v197, v207\n\t" \
    "v_mul_f32 v210, v198, v207\n\t" \
    "v_mfma_f32_16x16x32_bf16 v[188:191], v[52:55], v[184:187], v[188:191]\n\t" \
    "v_mul_f32 v211, v199, v207\n\t" \
    "v_mul_f32 v212, v200, v207\n\t" \
    "s_waitcnt lgkmcnt(5)\n\t" \
    "v_mfma_f32_16x16x32_bf16 v[188:191], v[56:59], v[160:163], v[188:191]\n\t" \
    "v_mul_f32 v213, v201, v207\n\t" \
    "v_mul_f32 v214, v202, v207\n\t" \
    "s_waitcnt lgkmcnt(4)\n\t" \
    "v_mfma_f32_16x16x32_bf16 v[188:191], v[60:63], v[164:167], v[188:191]\n\t" \
    "v_mul_f32 v215, v203, v207\n\t" \
    "v_mov_b64 v[216:217], -1\n\t" \
    "v_mfma_f32_16x16x32_bf16 v[192:195], v[136:139], v[160:163], v[192:195]\n\t" \
    "v_mov_b64 v[218:219], -1\n\t" \
    "buffer_store_dwordx4 v[216:219], %[l16], %[rs], %[sa] offen sc1\n\t" \
    "v_mfma_f32_16x16x32_bf16 v[192:195], v[140:143], v[164:167], v[192:195]\n\t" \
    "s_waitcnt lgkmcnt(3)\n\t" \
    "v_mfma_f32_16x16x32_bf16 v[192:195], v[144:147], v[168:171], v[192:195]\n\t" \
    "s_waitcnt lgkmcnt(2)\n\t" \
    "v_mfma_f32_16x16x32_bf16 v[192:195], v[148:151], v[172:175], v[192:195]\n\t" \
    "v_mfma_f32_16x16x32_bf16 v[188:191], v[64:67], v[168:171], v[188:191]\n\t" \
    "v_mfma_f32_16x16x32_bf16 v[188:191], v[68:71], v[172:175], v[188:191]\n\t" \
    "s_waitcnt lgkmcnt(1)\n\t" \
    "v_mfma_f32_16x16x32_bf16 v[188:191], v[72:75], v[176:179], v[188:191]\n\t" \
    "s_waitcnt lgkmcnt(0)\n\t" \
    "v_mfma_f32_16x16x32_bf16 v[188:191], v[76:79], v[180:183], v[188:191]\n\t" \
    "v_mfma_f32_16x16x32_bf16 v[192:195], v[152:155], v[176:179], v[192:195]\n\t" \
    "v_mfma_f32_16x16x32_bf16 v[192:195], v[156:159], v[180:183], v[192:195]\n\t" \
    "s_setprio 2\n\t" \
    "buffer_load_dwordx4 v[232:235], %[l16], %[rs], %[n0] offen offset:0 sc1\n\t" \
    "buffer_load_dwordx4 v[236:239], %[l16], %[rs], %[n0] offen offset:1024 sc1\n\t" \
    "buffer_load_dwordx4 v[240:243], %[l16], %[rs], %[n0] offen offset:2048 sc1\n\t" \
    "buffer_load_dwordx4 v[244:247], %[l16], %[rs], %[n0] offen offset:3072 sc1\n\t" \
    "buffer_load_dwordx4 v[248:251], %[l16], %[rs], %[n1] offen offset:0 sc1\n\t" \
    ASM_ST3 \
    "v_fma_f32 v188, v188, v206, v208\n\t" \
    "v_fma_f32 v189, v189, v206, v209\n\t" \
    "v_fma_f32 v190, v190, v206, v210\n\t" \
    "v_fma_f32 v191, v191, v206, v211\n\t" \
    "v_mul_f32 v160, v188, v188\n\t" \
    "v_mul_f32 v161, v189, v189\n\t" \
    "v_mul_f32 v162, v190, v190\n\t" \
    "v_mul_f32 v163, v191, v191\n\t" \
    "v_fma_f32 v160, v160, %[k1], v220\n\t" \
    "v_fma_f32 v161, v161, %[k1], v220\n\t" \
    "v_fma_f32 v162, v162, %[k1], v220\n\t" \
    "v_fma_f32 v163, v163, %[k1], v220\n\t" \
    "v_mul_f32 v160, v188, v160\n\t" \
    "v_mul_f32 v161, v189, v161\n\t" \
    "v_mul_f32 v162, v190, v162\n\t" \
    "v_mul_f32 v163, v191, v163\n\t" \
    "v_fma_f32 v192, v192, v206, v212\n\t" \
    "v_fma_f32 v193, v193, v206, v213\n\t" \
    "v_fma_f32 v194, v194, v206, v214\n\t" \
    "v_fma_f32 v195, v195, v206, v215\n\t" \
    "v_mul_f32 v164, v192, v192\n\t" \
    "v_mul_f32 v165, v193, v193\n\t" \
    "v_mul_f32 v166, v194, v194\n\t" \
    "v_mul_f32 v167, v195, v195\n\t" \
    "v_fma_f32 v164, v164, %[k1], v220\n\t" \
    "v_fma_f32 v165, v165, %[k1], v220\n\t" \
    "v_fma_f32 v166, v166, %[k1], v220\n\t" \
    "v_fma_f32 v167, v167, %[k1], v220\n\t" \
    "v_mul_f32 v164, v192, v164\n\t" \
    "v_mul_f32 v165, v193, v165\n\t" \
    "v_mul_f32 v166, v194, v166\n\t" \
    "v_mul_f32 v167, v195, v167\n\t" \
    "v_exp_f32 v160, v160\n\t" \
    "v_exp_f32 v161, v161\n\t" \
    "v_exp_f32 v162, v162\n\t" \
    "v_exp_f32 v163, v163\n\t" \
    "v_exp_f32 v164, v164\n\t" \
    "v_exp_f32 v165, v165\n\t" \
    "v_exp_f32 v166, v166\n\t" \
    "v_exp_f32 v167, v167\n\t" \
    "v_add_f32 v160, 1.0, v160\n\t" \
    "v_add_f32 v161, 1.0, v161\n\t" \
    "v_add_f32 v162, 1.0, v162\n\t" \
    "v_add_f32 v163, 1.0, v163\n\t" \
    "v_add_f32 v164, 1.0, v164\n\t" \
    "v_add_f32 v165, 1.0, v165\n\t" \
    "v_add_f32 v166, 1.0, v166\n\t" \
    "v_add_f32 v167, 1.0, v167\n\t" \
    "v_rcp_f32 v160, v160\n\t" \
    "v_rcp_f32 v161, v161\n\t" \
    "v_rcp_f32 v162, v162\n\t" \
    "v_rcp_f32 v163, v163\n\t" \
    "v_rcp_f32 v164, v164\n\t" \
    "v_rcp_f32 v165, v165\n\t" \
    "v_rcp_f32 v166, v166\n\t" \
    "v_rcp_f32 v167, v167\n\t" \
    "v_mul_f32 v188, v188, v160\n\t" \
    "v_mul_f32 v189, v189, v161\n\t" \
    "v_mul_f32 v190, v190, v162\n\t" \
    "v_mul_f32 v191, v191, v163\n\t" \
    "v_mul_f32 v192, v192, v164\n\t" \
    "v_mul_f32 v193, v193, v165\n\t" \
    "v_mul_f32 v194, v194, v166\n\t" \
    "v_mul_f32 v195, v195, v167\n\t" \
    "v_cvt_pk_bf16_f32 v188, v188, v189\n\t" \
    "v_cvt_pk_bf16_f32 v189, v190, v191\n\t" \
    "v_cvt_pk_bf16_f32 v190, v192, v193\n\t" \
    "v_cvt_pk_bf16_f32 v191, v194, v195\n\t" \
    "v_min_u32 v188, -2, v188\n\t" \
    "v_min_u32 v189, -2, v189\n\t" \
    "v_min_u32 v190, -2, v190\n\t" \
    "v_min_u32 v191, -2, v191\n\t" \
    "v_mov_b32 v216, 0x3f803f80\n\t" \
    "v_mov_b32 v217, 0x3f803f80\n\t" \
    "v_mov_b32 v218, 0x3f803f80\n\t" \
    "v_mov_b32 v219, 0x3f803f80\n\t" \
    "v_permlane16_swap_b32 v188, v190\n\t" \
    "v_permlane16_swap_b32 v189, v191\n\t" \
    "s_waitcnt vmcnt(6)\n\t" \
    "buffer_store_dwordx4 v[188:191], %[oa], %[rs], %[so] offen sc1\n\t" \
    "s_nop 1\n\t" \
    "s_setprio 0\n\t"
#define ASM_WT3_HI \
    "s_setprio 1\n\t" \
    "s_barrier\n\t" ASM_ST \
    "ds_read_b128 v[160:163], %[la] offset:0\n\t" \
    "ds_read_b128 v[164:167], %[la] offset:1024\n\t" \
    "ds_read_b128 v[168:171], %[la] offset:2048\n\t" \
    "ds_read2st64_b32 v[204:205], %[sr] offset0:20 offset1:24\n\t" \
    "ds_read_b128 v[172:175], %[la] offset:3072\n\t" \
    "ds_read_b128 v[176:179], %[la] offset:8192\n\t" \
    "ds_read_b128 v[180:183], %[la] offset:9216\n\t" \
    "ds_read_b128 v[184:187], %[la] offset:10240\n\t" \
    "s_waitcnt lgkmcnt(7)\n\t" \
    "v_mfma_f32_16x16x32_bf16 v[192:195], v[80:83], v[160:163], 0\n\t" \
    "s_waitcnt lgkmcnt(6)\n\t" \
    "v_mfma_f32_16x16x32_bf16 v[192:195], v[84:87], v[164:167], v[192:195]\n\t" \
    "v_mfma_f32_16x16x32_bf16 v[188:191], v[0:3], v[160:163], 0\n\t" \
    "ds_read_b128 v[160:163], %[la] offset:11264\n\t" \
    "v_mfma_f32_16x16x32_bf16 v[188:191], v[4:7], v[164:167], v[188:191]\n\t" \
    "ds_read_b128 v[164:167], %[la] offset:16384\n\t" \
    "s_waitcnt lgkmcnt(7)\n\t" \
    "v_mfma_f32_16x16x32_bf16 v[188:191], v[8:11], v[168:171], v[188:191]\n\t" \
    "s_waitcnt lgkmcnt(6)\n\t" \
    "s_waitcnt lgkmcnt(5)\n\t" \
    "v_mfma_f32_16x16x32_bf16 v[188:191], v[12:15], v[172:175], v[188:191]\n\t" \
    "v_permlane16_swap_b32 v204, v205\n\t" \
    "v_mfma_f32_16x16x32_bf16 v[192:195], v[88:91], v[168:171], v[192:195]\n\t" \
    "ds_read_b128 v[168:171], %[la] offset:17408\n\t" \
    "v_mfma_f32_16x16x32_bf16 v[192:195], v[92:95], v[172:175], v[192:195]\n\t" \
    "ds_read_b128 v[172:175], %[la] offset:18432\n\t" \
    "v_add_f32 v206, v204, v205\n\t" \
    "s_waitcnt lgkmcnt(6)\n\t" \
    "v_mfma_f32_16x16x32_bf16 v[192:195], v[96:99], v[176:179], v[192:195]\n\t" \
    "v_add_f32 v207, v204, v205\n\t" \
    "s_waitcnt lgkmcnt(5)\n\t" \
    "v_mfma_f32_16x16x32_bf16 v[192:195], v[100:103], v[180:183], v[192:195]\n\t" \
    "v_mfma_f32_16x16x32_bf16 v[188:191], v[16:19], v[176:179], v[188:191]\n\t" \
    "ds_read_b128 v[176:179], %[la] offset:19456\n\t" \
    "v_permlane32_swap_b32 v206, v207\n\t" \
    "v_mfma_f32_16x16x32_bf16 v[188:191], v[20:23], v[180:183], v[188:191]\n\t" \
    "ds_read_b128 v[180:183], %[la] offset:24576\n\t" \
    "s_waitcnt lgkmcnt(6)\n\t" \
    "v_mfma_f32_16x16x32_bf16 v[188:191], v[24:27], v[184:187], v[188:191]\n\t" \
    "v_add_f32 v204, v206, v207\n\t" \
    "s_waitcnt lgkmcnt(5)\n\t" \
    "v_mfma_f32_16x16x32_bf16 v[188:191], v[28:31], v[160:163], v[188:191]\n\t" \
    "v_add_f32 v205, v206, v207\n\t" \
    "v_mfma_f32_16x16x32_bf16 v[192:195], v[104:107], v[184:187], v[192:195]\n\t" \
    "ds_read_b128 v[184:187], %[la] offset:25600\n\t" \
    "v_mfma_f32_16x16x32_bf16 v[192:195], v[108:111], v[160:163], v[192:195]\n\t" \
    "ds_read_b128 v[160:163], %[la] offset:26624\n\t" \
    "v_permlane16_swap_b32 v204, v205\n\t" \
    "s_waitcnt lgkmcnt(6)\n\t" \
    "v_mfma_f32_16x16x32_bf16 v[192:195], v[112:115], v[164:167], v[192:195]\n\t" \
    "s_waitcnt lgkmcnt(5)\n\t" \
    "v_mfma_f32_16x16x32_bf16 v[192:195], v[116:119], v[168:171], v[192:195]\n\t" \
    "v_mul_f32 v205, %[kd], v205\n\t" \
    "v_mfma_f32_16x16x32_bf16 v[188:191], v[32:35], v[164:167], v[188:191]\n\t" \
    "ds_read_b128 v[164:167], %[la] offset:27648\n\t" \
    "v_mul_f32 v204, %[kd], v204\n\t" \
    "v_mfma_f32_16x16x32_bf16 v[188:191], v[36:39], v[168:171], v[188:191]\n\t" \
    "ds_read_b128 v[168:171], %[la] offset:4096\n\t" \
    "v_fma_f32 v204, -v205, v205, v204\n\t" \
    "s_waitcnt lgkmcnt(6)\n\t" \
    "v_mfma_f32_16x16x32_bf16 v[188:191], v[40:43], v[172:175], v[188:191]\n\t" \
    "v_max_f32 v204, 0, v204\n\t" \
    "s_waitcnt lgkmcnt(5)\n\t" \
    "v_mfma_f32_16x16x32_bf16 v[188:191], v[44:47], v[176:179], v[188:191]\n\t" \
    "v_add_f32 v206, %[ke], v204\n\t" \
    "v_mfma_f32_16x16x32_bf16 v[192:195], v[120:123], v[172:175], v[192:195]\n\t" \
    "ds_read_b128 v[172:175], %[la] offset:12288\n\t" \
    "v_rsq_f32 v206, v206\n\t" \
    "v_mfma_f32_16x16x32_bf16 v[192:195], v[124:127], v[176:179], v[192:195]\n\t" \
    "ds_read_b128 v[176:179], %[la] offset:20480\n\t" \
    "s_waitcnt lgkmcnt(6)\n\t" \
    "v_mfma_f32_16x16x32_bf16 v[192:195], v[128:131], v[180:183], v[192:195]\n\t" \
    "v_mul_f32 v207, -v205, v206\n\t" \
    "s_waitcnt lgkmcnt(5)\n\t" \
    "v_mfma_f32_16x16x32_bf16 v[192:195], v[132:135], v[184:187], v[192:195]\n\t" \
    "v_mul_f32 v208, v196, v207\n\t" \
    "v_mfma_f32_16x16x32_bf16 v[188:191], v[48:51], v[180:183], v[188:191]\n\t" \
    "ds_read_b128 v[180:183], %[la] offset:28672\n\t" \
    "v_mul_f32 v209, v197, v207\n\t" \
    "v_mul_f32 v210, v198, v207\n\t" \
    "v_mfma_f32_16x16x32_bf16 v[188:191], v[52:55], v[184:187], v[188:191]\n\t" \
    "v_mul_f32 v211, v199, v207\n\t" \
    "v_mul_f32 v212, v200, v207\n\t" \
    "s_waitcnt lgkmcnt(5)\n\t" \
    "v_mfma_f32_16x16x32_bf16 v[188:191], v[56:59], v[160:163], v[188:191]\n\t" \
    "v_mul_f32 v213, v201, v207\n\t" \
    "v_mul_f32 v214, v202, v207\n\t" \
    "s_waitcnt lgkmcnt(4)\n\t" \
    "v_mfma_f32_16x16x32_bf16 v[188:191], v[60:63], v[164:167], v[188:191]\n\t" \
    "v_mul_f32 v215, v203, v207\n\t" \
    "v_mov_b64 v[216:217], -1\n\t" \
    "v_mfma_f32_16x16x32_bf16 v[192:195], v[136:139], v[160:163], v[192:195]\n\t" \
    "v_mov_b64 v[218:219], -1\n\t" \
    "buffer_store_dwordx4 v[216:219], %[l16], %[rs], %[sa] offen sc1\n\t" \
    "v_mfma_f32_16x16x32_bf16 v[192:195], v[140:143], v[164:167], v[192:195]\n\t" \
    "s_waitcnt lgkmcnt(3)\n\t" \
    "v_mfma_f32_16x16x32_bf16 v[192:195], v[144:147], v[168:171], v[192:195]\n\t" \
    "s_waitcnt lgkmcnt(2)\n\t" \
    "v_mfma_f32_16x16x32_bf16 v[192:195], v[148:151], v[172:175], v[192:195]\n\t" \
    "v_mfma_f32_16x16x32_bf16 v[188:191], v[64:67], v[168:171], v[188:191]\n\t" \
    "v_mfma_f32_16x16x32_bf16 v[188:191], v[68:71], v[172:175], v[188:191]\n\t" \
    "s_waitcnt lgkmcnt(1)\n\t" \
    "v_mfma_f32_16x16x32_bf16 v[188:191], v[72:75], v[176:179], v[188:191]\n\t" \
    "s_waitcnt lgkmcnt(0)\n\t" \
    "v_mfma_f32_16x16x32_bf16 v[188:191], v[76:79], v[180:183], v[188:191]\n\t" \
    "v_mfma_f32_16x16x32_bf16 v[192:195], v[152:155], v[176:179], v[192:195]\n\t" \
    "v_mfma_f32_16x16x32_bf16 v[192:195], v[156:159], v[180:183], v[192:195]\n\t" \
    ASM_ST3 \
    "s_nop 1\n\t" \
    "v_fma_f32 v188, v188, v206, v208\n\t" \
    "v_fma_f32 v189, v189, v206, v209\n\t" \
    "v_fma_f32 v190, v190, v206, v210\n\t" \
    "v_fma_f32 v191, v191, v206, v211\n\t" \
    "v_mul_f32 v160, v188, v188\n\t" \
    "v_mul_f32 v161, v189, v189\n\t" \
    "v_mul_f32 v162, v190, v190\n\t" \
    "v_mul_f32 v163, v191, v191\n\t" \
    "v_fma_f32 v160, v160, %[k1], v220\n\t" \
    "v_fma_f32 v161, v161, %[k1], v220\n\t" \
    "v_fma_f32 v162, v162, %[k1], v220\n\t" \
    "v_fma_f32 v163, v163, %[k1], v220\n\t" \
    "v_mul_f32 v160, v188, v160\n\t" \
    "v_mul_f32 v161, v189, v161\n\t" \
    "v_mul_f32 v162, v190, v162\n\t" \
    "v_mul_f32 v163, v191, v163\n\t" \
    "v_fma_f32 v192, v192, v206, v212\n\t" \
    "v_fma_f32 v193, v193, v206, v213\n\t" \
    "v_fma_f32 v194, v194, v206, v214\n\t" \
    "v_fma_f32 v195, v195, v206, v215\n\t" \
    "v_mul_f32 v164, v192, v192\n\t" \
    "v_mul_f32 v165, v193, v193\n\t" \
    "v_mul_f32 v166, v194, v194\n\t" \
    "v_mul_f32 v167, v195, v195\n\t" \
    "v_fma_f32 v164, v164, %[k1], v220\n\t" \
    "v_fma_f32 v165, v165, %[k1], v220\n\t" \
    "v_fma_f32 v166, v166, %[k1], v220\n\t" \
    "v_fma_f32 v167, v167, %[k1], v220\n\t" \
    "v_mul_f32 v164, v192, v164\n\t" \
    "v_mul_f32 v165, v193, v165\n\t" \
    "v_mul_f32 v166, v194, v166\n\t" \
    "v_mul_f32 v167, v195, v167\n\t" \
    "v_exp_f32 v160, v160\n\t" \
    "v_exp_f32 v161, v161\n\t" \
    "v_exp_f32 v162, v162\n\t" \
    "v_exp_f32 v163, v163\n\t" \
    "v_exp_f32 v164, v164\n\t" \
    "v_exp_f32 v165, v165\n\t" \
    "v_exp_f32 v166, v166\n\t" \
    "v_exp_f32 v167, v167\n\t" \
    "v_add_f32 v160, 1.0, v160\n\t" \
    "v_add_f32 v161, 1.0, v161\n\t" \
    "v_add_f32 v162, 1.0, v162\n\t" \
    "v_add_f32 v163, 1.0, v163\n\t" \
    "v_add_f32 v164, 1.0, v164\n\t" \
    "v_add_f32 v165, 1.0, v165\n\t" \
    "v_add_f32 v166, 1.0, v166\n\t" \
    "v_add_f32 v167, 1.0, v167\n\t" \
    "v_rcp_f32 v160, v160\n\t" \
    "v_rcp_f32 v161, v161\n\t" \
    "v_rcp_f32 v162, v162\n\t" \
    "v_rcp_f32 v163, v163\n\t" \
    "v_rcp_f32 v164, v164\n\t" \
    "v_rcp_f32 v165, v165\n\t" \
    "v_rcp_f32 v166, v166\n\t" \
    "v_rcp_f32 v167, v167\n\t" \
    "v_mul_f32 v188, v188, v160\n\t" \
    "v_mul_f32 v189, v189, v161\n\t" \
    "v_mul_f32 v190, v190, v162\n\t" \
    "v_mul_f32 v191, v191, v163\n\t" \
    "v_mul_f32 v192, v192, v164\n\t" \
    "v_mul_f32 v193, v193, v165\n\t" \
    "v_mul_f32 v194, v194, v166\n\t" \
    "v_mul_f32 v195, v195, v167\n\t" \
    "v_cvt_pk_bf16_f32 v188, v188, v189\n\t" \
    "v_cvt_pk_bf16_f32 v189, v190, v191\n\t" \
    "v_cvt_pk_bf16_f32 v190, v192, v193\n\t" \
    "v_cvt_pk_bf16_f32 v191, v194, v195\n\t" \
    "v_min_u32 v188, -2, v188\n\t" \
    "v_min_u32 v189, -2, v189\n\t" \
    "v_min_u32 v190, -2, v190\n\t" \
    "v_min_u32 v191, -2, v191\n\t" \
    "s_nop 1\n\t" \
    "v_permlane16_swap_b32 v188, v190\n\t" \
    "v_permlane16_swap_b32 v189, v191\n\t" \
    "s_waitcnt vmcnt(1)\n\t" \
    "buffer_store_dwordx4 v[188:191], %[oa], %[rs], %[so] offen sc1\n\t" \
    "s_nop 1\n\t"
#define ASM_POLL5_I \
    "buffer_load_dwordx4 v[232:235], %[l16], %[rs], %[s0] offen offset:0 sc1\n\t" \
    "buffer_load_dwordx4 v[236:239], %[l16], %[rs], %[s0] offen offset:1024 sc1\n\t" \
    "buffer_load_dwordx4 v[240:243], %[l16], %[rs], %[s0] offen offset:2048 sc1\n\t" \
    "buffer_load_dwordx4 v[244:247], %[l16], %[rs], %[s0] offen offset:3072 sc1\n\t" \
    "buffer_load_dwordx4 v[248:251], %[l16], %[rs], %[s1] offen offset:0 sc1\n\t"
using u32x8 = __attribute__((ext_vector_type(8))) unsigned;
using u32x16 = __attribute__((ext_vector_type(16))) unsigned;
using u32x32 = __attribute__((ext_vector_type(32))) unsigned;
struct WRegs { u32x32 w[6]; u32x8 t; };
#define WREGS(W) "{v[0:31]}"(W.w[0]), "{v[32:63]}"(W.w[1]), "{v[64:95]}"(W.w[2]), "{v[96:127]}"(W.w[3]), "{v[128:159]}"(W.w[4]), "{v[160:191]}"(W.w[5]), "{v[192:199]}"(W.t)
__device__ __forceinline__ u32x32 cat32(u32x8 a, u32x8 b, u32x8 c, u32x8 d) {
    const u32x16 lo = __builtin_shufflevector(a, b, 0, 1, 2, 3, 4, 5, 6, 7, 8, 9, 10, 11, 12, 13, 14, 15);
    const u32x16 hi = __builtin_shufflevector(c, d, 0, 1, 2, 3, 4, 5, 6, 7, 8, 9, 10, 11, 12, 13, 14, 15);
    return __builtin_shufflevector(lo, hi, 0, 1, 2, 3, 4, 5, 6, 7, 8, 9, 10, 11, 12, 13, 14, 15, 16, 17, 18, 19, 20, 21, 22, 23, 24, 25, 26, 27, 28, 29, 30, 31);
}
#define WOUT(W) "=&{v[0:31]}"(W.w[0]), "=&{v[32:63]}"(W.w[1]), "=&{v[64:95]}"(W.w[2]), "=&{v[96:127]}"(W.w[3]), "=&{v[128:159]}"(W.w[4]), "=&{v[160:191]}"(W.w[5]), "=&{v[192:199]}"(W.t)
#define WTMP "memory", "scc", "v200", "v201", "v202", "v203", "v204", "v205", "v206", "v207", "v208", "v209", "v210", "v211", "v212", "v213", "v214", "v215", "v216", "v217", "v218", "v219", "v220", "v221", "v222", "v223", "v224", "v225", "v226", "v227", "v228", "v229", "v230", "v231", "v232", "v233", "v234", "v235", "v236", "v237", "v238", "v239"
template <bool NT>
__device__ __forceinline__ void load_w3(WRegs& W, const bf16_t* m, int wave, unsigned c0, bool hq) {
    const int ln = lane_id();
    unsigned o[3], s[3], t;
#pragma unroll
    for (int k = 0; k < 3; ++k) {
        const int row = (k * 64 + ln) / 24, sg = (k * 64 + ln) % 24;
        o[k] = row * (HD * 2) + c0 + (hq ? ((sg >> 1) & 1) * 384 + ((0x140 >> ((sg >> 3) * 4)) & 15) * 32 + (sg & 5) * 16 : sg * 16); s[k] = wave * 13824 + row * 416 + sg * 16;
    }
    const unsigned tr = (ln >> 4) * 3456 + ((ln >> 2) & 3) * 416 + (wave & 3) * 32 + (ln & 3) * 8;
#undef WNT
#define WNT " nt"
    if constexpr (NT) {
    asm volatile(ASM_WLOAD3 : WOUT(W), [t] "=&s"(t) : [rs] "s"(mkrsrc(m, DM * HD * 2)), [wx] "s"(wave), [o0] "v"(o[0]), [o1] "v"(o[1]), [o2] "v"(o[2]), [s0] "v"(s[0]), [s1] "v"(s[1]), [s2] "v"(s[2]),
                 [t0] "v"(tr + wave * 13824), [t1] "v"(tr + (wave ^ 1) * 13824), [t2] "v"(tr + (wave ^ 2) * 13824), [t3] "v"(tr + (wave ^ 3) * 13824), [t4] "v"(tr + (wave & 4) * 13824),
                 [wl] "v"(L_W + wave * 10240 + ln * 16) : WTMP);
    } else {
#undef WNT
#define WNT ""
    asm volatile(ASM_WLOAD3 : WOUT(W), [t] "=&s"(t) : [rs] "s"(mkrsrc(m, DM * HD * 2)), [wx] "s"(wave), [o0] "v"(o[0]), [o1] "v"(o[1]), [o2] "v"(o[2]), [s0] "v"(s[0]), [s1] "v"(s[1]), [s2] "v"(s[2]),
                 [t0] "v"(tr + wave * 13824), [t1] "v"(tr + (wave ^ 1) * 13824), [t2] "v"(tr + (wave ^ 2) * 13824), [t3] "v"(tr + (wave ^ 3) * 13824), [t4] "v"(tr + (wave & 4) * 13824),
                 [wl] "v"(L_W + wave * 10240 + ln * 16) : WTMP);
    }
}
#undef WNT
#define WNT " nt"
__device__ __forceinline__ void load_wo(WRegs& W, const bf16_t* m, int wave, int h) {
    const int ln = lane_id();
    unsigned o[5], s[5], t;
#pragma unroll
    for (int k = 0; k < 5; ++k) {
        const int row = (k * 64 + ln) / 20, sg = (k * 64 + ln) % 20;
        o[k] = (h * DH + row) * (DM * 2) + sg * 16 + (wave & 3) * 320; s[k] = wave * 13824 + row * 352 + sg * 16;
    }
    asm volatile(ASM_WLOADO : WOUT(W), [t] "=&s"(t) : [rs] "s"(mkrsrc(m, DM * HD * 2)), [o0] "v"(o[0]), [o1] "v"(o[1]), [o2] "v"(o[2]), [o3] "v"(o[3]), [o4] "v"(o[4]),
                 [s0] "v"(s[0]), [s1] "v"(s[1]), [s2] "v"(s[2]), [s3] "v"(s[3]), [s4] "v"(s[4]), [tr] "v"(wave * 13824 + (ln >> 2) * 352 + (ln & 3) * 8), [wl] "v"(L_W + wave * 10240 + ln * 16) : WTMP);
}
using s16x4 = __attribute__((ext_vector_type(4))) short;
template <int NT, int KS, int N>
__device__ __forceinline__ void load_wn(u32x4 (&f)[NT * KS], const bf16_t* m, int rows, int wave, int c0, int k0) {
    const int ln = lane_id();
    const rsrc_t rs = mkrsrc(m, rows * N * 2);
#pragma unroll
    for (int k = 0; k < KS; ++k)
#pragma unroll
        for (int n = 0; n < NT; ++n) f[n * KS + k] = __builtin_amdgcn_raw_buffer_load_b128(rs, ((n * 64 + ln) / (2 * NT)) * (N * 2) + ((n * 64 + ln) % (2 * NT)) * 16, (k0 + k) * (N * 64) + c0 * 32, 0);
    const unsigned st = wave * 10240 + ln * 16, tr = wave * 10240 + ((ln >> 4) * 8 + ((ln >> 2) & 3)) * (NT * 32) + (ln & 3) * 8;
#pragma unroll
    for (int k = 0; k < KS; ++k) {
#pragma unroll
        for (int n = 0; n < NT; ++n) lds_st<u32x4>(st + n * 1024, f[n * KS + k]);
        asm volatile("" ::: "memory");
#pragma unroll
        for (int n = 0; n < NT; ++n) {
            const u32x2 a = __builtin_bit_cast(u32x2, __builtin_amdgcn_ds_read_tr16_b64_v4i16((LAS s16x4*)(tr + n * 32)));
            const u32x2 b = __builtin_bit_cast(u32x2, __builtin_amdgcn_ds_read_tr16_b64_v4i16((LAS s16x4*)(tr + n * 32 + NT * 128)));
            f[n * KS + k] = (u32x4){a[0], a[1], b[0], b[1]};
        }
        asm volatile("" ::: "memory");
    }
}
struct C1Regs { u32x8 lw0, lw1, z1; u32x4 z0; };
#define XG_OUT "=&{v[200:207]}"(C.lw0), "=&{v[224:227]}"(C.z0), "=&{v[228:235]}"(C.lw1), "=&{v[244:251]}"(C.z1)
#define XG_IN WREGS(W), [xa] "v"(xa), [wl] "v"(wl), [qs] "s"(qs)
#define XG_CLOB "memory", "scc", "v208", "v209", "v210", "v211", "v212", "v213", "v214", "v215", "v216", "v217", "v218", "v219", "v220", "v221", "v222", "v223", "v236", "v237", "v238", "v239", "v240", "v241", "v242", "v243", "v252"
constexpr int XS0 = 8;
__device__ __forceinline__ unsigned init_wait(const Params& P, int c, int ph, unsigned need = 0x220080u) {
    unsigned w;
    for (int spins = 0;; ++spins) {
        unsigned o;
        asm volatile("v_mov_b32 %0, 0" : "=v"(o));
        w = __builtin_amdgcn_readfirstlane(__builtin_amdgcn_raw_buffer_load_b32(mkrsrc(P.ws + FL0, FL_BYTES), o, FL_IN + (ph * 8 + (c & 7)) * 128, 16));
        if ((w & need) == need) break;
        __builtin_amdgcn_s_sleep(8);
        if (spin_fail(spins, P)) break;
    }
    asm volatile("" ::: "memory");
    return w;
}
#define PLACE() const unsigned pw_ = init_wait(P, wave, L); const int par = (L ^ (pw_ >> 16)) & 1, cpy = x ^ ((pw_ >> 20) & 1); const rsrc_t rr = mkrsrc(P.ws + par * 8192, RG_L); (void)cpy; (void)rr
__device__ __forceinline__ void xn_wait(const Params& P, rsrc_t rr, int seg, int cpy) {
    for (int spins = 0;; ++spins) {
        const unsigned f = __builtin_amdgcn_raw_buffer_load_b32(rr, (lane_id() & 15) * 128, RG_FX + (cpy & 3) * 2048, 16);
        if (__all(f > (unsigned)seg)) break;
        __builtin_amdgcn_s_sleep(4);
        if (spin_fail(spins, P)) break;
    }
    asm volatile("" ::: "memory");
}
__device__ __forceinline__ unsigned c1_dst(int wave, bool ktype) {
    const int ln = lane_id(), l15 = ln & 15, hf = (wave >> 1) & 1;
    return (ktype ? L_KM + (l15 >> 2) * 128 + hf * 8 + (l15 & 3) * 2 + (ln >> 4) * 32
                  : L_Q + (l15 >> 3) * 256 + ((l15 >> 1) & 1) * 32 + ((l15 >> 2) & 1) * 8 + hf * 4 + (l15 & 1) * 2 + (ln >> 4) * 64) + (wave & 1) * 1536;
}
template <int NP>
__device__ __forceinline__ void crit1(const WRegs& W, C1Regs& C, rsrc_t rr, unsigned sbase, int wave, unsigned wl, unsigned dsto, unsigned& budget, unsigned& fv, unsigned fs
) {
    unsigned abase;
    asm volatile("v_and_b32 %0, 0x3ff, %1\n\tv_or_b32 %0, %2, %0" : "=&v"(abase) : "v"(wl), "s"(wave * 1024));
    ASM_ST_DECL;
#define C1_OUT "+{v[224:227]}"(C.z0), "+{v[244:251]}"(C.z1), "+{v[200:207]}"(C.lw0), "+{v[228:235]}"(C.lw1)
#define C1_IN WREGS(W), [ab] "v"(abase), [wl] "v"(wl), [dst] "v"(dsto)
#define C1_CLOB "memory", "vcc", "scc", "v208", "v209", "v210", "v211", "v212", "v213", "v214", "v215", "v216", "v217", "v218", "v219", "v220", "v221", "v222", "v223", "v236", "v237", "v238", "v239", "v240", "v241", "v242", "v243"
#define C1_POLL [rs] "s"(rr), [s0] "s"(sbase), [s1] "s"(sbase + 4096), [s2] "s"(sbase + 2 * BLK)
    ST(1);
    if constexpr (NP == 13) asm volatile(ASM_CRIT1_V3 "s_waitcnt vmcnt(0)" : C1_OUT, [n] "+s"(budget), [fv] "+v"(fv) ASM_ST_OUT ASM_ST2_OUT : C1_IN, C1_POLL, [fs] "s"(fs) : C1_CLOB);
    else if constexpr (NP == 12) asm volatile(ASM_CRIT1_V2 "s_waitcnt vmcnt(0)" : C1_OUT, [n] "+s"(budget), [fv] "+v"(fv) ASM_ST_OUT ASM_ST2_OUT : C1_IN, C1_POLL, [fs] "s"(fs) : C1_CLOB);
    else if constexpr (NP == 3) asm volatile(ASM_CRIT1_3 : C1_OUT, [n] "+s"(budget) ASM_ST_OUT ASM_ST2_OUT : C1_IN, C1_POLL : C1_CLOB);
    else if constexpr (NP == 2) asm volatile(ASM_CRIT1_2 : C1_OUT, [n] "+s"(budget) ASM_ST_OUT ASM_ST2_OUT : C1_IN, C1_POLL : C1_CLOB);
    else asm volatile(ASM_CRIT1_N : C1_OUT ASM_ST_OUT : C1_IN : C1_CLOB);
    SB();
    ASM_ST_SAVE(3);
    if constexpr (NP != 0) { ASM_ST2_SAVE(2); }
}
#define STARG

#define SRG 5
#undef STROLE
#define STROLE 1
__device__ __forceinline__ void s_role(const Params& P, int wave, int L, int h, int x) {
    const char* xn = P.ws + PG0 + PG_XN + L * PG_LX;
    auto xdma = [&](int seg, int hb, int par) {
        const unsigned l16 = lane_id() * 16;
        const int mt = (wave >> 1) & 1;
        const char* g = xn + pgb(seg, par);
        if (hb == 0) {
            dma4(g + blin(mt * 20480 + (wave & 1) * 4096), l16, L_XA + mt * 16384 + (wave & 1) * 4096);
            dmaq<0>(g + blin(mt * 20480 + 16384) + (wave & 1) * 1024, l16, L_XB + mt * 4096 + (wave & 1) * 1024);
        } else {
            dma4(g + blin(mt * 20480 + 8192), l16, L_XA + mt * 16384 + 8192); dma4(g + blin(mt * 20480 + 12288), l16, L_XA + mt * 16384 + 12288);
            dmaq<2>(g + blin(mt * 20480 + 16384), l16, L_XB + mt * 4096); dmaq<3>(g + blin(mt * 20480 + 16384), l16, L_XB + mt * 4096);
        }
    };
    const float SCL = 0.07216878364870322f * 1.4426950408889634f;
    auto run = [&](auto Cc) {
    constexpr int CLS = decltype(Cc)::value;
    WRegs wf;
    MARK(!L && h == 0 && wave == 0, 0);
    load_w3<(NTM & (CLS == 3 ? 2 : 1)) != 0>(wf, P.w[CLS == 3], wave, h * 384, false);
    MARKX(!L && wave == 0, 1);
    PLACE();
    if (CLS == 3) {
        if (L) xn_wait(P, rr, 0, cpy);
        xdma(0, 0, par);
        if (wave == 4 || wave == 7) xdma(0, 1, par);
        wait_vm<0>();
    }
    bar();
    if (CLS == 3 && wave == 7 && L) xn_wait(P, rr, 1, cpy);
    const unsigned qs = (CLS == 3 ? L_KX : L_QX) + (wave & 1) * 3072 + ((wave >> 1) & 1) * 8;
    unsigned xa, wl, dsto;
    if (CLS == 3) { xa = L_XA + wave * 2048 + lane_id() * 16; wl = L_W + wave * 10240 + lane_id() * 16; dsto = c1_dst(wave, true); }
    __builtin_amdgcn_s_setprio(CLS == 3 ? 1 : 2);
    unsigned budget = 1u << 18, tb2 = 0;
    STDECL;
    LDECL;
#pragma unroll 1
    for (int t = 0; t < NSEG; ++t) {
        const unsigned slot = t & (NSLOT - 1);
        C1Regs C;
        {
            const WRegs& W = wf;
            ASM_ST_DECL;
            if constexpr (CLS == 3) {
                const int mt = (wave >> 1) & 1;
                const char* g = xn + pgb(t + 1 < NSEG ? t + 1 : t, par);
                asm volatile(ASM_XG_S_HI : XG_OUT ASM_ST_OUT : XG_IN, [slp] "s"(SLP4), [g0] "s"(g + blin(mt * 20480 + (wave & 1) * 4096)), [d0] "s"(L_XA + mt * 16384 + (wave & 1) * 4096),
                             [g1] "s"(g + blin(mt * 20480 + 16384) + (wave & 1) * 1024), [d1] "s"(L_XB + mt * 4096 + (wave & 1) * 1024) : XG_CLOB);
                wait_vm<0>();
                LB(t);
            } else {
                const unsigned l16 = lane_id() * 16;
                xa = L_XA + wave * 2048 + l16; wl = L_W + wave * 10240 + l16;
#define XG_Q(A) asm volatile("s_cmp_lt_u32 %[t], 2\n\ts_cbranch_scc1 .Lq3%=\n\t" A "s_branch .Lqe%=\n\t.Lq3%=:\n\t" ASM_XG_S_LO ".Lqe%=:" : XG_OUT ASM_ST_OUT : XG_IN, [t] "s"(t) : XG_CLOB)
                if constexpr (CLS == 0 || CLS == 4) XG_Q(ASM_XG_S_LO1); else XG_Q(ASM_XG_S_LO2);
                __builtin_amdgcn_s_setprio(0);
                if constexpr (CLS == 0) {
                    const int ln = lane_id();
                    const u32x4 s4 = sent4();
                    const unsigned poff = (ln * 16 + ((ln < 8) ? h * 128 : (1 + (h >> 4)) * 2 * BLK - 128 + (h & 15) * 512)) | ((ln >= 40 || t < 2) ? 0x40000000u : 0u);
#pragma unroll
                    for (int c = 0; c < (int)NCP; c += (EO == 1 ? 2 : 1)) st128(s4, rr, poff, RG_XP + ((t - 2) & (NSLOT - 1)) * XP_SLOT + (c >> 1) * XP_PAIR + ((EO == 1 ? cpy : c) & 1) * BLK);
                    const unsigned pbase = RG_PX + ((t - 3) & (NSLOT - 1)) * PX_SLOT + (h >> 2) * BLK;
                    const unsigned o2 = ((ln & 16) ? 5120 + ((h >> 1) & 1) * 1024 + (h & 1) * 256 + (ln >> 5) * 512 : 4096 + (h & 3) * 256 + (ln >> 5) * 3072) + (ln & 15) * 16;
                    if (t >= 3) { st128(s4, rr, ln * 16, pbase + (h & 3) * 1024); st128(s4, rr, o2, pbase); }
                }
                if (CLS == 4 && t >= HT0) {
                    const int ln = lane_id(), u = h >> 2;
                    for (int spins = 0;; ++spins) {
                        const unsigned fl = __builtin_amdgcn_raw_buffer_load_b32(rr, (ln & 3) * 128, RG_FH + BLK + u * 512, 16);
                        if (__all(fl > 2u * t + 1u)) break;
                        __builtin_amdgcn_s_sleep(2);
                        if (spin_fail(spins, P)) break;
                    }
                    asm volatile("" ::: "memory");
                    const char* g = P.ws + PG0 + pgb(t, par) + PG_HV + (16 + u) * 3 * BLK;
                    unsigned t0, t1, vo;
                    static_assert(L_QX == 16384, "");
                    asm volatile("s_add_u32 %0, %4, 0\n\ts_and_b32 %1, %0, 0xe000\n\ts_add_u32 %0, %0, %1\n\tv_add_u32 %2, %0, %5\n\ts_mov_b32 m0, 16384\n\ts_nop 0\n\tglobal_load_lds_dwordx4 %2, %3 sc1\n\t"
                                 "s_add_u32 %0, %4, 1024\n\ts_and_b32 %1, %0, 0xe000\n\ts_add_u32 %0, %0, %1\n\tv_add_u32 %2, %0, %5\n\ts_mov_b32 m0, 17408\n\ts_nop 0\n\tglobal_load_lds_dwordx4 %2, %3 sc1\n\t"
                                 "s_add_u32 %0, %4, 2048\n\ts_and_b32 %1, %0, 0xe000\n\ts_add_u32 %0, %0, %1\n\tv_add_u32 %2, %0, %5\n\ts_mov_b32 m0, 19456\n\ts_nop 0\n\tglobal_load_lds_dwordx4 %2, %3 sc1\n\t"
                                 "s_add_u32 %0, %4, 6144\n\ts_and_b32 %1, %0, 0xe000\n\ts_add_u32 %0, %0, %1\n\tv_add_u32 %2, %0, %5\n\ts_mov_b32 m0, 22528\n\ts_nop 0\n\tglobal_load_lds_dwordx4 %2, %3 sc1\n\t"
                                 "s_add_u32 %0, %4, 7168\n\ts_and_b32 %1, %0, 0xe000\n\ts_add_u32 %0, %0, %1\n\tv_add_u32 %2, %0, %5\n\ts_mov_b32 m0, 23552\n\ts_nop 0\n\tglobal_load_lds_dwordx4 %2, %3 sc1\n\t"
                                 "s_add_u32 %0, %4, 8192\n\ts_and_b32 %1, %0, 0xe000\n\ts_add_u32 %0, %0, %1\n\tv_add_u32 %2, %0, %5\n\ts_mov_b32 m0, 25600\n\ts_nop 0\n\tglobal_load_lds_dwordx4 %2, %3 sc1\n\t"
                                 : "=&s"(t0), "=&s"(t1), "=&v"(vo) : "s"(g), "s"((((h >> 1) & 1) * 12 + (h & 1) * 3) * 1024), "v"(ln * 16) : "memory", "scc");
                }
                dsto = c1_dst(wave, false);
                while ((unsigned)__builtin_amdgcn_s_memtime() - tb2 < STD) __builtin_amdgcn_s_sleep(2);
            }
            ASM_ST_SAVE(7);
            ST(6);
        }
        ST(0);
        unsigned fv = 0;
        if (CLS == 2) fv = (lane_id() & 15) * 128;
        crit1<CLS == 3 ? 0 : CLS < 2 ? 3 : CLS == 2 ? 12 : 2>(wf, C, rr, RG_XM + slot * XM_SLOT + (cpy >> 1) * XM_PAIR + (cpy & 1) * BLK, wave, wl, dsto, budget, fv, RG_FX + (cpy & 3) * 2048 STARG);
        if (CLS != 3) tb2 = (unsigned)__builtin_amdgcn_s_memtime();
        if constexpr (CLS == 0) {
            const unsigned sp = RG_XP + slot * XP_SLOT + (EO == 1 ? (cpy & 1) * BLK : 0), sx = sp + (1 + (h >> 4)) * 2 * BLK + (h & 15) * 512;
            ASM_ST_DECL;
            asm volatile(ASM_SM : [c0] "+s"(budget) ASM_ST_OUT : WREGS(wf), [rr] "s"(rr), [sx] "s"(sx), [sy] "s"(sx + XP_PAIR), [sm] "s"(sp + h * 128), [cs] "s"(EO == 1 ? XP_PAIR : BLK), [cp] "s"(EO == 1 ? 0 : XP_PAIR), [c70] "s"(0x70), [scl] "s"(SCL)
                         : "memory", "v200", "v201", "v202", "v203", "v204", "v205", "v206", "v207", "v208", "v209", "v210", "v211", "v212", "v213", "v214", "v215", "v216", "v217", "v218", "v219", "v220", "v221", "v222", "v223", "v224", "v225", "v226", "v227", "v228", "v229", "v230", "v231", "v232", "v233", "v234", "v235", "v236", "v237", "v238", "v239", "v240", "v241", "v242", "v243", "v244", "v245", "v246", "v247", "v248", "v249", "v250", "v251", "v252", "v253", "v254", "v255");
            ASM_ST_SAVE(4);
            ST(5);
        } else if constexpr (CLS == 1 || CLS == 4) {
            const int ln = lane_id(), l15 = ln & 15, l4 = ln >> 4;
            constexpr int mt = (CLS == 4);
            const unsigned qa = L_QX + mt * 6144 + ln * 16, kmo = L_KM + l4 * 128 + (ln & 7) * 16;
            const unsigned xo = mt ? (h & 3) * 1024 : 5120 + ((h >> 1) & 1) * 1024 + (h & 1) * 256;
            const unsigned pm = l4 < 2 ? (mt ? 4096 : 7168) + (h & 3) * 256 - xo + l15 * 16 + l4 * 8 : OOB;
            const unsigned so = RG_PX + slot * PX_SLOT + (h >> 2) * BLK + xo;
            f32x4 sd;
#pragma unroll
            for (int r = 0; r < 4; ++r) sd[r] = (l4 * 4 + r > l15) ? -3.0e38f : 0.f;
            ASM_ST_DECL;
#define P4_ARGS : "+{v[240:243]}"(sd) ASM_ST_OUT : WREGS(wf), [qa] "v"(qa), [km] "v"(kmo), [pm] "v"(pm), [rr] "s"(rr), [so] "s"(so), [scl] "s"(SCL) : "memory", "v200", "v201", "v202", "v203", "v204", "v205", "v206", "v207", "v208", "v209", "v210", "v211", "v212", "v213", "v214", "v215", "v216", "v217", "v218", "v219", "v220", "v221", "v222", "v223", "v224", "v225", "v226", "v227", "v228", "v229", "v230", "v231", "v232", "v233", "v234", "v235", "v236", "v237", "v238", "v239", "v244", "v245", "v246", "v247", "v248", "v249", "v250", "v251"
            if constexpr (mt) asm volatile(ASM_P4_1 P4_ARGS); else asm volatile(ASM_P4_0 P4_ARGS);
            ASM_ST_SAVE(4);
            ST(5);
        } else {
            bar();
            ST(4);
            if (CLS == 2 && L && t + 2 < NSEG && !__all(fv > (unsigned)t + 2)) xn_wait(P, rr, t + 2, cpy);
            if (CLS == 2) { int tt = t; asm volatile("" : "+s"(tt)); if (tt + 2 == XS0) init_wait(P, h, 1); }
            if (CLS == 3) LA();
            {
                if (CLS == 3 && (wave == 4 || wave == 7)) {
                    if (t + 1 < NSEG) xdma(t + 1, 1, par);
                }
                ST(5);
            }
        }
        STFLUSH(t);
    }
    LF();
    };
    if (wave == 0) run(std::integral_constant<int, 0>{});
    else if (wave == 1) run(std::integral_constant<int, 1>{});
    else if (wave == 2) run(std::integral_constant<int, 4>{});
    else if (wave == 3) run(std::integral_constant<int, 2>{});
    else run(std::integral_constant<int, 3>{});
}

#undef STROLE
#define STROLE 8
__device__ __forceinline__ void h_role(const Params& P, int wave, int L, int i, int x) {
    const rsrc_t rp = mkrsrc(P.ws + PG0, PG_L);
    const char* xn = P.ws + PG0 + PG_XN + L * PG_LX;
    auto run = [&](auto Cc) {
    constexpr bool HI = decltype(Cc)::value;
    const int mt = (wave >> 1) & 1;
    WRegs wf;
    load_w3<(NTM & 16) != 0>(wf, i < 16 ? P.w[2] : P.w[0], wave, (i < 16 ? i * 2 + HI : (i - 16) * 4 + HI * 2) * 384, i >= 16);
    PLACE();
    const unsigned qs = (HI ? L_KX : L_QX) + (wave & 1) * 3072 + mt * 8, l16 = lane_id() * 16, wl = L_W + wave * 10240 + l16;
    unsigned xa = L_XA + wave * 2048 + l16;
    lds_st<unsigned>(L_MEM + wave * 4, 0u);
    __builtin_amdgcn_s_setprio(HI ? 1 : 2);
    if (HI) bar();
    LDECL;
#define HT_IN WREGS(wf), [xa] "v"(xa), [wl] "v"(wl), [l16] "v"(l16), [xb] "s"(L_XB + m * 4096), [qd] "s"(qs + m * 6144), [gw] "s"(L_MEM + m * 16), [rr] "s"(rr)
#define HT_CLOB "memory", "vcc", "scc", "v200", "v201", "v202", "v203", "v204", "v205", "v206", "v207", "v208", "v209", "v210", "v211", "v212", "v213", "v214", "v215", "v216", "v217", "v218", "v219", "v220", "v221", "v222", "v223", "v224", "v225", "v226", "v227", "v228", "v229", "v230", "v231", "v232", "v233", "v234", "v235", "v236", "v237", "v238", "v239", "v240", "v241", "v242", "v243", "v244", "v245", "v246", "v247", "v248", "v249", "v250", "v251", "v252"
    if constexpr (HI) {
#pragma unroll 1
        for (int u = 2 * HT0; u <= 2 * NSEG; ++u) {
            const int m = u & 1;
            const unsigned f = (mt * 12 + (m ^ 1) * 6 + (wave & 1) * 3) * 1024, so = pgb((u - 1) >> 1, par) + PG_HV + i * 3 * BLK;
            unsigned pb = 1;
            asm volatile("" : "+s"(pb));
            asm volatile(ASM_HT_HI : [pb] "+s"(pb) : HT_IN, [rp] "s"(rp), [ro] "s"(L_QX + f), [s0] "s"(so + blin(f)), [s1] "s"(so + blin(f + 1024)), [s2] "s"(so + blin(f + 2048)),
                         [fp] "s"(RG_FH + (i >> 4) * BLK + ((i & 15) * 4 + (wave & 3)) * 128), [tv] "s"(u), [end] "s"(u >> 7) : HT_CLOB);
            xa ^= L_XA ^ (L_XA + 16384);
            PMARK(L && wave == 7 && m, 6, u >> 1);
        }
    } else {
        unsigned go = 0, nx = 0, budget = 1u << 18, fl;
        asm volatile("s_sub_u32 %0, %1, 1" : "=s"(fl) : "s"(L) : "scc");
#pragma unroll 1
        for (int u = 2 * HT0; u < 2 * NSEG; ++u) {
            const int m = u & 1;
            const char *c = xn + pgb(u >> 1, par), *g = xn + pgb((u + 1) >> 1, par);
            unsigned a;
            { int uu = u; asm volatile("" : "+s"(uu)); if (uu + 1 == 2 * XS0) init_wait(P, wave, 1); }
            asm volatile(ASM_HT_LO : [go] "+s"(go), [nx] "+s"(nx), [n] "+s"(budget), [a] "=&s"(a)
                         : HT_IN, [c0] "s"(c + blin(m * 20480 + wave * 4096)), [e0] "s"(L_XA + m * 16384 + wave * 4096), [c1] "s"(c + blin(m * 20480 + 16384) + wave * 1024), [e1] "s"(L_XB + m * 4096 + wave * 1024),
                           [g0] "s"(g + blin((m ^ 1) * 20480 + wave * 4096)), [d0] "s"(L_XA + (m ^ 1) * 16384 + wave * 4096), [g1] "s"(g + blin((m ^ 1) * 20480 + 16384) + wave * 1024),
                           [d1] "s"(L_XB + (m ^ 1) * 4096 + wave * 1024), [fs] "s"(RG_FX + (cpy & 3) * 2048), [fl] "s"(fl), [m] "s"(m), [u1] "s"(u + 1), [u2] "s"(u + 2),
                           [k1] "s"(0x5555 + ((u + 1) & 128) * 512), [k2] "s"(0x5555 + ((u + 2) & 128) * 512), [gn] "s"(L_MEM + (m ^ 1) * 16 + wave * 4) : HT_CLOB);
            xa ^= L_XA ^ (L_XA + 16384);
            if (m) { LB(u >> 1); LA(); }
        }
        bar();
    }
    LF();
    };
    if (wave < 4) run(std::false_type{}); else run(std::true_type{});
}

#undef STROLE
#define STROLE 2
__device__ __forceinline__ void v_role(const Params& P, int wave, int L, int h, int x) {
    WRegs wf;
    STDECL;
    auto runv = [&](auto Cc) {
        constexpr unsigned LATE = decltype(Cc)::value == 2;
        load_w3<(NTM & 4) != 0>(wf, P.w[2], wave, h * 384, false);
        MARKX(!L && wave == 0, 6);
        PLACE();
        unsigned budget = 1u << 18;
        const unsigned wl = L_W + wave * 10240 + lane_id() * 16, dsto = c1_dst(wave, false);
        auto vfetch = [&](int seg, int tries) {
            const int ln = lane_id();
            for (int spins = 0; tries != -2; ++spins) {
                if (spins == tries) return false;
                const unsigned f = __builtin_amdgcn_raw_buffer_load_b32(rr, (ln & 3) * 128, RG_FH + (h >> 1) * 512, 16);
                if (__all(f > 2u * seg + LATE)) break;
                __builtin_amdgcn_s_sleep(4);
                if (spin_fail(spins, P)) break;
            }
            asm volatile("" ::: "memory");
            const char* g = P.ws + PG0 + pgb(seg, par) + PG_HV + (h >> 1) * 3 * BLK;
#pragma unroll
            for (int k = 0; k < 3; ++k) {
                const int f = (h & 1) * 12 + wave * 3 + k;
                dmaq<0>(g + (f >> 3) * BLK + (f & 7) * 1024, ln * 16, L_QX + (seg & 1) * 12288 + (wave * 3 + k) * 1024);
            }
            return true;
        };
#pragma unroll 1
        for (int t = 0; t < HT0; ++t) {
            if (L) xn_wait(P, rr, t, cpy);
            const unsigned l16 = lane_id() * 16;
            const char* g = P.ws + PG0 + PG_XN + L * PG_LX + pgb(t, par);
            dma4(g + blin(wave * 4096), l16, L_XA + wave * 4096); dma4(g + blin(20480 + wave * 4096), l16, L_XA + 16384 + wave * 4096);
            dmaq<0>(g + blin(16384) + wave * 1024, l16, L_XB + wave * 1024); dmaq<0>(g + blin(20480 + 16384) + wave * 1024, l16, L_XB + 4096 + wave * 1024);
            wait_vm<0>();
            bar();
            C1Regs C;
            const WRegs& W = wf;
            const unsigned xa = L_XA + wave * 2048 + l16, qs = L_QX + (wave & 1) * 3072 + ((wave >> 1) & 1) * 8 + t * 12288;
            asm volatile(ASM_XG_V : XG_OUT : XG_IN : XG_CLOB);
            if (t == 0) bar();
        }
        bool seen = false;
        unsigned tb2 = 0;
        for (int t = 0; t < NSEG; ++t) {
            const bool fe = t + 1 >= HT0 && t + 1 < NSEG;
            if (fe && (seen || !LATE)) vfetch(t + 1, seen ? -2 : -1);
            if (!LATE) wait_vm<0>();
            ST(7);
            bar();
            if (fe) {
                if (LATE) { if (!seen) vfetch(t + 1, -1); wait_vm<0>(); }
                bar();
            }
            C1Regs C;
            asm volatile("ds_read_b128 v[200:203], %4\n\tds_read_b128 v[204:207], %4 offset:1024\n\tds_read_b128 v[228:231], %4 offset:2048\n\tds_read_b128 v[232:235], %4 offset:3072\n\t"
                         "v_mov_b64 v[224:225], 0\n\tv_mov_b64 v[226:227], 0\n\tv_mov_b64 v[244:245], 0\n\tv_mov_b64 v[246:247], 0\n\tv_mov_b64 v[248:249], 0\n\tv_mov_b64 v[250:251], 0\n\ts_waitcnt lgkmcnt(0)"
                         : XG_OUT : "v"(wl) : "memory");
            ST(6);
            while ((unsigned)__builtin_amdgcn_s_memtime() - tb2 < VTD) __builtin_amdgcn_s_sleep(1);
            unsigned fv = (lane_id() & 3) * 128;
            crit1<10 + decltype(Cc)::value>(wf, C, rr, RG_XM + (t & (NSLOT - 1)) * XM_SLOT + (cpy >> 1) * XM_PAIR + (cpy & 1) * BLK, wave, wl, dsto, budget, fv, RG_FH + (h >> 1) * 512 STARG);
            __builtin_amdgcn_s_setprio(0);
            bar();
            seen = __all(fv > 2u * t + 4u + LATE);
            tb2 = (unsigned)__builtin_amdgcn_s_memtime();
            ST(4);
            STC(5);
            STFLUSH(t);
        }
    };
    if (wave < 2) runv(std::integral_constant<int, 3>{});
    else if (wave < 4) runv(std::integral_constant<int, 2>{});
    else {
        const int w4 = wave - 4;
        const rsrc_t rd = mkrsrc(P.ws + PG0, PG_L);
        load_wo(wf, P.w[3], wave, h);
        PLACE();
        bar();
        bar();
        for (int t = -1; t < NSEG; ++t) {
            const unsigned slot = t & (NSLOT - 1);
            bar();
            ST(7);
#pragma unroll 1
            for (int n = V1A ? t + (t <= 0) : t + (t != 1) + (t == 0); n <= (V1A && t >= 0 ? t : t + 1) && n < NSEG; ++n) {
                const unsigned l16 = lane_id() * 16;
                const unsigned wl = L_W + wave * 10240 + l16, va = L_QX + (n & 1) * 12288 + l16;
                unsigned so = ((n & 7) << 22) + pgb(64 + (w4 >> 1), par) + ((w4 & 1) * 40 + (h >> 3)) * BLK + (h & 7) * 1024;
                asm volatile(ASM_VOX : [so] "+s"(so) : WREGS(wf), [l16] "v"(l16), [wl] "v"(wl), [va] "v"(va), [rd] "s"(rd), [sy] "s"(n >> 1) : "memory", "scc", "v200", "v201", "v202", "v203", "v204", "v205", "v206", "v207", "v208", "v209", "v210", "v211", "v212", "v213", "v214", "v215", "v216", "v217", "v218", "v219", "v220", "v221", "v222", "v223", "v224", "v225", "v226", "v227", "v228", "v229", "v230", "v231", "v232", "v233", "v234", "v235", "v236", "v237", "v238", "v239", "v240", "v241", "v242", "v243", "v244", "v245", "v246", "v247", "v248", "v249", "v250", "v251");
                if (V1A || n < 2) {
                    wait_vm<0>();
                    const unsigned l4 = lane_id() * 4;
                    if (l4 < 128) __builtin_amdgcn_raw_buffer_store_b32(n + 1, rr, l4, RG_FV + (w4 >> 1) * BLK + ((w4 & 1) * 32 + h) * 128, 16);
                }
            }
            if (t < 0) continue;
            ST(0);
            unsigned wl = L_W + wave * 10240 + lane_id() * 16;
            bf16x16 lw = lds_frag(wl, 1024);
            u32x32 cz;
            asm volatile("v_mov_b64 v[220:221], 0\n\tv_mov_b64 v[222:223], 0\n\tv_mov_b64 v[224:225], 0\n\tv_mov_b64 v[226:227], 0\n\tv_mov_b64 v[228:229], 0\n\tv_mov_b64 v[230:231], 0\n\tv_mov_b64 v[232:233], 0\n\tv_mov_b64 v[234:235], 0\n\tv_mov_b64 v[236:237], 0\n\tv_mov_b64 v[238:239], 0\n\tv_mov_b64 v[240:241], 0\n\tv_mov_b64 v[242:243], 0\n\tv_mov_b64 v[244:245], 0\n\tv_mov_b64 v[246:247], 0\n\tv_mov_b64 v[248:249], 0\n\tv_mov_b64 v[250:251], 0" : "={v[220:251]}"(cz));
            unsigned vo = ((wl >> 9) & 1) * (1 + (w4 & 1) * 2) * BLK + ((wl >> 8) & 1) * 1024 + (wl & 255), vl = ((wl >> 9) & 1) * 8 + ((wl >> 8) & 1) * 1024 + (wl & 255);
            asm volatile("" : "+{v[212:219]}"(lw), "+{v[220:251]}"(cz), "+v"(wl), "+v"(vo), "+v"(vl));
            unsigned l16 = wl & 1023u;
            bar();
            wait_vm<0>();
            if (!V1A && l16 < 512 && t && t + 1 < NSEG) __builtin_amdgcn_raw_buffer_store_b32(t + 2, rr, l16 >> 2, RG_FV + (w4 >> 1) * BLK + ((w4 & 1) * 32 + h) * 128, 16);
            ST(1);
            const int sidx = (wl & 32u) ? 0xEEEE : 0x4444;
            unsigned so = RG_VM + slot * VM_SLOT + (unsigned)((w4 * 10 - (w4 & 1) + (h >> 4) * 2) * BLK + ((h >> 2) & 3) * 2048 + (h & 3) * 256);
            ST(3);
            ASM_ST_DECL;
            asm volatile(ASM_CRIT2 : [so] "+s"(so), "+{v[212:219]}"(lw), "+{v200}"(l16), "+{v[220:251]}"(cz) ASM_ST_OUT
                         : WREGS(wf), [wl] "v"(wl), [vo] "v"(vo), [vl] "v"(vl), [sidx] "v"(sidx), [rr] "s"(rr)
                         : "memory", "scc", "v201", "v202", "v203", "v204", "v205", "v206", "v207", "v208", "v209", "v210", "v211");
            ASM_ST_SAVE(4);
            ST(5);
            STFLUSH(t);
        }
    }
}

#undef STROLE
#define STROLE 4
constexpr unsigned R_RED = 0;
constexpr unsigned R_PART = 16384;
__device__ __forceinline__ void r_role(const Params& P, int wave, int L, int r5, int x) {
    PLACE();
    const int jj = r5 ^ (cpy ^ x) ^ (r5 >= 6 ? 1 : 0);
    const int lane = lane_id(), l15 = lane & 15, l4 = lane >> 4;
    const unsigned l16 = lane * 16;
    const rsrc_t rd = mkrsrc(P.ws + PG0, PG_L);
    const int sidx = (l15 & 2) ? 0xEEEE : 0x4444;
    unsigned offp[3];
    offp[0] = RG_XP + (cpy >> 1) * XP_PAIR + (cpy & 1) * BLK + (1 + (wave >> 2)) * 2 * BLK + (wave & 3) * 2048 + l16;
    offp[2] = (l15 & 2) ? OOB : RG_XP + (cpy >> 1) * XP_PAIR + (cpy & 1) * BLK + wave * 512 + l4 * 128 + (l15 >> 2) * 32 + (l15 & 1) * 16;
    const unsigned offv = RG_VM + ((jj >> 1) * 4 + (wave >> 2) * 2 + (jj & 1)) * BLK + (wave & 3) * 2048 + l16;
    const int j = jj * 2 + ((wave >> 1) & 1), fl = (wave & 1) * 32 + (lane & 31);
    const unsigned offo = RG_XM + (wave >> 2) * XM_PAIR + (lane >> 5) * BLK + (j & 31) * 256 + (j >> 5) * 2 * BLK + fl * 4;
    MARK(!L && jj == 0 && wave == 0, 5);
    {
        const int row = (fl >> 4) * 2 + ((fl >> 2) & 1), col = 16 * j + 4 * (fl & 3) + ((fl >> 2) & 2);
        __builtin_amdgcn_raw_buffer_store_b32(nosent(*(const unsigned*)(P.mem0 + row * DM + col)), rr, offo, 0, 16);
    }
    auto red_addr = [](int f) { return R_RED + f * 144 + (f >> 3) * 64; };
    const unsigned rw0 = red_addr(l4 * 16 + ((l15 >> 1) & 1) * 8 + (l15 >> 2)) + (l15 & 1) * 4 + (wave ^ (l4 & 1)) * 8;
    const unsigned rrd = red_addr(fl) + ((wave >> 1) & 1) * 64 + (lane >> 5) * 32;
    u32x32 vx;
    const unsigned fo = (lane & 3) * 128, fs = RG_FV + (jj / 10) * BLK + (((jj / 5) & 1) * 32 + wave * 4) * 128, vxo = pgb(64 + jj / 10, par) + (unsigned)((jj % 10) * 8 + (wave >> 1)) * BLK + (wave & 1) * 4096;
    const rsrc_t rp = mkrsrc(P.ws + PG0, PG_L);
    const unsigned xo = (l4 >> 1) * 256 + l15 * 16 + (l4 & 1) * 8, pw = R_PART + wave * 1024 + l16, pr = R_PART + (wave & 3) * 8192 + l16;
    const int fin = wave >> 2;
    asm volatile("" : "={v[128:159]}"(vx));
    auto vx_fetch = [&](int t) {
        for (int spins = 0;; ++spins) {
            const unsigned f = __builtin_amdgcn_raw_buffer_load_b32(rr, fo, fs, 16);
            if (__all(f > (unsigned)t)) break;
            __builtin_amdgcn_s_sleep(2);
            if (spin_fail(spins, P)) break;
        }
        const unsigned sv = ((t & 7) << 22) + vxo;
        asm volatile("buffer_load_dwordx4 v[128:131], %1, %2, %3 offen sc1\n\tbuffer_load_dwordx4 v[132:135], %1, %2, %3 offen offset:1024 sc1\n\t"
                     "buffer_load_dwordx4 v[136:139], %1, %2, %3 offen offset:2048 sc1\n\tbuffer_load_dwordx4 v[140:143], %1, %2, %3 offen offset:3072 sc1\n\t"
                     "buffer_load_dwordx4 v[144:147], %1, %2, %4 offen sc1\n\tbuffer_load_dwordx4 v[148:151], %1, %2, %4 offen offset:1024 sc1\n\t"
                     "buffer_load_dwordx4 v[152:155], %1, %2, %4 offen offset:2048 sc1\n\tbuffer_load_dwordx4 v[156:159], %1, %2, %4 offen offset:3072 sc1"
                     : "+{v[128:159]}"(vx) : "v"(l16), "s"(rp), "s"(sv), "s"(sv + 4 * BLK) : "memory");
    };
    vx_fetch(0);
    unsigned budget = 1u << 18, tpub = 0;
    STDECL;
    LDECL;
    for (int t = 0; t < NSEG; ++t) {
        const unsigned slot = t & (NSLOT - 1);
        ST(0);
        while ((unsigned)__builtin_amdgcn_s_memtime() - tpub < RTD) __builtin_amdgcn_s_sleep(1);
        u32x8 vm, pv;
        u32x4 pm;
        {
            unsigned a;
            ASM_ST_DECL;
            asm volatile(ASM_RCRIT : "=&{v[176:183]}"(vm), "=&{v[184:191]}"(pv), "=&{v[192:195]}"(pm), [a] "=&s"(a), [n] "+s"(budget) ASM_ST_OUT ASM_ST2_OUT ASM_ST3_OUT
                         : [rs] "s"(rr), [ov] "v"(offv), [sv] "s"(slot * VM_SLOT), [o0] "v"(offp[0]), [o2] "v"(offp[2]), [sp] "s"(slot * XP_SLOT),
                           "{v[128:159]}"(vx), [sidx] "v"(sidx), [rw] "v"(rw0), [rd] "v"(rrd), [oo] "v"(offo),
                           [so] "s"(((t + 1) & (NSLOT - 1)) * XM_SLOT), [pub] "s"(NSEG - 1 - t)
                         : "memory", "vcc", "scc", "v160", "v161", "v162", "v163", "v164", "v165", "v166", "v167", "v196", "v197", "v200", "v201", "v202", "v203", "v204", "v205", "v206", "v207");
            ASM_ST_SAVE(2); ASM_ST2_SAVE(1); ASM_ST3_SAVE(6);
        }
        ST(3);
        LA();
        MARK(!L && jj == 0 && wave == 0 && t < 4, 10 + t);
        PMARK(wave == 0, L * 7, t - 1 + L);
        tpub = (unsigned)__builtin_amdgcn_s_memtime();
        __builtin_amdgcn_s_sleep(RXDEL);
        unsigned early;
#pragma unroll 1
        for (int spins = 1023;; spins += 1024) {
            unsigned n;
            const unsigned sx = pgb(t, par) + blin(((wave >> 1) & 1) * 20480 + jj * 1024) + (wave & 1) * 512;
            const unsigned pxb = RG_PX + slot * PX_SLOT + wave * BLK, sv = (((t + 1) & 7) << 22) + vxo;
            ASM_ST_DECL;
            asm volatile(ASM_RX : "+{v[128:159]}"(vx), [n] "=&s"(n), [a] "=&s"(early) ASM_ST_OUT ASM_ST2_OUT
                         : "{v[176:183]}"(vm), "{v[184:191]}"(pv), "{v[192:195]}"(pm), [l16] "v"(l16), [rs] "s"(rr), [s0] "s"(pxb), [s1] "s"(pxb + 4096), [rd] "s"(rd), [rx] "s"(rd), [xo] "v"(xo),
                           [fo] "v"(fo), [fs] "s"(fs), [tn] "s"(t + 1), [sv0] "s"(sv), [sv1] "s"(sv + 4 * BLK), [rp] "s"(rp), [sx] "s"(PG_XR + L * PG_LX + sx), [sa] "s"(PG_XA + sx),
                           [pw] "v"(pw), [pr] "v"(pr), [fin] "s"(fin)
                         : "memory", "vcc", "scc", "v96", "v97", "v98", "v99", "v100", "v101", "v102", "v103", "v104", "v105", "v106", "v107", "v108", "v109", "v110", "v111", "v112", "v113", "v114", "v115", "v116", "v117", "v118", "v119", "v120", "v121", "v122", "v123", "v124", "v125", "v126", "v127", "v196", "v200", "v201", "v202", "v203", "v204", "v205", "v206", "v207", "v208", "v209", "v210", "v211", "v212", "v213", "v214", "v215", "v216", "v217", "v218", "v219", "v220", "v221", "v222", "v223", "v224", "v225", "v226", "v227", "v228", "v229", "v230", "v231", "v240", "v241", "v242", "v243", "v248", "v250", "v251");
            ASM_ST_SAVE(4); ASM_ST2_SAVE(5);
            if (__builtin_amdgcn_readfirstlane(n)) break;
            early = 0;
            if (spin_fail(spins, P)) { bar(); break; }
        }
        if (t + 1 < NSEG && !__builtin_amdgcn_readfirstlane(early)) vx_fetch(t + 1);
        PMARK(!L && wave == 7 && PMK == 1, 1, t - 1);
        st128(sent4(), rr, offv, slot * VM_SLOT); st128(sent4(), rr, offv + 1024, slot * VM_SLOT);
        __builtin_amdgcn_raw_buffer_store_b32(SENT, rr, offo, slot * XM_SLOT, 16);
        if (wave < 2 && t + AH < NSEG) st128(sent4(), rd, l16, PG_XA + pgb(t + AH, par) + blin(wave * 20480 + jj * 1024));
        ST(7);
        LB(t);
        STFLUSH(t);
    }
    LF();
}

#undef STROLE
#define STROLE 16
template <int WHICH>
__device__ __forceinline__ void w_role(const Params& P, int wave, int L, int cu, int x) {
    constexpr int KS = 20, N = (WHICH == 3) ? FF : DM, NU = 2 * NSEG;
    constexpr unsigned D_IN = WHICH == 1 ? PG_XA : WHICH == 2 ? PG_HS : PG_X1;
    constexpr unsigned D_OUT = WHICH == 1 ? PG_HS : WHICH == 2 ? PG_X1 : PG_H2;
    if (WHICH != 3 && cu * 8 + wave >= 20) {
        for (int u = 0; u <= NU; ++u) bar();
        return;
    }
    auto run = [&](auto Cc) {
    constexpr bool LO = decltype(Cc)::value;
    const int ln = lane_id(), l15 = ln & 15, l4 = ln >> 4;
    const unsigned l16 = ln * 16;
    const rsrc_t rd = mkrsrc(P.ws + PG0, PG_L);
    const int nt0 = (cu * 8 + wave) * 2;
    u32x32 w[5];
    {
        u32x4 f[2 * KS];
        load_wn<2, KS, N>(f, P.w[3 + WHICH], DM, wave, nt0, 0);
#pragma unroll
        for (int n = 0; n < 5; ++n) {
            u32x8 q[4];
#pragma unroll
            for (int i = 0; i < 4; ++i) q[i] = __builtin_shufflevector(f[n * 8 + 2 * i], f[n * 8 + 2 * i + 1], 0, 1, 2, 3, 4, 5, 6, 7);
            w[n] = cat32(q[0], q[1], q[2], q[3]);
        }
        asm volatile("" : "+{v[0:31]}"(w[0]), "+{v[32:63]}"(w[1]), "+{v[64:95]}"(w[2]), "+{v[96:127]}"(w[3]), "+{v[128:159]}"(w[4]));
    }
    PLACE();
    bar();
    const int ocol = nt0 * 16 + (l4 & 1) * 16 + (l4 >> 1) * 8;
    const unsigned o3 = (unsigned)((l4 & 1) * 512 + (l4 >> 1) * 256 + l15 * 16), ro = (l4 >> 1) * 256 + l15 * 16 + (l4 & 1) * 8;
    const int grp = nt0 / 10, sh = ((grp & 1) ? -1 : 1) * ((grp >> 1) + 1);
    if (WHICH == 1) {
        unsigned zv;
        asm volatile("v_mov_b32 %0, 0" : "=v"(zv));
        st128(((u32x4){zv, zv, zv, zv}), rd, l15 <= (grp >> 1) ? toff((grp & 1) ? S_ - 1 - l15 : l15, ocol, par) : OOB, PG_HS);
    }
    auto in0 = [&](int u) { return D_IN + pgb(u >> 1, par) + blin((u & 1) * 20480 + wave * 4096); };
    auto in1 = [&](int u) { return D_IN + pgb(u >> 1, par) + blin((u & 1) * 20480 + 16384) + wave * 1024; };
    u32x16 va;
    u32x4 vb;
    u32x8 cs, k8;
#define WT_W "{v[0:31]}"(w[0]), "{v[32:63]}"(w[1]), "{v[64:95]}"(w[2]), "{v[96:127]}"(w[3]), "{v[128:159]}"(w[4])
    asm volatile("v_mov_b32 v216, 0x3f803f80\n\tv_mov_b32 v217, v216\n\tv_mov_b32 v218, v216\n\tv_mov_b32 v219, v216\n\tv_mov_b32 v220, %[c0]\n\tv_mov_b32 v232, -1\n\tv_mov_b32 v236, -1\n\t"
                 "v_mov_b32 v240, -1\n\tv_mov_b32 v244, -1\n\tv_mov_b32 v248, -1\n\t" ASM_WCS
                 : "=&{v[196:203]}"(cs), "=&{v[216:223]}"(k8), "=&{v[232:247]}"(va), "=&{v[248:251]}"(vb) : WT_W, [c0] "s"(-2.0f * 0.7978845608028654f * 1.4426950408889634f));
    unsigned la = l16, lw = wave * 8192 + l16, sr = l4 * 8192 + ((l15 >> 2) * 16 + l15) * 16 + (l15 & 3) * 4, budget = 1u << 18;
    wait_vm<0>();
    STDECL;
    LDECL;
#pragma unroll 1
    for (int u = 0; u < NU; ++u) {
        const int mt = u & 1;
        const unsigned pg = pgb(u >> 1, par), c3 = D_OUT + blin(mt * (N * 32) + (nt0 >> 1) * 1024);
        unsigned oa = o3, ob = 0, so = pg + c3, sa = u + 2 * AH < NU ? pgb((u >> 1) + AH, par) + c3 : pg + PG_SS + 4096, a;
        if (WHICH == 1) {
            const int orow = u * 16 + l15 + sh;
            ob = toff(orow + AH * SEG, ocol, par);
            oa = (unsigned)orow < (unsigned)S_ ? (ob ^ 8192u) - (AH << 21) : OOB;
            if (orow + AH * SEG >= S_ + (sh < 0 ? sh : 0)) ob = OOB;
            so = sa = D_OUT;
        }
        if (mt) { ST(0); LB(u >> 1); } else { LA(); }
        ASM_ST_DECL;
#define WT_A WT_W, [la] "v"(la), [l16] "v"(l16), [oa] "v"(oa), [rs] "s"(rd), [so] "s"(so), [sa] "s"(sa)
#define WT_G [sr] "v"(sr), [kd] "s"(1.0f / DM), [ke] "s"(1e-5f), [k1] "s"(-2.0f * 0.7978845608028654f * 1.4426950408889634f * 0.044715f)
#define WT_1 WT_G, [ob] "v"(ob), [ss] "s"(pg + PG_SS + mt * 128), [sw] "s"(nt0)
#define WT_2 [ro] "v"(ro), [sx] "s"(pg + PG_XA + blin(mt * 20480 + nt0 * 512)), [ss] "s"(pg + PG_SS + mt * 128)
#define WT_C "memory", "vcc", "scc", "v160", "v161", "v162", "v163", "v164", "v165", "v166", "v167", "v168", "v169", "v170", "v171", "v172", "v173", "v174", "v175", "v176", "v177", "v178", "v179", "v180", "v181", "v182", "v183", "v184", "v185", "v186", "v187", "v188", "v189", "v190", "v191", "v192", "v193", "v194", "v195", "v204", "v205", "v206", "v207", "v208", "v209", "v210", "v211", "v212", "v213", "v214", "v215", "v252"
#define WT_LO(A, ...) asm volatile(A : "+{v[196:203]}"(cs), "+{v[216:223]}"(k8), "+{v[232:247]}"(va), "+{v[248:251]}"(vb), [n] "+s"(budget), [a] "=&s"(a) ASM_ST_OUT ASM_ST2_OUT ASM_ST3_OUT \
                              : WT_A, __VA_ARGS__, [lw] "v"(lw), [i0] "s"(in0(u)), [i1] "s"(in1(u)), [n0] "s"(in0(u + 1)), [n1] "s"(in1(u + 1)) : WT_C)
#define WT_HI(A, ...) asm volatile(A : "+{v[196:203]}"(cs), "+{v[216:223]}"(k8) ASM_ST_OUT ASM_ST3_OUT : WT_A, __VA_ARGS__ : WT_C)
        if constexpr (LO) { if constexpr (WHICH == 1) WT_LO(ASM_WT1_LO, WT_1); else if constexpr (WHICH == 2) WT_LO(ASM_WT2_LO, WT_2); else WT_LO(ASM_WT3_LO, WT_G); }
        else { if constexpr (WHICH == 1) WT_HI(ASM_WT1_HI, WT_1); else if constexpr (WHICH == 2) WT_HI(ASM_WT2_HI, WT_2); else WT_HI(ASM_WT3_HI, WT_G); }
        la ^= 32768u; lw ^= 32768u; sr ^= 32768u;
        PMARK(!L && mt == (WHICH != 1) && (PMK == 1 || WHICH == 3), PMK == 1 ? 1 + WHICH : 1, (u >> 1) - (WHICH == 1));
    }
    wait_vm<0>();
    if constexpr (LO) asm volatile("" :: "{v[232:247]}"(va), "{v[248:251]}"(vb));
    LF();
    };
    if (wave < 4) run(std::true_type{}); else run(std::false_type{});
}

__device__ __forceinline__ void w4_role(const Params& P, int wave, int L, int cu, int x) {
    constexpr unsigned W_FL = 122880;
    const int ln = lane_id(), l15 = ln & 15, l4 = ln >> 4;
    const unsigned l16 = ln * 16;
    const rsrc_t rd = mkrsrc(P.ws + PG0, PG_L);
    lds_st<unsigned>(W_FL + wave * 4, 0u);
    STDECL;
    if (wave < 6) {
        u32x32 w[5];
        {
            u32x4 g[40];
            load_wn<5, 8, DM>(g, P.w[7], FF, wave, cu * 5, wave * 8);
#pragma unroll
            for (int n = 0; n < 5; ++n) {
                u32x8 f[4];
#pragma unroll
                for (int i = 0; i < 4; ++i) f[i] = __builtin_shufflevector(g[n * 8 + 2 * i], g[n * 8 + 2 * i + 1], 0, 1, 2, 3, 4, 5, 6, 7);
                w[n] = cat32(f[0], f[1], f[2], f[3]);
            }
            asm volatile("" : "+{v[0:31]}"(w[0]), "+{v[32:63]}"(w[1]), "+{v[64:95]}"(w[2]), "+{v[96:127]}"(w[3]), "+{v[128:159]}"(w[4]));
        }
        PLACE();
        bar();
        unsigned budget = 1u << 18;
        u32x32 av;
        asm volatile("v_mov_b32 v160, -1\n\tv_mov_b32 v164, -1\n\tv_mov_b32 v168, -1\n\tv_mov_b32 v172, -1\n\tv_mov_b32 v176, -1\n\tv_mov_b32 v180, -1\n\tv_mov_b32 v184, -1\n\tv_mov_b32 v188, -1" : "={v[160:191]}"(av));
        for (int u = 0; u < 2 * NSEG; ++u) {
            if (u & 1) ST(0);
            for (int spins = 0;; ++spins) {
                asm volatile("" ::: "memory");
                if ((int)__builtin_amdgcn_readfirstlane(lds_ld<unsigned>(W_FL + 24 + (u & 1) * 4)) + 3 >= u) break;
                __builtin_amdgcn_s_sleep(2);
                if (spin_fail(spins, P)) break;
            }
            const unsigned gb = PG_H2 + pgb(u >> 1, par) + ((u & 1) * 6 + wave) * BLK, gn = PG_H2 + pgb((u + 1) >> 1, par) + ((~u & 1) * 6 + wave) * BLK;
            ASM_ST_DECL;
            asm volatile(ASM_W4_M : [n] "+s"(budget), "+{v[160:191]}"(av) ASM_ST2_OUT
                         : "{v[0:31]}"(w[0]), "{v[32:63]}"(w[1]), "{v[64:95]}"(w[2]), "{v[96:127]}"(w[3]), "{v[128:159]}"(w[4]),
                           [ld] "v"(l16 + wave * 1024 + (u & 3) * 30720), [l16] "v"(l16), [rs] "s"(rd), [s0] "s"(gb), [s1] "s"(gb + 4096), [n0] "s"(gn), [n1] "s"(gn + 4096), [un] "s"(u + 1), [fl] "v"(W_FL + wave * 4)
                         : "memory", "vcc", "scc", "v192", "v193", "v194", "v195", "v196", "v197", "v198", "v199", "v200", "v201", "v202", "v203", "v204", "v205", "v206", "v207", "v208", "v209", "v210", "v211", "v212");
            PMARK(!L && (u & 1) && PMK == 2, 2, u >> 1);
        }
        asm volatile("s_waitcnt vmcnt(0)" :: "{v[160:191]}"(av) : "memory");
    } else {
        const int mt = wave - 6;
        PLACE();
        bar();
        const unsigned c0 = mt * 20480 + cu * 2560, o8 = (l4 >> 1) * 256 + l15 * 16 + (l4 & 1) * 8;
        unsigned budget = 1u << 18;
        u32x16 ra;
        u32x4 rb;
        asm volatile("v_mov_b32 v164, -1" : "={v[164:179]}"(ra), "={v[180:183]}"(rb));
        __builtin_amdgcn_s_setprio(W4P);
#define W4F_OUT "+{v[164:179]}"(ra), "+{v[180:183]}"(rb), [n] "+s"(budget)
#define W4F_IN [o0] "v"(o8 + blin(c0)), [o1] "v"(o8 + blin(c0 + 512)), [o2] "v"(o8 + blin(c0 + 1024)), [o3] "v"(o8 + blin(c0 + 1536)), [o4] "v"(o8 + blin(c0 + 2048)), [rd] "s"(rd), \
               [c1] "s"(PG_X1 + pg), [ca] "s"(PG_XA + pg), [n1] "s"(PG_X1 + pn), [na] "s"(PG_XA + pn), [f0] "v"(W_FL + (ln & 3) * 4), [f1] "v"(W_FL + 16 + (ln & 1) * 4), [fd] "v"(W_FL + wave * 4), \
               [la] "v"((u & 3) * 30720 + l16), [u] "s"(u), [un] "s"(u + 1)
#define W4F_CLOB "memory", "vcc", "scc", "v64", "v65", "v66", "v67", "v68", "v69", "v70", "v71", "v72", "v73", "v74", "v75", "v76", "v77", "v78", "v79", "v80", "v81", "v82", "v83", "v84", "v85", "v86", "v87", "v88", "v89", "v90", "v91", "v92", "v93", "v94", "v95", "v96", "v97", "v98", "v99", "v100", "v101", "v102", "v103", "v104", "v105", "v106", "v107", "v108", "v109", "v110", "v111", "v112", "v113", "v114", "v115", "v116", "v117", "v118", "v119", "v120", "v121", "v122", "v123", "v124", "v125", "v126", "v127", "v128", "v129", "v130", "v131", "v132", "v133", "v134", "v135", "v136", "v137", "v138", "v139", "v140", "v141", "v142", "v143", "v144", "v145", "v146", "v147", "v148", "v149", "v150", "v151", "v152", "v153", "v154", "v155", "v156", "v157", "v158", "v159", "v160", "v161", "v162", "v163", "v184", "v185", "v186", "v187", "v188", "v189", "v190", "v191", "v192", "v193", "v194", "v195", "v196", "v197", "v198", "v199", "v200"
        if (L) {
            const rsrc_t ro = mkrsrc(P.out, ACT);
#pragma unroll 1
            for (int t = 0; t < NSEG; ++t) {
                const unsigned u = t * 2 + mt, pg = pgb(t, par), pn = pgb(t + 1, par);
                ASM_ST_DECL;
                asm volatile(ASM_W4F_1 : W4F_OUT ASM_ST2_OUT : W4F_IN, [pa] "v"(l15 * (DM * 2) + (l4 & 1) * 32 + (l4 >> 1) * 16), [pb] "v"(l15 * (DM * 2) + l4 * 8), [ro] "s"(ro), [so] "s"(u * 20480 + cu * 160) : W4F_CLOB);
            }
        } else {
            const rsrc_t rn = mkrsrc(P.ws + (par ^ 1) * 8192, RG_L);
#pragma unroll 1
            for (int t = 0; t < NSEG; ++t) {
                const unsigned u = t * 2 + mt, pg = pgb(t, par), pn = pgb(t + 1, par), sn = (pg ^ 8192) + PG_LX, st = RG_ST + (u >> 3) * BLK + (u & 7) * 1024;
                ASM_ST_DECL;
                asm volatile(ASM_W4F_0 : W4F_OUT ASM_ST_OUT ASM_ST2_OUT ASM_ST3_OUT
                             : W4F_IN, [pa] "v"(blin(c0 + (l4 & 1) * 512) + (l4 >> 1) * 256 + l15 * 16), [pb] "v"(blin(c0 + 1024 + (l4 & 1) * 512) + (l4 >> 1) * 256 + l15 * 16),
                               [so] "v"(ln < 16 ? ln * 8 : OOB), [si] "v"(ln * 8), [rr] "s"(rr), [sp] "s"(st + cu * 128), [sq] "s"(st), [xr] "s"(PG_XR + sn), [xn] "s"(PG_XN + sn),
                               [kd] "s"(1.0f / DM), [ke] "s"(1e-5f), [tn] "s"(t + 1), [fo] "v"(ln < 32 ? (ln >> 3) * 2048 + (ln & 7) * 16 : OOB), [rn] "s"(rn), [fs] "s"(RG_FX + (cu * 2 + mt) * 128) : W4F_CLOB);
            }
        }
        asm volatile("s_waitcnt vmcnt(0)" :: "{v[164:179]}"(ra), "{v[180:183]}"(rb) : "memory");
    }
}

__device__ __forceinline__ unsigned probe(char* ws, unsigned base, unsigned bit, int n) {
    const rsrc_t r = mkrsrc(ws, RG_SLOT);
    unsigned m0 = ~0u, m1 = ~0u;
    for (int i = 0; i < n; ++i) {
        const unsigned long long t0 = __builtin_amdgcn_s_memtime();
        __builtin_amdgcn_raw_buffer_store_b32(0u, r, 96 * BLK + base + (i & 1) * bit + ((i >> 2) & 1) * 4096 + (i >> 2) * 768, 0, 16);
        asm volatile("s_waitcnt vmcnt(0)" ::: "memory");
        const unsigned d = (unsigned)(__builtin_amdgcn_s_memtime() - t0);
        if (i & 1) m1 = d < m1 ? d : m1; else m0 = d < m0 ? d : m0;
    }
    return m0 > m1 ? 1u : 0u;
}
__device__ __forceinline__ void setup(const Params& P, int wave, int mt, int par0, int ph) {
    const bool xw = wave < 4 && (mt >= 2 * XS0) == (ph != 0);
    const int tid = threadIdx.x & 255, r15 = tid & 15, slot = tid >> 4;
    const rsrc_t rw = mkrsrc(P.ws, WS_END);
    u32x4 v[5];
    float s = 0.f, q = 0.f;
    if (xw) {
#pragma unroll
        for (int c = 0; c < 5; ++c) v[c] = *(const u32x4*)(P.x + (size_t)(mt * 16 + r15) * DM + (slot * 5 + c) * 8);
#pragma unroll
        for (int c = 0; c < 5; ++c)
#pragma unroll
            for (int e = 0; e < 4; ++e) s += lo2f(v[c][e]) + hi2f(v[c][e]);
        lds_st<float>((slot * 17 + r15) * 4, s);
    } else if (wave >= 4) {
        constexpr unsigned N0 = RG_ST / BLK, N1 = 4 * N0 + 16, N2 = PG_SS / BLK;
        const u32x4 s4 = sent4();
        for (unsigned k = mt; k < 2 * (N1 + 4 * N2); k += 128) {
            const unsigned i = k >> 1;
            const unsigned o = i < N1 ? (i < 4 * N0 ? (i / N0) * RG_SLOT + (i % N0) * BLK : (i - 3 * N0) * BLK) + (par0 ^ ph) * 8192 : PG0 + pgb((i - N1) / N2, par0 ^ ph) + ((i - N1) % N2) * BLK;
            st128(s4, rw, tid * 16, o + (k & 1) * 4096);
        }
        if (ph == 0 && mt >= 108) st128(~s4, rw, tid * 16, RG_FV + (mt - 108) * 4096);
    }
    bar();
    float mean = 0.f;
    if (xw) {
        s = 0.f;
#pragma unroll
        for (int k = 0; k < 16; ++k) s += lds_ld<float>((k * 17 + r15) * 4);
        mean = s * (1.0f / DM);
#pragma unroll
        for (int c = 0; c < 5; ++c)
#pragma unroll
            for (int e = 0; e < 4; ++e) { const float a = lo2f(v[c][e]) - mean, b = hi2f(v[c][e]) - mean; q += a * a + b * b; }
        lds_st<float>(2048 + (slot * 17 + r15) * 4, q);
    }
    bar();
    if (xw) {
        q = 0.f;
#pragma unroll
        for (int k = 0; k < 16; ++k) q += lds_ld<float>(2048 + (k * 17 + r15) * 4);
        const float m1 = rbf(mean), r = 1.0f / ln_den(q * (1.0f / DM));
#pragma unroll
        for (int c = 0; c < 5; ++c) {
            const unsigned o = toff(mt * 16 + r15, (slot * 5 + c) * 8, par0);
            st128(v[c], rw, o, PG0 + PG_XR);
            u32x4 n;
#pragma unroll
            for (int e = 0; e < 4; ++e) n[e] = pack2(rbf(lo2f(v[c][e]) - m1) * r, rbf(hi2f(v[c][e]) - m1) * r);
            st128(n, rw, o, PG0 + PG_XN);
        }
    }
    wait_vm<0>();
    bar();
    if (threadIdx.x < 8) __hip_atomic_fetch_add((unsigned*)(P.ws + FL0 + FL_IN + (ph * 8 + threadIdx.x) * 128), 1u, RLX_AGENT);
}

__global__ __launch_bounds__(512, 2) void mega_kernel(Params P) {
    const int wave = __builtin_amdgcn_readfirstlane(threadIdx.x >> 6);
    u32x4 cpf;
    {
        unsigned long long pc;
        unsigned len;
        asm volatile("s_getpc_b64 %0\n\ts_mov_b32 %1, _Z8code_endv-_Z11mega_kernel6Params" : "=s"(pc), "=s"(len));
        cpf = __builtin_amdgcn_raw_buffer_load_b128(mkrsrc((const void*)(pc & ~255ull), len), (blockIdx.x >> 3) * 8192 + threadIdx.x * 16, 0, 0);
    }
    {
        const rsrc_t rf = mkrsrc(P.ws + FL0, FL_BYTES);
        if (blockIdx.x == 0) {
            unsigned h = 0;
            if (wave == 7) h = (probe(P.ws, 0, 8192, CAL1) ^ ((unsigned)__builtin_amdgcn_s_getreg((3 << 11) | 20) >> 2)) & 1u;
            if (wave == 0) {
                const unsigned l16 = lane_id() * 16;
                u32x4 z = {0u, 0u, 0u, 0u};
                asm volatile("" : "+v"(z));
#pragma unroll
                for (int k = 0; k < 4; ++k) st128(z, rf, k * 1024 + l16 < FL_HB ? k * 1024 + l16 : OOB, 0);
                wait_vm<0>();
                for (unsigned c = 1; c < 200000u; ++c) {
                    __builtin_amdgcn_raw_buffer_store_b32(c, rf, l16 < 128 ? l16 * 8 : OOB, FL_HB, 16);
                    unsigned n = __builtin_amdgcn_raw_buffer_load_b32(rf, (l16 & 127u) * 8, FL_TK, 16), sum = 0;
                    n = n < 32u ? n : 32u;
#pragma unroll
                    for (int k = 0; k < 8; ++k) sum += __builtin_amdgcn_readlane(n, k);
                    if (sum >= 255u) break;
                }
            }
            bar();
            if (wave == 7 && lane_id() < 16) __hip_atomic_fetch_add((unsigned*)(P.ws + FL0 + FL_IN + (threadIdx.x & 15) * 128), (2u + h) << 16, RLX_AGENT);
        } else if (wave == 0) {
            unsigned o;
            asm volatile("v_mov_b32 %0, 0" : "=v"(o));
            const unsigned v0 = __builtin_amdgcn_readfirstlane(__builtin_amdgcn_raw_buffer_load_b32(rf, o, FL_HB + (blockIdx.x & 7) * 128, 16));
            for (int spins = 0; spins < 400000; ++spins) {
                asm volatile("" ::: "memory");
                if (__builtin_amdgcn_readfirstlane(__builtin_amdgcn_raw_buffer_load_b32(rf, o, FL_HB + (blockIdx.x & 7) * 128, 16)) != v0) break;
                __builtin_amdgcn_s_sleep(1);
            }
            asm volatile("" ::: "memory");
        }
    }
    if (threadIdx.x == 0) {
        unsigned g = (unsigned)__builtin_amdgcn_s_getreg((3 << 11) | 20) & 7u, k;
        for (int n = 0; (k = __hip_atomic_fetch_add((unsigned*)(P.ws + FL0 + FL_TK + g * 128), 1u, RLX_AGENT)) >= 32u && n < 8; ++n) g = (g + 1) & 7u;
        lds_st<unsigned>(0, g << 5 | (k & 31u));
    }
    bar();
    const unsigned tk = __builtin_amdgcn_readfirstlane(lds_ld<unsigned>(0));
    asm volatile("" :: "v"(cpf));
    bar();
    const int g4 = (tk >> 5) & 3, k = tk & 31;
    const int L = tk >> 7, x = g4, r = k;
    if (L) {
        const int par0 = (init_wait(P, wave, 1, 0x20000u) >> 16) & 1;
        if (tk == 254 && wave == 0) {
            const unsigned xc = (unsigned)__builtin_amdgcn_s_getreg((3 << 11) | 20);
            const unsigned f = (probe(P.ws, ((par0 ^ (xc >> 2)) & 1) * 8192, BLK, CAL2) ^ 1u ^ xc) & 1u;
            if (threadIdx.x < 16) __hip_atomic_fetch_add((unsigned*)(P.ws + FL0 + FL_IN + threadIdx.x * 128), (2u + f) << 20, RLX_AGENT);
        }
#pragma unroll 1
        for (int ph = 0; ph < 2; ++ph) {
            setup(P, wave, (tk & 31) * 4 + g4, par0, ph);
            for (int k = ph * DLY_L; k < DLY_L; ++k) __builtin_amdgcn_s_sleep(32);
        }
    }
    {
        const int i = (r - 26) * 4 + x;
        const int d = (r < 8 ? DLY_S : r < 21 ? 0 : i < 4 ? (L ? 0 : DLY_H) : DLY_W + DLY_X * (i < 7 ? 0 : i < 10 ? 1 : i < 16 ? 2 : 3));
        for (int k = 0; k < d; ++k) __builtin_amdgcn_s_sleep(32);
    }
    if (r < 8) s_role(P, wave, L, x * 8 + r, g4);
    else if (r < 16) v_role(P, wave ^ 4, L, x * 8 + r - 8, g4);
    else if (r < 21) r_role(P, wave, L, (r - 16) * 4 + x, g4);
    else {
        const int i = (r - 26) * 4 + x;
        if (i < 4) h_role(P, wave, L, r < 25 ? x * 4 + r - 21 : 16 + x * 2 + r - 25, g4);
        else if (i < 7) w_role<1>(P, wave, L, i - 4, g4);
        else if (i < 10) w_role<2>(P, wave, L, i - 7, g4);
        else if (i < 16) w_role<3>(P, wave, L, i - 10, g4);
        else w4_role(P, wave, L, i - 16, g4);
    }
}

__global__ void code_end() {}

extern "C" void kernel_launch(void* const* d_in, const int* in_sizes, int n_in, void* d_out, int out_size, void* d_ws,
                              size_t ws_size, hipStream_t stream) {
    char* ws = (char*)d_ws;
    Params P;
    P.ws = ws; P.x = (const bf16_t*)d_in[0]; P.mem0 = (const bf16_t*)d_in[1]; P.out = (bf16_t*)d_out;
    for (int i = 0; i < 8; ++i) P.w[i] = (const bf16_t*)d_in[i < 4 ? 2 + i : 4 + i];
    (void)hipFuncSetAttribute((const void*)mega_kernel, hipFuncAttributeMaxDynamicSharedMemorySize, MEGA_LDS);
    mega_kernel<<<256, 512, MEGA_LDS, stream>>>(P);
}
```
